```python
import torch

def _softmax(x, axis=-1):
    return torch.softmax(x, dim=axis)

def _gelu(x):
    return torch.nn.functional.gelu(x, approximate='tanh')

def _silu(x):
    return torch.nn.functional.silu(x)

def _sigmoid(x):
    return torch.sigmoid(x)

def _layernorm(x, axis=-1, eps=1e-05):
    m = x.mean(dim=axis, keepdim=True)
    v = x.var(dim=axis, keepdim=True, unbiased=False)
    return (x - m) / torch.sqrt(v + eps)

def _scatter_add(tgt, idx, src, axis=0):
    return tgt.clone().index_add_(axis, idx, src)

def _segment_sum(src, seg, n):
    out = torch.zeros((n,) + src.shape[1:], dtype=src.dtype)
    return out.index_add_(0, seg, src)

def _pad(x, pw):
    flat = tuple((v for pair in reversed(pw) for v in pair))
    return torch.nn.functional.pad(x, flat)
kernel_name = 'interleave_layers__memory_aug_recurrent_mem_tr__mixer_s_mlp__prompt'

def setup_inputs():

    _w = lambda a: (lambda t: t.to(torch.bfloat16) if t.is_floating_point() else t)(torch.as_tensor(a))

    def _A_setup():
        B, S, Dm = (1, 2048, 640)
        H, Dh = (32, 192)
        M, seg = (8, 32)
        seg = min(seg, S)
        S = S // seg * seg
        return {'x': _w((lambda *s: torch.randn(*s, dtype=torch.bfloat16) * 0.05)(B, S, Dm)), 'mem0': _w((lambda *s: torch.randn(*s, dtype=torch.bfloat16) * 0.8)(B, M, Dm)), 'Wq': _w((lambda *s: torch.randn(*s, dtype=torch.bfloat16) * 0.8)(Dm, H, Dh) * Dm ** (-0.5)), 'Wk': _w((lambda *s: torch.randn(*s, dtype=torch.bfloat16) * 0.8)(Dm, H, Dh) * Dm ** (-0.5)), 'Wv': _w((lambda *s: torch.randn(*s, dtype=torch.bfloat16) * 0.8)(Dm, H, Dh) * Dm ** (-0.5)), 'Wo': _w((lambda *s: torch.randn(*s, dtype=torch.bfloat16) * 0.8)(H, Dh, Dm) * (H * Dh) ** (-0.5)), 'seg_len': seg}
    _A_in = _A_setup()

    def _B_setup():
        B, S, D, F = (1, 2048, 640, 1536)
        return {'x': _w((lambda *s: torch.randn(*s, dtype=torch.bfloat16) * 0.05)(B, S, D)), 'w1': _w((lambda *s: torch.randn(*s, dtype=torch.bfloat16) * 0.8)(D, D) * D ** (-0.5)), 'w2': _w((lambda *s: torch.randn(*s, dtype=torch.bfloat16) * 0.8)(D, D) * D ** (-0.5)), 'w3': _w((lambda *s: torch.randn(*s, dtype=torch.bfloat16) * 0.8)(D, F) * D ** (-0.5)), 'w4': _w((lambda *s: torch.randn(*s, dtype=torch.bfloat16) * 0.8)(F, D) * F ** (-0.5))}
    _B_in = _B_setup()
    return {'A_x': _A_in['x'], 'A_mem0': _A_in['mem0'], 'A_Wq': _A_in['Wq'], 'A_Wk': _A_in['Wk'], 'A_Wv': _A_in['Wv'], 'A_Wo': _A_in['Wo'], 'A_seg_len': _A_in['seg_len'], 'B_x': _B_in['x'], 'B_w1': _B_in['w1'], 'B_w2': _B_in['w2'], 'B_w3': _B_in['w3'], 'B_w4': _B_in['w4']}

def reference(A_x, A_mem0, A_Wq, A_Wk, A_Wv, A_Wo, A_seg_len, B_x, B_w1, B_w2, B_w3, B_w4):

    def _A_ref(x, mem0, Wq, Wk, Wv, Wo, seg_len):
        B, S, Dm = x.shape
        H, Dh = (Wq.shape[1], Wq.shape[2])
        M = mem0.shape[1]
        scale = Dh ** (-0.5)
        n_seg = S // seg_len
        L = M + seg_len
        idx = torch.arange(L)
        mask = (idx[:, None] < M) | (idx[:, None] >= idx[None, :])
        mem = mem0
        outs = []
        for s in range(n_seg):
            xs = x[:, s * seg_len:(s + 1) * seg_len, :].clone()
            h = torch.concatenate([mem, xs], axis=1)
            q = torch.einsum('btd,dhe->bhte', h, Wq)
            k = torch.einsum('btd,dhe->bhte', h, Wk)
            v = torch.einsum('btd,dhe->bhte', h, Wv)
            scores = torch.einsum('bhqe,bhke->bhqk', q, k) * scale
            scores = torch.where(mask[None, None], scores, -1000000000.0)
            p = _softmax(scores, axis=-1)
            o = torch.einsum('bhqk,bhke->bhqe', p, v)
            o = torch.einsum('bhte,hed->btd', o, Wo)
            mem = o[:, :M, :].clone()
            outs.append(o[:, M:, :])
        return torch.concatenate(outs, axis=1)

    def _B_ref(x, w1, w2, w3, w4):
        B, S, D = x.shape
        h = _gelu(_layernorm(x, axis=-1) @ w1)
        c = D // 4
        g0 = _pad(h[:, :, 0 * c:1 * c], ((0, 0), (1, 0), (0, 0)))[:, :S, :]
        g1 = _pad(h[:, :, 1 * c:2 * c], ((0, 0), (0, 1), (0, 0)))[:, 1:, :]
        g2 = _pad(h[:, :, 2 * c:3 * c], ((0, 0), (2, 0), (0, 0)))[:, :S, :]
        g3 = _pad(h[:, :, 3 * c:], ((0, 0), (0, 2), (0, 0)))[:, 2:, :]
        hs = torch.concatenate([g0, g1, g2, g3], axis=-1) @ w2
        x1 = x + hs
        h2 = _gelu(_layernorm(x1, axis=-1) @ w3) @ w4
        return x1 + h2
    x = A_x
    x = x + _A_ref(_layernorm(x, axis=-1), A_mem0, A_Wq, A_Wk, A_Wv, A_Wo, A_seg_len)
    x = x + _B_ref(_layernorm(x, axis=-1), B_w1, B_w2, B_w3, B_w4)
    x = x + _A_ref(_layernorm(x, axis=-1), A_mem0, A_Wq, A_Wk, A_Wv, A_Wo, A_seg_len)
    x = x + _B_ref(_layernorm(x, axis=-1), B_w1, B_w2, B_w3, B_w4)
    return x
```

```cpp
#include <hip/hip_runtime.h>
#include <stdint.h>
#include <type_traits>

typedef unsigned short bf16_t;
using bf16x2 = __attribute__((ext_vector_type(2))) __bf16;
using bf16x8 = __attribute__((ext_vector_type(8))) __bf16;
using bf16x16 = __attribute__((ext_vector_type(16))) __bf16;
using f32x2 = __attribute__((ext_vector_type(2))) float;
using f32x4 = __attribute__((ext_vector_type(4))) float;
using u32x2 = __attribute__((ext_vector_type(2))) unsigned;
using u32x4 = __attribute__((ext_vector_type(4))) unsigned;
typedef __amdgpu_buffer_rsrc_t rsrc_t;

__device__ __forceinline__ unsigned pack2(float a, float b) {
    bf16x2 r = __builtin_convertvector((f32x2){a, b}, bf16x2);
    return __builtin_bit_cast(unsigned, r);
}
__device__ __forceinline__ float rbf(float f) { return __uint_as_float(pack2(f, 0.f) << 16); }
__device__ __forceinline__ float lo2f(unsigned u) { return __uint_as_float(u << 16); }
__device__ __forceinline__ float hi2f(unsigned u) { return __uint_as_float(u & 0xFFFF0000u); }
__device__ __forceinline__ rsrc_t mkrsrc(const void* p, unsigned bytes) {
    return __builtin_amdgcn_make_buffer_rsrc((void*)p, 0, bytes, 0x00020000);
}

constexpr int S_ = 2048, DM = 640, NH = 32, DH = 192, HD = NH * DH  , SEG = 32, NSEG = 64;
constexpr int FF = 1536;

#define XMAP
#define NCPY 4
constexpr unsigned NSLOT = 4, NCP = NCPY;
constexpr unsigned XP_COPY = 20480, XP_SLOT = NCP * XP_COPY;
constexpr unsigned XM_COPY = 10240, XM_SLOT = NCP * XM_COPY;
constexpr unsigned VM_SLOT = 40 * 8 * 1024;
constexpr unsigned PX_SLOT = 8 * 8192;
constexpr unsigned RG_XP = 0, RG_XM = RG_XP + NSLOT * XP_SLOT, RG_VM = RG_XM + NSLOT * XM_SLOT, RG_PX = RG_VM + NSLOT * VM_SLOT;
constexpr unsigned VX_SLOT = 40 * 32 * 1024;
constexpr unsigned RG_ST = RG_PX + NSLOT * PX_SLOT, RG_L = RG_ST + NSEG * 8 * 256;
constexpr unsigned FL0 = 2 * RG_L, FL_VO = 512, FL_BYTES = FL_VO + 2 * 128 * 128;
constexpr unsigned ACT = S_ * DM * 2;
constexpr unsigned D_XR = 0, D_XN = ACT, D_XA = 2 * ACT, D_HS = 3 * ACT, D_X1 = 4 * ACT, D_H2 = 5 * ACT, D_SS = D_H2 + S_ * FF * 2;
#define PXROT 5
#define W4SL 6
#define RXDEL 4
#define VXD 32
#define VXAUX 16
constexpr unsigned D_VX = D_SS + S_ * 8, D_L = D_VX + VXD * VX_SLOT;
constexpr unsigned DATA0 = (FL0 + FL_BYTES + 4095) & ~4095u;
constexpr int AH = 3;
constexpr unsigned WT0 = DATA0 + 2 * D_L;
constexpr unsigned WT_QKV = WT0, WT_O = WT_QKV + 3 * HD * DM * 2, WT_1 = WT_O + HD * DM * 2, WT_2 = WT_1 + DM * DM * 2;
constexpr unsigned WT_3 = WT_2 + DM * DM * 2, WT_4 = WT_3 + DM * FF * 2, WS_END = WT_4 + DM * FF * 2;

struct TileJob { const bf16_t* W; bf16_t* out; int perm, N, ksh, blk0; };
struct TileJobs { TileJob j[8]; };
__device__ __forceinline__ void tile_body(const TileJobs& J, int b) {
    __shared__ __attribute__((aligned(16))) bf16_t t[32][136];
    int ji = 0;
#pragma unroll
    for (int i = 1; i < 8; ++i) if (b >= J.j[i].blk0) ji = i;
    const bf16_t* W = J.j[ji].W; bf16_t* out = J.j[ji].out;
    const int N = J.j[ji].N, ksh = J.j[ji].ksh, perm = J.j[ji].perm;
    b -= J.j[ji].blk0;
    const int nb = N / 128;
    const int ksg = b / nb, n0 = (b % nb) * 128;
    const int tid = threadIdx.x;
    const int grp = ksg / ksh, ks = ksg % ksh;
#pragma unroll
    for (int i = 0; i < 2; ++i) {
        const int u = tid + i * 256, r = u >> 4, c = u & 15;
        const int row = perm ? grp * DH + ks * 16 + (r >> 4) * 96 + (r & 15) : ksg * 32 + r;
        *(u32x4*)&t[r][c * 8] = *(const u32x4*)(W + (size_t)row * N + n0 + c * 8);
    }
    __syncthreads();
    const int lane = tid & 63;
#pragma unroll
    for (int i = 0; i < 2; ++i) {
        const int ntl = (tid >> 6) + i * 4, n = ntl * 16 + (lane & 15);
        unsigned w[4];
#pragma unroll
        for (int j = 0; j < 4; ++j) {
            const int kk = perm ? (j >> 1) * 16 + (lane >> 4) * 4 + (j & 1) * 2 : (lane >> 4) * 8 + 2 * j;
            w[j] = (unsigned)t[kk][n] | ((unsigned)t[kk + 1][n] << 16);
        }
        *(u32x4*)(out + ((size_t)(grp * (N / 16) + n0 / 16 + ntl) * ksh + ks) * 512 + lane * 8) = (u32x4){w[0], w[1], w[2], w[3]};
    }
}

#define SB() __builtin_amdgcn_sched_barrier(0)
#define LAS __attribute__((address_space(3)))
template <class T> __device__ __forceinline__ T lds_ld(unsigned a) { return *(const LAS T*)(a); }
template <class T> __device__ __forceinline__ void lds_st(unsigned a, T v) { *(LAS T*)(a) = v; }
constexpr unsigned OOB = 0x7FFFFF00u;
template <int N> __device__ __forceinline__ void wait_vm() { asm volatile("s_waitcnt vmcnt(%0)" :: "i"(N) : "memory"); }
__device__ __forceinline__ void bar() {
    asm volatile("s_waitcnt lgkmcnt(0)" ::: "memory");
    __builtin_amdgcn_s_barrier();
    asm volatile("" ::: "memory");
}
__device__ __forceinline__ int lane_id() {
    int ln;
    asm volatile("v_mbcnt_lo_u32_b32 %0, -1, 0\n\tv_mbcnt_hi_u32_b32 %0, -1, %0" : "=v"(ln));
    return ln;
}
__device__ __forceinline__ void swap16(unsigned& a, unsigned& b) { asm volatile("s_nop 3\n\tv_permlane16_swap_b32 %0, %1\n\ts_nop 1" : "+v"(a), "+v"(b)); }
__device__ __forceinline__ void swap32(unsigned& a, unsigned& b) { asm volatile("s_nop 3\n\tv_permlane32_swap_b32 %0, %1\n\ts_nop 1" : "+v"(a), "+v"(b)); }
__device__ __forceinline__ float sum_x16(float x) { unsigned a = __float_as_uint(x), b = a; swap16(a, b); return __uint_as_float(a) + __uint_as_float(b); }
__device__ __forceinline__ float sum_x32(float x) { unsigned a = __float_as_uint(x), b = a; swap32(a, b); return __uint_as_float(a) + __uint_as_float(b); }
__device__ __forceinline__ float max_x16(float x) { unsigned a = __float_as_uint(x), b = a; swap16(a, b); return __builtin_fmaxf(__uint_as_float(a), __uint_as_float(b)); }
__device__ __forceinline__ float max_x32(float x) { unsigned a = __float_as_uint(x), b = a; swap32(a, b); return __builtin_fmaxf(__uint_as_float(a), __uint_as_float(b)); }
__device__ __forceinline__ void sum2_rows(float& s, float& q) {
    unsigned a = __float_as_uint(s), b = a, c = __float_as_uint(q), d = c;
    asm volatile("s_nop 2\n\tv_permlane16_swap_b32 %0, %1\n\tv_permlane16_swap_b32 %2, %3\n\tv_add_f32 %0, %0, %1\n\tv_add_f32 %2, %2, %3\n\tv_mov_b32 %1, %0\n\tv_mov_b32 %3, %2\n\t"
                 "s_nop 1\n\tv_permlane32_swap_b32 %0, %1\n\tv_permlane32_swap_b32 %2, %3\n\tv_add_f32 %0, %0, %1\n\tv_add_f32 %2, %2, %3" : "+v"(a), "+v"(b), "+v"(c), "+v"(d));
    s = __uint_as_float(a); q = __uint_as_float(c);
}
__device__ __forceinline__ u32x4 merge16(u32x2 a, u32x2 b) {
    unsigned a0 = a[0], a1 = a[1], b0 = b[0], b1 = b[1];
    asm volatile("s_nop 3\n\tv_permlane16_swap_b32 %0, %2\n\tv_permlane16_swap_b32 %1, %3\n\ts_nop 1" : "+v"(a0), "+v"(a1), "+v"(b0), "+v"(b1));
    return (u32x4){a0, a1, b0, b1};
}
template <int AUXV>
__device__ __forceinline__ void st128_(u32x4 v, rsrc_t r, unsigned voff, unsigned soff) {
    __builtin_amdgcn_raw_buffer_store_b128(v, r, voff, soff, AUXV);
    asm volatile("s_nop 1" :: "v"(v) : "memory");
}
#define st128(V, R, VO_, SO_) st128_<16>(V, R, VO_, SO_)
#define LD128(R, VO_, SO_) __builtin_amdgcn_raw_buffer_load_b128(R, VO_, SO_, 16)
#define LD64(R, VO_, SO_) __builtin_amdgcn_raw_buffer_load_b64(R, VO_, SO_, 16)
#define BF8(X) __builtin_bit_cast(bf16x8, X)
__device__ __forceinline__ float gelu_tanh(float x) {
    const float c0 = -2.0f * 0.7978845608028654f * 1.4426950408889634f, c1 = c0 * 0.044715f;
    const float e = __builtin_amdgcn_exp2f(x * __builtin_fmaf(x * x, c1, c0));
    return x * __builtin_amdgcn_rcpf(1.0f + e);
}
#define MFMA(A_, B_, C_) C_ = __builtin_amdgcn_mfma_f32_16x16x32_bf16(A_, B_, C_, 0, 0, 0)
#define SMF(A_, B_, C_) C_ = __builtin_amdgcn_smfmac_f32_16x16x64_bf16(A_, B_, C_, sidx, 0, 0)
__device__ __forceinline__ float ln_den(float var) { return rbf(__builtin_amdgcn_sqrtf(rbf(rbf(var) + 1e-5f))); }
__device__ __forceinline__ float rcp(float x) { return __builtin_amdgcn_rcpf(x); }

struct Params {
    char* ws;
    const bf16_t* mem0;
    bf16_t* out;
};

#define RLX_AGENT __ATOMIC_RELAXED, __HIP_MEMORY_SCOPE_AGENT
#define SENT 0xFFFFFFFFu
constexpr int SPIN_CAP = 400000;
__device__ __forceinline__ bool spin_fail(int spins, const Params& P) {
    if ((spins & 1023) == 1023) {
        const rsrc_t rt = mkrsrc(P.ws + FL0, FL_BYTES);
        unsigned o;
        asm volatile("v_mov_b32 %0, 0x100" : "=v"(o));
        if (__builtin_amdgcn_readfirstlane(__builtin_amdgcn_raw_buffer_load_b32(rt, o, 0, 16)) != 0u) return true;
        if (spins > SPIN_CAP) { __builtin_amdgcn_raw_buffer_store_b32(o, rt, o, 0, 16); return true; }
    }
    return false;
}
__device__ __forceinline__ bool ok4(u32x4 v) { return v[0] != SENT && v[1] != SENT && v[2] != SENT && v[3] != SENT; }
__device__ __forceinline__ unsigned nosent(unsigned d) { return d < 0xFFFFFFFEu ? d : 0xFFFFFFFEu; }
__device__ __forceinline__ u32x4 nosent4(u32x4 v) { return (u32x4){nosent(v[0]), nosent(v[1]), nosent(v[2]), nosent(v[3])}; }
__device__ __forceinline__ unsigned umax3(unsigned a, unsigned b, unsigned c) { const unsigned m = a > b ? a : b; return m > c ? m : c; }
__device__ __forceinline__ unsigned umax4(unsigned m, u32x4 v) { return umax3(umax3(m, v[0], v[1]), v[2], v[3]); }
__device__ __forceinline__ u32x4 sent4() { unsigned sv; asm volatile("v_mov_b32 %0, -1" : "=v"(sv)); return (u32x4){sv, sv, sv, sv}; }
template <int N>
__device__ __forceinline__ void poll(rsrc_t rs, unsigned base, const unsigned (&off)[N], u32x4 (&v)[N], const Params& P) {
    for (int spins = 0;; ++spins) {
        unsigned m = 0;
#pragma unroll
        for (int k = 0; k < N; ++k) v[k] = LD128(rs, off[k], base);
#pragma unroll
        for (int k = 0; k < N; ++k) m = umax4(m, v[k]);
        if (__all(m != SENT)) return;
        asm volatile("" ::: "memory");
        if (spin_fail(spins, P)) return;
    }
}
__device__ __forceinline__ bf16x16 cat16(bf16x8 a, bf16x8 b) {
    return __builtin_shufflevector(a, b, 0, 1, 2, 3, 4, 5, 6, 7, 8, 9, 10, 11, 12, 13, 14, 15);
}
__device__ __forceinline__ bf16x8 half8(bf16x16 v, int h) {
    return h ? __builtin_shufflevector(v, v, 8, 9, 10, 11, 12, 13, 14, 15) : __builtin_shufflevector(v, v, 0, 1, 2, 3, 4, 5, 6, 7);
}
__device__ __forceinline__ bf16x16 lds_frag(unsigned a, unsigned step) { return cat16(lds_ld<bf16x8>(a), lds_ld<bf16x8>(a + step)); }
template <int I>
__device__ __forceinline__ void dmaq(const char* g, unsigned l16, unsigned dst) {
    asm volatile("s_mov_b32 m0, %2\n\ts_nop 0\n\tglobal_load_lds_dwordx4 %0, %1 offset:%3 sc1" :: "v"(l16), "s"(g), "s"(dst), "i"(I * 1024) : "memory");
}
__device__ __forceinline__ void dma4(const char* g, unsigned l16, unsigned dst) {
    asm volatile("s_mov_b32 m0, %2\n\ts_nop 0\n\tglobal_load_lds_dwordx4 %0, %1 sc1\n\tglobal_load_lds_dwordx4 %0, %1 offset:1024 sc1\n\t"
                 "global_load_lds_dwordx4 %0, %1 offset:2048 sc1\n\tglobal_load_lds_dwordx4 %0, %1 offset:3072 sc1" :: "v"(l16), "s"(g), "s"(dst) : "memory");
}
__device__ __forceinline__ unsigned toff(int row, int col, int ncols) {
    return (unsigned)(row >> 4) * (unsigned)(ncols * 32) + (unsigned)(col >> 3) * 256u + (unsigned)(row & 15) * 16u + (unsigned)(col & 7) * 2u;
}

#define ASM_ST
#define ASM_ST_DECL
#define ASM_ST_OUT
#define ASM_ST_SAVE(k)
#define ASM_ST2
#define ASM_ST2_OUT
#define ASM_ST2_SAVE(k)
#define STDECL
#define ST(k)
#define STFLUSH(t)

constexpr unsigned L_MEM = 0;
constexpr unsigned L_Q = 10240;
constexpr unsigned L_KM = 13312;
constexpr unsigned L_QX = 16384;
constexpr unsigned L_KX = 28672;
constexpr unsigned L_XB = 40960;
constexpr unsigned L_XA = 49152;
constexpr unsigned L_W = 81920;
constexpr int MEGA_LDS = 163840;
constexpr int wi(int i, int j) { return (i >> 1) * 5 + ((i & 1) ? 2 + j : j - 1); }

#define RPUB2
#define ASM_XG_LO_A \
    "s_waitcnt lgkmcnt(0)\n\t" \
    "ds_read_b128 v[224:227], %[xa] offset:0\n\t" \
    "ds_read_b128 v[244:247], %[wl] offset:0\n\t" \
    "ds_read_b128 v[228:231], %[xa] offset:16384\n\t" \
    "ds_read_b128 v[232:235], %[xa] offset:1024\n\t" \
    "ds_read_b128 v[236:239], %[xa] offset:17408\n\t" \
    "ds_read_b128 v[248:251], %[wl] offset:1024\n\t" \
    "s_waitcnt lgkmcnt(5)\n\t" \
    "v_mfma_f32_16x16x32_bf16 v[204:207], v[0:3], v[224:227], 0\n\t" \
    "v_xor_b32 v252, 0x800, %[xa]\n\t" \
    "ds_read_b128 v[240:243], v252 offset:0\n\t" \
    "v_mfma_f32_16x16x32_bf16 v[208:211], v[8:11], v[224:227], 0\n\t" \
    "s_waitcnt lgkmcnt(5)\n\t" \
    "v_mfma_f32_16x16x32_bf16 v[200:203], v[244:247], v[224:227], 0\n\t" \
    "s_waitcnt lgkmcnt(4)\n\t" \
    "v_mfma_f32_16x16x32_bf16 v[216:219], v[0:3], v[228:231], 0\n\t" \
    "ds_read_b128 v[224:227], v252 offset:16384\n\t" \
    "v_mfma_f32_16x16x32_bf16 v[220:223], v[8:11], v[228:231], 0\n\t" \
    "v_mfma_f32_16x16x32_bf16 v[212:215], v[244:247], v[228:231], 0\n\t" \
    "s_waitcnt lgkmcnt(4)\n\t" \
    "v_mfma_f32_16x16x32_bf16 v[204:207], v[4:7], v[232:235], v[204:207]\n\t" \
    "ds_read_b128 v[228:231], v252 offset:1024\n\t" \
    "v_mfma_f32_16x16x32_bf16 v[208:211], v[12:15], v[232:235], v[208:211]\n\t" \
    "ds_read_b128 v[244:247], %[wl] offset:2048\n\t" \
    "s_waitcnt lgkmcnt(4)\n\t" \
    "v_mfma_f32_16x16x32_bf16 v[200:203], v[248:251], v[232:235], v[200:203]\n\t" \
    "v_mfma_f32_16x16x32_bf16 v[216:219], v[4:7], v[236:239], v[216:219]\n\t" \
    "ds_read_b128 v[232:235], v252 offset:17408\n\t" \
    "v_mfma_f32_16x16x32_bf16 v[220:223], v[12:15], v[236:239], v[220:223]\n\t" \
    "v_mfma_f32_16x16x32_bf16 v[212:215], v[248:251], v[236:239], v[212:215]\n\t" \
    "s_waitcnt lgkmcnt(4)\n\t" \
    "v_mfma_f32_16x16x32_bf16 v[204:207], v[24:27], v[240:243], v[204:207]\n\t" \
    "v_xor_b32 v252, 0x1000, %[xa]\n\t" \
    "ds_read_b128 v[236:239], v252 offset:0\n\t" \
    "v_mfma_f32_16x16x32_bf16 v[208:211], v[32:35], v[240:243], v[208:211]\n\t" \
    "ds_read_b128 v[248:251], %[wl] offset:3072\n\t" \
    "v_mfma_f32_16x16x32_bf16 v[200:203], v[16:19], v[240:243], v[200:203]\n\t" \
    "s_waitcnt lgkmcnt(5)\n\t" \
    "v_mfma_f32_16x16x32_bf16 v[216:219], v[24:27], v[224:227], v[216:219]\n\t" \
    "ds_read_b128 v[240:243], v252 offset:16384\n\t" \
    "v_mfma_f32_16x16x32_bf16 v[220:223], v[32:35], v[224:227], v[220:223]\n\t" \
    "v_mfma_f32_16x16x32_bf16 v[212:215], v[16:19], v[224:227], v[212:215]\n\t" \
    "s_waitcnt lgkmcnt(5)\n\t" \
    "v_mfma_f32_16x16x32_bf16 v[204:207], v[28:31], v[228:231], v[204:207]\n\t" \
    "ds_read_b128 v[224:227], v252 offset:1024\n\t" \
    "v_mfma_f32_16x16x32_bf16 v[208:211], v[36:39], v[228:231], v[208:211]\n\t" \
    "v_mfma_f32_16x16x32_bf16 v[200:203], v[20:23], v[228:231], v[200:203]\n\t" \
    "s_waitcnt lgkmcnt(4)\n\t" \
    "v_mfma_f32_16x16x32_bf16 v[216:219], v[28:31], v[232:235], v[216:219]\n\t" \
    "ds_read_b128 v[228:231], v252 offset:17408\n\t" \
    "v_mfma_f32_16x16x32_bf16 v[220:223], v[36:39], v[232:235], v[220:223]\n\t" \
    "v_mfma_f32_16x16x32_bf16 v[212:215], v[20:23], v[232:235], v[212:215]\n\t" \
    "s_waitcnt lgkmcnt(4)\n\t" \
    "v_mfma_f32_16x16x32_bf16 v[204:207], v[40:43], v[236:239], v[204:207]\n\t" \
    "v_xor_b32 v252, 0x1800, %[xa]\n\t" \
    "ds_read_b128 v[232:235], v252 offset:0\n\t" \
    "v_mfma_f32_16x16x32_bf16 v[208:211], v[48:51], v[236:239], v[208:211]\n\t" \
    "v_mfma_f32_16x16x32_bf16 v[200:203], v[244:247], v[236:239], v[200:203]\n\t" \
    "s_waitcnt lgkmcnt(3)\n\t" \
    "v_mfma_f32_16x16x32_bf16 v[216:219], v[40:43], v[240:243], v[216:219]\n\t" \
    "ds_read_b128 v[236:239], v252 offset:16384\n\t" \
    "v_mfma_f32_16x16x32_bf16 v[220:223], v[48:51], v[240:243], v[220:223]\n\t" \
    "v_mfma_f32_16x16x32_bf16 v[212:215], v[244:247], v[240:243], v[212:215]\n\t" \
    "s_waitcnt lgkmcnt(3)\n\t" \
    "v_mfma_f32_16x16x32_bf16 v[204:207], v[44:47], v[224:227], v[204:207]\n\t" \
    "ds_read_b128 v[240:243], v252 offset:1024\n\t" \
    "v_mfma_f32_16x16x32_bf16 v[208:211], v[52:55], v[224:227], v[208:211]\n\t" \
    "ds_read_b128 v[244:247], %[wl] offset:8192\n\t" \
    "v_mfma_f32_16x16x32_bf16 v[200:203], v[248:251], v[224:227], v[200:203]\n\t" \
    "s_waitcnt lgkmcnt(4)\n\t" \
    "v_mfma_f32_16x16x32_bf16 v[216:219], v[44:47], v[228:231], v[216:219]\n\t" \
    "ds_read_b128 v[224:227], v252 offset:17408\n\t" \
    "v_mfma_f32_16x16x32_bf16 v[220:223], v[52:55], v[228:231], v[220:223]\n\t" \
    "v_mfma_f32_16x16x32_bf16 v[212:215], v[248:251], v[228:231], v[212:215]\n\t" \
    "s_waitcnt lgkmcnt(4)\n\t" \
    "v_mfma_f32_16x16x32_bf16 v[204:207], v[64:67], v[232:235], v[204:207]\n\t" \
    "v_and_b32 v252, 0x3ff, %[xa]\n\t" \
    "ds_read_b128 v[228:231], v252 offset:40960\n\t" \
    "v_mfma_f32_16x16x32_bf16 v[208:211], v[72:75], v[232:235], v[208:211]\n\t" \
    "ds_read_b128 v[248:251], %[wl] offset:9216\n\t" \
    "v_mfma_f32_16x16x32_bf16 v[200:203], v[56:59], v[232:235], v[200:203]\n\t" \
    "s_waitcnt lgkmcnt(5)\n\t" \
    "v_mfma_f32_16x16x32_bf16 v[216:219], v[64:67], v[236:239], v[216:219]\n\t" \
    "ds_read_b128 v[232:235], v252 offset:45056\n\t" \
    "v_mfma_f32_16x16x32_bf16 v[220:223], v[72:75], v[236:239], v[220:223]\n\t" \
    "v_mfma_f32_16x16x32_bf16 v[212:215], v[56:59], v[236:239], v[212:215]\n\t" \
    "s_waitcnt lgkmcnt(5)\n\t" \
    "v_mfma_f32_16x16x32_bf16 v[204:207], v[68:71], v[240:243], v[204:207]\n\t" \
    "ds_read_b128 v[236:239], v252 offset:41984\n\t" \
    "v_mfma_f32_16x16x32_bf16 v[208:211], v[76:79], v[240:243], v[208:211]\n\t" \
    "v_mfma_f32_16x16x32_bf16 v[200:203], v[60:63], v[240:243], v[200:203]\n\t" \
    "s_waitcnt lgkmcnt(4)\n\t" \
    "v_mfma_f32_16x16x32_bf16 v[216:219], v[68:71], v[224:227], v[216:219]\n\t" \
    "ds_read_b128 v[240:243], v252 offset:46080\n\t" \
    "v_mfma_f32_16x16x32_bf16 v[220:223], v[76:79], v[224:227], v[220:223]\n\t" \
    "v_mfma_f32_16x16x32_bf16 v[212:215], v[60:63], v[224:227], v[212:215]\n\t" \
    "s_waitcnt lgkmcnt(4)\n\t" \
    "v_mfma_f32_16x16x32_bf16 v[204:207], v[160:163], v[228:231], v[204:207]\n\t" \
    "v_mfma_f32_16x16x32_bf16 v[208:211], v[168:171], v[228:231], v[208:211]\n\t" \
    "v_mfma_f32_16x16x32_bf16 v[200:203], v[244:247], v[228:231], v[200:203]\n\t" \
    "s_waitcnt lgkmcnt(2)\n\t" \
    "v_mfma_f32_16x16x32_bf16 v[216:219], v[160:163], v[232:235], v[216:219]\n\t" \
    "v_mfma_f32_16x16x32_bf16 v[220:223], v[168:171], v[232:235], v[220:223]\n\t" \
    "v_mfma_f32_16x16x32_bf16 v[212:215], v[244:247], v[232:235], v[212:215]\n\t" \
    "s_waitcnt lgkmcnt(1)\n\t" \
    "v_mfma_f32_16x16x32_bf16 v[204:207], v[164:167], v[236:239], v[204:207]\n\t" \
    "v_mfma_f32_16x16x32_bf16 v[208:211], v[172:175], v[236:239], v[208:211]\n\t" \
    "v_mfma_f32_16x16x32_bf16 v[200:203], v[248:251], v[236:239], v[200:203]\n\t" \
    "s_waitcnt lgkmcnt(0)\n\t" \
    "v_mfma_f32_16x16x32_bf16 v[216:219], v[164:167], v[240:243], v[216:219]\n\t" \
    "v_mfma_f32_16x16x32_bf16 v[220:223], v[172:175], v[240:243], v[220:223]\n\t" \
    "v_mfma_f32_16x16x32_bf16 v[212:215], v[248:251], v[240:243], v[212:215]\n\t" \
    "s_nop 7\n\t"
#define ASM_XG_LO_B \
    "s_waitcnt lgkmcnt(0)\n\t" \
    "v_xor_b32 v252, 0x2000, %[xa]\n\t" \
    "ds_read_b128 v[224:227], v252 offset:0\n\t" \
    "ds_read_b128 v[244:247], %[wl] offset:4096\n\t" \
    "ds_read_b128 v[228:231], v252 offset:16384\n\t" \
    "ds_read_b128 v[232:235], v252 offset:1024\n\t" \
    "ds_read_b128 v[236:239], v252 offset:17408\n\t" \
    "ds_read_b128 v[248:251], %[wl] offset:5120\n\t" \
    "s_waitcnt lgkmcnt(5)\n\t" \
    "v_mfma_f32_16x16x32_bf16 v[204:207], v[80:83], v[224:227], v[204:207]\n\t" \
    "v_xor_b32 v252, 0x2800, %[xa]\n\t" \
    "ds_read_b128 v[240:243], v252 offset:0\n\t" \
    "v_mfma_f32_16x16x32_bf16 v[208:211], v[88:91], v[224:227], v[208:211]\n\t" \
    "s_waitcnt lgkmcnt(5)\n\t" \
    "v_mfma_f32_16x16x32_bf16 v[200:203], v[244:247], v[224:227], v[200:203]\n\t" \
    "s_waitcnt lgkmcnt(4)\n\t" \
    "v_mfma_f32_16x16x32_bf16 v[216:219], v[80:83], v[228:231], v[216:219]\n\t" \
    "ds_read_b128 v[224:227], v252 offset:16384\n\t" \
    "v_mfma_f32_16x16x32_bf16 v[220:223], v[88:91], v[228:231], v[220:223]\n\t" \
    "v_mfma_f32_16x16x32_bf16 v[212:215], v[244:247], v[228:231], v[212:215]\n\t" \
    "s_waitcnt lgkmcnt(4)\n\t" \
    "v_mfma_f32_16x16x32_bf16 v[204:207], v[84:87], v[232:235], v[204:207]\n\t" \
    "ds_read_b128 v[228:231], v252 offset:1024\n\t" \
    "v_mfma_f32_16x16x32_bf16 v[208:211], v[92:95], v[232:235], v[208:211]\n\t" \
    "ds_read_b128 v[244:247], %[wl] offset:6144\n\t" \
    "s_waitcnt lgkmcnt(4)\n\t" \
    "v_mfma_f32_16x16x32_bf16 v[200:203], v[248:251], v[232:235], v[200:203]\n\t" \
    "v_mfma_f32_16x16x32_bf16 v[216:219], v[84:87], v[236:239], v[216:219]\n\t" \
    "ds_read_b128 v[232:235], v252 offset:17408\n\t" \
    "v_mfma_f32_16x16x32_bf16 v[220:223], v[92:95], v[236:239], v[220:223]\n\t" \
    "v_mfma_f32_16x16x32_bf16 v[212:215], v[248:251], v[236:239], v[212:215]\n\t" \
    "s_waitcnt lgkmcnt(4)\n\t" \
    "v_mfma_f32_16x16x32_bf16 v[204:207], v[104:107], v[240:243], v[204:207]\n\t" \
    "v_xor_b32 v252, 0x3000, %[xa]\n\t" \
    "ds_read_b128 v[236:239], v252 offset:0\n\t" \
    "v_mfma_f32_16x16x32_bf16 v[208:211], v[112:115], v[240:243], v[208:211]\n\t" \
    "ds_read_b128 v[248:251], %[wl] offset:7168\n\t" \
    "v_mfma_f32_16x16x32_bf16 v[200:203], v[96:99], v[240:243], v[200:203]\n\t" \
    "s_waitcnt lgkmcnt(5)\n\t" \
    "v_mfma_f32_16x16x32_bf16 v[216:219], v[104:107], v[224:227], v[216:219]\n\t" \
    "ds_read_b128 v[240:243], v252 offset:16384\n\t" \
    "v_mfma_f32_16x16x32_bf16 v[220:223], v[112:115], v[224:227], v[220:223]\n\t" \
    "v_mfma_f32_16x16x32_bf16 v[212:215], v[96:99], v[224:227], v[212:215]\n\t" \
    "s_waitcnt lgkmcnt(5)\n\t" \
    "v_mfma_f32_16x16x32_bf16 v[204:207], v[108:111], v[228:231], v[204:207]\n\t" \
    "ds_read_b128 v[224:227], v252 offset:1024\n\t" \
    "v_mfma_f32_16x16x32_bf16 v[208:211], v[116:119], v[228:231], v[208:211]\n\t" \
    "v_mfma_f32_16x16x32_bf16 v[200:203], v[100:103], v[228:231], v[200:203]\n\t" \
    "s_waitcnt lgkmcnt(4)\n\t" \
    "v_mfma_f32_16x16x32_bf16 v[216:219], v[108:111], v[232:235], v[216:219]\n\t" \
    "ds_read_b128 v[228:231], v252 offset:17408\n\t" \
    "v_mfma_f32_16x16x32_bf16 v[220:223], v[116:119], v[232:235], v[220:223]\n\t" \
    "v_mfma_f32_16x16x32_bf16 v[212:215], v[100:103], v[232:235], v[212:215]\n\t" \
    "s_waitcnt lgkmcnt(4)\n\t" \
    "v_mfma_f32_16x16x32_bf16 v[204:207], v[120:123], v[236:239], v[204:207]\n\t" \
    "v_xor_b32 v252, 0x3800, %[xa]\n\t" \
    "ds_read_b128 v[232:235], v252 offset:0\n\t" \
    "v_mfma_f32_16x16x32_bf16 v[208:211], v[128:131], v[236:239], v[208:211]\n\t" \
    "v_mfma_f32_16x16x32_bf16 v[200:203], v[244:247], v[236:239], v[200:203]\n\t" \
    "s_waitcnt lgkmcnt(3)\n\t" \
    "v_mfma_f32_16x16x32_bf16 v[216:219], v[120:123], v[240:243], v[216:219]\n\t" \
    "ds_read_b128 v[236:239], v252 offset:16384\n\t" \
    "v_mfma_f32_16x16x32_bf16 v[220:223], v[128:131], v[240:243], v[220:223]\n\t" \
    "v_mfma_f32_16x16x32_bf16 v[212:215], v[244:247], v[240:243], v[212:215]\n\t" \
    "s_waitcnt lgkmcnt(3)\n\t" \
    "v_mfma_f32_16x16x32_bf16 v[204:207], v[124:127], v[224:227], v[204:207]\n\t" \
    "ds_read_b128 v[240:243], v252 offset:1024\n\t" \
    "v_mfma_f32_16x16x32_bf16 v[208:211], v[132:135], v[224:227], v[208:211]\n\t" \
    "v_mfma_f32_16x16x32_bf16 v[200:203], v[248:251], v[224:227], v[200:203]\n\t" \
    "s_waitcnt lgkmcnt(3)\n\t" \
    "v_mfma_f32_16x16x32_bf16 v[216:219], v[124:127], v[228:231], v[216:219]\n\t" \
    "ds_read_b128 v[224:227], v252 offset:17408\n\t" \
    "v_mfma_f32_16x16x32_bf16 v[220:223], v[132:135], v[228:231], v[220:223]\n\t" \
    "v_mfma_f32_16x16x32_bf16 v[212:215], v[248:251], v[228:231], v[212:215]\n\t" \
    "s_waitcnt lgkmcnt(3)\n\t" \
    "v_mfma_f32_16x16x32_bf16 v[204:207], v[144:147], v[232:235], v[204:207]\n\t" \
    "v_and_b32 v252, 0x3ff, %[xa]\n\t" \
    "ds_read_b128 v[228:231], v252 offset:43008\n\t" \
    "v_mov_b64 v[244:245], 0\n\t" \
    "v_mfma_f32_16x16x32_bf16 v[208:211], v[152:155], v[232:235], v[208:211]\n\t" \
    "v_mov_b64 v[246:247], 0\n\t" \
    "v_mfma_f32_16x16x32_bf16 v[200:203], v[136:139], v[232:235], v[200:203]\n\t" \
    "v_mov_b64 v[248:249], 0\n\t" \
    "s_waitcnt lgkmcnt(3)\n\t" \
    "v_mfma_f32_16x16x32_bf16 v[216:219], v[144:147], v[236:239], v[216:219]\n\t" \
    "ds_read_b128 v[232:235], v252 offset:44032\n\t" \
    "v_mov_b64 v[250:251], 0\n\t" \
    "v_mfma_f32_16x16x32_bf16 v[220:223], v[152:155], v[236:239], v[220:223]\n\t" \
    "v_mfma_f32_16x16x32_bf16 v[212:215], v[136:139], v[236:239], v[212:215]\n\t" \
    "s_waitcnt lgkmcnt(3)\n\t" \
    "v_mfma_f32_16x16x32_bf16 v[204:207], v[148:151], v[240:243], v[204:207]\n\t" \
    "ds_read_b128 v[236:239], v252 offset:47104\n\t" \
    "v_mfma_f32_16x16x32_bf16 v[208:211], v[156:159], v[240:243], v[208:211]\n\t" \
    "v_mfma_f32_16x16x32_bf16 v[200:203], v[140:143], v[240:243], v[200:203]\n\t" \
    "s_waitcnt lgkmcnt(3)\n\t" \
    "v_mfma_f32_16x16x32_bf16 v[216:219], v[148:151], v[224:227], v[216:219]\n\t" \
    "ds_read_b128 v[240:243], v252 offset:48128\n\t" \
    "v_mfma_f32_16x16x32_bf16 v[220:223], v[156:159], v[224:227], v[220:223]\n\t" \
    "v_mfma_f32_16x16x32_bf16 v[212:215], v[140:143], v[224:227], v[212:215]\n\t" \
    "s_waitcnt lgkmcnt(3)\n\t" \
    "v_mfma_f32_16x16x32_bf16 v[204:207], v[184:187], v[228:231], v[204:207]\n\t" \
    "v_add_u32 v252, %[qs], v252\n\t" \
    "v_mfma_f32_16x16x32_bf16 v[208:211], v[192:195], v[228:231], v[208:211]\n\t" \
    "v_mov_b64 v[224:225], 0\n\t" \
    "v_mfma_f32_16x16x32_bf16 v[200:203], v[176:179], v[228:231], v[200:203]\n\t" \
    "v_mov_b64 v[226:227], 0\n\t" \
    "s_waitcnt lgkmcnt(2)\n\t" \
    "v_mfma_f32_16x16x32_bf16 v[204:207], v[188:191], v[232:235], v[204:207]\n\t" \
    "v_mfma_f32_16x16x32_bf16 v[208:211], v[196:199], v[232:235], v[208:211]\n\t" \
    "v_mfma_f32_16x16x32_bf16 v[200:203], v[180:183], v[232:235], v[200:203]\n\t" \
    "s_waitcnt lgkmcnt(1)\n\t" \
    "v_mfma_f32_16x16x32_bf16 v[216:219], v[184:187], v[236:239], v[216:219]\n\t" \
    "ds_read_b128 v[228:231], %[wl] offset:2048\n\t" \
    "ds_read_b128 v[232:235], %[wl] offset:3072\n\t" \
    "v_mfma_f32_16x16x32_bf16 v[220:223], v[192:195], v[236:239], v[220:223]\n\t" \
    "v_cvt_pk_bf16_f32 v204, v204, v205\n\t" \
    "v_cvt_pk_bf16_f32 v205, v206, v207\n\t" \
    "v_mfma_f32_16x16x32_bf16 v[212:215], v[176:179], v[236:239], v[212:215]\n\t" \
    "ds_write_b64 v252, v[204:205] offset:1024\n\t" \
    "v_cvt_pk_bf16_f32 v208, v208, v209\n\t" \
    "s_waitcnt lgkmcnt(3)\n\t" \
    "v_mfma_f32_16x16x32_bf16 v[216:219], v[188:191], v[240:243], v[216:219]\n\t" \
    "v_cvt_pk_bf16_f32 v209, v210, v211\n\t" \
    "ds_write_b64 v252, v[208:209] offset:2048\n\t" \
    "v_mfma_f32_16x16x32_bf16 v[220:223], v[196:199], v[240:243], v[220:223]\n\t" \
    "v_cvt_pk_bf16_f32 v200, v200, v201\n\t" \
    "v_cvt_pk_bf16_f32 v201, v202, v203\n\t" \
    "v_mfma_f32_16x16x32_bf16 v[212:215], v[180:183], v[240:243], v[212:215]\n\t" \
    "ds_write_b64 v252, v[200:201] offset:0\n\t" \
    "ds_read_b128 v[200:203], %[wl] offset:0\n\t" \
    "ds_read_b128 v[204:207], %[wl] offset:1024\n\t" \
    "v_cvt_pk_bf16_f32 v216, v216, v217\n\t" \
    "v_cvt_pk_bf16_f32 v217, v218, v219\n\t" \
    "ds_write_b64 v252, v[216:217] offset:7168\n\t" \
    "v_cvt_pk_bf16_f32 v220, v220, v221\n\t" \
    "v_cvt_pk_bf16_f32 v221, v222, v223\n\t" \
    "ds_write_b64 v252, v[220:221] offset:8192\n\t" \
    "v_cvt_pk_bf16_f32 v212, v212, v213\n\t" \
    "v_cvt_pk_bf16_f32 v213, v214, v215\n\t" \
    "ds_write_b64 v252, v[212:213] offset:6144\n\t" \
    "s_waitcnt lgkmcnt(0)\n\t"
#define ASM_XG_HI_A \
    "s_waitcnt lgkmcnt(0)\n\t" \
    "v_xor_b32 v252, 0x2000, %[xa]\n\t" \
    "ds_read_b128 v[224:227], v252 offset:0\n\t" \
    "ds_read_b128 v[244:247], %[wl] offset:4096\n\t" \
    "ds_read_b128 v[228:231], v252 offset:16384\n\t" \
    "ds_read_b128 v[232:235], v252 offset:1024\n\t" \
    "ds_read_b128 v[236:239], v252 offset:17408\n\t" \
    "ds_read_b128 v[248:251], %[wl] offset:5120\n\t" \
    "s_waitcnt lgkmcnt(5)\n\t" \
    "v_mfma_f32_16x16x32_bf16 v[204:207], v[80:83], v[224:227], 0\n\t" \
    "v_xor_b32 v252, 0x2800, %[xa]\n\t" \
    "ds_read_b128 v[240:243], v252 offset:0\n\t" \
    "v_mfma_f32_16x16x32_bf16 v[208:211], v[88:91], v[224:227], 0\n\t" \
    "s_waitcnt lgkmcnt(5)\n\t" \
    "v_mfma_f32_16x16x32_bf16 v[200:203], v[244:247], v[224:227], 0\n\t" \
    "s_waitcnt lgkmcnt(4)\n\t" \
    "v_mfma_f32_16x16x32_bf16 v[216:219], v[80:83], v[228:231], 0\n\t" \
    "ds_read_b128 v[224:227], v252 offset:16384\n\t" \
    "v_mfma_f32_16x16x32_bf16 v[220:223], v[88:91], v[228:231], 0\n\t" \
    "v_mfma_f32_16x16x32_bf16 v[212:215], v[244:247], v[228:231], 0\n\t" \
    "s_waitcnt lgkmcnt(4)\n\t" \
    "v_mfma_f32_16x16x32_bf16 v[204:207], v[84:87], v[232:235], v[204:207]\n\t" \
    "ds_read_b128 v[228:231], v252 offset:1024\n\t" \
    "v_mfma_f32_16x16x32_bf16 v[208:211], v[92:95], v[232:235], v[208:211]\n\t" \
    "ds_read_b128 v[244:247], %[wl] offset:6144\n\t" \
    "s_waitcnt lgkmcnt(4)\n\t" \
    "v_mfma_f32_16x16x32_bf16 v[200:203], v[248:251], v[232:235], v[200:203]\n\t" \
    "v_mfma_f32_16x16x32_bf16 v[216:219], v[84:87], v[236:239], v[216:219]\n\t" \
    "ds_read_b128 v[232:235], v252 offset:17408\n\t" \
    "v_mfma_f32_16x16x32_bf16 v[220:223], v[92:95], v[236:239], v[220:223]\n\t" \
    "v_mfma_f32_16x16x32_bf16 v[212:215], v[248:251], v[236:239], v[212:215]\n\t" \
    "s_waitcnt lgkmcnt(4)\n\t" \
    "v_mfma_f32_16x16x32_bf16 v[204:207], v[104:107], v[240:243], v[204:207]\n\t" \
    "v_xor_b32 v252, 0x3000, %[xa]\n\t" \
    "ds_read_b128 v[236:239], v252 offset:0\n\t" \
    "v_mfma_f32_16x16x32_bf16 v[208:211], v[112:115], v[240:243], v[208:211]\n\t" \
    "ds_read_b128 v[248:251], %[wl] offset:7168\n\t" \
    "v_mfma_f32_16x16x32_bf16 v[200:203], v[96:99], v[240:243], v[200:203]\n\t" \
    "s_waitcnt lgkmcnt(5)\n\t" \
    "v_mfma_f32_16x16x32_bf16 v[216:219], v[104:107], v[224:227], v[216:219]\n\t" \
    "ds_read_b128 v[240:243], v252 offset:16384\n\t" \
    "v_mfma_f32_16x16x32_bf16 v[220:223], v[112:115], v[224:227], v[220:223]\n\t" \
    "v_mfma_f32_16x16x32_bf16 v[212:215], v[96:99], v[224:227], v[212:215]\n\t" \
    "s_waitcnt lgkmcnt(5)\n\t" \
    "v_mfma_f32_16x16x32_bf16 v[204:207], v[108:111], v[228:231], v[204:207]\n\t" \
    "ds_read_b128 v[224:227], v252 offset:1024\n\t" \
    "v_mfma_f32_16x16x32_bf16 v[208:211], v[116:119], v[228:231], v[208:211]\n\t" \
    "v_mfma_f32_16x16x32_bf16 v[200:203], v[100:103], v[228:231], v[200:203]\n\t" \
    "s_waitcnt lgkmcnt(4)\n\t" \
    "v_mfma_f32_16x16x32_bf16 v[216:219], v[108:111], v[232:235], v[216:219]\n\t" \
    "ds_read_b128 v[228:231], v252 offset:17408\n\t" \
    "v_mfma_f32_16x16x32_bf16 v[220:223], v[116:119], v[232:235], v[220:223]\n\t" \
    "v_mfma_f32_16x16x32_bf16 v[212:215], v[100:103], v[232:235], v[212:215]\n\t" \
    "s_waitcnt lgkmcnt(4)\n\t" \
    "v_mfma_f32_16x16x32_bf16 v[204:207], v[120:123], v[236:239], v[204:207]\n\t" \
    "v_xor_b32 v252, 0x3800, %[xa]\n\t" \
    "ds_read_b128 v[232:235], v252 offset:0\n\t" \
    "v_mfma_f32_16x16x32_bf16 v[208:211], v[128:131], v[236:239], v[208:211]\n\t" \
    "v_mfma_f32_16x16x32_bf16 v[200:203], v[244:247], v[236:239], v[200:203]\n\t" \
    "s_waitcnt lgkmcnt(3)\n\t" \
    "v_mfma_f32_16x16x32_bf16 v[216:219], v[120:123], v[240:243], v[216:219]\n\t" \
    "ds_read_b128 v[236:239], v252 offset:16384\n\t" \
    "v_mfma_f32_16x16x32_bf16 v[220:223], v[128:131], v[240:243], v[220:223]\n\t" \
    "v_mfma_f32_16x16x32_bf16 v[212:215], v[244:247], v[240:243], v[212:215]\n\t" \
    "s_waitcnt lgkmcnt(3)\n\t" \
    "v_mfma_f32_16x16x32_bf16 v[204:207], v[124:127], v[224:227], v[204:207]\n\t" \
    "ds_read_b128 v[240:243], v252 offset:1024\n\t" \
    "v_mfma_f32_16x16x32_bf16 v[208:211], v[132:135], v[224:227], v[208:211]\n\t" \
    "ds_read_b128 v[244:247], %[wl] offset:8192\n\t" \
    "v_mfma_f32_16x16x32_bf16 v[200:203], v[248:251], v[224:227], v[200:203]\n\t" \
    "s_waitcnt lgkmcnt(4)\n\t" \
    "v_mfma_f32_16x16x32_bf16 v[216:219], v[124:127], v[228:231], v[216:219]\n\t" \
    "ds_read_b128 v[224:227], v252 offset:17408\n\t" \
    "v_mfma_f32_16x16x32_bf16 v[220:223], v[132:135], v[228:231], v[220:223]\n\t" \
    "v_mfma_f32_16x16x32_bf16 v[212:215], v[248:251], v[228:231], v[212:215]\n\t" \
    "s_waitcnt lgkmcnt(4)\n\t" \
    "v_mfma_f32_16x16x32_bf16 v[204:207], v[144:147], v[232:235], v[204:207]\n\t" \
    "v_and_b32 v252, 0x3ff, %[xa]\n\t" \
    "ds_read_b128 v[228:231], v252 offset:40960\n\t" \
    "v_mfma_f32_16x16x32_bf16 v[208:211], v[152:155], v[232:235], v[208:211]\n\t" \
    "ds_read_b128 v[248:251], %[wl] offset:9216\n\t" \
    "v_mfma_f32_16x16x32_bf16 v[200:203], v[136:139], v[232:235], v[200:203]\n\t" \
    "s_waitcnt lgkmcnt(5)\n\t" \
    "v_mfma_f32_16x16x32_bf16 v[216:219], v[144:147], v[236:239], v[216:219]\n\t" \
    "ds_read_b128 v[232:235], v252 offset:45056\n\t" \
    "v_mfma_f32_16x16x32_bf16 v[220:223], v[152:155], v[236:239], v[220:223]\n\t" \
    "v_mfma_f32_16x16x32_bf16 v[212:215], v[136:139], v[236:239], v[212:215]\n\t" \
    "s_waitcnt lgkmcnt(5)\n\t" \
    "v_mfma_f32_16x16x32_bf16 v[204:207], v[148:151], v[240:243], v[204:207]\n\t" \
    "ds_read_b128 v[236:239], v252 offset:41984\n\t" \
    "v_mfma_f32_16x16x32_bf16 v[208:211], v[156:159], v[240:243], v[208:211]\n\t" \
    "v_mfma_f32_16x16x32_bf16 v[200:203], v[140:143], v[240:243], v[200:203]\n\t" \
    "s_waitcnt lgkmcnt(4)\n\t" \
    "v_mfma_f32_16x16x32_bf16 v[216:219], v[148:151], v[224:227], v[216:219]\n\t" \
    "ds_read_b128 v[240:243], v252 offset:46080\n\t" \
    "v_mfma_f32_16x16x32_bf16 v[220:223], v[156:159], v[224:227], v[220:223]\n\t" \
    "v_mfma_f32_16x16x32_bf16 v[212:215], v[140:143], v[224:227], v[212:215]\n\t" \
    "s_waitcnt lgkmcnt(4)\n\t" \
    "v_mfma_f32_16x16x32_bf16 v[204:207], v[160:163], v[228:231], v[204:207]\n\t" \
    "v_mfma_f32_16x16x32_bf16 v[208:211], v[168:171], v[228:231], v[208:211]\n\t" \
    "v_mfma_f32_16x16x32_bf16 v[200:203], v[244:247], v[228:231], v[200:203]\n\t" \
    "s_waitcnt lgkmcnt(2)\n\t" \
    "v_mfma_f32_16x16x32_bf16 v[216:219], v[160:163], v[232:235], v[216:219]\n\t" \
    "v_mfma_f32_16x16x32_bf16 v[220:223], v[168:171], v[232:235], v[220:223]\n\t" \
    "v_mfma_f32_16x16x32_bf16 v[212:215], v[244:247], v[232:235], v[212:215]\n\t" \
    "s_waitcnt lgkmcnt(1)\n\t" \
    "v_mfma_f32_16x16x32_bf16 v[204:207], v[164:167], v[236:239], v[204:207]\n\t" \
    "v_mfma_f32_16x16x32_bf16 v[208:211], v[172:175], v[236:239], v[208:211]\n\t" \
    "v_mfma_f32_16x16x32_bf16 v[200:203], v[248:251], v[236:239], v[200:203]\n\t" \
    "s_waitcnt lgkmcnt(0)\n\t" \
    "v_mfma_f32_16x16x32_bf16 v[216:219], v[164:167], v[240:243], v[216:219]\n\t" \
    "v_mfma_f32_16x16x32_bf16 v[220:223], v[172:175], v[240:243], v[220:223]\n\t" \
    "v_mfma_f32_16x16x32_bf16 v[212:215], v[248:251], v[240:243], v[212:215]\n\t" \
    "s_nop 7\n\t"
#define ASM_XG_HI_B \
    "s_waitcnt lgkmcnt(0)\n\t" \
    "ds_read_b128 v[224:227], %[xa] offset:0\n\t" \
    "ds_read_b128 v[244:247], %[wl] offset:0\n\t" \
    "ds_read_b128 v[228:231], %[xa] offset:16384\n\t" \
    "ds_read_b128 v[232:235], %[xa] offset:1024\n\t" \
    "ds_read_b128 v[236:239], %[xa] offset:17408\n\t" \
    "ds_read_b128 v[248:251], %[wl] offset:1024\n\t" \
    "s_waitcnt lgkmcnt(5)\n\t" \
    "v_mfma_f32_16x16x32_bf16 v[204:207], v[0:3], v[224:227], v[204:207]\n\t" \
    "v_xor_b32 v252, 0x800, %[xa]\n\t" \
    "ds_read_b128 v[240:243], v252 offset:0\n\t" \
    "v_mfma_f32_16x16x32_bf16 v[208:211], v[8:11], v[224:227], v[208:211]\n\t" \
    "s_waitcnt lgkmcnt(5)\n\t" \
    "v_mfma_f32_16x16x32_bf16 v[200:203], v[244:247], v[224:227], v[200:203]\n\t" \
    "s_waitcnt lgkmcnt(4)\n\t" \
    "v_mfma_f32_16x16x32_bf16 v[216:219], v[0:3], v[228:231], v[216:219]\n\t" \
    "ds_read_b128 v[224:227], v252 offset:16384\n\t" \
    "v_mfma_f32_16x16x32_bf16 v[220:223], v[8:11], v[228:231], v[220:223]\n\t" \
    "v_mfma_f32_16x16x32_bf16 v[212:215], v[244:247], v[228:231], v[212:215]\n\t" \
    "s_waitcnt lgkmcnt(4)\n\t" \
    "v_mfma_f32_16x16x32_bf16 v[204:207], v[4:7], v[232:235], v[204:207]\n\t" \
    "ds_read_b128 v[228:231], v252 offset:1024\n\t" \
    "v_mfma_f32_16x16x32_bf16 v[208:211], v[12:15], v[232:235], v[208:211]\n\t" \
    "ds_read_b128 v[244:247], %[wl] offset:2048\n\t" \
    "s_waitcnt lgkmcnt(4)\n\t" \
    "v_mfma_f32_16x16x32_bf16 v[200:203], v[248:251], v[232:235], v[200:203]\n\t" \
    "v_mfma_f32_16x16x32_bf16 v[216:219], v[4:7], v[236:239], v[216:219]\n\t" \
    "ds_read_b128 v[232:235], v252 offset:17408\n\t" \
    "v_mfma_f32_16x16x32_bf16 v[220:223], v[12:15], v[236:239], v[220:223]\n\t" \
    "v_mfma_f32_16x16x32_bf16 v[212:215], v[248:251], v[236:239], v[212:215]\n\t" \
    "s_waitcnt lgkmcnt(4)\n\t" \
    "v_mfma_f32_16x16x32_bf16 v[204:207], v[24:27], v[240:243], v[204:207]\n\t" \
    "v_xor_b32 v252, 0x1000, %[xa]\n\t" \
    "ds_read_b128 v[236:239], v252 offset:0\n\t" \
    "v_mfma_f32_16x16x32_bf16 v[208:211], v[32:35], v[240:243], v[208:211]\n\t" \
    "ds_read_b128 v[248:251], %[wl] offset:3072\n\t" \
    "v_mfma_f32_16x16x32_bf16 v[200:203], v[16:19], v[240:243], v[200:203]\n\t" \
    "s_waitcnt lgkmcnt(5)\n\t" \
    "v_mfma_f32_16x16x32_bf16 v[216:219], v[24:27], v[224:227], v[216:219]\n\t" \
    "ds_read_b128 v[240:243], v252 offset:16384\n\t" \
    "v_mfma_f32_16x16x32_bf16 v[220:223], v[32:35], v[224:227], v[220:223]\n\t" \
    "v_mfma_f32_16x16x32_bf16 v[212:215], v[16:19], v[224:227], v[212:215]\n\t" \
    "s_waitcnt lgkmcnt(5)\n\t" \
    "v_mfma_f32_16x16x32_bf16 v[204:207], v[28:31], v[228:231], v[204:207]\n\t" \
    "ds_read_b128 v[224:227], v252 offset:1024\n\t" \
    "v_mfma_f32_16x16x32_bf16 v[208:211], v[36:39], v[228:231], v[208:211]\n\t" \
    "v_mfma_f32_16x16x32_bf16 v[200:203], v[20:23], v[228:231], v[200:203]\n\t" \
    "s_waitcnt lgkmcnt(4)\n\t" \
    "v_mfma_f32_16x16x32_bf16 v[216:219], v[28:31], v[232:235], v[216:219]\n\t" \
    "ds_read_b128 v[228:231], v252 offset:17408\n\t" \
    "v_mfma_f32_16x16x32_bf16 v[220:223], v[36:39], v[232:235], v[220:223]\n\t" \
    "v_mfma_f32_16x16x32_bf16 v[212:215], v[20:23], v[232:235], v[212:215]\n\t" \
    "s_waitcnt lgkmcnt(4)\n\t" \
    "v_mfma_f32_16x16x32_bf16 v[204:207], v[40:43], v[236:239], v[204:207]\n\t" \
    "v_xor_b32 v252, 0x1800, %[xa]\n\t" \
    "ds_read_b128 v[232:235], v252 offset:0\n\t" \
    "v_mfma_f32_16x16x32_bf16 v[208:211], v[48:51], v[236:239], v[208:211]\n\t" \
    "v_mfma_f32_16x16x32_bf16 v[200:203], v[244:247], v[236:239], v[200:203]\n\t" \
    "s_waitcnt lgkmcnt(3)\n\t" \
    "v_mfma_f32_16x16x32_bf16 v[216:219], v[40:43], v[240:243], v[216:219]\n\t" \
    "ds_read_b128 v[236:239], v252 offset:16384\n\t" \
    "v_mfma_f32_16x16x32_bf16 v[220:223], v[48:51], v[240:243], v[220:223]\n\t" \
    "v_mfma_f32_16x16x32_bf16 v[212:215], v[244:247], v[240:243], v[212:215]\n\t" \
    "s_waitcnt lgkmcnt(3)\n\t" \
    "v_mfma_f32_16x16x32_bf16 v[204:207], v[44:47], v[224:227], v[204:207]\n\t" \
    "ds_read_b128 v[240:243], v252 offset:1024\n\t" \
    "v_mfma_f32_16x16x32_bf16 v[208:211], v[52:55], v[224:227], v[208:211]\n\t" \
    "v_mfma_f32_16x16x32_bf16 v[200:203], v[248:251], v[224:227], v[200:203]\n\t" \
    "s_waitcnt lgkmcnt(3)\n\t" \
    "v_mfma_f32_16x16x32_bf16 v[216:219], v[44:47], v[228:231], v[216:219]\n\t" \
    "ds_read_b128 v[224:227], v252 offset:17408\n\t" \
    "v_mfma_f32_16x16x32_bf16 v[220:223], v[52:55], v[228:231], v[220:223]\n\t" \
    "v_mfma_f32_16x16x32_bf16 v[212:215], v[248:251], v[228:231], v[212:215]\n\t" \
    "s_waitcnt lgkmcnt(3)\n\t" \
    "v_mfma_f32_16x16x32_bf16 v[204:207], v[64:67], v[232:235], v[204:207]\n\t" \
    "v_and_b32 v252, 0x3ff, %[xa]\n\t" \
    "ds_read_b128 v[228:231], v252 offset:43008\n\t" \
    "v_mov_b64 v[244:245], 0\n\t" \
    "v_mfma_f32_16x16x32_bf16 v[208:211], v[72:75], v[232:235], v[208:211]\n\t" \
    "v_mov_b64 v[246:247], 0\n\t" \
    "v_mfma_f32_16x16x32_bf16 v[200:203], v[56:59], v[232:235], v[200:203]\n\t" \
    "v_mov_b64 v[248:249], 0\n\t" \
    "s_waitcnt lgkmcnt(3)\n\t" \
    "v_mfma_f32_16x16x32_bf16 v[216:219], v[64:67], v[236:239], v[216:219]\n\t" \
    "ds_read_b128 v[232:235], v252 offset:44032\n\t" \
    "v_mov_b64 v[250:251], 0\n\t" \
    "v_mfma_f32_16x16x32_bf16 v[220:223], v[72:75], v[236:239], v[220:223]\n\t" \
    "v_mfma_f32_16x16x32_bf16 v[212:215], v[56:59], v[236:239], v[212:215]\n\t" \
    "s_waitcnt lgkmcnt(3)\n\t" \
    "v_mfma_f32_16x16x32_bf16 v[204:207], v[68:71], v[240:243], v[204:207]\n\t" \
    "ds_read_b128 v[236:239], v252 offset:47104\n\t" \
    "v_mfma_f32_16x16x32_bf16 v[208:211], v[76:79], v[240:243], v[208:211]\n\t" \
    "v_mfma_f32_16x16x32_bf16 v[200:203], v[60:63], v[240:243], v[200:203]\n\t" \
    "s_waitcnt lgkmcnt(3)\n\t" \
    "v_mfma_f32_16x16x32_bf16 v[216:219], v[68:71], v[224:227], v[216:219]\n\t" \
    "ds_read_b128 v[240:243], v252 offset:48128\n\t" \
    "v_mfma_f32_16x16x32_bf16 v[220:223], v[76:79], v[224:227], v[220:223]\n\t" \
    "v_mfma_f32_16x16x32_bf16 v[212:215], v[60:63], v[224:227], v[212:215]\n\t" \
    "s_waitcnt lgkmcnt(3)\n\t" \
    "v_mfma_f32_16x16x32_bf16 v[204:207], v[184:187], v[228:231], v[204:207]\n\t" \
    "v_add_u32 v252, %[qs], v252\n\t" \
    "v_mfma_f32_16x16x32_bf16 v[208:211], v[192:195], v[228:231], v[208:211]\n\t" \
    "v_mov_b64 v[224:225], 0\n\t" \
    "v_mfma_f32_16x16x32_bf16 v[200:203], v[176:179], v[228:231], v[200:203]\n\t" \
    "v_mov_b64 v[226:227], 0\n\t" \
    "s_waitcnt lgkmcnt(2)\n\t" \
    "v_mfma_f32_16x16x32_bf16 v[204:207], v[188:191], v[232:235], v[204:207]\n\t" \
    "v_mfma_f32_16x16x32_bf16 v[208:211], v[196:199], v[232:235], v[208:211]\n\t" \
    "v_mfma_f32_16x16x32_bf16 v[200:203], v[180:183], v[232:235], v[200:203]\n\t" \
    "s_waitcnt lgkmcnt(1)\n\t" \
    "v_mfma_f32_16x16x32_bf16 v[216:219], v[184:187], v[236:239], v[216:219]\n\t" \
    "ds_read_b128 v[228:231], %[wl] offset:2048\n\t" \
    "ds_read_b128 v[232:235], %[wl] offset:3072\n\t" \
    "v_mfma_f32_16x16x32_bf16 v[220:223], v[192:195], v[236:239], v[220:223]\n\t" \
    "v_cvt_pk_bf16_f32 v204, v204, v205\n\t" \
    "v_cvt_pk_bf16_f32 v205, v206, v207\n\t" \
    "v_mfma_f32_16x16x32_bf16 v[212:215], v[176:179], v[236:239], v[212:215]\n\t" \
    "ds_write_b64 v252, v[204:205] offset:1024\n\t" \
    "v_cvt_pk_bf16_f32 v208, v208, v209\n\t" \
    "s_waitcnt lgkmcnt(3)\n\t" \
    "v_mfma_f32_16x16x32_bf16 v[216:219], v[188:191], v[240:243], v[216:219]\n\t" \
    "v_cvt_pk_bf16_f32 v209, v210, v211\n\t" \
    "ds_write_b64 v252, v[208:209] offset:2048\n\t" \
    "v_mfma_f32_16x16x32_bf16 v[220:223], v[196:199], v[240:243], v[220:223]\n\t" \
    "v_cvt_pk_bf16_f32 v200, v200, v201\n\t" \
    "v_cvt_pk_bf16_f32 v201, v202, v203\n\t" \
    "v_mfma_f32_16x16x32_bf16 v[212:215], v[180:183], v[240:243], v[212:215]\n\t" \
    "ds_write_b64 v252, v[200:201] offset:0\n\t" \
    "ds_read_b128 v[200:203], %[wl] offset:0\n\t" \
    "ds_read_b128 v[204:207], %[wl] offset:1024\n\t" \
    "v_cvt_pk_bf16_f32 v216, v216, v217\n\t" \
    "v_cvt_pk_bf16_f32 v217, v218, v219\n\t" \
    "ds_write_b64 v252, v[216:217] offset:7168\n\t" \
    "v_cvt_pk_bf16_f32 v220, v220, v221\n\t" \
    "v_cvt_pk_bf16_f32 v221, v222, v223\n\t" \
    "ds_write_b64 v252, v[220:221] offset:8192\n\t" \
    "v_cvt_pk_bf16_f32 v212, v212, v213\n\t" \
    "v_cvt_pk_bf16_f32 v213, v214, v215\n\t" \
    "ds_write_b64 v252, v[212:213] offset:6144\n\t" \
    "s_waitcnt lgkmcnt(0)\n\t"
#define ASM_XG_LO_AB \
    "s_waitcnt lgkmcnt(0)\n\t" \
    "ds_read_b128 v[244:247], %[wl] offset:0\n\t" \
    "ds_read_b128 v[248:251], %[wl] offset:1024\n\t" \
    "s_barrier \n\t" \
    "ds_read_b128 v[224:227], %[xa] offset:0\n\t" \
    "ds_read_b128 v[228:231], %[xa] offset:16384\n\t" \
    "ds_read_b128 v[232:235], %[xa] offset:1024\n\t" \
    "ds_read_b128 v[236:239], %[xa] offset:17408\n\t" \
    "s_waitcnt lgkmcnt(3)\n\t" \
    "v_mfma_f32_16x16x32_bf16 v[204:207], v[0:3], v[224:227], 0\n\t" \
    "v_xor_b32 v252, 0x800, %[xa]\n\t" \
    "ds_read_b128 v[240:243], v252 offset:0\n\t" \
    "v_mfma_f32_16x16x32_bf16 v[208:211], v[8:11], v[224:227], 0\n\t" \
    "v_mfma_f32_16x16x32_bf16 v[200:203], v[244:247], v[224:227], 0\n\t" \
    "s_waitcnt lgkmcnt(3)\n\t" \
    "v_mfma_f32_16x16x32_bf16 v[216:219], v[0:3], v[228:231], 0\n\t" \
    "ds_read_b128 v[224:227], v252 offset:16384\n\t" \
    "v_mfma_f32_16x16x32_bf16 v[220:223], v[8:11], v[228:231], 0\n\t" \
    "v_mfma_f32_16x16x32_bf16 v[212:215], v[244:247], v[228:231], 0\n\t" \
    "s_waitcnt lgkmcnt(3)\n\t" \
    "v_mfma_f32_16x16x32_bf16 v[204:207], v[4:7], v[232:235], v[204:207]\n\t" \
    "ds_read_b128 v[228:231], v252 offset:1024\n\t" \
    "v_mfma_f32_16x16x32_bf16 v[208:211], v[12:15], v[232:235], v[208:211]\n\t" \
    "ds_read_b128 v[244:247], %[wl] offset:2048\n\t" \
    "v_mfma_f32_16x16x32_bf16 v[200:203], v[248:251], v[232:235], v[200:203]\n\t" \
    "s_waitcnt lgkmcnt(4)\n\t" \
    "v_mfma_f32_16x16x32_bf16 v[216:219], v[4:7], v[236:239], v[216:219]\n\t" \
    "ds_read_b128 v[232:235], v252 offset:17408\n\t" \
    "v_mfma_f32_16x16x32_bf16 v[220:223], v[12:15], v[236:239], v[220:223]\n\t" \
    "v_mfma_f32_16x16x32_bf16 v[212:215], v[248:251], v[236:239], v[212:215]\n\t" \
    "s_waitcnt lgkmcnt(4)\n\t" \
    "v_mfma_f32_16x16x32_bf16 v[204:207], v[24:27], v[240:243], v[204:207]\n\t" \
    "v_xor_b32 v252, 0x1000, %[xa]\n\t" \
    "ds_read_b128 v[236:239], v252 offset:0\n\t" \
    "v_mfma_f32_16x16x32_bf16 v[208:211], v[32:35], v[240:243], v[208:211]\n\t" \
    "ds_read_b128 v[248:251], %[wl] offset:3072\n\t" \
    "v_mfma_f32_16x16x32_bf16 v[200:203], v[16:19], v[240:243], v[200:203]\n\t" \
    "s_waitcnt lgkmcnt(5)\n\t" \
    "v_mfma_f32_16x16x32_bf16 v[216:219], v[24:27], v[224:227], v[216:219]\n\t" \
    "ds_read_b128 v[240:243], v252 offset:16384\n\t" \
    "v_mfma_f32_16x16x32_bf16 v[220:223], v[32:35], v[224:227], v[220:223]\n\t" \
    "v_mfma_f32_16x16x32_bf16 v[212:215], v[16:19], v[224:227], v[212:215]\n\t" \
    "s_waitcnt lgkmcnt(5)\n\t" \
    "v_mfma_f32_16x16x32_bf16 v[204:207], v[28:31], v[228:231], v[204:207]\n\t" \
    "ds_read_b128 v[224:227], v252 offset:1024\n\t" \
    "v_mfma_f32_16x16x32_bf16 v[208:211], v[36:39], v[228:231], v[208:211]\n\t" \
    "v_mfma_f32_16x16x32_bf16 v[200:203], v[20:23], v[228:231], v[200:203]\n\t" \
    "s_waitcnt lgkmcnt(4)\n\t" \
    "v_mfma_f32_16x16x32_bf16 v[216:219], v[28:31], v[232:235], v[216:219]\n\t" \
    "ds_read_b128 v[228:231], v252 offset:17408\n\t" \
    "v_mfma_f32_16x16x32_bf16 v[220:223], v[36:39], v[232:235], v[220:223]\n\t" \
    "v_mfma_f32_16x16x32_bf16 v[212:215], v[20:23], v[232:235], v[212:215]\n\t" \
    "s_waitcnt lgkmcnt(4)\n\t" \
    "v_mfma_f32_16x16x32_bf16 v[204:207], v[40:43], v[236:239], v[204:207]\n\t" \
    "v_xor_b32 v252, 0x1800, %[xa]\n\t" \
    "ds_read_b128 v[232:235], v252 offset:0\n\t" \
    "v_mfma_f32_16x16x32_bf16 v[208:211], v[48:51], v[236:239], v[208:211]\n\t" \
    "v_mfma_f32_16x16x32_bf16 v[200:203], v[244:247], v[236:239], v[200:203]\n\t" \
    "s_waitcnt lgkmcnt(3)\n\t" \
    "v_mfma_f32_16x16x32_bf16 v[216:219], v[40:43], v[240:243], v[216:219]\n\t" \
    "ds_read_b128 v[236:239], v252 offset:16384\n\t" \
    "v_mfma_f32_16x16x32_bf16 v[220:223], v[48:51], v[240:243], v[220:223]\n\t" \
    "v_mfma_f32_16x16x32_bf16 v[212:215], v[244:247], v[240:243], v[212:215]\n\t" \
    "s_waitcnt lgkmcnt(3)\n\t" \
    "v_mfma_f32_16x16x32_bf16 v[204:207], v[44:47], v[224:227], v[204:207]\n\t" \
    "ds_read_b128 v[240:243], v252 offset:1024\n\t" \
    "v_mfma_f32_16x16x32_bf16 v[208:211], v[52:55], v[224:227], v[208:211]\n\t" \
    "ds_read_b128 v[244:247], %[wl] offset:8192\n\t" \
    "v_mfma_f32_16x16x32_bf16 v[200:203], v[248:251], v[224:227], v[200:203]\n\t" \
    "s_waitcnt lgkmcnt(4)\n\t" \
    "v_mfma_f32_16x16x32_bf16 v[216:219], v[44:47], v[228:231], v[216:219]\n\t" \
    "ds_read_b128 v[224:227], v252 offset:17408\n\t" \
    "v_mfma_f32_16x16x32_bf16 v[220:223], v[52:55], v[228:231], v[220:223]\n\t" \
    "v_mfma_f32_16x16x32_bf16 v[212:215], v[248:251], v[228:231], v[212:215]\n\t" \
    "s_waitcnt lgkmcnt(4)\n\t" \
    "v_mfma_f32_16x16x32_bf16 v[204:207], v[64:67], v[232:235], v[204:207]\n\t" \
    "v_and_b32 v252, 0x3ff, %[xa]\n\t" \
    "ds_read_b128 v[228:231], v252 offset:40960\n\t" \
    "v_mfma_f32_16x16x32_bf16 v[208:211], v[72:75], v[232:235], v[208:211]\n\t" \
    "ds_read_b128 v[248:251], %[wl] offset:9216\n\t" \
    "v_mfma_f32_16x16x32_bf16 v[200:203], v[56:59], v[232:235], v[200:203]\n\t" \
    "s_waitcnt lgkmcnt(5)\n\t" \
    "v_mfma_f32_16x16x32_bf16 v[216:219], v[64:67], v[236:239], v[216:219]\n\t" \
    "ds_read_b128 v[232:235], v252 offset:45056\n\t" \
    "v_mfma_f32_16x16x32_bf16 v[220:223], v[72:75], v[236:239], v[220:223]\n\t" \
    "v_mfma_f32_16x16x32_bf16 v[212:215], v[56:59], v[236:239], v[212:215]\n\t" \
    "s_waitcnt lgkmcnt(5)\n\t" \
    "v_mfma_f32_16x16x32_bf16 v[204:207], v[68:71], v[240:243], v[204:207]\n\t" \
    "ds_read_b128 v[236:239], v252 offset:41984\n\t" \
    "v_mfma_f32_16x16x32_bf16 v[208:211], v[76:79], v[240:243], v[208:211]\n\t" \
    "v_mfma_f32_16x16x32_bf16 v[200:203], v[60:63], v[240:243], v[200:203]\n\t" \
    "s_waitcnt lgkmcnt(4)\n\t" \
    "v_mfma_f32_16x16x32_bf16 v[216:219], v[68:71], v[224:227], v[216:219]\n\t" \
    "ds_read_b128 v[240:243], v252 offset:46080\n\t" \
    "v_mfma_f32_16x16x32_bf16 v[220:223], v[76:79], v[224:227], v[220:223]\n\t" \
    "v_mfma_f32_16x16x32_bf16 v[212:215], v[60:63], v[224:227], v[212:215]\n\t" \
    "s_waitcnt lgkmcnt(4)\n\t" \
    "v_mfma_f32_16x16x32_bf16 v[204:207], v[160:163], v[228:231], v[204:207]\n\t" \
    "v_xor_b32 v252, 0x2000, %[xa]\n\t" \
    "ds_read_b128 v[224:227], v252 offset:0\n\t" \
    "v_mfma_f32_16x16x32_bf16 v[208:211], v[168:171], v[228:231], v[208:211]\n\t" \
    "v_mfma_f32_16x16x32_bf16 v[200:203], v[244:247], v[228:231], v[200:203]\n\t" \
    "s_waitcnt lgkmcnt(3)\n\t" \
    "v_mfma_f32_16x16x32_bf16 v[216:219], v[160:163], v[232:235], v[216:219]\n\t" \
    "ds_read_b128 v[228:231], v252 offset:16384\n\t" \
    "v_mfma_f32_16x16x32_bf16 v[220:223], v[168:171], v[232:235], v[220:223]\n\t" \
    "v_mfma_f32_16x16x32_bf16 v[212:215], v[244:247], v[232:235], v[212:215]\n\t" \
    "s_waitcnt lgkmcnt(3)\n\t" \
    "v_mfma_f32_16x16x32_bf16 v[204:207], v[164:167], v[236:239], v[204:207]\n\t" \
    "ds_read_b128 v[232:235], v252 offset:1024\n\t" \
    "v_mfma_f32_16x16x32_bf16 v[208:211], v[172:175], v[236:239], v[208:211]\n\t" \
    "ds_read_b128 v[244:247], %[wl] offset:4096\n\t" \
    "v_mfma_f32_16x16x32_bf16 v[200:203], v[248:251], v[236:239], v[200:203]\n\t" \
    "s_waitcnt lgkmcnt(4)\n\t" \
    "v_mfma_f32_16x16x32_bf16 v[216:219], v[164:167], v[240:243], v[216:219]\n\t" \
    "ds_read_b128 v[236:239], v252 offset:17408\n\t" \
    "v_mfma_f32_16x16x32_bf16 v[220:223], v[172:175], v[240:243], v[220:223]\n\t" \
    "v_mfma_f32_16x16x32_bf16 v[212:215], v[248:251], v[240:243], v[212:215]\n\t" \
    "s_waitcnt lgkmcnt(4)\n\t" \
    "v_mfma_f32_16x16x32_bf16 v[204:207], v[80:83], v[224:227], v[204:207]\n\t" \
    "v_xor_b32 v252, 0x2800, %[xa]\n\t" \
    "ds_read_b128 v[240:243], v252 offset:0\n\t" \
    "v_mfma_f32_16x16x32_bf16 v[208:211], v[88:91], v[224:227], v[208:211]\n\t" \
    "ds_read_b128 v[248:251], %[wl] offset:5120\n\t" \
    "s_waitcnt lgkmcnt(3)\n\t" \
    "v_mfma_f32_16x16x32_bf16 v[200:203], v[244:247], v[224:227], v[200:203]\n\t" \
    "v_mfma_f32_16x16x32_bf16 v[216:219], v[80:83], v[228:231], v[216:219]\n\t" \
    "ds_read_b128 v[224:227], v252 offset:16384\n\t" \
    "v_mfma_f32_16x16x32_bf16 v[220:223], v[88:91], v[228:231], v[220:223]\n\t" \
    "v_mfma_f32_16x16x32_bf16 v[212:215], v[244:247], v[228:231], v[212:215]\n\t" \
    "v_mfma_f32_16x16x32_bf16 v[204:207], v[84:87], v[232:235], v[204:207]\n\t" \
    "ds_read_b128 v[228:231], v252 offset:1024\n\t" \
    "v_mfma_f32_16x16x32_bf16 v[208:211], v[92:95], v[232:235], v[208:211]\n\t" \
    "ds_read_b128 v[244:247], %[wl] offset:6144\n\t" \
    "s_waitcnt lgkmcnt(3)\n\t" \
    "v_mfma_f32_16x16x32_bf16 v[200:203], v[248:251], v[232:235], v[200:203]\n\t" \
    "v_mfma_f32_16x16x32_bf16 v[216:219], v[84:87], v[236:239], v[216:219]\n\t" \
    "ds_read_b128 v[232:235], v252 offset:17408\n\t" \
    "v_mfma_f32_16x16x32_bf16 v[220:223], v[92:95], v[236:239], v[220:223]\n\t" \
    "v_mfma_f32_16x16x32_bf16 v[212:215], v[248:251], v[236:239], v[212:215]\n\t" \
    "v_mfma_f32_16x16x32_bf16 v[204:207], v[104:107], v[240:243], v[204:207]\n\t" \
    "v_xor_b32 v252, 0x3000, %[xa]\n\t" \
    "ds_read_b128 v[236:239], v252 offset:0\n\t" \
    "v_mfma_f32_16x16x32_bf16 v[208:211], v[112:115], v[240:243], v[208:211]\n\t" \
    "ds_read_b128 v[248:251], %[wl] offset:7168\n\t" \
    "v_mfma_f32_16x16x32_bf16 v[200:203], v[96:99], v[240:243], v[200:203]\n\t" \
    "s_waitcnt lgkmcnt(5)\n\t" \
    "v_mfma_f32_16x16x32_bf16 v[216:219], v[104:107], v[224:227], v[216:219]\n\t" \
    "ds_read_b128 v[240:243], v252 offset:16384\n\t" \
    "v_mfma_f32_16x16x32_bf16 v[220:223], v[112:115], v[224:227], v[220:223]\n\t" \
    "v_mfma_f32_16x16x32_bf16 v[212:215], v[96:99], v[224:227], v[212:215]\n\t" \
    "s_waitcnt lgkmcnt(5)\n\t" \
    "v_mfma_f32_16x16x32_bf16 v[204:207], v[108:111], v[228:231], v[204:207]\n\t" \
    "ds_read_b128 v[224:227], v252 offset:1024\n\t" \
    "v_mfma_f32_16x16x32_bf16 v[208:211], v[116:119], v[228:231], v[208:211]\n\t" \
    "v_mfma_f32_16x16x32_bf16 v[200:203], v[100:103], v[228:231], v[200:203]\n\t" \
    "s_waitcnt lgkmcnt(4)\n\t" \
    "v_mfma_f32_16x16x32_bf16 v[216:219], v[108:111], v[232:235], v[216:219]\n\t" \
    "ds_read_b128 v[228:231], v252 offset:17408\n\t" \
    "v_mfma_f32_16x16x32_bf16 v[220:223], v[116:119], v[232:235], v[220:223]\n\t" \
    "v_mfma_f32_16x16x32_bf16 v[212:215], v[100:103], v[232:235], v[212:215]\n\t" \
    "s_waitcnt lgkmcnt(4)\n\t" \
    "v_mfma_f32_16x16x32_bf16 v[204:207], v[120:123], v[236:239], v[204:207]\n\t" \
    "v_xor_b32 v252, 0x3800, %[xa]\n\t" \
    "ds_read_b128 v[232:235], v252 offset:0\n\t" \
    "v_mfma_f32_16x16x32_bf16 v[208:211], v[128:131], v[236:239], v[208:211]\n\t" \
    "v_mfma_f32_16x16x32_bf16 v[200:203], v[244:247], v[236:239], v[200:203]\n\t" \
    "s_waitcnt lgkmcnt(3)\n\t" \
    "v_mfma_f32_16x16x32_bf16 v[216:219], v[120:123], v[240:243], v[216:219]\n\t" \
    "ds_read_b128 v[236:239], v252 offset:16384\n\t" \
    "v_mfma_f32_16x16x32_bf16 v[220:223], v[128:131], v[240:243], v[220:223]\n\t" \
    "v_mfma_f32_16x16x32_bf16 v[212:215], v[244:247], v[240:243], v[212:215]\n\t" \
    "s_waitcnt lgkmcnt(3)\n\t" \
    "v_mfma_f32_16x16x32_bf16 v[204:207], v[124:127], v[224:227], v[204:207]\n\t" \
    "ds_read_b128 v[240:243], v252 offset:1024\n\t" \
    "v_mfma_f32_16x16x32_bf16 v[208:211], v[132:135], v[224:227], v[208:211]\n\t" \
    "v_mfma_f32_16x16x32_bf16 v[200:203], v[248:251], v[224:227], v[200:203]\n\t" \
    "s_waitcnt lgkmcnt(3)\n\t" \
    "v_mfma_f32_16x16x32_bf16 v[216:219], v[124:127], v[228:231], v[216:219]\n\t" \
    "ds_read_b128 v[224:227], v252 offset:17408\n\t" \
    "v_mfma_f32_16x16x32_bf16 v[220:223], v[132:135], v[228:231], v[220:223]\n\t" \
    "v_mfma_f32_16x16x32_bf16 v[212:215], v[248:251], v[228:231], v[212:215]\n\t" \
    "s_waitcnt lgkmcnt(3)\n\t" \
    "v_mfma_f32_16x16x32_bf16 v[204:207], v[144:147], v[232:235], v[204:207]\n\t" \
    "v_and_b32 v252, 0x3ff, %[xa]\n\t" \
    "ds_read_b128 v[228:231], v252 offset:43008\n\t" \
    "v_mov_b64 v[244:245], 0\n\t" \
    "v_mfma_f32_16x16x32_bf16 v[208:211], v[152:155], v[232:235], v[208:211]\n\t" \
    "v_mov_b64 v[246:247], 0\n\t" \
    "v_mfma_f32_16x16x32_bf16 v[200:203], v[136:139], v[232:235], v[200:203]\n\t" \
    "v_mov_b64 v[248:249], 0\n\t" \
    "s_waitcnt lgkmcnt(3)\n\t" \
    "v_mfma_f32_16x16x32_bf16 v[216:219], v[144:147], v[236:239], v[216:219]\n\t" \
    "ds_read_b128 v[232:235], v252 offset:44032\n\t" \
    "v_mov_b64 v[250:251], 0\n\t" \
    "v_mfma_f32_16x16x32_bf16 v[220:223], v[152:155], v[236:239], v[220:223]\n\t" \
    "v_mfma_f32_16x16x32_bf16 v[212:215], v[136:139], v[236:239], v[212:215]\n\t" \
    "s_waitcnt lgkmcnt(3)\n\t" \
    "v_mfma_f32_16x16x32_bf16 v[204:207], v[148:151], v[240:243], v[204:207]\n\t" \
    "ds_read_b128 v[236:239], v252 offset:47104\n\t" \
    "v_mfma_f32_16x16x32_bf16 v[208:211], v[156:159], v[240:243], v[208:211]\n\t" \
    "v_mfma_f32_16x16x32_bf16 v[200:203], v[140:143], v[240:243], v[200:203]\n\t" \
    "s_waitcnt lgkmcnt(3)\n\t" \
    "v_mfma_f32_16x16x32_bf16 v[216:219], v[148:151], v[224:227], v[216:219]\n\t" \
    "ds_read_b128 v[240:243], v252 offset:48128\n\t" \
    "v_mfma_f32_16x16x32_bf16 v[220:223], v[156:159], v[224:227], v[220:223]\n\t" \
    "v_mfma_f32_16x16x32_bf16 v[212:215], v[140:143], v[224:227], v[212:215]\n\t" \
    "s_waitcnt lgkmcnt(3)\n\t" \
    "v_mfma_f32_16x16x32_bf16 v[204:207], v[184:187], v[228:231], v[204:207]\n\t" \
    "v_add_u32 v252, %[qs], v252\n\t" \
    "v_mfma_f32_16x16x32_bf16 v[208:211], v[192:195], v[228:231], v[208:211]\n\t" \
    "v_mov_b64 v[224:225], 0\n\t" \
    "v_mfma_f32_16x16x32_bf16 v[200:203], v[176:179], v[228:231], v[200:203]\n\t" \
    "v_mov_b64 v[226:227], 0\n\t" \
    "s_waitcnt lgkmcnt(2)\n\t" \
    "v_mfma_f32_16x16x32_bf16 v[204:207], v[188:191], v[232:235], v[204:207]\n\t" \
    "v_mfma_f32_16x16x32_bf16 v[208:211], v[196:199], v[232:235], v[208:211]\n\t" \
    "v_mfma_f32_16x16x32_bf16 v[200:203], v[180:183], v[232:235], v[200:203]\n\t" \
    "s_waitcnt lgkmcnt(1)\n\t" \
    "v_mfma_f32_16x16x32_bf16 v[216:219], v[184:187], v[236:239], v[216:219]\n\t" \
    "ds_read_b128 v[228:231], %[wl] offset:2048\n\t" \
    "ds_read_b128 v[232:235], %[wl] offset:3072\n\t" \
    "v_mfma_f32_16x16x32_bf16 v[220:223], v[192:195], v[236:239], v[220:223]\n\t" \
    "v_cvt_pk_bf16_f32 v204, v204, v205\n\t" \
    "v_cvt_pk_bf16_f32 v205, v206, v207\n\t" \
    "v_mfma_f32_16x16x32_bf16 v[212:215], v[176:179], v[236:239], v[212:215]\n\t" \
    "ds_write_b64 v252, v[204:205] offset:1024\n\t" \
    "v_cvt_pk_bf16_f32 v208, v208, v209\n\t" \
    "s_waitcnt lgkmcnt(3)\n\t" \
    "v_mfma_f32_16x16x32_bf16 v[216:219], v[188:191], v[240:243], v[216:219]\n\t" \
    "v_cvt_pk_bf16_f32 v209, v210, v211\n\t" \
    "ds_write_b64 v252, v[208:209] offset:2048\n\t" \
    "v_mfma_f32_16x16x32_bf16 v[220:223], v[196:199], v[240:243], v[220:223]\n\t" \
    "v_cvt_pk_bf16_f32 v200, v200, v201\n\t" \
    "v_cvt_pk_bf16_f32 v201, v202, v203\n\t" \
    "v_mfma_f32_16x16x32_bf16 v[212:215], v[180:183], v[240:243], v[212:215]\n\t" \
    "ds_write_b64 v252, v[200:201] offset:0\n\t" \
    "ds_read_b128 v[200:203], %[wl] offset:0\n\t" \
    "ds_read_b128 v[204:207], %[wl] offset:1024\n\t" \
    "v_cvt_pk_bf16_f32 v216, v216, v217\n\t" \
    "v_cvt_pk_bf16_f32 v217, v218, v219\n\t" \
    "ds_write_b64 v252, v[216:217] offset:7168\n\t" \
    "v_cvt_pk_bf16_f32 v220, v220, v221\n\t" \
    "v_cvt_pk_bf16_f32 v221, v222, v223\n\t" \
    "ds_write_b64 v252, v[220:221] offset:8192\n\t" \
    "v_cvt_pk_bf16_f32 v212, v212, v213\n\t" \
    "v_cvt_pk_bf16_f32 v213, v214, v215\n\t" \
    "ds_write_b64 v252, v[212:213] offset:6144\n\t" \
    "s_waitcnt lgkmcnt(0)\n\t"
#define ASM_CRIT1 \
    "v_bfe_i32 v241, %[ab], 7, 1\n\t" \
    "v_and_b32 v241, 0xaaaa, v241\n\t" \
    "v_or_b32 v241, 0x4444, v241\n\t" \
    "v_xor_b32 v212, 0x400, %[ab]\n\t" \
    "v_xor_b32 v216, 0x800, %[ab]\n\t" \
    "v_smfmac_f32_16x16x64_bf16 v[224:227], v[208:211], v[200:207], v241\n\t" \
    "v_smfmac_f32_16x16x64_bf16 v[244:247], v[208:211], v[0:7], v241\n\t" \
    "v_smfmac_f32_16x16x64_bf16 v[248:251], v[208:211], v[8:15], v241\n\t" \
    "v_xor_b32 v220, 0xc00, %[ab]\n\t" \
    "v_xor_b32 v236, 0x1000, %[ab]\n\t" \
    "v_xor_b32 v240, 0x1400, %[ab]\n\t" \
    "s_waitcnt lgkmcnt(0)\n\t" \
    "s_barrier\n\t" ASM_ST \
    "ds_read_b128 v[212:215], v212 offset:0\n\t" \
    "ds_read_b128 v[216:219], v216 offset:0\n\t" \
    "ds_read_b128 v[220:223], v220 offset:0\n\t" \
    "ds_read_b128 v[236:239], v236 offset:0\n\t" \
    "ds_read_b128 v[208:211], v240 offset:0\n\t" \
    "ds_read_b128 v[200:203], %[wl] offset:4096\n\t" \
    "ds_read_b128 v[204:207], %[wl] offset:5120\n\t" \
    "s_waitcnt lgkmcnt(6)\n\t" \
    "v_smfmac_f32_16x16x64_bf16 v[224:227], v[212:215], v[16:23], v241\n\t" \
    "v_smfmac_f32_16x16x64_bf16 v[244:247], v[212:215], v[24:31], v241\n\t" \
    "v_xor_b32 v240, 0x1800, %[ab]\n\t" \
    "v_smfmac_f32_16x16x64_bf16 v[248:251], v[212:215], v[32:39], v241\n\t" \
    "ds_read_b128 v[212:215], v240 offset:0\n\t" \
    "s_waitcnt lgkmcnt(6)\n\t" \
    "v_smfmac_f32_16x16x64_bf16 v[224:227], v[216:219], v[228:235], v241\n\t" \
    "ds_read_b128 v[228:231], %[wl] offset:6144\n\t" \
    "ds_read_b128 v[232:235], %[wl] offset:7168\n\t" \
    "v_smfmac_f32_16x16x64_bf16 v[244:247], v[216:219], v[40:47], v241\n\t" \
    "v_xor_b32 v240, 0x1c00, %[ab]\n\t" \
    "v_smfmac_f32_16x16x64_bf16 v[248:251], v[216:219], v[48:55], v241\n\t" \
    "ds_read_b128 v[216:219], v240 offset:0\n\t" \
    "s_waitcnt lgkmcnt(8)\n\t" \
    "v_smfmac_f32_16x16x64_bf16 v[224:227], v[220:223], v[56:63], v241\n\t" \
    "v_smfmac_f32_16x16x64_bf16 v[244:247], v[220:223], v[64:71], v241\n\t" \
    "v_and_b32 v240, 0x3ff, %[ab]\n\t" \
    "v_smfmac_f32_16x16x64_bf16 v[248:251], v[220:223], v[72:79], v241\n\t" \
    "ds_read_b128 v[220:223], v240 offset:8192\n\t" \
    "s_waitcnt lgkmcnt(5)\n\t" \
    "v_smfmac_f32_16x16x64_bf16 v[224:227], v[236:239], v[200:207], v241\n\t" \
    "ds_read_b128 v[200:203], %[wl] offset:8192\n\t" \
    "ds_read_b128 v[204:207], %[wl] offset:9216\n\t" \
    "v_smfmac_f32_16x16x64_bf16 v[244:247], v[236:239], v[80:87], v241\n\t" \
    "v_smfmac_f32_16x16x64_bf16 v[248:251], v[236:239], v[88:95], v241\n\t" \
    "ds_read_b128 v[236:239], v240 offset:9216\n\t" \
    "v_smfmac_f32_16x16x64_bf16 v[244:247], v[208:211], v[104:111], v241\n\t" \
    "s_waitcnt lgkmcnt(7)\n\t" \
    "v_smfmac_f32_16x16x64_bf16 v[244:247], v[212:215], v[120:127], v241\n\t" \
    "s_waitcnt lgkmcnt(4)\n\t" \
    "v_smfmac_f32_16x16x64_bf16 v[244:247], v[216:219], v[144:151], v241\n\t" \
    "s_waitcnt lgkmcnt(3)\n\t" \
    "v_smfmac_f32_16x16x64_bf16 v[244:247], v[220:223], v[160:167], v241\n\t" \
    "s_waitcnt lgkmcnt(0)\n\t" \
    "v_smfmac_f32_16x16x64_bf16 v[244:247], v[236:239], v[184:191], v241\n\t" \
    "v_smfmac_f32_16x16x64_bf16 v[248:251], v[208:211], v[112:119], v241\n\t" \
    "v_smfmac_f32_16x16x64_bf16 v[248:251], v[212:215], v[128:135], v241\n\t" \
    "v_smfmac_f32_16x16x64_bf16 v[248:251], v[216:219], v[152:159], v241\n\t" \
    "v_permlane32_swap_b32 v244, v246\n\t" \
    "v_permlane32_swap_b32 v245, v247\n\t" \
    "v_smfmac_f32_16x16x64_bf16 v[248:251], v[220:223], v[168:175], v241\n\t" \
    "v_add_f32 v244, v244, v246\n\t" \
    "v_add_f32 v245, v245, v247\n\t" \
    "v_smfmac_f32_16x16x64_bf16 v[248:251], v[236:239], v[192:199], v241\n\t" \
    "v_cvt_pk_bf16_f32 v244, v244, v245\n\t" \
    "ds_write_b16 %[dst], v244 offset:512\n\t" \
    "ds_write_b16_d16_hi %[dst], v244 offset:528\n\t" \
    "v_smfmac_f32_16x16x64_bf16 v[224:227], v[208:211], v[96:103], v241\n\t" \
    "v_smfmac_f32_16x16x64_bf16 v[224:227], v[212:215], v[228:235], v241\n\t" \
    "v_smfmac_f32_16x16x64_bf16 v[224:227], v[216:219], v[136:143], v241\n\t" \
    "v_permlane32_swap_b32 v248, v250\n\t" \
    "v_permlane32_swap_b32 v249, v251\n\t" \
    "v_smfmac_f32_16x16x64_bf16 v[224:227], v[220:223], v[200:207], v241\n\t" \
    "v_add_f32 v248, v248, v250\n\t" \
    "v_add_f32 v249, v249, v251\n\t" \
    "v_smfmac_f32_16x16x64_bf16 v[224:227], v[236:239], v[176:183], v241\n\t" \
    "v_cvt_pk_bf16_f32 v248, v248, v249\n\t" \
    "ds_write_b16 %[dst], v248 offset:1024\n\t" \
    "ds_write_b16_d16_hi %[dst], v248 offset:1040\n\t" \
    "s_nop 4\n\t" \
    "v_permlane32_swap_b32 v224, v226\n\t" \
    "v_permlane32_swap_b32 v225, v227\n\t" \
    "v_add_f32 v224, v224, v226\n\t" \
    "v_add_f32 v225, v225, v227\n\t" \
    "v_cvt_pk_bf16_f32 v224, v224, v225\n\t" \
    "ds_write_b16 %[dst], v224 offset:0\n\t" \
    "ds_write_b16_d16_hi %[dst], v224 offset:16\n\t"
#define ASM_CRIT1_N \
    "v_bfe_i32 v241, %[ab], 7, 1\n\t" \
    "v_and_b32 v241, 0xaaaa, v241\n\t" \
    "v_or_b32 v241, 0x4444, v241\n\t" \
    "v_xor_b32 v212, 0x400, %[ab]\n\t" \
    "v_xor_b32 v216, 0x800, %[ab]\n\t" \
    "v_xor_b32 v220, 0xc00, %[ab]\n\t" \
    "v_xor_b32 v236, 0x1000, %[ab]\n\t" \
    "v_xor_b32 v240, 0x1400, %[ab]\n\t" \
    "s_waitcnt lgkmcnt(0)\n\t" \
    "s_barrier\n\t" ASM_ST \
    "ds_read_b128 v[208:211], %[ab] offset:0\n\t" \
    "ds_read_b128 v[212:215], v212 offset:0\n\t" \
    "ds_read_b128 v[216:219], v216 offset:0\n\t" \
    "ds_read_b128 v[220:223], v220 offset:0\n\t" \
    "ds_read_b128 v[236:239], v236 offset:0\n\t" \
    "s_waitcnt lgkmcnt(4)\n\t" \
    "v_smfmac_f32_16x16x64_bf16 v[224:227], v[208:211], v[200:207], v241\n\t" \
    "v_smfmac_f32_16x16x64_bf16 v[244:247], v[208:211], v[0:7], v241\n\t" \
    "v_smfmac_f32_16x16x64_bf16 v[248:251], v[208:211], v[8:15], v241\n\t" \
    "ds_read_b128 v[208:211], v240 offset:0\n\t" \
    "ds_read_b128 v[200:203], %[wl] offset:4096\n\t" \
    "ds_read_b128 v[204:207], %[wl] offset:5120\n\t" \
    "s_waitcnt lgkmcnt(6)\n\t" \
    "v_smfmac_f32_16x16x64_bf16 v[224:227], v[212:215], v[16:23], v241\n\t" \
    "v_smfmac_f32_16x16x64_bf16 v[244:247], v[212:215], v[24:31], v241\n\t" \
    "v_xor_b32 v240, 0x1800, %[ab]\n\t" \
    "v_smfmac_f32_16x16x64_bf16 v[248:251], v[212:215], v[32:39], v241\n\t" \
    "ds_read_b128 v[212:215], v240 offset:0\n\t" \
    "s_waitcnt lgkmcnt(6)\n\t" \
    "v_smfmac_f32_16x16x64_bf16 v[224:227], v[216:219], v[228:235], v241\n\t" \
    "ds_read_b128 v[228:231], %[wl] offset:6144\n\t" \
    "ds_read_b128 v[232:235], %[wl] offset:7168\n\t" \
    "v_smfmac_f32_16x16x64_bf16 v[244:247], v[216:219], v[40:47], v241\n\t" \
    "v_xor_b32 v240, 0x1c00, %[ab]\n\t" \
    "v_smfmac_f32_16x16x64_bf16 v[248:251], v[216:219], v[48:55], v241\n\t" \
    "ds_read_b128 v[216:219], v240 offset:0\n\t" \
    "s_waitcnt lgkmcnt(8)\n\t" \
    "v_smfmac_f32_16x16x64_bf16 v[224:227], v[220:223], v[56:63], v241\n\t" \
    "v_smfmac_f32_16x16x64_bf16 v[244:247], v[220:223], v[64:71], v241\n\t" \
    "v_and_b32 v240, 0x3ff, %[ab]\n\t" \
    "v_smfmac_f32_16x16x64_bf16 v[248:251], v[220:223], v[72:79], v241\n\t" \
    "ds_read_b128 v[220:223], v240 offset:8192\n\t" \
    "s_waitcnt lgkmcnt(5)\n\t" \
    "v_smfmac_f32_16x16x64_bf16 v[224:227], v[236:239], v[200:207], v241\n\t" \
    "ds_read_b128 v[200:203], %[wl] offset:8192\n\t" \
    "ds_read_b128 v[204:207], %[wl] offset:9216\n\t" \
    "v_smfmac_f32_16x16x64_bf16 v[244:247], v[236:239], v[80:87], v241\n\t" \
    "v_smfmac_f32_16x16x64_bf16 v[248:251], v[236:239], v[88:95], v241\n\t" \
    "ds_read_b128 v[236:239], v240 offset:9216\n\t" \
    "v_smfmac_f32_16x16x64_bf16 v[244:247], v[208:211], v[104:111], v241\n\t" \
    "s_waitcnt lgkmcnt(7)\n\t" \
    "v_smfmac_f32_16x16x64_bf16 v[244:247], v[212:215], v[120:127], v241\n\t" \
    "s_waitcnt lgkmcnt(4)\n\t" \
    "v_smfmac_f32_16x16x64_bf16 v[244:247], v[216:219], v[144:151], v241\n\t" \
    "s_waitcnt lgkmcnt(3)\n\t" \
    "v_smfmac_f32_16x16x64_bf16 v[244:247], v[220:223], v[160:167], v241\n\t" \
    "s_waitcnt lgkmcnt(0)\n\t" \
    "v_smfmac_f32_16x16x64_bf16 v[244:247], v[236:239], v[184:191], v241\n\t" \
    "v_smfmac_f32_16x16x64_bf16 v[248:251], v[208:211], v[112:119], v241\n\t" \
    "v_smfmac_f32_16x16x64_bf16 v[248:251], v[212:215], v[128:135], v241\n\t" \
    "v_smfmac_f32_16x16x64_bf16 v[248:251], v[216:219], v[152:159], v241\n\t" \
    "v_permlane32_swap_b32 v244, v246\n\t" \
    "v_permlane32_swap_b32 v245, v247\n\t" \
    "v_smfmac_f32_16x16x64_bf16 v[248:251], v[220:223], v[168:175], v241\n\t" \
    "v_add_f32 v244, v244, v246\n\t" \
    "v_add_f32 v245, v245, v247\n\t" \
    "v_smfmac_f32_16x16x64_bf16 v[248:251], v[236:239], v[192:199], v241\n\t" \
    "v_cvt_pk_bf16_f32 v244, v244, v245\n\t" \
    "ds_write_b16 %[dst], v244 offset:512\n\t" \
    "ds_write_b16_d16_hi %[dst], v244 offset:528\n\t" \
    "v_smfmac_f32_16x16x64_bf16 v[224:227], v[208:211], v[96:103], v241\n\t" \
    "v_smfmac_f32_16x16x64_bf16 v[224:227], v[212:215], v[228:235], v241\n\t" \
    "v_smfmac_f32_16x16x64_bf16 v[224:227], v[216:219], v[136:143], v241\n\t" \
    "v_permlane32_swap_b32 v248, v250\n\t" \
    "v_permlane32_swap_b32 v249, v251\n\t" \
    "v_smfmac_f32_16x16x64_bf16 v[224:227], v[220:223], v[200:207], v241\n\t" \
    "v_add_f32 v248, v248, v250\n\t" \
    "v_add_f32 v249, v249, v251\n\t" \
    "v_smfmac_f32_16x16x64_bf16 v[224:227], v[236:239], v[176:183], v241\n\t" \
    "v_cvt_pk_bf16_f32 v248, v248, v249\n\t" \
    "ds_write_b16 %[dst], v248 offset:1024\n\t" \
    "ds_write_b16_d16_hi %[dst], v248 offset:1040\n\t" \
    "s_nop 4\n\t" \
    "v_permlane32_swap_b32 v224, v226\n\t" \
    "v_permlane32_swap_b32 v225, v227\n\t" \
    "v_add_f32 v224, v224, v226\n\t" \
    "v_add_f32 v225, v225, v227\n\t" \
    "v_cvt_pk_bf16_f32 v224, v224, v225\n\t" \
    "ds_write_b16 %[dst], v224 offset:0\n\t" \
    "ds_write_b16_d16_hi %[dst], v224 offset:16\n\t"
#define ASM_VOX \
    "s_waitcnt lgkmcnt(0)\n\t" \
    "ds_read_b128 v[224:227], %[va] offset:0\n\t" \
    "ds_read_b128 v[248:251], %[wl] offset:0\n\t" \
    "ds_read_b128 v[228:231], %[va] offset:6144\n\t" \
    "ds_read_b128 v[232:235], %[va] offset:2048\n\t" \
    "ds_read_b128 v[236:239], %[va] offset:8192\n\t" \
    "ds_read_b128 v[240:243], %[va] offset:3072\n\t" \
    "s_waitcnt lgkmcnt(4)\n\t" \
    "v_mfma_f32_16x16x32_bf16 v[200:203], v[224:227], v[248:251], 0\n\t" \
    "ds_read_b128 v[244:247], %[va] offset:9216\n\t" \
    "s_waitcnt lgkmcnt(4)\n\t" \
    "v_mfma_f32_16x16x32_bf16 v[204:207], v[228:231], v[248:251], 0\n\t" \
    "ds_read_b128 v[224:227], %[va] offset:1024\n\t" \
    "s_waitcnt lgkmcnt(4)\n\t" \
    "v_mfma_f32_16x16x32_bf16 v[200:203], v[232:235], v[0:3], v[200:203]\n\t" \
    "ds_read_b128 v[228:231], %[va] offset:7168\n\t" \
    "ds_read_b128 v[248:251], %[wl] offset:1024\n\t" \
    "s_waitcnt lgkmcnt(5)\n\t" \
    "v_mfma_f32_16x16x32_bf16 v[204:207], v[236:239], v[0:3], v[204:207]\n\t" \
    "ds_read_b128 v[232:235], %[va] offset:4096\n\t" \
    "s_waitcnt lgkmcnt(5)\n\t" \
    "v_mfma_f32_16x16x32_bf16 v[200:203], v[240:243], v[4:7], v[200:203]\n\t" \
    "ds_read_b128 v[236:239], %[va] offset:10240\n\t" \
    "s_waitcnt lgkmcnt(5)\n\t" \
    "v_mfma_f32_16x16x32_bf16 v[204:207], v[244:247], v[4:7], v[204:207]\n\t" \
    "ds_read_b128 v[240:243], %[va] offset:5120\n\t" \
    "s_waitcnt lgkmcnt(3)\n\t" \
    "v_mfma_f32_16x16x32_bf16 v[200:203], v[224:227], v[248:251], v[200:203]\n\t" \
    "ds_read_b128 v[244:247], %[va] offset:11264\n\t" \
    "v_mfma_f32_16x16x32_bf16 v[208:211], v[224:227], v[20:23], 0\n\t" \
    "v_mfma_f32_16x16x32_bf16 v[204:207], v[228:231], v[248:251], v[204:207]\n\t" \
    "ds_read_b128 v[224:227], %[va] offset:0\n\t" \
    "v_mfma_f32_16x16x32_bf16 v[212:215], v[228:231], v[20:23], 0\n\t" \
    "s_waitcnt lgkmcnt(4)\n\t" \
    "v_mfma_f32_16x16x32_bf16 v[200:203], v[232:235], v[8:11], v[200:203]\n\t" \
    "ds_read_b128 v[228:231], %[va] offset:6144\n\t" \
    "ds_read_b128 v[248:251], %[wl] offset:2048\n\t" \
    "v_mfma_f32_16x16x32_bf16 v[208:211], v[232:235], v[32:35], v[208:211]\n\t" \
    "s_waitcnt lgkmcnt(5)\n\t" \
    "v_mfma_f32_16x16x32_bf16 v[204:207], v[236:239], v[8:11], v[204:207]\n\t" \
    "ds_read_b128 v[232:235], %[va] offset:2048\n\t" \
    "v_mfma_f32_16x16x32_bf16 v[212:215], v[236:239], v[32:35], v[212:215]\n\t" \
    "s_waitcnt lgkmcnt(5)\n\t" \
    "v_mfma_f32_16x16x32_bf16 v[200:203], v[240:243], v[12:15], v[200:203]\n\t" \
    "ds_read_b128 v[236:239], %[va] offset:8192\n\t" \
    "v_mfma_f32_16x16x32_bf16 v[208:211], v[240:243], v[36:39], v[208:211]\n\t" \
    "s_waitcnt lgkmcnt(5)\n\t" \
    "v_mfma_f32_16x16x32_bf16 v[204:207], v[244:247], v[12:15], v[204:207]\n\t" \
    "ds_read_b128 v[240:243], %[va] offset:3072\n\t" \
    "v_mfma_f32_16x16x32_bf16 v[212:215], v[244:247], v[36:39], v[212:215]\n\t" \
    "s_waitcnt lgkmcnt(3)\n\t" \
    "v_mfma_f32_16x16x32_bf16 v[216:219], v[224:227], v[248:251], 0\n\t" \
    "ds_read_b128 v[244:247], %[va] offset:9216\n\t" \
    "v_mfma_f32_16x16x32_bf16 v[208:211], v[224:227], v[16:19], v[208:211]\n\t" \
    "v_mfma_f32_16x16x32_bf16 v[220:223], v[228:231], v[248:251], 0\n\t" \
    "ds_read_b128 v[224:227], %[va] offset:1024\n\t" \
    "v_mfma_f32_16x16x32_bf16 v[212:215], v[228:231], v[16:19], v[212:215]\n\t" \
    "v_cvt_pk_bf16_f32 v200, v200, v201\n\t" \
    "s_waitcnt lgkmcnt(4)\n\t" \
    "v_mfma_f32_16x16x32_bf16 v[216:219], v[232:235], v[40:43], v[216:219]\n\t" \
    "ds_read_b128 v[228:231], %[va] offset:7168\n\t" \
    "ds_read_b128 v[248:251], %[wl] offset:3072\n\t" \
    "v_cvt_pk_bf16_f32 v201, v202, v203\n\t" \
    "v_mfma_f32_16x16x32_bf16 v[208:211], v[232:235], v[24:27], v[208:211]\n\t" \
    "v_cvt_pk_bf16_f32 v202, v204, v205\n\t" \
    "s_waitcnt lgkmcnt(5)\n\t" \
    "v_mfma_f32_16x16x32_bf16 v[220:223], v[236:239], v[40:43], v[220:223]\n\t" \
    "ds_read_b128 v[232:235], %[va] offset:4096\n\t" \
    "v_cvt_pk_bf16_f32 v203, v206, v207\n\t" \
    "v_mfma_f32_16x16x32_bf16 v[212:215], v[236:239], v[24:27], v[212:215]\n\t" \
    "buffer_store_dwordx4 v[200:203], %[l16], %[rd], %[so] offen sc1\n\t" \
    "s_waitcnt lgkmcnt(5)\n\t" \
    "v_mfma_f32_16x16x32_bf16 v[216:219], v[240:243], v[44:47], v[216:219]\n\t" \
    "ds_read_b128 v[236:239], %[va] offset:10240\n\t" \
    "s_add_u32 %[so], %[so], 0x8000\n\t" \
    "v_mfma_f32_16x16x32_bf16 v[208:211], v[240:243], v[28:31], v[208:211]\n\t" \
    "s_waitcnt lgkmcnt(5)\n\t" \
    "v_mfma_f32_16x16x32_bf16 v[220:223], v[244:247], v[44:47], v[220:223]\n\t" \
    "ds_read_b128 v[240:243], %[va] offset:5120\n\t" \
    "v_mfma_f32_16x16x32_bf16 v[212:215], v[244:247], v[28:31], v[212:215]\n\t" \
    "s_waitcnt lgkmcnt(3)\n\t" \
    "v_mfma_f32_16x16x32_bf16 v[216:219], v[224:227], v[248:251], v[216:219]\n\t" \
    "ds_read_b128 v[244:247], %[va] offset:11264\n\t" \
    "v_mfma_f32_16x16x32_bf16 v[200:203], v[224:227], v[60:63], 0\n\t" \
    "v_mfma_f32_16x16x32_bf16 v[220:223], v[228:231], v[248:251], v[220:223]\n\t" \
    "ds_read_b128 v[224:227], %[va] offset:0\n\t" \
    "v_mfma_f32_16x16x32_bf16 v[204:207], v[228:231], v[60:63], 0\n\t" \
    "v_cvt_pk_bf16_f32 v208, v208, v209\n\t" \
    "s_waitcnt lgkmcnt(4)\n\t" \
    "v_mfma_f32_16x16x32_bf16 v[216:219], v[232:235], v[48:51], v[216:219]\n\t" \
    "ds_read_b128 v[228:231], %[va] offset:6144\n\t" \
    "ds_read_b128 v[248:251], %[wl] offset:4096\n\t" \
    "v_cvt_pk_bf16_f32 v209, v210, v211\n\t" \
    "v_mfma_f32_16x16x32_bf16 v[200:203], v[232:235], v[72:75], v[200:203]\n\t" \
    "v_cvt_pk_bf16_f32 v210, v212, v213\n\t" \
    "s_waitcnt lgkmcnt(5)\n\t" \
    "v_mfma_f32_16x16x32_bf16 v[220:223], v[236:239], v[48:51], v[220:223]\n\t" \
    "ds_read_b128 v[232:235], %[va] offset:2048\n\t" \
    "v_cvt_pk_bf16_f32 v211, v214, v215\n\t" \
    "v_mfma_f32_16x16x32_bf16 v[204:207], v[236:239], v[72:75], v[204:207]\n\t" \
    "buffer_store_dwordx4 v[208:211], %[l16], %[rd], %[so] offen sc1\n\t" \
    "s_waitcnt lgkmcnt(5)\n\t" \
    "v_mfma_f32_16x16x32_bf16 v[216:219], v[240:243], v[52:55], v[216:219]\n\t" \
    "ds_read_b128 v[236:239], %[va] offset:8192\n\t" \
    "s_add_u32 %[so], %[so], 0x8000\n\t" \
    "v_mfma_f32_16x16x32_bf16 v[200:203], v[240:243], v[76:79], v[200:203]\n\t" \
    "s_waitcnt lgkmcnt(5)\n\t" \
    "v_mfma_f32_16x16x32_bf16 v[220:223], v[244:247], v[52:55], v[220:223]\n\t" \
    "ds_read_b128 v[240:243], %[va] offset:3072\n\t" \
    "v_mfma_f32_16x16x32_bf16 v[204:207], v[244:247], v[76:79], v[204:207]\n\t" \
    "s_waitcnt lgkmcnt(3)\n\t" \
    "v_mfma_f32_16x16x32_bf16 v[208:211], v[224:227], v[248:251], 0\n\t" \
    "ds_read_b128 v[244:247], %[va] offset:9216\n\t" \
    "v_mfma_f32_16x16x32_bf16 v[200:203], v[224:227], v[56:59], v[200:203]\n\t" \
    "v_mfma_f32_16x16x32_bf16 v[212:215], v[228:231], v[248:251], 0\n\t" \
    "ds_read_b128 v[224:227], %[va] offset:1024\n\t" \
    "v_mfma_f32_16x16x32_bf16 v[204:207], v[228:231], v[56:59], v[204:207]\n\t" \
    "v_cvt_pk_bf16_f32 v216, v216, v217\n\t" \
    "s_waitcnt lgkmcnt(4)\n\t" \
    "v_mfma_f32_16x16x32_bf16 v[208:211], v[232:235], v[80:83], v[208:211]\n\t" \
    "ds_read_b128 v[228:231], %[va] offset:7168\n\t" \
    "ds_read_b128 v[248:251], %[wl] offset:5120\n\t" \
    "v_cvt_pk_bf16_f32 v217, v218, v219\n\t" \
    "v_mfma_f32_16x16x32_bf16 v[200:203], v[232:235], v[64:67], v[200:203]\n\t" \
    "v_cvt_pk_bf16_f32 v218, v220, v221\n\t" \
    "s_waitcnt lgkmcnt(5)\n\t" \
    "v_mfma_f32_16x16x32_bf16 v[212:215], v[236:239], v[80:83], v[212:215]\n\t" \
    "ds_read_b128 v[232:235], %[va] offset:4096\n\t" \
    "v_cvt_pk_bf16_f32 v219, v222, v223\n\t" \
    "v_mfma_f32_16x16x32_bf16 v[204:207], v[236:239], v[64:67], v[204:207]\n\t" \
    "buffer_store_dwordx4 v[216:219], %[l16], %[rd], %[so] offen sc1\n\t" \
    "s_waitcnt lgkmcnt(5)\n\t" \
    "v_mfma_f32_16x16x32_bf16 v[208:211], v[240:243], v[84:87], v[208:211]\n\t" \
    "ds_read_b128 v[236:239], %[va] offset:10240\n\t" \
    "s_add_u32 %[so], %[so], 0x8000\n\t" \
    "v_mfma_f32_16x16x32_bf16 v[200:203], v[240:243], v[68:71], v[200:203]\n\t" \
    "s_waitcnt lgkmcnt(5)\n\t" \
    "v_mfma_f32_16x16x32_bf16 v[212:215], v[244:247], v[84:87], v[212:215]\n\t" \
    "ds_read_b128 v[240:243], %[va] offset:5120\n\t" \
    "v_mfma_f32_16x16x32_bf16 v[204:207], v[244:247], v[68:71], v[204:207]\n\t" \
    "s_waitcnt lgkmcnt(3)\n\t" \
    "v_mfma_f32_16x16x32_bf16 v[208:211], v[224:227], v[248:251], v[208:211]\n\t" \
    "ds_read_b128 v[244:247], %[va] offset:11264\n\t" \
    "v_mfma_f32_16x16x32_bf16 v[216:219], v[224:227], v[100:103], 0\n\t" \
    "v_mfma_f32_16x16x32_bf16 v[212:215], v[228:231], v[248:251], v[212:215]\n\t" \
    "ds_read_b128 v[224:227], %[va] offset:0\n\t" \
    "v_mfma_f32_16x16x32_bf16 v[220:223], v[228:231], v[100:103], 0\n\t" \
    "v_cvt_pk_bf16_f32 v200, v200, v201\n\t" \
    "s_waitcnt lgkmcnt(4)\n\t" \
    "v_mfma_f32_16x16x32_bf16 v[208:211], v[232:235], v[88:91], v[208:211]\n\t" \
    "ds_read_b128 v[228:231], %[va] offset:6144\n\t" \
    "ds_read_b128 v[248:251], %[wl] offset:6144\n\t" \
    "v_cvt_pk_bf16_f32 v201, v202, v203\n\t" \
    "v_mfma_f32_16x16x32_bf16 v[216:219], v[232:235], v[112:115], v[216:219]\n\t" \
    "v_cvt_pk_bf16_f32 v202, v204, v205\n\t" \
    "s_waitcnt lgkmcnt(5)\n\t" \
    "v_mfma_f32_16x16x32_bf16 v[212:215], v[236:239], v[88:91], v[212:215]\n\t" \
    "ds_read_b128 v[232:235], %[va] offset:2048\n\t" \
    "v_cvt_pk_bf16_f32 v203, v206, v207\n\t" \
    "v_mfma_f32_16x16x32_bf16 v[220:223], v[236:239], v[112:115], v[220:223]\n\t" \
    "buffer_store_dwordx4 v[200:203], %[l16], %[rd], %[so] offen sc1\n\t" \
    "s_waitcnt lgkmcnt(5)\n\t" \
    "v_mfma_f32_16x16x32_bf16 v[208:211], v[240:243], v[92:95], v[208:211]\n\t" \
    "ds_read_b128 v[236:239], %[va] offset:8192\n\t" \
    "s_add_u32 %[so], %[so], 0x8000\n\t" \
    "v_mfma_f32_16x16x32_bf16 v[216:219], v[240:243], v[116:119], v[216:219]\n\t" \
    "s_waitcnt lgkmcnt(5)\n\t" \
    "v_mfma_f32_16x16x32_bf16 v[212:215], v[244:247], v[92:95], v[212:215]\n\t" \
    "ds_read_b128 v[240:243], %[va] offset:3072\n\t" \
    "v_mfma_f32_16x16x32_bf16 v[220:223], v[244:247], v[116:119], v[220:223]\n\t" \
    "s_waitcnt lgkmcnt(3)\n\t" \
    "v_mfma_f32_16x16x32_bf16 v[200:203], v[224:227], v[248:251], 0\n\t" \
    "ds_read_b128 v[244:247], %[va] offset:9216\n\t" \
    "v_mfma_f32_16x16x32_bf16 v[216:219], v[224:227], v[96:99], v[216:219]\n\t" \
    "v_mfma_f32_16x16x32_bf16 v[204:207], v[228:231], v[248:251], 0\n\t" \
    "ds_read_b128 v[224:227], %[va] offset:1024\n\t" \
    "v_mfma_f32_16x16x32_bf16 v[220:223], v[228:231], v[96:99], v[220:223]\n\t" \
    "v_cvt_pk_bf16_f32 v208, v208, v209\n\t" \
    "s_waitcnt lgkmcnt(4)\n\t" \
    "v_mfma_f32_16x16x32_bf16 v[200:203], v[232:235], v[120:123], v[200:203]\n\t" \
    "ds_read_b128 v[228:231], %[va] offset:7168\n\t" \
    "ds_read_b128 v[248:251], %[wl] offset:7168\n\t" \
    "v_cvt_pk_bf16_f32 v209, v210, v211\n\t" \
    "v_mfma_f32_16x16x32_bf16 v[216:219], v[232:235], v[104:107], v[216:219]\n\t" \
    "v_cvt_pk_bf16_f32 v210, v212, v213\n\t" \
    "s_waitcnt lgkmcnt(5)\n\t" \
    "v_mfma_f32_16x16x32_bf16 v[204:207], v[236:239], v[120:123], v[204:207]\n\t" \
    "ds_read_b128 v[232:235], %[va] offset:4096\n\t" \
    "v_cvt_pk_bf16_f32 v211, v214, v215\n\t" \
    "v_mfma_f32_16x16x32_bf16 v[220:223], v[236:239], v[104:107], v[220:223]\n\t" \
    "buffer_store_dwordx4 v[208:211], %[l16], %[rd], %[so] offen sc1\n\t" \
    "s_waitcnt lgkmcnt(5)\n\t" \
    "v_mfma_f32_16x16x32_bf16 v[200:203], v[240:243], v[124:127], v[200:203]\n\t" \
    "ds_read_b128 v[236:239], %[va] offset:10240\n\t" \
    "s_add_u32 %[so], %[so], 0x8000\n\t" \
    "v_mfma_f32_16x16x32_bf16 v[216:219], v[240:243], v[108:111], v[216:219]\n\t" \
    "s_waitcnt lgkmcnt(5)\n\t" \
    "v_mfma_f32_16x16x32_bf16 v[204:207], v[244:247], v[124:127], v[204:207]\n\t" \
    "ds_read_b128 v[240:243], %[va] offset:5120\n\t" \
    "v_mfma_f32_16x16x32_bf16 v[220:223], v[244:247], v[108:111], v[220:223]\n\t" \
    "s_waitcnt lgkmcnt(3)\n\t" \
    "v_mfma_f32_16x16x32_bf16 v[200:203], v[224:227], v[248:251], v[200:203]\n\t" \
    "ds_read_b128 v[244:247], %[va] offset:11264\n\t" \
    "v_mfma_f32_16x16x32_bf16 v[208:211], v[224:227], v[140:143], 0\n\t" \
    "v_mfma_f32_16x16x32_bf16 v[204:207], v[228:231], v[248:251], v[204:207]\n\t" \
    "ds_read_b128 v[224:227], %[va] offset:0\n\t" \
    "v_mfma_f32_16x16x32_bf16 v[212:215], v[228:231], v[140:143], 0\n\t" \
    "v_cvt_pk_bf16_f32 v216, v216, v217\n\t" \
    "s_waitcnt lgkmcnt(4)\n\t" \
    "v_mfma_f32_16x16x32_bf16 v[200:203], v[232:235], v[128:131], v[200:203]\n\t" \
    "ds_read_b128 v[228:231], %[va] offset:6144\n\t" \
    "ds_read_b128 v[248:251], %[wl] offset:8192\n\t" \
    "v_cvt_pk_bf16_f32 v217, v218, v219\n\t" \
    "v_mfma_f32_16x16x32_bf16 v[208:211], v[232:235], v[152:155], v[208:211]\n\t" \
    "v_cvt_pk_bf16_f32 v218, v220, v221\n\t" \
    "s_waitcnt lgkmcnt(5)\n\t" \
    "v_mfma_f32_16x16x32_bf16 v[204:207], v[236:239], v[128:131], v[204:207]\n\t" \
    "ds_read_b128 v[232:235], %[va] offset:2048\n\t" \
    "v_cvt_pk_bf16_f32 v219, v222, v223\n\t" \
    "v_mfma_f32_16x16x32_bf16 v[212:215], v[236:239], v[152:155], v[212:215]\n\t" \
    "buffer_store_dwordx4 v[216:219], %[l16], %[rd], %[so] offen sc1\n\t" \
    "s_waitcnt lgkmcnt(5)\n\t" \
    "v_mfma_f32_16x16x32_bf16 v[200:203], v[240:243], v[132:135], v[200:203]\n\t" \
    "ds_read_b128 v[236:239], %[va] offset:8192\n\t" \
    "s_add_u32 %[so], %[so], 0x8000\n\t" \
    "v_mfma_f32_16x16x32_bf16 v[208:211], v[240:243], v[156:159], v[208:211]\n\t" \
    "s_waitcnt lgkmcnt(5)\n\t" \
    "v_mfma_f32_16x16x32_bf16 v[204:207], v[244:247], v[132:135], v[204:207]\n\t" \
    "ds_read_b128 v[240:243], %[va] offset:3072\n\t" \
    "v_mfma_f32_16x16x32_bf16 v[212:215], v[244:247], v[156:159], v[212:215]\n\t" \
    "s_waitcnt lgkmcnt(3)\n\t" \
    "v_mfma_f32_16x16x32_bf16 v[216:219], v[224:227], v[248:251], 0\n\t" \
    "ds_read_b128 v[244:247], %[va] offset:9216\n\t" \
    "v_mfma_f32_16x16x32_bf16 v[208:211], v[224:227], v[136:139], v[208:211]\n\t" \
    "v_mfma_f32_16x16x32_bf16 v[220:223], v[228:231], v[248:251], 0\n\t" \
    "ds_read_b128 v[224:227], %[va] offset:1024\n\t" \
    "v_mfma_f32_16x16x32_bf16 v[212:215], v[228:231], v[136:139], v[212:215]\n\t" \
    "v_cvt_pk_bf16_f32 v200, v200, v201\n\t" \
    "s_waitcnt lgkmcnt(4)\n\t" \
    "v_mfma_f32_16x16x32_bf16 v[216:219], v[232:235], v[160:163], v[216:219]\n\t" \
    "ds_read_b128 v[228:231], %[va] offset:7168\n\t" \
    "ds_read_b128 v[248:251], %[wl] offset:9216\n\t" \
    "v_cvt_pk_bf16_f32 v201, v202, v203\n\t" \
    "v_mfma_f32_16x16x32_bf16 v[208:211], v[232:235], v[144:147], v[208:211]\n\t" \
    "v_cvt_pk_bf16_f32 v202, v204, v205\n\t" \
    "s_waitcnt lgkmcnt(5)\n\t" \
    "v_mfma_f32_16x16x32_bf16 v[220:223], v[236:239], v[160:163], v[220:223]\n\t" \
    "ds_read_b128 v[232:235], %[va] offset:4096\n\t" \
    "v_cvt_pk_bf16_f32 v203, v206, v207\n\t" \
    "v_mfma_f32_16x16x32_bf16 v[212:215], v[236:239], v[144:147], v[212:215]\n\t" \
    "buffer_store_dwordx4 v[200:203], %[l16], %[rd], %[so] offen sc1\n\t" \
    "s_waitcnt lgkmcnt(5)\n\t" \
    "v_mfma_f32_16x16x32_bf16 v[216:219], v[240:243], v[164:167], v[216:219]\n\t" \
    "ds_read_b128 v[236:239], %[va] offset:10240\n\t" \
    "s_add_u32 %[so], %[so], 0x8000\n\t" \
    "v_mfma_f32_16x16x32_bf16 v[208:211], v[240:243], v[148:151], v[208:211]\n\t" \
    "s_waitcnt lgkmcnt(5)\n\t" \
    "v_mfma_f32_16x16x32_bf16 v[220:223], v[244:247], v[164:167], v[220:223]\n\t" \
    "ds_read_b128 v[240:243], %[va] offset:5120\n\t" \
    "v_mfma_f32_16x16x32_bf16 v[212:215], v[244:247], v[148:151], v[212:215]\n\t" \
    "s_waitcnt lgkmcnt(3)\n\t" \
    "v_mfma_f32_16x16x32_bf16 v[216:219], v[224:227], v[248:251], v[216:219]\n\t" \
    "ds_read_b128 v[244:247], %[va] offset:11264\n\t" \
    "v_mfma_f32_16x16x32_bf16 v[200:203], v[224:227], v[180:183], 0\n\t" \
    "v_mfma_f32_16x16x32_bf16 v[220:223], v[228:231], v[248:251], v[220:223]\n\t" \
    "ds_read_b128 v[224:227], %[va] offset:0\n\t" \
    "v_mfma_f32_16x16x32_bf16 v[204:207], v[228:231], v[180:183], 0\n\t" \
    "v_cvt_pk_bf16_f32 v208, v208, v209\n\t" \
    "s_waitcnt lgkmcnt(4)\n\t" \
    "v_mfma_f32_16x16x32_bf16 v[216:219], v[232:235], v[168:171], v[216:219]\n\t" \
    "ds_read_b128 v[228:231], %[va] offset:6144\n\t" \
    "v_cvt_pk_bf16_f32 v209, v210, v211\n\t" \
    "v_mfma_f32_16x16x32_bf16 v[200:203], v[232:235], v[192:195], v[200:203]\n\t" \
    "v_cvt_pk_bf16_f32 v210, v212, v213\n\t" \
    "s_waitcnt lgkmcnt(4)\n\t" \
    "v_mfma_f32_16x16x32_bf16 v[220:223], v[236:239], v[168:171], v[220:223]\n\t" \
    "ds_read_b128 v[232:235], %[va] offset:2048\n\t" \
    "v_cvt_pk_bf16_f32 v211, v214, v215\n\t" \
    "v_mfma_f32_16x16x32_bf16 v[204:207], v[236:239], v[192:195], v[204:207]\n\t" \
    "buffer_store_dwordx4 v[208:211], %[l16], %[rd], %[so] offen sc1\n\t" \
    "s_waitcnt lgkmcnt(4)\n\t" \
    "v_mfma_f32_16x16x32_bf16 v[216:219], v[240:243], v[172:175], v[216:219]\n\t" \
    "ds_read_b128 v[236:239], %[va] offset:8192\n\t" \
    "s_add_u32 %[so], %[so], 0x8000\n\t" \
    "v_mfma_f32_16x16x32_bf16 v[200:203], v[240:243], v[196:199], v[200:203]\n\t" \
    "s_waitcnt lgkmcnt(4)\n\t" \
    "v_mfma_f32_16x16x32_bf16 v[220:223], v[244:247], v[172:175], v[220:223]\n\t" \
    "ds_read_b128 v[240:243], %[va] offset:3072\n\t" \
    "v_mfma_f32_16x16x32_bf16 v[204:207], v[244:247], v[196:199], v[204:207]\n\t" \
    "s_barrier \n\t" \
    "s_waitcnt lgkmcnt(4)\n\t" \
    "v_mfma_f32_16x16x32_bf16 v[200:203], v[224:227], v[176:179], v[200:203]\n\t" \
    "ds_read_b128 v[244:247], %[va] offset:9216\n\t" \
    "s_waitcnt lgkmcnt(4)\n\t" \
    "v_mfma_f32_16x16x32_bf16 v[204:207], v[228:231], v[176:179], v[204:207]\n\t" \
    "s_waitcnt lgkmcnt(3)\n\t" \
    "v_mfma_f32_16x16x32_bf16 v[200:203], v[232:235], v[184:187], v[200:203]\n\t" \
    "s_waitcnt lgkmcnt(2)\n\t" \
    "v_mfma_f32_16x16x32_bf16 v[204:207], v[236:239], v[184:187], v[204:207]\n\t" \
    "v_cvt_pk_bf16_f32 v216, v216, v217\n\t" \
    "s_waitcnt lgkmcnt(1)\n\t" \
    "v_mfma_f32_16x16x32_bf16 v[200:203], v[240:243], v[188:191], v[200:203]\n\t" \
    "v_cvt_pk_bf16_f32 v217, v218, v219\n\t" \
    "s_waitcnt lgkmcnt(0)\n\t" \
    "v_mfma_f32_16x16x32_bf16 v[204:207], v[244:247], v[188:191], v[204:207]\n\t" \
    "v_cvt_pk_bf16_f32 v218, v220, v221\n\t" \
    "v_cvt_pk_bf16_f32 v219, v222, v223\n\t" \
    "buffer_store_dwordx4 v[216:219], %[l16], %[rd], %[so] offen sc1\n\t" \
    "s_add_u32 %[so], %[so], 0x8000\n\t" \
    "s_nop 0\n\t" \
    "v_cvt_pk_bf16_f32 v200, v200, v201\n\t" \
    "v_cvt_pk_bf16_f32 v201, v202, v203\n\t" \
    "s_nop 1\n\t" \
    "v_cvt_pk_bf16_f32 v202, v204, v205\n\t" \
    "v_cvt_pk_bf16_f32 v203, v206, v207\n\t" \
    "buffer_store_dwordx4 v[200:203], %[l16], %[rd], %[so] offen sc1\n\t" \
    "s_add_u32 %[so], %[so], 0x8000\n\t" \
    "s_nop 1\n\t"
#define ASM_CRIT2 \
    "s_waitcnt lgkmcnt(0)\n\t" \
    "s_barrier\n\t" ASM_ST \
    "ds_read_b128 v[204:207], v200 offset:11264\n\t" \
    "ds_read_b128 v[208:211], v200 offset:12288\n\t" \
    "ds_read_b128 v[200:203], v200 offset:10240\n\t" \
    "s_waitcnt lgkmcnt(2)\n\t" \
    "v_smfmac_f32_16x16x64_bf16 v[224:227], v[204:207], v[24:31], %[sidx]\n\t" \
    "v_smfmac_f32_16x16x64_bf16 v[220:223], v[204:207], v[0:7], %[sidx]\n\t" \
    "s_waitcnt lgkmcnt(1)\n\t" \
    "v_smfmac_f32_16x16x64_bf16 v[224:227], v[208:211], v[32:39], %[sidx]\n\t" \
    "v_smfmac_f32_16x16x64_bf16 v[220:223], v[208:211], v[8:15], %[sidx]\n\t" \
    "s_waitcnt lgkmcnt(0)\n\t" \
    "v_smfmac_f32_16x16x64_bf16 v[224:227], v[200:203], v[16:23], %[sidx]\n\t" \
    "v_smfmac_f32_16x16x64_bf16 v[220:223], v[200:203], v[212:219], %[sidx]\n\t" \
    "ds_read_b128 v[212:215], %[wl] offset:2048\n\t" \
    "ds_read_b128 v[216:219], %[wl] offset:3072\n\t" \
    "v_smfmac_f32_16x16x64_bf16 v[232:235], v[204:207], v[64:71], %[sidx]\n\t" \
    "v_smfmac_f32_16x16x64_bf16 v[228:231], v[204:207], v[40:47], %[sidx]\n\t" \
    "v_smfmac_f32_16x16x64_bf16 v[232:235], v[208:211], v[72:79], %[sidx]\n\t" \
    "v_permlane32_swap_b32 v220, v224\n\t" \
    "v_permlane32_swap_b32 v221, v225\n\t" \
    "v_permlane32_swap_b32 v222, v226\n\t" \
    "v_smfmac_f32_16x16x64_bf16 v[228:231], v[208:211], v[48:55], %[sidx]\n\t" \
    "v_permlane32_swap_b32 v223, v227\n\t" \
    "v_add_f32 v220, v220, v224\n\t" \
    "v_add_f32 v221, v221, v225\n\t" \
    "v_smfmac_f32_16x16x64_bf16 v[232:235], v[200:203], v[56:63], %[sidx]\n\t" \
    "v_add_f32 v222, v222, v226\n\t" \
    "v_add_f32 v223, v223, v227\n\t" \
    "v_cvt_pk_bf16_f32 v226, v220, v221\n\t" \
    "s_waitcnt lgkmcnt(0)\n\t" \
    "v_smfmac_f32_16x16x64_bf16 v[228:231], v[200:203], v[212:219], %[sidx]\n\t" \
    "ds_read_b128 v[212:215], %[wl] offset:4096\n\t" \
    "ds_read_b128 v[216:219], %[wl] offset:5120\n\t" \
    "v_cvt_pk_bf16_f32 v227, v222, v223\n\t" \
    "v_min_u32 v226, -2, v226\n\t" \
    "v_min_u32 v227, -2, v227\n\t" \
    "v_smfmac_f32_16x16x64_bf16 v[240:243], v[204:207], v[104:111], %[sidx]\n\t" \
    "v_smfmac_f32_16x16x64_bf16 v[236:239], v[204:207], v[80:87], %[sidx]\n\t" \
    "v_smfmac_f32_16x16x64_bf16 v[240:243], v[208:211], v[112:119], %[sidx]\n\t" \
    "v_permlane32_swap_b32 v228, v232\n\t" \
    "v_permlane32_swap_b32 v229, v233\n\t" \
    "v_permlane32_swap_b32 v230, v234\n\t" \
    "v_smfmac_f32_16x16x64_bf16 v[236:239], v[208:211], v[88:95], %[sidx]\n\t" \
    "v_permlane32_swap_b32 v231, v235\n\t" \
    "v_add_f32 v228, v228, v232\n\t" \
    "v_add_f32 v229, v229, v233\n\t" \
    "v_smfmac_f32_16x16x64_bf16 v[240:243], v[200:203], v[96:103], %[sidx]\n\t" \
    "v_add_f32 v230, v230, v234\n\t" \
    "v_add_f32 v231, v231, v235\n\t" \
    "v_cvt_pk_bf16_f32 v228, v228, v229\n\t" \
    "s_waitcnt lgkmcnt(0)\n\t" \
    "v_smfmac_f32_16x16x64_bf16 v[236:239], v[200:203], v[212:219], %[sidx]\n\t" \
    "ds_read_b128 v[212:215], %[wl] offset:6144\n\t" \
    "ds_read_b128 v[216:219], %[wl] offset:7168\n\t" \
    "v_cvt_pk_bf16_f32 v229, v230, v231\n\t" \
    "v_min_u32 v228, -2, v228\n\t" \
    "v_min_u32 v229, -2, v229\n\t" \
    "s_nop 1\n\t" \
    "v_permlane16_swap_b32 v226, v228\n\t" \
    "v_permlane16_swap_b32 v227, v229\n\t" \
    "buffer_store_dwordx4 v[226:229], %[vo], %[rr], %[so] offen sc1\n\t" \
    "s_add_u32 %[so], %[so], 0x8000\n\t" \
    "v_smfmac_f32_16x16x64_bf16 v[248:251], v[204:207], v[144:151], %[sidx]\n\t" \
    "v_mov_b64 v[220:221], 0\n\t" \
    "v_mov_b64 v[222:223], 0\n\t" \
    "v_smfmac_f32_16x16x64_bf16 v[244:247], v[204:207], v[120:127], %[sidx]\n\t" \
    "v_mov_b64 v[224:225], 0\n\t" \
    "v_mov_b64 v[226:227], 0\n\t" \
    "v_smfmac_f32_16x16x64_bf16 v[248:251], v[208:211], v[152:159], %[sidx]\n\t" \
    "v_permlane32_swap_b32 v236, v240\n\t" \
    "v_permlane32_swap_b32 v237, v241\n\t" \
    "v_permlane32_swap_b32 v238, v242\n\t" \
    "v_smfmac_f32_16x16x64_bf16 v[244:247], v[208:211], v[128:135], %[sidx]\n\t" \
    "v_permlane32_swap_b32 v239, v243\n\t" \
    "v_add_f32 v236, v236, v240\n\t" \
    "v_add_f32 v237, v237, v241\n\t" \
    "v_smfmac_f32_16x16x64_bf16 v[248:251], v[200:203], v[136:143], %[sidx]\n\t" \
    "v_add_f32 v238, v238, v242\n\t" \
    "v_add_f32 v239, v239, v243\n\t" \
    "v_cvt_pk_bf16_f32 v242, v236, v237\n\t" \
    "s_waitcnt lgkmcnt(0)\n\t" \
    "v_smfmac_f32_16x16x64_bf16 v[244:247], v[200:203], v[212:219], %[sidx]\n\t" \
    "ds_read_b128 v[212:215], %[wl] offset:8192\n\t" \
    "ds_read_b128 v[216:219], %[wl] offset:9216\n\t" \
    "v_cvt_pk_bf16_f32 v243, v238, v239\n\t" \
    "v_min_u32 v242, -2, v242\n\t" \
    "v_min_u32 v243, -2, v243\n\t" \
    "v_smfmac_f32_16x16x64_bf16 v[224:227], v[204:207], v[184:191], %[sidx]\n\t" \
    "v_and_b32 v232, 0x3fff, %[vo]\n\t" \
    "v_lshrrev_b32 v233, 14, %[vo]\n\t" \
    "v_smfmac_f32_16x16x64_bf16 v[220:223], v[204:207], v[160:167], %[sidx]\n\t" \
    "v_lshl_add_u32 v232, v233, 3, v232\n\t" \
    "v_permlane32_swap_b32 v244, v248\n\t" \
    "v_smfmac_f32_16x16x64_bf16 v[224:227], v[208:211], v[192:199], %[sidx]\n\t" \
    "v_permlane32_swap_b32 v245, v249\n\t" \
    "v_permlane32_swap_b32 v246, v250\n\t" \
    "v_permlane32_swap_b32 v247, v251\n\t" \
    "v_smfmac_f32_16x16x64_bf16 v[220:223], v[208:211], v[168:175], %[sidx]\n\t" \
    "v_add_f32 v244, v244, v248\n\t" \
    "v_add_f32 v245, v245, v249\n\t" \
    "v_add_f32 v246, v246, v250\n\t" \
    "v_smfmac_f32_16x16x64_bf16 v[224:227], v[200:203], v[176:183], %[sidx]\n\t" \
    "v_add_f32 v247, v247, v251\n\t" \
    "v_cvt_pk_bf16_f32 v244, v244, v245\n\t" \
    "v_cvt_pk_bf16_f32 v245, v246, v247\n\t" \
    "s_waitcnt lgkmcnt(0)\n\t" \
    "v_smfmac_f32_16x16x64_bf16 v[220:223], v[200:203], v[212:219], %[sidx]\n\t" \
    "v_min_u32 v244, -2, v244\n\t" \
    "v_min_u32 v245, -2, v245\n\t" \
    "s_nop 1\n\t" \
    "v_permlane16_swap_b32 v242, v244\n\t" \
    "v_permlane16_swap_b32 v243, v245\n\t" \
    "buffer_store_dwordx4 v[242:245], %[vo], %[rr], %[so] offen sc1\n\t" \
    "s_add_u32 %[so], %[so], 0x8000\n\t" \
    "s_nop 0\n\t" \
    "v_permlane32_swap_b32 v220, v224\n\t" \
    "v_permlane32_swap_b32 v221, v225\n\t" \
    "v_permlane32_swap_b32 v222, v226\n\t" \
    "v_permlane32_swap_b32 v223, v227\n\t" \
    "v_add_f32 v220, v220, v224\n\t" \
    "v_add_f32 v221, v221, v225\n\t" \
    "v_add_f32 v222, v222, v226\n\t" \
    "v_add_f32 v223, v223, v227\n\t" \
    "v_cvt_pk_bf16_f32 v220, v220, v221\n\t" \
    "v_cvt_pk_bf16_f32 v221, v222, v223\n\t" \
    "v_min_u32 v220, -2, v220\n\t" \
    "v_min_u32 v221, -2, v221\n\t" \
    "buffer_store_dwordx2 v[220:221], v232, %[rr], %[so] offen sc1\n\t" \
    "s_barrier \n\t"
#define ASM_P4_0 \
    "ds_read_b128 v[200:203], %[qa] offset:0\n\t" \
    "ds_read_b128 v[204:207], %[qa] offset:1024\n\t" \
    "ds_read_b128 v[208:211], %[qa] offset:2048\n\t" \
    "ds_read_b128 v[212:215], %[qa] offset:3072\n\t" \
    "ds_read_b128 v[216:219], %[qa] offset:4096\n\t" \
    "ds_read_b128 v[220:223], %[qa] offset:5120\n\t" \
    "ds_read_b128 v[224:227], %[qa] offset:12288\n\t" \
    "ds_read_b128 v[228:231], %[qa] offset:13312\n\t" \
    "ds_read_b128 v[232:235], %[qa] offset:14336\n\t" \
    "ds_read_b128 v[236:239], %[qa] offset:15360\n\t" \
    "s_waitcnt lgkmcnt(0)\n\t" \
    "s_barrier\n\t" ASM_ST \
    "v_mfma_f32_16x16x32_bf16 v[240:243], v[224:227], v[200:203], v[240:243]\n\t" \
    "ds_read_b128 v[224:227], %[qa] offset:16384\n\t" \
    "v_mfma_f32_16x16x32_bf16 v[240:243], v[228:231], v[204:207], v[240:243]\n\t" \
    "ds_read_b128 v[228:231], %[qa] offset:17408\n\t" \
    "v_mfma_f32_16x16x32_bf16 v[240:243], v[232:235], v[208:211], v[240:243]\n\t" \
    "ds_read_b128 v[232:235], %[km] offset:0\n\t" \
    "v_mfma_f32_16x16x32_bf16 v[240:243], v[236:239], v[212:215], v[240:243]\n\t" \
    "ds_read_b128 v[236:239], %[km] offset:512\n\t" \
    "s_waitcnt lgkmcnt(3)\n\t" \
    "v_mfma_f32_16x16x32_bf16 v[240:243], v[224:227], v[216:219], v[240:243]\n\t" \
    "ds_read_b128 v[224:227], %[km] offset:1024\n\t" \
    "s_waitcnt lgkmcnt(3)\n\t" \
    "v_mfma_f32_16x16x32_bf16 v[240:243], v[228:231], v[220:223], v[240:243]\n\t" \
    "ds_read_b128 v[228:231], %[km] offset:1536\n\t" \
    "s_waitcnt lgkmcnt(3)\n\t" \
    "v_mfma_f32_16x16x32_bf16 v[248:251], v[232:235], v[200:203], 0\n\t" \
    "ds_read_b128 v[232:235], %[km] offset:2048\n\t" \
    "s_waitcnt lgkmcnt(3)\n\t" \
    "v_mfma_f32_16x16x32_bf16 v[248:251], v[236:239], v[204:207], v[248:251]\n\t" \
    "ds_read_b128 v[236:239], %[km] offset:2560\n\t" \
    "s_waitcnt lgkmcnt(3)\n\t" \
    "v_mfma_f32_16x16x32_bf16 v[248:251], v[224:227], v[208:211], v[248:251]\n\t" \
    "s_waitcnt lgkmcnt(2)\n\t" \
    "v_mfma_f32_16x16x32_bf16 v[248:251], v[228:231], v[212:215], v[248:251]\n\t" \
    "s_waitcnt lgkmcnt(1)\n\t" \
    "v_mfma_f32_16x16x32_bf16 v[248:251], v[232:235], v[216:219], v[248:251]\n\t" \
    "s_waitcnt lgkmcnt(0)\n\t" \
    "v_mfma_f32_16x16x32_bf16 v[248:251], v[236:239], v[220:223], v[248:251]\n\t" \
    "v_max3_f32 v224, v240, v241, v242\n\t" \
    "v_mov_b32 v225, v243\n\t" \
    "v_lshlrev_b32 v201, 23, %[qa]\n\t" \
    "v_and_b32 v201, 0x80000000, v201\n\t" \
    "v_and_b32 v200, 0x3ff, %[qa]\n\t" \
    "v_or_b32 v200, v201, v200\n\t" \
    "s_nop 1\n\t" \
    "v_max3_f32 v224, v224, v248, v249\n\t" \
    "v_max3_f32 v225, v225, v250, v251\n\t" \
    "v_max_f32 v224, v224, v225\n\t" \
    "v_mov_b32 v225, v224\n\t" \
    "s_nop 1\n\t" \
    "v_permlane16_swap_b32 v224, v225\n\t" \
    "v_max_f32 v224, v224, v225\n\t" \
    "v_mov_b32 v225, v224\n\t" \
    "s_nop 1\n\t" \
    "v_permlane32_swap_b32 v224, v225\n\t" \
    "v_max_f32 v224, v224, v225\n\t" \
    "v_mul_f32 v224, %[scl], v224\n\t" \
    "v_fma_f32 v240, v240, %[scl], -v224\n\t" \
    "v_fma_f32 v241, v241, %[scl], -v224\n\t" \
    "v_exp_f32 v240, v240\n\t" \
    "v_fma_f32 v242, v242, %[scl], -v224\n\t" \
    "v_exp_f32 v241, v241\n\t" \
    "v_fma_f32 v243, v243, %[scl], -v224\n\t" \
    "v_exp_f32 v242, v242\n\t" \
    "v_fma_f32 v248, v248, %[scl], -v224\n\t" \
    "v_exp_f32 v243, v243\n\t" \
    "v_fma_f32 v249, v249, %[scl], -v224\n\t" \
    "v_exp_f32 v248, v248\n\t" \
    "v_fma_f32 v250, v250, %[scl], -v224\n\t" \
    "v_exp_f32 v249, v249\n\t" \
    "v_fma_f32 v251, v251, %[scl], -v224\n\t" \
    "v_exp_f32 v250, v250\n\t" \
    "v_exp_f32 v251, v251\n\t" \
    "v_add_f32 v224, v240, v241\n\t" \
    "v_add_f32 v225, v242, v243\n\t" \
    "v_add_f32 v232, v248, v249\n\t" \
    "v_add_f32 v233, v250, v251\n\t" \
    "v_add_f32 v224, v224, v225\n\t" \
    "v_add_f32 v232, v232, v233\n\t" \
    "v_fma_f32 v224, v232, 0.5, v224\n\t" \
    "v_mov_b32 v225, v224\n\t" \
    "s_nop 1\n\t" \
    "v_permlane16_swap_b32 v224, v225\n\t" \
    "v_add_f32 v224, v224, v225\n\t" \
    "v_mov_b32 v225, v224\n\t" \
    "s_nop 1\n\t" \
    "v_permlane32_swap_b32 v224, v225\n\t" \
    "v_add_f32 v224, v224, v225\n\t" \
    "v_rcp_f32 v224, v224\n\t" \
    "s_nop 0\n\t" \
    "v_mul_f32 v240, v240, v224\n\t" \
    "v_mul_f32 v241, v241, v224\n\t" \
    "v_mul_f32 v242, v242, v224\n\t" \
    "v_mul_f32 v243, v243, v224\n\t" \
    "v_mul_f32 v248, v248, v224\n\t" \
    "v_mul_f32 v249, v249, v224\n\t" \
    "v_mul_f32 v250, v250, v224\n\t" \
    "v_mul_f32 v251, v251, v224\n\t" \
    "v_cvt_pk_bf16_f32 v228, |v240|, |v241|\n\t" \
    "v_cvt_pk_bf16_f32 v229, |v242|, |v243|\n\t" \
    "v_cvt_pk_bf16_f32 v232, |v248|, |v249|\n\t" \
    "v_cvt_pk_bf16_f32 v233, |v250|, |v251|\n\t" \
    "v_permlane16_swap_b32 v228, v230\n\t" \
    "v_permlane16_swap_b32 v229, v231\n\t" \
    "buffer_store_dwordx4 v[228:231], v200, %[rr], %[so] offen sc1\n\t" \
    "buffer_store_dwordx2 v[232:233], %[pm], %[rr], %[so] offen sc1\n\t" \
    "s_nop 1\n\t"
#define ASM_P4_1 \
    "ds_read_b128 v[200:203], %[qa] offset:0\n\t" \
    "ds_read_b128 v[204:207], %[qa] offset:1024\n\t" \
    "ds_read_b128 v[208:211], %[qa] offset:2048\n\t" \
    "ds_read_b128 v[212:215], %[qa] offset:3072\n\t" \
    "ds_read_b128 v[216:219], %[qa] offset:4096\n\t" \
    "ds_read_b128 v[220:223], %[qa] offset:5120\n\t" \
    "ds_read_b128 v[224:227], %[qa] offset:12288\n\t" \
    "ds_read_b128 v[228:231], %[qa] offset:13312\n\t" \
    "ds_read_b128 v[232:235], %[qa] offset:14336\n\t" \
    "ds_read_b128 v[236:239], %[qa] offset:15360\n\t" \
    "s_waitcnt lgkmcnt(0)\n\t" \
    "s_barrier\n\t" ASM_ST \
    "v_mfma_f32_16x16x32_bf16 v[240:243], v[224:227], v[200:203], v[240:243]\n\t" \
    "ds_read_b128 v[224:227], %[qa] offset:16384\n\t" \
    "v_mfma_f32_16x16x32_bf16 v[240:243], v[228:231], v[204:207], v[240:243]\n\t" \
    "ds_read_b128 v[228:231], %[qa] offset:17408\n\t" \
    "v_mfma_f32_16x16x32_bf16 v[240:243], v[232:235], v[208:211], v[240:243]\n\t" \
    "ds_read_b128 v[232:235], %[qa] offset:6144\n\t" \
    "v_mfma_f32_16x16x32_bf16 v[240:243], v[236:239], v[212:215], v[240:243]\n\t" \
    "ds_read_b128 v[236:239], %[qa] offset:7168\n\t" \
    "s_waitcnt lgkmcnt(3)\n\t" \
    "v_mfma_f32_16x16x32_bf16 v[240:243], v[224:227], v[216:219], v[240:243]\n\t" \
    "ds_read_b128 v[224:227], %[qa] offset:8192\n\t" \
    "s_waitcnt lgkmcnt(3)\n\t" \
    "v_mfma_f32_16x16x32_bf16 v[240:243], v[228:231], v[220:223], v[240:243]\n\t" \
    "ds_read_b128 v[228:231], %[qa] offset:9216\n\t" \
    "s_waitcnt lgkmcnt(3)\n\t" \
    "v_mfma_f32_16x16x32_bf16 v[244:247], v[232:235], v[200:203], 0\n\t" \
    "ds_read_b128 v[232:235], %[qa] offset:10240\n\t" \
    "s_waitcnt lgkmcnt(3)\n\t" \
    "v_mfma_f32_16x16x32_bf16 v[244:247], v[236:239], v[204:207], v[244:247]\n\t" \
    "ds_read_b128 v[236:239], %[qa] offset:11264\n\t" \
    "s_waitcnt lgkmcnt(3)\n\t" \
    "v_mfma_f32_16x16x32_bf16 v[244:247], v[224:227], v[208:211], v[244:247]\n\t" \
    "ds_read_b128 v[224:227], %[km] offset:0\n\t" \
    "s_waitcnt lgkmcnt(3)\n\t" \
    "v_mfma_f32_16x16x32_bf16 v[244:247], v[228:231], v[212:215], v[244:247]\n\t" \
    "ds_read_b128 v[228:231], %[km] offset:512\n\t" \
    "s_waitcnt lgkmcnt(3)\n\t" \
    "v_mfma_f32_16x16x32_bf16 v[244:247], v[232:235], v[216:219], v[244:247]\n\t" \
    "ds_read_b128 v[232:235], %[km] offset:1024\n\t" \
    "s_waitcnt lgkmcnt(3)\n\t" \
    "v_mfma_f32_16x16x32_bf16 v[244:247], v[236:239], v[220:223], v[244:247]\n\t" \
    "ds_read_b128 v[236:239], %[km] offset:1536\n\t" \
    "s_waitcnt lgkmcnt(3)\n\t" \
    "v_mfma_f32_16x16x32_bf16 v[248:251], v[224:227], v[200:203], 0\n\t" \
    "ds_read_b128 v[224:227], %[km] offset:2048\n\t" \
    "s_waitcnt lgkmcnt(3)\n\t" \
    "v_mfma_f32_16x16x32_bf16 v[248:251], v[228:231], v[204:207], v[248:251]\n\t" \
    "ds_read_b128 v[228:231], %[km] offset:2560\n\t" \
    "s_waitcnt lgkmcnt(3)\n\t" \
    "v_mfma_f32_16x16x32_bf16 v[248:251], v[232:235], v[208:211], v[248:251]\n\t" \
    "s_waitcnt lgkmcnt(2)\n\t" \
    "v_mfma_f32_16x16x32_bf16 v[248:251], v[236:239], v[212:215], v[248:251]\n\t" \
    "s_waitcnt lgkmcnt(1)\n\t" \
    "v_mfma_f32_16x16x32_bf16 v[248:251], v[224:227], v[216:219], v[248:251]\n\t" \
    "s_waitcnt lgkmcnt(0)\n\t" \
    "v_mfma_f32_16x16x32_bf16 v[248:251], v[228:231], v[220:223], v[248:251]\n\t" \
    "v_max3_f32 v224, v240, v241, v242\n\t" \
    "v_max3_f32 v225, v244, v245, v246\n\t" \
    "v_max3_f32 v224, v224, v243, v247\n\t" \
    "v_and_b32 v200, 0x3ff, %[qa]\n\t" \
    "s_nop 3\n\t" \
    "v_max3_f32 v224, v224, v248, v249\n\t" \
    "v_max3_f32 v225, v225, v250, v251\n\t" \
    "v_max_f32 v224, v224, v225\n\t" \
    "v_mov_b32 v225, v224\n\t" \
    "s_nop 1\n\t" \
    "v_permlane16_swap_b32 v224, v225\n\t" \
    "v_max_f32 v224, v224, v225\n\t" \
    "v_mov_b32 v225, v224\n\t" \
    "s_nop 1\n\t" \
    "v_permlane32_swap_b32 v224, v225\n\t" \
    "v_max_f32 v224, v224, v225\n\t" \
    "v_mul_f32 v224, %[scl], v224\n\t" \
    "v_fma_f32 v240, v240, %[scl], -v224\n\t" \
    "v_fma_f32 v241, v241, %[scl], -v224\n\t" \
    "v_exp_f32 v240, v240\n\t" \
    "v_fma_f32 v242, v242, %[scl], -v224\n\t" \
    "v_exp_f32 v241, v241\n\t" \
    "v_fma_f32 v243, v243, %[scl], -v224\n\t" \
    "v_exp_f32 v242, v242\n\t" \
    "v_fma_f32 v244, v244, %[scl], -v224\n\t" \
    "v_exp_f32 v243, v243\n\t" \
    "v_fma_f32 v245, v245, %[scl], -v224\n\t" \
    "v_exp_f32 v244, v244\n\t" \
    "v_fma_f32 v246, v246, %[scl], -v224\n\t" \
    "v_exp_f32 v245, v245\n\t" \
    "v_fma_f32 v247, v247, %[scl], -v224\n\t" \
    "v_exp_f32 v246, v246\n\t" \
    "v_fma_f32 v248, v248, %[scl], -v224\n\t" \
    "v_exp_f32 v247, v247\n\t" \
    "v_fma_f32 v249, v249, %[scl], -v224\n\t" \
    "v_exp_f32 v248, v248\n\t" \
    "v_fma_f32 v250, v250, %[scl], -v224\n\t" \
    "v_exp_f32 v249, v249\n\t" \
    "v_fma_f32 v251, v251, %[scl], -v224\n\t" \
    "v_exp_f32 v250, v250\n\t" \
    "v_exp_f32 v251, v251\n\t" \
    "v_add_f32 v224, v240, v241\n\t" \
    "v_add_f32 v225, v242, v243\n\t" \
    "v_add_f32 v226, v244, v245\n\t" \
    "v_add_f32 v227, v246, v247\n\t" \
    "v_add_f32 v232, v248, v249\n\t" \
    "v_add_f32 v233, v250, v251\n\t" \
    "v_add_f32 v224, v224, v225\n\t" \
    "v_add_f32 v226, v226, v227\n\t" \
    "v_add_f32 v232, v232, v233\n\t" \
    "v_add_f32 v224, v224, v226\n\t" \
    "v_fma_f32 v224, v232, 0.5, v224\n\t" \
    "v_mov_b32 v225, v224\n\t" \
    "s_nop 1\n\t" \
    "v_permlane16_swap_b32 v224, v225\n\t" \
    "v_add_f32 v224, v224, v225\n\t" \
    "v_mov_b32 v225, v224\n\t" \
    "s_nop 1\n\t" \
    "v_permlane32_swap_b32 v224, v225\n\t" \
    "v_add_f32 v224, v224, v225\n\t" \
    "v_rcp_f32 v224, v224\n\t" \
    "s_nop 0\n\t" \
    "v_mul_f32 v240, v240, v224\n\t" \
    "v_mul_f32 v241, v241, v224\n\t" \
    "v_mul_f32 v242, v242, v224\n\t" \
    "v_mul_f32 v243, v243, v224\n\t" \
    "v_mul_f32 v244, v244, v224\n\t" \
    "v_mul_f32 v245, v245, v224\n\t" \
    "v_mul_f32 v246, v246, v224\n\t" \
    "v_mul_f32 v247, v247, v224\n\t" \
    "v_mul_f32 v248, v248, v224\n\t" \
    "v_mul_f32 v249, v249, v224\n\t" \
    "v_mul_f32 v250, v250, v224\n\t" \
    "v_mul_f32 v251, v251, v224\n\t" \
    "v_cvt_pk_bf16_f32 v230, |v240|, |v241|\n\t" \
    "v_cvt_pk_bf16_f32 v231, |v242|, |v243|\n\t" \
    "v_cvt_pk_bf16_f32 v228, |v244|, |v245|\n\t" \
    "v_cvt_pk_bf16_f32 v229, |v246|, |v247|\n\t" \
    "v_cvt_pk_bf16_f32 v232, |v248|, |v249|\n\t" \
    "v_cvt_pk_bf16_f32 v233, |v250|, |v251|\n\t" \
    "buffer_store_dwordx4 v[228:231], v200, %[rr], %[so] offen sc1\n\t" \
    "buffer_store_dwordx2 v[232:233], %[pm], %[rr], %[so] offen sc1\n\t" \
    "s_nop 1\n\t"
#define ASM_RCRIT \
    "s_movk_i32 %[n], 0x400\n\t" \
    ".Lfa%=:\n\t" \
    "v_max_u32 v196, v180, v181\n\t" \
    "v_max3_u32 v196, v182, v183, v196\n\t" \
    "v_cmp_eq_u32 vcc, -1, v196\n\t" \
    "s_cbranch_vccz .Lha%=\n\t" \
    "buffer_load_dwordx4 v[180:183], %[ov], %[rs], %[sv] offen sc1\n\t" \
    "s_waitcnt vmcnt(12)\n\t" \
    "v_max_u32 v196, v180, v181\n\t" \
    "v_max3_u32 v196, v182, v183, v196\n\t" \
    "v_cmp_eq_u32 vcc, -1, v196\n\t" \
    "s_cbranch_vccz .Lha%=\n\t" \
    "s_nop 7\n\t" \
    "v_max_u32 v196, v180, v181\n\t" \
    "v_max3_u32 v196, v182, v183, v196\n\t" \
    "v_cmp_eq_u32 vcc, -1, v196\n\t" \
    "s_cbranch_vccz .Lha%=\n\t" \
    "s_nop 7\n\t" \
    "v_max_u32 v196, v180, v181\n\t" \
    "v_max3_u32 v196, v182, v183, v196\n\t" \
    "v_cmp_eq_u32 vcc, -1, v196\n\t" \
    "s_cbranch_vccz .Lha%=\n\t" \
    "s_nop 7\n\t" \
    "v_max_u32 v196, v180, v181\n\t" \
    "v_max3_u32 v196, v182, v183, v196\n\t" \
    "v_cmp_eq_u32 vcc, -1, v196\n\t" \
    "s_cbranch_vccz .Lha%=\n\t" \
    "s_nop 7\n\t" \
    "s_sub_u32 %[n], %[n], 1\n\t" \
    "s_cmp_lg_u32 %[n], 0\n\t" \
    "s_cbranch_scc1 .Lfa%=\n\t" \
    ".Lha%=:\n\t" \
    "s_cmp_eq_u32 %[n], 0\n\t" \
    "s_cbranch_scc1 .Lre%=\n\t" \
    ".Lfb%=:\n\t" \
    "s_mov_b32 %[a], 0\n\t" \
    "v_max_u32 v196, v184, v185\n\t" \
    "v_max3_u32 v196, v186, v187, v196\n\t" \
    "v_cmp_eq_u32 vcc, -1, v196\n\t" \
    "s_cbranch_vccz .Lgb0_%=\n\t" \
    "buffer_load_dwordx4 v[184:187], %[o0], %[rs], %[sp] offen sc1\n\t" \
    "s_mov_b32 %[a], 1\n\t" \
    ".Lgb0_%=:\n\t" \
    "v_max_u32 v196, v188, v189\n\t" \
    "v_max3_u32 v196, v190, v191, v196\n\t" \
    "v_cmp_eq_u32 vcc, -1, v196\n\t" \
    "s_cbranch_vccz .Lgb1_%=\n\t" \
    "buffer_load_dwordx4 v[188:191], %[o1], %[rs], %[sp] offen sc1\n\t" \
    "s_mov_b32 %[a], 1\n\t" \
    ".Lgb1_%=:\n\t" \
    "v_max_u32 v196, v192, v193\n\t" \
    "v_max3_u32 v196, v194, v195, v196\n\t" \
    "v_cmp_eq_u32 vcc, -1, v196\n\t" \
    "s_cbranch_vccz .Lgb2_%=\n\t" \
    "buffer_load_dwordx4 v[192:195], %[o2], %[rs], %[sp] offen sc1\n\t" \
    "s_mov_b32 %[a], 1\n\t" \
    ".Lgb2_%=:\n\t" \
    "s_cmp_eq_u32 %[a], 0\n\t" \
    "s_cbranch_scc1 .Lhb%=\n\t" \
    "s_waitcnt vmcnt(12)\n\t" \
    "v_max_u32 v196, v184, v185\n\t" \
    "v_max3_u32 v196, v186, v187, v196\n\t" \
    "v_max3_u32 v196, v188, v189, v196\n\t" \
    "v_max3_u32 v196, v190, v191, v196\n\t" \
    "v_max3_u32 v196, v192, v193, v196\n\t" \
    "v_max3_u32 v196, v194, v195, v196\n\t" \
    "v_cmp_eq_u32 vcc, -1, v196\n\t" \
    "s_cbranch_vccz .Lhb%=\n\t" \
    "s_nop 7\n\t" \
    "v_max_u32 v196, v184, v185\n\t" \
    "v_max3_u32 v196, v186, v187, v196\n\t" \
    "v_max3_u32 v196, v188, v189, v196\n\t" \
    "v_max3_u32 v196, v190, v191, v196\n\t" \
    "v_max3_u32 v196, v192, v193, v196\n\t" \
    "v_max3_u32 v196, v194, v195, v196\n\t" \
    "v_cmp_eq_u32 vcc, -1, v196\n\t" \
    "s_cbranch_vccz .Lhb%=\n\t" \
    "s_nop 7\n\t" \
    "v_max_u32 v196, v184, v185\n\t" \
    "v_max3_u32 v196, v186, v187, v196\n\t" \
    "v_max3_u32 v196, v188, v189, v196\n\t" \
    "v_max3_u32 v196, v190, v191, v196\n\t" \
    "v_max3_u32 v196, v192, v193, v196\n\t" \
    "v_max3_u32 v196, v194, v195, v196\n\t" \
    "v_cmp_eq_u32 vcc, -1, v196\n\t" \
    "s_cbranch_vccz .Lhb%=\n\t" \
    "s_nop 7\n\t" \
    "v_max_u32 v196, v184, v185\n\t" \
    "v_max3_u32 v196, v186, v187, v196\n\t" \
    "v_max3_u32 v196, v188, v189, v196\n\t" \
    "v_max3_u32 v196, v190, v191, v196\n\t" \
    "v_max3_u32 v196, v192, v193, v196\n\t" \
    "v_max3_u32 v196, v194, v195, v196\n\t" \
    "v_cmp_eq_u32 vcc, -1, v196\n\t" \
    "s_cbranch_vccz .Lhb%=\n\t" \
    "s_nop 7\n\t" \
    "v_max_u32 v196, v184, v185\n\t" \
    "v_max3_u32 v196, v186, v187, v196\n\t" \
    "v_max3_u32 v196, v188, v189, v196\n\t" \
    "v_max3_u32 v196, v190, v191, v196\n\t" \
    "v_max3_u32 v196, v192, v193, v196\n\t" \
    "v_max3_u32 v196, v194, v195, v196\n\t" \
    "v_cmp_eq_u32 vcc, -1, v196\n\t" \
    "s_cbranch_vccz .Lhb%=\n\t" \
    "s_nop 7\n\t" \
    "v_max_u32 v196, v184, v185\n\t" \
    "v_max3_u32 v196, v186, v187, v196\n\t" \
    "v_max3_u32 v196, v188, v189, v196\n\t" \
    "v_max3_u32 v196, v190, v191, v196\n\t" \
    "v_max3_u32 v196, v192, v193, v196\n\t" \
    "v_max3_u32 v196, v194, v195, v196\n\t" \
    "v_cmp_eq_u32 vcc, -1, v196\n\t" \
    "s_cbranch_vccz .Lhb%=\n\t" \
    "s_nop 7\n\t" \
    "v_max_u32 v196, v184, v185\n\t" \
    "v_max3_u32 v196, v186, v187, v196\n\t" \
    "v_max3_u32 v196, v188, v189, v196\n\t" \
    "v_max3_u32 v196, v190, v191, v196\n\t" \
    "v_max3_u32 v196, v192, v193, v196\n\t" \
    "v_max3_u32 v196, v194, v195, v196\n\t" \
    "v_cmp_eq_u32 vcc, -1, v196\n\t" \
    "s_cbranch_vccz .Lhb%=\n\t" \
    "s_nop 7\n\t" \
    "v_max_u32 v196, v184, v185\n\t" \
    "v_max3_u32 v196, v186, v187, v196\n\t" \
    "v_max3_u32 v196, v188, v189, v196\n\t" \
    "v_max3_u32 v196, v190, v191, v196\n\t" \
    "v_max3_u32 v196, v192, v193, v196\n\t" \
    "v_max3_u32 v196, v194, v195, v196\n\t" \
    "v_cmp_eq_u32 vcc, -1, v196\n\t" \
    "s_cbranch_vccz .Lhb%=\n\t" \
    "s_nop 7\n\t" \
    "s_sub_u32 %[n], %[n], 1\n\t" \
    "s_cmp_lg_u32 %[n], 0\n\t" \
    "s_cbranch_scc1 .Lfb%=\n\t" \
    "s_branch .Lre%=\n\t" \
    ".Lhb%=:\n\t" \
    ASM_ST2 \
    "v_mfma_f32_16x16x32_bf16 v[176:179], v[192:195], v[180:183], 0\n\t" \
    "v_smfmac_f32_16x16x64_bf16 v[176:179], v[184:187], v[160:167], %[sidx]\n\t" \
    "v_smfmac_f32_16x16x64_bf16 v[176:179], v[188:191], v[168:175], %[sidx]\n\t" \
    "s_cmp_eq_u32 %[pub], 0\n\t" \
    "s_nop 7\n\t" \
    "v_permlane32_swap_b32 v176, v178\n\t" \
    "v_permlane32_swap_b32 v177, v179\n\t" \
    "v_add_f32 v176, v176, v178\n\t" \
    "v_add_f32 v177, v177, v179\n\t" \
    "ds_write2_b32 %[rw], v176, v177 offset1:80\n\t" \
    "s_waitcnt lgkmcnt(0)\n\t" \
    "s_barrier\n\t" ASM_ST \
    "s_cbranch_scc1 .Lre%=\n\t" \
    "ds_read_b128 v[200:203], %[rd] offset:0\n\t" \
    "ds_read_b128 v[204:207], %[rd] offset:16\n\t" \
    "ds_read_b128 v[208:211], %[rd] offset:32\n\t" \
    "ds_read_b128 v[212:215], %[rd] offset:48\n\t" \
    "s_waitcnt lgkmcnt(2)\n\t" \
    "v_pk_add_f32 v[200:201], v[200:201], v[202:203]\n\t" \
    "v_pk_add_f32 v[204:205], v[204:205], v[206:207]\n\t" \
    "s_waitcnt lgkmcnt(0)\n\t" \
    "v_pk_add_f32 v[208:209], v[208:209], v[210:211]\n\t" \
    "v_pk_add_f32 v[212:213], v[212:213], v[214:215]\n\t" \
    "v_pk_add_f32 v[200:201], v[200:201], v[204:205]\n\t" \
    "v_pk_add_f32 v[208:209], v[208:209], v[212:213]\n\t" \
    "v_pk_add_f32 v[200:201], v[200:201], v[208:209]\n\t" \
    "v_cvt_pk_bf16_f32 v200, v200, v201\n\t" \
    "v_min_u32 v200, -2, v200\n\t" \
    "buffer_store_dword v200, %[oo], %[rs], %[so] offen sc1\n\t" \
    RPUB2 \
    ".Lre%=:\n\t"
#define ASM_RX \
    "s_cmp_eq_u32 %[fin], 0\n\t" \
    "s_cbranch_scc1 .Lx0%=\n\t" \
    "buffer_load_dwordx2 v[250:251], %[xo], %[rd], %[sx] offen sc1\n\t" \
    ".Lx0%=:\n\t" \
    "buffer_load_dwordx4 v[200:203], %[l16], %[rs], %[s0] offen offset:0 sc1\n\t" \
    "buffer_load_dwordx4 v[204:207], %[l16], %[rs], %[s0] offen offset:1024 sc1\n\t" \
    "buffer_load_dwordx4 v[208:211], %[l16], %[rs], %[s0] offen offset:2048 sc1\n\t" \
    "buffer_load_dwordx4 v[212:215], %[l16], %[rs], %[s0] offen offset:3072 sc1\n\t" \
    "buffer_load_dwordx4 v[216:219], %[l16], %[rs], %[s1] offen offset:0 sc1\n\t" \
    "buffer_load_dwordx4 v[220:223], %[l16], %[rs], %[s1] offen offset:1024 sc1\n\t" \
    "buffer_load_dwordx4 v[224:227], %[l16], %[rs], %[s1] offen offset:2048 sc1\n\t" \
    "buffer_load_dwordx4 v[228:231], %[l16], %[rs], %[s1] offen offset:3072 sc1\n\t" \
    "buffer_load_dword v248, %[fo], %[rf], %[fs] offen sc1\n\t" \
    "v_mov_b64 v[232:233], v[160:161]\n\t" \
    "v_mov_b64 v[234:235], v[164:165]\n\t" \
    "v_mov_b64 v[236:237], v[168:169]\n\t" \
    "v_mov_b64 v[238:239], v[172:173]\n\t" \
    "s_movk_i32 %[n], 0x200\n\t" \
    "s_nop 1\n\t" \
    "v_permlane16_swap_b32 v232, v234\n\t" \
    "v_permlane16_swap_b32 v233, v235\n\t" \
    "v_permlane16_swap_b32 v236, v238\n\t" \
    "v_permlane16_swap_b32 v237, v239\n\t" \
    "s_waitcnt vmcnt(8)\n\t" \
    "v_max_u32 v196, v200, v201\n\t" \
    "v_max3_u32 v196, v202, v203, v196\n\t" \
    "s_waitcnt vmcnt(7)\n\t" \
    "v_max3_u32 v196, v204, v205, v196\n\t" \
    "v_max3_u32 v196, v206, v207, v196\n\t" \
    "s_waitcnt vmcnt(6)\n\t" \
    "v_max3_u32 v196, v208, v209, v196\n\t" \
    "v_max3_u32 v196, v210, v211, v196\n\t" \
    "s_waitcnt vmcnt(5)\n\t" \
    "v_max3_u32 v196, v212, v213, v196\n\t" \
    "v_max3_u32 v196, v214, v215, v196\n\t" \
    "s_waitcnt vmcnt(4)\n\t" \
    "v_max3_u32 v196, v216, v217, v196\n\t" \
    "v_max3_u32 v196, v218, v219, v196\n\t" \
    "s_waitcnt vmcnt(3)\n\t" \
    "v_max3_u32 v196, v220, v221, v196\n\t" \
    "v_max3_u32 v196, v222, v223, v196\n\t" \
    "s_waitcnt vmcnt(2)\n\t" \
    "v_max3_u32 v196, v224, v225, v196\n\t" \
    "v_max3_u32 v196, v226, v227, v196\n\t" \
    "s_waitcnt vmcnt(1)\n\t" \
    "v_max3_u32 v196, v228, v229, v196\n\t" \
    "v_max3_u32 v196, v230, v231, v196\n\t" \
    "v_cmp_eq_u32 vcc, -1, v196\n\t" \
    "s_cbranch_vccz .Lxd%=\n\t" \
    ".Lxl%=:\n\t" \
    "s_mov_b32 %[a], 0\n\t" \
    "v_max_u32 v196, v200, v201\n\t" \
    "v_max3_u32 v196, v202, v203, v196\n\t" \
    "v_cmp_eq_u32 vcc, -1, v196\n\t" \
    "s_cbranch_vccz .Lxs0_%=\n\t" \
    "buffer_load_dwordx4 v[200:203], %[l16], %[rs], %[s0] offen offset:0 sc1\n\t" \
    "s_mov_b32 %[a], 1\n\t" \
    ".Lxs0_%=:\n\t" \
    "v_max_u32 v196, v204, v205\n\t" \
    "v_max3_u32 v196, v206, v207, v196\n\t" \
    "v_cmp_eq_u32 vcc, -1, v196\n\t" \
    "s_cbranch_vccz .Lxs1_%=\n\t" \
    "buffer_load_dwordx4 v[204:207], %[l16], %[rs], %[s0] offen offset:1024 sc1\n\t" \
    "s_mov_b32 %[a], 1\n\t" \
    ".Lxs1_%=:\n\t" \
    "v_max_u32 v196, v208, v209\n\t" \
    "v_max3_u32 v196, v210, v211, v196\n\t" \
    "v_cmp_eq_u32 vcc, -1, v196\n\t" \
    "s_cbranch_vccz .Lxs2_%=\n\t" \
    "buffer_load_dwordx4 v[208:211], %[l16], %[rs], %[s0] offen offset:2048 sc1\n\t" \
    "s_mov_b32 %[a], 1\n\t" \
    ".Lxs2_%=:\n\t" \
    "v_max_u32 v196, v212, v213\n\t" \
    "v_max3_u32 v196, v214, v215, v196\n\t" \
    "v_cmp_eq_u32 vcc, -1, v196\n\t" \
    "s_cbranch_vccz .Lxs3_%=\n\t" \
    "buffer_load_dwordx4 v[212:215], %[l16], %[rs], %[s0] offen offset:3072 sc1\n\t" \
    "s_mov_b32 %[a], 1\n\t" \
    ".Lxs3_%=:\n\t" \
    "v_max_u32 v196, v216, v217\n\t" \
    "v_max3_u32 v196, v218, v219, v196\n\t" \
    "v_cmp_eq_u32 vcc, -1, v196\n\t" \
    "s_cbranch_vccz .Lxs4_%=\n\t" \
    "buffer_load_dwordx4 v[216:219], %[l16], %[rs], %[s1] offen offset:0 sc1\n\t" \
    "s_mov_b32 %[a], 1\n\t" \
    ".Lxs4_%=:\n\t" \
    "v_max_u32 v196, v220, v221\n\t" \
    "v_max3_u32 v196, v222, v223, v196\n\t" \
    "v_cmp_eq_u32 vcc, -1, v196\n\t" \
    "s_cbranch_vccz .Lxs5_%=\n\t" \
    "buffer_load_dwordx4 v[220:223], %[l16], %[rs], %[s1] offen offset:1024 sc1\n\t" \
    "s_mov_b32 %[a], 1\n\t" \
    ".Lxs5_%=:\n\t" \
    "v_max_u32 v196, v224, v225\n\t" \
    "v_max3_u32 v196, v226, v227, v196\n\t" \
    "v_cmp_eq_u32 vcc, -1, v196\n\t" \
    "s_cbranch_vccz .Lxs6_%=\n\t" \
    "buffer_load_dwordx4 v[224:227], %[l16], %[rs], %[s1] offen offset:2048 sc1\n\t" \
    "s_mov_b32 %[a], 1\n\t" \
    ".Lxs6_%=:\n\t" \
    "v_max_u32 v196, v228, v229\n\t" \
    "v_max3_u32 v196, v230, v231, v196\n\t" \
    "v_cmp_eq_u32 vcc, -1, v196\n\t" \
    "s_cbranch_vccz .Lxs7_%=\n\t" \
    "buffer_load_dwordx4 v[228:231], %[l16], %[rs], %[s1] offen offset:3072 sc1\n\t" \
    "s_mov_b32 %[a], 1\n\t" \
    ".Lxs7_%=:\n\t" \
    "s_cmp_eq_u32 %[a], 0\n\t" \
    "s_cbranch_scc1 .Lxd%=\n\t" \
    "s_waitcnt vmcnt(0)\n\t" \
    "s_sub_u32 %[n], %[n], 1\n\t" \
    "s_cmp_lg_u32 %[n], 0\n\t" \
    "s_cbranch_scc1 .Lxl%=\n\t" \
    "s_branch .Lxe%=\n\t" \
    ".Lxd%=:\n\t" \
    ASM_ST2 \
    "v_mfma_f32_16x16x32_bf16 v[244:247], v[160:163], v[200:203], 0\n\t" \
    "v_mfma_f32_16x16x32_bf16 v[244:247], v[164:167], v[204:207], v[244:247]\n\t" \
    "v_mfma_f32_16x16x32_bf16 v[244:247], v[168:171], v[208:211], v[244:247]\n\t" \
    "v_mfma_f32_16x16x32_bf16 v[244:247], v[172:175], v[212:215], v[244:247]\n\t" \
    "v_mfma_f32_16x16x32_bf16 v[240:243], v[232:235], v[220:223], 0\n\t" \
    "v_mfma_f32_16x16x32_bf16 v[244:247], v[180:183], v[216:219], v[244:247]\n\t" \
    "v_mfma_f32_16x16x32_bf16 v[240:243], v[236:239], v[224:227], v[240:243]\n\t" \
    "v_mfma_f32_16x16x32_bf16 v[240:243], v[180:183], v[228:231], v[240:243]\n\t" \
    "s_waitcnt vmcnt(0)\n\t" \
    "v_cmp_ge_u32 vcc, %[tn], v248\n\t" \
    "s_mov_b32 %[a], 0\n\t" \
    "s_cbranch_vccnz .Lxv%=\n\t" \
    "buffer_load_dwordx4 v[160:163], %[l16], %[rd], %[sv] offen offset:0 sc1\n\t" \
    "buffer_load_dwordx4 v[164:167], %[l16], %[rd], %[sv] offen offset:1024 sc1\n\t" \
    "buffer_load_dwordx4 v[168:171], %[l16], %[rd], %[sv] offen offset:2048 sc1\n\t" \
    "buffer_load_dwordx4 v[172:175], %[l16], %[rd], %[sv] offen offset:3072 sc1\n\t" \
    "s_mov_b32 %[a], 1\n\t" \
    ".Lxv%=:\n\t" \
    "s_nop 7\n\t" \
    "ds_write_b128 %[pw], v[244:247] offset:8192\n\t" \
    "ds_write_b128 %[pw], v[240:243]\n\t" \
    "s_cmp_eq_u32 %[fin], 0\n\t" \
    "s_waitcnt lgkmcnt(0)\n\t" \
    "s_barrier\n\t" ASM_ST \
    "s_cbranch_scc1 .Lxe%=\n\t" \
    "ds_read_b128 v[200:203], %[pr] offset:0\n\t" \
    "ds_read_b128 v[204:207], %[pr] offset:1024\n\t" \
    "ds_read_b128 v[208:211], %[pr] offset:2048\n\t" \
    "ds_read_b128 v[212:215], %[pr] offset:3072\n\t" \
    "ds_read_b128 v[216:219], %[pr] offset:4096\n\t" \
    "ds_read_b128 v[220:223], %[pr] offset:5120\n\t" \
    "ds_read_b128 v[224:227], %[pr] offset:6144\n\t" \
    "ds_read_b128 v[228:231], %[pr] offset:7168\n\t" \
    "v_lshlrev_b32 v240, 16, v250\n\t" \
    "v_and_b32 v241, 0xffff0000, v250\n\t" \
    "v_lshlrev_b32 v242, 16, v251\n\t" \
    "v_and_b32 v243, 0xffff0000, v251\n\t" \
    "s_waitcnt lgkmcnt(6)\n\t" \
    "v_pk_add_f32 v[200:201], v[200:201], v[204:205]\n\t" \
    "v_pk_add_f32 v[202:203], v[202:203], v[206:207]\n\t" \
    "s_waitcnt lgkmcnt(4)\n\t" \
    "v_pk_add_f32 v[208:209], v[208:209], v[212:213]\n\t" \
    "v_pk_add_f32 v[210:211], v[210:211], v[214:215]\n\t" \
    "v_pk_add_f32 v[200:201], v[200:201], v[208:209]\n\t" \
    "v_pk_add_f32 v[202:203], v[202:203], v[210:211]\n\t" \
    "s_waitcnt lgkmcnt(2)\n\t" \
    "v_pk_add_f32 v[216:217], v[216:217], v[220:221]\n\t" \
    "v_pk_add_f32 v[218:219], v[218:219], v[222:223]\n\t" \
    "s_waitcnt lgkmcnt(0)\n\t" \
    "v_pk_add_f32 v[224:225], v[224:225], v[228:229]\n\t" \
    "v_pk_add_f32 v[226:227], v[226:227], v[230:231]\n\t" \
    "v_pk_add_f32 v[216:217], v[216:217], v[224:225]\n\t" \
    "v_pk_add_f32 v[218:219], v[218:219], v[226:227]\n\t" \
    "v_pk_add_f32 v[200:201], v[200:201], v[216:217]\n\t" \
    "v_pk_add_f32 v[202:203], v[202:203], v[218:219]\n\t" \
    "v_pk_add_f32 v[200:201], v[200:201], v[240:241]\n\t" \
    "v_pk_add_f32 v[202:203], v[202:203], v[242:243]\n\t" \
    "v_cvt_pk_bf16_f32 v200, v200, v201\n\t" \
    "v_cvt_pk_bf16_f32 v201, v202, v203\n\t" \
    "v_min_u32 v200, -2, v200\n\t" \
    "v_min_u32 v201, -2, v201\n\t" \
    "s_add_u32 %[sx], %[sx], 0x500000\n\t" \
    "buffer_store_dwordx2 v[200:201], %[xo], %[rd], %[sx] offen sc1\n\t" \
    ".Lxe%=:\n\t"
#define ASM_MPOLLV3 \
    "s_movk_i32 %[n], 0x400\n\t" \
    ".Lfm%=:\n\t" \
    "s_mov_b32 %[a], 0\n\t" \
    "v_max_u32 v252, v200, v201\n\t" \
    "v_max3_u32 v252, v202, v203, v252\n\t" \
    "v_cmp_eq_u32 vcc, -1, v252\n\t" \
    "s_cbranch_vccz .Lgm0_%=\n\t" \
    "buffer_load_dwordx4 v[200:203], %[l16], %[rs], %[s0] offen sc1\n\t" \
    "s_mov_b32 %[a], 1\n\t" \
    ".Lgm0_%=:\n\t" \
    "v_max_u32 v252, v204, v205\n\t" \
    "v_max3_u32 v252, v206, v207, v252\n\t" \
    "v_cmp_eq_u32 vcc, -1, v252\n\t" \
    "s_cbranch_vccz .Lgm1_%=\n\t" \
    "buffer_load_dwordx4 v[204:207], %[l16], %[rs], %[s1] offen sc1\n\t" \
    "s_mov_b32 %[a], 1\n\t" \
    ".Lgm1_%=:\n\t" \
    "v_max_u32 v252, v208, v209\n\t" \
    "v_max3_u32 v252, v210, v211, v252\n\t" \
    "v_cmp_eq_u32 vcc, -1, v252\n\t" \
    "s_cbranch_vccz .Lgm2_%=\n\t" \
    "buffer_load_dwordx4 v[208:211], %[l16], %[rs], %[s2] offen sc1\n\t" \
    "s_mov_b32 %[a], 1\n\t" \
    ".Lgm2_%=:\n\t" \
    "s_cmp_eq_u32 %[a], 0\n\t" \
    "s_cbranch_scc1 .Lhm%=\n\t" \
    "s_waitcnt vmcnt(12)\n\t" \
    "v_max_u32 v252, v200, v201\n\t" \
    "v_max3_u32 v252, v202, v203, v252\n\t" \
    "v_max3_u32 v252, v204, v205, v252\n\t" \
    "v_max3_u32 v252, v206, v207, v252\n\t" \
    "v_max3_u32 v252, v208, v209, v252\n\t" \
    "v_max3_u32 v252, v210, v211, v252\n\t" \
    "v_cmp_eq_u32 vcc, -1, v252\n\t" \
    "s_cbranch_vccz .Lhm%=\n\t" \
    "s_nop 7\n\t" \
    "v_max_u32 v252, v200, v201\n\t" \
    "v_max3_u32 v252, v202, v203, v252\n\t" \
    "v_max3_u32 v252, v204, v205, v252\n\t" \
    "v_max3_u32 v252, v206, v207, v252\n\t" \
    "v_max3_u32 v252, v208, v209, v252\n\t" \
    "v_max3_u32 v252, v210, v211, v252\n\t" \
    "v_cmp_eq_u32 vcc, -1, v252\n\t" \
    "s_cbranch_vccz .Lhm%=\n\t" \
    "s_nop 7\n\t" \
    "s_sub_u32 %[n], %[n], 1\n\t" \
    "s_cmp_lg_u32 %[n], 0\n\t" \
    "s_cbranch_scc1 .Lfm%=\n\t" \
    ".Lhm%=:\n\t"
#define ASM_MPOLLV2 \
    "s_movk_i32 %[n], 0x400\n\t" \
    ".Lfm%=:\n\t" \
    "s_mov_b32 %[a], 0\n\t" \
    "v_max_u32 v252, v200, v201\n\t" \
    "v_max3_u32 v252, v202, v203, v252\n\t" \
    "v_cmp_eq_u32 vcc, -1, v252\n\t" \
    "s_cbranch_vccz .Lgm0_%=\n\t" \
    "buffer_load_dwordx4 v[200:203], %[l16], %[rs], %[s0] offen sc1\n\t" \
    "s_mov_b32 %[a], 1\n\t" \
    ".Lgm0_%=:\n\t" \
    "v_max_u32 v252, v204, v205\n\t" \
    "v_max3_u32 v252, v206, v207, v252\n\t" \
    "v_cmp_eq_u32 vcc, -1, v252\n\t" \
    "s_cbranch_vccz .Lgm1_%=\n\t" \
    "buffer_load_dwordx4 v[204:207], %[l16], %[rs], %[s1] offen sc1\n\t" \
    "s_mov_b32 %[a], 1\n\t" \
    ".Lgm1_%=:\n\t" \
    "s_cmp_eq_u32 %[a], 0\n\t" \
    "s_cbranch_scc1 .Lhm%=\n\t" \
    "s_waitcnt vmcnt(12)\n\t" \
    "v_max_u32 v252, v200, v201\n\t" \
    "v_max3_u32 v252, v202, v203, v252\n\t" \
    "v_max3_u32 v252, v204, v205, v252\n\t" \
    "v_max3_u32 v252, v206, v207, v252\n\t" \
    "v_cmp_eq_u32 vcc, -1, v252\n\t" \
    "s_cbranch_vccz .Lhm%=\n\t" \
    "s_nop 7\n\t" \
    "v_max_u32 v252, v200, v201\n\t" \
    "v_max3_u32 v252, v202, v203, v252\n\t" \
    "v_max3_u32 v252, v204, v205, v252\n\t" \
    "v_max3_u32 v252, v206, v207, v252\n\t" \
    "v_cmp_eq_u32 vcc, -1, v252\n\t" \
    "s_cbranch_vccz .Lhm%=\n\t" \
    "s_nop 7\n\t" \
    "s_sub_u32 %[n], %[n], 1\n\t" \
    "s_cmp_lg_u32 %[n], 0\n\t" \
    "s_cbranch_scc1 .Lfm%=\n\t" \
    ".Lhm%=:\n\t"
#define ASM_W4_M \
    "buffer_load_dwordx4 v[184:187], %[l16], %[rs], %[s1] offen offset:2048 sc1\n\t" \
    "buffer_load_dwordx4 v[180:183], %[l16], %[rs], %[s1] offen offset:1024 sc1\n\t" \
    "buffer_load_dwordx4 v[176:179], %[l16], %[rs], %[s1] offen offset:0 sc1\n\t" \
    "buffer_load_dwordx4 v[172:175], %[l16], %[rs], %[s0] offen offset:3072 sc1\n\t" \
    "buffer_load_dwordx4 v[168:171], %[l16], %[rs], %[s0] offen offset:2048 sc1\n\t" \
    "buffer_load_dwordx4 v[164:167], %[l16], %[rs], %[s0] offen offset:1024 sc1\n\t" \
    "buffer_load_dwordx4 v[160:163], %[l16], %[rs], %[s0] offen offset:0 sc1\n\t" \
    "buffer_load_dwordx4 v[220:223], %[l16], %[rs], %[s3] offen offset:3072 sc1\n\t" \
    "v_mfma_f32_16x16x32_bf16 v[224:227], v[28:31], v[188:191], 0\n\t" \
    "v_mfma_f32_16x16x32_bf16 v[228:231], v[60:63], v[188:191], 0\n\t" \
    "v_mfma_f32_16x16x32_bf16 v[232:235], v[92:95], v[188:191], 0\n\t" \
    "v_mfma_f32_16x16x32_bf16 v[236:239], v[124:127], v[188:191], 0\n\t" \
    "v_mfma_f32_16x16x32_bf16 v[240:243], v[156:159], v[188:191], 0\n\t" \
    "s_waitcnt vmcnt(7)\n\t" \
    ".Lc06_%=:\n\t" \
    "v_max_u32 v244, v184, v185\n\t" \
    "v_max3_u32 v244, v186, v187, v244\n\t" \
    "v_cmp_eq_u32 vcc, -1, v244\n\t" \
    "s_cbranch_vccnz .Ls06_%=\n\t" \
    ".Lk06_%=:\n\t" \
    "v_mfma_f32_16x16x32_bf16 v[224:227], v[24:27], v[184:187], v[224:227]\n\t" \
    "v_mfma_f32_16x16x32_bf16 v[228:231], v[56:59], v[184:187], v[228:231]\n\t" \
    "v_mfma_f32_16x16x32_bf16 v[232:235], v[88:91], v[184:187], v[232:235]\n\t" \
    "v_mfma_f32_16x16x32_bf16 v[236:239], v[120:123], v[184:187], v[236:239]\n\t" \
    "v_mfma_f32_16x16x32_bf16 v[240:243], v[152:155], v[184:187], v[240:243]\n\t" \
    "s_waitcnt vmcnt(6)\n\t" \
    ".Lc05_%=:\n\t" \
    "v_max_u32 v244, v180, v181\n\t" \
    "v_max3_u32 v244, v182, v183, v244\n\t" \
    "v_cmp_eq_u32 vcc, -1, v244\n\t" \
    "s_cbranch_vccnz .Ls05_%=\n\t" \
    ".Lk05_%=:\n\t" \
    "v_mfma_f32_16x16x32_bf16 v[224:227], v[20:23], v[180:183], v[224:227]\n\t" \
    "v_mfma_f32_16x16x32_bf16 v[228:231], v[52:55], v[180:183], v[228:231]\n\t" \
    "v_mfma_f32_16x16x32_bf16 v[232:235], v[84:87], v[180:183], v[232:235]\n\t" \
    "v_mfma_f32_16x16x32_bf16 v[236:239], v[116:119], v[180:183], v[236:239]\n\t" \
    "v_mfma_f32_16x16x32_bf16 v[240:243], v[148:151], v[180:183], v[240:243]\n\t" \
    "s_waitcnt vmcnt(5)\n\t" \
    ".Lc04_%=:\n\t" \
    "v_max_u32 v244, v176, v177\n\t" \
    "v_max3_u32 v244, v178, v179, v244\n\t" \
    "v_cmp_eq_u32 vcc, -1, v244\n\t" \
    "s_cbranch_vccnz .Ls04_%=\n\t" \
    ".Lk04_%=:\n\t" \
    "v_mfma_f32_16x16x32_bf16 v[224:227], v[16:19], v[176:179], v[224:227]\n\t" \
    "v_mfma_f32_16x16x32_bf16 v[228:231], v[48:51], v[176:179], v[228:231]\n\t" \
    "v_mfma_f32_16x16x32_bf16 v[232:235], v[80:83], v[176:179], v[232:235]\n\t" \
    "v_mfma_f32_16x16x32_bf16 v[236:239], v[112:115], v[176:179], v[236:239]\n\t" \
    "v_mfma_f32_16x16x32_bf16 v[240:243], v[144:147], v[176:179], v[240:243]\n\t" \
    "s_waitcnt vmcnt(4)\n\t" \
    ".Lc03_%=:\n\t" \
    "v_max_u32 v244, v172, v173\n\t" \
    "v_max3_u32 v244, v174, v175, v244\n\t" \
    "v_cmp_eq_u32 vcc, -1, v244\n\t" \
    "s_cbranch_vccnz .Ls03_%=\n\t" \
    ".Lk03_%=:\n\t" \
    "v_mfma_f32_16x16x32_bf16 v[224:227], v[12:15], v[172:175], v[224:227]\n\t" \
    "v_mfma_f32_16x16x32_bf16 v[228:231], v[44:47], v[172:175], v[228:231]\n\t" \
    "v_mfma_f32_16x16x32_bf16 v[232:235], v[76:79], v[172:175], v[232:235]\n\t" \
    "v_mfma_f32_16x16x32_bf16 v[236:239], v[108:111], v[172:175], v[236:239]\n\t" \
    "v_mfma_f32_16x16x32_bf16 v[240:243], v[140:143], v[172:175], v[240:243]\n\t" \
    "s_waitcnt vmcnt(3)\n\t" \
    ".Lc02_%=:\n\t" \
    "v_max_u32 v244, v168, v169\n\t" \
    "v_max3_u32 v244, v170, v171, v244\n\t" \
    "v_cmp_eq_u32 vcc, -1, v244\n\t" \
    "s_cbranch_vccnz .Ls02_%=\n\t" \
    ".Lk02_%=:\n\t" \
    "v_mfma_f32_16x16x32_bf16 v[224:227], v[8:11], v[168:171], v[224:227]\n\t" \
    "v_mfma_f32_16x16x32_bf16 v[228:231], v[40:43], v[168:171], v[228:231]\n\t" \
    "v_mfma_f32_16x16x32_bf16 v[232:235], v[72:75], v[168:171], v[232:235]\n\t" \
    "v_mfma_f32_16x16x32_bf16 v[236:239], v[104:107], v[168:171], v[236:239]\n\t" \
    "v_mfma_f32_16x16x32_bf16 v[240:243], v[136:139], v[168:171], v[240:243]\n\t" \
    "s_waitcnt vmcnt(2)\n\t" \
    ".Lc01_%=:\n\t" \
    "v_max_u32 v244, v164, v165\n\t" \
    "v_max3_u32 v244, v166, v167, v244\n\t" \
    "v_cmp_eq_u32 vcc, -1, v244\n\t" \
    "s_cbranch_vccnz .Ls01_%=\n\t" \
    ".Lk01_%=:\n\t" \
    "v_mfma_f32_16x16x32_bf16 v[224:227], v[4:7], v[164:167], v[224:227]\n\t" \
    "v_mfma_f32_16x16x32_bf16 v[228:231], v[36:39], v[164:167], v[228:231]\n\t" \
    "v_mfma_f32_16x16x32_bf16 v[232:235], v[68:71], v[164:167], v[232:235]\n\t" \
    "v_mfma_f32_16x16x32_bf16 v[236:239], v[100:103], v[164:167], v[236:239]\n\t" \
    "v_mfma_f32_16x16x32_bf16 v[240:243], v[132:135], v[164:167], v[240:243]\n\t" \
    "s_waitcnt vmcnt(1)\n\t" \
    ".Lc00_%=:\n\t" \
    "v_max_u32 v244, v160, v161\n\t" \
    "v_max3_u32 v244, v162, v163, v244\n\t" \
    "v_cmp_eq_u32 vcc, -1, v244\n\t" \
    "s_cbranch_vccnz .Ls00_%=\n\t" \
    ".Lk00_%=:\n\t" \
    "v_mfma_f32_16x16x32_bf16 v[224:227], v[0:3], v[160:163], v[224:227]\n\t" \
    "v_mfma_f32_16x16x32_bf16 v[228:231], v[32:35], v[160:163], v[228:231]\n\t" \
    "v_mfma_f32_16x16x32_bf16 v[232:235], v[64:67], v[160:163], v[232:235]\n\t" \
    "v_mfma_f32_16x16x32_bf16 v[236:239], v[96:99], v[160:163], v[236:239]\n\t" \
    "v_mfma_f32_16x16x32_bf16 v[240:243], v[128:131], v[160:163], v[240:243]\n\t" \
    "s_waitcnt vmcnt(0)\n\t" \
    ".Lc17_%=:\n\t" \
    "v_max_u32 v244, v220, v221\n\t" \
    "v_max3_u32 v244, v222, v223, v244\n\t" \
    "v_cmp_eq_u32 vcc, -1, v244\n\t" \
    "s_cbranch_vccnz .Ls17_%=\n\t" \
    ".Lk17_%=:\n\t" \
    "buffer_load_dwordx4 v[216:219], %[l16], %[rs], %[s3] offen offset:2048 sc1\n\t" \
    "buffer_load_dwordx4 v[212:215], %[l16], %[rs], %[s3] offen offset:1024 sc1\n\t" \
    "buffer_load_dwordx4 v[208:211], %[l16], %[rs], %[s3] offen offset:0 sc1\n\t" \
    "buffer_load_dwordx4 v[204:207], %[l16], %[rs], %[s2] offen offset:3072 sc1\n\t" \
    "buffer_load_dwordx4 v[200:203], %[l16], %[rs], %[s2] offen offset:2048 sc1\n\t" \
    "buffer_load_dwordx4 v[196:199], %[l16], %[rs], %[s2] offen offset:1024 sc1\n\t" \
    "buffer_load_dwordx4 v[192:195], %[l16], %[rs], %[s2] offen offset:0 sc1\n\t" \
    "v_mfma_f32_16x16x32_bf16 v[160:163], v[28:31], v[220:223], 0\n\t" \
    "ds_write_b128 %[ld], v[224:227] offset:0\n\t" \
    "v_mfma_f32_16x16x32_bf16 v[164:167], v[60:63], v[220:223], 0\n\t" \
    "ds_write_b128 %[ld], v[228:231] offset:6144\n\t" \
    "v_mfma_f32_16x16x32_bf16 v[168:171], v[92:95], v[220:223], 0\n\t" \
    "ds_write_b128 %[ld], v[232:235] offset:12288\n\t" \
    "v_mfma_f32_16x16x32_bf16 v[172:175], v[124:127], v[220:223], 0\n\t" \
    "ds_write_b128 %[ld], v[236:239] offset:18432\n\t" \
    "v_mfma_f32_16x16x32_bf16 v[176:179], v[156:159], v[220:223], 0\n\t" \
    "ds_write_b128 %[ld], v[240:243] offset:24576\n\t" \
    "s_waitcnt vmcnt(6)\n\t" \
    ".Lc16_%=:\n\t" \
    "v_max_u32 v244, v216, v217\n\t" \
    "v_max3_u32 v244, v218, v219, v244\n\t" \
    "v_cmp_eq_u32 vcc, -1, v244\n\t" \
    "s_cbranch_vccnz .Ls16_%=\n\t" \
    ".Lk16_%=:\n\t" \
    "v_mfma_f32_16x16x32_bf16 v[160:163], v[24:27], v[216:219], v[160:163]\n\t" \
    "v_mfma_f32_16x16x32_bf16 v[164:167], v[56:59], v[216:219], v[164:167]\n\t" \
    "v_mfma_f32_16x16x32_bf16 v[168:171], v[88:91], v[216:219], v[168:171]\n\t" \
    "v_mfma_f32_16x16x32_bf16 v[172:175], v[120:123], v[216:219], v[172:175]\n\t" \
    "v_mfma_f32_16x16x32_bf16 v[176:179], v[152:155], v[216:219], v[176:179]\n\t" \
    "s_waitcnt vmcnt(5)\n\t" \
    ".Lc15_%=:\n\t" \
    "v_max_u32 v244, v212, v213\n\t" \
    "v_max3_u32 v244, v214, v215, v244\n\t" \
    "v_cmp_eq_u32 vcc, -1, v244\n\t" \
    "s_cbranch_vccnz .Ls15_%=\n\t" \
    ".Lk15_%=:\n\t" \
    "v_mfma_f32_16x16x32_bf16 v[160:163], v[20:23], v[212:215], v[160:163]\n\t" \
    "v_mfma_f32_16x16x32_bf16 v[164:167], v[52:55], v[212:215], v[164:167]\n\t" \
    "v_mfma_f32_16x16x32_bf16 v[168:171], v[84:87], v[212:215], v[168:171]\n\t" \
    "v_mfma_f32_16x16x32_bf16 v[172:175], v[116:119], v[212:215], v[172:175]\n\t" \
    "v_mfma_f32_16x16x32_bf16 v[176:179], v[148:151], v[212:215], v[176:179]\n\t" \
    "s_waitcnt vmcnt(4)\n\t" \
    ".Lc14_%=:\n\t" \
    "v_max_u32 v244, v208, v209\n\t" \
    "v_max3_u32 v244, v210, v211, v244\n\t" \
    "v_cmp_eq_u32 vcc, -1, v244\n\t" \
    "s_cbranch_vccnz .Ls14_%=\n\t" \
    ".Lk14_%=:\n\t" \
    "v_mfma_f32_16x16x32_bf16 v[160:163], v[16:19], v[208:211], v[160:163]\n\t" \
    "v_mfma_f32_16x16x32_bf16 v[164:167], v[48:51], v[208:211], v[164:167]\n\t" \
    "v_mfma_f32_16x16x32_bf16 v[168:171], v[80:83], v[208:211], v[168:171]\n\t" \
    "v_mfma_f32_16x16x32_bf16 v[172:175], v[112:115], v[208:211], v[172:175]\n\t" \
    "v_mfma_f32_16x16x32_bf16 v[176:179], v[144:147], v[208:211], v[176:179]\n\t" \
    "s_waitcnt vmcnt(3)\n\t" \
    ".Lc13_%=:\n\t" \
    "v_max_u32 v244, v204, v205\n\t" \
    "v_max3_u32 v244, v206, v207, v244\n\t" \
    "v_cmp_eq_u32 vcc, -1, v244\n\t" \
    "s_cbranch_vccnz .Ls13_%=\n\t" \
    ".Lk13_%=:\n\t" \
    "v_mfma_f32_16x16x32_bf16 v[160:163], v[12:15], v[204:207], v[160:163]\n\t" \
    "v_mfma_f32_16x16x32_bf16 v[164:167], v[44:47], v[204:207], v[164:167]\n\t" \
    "v_mfma_f32_16x16x32_bf16 v[168:171], v[76:79], v[204:207], v[168:171]\n\t" \
    "v_mfma_f32_16x16x32_bf16 v[172:175], v[108:111], v[204:207], v[172:175]\n\t" \
    "v_mfma_f32_16x16x32_bf16 v[176:179], v[140:143], v[204:207], v[176:179]\n\t" \
    "s_waitcnt vmcnt(2)\n\t" \
    ".Lc12_%=:\n\t" \
    "v_max_u32 v244, v200, v201\n\t" \
    "v_max3_u32 v244, v202, v203, v244\n\t" \
    "v_cmp_eq_u32 vcc, -1, v244\n\t" \
    "s_cbranch_vccnz .Ls12_%=\n\t" \
    ".Lk12_%=:\n\t" \
    "v_mfma_f32_16x16x32_bf16 v[160:163], v[8:11], v[200:203], v[160:163]\n\t" \
    "v_mfma_f32_16x16x32_bf16 v[164:167], v[40:43], v[200:203], v[164:167]\n\t" \
    "v_mfma_f32_16x16x32_bf16 v[168:171], v[72:75], v[200:203], v[168:171]\n\t" \
    "v_mfma_f32_16x16x32_bf16 v[172:175], v[104:107], v[200:203], v[172:175]\n\t" \
    "v_mfma_f32_16x16x32_bf16 v[176:179], v[136:139], v[200:203], v[176:179]\n\t" \
    "s_waitcnt vmcnt(1)\n\t" \
    ".Lc11_%=:\n\t" \
    "v_max_u32 v244, v196, v197\n\t" \
    "v_max3_u32 v244, v198, v199, v244\n\t" \
    "v_cmp_eq_u32 vcc, -1, v244\n\t" \
    "s_cbranch_vccnz .Ls11_%=\n\t" \
    ".Lk11_%=:\n\t" \
    "v_mfma_f32_16x16x32_bf16 v[160:163], v[4:7], v[196:199], v[160:163]\n\t" \
    "v_mfma_f32_16x16x32_bf16 v[164:167], v[36:39], v[196:199], v[164:167]\n\t" \
    "v_mfma_f32_16x16x32_bf16 v[168:171], v[68:71], v[196:199], v[168:171]\n\t" \
    "v_mfma_f32_16x16x32_bf16 v[172:175], v[100:103], v[196:199], v[172:175]\n\t" \
    "v_mfma_f32_16x16x32_bf16 v[176:179], v[132:135], v[196:199], v[176:179]\n\t" \
    "s_waitcnt vmcnt(0)\n\t" \
    ".Lc10_%=:\n\t" \
    "v_max_u32 v244, v192, v193\n\t" \
    "v_max3_u32 v244, v194, v195, v244\n\t" \
    "v_cmp_eq_u32 vcc, -1, v244\n\t" \
    "s_cbranch_vccnz .Ls10_%=\n\t" \
    ".Lk10_%=:\n\t" \
    "v_mfma_f32_16x16x32_bf16 v[160:163], v[0:3], v[192:195], v[160:163]\n\t" \
    "v_mfma_f32_16x16x32_bf16 v[164:167], v[32:35], v[192:195], v[164:167]\n\t" \
    "v_mfma_f32_16x16x32_bf16 v[168:171], v[64:67], v[192:195], v[168:171]\n\t" \
    "v_mfma_f32_16x16x32_bf16 v[172:175], v[96:99], v[192:195], v[172:175]\n\t" \
    "v_mfma_f32_16x16x32_bf16 v[176:179], v[128:131], v[192:195], v[176:179]\n\t" \
    "ds_write_b128 %[ld], v[160:163] offset:30720\n\t" \
    "ds_write_b128 %[ld], v[164:167] offset:36864\n\t" \
    "ds_write_b128 %[ld], v[168:171] offset:43008\n\t" \
    "s_nop 1\n\t" \
    "ds_write_b128 %[ld], v[172:175] offset:49152\n\t" \
    "s_nop 1\n\t" \
    "ds_write_b128 %[ld], v[176:179] offset:55296\n\t" \
    "s_branch .Lme%=\n\t" \
    ".Ls06_%=:\n\t" \
    "s_cmp_eq_u32 %[n], 0\n\t" \
    "s_cbranch_scc1 .Lk06_%=\n\t" \
    "s_sub_u32 %[n], %[n], 1\n\t" \
    "buffer_load_dwordx4 v[184:187], %[l16], %[rs], %[s1] offen offset:2048 sc1\n\t" \
    "s_waitcnt vmcnt(0)\n\t" \
    "s_branch .Lc06_%=\n\t" \
    ".Ls05_%=:\n\t" \
    "s_cmp_eq_u32 %[n], 0\n\t" \
    "s_cbranch_scc1 .Lk05_%=\n\t" \
    "s_sub_u32 %[n], %[n], 1\n\t" \
    "buffer_load_dwordx4 v[180:183], %[l16], %[rs], %[s1] offen offset:1024 sc1\n\t" \
    "s_waitcnt vmcnt(0)\n\t" \
    "s_branch .Lc05_%=\n\t" \
    ".Ls04_%=:\n\t" \
    "s_cmp_eq_u32 %[n], 0\n\t" \
    "s_cbranch_scc1 .Lk04_%=\n\t" \
    "s_sub_u32 %[n], %[n], 1\n\t" \
    "buffer_load_dwordx4 v[176:179], %[l16], %[rs], %[s1] offen offset:0 sc1\n\t" \
    "s_waitcnt vmcnt(0)\n\t" \
    "s_branch .Lc04_%=\n\t" \
    ".Ls03_%=:\n\t" \
    "s_cmp_eq_u32 %[n], 0\n\t" \
    "s_cbranch_scc1 .Lk03_%=\n\t" \
    "s_sub_u32 %[n], %[n], 1\n\t" \
    "buffer_load_dwordx4 v[172:175], %[l16], %[rs], %[s0] offen offset:3072 sc1\n\t" \
    "s_waitcnt vmcnt(0)\n\t" \
    "s_branch .Lc03_%=\n\t" \
    ".Ls02_%=:\n\t" \
    "s_cmp_eq_u32 %[n], 0\n\t" \
    "s_cbranch_scc1 .Lk02_%=\n\t" \
    "s_sub_u32 %[n], %[n], 1\n\t" \
    "buffer_load_dwordx4 v[168:171], %[l16], %[rs], %[s0] offen offset:2048 sc1\n\t" \
    "s_waitcnt vmcnt(0)\n\t" \
    "s_branch .Lc02_%=\n\t" \
    ".Ls01_%=:\n\t" \
    "s_cmp_eq_u32 %[n], 0\n\t" \
    "s_cbranch_scc1 .Lk01_%=\n\t" \
    "s_sub_u32 %[n], %[n], 1\n\t" \
    "buffer_load_dwordx4 v[164:167], %[l16], %[rs], %[s0] offen offset:1024 sc1\n\t" \
    "s_waitcnt vmcnt(0)\n\t" \
    "s_branch .Lc01_%=\n\t" \
    ".Ls00_%=:\n\t" \
    "s_cmp_eq_u32 %[n], 0\n\t" \
    "s_cbranch_scc1 .Lk00_%=\n\t" \
    "s_sub_u32 %[n], %[n], 1\n\t" \
    "buffer_load_dwordx4 v[160:163], %[l16], %[rs], %[s0] offen offset:0 sc1\n\t" \
    "s_waitcnt vmcnt(0)\n\t" \
    "s_branch .Lc00_%=\n\t" \
    ".Ls17_%=:\n\t" \
    "s_cmp_eq_u32 %[n], 0\n\t" \
    "s_cbranch_scc1 .Lk17_%=\n\t" \
    "s_sub_u32 %[n], %[n], 1\n\t" \
    "buffer_load_dwordx4 v[220:223], %[l16], %[rs], %[s3] offen offset:3072 sc1\n\t" \
    "s_waitcnt vmcnt(0)\n\t" \
    "s_branch .Lc17_%=\n\t" \
    ".Ls16_%=:\n\t" \
    "s_cmp_eq_u32 %[n], 0\n\t" \
    "s_cbranch_scc1 .Lk16_%=\n\t" \
    "s_sub_u32 %[n], %[n], 1\n\t" \
    "buffer_load_dwordx4 v[216:219], %[l16], %[rs], %[s3] offen offset:2048 sc1\n\t" \
    "s_waitcnt vmcnt(0)\n\t" \
    "s_branch .Lc16_%=\n\t" \
    ".Ls15_%=:\n\t" \
    "s_cmp_eq_u32 %[n], 0\n\t" \
    "s_cbranch_scc1 .Lk15_%=\n\t" \
    "s_sub_u32 %[n], %[n], 1\n\t" \
    "buffer_load_dwordx4 v[212:215], %[l16], %[rs], %[s3] offen offset:1024 sc1\n\t" \
    "s_waitcnt vmcnt(0)\n\t" \
    "s_branch .Lc15_%=\n\t" \
    ".Ls14_%=:\n\t" \
    "s_cmp_eq_u32 %[n], 0\n\t" \
    "s_cbranch_scc1 .Lk14_%=\n\t" \
    "s_sub_u32 %[n], %[n], 1\n\t" \
    "buffer_load_dwordx4 v[208:211], %[l16], %[rs], %[s3] offen offset:0 sc1\n\t" \
    "s_waitcnt vmcnt(0)\n\t" \
    "s_branch .Lc14_%=\n\t" \
    ".Ls13_%=:\n\t" \
    "s_cmp_eq_u32 %[n], 0\n\t" \
    "s_cbranch_scc1 .Lk13_%=\n\t" \
    "s_sub_u32 %[n], %[n], 1\n\t" \
    "buffer_load_dwordx4 v[204:207], %[l16], %[rs], %[s2] offen offset:3072 sc1\n\t" \
    "s_waitcnt vmcnt(0)\n\t" \
    "s_branch .Lc13_%=\n\t" \
    ".Ls12_%=:\n\t" \
    "s_cmp_eq_u32 %[n], 0\n\t" \
    "s_cbranch_scc1 .Lk12_%=\n\t" \
    "s_sub_u32 %[n], %[n], 1\n\t" \
    "buffer_load_dwordx4 v[200:203], %[l16], %[rs], %[s2] offen offset:2048 sc1\n\t" \
    "s_waitcnt vmcnt(0)\n\t" \
    "s_branch .Lc12_%=\n\t" \
    ".Ls11_%=:\n\t" \
    "s_cmp_eq_u32 %[n], 0\n\t" \
    "s_cbranch_scc1 .Lk11_%=\n\t" \
    "s_sub_u32 %[n], %[n], 1\n\t" \
    "buffer_load_dwordx4 v[196:199], %[l16], %[rs], %[s2] offen offset:1024 sc1\n\t" \
    "s_waitcnt vmcnt(0)\n\t" \
    "s_branch .Lc11_%=\n\t" \
    ".Ls10_%=:\n\t" \
    "s_cmp_eq_u32 %[n], 0\n\t" \
    "s_cbranch_scc1 .Lk10_%=\n\t" \
    "s_sub_u32 %[n], %[n], 1\n\t" \
    "buffer_load_dwordx4 v[192:195], %[l16], %[rs], %[s2] offen offset:0 sc1\n\t" \
    "s_waitcnt vmcnt(0)\n\t" \
    "s_branch .Lc10_%=\n\t" \
    ".Lme%=:\n\t"
#define ASM_POLL5_I \
    "buffer_load_dwordx4 v[232:235], %[l16], %[rs], %[s0] offen offset:0 sc1\n\t" \
    "buffer_load_dwordx4 v[236:239], %[l16], %[rs], %[s0] offen offset:1024 sc1\n\t" \
    "buffer_load_dwordx4 v[240:243], %[l16], %[rs], %[s0] offen offset:2048 sc1\n\t" \
    "buffer_load_dwordx4 v[244:247], %[l16], %[rs], %[s0] offen offset:3072 sc1\n\t" \
    "buffer_load_dwordx4 v[248:251], %[l16], %[rs], %[s1] offen offset:0 sc1\n\t"
#define ASM_POLL5_C \
    "s_movk_i32 %[n], 0x200\n\t" \
    "v_max_u32 v252, v232, v233\n\t" \
    "v_max3_u32 v252, v234, v235, v252\n\t" \
    "v_max3_u32 v252, v236, v237, v252\n\t" \
    "v_max3_u32 v252, v238, v239, v252\n\t" \
    "v_max3_u32 v252, v240, v241, v252\n\t" \
    "v_max3_u32 v252, v242, v243, v252\n\t" \
    "v_max3_u32 v252, v244, v245, v252\n\t" \
    "v_max3_u32 v252, v246, v247, v252\n\t" \
    "v_max3_u32 v252, v248, v249, v252\n\t" \
    "v_max3_u32 v252, v250, v251, v252\n\t" \
    "v_cmp_eq_u32 vcc, -1, v252\n\t" \
    "s_cbranch_vccz .Lhw%=\n\t" \
    ".Lfw%=:\n\t" \
    "s_mov_b32 %[a], 0\n\t" \
    "v_max_u32 v252, v232, v233\n\t" \
    "v_max3_u32 v252, v234, v235, v252\n\t" \
    "v_cmp_eq_u32 vcc, -1, v252\n\t" \
    "s_cbranch_vccz .Lgw0_%=\n\t" \
    "buffer_load_dwordx4 v[232:235], %[l16], %[rs], %[s0] offen offset:0 sc1\n\t" \
    "s_mov_b32 %[a], 1\n\t" \
    ".Lgw0_%=:\n\t" \
    "v_max_u32 v252, v236, v237\n\t" \
    "v_max3_u32 v252, v238, v239, v252\n\t" \
    "v_cmp_eq_u32 vcc, -1, v252\n\t" \
    "s_cbranch_vccz .Lgw1_%=\n\t" \
    "buffer_load_dwordx4 v[236:239], %[l16], %[rs], %[s0] offen offset:1024 sc1\n\t" \
    "s_mov_b32 %[a], 1\n\t" \
    ".Lgw1_%=:\n\t" \
    "v_max_u32 v252, v240, v241\n\t" \
    "v_max3_u32 v252, v242, v243, v252\n\t" \
    "v_cmp_eq_u32 vcc, -1, v252\n\t" \
    "s_cbranch_vccz .Lgw2_%=\n\t" \
    "buffer_load_dwordx4 v[240:243], %[l16], %[rs], %[s0] offen offset:2048 sc1\n\t" \
    "s_mov_b32 %[a], 1\n\t" \
    ".Lgw2_%=:\n\t" \
    "v_max_u32 v252, v244, v245\n\t" \
    "v_max3_u32 v252, v246, v247, v252\n\t" \
    "v_cmp_eq_u32 vcc, -1, v252\n\t" \
    "s_cbranch_vccz .Lgw3_%=\n\t" \
    "buffer_load_dwordx4 v[244:247], %[l16], %[rs], %[s0] offen offset:3072 sc1\n\t" \
    "s_mov_b32 %[a], 1\n\t" \
    ".Lgw3_%=:\n\t" \
    "v_max_u32 v252, v248, v249\n\t" \
    "v_max3_u32 v252, v250, v251, v252\n\t" \
    "v_cmp_eq_u32 vcc, -1, v252\n\t" \
    "s_cbranch_vccz .Lgw4_%=\n\t" \
    "buffer_load_dwordx4 v[248:251], %[l16], %[rs], %[s1] offen offset:0 sc1\n\t" \
    "s_mov_b32 %[a], 1\n\t" \
    ".Lgw4_%=:\n\t" \
    "s_cmp_eq_u32 %[a], 0\n\t" \
    "s_cbranch_scc1 .Lhw%=\n\t" \
    "s_waitcnt vmcnt(20)\n\t" \
    "v_max_u32 v252, v232, v233\n\t" \
    "v_max3_u32 v252, v234, v235, v252\n\t" \
    "v_max3_u32 v252, v236, v237, v252\n\t" \
    "v_max3_u32 v252, v238, v239, v252\n\t" \
    "v_max3_u32 v252, v240, v241, v252\n\t" \
    "v_max3_u32 v252, v242, v243, v252\n\t" \
    "v_max3_u32 v252, v244, v245, v252\n\t" \
    "v_max3_u32 v252, v246, v247, v252\n\t" \
    "v_max3_u32 v252, v248, v249, v252\n\t" \
    "v_max3_u32 v252, v250, v251, v252\n\t" \
    "v_cmp_eq_u32 vcc, -1, v252\n\t" \
    "s_cbranch_vccz .Lhw%=\n\t" \
    "s_sleep 1\n\t" \
    "v_max_u32 v252, v232, v233\n\t" \
    "v_max3_u32 v252, v234, v235, v252\n\t" \
    "v_max3_u32 v252, v236, v237, v252\n\t" \
    "v_max3_u32 v252, v238, v239, v252\n\t" \
    "v_max3_u32 v252, v240, v241, v252\n\t" \
    "v_max3_u32 v252, v242, v243, v252\n\t" \
    "v_max3_u32 v252, v244, v245, v252\n\t" \
    "v_max3_u32 v252, v246, v247, v252\n\t" \
    "v_max3_u32 v252, v248, v249, v252\n\t" \
    "v_max3_u32 v252, v250, v251, v252\n\t" \
    "v_cmp_eq_u32 vcc, -1, v252\n\t" \
    "s_cbranch_vccz .Lhw%=\n\t" \
    "s_sleep 1\n\t" \
    "v_max_u32 v252, v232, v233\n\t" \
    "v_max3_u32 v252, v234, v235, v252\n\t" \
    "v_max3_u32 v252, v236, v237, v252\n\t" \
    "v_max3_u32 v252, v238, v239, v252\n\t" \
    "v_max3_u32 v252, v240, v241, v252\n\t" \
    "v_max3_u32 v252, v242, v243, v252\n\t" \
    "v_max3_u32 v252, v244, v245, v252\n\t" \
    "v_max3_u32 v252, v246, v247, v252\n\t" \
    "v_max3_u32 v252, v248, v249, v252\n\t" \
    "v_max3_u32 v252, v250, v251, v252\n\t" \
    "v_cmp_eq_u32 vcc, -1, v252\n\t" \
    "s_cbranch_vccz .Lhw%=\n\t" \
    "s_sleep 1\n\t" \
    "v_max_u32 v252, v232, v233\n\t" \
    "v_max3_u32 v252, v234, v235, v252\n\t" \
    "v_max3_u32 v252, v236, v237, v252\n\t" \
    "v_max3_u32 v252, v238, v239, v252\n\t" \
    "v_max3_u32 v252, v240, v241, v252\n\t" \
    "v_max3_u32 v252, v242, v243, v252\n\t" \
    "v_max3_u32 v252, v244, v245, v252\n\t" \
    "v_max3_u32 v252, v246, v247, v252\n\t" \
    "v_max3_u32 v252, v248, v249, v252\n\t" \
    "v_max3_u32 v252, v250, v251, v252\n\t" \
    "v_cmp_eq_u32 vcc, -1, v252\n\t" \
    "s_cbranch_vccz .Lhw%=\n\t" \
    "s_sleep 1\n\t" \
    "v_max_u32 v252, v232, v233\n\t" \
    "v_max3_u32 v252, v234, v235, v252\n\t" \
    "v_max3_u32 v252, v236, v237, v252\n\t" \
    "v_max3_u32 v252, v238, v239, v252\n\t" \
    "v_max3_u32 v252, v240, v241, v252\n\t" \
    "v_max3_u32 v252, v242, v243, v252\n\t" \
    "v_max3_u32 v252, v244, v245, v252\n\t" \
    "v_max3_u32 v252, v246, v247, v252\n\t" \
    "v_max3_u32 v252, v248, v249, v252\n\t" \
    "v_max3_u32 v252, v250, v251, v252\n\t" \
    "v_cmp_eq_u32 vcc, -1, v252\n\t" \
    "s_cbranch_vccz .Lhw%=\n\t" \
    "s_sleep 1\n\t" \
    "v_max_u32 v252, v232, v233\n\t" \
    "v_max3_u32 v252, v234, v235, v252\n\t" \
    "v_max3_u32 v252, v236, v237, v252\n\t" \
    "v_max3_u32 v252, v238, v239, v252\n\t" \
    "v_max3_u32 v252, v240, v241, v252\n\t" \
    "v_max3_u32 v252, v242, v243, v252\n\t" \
    "v_max3_u32 v252, v244, v245, v252\n\t" \
    "v_max3_u32 v252, v246, v247, v252\n\t" \
    "v_max3_u32 v252, v248, v249, v252\n\t" \
    "v_max3_u32 v252, v250, v251, v252\n\t" \
    "v_cmp_eq_u32 vcc, -1, v252\n\t" \
    "s_cbranch_vccz .Lhw%=\n\t" \
    "s_sleep 1\n\t" \
    "s_sub_u32 %[n], %[n], 1\n\t" \
    "s_cmp_lg_u32 %[n], 0\n\t" \
    "s_cbranch_scc1 .Lfw%=\n\t" \
    ".Lhw%=:\n\t"
#define ASM_POLLS3 \
    "buffer_load_dwordx4 v[208:211], %[l16], %[rs], %[s0] offen offset:0 sc1\n\t" \
    "buffer_load_dwordx4 v[212:215], %[l16], %[rs], %[s1] offen offset:0 sc1\n\t" \
    "buffer_load_dwordx4 v[216:219], %[l16], %[rs], %[s2] offen offset:0 sc1\n\t" \
    "s_movk_i32 %[n], 0x200\n\t" \
    "s_waitcnt vmcnt(2)\n\t" \
    "v_max_u32 v240, v208, v209\n\t" \
    "v_max3_u32 v240, v210, v211, v240\n\t" \
    "s_waitcnt vmcnt(1)\n\t" \
    "v_max3_u32 v240, v212, v213, v240\n\t" \
    "v_max3_u32 v240, v214, v215, v240\n\t" \
    "s_waitcnt vmcnt(0)\n\t" \
    "v_max3_u32 v240, v216, v217, v240\n\t" \
    "v_max3_u32 v240, v218, v219, v240\n\t" \
    "v_cmp_eq_u32 vcc, -1, v240\n\t" \
    "s_cbranch_vccz .Lpd%=\n\t" \
    ".Lpl%=:\n\t" \
    "s_mov_b32 %[a], 0\n\t" \
    "v_max_u32 v240, v208, v209\n\t" \
    "v_max3_u32 v240, v210, v211, v240\n\t" \
    "v_cmp_eq_u32 vcc, -1, v240\n\t" \
    "s_cbranch_vccz .Lps0_%=\n\t" \
    "buffer_load_dwordx4 v[208:211], %[l16], %[rs], %[s0] offen offset:0 sc1\n\t" \
    "s_mov_b32 %[a], 1\n\t" \
    ".Lps0_%=:\n\t" \
    "v_max_u32 v240, v212, v213\n\t" \
    "v_max3_u32 v240, v214, v215, v240\n\t" \
    "v_cmp_eq_u32 vcc, -1, v240\n\t" \
    "s_cbranch_vccz .Lps1_%=\n\t" \
    "buffer_load_dwordx4 v[212:215], %[l16], %[rs], %[s1] offen offset:0 sc1\n\t" \
    "s_mov_b32 %[a], 1\n\t" \
    ".Lps1_%=:\n\t" \
    "v_max_u32 v240, v216, v217\n\t" \
    "v_max3_u32 v240, v218, v219, v240\n\t" \
    "v_cmp_eq_u32 vcc, -1, v240\n\t" \
    "s_cbranch_vccz .Lps2_%=\n\t" \
    "buffer_load_dwordx4 v[216:219], %[l16], %[rs], %[s2] offen offset:0 sc1\n\t" \
    "s_mov_b32 %[a], 1\n\t" \
    ".Lps2_%=:\n\t" \
    "s_cmp_eq_u32 %[a], 0\n\t" \
    "s_cbranch_scc1 .Lpd%=\n\t" \
    "s_waitcnt vmcnt(0)\n\t" \
    "s_sub_u32 %[n], %[n], 1\n\t" \
    "s_cmp_lg_u32 %[n], 0\n\t" \
    "s_cbranch_scc1 .Lpl%=\n\t" \
    ".Lpd%=:\n\t"
#define ASM_POLLS2 \
    "buffer_load_dwordx4 v[208:211], %[l16], %[rs], %[s0] offen offset:0 sc1\n\t" \
    "buffer_load_dwordx4 v[212:215], %[l16], %[rs], %[s1] offen offset:0 sc1\n\t" \
    "s_movk_i32 %[n], 0x200\n\t" \
    "s_waitcnt vmcnt(1)\n\t" \
    "v_max_u32 v240, v208, v209\n\t" \
    "v_max3_u32 v240, v210, v211, v240\n\t" \
    "s_waitcnt vmcnt(0)\n\t" \
    "v_max3_u32 v240, v212, v213, v240\n\t" \
    "v_max3_u32 v240, v214, v215, v240\n\t" \
    "v_cmp_eq_u32 vcc, -1, v240\n\t" \
    "s_cbranch_vccz .Lpd%=\n\t" \
    ".Lpl%=:\n\t" \
    "s_mov_b32 %[a], 0\n\t" \
    "v_max_u32 v240, v208, v209\n\t" \
    "v_max3_u32 v240, v210, v211, v240\n\t" \
    "v_cmp_eq_u32 vcc, -1, v240\n\t" \
    "s_cbranch_vccz .Lps0_%=\n\t" \
    "buffer_load_dwordx4 v[208:211], %[l16], %[rs], %[s0] offen offset:0 sc1\n\t" \
    "s_mov_b32 %[a], 1\n\t" \
    ".Lps0_%=:\n\t" \
    "v_max_u32 v240, v212, v213\n\t" \
    "v_max3_u32 v240, v214, v215, v240\n\t" \
    "v_cmp_eq_u32 vcc, -1, v240\n\t" \
    "s_cbranch_vccz .Lps1_%=\n\t" \
    "buffer_load_dwordx4 v[212:215], %[l16], %[rs], %[s1] offen offset:0 sc1\n\t" \
    "s_mov_b32 %[a], 1\n\t" \
    ".Lps1_%=:\n\t" \
    "s_cmp_eq_u32 %[a], 0\n\t" \
    "s_cbranch_scc1 .Lpd%=\n\t" \
    "s_waitcnt vmcnt(0)\n\t" \
    "s_sub_u32 %[n], %[n], 1\n\t" \
    "s_cmp_lg_u32 %[n], 0\n\t" \
    "s_cbranch_scc1 .Lpl%=\n\t" \
    ".Lpd%=:\n\t"
#define ASM_POLLV3 \
    "buffer_load_dwordx4 v[200:203], %[l16], %[rs], %[s0] offen offset:0 sc1\n\t" \
    "buffer_load_dwordx4 v[204:207], %[l16], %[rs], %[s1] offen offset:0 sc1\n\t" \
    "buffer_load_dwordx4 v[208:211], %[l16], %[rs], %[s2] offen offset:0 sc1\n\t" \
    "s_movk_i32 %[n], 0x200\n\t" \
    "s_waitcnt vmcnt(2)\n\t" \
    "v_max_u32 v252, v200, v201\n\t" \
    "v_max3_u32 v252, v202, v203, v252\n\t" \
    "s_waitcnt vmcnt(1)\n\t" \
    "v_max3_u32 v252, v204, v205, v252\n\t" \
    "v_max3_u32 v252, v206, v207, v252\n\t" \
    "s_waitcnt vmcnt(0)\n\t" \
    "v_max3_u32 v252, v208, v209, v252\n\t" \
    "v_max3_u32 v252, v210, v211, v252\n\t" \
    "v_cmp_eq_u32 vcc, -1, v252\n\t" \
    "s_cbranch_vccz .Lpd%=\n\t" \
    ".Lpl%=:\n\t" \
    "s_mov_b32 %[a], 0\n\t" \
    "v_max_u32 v252, v200, v201\n\t" \
    "v_max3_u32 v252, v202, v203, v252\n\t" \
    "v_cmp_eq_u32 vcc, -1, v252\n\t" \
    "s_cbranch_vccz .Lps0_%=\n\t" \
    "buffer_load_dwordx4 v[200:203], %[l16], %[rs], %[s0] offen offset:0 sc1\n\t" \
    "s_mov_b32 %[a], 1\n\t" \
    ".Lps0_%=:\n\t" \
    "v_max_u32 v252, v204, v205\n\t" \
    "v_max3_u32 v252, v206, v207, v252\n\t" \
    "v_cmp_eq_u32 vcc, -1, v252\n\t" \
    "s_cbranch_vccz .Lps1_%=\n\t" \
    "buffer_load_dwordx4 v[204:207], %[l16], %[rs], %[s1] offen offset:0 sc1\n\t" \
    "s_mov_b32 %[a], 1\n\t" \
    ".Lps1_%=:\n\t" \
    "v_max_u32 v252, v208, v209\n\t" \
    "v_max3_u32 v252, v210, v211, v252\n\t" \
    "v_cmp_eq_u32 vcc, -1, v252\n\t" \
    "s_cbranch_vccz .Lps2_%=\n\t" \
    "buffer_load_dwordx4 v[208:211], %[l16], %[rs], %[s2] offen offset:0 sc1\n\t" \
    "s_mov_b32 %[a], 1\n\t" \
    ".Lps2_%=:\n\t" \
    "s_cmp_eq_u32 %[a], 0\n\t" \
    "s_cbranch_scc1 .Lpd%=\n\t" \
    "s_waitcnt vmcnt(0)\n\t" \
    "s_sub_u32 %[n], %[n], 1\n\t" \
    "s_cmp_lg_u32 %[n], 0\n\t" \
    "s_cbranch_scc1 .Lpl%=\n\t" \
    ".Lpd%=:\n\t"
#define ASM_POLLV2 \
    "buffer_load_dwordx4 v[200:203], %[l16], %[rs], %[s0] offen offset:0 sc1\n\t" \
    "buffer_load_dwordx4 v[204:207], %[l16], %[rs], %[s1] offen offset:0 sc1\n\t" \
    "s_movk_i32 %[n], 0x200\n\t" \
    "s_waitcnt vmcnt(1)\n\t" \
    "v_max_u32 v252, v200, v201\n\t" \
    "v_max3_u32 v252, v202, v203, v252\n\t" \
    "s_waitcnt vmcnt(0)\n\t" \
    "v_max3_u32 v252, v204, v205, v252\n\t" \
    "v_max3_u32 v252, v206, v207, v252\n\t" \
    "v_cmp_eq_u32 vcc, -1, v252\n\t" \
    "s_cbranch_vccz .Lpd%=\n\t" \
    ".Lpl%=:\n\t" \
    "s_mov_b32 %[a], 0\n\t" \
    "v_max_u32 v252, v200, v201\n\t" \
    "v_max3_u32 v252, v202, v203, v252\n\t" \
    "v_cmp_eq_u32 vcc, -1, v252\n\t" \
    "s_cbranch_vccz .Lps0_%=\n\t" \
    "buffer_load_dwordx4 v[200:203], %[l16], %[rs], %[s0] offen offset:0 sc1\n\t" \
    "s_mov_b32 %[a], 1\n\t" \
    ".Lps0_%=:\n\t" \
    "v_max_u32 v252, v204, v205\n\t" \
    "v_max3_u32 v252, v206, v207, v252\n\t" \
    "v_cmp_eq_u32 vcc, -1, v252\n\t" \
    "s_cbranch_vccz .Lps1_%=\n\t" \
    "buffer_load_dwordx4 v[204:207], %[l16], %[rs], %[s1] offen offset:0 sc1\n\t" \
    "s_mov_b32 %[a], 1\n\t" \
    ".Lps1_%=:\n\t" \
    "s_cmp_eq_u32 %[a], 0\n\t" \
    "s_cbranch_scc1 .Lpd%=\n\t" \
    "s_waitcnt vmcnt(0)\n\t" \
    "s_sub_u32 %[n], %[n], 1\n\t" \
    "s_cmp_lg_u32 %[n], 0\n\t" \
    "s_cbranch_scc1 .Lpl%=\n\t" \
    ".Lpd%=:\n\t"
__device__ __forceinline__ void poll5_issue(rsrc_t rs, unsigned l16, unsigned base, u32x4 (&v)[5]) {
    asm volatile(ASM_POLL5_I : "+{v[232:235]}"(v[0]), "+{v[236:239]}"(v[1]), "+{v[240:243]}"(v[2]), "+{v[244:247]}"(v[3]), "+{v[248:251]}"(v[4]) : [l16] "v"(l16), [rs] "s"(rs), [s0] "s"(base), [s1] "s"(base + 4096) : "memory");
}
__device__ __forceinline__ void poll5(rsrc_t rs, unsigned l16, unsigned base, u32x4 (&v)[5], const Params& P) {
#pragma unroll 1
    for (int spins = 1023;; spins += 1024) {
        unsigned n, a;
        asm volatile(ASM_POLL5_C : "+{v[232:235]}"(v[0]), "+{v[236:239]}"(v[1]), "+{v[240:243]}"(v[2]), "+{v[244:247]}"(v[3]), "+{v[248:251]}"(v[4]), [n] "=&s"(n), [a] "=&s"(a) : [l16] "v"(l16), [rs] "s"(rs), [s0] "s"(base), [s1] "s"(base + 4096) : "memory", "vcc", "scc", "v252");
        if (n || spin_fail(spins, P)) break;
    }
}
using u32x8 = __attribute__((ext_vector_type(8))) unsigned;
using u32x16 = __attribute__((ext_vector_type(16))) unsigned;
using u32x32 = __attribute__((ext_vector_type(32))) unsigned;
struct WRegs { u32x32 w[6]; u32x8 t; };
#define WREGS(W) "{v[0:31]}"(W.w[0]), "{v[32:63]}"(W.w[1]), "{v[64:95]}"(W.w[2]), "{v[96:127]}"(W.w[3]), "{v[128:159]}"(W.w[4]), "{v[160:191]}"(W.w[5]), "{v[192:199]}"(W.t)
__device__ __forceinline__ u32x32 cat32(u32x8 a, u32x8 b, u32x8 c, u32x8 d) {
    const u32x16 lo = __builtin_shufflevector(a, b, 0, 1, 2, 3, 4, 5, 6, 7, 8, 9, 10, 11, 12, 13, 14, 15);
    const u32x16 hi = __builtin_shufflevector(c, d, 0, 1, 2, 3, 4, 5, 6, 7, 8, 9, 10, 11, 12, 13, 14, 15);
    return __builtin_shufflevector(lo, hi, 0, 1, 2, 3, 4, 5, 6, 7, 8, 9, 10, 11, 12, 13, 14, 15, 16, 17, 18, 19, 20, 21, 22, 23, 24, 25, 26, 27, 28, 29, 30, 31);
}
template <class F>
__device__ __forceinline__ void load_weights(WRegs& W, unsigned wl, F src) {
    u32x8 f[25];
#pragma unroll
    for (int i = 0; i < 10; ++i)
#pragma unroll
        for (int j = 0; j < 3; ++j) {
            const bf16_t* p = src(i, j);
            const u32x4 a = *(const u32x4*)p, b = *(const u32x4*)(p + 512);
            if ((i & 1) || j) f[wi(i, j)] = __builtin_shufflevector(a, b, 0, 1, 2, 3, 4, 5, 6, 7);
            else { lds_st<u32x4>(wl + (i >> 1) * 2048, a); lds_st<u32x4>(wl + (i >> 1) * 2048 + 1024, b); }
        }
#pragma unroll
    for (int k = 0; k < 6; ++k) {
        const u32x16 lo = __builtin_shufflevector(f[4 * k], f[4 * k + 1], 0, 1, 2, 3, 4, 5, 6, 7, 8, 9, 10, 11, 12, 13, 14, 15);
        const u32x16 hi = __builtin_shufflevector(f[4 * k + 2], f[4 * k + 3], 0, 1, 2, 3, 4, 5, 6, 7, 8, 9, 10, 11, 12, 13, 14, 15);
        W.w[k] = __builtin_shufflevector(lo, hi, 0, 1, 2, 3, 4, 5, 6, 7, 8, 9, 10, 11, 12, 13, 14, 15, 16, 17, 18, 19, 20, 21, 22, 23, 24, 25, 26, 27, 28, 29, 30, 31);
    }
    W.t = f[24];
    asm volatile("" : "+{v[0:31]}"(W.w[0]), "+{v[32:63]}"(W.w[1]), "+{v[64:95]}"(W.w[2]), "+{v[96:127]}"(W.w[3]), "+{v[128:159]}"(W.w[4]), "+{v[160:191]}"(W.w[5]), "+{v[192:199]}"(W.t));
}
struct C1Regs { u32x8 lw0, lw1, z1; u32x4 z0; };
struct XAcc { u32x8 a0; u32x16 a1; };
template <bool HI>
__device__ __forceinline__ void xgemm_a(XAcc& A, const WRegs& W, unsigned xa, unsigned wl) {
#define XGA_ARGS : "=&{v[200:207]}"(A.a0), "=&{v[208:223]}"(A.a1) : WREGS(W), [xa] "v"(xa), [wl] "v"(wl) : "memory", "v224", "v225", "v226", "v227", "v228", "v229", "v230", "v231", "v232", "v233", "v234", "v235", "v236", "v237", "v238", "v239", "v240", "v241", "v242", "v243", "v244", "v245", "v246", "v247", "v248", "v249", "v250", "v251", "v252"
    if (HI) asm volatile(ASM_XG_HI_A XGA_ARGS); else asm volatile(ASM_XG_LO_A XGA_ARGS);
}
template <int V>
__device__ __forceinline__ void xgemm_b(XAcc& A, C1Regs& C, const WRegs& W, unsigned xa, unsigned wl, unsigned qs) {
#define XGB_ARGS(K) : K "{v[200:207]}"(A.a0), K "{v[208:223]}"(A.a1), "=&{v[224:227]}"(C.z0), "=&{v[228:235]}"(C.lw1), "=&{v[244:251]}"(C.z1) \
                    : WREGS(W), [xa] "v"(xa), [wl] "v"(wl), [qs] "s"(qs) : "memory", "v236", "v237", "v238", "v239", "v240", "v241", "v242", "v243", "v252"
    if (V == 0) asm volatile(ASM_XG_LO_B XGB_ARGS("+"));
    if (V == 1) asm volatile(ASM_XG_HI_B XGB_ARGS("+"));
    if (V == 2) asm volatile(ASM_XG_LO_AB XGB_ARGS("=&"));
    C.lw0 = A.a0;
}
__device__ __forceinline__ unsigned c1_dst(int wave, bool ktype) {
    const int ln = lane_id(), l15 = ln & 15, hf = (wave >> 1) & 1;
    const int rb16 = ((ln >> 2) & 4) * 16 + (ln >> 5) * 32;
    return (ktype ? L_KM + (l15 >> 2) * 128 + hf * 8 + (l15 & 3) * 2
                  : L_Q + (l15 >> 3) * 256 + ((l15 >> 1) & 1) * 128 + ((l15 >> 2) & 1) * 8 + hf * 4 + (l15 & 1) * 2) + (wave & 1) * 1536 + rb16;
}
template <bool VR>
__device__ __forceinline__ void mem_stage(const Params& P, rsrc_t rr, unsigned sbase, int w, unsigned abase, u32x4 (&v)[3]) {
#define PM_IN [l16] "v"(abase), [rs] "s"(rr), [s0] "s"(sbase), [s1] "s"(sbase + 4096)
#pragma unroll 1
    for (int spins = 1023;; spins += 1024) {
        unsigned n, a;
        if (w < 2) {
            if (VR) asm volatile(ASM_POLLV3 : "=&{v[200:203]}"(v[0]), "=&{v[204:207]}"(v[1]), "=&{v[208:211]}"(v[2]), [n] "=&s"(n), [a] "=&s"(a) : PM_IN, [s2] "s"(sbase + 8192) : "memory", "vcc", "scc", "v252");
            else asm volatile(ASM_POLLS3 : "=&{v[208:211]}"(v[0]), "=&{v[212:215]}"(v[1]), "=&{v[216:219]}"(v[2]), [n] "=&s"(n), [a] "=&s"(a) : PM_IN, [s2] "s"(sbase + 8192) : "memory", "vcc", "scc", "v240");
            lds_st<u32x4>(abase + 8192, v[2]);
        } else {
            if (VR) asm volatile(ASM_POLLV2 : "=&{v[200:203]}"(v[0]), "=&{v[204:207]}"(v[1]), [n] "=&s"(n), [a] "=&s"(a) : PM_IN : "memory", "vcc", "scc", "v252");
            else asm volatile(ASM_POLLS2 : "=&{v[208:211]}"(v[0]), "=&{v[212:215]}"(v[1]), [n] "=&s"(n), [a] "=&s"(a) : PM_IN : "memory", "vcc", "scc", "v240");
        }
        lds_st<u32x4>(abase, v[0]);
        lds_st<u32x4>(abase + 4096, v[1]);
        if (__builtin_amdgcn_readfirstlane(n) || spin_fail(spins, P)) break;
    }
}
template <bool POLL>
__device__ __forceinline__ void crit1(const Params& P, const WRegs& W, C1Regs& C, rsrc_t rr, unsigned sbase, int wave, unsigned wl, unsigned dsto
) {
    unsigned abase;
    asm volatile("v_and_b32 %0, 0x3ff, %1\n\tv_or_b32 %0, %2, %0" : "=&v"(abase) : "v"(wl), "s"(wave * 1024));
    ASM_ST_DECL;
#define C1_OUT "+{v[224:227]}"(C.z0), "+{v[244:251]}"(C.z1), "+{v[200:207]}"(C.lw0), "+{v[228:235]}"(C.lw1)
#define C1_IN WREGS(W), [ab] "v"(abase), [wl] "v"(wl), [dst] "v"(dsto)
    if constexpr (POLL) {
        asm volatile("" :: "v"(dsto));
        SB();
        ST(1);
        u32x4 v[3];
        mem_stage<false>(P, rr, sbase, wave, abase, v);
        ST(2);
        asm volatile(ASM_CRIT1 : C1_OUT, "+{v[208:211]}"(v[0]) ASM_ST_OUT : C1_IN : "memory", "v212", "v213", "v214", "v215", "v216", "v217", "v218", "v219", "v220", "v221", "v222", "v223", "v236", "v237", "v238", "v239", "v240", "v241");
    } else {
        asm volatile(ASM_CRIT1_N : C1_OUT ASM_ST_OUT : C1_IN : "memory", "v208", "v209", "v210", "v211", "v212", "v213", "v214", "v215", "v216", "v217", "v218", "v219", "v220", "v221", "v222", "v223", "v236", "v237", "v238", "v239", "v240", "v241");
    }
    SB();
    ASM_ST_SAVE(3);
}
#define STARG

#define SRG 5
__device__ __forceinline__ void s_role(const Params& P, int wave, int L, int h, int cpy) {
    const rsrc_t rr = mkrsrc(P.ws + L * RG_L, RG_L);
    const char* xn = P.ws + DATA0 + L * D_L + D_XN;
    auto xdma = [&](int seg, int hb) {
        const unsigned l16 = lane_id() * 16;
        const int mt = (wave >> 1) & 1;
        const char* g = xn + seg * 40960 + mt * 20480;
        if (hb == 0) {
            dma4(g + (wave & 1) * 4096, l16, L_XA + mt * 16384 + (wave & 1) * 4096);
            dmaq<0>(g + 16384 + (wave & 1) * 1024, l16, L_XB + mt * 4096 + (wave & 1) * 1024);
        } else {
            dma4(g + 8192, l16, L_XA + mt * 16384 + 8192); dma4(g + 12288, l16, L_XA + mt * 16384 + 12288);
            dmaq<2>(g + 16384, l16, L_XB + mt * 4096); dmaq<3>(g + 16384, l16, L_XB + mt * 4096);
        }
    };
    auto xcheck = [&](int seg, int hb) {
        for (int spins = 0;; ++spins) {
            const int mt = (wave >> 1) & 1;
            const unsigned l16 = lane_id() * 16;
            const unsigned d = L_XA + mt * 16384 + (hb ? 8192 : (wave & 1) * 4096) + l16, d2 = L_XB + mt * 4096 + (hb ? 2048 : (wave & 1) * 1024) + l16;
            unsigned m = umax4(0u, lds_ld<u32x4>(d2));
            if (hb) m = umax4(m, lds_ld<u32x4>(d2 + 1024));
#pragma unroll
            for (int k = 0; k < (hb ? 8 : 4); ++k) m = umax4(m, lds_ld<u32x4>(d + k * 1024));
            if (__all(m != SENT)) break;
            __builtin_amdgcn_s_sleep(8);
            if (spin_fail(spins * 8 + 7, P)) break;
            xdma(seg, hb);
            wait_vm<0>();
        }
    };
    const float SCL = 0.07216878364870322f * 1.4426950408889634f;
    auto run = [&](auto Cc) {
    constexpr int CLS = decltype(Cc)::value;
    WRegs wf;
    {
        const int lane = lane_id();
        const bf16_t* wsrc = (const bf16_t*)(P.ws + WT_QKV) + ((size_t)((wave >> 2) * 384 + h * 12 + (wave & 3) * 3) * 20) * 512 + lane * 8;
        load_weights(wf, L_W + wave * 10240 + lane * 16, [&](int i, int j) { return wsrc + (size_t)(j * 20 + 2 * (i < 8 ? (wave ^ i) : i)) * 512; });
    }
    if (CLS != 3) { xdma(0, 0); wait_vm<0>(); if (L) xcheck(0, 0); }
    else if (wave == 4 || wave == 7) { xdma(0, 1); wait_vm<0>(); if (L) xcheck(0, 1); }
    bar();
    const unsigned qs = (CLS == 3 ? L_KX : L_QX) + (wave & 1) * 3072 + ((wave >> 1) & 1) * 8;
    unsigned xa, wl, dsto;
    if (CLS == 3) { xa = L_XA + wave * 2048 + lane_id() * 16; wl = L_W + wave * 10240 + lane_id() * 16; dsto = c1_dst(wave, true); }
    STDECL;
    for (int t = 0; t < NSEG; ++t) {
        const unsigned slot = t & (NSLOT - 1);
        C1Regs c1;
        {
            XAcc acc;
            if (CLS != 3) { const unsigned l16 = lane_id() * 16; xa = L_XA + wave * 2048 + l16; wl = L_W + wave * 10240 + l16; }
            xgemm_a<CLS == 3>(acc, wf, xa, wl);
            bar();
            ST(7);
            if (CLS != 3 && t + 1 < NSEG) xdma(t + 1, 0);
            xgemm_b<CLS == 3>(acc, c1, wf, xa, wl, qs);
            ST(6);
            if (CLS != 3) {
                if (L && t + 1 < NSEG) { wait_vm<0>(); xcheck(t + 1, 0); }
                dsto = c1_dst(wave, false);
            }
        }
        ST(0);
        crit1<CLS != 3>(P, wf, c1, rr, RG_XM + slot * XM_SLOT + cpy * XM_COPY, wave, wl, dsto STARG);
        if constexpr (CLS == 0) {
            int ln = lane_id();
            const int sidx = ((ln & 15) < 8) ? 0x4444 : 0xEEEE;
            const unsigned l16 = ln * 16;
            bf16x16 F0, F1, F2, F3;
            F0 = lds_frag(L_KX + l16, 1024); F1 = lds_frag(L_KX + 6144 + l16, 1024); F2 = lds_frag(L_KX + 2048 + l16, 1024);
            f32x4 sc[3];
#pragma unroll
            for (int j = 0; j < 3; ++j) sc[j] = (f32x4){0.f, 0.f, 0.f, 0.f};
            unsigned kmo = L_KM + (ln >> 4) * 128 + (ln & 7) * 16;
            asm volatile("s_waitcnt lgkmcnt(0)" : "+v"(kmo), "+{v[244:247]}"(sc[0]), "+{v[252:255]}"(sc[1]), "+{v[240:243]}"(sc[2]),
                         "+v"(F0), "+v"(F1), "+v"(F2) :: "memory");
            SB();
            __builtin_amdgcn_s_barrier();
            asm volatile("" ::: "memory");
            __builtin_amdgcn_s_setprio(3);
            bf16x8 qa0 = lds_ld<bf16x8>(L_Q + l16);
            bf16x8 qa1 = lds_ld<bf16x8>(L_Q + 1024 + l16);
            F3 = lds_frag(kmo, 512);
            SB();
            SMF(qa0, F0, sc[0]);
            SB();
            F0 = lds_frag(kmo + 1024, 512);
            SMF(qa0, F1, sc[1]);
            SB();
            F1 = lds_frag(L_KX + 6144 + 2048 + l16, 1024);
            SMF(qa1, F2, sc[0]);
            SB();
            F2 = lds_frag(L_KX + 4096 + l16, 1024);
            SMF(qa0, F3, sc[2]);
            SB();
            qa0 = lds_ld<bf16x8>(L_Q + 2048 + l16);
            F3 = lds_frag(L_KX + 6144 + 4096 + l16, 1024);
            SMF(qa1, F0, sc[2]);
            SB();
            F0 = lds_frag(kmo + 2048, 512);
            SMF(qa1, F1, sc[1]);
            SB();
            ln = lane_id();
            const int l15 = ln & 15;
            const int rb16 = ((ln >> 2) & 4) * 16 + (ln >> 5) * 32;
            unsigned pb = L_Q + rb16 + (l15 & 7) * 2;
            unsigned pbx = L_Q + 128 + (l15 >> 3) * 256 + ((l15 >> 1) & 1) * 128 + rb16 + ((l15 >> 2) & 1) * 8 + (l15 & 1) * 2;
            unsigned poff = (l16 + ((ln < 8) ? h * 128 : 4096 - 128 + h * 512)) | ((ln >= 40) ? 0x40000000u : 0u);
            asm volatile("" : "+v"(pb), "+v"(pbx), "+v"(poff));
            SB();
            SMF(qa0, F2, sc[0]);
            SB();
            SMF(qa0, F3, sc[1]);
            SB();
            SMF(qa0, F0, sc[2]);
            SB();
            f32x4 mz;
            asm volatile(
                "s_nop 5\n\t"
                "v_permlane32_swap_b32 v244, v246\n\t"
                "v_permlane32_swap_b32 v245, v247\n\t"
                "v_permlane32_swap_b32 v252, v254\n\t"
                "v_permlane32_swap_b32 v253, v255\n\t"
                "v_permlane32_swap_b32 v240, v242\n\t"
                "v_permlane32_swap_b32 v241, v243\n\t"
                "v_pk_add_f32 v[244:245], v[244:245], v[246:247]\n\t"
                "v_pk_add_f32 v[246:247], v[252:253], v[254:255]\n\t"
                "v_pk_add_f32 v[240:241], v[240:241], v[242:243]\n\t"
                "s_nop 0\n\t"
                "v_max3_f32 v248, v244, v246, v240\n\t"
                "v_max3_f32 v249, v245, v247, v241\n\t"
                "s_nop 0\n\t"
                "v_max_f32_dpp v248, v248, v248 quad_perm:[1,0,3,2] row_mask:0xf bank_mask:0xf\n\t"
                "v_max_f32_dpp v249, v249, v249 quad_perm:[1,0,3,2] row_mask:0xf bank_mask:0xf\n\t"
                "s_nop 0\n\t"
                "v_max_f32_dpp v248, v248, v248 quad_perm:[2,3,0,1] row_mask:0xf bank_mask:0xf\n\t"
                "v_max_f32_dpp v249, v249, v249 quad_perm:[2,3,0,1] row_mask:0xf bank_mask:0xf\n\t"
                "s_nop 0\n\t"
                "v_max_f32_dpp v248, v248, v248 row_half_mirror row_mask:0xf bank_mask:0xf\n\t"
                "v_max_f32_dpp v249, v249, v249 row_half_mirror row_mask:0xf bank_mask:0xf\n\t"
                "s_nop 0\n\t"
                "v_max_f32_dpp v248, v248, v248 row_mirror row_mask:0xf bank_mask:0xf\n\t"
                "v_max_f32_dpp v249, v249, v249 row_mirror row_mask:0xf bank_mask:0xf\n\t"
                "v_mul_f32 v248, %[scl], v248\n\t"
                "v_mul_f32 v249, %[scl], v249\n\t"
                "v_fma_f32 v244, v244, %[scl], -v248\n\t"
                "v_fma_f32 v245, v245, %[scl], -v249\n\t"
                "v_fma_f32 v246, v246, %[scl], -v248\n\t"
                "v_fma_f32 v247, v247, %[scl], -v249\n\t"
                "v_exp_f32 v244, v244\n\t"
                "v_exp_f32 v245, v245\n\t"
                "v_fma_f32 v240, v240, %[scl], -v248\n\t"
                "v_fma_f32 v241, v241, %[scl], -v249\n\t"
                "v_exp_f32 v246, v246\n\t"
                "v_exp_f32 v247, v247\n\t"
                "v_exp_f32 v240, v240\n\t"
                "v_exp_f32 v241, v241\n\t"
                "v_add_f32 v250, v244, v246\n\t"
                "v_add_f32 v251, v245, v247\n\t"
                "v_fma_f32 v250, v240, 0.5, v250\n\t"
                "v_fma_f32 v251, v241, 0.5, v251\n\t"
                "s_nop 0\n\t"
                "v_add_f32_dpp v250, v250, v250 quad_perm:[1,0,3,2] row_mask:0xf bank_mask:0xf\n\t"
                "v_add_f32_dpp v251, v251, v251 quad_perm:[1,0,3,2] row_mask:0xf bank_mask:0xf\n\t"
                "s_nop 0\n\t"
                "v_add_f32_dpp v250, v250, v250 quad_perm:[2,3,0,1] row_mask:0xf bank_mask:0xf\n\t"
                "v_add_f32_dpp v251, v251, v251 quad_perm:[2,3,0,1] row_mask:0xf bank_mask:0xf\n\t"
                "s_nop 0\n\t"
                "v_add_f32_dpp v250, v250, v250 row_half_mirror row_mask:0xf bank_mask:0xf\n\t"
                "v_add_f32_dpp v251, v251, v251 row_half_mirror row_mask:0xf bank_mask:0xf\n\t"
                "s_nop 0\n\t"
                "v_add_f32_dpp v250, v250, v250 row_mirror row_mask:0xf bank_mask:0xf\n\t"
                "v_add_f32_dpp v251, v251, v251 row_mirror row_mask:0xf bank_mask:0xf\n\t"
                "v_rcp_f32 v250, v250\n\t"
                "v_rcp_f32 v251, v251\n\t"
                "s_nop 0\n\t"
                "v_pk_mul_f32 v[244:245], v[244:245], v[250:251]\n\t"
                "v_pk_mul_f32 v[246:247], v[246:247], v[250:251]\n\t"
                "v_pk_mul_f32 v[240:241], v[240:241], v[250:251]\n\t"
                "v_cvt_pk_bf16_f32 v244, |v244|, |v245|\n\t"
                "v_cvt_pk_bf16_f32 v246, |v246|, |v247|\n\t"
                "v_cvt_pk_bf16_f32 v240, |v240|, |v241|\n\t"
                "ds_write_b16 %[pbx], v244\n\t"
                "ds_write_b16_d16_hi %[pbx], v244 offset:16\n\t"
                "ds_write_b16 %[pbx], v246 offset:4\n\t"
                "ds_write_b16_d16_hi %[pbx], v246 offset:20\n\t"
                "ds_write_b16 %[pb], v240\n\t"
                "ds_write_b16_d16_hi %[pb], v240 offset:16"
                : "+{v[240:243]}"(sc[2]), "+{v[244:247]}"(sc[0]), "=&{v[248:251]}"(mz), "+{v[252:255]}"(sc[1])
                : [pb] "v"(pb), [pbx] "v"(pbx), [scl] "s"(SCL) : "memory");
            asm volatile("s_waitcnt lgkmcnt(0)" ::: "memory");
            const u32x4 v = lds_ld<u32x4>(L_Q + l16);
#pragma unroll
            for (int c = 0; c < (int)NCP; ++c) __builtin_amdgcn_raw_buffer_store_b128(v, rr, poff, RG_XP + slot * XP_SLOT + c * XP_COPY, 16);
            asm volatile("s_nop 1" :: "v"(v) : "memory");
            __builtin_amdgcn_s_setprio(0);
            ST(5);
        } else if constexpr (CLS == 1 || CLS == 4) {
            const int ln = lane_id(), l15 = ln & 15, l4 = ln >> 4;
            constexpr int mt = (CLS == 4);
            const unsigned qa = L_QX + mt * 6144 + ln * 16, kmo = L_KM + l4 * 128 + (ln & 7) * 16;
            const unsigned xo = mt ? (h & 3) * 1024 : 5120 + ((h >> 1) & 1) * 1024 + (h & 1) * 256;
            const unsigned pm = l4 < 2 ? (mt ? 4096 : 7168) + (h & 3) * 256 - xo + l15 * 16 + l4 * 8 : OOB;
            const unsigned so = RG_PX + slot * PX_SLOT + (h >> 2) * 8192 + xo;
            f32x4 sd;
#pragma unroll
            for (int r = 0; r < 4; ++r) sd[r] = (l4 * 4 + r > l15) ? -3.0e38f : 0.f;
            ASM_ST_DECL;
#define P4_ARGS : "+{v[240:243]}"(sd) ASM_ST_OUT : WREGS(wf), [qa] "v"(qa), [km] "v"(kmo), [pm] "v"(pm), [rr] "s"(rr), [so] "s"(so), [scl] "s"(SCL) : "memory", "v200", "v201", "v202", "v203", "v204", "v205", "v206", "v207", "v208", "v209", "v210", "v211", "v212", "v213", "v214", "v215", "v216", "v217", "v218", "v219", "v220", "v221", "v222", "v223", "v224", "v225", "v226", "v227", "v228", "v229", "v230", "v231", "v232", "v233", "v234", "v235", "v236", "v237", "v238", "v239", "v244", "v245", "v246", "v247", "v248", "v249", "v250", "v251"
            if constexpr (mt) asm volatile(ASM_P4_1 P4_ARGS); else asm volatile(ASM_P4_0 P4_ARGS);
            ASM_ST_SAVE(4);
            ST(5);
        } else {
            bar();
            ST(4);
            const int ln = lane_id();
            const unsigned l16 = ln * 16;
            const int l15 = ln & 15;
            {
                if (CLS == 3 && (wave == 4 || wave == 7)) {
                    if (t + 1 < NSEG) xdma(t + 1, 1);
                    wait_vm<0>();
                    const u32x4 s4 = sent4();
                    if (wave == 4) {
                        const unsigned poff = (l16 + ((ln < 8) ? h * 128 : 4096 - 128 + h * 512)) | ((ln >= 40 || t < 1) ? 0x40000000u : 0u);
#pragma unroll
                        for (int c = 0; c < (int)NCP; ++c) st128(s4, rr, poff, RG_XP + ((t - 1) & (NSLOT - 1)) * XP_SLOT + c * XP_COPY);
                    } else {
                        const unsigned pbase = RG_PX + ((t - 2) & (NSLOT - 1)) * PX_SLOT + (h >> 2) * 8192;
                        const unsigned o2 = ((ln & 16) ? 5120 + ((h >> 1) & 1) * 1024 + (h & 1) * 256 + (ln >> 5) * 512 : 4096 + (h & 3) * 256 + (ln >> 5) * 3072) + l15 * 16;
                        if (t >= 2) { st128(s4, rr, l16, pbase + (h & 3) * 1024); st128(s4, rr, o2, pbase); }
                    }
                    if (L && t + 1 < NSEG) xcheck(t + 1, 1);
                }
                ST(5);
            }
        }
        STFLUSH(t);
    }
    };
    if (wave == 0) run(std::integral_constant<int, 0>{});
    else if (wave == 1) run(std::integral_constant<int, 1>{});
    else if (wave == 2) run(std::integral_constant<int, 4>{});
    else if (wave == 3) run(std::integral_constant<int, 2>{});
    else run(std::integral_constant<int, 3>{});
}

__device__ __forceinline__ void v_role(const Params& P, int wave, int L, int h, int cpy) {
    const rsrc_t rr = mkrsrc(P.ws + L * RG_L, RG_L);
    WRegs wf;
    STDECL;
    if (wave < 4) {
        const char* xn = P.ws + DATA0 + L * D_L + D_XN;
        auto xdma = [&](int seg) {
            const unsigned l16 = lane_id() * 16;
            const char* g = xn + seg * 40960;
            dma4(g + wave * 4096, l16, L_XA + wave * 4096);
            dma4(g + 20480 + wave * 4096, l16, L_XA + 16384 + wave * 4096);
            dmaq<0>(g + 16384 + wave * 1024, l16, L_XB + wave * 1024);
            dmaq<0>(g + 20480 + 16384 + wave * 1024, l16, L_XB + 4096 + wave * 1024);
        };
        {
            const int lane = lane_id();
            const bf16_t* wsrc = (const bf16_t*)(P.ws + WT_QKV) + ((size_t)(768 + h * 12 + wave * 3) * 20) * 512 + lane * 8;
            load_weights(wf, L_W + wave * 10240 + lane * 16, [&](int i, int j) { return wsrc + (size_t)(j * 20 + 2 * (i < 8 ? (wave ^ i) : i)) * 512; });
        }
        auto xstage = [&](int seg) {
            for (int spins = 0;; ++spins) {
                xdma(seg);
                wait_vm<0>();
                if (!L) break;
                const unsigned l16 = lane_id() * 16, d = L_XA + wave * 4096 + l16;
                unsigned m = umax4(umax4(0u, lds_ld<u32x4>(L_XB + wave * 1024 + l16)), lds_ld<u32x4>(L_XB + 4096 + wave * 1024 + l16));
#pragma unroll
                for (int k = 0; k < 4; ++k) { m = umax4(m, lds_ld<u32x4>(d + k * 1024)); m = umax4(m, lds_ld<u32x4>(d + 16384 + k * 1024)); }
                if (__all(m != SENT)) break;
                __builtin_amdgcn_s_sleep(8);
                if (spin_fail(spins * 8 + 7, P)) break;
            }
        };
        const unsigned xa = L_XA + wave * 2048 + lane_id() * 16, wl = L_W + wave * 10240 + lane_id() * 16, dsto = c1_dst(wave, false);
        const unsigned qs = L_QX + (wave & 1) * 3072 + ((wave >> 1) & 1) * 8;
        for (int t = -2; t < NSEG - 1; ++t) {
            if (t + 2 < NSEG) xstage(t + 2);
            ST(7);
            XAcc acc;
            C1Regs c1;
            xgemm_b<2>(acc, c1, wf, xa, wl, qs + (t & 1) * 12288);
            ST(6);
            if (t >= -1) {
                crit1<false>(P, wf, c1, rr, RG_XM + ((t + 1) & (NSLOT - 1)) * XM_SLOT + cpy * XM_COPY, wave, wl, dsto STARG);
                bar();
                ST(4);
                STFLUSH(t + 1);
            }
            bar();
        }
    } else {
        const int w4 = wave - 4;
        const rsrc_t rd = mkrsrc(P.ws + DATA0 + L * D_L + D_VX, VXD * VX_SLOT);
        {
            const int lane = lane_id();
            const bf16_t* wsrc = (const bf16_t*)(P.ws + WT_O) + ((size_t)(h * 40 + w4 * 10) * 6) * 512 + lane * 8;
            load_weights(wf, L_W + wave * 10240 + lane * 16, [&](int i, int j) { return wsrc + (size_t)(i * 6 + 2 * j) * 512; });
        }
        bar();
        bar();
        for (int t = 0; t < NSEG; ++t) {
            const unsigned slot = t & (NSLOT - 1);
            ST(7);
            {
                const unsigned l16 = lane_id() * 16;
                const unsigned wl = L_W + wave * 10240 + l16, va = L_QX + (t & 1) * 12288 + l16;
                unsigned so = (t & (VXD - 1)) * VX_SLOT + (unsigned)(w4 * 10 * 32 + h) * 1024;
                asm volatile(ASM_VOX : [so] "+s"(so) : WREGS(wf), [l16] "v"(l16), [wl] "v"(wl), [va] "v"(va), [rd] "s"(rd) : "memory", "scc", "v200", "v201", "v202", "v203", "v204", "v205", "v206", "v207", "v208", "v209", "v210", "v211", "v212", "v213", "v214", "v215", "v216", "v217", "v218", "v219", "v220", "v221", "v222", "v223", "v224", "v225", "v226", "v227", "v228", "v229", "v230", "v231", "v232", "v233", "v234", "v235", "v236", "v237", "v238", "v239", "v240", "v241", "v242", "v243", "v244", "v245", "v246", "v247", "v248", "v249", "v250", "v251");
            }
            ST(0);
            unsigned wl = L_W + wave * 10240 + lane_id() * 16;
            bf16x16 lw = lds_frag(wl, 1024);
            u32x32 cz = {};
            unsigned vo = ((wl >> 8) & 1) * 16384 + ((wl >> 9) & 1) * 8192 + (wl & 255);
            asm volatile("" : "+{v[212:219]}"(lw), "+{v[220:251]}"(cz), "+v"(wl), "+v"(vo));
            wait_vm<0>();
            {
                const unsigned l16 = lane_id() * 16;
                if (l16 < 512) __builtin_amdgcn_raw_buffer_store_b32(t + 1, mkrsrc(P.ws + FL0, FL_BYTES), l16 >> 2, FL_VO + (L * 128 + w4 * 32 + h) * 128, 16);
                ST(1);
                u32x4 v[3];
                mem_stage<true>(P, rr, RG_XM + slot * XM_SLOT + cpy * XM_COPY, w4, L_MEM + w4 * 1024 + l16, v);
            }
            ST(2);
            bar();
            ST(3);
            unsigned l16 = wl & 1023u;
            const int sidx = (wl & 128u) ? 0xEEEE : 0x4444;
            unsigned so = RG_VM + slot * VM_SLOT + (unsigned)((w4 * 10 * 8 + (h >> 2)) * 1024 + (h & 3) * 256);
            ASM_ST_DECL;
            asm volatile(ASM_CRIT2 : [so] "+s"(so), "+{v[212:219]}"(lw), "+{v200}"(l16), "+{v[220:251]}"(cz) ASM_ST_OUT
                         : WREGS(wf), [wl] "v"(wl), [vo] "v"(vo), [sidx] "v"(sidx), [rr] "s"(rr)
                         : "memory", "scc", "v201", "v202", "v203", "v204", "v205", "v206", "v207", "v208", "v209", "v210", "v211");
            ASM_ST_SAVE(4);
            ST(5);
            STFLUSH(t);
        }
    }
}

constexpr unsigned R_RED = 0;
constexpr unsigned R_PART = 8192;
__device__ __forceinline__ void r_role(const Params& P, int wave, int L, int j, int cpy) {
    const int lane = lane_id(), l15 = lane & 15, l4 = lane >> 4;
    const unsigned l16 = lane * 16;
    const rsrc_t rr = mkrsrc(P.ws + L * RG_L, RG_L), rd = mkrsrc(P.ws + DATA0 + L * D_L, D_L);
    const int sidx = (l15 < 8) ? 0x4444 : 0xEEEE;
    unsigned offp[3];
    offp[0] = RG_XP + cpy * XP_COPY + 4096 + wave * 2048 + l16;
    offp[1] = offp[0] + 1024;
    offp[2] = (l15 < 8) ? RG_XP + cpy * XP_COPY + wave * 512 + l4 * 128 + l15 * 16 : OOB;
    const unsigned offv = RG_VM + (j * 8 + wave) * 1024 + l16;
    const unsigned offo = (wave < (int)NCP) ? RG_XM + wave * XM_COPY + j * 256 + lane * 4 : OOB;
    const int pubw = 1 - (wave >> 2);
    {
        const int row = (lane >> 2) & 7, col = 16 * j + 4 * (lane & 3) + 2 * (lane >> 5);
        __builtin_amdgcn_raw_buffer_store_b32(nosent(*(const unsigned*)(P.mem0 + row * DM + col)), rr, offo, 0, 16);
    }
    const int rb = 4 * (l4 & 1) + 2 * (lane >> 5);
    auto red_addr = [](int fl) { return R_RED + fl * 80 + (fl >> 4) * 16; };
    const unsigned rw0 = red_addr((((l15 >> 1) & 1) * 8 + rb) * 4 + (l15 >> 2)) + (l15 & 1) * 4 + (wave ^ (l15 >> 3)) * 8;
    const unsigned rrd = red_addr(lane);
    u32x16 vx;
    const rsrc_t rf = mkrsrc(P.ws + FL0, FL_BYTES);
    const unsigned fo = (lane & 3) * 128, fs = FL_VO + (L * 128 + (j / 10) * 32 + wave * 4) * 128, vxo = (unsigned)(j * 32 + wave * 4) * 1024;
    const unsigned xo = (l4 >> 1) * 256 + l15 * 16 + (l4 & 1) * 8, pw = R_PART + wave * 1024 + l16, pr = R_PART + (wave & 1) * 8192 + l16;
    const int fin = (wave + 2) >> 3;
    asm volatile("" : "={v[160:175]}"(vx));
    auto vx_fetch = [&](int t) {
        for (int spins = 0;; ++spins) {
            const unsigned f = __builtin_amdgcn_raw_buffer_load_b32(rf, fo, fs, 16);
            if (__all(f > (unsigned)t)) break;
            __builtin_amdgcn_s_sleep(2);
            if (spin_fail(spins, P)) break;
        }
        asm volatile("buffer_load_dwordx4 v[160:163], %1, %2, %3 offen sc1\n\tbuffer_load_dwordx4 v[164:167], %1, %2, %3 offen offset:1024 sc1\n\t"
                     "buffer_load_dwordx4 v[168:171], %1, %2, %3 offen offset:2048 sc1\n\tbuffer_load_dwordx4 v[172:175], %1, %2, %3 offen offset:3072 sc1"
                     : "+{v[160:175]}"(vx) : "v"(l16), "s"(rd), "s"(D_VX + (t & (VXD - 1)) * VX_SLOT + vxo) : "memory");
    };
    vx_fetch(0);
    STDECL;
    for (int t = 0; t < NSEG; ++t) {
        const unsigned slot = t & (NSLOT - 1);
        ST(0);
        u32x4 vm = sent4(), pm = vm;
        u32x8 pv = __builtin_shufflevector(vm, vm, 0, 1, 2, 3, 0, 1, 2, 3);
#pragma unroll 1
        for (int spins = 1023;; spins += 1024) {
            unsigned n, a;
            ASM_ST_DECL;
            asm volatile(ASM_RCRIT : "+{v[180:183]}"(vm), "+{v[184:191]}"(pv), "+{v[192:195]}"(pm), [n] "=&s"(n), [a] "=&s"(a) ASM_ST_OUT ASM_ST2_OUT
                         : [rs] "s"(rr), [ov] "v"(offv), [sv] "s"(slot * VM_SLOT), [o0] "v"(offp[0]), [o1] "v"(offp[1]), [o2] "v"(offp[2]), [sp] "s"(slot * XP_SLOT),
                           "{v[160:175]}"(vx), [sidx] "v"(sidx), [rw] "v"(rw0), [rd] "v"(rrd), [oo] "v"(offo),
                           [so] "s"(((t + 1) & (NSLOT - 1)) * XM_SLOT), [pub] "s"(pubw * (NSEG - 1 - t))
                         : "memory", "vcc", "scc", "v176", "v177", "v178", "v179", "v196", "v200", "v201", "v202", "v203", "v204", "v205", "v206", "v207", "v208", "v209", "v210", "v211", "v212", "v213", "v214", "v215");
            ASM_ST_SAVE(2); ASM_ST2_SAVE(1);
            if (__builtin_amdgcn_readfirstlane(n)) break;
            if (spin_fail(spins, P)) { bar(); break; }
        }
        ST(3);
        if (wave >= 4) __builtin_amdgcn_s_sleep(RXDEL);
        unsigned early;
#pragma unroll 1
        for (int spins = 1023;; spins += 1024) {
            unsigned n, sx = D_XR + (unsigned)(t * 2 + (wave & 1)) * 20480 + j * 512;
            const unsigned pxb = RG_PX + slot * PX_SLOT + wave * 8192;
            ASM_ST_DECL;
            asm volatile(ASM_RX : "+{v[160:175]}"(vx), [n] "=&s"(n), [a] "=&s"(early), [sx] "+s"(sx) ASM_ST_OUT ASM_ST2_OUT
                         : "{v[180:183]}"(vm), "{v[184:191]}"(pv), "{v[192:195]}"(pm), [l16] "v"(l16), [rs] "s"(rr), [s0] "s"(pxb), [s1] "s"(pxb + 4096), [rd] "s"(rd), [xo] "v"(xo),
                           [fo] "v"(fo), [rf] "s"(rf), [fs] "s"(fs), [tn] "s"(t + 1), [sv] "s"(D_VX + ((t + 1) & (VXD - 1)) * VX_SLOT + vxo),
                           [pw] "v"(pw), [pr] "v"(pr), [fin] "s"(fin)
                         : "memory", "vcc", "scc", "v196", "v200", "v201", "v202", "v203", "v204", "v205", "v206", "v207", "v208", "v209", "v210", "v211", "v212", "v213", "v214", "v215", "v216", "v217", "v218", "v219", "v220", "v221", "v222", "v223", "v224", "v225", "v226", "v227", "v228", "v229", "v230", "v231", "v232", "v233", "v234", "v235", "v236", "v237", "v238", "v239", "v240", "v241", "v242", "v243", "v244", "v245", "v246", "v247", "v248", "v249", "v250", "v251");
            ASM_ST_SAVE(4); ASM_ST2_SAVE(5);
            if (__builtin_amdgcn_readfirstlane(n)) break;
            early = 0;
            if (spin_fail(spins, P)) { bar(); break; }
        }
        ST(6);
        st128(sent4(), rr, offv, slot * VM_SLOT);
        __builtin_amdgcn_raw_buffer_store_b32(SENT, rr, offo, slot * XM_SLOT, 16);
        if (wave == 5 && t + AH < NSEG) st128(sent4(), rd, (lane >> 5) * 20480 + (lane & 31) * 16, D_XA + (unsigned)(t + AH) * 40960 + j * 512);
        if (t + 1 < NSEG && !__builtin_amdgcn_readfirstlane(early)) vx_fetch(t + 1);
        ST(7);
        STFLUSH(t);
    }
}

template <int WHICH>
__device__ __forceinline__ void w_role(const Params& P, int wave, int L, int cu) {
    constexpr int NT = (WHICH == 3) ? 2 : 1;
    constexpr int KS = 20, PW = 5;
    constexpr int N = (WHICH == 3) ? FF : DM;
    constexpr unsigned W_ST = 2 * 49152;
    const int ln = lane_id(), l15 = ln & 15, l4 = ln >> 4;
    const unsigned l16 = ln * 16;
    const rsrc_t rd = mkrsrc(P.ws + DATA0 + L * D_L, D_L);
    const int nt0 = (cu * 8 + wave) * NT;
    bf16x8 wf[NT][KS];
    {
        const bf16_t* w = (const bf16_t*)(P.ws + (WHICH == 1 ? WT_1 : WHICH == 2 ? WT_2 : WT_3)) + (size_t)nt0 * KS * 512 + ln * 8;
#pragma unroll
        for (int n = 0; n < NT; ++n)
#pragma unroll
            for (int k = 0; k < KS; ++k) wf[n][k] = *(const bf16x8*)(w + (n * KS + k) * 512);
    }
    f32x4 cs[NT];
    if (WHICH != 2) {
        const bf16x8 ones = __builtin_bit_cast(bf16x8, ((u32x4){0x3F803F80u, 0x3F803F80u, 0x3F803F80u, 0x3F803F80u}));
#pragma unroll
        for (int n = 0; n < NT; ++n) {
            cs[n] = (f32x4){0.f, 0.f, 0.f, 0.f};
#pragma unroll
            for (int k = 0; k < KS; ++k) MFMA(wf[n][k], ones, cs[n]);
        }
    }
    const int grp = nt0 / 10, sh = (WHICH == 1) ? ((grp & 1) ? -1 : 1) * ((grp >> 1) + 1) : 0;
    if (WHICH == 1) {
        const int zr = (grp & 1) ? S_ - 1 - l15 : l15;
        unsigned zv;
        asm volatile("v_mov_b32 %0, 0" : "=v"(zv));
        st128(((u32x4){zv, zv, zv, zv}), rd, (l15 <= (grp >> 1) && l4 < 2) ? toff(zr, nt0 * 16 + l4 * 8, DM) : OOB, D_HS);
    }
    constexpr unsigned D_IN = WHICH == 1 ? D_XA : WHICH == 2 ? D_HS : D_X1;
    constexpr unsigned D_OUT = WHICH == 1 ? D_HS : WHICH == 2 ? D_X1 : D_H2;
    u32x4 v[PW] = {};
    poll5_issue(rd, l16, D_IN + wave * PW * 1024, v);
    wait_vm<0>();
    STDECL;
    for (int t = 0; t < NSEG; ++t) {
        const unsigned ab = (t & 1) * 49152u;
        ST(0);
        {
            poll5(rd, l16, D_IN + (unsigned)t * 40960 + wave * PW * 1024, v, P);
            ST(1);
#pragma unroll
            for (int k = 0; k < PW; ++k) lds_st<u32x4>(ab + (wave * PW + k) * 1024 + l16, v[k]);
            if (WHICH != 2) {
                float s = 0.f, q = 0.f;
                const bf16x2 one2 = __builtin_bit_cast(bf16x2, 0x3F803F80u);
#pragma unroll
                for (int k = 0; k < PW; ++k)
#pragma unroll
                    for (int e = 0; e < 4; ++e) {
                        const unsigned u = v[k][e];
                        const bf16x2 x = __builtin_bit_cast(bf16x2, u);
                        s = __builtin_amdgcn_fdot2_f32_bf16(x, one2, s, false);
                        q = __builtin_amdgcn_fdot2_f32_bf16(x, x, q, false);
                    }
                sum2_rows(s, q);
                if (ln < 16) lds_st<f32x2>(W_ST + (t & 1) * 1024 + wave * 128 + ln * 8, (f32x2){s, q});
            }
        }
        bar();
        ST(2);
        u32x2 r1[2], sv[2];
        if (WHICH == 2) {
            const unsigned ro = toff(t * SEG + l15, nt0 * 16 + l4 * 4, DM);
#pragma unroll
            for (int mt = 0; mt < 2; ++mt) { r1[mt] = LD64(rd, ro + mt * (DM * 32), D_XA); sv[mt] = LD64(rd, (t * SEG + mt * 16 + l15) * 8, D_SS); }
        }
        float mr[2], rs[2];
        f32x2 st[2][4];
        if (WHICH != 2) {
#pragma unroll
            for (int w = 0; w < 8; ++w) st[w >> 2][w & 3] = lds_ld<f32x2>(W_ST + (t & 1) * 1024 + w * 128 + l15 * 8);
        }
        auto stats = [&](int mt) {
            const f32x2 a = (st[mt][0] + st[mt][1]) + (st[mt][2] + st[mt][3]);
            const float mean = a[0] * (1.0f / DM), var = __builtin_fmaxf(a[1] * (1.0f / DM) - mean * mean, 0.f);
            const float m1 = rbf(mean), d1 = ln_den(var), r1i = rcp(d1);
            if (WHICH == 1) {
                const float m2 = rbf((mean - m1) * r1i), d2 = ln_den(var * r1i * r1i);
                rs[mt] = r1i * rcp(d2); mr[mt] = -(m1 + m2 * d1) * rs[mt];
                if (cu == 0 && wave == 0)
                    __builtin_amdgcn_raw_buffer_store_b64((u32x2){__float_as_uint(m1), __float_as_uint(r1i)}, rd, ln < 16 ? (t * SEG + mt * 16 + ln) * 8 : OOB, D_SS, 16);
            } else { rs[mt] = r1i; mr[mt] = -m1 * r1i; }
            asm volatile("" : "+v"(rs[mt]), "+v"(mr[mt]));
        };
        f32x4 acc[NT][2];
        unsigned o[2][NT][2];
        auto fin = [&](int mt, int n, int hf) {
            float v[2];
#pragma unroll
            for (int e = 0; e < 2; ++e) {
                if (WHICH != 2) v[e] = gelu_tanh(__builtin_fmaf(acc[n][mt][hf * 2 + e], rs[mt], cs[n][hf * 2 + e] * mr[mt]));
                else v[e] = rbf(((e ? hi2f(r1[mt][hf]) : lo2f(r1[mt][hf])) - __uint_as_float(sv[mt][0])) * __uint_as_float(sv[mt][1])) + acc[n][mt][hf * 2 + e];
            }
            o[mt][n][hf] = nosent(pack2(v[0], v[1]));
            asm volatile("" : "+v"(o[mt][n][hf]));
        };
        const unsigned o3 = (unsigned)((l4 & 1) * 512 + (l4 >> 1) * 256 + l15 * 16), b3 = D_H2 + (unsigned)t * 98304 + (nt0 >> 1) * 1024;
#pragma unroll
        for (int n = 0; n < NT; ++n) { acc[n][0] = (f32x4){0.f, 0.f, 0.f, 0.f}; acc[n][1] = acc[n][0]; }
        {
            constexpr int RG = 8;
            bf16x8 a[RG];
#pragma unroll
            for (int u = 0; u < RG - 1; ++u) a[u] = lds_ld<bf16x8>(ab + l16 + u * 1024);
#pragma unroll
            for (int u = 0; u < 2 * KS; ++u) {
                if (u + RG - 1 < 2 * KS) a[(u + RG - 1) % RG] = lds_ld<bf16x8>(ab + l16 + (u + RG - 1) * 1024);
                SB();
#pragma unroll
                for (int n = 0; n < NT; ++n) MFMA(wf[n][u % KS], a[u % RG], acc[n][u / KS]);
                if (WHICH != 2 && (u == 4 || u == 12)) stats(u == 12);
                if (u == (WHICH == 3 ? 22 : 6)) poll5_issue(rd, l16, D_IN + (unsigned)(t + 1) * 40960 + wave * PW * 1024, v);
                if (WHICH == 3 && u == 38) wait_vm<0>();
                if (u >= 24 && u % 4 == 0 && (u - 24) / 4 < 2 * NT) fin(0, (u - 24) / 8, ((u - 24) / 4) & 1);
                if (WHICH == 3 && u == 38) st128(merge16((u32x2){o[0][0][0], o[0][0][1]}, (u32x2){o[0][NT - 1][0], o[0][NT - 1][1]}), rd, o3, b3);
                SB();
            }
        }
        ST(3);
#pragma unroll
        for (int k = 0; k < 2 * NT; ++k) fin(1, k >> 1, k & 1);
        if (WHICH == 3) {
            st128(merge16((u32x2){o[1][0][0], o[1][0][1]}, (u32x2){o[1][NT - 1][0], o[1][NT - 1][1]}), rd, o3, b3 + 49152);
            ST(4);
            if (t + AH < NSEG) { st128(sent4(), rd, l16, b3 + AH * 98304); st128(sent4(), rd, l16, b3 + AH * 98304 + 49152); }
        } else {
            wait_vm<0>();
            const int orow = t * SEG + (l4 & 1) * 16 + l15 + sh;
            st128(merge16((u32x2){o[0][0][0], o[0][0][1]}, (u32x2){o[1][0][0], o[1][0][1]}), rd, (unsigned)orow < (unsigned)S_ ? toff(orow, nt0 * 16 + (l4 >> 1) * 8, DM) : OOB, D_OUT);
            ST(4);
            if (t + AH < NSEG) st128(sent4(), rd, orow + AH * SEG < S_ ? toff(orow + AH * SEG, nt0 * 16 + (l4 >> 1) * 8, DM) : OOB, D_OUT);
        }
        STFLUSH(t);
    }
    wait_vm<0>();
    asm volatile("" :: "v"(v[0]), "v"(v[1]), "v"(v[2]), "v"(v[3]), "v"(v[4]));
}

__device__ __forceinline__ void w4_role(const Params& P, int wave, int L, int cu) {
    const int ln = lane_id(), l15 = ln & 15, l4 = ln >> 4;
    const unsigned l16 = ln * 16;
    const rsrc_t rd = mkrsrc(P.ws + DATA0 + L * D_L, D_L);
    STDECL;
    if (wave < 6) {
        u32x32 w[5];
        {
            const char* ws = P.ws + WT_4 + (size_t)(cu * 5 * 48 + wave * 8) * 1024 + l16;
#pragma unroll
            for (int n = 0; n < 5; ++n) {
                u32x8 f[4];
#pragma unroll
                for (int i = 0; i < 4; ++i) f[i] = __builtin_shufflevector(*(const u32x4*)(ws + n * 49152 + i * 2048), *(const u32x4*)(ws + n * 49152 + i * 2048 + 1024), 0, 1, 2, 3, 4, 5, 6, 7);
                w[n] = cat32(f[0], f[1], f[2], f[3]);
            }
            asm volatile("" : "+{v[0:31]}"(w[0]), "+{v[32:63]}"(w[1]), "+{v[64:95]}"(w[2]), "+{v[96:127]}"(w[3]), "+{v[128:159]}"(w[4]));
        }
        unsigned budget = 1u << 17;
        for (int t = 0; t < NSEG; ++t) {
            ST(0);
            const unsigned gb = D_H2 + (unsigned)t * 98304 + wave * 8192;
            u32x4 can;
            for (int spins = 0;; ++spins) {
                can = LD128(rd, l16, gb + 7168);
                if (__all(umax4(0u, can) != SENT)) break;
                __builtin_amdgcn_s_sleep(1);
                if (spin_fail(spins, P)) { budget = 0; break; }
            }
            ST(1);
            asm volatile(ASM_W4_M : [n] "+s"(budget)
                         : "{v[188:191]}"(can), "{v[0:31]}"(w[0]), "{v[32:63]}"(w[1]), "{v[64:95]}"(w[2]), "{v[96:127]}"(w[3]), "{v[128:159]}"(w[4]),
                           [ld] "v"(l16 + wave * 1024 + (t & 1) * 61440), [l16] "v"(l16), [rs] "s"(rd), [s0] "s"(gb), [s1] "s"(gb + 4096), [s2] "s"(gb + 49152), [s3] "s"(gb + 53248)
                         : "memory", "vcc", "scc", "v160", "v161", "v162", "v163", "v164", "v165", "v166", "v167", "v168", "v169", "v170", "v171", "v172", "v173", "v174", "v175", "v176", "v177", "v178", "v179", "v180", "v181", "v182", "v183", "v184", "v185", "v186", "v187", "v192", "v193", "v194", "v195", "v196", "v197", "v198", "v199", "v200", "v201", "v202", "v203", "v204", "v205", "v206", "v207", "v208", "v209", "v210", "v211", "v212", "v213", "v214", "v215", "v216", "v217", "v218", "v219", "v220", "v221", "v222", "v223", "v224", "v225", "v226", "v227", "v228", "v229", "v230", "v231", "v232", "v233", "v234", "v235", "v236", "v237", "v238", "v239", "v240", "v241", "v242", "v243", "v244");
            ST(2);
            bar();
            ST(3);
            STFLUSH(t);
        }
    } else {
        const int mt = wave - 6;
        const rsrc_t rn = mkrsrc(P.ws + DATA0 + D_L, D_L), rr = mkrsrc(P.ws, RG_L), ro = mkrsrc(P.out, ACT);
        const unsigned o8 = (l4 >> 1) * 256 + l15 * 16 + (l4 & 1) * 8, o16 = (l4 & 1) * 512 + (l4 >> 1) * 256 + l15 * 16;
        const unsigned q8 = l15 * (DM * 2) + l4 * 8, q16 = l15 * (DM * 2) + (l4 & 1) * 32 + (l4 >> 1) * 16;
        for (int t = 0; t < NSEG; ++t) {
            const unsigned sb = (unsigned)(t * 2 + mt) * 20480 + cu * 2560;
            ST(0);
            f32x4 res[5];
            for (int spins = 0;; ++spins) {
                u32x2 r1[5], r2[5];
                unsigned m = 0;
#pragma unroll
                for (int n = 0; n < 5; ++n) { r1[n] = LD64(rd, o8 + n * 512, D_X1 + sb); r2[n] = LD64(rd, o8 + n * 512, D_XA + sb); }
#pragma unroll
                for (int n = 0; n < 5; ++n) {
                    m = umax4(m, (u32x4){r1[n][0], r1[n][1], r2[n][0], r2[n][1]});
                    res[n] = (f32x4){lo2f(r1[n][0]), hi2f(r1[n][0]), lo2f(r1[n][1]), hi2f(r1[n][1])} + (f32x4){lo2f(r2[n][0]), hi2f(r2[n][0]), lo2f(r2[n][1]), hi2f(r2[n][1])};
                }
                if (__all(m != SENT)) break;
                __builtin_amdgcn_s_sleep(8);
                if (spin_fail(spins * 4 + 3, P)) break;
            }
            asm volatile("" : "+v"(res[0]), "+v"(res[1]), "+v"(res[2]), "+v"(res[3]), "+v"(res[4]));
            ST(1);
            bar();
            ST(2);
            {
                const unsigned la = (t & 1) * 61440 + mt * 30720 + l16;
                f32x4 c[30];
#pragma unroll
                for (int i = 0; i < 30; ++i) c[i] = lds_ld<f32x4>(la + ((i % 5) * 6 + i / 5) * 1024);
#pragma unroll
                for (int i = 0; i < 30; ++i) { res[i % 5] += c[i]; asm volatile("" : "+v"(res[i % 5])); }
            }
            u32x2 o[5];
#pragma unroll
            for (int n = 0; n < 5; ++n) o[n] = (u32x2){pack2(res[n][0], res[n][1]), pack2(res[n][2], res[n][3])};
            if (L) {
                const unsigned ob = (unsigned)(t * 2 + mt) * 20480 + cu * 160;
                st128_<0>(merge16(o[0], o[1]), ro, q16, ob); st128_<0>(merge16(o[2], o[3]), ro, q16, ob + 64);
                __builtin_amdgcn_raw_buffer_store_b64(o[4], ro, q8, ob + 128, 0);
            } else {
                float s = 0.f, q = 0.f;
                const bf16x2 one2 = __builtin_bit_cast(bf16x2, 0x3F803F80u);
#pragma unroll
                for (int e = 0; e < 10; ++e) {
                    const unsigned u = o[e >> 1][e & 1];
                    const bf16x2 x = __builtin_bit_cast(bf16x2, u);
                    s = __builtin_amdgcn_fdot2_f32_bf16(x, one2, s, false);
                    q = __builtin_amdgcn_fdot2_f32_bf16(x, x, q, false);
                }
                sum2_rows(s, q);
                const unsigned st = RG_ST + (unsigned)(t * 2 + mt) * 1024;
                __builtin_amdgcn_raw_buffer_store_b64((u32x2){nosent(__float_as_uint(s)), nosent(__float_as_uint(q))}, rr, ln < 16 ? ln * 8 : OOB, st + cu * 128, 16);
                ST(3);
                st128(merge16(o[0], o[1]), rn, o16, D_XR + sb); st128(merge16(o[2], o[3]), rn, o16, D_XR + sb + 1024);
                __builtin_amdgcn_raw_buffer_store_b64(o[4], rn, o8, D_XR + sb + 2048, 16);
                __builtin_amdgcn_s_sleep(W4SL);
                for (int spins = 0;; ++spins) {
                    const u32x2 a = LD64(rr, ln * 8, st), b = LD64(rr, ln * 8 + 512, st);
                    s = __uint_as_float(a[0]) + __uint_as_float(b[0]); q = __uint_as_float(a[1]) + __uint_as_float(b[1]);
                    if (__all(umax4(0u, (u32x4){a[0], a[1], b[0], b[1]}) != SENT)) break;
                    asm volatile("" ::: "memory");
                    if (spin_fail(spins, P)) break;
                }
                ST(4);
                sum2_rows(s, q);
                const float mean = s * (1.0f / DM), var = __builtin_fmaxf(q * (1.0f / DM) - mean * mean, 0.f);
                const float r = rcp(ln_den(var)), mr = -rbf(mean) * r;
#pragma unroll
                for (int n = 0; n < 5; ++n)
                    o[n] = (u32x2){nosent(pack2(__builtin_fmaf(res[n][0], r, mr), __builtin_fmaf(res[n][1], r, mr))), nosent(pack2(__builtin_fmaf(res[n][2], r, mr), __builtin_fmaf(res[n][3], r, mr)))};
                st128(merge16(o[0], o[1]), rn, o16, D_XN + sb); st128(merge16(o[2], o[3]), rn, o16, D_XN + sb + 1024);
                __builtin_amdgcn_raw_buffer_store_b64(o[4], rn, o8, D_XN + sb + 2048, 16);
                if (t + AH < NSEG) {
                    const u32x4 s4 = sent4();
                    st128(s4, rn, l16, D_XN + sb + AH * 40960); st128(s4, rn, l16, D_XN + sb + AH * 40960 + 1024);
                    st128(s4, rn, ln < 32 ? l16 : OOB, D_XN + sb + AH * 40960 + 2048);
                }
            }
            ST(5);
            STFLUSH(t);
        }
    }
}

__global__ __launch_bounds__(512, 2) void mega_kernel(Params P) {
    const int wg = blockIdx.x;
    const int wave = __builtin_amdgcn_readfirstlane(threadIdx.x >> 6);
    const int L = (wg >> 3) & 1, x = wg & 3, r = (wg >> 4) * 2 + ((wg >> 2) & 1), cpy = wg & (NCP - 1);
    if (r < 8) s_role(P, wave, L, x * 8 + r, cpy);
    else if (r < 16) v_role(P, wave ^ 4, L, x * 8 + r - 8, cpy);
    else if (r < 26) r_role(P, wave, L, (r - 16) * 4 + x, cpy);
    else {
        const int i = (r - 26) * 4 + x;
        if (i < 5) w_role<1>(P, wave, L, i);
        else if (i < 10) w_role<2>(P, wave, L, i - 5);
        else if (i < 16) w_role<3>(P, wave, L, i - 10);
        else w4_role(P, wave, L, i - 16);
    }
}

__device__ __forceinline__ void xtile_body(int mt, const bf16_t* __restrict__ x, char* __restrict__ ws) {
    __shared__ float red[2][16][17];
    const int r15 = threadIdx.x & 15, slot = threadIdx.x >> 4;
    u32x4 v[5];
    float s = 0.f;
#pragma unroll
    for (int c = 0; c < 5; ++c) {
        v[c] = *(const u32x4*)(x + (size_t)(mt * 16 + r15) * DM + (slot * 5 + c) * 8);
#pragma unroll
        for (int e = 0; e < 4; ++e) s += lo2f(v[c][e]) + hi2f(v[c][e]);
    }
    red[0][slot][r15] = s;
    __syncthreads();
    s = 0.f;
#pragma unroll
    for (int k = 0; k < 16; ++k) s += red[0][k][r15];
    const float mean = s * (1.0f / DM);
    float q = 0.f;
#pragma unroll
    for (int c = 0; c < 5; ++c)
#pragma unroll
        for (int e = 0; e < 4; ++e) { const float a = lo2f(v[c][e]) - mean, b = hi2f(v[c][e]) - mean; q += a * a + b * b; }
    red[1][slot][r15] = q;
    __syncthreads();
    q = 0.f;
#pragma unroll
    for (int k = 0; k < 16; ++k) q += red[1][k][r15];
    const float m1 = rbf(mean), r = 1.0f / ln_den(q * (1.0f / DM));
#pragma unroll
    for (int c = 0; c < 5; ++c) {
        const unsigned o = (unsigned)(mt * 80 + slot * 5 + c) * 256 + r15 * 16;
        *(u32x4*)(ws + DATA0 + D_XR + o) = v[c];
        u32x4 n;
#pragma unroll
        for (int e = 0; e < 4; ++e) n[e] = pack2(rbf(lo2f(v[c][e]) - m1) * r, rbf(hi2f(v[c][e]) - m1) * r);
        *(u32x4*)(ws + DATA0 + D_XN + o) = n;
    }
}

struct Prep1 { TileJobs J; const bf16_t* x; char* ws; };
__global__ __launch_bounds__(256) void prep1_kernel(Prep1 A) {
    int b = blockIdx.x;
    if (b < 256) {
        for (unsigned i = b * 256 + threadIdx.x; i < FL0 / 16; i += 256 * 256) ((u32x4*)A.ws)[i] = (u32x4){SENT, SENT, SENT, SENT};
        if (b == 0) for (unsigned i = threadIdx.x; i < FL_BYTES / 16; i += 256) ((u32x4*)(A.ws + FL0))[i] = (u32x4){0u, 0u, 0u, 0u};
        for (int u = b >> 3; u < 10; u += 32) {
            if (u == 0) continue;
            const unsigned n16 = (AH + 1) * ((u % 5 == 4) ? 98304 : 40960) / 16;
            u32x4* p = (u32x4*)(A.ws + DATA0 + (u / 5) * D_L + (u % 5 + 1) * ACT);
            for (unsigned i = (b & 7) * 256 + threadIdx.x; i < n16; i += 2048) p[i] = (u32x4){SENT, SENT, SENT, SENT};
        }
        return;
    }
    b -= 256;
    if (b < S_ / 16) { xtile_body(b, A.x, A.ws); return; }
    tile_body(A.J, b - S_ / 16);
}

extern "C" void kernel_launch(void* const* d_in, const int* in_sizes, int n_in, void* d_out, int out_size, void* d_ws,
                              size_t ws_size, hipStream_t stream) {
    char* ws = (char*)d_ws;
    Params P;
    P.ws = ws; P.mem0 = (const bf16_t*)d_in[1]; P.out = (bf16_t*)d_out;
    (void)hipFuncSetAttribute((const void*)mega_kernel, hipFuncAttributeMaxDynamicSharedMemorySize, MEGA_LDS);

    Prep1 A1;
    int blk = 0;
    auto job = [&](int i, int in, unsigned o, int Kd, int Nd, int ksh) { A1.J.j[i] = TileJob{(const bf16_t*)d_in[in], (bf16_t*)(ws + o), i == 3 ? 1 : 0, Nd, ksh, blk}; blk += (Kd / 32) * (Nd / 128); };
    job(0, 2, WT_QKV, DM, HD, 20);
    job(1, 3, WT_QKV + HD * DM * 2, DM, HD, 20);
    job(2, 4, WT_QKV + 2 * HD * DM * 2, DM, HD, 20);
    job(3, 5, WT_O, HD, DM, 6);
    job(4, 8, WT_1, DM, DM, 20);
    job(5, 9, WT_2, DM, DM, 20);
    job(6, 10, WT_3, DM, FF, 20);
    job(7, 11, WT_4, FF, DM, 48);
    A1.x = (const bf16_t*)d_in[0]; A1.ws = ws;
    prep1_kernel<<<256 + S_ / 16 + blk, 256, 0, stream>>>(A1);
    mega_kernel<<<256, 512, MEGA_LDS, stream>>>(P);
}
```

```cpp
#include <hip/hip_runtime.h>
#include <stdint.h>
#include <type_traits>

typedef unsigned short bf16_t;
using bf16x2 = __attribute__((ext_vector_type(2))) __bf16;
using bf16x8 = __attribute__((ext_vector_type(8))) __bf16;
using bf16x16 = __attribute__((ext_vector_type(16))) __bf16;
using f32x2 = __attribute__((ext_vector_type(2))) float;
using f32x4 = __attribute__((ext_vector_type(4))) float;
using u32x2 = __attribute__((ext_vector_type(2))) unsigned;
using u32x4 = __attribute__((ext_vector_type(4))) unsigned;
typedef __amdgpu_buffer_rsrc_t rsrc_t;

__device__ __forceinline__ unsigned pack2(float a, float b) {
    bf16x2 r = __builtin_convertvector((f32x2){a, b}, bf16x2);
    return __builtin_bit_cast(unsigned, r);
}
__device__ __forceinline__ float rbf(float f) { return __uint_as_float(pack2(f, 0.f) << 16); }
__device__ __forceinline__ float lo2f(unsigned u) { return __uint_as_float(u << 16); }
__device__ __forceinline__ float hi2f(unsigned u) { return __uint_as_float(u & 0xFFFF0000u); }
__device__ __forceinline__ rsrc_t mkrsrc(const void* p, unsigned bytes) {
    return __builtin_amdgcn_make_buffer_rsrc((void*)p, 0, bytes, 0x00020000);
}

constexpr int S_ = 2048, DM = 640, NH = 32, DH = 192, HD = NH * DH  , SEG = 32, NSEG = 64;
constexpr int FF = 1536;

#define NCPY 4
constexpr unsigned NSLOT = 4, NCP = NCPY;
constexpr unsigned WIN = 16384, REG = 2u << 20, REGS = REG + 8192;
constexpr unsigned XP_COPY = 3 * WIN, XP_SLOT = NCP * XP_COPY;
constexpr unsigned XM_COPY = 2 * WIN, XM_SLOT = NCP * XM_COPY;
constexpr unsigned VM_SLOT = REGS;
constexpr unsigned PX_SLOT = 8 * WIN;
constexpr unsigned RG_XM = 0, RG_XP = RG_XM + NSLOT * XM_SLOT, RG_PX = RG_XP + NSLOT * XP_SLOT, RG_ST = RG_PX + NSLOT * PX_SLOT, RG_VM = 2 * REG;
static_assert(RG_ST + 16 * WIN <= REG, "ring region");
constexpr unsigned FL0 = REG, FL_PAR = REG + 4096;
constexpr unsigned FL_VO = RG_VM + 48 * WIN, FL_XN = FL_VO + 2 * WIN, D_SS = FL_VO + 4 * WIN, CAL0 = RG_VM + 64 * WIN;
constexpr unsigned ACT = S_ * DM * 2;
constexpr unsigned D_XR = 6 * REG, D_XN = 10 * REG, D_XA = 14 * REG, D_HS = 18 * REG, D_X1 = 22 * REG, D_H2 = 26 * REG, D_VX = 34 * REG;
__host__ __device__ constexpr unsigned segb(unsigned seg) { return seg * (5 * WIN) + (seg >> 4) * (REGS - 80 * WIN); }
__host__ __device__ constexpr unsigned segh(unsigned seg) { return seg * (12 * WIN) + (seg >> 3) * (REGS - 96 * WIN); }
#define PXROT 5
#define W4SL 6
#define RXDEL 4
#define VXD 16
#define VXAUX 16
__host__ __device__ constexpr unsigned vxb(unsigned q, unsigned t) { return D_VX + q * (VXD / 2 * REG) + (t & (VXD - 1)) * (REG / 2) + (t & 2) * 4096; }
constexpr int AH = 3;
constexpr unsigned WT0 = D_VX + 2 * VXD * REG, RG_L = WT0;
constexpr unsigned WT_QKV = WT0, WT_O = WT_QKV + 3 * HD * DM * 2, WT_1 = WT_O + HD * DM * 2, WT_2 = WT_1 + DM * DM * 2;
constexpr unsigned WT_3 = WT_2 + DM * DM * 2, WT_4 = WT_3 + DM * FF * 2, WS_END = WT_4 + DM * FF * 2;

struct TileJob { const bf16_t* W; bf16_t* out; int perm, N, ksh, blk0; };
struct TileJobs { TileJob j[8]; };
__device__ __forceinline__ void tile_body(const TileJobs& J, int b) {
    __shared__ __attribute__((aligned(16))) bf16_t t[32][136];
    int ji = 0;
#pragma unroll
    for (int i = 1; i < 8; ++i) if (b >= J.j[i].blk0) ji = i;
    const bf16_t* W = J.j[ji].W; bf16_t* out = J.j[ji].out;
    const int N = J.j[ji].N, ksh = J.j[ji].ksh, perm = J.j[ji].perm;
    b -= J.j[ji].blk0;
    const int nb = N / 128;
    const int ksg = b / nb, n0 = (b % nb) * 128;
    const int tid = threadIdx.x;
    const int grp = ksg / ksh, ks = ksg % ksh;
#pragma unroll
    for (int i = 0; i < 2; ++i) {
        const int u = tid + i * 256, r = u >> 4, c = u & 15;
        const int row = perm ? grp * DH + ks * 16 + (r >> 4) * 96 + (r & 15) : ksg * 32 + r;
        *(u32x4*)&t[r][c * 8] = *(const u32x4*)(W + (size_t)row * N + n0 + c * 8);
    }
    __syncthreads();
    const int lane = tid & 63;
#pragma unroll
    for (int i = 0; i < 2; ++i) {
        const int ntl = (tid >> 6) + i * 4, n = ntl * 16 + (lane & 15);
        unsigned w[4];
#pragma unroll
        for (int j = 0; j < 4; ++j) {
            const int kk = perm ? (j >> 1) * 16 + (lane >> 4) * 4 + (j & 1) * 2 : (lane >> 4) * 8 + 2 * j;
            w[j] = (unsigned)t[kk][n] | ((unsigned)t[kk + 1][n] << 16);
        }
        *(u32x4*)(out + ((size_t)(grp * (N / 16) + n0 / 16 + ntl) * ksh + ks) * 512 + lane * 8) = (u32x4){w[0], w[1], w[2], w[3]};
    }
}

#define SB() __builtin_amdgcn_sched_barrier(0)
#define LAS __attribute__((address_space(3)))
template <class T> __device__ __forceinline__ T lds_ld(unsigned a) { return *(const LAS T*)(a); }
template <class T> __device__ __forceinline__ void lds_st(unsigned a, T v) { *(LAS T*)(a) = v; }
constexpr unsigned OOB = 0x7FFFFF00u;
template <int N> __device__ __forceinline__ void wait_vm() { asm volatile("s_waitcnt vmcnt(%0)" :: "i"(N) : "memory"); }
__device__ __forceinline__ void bar() {
    asm volatile("s_waitcnt lgkmcnt(0)" ::: "memory");
    __builtin_amdgcn_s_barrier();
    asm volatile("" ::: "memory");
}
__device__ __forceinline__ int lane_id() {
    int ln;
    asm volatile("v_mbcnt_lo_u32_b32 %0, -1, 0\n\tv_mbcnt_hi_u32_b32 %0, -1, %0" : "=v"(ln));
    return ln;
}
__device__ __forceinline__ void swap16(unsigned& a, unsigned& b) { asm volatile("s_nop 3\n\tv_permlane16_swap_b32 %0, %1\n\ts_nop 1" : "+v"(a), "+v"(b)); }
__device__ __forceinline__ void swap32(unsigned& a, unsigned& b) { asm volatile("s_nop 3\n\tv_permlane32_swap_b32 %0, %1\n\ts_nop 1" : "+v"(a), "+v"(b)); }
__device__ __forceinline__ float sum_x16(float x) { unsigned a = __float_as_uint(x), b = a; swap16(a, b); return __uint_as_float(a) + __uint_as_float(b); }
__device__ __forceinline__ float sum_x32(float x) { unsigned a = __float_as_uint(x), b = a; swap32(a, b); return __uint_as_float(a) + __uint_as_float(b); }
__device__ __forceinline__ float max_x16(float x) { unsigned a = __float_as_uint(x), b = a; swap16(a, b); return __builtin_fmaxf(__uint_as_float(a), __uint_as_float(b)); }
__device__ __forceinline__ float max_x32(float x) { unsigned a = __float_as_uint(x), b = a; swap32(a, b); return __builtin_fmaxf(__uint_as_float(a), __uint_as_float(b)); }
__device__ __forceinline__ void sum2_rows(float& s, float& q) {
    unsigned a = __float_as_uint(s), b = a, c = __float_as_uint(q), d = c;
    asm volatile("s_nop 2\n\tv_permlane16_swap_b32 %0, %1\n\tv_permlane16_swap_b32 %2, %3\n\tv_add_f32 %0, %0, %1\n\tv_add_f32 %2, %2, %3\n\tv_mov_b32 %1, %0\n\tv_mov_b32 %3, %2\n\t"
                 "s_nop 1\n\tv_permlane32_swap_b32 %0, %1\n\tv_permlane32_swap_b32 %2, %3\n\tv_add_f32 %0, %0, %1\n\tv_add_f32 %2, %2, %3" : "+v"(a), "+v"(b), "+v"(c), "+v"(d));
    s = __uint_as_float(a); q = __uint_as_float(c);
}
__device__ __forceinline__ u32x4 merge16(u32x2 a, u32x2 b) {
    unsigned a0 = a[0], a1 = a[1], b0 = b[0], b1 = b[1];
    asm volatile("s_nop 3\n\tv_permlane16_swap_b32 %0, %2\n\tv_permlane16_swap_b32 %1, %3\n\ts_nop 1" : "+v"(a0), "+v"(a1), "+v"(b0), "+v"(b1));
    return (u32x4){a0, a1, b0, b1};
}
template <int AUXV>
__device__ __forceinline__ void st128_(u32x4 v, rsrc_t r, unsigned voff, unsigned soff) {
    __builtin_amdgcn_raw_buffer_store_b128(v, r, voff, soff, AUXV);
    asm volatile("s_nop 1" :: "v"(v) : "memory");
}
#define st128(V, R, VO_, SO_) st128_<16>(V, R, VO_, SO_)
#define LD128(R, VO_, SO_) __builtin_amdgcn_raw_buffer_load_b128(R, VO_, SO_, 16)
#define LD64(R, VO_, SO_) __builtin_amdgcn_raw_buffer_load_b64(R, VO_, SO_, 16)
#define BF8(X) __builtin_bit_cast(bf16x8, X)
__device__ __forceinline__ float gelu_tanh(float x) {
    const float c0 = -2.0f * 0.7978845608028654f * 1.4426950408889634f, c1 = c0 * 0.044715f;
    const float e = __builtin_amdgcn_exp2f(x * __builtin_fmaf(x * x, c1, c0));
    return x * __builtin_amdgcn_rcpf(1.0f + e);
}
#define MFMA(A_, B_, C_) C_ = __builtin_amdgcn_mfma_f32_16x16x32_bf16(A_, B_, C_, 0, 0, 0)
#define SMF(A_, B_, C_) C_ = __builtin_amdgcn_smfmac_f32_16x16x64_bf16(A_, B_, C_, sidx, 0, 0)
__device__ __forceinline__ float ln_den(float var) { return rbf(__builtin_amdgcn_sqrtf(rbf(rbf(var) + 1e-5f))); }
__device__ __forceinline__ float rcp(float x) { return __builtin_amdgcn_rcpf(x); }

struct Params {
    char* ws;
    const bf16_t* mem0;
    bf16_t* out;
    unsigned par, pad;
};

#define RLX_AGENT __ATOMIC_RELAXED, __HIP_MEMORY_SCOPE_AGENT
#define SENT 0xFFFFFFFFu
constexpr int SPIN_CAP = 400000;
__device__ __forceinline__ bool spin_fail(int spins, const Params& P) {
    if ((spins & 1023) == 1023) {
        const rsrc_t rt = mkrsrc(P.ws + FL0, 512);
        unsigned o;
        asm volatile("v_mov_b32 %0, 0x100" : "=v"(o));
        if (__builtin_amdgcn_readfirstlane(__builtin_amdgcn_raw_buffer_load_b32(rt, o, 0, 16)) != 0u) return true;
        if (spins > SPIN_CAP) { __builtin_amdgcn_raw_buffer_store_b32(o, rt, o, 0, 16); return true; }
    }
    return false;
}
__device__ __forceinline__ bool ok4(u32x4 v) { return v[0] != SENT && v[1] != SENT && v[2] != SENT && v[3] != SENT; }
__device__ __forceinline__ unsigned nosent(unsigned d) { return d < 0xFFFFFFFEu ? d : 0xFFFFFFFEu; }
__device__ __forceinline__ u32x4 nosent4(u32x4 v) { return (u32x4){nosent(v[0]), nosent(v[1]), nosent(v[2]), nosent(v[3])}; }
__device__ __forceinline__ unsigned umax3(unsigned a, unsigned b, unsigned c) { const unsigned m = a > b ? a : b; return m > c ? m : c; }
__device__ __forceinline__ unsigned umax4(unsigned m, u32x4 v) { return umax3(umax3(m, v[0], v[1]), v[2], v[3]); }
__device__ __forceinline__ u32x4 sent4() { unsigned sv; asm volatile("v_mov_b32 %0, -1" : "=v"(sv)); return (u32x4){sv, sv, sv, sv}; }
template <int N>
__device__ __forceinline__ void poll(rsrc_t rs, unsigned base, const unsigned (&off)[N], u32x4 (&v)[N], const Params& P) {
    for (int spins = 0;; ++spins) {
        unsigned m = 0;
#pragma unroll
        for (int k = 0; k < N; ++k) v[k] = LD128(rs, off[k], base);
#pragma unroll
        for (int k = 0; k < N; ++k) m = umax4(m, v[k]);
        if (__all(m != SENT)) return;
        asm volatile("" ::: "memory");
        if (spin_fail(spins, P)) return;
    }
}
__device__ __forceinline__ bf16x16 cat16(bf16x8 a, bf16x8 b) {
    return __builtin_shufflevector(a, b, 0, 1, 2, 3, 4, 5, 6, 7, 8, 9, 10, 11, 12, 13, 14, 15);
}
__device__ __forceinline__ bf16x8 half8(bf16x16 v, int h) {
    return h ? __builtin_shufflevector(v, v, 8, 9, 10, 11, 12, 13, 14, 15) : __builtin_shufflevector(v, v, 0, 1, 2, 3, 4, 5, 6, 7);
}
__device__ __forceinline__ bf16x16 lds_frag(unsigned a, unsigned step) { return cat16(lds_ld<bf16x8>(a), lds_ld<bf16x8>(a + step)); }
template <int I>
__device__ __forceinline__ void dmaq(const char* g, unsigned l16, unsigned dst) {
    asm volatile("s_mov_b32 m0, %2\n\ts_nop 0\n\tglobal_load_lds_dwordx4 %0, %1 offset:%3 sc1" :: "v"(l16), "s"(g), "s"(dst), "i"(I * 1024) : "memory");
}
__device__ __forceinline__ void dma4(const char* g, unsigned l16, unsigned dst) {
    asm volatile("s_mov_b32 m0, %2\n\ts_nop 0\n\tglobal_load_lds_dwordx4 %0, %1 sc1\n\tglobal_load_lds_dwordx4 %0, %1 offset:1024 sc1\n\t"
                 "global_load_lds_dwordx4 %0, %1 offset:2048 sc1\n\tglobal_load_lds_dwordx4 %0, %1 offset:3072 sc1" :: "v"(l16), "s"(g), "s"(dst) : "memory");
}
__device__ __forceinline__ unsigned toff(int row, int col) {
    return segb((unsigned)row >> 5) + (unsigned)(col >> 7) * WIN + (unsigned)(row & 16) * 256u + (unsigned)((col >> 3) & 15) * 256u + (unsigned)(row & 15) * 16u + (unsigned)(col & 7) * 2u;
}
__device__ __forceinline__ char* lbase(const Params& P, int L) { return P.ws + ((P.par ^ L) << 13); }

#define ASM_ST
#define ASM_ST_DECL
#define ASM_ST_OUT
#define ASM_ST_SAVE(k)
#define ASM_ST2
#define ASM_ST2_OUT
#define ASM_ST2_SAVE(k)
#define LST0
#define LST1
#define LST2
#define LST3
#define LST4
#define LST5
#define LST6
#define LST7
#define LSTINIT
#define LFLUSH200
#define LFLUSH212
#define LST_IN
#define STDECL
#define ST(k)
#define STFLUSH(t)

constexpr unsigned L_X = 0;
constexpr unsigned L_MEM = 40960;
constexpr unsigned L_Q = 51200;
constexpr unsigned L_KM = 54272;
constexpr unsigned L_QX = 57344;
constexpr unsigned L_KX = 69632;
constexpr unsigned L_W = 81920;
constexpr int MEGA_LDS = 163840;
constexpr int wi(int i, int j) { return (i >> 1) * 5 + ((i & 1) ? 2 + j : j - 1); }

#define RPUB2
#define ASM_S_LOOP \
    "s_mov_b32 s40, 0\n\t" \
    "s_mov_b32 s41, 0x20000\n\t" \
    LSTINIT \
    "s_cmp_eq_u32 %[wave], 0\n\t" \
    "s_cbranch_scc1 .Ls0_%=\n\t" \
    "s_cmp_eq_u32 %[wave], 1\n\t" \
    "s_cbranch_scc1 .Ls1_%=\n\t" \
    "s_cmp_eq_u32 %[wave], 2\n\t" \
    "s_cbranch_scc1 .Ls2_%=\n\t" \
    "s_cmp_eq_u32 %[wave], 3\n\t" \
    "s_cbranch_scc1 .Ls3_%=\n\t" \
    ".Ls4_%=:\n\t" \
    "s_mov_b32 s42, %[xs]\n\t" \
    "ds_read_b128 v[224:227], v253 offset:0\n\t" \
    "ds_read_b128 v[244:247], v254 offset:0\n\t" \
    "ds_read_b128 v[228:231], v253 offset:20480\n\t" \
    "ds_read_b128 v[232:235], v253 offset:1024\n\t" \
    "ds_read_b128 v[236:239], v253 offset:21504\n\t" \
    "ds_read_b128 v[248:251], v254 offset:1024\n\t" \
    ".Ls41_%=:\n\t" \
    "s_add_u32 s44, s42, 0xc000\n\t" \
    "s_add_u32 s45, %[m0], 0x3000\n\t" \
    "s_mov_b32 m0, s45\n\t" \
    "s_nop 0\n\t" \
    "buffer_load_dwordx4 v253, %[rs], s44 offen offset:0 sc1 lds\n\t" \
    "buffer_load_dwordx4 v253, %[rs], s44 offen offset:1024 sc1 lds\n\t" \
    "buffer_load_dwordx4 v253, %[rs], s44 offen offset:2048 sc1 lds\n\t" \
    "buffer_load_dwordx4 v253, %[rs], s44 offen offset:3072 sc1 lds\n\t" \
    "s_add_u32 s44, s42, %[x1]\n\t" \
    "s_add_u32 s44, s44, 0x800\n\t" \
    "s_add_u32 s45, %[m1], 0x800\n\t" \
    "s_mov_b32 m0, s45\n\t" \
    "s_nop 0\n\t" \
    "buffer_load_dwordx4 v253, %[rs], s44 offen offset:0 sc1 lds\n\t" \
    "s_cmp_lg_u32 %[wave], 4\n\t" \
    "s_cbranch_scc1 .Ls42_%=\n\t" \
    "s_sleep 5\n\t" \
    ".Ls42_%=:\n\t" \
    "s_waitcnt lgkmcnt(5)\n\t" \
    "v_mfma_f32_16x16x32_bf16 v[204:207], v[0:3], v[224:227], 0\n\t" \
    "ds_read_b128 v[240:243], v253 offset:2048\n\t" \
    "v_mfma_f32_16x16x32_bf16 v[208:211], v[8:11], v[224:227], 0\n\t" \
    "s_waitcnt lgkmcnt(5)\n\t" \
    "v_mfma_f32_16x16x32_bf16 v[200:203], v[244:247], v[224:227], 0\n\t" \
    "s_waitcnt lgkmcnt(4)\n\t" \
    "v_mfma_f32_16x16x32_bf16 v[216:219], v[0:3], v[228:231], 0\n\t" \
    "ds_read_b128 v[224:227], v253 offset:22528\n\t" \
    "v_mfma_f32_16x16x32_bf16 v[220:223], v[8:11], v[228:231], 0\n\t" \
    "v_mfma_f32_16x16x32_bf16 v[212:215], v[244:247], v[228:231], 0\n\t" \
    "ds_read_b128 v[244:247], v254 offset:2048\n\t" \
    "s_waitcnt lgkmcnt(5)\n\t" \
    "v_mfma_f32_16x16x32_bf16 v[204:207], v[4:7], v[232:235], v[204:207]\n\t" \
    "ds_read_b128 v[228:231], v253 offset:3072\n\t" \
    "v_mfma_f32_16x16x32_bf16 v[208:211], v[12:15], v[232:235], v[208:211]\n\t" \
    "s_waitcnt lgkmcnt(4)\n\t" \
    "v_mfma_f32_16x16x32_bf16 v[200:203], v[248:251], v[232:235], v[200:203]\n\t" \
    "v_mfma_f32_16x16x32_bf16 v[216:219], v[4:7], v[236:239], v[216:219]\n\t" \
    "ds_read_b128 v[232:235], v253 offset:23552\n\t" \
    "v_mfma_f32_16x16x32_bf16 v[220:223], v[12:15], v[236:239], v[220:223]\n\t" \
    "v_mfma_f32_16x16x32_bf16 v[212:215], v[248:251], v[236:239], v[212:215]\n\t" \
    "ds_read_b128 v[248:251], v254 offset:3072\n\t" \
    "s_waitcnt lgkmcnt(5)\n\t" \
    "v_mfma_f32_16x16x32_bf16 v[204:207], v[24:27], v[240:243], v[204:207]\n\t" \
    "ds_read_b128 v[236:239], v253 offset:4096\n\t" \
    "v_mfma_f32_16x16x32_bf16 v[208:211], v[32:35], v[240:243], v[208:211]\n\t" \
    "v_mfma_f32_16x16x32_bf16 v[200:203], v[16:19], v[240:243], v[200:203]\n\t" \
    "s_waitcnt lgkmcnt(5)\n\t" \
    "v_mfma_f32_16x16x32_bf16 v[216:219], v[24:27], v[224:227], v[216:219]\n\t" \
    "ds_read_b128 v[240:243], v253 offset:24576\n\t" \
    "v_mfma_f32_16x16x32_bf16 v[220:223], v[32:35], v[224:227], v[220:223]\n\t" \
    "v_mfma_f32_16x16x32_bf16 v[212:215], v[16:19], v[224:227], v[212:215]\n\t" \
    "s_waitcnt lgkmcnt(4)\n\t" \
    "v_mfma_f32_16x16x32_bf16 v[204:207], v[28:31], v[228:231], v[204:207]\n\t" \
    "ds_read_b128 v[224:227], v253 offset:5120\n\t" \
    "v_mfma_f32_16x16x32_bf16 v[208:211], v[36:39], v[228:231], v[208:211]\n\t" \
    "v_mfma_f32_16x16x32_bf16 v[200:203], v[20:23], v[228:231], v[200:203]\n\t" \
    "s_waitcnt lgkmcnt(4)\n\t" \
    "v_mfma_f32_16x16x32_bf16 v[216:219], v[28:31], v[232:235], v[216:219]\n\t" \
    "ds_read_b128 v[228:231], v253 offset:25600\n\t" \
    "v_mfma_f32_16x16x32_bf16 v[220:223], v[36:39], v[232:235], v[220:223]\n\t" \
    "v_mfma_f32_16x16x32_bf16 v[212:215], v[20:23], v[232:235], v[212:215]\n\t" \
    "s_waitcnt lgkmcnt(3)\n\t" \
    "v_mfma_f32_16x16x32_bf16 v[204:207], v[40:43], v[236:239], v[204:207]\n\t" \
    "ds_read_b128 v[232:235], v253 offset:6144\n\t" \
    "v_mfma_f32_16x16x32_bf16 v[208:211], v[48:51], v[236:239], v[208:211]\n\t" \
    "v_mfma_f32_16x16x32_bf16 v[200:203], v[244:247], v[236:239], v[200:203]\n\t" \
    "s_cmp_eq_u32 %[fl], 0\n\t" \
    "s_cbranch_scc1 .Ls43_%=\n\t" \
    "v_and_b32 v252, 0xf0, v253\n\t" \
    "v_lshlrev_b32 v252, 3, v252\n\t" \
    "buffer_load_dword v252, v252, %[rs], %[fl] offen sc1\n\t" \
    ".Ls43_%=:\n\t" \
    "s_waitcnt lgkmcnt(3)\n\t" \
    "v_mfma_f32_16x16x32_bf16 v[216:219], v[40:43], v[240:243], v[216:219]\n\t" \
    "ds_read_b128 v[236:239], v253 offset:26624\n\t" \
    "v_mfma_f32_16x16x32_bf16 v[220:223], v[48:51], v[240:243], v[220:223]\n\t" \
    "v_mfma_f32_16x16x32_bf16 v[212:215], v[244:247], v[240:243], v[212:215]\n\t" \
    "ds_read_b128 v[244:247], v254 offset:4096\n\t" \
    "s_waitcnt lgkmcnt(4)\n\t" \
    "v_mfma_f32_16x16x32_bf16 v[204:207], v[44:47], v[224:227], v[204:207]\n\t" \
    "ds_read_b128 v[240:243], v253 offset:7168\n\t" \
    "v_mfma_f32_16x16x32_bf16 v[208:211], v[52:55], v[224:227], v[208:211]\n\t" \
    "v_mfma_f32_16x16x32_bf16 v[200:203], v[248:251], v[224:227], v[200:203]\n\t" \
    "s_waitcnt lgkmcnt(4)\n\t" \
    "v_mfma_f32_16x16x32_bf16 v[216:219], v[44:47], v[228:231], v[216:219]\n\t" \
    "ds_read_b128 v[224:227], v253 offset:27648\n\t" \
    "v_mfma_f32_16x16x32_bf16 v[220:223], v[52:55], v[228:231], v[220:223]\n\t" \
    "v_mfma_f32_16x16x32_bf16 v[212:215], v[248:251], v[228:231], v[212:215]\n\t" \
    "ds_read_b128 v[248:251], v254 offset:5120\n\t" \
    "s_waitcnt lgkmcnt(5)\n\t" \
    "v_mfma_f32_16x16x32_bf16 v[204:207], v[64:67], v[232:235], v[204:207]\n\t" \
    "ds_read_b128 v[228:231], v253 offset:8192\n\t" \
    "v_mfma_f32_16x16x32_bf16 v[208:211], v[72:75], v[232:235], v[208:211]\n\t" \
    "v_mfma_f32_16x16x32_bf16 v[200:203], v[56:59], v[232:235], v[200:203]\n\t" \
    "s_waitcnt lgkmcnt(5)\n\t" \
    "v_mfma_f32_16x16x32_bf16 v[216:219], v[64:67], v[236:239], v[216:219]\n\t" \
    "ds_read_b128 v[232:235], v253 offset:28672\n\t" \
    "s_add_u32 s44, s40, 1\n\t" \
    "v_mfma_f32_16x16x32_bf16 v[220:223], v[72:75], v[236:239], v[220:223]\n\t" \
    "s_and_b32 s45, s44, 15\n\t" \
    "v_mfma_f32_16x16x32_bf16 v[212:215], v[56:59], v[236:239], v[212:215]\n\t" \
    "s_cmp_eq_u32 s45, 0\n\t" \
    "s_waitcnt lgkmcnt(4)\n\t" \
    "v_mfma_f32_16x16x32_bf16 v[204:207], v[68:71], v[240:243], v[204:207]\n\t" \
    "ds_read_b128 v[236:239], v253 offset:9216\n\t" \
    "s_cselect_b32 s45, %[cj], %[cs]\n\t" \
    "v_mfma_f32_16x16x32_bf16 v[208:211], v[76:79], v[240:243], v[208:211]\n\t" \
    "s_cmp_eq_u32 s44, 64\n\t" \
    "v_mfma_f32_16x16x32_bf16 v[200:203], v[60:63], v[240:243], v[200:203]\n\t" \
    "s_cselect_b32 s45, 0, s45\n\t" \
    "s_waitcnt lgkmcnt(4)\n\t" \
    "v_mfma_f32_16x16x32_bf16 v[216:219], v[68:71], v[224:227], v[216:219]\n\t" \
    "ds_read_b128 v[240:243], v253 offset:29696\n\t" \
    "s_add_u32 s42, s42, s45\n\t" \
    "v_mfma_f32_16x16x32_bf16 v[220:223], v[76:79], v[224:227], v[220:223]\n\t" \
    "v_mfma_f32_16x16x32_bf16 v[212:215], v[60:63], v[224:227], v[212:215]\n\t" \
    "s_waitcnt vmcnt(0) lgkmcnt(0)\n\t" \
    "s_barrier \n\t" \
    LST7 \
    "s_cmp_eq_u32 %[fl], 0\n\t" \
    "s_cbranch_scc1 .Ls45_%=\n\t" \
    "s_add_u32 s44, s40, 1\n\t" \
    "s_min_u32 s44, s44, 63\n\t" \
    ".Ls44_%=:\n\t" \
    "v_cmp_ge_u32 vcc, s44, v252\n\t" \
    "s_cbranch_vccz .Ls45_%=\n\t" \
    "s_cmp_eq_u32 s41, 0\n\t" \
    "s_cbranch_scc1 .Ls45_%=\n\t" \
    "s_sub_u32 s41, s41, 1\n\t" \
    "s_sleep 2\n\t" \
    "v_and_b32 v252, 0xf0, v253\n\t" \
    "v_lshlrev_b32 v252, 3, v252\n\t" \
    "buffer_load_dword v252, v252, %[rs], %[fl] offen sc1\n\t" \
    "s_waitcnt vmcnt(0)\n\t" \
    "s_branch .Ls44_%=\n\t" \
    ".Ls45_%=:\n\t" \
    "s_mov_b32 m0, %[m0]\n\t" \
    "s_nop 0\n\t" \
    "buffer_load_dwordx4 v253, %[rs], s42 offen offset:0 sc1 lds\n\t" \
    "buffer_load_dwordx4 v253, %[rs], s42 offen offset:1024 sc1 lds\n\t" \
    "buffer_load_dwordx4 v253, %[rs], s42 offen offset:2048 sc1 lds\n\t" \
    "buffer_load_dwordx4 v253, %[rs], s42 offen offset:3072 sc1 lds\n\t" \
    "s_add_u32 s44, s42, %[x1]\n\t" \
    "s_mov_b32 m0, %[m1]\n\t" \
    "s_nop 0\n\t" \
    "buffer_load_dwordx4 v253, %[rs], s44 offen offset:0 sc1 lds\n\t" \
    "v_mfma_f32_16x16x32_bf16 v[204:207], v[80:83], v[228:231], v[204:207]\n\t" \
    "ds_read_b128 v[224:227], v253 offset:12288\n\t" \
    "v_mfma_f32_16x16x32_bf16 v[208:211], v[88:91], v[228:231], v[208:211]\n\t" \
    "v_mfma_f32_16x16x32_bf16 v[200:203], v[244:247], v[228:231], v[200:203]\n\t" \
    "v_mfma_f32_16x16x32_bf16 v[216:219], v[80:83], v[232:235], v[216:219]\n\t" \
    "ds_read_b128 v[228:231], v253 offset:32768\n\t" \
    "v_mfma_f32_16x16x32_bf16 v[220:223], v[88:91], v[232:235], v[220:223]\n\t" \
    "v_mfma_f32_16x16x32_bf16 v[212:215], v[244:247], v[232:235], v[212:215]\n\t" \
    "ds_read_b128 v[244:247], v254 offset:6144\n\t" \
    "v_mfma_f32_16x16x32_bf16 v[204:207], v[84:87], v[236:239], v[204:207]\n\t" \
    "ds_read_b128 v[232:235], v253 offset:13312\n\t" \
    "v_mfma_f32_16x16x32_bf16 v[208:211], v[92:95], v[236:239], v[208:211]\n\t" \
    "v_mfma_f32_16x16x32_bf16 v[200:203], v[248:251], v[236:239], v[200:203]\n\t" \
    "v_mfma_f32_16x16x32_bf16 v[216:219], v[84:87], v[240:243], v[216:219]\n\t" \
    "ds_read_b128 v[236:239], v253 offset:33792\n\t" \
    "v_mfma_f32_16x16x32_bf16 v[220:223], v[92:95], v[240:243], v[220:223]\n\t" \
    "v_mfma_f32_16x16x32_bf16 v[212:215], v[248:251], v[240:243], v[212:215]\n\t" \
    "ds_read_b128 v[248:251], v254 offset:7168\n\t" \
    "s_waitcnt lgkmcnt(5)\n\t" \
    "v_mfma_f32_16x16x32_bf16 v[204:207], v[120:123], v[224:227], v[204:207]\n\t" \
    "ds_read_b128 v[240:243], v253 offset:10240\n\t" \
    "v_mfma_f32_16x16x32_bf16 v[208:211], v[128:131], v[224:227], v[208:211]\n\t" \
    "s_waitcnt lgkmcnt(4)\n\t" \
    "v_mfma_f32_16x16x32_bf16 v[200:203], v[244:247], v[224:227], v[200:203]\n\t" \
    "v_mfma_f32_16x16x32_bf16 v[216:219], v[120:123], v[228:231], v[216:219]\n\t" \
    "ds_read_b128 v[224:227], v253 offset:30720\n\t" \
    "v_mfma_f32_16x16x32_bf16 v[220:223], v[128:131], v[228:231], v[220:223]\n\t" \
    "v_mfma_f32_16x16x32_bf16 v[212:215], v[244:247], v[228:231], v[212:215]\n\t" \
    "ds_read_b128 v[244:247], v254 offset:8192\n\t" \
    "s_waitcnt lgkmcnt(5)\n\t" \
    "v_mfma_f32_16x16x32_bf16 v[204:207], v[124:127], v[232:235], v[204:207]\n\t" \
    "ds_read_b128 v[228:231], v253 offset:11264\n\t" \
    "v_mfma_f32_16x16x32_bf16 v[208:211], v[132:135], v[232:235], v[208:211]\n\t" \
    "s_waitcnt lgkmcnt(4)\n\t" \
    "v_mfma_f32_16x16x32_bf16 v[200:203], v[248:251], v[232:235], v[200:203]\n\t" \
    "v_mfma_f32_16x16x32_bf16 v[216:219], v[124:127], v[236:239], v[216:219]\n\t" \
    "ds_read_b128 v[232:235], v253 offset:31744\n\t" \
    "v_mfma_f32_16x16x32_bf16 v[220:223], v[132:135], v[236:239], v[220:223]\n\t" \
    "v_mfma_f32_16x16x32_bf16 v[212:215], v[248:251], v[236:239], v[212:215]\n\t" \
    "ds_read_b128 v[248:251], v254 offset:9216\n\t" \
    "s_waitcnt lgkmcnt(5)\n\t" \
    "v_mfma_f32_16x16x32_bf16 v[204:207], v[104:107], v[240:243], v[204:207]\n\t" \
    "ds_read_b128 v[236:239], v253 offset:16384\n\t" \
    "v_mfma_f32_16x16x32_bf16 v[208:211], v[112:115], v[240:243], v[208:211]\n\t" \
    "v_mfma_f32_16x16x32_bf16 v[200:203], v[96:99], v[240:243], v[200:203]\n\t" \
    "s_waitcnt lgkmcnt(5)\n\t" \
    "v_mfma_f32_16x16x32_bf16 v[216:219], v[104:107], v[224:227], v[216:219]\n\t" \
    "ds_read_b128 v[240:243], v253 offset:36864\n\t" \
    "v_mfma_f32_16x16x32_bf16 v[220:223], v[112:115], v[224:227], v[220:223]\n\t" \
    "v_mfma_f32_16x16x32_bf16 v[212:215], v[96:99], v[224:227], v[212:215]\n\t" \
    "s_waitcnt lgkmcnt(4)\n\t" \
    "v_mfma_f32_16x16x32_bf16 v[204:207], v[108:111], v[228:231], v[204:207]\n\t" \
    "ds_read_b128 v[224:227], v253 offset:17408\n\t" \
    "v_mfma_f32_16x16x32_bf16 v[208:211], v[116:119], v[228:231], v[208:211]\n\t" \
    "v_mfma_f32_16x16x32_bf16 v[200:203], v[100:103], v[228:231], v[200:203]\n\t" \
    "s_waitcnt lgkmcnt(4)\n\t" \
    "v_mfma_f32_16x16x32_bf16 v[216:219], v[108:111], v[232:235], v[216:219]\n\t" \
    "ds_read_b128 v[228:231], v253 offset:37888\n\t" \
    "v_mfma_f32_16x16x32_bf16 v[220:223], v[116:119], v[232:235], v[220:223]\n\t" \
    "v_mfma_f32_16x16x32_bf16 v[212:215], v[100:103], v[232:235], v[212:215]\n\t" \
    "s_waitcnt lgkmcnt(3)\n\t" \
    "v_mfma_f32_16x16x32_bf16 v[204:207], v[160:163], v[236:239], v[204:207]\n\t" \
    "ds_read_b128 v[232:235], v253 offset:14336\n\t" \
    "v_mfma_f32_16x16x32_bf16 v[208:211], v[168:171], v[236:239], v[208:211]\n\t" \
    "v_mfma_f32_16x16x32_bf16 v[200:203], v[244:247], v[236:239], v[200:203]\n\t" \
    "s_waitcnt lgkmcnt(3)\n\t" \
    "v_mfma_f32_16x16x32_bf16 v[216:219], v[160:163], v[240:243], v[216:219]\n\t" \
    "ds_read_b128 v[236:239], v253 offset:15360\n\t" \
    "v_mfma_f32_16x16x32_bf16 v[220:223], v[168:171], v[240:243], v[220:223]\n\t" \
    "v_mfma_f32_16x16x32_bf16 v[212:215], v[244:247], v[240:243], v[212:215]\n\t" \
    "s_waitcnt lgkmcnt(3)\n\t" \
    "v_mfma_f32_16x16x32_bf16 v[204:207], v[164:167], v[224:227], v[204:207]\n\t" \
    "ds_read_b128 v[240:243], v253 offset:18432\n\t" \
    "v_mfma_f32_16x16x32_bf16 v[208:211], v[172:175], v[224:227], v[208:211]\n\t" \
    "v_mfma_f32_16x16x32_bf16 v[200:203], v[248:251], v[224:227], v[200:203]\n\t" \
    "s_waitcnt lgkmcnt(3)\n\t" \
    "v_mfma_f32_16x16x32_bf16 v[216:219], v[164:167], v[228:231], v[216:219]\n\t" \
    "ds_read_b128 v[224:227], v253 offset:19456\n\t" \
    "v_mfma_f32_16x16x32_bf16 v[220:223], v[172:175], v[228:231], v[220:223]\n\t" \
    "v_mfma_f32_16x16x32_bf16 v[212:215], v[248:251], v[228:231], v[212:215]\n\t" \
    "s_waitcnt lgkmcnt(3)\n\t" \
    "v_mfma_f32_16x16x32_bf16 v[204:207], v[144:147], v[232:235], v[204:207]\n\t" \
    "ds_read_b128 v[228:231], v253 offset:34816\n\t" \
    "v_mov_b64 v[244:245], 0\n\t" \
    "v_mfma_f32_16x16x32_bf16 v[208:211], v[152:155], v[232:235], v[208:211]\n\t" \
    "v_mov_b64 v[246:247], 0\n\t" \
    "v_mfma_f32_16x16x32_bf16 v[200:203], v[136:139], v[232:235], v[200:203]\n\t" \
    "v_mov_b64 v[248:249], 0\n\t" \
    "s_waitcnt lgkmcnt(3)\n\t" \
    "v_mfma_f32_16x16x32_bf16 v[204:207], v[148:151], v[236:239], v[204:207]\n\t" \
    "ds_read_b128 v[232:235], v253 offset:35840\n\t" \
    "v_mov_b64 v[250:251], 0\n\t" \
    "v_mfma_f32_16x16x32_bf16 v[208:211], v[156:159], v[236:239], v[208:211]\n\t" \
    "v_add_u32 v252, %[qs], v253\n\t" \
    "v_mfma_f32_16x16x32_bf16 v[200:203], v[140:143], v[236:239], v[200:203]\n\t" \
    "s_waitcnt lgkmcnt(3)\n\t" \
    "v_mfma_f32_16x16x32_bf16 v[204:207], v[184:187], v[240:243], v[204:207]\n\t" \
    "ds_read_b128 v[236:239], v253 offset:38912\n\t" \
    "v_mfma_f32_16x16x32_bf16 v[208:211], v[192:195], v[240:243], v[208:211]\n\t" \
    "v_mfma_f32_16x16x32_bf16 v[200:203], v[176:179], v[240:243], v[200:203]\n\t" \
    "s_waitcnt lgkmcnt(3)\n\t" \
    "v_mfma_f32_16x16x32_bf16 v[204:207], v[188:191], v[224:227], v[204:207]\n\t" \
    "ds_read_b128 v[240:243], v253 offset:39936\n\t" \
    "v_mfma_f32_16x16x32_bf16 v[208:211], v[196:199], v[224:227], v[208:211]\n\t" \
    "v_mfma_f32_16x16x32_bf16 v[200:203], v[180:183], v[224:227], v[200:203]\n\t" \
    "s_waitcnt lgkmcnt(3)\n\t" \
    "v_mfma_f32_16x16x32_bf16 v[216:219], v[144:147], v[228:231], v[216:219]\n\t" \
    "s_waitcnt lgkmcnt(2)\n\t" \
    "v_mfma_f32_16x16x32_bf16 v[216:219], v[148:151], v[232:235], v[216:219]\n\t" \
    "v_cvt_pk_bf16_f32 v204, v204, v205\n\t" \
    "v_cvt_pk_bf16_f32 v205, v206, v207\n\t" \
    "s_waitcnt lgkmcnt(1)\n\t" \
    "v_mfma_f32_16x16x32_bf16 v[216:219], v[184:187], v[236:239], v[216:219]\n\t" \
    "ds_write_b64 v252, v[204:205] offset:1024\n\t" \
    "v_cvt_pk_bf16_f32 v208, v208, v209\n\t" \
    "s_waitcnt lgkmcnt(1)\n\t" \
    "v_mfma_f32_16x16x32_bf16 v[216:219], v[188:191], v[240:243], v[216:219]\n\t" \
    "v_cvt_pk_bf16_f32 v209, v210, v211\n\t" \
    "ds_write_b64 v252, v[208:209] offset:2048\n\t" \
    "v_mfma_f32_16x16x32_bf16 v[220:223], v[152:155], v[228:231], v[220:223]\n\t" \
    "v_cvt_pk_bf16_f32 v200, v200, v201\n\t" \
    "v_cvt_pk_bf16_f32 v201, v202, v203\n\t" \
    "v_mfma_f32_16x16x32_bf16 v[220:223], v[156:159], v[232:235], v[220:223]\n\t" \
    "ds_write_b64 v252, v[200:201] offset:0\n\t" \
    "ds_read_b128 v[200:203], v254 offset:0\n\t" \
    "v_mfma_f32_16x16x32_bf16 v[220:223], v[192:195], v[236:239], v[220:223]\n\t" \
    "ds_read_b128 v[204:207], v254 offset:1024\n\t" \
    "v_mov_b64 v[208:209], 0\n\t" \
    "v_mfma_f32_16x16x32_bf16 v[220:223], v[196:199], v[240:243], v[220:223]\n\t" \
    "v_mov_b64 v[210:211], 0\n\t" \
    "v_cvt_pk_bf16_f32 v216, v216, v217\n\t" \
    "v_mfma_f32_16x16x32_bf16 v[212:215], v[136:139], v[228:231], v[212:215]\n\t" \
    "v_cvt_pk_bf16_f32 v217, v218, v219\n\t" \
    "ds_write_b64 v252, v[216:217] offset:7168\n\t" \
    "v_mfma_f32_16x16x32_bf16 v[212:215], v[140:143], v[232:235], v[212:215]\n\t" \
    "v_mfma_f32_16x16x32_bf16 v[212:215], v[176:179], v[236:239], v[212:215]\n\t" \
    "v_cvt_pk_bf16_f32 v220, v220, v221\n\t" \
    "v_cvt_pk_bf16_f32 v221, v222, v223\n\t" \
    "v_mfma_f32_16x16x32_bf16 v[212:215], v[180:183], v[240:243], v[212:215]\n\t" \
    "ds_write_b64 v252, v[220:221] offset:8192\n\t" \
    "ds_read_b128 v[216:219], v254 offset:2048\n\t" \
    "ds_read_b128 v[220:223], v254 offset:3072\n\t" \
    "s_nop 4\n\t" \
    "v_cvt_pk_bf16_f32 v212, v212, v213\n\t" \
    "v_cvt_pk_bf16_f32 v213, v214, v215\n\t" \
    "ds_write_b64 v252, v[212:213] offset:6144\n\t" \
    LST6 \
    "s_waitcnt vmcnt(0) lgkmcnt(0)\n\t" \
    "s_barrier \n\t" \
    LST3 \
    "ds_read_b128 v[224:227], v253 offset:40960\n\t" \
    "ds_read_b128 v[228:231], v253 offset:41984\n\t" \
    "ds_read_b128 v[232:235], v253 offset:43008\n\t" \
    "ds_read_b128 v[236:239], v253 offset:44032\n\t" \
    "ds_read_b128 v[240:243], v253 offset:45056\n\t" \
    "v_bfe_i32 v212, v253, 5, 1\n\t" \
    "v_and_b32 v212, 0xaaaa, v212\n\t" \
    "v_or_b32 v212, 0x4444, v212\n\t" \
    "s_nop 1\n\t" \
    "s_waitcnt lgkmcnt(4)\n\t" \
    "v_smfmac_f32_16x16x64_bf16 v[208:211], v[224:227], v[200:207], v212\n\t" \
    "ds_read_b128 v[200:203], v254 offset:4096\n\t" \
    "ds_read_b128 v[204:207], v254 offset:5120\n\t" \
    "v_smfmac_f32_16x16x64_bf16 v[244:247], v[224:227], v[0:7], v212\n\t" \
    "v_smfmac_f32_16x16x64_bf16 v[248:251], v[224:227], v[8:15], v212\n\t" \
    "ds_read_b128 v[224:227], v253 offset:46080\n\t" \
    "s_waitcnt lgkmcnt(6)\n\t" \
    "v_smfmac_f32_16x16x64_bf16 v[208:211], v[228:231], v[16:23], v212\n\t" \
    "v_smfmac_f32_16x16x64_bf16 v[244:247], v[228:231], v[24:31], v212\n\t" \
    "v_smfmac_f32_16x16x64_bf16 v[248:251], v[228:231], v[32:39], v212\n\t" \
    "ds_read_b128 v[228:231], v253 offset:47104\n\t" \
    "s_waitcnt lgkmcnt(6)\n\t" \
    "v_smfmac_f32_16x16x64_bf16 v[208:211], v[232:235], v[216:223], v212\n\t" \
    "ds_read_b128 v[216:219], v254 offset:6144\n\t" \
    "ds_read_b128 v[220:223], v254 offset:7168\n\t" \
    "v_smfmac_f32_16x16x64_bf16 v[244:247], v[232:235], v[40:47], v212\n\t" \
    "v_smfmac_f32_16x16x64_bf16 v[248:251], v[232:235], v[48:55], v212\n\t" \
    "ds_read_b128 v[232:235], v253 offset:48128\n\t" \
    "s_waitcnt lgkmcnt(8)\n\t" \
    "v_smfmac_f32_16x16x64_bf16 v[208:211], v[236:239], v[56:63], v212\n\t" \
    "v_smfmac_f32_16x16x64_bf16 v[244:247], v[236:239], v[64:71], v212\n\t" \
    "v_smfmac_f32_16x16x64_bf16 v[248:251], v[236:239], v[72:79], v212\n\t" \
    "ds_read_b128 v[236:239], v253 offset:49152\n\t" \
    "s_waitcnt lgkmcnt(6)\n\t" \
    "v_smfmac_f32_16x16x64_bf16 v[208:211], v[240:243], v[200:207], v212\n\t" \
    "ds_read_b128 v[200:203], v254 offset:8192\n\t" \
    "ds_read_b128 v[204:207], v254 offset:9216\n\t" \
    "v_smfmac_f32_16x16x64_bf16 v[244:247], v[240:243], v[80:87], v212\n\t" \
    "v_smfmac_f32_16x16x64_bf16 v[248:251], v[240:243], v[88:95], v212\n\t" \
    "ds_read_b128 v[240:243], v253 offset:50176\n\t" \
    "s_waitcnt lgkmcnt(8)\n\t" \
    "v_smfmac_f32_16x16x64_bf16 v[244:247], v[224:227], v[104:111], v212\n\t" \
    "s_waitcnt lgkmcnt(7)\n\t" \
    "v_smfmac_f32_16x16x64_bf16 v[244:247], v[228:231], v[120:127], v212\n\t" \
    "s_waitcnt lgkmcnt(4)\n\t" \
    "v_smfmac_f32_16x16x64_bf16 v[244:247], v[232:235], v[144:151], v212\n\t" \
    "s_waitcnt lgkmcnt(3)\n\t" \
    "v_smfmac_f32_16x16x64_bf16 v[244:247], v[236:239], v[160:167], v212\n\t" \
    "s_waitcnt lgkmcnt(0)\n\t" \
    "v_smfmac_f32_16x16x64_bf16 v[244:247], v[240:243], v[184:191], v212\n\t" \
    "v_smfmac_f32_16x16x64_bf16 v[248:251], v[224:227], v[112:119], v212\n\t" \
    "v_smfmac_f32_16x16x64_bf16 v[248:251], v[228:231], v[128:135], v212\n\t" \
    "v_smfmac_f32_16x16x64_bf16 v[248:251], v[232:235], v[152:159], v212\n\t" \
    "v_add_f32 v244, v244, v246\n\t" \
    "v_add_f32 v245, v245, v247\n\t" \
    "v_smfmac_f32_16x16x64_bf16 v[248:251], v[236:239], v[168:175], v212\n\t" \
    "v_cvt_pk_bf16_f32 v244, v244, v245\n\t" \
    "ds_write_b16 v255, v244 offset:1024\n\t" \
    "v_smfmac_f32_16x16x64_bf16 v[248:251], v[240:243], v[192:199], v212\n\t" \
    "ds_write_b16_d16_hi v255, v244 offset:1040\n\t" \
    "v_smfmac_f32_16x16x64_bf16 v[208:211], v[224:227], v[96:103], v212\n\t" \
    "v_smfmac_f32_16x16x64_bf16 v[208:211], v[228:231], v[216:223], v212\n\t" \
    "v_smfmac_f32_16x16x64_bf16 v[208:211], v[232:235], v[136:143], v212\n\t" \
    "v_add_f32 v248, v248, v250\n\t" \
    "v_add_f32 v249, v249, v251\n\t" \
    "v_smfmac_f32_16x16x64_bf16 v[208:211], v[236:239], v[200:207], v212\n\t" \
    "v_cvt_pk_bf16_f32 v248, v248, v249\n\t" \
    "ds_write_b16 v255, v248 offset:2048\n\t" \
    "v_smfmac_f32_16x16x64_bf16 v[208:211], v[240:243], v[176:183], v212\n\t" \
    "ds_write_b16_d16_hi v255, v248 offset:2064\n\t" \
    "ds_read_b128 v[224:227], v253 offset:0\n\t" \
    "ds_read_b128 v[244:247], v254 offset:0\n\t" \
    "ds_read_b128 v[228:231], v253 offset:20480\n\t" \
    "ds_read_b128 v[232:235], v253 offset:1024\n\t" \
    "ds_read_b128 v[236:239], v253 offset:21504\n\t" \
    "ds_read_b128 v[248:251], v254 offset:1024\n\t" \
    "s_nop 0\n\t" \
    "v_add_f32 v208, v208, v210\n\t" \
    "v_add_f32 v209, v209, v211\n\t" \
    "v_cvt_pk_bf16_f32 v208, v208, v209\n\t" \
    "ds_write_b16 v255, v208 offset:0\n\t" \
    "ds_write_b16_d16_hi v255, v208 offset:16\n\t" \
    "s_waitcnt lgkmcnt(0)\n\t" \
    "s_barrier \n\t" \
    LST4 \
    LFLUSH200 \
    "s_add_u32 s40, s40, 1\n\t" \
    "s_cmp_lg_u32 s40, 64\n\t" \
    "s_cbranch_scc1 .Ls41_%=\n\t" \
    "s_branch .Lend%=\n\t" \
    ".Ls3_%=:\n\t" \
    "ds_read_b128 v[208:211], v254 offset:24576\n\t" \
    "ds_read_b128 v[224:227], v253 offset:0\n\t" \
    "ds_read_b128 v[228:231], v253 offset:20480\n\t" \
    "ds_read_b128 v[212:215], v254 offset:25600\n\t" \
    "ds_read_b128 v[232:235], v253 offset:1024\n\t" \
    "ds_read_b128 v[236:239], v253 offset:21504\n\t" \
    "ds_read_b128 v[216:219], v254 offset:26624\n\t" \
    "ds_read_b128 v[240:243], v253 offset:2048\n\t" \
    "ds_read_b128 v[244:247], v253 offset:22528\n\t" \
    "ds_read_b128 v[220:223], v254 offset:27648\n\t" \
    "ds_read_b128 v[248:251], v253 offset:3072\n\t" \
    ".Ls31_%=:\n\t" \
    "s_waitcnt lgkmcnt(9)\n\t" \
    "v_mfma_f32_16x16x32_bf16 v[200:203], v[208:211], v[224:227], 0\n\t" \
    "ds_read_b128 v[224:227], v253 offset:23552\n\t" \
    "s_waitcnt lgkmcnt(9)\n\t" \
    "v_mfma_f32_16x16x32_bf16 v[204:207], v[208:211], v[228:231], 0\n\t" \
    "ds_read_b128 v[228:231], v253 offset:4096\n\t" \
    "ds_read_b128 v[208:211], v254 offset:28672\n\t" \
    "s_waitcnt lgkmcnt(9)\n\t" \
    "v_mfma_f32_16x16x32_bf16 v[200:203], v[212:215], v[232:235], v[200:203]\n\t" \
    "ds_read_b128 v[232:235], v253 offset:24576\n\t" \
    "s_waitcnt lgkmcnt(9)\n\t" \
    "v_mfma_f32_16x16x32_bf16 v[204:207], v[212:215], v[236:239], v[204:207]\n\t" \
    "ds_read_b128 v[236:239], v253 offset:5120\n\t" \
    "ds_read_b128 v[212:215], v254 offset:29696\n\t" \
    "s_waitcnt lgkmcnt(9)\n\t" \
    "v_mfma_f32_16x16x32_bf16 v[200:203], v[216:219], v[240:243], v[200:203]\n\t" \
    "ds_read_b128 v[240:243], v253 offset:25600\n\t" \
    "s_waitcnt lgkmcnt(9)\n\t" \
    "v_mfma_f32_16x16x32_bf16 v[204:207], v[216:219], v[244:247], v[204:207]\n\t" \
    "ds_read_b128 v[244:247], v253 offset:6144\n\t" \
    "ds_read_b128 v[216:219], v254 offset:30720\n\t" \
    "s_waitcnt lgkmcnt(9)\n\t" \
    "v_mfma_f32_16x16x32_bf16 v[200:203], v[220:223], v[248:251], v[200:203]\n\t" \
    "ds_read_b128 v[248:251], v253 offset:26624\n\t" \
    "s_waitcnt lgkmcnt(9)\n\t" \
    "v_mfma_f32_16x16x32_bf16 v[204:207], v[220:223], v[224:227], v[204:207]\n\t" \
    "ds_read_b128 v[224:227], v253 offset:7168\n\t" \
    "ds_read_b128 v[220:223], v254 offset:31744\n\t" \
    "s_waitcnt lgkmcnt(9)\n\t" \
    "v_mfma_f32_16x16x32_bf16 v[200:203], v[208:211], v[228:231], v[200:203]\n\t" \
    "ds_read_b128 v[228:231], v253 offset:27648\n\t" \
    "s_waitcnt lgkmcnt(9)\n\t" \
    "v_mfma_f32_16x16x32_bf16 v[204:207], v[208:211], v[232:235], v[204:207]\n\t" \
    "ds_read_b128 v[232:235], v253 offset:8192\n\t" \
    "ds_read_b128 v[208:211], v254 offset:32768\n\t" \
    "s_waitcnt lgkmcnt(9)\n\t" \
    "v_mfma_f32_16x16x32_bf16 v[200:203], v[212:215], v[236:239], v[200:203]\n\t" \
    "ds_read_b128 v[236:239], v253 offset:28672\n\t" \
    "s_waitcnt lgkmcnt(9)\n\t" \
    "v_mfma_f32_16x16x32_bf16 v[204:207], v[212:215], v[240:243], v[204:207]\n\t" \
    "ds_read_b128 v[240:243], v253 offset:9216\n\t" \
    "ds_read_b128 v[212:215], v254 offset:33792\n\t" \
    "s_waitcnt lgkmcnt(9)\n\t" \
    "v_mfma_f32_16x16x32_bf16 v[200:203], v[216:219], v[244:247], v[200:203]\n\t" \
    "ds_read_b128 v[244:247], v253 offset:29696\n\t" \
    "s_waitcnt lgkmcnt(9)\n\t" \
    "v_mfma_f32_16x16x32_bf16 v[204:207], v[216:219], v[248:251], v[204:207]\n\t" \
    "s_waitcnt lgkmcnt(7)\n\t" \
    "v_mfma_f32_16x16x32_bf16 v[200:203], v[220:223], v[224:227], v[200:203]\n\t" \
    "s_waitcnt lgkmcnt(6)\n\t" \
    "v_mfma_f32_16x16x32_bf16 v[204:207], v[220:223], v[228:231], v[204:207]\n\t" \
    "s_waitcnt lgkmcnt(4)\n\t" \
    "v_mfma_f32_16x16x32_bf16 v[200:203], v[208:211], v[232:235], v[200:203]\n\t" \
    "s_waitcnt lgkmcnt(3)\n\t" \
    "v_mfma_f32_16x16x32_bf16 v[204:207], v[208:211], v[236:239], v[204:207]\n\t" \
    "s_waitcnt lgkmcnt(1)\n\t" \
    "v_mfma_f32_16x16x32_bf16 v[200:203], v[212:215], v[240:243], v[200:203]\n\t" \
    "s_waitcnt lgkmcnt(0)\n\t" \
    "v_mfma_f32_16x16x32_bf16 v[204:207], v[212:215], v[244:247], v[204:207]\n\t" \
    "ds_read_b128 v[224:227], v253 offset:0\n\t" \
    "ds_read_b128 v[244:247], v254 offset:57344\n\t" \
    "ds_read_b128 v[228:231], v253 offset:20480\n\t" \
    "ds_read_b128 v[232:235], v253 offset:1024\n\t" \
    "ds_read_b128 v[236:239], v253 offset:21504\n\t" \
    "ds_read_b128 v[248:251], v254 offset:58368\n\t" \
    "s_nop 1\n\t" \
    "ds_write_b128 v253, v[200:203] offset:40960\n\t" \
    "ds_write_b128 v253, v[204:207] offset:41984\n\t" \
    "s_nop 1\n\t" \
    "s_waitcnt lgkmcnt(7)\n\t" \
    "v_mfma_f32_16x16x32_bf16 v[204:207], v[40:43], v[224:227], 0\n\t" \
    "ds_read_b128 v[240:243], v253 offset:2048\n\t" \
    "v_mfma_f32_16x16x32_bf16 v[208:211], v[48:51], v[224:227], 0\n\t" \
    "s_waitcnt lgkmcnt(7)\n\t" \
    "v_mfma_f32_16x16x32_bf16 v[200:203], v[244:247], v[224:227], 0\n\t" \
    "s_waitcnt lgkmcnt(6)\n\t" \
    "v_mfma_f32_16x16x32_bf16 v[216:219], v[40:43], v[228:231], 0\n\t" \
    "ds_read_b128 v[224:227], v253 offset:22528\n\t" \
    "v_mfma_f32_16x16x32_bf16 v[220:223], v[48:51], v[228:231], 0\n\t" \
    "v_mfma_f32_16x16x32_bf16 v[212:215], v[244:247], v[228:231], 0\n\t" \
    "ds_read_b128 v[244:247], v254 offset:59392\n\t" \
    "s_waitcnt lgkmcnt(7)\n\t" \
    "v_mfma_f32_16x16x32_bf16 v[204:207], v[44:47], v[232:235], v[204:207]\n\t" \
    "ds_read_b128 v[228:231], v253 offset:3072\n\t" \
    "v_mfma_f32_16x16x32_bf16 v[208:211], v[52:55], v[232:235], v[208:211]\n\t" \
    "s_waitcnt lgkmcnt(6)\n\t" \
    "v_mfma_f32_16x16x32_bf16 v[200:203], v[248:251], v[232:235], v[200:203]\n\t" \
    "v_mfma_f32_16x16x32_bf16 v[216:219], v[44:47], v[236:239], v[216:219]\n\t" \
    "ds_read_b128 v[232:235], v253 offset:23552\n\t" \
    "v_mfma_f32_16x16x32_bf16 v[220:223], v[52:55], v[236:239], v[220:223]\n\t" \
    "v_mfma_f32_16x16x32_bf16 v[212:215], v[248:251], v[236:239], v[212:215]\n\t" \
    "ds_read_b128 v[248:251], v254 offset:60416\n\t" \
    "s_waitcnt lgkmcnt(5)\n\t" \
    "v_mfma_f32_16x16x32_bf16 v[204:207], v[64:67], v[240:243], v[204:207]\n\t" \
    "ds_read_b128 v[236:239], v253 offset:4096\n\t" \
    "v_mfma_f32_16x16x32_bf16 v[208:211], v[72:75], v[240:243], v[208:211]\n\t" \
    "v_mfma_f32_16x16x32_bf16 v[200:203], v[56:59], v[240:243], v[200:203]\n\t" \
    "s_waitcnt lgkmcnt(5)\n\t" \
    "v_mfma_f32_16x16x32_bf16 v[216:219], v[64:67], v[224:227], v[216:219]\n\t" \
    "ds_read_b128 v[240:243], v253 offset:24576\n\t" \
    "v_mfma_f32_16x16x32_bf16 v[220:223], v[72:75], v[224:227], v[220:223]\n\t" \
    "v_mfma_f32_16x16x32_bf16 v[212:215], v[56:59], v[224:227], v[212:215]\n\t" \
    "s_waitcnt lgkmcnt(4)\n\t" \
    "v_mfma_f32_16x16x32_bf16 v[204:207], v[68:71], v[228:231], v[204:207]\n\t" \
    "ds_read_b128 v[224:227], v253 offset:5120\n\t" \
    "v_mfma_f32_16x16x32_bf16 v[208:211], v[76:79], v[228:231], v[208:211]\n\t" \
    "v_mfma_f32_16x16x32_bf16 v[200:203], v[60:63], v[228:231], v[200:203]\n\t" \
    "s_waitcnt lgkmcnt(4)\n\t" \
    "v_mfma_f32_16x16x32_bf16 v[216:219], v[68:71], v[232:235], v[216:219]\n\t" \
    "ds_read_b128 v[228:231], v253 offset:25600\n\t" \
    "v_mfma_f32_16x16x32_bf16 v[220:223], v[76:79], v[232:235], v[220:223]\n\t" \
    "v_mfma_f32_16x16x32_bf16 v[212:215], v[60:63], v[232:235], v[212:215]\n\t" \
    "s_waitcnt lgkmcnt(3)\n\t" \
    "v_mfma_f32_16x16x32_bf16 v[204:207], v[80:83], v[236:239], v[204:207]\n\t" \
    "ds_read_b128 v[232:235], v253 offset:6144\n\t" \
    "v_mfma_f32_16x16x32_bf16 v[208:211], v[88:91], v[236:239], v[208:211]\n\t" \
    "v_mfma_f32_16x16x32_bf16 v[200:203], v[244:247], v[236:239], v[200:203]\n\t" \
    "s_waitcnt lgkmcnt(3)\n\t" \
    "v_mfma_f32_16x16x32_bf16 v[216:219], v[80:83], v[240:243], v[216:219]\n\t" \
    "ds_read_b128 v[236:239], v253 offset:26624\n\t" \
    "v_mfma_f32_16x16x32_bf16 v[220:223], v[88:91], v[240:243], v[220:223]\n\t" \
    "v_mfma_f32_16x16x32_bf16 v[212:215], v[244:247], v[240:243], v[212:215]\n\t" \
    "ds_read_b128 v[244:247], v254 offset:61440\n\t" \
    "s_waitcnt lgkmcnt(4)\n\t" \
    "v_mfma_f32_16x16x32_bf16 v[204:207], v[84:87], v[224:227], v[204:207]\n\t" \
    "ds_read_b128 v[240:243], v253 offset:7168\n\t" \
    "v_mfma_f32_16x16x32_bf16 v[208:211], v[92:95], v[224:227], v[208:211]\n\t" \
    "v_mfma_f32_16x16x32_bf16 v[200:203], v[248:251], v[224:227], v[200:203]\n\t" \
    "s_waitcnt lgkmcnt(4)\n\t" \
    "v_mfma_f32_16x16x32_bf16 v[216:219], v[84:87], v[228:231], v[216:219]\n\t" \
    "ds_read_b128 v[224:227], v253 offset:27648\n\t" \
    "v_mfma_f32_16x16x32_bf16 v[220:223], v[92:95], v[228:231], v[220:223]\n\t" \
    "v_mfma_f32_16x16x32_bf16 v[212:215], v[248:251], v[228:231], v[212:215]\n\t" \
    "ds_read_b128 v[248:251], v254 offset:62464\n\t" \
    "s_waitcnt lgkmcnt(5)\n\t" \
    "v_mfma_f32_16x16x32_bf16 v[204:207], v[104:107], v[232:235], v[204:207]\n\t" \
    "ds_read_b128 v[228:231], v253 offset:8192\n\t" \
    "v_mfma_f32_16x16x32_bf16 v[208:211], v[112:115], v[232:235], v[208:211]\n\t" \
    "v_mfma_f32_16x16x32_bf16 v[200:203], v[96:99], v[232:235], v[200:203]\n\t" \
    "s_waitcnt lgkmcnt(5)\n\t" \
    "v_mfma_f32_16x16x32_bf16 v[216:219], v[104:107], v[236:239], v[216:219]\n\t" \
    "ds_read_b128 v[232:235], v253 offset:28672\n\t" \
    "v_mfma_f32_16x16x32_bf16 v[220:223], v[112:115], v[236:239], v[220:223]\n\t" \
    "v_mfma_f32_16x16x32_bf16 v[212:215], v[96:99], v[236:239], v[212:215]\n\t" \
    "s_waitcnt lgkmcnt(4)\n\t" \
    "v_mfma_f32_16x16x32_bf16 v[204:207], v[108:111], v[240:243], v[204:207]\n\t" \
    "ds_read_b128 v[236:239], v253 offset:9216\n\t" \
    "v_mfma_f32_16x16x32_bf16 v[208:211], v[116:119], v[240:243], v[208:211]\n\t" \
    "v_mfma_f32_16x16x32_bf16 v[200:203], v[100:103], v[240:243], v[200:203]\n\t" \
    "s_waitcnt lgkmcnt(4)\n\t" \
    "v_mfma_f32_16x16x32_bf16 v[216:219], v[108:111], v[224:227], v[216:219]\n\t" \
    "ds_read_b128 v[240:243], v253 offset:29696\n\t" \
    "v_mfma_f32_16x16x32_bf16 v[220:223], v[116:119], v[224:227], v[220:223]\n\t" \
    "v_mfma_f32_16x16x32_bf16 v[212:215], v[100:103], v[224:227], v[212:215]\n\t" \
    "s_waitcnt lgkmcnt(0)\n\t" \
    "s_barrier \n\t" \
    LST7 \
    "v_mfma_f32_16x16x32_bf16 v[204:207], v[120:123], v[228:231], v[204:207]\n\t" \
    "ds_read_b128 v[224:227], v253 offset:12288\n\t" \
    "v_mfma_f32_16x16x32_bf16 v[208:211], v[128:131], v[228:231], v[208:211]\n\t" \
    "v_mfma_f32_16x16x32_bf16 v[200:203], v[244:247], v[228:231], v[200:203]\n\t" \
    "v_mfma_f32_16x16x32_bf16 v[216:219], v[120:123], v[232:235], v[216:219]\n\t" \
    "ds_read_b128 v[228:231], v253 offset:32768\n\t" \
    "v_mfma_f32_16x16x32_bf16 v[220:223], v[128:131], v[232:235], v[220:223]\n\t" \
    "v_mfma_f32_16x16x32_bf16 v[212:215], v[244:247], v[232:235], v[212:215]\n\t" \
    "ds_read_b128 v[244:247], v254 offset:63488\n\t" \
    "v_mfma_f32_16x16x32_bf16 v[204:207], v[124:127], v[236:239], v[204:207]\n\t" \
    "ds_read_b128 v[232:235], v253 offset:13312\n\t" \
    "v_mfma_f32_16x16x32_bf16 v[208:211], v[132:135], v[236:239], v[208:211]\n\t" \
    "v_mfma_f32_16x16x32_bf16 v[200:203], v[248:251], v[236:239], v[200:203]\n\t" \
    "v_mfma_f32_16x16x32_bf16 v[216:219], v[124:127], v[240:243], v[216:219]\n\t" \
    "ds_read_b128 v[236:239], v253 offset:33792\n\t" \
    "v_mfma_f32_16x16x32_bf16 v[220:223], v[132:135], v[240:243], v[220:223]\n\t" \
    "v_mfma_f32_16x16x32_bf16 v[212:215], v[248:251], v[240:243], v[212:215]\n\t" \
    "ds_read_b128 v[248:251], v254 offset:64512\n\t" \
    "s_waitcnt lgkmcnt(5)\n\t" \
    "v_mfma_f32_16x16x32_bf16 v[204:207], v[160:163], v[224:227], v[204:207]\n\t" \
    "ds_read_b128 v[240:243], v253 offset:10240\n\t" \
    "s_and_b32 s44, s40, 3\n\t" \
    "v_mfma_f32_16x16x32_bf16 v[208:211], v[168:171], v[224:227], v[208:211]\n\t" \
    "s_lshl_b32 s44, s44, 17\n\t" \
    "s_waitcnt lgkmcnt(4)\n\t" \
    "v_mfma_f32_16x16x32_bf16 v[200:203], v[244:247], v[224:227], v[200:203]\n\t" \
    "s_add_u32 s44, s44, %[mb]\n\t" \
    "v_mfma_f32_16x16x32_bf16 v[216:219], v[160:163], v[228:231], v[216:219]\n\t" \
    "ds_read_b128 v[224:227], v253 offset:30720\n\t" \
    "s_add_u32 s46, s44, 0x4000\n\t" \
    "v_mfma_f32_16x16x32_bf16 v[220:223], v[168:171], v[228:231], v[220:223]\n\t" \
    "s_add_u32 s47, s44, 0x4400\n\t" \
    "v_mfma_f32_16x16x32_bf16 v[212:215], v[244:247], v[228:231], v[212:215]\n\t" \
    "ds_read_b128 v[244:247], v254 offset:55296\n\t" \
    "s_waitcnt lgkmcnt(5)\n\t" \
    "v_mfma_f32_16x16x32_bf16 v[204:207], v[164:167], v[232:235], v[204:207]\n\t" \
    "ds_read_b128 v[228:231], v253 offset:11264\n\t" \
    "v_mfma_f32_16x16x32_bf16 v[208:211], v[172:175], v[232:235], v[208:211]\n\t" \
    "s_waitcnt lgkmcnt(4)\n\t" \
    "v_mfma_f32_16x16x32_bf16 v[200:203], v[248:251], v[232:235], v[200:203]\n\t" \
    "v_mfma_f32_16x16x32_bf16 v[216:219], v[164:167], v[236:239], v[216:219]\n\t" \
    "ds_read_b128 v[232:235], v253 offset:31744\n\t" \
    "v_mfma_f32_16x16x32_bf16 v[220:223], v[172:175], v[236:239], v[220:223]\n\t" \
    "v_mfma_f32_16x16x32_bf16 v[212:215], v[248:251], v[236:239], v[212:215]\n\t" \
    "ds_read_b128 v[248:251], v254 offset:56320\n\t" \
    "s_waitcnt lgkmcnt(5)\n\t" \
    "v_mfma_f32_16x16x32_bf16 v[204:207], v[144:147], v[240:243], v[204:207]\n\t" \
    "ds_read_b128 v[236:239], v253 offset:16384\n\t" \
    "v_mfma_f32_16x16x32_bf16 v[208:211], v[152:155], v[240:243], v[208:211]\n\t" \
    "v_mfma_f32_16x16x32_bf16 v[200:203], v[136:139], v[240:243], v[200:203]\n\t" \
    "s_waitcnt lgkmcnt(5)\n\t" \
    "v_mfma_f32_16x16x32_bf16 v[216:219], v[144:147], v[224:227], v[216:219]\n\t" \
    "ds_read_b128 v[240:243], v253 offset:36864\n\t" \
    "v_mfma_f32_16x16x32_bf16 v[220:223], v[152:155], v[224:227], v[220:223]\n\t" \
    "v_mfma_f32_16x16x32_bf16 v[212:215], v[136:139], v[224:227], v[212:215]\n\t" \
    "s_waitcnt lgkmcnt(4)\n\t" \
    "v_mfma_f32_16x16x32_bf16 v[204:207], v[148:151], v[228:231], v[204:207]\n\t" \
    "ds_read_b128 v[224:227], v253 offset:17408\n\t" \
    "v_mfma_f32_16x16x32_bf16 v[208:211], v[156:159], v[228:231], v[208:211]\n\t" \
    "v_mfma_f32_16x16x32_bf16 v[200:203], v[140:143], v[228:231], v[200:203]\n\t" \
    "s_waitcnt lgkmcnt(4)\n\t" \
    "v_mfma_f32_16x16x32_bf16 v[216:219], v[148:151], v[232:235], v[216:219]\n\t" \
    "ds_read_b128 v[228:231], v253 offset:37888\n\t" \
    "v_mfma_f32_16x16x32_bf16 v[220:223], v[156:159], v[232:235], v[220:223]\n\t" \
    "v_mfma_f32_16x16x32_bf16 v[212:215], v[140:143], v[232:235], v[212:215]\n\t" \
    "s_waitcnt lgkmcnt(3)\n\t" \
    "v_mfma_f32_16x16x32_bf16 v[204:207], v[0:3], v[236:239], v[204:207]\n\t" \
    "ds_read_b128 v[232:235], v253 offset:14336\n\t" \
    "v_mfma_f32_16x16x32_bf16 v[208:211], v[8:11], v[236:239], v[208:211]\n\t" \
    "v_mfma_f32_16x16x32_bf16 v[200:203], v[244:247], v[236:239], v[200:203]\n\t" \
    "s_waitcnt lgkmcnt(3)\n\t" \
    "v_mfma_f32_16x16x32_bf16 v[216:219], v[0:3], v[240:243], v[216:219]\n\t" \
    "ds_read_b128 v[236:239], v253 offset:15360\n\t" \
    "v_mfma_f32_16x16x32_bf16 v[220:223], v[8:11], v[240:243], v[220:223]\n\t" \
    "v_mfma_f32_16x16x32_bf16 v[212:215], v[244:247], v[240:243], v[212:215]\n\t" \
    "s_waitcnt lgkmcnt(3)\n\t" \
    "v_mfma_f32_16x16x32_bf16 v[204:207], v[4:7], v[224:227], v[204:207]\n\t" \
    "ds_read_b128 v[240:243], v253 offset:18432\n\t" \
    "v_mfma_f32_16x16x32_bf16 v[208:211], v[12:15], v[224:227], v[208:211]\n\t" \
    "v_mfma_f32_16x16x32_bf16 v[200:203], v[248:251], v[224:227], v[200:203]\n\t" \
    "s_waitcnt lgkmcnt(3)\n\t" \
    "v_mfma_f32_16x16x32_bf16 v[216:219], v[4:7], v[228:231], v[216:219]\n\t" \
    "ds_read_b128 v[224:227], v253 offset:19456\n\t" \
    "v_mfma_f32_16x16x32_bf16 v[220:223], v[12:15], v[228:231], v[220:223]\n\t" \
    "v_mfma_f32_16x16x32_bf16 v[212:215], v[248:251], v[228:231], v[212:215]\n\t" \
    "s_waitcnt lgkmcnt(3)\n\t" \
    "v_mfma_f32_16x16x32_bf16 v[204:207], v[184:187], v[232:235], v[204:207]\n\t" \
    "ds_read_b128 v[228:231], v253 offset:34816\n\t" \
    "v_mov_b64 v[244:245], 0\n\t" \
    "v_mfma_f32_16x16x32_bf16 v[208:211], v[192:195], v[232:235], v[208:211]\n\t" \
    "v_mov_b64 v[246:247], 0\n\t" \
    "v_mfma_f32_16x16x32_bf16 v[200:203], v[176:179], v[232:235], v[200:203]\n\t" \
    "v_mov_b64 v[248:249], 0\n\t" \
    "s_waitcnt lgkmcnt(3)\n\t" \
    "v_mfma_f32_16x16x32_bf16 v[204:207], v[188:191], v[236:239], v[204:207]\n\t" \
    "ds_read_b128 v[232:235], v253 offset:35840\n\t" \
    "v_mov_b64 v[250:251], 0\n\t" \
    "v_mfma_f32_16x16x32_bf16 v[208:211], v[196:199], v[236:239], v[208:211]\n\t" \
    "v_mfma_f32_16x16x32_bf16 v[200:203], v[180:183], v[236:239], v[200:203]\n\t" \
    "s_waitcnt lgkmcnt(3)\n\t" \
    "v_mfma_f32_16x16x32_bf16 v[204:207], v[24:27], v[240:243], v[204:207]\n\t" \
    "ds_read_b128 v[236:239], v253 offset:38912\n\t" \
    "v_mfma_f32_16x16x32_bf16 v[208:211], v[32:35], v[240:243], v[208:211]\n\t" \
    "v_mfma_f32_16x16x32_bf16 v[200:203], v[16:19], v[240:243], v[200:203]\n\t" \
    "s_waitcnt lgkmcnt(3)\n\t" \
    "v_mfma_f32_16x16x32_bf16 v[204:207], v[28:31], v[224:227], v[204:207]\n\t" \
    "ds_read_b128 v[240:243], v253 offset:39936\n\t" \
    "v_mfma_f32_16x16x32_bf16 v[208:211], v[36:39], v[224:227], v[208:211]\n\t" \
    "v_mfma_f32_16x16x32_bf16 v[200:203], v[20:23], v[224:227], v[200:203]\n\t" \
    "s_waitcnt lgkmcnt(3)\n\t" \
    "v_mfma_f32_16x16x32_bf16 v[216:219], v[184:187], v[228:231], v[216:219]\n\t" \
    "s_waitcnt lgkmcnt(2)\n\t" \
    "v_mfma_f32_16x16x32_bf16 v[216:219], v[188:191], v[232:235], v[216:219]\n\t" \
    "v_cvt_pk_bf16_f32 v204, v204, v205\n\t" \
    "v_cvt_pk_bf16_f32 v205, v206, v207\n\t" \
    "s_waitcnt lgkmcnt(1)\n\t" \
    "v_mfma_f32_16x16x32_bf16 v[216:219], v[24:27], v[236:239], v[216:219]\n\t" \
    "ds_write_b64 v254, v[204:205] offset:4104\n\t" \
    "v_cvt_pk_bf16_f32 v208, v208, v209\n\t" \
    "s_waitcnt lgkmcnt(1)\n\t" \
    "v_mfma_f32_16x16x32_bf16 v[216:219], v[28:31], v[240:243], v[216:219]\n\t" \
    "v_cvt_pk_bf16_f32 v209, v210, v211\n\t" \
    "ds_write_b64 v254, v[208:209] offset:5128\n\t" \
    "v_mfma_f32_16x16x32_bf16 v[220:223], v[192:195], v[228:231], v[220:223]\n\t" \
    "v_cvt_pk_bf16_f32 v200, v200, v201\n\t" \
    "v_cvt_pk_bf16_f32 v201, v202, v203\n\t" \
    "v_mfma_f32_16x16x32_bf16 v[220:223], v[196:199], v[232:235], v[220:223]\n\t" \
    "ds_write_b64 v254, v[200:201] offset:3080\n\t" \
    "ds_read_b128 v[200:203], v254 offset:55296\n\t" \
    "v_mfma_f32_16x16x32_bf16 v[220:223], v[32:35], v[236:239], v[220:223]\n\t" \
    "ds_read_b128 v[204:207], v254 offset:56320\n\t" \
    "v_mov_b64 v[208:209], 0\n\t" \
    "v_mfma_f32_16x16x32_bf16 v[220:223], v[36:39], v[240:243], v[220:223]\n\t" \
    "v_mov_b64 v[210:211], 0\n\t" \
    "v_cvt_pk_bf16_f32 v216, v216, v217\n\t" \
    "v_mfma_f32_16x16x32_bf16 v[212:215], v[176:179], v[228:231], v[212:215]\n\t" \
    "v_cvt_pk_bf16_f32 v217, v218, v219\n\t" \
    "ds_write_b64 v254, v[216:217] offset:10248\n\t" \
    "v_mfma_f32_16x16x32_bf16 v[212:215], v[180:183], v[232:235], v[212:215]\n\t" \
    "v_mfma_f32_16x16x32_bf16 v[212:215], v[16:19], v[236:239], v[212:215]\n\t" \
    "v_cvt_pk_bf16_f32 v220, v220, v221\n\t" \
    "v_cvt_pk_bf16_f32 v221, v222, v223\n\t" \
    "v_mfma_f32_16x16x32_bf16 v[212:215], v[20:23], v[240:243], v[212:215]\n\t" \
    "ds_write_b64 v254, v[220:221] offset:11272\n\t" \
    "ds_read_b128 v[216:219], v254 offset:57344\n\t" \
    "ds_read_b128 v[220:223], v254 offset:58368\n\t" \
    "s_nop 4\n\t" \
    "v_cvt_pk_bf16_f32 v212, v212, v213\n\t" \
    "v_cvt_pk_bf16_f32 v213, v214, v215\n\t" \
    "ds_write_b64 v254, v[212:213] offset:9224\n\t" \
    LST6 \
    "buffer_load_dwordx4 v[224:227], v253, %[rs], s46 offen sc1\n\t" \
    "buffer_load_dwordx4 v[228:231], v253, %[rs], s47 offen sc1\n\t" \
    "s_waitcnt lgkmcnt(0)\n\t" \
    "v_bfe_i32 v212, v253, 5, 1\n\t" \
    "v_and_b32 v212, 0xaaaa, v212\n\t" \
    "v_or_b32 v212, 0x4444, v212\n\t" \
    ".Ls32_%=:\n\t" \
    "s_waitcnt vmcnt(1)\n\t" \
    "v_max_u32 v214, v224, v225\n\t" \
    "v_max3_u32 v214, v226, v227, v214\n\t" \
    "s_waitcnt vmcnt(0)\n\t" \
    "v_max3_u32 v214, v228, v229, v214\n\t" \
    "v_max3_u32 v214, v230, v231, v214\n\t" \
    "v_cmp_eq_u32 vcc, -1, v214\n\t" \
    "s_cbranch_vccz .Ls33_%=\n\t" \
    "v_max_u32 v214, v224, v225\n\t" \
    "v_max3_u32 v214, v226, v227, v214\n\t" \
    "v_cmp_eq_u32 vcc, -1, v214\n\t" \
    "s_cbranch_vccz .Ls34_%=\n\t" \
    "buffer_load_dwordx4 v[224:227], v253, %[rs], s46 offen sc1\n\t" \
    ".Ls34_%=:\n\t" \
    "v_max_u32 v214, v228, v229\n\t" \
    "v_max3_u32 v214, v230, v231, v214\n\t" \
    "v_cmp_eq_u32 vcc, -1, v214\n\t" \
    "s_cbranch_vccz .Ls35_%=\n\t" \
    "buffer_load_dwordx4 v[228:231], v253, %[rs], s47 offen sc1\n\t" \
    ".Ls35_%=:\n\t" \
    "s_cmp_eq_u32 s41, 0\n\t" \
    "s_cbranch_scc1 .Ls33_%=\n\t" \
    "s_sub_u32 s41, s41, 1\n\t" \
    "s_waitcnt vmcnt(0)\n\t" \
    "s_branch .Ls32_%=\n\t" \
    ".Ls33_%=:\n\t" \
    LST2 \
    "ds_write_b128 v253, v[224:227] offset:49152\n\t" \
    "ds_write_b128 v253, v[228:231] offset:50176\n\t" \
    "v_smfmac_f32_16x16x64_bf16 v[244:247], v[224:227], v[0:7], v212\n\t" \
    "v_smfmac_f32_16x16x64_bf16 v[248:251], v[224:227], v[8:15], v212\n\t" \
    "v_smfmac_f32_16x16x64_bf16 v[208:211], v[224:227], v[200:207], v212\n\t" \
    "s_waitcnt lgkmcnt(0)\n\t" \
    "s_barrier \n\t" \
    LST3 \
    "ds_read_b128 v[232:235], v253 offset:40960\n\t" \
    "ds_read_b128 v[236:239], v253 offset:41984\n\t" \
    "ds_read_b128 v[240:243], v253 offset:43008\n\t" \
    "ds_read_b128 v[224:227], v253 offset:44032\n\t" \
    "ds_read_b128 v[200:203], v254 offset:59392\n\t" \
    "ds_read_b128 v[204:207], v254 offset:60416\n\t" \
    "v_smfmac_f32_16x16x64_bf16 v[208:211], v[228:231], v[16:23], v212\n\t" \
    "v_smfmac_f32_16x16x64_bf16 v[244:247], v[228:231], v[24:31], v212\n\t" \
    "v_smfmac_f32_16x16x64_bf16 v[248:251], v[228:231], v[32:39], v212\n\t" \
    "ds_read_b128 v[228:231], v253 offset:45056\n\t" \
    "s_waitcnt lgkmcnt(6)\n\t" \
    "v_smfmac_f32_16x16x64_bf16 v[208:211], v[232:235], v[216:223], v212\n\t" \
    "ds_read_b128 v[216:219], v254 offset:61440\n\t" \
    "ds_read_b128 v[220:223], v254 offset:62464\n\t" \
    "v_smfmac_f32_16x16x64_bf16 v[244:247], v[232:235], v[40:47], v212\n\t" \
    "v_smfmac_f32_16x16x64_bf16 v[248:251], v[232:235], v[48:55], v212\n\t" \
    "ds_read_b128 v[232:235], v253 offset:46080\n\t" \
    "s_waitcnt lgkmcnt(8)\n\t" \
    "v_smfmac_f32_16x16x64_bf16 v[208:211], v[236:239], v[56:63], v212\n\t" \
    "v_smfmac_f32_16x16x64_bf16 v[244:247], v[236:239], v[64:71], v212\n\t" \
    "v_smfmac_f32_16x16x64_bf16 v[248:251], v[236:239], v[72:79], v212\n\t" \
    "ds_read_b128 v[236:239], v253 offset:47104\n\t" \
    "s_waitcnt lgkmcnt(5)\n\t" \
    "v_smfmac_f32_16x16x64_bf16 v[208:211], v[240:243], v[200:207], v212\n\t" \
    "ds_read_b128 v[200:203], v254 offset:63488\n\t" \
    "ds_read_b128 v[204:207], v254 offset:64512\n\t" \
    "v_smfmac_f32_16x16x64_bf16 v[244:247], v[240:243], v[80:87], v212\n\t" \
    "v_smfmac_f32_16x16x64_bf16 v[248:251], v[240:243], v[88:95], v212\n\t" \
    "ds_read_b128 v[240:243], v253 offset:48128\n\t" \
    "v_smfmac_f32_16x16x64_bf16 v[244:247], v[224:227], v[104:111], v212\n\t" \
    "s_waitcnt lgkmcnt(7)\n\t" \
    "v_smfmac_f32_16x16x64_bf16 v[244:247], v[228:231], v[120:127], v212\n\t" \
    "s_waitcnt lgkmcnt(4)\n\t" \
    "v_smfmac_f32_16x16x64_bf16 v[244:247], v[232:235], v[144:151], v212\n\t" \
    "s_waitcnt lgkmcnt(3)\n\t" \
    "v_smfmac_f32_16x16x64_bf16 v[244:247], v[236:239], v[160:167], v212\n\t" \
    "s_waitcnt lgkmcnt(0)\n\t" \
    "v_smfmac_f32_16x16x64_bf16 v[244:247], v[240:243], v[184:191], v212\n\t" \
    "v_smfmac_f32_16x16x64_bf16 v[248:251], v[224:227], v[112:119], v212\n\t" \
    "v_smfmac_f32_16x16x64_bf16 v[248:251], v[228:231], v[128:135], v212\n\t" \
    "v_smfmac_f32_16x16x64_bf16 v[248:251], v[232:235], v[152:159], v212\n\t" \
    "v_add_f32 v244, v244, v246\n\t" \
    "v_add_f32 v245, v245, v247\n\t" \
    "v_smfmac_f32_16x16x64_bf16 v[248:251], v[236:239], v[168:175], v212\n\t" \
    "v_cvt_pk_bf16_f32 v244, v244, v245\n\t" \
    "ds_write_b16 v255, v244 offset:512\n\t" \
    "v_smfmac_f32_16x16x64_bf16 v[248:251], v[240:243], v[192:199], v212\n\t" \
    "ds_write_b16_d16_hi v255, v244 offset:528\n\t" \
    "v_smfmac_f32_16x16x64_bf16 v[208:211], v[224:227], v[96:103], v212\n\t" \
    "v_smfmac_f32_16x16x64_bf16 v[208:211], v[228:231], v[216:223], v212\n\t" \
    "v_smfmac_f32_16x16x64_bf16 v[208:211], v[232:235], v[136:143], v212\n\t" \
    "v_add_f32 v248, v248, v250\n\t" \
    "v_add_f32 v249, v249, v251\n\t" \
    "v_smfmac_f32_16x16x64_bf16 v[208:211], v[236:239], v[200:207], v212\n\t" \
    "v_cvt_pk_bf16_f32 v248, v248, v249\n\t" \
    "ds_write_b16 v255, v248 offset:1024\n\t" \
    "v_smfmac_f32_16x16x64_bf16 v[208:211], v[240:243], v[176:183], v212\n\t" \
    "ds_write_b16_d16_hi v255, v248 offset:1040\n\t" \
    "s_nop 6\n\t" \
    "v_add_f32 v208, v208, v210\n\t" \
    "v_add_f32 v209, v209, v211\n\t" \
    "v_cvt_pk_bf16_f32 v208, v208, v209\n\t" \
    "ds_write_b16 v255, v208 offset:0\n\t" \
    "ds_write_b16_d16_hi v255, v208 offset:16\n\t" \
    "s_waitcnt lgkmcnt(0)\n\t" \
    "ds_read_b128 v[208:211], v254 offset:24576\n\t" \
    "ds_read_b128 v[224:227], v253 offset:0\n\t" \
    "ds_read_b128 v[228:231], v253 offset:20480\n\t" \
    "ds_read_b128 v[212:215], v254 offset:25600\n\t" \
    "ds_read_b128 v[232:235], v253 offset:1024\n\t" \
    "ds_read_b128 v[236:239], v253 offset:21504\n\t" \
    "ds_read_b128 v[216:219], v254 offset:26624\n\t" \
    "ds_read_b128 v[240:243], v253 offset:2048\n\t" \
    "ds_read_b128 v[244:247], v253 offset:22528\n\t" \
    "ds_read_b128 v[220:223], v254 offset:27648\n\t" \
    "ds_read_b128 v[248:251], v253 offset:3072\n\t" \
    "s_waitcnt lgkmcnt(0)\n\t" \
    "s_barrier \n\t" \
    LST4 \
    LFLUSH200 \
    "s_add_u32 s40, s40, 1\n\t" \
    "s_cmp_lg_u32 s40, 64\n\t" \
    "s_cbranch_scc1 .Ls31_%=\n\t" \
    "s_branch .Lend%=\n\t" \
    ".Ls2_%=:\n\t" \
    "ds_read_b128 v[224:227], v253 offset:0\n\t" \
    "ds_read_b128 v[244:247], v254 offset:53248\n\t" \
    "ds_read_b128 v[228:231], v253 offset:20480\n\t" \
    "ds_read_b128 v[232:235], v253 offset:1024\n\t" \
    "ds_read_b128 v[236:239], v253 offset:21504\n\t" \
    "ds_read_b128 v[248:251], v254 offset:54272\n\t" \
    ".Ls21_%=:\n\t" \
    "s_waitcnt lgkmcnt(5)\n\t" \
    "v_mfma_f32_16x16x32_bf16 v[204:207], v[40:43], v[224:227], 0\n\t" \
    "ds_read_b128 v[240:243], v253 offset:2048\n\t" \
    "v_mfma_f32_16x16x32_bf16 v[208:211], v[48:51], v[224:227], 0\n\t" \
    "s_waitcnt lgkmcnt(5)\n\t" \
    "v_mfma_f32_16x16x32_bf16 v[200:203], v[244:247], v[224:227], 0\n\t" \
    "s_waitcnt lgkmcnt(4)\n\t" \
    "v_mfma_f32_16x16x32_bf16 v[216:219], v[40:43], v[228:231], 0\n\t" \
    "ds_read_b128 v[224:227], v253 offset:22528\n\t" \
    "v_mfma_f32_16x16x32_bf16 v[220:223], v[48:51], v[228:231], 0\n\t" \
    "v_mfma_f32_16x16x32_bf16 v[212:215], v[244:247], v[228:231], 0\n\t" \
    "ds_read_b128 v[244:247], v254 offset:55296\n\t" \
    "s_waitcnt lgkmcnt(5)\n\t" \
    "v_mfma_f32_16x16x32_bf16 v[204:207], v[44:47], v[232:235], v[204:207]\n\t" \
    "ds_read_b128 v[228:231], v253 offset:3072\n\t" \
    "v_mfma_f32_16x16x32_bf16 v[208:211], v[52:55], v[232:235], v[208:211]\n\t" \
    "s_waitcnt lgkmcnt(4)\n\t" \
    "v_mfma_f32_16x16x32_bf16 v[200:203], v[248:251], v[232:235], v[200:203]\n\t" \
    "v_mfma_f32_16x16x32_bf16 v[216:219], v[44:47], v[236:239], v[216:219]\n\t" \
    "ds_read_b128 v[232:235], v253 offset:23552\n\t" \
    "v_mfma_f32_16x16x32_bf16 v[220:223], v[52:55], v[236:239], v[220:223]\n\t" \
    "v_mfma_f32_16x16x32_bf16 v[212:215], v[248:251], v[236:239], v[212:215]\n\t" \
    "ds_read_b128 v[248:251], v254 offset:56320\n\t" \
    "s_waitcnt lgkmcnt(5)\n\t" \
    "v_mfma_f32_16x16x32_bf16 v[204:207], v[64:67], v[240:243], v[204:207]\n\t" \
    "ds_read_b128 v[236:239], v253 offset:4096\n\t" \
    "v_mfma_f32_16x16x32_bf16 v[208:211], v[72:75], v[240:243], v[208:211]\n\t" \
    "v_mfma_f32_16x16x32_bf16 v[200:203], v[56:59], v[240:243], v[200:203]\n\t" \
    "s_waitcnt lgkmcnt(5)\n\t" \
    "v_mfma_f32_16x16x32_bf16 v[216:219], v[64:67], v[224:227], v[216:219]\n\t" \
    "ds_read_b128 v[240:243], v253 offset:24576\n\t" \
    "v_mfma_f32_16x16x32_bf16 v[220:223], v[72:75], v[224:227], v[220:223]\n\t" \
    "v_mfma_f32_16x16x32_bf16 v[212:215], v[56:59], v[224:227], v[212:215]\n\t" \
    "s_waitcnt lgkmcnt(4)\n\t" \
    "v_mfma_f32_16x16x32_bf16 v[204:207], v[68:71], v[228:231], v[204:207]\n\t" \
    "ds_read_b128 v[224:227], v253 offset:5120\n\t" \
    "v_mfma_f32_16x16x32_bf16 v[208:211], v[76:79], v[228:231], v[208:211]\n\t" \
    "v_mfma_f32_16x16x32_bf16 v[200:203], v[60:63], v[228:231], v[200:203]\n\t" \
    "s_waitcnt lgkmcnt(4)\n\t" \
    "v_mfma_f32_16x16x32_bf16 v[216:219], v[68:71], v[232:235], v[216:219]\n\t" \
    "ds_read_b128 v[228:231], v253 offset:25600\n\t" \
    "v_mfma_f32_16x16x32_bf16 v[220:223], v[76:79], v[232:235], v[220:223]\n\t" \
    "v_mfma_f32_16x16x32_bf16 v[212:215], v[60:63], v[232:235], v[212:215]\n\t" \
    "s_waitcnt lgkmcnt(3)\n\t" \
    "v_mfma_f32_16x16x32_bf16 v[204:207], v[80:83], v[236:239], v[204:207]\n\t" \
    "ds_read_b128 v[232:235], v253 offset:6144\n\t" \
    "v_mfma_f32_16x16x32_bf16 v[208:211], v[88:91], v[236:239], v[208:211]\n\t" \
    "v_mfma_f32_16x16x32_bf16 v[200:203], v[244:247], v[236:239], v[200:203]\n\t" \
    "s_waitcnt lgkmcnt(3)\n\t" \
    "v_mfma_f32_16x16x32_bf16 v[216:219], v[80:83], v[240:243], v[216:219]\n\t" \
    "ds_read_b128 v[236:239], v253 offset:26624\n\t" \
    "v_mfma_f32_16x16x32_bf16 v[220:223], v[88:91], v[240:243], v[220:223]\n\t" \
    "v_mfma_f32_16x16x32_bf16 v[212:215], v[244:247], v[240:243], v[212:215]\n\t" \
    "ds_read_b128 v[244:247], v254 offset:51200\n\t" \
    "s_waitcnt lgkmcnt(4)\n\t" \
    "v_mfma_f32_16x16x32_bf16 v[204:207], v[84:87], v[224:227], v[204:207]\n\t" \
    "ds_read_b128 v[240:243], v253 offset:7168\n\t" \
    "v_mfma_f32_16x16x32_bf16 v[208:211], v[92:95], v[224:227], v[208:211]\n\t" \
    "v_mfma_f32_16x16x32_bf16 v[200:203], v[248:251], v[224:227], v[200:203]\n\t" \
    "s_waitcnt lgkmcnt(4)\n\t" \
    "v_mfma_f32_16x16x32_bf16 v[216:219], v[84:87], v[228:231], v[216:219]\n\t" \
    "ds_read_b128 v[224:227], v253 offset:27648\n\t" \
    "v_mfma_f32_16x16x32_bf16 v[220:223], v[92:95], v[228:231], v[220:223]\n\t" \
    "v_mfma_f32_16x16x32_bf16 v[212:215], v[248:251], v[228:231], v[212:215]\n\t" \
    "ds_read_b128 v[248:251], v254 offset:52224\n\t" \
    "s_waitcnt lgkmcnt(5)\n\t" \
    "v_mfma_f32_16x16x32_bf16 v[204:207], v[104:107], v[232:235], v[204:207]\n\t" \
    "ds_read_b128 v[228:231], v253 offset:8192\n\t" \
    "v_mfma_f32_16x16x32_bf16 v[208:211], v[112:115], v[232:235], v[208:211]\n\t" \
    "v_mfma_f32_16x16x32_bf16 v[200:203], v[96:99], v[232:235], v[200:203]\n\t" \
    "s_waitcnt lgkmcnt(5)\n\t" \
    "v_mfma_f32_16x16x32_bf16 v[216:219], v[104:107], v[236:239], v[216:219]\n\t" \
    "ds_read_b128 v[232:235], v253 offset:28672\n\t" \
    "v_mfma_f32_16x16x32_bf16 v[220:223], v[112:115], v[236:239], v[220:223]\n\t" \
    "v_mfma_f32_16x16x32_bf16 v[212:215], v[96:99], v[236:239], v[212:215]\n\t" \
    "s_waitcnt lgkmcnt(4)\n\t" \
    "v_mfma_f32_16x16x32_bf16 v[204:207], v[108:111], v[240:243], v[204:207]\n\t" \
    "ds_read_b128 v[236:239], v253 offset:9216\n\t" \
    "v_mfma_f32_16x16x32_bf16 v[208:211], v[116:119], v[240:243], v[208:211]\n\t" \
    "v_mfma_f32_16x16x32_bf16 v[200:203], v[100:103], v[240:243], v[200:203]\n\t" \
    "s_waitcnt lgkmcnt(4)\n\t" \
    "v_mfma_f32_16x16x32_bf16 v[216:219], v[108:111], v[224:227], v[216:219]\n\t" \
    "ds_read_b128 v[240:243], v253 offset:29696\n\t" \
    "v_mfma_f32_16x16x32_bf16 v[220:223], v[116:119], v[224:227], v[220:223]\n\t" \
    "v_mfma_f32_16x16x32_bf16 v[212:215], v[100:103], v[224:227], v[212:215]\n\t" \
    "s_waitcnt lgkmcnt(0)\n\t" \
    "s_barrier \n\t" \
    LST7 \
    "v_mfma_f32_16x16x32_bf16 v[204:207], v[0:3], v[228:231], v[204:207]\n\t" \
    "ds_read_b128 v[224:227], v253 offset:10240\n\t" \
    "v_mfma_f32_16x16x32_bf16 v[208:211], v[8:11], v[228:231], v[208:211]\n\t" \
    "v_mfma_f32_16x16x32_bf16 v[200:203], v[244:247], v[228:231], v[200:203]\n\t" \
    "v_mfma_f32_16x16x32_bf16 v[216:219], v[0:3], v[232:235], v[216:219]\n\t" \
    "ds_read_b128 v[228:231], v253 offset:30720\n\t" \
    "v_mfma_f32_16x16x32_bf16 v[220:223], v[8:11], v[232:235], v[220:223]\n\t" \
    "v_mfma_f32_16x16x32_bf16 v[212:215], v[244:247], v[232:235], v[212:215]\n\t" \
    "ds_read_b128 v[244:247], v254 offset:57344\n\t" \
    "v_mfma_f32_16x16x32_bf16 v[204:207], v[4:7], v[236:239], v[204:207]\n\t" \
    "ds_read_b128 v[232:235], v253 offset:11264\n\t" \
    "v_mfma_f32_16x16x32_bf16 v[208:211], v[12:15], v[236:239], v[208:211]\n\t" \
    "v_mfma_f32_16x16x32_bf16 v[200:203], v[248:251], v[236:239], v[200:203]\n\t" \
    "v_mfma_f32_16x16x32_bf16 v[216:219], v[4:7], v[240:243], v[216:219]\n\t" \
    "ds_read_b128 v[236:239], v253 offset:31744\n\t" \
    "v_mfma_f32_16x16x32_bf16 v[220:223], v[12:15], v[240:243], v[220:223]\n\t" \
    "v_mfma_f32_16x16x32_bf16 v[212:215], v[248:251], v[240:243], v[212:215]\n\t" \
    "ds_read_b128 v[248:251], v254 offset:58368\n\t" \
    "s_waitcnt lgkmcnt(5)\n\t" \
    "v_mfma_f32_16x16x32_bf16 v[204:207], v[120:123], v[224:227], v[204:207]\n\t" \
    "ds_read_b128 v[240:243], v253 offset:12288\n\t" \
    "s_and_b32 s44, s40, 3\n\t" \
    "v_mfma_f32_16x16x32_bf16 v[208:211], v[128:131], v[224:227], v[208:211]\n\t" \
    "s_lshl_b32 s44, s44, 17\n\t" \
    "s_waitcnt lgkmcnt(4)\n\t" \
    "v_mfma_f32_16x16x32_bf16 v[200:203], v[244:247], v[224:227], v[200:203]\n\t" \
    "s_add_u32 s44, s44, %[mb]\n\t" \
    "v_mfma_f32_16x16x32_bf16 v[216:219], v[120:123], v[228:231], v[216:219]\n\t" \
    "ds_read_b128 v[224:227], v253 offset:32768\n\t" \
    "s_add_u32 s46, s44, 0x1000\n\t" \
    "v_mfma_f32_16x16x32_bf16 v[220:223], v[128:131], v[228:231], v[220:223]\n\t" \
    "s_add_u32 s47, s44, 0x1c00\n\t" \
    "v_mfma_f32_16x16x32_bf16 v[212:215], v[244:247], v[228:231], v[212:215]\n\t" \
    "ds_read_b128 v[244:247], v254 offset:59392\n\t" \
    "s_waitcnt lgkmcnt(5)\n\t" \
    "v_mfma_f32_16x16x32_bf16 v[204:207], v[124:127], v[232:235], v[204:207]\n\t" \
    "ds_read_b128 v[228:231], v253 offset:13312\n\t" \
    "v_mfma_f32_16x16x32_bf16 v[208:211], v[132:135], v[232:235], v[208:211]\n\t" \
    "s_waitcnt lgkmcnt(4)\n\t" \
    "v_mfma_f32_16x16x32_bf16 v[200:203], v[248:251], v[232:235], v[200:203]\n\t" \
    "v_mfma_f32_16x16x32_bf16 v[216:219], v[124:127], v[236:239], v[216:219]\n\t" \
    "ds_read_b128 v[232:235], v253 offset:33792\n\t" \
    "v_mfma_f32_16x16x32_bf16 v[220:223], v[132:135], v[236:239], v[220:223]\n\t" \
    "v_mfma_f32_16x16x32_bf16 v[212:215], v[248:251], v[236:239], v[212:215]\n\t" \
    "ds_read_b128 v[248:251], v254 offset:60416\n\t" \
    "s_waitcnt lgkmcnt(5)\n\t" \
    "v_mfma_f32_16x16x32_bf16 v[204:207], v[144:147], v[240:243], v[204:207]\n\t" \
    "ds_read_b128 v[236:239], v253 offset:16384\n\t" \
    "v_mfma_f32_16x16x32_bf16 v[208:211], v[152:155], v[240:243], v[208:211]\n\t" \
    "v_mfma_f32_16x16x32_bf16 v[200:203], v[136:139], v[240:243], v[200:203]\n\t" \
    "s_waitcnt lgkmcnt(5)\n\t" \
    "v_mfma_f32_16x16x32_bf16 v[216:219], v[144:147], v[224:227], v[216:219]\n\t" \
    "ds_read_b128 v[240:243], v253 offset:36864\n\t" \
    "v_mfma_f32_16x16x32_bf16 v[220:223], v[152:155], v[224:227], v[220:223]\n\t" \
    "v_mfma_f32_16x16x32_bf16 v[212:215], v[136:139], v[224:227], v[212:215]\n\t" \
    "s_waitcnt lgkmcnt(4)\n\t" \
    "v_mfma_f32_16x16x32_bf16 v[204:207], v[148:151], v[228:231], v[204:207]\n\t" \
    "ds_read_b128 v[224:227], v253 offset:17408\n\t" \
    "v_mfma_f32_16x16x32_bf16 v[208:211], v[156:159], v[228:231], v[208:211]\n\t" \
    "v_mfma_f32_16x16x32_bf16 v[200:203], v[140:143], v[228:231], v[200:203]\n\t" \
    "s_waitcnt lgkmcnt(4)\n\t" \
    "v_mfma_f32_16x16x32_bf16 v[216:219], v[148:151], v[232:235], v[216:219]\n\t" \
    "ds_read_b128 v[228:231], v253 offset:37888\n\t" \
    "v_mfma_f32_16x16x32_bf16 v[220:223], v[156:159], v[232:235], v[220:223]\n\t" \
    "v_mfma_f32_16x16x32_bf16 v[212:215], v[140:143], v[232:235], v[212:215]\n\t" \
    "s_waitcnt lgkmcnt(3)\n\t" \
    "v_mfma_f32_16x16x32_bf16 v[204:207], v[160:163], v[236:239], v[204:207]\n\t" \
    "ds_read_b128 v[232:235], v253 offset:14336\n\t" \
    "v_mfma_f32_16x16x32_bf16 v[208:211], v[168:171], v[236:239], v[208:211]\n\t" \
    "v_mfma_f32_16x16x32_bf16 v[200:203], v[244:247], v[236:239], v[200:203]\n\t" \
    "s_waitcnt lgkmcnt(3)\n\t" \
    "v_mfma_f32_16x16x32_bf16 v[216:219], v[160:163], v[240:243], v[216:219]\n\t" \
    "ds_read_b128 v[236:239], v253 offset:15360\n\t" \
    "v_mfma_f32_16x16x32_bf16 v[220:223], v[168:171], v[240:243], v[220:223]\n\t" \
    "v_mfma_f32_16x16x32_bf16 v[212:215], v[244:247], v[240:243], v[212:215]\n\t" \
    "s_waitcnt lgkmcnt(3)\n\t" \
    "v_mfma_f32_16x16x32_bf16 v[204:207], v[164:167], v[224:227], v[204:207]\n\t" \
    "ds_read_b128 v[240:243], v253 offset:18432\n\t" \
    "v_mfma_f32_16x16x32_bf16 v[208:211], v[172:175], v[224:227], v[208:211]\n\t" \
    "v_mfma_f32_16x16x32_bf16 v[200:203], v[248:251], v[224:227], v[200:203]\n\t" \
    "s_waitcnt lgkmcnt(3)\n\t" \
    "v_mfma_f32_16x16x32_bf16 v[216:219], v[164:167], v[228:231], v[216:219]\n\t" \
    "ds_read_b128 v[224:227], v253 offset:19456\n\t" \
    "v_mfma_f32_16x16x32_bf16 v[220:223], v[172:175], v[228:231], v[220:223]\n\t" \
    "v_mfma_f32_16x16x32_bf16 v[212:215], v[248:251], v[228:231], v[212:215]\n\t" \
    "s_waitcnt lgkmcnt(3)\n\t" \
    "v_mfma_f32_16x16x32_bf16 v[204:207], v[24:27], v[232:235], v[204:207]\n\t" \
    "ds_read_b128 v[228:231], v253 offset:34816\n\t" \
    "v_mov_b64 v[244:245], 0\n\t" \
    "v_mfma_f32_16x16x32_bf16 v[208:211], v[32:35], v[232:235], v[208:211]\n\t" \
    "v_mov_b64 v[246:247], 0\n\t" \
    "v_mfma_f32_16x16x32_bf16 v[200:203], v[16:19], v[232:235], v[200:203]\n\t" \
    "v_mov_b64 v[248:249], 0\n\t" \
    "s_waitcnt lgkmcnt(3)\n\t" \
    "v_mfma_f32_16x16x32_bf16 v[204:207], v[28:31], v[236:239], v[204:207]\n\t" \
    "ds_read_b128 v[232:235], v253 offset:35840\n\t" \
    "v_mov_b64 v[250:251], 0\n\t" \
    "v_mfma_f32_16x16x32_bf16 v[208:211], v[36:39], v[236:239], v[208:211]\n\t" \
    "v_mfma_f32_16x16x32_bf16 v[200:203], v[20:23], v[236:239], v[200:203]\n\t" \
    "s_waitcnt lgkmcnt(3)\n\t" \
    "v_mfma_f32_16x16x32_bf16 v[204:207], v[184:187], v[240:243], v[204:207]\n\t" \
    "ds_read_b128 v[236:239], v253 offset:38912\n\t" \
    "v_mfma_f32_16x16x32_bf16 v[208:211], v[192:195], v[240:243], v[208:211]\n\t" \
    "v_mfma_f32_16x16x32_bf16 v[200:203], v[176:179], v[240:243], v[200:203]\n\t" \
    "s_waitcnt lgkmcnt(3)\n\t" \
    "v_mfma_f32_16x16x32_bf16 v[204:207], v[188:191], v[224:227], v[204:207]\n\t" \
    "ds_read_b128 v[240:243], v253 offset:39936\n\t" \
    "v_mfma_f32_16x16x32_bf16 v[208:211], v[196:199], v[224:227], v[208:211]\n\t" \
    "v_mfma_f32_16x16x32_bf16 v[200:203], v[180:183], v[224:227], v[200:203]\n\t" \
    "s_waitcnt lgkmcnt(3)\n\t" \
    "v_mfma_f32_16x16x32_bf16 v[216:219], v[24:27], v[228:231], v[216:219]\n\t" \
    "s_waitcnt lgkmcnt(2)\n\t" \
    "v_mfma_f32_16x16x32_bf16 v[216:219], v[28:31], v[232:235], v[216:219]\n\t" \
    "v_cvt_pk_bf16_f32 v204, v204, v205\n\t" \
    "v_cvt_pk_bf16_f32 v205, v206, v207\n\t" \
    "s_waitcnt lgkmcnt(1)\n\t" \
    "v_mfma_f32_16x16x32_bf16 v[216:219], v[184:187], v[236:239], v[216:219]\n\t" \
    "ds_write_b64 v254, v[204:205] offset:7176\n\t" \
    "v_cvt_pk_bf16_f32 v208, v208, v209\n\t" \
    "s_waitcnt lgkmcnt(1)\n\t" \
    "v_mfma_f32_16x16x32_bf16 v[216:219], v[188:191], v[240:243], v[216:219]\n\t" \
    "v_cvt_pk_bf16_f32 v209, v210, v211\n\t" \
    "ds_write_b64 v254, v[208:209] offset:8200\n\t" \
    "v_mfma_f32_16x16x32_bf16 v[220:223], v[32:35], v[228:231], v[220:223]\n\t" \
    "v_cvt_pk_bf16_f32 v200, v200, v201\n\t" \
    "v_cvt_pk_bf16_f32 v201, v202, v203\n\t" \
    "v_mfma_f32_16x16x32_bf16 v[220:223], v[36:39], v[232:235], v[220:223]\n\t" \
    "ds_write_b64 v254, v[200:201] offset:6152\n\t" \
    "ds_read_b128 v[200:203], v254 offset:51200\n\t" \
    "v_mfma_f32_16x16x32_bf16 v[220:223], v[192:195], v[236:239], v[220:223]\n\t" \
    "ds_read_b128 v[204:207], v254 offset:52224\n\t" \
    "v_mov_b64 v[208:209], 0\n\t" \
    "v_mfma_f32_16x16x32_bf16 v[220:223], v[196:199], v[240:243], v[220:223]\n\t" \
    "v_mov_b64 v[210:211], 0\n\t" \
    "v_cvt_pk_bf16_f32 v216, v216, v217\n\t" \
    "v_mfma_f32_16x16x32_bf16 v[212:215], v[16:19], v[228:231], v[212:215]\n\t" \
    "v_cvt_pk_bf16_f32 v217, v218, v219\n\t" \
    "ds_write_b64 v254, v[216:217] offset:13320\n\t" \
    "v_mfma_f32_16x16x32_bf16 v[212:215], v[20:23], v[232:235], v[212:215]\n\t" \
    "v_mfma_f32_16x16x32_bf16 v[212:215], v[176:179], v[236:239], v[212:215]\n\t" \
    "v_cvt_pk_bf16_f32 v220, v220, v221\n\t" \
    "v_cvt_pk_bf16_f32 v221, v222, v223\n\t" \
    "v_mfma_f32_16x16x32_bf16 v[212:215], v[180:183], v[240:243], v[212:215]\n\t" \
    "ds_write_b64 v254, v[220:221] offset:14344\n\t" \
    "ds_read_b128 v[216:219], v254 offset:53248\n\t" \
    "ds_read_b128 v[220:223], v254 offset:54272\n\t" \
    "s_nop 4\n\t" \
    "v_cvt_pk_bf16_f32 v212, v212, v213\n\t" \
    "v_cvt_pk_bf16_f32 v213, v214, v215\n\t" \
    "ds_write_b64 v254, v[212:213] offset:12296\n\t" \
    LST6 \
    "buffer_load_dwordx4 v[224:227], v253, %[rs], s46 offen sc1\n\t" \
    "buffer_load_dwordx4 v[228:231], v253, %[rs], s47 offen sc1\n\t" \
    "s_waitcnt lgkmcnt(0)\n\t" \
    "v_bfe_i32 v212, v253, 5, 1\n\t" \
    "v_and_b32 v212, 0xaaaa, v212\n\t" \
    "v_or_b32 v212, 0x4444, v212\n\t" \
    ".Ls22_%=:\n\t" \
    "s_waitcnt vmcnt(1)\n\t" \
    "v_max_u32 v214, v224, v225\n\t" \
    "v_max3_u32 v214, v226, v227, v214\n\t" \
    "s_waitcnt vmcnt(0)\n\t" \
    "v_max3_u32 v214, v228, v229, v214\n\t" \
    "v_max3_u32 v214, v230, v231, v214\n\t" \
    "v_cmp_eq_u32 vcc, -1, v214\n\t" \
    "s_cbranch_vccz .Ls23_%=\n\t" \
    "v_max_u32 v214, v224, v225\n\t" \
    "v_max3_u32 v214, v226, v227, v214\n\t" \
    "v_cmp_eq_u32 vcc, -1, v214\n\t" \
    "s_cbranch_vccz .Ls24_%=\n\t" \
    "buffer_load_dwordx4 v[224:227], v253, %[rs], s46 offen sc1\n\t" \
    ".Ls24_%=:\n\t" \
    "v_max_u32 v214, v228, v229\n\t" \
    "v_max3_u32 v214, v230, v231, v214\n\t" \
    "v_cmp_eq_u32 vcc, -1, v214\n\t" \
    "s_cbranch_vccz .Ls25_%=\n\t" \
    "buffer_load_dwordx4 v[228:231], v253, %[rs], s47 offen sc1\n\t" \
    ".Ls25_%=:\n\t" \
    "s_cmp_eq_u32 s41, 0\n\t" \
    "s_cbranch_scc1 .Ls23_%=\n\t" \
    "s_sub_u32 s41, s41, 1\n\t" \
    "s_waitcnt vmcnt(0)\n\t" \
    "s_branch .Ls22_%=\n\t" \
    ".Ls23_%=:\n\t" \
    LST2 \
    "ds_write_b128 v253, v[224:227] offset:45056\n\t" \
    "ds_write_b128 v253, v[228:231] offset:48128\n\t" \
    "v_smfmac_f32_16x16x64_bf16 v[244:247], v[224:227], v[0:7], v212\n\t" \
    "v_smfmac_f32_16x16x64_bf16 v[248:251], v[224:227], v[8:15], v212\n\t" \
    "v_smfmac_f32_16x16x64_bf16 v[208:211], v[224:227], v[200:207], v212\n\t" \
    "s_waitcnt lgkmcnt(0)\n\t" \
    "s_barrier \n\t" \
    LST3 \
    "ds_read_b128 v[232:235], v253 offset:40960\n\t" \
    "ds_read_b128 v[236:239], v253 offset:41984\n\t" \
    "ds_read_b128 v[240:243], v253 offset:43008\n\t" \
    "ds_read_b128 v[224:227], v253 offset:44032\n\t" \
    "ds_read_b128 v[200:203], v254 offset:55296\n\t" \
    "ds_read_b128 v[204:207], v254 offset:56320\n\t" \
    "v_smfmac_f32_16x16x64_bf16 v[208:211], v[228:231], v[16:23], v212\n\t" \
    "v_smfmac_f32_16x16x64_bf16 v[244:247], v[228:231], v[24:31], v212\n\t" \
    "v_smfmac_f32_16x16x64_bf16 v[248:251], v[228:231], v[32:39], v212\n\t" \
    "ds_read_b128 v[228:231], v253 offset:46080\n\t" \
    "s_waitcnt lgkmcnt(6)\n\t" \
    "v_smfmac_f32_16x16x64_bf16 v[208:211], v[232:235], v[216:223], v212\n\t" \
    "ds_read_b128 v[216:219], v254 offset:57344\n\t" \
    "ds_read_b128 v[220:223], v254 offset:58368\n\t" \
    "v_smfmac_f32_16x16x64_bf16 v[244:247], v[232:235], v[40:47], v212\n\t" \
    "v_smfmac_f32_16x16x64_bf16 v[248:251], v[232:235], v[48:55], v212\n\t" \
    "ds_read_b128 v[232:235], v253 offset:47104\n\t" \
    "s_waitcnt lgkmcnt(8)\n\t" \
    "v_smfmac_f32_16x16x64_bf16 v[208:211], v[236:239], v[56:63], v212\n\t" \
    "v_smfmac_f32_16x16x64_bf16 v[244:247], v[236:239], v[64:71], v212\n\t" \
    "v_smfmac_f32_16x16x64_bf16 v[248:251], v[236:239], v[72:79], v212\n\t" \
    "ds_read_b128 v[236:239], v253 offset:49152\n\t" \
    "s_waitcnt lgkmcnt(5)\n\t" \
    "v_smfmac_f32_16x16x64_bf16 v[208:211], v[240:243], v[200:207], v212\n\t" \
    "ds_read_b128 v[200:203], v254 offset:59392\n\t" \
    "ds_read_b128 v[204:207], v254 offset:60416\n\t" \
    "v_smfmac_f32_16x16x64_bf16 v[244:247], v[240:243], v[80:87], v212\n\t" \
    "v_smfmac_f32_16x16x64_bf16 v[248:251], v[240:243], v[88:95], v212\n\t" \
    "ds_read_b128 v[240:243], v253 offset:50176\n\t" \
    "v_smfmac_f32_16x16x64_bf16 v[244:247], v[224:227], v[104:111], v212\n\t" \
    "s_waitcnt lgkmcnt(7)\n\t" \
    "v_smfmac_f32_16x16x64_bf16 v[244:247], v[228:231], v[120:127], v212\n\t" \
    "s_waitcnt lgkmcnt(4)\n\t" \
    "v_smfmac_f32_16x16x64_bf16 v[244:247], v[232:235], v[144:151], v212\n\t" \
    "s_waitcnt lgkmcnt(3)\n\t" \
    "v_smfmac_f32_16x16x64_bf16 v[244:247], v[236:239], v[160:167], v212\n\t" \
    "s_waitcnt lgkmcnt(0)\n\t" \
    "v_smfmac_f32_16x16x64_bf16 v[244:247], v[240:243], v[184:191], v212\n\t" \
    "v_smfmac_f32_16x16x64_bf16 v[248:251], v[224:227], v[112:119], v212\n\t" \
    "v_smfmac_f32_16x16x64_bf16 v[248:251], v[228:231], v[128:135], v212\n\t" \
    "v_smfmac_f32_16x16x64_bf16 v[248:251], v[232:235], v[152:159], v212\n\t" \
    "v_add_f32 v244, v244, v246\n\t" \
    "v_add_f32 v245, v245, v247\n\t" \
    "v_smfmac_f32_16x16x64_bf16 v[248:251], v[236:239], v[168:175], v212\n\t" \
    "v_cvt_pk_bf16_f32 v244, v244, v245\n\t" \
    "ds_write_b16 v255, v244 offset:512\n\t" \
    "v_smfmac_f32_16x16x64_bf16 v[248:251], v[240:243], v[192:199], v212\n\t" \
    "ds_write_b16_d16_hi v255, v244 offset:528\n\t" \
    "v_smfmac_f32_16x16x64_bf16 v[208:211], v[224:227], v[96:103], v212\n\t" \
    "v_smfmac_f32_16x16x64_bf16 v[208:211], v[228:231], v[216:223], v212\n\t" \
    "v_smfmac_f32_16x16x64_bf16 v[208:211], v[232:235], v[136:143], v212\n\t" \
    "v_add_f32 v248, v248, v250\n\t" \
    "v_add_f32 v249, v249, v251\n\t" \
    "v_smfmac_f32_16x16x64_bf16 v[208:211], v[236:239], v[200:207], v212\n\t" \
    "v_cvt_pk_bf16_f32 v248, v248, v249\n\t" \
    "ds_write_b16 v255, v248 offset:1024\n\t" \
    "v_smfmac_f32_16x16x64_bf16 v[208:211], v[240:243], v[176:183], v212\n\t" \
    "ds_write_b16_d16_hi v255, v248 offset:1040\n\t" \
    "s_nop 6\n\t" \
    "v_add_f32 v208, v208, v210\n\t" \
    "v_add_f32 v209, v209, v211\n\t" \
    "v_cvt_pk_bf16_f32 v208, v208, v209\n\t" \
    "ds_write_b16 v255, v208 offset:0\n\t" \
    "ds_write_b16_d16_hi v255, v208 offset:16\n\t" \
    "ds_read_b128 v[200:203], v254 offset:12288\n\t" \
    "ds_read_b128 v[204:207], v254 offset:13312\n\t" \
    "ds_read_b128 v[208:211], v254 offset:14336\n\t" \
    "ds_read_b128 v[212:215], v254 offset:15360\n\t" \
    "ds_read_b128 v[216:219], v254 offset:16384\n\t" \
    "ds_read_b128 v[220:223], v254 offset:17408\n\t" \
    "ds_read_b128 v[224:227], v254 offset:24576\n\t" \
    "ds_read_b128 v[228:231], v254 offset:25600\n\t" \
    "ds_read_b128 v[232:235], v254 offset:26624\n\t" \
    "ds_read_b128 v[236:239], v254 offset:27648\n\t" \
    "s_and_b32 s44, s40, 3\n\t" \
    "s_lshl_b32 s44, s44, 17\n\t" \
    "s_add_u32 s50, s44, %[pb]\n\t" \
    "s_add_u32 s51, s44, %[pm]\n\t" \
    "s_mov_b32 s52, 0xf0000\n\t" \
    "s_mov_b32 s53, 0xfff00ff\n\t" \
    "s_mov_b32 s54, 0x1f0001\n\t" \
    "s_mov_b32 s55, 0x1fff01ff\n\t" \
    "s_mov_b32 s56, 0x3f0003\n\t" \
    "s_mov_b32 s57, 0x3fff03ff\n\t" \
    "s_mov_b32 s58, 0x7f0007\n\t" \
    "s_mov_b32 s59, 0x7fff07ff\n\t" \
    "s_waitcnt lgkmcnt(0)\n\t" \
    "s_barrier \n\t" \
    LST4 \
    "v_and_b32 v252, 0xffffff7f, v254\n\t" \
    "v_mfma_f32_16x16x32_bf16 v[240:243], v[224:227], v[200:203], 0\n\t" \
    "ds_read_b128 v[224:227], v254 offset:28672\n\t" \
    "v_mfma_f32_16x16x32_bf16 v[240:243], v[228:231], v[204:207], v[240:243]\n\t" \
    "ds_read_b128 v[228:231], v254 offset:29696\n\t" \
    "v_mfma_f32_16x16x32_bf16 v[240:243], v[232:235], v[208:211], v[240:243]\n\t" \
    "ds_read_b128 v[232:235], v254 offset:18432\n\t" \
    "v_mfma_f32_16x16x32_bf16 v[240:243], v[236:239], v[212:215], v[240:243]\n\t" \
    "ds_read_b128 v[236:239], v254 offset:19456\n\t" \
    "s_waitcnt lgkmcnt(3)\n\t" \
    "v_mfma_f32_16x16x32_bf16 v[240:243], v[224:227], v[216:219], v[240:243]\n\t" \
    "ds_read_b128 v[224:227], v254 offset:20480\n\t" \
    "s_waitcnt lgkmcnt(3)\n\t" \
    "v_mfma_f32_16x16x32_bf16 v[240:243], v[228:231], v[220:223], v[240:243]\n\t" \
    "ds_read_b128 v[228:231], v254 offset:21504\n\t" \
    "s_waitcnt lgkmcnt(3)\n\t" \
    "v_mfma_f32_16x16x32_bf16 v[244:247], v[232:235], v[200:203], 0\n\t" \
    "ds_read_b128 v[232:235], v254 offset:22528\n\t" \
    "s_waitcnt lgkmcnt(3)\n\t" \
    "v_mfma_f32_16x16x32_bf16 v[244:247], v[236:239], v[204:207], v[244:247]\n\t" \
    "ds_read_b128 v[236:239], v254 offset:23552\n\t" \
    "s_waitcnt lgkmcnt(3)\n\t" \
    "v_mfma_f32_16x16x32_bf16 v[244:247], v[224:227], v[208:211], v[244:247]\n\t" \
    "ds_read_b128 v[224:227], v252 offset:3072\n\t" \
    "s_waitcnt lgkmcnt(3)\n\t" \
    "v_mfma_f32_16x16x32_bf16 v[244:247], v[228:231], v[212:215], v[244:247]\n\t" \
    "ds_read_b128 v[228:231], v252 offset:4096\n\t" \
    "s_waitcnt lgkmcnt(3)\n\t" \
    "v_mfma_f32_16x16x32_bf16 v[244:247], v[232:235], v[216:219], v[244:247]\n\t" \
    "ds_read_b128 v[232:235], v252 offset:5120\n\t" \
    "s_waitcnt lgkmcnt(3)\n\t" \
    "v_mfma_f32_16x16x32_bf16 v[244:247], v[236:239], v[220:223], v[244:247]\n\t" \
    "ds_read_b128 v[236:239], v252 offset:3200\n\t" \
    "s_waitcnt lgkmcnt(3)\n\t" \
    "v_mfma_f32_16x16x32_bf16 v[248:251], v[224:227], v[200:203], 0\n\t" \
    "ds_read_b128 v[224:227], v252 offset:4224\n\t" \
    "s_waitcnt lgkmcnt(3)\n\t" \
    "v_mfma_f32_16x16x32_bf16 v[248:251], v[228:231], v[204:207], v[248:251]\n\t" \
    "ds_read_b128 v[228:231], v252 offset:5248\n\t" \
    "s_waitcnt lgkmcnt(3)\n\t" \
    "v_mfma_f32_16x16x32_bf16 v[248:251], v[232:235], v[208:211], v[248:251]\n\t" \
    "s_waitcnt lgkmcnt(2)\n\t" \
    "v_mfma_f32_16x16x32_bf16 v[248:251], v[236:239], v[212:215], v[248:251]\n\t" \
    "s_waitcnt lgkmcnt(1)\n\t" \
    "v_mfma_f32_16x16x32_bf16 v[248:251], v[224:227], v[216:219], v[248:251]\n\t" \
    "s_waitcnt lgkmcnt(0)\n\t" \
    "v_mfma_f32_16x16x32_bf16 v[248:251], v[228:231], v[220:223], v[248:251]\n\t" \
    "v_mov_b32 v226, 0xff7fffff\n\t" \
    "v_cndmask_b32 v240, v240, v226, s[52:53]\n\t" \
    "v_cndmask_b32 v241, v241, v226, s[54:55]\n\t" \
    "v_cndmask_b32 v242, v242, v226, s[56:57]\n\t" \
    "v_cndmask_b32 v243, v243, v226, s[58:59]\n\t" \
    "v_max3_f32 v224, v240, v241, v242\n\t" \
    "v_max3_f32 v225, v244, v245, v246\n\t" \
    "v_max3_f32 v224, v224, v243, v247\n\t" \
    "v_lshrrev_b32 v200, 5, v253\n\t" \
    "v_and_b32 v201, 0xf0, v253\n\t" \
    "v_and_or_b32 v200, v200, 8, v201\n\t" \
    "v_max3_f32 v224, v224, v248, v249\n\t" \
    "v_max3_f32 v225, v225, v250, v251\n\t" \
    "v_max_f32 v224, v224, v225\n\t" \
    "v_mov_b32 v225, v224\n\t" \
    "s_nop 1\n\t" \
    "v_permlane16_swap_b32 v224, v225\n\t" \
    "v_max_f32 v224, v224, v225\n\t" \
    "v_mov_b32 v225, v224\n\t" \
    "s_nop 1\n\t" \
    "v_permlane32_swap_b32 v224, v225\n\t" \
    "v_max_f32 v224, v224, v225\n\t" \
    "v_mul_f32 v224, %[scl], v224\n\t" \
    "v_fma_f32 v240, v240, %[scl], -v224\n\t" \
    "v_fma_f32 v241, v241, %[scl], -v224\n\t" \
    "v_exp_f32 v240, v240\n\t" \
    "v_fma_f32 v242, v242, %[scl], -v224\n\t" \
    "v_exp_f32 v241, v241\n\t" \
    "v_fma_f32 v243, v243, %[scl], -v224\n\t" \
    "v_exp_f32 v242, v242\n\t" \
    "v_fma_f32 v244, v244, %[scl], -v224\n\t" \
    "v_exp_f32 v243, v243\n\t" \
    "v_fma_f32 v245, v245, %[scl], -v224\n\t" \
    "v_exp_f32 v244, v244\n\t" \
    "v_fma_f32 v246, v246, %[scl], -v224\n\t" \
    "v_exp_f32 v245, v245\n\t" \
    "v_fma_f32 v247, v247, %[scl], -v224\n\t" \
    "v_exp_f32 v246, v246\n\t" \
    "v_fma_f32 v248, v248, %[scl], -v224\n\t" \
    "v_exp_f32 v247, v247\n\t" \
    "v_fma_f32 v249, v249, %[scl], -v224\n\t" \
    "v_exp_f32 v248, v248\n\t" \
    "v_fma_f32 v250, v250, %[scl], -v224\n\t" \
    "v_exp_f32 v249, v249\n\t" \
    "v_fma_f32 v251, v251, %[scl], -v224\n\t" \
    "v_exp_f32 v250, v250\n\t" \
    "v_exp_f32 v251, v251\n\t" \
    "v_add_f32 v224, v240, v241\n\t" \
    "v_add_f32 v225, v242, v243\n\t" \
    "v_add_f32 v226, v244, v245\n\t" \
    "v_add_f32 v227, v246, v247\n\t" \
    "v_add_f32 v232, v248, v249\n\t" \
    "v_add_f32 v233, v250, v251\n\t" \
    "v_add_f32 v224, v224, v225\n\t" \
    "v_add_f32 v226, v226, v227\n\t" \
    "v_add_f32 v232, v232, v233\n\t" \
    "v_add_f32 v224, v224, v226\n\t" \
    "v_fma_f32 v224, v232, 0.5, v224\n\t" \
    "v_mov_b32 v225, v224\n\t" \
    "s_nop 1\n\t" \
    "v_permlane16_swap_b32 v224, v225\n\t" \
    "v_add_f32 v224, v224, v225\n\t" \
    "v_mov_b32 v225, v224\n\t" \
    "s_nop 1\n\t" \
    "v_permlane32_swap_b32 v224, v225\n\t" \
    "v_add_f32 v224, v224, v225\n\t" \
    "v_rcp_f32 v224, v224\n\t" \
    "s_nop 0\n\t" \
    "v_mul_f32 v240, v240, v224\n\t" \
    "v_mul_f32 v241, v241, v224\n\t" \
    "v_mul_f32 v242, v242, v224\n\t" \
    "v_mul_f32 v243, v243, v224\n\t" \
    "v_mul_f32 v244, v244, v224\n\t" \
    "v_mul_f32 v245, v245, v224\n\t" \
    "v_mul_f32 v246, v246, v224\n\t" \
    "v_mul_f32 v247, v247, v224\n\t" \
    "v_mul_f32 v248, v248, v224\n\t" \
    "v_mul_f32 v249, v249, v224\n\t" \
    "v_mul_f32 v250, v250, v224\n\t" \
    "v_mul_f32 v251, v251, v224\n\t" \
    "v_cvt_pk_bf16_f32 v206, |v240|, |v241|\n\t" \
    "v_cvt_pk_bf16_f32 v207, |v242|, |v243|\n\t" \
    "v_cvt_pk_bf16_f32 v204, |v244|, |v245|\n\t" \
    "v_cvt_pk_bf16_f32 v205, |v246|, |v247|\n\t" \
    "v_cvt_pk_bf16_f32 v208, |v248|, |v249|\n\t" \
    "v_cvt_pk_bf16_f32 v209, |v250|, |v251|\n\t" \
    "buffer_store_dwordx4 v[204:207], v253, %[rs], s50 offen sc1\n\t" \
    "s_mov_b32 exec_lo, -1\n\t" \
    "s_mov_b32 exec_hi, 0\n\t" \
    "buffer_store_dwordx2 v[208:209], v200, %[rs], s51 offen sc1\n\t" \
    "s_mov_b32 exec_hi, -1\n\t" \
    LST5 \
    LFLUSH212 \
    "ds_read_b128 v[224:227], v253 offset:0\n\t" \
    "ds_read_b128 v[244:247], v254 offset:53248\n\t" \
    "ds_read_b128 v[228:231], v253 offset:20480\n\t" \
    "ds_read_b128 v[232:235], v253 offset:1024\n\t" \
    "ds_read_b128 v[236:239], v253 offset:21504\n\t" \
    "ds_read_b128 v[248:251], v254 offset:54272\n\t" \
    "s_add_u32 s40, s40, 1\n\t" \
    "s_cmp_lg_u32 s40, 64\n\t" \
    "s_cbranch_scc1 .Ls21_%=\n\t" \
    "s_branch .Lend%=\n\t" \
    ".Ls1_%=:\n\t" \
    "ds_read_b128 v[224:227], v253 offset:0\n\t" \
    "ds_read_b128 v[244:247], v254 offset:40960\n\t" \
    "ds_read_b128 v[228:231], v253 offset:20480\n\t" \
    "ds_read_b128 v[232:235], v253 offset:1024\n\t" \
    "ds_read_b128 v[236:239], v253 offset:21504\n\t" \
    "ds_read_b128 v[248:251], v254 offset:41984\n\t" \
    ".Ls11_%=:\n\t" \
    "s_waitcnt lgkmcnt(5)\n\t" \
    "v_mfma_f32_16x16x32_bf16 v[204:207], v[64:67], v[224:227], 0\n\t" \
    "ds_read_b128 v[240:243], v253 offset:2048\n\t" \
    "v_mfma_f32_16x16x32_bf16 v[208:211], v[72:75], v[224:227], 0\n\t" \
    "v_mfma_f32_16x16x32_bf16 v[200:203], v[56:59], v[224:227], 0\n\t" \
    "s_waitcnt lgkmcnt(4)\n\t" \
    "v_mfma_f32_16x16x32_bf16 v[216:219], v[64:67], v[228:231], 0\n\t" \
    "ds_read_b128 v[224:227], v253 offset:22528\n\t" \
    "v_mfma_f32_16x16x32_bf16 v[220:223], v[72:75], v[228:231], 0\n\t" \
    "v_mfma_f32_16x16x32_bf16 v[212:215], v[56:59], v[228:231], 0\n\t" \
    "s_waitcnt lgkmcnt(4)\n\t" \
    "v_mfma_f32_16x16x32_bf16 v[204:207], v[68:71], v[232:235], v[204:207]\n\t" \
    "ds_read_b128 v[228:231], v253 offset:3072\n\t" \
    "v_mfma_f32_16x16x32_bf16 v[208:211], v[76:79], v[232:235], v[208:211]\n\t" \
    "v_mfma_f32_16x16x32_bf16 v[200:203], v[60:63], v[232:235], v[200:203]\n\t" \
    "s_waitcnt lgkmcnt(4)\n\t" \
    "v_mfma_f32_16x16x32_bf16 v[216:219], v[68:71], v[236:239], v[216:219]\n\t" \
    "ds_read_b128 v[232:235], v253 offset:23552\n\t" \
    "v_mfma_f32_16x16x32_bf16 v[220:223], v[76:79], v[236:239], v[220:223]\n\t" \
    "v_mfma_f32_16x16x32_bf16 v[212:215], v[60:63], v[236:239], v[212:215]\n\t" \
    "s_waitcnt lgkmcnt(3)\n\t" \
    "v_mfma_f32_16x16x32_bf16 v[204:207], v[104:107], v[240:243], v[204:207]\n\t" \
    "ds_read_b128 v[236:239], v253 offset:4096\n\t" \
    "v_mfma_f32_16x16x32_bf16 v[208:211], v[112:115], v[240:243], v[208:211]\n\t" \
    "v_mfma_f32_16x16x32_bf16 v[200:203], v[96:99], v[240:243], v[200:203]\n\t" \
    "s_waitcnt lgkmcnt(3)\n\t" \
    "v_mfma_f32_16x16x32_bf16 v[216:219], v[104:107], v[224:227], v[216:219]\n\t" \
    "ds_read_b128 v[240:243], v253 offset:24576\n\t" \
    "v_mfma_f32_16x16x32_bf16 v[220:223], v[112:115], v[224:227], v[220:223]\n\t" \
    "v_mfma_f32_16x16x32_bf16 v[212:215], v[96:99], v[224:227], v[212:215]\n\t" \
    "s_waitcnt lgkmcnt(3)\n\t" \
    "v_mfma_f32_16x16x32_bf16 v[204:207], v[108:111], v[228:231], v[204:207]\n\t" \
    "ds_read_b128 v[224:227], v253 offset:5120\n\t" \
    "v_mfma_f32_16x16x32_bf16 v[208:211], v[116:119], v[228:231], v[208:211]\n\t" \
    "v_mfma_f32_16x16x32_bf16 v[200:203], v[100:103], v[228:231], v[200:203]\n\t" \
    "s_waitcnt lgkmcnt(3)\n\t" \
    "v_mfma_f32_16x16x32_bf16 v[216:219], v[108:111], v[232:235], v[216:219]\n\t" \
    "ds_read_b128 v[228:231], v253 offset:25600\n\t" \
    "v_mfma_f32_16x16x32_bf16 v[220:223], v[116:119], v[232:235], v[220:223]\n\t" \
    "v_mfma_f32_16x16x32_bf16 v[212:215], v[100:103], v[232:235], v[212:215]\n\t" \
    "s_waitcnt lgkmcnt(3)\n\t" \
    "v_mfma_f32_16x16x32_bf16 v[204:207], v[0:3], v[236:239], v[204:207]\n\t" \
    "ds_read_b128 v[232:235], v253 offset:6144\n\t" \
    "v_mfma_f32_16x16x32_bf16 v[208:211], v[8:11], v[236:239], v[208:211]\n\t" \
    "v_mfma_f32_16x16x32_bf16 v[200:203], v[244:247], v[236:239], v[200:203]\n\t" \
    "s_waitcnt lgkmcnt(3)\n\t" \
    "v_mfma_f32_16x16x32_bf16 v[216:219], v[0:3], v[240:243], v[216:219]\n\t" \
    "ds_read_b128 v[236:239], v253 offset:26624\n\t" \
    "v_mfma_f32_16x16x32_bf16 v[220:223], v[8:11], v[240:243], v[220:223]\n\t" \
    "v_mfma_f32_16x16x32_bf16 v[212:215], v[244:247], v[240:243], v[212:215]\n\t" \
    "ds_read_b128 v[244:247], v254 offset:43008\n\t" \
    "s_waitcnt lgkmcnt(4)\n\t" \
    "v_mfma_f32_16x16x32_bf16 v[204:207], v[4:7], v[224:227], v[204:207]\n\t" \
    "ds_read_b128 v[240:243], v253 offset:7168\n\t" \
    "v_mfma_f32_16x16x32_bf16 v[208:211], v[12:15], v[224:227], v[208:211]\n\t" \
    "v_mfma_f32_16x16x32_bf16 v[200:203], v[248:251], v[224:227], v[200:203]\n\t" \
    "s_waitcnt lgkmcnt(4)\n\t" \
    "v_mfma_f32_16x16x32_bf16 v[216:219], v[4:7], v[228:231], v[216:219]\n\t" \
    "ds_read_b128 v[224:227], v253 offset:27648\n\t" \
    "v_mfma_f32_16x16x32_bf16 v[220:223], v[12:15], v[228:231], v[220:223]\n\t" \
    "v_mfma_f32_16x16x32_bf16 v[212:215], v[248:251], v[228:231], v[212:215]\n\t" \
    "ds_read_b128 v[248:251], v254 offset:44032\n\t" \
    "s_waitcnt lgkmcnt(5)\n\t" \
    "v_mfma_f32_16x16x32_bf16 v[204:207], v[40:43], v[232:235], v[204:207]\n\t" \
    "ds_read_b128 v[228:231], v253 offset:8192\n\t" \
    "v_mfma_f32_16x16x32_bf16 v[208:211], v[48:51], v[232:235], v[208:211]\n\t" \
    "s_waitcnt lgkmcnt(4)\n\t" \
    "v_mfma_f32_16x16x32_bf16 v[200:203], v[244:247], v[232:235], v[200:203]\n\t" \
    "v_mfma_f32_16x16x32_bf16 v[216:219], v[40:43], v[236:239], v[216:219]\n\t" \
    "ds_read_b128 v[232:235], v253 offset:28672\n\t" \
    "v_mfma_f32_16x16x32_bf16 v[220:223], v[48:51], v[236:239], v[220:223]\n\t" \
    "v_mfma_f32_16x16x32_bf16 v[212:215], v[244:247], v[236:239], v[212:215]\n\t" \
    "ds_read_b128 v[244:247], v254 offset:45056\n\t" \
    "s_waitcnt lgkmcnt(5)\n\t" \
    "v_mfma_f32_16x16x32_bf16 v[204:207], v[44:47], v[240:243], v[204:207]\n\t" \
    "ds_read_b128 v[236:239], v253 offset:9216\n\t" \
    "v_mfma_f32_16x16x32_bf16 v[208:211], v[52:55], v[240:243], v[208:211]\n\t" \
    "s_waitcnt lgkmcnt(4)\n\t" \
    "v_mfma_f32_16x16x32_bf16 v[200:203], v[248:251], v[240:243], v[200:203]\n\t" \
    "v_mfma_f32_16x16x32_bf16 v[216:219], v[44:47], v[224:227], v[216:219]\n\t" \
    "ds_read_b128 v[240:243], v253 offset:29696\n\t" \
    "v_mfma_f32_16x16x32_bf16 v[220:223], v[52:55], v[224:227], v[220:223]\n\t" \
    "v_mfma_f32_16x16x32_bf16 v[212:215], v[248:251], v[224:227], v[212:215]\n\t" \
    "ds_read_b128 v[248:251], v254 offset:46080\n\t" \
    "s_waitcnt lgkmcnt(0)\n\t" \
    "s_barrier \n\t" \
    LST7 \
    "v_mfma_f32_16x16x32_bf16 v[204:207], v[144:147], v[228:231], v[204:207]\n\t" \
    "ds_read_b128 v[224:227], v253 offset:10240\n\t" \
    "v_mfma_f32_16x16x32_bf16 v[208:211], v[152:155], v[228:231], v[208:211]\n\t" \
    "v_mfma_f32_16x16x32_bf16 v[200:203], v[136:139], v[228:231], v[200:203]\n\t" \
    "v_mfma_f32_16x16x32_bf16 v[216:219], v[144:147], v[232:235], v[216:219]\n\t" \
    "ds_read_b128 v[228:231], v253 offset:30720\n\t" \
    "v_mfma_f32_16x16x32_bf16 v[220:223], v[152:155], v[232:235], v[220:223]\n\t" \
    "v_mfma_f32_16x16x32_bf16 v[212:215], v[136:139], v[232:235], v[212:215]\n\t" \
    "v_mfma_f32_16x16x32_bf16 v[204:207], v[148:151], v[236:239], v[204:207]\n\t" \
    "ds_read_b128 v[232:235], v253 offset:11264\n\t" \
    "v_mfma_f32_16x16x32_bf16 v[208:211], v[156:159], v[236:239], v[208:211]\n\t" \
    "v_mfma_f32_16x16x32_bf16 v[200:203], v[140:143], v[236:239], v[200:203]\n\t" \
    "v_mfma_f32_16x16x32_bf16 v[216:219], v[148:151], v[240:243], v[216:219]\n\t" \
    "ds_read_b128 v[236:239], v253 offset:31744\n\t" \
    "v_mfma_f32_16x16x32_bf16 v[220:223], v[156:159], v[240:243], v[220:223]\n\t" \
    "v_mfma_f32_16x16x32_bf16 v[212:215], v[140:143], v[240:243], v[212:215]\n\t" \
    "s_waitcnt lgkmcnt(3)\n\t" \
    "v_mfma_f32_16x16x32_bf16 v[204:207], v[80:83], v[224:227], v[204:207]\n\t" \
    "ds_read_b128 v[240:243], v253 offset:14336\n\t" \
    "s_and_b32 s44, s40, 3\n\t" \
    "v_mfma_f32_16x16x32_bf16 v[208:211], v[88:91], v[224:227], v[208:211]\n\t" \
    "s_lshl_b32 s44, s44, 17\n\t" \
    "v_mfma_f32_16x16x32_bf16 v[200:203], v[244:247], v[224:227], v[200:203]\n\t" \
    "s_add_u32 s44, s44, %[mb]\n\t" \
    "s_waitcnt lgkmcnt(3)\n\t" \
    "v_mfma_f32_16x16x32_bf16 v[216:219], v[80:83], v[228:231], v[216:219]\n\t" \
    "ds_read_b128 v[224:227], v253 offset:34816\n\t" \
    "s_add_u32 s46, s44, 0x800\n\t" \
    "v_mfma_f32_16x16x32_bf16 v[220:223], v[88:91], v[228:231], v[220:223]\n\t" \
    "s_add_u32 s47, s44, 0x1800\n\t" \
    "v_mfma_f32_16x16x32_bf16 v[212:215], v[244:247], v[228:231], v[212:215]\n\t" \
    "ds_read_b128 v[244:247], v254 offset:47104\n\t" \
    "s_add_u32 s48, s44, 0xc00\n\t" \
    "s_waitcnt lgkmcnt(4)\n\t" \
    "v_mfma_f32_16x16x32_bf16 v[204:207], v[84:87], v[232:235], v[204:207]\n\t" \
    "ds_read_b128 v[228:231], v253 offset:15360\n\t" \
    "v_mfma_f32_16x16x32_bf16 v[208:211], v[92:95], v[232:235], v[208:211]\n\t" \
    "v_mfma_f32_16x16x32_bf16 v[200:203], v[248:251], v[232:235], v[200:203]\n\t" \
    "s_waitcnt lgkmcnt(4)\n\t" \
    "v_mfma_f32_16x16x32_bf16 v[216:219], v[84:87], v[236:239], v[216:219]\n\t" \
    "ds_read_b128 v[232:235], v253 offset:35840\n\t" \
    "v_mfma_f32_16x16x32_bf16 v[220:223], v[92:95], v[236:239], v[220:223]\n\t" \
    "v_mfma_f32_16x16x32_bf16 v[212:215], v[248:251], v[236:239], v[212:215]\n\t" \
    "ds_read_b128 v[248:251], v254 offset:48128\n\t" \
    "s_waitcnt lgkmcnt(5)\n\t" \
    "v_mfma_f32_16x16x32_bf16 v[204:207], v[120:123], v[240:243], v[204:207]\n\t" \
    "ds_read_b128 v[236:239], v253 offset:16384\n\t" \
    "v_mfma_f32_16x16x32_bf16 v[208:211], v[128:131], v[240:243], v[208:211]\n\t" \
    "s_waitcnt lgkmcnt(4)\n\t" \
    "v_mfma_f32_16x16x32_bf16 v[200:203], v[244:247], v[240:243], v[200:203]\n\t" \
    "v_mfma_f32_16x16x32_bf16 v[216:219], v[120:123], v[224:227], v[216:219]\n\t" \
    "ds_read_b128 v[240:243], v253 offset:36864\n\t" \
    "v_mfma_f32_16x16x32_bf16 v[220:223], v[128:131], v[224:227], v[220:223]\n\t" \
    "v_mfma_f32_16x16x32_bf16 v[212:215], v[244:247], v[224:227], v[212:215]\n\t" \
    "ds_read_b128 v[244:247], v254 offset:49152\n\t" \
    "s_waitcnt lgkmcnt(5)\n\t" \
    "v_mfma_f32_16x16x32_bf16 v[204:207], v[124:127], v[228:231], v[204:207]\n\t" \
    "ds_read_b128 v[224:227], v253 offset:17408\n\t" \
    "v_mfma_f32_16x16x32_bf16 v[208:211], v[132:135], v[228:231], v[208:211]\n\t" \
    "s_waitcnt lgkmcnt(4)\n\t" \
    "v_mfma_f32_16x16x32_bf16 v[200:203], v[248:251], v[228:231], v[200:203]\n\t" \
    "v_mfma_f32_16x16x32_bf16 v[216:219], v[124:127], v[232:235], v[216:219]\n\t" \
    "ds_read_b128 v[228:231], v253 offset:37888\n\t" \
    "v_mfma_f32_16x16x32_bf16 v[220:223], v[132:135], v[232:235], v[220:223]\n\t" \
    "v_mfma_f32_16x16x32_bf16 v[212:215], v[248:251], v[232:235], v[212:215]\n\t" \
    "ds_read_b128 v[248:251], v254 offset:50176\n\t" \
    "s_waitcnt lgkmcnt(5)\n\t" \
    "v_mfma_f32_16x16x32_bf16 v[204:207], v[160:163], v[236:239], v[204:207]\n\t" \
    "ds_read_b128 v[232:235], v253 offset:12288\n\t" \
    "v_mfma_f32_16x16x32_bf16 v[208:211], v[168:171], v[236:239], v[208:211]\n\t" \
    "s_waitcnt lgkmcnt(4)\n\t" \
    "v_mfma_f32_16x16x32_bf16 v[200:203], v[244:247], v[236:239], v[200:203]\n\t" \
    "v_mfma_f32_16x16x32_bf16 v[216:219], v[160:163], v[240:243], v[216:219]\n\t" \
    "ds_read_b128 v[236:239], v253 offset:13312\n\t" \
    "v_mfma_f32_16x16x32_bf16 v[220:223], v[168:171], v[240:243], v[220:223]\n\t" \
    "v_mfma_f32_16x16x32_bf16 v[212:215], v[244:247], v[240:243], v[212:215]\n\t" \
    "s_waitcnt lgkmcnt(4)\n\t" \
    "v_mfma_f32_16x16x32_bf16 v[204:207], v[164:167], v[224:227], v[204:207]\n\t" \
    "ds_read_b128 v[240:243], v253 offset:18432\n\t" \
    "v_mfma_f32_16x16x32_bf16 v[208:211], v[172:175], v[224:227], v[208:211]\n\t" \
    "s_waitcnt lgkmcnt(3)\n\t" \
    "v_mfma_f32_16x16x32_bf16 v[200:203], v[248:251], v[224:227], v[200:203]\n\t" \
    "v_mfma_f32_16x16x32_bf16 v[216:219], v[164:167], v[228:231], v[216:219]\n\t" \
    "ds_read_b128 v[224:227], v253 offset:19456\n\t" \
    "v_mfma_f32_16x16x32_bf16 v[220:223], v[172:175], v[228:231], v[220:223]\n\t" \
    "v_mfma_f32_16x16x32_bf16 v[212:215], v[248:251], v[228:231], v[212:215]\n\t" \
    "s_waitcnt lgkmcnt(3)\n\t" \
    "v_mfma_f32_16x16x32_bf16 v[204:207], v[24:27], v[232:235], v[204:207]\n\t" \
    "ds_read_b128 v[228:231], v253 offset:32768\n\t" \
    "v_mov_b64 v[244:245], 0\n\t" \
    "v_mfma_f32_16x16x32_bf16 v[208:211], v[32:35], v[232:235], v[208:211]\n\t" \
    "v_mov_b64 v[246:247], 0\n\t" \
    "v_mfma_f32_16x16x32_bf16 v[200:203], v[16:19], v[232:235], v[200:203]\n\t" \
    "v_mov_b64 v[248:249], 0\n\t" \
    "s_waitcnt lgkmcnt(3)\n\t" \
    "v_mfma_f32_16x16x32_bf16 v[204:207], v[28:31], v[236:239], v[204:207]\n\t" \
    "ds_read_b128 v[232:235], v253 offset:33792\n\t" \
    "v_mov_b64 v[250:251], 0\n\t" \
    "v_mfma_f32_16x16x32_bf16 v[208:211], v[36:39], v[236:239], v[208:211]\n\t" \
    "v_mfma_f32_16x16x32_bf16 v[200:203], v[20:23], v[236:239], v[200:203]\n\t" \
    "s_waitcnt lgkmcnt(3)\n\t" \
    "v_mfma_f32_16x16x32_bf16 v[204:207], v[184:187], v[240:243], v[204:207]\n\t" \
    "ds_read_b128 v[236:239], v253 offset:38912\n\t" \
    "v_mfma_f32_16x16x32_bf16 v[208:211], v[192:195], v[240:243], v[208:211]\n\t" \
    "v_mfma_f32_16x16x32_bf16 v[200:203], v[176:179], v[240:243], v[200:203]\n\t" \
    "s_waitcnt lgkmcnt(3)\n\t" \
    "v_mfma_f32_16x16x32_bf16 v[204:207], v[188:191], v[224:227], v[204:207]\n\t" \
    "ds_read_b128 v[240:243], v253 offset:39936\n\t" \
    "v_mfma_f32_16x16x32_bf16 v[208:211], v[196:199], v[224:227], v[208:211]\n\t" \
    "v_mfma_f32_16x16x32_bf16 v[200:203], v[180:183], v[224:227], v[200:203]\n\t" \
    "s_waitcnt lgkmcnt(3)\n\t" \
    "v_mfma_f32_16x16x32_bf16 v[216:219], v[24:27], v[228:231], v[216:219]\n\t" \
    "s_waitcnt lgkmcnt(2)\n\t" \
    "v_mfma_f32_16x16x32_bf16 v[216:219], v[28:31], v[232:235], v[216:219]\n\t" \
    "v_cvt_pk_bf16_f32 v204, v204, v205\n\t" \
    "v_cvt_pk_bf16_f32 v205, v206, v207\n\t" \
    "s_waitcnt lgkmcnt(1)\n\t" \
    "v_mfma_f32_16x16x32_bf16 v[216:219], v[184:187], v[236:239], v[216:219]\n\t" \
    "ds_write_b64 v254, v[204:205] offset:10240\n\t" \
    "v_cvt_pk_bf16_f32 v208, v208, v209\n\t" \
    "s_waitcnt lgkmcnt(1)\n\t" \
    "v_mfma_f32_16x16x32_bf16 v[216:219], v[188:191], v[240:243], v[216:219]\n\t" \
    "v_cvt_pk_bf16_f32 v209, v210, v211\n\t" \
    "ds_write_b64 v254, v[208:209] offset:11264\n\t" \
    "v_mfma_f32_16x16x32_bf16 v[220:223], v[32:35], v[228:231], v[220:223]\n\t" \
    "v_cvt_pk_bf16_f32 v200, v200, v201\n\t" \
    "v_cvt_pk_bf16_f32 v201, v202, v203\n\t" \
    "v_mfma_f32_16x16x32_bf16 v[220:223], v[36:39], v[232:235], v[220:223]\n\t" \
    "ds_write_b64 v254, v[200:201] offset:9216\n\t" \
    "ds_read_b128 v[200:203], v254 offset:40960\n\t" \
    "v_mfma_f32_16x16x32_bf16 v[220:223], v[192:195], v[236:239], v[220:223]\n\t" \
    "ds_read_b128 v[204:207], v254 offset:41984\n\t" \
    "v_mov_b64 v[208:209], 0\n\t" \
    "v_mfma_f32_16x16x32_bf16 v[220:223], v[196:199], v[240:243], v[220:223]\n\t" \
    "v_mov_b64 v[210:211], 0\n\t" \
    "v_cvt_pk_bf16_f32 v216, v216, v217\n\t" \
    "v_mfma_f32_16x16x32_bf16 v[212:215], v[16:19], v[228:231], v[212:215]\n\t" \
    "v_cvt_pk_bf16_f32 v217, v218, v219\n\t" \
    "ds_write_b64 v254, v[216:217] offset:16384\n\t" \
    "v_mfma_f32_16x16x32_bf16 v[212:215], v[20:23], v[232:235], v[212:215]\n\t" \
    "v_mfma_f32_16x16x32_bf16 v[212:215], v[176:179], v[236:239], v[212:215]\n\t" \
    "v_cvt_pk_bf16_f32 v220, v220, v221\n\t" \
    "v_cvt_pk_bf16_f32 v221, v222, v223\n\t" \
    "v_mfma_f32_16x16x32_bf16 v[212:215], v[180:183], v[240:243], v[212:215]\n\t" \
    "ds_write_b64 v254, v[220:221] offset:17408\n\t" \
    "ds_read_b128 v[216:219], v254 offset:43008\n\t" \
    "ds_read_b128 v[220:223], v254 offset:44032\n\t" \
    "s_nop 4\n\t" \
    "v_cvt_pk_bf16_f32 v212, v212, v213\n\t" \
    "v_cvt_pk_bf16_f32 v213, v214, v215\n\t" \
    "ds_write_b64 v254, v[212:213] offset:15360\n\t" \
    LST6 \
    "buffer_load_dwordx4 v[224:227], v253, %[rs], s46 offen sc1\n\t" \
    "buffer_load_dwordx4 v[228:231], v253, %[rs], s47 offen sc1\n\t" \
    "buffer_load_dwordx4 v[232:235], v253, %[rs], s48 offen sc1\n\t" \
    "s_waitcnt lgkmcnt(0)\n\t" \
    "v_bfe_i32 v212, v253, 5, 1\n\t" \
    "v_and_b32 v212, 0xaaaa, v212\n\t" \
    "v_or_b32 v212, 0x4444, v212\n\t" \
    ".Ls12_%=:\n\t" \
    "s_waitcnt vmcnt(2)\n\t" \
    "v_max_u32 v214, v224, v225\n\t" \
    "v_max3_u32 v214, v226, v227, v214\n\t" \
    "s_waitcnt vmcnt(1)\n\t" \
    "v_max3_u32 v214, v228, v229, v214\n\t" \
    "v_max3_u32 v214, v230, v231, v214\n\t" \
    "s_waitcnt vmcnt(0)\n\t" \
    "v_max3_u32 v214, v232, v233, v214\n\t" \
    "v_max3_u32 v214, v234, v235, v214\n\t" \
    "v_cmp_eq_u32 vcc, -1, v214\n\t" \
    "s_cbranch_vccz .Ls13_%=\n\t" \
    "v_max_u32 v214, v224, v225\n\t" \
    "v_max3_u32 v214, v226, v227, v214\n\t" \
    "v_cmp_eq_u32 vcc, -1, v214\n\t" \
    "s_cbranch_vccz .Ls14_%=\n\t" \
    "buffer_load_dwordx4 v[224:227], v253, %[rs], s46 offen sc1\n\t" \
    ".Ls14_%=:\n\t" \
    "v_max_u32 v214, v228, v229\n\t" \
    "v_max3_u32 v214, v230, v231, v214\n\t" \
    "v_cmp_eq_u32 vcc, -1, v214\n\t" \
    "s_cbranch_vccz .Ls15_%=\n\t" \
    "buffer_load_dwordx4 v[228:231], v253, %[rs], s47 offen sc1\n\t" \
    ".Ls15_%=:\n\t" \
    "v_max_u32 v214, v232, v233\n\t" \
    "v_max3_u32 v214, v234, v235, v214\n\t" \
    "v_cmp_eq_u32 vcc, -1, v214\n\t" \
    "s_cbranch_vccz .Ls16_%=\n\t" \
    "buffer_load_dwordx4 v[232:235], v253, %[rs], s48 offen sc1\n\t" \
    ".Ls16_%=:\n\t" \
    "s_cmp_eq_u32 s41, 0\n\t" \
    "s_cbranch_scc1 .Ls13_%=\n\t" \
    "s_sub_u32 s41, s41, 1\n\t" \
    "s_waitcnt vmcnt(0)\n\t" \
    "s_branch .Ls12_%=\n\t" \
    ".Ls13_%=:\n\t" \
    LST2 \
    "ds_write_b128 v253, v[224:227] offset:43008\n\t" \
    "ds_write_b128 v253, v[228:231] offset:47104\n\t" \
    "ds_write_b128 v253, v[232:235] offset:44032\n\t" \
    "v_smfmac_f32_16x16x64_bf16 v[244:247], v[224:227], v[0:7], v212\n\t" \
    "v_smfmac_f32_16x16x64_bf16 v[248:251], v[224:227], v[8:15], v212\n\t" \
    "v_smfmac_f32_16x16x64_bf16 v[208:211], v[224:227], v[200:207], v212\n\t" \
    "s_waitcnt lgkmcnt(0)\n\t" \
    "s_barrier \n\t" \
    LST3 \
    "ds_read_b128 v[236:239], v253 offset:40960\n\t" \
    "ds_read_b128 v[240:243], v253 offset:46080\n\t" \
    "ds_read_b128 v[224:227], v253 offset:41984\n\t" \
    "ds_read_b128 v[200:203], v254 offset:45056\n\t" \
    "ds_read_b128 v[204:207], v254 offset:46080\n\t" \
    "v_smfmac_f32_16x16x64_bf16 v[208:211], v[228:231], v[16:23], v212\n\t" \
    "v_smfmac_f32_16x16x64_bf16 v[244:247], v[228:231], v[24:31], v212\n\t" \
    "v_smfmac_f32_16x16x64_bf16 v[248:251], v[228:231], v[32:39], v212\n\t" \
    "ds_read_b128 v[228:231], v253 offset:48128\n\t" \
    "v_smfmac_f32_16x16x64_bf16 v[208:211], v[232:235], v[216:223], v212\n\t" \
    "ds_read_b128 v[216:219], v254 offset:47104\n\t" \
    "ds_read_b128 v[220:223], v254 offset:48128\n\t" \
    "v_smfmac_f32_16x16x64_bf16 v[244:247], v[232:235], v[40:47], v212\n\t" \
    "v_smfmac_f32_16x16x64_bf16 v[248:251], v[232:235], v[48:55], v212\n\t" \
    "ds_read_b128 v[232:235], v253 offset:45056\n\t" \
    "s_waitcnt lgkmcnt(8)\n\t" \
    "v_smfmac_f32_16x16x64_bf16 v[208:211], v[236:239], v[56:63], v212\n\t" \
    "v_smfmac_f32_16x16x64_bf16 v[244:247], v[236:239], v[64:71], v212\n\t" \
    "v_smfmac_f32_16x16x64_bf16 v[248:251], v[236:239], v[72:79], v212\n\t" \
    "ds_read_b128 v[236:239], v253 offset:49152\n\t" \
    "s_waitcnt lgkmcnt(5)\n\t" \
    "v_smfmac_f32_16x16x64_bf16 v[208:211], v[240:243], v[200:207], v212\n\t" \
    "ds_read_b128 v[200:203], v254 offset:49152\n\t" \
    "ds_read_b128 v[204:207], v254 offset:50176\n\t" \
    "v_smfmac_f32_16x16x64_bf16 v[244:247], v[240:243], v[80:87], v212\n\t" \
    "v_smfmac_f32_16x16x64_bf16 v[248:251], v[240:243], v[88:95], v212\n\t" \
    "ds_read_b128 v[240:243], v253 offset:50176\n\t" \
    "v_smfmac_f32_16x16x64_bf16 v[244:247], v[224:227], v[104:111], v212\n\t" \
    "s_waitcnt lgkmcnt(7)\n\t" \
    "v_smfmac_f32_16x16x64_bf16 v[244:247], v[228:231], v[120:127], v212\n\t" \
    "s_waitcnt lgkmcnt(4)\n\t" \
    "v_smfmac_f32_16x16x64_bf16 v[244:247], v[232:235], v[144:151], v212\n\t" \
    "s_waitcnt lgkmcnt(3)\n\t" \
    "v_smfmac_f32_16x16x64_bf16 v[244:247], v[236:239], v[160:167], v212\n\t" \
    "s_waitcnt lgkmcnt(0)\n\t" \
    "v_smfmac_f32_16x16x64_bf16 v[244:247], v[240:243], v[184:191], v212\n\t" \
    "v_smfmac_f32_16x16x64_bf16 v[248:251], v[224:227], v[112:119], v212\n\t" \
    "v_smfmac_f32_16x16x64_bf16 v[248:251], v[228:231], v[128:135], v212\n\t" \
    "v_smfmac_f32_16x16x64_bf16 v[248:251], v[232:235], v[152:159], v212\n\t" \
    "v_add_f32 v244, v244, v246\n\t" \
    "v_add_f32 v245, v245, v247\n\t" \
    "v_smfmac_f32_16x16x64_bf16 v[248:251], v[236:239], v[168:175], v212\n\t" \
    "v_cvt_pk_bf16_f32 v244, v244, v245\n\t" \
    "ds_write_b16 v255, v244 offset:512\n\t" \
    "v_smfmac_f32_16x16x64_bf16 v[248:251], v[240:243], v[192:199], v212\n\t" \
    "ds_write_b16_d16_hi v255, v244 offset:528\n\t" \
    "v_smfmac_f32_16x16x64_bf16 v[208:211], v[224:227], v[96:103], v212\n\t" \
    "v_smfmac_f32_16x16x64_bf16 v[208:211], v[228:231], v[216:223], v212\n\t" \
    "v_smfmac_f32_16x16x64_bf16 v[208:211], v[232:235], v[136:143], v212\n\t" \
    "v_add_f32 v248, v248, v250\n\t" \
    "v_add_f32 v249, v249, v251\n\t" \
    "v_smfmac_f32_16x16x64_bf16 v[208:211], v[236:239], v[200:207], v212\n\t" \
    "v_cvt_pk_bf16_f32 v248, v248, v249\n\t" \
    "ds_write_b16 v255, v248 offset:1024\n\t" \
    "v_smfmac_f32_16x16x64_bf16 v[208:211], v[240:243], v[176:183], v212\n\t" \
    "ds_write_b16_d16_hi v255, v248 offset:1040\n\t" \
    "s_nop 6\n\t" \
    "v_add_f32 v208, v208, v210\n\t" \
    "v_add_f32 v209, v209, v211\n\t" \
    "v_cvt_pk_bf16_f32 v208, v208, v209\n\t" \
    "ds_write_b16 v255, v208 offset:0\n\t" \
    "ds_write_b16_d16_hi v255, v208 offset:16\n\t" \
    "ds_read_b128 v[200:203], v254 offset:6144\n\t" \
    "ds_read_b128 v[204:207], v254 offset:7168\n\t" \
    "ds_read_b128 v[208:211], v254 offset:8192\n\t" \
    "ds_read_b128 v[212:215], v254 offset:9216\n\t" \
    "ds_read_b128 v[216:219], v254 offset:10240\n\t" \
    "ds_read_b128 v[220:223], v254 offset:11264\n\t" \
    "ds_read_b128 v[224:227], v254 offset:18432\n\t" \
    "ds_read_b128 v[228:231], v254 offset:19456\n\t" \
    "ds_read_b128 v[232:235], v254 offset:20480\n\t" \
    "ds_read_b128 v[236:239], v254 offset:21504\n\t" \
    "s_and_b32 s44, s40, 3\n\t" \
    "s_lshl_b32 s44, s44, 17\n\t" \
    "s_add_u32 s50, s44, %[pb]\n\t" \
    "s_add_u32 s51, s44, %[pm]\n\t" \
    "s_mov_b32 s52, 0xf0000\n\t" \
    "s_mov_b32 s53, 0xfff00ff\n\t" \
    "s_mov_b32 s54, 0x1f0001\n\t" \
    "s_mov_b32 s55, 0x1fff01ff\n\t" \
    "s_mov_b32 s56, 0x3f0003\n\t" \
    "s_mov_b32 s57, 0x3fff03ff\n\t" \
    "s_mov_b32 s58, 0x7f0007\n\t" \
    "s_mov_b32 s59, 0x7fff07ff\n\t" \
    "s_waitcnt lgkmcnt(0)\n\t" \
    "s_barrier \n\t" \
    LST4 \
    "v_and_b32 v252, 0xffffff7f, v254\n\t" \
    "v_mfma_f32_16x16x32_bf16 v[240:243], v[224:227], v[200:203], 0\n\t" \
    "ds_read_b128 v[224:227], v254 offset:22528\n\t" \
    "v_mfma_f32_16x16x32_bf16 v[240:243], v[228:231], v[204:207], v[240:243]\n\t" \
    "ds_read_b128 v[228:231], v254 offset:23552\n\t" \
    "v_mfma_f32_16x16x32_bf16 v[240:243], v[232:235], v[208:211], v[240:243]\n\t" \
    "ds_read_b128 v[232:235], v252 offset:3072\n\t" \
    "v_mfma_f32_16x16x32_bf16 v[240:243], v[236:239], v[212:215], v[240:243]\n\t" \
    "ds_read_b128 v[236:239], v252 offset:4096\n\t" \
    "s_waitcnt lgkmcnt(3)\n\t" \
    "v_mfma_f32_16x16x32_bf16 v[240:243], v[224:227], v[216:219], v[240:243]\n\t" \
    "ds_read_b128 v[224:227], v252 offset:5120\n\t" \
    "s_waitcnt lgkmcnt(3)\n\t" \
    "v_mfma_f32_16x16x32_bf16 v[240:243], v[228:231], v[220:223], v[240:243]\n\t" \
    "ds_read_b128 v[228:231], v252 offset:3200\n\t" \
    "s_waitcnt lgkmcnt(3)\n\t" \
    "v_mfma_f32_16x16x32_bf16 v[248:251], v[232:235], v[200:203], 0\n\t" \
    "ds_read_b128 v[232:235], v252 offset:4224\n\t" \
    "s_waitcnt lgkmcnt(3)\n\t" \
    "v_mfma_f32_16x16x32_bf16 v[248:251], v[236:239], v[204:207], v[248:251]\n\t" \
    "ds_read_b128 v[236:239], v252 offset:5248\n\t" \
    "s_waitcnt lgkmcnt(3)\n\t" \
    "v_mfma_f32_16x16x32_bf16 v[248:251], v[224:227], v[208:211], v[248:251]\n\t" \
    "s_waitcnt lgkmcnt(2)\n\t" \
    "v_mfma_f32_16x16x32_bf16 v[248:251], v[228:231], v[212:215], v[248:251]\n\t" \
    "s_waitcnt lgkmcnt(1)\n\t" \
    "v_mfma_f32_16x16x32_bf16 v[248:251], v[232:235], v[216:219], v[248:251]\n\t" \
    "s_waitcnt lgkmcnt(0)\n\t" \
    "v_mfma_f32_16x16x32_bf16 v[248:251], v[236:239], v[220:223], v[248:251]\n\t" \
    "v_mov_b32 v226, 0xff7fffff\n\t" \
    "v_cndmask_b32 v240, v240, v226, s[52:53]\n\t" \
    "v_cndmask_b32 v241, v241, v226, s[54:55]\n\t" \
    "v_cndmask_b32 v242, v242, v226, s[56:57]\n\t" \
    "v_cndmask_b32 v243, v243, v226, s[58:59]\n\t" \
    "v_max3_f32 v224, v240, v241, v242\n\t" \
    "v_mov_b32 v225, v243\n\t" \
    "v_lshrrev_b32 v200, 5, v253\n\t" \
    "v_and_b32 v201, 0xf0, v253\n\t" \
    "v_and_or_b32 v200, v200, 8, v201\n\t" \
    "v_max3_f32 v224, v224, v248, v249\n\t" \
    "v_max3_f32 v225, v225, v250, v251\n\t" \
    "v_max_f32 v224, v224, v225\n\t" \
    "v_mov_b32 v225, v224\n\t" \
    "s_nop 1\n\t" \
    "v_permlane16_swap_b32 v224, v225\n\t" \
    "v_max_f32 v224, v224, v225\n\t" \
    "v_mov_b32 v225, v224\n\t" \
    "s_nop 1\n\t" \
    "v_permlane32_swap_b32 v224, v225\n\t" \
    "v_max_f32 v224, v224, v225\n\t" \
    "v_mul_f32 v224, %[scl], v224\n\t" \
    "v_fma_f32 v240, v240, %[scl], -v224\n\t" \
    "v_fma_f32 v241, v241, %[scl], -v224\n\t" \
    "v_exp_f32 v240, v240\n\t" \
    "v_fma_f32 v242, v242, %[scl], -v224\n\t" \
    "v_exp_f32 v241, v241\n\t" \
    "v_fma_f32 v243, v243, %[scl], -v224\n\t" \
    "v_exp_f32 v242, v242\n\t" \
    "v_fma_f32 v248, v248, %[scl], -v224\n\t" \
    "v_exp_f32 v243, v243\n\t" \
    "v_fma_f32 v249, v249, %[scl], -v224\n\t" \
    "v_exp_f32 v248, v248\n\t" \
    "v_fma_f32 v250, v250, %[scl], -v224\n\t" \
    "v_exp_f32 v249, v249\n\t" \
    "v_fma_f32 v251, v251, %[scl], -v224\n\t" \
    "v_exp_f32 v250, v250\n\t" \
    "v_exp_f32 v251, v251\n\t" \
    "v_add_f32 v224, v240, v241\n\t" \
    "v_add_f32 v225, v242, v243\n\t" \
    "v_add_f32 v232, v248, v249\n\t" \
    "v_add_f32 v233, v250, v251\n\t" \
    "v_add_f32 v224, v224, v225\n\t" \
    "v_add_f32 v232, v232, v233\n\t" \
    "v_fma_f32 v224, v232, 0.5, v224\n\t" \
    "v_mov_b32 v225, v224\n\t" \
    "s_nop 1\n\t" \
    "v_permlane16_swap_b32 v224, v225\n\t" \
    "v_add_f32 v224, v224, v225\n\t" \
    "v_mov_b32 v225, v224\n\t" \
    "s_nop 1\n\t" \
    "v_permlane32_swap_b32 v224, v225\n\t" \
    "v_add_f32 v224, v224, v225\n\t" \
    "v_rcp_f32 v224, v224\n\t" \
    "s_nop 0\n\t" \
    "v_mul_f32 v240, v240, v224\n\t" \
    "v_mul_f32 v241, v241, v224\n\t" \
    "v_mul_f32 v242, v242, v224\n\t" \
    "v_mul_f32 v243, v243, v224\n\t" \
    "v_mul_f32 v248, v248, v224\n\t" \
    "v_mul_f32 v249, v249, v224\n\t" \
    "v_mul_f32 v250, v250, v224\n\t" \
    "v_mul_f32 v251, v251, v224\n\t" \
    "v_cvt_pk_bf16_f32 v204, |v240|, |v241|\n\t" \
    "v_cvt_pk_bf16_f32 v205, |v242|, |v243|\n\t" \
    "v_cvt_pk_bf16_f32 v208, |v248|, |v249|\n\t" \
    "v_cvt_pk_bf16_f32 v209, |v250|, |v251|\n\t" \
    "v_permlane16_swap_b32 v204, v206\n\t" \
    "v_permlane16_swap_b32 v205, v207\n\t" \
    "s_mov_b32 exec_lo, 0xffff\n\t" \
    "s_mov_b32 exec_hi, 0xffff\n\t" \
    "buffer_store_dwordx4 v[204:207], v253, %[rs], s50 offen sc1\n\t" \
    "s_mov_b32 exec_lo, -1\n\t" \
    "s_mov_b32 exec_hi, 0\n\t" \
    "buffer_store_dwordx2 v[208:209], v200, %[rs], s51 offen sc1\n\t" \
    "s_mov_b32 exec_hi, -1\n\t" \
    LST5 \
    LFLUSH212 \
    "ds_read_b128 v[224:227], v253 offset:0\n\t" \
    "ds_read_b128 v[244:247], v254 offset:40960\n\t" \
    "ds_read_b128 v[228:231], v253 offset:20480\n\t" \
    "ds_read_b128 v[232:235], v253 offset:1024\n\t" \
    "ds_read_b128 v[236:239], v253 offset:21504\n\t" \
    "ds_read_b128 v[248:251], v254 offset:41984\n\t" \
    "s_add_u32 s40, s40, 1\n\t" \
    "s_cmp_lg_u32 s40, 64\n\t" \
    "s_cbranch_scc1 .Ls11_%=\n\t" \
    "s_branch .Lend%=\n\t" \
    ".Ls0_%=:\n\t" \
    "ds_read_b128 v[224:227], v253 offset:0\n\t" \
    "ds_read_b128 v[228:231], v253 offset:20480\n\t" \
    "ds_read_b128 v[232:235], v253 offset:1024\n\t" \
    "ds_read_b128 v[236:239], v253 offset:21504\n\t" \
    ".Ls01_%=:\n\t" \
    "s_waitcnt lgkmcnt(3)\n\t" \
    "v_mfma_f32_16x16x32_bf16 v[204:207], v[0:3], v[224:227], 0\n\t" \
    "ds_read_b128 v[240:243], v253 offset:2048\n\t" \
    "v_mfma_f32_16x16x32_bf16 v[208:211], v[8:11], v[224:227], 0\n\t" \
    "s_waitcnt lgkmcnt(3)\n\t" \
    "v_mfma_f32_16x16x32_bf16 v[216:219], v[0:3], v[228:231], 0\n\t" \
    "ds_read_b128 v[224:227], v253 offset:22528\n\t" \
    "v_mfma_f32_16x16x32_bf16 v[220:223], v[8:11], v[228:231], 0\n\t" \
    "s_waitcnt lgkmcnt(3)\n\t" \
    "v_mfma_f32_16x16x32_bf16 v[204:207], v[4:7], v[232:235], v[204:207]\n\t" \
    "ds_read_b128 v[228:231], v253 offset:3072\n\t" \
    "v_mfma_f32_16x16x32_bf16 v[208:211], v[12:15], v[232:235], v[208:211]\n\t" \
    "s_waitcnt lgkmcnt(3)\n\t" \
    "v_mfma_f32_16x16x32_bf16 v[216:219], v[4:7], v[236:239], v[216:219]\n\t" \
    "ds_read_b128 v[232:235], v253 offset:23552\n\t" \
    "v_mfma_f32_16x16x32_bf16 v[220:223], v[12:15], v[236:239], v[220:223]\n\t" \
    "s_waitcnt lgkmcnt(3)\n\t" \
    "v_mfma_f32_16x16x32_bf16 v[204:207], v[40:43], v[240:243], v[204:207]\n\t" \
    "ds_read_b128 v[236:239], v253 offset:4096\n\t" \
    "v_mfma_f32_16x16x32_bf16 v[208:211], v[48:51], v[240:243], v[208:211]\n\t" \
    "s_waitcnt lgkmcnt(3)\n\t" \
    "v_mfma_f32_16x16x32_bf16 v[216:219], v[40:43], v[224:227], v[216:219]\n\t" \
    "ds_read_b128 v[240:243], v253 offset:24576\n\t" \
    "v_mfma_f32_16x16x32_bf16 v[220:223], v[48:51], v[224:227], v[220:223]\n\t" \
    "s_waitcnt lgkmcnt(3)\n\t" \
    "v_mfma_f32_16x16x32_bf16 v[204:207], v[44:47], v[228:231], v[204:207]\n\t" \
    "ds_read_b128 v[224:227], v253 offset:5120\n\t" \
    "v_mfma_f32_16x16x32_bf16 v[208:211], v[52:55], v[228:231], v[208:211]\n\t" \
    "s_waitcnt lgkmcnt(3)\n\t" \
    "v_mfma_f32_16x16x32_bf16 v[216:219], v[44:47], v[232:235], v[216:219]\n\t" \
    "ds_read_b128 v[228:231], v253 offset:25600\n\t" \
    "v_mfma_f32_16x16x32_bf16 v[220:223], v[52:55], v[232:235], v[220:223]\n\t" \
    "s_waitcnt lgkmcnt(3)\n\t" \
    "v_mfma_f32_16x16x32_bf16 v[204:207], v[80:83], v[236:239], v[204:207]\n\t" \
    "ds_read_b128 v[232:235], v253 offset:6144\n\t" \
    "v_mfma_f32_16x16x32_bf16 v[208:211], v[88:91], v[236:239], v[208:211]\n\t" \
    "s_waitcnt lgkmcnt(3)\n\t" \
    "v_mfma_f32_16x16x32_bf16 v[216:219], v[80:83], v[240:243], v[216:219]\n\t" \
    "ds_read_b128 v[236:239], v253 offset:26624\n\t" \
    "v_mfma_f32_16x16x32_bf16 v[220:223], v[88:91], v[240:243], v[220:223]\n\t" \
    "s_waitcnt lgkmcnt(3)\n\t" \
    "v_mfma_f32_16x16x32_bf16 v[204:207], v[84:87], v[224:227], v[204:207]\n\t" \
    "ds_read_b128 v[240:243], v253 offset:7168\n\t" \
    "v_mfma_f32_16x16x32_bf16 v[208:211], v[92:95], v[224:227], v[208:211]\n\t" \
    "s_waitcnt lgkmcnt(3)\n\t" \
    "v_mfma_f32_16x16x32_bf16 v[216:219], v[84:87], v[228:231], v[216:219]\n\t" \
    "ds_read_b128 v[224:227], v253 offset:27648\n\t" \
    "v_mfma_f32_16x16x32_bf16 v[220:223], v[92:95], v[228:231], v[220:223]\n\t" \
    "s_waitcnt lgkmcnt(3)\n\t" \
    "v_mfma_f32_16x16x32_bf16 v[204:207], v[120:123], v[232:235], v[204:207]\n\t" \
    "ds_read_b128 v[228:231], v253 offset:8192\n\t" \
    "v_mfma_f32_16x16x32_bf16 v[208:211], v[128:131], v[232:235], v[208:211]\n\t" \
    "s_waitcnt lgkmcnt(3)\n\t" \
    "v_mfma_f32_16x16x32_bf16 v[216:219], v[120:123], v[236:239], v[216:219]\n\t" \
    "ds_read_b128 v[232:235], v253 offset:28672\n\t" \
    "v_mfma_f32_16x16x32_bf16 v[220:223], v[128:131], v[236:239], v[220:223]\n\t" \
    "s_waitcnt lgkmcnt(3)\n\t" \
    "v_mfma_f32_16x16x32_bf16 v[204:207], v[124:127], v[240:243], v[204:207]\n\t" \
    "ds_read_b128 v[236:239], v253 offset:9216\n\t" \
    "v_mfma_f32_16x16x32_bf16 v[208:211], v[132:135], v[240:243], v[208:211]\n\t" \
    "s_waitcnt lgkmcnt(3)\n\t" \
    "v_mfma_f32_16x16x32_bf16 v[216:219], v[124:127], v[224:227], v[216:219]\n\t" \
    "ds_read_b128 v[240:243], v253 offset:29696\n\t" \
    "v_mfma_f32_16x16x32_bf16 v[220:223], v[132:135], v[224:227], v[220:223]\n\t" \
    "s_waitcnt lgkmcnt(0)\n\t" \
    "s_barrier \n\t" \
    LST7 \
    "ds_read_b128 v[200:203], v253 offset:40960\n\t" \
    "ds_read_b128 v[212:215], v253 offset:41984\n\t" \
    "v_mfma_f32_16x16x32_bf16 v[204:207], v[160:163], v[228:231], v[204:207]\n\t" \
    "ds_read_b128 v[224:227], v253 offset:10240\n\t" \
    "v_mfma_f32_16x16x32_bf16 v[208:211], v[168:171], v[228:231], v[208:211]\n\t" \
    "v_mfma_f32_16x16x32_bf16 v[216:219], v[160:163], v[232:235], v[216:219]\n\t" \
    "ds_read_b128 v[228:231], v253 offset:30720\n\t" \
    "v_mfma_f32_16x16x32_bf16 v[220:223], v[168:171], v[232:235], v[220:223]\n\t" \
    "v_mfma_f32_16x16x32_bf16 v[204:207], v[164:167], v[236:239], v[204:207]\n\t" \
    "ds_read_b128 v[232:235], v253 offset:11264\n\t" \
    "v_mfma_f32_16x16x32_bf16 v[208:211], v[172:175], v[236:239], v[208:211]\n\t" \
    "v_mfma_f32_16x16x32_bf16 v[216:219], v[164:167], v[240:243], v[216:219]\n\t" \
    "ds_read_b128 v[236:239], v253 offset:31744\n\t" \
    "v_mfma_f32_16x16x32_bf16 v[220:223], v[172:175], v[240:243], v[220:223]\n\t" \
    "s_waitcnt lgkmcnt(3)\n\t" \
    "v_mfma_f32_16x16x32_bf16 v[204:207], v[24:27], v[224:227], v[204:207]\n\t" \
    "ds_read_b128 v[240:243], v253 offset:12288\n\t" \
    "s_and_b32 s44, s40, 3\n\t" \
    "v_mfma_f32_16x16x32_bf16 v[208:211], v[32:35], v[224:227], v[208:211]\n\t" \
    "s_lshl_b32 s44, s44, 17\n\t" \
    "v_mfma_f32_16x16x32_bf16 v[200:203], v[16:19], v[224:227], v[200:203]\n\t" \
    "s_add_u32 s44, s44, %[mb]\n\t" \
    "s_waitcnt lgkmcnt(3)\n\t" \
    "v_mfma_f32_16x16x32_bf16 v[216:219], v[24:27], v[228:231], v[216:219]\n\t" \
    "ds_read_b128 v[224:227], v253 offset:32768\n\t" \
    "s_add_u32 s46, s44, 0x0\n\t" \
    "v_mfma_f32_16x16x32_bf16 v[220:223], v[32:35], v[228:231], v[220:223]\n\t" \
    "s_add_u32 s47, s44, 0x1400\n\t" \
    "v_mfma_f32_16x16x32_bf16 v[212:215], v[16:19], v[228:231], v[212:215]\n\t" \
    "s_add_u32 s48, s44, 0x400\n\t" \
    "s_waitcnt lgkmcnt(3)\n\t" \
    "v_mfma_f32_16x16x32_bf16 v[204:207], v[28:31], v[232:235], v[204:207]\n\t" \
    "ds_read_b128 v[228:231], v253 offset:13312\n\t" \
    "v_mfma_f32_16x16x32_bf16 v[208:211], v[36:39], v[232:235], v[208:211]\n\t" \
    "v_mfma_f32_16x16x32_bf16 v[200:203], v[20:23], v[232:235], v[200:203]\n\t" \
    "s_waitcnt lgkmcnt(3)\n\t" \
    "v_mfma_f32_16x16x32_bf16 v[216:219], v[28:31], v[236:239], v[216:219]\n\t" \
    "ds_read_b128 v[232:235], v253 offset:33792\n\t" \
    "v_mfma_f32_16x16x32_bf16 v[220:223], v[36:39], v[236:239], v[220:223]\n\t" \
    "v_mfma_f32_16x16x32_bf16 v[212:215], v[20:23], v[236:239], v[212:215]\n\t" \
    "s_waitcnt lgkmcnt(3)\n\t" \
    "v_mfma_f32_16x16x32_bf16 v[204:207], v[64:67], v[240:243], v[204:207]\n\t" \
    "ds_read_b128 v[236:239], v253 offset:14336\n\t" \
    "v_mfma_f32_16x16x32_bf16 v[208:211], v[72:75], v[240:243], v[208:211]\n\t" \
    "v_mfma_f32_16x16x32_bf16 v[200:203], v[56:59], v[240:243], v[200:203]\n\t" \
    "s_waitcnt lgkmcnt(3)\n\t" \
    "v_mfma_f32_16x16x32_bf16 v[216:219], v[64:67], v[224:227], v[216:219]\n\t" \
    "ds_read_b128 v[240:243], v253 offset:34816\n\t" \
    "v_mfma_f32_16x16x32_bf16 v[220:223], v[72:75], v[224:227], v[220:223]\n\t" \
    "v_mfma_f32_16x16x32_bf16 v[212:215], v[56:59], v[224:227], v[212:215]\n\t" \
    "s_waitcnt lgkmcnt(3)\n\t" \
    "v_mfma_f32_16x16x32_bf16 v[204:207], v[68:71], v[228:231], v[204:207]\n\t" \
    "ds_read_b128 v[224:227], v253 offset:15360\n\t" \
    "v_mfma_f32_16x16x32_bf16 v[208:211], v[76:79], v[228:231], v[208:211]\n\t" \
    "v_mfma_f32_16x16x32_bf16 v[200:203], v[60:63], v[228:231], v[200:203]\n\t" \
    "s_waitcnt lgkmcnt(3)\n\t" \
    "v_mfma_f32_16x16x32_bf16 v[216:219], v[68:71], v[232:235], v[216:219]\n\t" \
    "ds_read_b128 v[228:231], v253 offset:35840\n\t" \
    "v_mfma_f32_16x16x32_bf16 v[220:223], v[76:79], v[232:235], v[220:223]\n\t" \
    "v_mfma_f32_16x16x32_bf16 v[212:215], v[60:63], v[232:235], v[212:215]\n\t" \
    "s_waitcnt lgkmcnt(3)\n\t" \
    "v_mfma_f32_16x16x32_bf16 v[204:207], v[104:107], v[236:239], v[204:207]\n\t" \
    "ds_read_b128 v[232:235], v253 offset:16384\n\t" \
    "v_mfma_f32_16x16x32_bf16 v[208:211], v[112:115], v[236:239], v[208:211]\n\t" \
    "v_mfma_f32_16x16x32_bf16 v[200:203], v[96:99], v[236:239], v[200:203]\n\t" \
    "s_waitcnt lgkmcnt(3)\n\t" \
    "v_mfma_f32_16x16x32_bf16 v[216:219], v[104:107], v[240:243], v[216:219]\n\t" \
    "ds_read_b128 v[236:239], v253 offset:17408\n\t" \
    "v_mfma_f32_16x16x32_bf16 v[220:223], v[112:115], v[240:243], v[220:223]\n\t" \
    "v_mfma_f32_16x16x32_bf16 v[212:215], v[96:99], v[240:243], v[212:215]\n\t" \
    "s_waitcnt lgkmcnt(3)\n\t" \
    "v_mfma_f32_16x16x32_bf16 v[204:207], v[108:111], v[224:227], v[204:207]\n\t" \
    "ds_read_b128 v[240:243], v253 offset:18432\n\t" \
    "v_mfma_f32_16x16x32_bf16 v[208:211], v[116:119], v[224:227], v[208:211]\n\t" \
    "v_mfma_f32_16x16x32_bf16 v[200:203], v[100:103], v[224:227], v[200:203]\n\t" \
    "s_waitcnt lgkmcnt(3)\n\t" \
    "v_mfma_f32_16x16x32_bf16 v[216:219], v[108:111], v[228:231], v[216:219]\n\t" \
    "ds_read_b128 v[224:227], v253 offset:19456\n\t" \
    "v_mfma_f32_16x16x32_bf16 v[220:223], v[116:119], v[228:231], v[220:223]\n\t" \
    "v_mfma_f32_16x16x32_bf16 v[212:215], v[100:103], v[228:231], v[212:215]\n\t" \
    "s_waitcnt lgkmcnt(3)\n\t" \
    "v_mfma_f32_16x16x32_bf16 v[204:207], v[144:147], v[232:235], v[204:207]\n\t" \
    "ds_read_b128 v[228:231], v253 offset:36864\n\t" \
    "v_mov_b64 v[244:245], 0\n\t" \
    "v_mfma_f32_16x16x32_bf16 v[208:211], v[152:155], v[232:235], v[208:211]\n\t" \
    "v_mov_b64 v[246:247], 0\n\t" \
    "v_mfma_f32_16x16x32_bf16 v[200:203], v[136:139], v[232:235], v[200:203]\n\t" \
    "v_mov_b64 v[248:249], 0\n\t" \
    "s_waitcnt lgkmcnt(3)\n\t" \
    "v_mfma_f32_16x16x32_bf16 v[204:207], v[148:151], v[236:239], v[204:207]\n\t" \
    "ds_read_b128 v[232:235], v253 offset:37888\n\t" \
    "v_mov_b64 v[250:251], 0\n\t" \
    "v_mfma_f32_16x16x32_bf16 v[208:211], v[156:159], v[236:239], v[208:211]\n\t" \
    "v_mfma_f32_16x16x32_bf16 v[200:203], v[140:143], v[236:239], v[200:203]\n\t" \
    "s_waitcnt lgkmcnt(3)\n\t" \
    "v_mfma_f32_16x16x32_bf16 v[204:207], v[184:187], v[240:243], v[204:207]\n\t" \
    "ds_read_b128 v[236:239], v253 offset:38912\n\t" \
    "v_mfma_f32_16x16x32_bf16 v[208:211], v[192:195], v[240:243], v[208:211]\n\t" \
    "v_mfma_f32_16x16x32_bf16 v[200:203], v[176:179], v[240:243], v[200:203]\n\t" \
    "s_waitcnt lgkmcnt(3)\n\t" \
    "v_mfma_f32_16x16x32_bf16 v[204:207], v[188:191], v[224:227], v[204:207]\n\t" \
    "ds_read_b128 v[240:243], v253 offset:39936\n\t" \
    "v_mfma_f32_16x16x32_bf16 v[208:211], v[196:199], v[224:227], v[208:211]\n\t" \
    "v_mfma_f32_16x16x32_bf16 v[200:203], v[180:183], v[224:227], v[200:203]\n\t" \
    "s_waitcnt lgkmcnt(3)\n\t" \
    "v_mfma_f32_16x16x32_bf16 v[216:219], v[144:147], v[228:231], v[216:219]\n\t" \
    "s_waitcnt lgkmcnt(2)\n\t" \
    "v_mfma_f32_16x16x32_bf16 v[216:219], v[148:151], v[232:235], v[216:219]\n\t" \
    "v_cvt_pk_bf16_f32 v204, v204, v205\n\t" \
    "v_cvt_pk_bf16_f32 v205, v206, v207\n\t" \
    "s_waitcnt lgkmcnt(1)\n\t" \
    "v_mfma_f32_16x16x32_bf16 v[216:219], v[184:187], v[236:239], v[216:219]\n\t" \
    "ds_write_b64 v254, v[204:205] offset:7168\n\t" \
    "v_cvt_pk_bf16_f32 v208, v208, v209\n\t" \
    "s_waitcnt lgkmcnt(1)\n\t" \
    "v_mfma_f32_16x16x32_bf16 v[216:219], v[188:191], v[240:243], v[216:219]\n\t" \
    "v_cvt_pk_bf16_f32 v209, v210, v211\n\t" \
    "ds_write_b64 v254, v[208:209] offset:8192\n\t" \
    "v_mfma_f32_16x16x32_bf16 v[220:223], v[152:155], v[228:231], v[220:223]\n\t" \
    "v_cvt_pk_bf16_f32 v200, v200, v201\n\t" \
    "v_cvt_pk_bf16_f32 v201, v202, v203\n\t" \
    "v_mfma_f32_16x16x32_bf16 v[220:223], v[156:159], v[232:235], v[220:223]\n\t" \
    "ds_write_b64 v254, v[200:201] offset:6144\n\t" \
    "ds_read_b128 v[200:203], v254 offset:30720\n\t" \
    "v_mfma_f32_16x16x32_bf16 v[220:223], v[192:195], v[236:239], v[220:223]\n\t" \
    "ds_read_b128 v[204:207], v254 offset:31744\n\t" \
    "v_mov_b64 v[208:209], 0\n\t" \
    "v_mfma_f32_16x16x32_bf16 v[220:223], v[196:199], v[240:243], v[220:223]\n\t" \
    "v_mov_b64 v[210:211], 0\n\t" \
    "v_cvt_pk_bf16_f32 v216, v216, v217\n\t" \
    "v_mfma_f32_16x16x32_bf16 v[212:215], v[136:139], v[228:231], v[212:215]\n\t" \
    "v_cvt_pk_bf16_f32 v217, v218, v219\n\t" \
    "ds_write_b64 v254, v[216:217] offset:13312\n\t" \
    "v_mfma_f32_16x16x32_bf16 v[212:215], v[140:143], v[232:235], v[212:215]\n\t" \
    "v_mfma_f32_16x16x32_bf16 v[212:215], v[176:179], v[236:239], v[212:215]\n\t" \
    "v_cvt_pk_bf16_f32 v220, v220, v221\n\t" \
    "v_cvt_pk_bf16_f32 v221, v222, v223\n\t" \
    "v_mfma_f32_16x16x32_bf16 v[212:215], v[180:183], v[240:243], v[212:215]\n\t" \
    "ds_write_b64 v254, v[220:221] offset:14336\n\t" \
    "ds_read_b128 v[216:219], v254 offset:32768\n\t" \
    "ds_read_b128 v[220:223], v254 offset:33792\n\t" \
    "s_nop 4\n\t" \
    "v_cvt_pk_bf16_f32 v212, v212, v213\n\t" \
    "v_cvt_pk_bf16_f32 v213, v214, v215\n\t" \
    "ds_write_b64 v254, v[212:213] offset:12288\n\t" \
    LST6 \
    "buffer_load_dwordx4 v[224:227], v253, %[rs], s46 offen sc1\n\t" \
    "buffer_load_dwordx4 v[228:231], v253, %[rs], s47 offen sc1\n\t" \
    "buffer_load_dwordx4 v[232:235], v253, %[rs], s48 offen sc1\n\t" \
    "s_waitcnt lgkmcnt(0)\n\t" \
    "v_bfe_i32 v212, v253, 5, 1\n\t" \
    "v_and_b32 v212, 0xaaaa, v212\n\t" \
    "v_or_b32 v212, 0x4444, v212\n\t" \
    ".Ls02_%=:\n\t" \
    "s_waitcnt vmcnt(2)\n\t" \
    "v_max_u32 v214, v224, v225\n\t" \
    "v_max3_u32 v214, v226, v227, v214\n\t" \
    "s_waitcnt vmcnt(1)\n\t" \
    "v_max3_u32 v214, v228, v229, v214\n\t" \
    "v_max3_u32 v214, v230, v231, v214\n\t" \
    "s_waitcnt vmcnt(0)\n\t" \
    "v_max3_u32 v214, v232, v233, v214\n\t" \
    "v_max3_u32 v214, v234, v235, v214\n\t" \
    "v_cmp_eq_u32 vcc, -1, v214\n\t" \
    "s_cbranch_vccz .Ls03_%=\n\t" \
    "v_max_u32 v214, v224, v225\n\t" \
    "v_max3_u32 v214, v226, v227, v214\n\t" \
    "v_cmp_eq_u32 vcc, -1, v214\n\t" \
    "s_cbranch_vccz .Ls04_%=\n\t" \
    "buffer_load_dwordx4 v[224:227], v253, %[rs], s46 offen sc1\n\t" \
    ".Ls04_%=:\n\t" \
    "v_max_u32 v214, v228, v229\n\t" \
    "v_max3_u32 v214, v230, v231, v214\n\t" \
    "v_cmp_eq_u32 vcc, -1, v214\n\t" \
    "s_cbranch_vccz .Ls05_%=\n\t" \
    "buffer_load_dwordx4 v[228:231], v253, %[rs], s47 offen sc1\n\t" \
    ".Ls05_%=:\n\t" \
    "v_max_u32 v214, v232, v233\n\t" \
    "v_max3_u32 v214, v234, v235, v214\n\t" \
    "v_cmp_eq_u32 vcc, -1, v214\n\t" \
    "s_cbranch_vccz .Ls06_%=\n\t" \
    "buffer_load_dwordx4 v[232:235], v253, %[rs], s48 offen sc1\n\t" \
    ".Ls06_%=:\n\t" \
    "s_cmp_eq_u32 s41, 0\n\t" \
    "s_cbranch_scc1 .Ls03_%=\n\t" \
    "s_sub_u32 s41, s41, 1\n\t" \
    "s_waitcnt vmcnt(0)\n\t" \
    "s_branch .Ls02_%=\n\t" \
    ".Ls03_%=:\n\t" \
    LST2 \
    "ds_write_b128 v253, v[224:227] offset:40960\n\t" \
    "ds_write_b128 v253, v[228:231] offset:46080\n\t" \
    "ds_write_b128 v253, v[232:235] offset:41984\n\t" \
    "v_smfmac_f32_16x16x64_bf16 v[244:247], v[224:227], v[0:7], v212\n\t" \
    "v_smfmac_f32_16x16x64_bf16 v[248:251], v[224:227], v[8:15], v212\n\t" \
    "v_smfmac_f32_16x16x64_bf16 v[208:211], v[224:227], v[200:207], v212\n\t" \
    "s_waitcnt lgkmcnt(0)\n\t" \
    "s_barrier \n\t" \
    LST3 \
    "ds_read_b128 v[236:239], v253 offset:47104\n\t" \
    "ds_read_b128 v[240:243], v253 offset:43008\n\t" \
    "ds_read_b128 v[224:227], v253 offset:48128\n\t" \
    "ds_read_b128 v[200:203], v254 offset:34816\n\t" \
    "ds_read_b128 v[204:207], v254 offset:35840\n\t" \
    "v_smfmac_f32_16x16x64_bf16 v[208:211], v[228:231], v[16:23], v212\n\t" \
    "v_smfmac_f32_16x16x64_bf16 v[244:247], v[228:231], v[24:31], v212\n\t" \
    "v_smfmac_f32_16x16x64_bf16 v[248:251], v[228:231], v[32:39], v212\n\t" \
    "ds_read_b128 v[228:231], v253 offset:44032\n\t" \
    "v_smfmac_f32_16x16x64_bf16 v[208:211], v[232:235], v[216:223], v212\n\t" \
    "ds_read_b128 v[216:219], v254 offset:36864\n\t" \
    "ds_read_b128 v[220:223], v254 offset:37888\n\t" \
    "v_smfmac_f32_16x16x64_bf16 v[244:247], v[232:235], v[40:47], v212\n\t" \
    "v_smfmac_f32_16x16x64_bf16 v[248:251], v[232:235], v[48:55], v212\n\t" \
    "ds_read_b128 v[232:235], v253 offset:49152\n\t" \
    "s_waitcnt lgkmcnt(8)\n\t" \
    "v_smfmac_f32_16x16x64_bf16 v[208:211], v[236:239], v[56:63], v212\n\t" \
    "v_smfmac_f32_16x16x64_bf16 v[244:247], v[236:239], v[64:71], v212\n\t" \
    "v_smfmac_f32_16x16x64_bf16 v[248:251], v[236:239], v[72:79], v212\n\t" \
    "ds_read_b128 v[236:239], v253 offset:45056\n\t" \
    "s_waitcnt lgkmcnt(5)\n\t" \
    "v_smfmac_f32_16x16x64_bf16 v[208:211], v[240:243], v[200:207], v212\n\t" \
    "ds_read_b128 v[200:203], v254 offset:38912\n\t" \
    "ds_read_b128 v[204:207], v254 offset:39936\n\t" \
    "v_smfmac_f32_16x16x64_bf16 v[244:247], v[240:243], v[80:87], v212\n\t" \
    "v_smfmac_f32_16x16x64_bf16 v[248:251], v[240:243], v[88:95], v212\n\t" \
    "ds_read_b128 v[240:243], v253 offset:50176\n\t" \
    "v_smfmac_f32_16x16x64_bf16 v[244:247], v[224:227], v[104:111], v212\n\t" \
    "s_waitcnt lgkmcnt(7)\n\t" \
    "v_smfmac_f32_16x16x64_bf16 v[244:247], v[228:231], v[120:127], v212\n\t" \
    "s_waitcnt lgkmcnt(4)\n\t" \
    "v_smfmac_f32_16x16x64_bf16 v[244:247], v[232:235], v[144:151], v212\n\t" \
    "s_waitcnt lgkmcnt(3)\n\t" \
    "v_smfmac_f32_16x16x64_bf16 v[244:247], v[236:239], v[160:167], v212\n\t" \
    "s_waitcnt lgkmcnt(0)\n\t" \
    "v_smfmac_f32_16x16x64_bf16 v[244:247], v[240:243], v[184:191], v212\n\t" \
    "v_smfmac_f32_16x16x64_bf16 v[248:251], v[224:227], v[112:119], v212\n\t" \
    "v_smfmac_f32_16x16x64_bf16 v[248:251], v[228:231], v[128:135], v212\n\t" \
    "v_smfmac_f32_16x16x64_bf16 v[248:251], v[232:235], v[152:159], v212\n\t" \
    "v_add_f32 v244, v244, v246\n\t" \
    "v_add_f32 v245, v245, v247\n\t" \
    "v_smfmac_f32_16x16x64_bf16 v[248:251], v[236:239], v[168:175], v212\n\t" \
    "v_cvt_pk_bf16_f32 v244, v244, v245\n\t" \
    "ds_write_b16 v255, v244 offset:512\n\t" \
    "v_smfmac_f32_16x16x64_bf16 v[248:251], v[240:243], v[192:199], v212\n\t" \
    "ds_write_b16_d16_hi v255, v244 offset:528\n\t" \
    "v_smfmac_f32_16x16x64_bf16 v[208:211], v[224:227], v[96:103], v212\n\t" \
    "v_smfmac_f32_16x16x64_bf16 v[208:211], v[228:231], v[216:223], v212\n\t" \
    "v_smfmac_f32_16x16x64_bf16 v[208:211], v[232:235], v[136:143], v212\n\t" \
    "v_add_f32 v248, v248, v250\n\t" \
    "v_add_f32 v249, v249, v251\n\t" \
    "v_smfmac_f32_16x16x64_bf16 v[208:211], v[236:239], v[200:207], v212\n\t" \
    "v_cvt_pk_bf16_f32 v248, v248, v249\n\t" \
    "ds_write_b16 v255, v248 offset:1024\n\t" \
    "v_smfmac_f32_16x16x64_bf16 v[208:211], v[240:243], v[176:183], v212\n\t" \
    "ds_write_b16_d16_hi v255, v248 offset:1040\n\t" \
    "s_nop 6\n\t" \
    "v_add_f32 v208, v208, v210\n\t" \
    "v_add_f32 v209, v209, v211\n\t" \
    "v_cvt_pk_bf16_f32 v208, v208, v209\n\t" \
    "ds_write_b16 v255, v208 offset:0\n\t" \
    "ds_write_b16_d16_hi v255, v208 offset:16\n\t" \
    "ds_read_b128 v[200:203], v254 offset:18432\n\t" \
    "ds_read_b128 v[204:207], v254 offset:19456\n\t" \
    "ds_read_b128 v[214:217], v254 offset:24576\n\t" \
    "ds_read_b128 v[218:221], v254 offset:25600\n\t" \
    "ds_read_b128 v[222:225], v254 offset:20480\n\t" \
    "ds_read_b128 v[226:229], v254 offset:21504\n\t" \
    "ds_read_b128 v[230:233], v254 offset:26624\n\t" \
    "ds_read_b128 v[234:237], v254 offset:27648\n\t" \
    "v_and_b32 v213, 0xffffff7f, v254\n\t" \
    "v_mov_b64 v[242:243], 0\n\t" \
    "v_mov_b64 v[244:245], 0\n\t" \
    "v_mov_b64 v[246:247], 0\n\t" \
    "v_mov_b64 v[248:249], 0\n\t" \
    "v_mov_b64 v[250:251], 0\n\t" \
    "v_mov_b64 v[252:253], 0\n\t" \
    "s_and_b32 s44, s40, 3\n\t" \
    "s_mul_i32 s44, s44, 0x30000\n\t" \
    "s_add_u32 s50, s44, %[pb]\n\t" \
    "s_add_u32 s51, s50, 0xc000\n\t" \
    "s_add_u32 s52, s50, 0x18000\n\t" \
    "s_add_u32 s53, s50, 0x24000\n\t" \
    "s_setprio 3\n\t" \
    "s_waitcnt lgkmcnt(0)\n\t" \
    "s_barrier \n\t" \
    LST4 \
    "ds_read_b128 v[208:211], v254 offset:0\n\t" \
    "ds_read_b128 v[238:241], v254 offset:1024\n\t" \
    "s_waitcnt lgkmcnt(1)\n\t" \
    "v_smfmac_f32_16x16x64_bf16 v[242:245], v[208:211], v[200:207], v212\n\t" \
    "ds_read_b128 v[200:203], v213 offset:3072\n\t" \
    "ds_read_b128 v[204:207], v213 offset:4096\n\t" \
    "v_smfmac_f32_16x16x64_bf16 v[246:249], v[208:211], v[214:221], v212\n\t" \
    "ds_read_b128 v[214:217], v213 offset:5120\n\t" \
    "ds_read_b128 v[218:221], v213 offset:3200\n\t" \
    "s_waitcnt lgkmcnt(4)\n\t" \
    "v_smfmac_f32_16x16x64_bf16 v[242:245], v[238:241], v[222:229], v212\n\t" \
    "ds_read_b128 v[222:225], v254 offset:22528\n\t" \
    "ds_read_b128 v[226:229], v254 offset:23552\n\t" \
    "v_smfmac_f32_16x16x64_bf16 v[246:249], v[238:241], v[230:237], v212\n\t" \
    "ds_read_b128 v[230:233], v254 offset:28672\n\t" \
    "ds_read_b128 v[234:237], v254 offset:29696\n\t" \
    "s_waitcnt lgkmcnt(6)\n\t" \
    "v_smfmac_f32_16x16x64_bf16 v[250:253], v[208:211], v[200:207], v212\n\t" \
    "ds_read_b128 v[208:211], v254 offset:2048\n\t" \
    "ds_read_b128 v[200:203], v213 offset:4224\n\t" \
    "ds_read_b128 v[204:207], v213 offset:5248\n\t" \
    "s_waitcnt lgkmcnt(7)\n\t" \
    "v_smfmac_f32_16x16x64_bf16 v[250:253], v[238:241], v[214:221], v212\n\t" \
    "v_lshrrev_b32 v238, 3, v254\n\t" \
    "v_and_b32 v238, 0x6e, v238\n\t" \
    "s_waitcnt lgkmcnt(2)\n\t" \
    "v_smfmac_f32_16x16x64_bf16 v[242:245], v[208:211], v[222:229], v212\n\t" \
    "v_smfmac_f32_16x16x64_bf16 v[246:249], v[208:211], v[230:237], v212\n\t" \
    "s_waitcnt lgkmcnt(0)\n\t" \
    "v_smfmac_f32_16x16x64_bf16 v[250:253], v[208:211], v[200:207], v212\n\t" \
    "s_nop 1\n\t" \
    "v_add_f32 v242, v242, v244\n\t" \
    "v_add_f32 v243, v243, v245\n\t" \
    "s_nop 0\n\t" \
    "v_add_f32 v246, v246, v248\n\t" \
    "v_add_f32 v247, v247, v249\n\t" \
    "s_nop 0\n\t" \
    "v_add_f32 v250, v250, v252\n\t" \
    "v_add_f32 v251, v251, v253\n\t" \
    "v_max3_f32 v244, v242, v246, v250\n\t" \
    "v_max3_f32 v245, v243, v247, v251\n\t" \
    "s_nop 0\n\t" \
    "v_max_f32_dpp v244, v244, v244 quad_perm:[1,0,3,2] row_mask:0xf bank_mask:0xf\n\t" \
    "v_max_f32_dpp v245, v245, v245 quad_perm:[1,0,3,2] row_mask:0xf bank_mask:0xf\n\t" \
    "s_nop 0\n\t" \
    "v_max_f32_dpp v244, v244, v244 quad_perm:[2,3,0,1] row_mask:0xf bank_mask:0xf\n\t" \
    "v_max_f32_dpp v245, v245, v245 quad_perm:[2,3,0,1] row_mask:0xf bank_mask:0xf\n\t" \
    "s_nop 0\n\t" \
    "v_max_f32_dpp v244, v244, v244 row_half_mirror row_mask:0xf bank_mask:0xf\n\t" \
    "v_max_f32_dpp v245, v245, v245 row_half_mirror row_mask:0xf bank_mask:0xf\n\t" \
    "s_nop 0\n\t" \
    "v_max_f32_dpp v244, v244, v244 row_mirror row_mask:0xf bank_mask:0xf\n\t" \
    "v_max_f32_dpp v245, v245, v245 row_mirror row_mask:0xf bank_mask:0xf\n\t" \
    "v_mul_f32 v244, %[scl], v244\n\t" \
    "v_mul_f32 v245, %[scl], v245\n\t" \
    "v_fma_f32 v242, v242, %[scl], -v244\n\t" \
    "v_fma_f32 v243, v243, %[scl], -v245\n\t" \
    "v_fma_f32 v246, v246, %[scl], -v244\n\t" \
    "v_fma_f32 v247, v247, %[scl], -v245\n\t" \
    "v_exp_f32 v242, v242\n\t" \
    "v_exp_f32 v243, v243\n\t" \
    "v_fma_f32 v250, v250, %[scl], -v244\n\t" \
    "v_fma_f32 v251, v251, %[scl], -v245\n\t" \
    "v_exp_f32 v246, v246\n\t" \
    "v_exp_f32 v247, v247\n\t" \
    "v_exp_f32 v250, v250\n\t" \
    "v_exp_f32 v251, v251\n\t" \
    "v_add_f32 v244, v242, v246\n\t" \
    "v_add_f32 v245, v243, v247\n\t" \
    "v_fma_f32 v244, v250, 0.5, v244\n\t" \
    "v_fma_f32 v245, v251, 0.5, v245\n\t" \
    "s_nop 0\n\t" \
    "v_add_f32_dpp v244, v244, v244 quad_perm:[1,0,3,2] row_mask:0xf bank_mask:0xf\n\t" \
    "v_add_f32_dpp v245, v245, v245 quad_perm:[1,0,3,2] row_mask:0xf bank_mask:0xf\n\t" \
    "s_nop 0\n\t" \
    "v_add_f32_dpp v244, v244, v244 quad_perm:[2,3,0,1] row_mask:0xf bank_mask:0xf\n\t" \
    "v_add_f32_dpp v245, v245, v245 quad_perm:[2,3,0,1] row_mask:0xf bank_mask:0xf\n\t" \
    "s_nop 0\n\t" \
    "v_add_f32_dpp v244, v244, v244 row_half_mirror row_mask:0xf bank_mask:0xf\n\t" \
    "v_add_f32_dpp v245, v245, v245 row_half_mirror row_mask:0xf bank_mask:0xf\n\t" \
    "s_nop 0\n\t" \
    "v_add_f32_dpp v244, v244, v244 row_mirror row_mask:0xf bank_mask:0xf\n\t" \
    "v_add_f32_dpp v245, v245, v245 row_mirror row_mask:0xf bank_mask:0xf\n\t" \
    "v_rcp_f32 v244, v244\n\t" \
    "v_rcp_f32 v245, v245\n\t" \
    "v_mul_f32 v242, v242, v244\n\t" \
    "v_mul_f32 v243, v243, v245\n\t" \
    "v_mul_f32 v246, v246, v244\n\t" \
    "v_mul_f32 v247, v247, v245\n\t" \
    "v_mul_f32 v250, v250, v244\n\t" \
    "v_mul_f32 v251, v251, v245\n\t" \
    "v_cvt_pk_bf16_f32 v242, |v242|, |v243|\n\t" \
    "ds_write_b16 v255, v242 offset:128\n\t" \
    "ds_write_b16_d16_hi v255, v242 offset:144\n\t" \
    "v_cvt_pk_bf16_f32 v246, |v246|, |v247|\n\t" \
    "ds_write_b16 v255, v246 offset:132\n\t" \
    "ds_write_b16_d16_hi v255, v246 offset:148\n\t" \
    "v_cvt_pk_bf16_f32 v250, |v250|, |v251|\n\t" \
    "ds_write_b16 v238, v250 offset:51200\n\t" \
    "ds_write_b16_d16_hi v238, v250 offset:51216\n\t" \
    "ds_read_b128 v[200:203], v254 offset:0\n\t" \
    "s_wakeup\n\t" \
    "v_and_b32 v253, 0x3ff, v254\n\t" \
    "s_mov_b32 exec_hi, 0xff\n\t" \
    "s_waitcnt lgkmcnt(0)\n\t" \
    "buffer_store_dwordx4 v[200:203], v254, %[rs], s50 offen sc1\n\t" \
    "buffer_store_dwordx4 v[200:203], v254, %[rs], s51 offen sc1\n\t" \
    "buffer_store_dwordx4 v[200:203], v254, %[rs], s52 offen sc1\n\t" \
    "buffer_store_dwordx4 v[200:203], v254, %[rs], s53 offen sc1\n\t" \
    "s_mov_b32 exec_hi, -1\n\t" \
    "s_setprio 0\n\t" \
    LST5 \
    LFLUSH212 \
    "ds_read_b128 v[224:227], v253 offset:0\n\t" \
    "ds_read_b128 v[228:231], v253 offset:20480\n\t" \
    "ds_read_b128 v[232:235], v253 offset:1024\n\t" \
    "ds_read_b128 v[236:239], v253 offset:21504\n\t" \
    "s_add_u32 s40, s40, 1\n\t" \
    "s_cmp_lg_u32 s40, 64\n\t" \
    "s_cbranch_scc1 .Ls01_%=\n\t" \
    "s_branch .Lend%=\n\t" \
    ".Lend%=:\n\t" \
    "s_waitcnt vmcnt(0) lgkmcnt(0)\n\t" \
    "s_mov_b32 %[bud], s41\n\t"
#define ASM_V_LOOP \
    "s_mov_b32 s40, 0\n\t" \
    "s_mov_b32 s41, 0x20000\n\t" \
    LSTINIT \
    "s_cmp_eq_u32 %[wave], 0\n\t" \
    "s_cbranch_scc1 .Lv0_%=\n\t" \
    "s_cmp_eq_u32 %[wave], 1\n\t" \
    "s_cbranch_scc1 .Lv1_%=\n\t" \
    "s_cmp_eq_u32 %[wave], 2\n\t" \
    "s_cbranch_scc1 .Lv2_%=\n\t" \
    "s_cmp_eq_u32 %[wave], 3\n\t" \
    "s_cbranch_scc1 .Lv3_%=\n\t" \
    ".Lv4_%=:\n\t" \
    "v_bfe_i32 v252, v253, 5, 1\n\t" \
    "v_and_b32 v252, 0xaaaa, v252\n\t" \
    "v_or_b32 v252, 0x4444, v252\n\t" \
    ".Lv41_%=:\n\t" \
    "ds_read_b128 v[248:251], v254 offset:0\n\t" \
    "s_and_b32 s44, s40, %[vm]\n\t" \
    "s_lshl_b32 s44, s44, 20\n\t" \
    "s_and_b32 s45, s40, 2\n\t" \
    "s_lshl_b32 s45, s45, 12\n\t" \
    "s_add_u32 s44, s44, s45\n\t" \
    "s_add_u32 s42, s44, %[vx]\n\t" \
    "s_waitcnt lgkmcnt(0)\n\t" \
    "s_barrier \n\t" \
    LST7 \
    "ds_read_b128 v[224:227], v253 offset:6144\n\t" \
    "ds_read_b128 v[228:231], v253 offset:12288\n\t" \
    "ds_read_b128 v[232:235], v253 offset:8192\n\t" \
    "ds_read_b128 v[236:239], v253 offset:14336\n\t" \
    "ds_read_b128 v[240:243], v253 offset:9216\n\t" \
    "s_waitcnt lgkmcnt(4)\n\t" \
    "v_mfma_f32_16x16x32_bf16 v[200:203], v[224:227], v[248:251], 0\n\t" \
    "ds_read_b128 v[244:247], v253 offset:15360\n\t" \
    "s_waitcnt lgkmcnt(4)\n\t" \
    "v_mfma_f32_16x16x32_bf16 v[204:207], v[228:231], v[248:251], 0\n\t" \
    "ds_read_b128 v[224:227], v253 offset:7168\n\t" \
    "s_waitcnt lgkmcnt(4)\n\t" \
    "v_mfma_f32_16x16x32_bf16 v[200:203], v[232:235], v[0:3], v[200:203]\n\t" \
    "ds_read_b128 v[228:231], v253 offset:13312\n\t" \
    "ds_read_b128 v[248:251], v254 offset:1024\n\t" \
    "s_waitcnt lgkmcnt(5)\n\t" \
    "v_mfma_f32_16x16x32_bf16 v[204:207], v[236:239], v[0:3], v[204:207]\n\t" \
    "ds_read_b128 v[232:235], v253 offset:10240\n\t" \
    "s_waitcnt lgkmcnt(5)\n\t" \
    "v_mfma_f32_16x16x32_bf16 v[200:203], v[240:243], v[4:7], v[200:203]\n\t" \
    "ds_read_b128 v[236:239], v253 offset:16384\n\t" \
    "s_waitcnt lgkmcnt(5)\n\t" \
    "v_mfma_f32_16x16x32_bf16 v[204:207], v[244:247], v[4:7], v[204:207]\n\t" \
    "ds_read_b128 v[240:243], v253 offset:11264\n\t" \
    "s_waitcnt lgkmcnt(3)\n\t" \
    "v_mfma_f32_16x16x32_bf16 v[200:203], v[224:227], v[248:251], v[200:203]\n\t" \
    "ds_read_b128 v[244:247], v253 offset:17408\n\t" \
    "v_mfma_f32_16x16x32_bf16 v[208:211], v[224:227], v[20:23], 0\n\t" \
    "v_mfma_f32_16x16x32_bf16 v[204:207], v[228:231], v[248:251], v[204:207]\n\t" \
    "ds_read_b128 v[224:227], v253 offset:6144\n\t" \
    "v_mfma_f32_16x16x32_bf16 v[212:215], v[228:231], v[20:23], 0\n\t" \
    "s_waitcnt lgkmcnt(4)\n\t" \
    "v_mfma_f32_16x16x32_bf16 v[200:203], v[232:235], v[8:11], v[200:203]\n\t" \
    "ds_read_b128 v[228:231], v253 offset:12288\n\t" \
    "ds_read_b128 v[248:251], v254 offset:2048\n\t" \
    "v_mfma_f32_16x16x32_bf16 v[208:211], v[232:235], v[32:35], v[208:211]\n\t" \
    "s_waitcnt lgkmcnt(5)\n\t" \
    "v_mfma_f32_16x16x32_bf16 v[204:207], v[236:239], v[8:11], v[204:207]\n\t" \
    "ds_read_b128 v[232:235], v253 offset:8192\n\t" \
    "v_mfma_f32_16x16x32_bf16 v[212:215], v[236:239], v[32:35], v[212:215]\n\t" \
    "s_waitcnt lgkmcnt(5)\n\t" \
    "v_mfma_f32_16x16x32_bf16 v[200:203], v[240:243], v[12:15], v[200:203]\n\t" \
    "ds_read_b128 v[236:239], v253 offset:14336\n\t" \
    "v_mfma_f32_16x16x32_bf16 v[208:211], v[240:243], v[36:39], v[208:211]\n\t" \
    "s_waitcnt lgkmcnt(5)\n\t" \
    "v_mfma_f32_16x16x32_bf16 v[204:207], v[244:247], v[12:15], v[204:207]\n\t" \
    "ds_read_b128 v[240:243], v253 offset:9216\n\t" \
    "v_mfma_f32_16x16x32_bf16 v[212:215], v[244:247], v[36:39], v[212:215]\n\t" \
    "s_waitcnt lgkmcnt(3)\n\t" \
    "v_mfma_f32_16x16x32_bf16 v[216:219], v[224:227], v[248:251], 0\n\t" \
    "ds_read_b128 v[244:247], v253 offset:15360\n\t" \
    "v_mfma_f32_16x16x32_bf16 v[208:211], v[224:227], v[16:19], v[208:211]\n\t" \
    "v_mfma_f32_16x16x32_bf16 v[220:223], v[228:231], v[248:251], 0\n\t" \
    "ds_read_b128 v[224:227], v253 offset:7168\n\t" \
    "v_mfma_f32_16x16x32_bf16 v[212:215], v[228:231], v[16:19], v[212:215]\n\t" \
    "v_cvt_pk_bf16_f32 v200, v200, v201\n\t" \
    "s_waitcnt lgkmcnt(4)\n\t" \
    "v_mfma_f32_16x16x32_bf16 v[216:219], v[232:235], v[40:43], v[216:219]\n\t" \
    "ds_read_b128 v[228:231], v253 offset:13312\n\t" \
    "ds_read_b128 v[248:251], v254 offset:3072\n\t" \
    "v_cvt_pk_bf16_f32 v201, v202, v203\n\t" \
    "v_mfma_f32_16x16x32_bf16 v[208:211], v[232:235], v[24:27], v[208:211]\n\t" \
    "v_cvt_pk_bf16_f32 v202, v204, v205\n\t" \
    "s_waitcnt lgkmcnt(5)\n\t" \
    "v_mfma_f32_16x16x32_bf16 v[220:223], v[236:239], v[40:43], v[220:223]\n\t" \
    "ds_read_b128 v[232:235], v253 offset:10240\n\t" \
    "v_cvt_pk_bf16_f32 v203, v206, v207\n\t" \
    "v_mfma_f32_16x16x32_bf16 v[212:215], v[236:239], v[24:27], v[212:215]\n\t" \
    "buffer_store_dwordx4 v[200:203], v253, %[rs], s42 offen sc1\n\t" \
    "s_waitcnt lgkmcnt(5)\n\t" \
    "v_mfma_f32_16x16x32_bf16 v[216:219], v[240:243], v[44:47], v[216:219]\n\t" \
    "ds_read_b128 v[236:239], v253 offset:16384\n\t" \
    "s_add_u32 s42, s42, 0x10000\n\t" \
    "v_mfma_f32_16x16x32_bf16 v[208:211], v[240:243], v[28:31], v[208:211]\n\t" \
    "s_waitcnt lgkmcnt(5)\n\t" \
    "v_mfma_f32_16x16x32_bf16 v[220:223], v[244:247], v[44:47], v[220:223]\n\t" \
    "ds_read_b128 v[240:243], v253 offset:11264\n\t" \
    "v_mfma_f32_16x16x32_bf16 v[212:215], v[244:247], v[28:31], v[212:215]\n\t" \
    "s_waitcnt lgkmcnt(3)\n\t" \
    "v_mfma_f32_16x16x32_bf16 v[216:219], v[224:227], v[248:251], v[216:219]\n\t" \
    "ds_read_b128 v[244:247], v253 offset:17408\n\t" \
    "v_mfma_f32_16x16x32_bf16 v[200:203], v[224:227], v[60:63], 0\n\t" \
    "v_mfma_f32_16x16x32_bf16 v[220:223], v[228:231], v[248:251], v[220:223]\n\t" \
    "ds_read_b128 v[224:227], v253 offset:6144\n\t" \
    "v_mfma_f32_16x16x32_bf16 v[204:207], v[228:231], v[60:63], 0\n\t" \
    "v_cvt_pk_bf16_f32 v208, v208, v209\n\t" \
    "s_waitcnt lgkmcnt(4)\n\t" \
    "v_mfma_f32_16x16x32_bf16 v[216:219], v[232:235], v[48:51], v[216:219]\n\t" \
    "ds_read_b128 v[228:231], v253 offset:12288\n\t" \
    "ds_read_b128 v[248:251], v254 offset:4096\n\t" \
    "v_cvt_pk_bf16_f32 v209, v210, v211\n\t" \
    "v_mfma_f32_16x16x32_bf16 v[200:203], v[232:235], v[72:75], v[200:203]\n\t" \
    "v_cvt_pk_bf16_f32 v210, v212, v213\n\t" \
    "s_waitcnt lgkmcnt(5)\n\t" \
    "v_mfma_f32_16x16x32_bf16 v[220:223], v[236:239], v[48:51], v[220:223]\n\t" \
    "ds_read_b128 v[232:235], v253 offset:8192\n\t" \
    "v_cvt_pk_bf16_f32 v211, v214, v215\n\t" \
    "v_mfma_f32_16x16x32_bf16 v[204:207], v[236:239], v[72:75], v[204:207]\n\t" \
    "buffer_store_dwordx4 v[208:211], v253, %[rs], s42 offen sc1\n\t" \
    "s_waitcnt lgkmcnt(5)\n\t" \
    "v_mfma_f32_16x16x32_bf16 v[216:219], v[240:243], v[52:55], v[216:219]\n\t" \
    "ds_read_b128 v[236:239], v253 offset:14336\n\t" \
    "s_add_u32 s42, s42, 0x10000\n\t" \
    "v_mfma_f32_16x16x32_bf16 v[200:203], v[240:243], v[76:79], v[200:203]\n\t" \
    "s_waitcnt lgkmcnt(5)\n\t" \
    "v_mfma_f32_16x16x32_bf16 v[220:223], v[244:247], v[52:55], v[220:223]\n\t" \
    "ds_read_b128 v[240:243], v253 offset:9216\n\t" \
    "v_mfma_f32_16x16x32_bf16 v[204:207], v[244:247], v[76:79], v[204:207]\n\t" \
    "s_waitcnt lgkmcnt(3)\n\t" \
    "v_mfma_f32_16x16x32_bf16 v[208:211], v[224:227], v[248:251], 0\n\t" \
    "ds_read_b128 v[244:247], v253 offset:15360\n\t" \
    "v_mfma_f32_16x16x32_bf16 v[200:203], v[224:227], v[56:59], v[200:203]\n\t" \
    "v_mfma_f32_16x16x32_bf16 v[212:215], v[228:231], v[248:251], 0\n\t" \
    "ds_read_b128 v[224:227], v253 offset:7168\n\t" \
    "v_mfma_f32_16x16x32_bf16 v[204:207], v[228:231], v[56:59], v[204:207]\n\t" \
    "v_cvt_pk_bf16_f32 v216, v216, v217\n\t" \
    "s_waitcnt lgkmcnt(4)\n\t" \
    "v_mfma_f32_16x16x32_bf16 v[208:211], v[232:235], v[80:83], v[208:211]\n\t" \
    "ds_read_b128 v[228:231], v253 offset:13312\n\t" \
    "ds_read_b128 v[248:251], v254 offset:5120\n\t" \
    "v_cvt_pk_bf16_f32 v217, v218, v219\n\t" \
    "v_mfma_f32_16x16x32_bf16 v[200:203], v[232:235], v[64:67], v[200:203]\n\t" \
    "v_cvt_pk_bf16_f32 v218, v220, v221\n\t" \
    "s_waitcnt lgkmcnt(5)\n\t" \
    "v_mfma_f32_16x16x32_bf16 v[212:215], v[236:239], v[80:83], v[212:215]\n\t" \
    "ds_read_b128 v[232:235], v253 offset:10240\n\t" \
    "v_cvt_pk_bf16_f32 v219, v222, v223\n\t" \
    "v_mfma_f32_16x16x32_bf16 v[204:207], v[236:239], v[64:67], v[204:207]\n\t" \
    "buffer_store_dwordx4 v[216:219], v253, %[rs], s42 offen sc1\n\t" \
    "s_waitcnt lgkmcnt(5)\n\t" \
    "v_mfma_f32_16x16x32_bf16 v[208:211], v[240:243], v[84:87], v[208:211]\n\t" \
    "ds_read_b128 v[236:239], v253 offset:16384\n\t" \
    "s_add_u32 s42, s42, 0x10000\n\t" \
    "v_mfma_f32_16x16x32_bf16 v[200:203], v[240:243], v[68:71], v[200:203]\n\t" \
    "s_waitcnt lgkmcnt(5)\n\t" \
    "v_mfma_f32_16x16x32_bf16 v[212:215], v[244:247], v[84:87], v[212:215]\n\t" \
    "ds_read_b128 v[240:243], v253 offset:11264\n\t" \
    "v_mfma_f32_16x16x32_bf16 v[204:207], v[244:247], v[68:71], v[204:207]\n\t" \
    "s_waitcnt lgkmcnt(3)\n\t" \
    "v_mfma_f32_16x16x32_bf16 v[208:211], v[224:227], v[248:251], v[208:211]\n\t" \
    "ds_read_b128 v[244:247], v253 offset:17408\n\t" \
    "v_mfma_f32_16x16x32_bf16 v[216:219], v[224:227], v[100:103], 0\n\t" \
    "v_mfma_f32_16x16x32_bf16 v[212:215], v[228:231], v[248:251], v[212:215]\n\t" \
    "ds_read_b128 v[224:227], v253 offset:6144\n\t" \
    "v_mfma_f32_16x16x32_bf16 v[220:223], v[228:231], v[100:103], 0\n\t" \
    "v_cvt_pk_bf16_f32 v200, v200, v201\n\t" \
    "s_waitcnt lgkmcnt(4)\n\t" \
    "v_mfma_f32_16x16x32_bf16 v[208:211], v[232:235], v[88:91], v[208:211]\n\t" \
    "ds_read_b128 v[228:231], v253 offset:12288\n\t" \
    "ds_read_b128 v[248:251], v254 offset:6144\n\t" \
    "v_cvt_pk_bf16_f32 v201, v202, v203\n\t" \
    "v_mfma_f32_16x16x32_bf16 v[216:219], v[232:235], v[112:115], v[216:219]\n\t" \
    "v_cvt_pk_bf16_f32 v202, v204, v205\n\t" \
    "s_waitcnt lgkmcnt(5)\n\t" \
    "v_mfma_f32_16x16x32_bf16 v[212:215], v[236:239], v[88:91], v[212:215]\n\t" \
    "ds_read_b128 v[232:235], v253 offset:8192\n\t" \
    "v_cvt_pk_bf16_f32 v203, v206, v207\n\t" \
    "v_mfma_f32_16x16x32_bf16 v[220:223], v[236:239], v[112:115], v[220:223]\n\t" \
    "buffer_store_dwordx4 v[200:203], v253, %[rs], s42 offen sc1\n\t" \
    "s_waitcnt lgkmcnt(5)\n\t" \
    "v_mfma_f32_16x16x32_bf16 v[208:211], v[240:243], v[92:95], v[208:211]\n\t" \
    "ds_read_b128 v[236:239], v253 offset:14336\n\t" \
    "s_add_u32 s42, s42, 0x10000\n\t" \
    "v_mfma_f32_16x16x32_bf16 v[216:219], v[240:243], v[116:119], v[216:219]\n\t" \
    "s_waitcnt lgkmcnt(5)\n\t" \
    "v_mfma_f32_16x16x32_bf16 v[212:215], v[244:247], v[92:95], v[212:215]\n\t" \
    "ds_read_b128 v[240:243], v253 offset:9216\n\t" \
    "v_mfma_f32_16x16x32_bf16 v[220:223], v[244:247], v[116:119], v[220:223]\n\t" \
    "s_waitcnt lgkmcnt(3)\n\t" \
    "v_mfma_f32_16x16x32_bf16 v[200:203], v[224:227], v[248:251], 0\n\t" \
    "ds_read_b128 v[244:247], v253 offset:15360\n\t" \
    "v_mfma_f32_16x16x32_bf16 v[216:219], v[224:227], v[96:99], v[216:219]\n\t" \
    "v_mfma_f32_16x16x32_bf16 v[204:207], v[228:231], v[248:251], 0\n\t" \
    "ds_read_b128 v[224:227], v253 offset:7168\n\t" \
    "v_mfma_f32_16x16x32_bf16 v[220:223], v[228:231], v[96:99], v[220:223]\n\t" \
    "v_cvt_pk_bf16_f32 v208, v208, v209\n\t" \
    "s_waitcnt lgkmcnt(4)\n\t" \
    "v_mfma_f32_16x16x32_bf16 v[200:203], v[232:235], v[120:123], v[200:203]\n\t" \
    "ds_read_b128 v[228:231], v253 offset:13312\n\t" \
    "ds_read_b128 v[248:251], v254 offset:7168\n\t" \
    "v_cvt_pk_bf16_f32 v209, v210, v211\n\t" \
    "v_mfma_f32_16x16x32_bf16 v[216:219], v[232:235], v[104:107], v[216:219]\n\t" \
    "v_cvt_pk_bf16_f32 v210, v212, v213\n\t" \
    "s_waitcnt lgkmcnt(5)\n\t" \
    "v_mfma_f32_16x16x32_bf16 v[204:207], v[236:239], v[120:123], v[204:207]\n\t" \
    "ds_read_b128 v[232:235], v253 offset:10240\n\t" \
    "v_cvt_pk_bf16_f32 v211, v214, v215\n\t" \
    "v_mfma_f32_16x16x32_bf16 v[220:223], v[236:239], v[104:107], v[220:223]\n\t" \
    "buffer_store_dwordx4 v[208:211], v253, %[rs], s42 offen sc1\n\t" \
    "s_waitcnt lgkmcnt(5)\n\t" \
    "v_mfma_f32_16x16x32_bf16 v[200:203], v[240:243], v[124:127], v[200:203]\n\t" \
    "ds_read_b128 v[236:239], v253 offset:16384\n\t" \
    "s_add_u32 s42, s42, 0x10000\n\t" \
    "v_mfma_f32_16x16x32_bf16 v[216:219], v[240:243], v[108:111], v[216:219]\n\t" \
    "s_waitcnt lgkmcnt(5)\n\t" \
    "v_mfma_f32_16x16x32_bf16 v[204:207], v[244:247], v[124:127], v[204:207]\n\t" \
    "ds_read_b128 v[240:243], v253 offset:11264\n\t" \
    "v_mfma_f32_16x16x32_bf16 v[220:223], v[244:247], v[108:111], v[220:223]\n\t" \
    "s_waitcnt lgkmcnt(3)\n\t" \
    "v_mfma_f32_16x16x32_bf16 v[200:203], v[224:227], v[248:251], v[200:203]\n\t" \
    "ds_read_b128 v[244:247], v253 offset:17408\n\t" \
    "v_mfma_f32_16x16x32_bf16 v[208:211], v[224:227], v[140:143], 0\n\t" \
    "v_mfma_f32_16x16x32_bf16 v[204:207], v[228:231], v[248:251], v[204:207]\n\t" \
    "ds_read_b128 v[224:227], v253 offset:6144\n\t" \
    "v_mfma_f32_16x16x32_bf16 v[212:215], v[228:231], v[140:143], 0\n\t" \
    "v_cvt_pk_bf16_f32 v216, v216, v217\n\t" \
    "s_waitcnt lgkmcnt(4)\n\t" \
    "v_mfma_f32_16x16x32_bf16 v[200:203], v[232:235], v[128:131], v[200:203]\n\t" \
    "ds_read_b128 v[228:231], v253 offset:12288\n\t" \
    "ds_read_b128 v[248:251], v254 offset:8192\n\t" \
    "v_cvt_pk_bf16_f32 v217, v218, v219\n\t" \
    "v_mfma_f32_16x16x32_bf16 v[208:211], v[232:235], v[152:155], v[208:211]\n\t" \
    "v_cvt_pk_bf16_f32 v218, v220, v221\n\t" \
    "s_waitcnt lgkmcnt(5)\n\t" \
    "v_mfma_f32_16x16x32_bf16 v[204:207], v[236:239], v[128:131], v[204:207]\n\t" \
    "ds_read_b128 v[232:235], v253 offset:8192\n\t" \
    "v_cvt_pk_bf16_f32 v219, v222, v223\n\t" \
    "v_mfma_f32_16x16x32_bf16 v[212:215], v[236:239], v[152:155], v[212:215]\n\t" \
    "buffer_store_dwordx4 v[216:219], v253, %[rs], s42 offen sc1\n\t" \
    "s_waitcnt lgkmcnt(5)\n\t" \
    "v_mfma_f32_16x16x32_bf16 v[200:203], v[240:243], v[132:135], v[200:203]\n\t" \
    "ds_read_b128 v[236:239], v253 offset:14336\n\t" \
    "s_add_u32 s42, s42, 0x10000\n\t" \
    "v_mfma_f32_16x16x32_bf16 v[208:211], v[240:243], v[156:159], v[208:211]\n\t" \
    "s_waitcnt lgkmcnt(5)\n\t" \
    "v_mfma_f32_16x16x32_bf16 v[204:207], v[244:247], v[132:135], v[204:207]\n\t" \
    "ds_read_b128 v[240:243], v253 offset:9216\n\t" \
    "v_mfma_f32_16x16x32_bf16 v[212:215], v[244:247], v[156:159], v[212:215]\n\t" \
    "s_waitcnt lgkmcnt(3)\n\t" \
    "v_mfma_f32_16x16x32_bf16 v[216:219], v[224:227], v[248:251], 0\n\t" \
    "ds_read_b128 v[244:247], v253 offset:15360\n\t" \
    "v_mfma_f32_16x16x32_bf16 v[208:211], v[224:227], v[136:139], v[208:211]\n\t" \
    "v_mfma_f32_16x16x32_bf16 v[220:223], v[228:231], v[248:251], 0\n\t" \
    "ds_read_b128 v[224:227], v253 offset:7168\n\t" \
    "v_mfma_f32_16x16x32_bf16 v[212:215], v[228:231], v[136:139], v[212:215]\n\t" \
    "v_cvt_pk_bf16_f32 v200, v200, v201\n\t" \
    "s_waitcnt lgkmcnt(4)\n\t" \
    "v_mfma_f32_16x16x32_bf16 v[216:219], v[232:235], v[160:163], v[216:219]\n\t" \
    "ds_read_b128 v[228:231], v253 offset:13312\n\t" \
    "ds_read_b128 v[248:251], v254 offset:9216\n\t" \
    "v_cvt_pk_bf16_f32 v201, v202, v203\n\t" \
    "v_mfma_f32_16x16x32_bf16 v[208:211], v[232:235], v[144:147], v[208:211]\n\t" \
    "v_cvt_pk_bf16_f32 v202, v204, v205\n\t" \
    "s_waitcnt lgkmcnt(5)\n\t" \
    "v_mfma_f32_16x16x32_bf16 v[220:223], v[236:239], v[160:163], v[220:223]\n\t" \
    "ds_read_b128 v[232:235], v253 offset:10240\n\t" \
    "v_cvt_pk_bf16_f32 v203, v206, v207\n\t" \
    "v_mfma_f32_16x16x32_bf16 v[212:215], v[236:239], v[144:147], v[212:215]\n\t" \
    "buffer_store_dwordx4 v[200:203], v253, %[rs], s42 offen sc1\n\t" \
    "s_waitcnt lgkmcnt(5)\n\t" \
    "v_mfma_f32_16x16x32_bf16 v[216:219], v[240:243], v[164:167], v[216:219]\n\t" \
    "ds_read_b128 v[236:239], v253 offset:16384\n\t" \
    "s_add_u32 s42, s42, 0x10000\n\t" \
    "v_mfma_f32_16x16x32_bf16 v[208:211], v[240:243], v[148:151], v[208:211]\n\t" \
    "s_waitcnt lgkmcnt(5)\n\t" \
    "v_mfma_f32_16x16x32_bf16 v[220:223], v[244:247], v[164:167], v[220:223]\n\t" \
    "ds_read_b128 v[240:243], v253 offset:11264\n\t" \
    "v_mfma_f32_16x16x32_bf16 v[212:215], v[244:247], v[148:151], v[212:215]\n\t" \
    "s_waitcnt lgkmcnt(3)\n\t" \
    "v_mfma_f32_16x16x32_bf16 v[216:219], v[224:227], v[248:251], v[216:219]\n\t" \
    "ds_read_b128 v[244:247], v253 offset:17408\n\t" \
    "v_mfma_f32_16x16x32_bf16 v[200:203], v[224:227], v[180:183], 0\n\t" \
    "v_mfma_f32_16x16x32_bf16 v[220:223], v[228:231], v[248:251], v[220:223]\n\t" \
    "ds_read_b128 v[224:227], v253 offset:6144\n\t" \
    "v_mfma_f32_16x16x32_bf16 v[204:207], v[228:231], v[180:183], 0\n\t" \
    "v_cvt_pk_bf16_f32 v208, v208, v209\n\t" \
    "s_waitcnt lgkmcnt(4)\n\t" \
    "v_mfma_f32_16x16x32_bf16 v[216:219], v[232:235], v[168:171], v[216:219]\n\t" \
    "ds_read_b128 v[228:231], v253 offset:12288\n\t" \
    "v_cvt_pk_bf16_f32 v209, v210, v211\n\t" \
    "v_mfma_f32_16x16x32_bf16 v[200:203], v[232:235], v[192:195], v[200:203]\n\t" \
    "v_cvt_pk_bf16_f32 v210, v212, v213\n\t" \
    "s_waitcnt lgkmcnt(4)\n\t" \
    "v_mfma_f32_16x16x32_bf16 v[220:223], v[236:239], v[168:171], v[220:223]\n\t" \
    "ds_read_b128 v[232:235], v253 offset:8192\n\t" \
    "v_cvt_pk_bf16_f32 v211, v214, v215\n\t" \
    "v_mfma_f32_16x16x32_bf16 v[204:207], v[236:239], v[192:195], v[204:207]\n\t" \
    "buffer_store_dwordx4 v[208:211], v253, %[rs], s42 offen sc1\n\t" \
    "s_waitcnt lgkmcnt(4)\n\t" \
    "v_mfma_f32_16x16x32_bf16 v[216:219], v[240:243], v[172:175], v[216:219]\n\t" \
    "ds_read_b128 v[236:239], v253 offset:14336\n\t" \
    "s_add_u32 s42, s42, 0x10000\n\t" \
    "v_mfma_f32_16x16x32_bf16 v[200:203], v[240:243], v[196:199], v[200:203]\n\t" \
    "s_waitcnt lgkmcnt(4)\n\t" \
    "v_mfma_f32_16x16x32_bf16 v[220:223], v[244:247], v[172:175], v[220:223]\n\t" \
    "ds_read_b128 v[240:243], v253 offset:9216\n\t" \
    "v_mfma_f32_16x16x32_bf16 v[204:207], v[244:247], v[196:199], v[204:207]\n\t" \
    "s_waitcnt lgkmcnt(4)\n\t" \
    "v_mfma_f32_16x16x32_bf16 v[200:203], v[224:227], v[176:179], v[200:203]\n\t" \
    "ds_read_b128 v[244:247], v253 offset:15360\n\t" \
    "s_waitcnt lgkmcnt(4)\n\t" \
    "v_mfma_f32_16x16x32_bf16 v[204:207], v[228:231], v[176:179], v[204:207]\n\t" \
    "s_waitcnt lgkmcnt(3)\n\t" \
    "v_mfma_f32_16x16x32_bf16 v[200:203], v[232:235], v[184:187], v[200:203]\n\t" \
    "s_waitcnt lgkmcnt(2)\n\t" \
    "v_mfma_f32_16x16x32_bf16 v[204:207], v[236:239], v[184:187], v[204:207]\n\t" \
    "v_cvt_pk_bf16_f32 v216, v216, v217\n\t" \
    "s_waitcnt lgkmcnt(1)\n\t" \
    "v_mfma_f32_16x16x32_bf16 v[200:203], v[240:243], v[188:191], v[200:203]\n\t" \
    "v_cvt_pk_bf16_f32 v217, v218, v219\n\t" \
    "s_waitcnt lgkmcnt(0)\n\t" \
    "v_mfma_f32_16x16x32_bf16 v[204:207], v[244:247], v[188:191], v[204:207]\n\t" \
    "v_cvt_pk_bf16_f32 v218, v220, v221\n\t" \
    "v_cvt_pk_bf16_f32 v219, v222, v223\n\t" \
    "buffer_store_dwordx4 v[216:219], v253, %[rs], s42 offen sc1\n\t" \
    "s_add_u32 s42, s42, 0x10000\n\t" \
    "s_nop 0\n\t" \
    "v_cvt_pk_bf16_f32 v200, v200, v201\n\t" \
    "v_cvt_pk_bf16_f32 v201, v202, v203\n\t" \
    "s_nop 1\n\t" \
    "v_cvt_pk_bf16_f32 v202, v204, v205\n\t" \
    "v_cvt_pk_bf16_f32 v203, v206, v207\n\t" \
    "buffer_store_dwordx4 v[200:203], v253, %[rs], s42 offen sc1\n\t" \
    "s_add_u32 s42, s42, 0x10000\n\t" \
    LST0 \
    "s_waitcnt lgkmcnt(0)\n\t" \
    "s_barrier \n\t" \
    LST3 \
    "ds_read_b128 v[224:227], v253 offset:3072\n\t" \
    "ds_read_b128 v[228:231], v253 offset:3072\n\t" \
    "ds_read_b128 v[232:235], v253 offset:3072\n\t" \
    "ds_read_b128 v[236:239], v253 offset:3072\n\t" \
    "ds_read_b128 v[240:243], v253 offset:3072\n\t" \
    "ds_read_b128 v[244:247], v253 offset:3072\n\t" \
    "ds_read_b128 v[248:251], v253 offset:3072\n\t" \
    "s_add_u32 s44, s40, 1\n\t" \
    "s_mov_b32 s45, s44\n\t" \
    "s_waitcnt vmcnt(0)\n\t" \
    "v_mov_b64 v[200:201], s[44:45]\n\t" \
    "v_mov_b64 v[202:203], s[44:45]\n\t" \
    "s_mov_b64 exec, 0xff\n\t" \
    "buffer_store_dwordx4 v[200:203], v253, %[rs], %[fo] offen sc1\n\t" \
    "s_mov_b64 exec, -1\n\t" \
    LST1 \
    "ds_read_b128 v[220:223], v253 offset:3072\n\t" \
    "ds_read_b128 v[212:215], v254 offset:0\n\t" \
    "ds_read_b128 v[216:219], v254 offset:1024\n\t" \
    "s_and_b32 s44, s40, 3\n\t" \
    "s_mul_i32 s44, s44, 0x202000\n\t" \
    "s_add_u32 s42, s44, %[vo]\n\t" \
    "s_setprio 3\n\t" \
    "s_waitcnt lgkmcnt(0)\n\t" \
    "s_barrier \n\t" \
    LST4 \
    "ds_read_b128 v[204:207], v253 offset:1024\n\t" \
    "ds_read_b128 v[208:211], v253 offset:2048\n\t" \
    "ds_read_b128 v[200:203], v253 offset:0\n\t" \
    "s_waitcnt lgkmcnt(2)\n\t" \
    "v_smfmac_f32_16x16x64_bf16 v[224:227], v[204:207], v[24:31], v252\n\t" \
    "v_smfmac_f32_16x16x64_bf16 v[220:223], v[204:207], v[0:7], v252\n\t" \
    "s_waitcnt lgkmcnt(1)\n\t" \
    "v_smfmac_f32_16x16x64_bf16 v[224:227], v[208:211], v[32:39], v252\n\t" \
    "v_smfmac_f32_16x16x64_bf16 v[220:223], v[208:211], v[8:15], v252\n\t" \
    "s_waitcnt lgkmcnt(0)\n\t" \
    "v_smfmac_f32_16x16x64_bf16 v[224:227], v[200:203], v[16:23], v252\n\t" \
    "v_smfmac_f32_16x16x64_bf16 v[220:223], v[200:203], v[212:219], v252\n\t" \
    "ds_read_b128 v[212:215], v254 offset:2048\n\t" \
    "ds_read_b128 v[216:219], v254 offset:3072\n\t" \
    "v_smfmac_f32_16x16x64_bf16 v[232:235], v[204:207], v[64:71], v252\n\t" \
    "v_smfmac_f32_16x16x64_bf16 v[228:231], v[204:207], v[40:47], v252\n\t" \
    "v_smfmac_f32_16x16x64_bf16 v[232:235], v[208:211], v[72:79], v252\n\t" \
    "v_add_f32 v220, v220, v222\n\t" \
    "v_add_f32 v221, v221, v223\n\t" \
    "v_cvt_pk_bf16_f32 v220, v220, v221\n\t" \
    "v_smfmac_f32_16x16x64_bf16 v[228:231], v[208:211], v[48:55], v252\n\t" \
    "v_min_u32 v220, -2, v220\n\t" \
    "v_add_f32 v224, v224, v226\n\t" \
    "v_add_f32 v225, v225, v227\n\t" \
    "v_smfmac_f32_16x16x64_bf16 v[232:235], v[200:203], v[56:63], v252\n\t" \
    "v_cvt_pk_bf16_f32 v221, v224, v225\n\t" \
    "v_min_u32 v221, -2, v221\n\t" \
    "s_waitcnt lgkmcnt(0)\n\t" \
    "v_smfmac_f32_16x16x64_bf16 v[228:231], v[200:203], v[212:219], v252\n\t" \
    "ds_read_b128 v[212:215], v254 offset:4096\n\t" \
    "ds_read_b128 v[216:219], v254 offset:5120\n\t" \
    "v_smfmac_f32_16x16x64_bf16 v[240:243], v[204:207], v[104:111], v252\n\t" \
    "v_smfmac_f32_16x16x64_bf16 v[236:239], v[204:207], v[80:87], v252\n\t" \
    "v_smfmac_f32_16x16x64_bf16 v[240:243], v[208:211], v[112:119], v252\n\t" \
    "v_add_f32 v228, v228, v230\n\t" \
    "v_add_f32 v229, v229, v231\n\t" \
    "v_cvt_pk_bf16_f32 v222, v228, v229\n\t" \
    "v_smfmac_f32_16x16x64_bf16 v[236:239], v[208:211], v[88:95], v252\n\t" \
    "v_min_u32 v222, -2, v222\n\t" \
    "v_add_f32 v232, v232, v234\n\t" \
    "v_add_f32 v233, v233, v235\n\t" \
    "v_smfmac_f32_16x16x64_bf16 v[240:243], v[200:203], v[96:103], v252\n\t" \
    "v_cvt_pk_bf16_f32 v223, v232, v233\n\t" \
    "v_min_u32 v223, -2, v223\n\t" \
    "v_permlane32_swap_b32 v220, v222\n\t" \
    "s_waitcnt lgkmcnt(0)\n\t" \
    "v_smfmac_f32_16x16x64_bf16 v[236:239], v[200:203], v[212:219], v252\n\t" \
    "ds_read_b128 v[212:215], v254 offset:6144\n\t" \
    "ds_read_b128 v[216:219], v254 offset:7168\n\t" \
    "s_nop 0\n\t" \
    "v_permlane32_swap_b32 v221, v223\n\t" \
    "s_nop 1\n\t" \
    "v_permlane16_swap_b32 v220, v221\n\t" \
    "v_permlane16_swap_b32 v222, v223\n\t" \
    "buffer_store_dwordx4 v[220:223], v255, %[rs], s42 offen sc1\n\t" \
    "s_add_u32 s42, s42, 0x10000\n\t" \
    "ds_read_b128 v[224:227], v253 offset:3072\n\t" \
    "ds_read_b128 v[228:231], v253 offset:3072\n\t" \
    "v_smfmac_f32_16x16x64_bf16 v[248:251], v[204:207], v[144:151], v252\n\t" \
    "v_smfmac_f32_16x16x64_bf16 v[244:247], v[204:207], v[120:127], v252\n\t" \
    "v_smfmac_f32_16x16x64_bf16 v[248:251], v[208:211], v[152:159], v252\n\t" \
    "v_add_f32 v236, v236, v238\n\t" \
    "v_add_f32 v237, v237, v239\n\t" \
    "v_cvt_pk_bf16_f32 v236, v236, v237\n\t" \
    "v_smfmac_f32_16x16x64_bf16 v[244:247], v[208:211], v[128:135], v252\n\t" \
    "v_min_u32 v236, -2, v236\n\t" \
    "v_add_f32 v240, v240, v242\n\t" \
    "v_add_f32 v241, v241, v243\n\t" \
    "v_smfmac_f32_16x16x64_bf16 v[248:251], v[200:203], v[136:143], v252\n\t" \
    "v_cvt_pk_bf16_f32 v237, v240, v241\n\t" \
    "v_min_u32 v237, -2, v237\n\t" \
    "s_waitcnt lgkmcnt(2)\n\t" \
    "v_smfmac_f32_16x16x64_bf16 v[244:247], v[200:203], v[212:219], v252\n\t" \
    "ds_read_b128 v[212:215], v254 offset:8192\n\t" \
    "ds_read_b128 v[216:219], v254 offset:9216\n\t" \
    "s_waitcnt lgkmcnt(2)\n\t" \
    "v_smfmac_f32_16x16x64_bf16 v[228:231], v[204:207], v[184:191], v252\n\t" \
    "v_smfmac_f32_16x16x64_bf16 v[224:227], v[204:207], v[160:167], v252\n\t" \
    "v_smfmac_f32_16x16x64_bf16 v[228:231], v[208:211], v[192:199], v252\n\t" \
    "v_and_b32 v232, 0x7fff, v255\n\t" \
    "v_lshrrev_b32 v233, 15, v255\n\t" \
    "v_lshl_add_u32 v232, v233, 3, v232\n\t" \
    "v_smfmac_f32_16x16x64_bf16 v[224:227], v[208:211], v[168:175], v252\n\t" \
    "v_add_f32 v244, v244, v246\n\t" \
    "v_add_f32 v245, v245, v247\n\t" \
    "v_cvt_pk_bf16_f32 v238, v244, v245\n\t" \
    "v_smfmac_f32_16x16x64_bf16 v[228:231], v[200:203], v[176:183], v252\n\t" \
    "v_min_u32 v238, -2, v238\n\t" \
    "v_add_f32 v248, v248, v250\n\t" \
    "v_add_f32 v249, v249, v251\n\t" \
    "s_waitcnt lgkmcnt(0)\n\t" \
    "v_smfmac_f32_16x16x64_bf16 v[224:227], v[200:203], v[212:219], v252\n\t" \
    "v_cvt_pk_bf16_f32 v239, v248, v249\n\t" \
    "v_min_u32 v239, -2, v239\n\t" \
    "v_permlane32_swap_b32 v236, v238\n\t" \
    "s_nop 0\n\t" \
    "v_permlane32_swap_b32 v237, v239\n\t" \
    "s_nop 1\n\t" \
    "v_permlane16_swap_b32 v236, v237\n\t" \
    "v_permlane16_swap_b32 v238, v239\n\t" \
    "buffer_store_dwordx4 v[236:239], v255, %[rs], s42 offen sc1\n\t" \
    "s_add_u32 s42, s42, 0x10000\n\t" \
    "v_add_f32 v228, v228, v230\n\t" \
    "v_add_f32 v229, v229, v231\n\t" \
    "v_add_f32 v224, v224, v226\n\t" \
    "v_add_f32 v225, v225, v227\n\t" \
    "v_cvt_pk_bf16_f32 v229, v228, v229\n\t" \
    "v_cvt_pk_bf16_f32 v228, v224, v225\n\t" \
    "v_min_u32 v229, -2, v229\n\t" \
    "v_min_u32 v228, -2, v228\n\t" \
    "s_nop 1\n\t" \
    "v_permlane16_swap_b32 v228, v229\n\t" \
    "buffer_store_dwordx2 v[228:229], v232, %[rs], s42 offen sc1\n\t" \
    "s_setprio 0\n\t" \
    LST5 \
    LFLUSH200 \
    "s_add_u32 s40, s40, 1\n\t" \
    "s_cmp_lg_u32 s40, 64\n\t" \
    "s_cbranch_scc1 .Lv41_%=\n\t" \
    "s_branch .Lend%=\n\t" \
    ".Lv3_%=:\n\t" \
    "s_mov_b32 s42, %[xs]\n\t" \
    "ds_read_b128 v[224:227], v253 offset:0\n\t" \
    "ds_read_b128 v[244:247], v254 offset:57344\n\t" \
    "ds_read_b128 v[228:231], v253 offset:20480\n\t" \
    "ds_read_b128 v[232:235], v253 offset:1024\n\t" \
    "ds_read_b128 v[236:239], v253 offset:21504\n\t" \
    "ds_read_b128 v[248:251], v254 offset:58368\n\t" \
    ".Lv31_%=:\n\t" \
    LST7 \
    "s_waitcnt lgkmcnt(5)\n\t" \
    "v_mfma_f32_16x16x32_bf16 v[204:207], v[40:43], v[224:227], 0\n\t" \
    "ds_read_b128 v[240:243], v253 offset:2048\n\t" \
    "v_mfma_f32_16x16x32_bf16 v[208:211], v[48:51], v[224:227], 0\n\t" \
    "s_waitcnt lgkmcnt(5)\n\t" \
    "v_mfma_f32_16x16x32_bf16 v[200:203], v[244:247], v[224:227], 0\n\t" \
    "s_waitcnt lgkmcnt(4)\n\t" \
    "v_mfma_f32_16x16x32_bf16 v[216:219], v[40:43], v[228:231], 0\n\t" \
    "ds_read_b128 v[224:227], v253 offset:22528\n\t" \
    "v_mfma_f32_16x16x32_bf16 v[220:223], v[48:51], v[228:231], 0\n\t" \
    "v_mfma_f32_16x16x32_bf16 v[212:215], v[244:247], v[228:231], 0\n\t" \
    "ds_read_b128 v[244:247], v254 offset:59392\n\t" \
    "s_waitcnt lgkmcnt(5)\n\t" \
    "v_mfma_f32_16x16x32_bf16 v[204:207], v[44:47], v[232:235], v[204:207]\n\t" \
    "ds_read_b128 v[228:231], v253 offset:3072\n\t" \
    "v_mfma_f32_16x16x32_bf16 v[208:211], v[52:55], v[232:235], v[208:211]\n\t" \
    "s_waitcnt lgkmcnt(4)\n\t" \
    "v_mfma_f32_16x16x32_bf16 v[200:203], v[248:251], v[232:235], v[200:203]\n\t" \
    "v_mfma_f32_16x16x32_bf16 v[216:219], v[44:47], v[236:239], v[216:219]\n\t" \
    "ds_read_b128 v[232:235], v253 offset:23552\n\t" \
    "v_mfma_f32_16x16x32_bf16 v[220:223], v[52:55], v[236:239], v[220:223]\n\t" \
    "v_mfma_f32_16x16x32_bf16 v[212:215], v[248:251], v[236:239], v[212:215]\n\t" \
    "ds_read_b128 v[248:251], v254 offset:60416\n\t" \
    "s_waitcnt lgkmcnt(5)\n\t" \
    "v_mfma_f32_16x16x32_bf16 v[204:207], v[64:67], v[240:243], v[204:207]\n\t" \
    "ds_read_b128 v[236:239], v253 offset:4096\n\t" \
    "v_mfma_f32_16x16x32_bf16 v[208:211], v[72:75], v[240:243], v[208:211]\n\t" \
    "v_mfma_f32_16x16x32_bf16 v[200:203], v[56:59], v[240:243], v[200:203]\n\t" \
    "s_waitcnt lgkmcnt(5)\n\t" \
    "v_mfma_f32_16x16x32_bf16 v[216:219], v[64:67], v[224:227], v[216:219]\n\t" \
    "ds_read_b128 v[240:243], v253 offset:24576\n\t" \
    "v_mfma_f32_16x16x32_bf16 v[220:223], v[72:75], v[224:227], v[220:223]\n\t" \
    "v_mfma_f32_16x16x32_bf16 v[212:215], v[56:59], v[224:227], v[212:215]\n\t" \
    "s_waitcnt lgkmcnt(4)\n\t" \
    "v_mfma_f32_16x16x32_bf16 v[204:207], v[68:71], v[228:231], v[204:207]\n\t" \
    "ds_read_b128 v[224:227], v253 offset:5120\n\t" \
    "v_mfma_f32_16x16x32_bf16 v[208:211], v[76:79], v[228:231], v[208:211]\n\t" \
    "v_mfma_f32_16x16x32_bf16 v[200:203], v[60:63], v[228:231], v[200:203]\n\t" \
    "s_waitcnt lgkmcnt(4)\n\t" \
    "v_mfma_f32_16x16x32_bf16 v[216:219], v[68:71], v[232:235], v[216:219]\n\t" \
    "ds_read_b128 v[228:231], v253 offset:25600\n\t" \
    "v_mfma_f32_16x16x32_bf16 v[220:223], v[76:79], v[232:235], v[220:223]\n\t" \
    "v_mfma_f32_16x16x32_bf16 v[212:215], v[60:63], v[232:235], v[212:215]\n\t" \
    "s_waitcnt lgkmcnt(3)\n\t" \
    "v_mfma_f32_16x16x32_bf16 v[204:207], v[80:83], v[236:239], v[204:207]\n\t" \
    "ds_read_b128 v[232:235], v253 offset:6144\n\t" \
    "s_add_u32 s44, s40, 1\n\t" \
    "v_mfma_f32_16x16x32_bf16 v[208:211], v[88:91], v[236:239], v[208:211]\n\t" \
    "s_and_b32 s45, s44, 15\n\t" \
    "v_mfma_f32_16x16x32_bf16 v[200:203], v[244:247], v[236:239], v[200:203]\n\t" \
    "s_cmp_eq_u32 s45, 0\n\t" \
    "s_waitcnt lgkmcnt(3)\n\t" \
    "v_mfma_f32_16x16x32_bf16 v[216:219], v[80:83], v[240:243], v[216:219]\n\t" \
    "ds_read_b128 v[236:239], v253 offset:26624\n\t" \
    "s_cselect_b32 s45, %[cj], %[cs]\n\t" \
    "v_mfma_f32_16x16x32_bf16 v[220:223], v[88:91], v[240:243], v[220:223]\n\t" \
    "s_cmp_eq_u32 s44, 64\n\t" \
    "v_mfma_f32_16x16x32_bf16 v[212:215], v[244:247], v[240:243], v[212:215]\n\t" \
    "ds_read_b128 v[244:247], v254 offset:61440\n\t" \
    "s_cselect_b32 s45, 0, s45\n\t" \
    "s_waitcnt lgkmcnt(4)\n\t" \
    "v_mfma_f32_16x16x32_bf16 v[204:207], v[84:87], v[224:227], v[204:207]\n\t" \
    "ds_read_b128 v[240:243], v253 offset:7168\n\t" \
    "s_add_u32 s42, s42, s45\n\t" \
    "v_mfma_f32_16x16x32_bf16 v[208:211], v[92:95], v[224:227], v[208:211]\n\t" \
    "v_mfma_f32_16x16x32_bf16 v[200:203], v[248:251], v[224:227], v[200:203]\n\t" \
    "s_waitcnt lgkmcnt(4)\n\t" \
    "v_mfma_f32_16x16x32_bf16 v[216:219], v[84:87], v[228:231], v[216:219]\n\t" \
    "ds_read_b128 v[224:227], v253 offset:27648\n\t" \
    "v_mfma_f32_16x16x32_bf16 v[220:223], v[92:95], v[228:231], v[220:223]\n\t" \
    "v_mfma_f32_16x16x32_bf16 v[212:215], v[248:251], v[228:231], v[212:215]\n\t" \
    "ds_read_b128 v[248:251], v254 offset:62464\n\t" \
    "s_waitcnt lgkmcnt(5)\n\t" \
    "v_mfma_f32_16x16x32_bf16 v[204:207], v[104:107], v[232:235], v[204:207]\n\t" \
    "ds_read_b128 v[228:231], v253 offset:8192\n\t" \
    "v_mfma_f32_16x16x32_bf16 v[208:211], v[112:115], v[232:235], v[208:211]\n\t" \
    "v_mfma_f32_16x16x32_bf16 v[200:203], v[96:99], v[232:235], v[200:203]\n\t" \
    "s_waitcnt lgkmcnt(5)\n\t" \
    "v_mfma_f32_16x16x32_bf16 v[216:219], v[104:107], v[236:239], v[216:219]\n\t" \
    "ds_read_b128 v[232:235], v253 offset:28672\n\t" \
    "v_mfma_f32_16x16x32_bf16 v[220:223], v[112:115], v[236:239], v[220:223]\n\t" \
    "v_mfma_f32_16x16x32_bf16 v[212:215], v[96:99], v[236:239], v[212:215]\n\t" \
    "s_waitcnt lgkmcnt(4)\n\t" \
    "v_mfma_f32_16x16x32_bf16 v[204:207], v[108:111], v[240:243], v[204:207]\n\t" \
    "ds_read_b128 v[236:239], v253 offset:9216\n\t" \
    "v_mfma_f32_16x16x32_bf16 v[208:211], v[116:119], v[240:243], v[208:211]\n\t" \
    "v_mfma_f32_16x16x32_bf16 v[200:203], v[100:103], v[240:243], v[200:203]\n\t" \
    "s_waitcnt lgkmcnt(4)\n\t" \
    "v_mfma_f32_16x16x32_bf16 v[216:219], v[108:111], v[224:227], v[216:219]\n\t" \
    "ds_read_b128 v[240:243], v253 offset:29696\n\t" \
    "v_mfma_f32_16x16x32_bf16 v[220:223], v[116:119], v[224:227], v[220:223]\n\t" \
    "v_mfma_f32_16x16x32_bf16 v[212:215], v[100:103], v[224:227], v[212:215]\n\t" \
    "s_waitcnt lgkmcnt(3)\n\t" \
    "v_mfma_f32_16x16x32_bf16 v[204:207], v[120:123], v[228:231], v[204:207]\n\t" \
    "ds_read_b128 v[224:227], v253 offset:12288\n\t" \
    "v_mfma_f32_16x16x32_bf16 v[208:211], v[128:131], v[228:231], v[208:211]\n\t" \
    "v_mfma_f32_16x16x32_bf16 v[200:203], v[244:247], v[228:231], v[200:203]\n\t" \
    "s_waitcnt lgkmcnt(3)\n\t" \
    "v_mfma_f32_16x16x32_bf16 v[216:219], v[120:123], v[232:235], v[216:219]\n\t" \
    "ds_read_b128 v[228:231], v253 offset:32768\n\t" \
    "v_mfma_f32_16x16x32_bf16 v[220:223], v[128:131], v[232:235], v[220:223]\n\t" \
    "v_mfma_f32_16x16x32_bf16 v[212:215], v[244:247], v[232:235], v[212:215]\n\t" \
    "ds_read_b128 v[244:247], v254 offset:63488\n\t" \
    "s_waitcnt lgkmcnt(4)\n\t" \
    "v_mfma_f32_16x16x32_bf16 v[204:207], v[124:127], v[236:239], v[204:207]\n\t" \
    "ds_read_b128 v[232:235], v253 offset:13312\n\t" \
    "v_mfma_f32_16x16x32_bf16 v[208:211], v[132:135], v[236:239], v[208:211]\n\t" \
    "v_mfma_f32_16x16x32_bf16 v[200:203], v[248:251], v[236:239], v[200:203]\n\t" \
    "s_waitcnt lgkmcnt(4)\n\t" \
    "v_mfma_f32_16x16x32_bf16 v[216:219], v[124:127], v[240:243], v[216:219]\n\t" \
    "ds_read_b128 v[236:239], v253 offset:33792\n\t" \
    "v_mfma_f32_16x16x32_bf16 v[220:223], v[132:135], v[240:243], v[220:223]\n\t" \
    "v_mfma_f32_16x16x32_bf16 v[212:215], v[248:251], v[240:243], v[212:215]\n\t" \
    "ds_read_b128 v[248:251], v254 offset:64512\n\t" \
    "s_waitcnt lgkmcnt(5)\n\t" \
    "v_mfma_f32_16x16x32_bf16 v[204:207], v[160:163], v[224:227], v[204:207]\n\t" \
    "ds_read_b128 v[240:243], v253 offset:10240\n\t" \
    "s_and_b32 s44, s40, 3\n\t" \
    "v_mfma_f32_16x16x32_bf16 v[208:211], v[168:171], v[224:227], v[208:211]\n\t" \
    "s_lshl_b32 s44, s44, 17\n\t" \
    "s_waitcnt lgkmcnt(4)\n\t" \
    "v_mfma_f32_16x16x32_bf16 v[200:203], v[244:247], v[224:227], v[200:203]\n\t" \
    "s_add_u32 s44, s44, %[mb]\n\t" \
    "v_mfma_f32_16x16x32_bf16 v[216:219], v[160:163], v[228:231], v[216:219]\n\t" \
    "ds_read_b128 v[224:227], v253 offset:30720\n\t" \
    "s_add_u32 s46, s44, 0x4000\n\t" \
    "v_mfma_f32_16x16x32_bf16 v[220:223], v[168:171], v[228:231], v[220:223]\n\t" \
    "s_add_u32 s47, s44, 0x4400\n\t" \
    "v_mfma_f32_16x16x32_bf16 v[212:215], v[244:247], v[228:231], v[212:215]\n\t" \
    "ds_read_b128 v[244:247], v254 offset:55296\n\t" \
    "s_waitcnt lgkmcnt(5)\n\t" \
    "v_mfma_f32_16x16x32_bf16 v[204:207], v[164:167], v[232:235], v[204:207]\n\t" \
    "ds_read_b128 v[228:231], v253 offset:11264\n\t" \
    "v_mfma_f32_16x16x32_bf16 v[208:211], v[172:175], v[232:235], v[208:211]\n\t" \
    "s_waitcnt lgkmcnt(4)\n\t" \
    "v_mfma_f32_16x16x32_bf16 v[200:203], v[248:251], v[232:235], v[200:203]\n\t" \
    "v_mfma_f32_16x16x32_bf16 v[216:219], v[164:167], v[236:239], v[216:219]\n\t" \
    "ds_read_b128 v[232:235], v253 offset:31744\n\t" \
    "v_mfma_f32_16x16x32_bf16 v[220:223], v[172:175], v[236:239], v[220:223]\n\t" \
    "v_mfma_f32_16x16x32_bf16 v[212:215], v[248:251], v[236:239], v[212:215]\n\t" \
    "ds_read_b128 v[248:251], v254 offset:56320\n\t" \
    "s_waitcnt lgkmcnt(5)\n\t" \
    "v_mfma_f32_16x16x32_bf16 v[204:207], v[144:147], v[240:243], v[204:207]\n\t" \
    "ds_read_b128 v[236:239], v253 offset:16384\n\t" \
    "v_mfma_f32_16x16x32_bf16 v[208:211], v[152:155], v[240:243], v[208:211]\n\t" \
    "v_mfma_f32_16x16x32_bf16 v[200:203], v[136:139], v[240:243], v[200:203]\n\t" \
    "s_waitcnt lgkmcnt(5)\n\t" \
    "v_mfma_f32_16x16x32_bf16 v[216:219], v[144:147], v[224:227], v[216:219]\n\t" \
    "ds_read_b128 v[240:243], v253 offset:36864\n\t" \
    "v_mfma_f32_16x16x32_bf16 v[220:223], v[152:155], v[224:227], v[220:223]\n\t" \
    "v_mfma_f32_16x16x32_bf16 v[212:215], v[136:139], v[224:227], v[212:215]\n\t" \
    "s_waitcnt lgkmcnt(4)\n\t" \
    "v_mfma_f32_16x16x32_bf16 v[204:207], v[148:151], v[228:231], v[204:207]\n\t" \
    "ds_read_b128 v[224:227], v253 offset:17408\n\t" \
    "v_mfma_f32_16x16x32_bf16 v[208:211], v[156:159], v[228:231], v[208:211]\n\t" \
    "v_mfma_f32_16x16x32_bf16 v[200:203], v[140:143], v[228:231], v[200:203]\n\t" \
    "s_waitcnt lgkmcnt(4)\n\t" \
    "v_mfma_f32_16x16x32_bf16 v[216:219], v[148:151], v[232:235], v[216:219]\n\t" \
    "ds_read_b128 v[228:231], v253 offset:37888\n\t" \
    "v_mfma_f32_16x16x32_bf16 v[220:223], v[156:159], v[232:235], v[220:223]\n\t" \
    "v_mfma_f32_16x16x32_bf16 v[212:215], v[140:143], v[232:235], v[212:215]\n\t" \
    "s_cmp_eq_u32 %[fl], 0\n\t" \
    "s_cbranch_scc1 .Lv32_%=\n\t" \
    "v_and_b32 v252, 0xf0, v253\n\t" \
    "v_lshlrev_b32 v252, 3, v252\n\t" \
    "buffer_load_dword v252, v252, %[rs], %[fl] offen sc1\n\t" \
    ".Lv32_%=:\n\t" \
    "s_waitcnt lgkmcnt(3)\n\t" \
    "v_mfma_f32_16x16x32_bf16 v[204:207], v[0:3], v[236:239], v[204:207]\n\t" \
    "ds_read_b128 v[232:235], v253 offset:14336\n\t" \
    "v_mfma_f32_16x16x32_bf16 v[208:211], v[8:11], v[236:239], v[208:211]\n\t" \
    "v_mfma_f32_16x16x32_bf16 v[200:203], v[244:247], v[236:239], v[200:203]\n\t" \
    "s_waitcnt lgkmcnt(3)\n\t" \
    "v_mfma_f32_16x16x32_bf16 v[216:219], v[0:3], v[240:243], v[216:219]\n\t" \
    "ds_read_b128 v[236:239], v253 offset:15360\n\t" \
    "v_mfma_f32_16x16x32_bf16 v[220:223], v[8:11], v[240:243], v[220:223]\n\t" \
    "v_mfma_f32_16x16x32_bf16 v[212:215], v[244:247], v[240:243], v[212:215]\n\t" \
    "s_waitcnt lgkmcnt(3)\n\t" \
    "v_mfma_f32_16x16x32_bf16 v[204:207], v[4:7], v[224:227], v[204:207]\n\t" \
    "ds_read_b128 v[240:243], v253 offset:18432\n\t" \
    "v_mfma_f32_16x16x32_bf16 v[208:211], v[12:15], v[224:227], v[208:211]\n\t" \
    "v_mfma_f32_16x16x32_bf16 v[200:203], v[248:251], v[224:227], v[200:203]\n\t" \
    "s_waitcnt lgkmcnt(3)\n\t" \
    "v_mfma_f32_16x16x32_bf16 v[216:219], v[4:7], v[228:231], v[216:219]\n\t" \
    "ds_read_b128 v[224:227], v253 offset:19456\n\t" \
    "v_mfma_f32_16x16x32_bf16 v[220:223], v[12:15], v[228:231], v[220:223]\n\t" \
    "v_mfma_f32_16x16x32_bf16 v[212:215], v[248:251], v[228:231], v[212:215]\n\t" \
    "s_waitcnt lgkmcnt(3)\n\t" \
    "v_mfma_f32_16x16x32_bf16 v[204:207], v[184:187], v[232:235], v[204:207]\n\t" \
    "ds_read_b128 v[228:231], v253 offset:34816\n\t" \
    "v_mov_b64 v[244:245], 0\n\t" \
    "v_mfma_f32_16x16x32_bf16 v[208:211], v[192:195], v[232:235], v[208:211]\n\t" \
    "v_mov_b64 v[246:247], 0\n\t" \
    "v_mfma_f32_16x16x32_bf16 v[200:203], v[176:179], v[232:235], v[200:203]\n\t" \
    "v_mov_b64 v[248:249], 0\n\t" \
    "s_waitcnt lgkmcnt(3)\n\t" \
    "v_mfma_f32_16x16x32_bf16 v[204:207], v[188:191], v[236:239], v[204:207]\n\t" \
    "ds_read_b128 v[232:235], v253 offset:35840\n\t" \
    "v_mov_b64 v[250:251], 0\n\t" \
    "v_mfma_f32_16x16x32_bf16 v[208:211], v[196:199], v[236:239], v[208:211]\n\t" \
    "v_mfma_f32_16x16x32_bf16 v[200:203], v[180:183], v[236:239], v[200:203]\n\t" \
    "s_waitcnt lgkmcnt(3)\n\t" \
    "v_mfma_f32_16x16x32_bf16 v[204:207], v[24:27], v[240:243], v[204:207]\n\t" \
    "ds_read_b128 v[236:239], v253 offset:38912\n\t" \
    "v_mfma_f32_16x16x32_bf16 v[208:211], v[32:35], v[240:243], v[208:211]\n\t" \
    "v_mfma_f32_16x16x32_bf16 v[200:203], v[16:19], v[240:243], v[200:203]\n\t" \
    "s_waitcnt lgkmcnt(3)\n\t" \
    "v_mfma_f32_16x16x32_bf16 v[204:207], v[28:31], v[224:227], v[204:207]\n\t" \
    "ds_read_b128 v[240:243], v253 offset:39936\n\t" \
    "v_mfma_f32_16x16x32_bf16 v[208:211], v[36:39], v[224:227], v[208:211]\n\t" \
    "v_mfma_f32_16x16x32_bf16 v[200:203], v[20:23], v[224:227], v[200:203]\n\t" \
    "s_waitcnt lgkmcnt(3)\n\t" \
    "v_mfma_f32_16x16x32_bf16 v[216:219], v[184:187], v[228:231], v[216:219]\n\t" \
    "s_waitcnt lgkmcnt(2)\n\t" \
    "v_mfma_f32_16x16x32_bf16 v[216:219], v[188:191], v[232:235], v[216:219]\n\t" \
    "v_cvt_pk_bf16_f32 v204, v204, v205\n\t" \
    "v_cvt_pk_bf16_f32 v205, v206, v207\n\t" \
    "s_waitcnt lgkmcnt(1)\n\t" \
    "v_mfma_f32_16x16x32_bf16 v[216:219], v[24:27], v[236:239], v[216:219]\n\t" \
    "ds_write_b64 v254, v[204:205] offset:4104\n\t" \
    "v_cvt_pk_bf16_f32 v208, v208, v209\n\t" \
    "s_waitcnt lgkmcnt(1)\n\t" \
    "v_mfma_f32_16x16x32_bf16 v[216:219], v[28:31], v[240:243], v[216:219]\n\t" \
    "v_cvt_pk_bf16_f32 v209, v210, v211\n\t" \
    "ds_write_b64 v254, v[208:209] offset:5128\n\t" \
    "v_mfma_f32_16x16x32_bf16 v[220:223], v[192:195], v[228:231], v[220:223]\n\t" \
    "v_cvt_pk_bf16_f32 v200, v200, v201\n\t" \
    "v_cvt_pk_bf16_f32 v201, v202, v203\n\t" \
    "v_mfma_f32_16x16x32_bf16 v[220:223], v[196:199], v[232:235], v[220:223]\n\t" \
    "ds_write_b64 v254, v[200:201] offset:3080\n\t" \
    "ds_read_b128 v[200:203], v254 offset:55296\n\t" \
    "v_mfma_f32_16x16x32_bf16 v[220:223], v[32:35], v[236:239], v[220:223]\n\t" \
    "ds_read_b128 v[204:207], v254 offset:56320\n\t" \
    "v_mov_b64 v[208:209], 0\n\t" \
    "v_mfma_f32_16x16x32_bf16 v[220:223], v[36:39], v[240:243], v[220:223]\n\t" \
    "v_mov_b64 v[210:211], 0\n\t" \
    "v_cvt_pk_bf16_f32 v216, v216, v217\n\t" \
    "v_mfma_f32_16x16x32_bf16 v[212:215], v[176:179], v[228:231], v[212:215]\n\t" \
    "v_cvt_pk_bf16_f32 v217, v218, v219\n\t" \
    "ds_write_b64 v254, v[216:217] offset:10248\n\t" \
    "v_mfma_f32_16x16x32_bf16 v[212:215], v[180:183], v[232:235], v[212:215]\n\t" \
    "v_mfma_f32_16x16x32_bf16 v[212:215], v[16:19], v[236:239], v[212:215]\n\t" \
    "v_cvt_pk_bf16_f32 v220, v220, v221\n\t" \
    "v_cvt_pk_bf16_f32 v221, v222, v223\n\t" \
    "v_mfma_f32_16x16x32_bf16 v[212:215], v[20:23], v[240:243], v[212:215]\n\t" \
    "ds_write_b64 v254, v[220:221] offset:11272\n\t" \
    "ds_read_b128 v[216:219], v254 offset:57344\n\t" \
    "ds_read_b128 v[220:223], v254 offset:58368\n\t" \
    "s_nop 4\n\t" \
    "v_cvt_pk_bf16_f32 v212, v212, v213\n\t" \
    "v_cvt_pk_bf16_f32 v213, v214, v215\n\t" \
    "ds_write_b64 v254, v[212:213] offset:9224\n\t" \
    LST6 \
    "s_waitcnt vmcnt(0) lgkmcnt(0)\n\t" \
    "s_barrier \n\t" \
    "s_setprio 2\n\t" \
    "s_cmp_eq_u32 %[fl], 0\n\t" \
    "s_cbranch_scc1 .Lv34_%=\n\t" \
    "s_add_u32 s44, s40, 1\n\t" \
    "s_min_u32 s44, s44, 63\n\t" \
    ".Lv33_%=:\n\t" \
    "v_cmp_ge_u32 vcc, s44, v252\n\t" \
    "s_cbranch_vccz .Lv34_%=\n\t" \
    "s_cmp_eq_u32 s41, 0\n\t" \
    "s_cbranch_scc1 .Lv34_%=\n\t" \
    "s_sub_u32 s41, s41, 1\n\t" \
    "s_sleep 2\n\t" \
    "v_and_b32 v252, 0xf0, v253\n\t" \
    "v_lshlrev_b32 v252, 3, v252\n\t" \
    "buffer_load_dword v252, v252, %[rs], %[fl] offen sc1\n\t" \
    "s_waitcnt vmcnt(0)\n\t" \
    "s_branch .Lv33_%=\n\t" \
    ".Lv34_%=:\n\t" \
    "s_movk_i32 s45, 0x3000\n\t" \
    "s_mov_b32 m0, s45\n\t" \
    "s_nop 0\n\t" \
    "buffer_load_dwordx4 v253, %[rs], s42 offen offset:0 sc1 lds\n\t" \
    "buffer_load_dwordx4 v253, %[rs], s42 offen offset:1024 sc1 lds\n\t" \
    "buffer_load_dwordx4 v253, %[rs], s42 offen offset:2048 sc1 lds\n\t" \
    "buffer_load_dwordx4 v253, %[rs], s42 offen offset:3072 sc1 lds\n\t" \
    "s_add_u32 s44, s42, 0x1000\n\t" \
    "s_mov_b32 s45, 0x8000\n\t" \
    "s_mov_b32 m0, s45\n\t" \
    "s_nop 0\n\t" \
    "buffer_load_dwordx4 v253, %[rs], s44 offen offset:0 sc1 lds\n\t" \
    "buffer_load_dwordx4 v253, %[rs], s44 offen offset:1024 sc1 lds\n\t" \
    "buffer_load_dwordx4 v253, %[rs], s44 offen offset:2048 sc1 lds\n\t" \
    "buffer_load_dwordx4 v253, %[rs], s44 offen offset:3072 sc1 lds\n\t" \
    "s_add_u32 s44, s42, 0x4c00\n\t" \
    "s_mov_b32 s45, 0x4c00\n\t" \
    "s_mov_b32 m0, s45\n\t" \
    "s_nop 0\n\t" \
    "buffer_load_dwordx4 v253, %[rs], s44 offen offset:0 sc1 lds\n\t" \
    "s_add_u32 s44, s44, 0x1000\n\t" \
    "s_mov_b32 s45, 0x9c00\n\t" \
    "s_mov_b32 m0, s45\n\t" \
    "s_nop 0\n\t" \
    "buffer_load_dwordx4 v253, %[rs], s44 offen offset:0 sc1 lds\n\t" \
    LST1 \
    "buffer_load_dwordx4 v[224:227], v253, %[rs], s46 offen sc1\n\t" \
    "buffer_load_dwordx4 v[228:231], v253, %[rs], s47 offen sc1\n\t" \
    "s_waitcnt lgkmcnt(0)\n\t" \
    "v_bfe_i32 v212, v253, 5, 1\n\t" \
    "v_and_b32 v212, 0xaaaa, v212\n\t" \
    "v_or_b32 v212, 0x4444, v212\n\t" \
    ".Lv35_%=:\n\t" \
    "s_waitcnt vmcnt(1)\n\t" \
    "v_max_u32 v214, v224, v225\n\t" \
    "v_max3_u32 v214, v226, v227, v214\n\t" \
    "s_waitcnt vmcnt(0)\n\t" \
    "v_max3_u32 v214, v228, v229, v214\n\t" \
    "v_max3_u32 v214, v230, v231, v214\n\t" \
    "v_cmp_eq_u32 vcc, -1, v214\n\t" \
    "s_cbranch_vccz .Lv36_%=\n\t" \
    "v_max_u32 v214, v224, v225\n\t" \
    "v_max3_u32 v214, v226, v227, v214\n\t" \
    "v_cmp_eq_u32 vcc, -1, v214\n\t" \
    "s_cbranch_vccz .Lv37_%=\n\t" \
    "buffer_load_dwordx4 v[224:227], v253, %[rs], s46 offen sc1\n\t" \
    ".Lv37_%=:\n\t" \
    "v_max_u32 v214, v228, v229\n\t" \
    "v_max3_u32 v214, v230, v231, v214\n\t" \
    "v_cmp_eq_u32 vcc, -1, v214\n\t" \
    "s_cbranch_vccz .Lv38_%=\n\t" \
    "buffer_load_dwordx4 v[228:231], v253, %[rs], s47 offen sc1\n\t" \
    ".Lv38_%=:\n\t" \
    "s_cmp_eq_u32 s41, 0\n\t" \
    "s_cbranch_scc1 .Lv36_%=\n\t" \
    "s_sub_u32 s41, s41, 1\n\t" \
    "s_waitcnt vmcnt(0)\n\t" \
    "s_branch .Lv35_%=\n\t" \
    ".Lv36_%=:\n\t" \
    LST2 \
    "ds_write_b128 v253, v[224:227] offset:49152\n\t" \
    "ds_write_b128 v253, v[228:231] offset:50176\n\t" \
    "v_smfmac_f32_16x16x64_bf16 v[244:247], v[224:227], v[0:7], v212\n\t" \
    "v_smfmac_f32_16x16x64_bf16 v[248:251], v[224:227], v[8:15], v212\n\t" \
    "v_smfmac_f32_16x16x64_bf16 v[208:211], v[224:227], v[200:207], v212\n\t" \
    "s_waitcnt lgkmcnt(0)\n\t" \
    "s_barrier \n\t" \
    LST3 \
    "ds_read_b128 v[232:235], v253 offset:40960\n\t" \
    "ds_read_b128 v[236:239], v253 offset:41984\n\t" \
    "ds_read_b128 v[240:243], v253 offset:43008\n\t" \
    "ds_read_b128 v[224:227], v253 offset:44032\n\t" \
    "ds_read_b128 v[200:203], v254 offset:59392\n\t" \
    "ds_read_b128 v[204:207], v254 offset:60416\n\t" \
    "v_smfmac_f32_16x16x64_bf16 v[208:211], v[228:231], v[16:23], v212\n\t" \
    "v_smfmac_f32_16x16x64_bf16 v[244:247], v[228:231], v[24:31], v212\n\t" \
    "v_smfmac_f32_16x16x64_bf16 v[248:251], v[228:231], v[32:39], v212\n\t" \
    "ds_read_b128 v[228:231], v253 offset:45056\n\t" \
    "s_waitcnt lgkmcnt(6)\n\t" \
    "v_smfmac_f32_16x16x64_bf16 v[208:211], v[232:235], v[216:223], v212\n\t" \
    "ds_read_b128 v[216:219], v254 offset:61440\n\t" \
    "ds_read_b128 v[220:223], v254 offset:62464\n\t" \
    "v_smfmac_f32_16x16x64_bf16 v[244:247], v[232:235], v[40:47], v212\n\t" \
    "v_smfmac_f32_16x16x64_bf16 v[248:251], v[232:235], v[48:55], v212\n\t" \
    "ds_read_b128 v[232:235], v253 offset:46080\n\t" \
    "s_waitcnt lgkmcnt(8)\n\t" \
    "v_smfmac_f32_16x16x64_bf16 v[208:211], v[236:239], v[56:63], v212\n\t" \
    "v_smfmac_f32_16x16x64_bf16 v[244:247], v[236:239], v[64:71], v212\n\t" \
    "v_smfmac_f32_16x16x64_bf16 v[248:251], v[236:239], v[72:79], v212\n\t" \
    "ds_read_b128 v[236:239], v253 offset:47104\n\t" \
    "s_waitcnt lgkmcnt(5)\n\t" \
    "v_smfmac_f32_16x16x64_bf16 v[208:211], v[240:243], v[200:207], v212\n\t" \
    "ds_read_b128 v[200:203], v254 offset:63488\n\t" \
    "ds_read_b128 v[204:207], v254 offset:64512\n\t" \
    "v_smfmac_f32_16x16x64_bf16 v[244:247], v[240:243], v[80:87], v212\n\t" \
    "v_smfmac_f32_16x16x64_bf16 v[248:251], v[240:243], v[88:95], v212\n\t" \
    "ds_read_b128 v[240:243], v253 offset:48128\n\t" \
    "v_smfmac_f32_16x16x64_bf16 v[244:247], v[224:227], v[104:111], v212\n\t" \
    "s_waitcnt lgkmcnt(7)\n\t" \
    "v_smfmac_f32_16x16x64_bf16 v[244:247], v[228:231], v[120:127], v212\n\t" \
    "s_waitcnt lgkmcnt(4)\n\t" \
    "v_smfmac_f32_16x16x64_bf16 v[244:247], v[232:235], v[144:151], v212\n\t" \
    "s_waitcnt lgkmcnt(3)\n\t" \
    "v_smfmac_f32_16x16x64_bf16 v[244:247], v[236:239], v[160:167], v212\n\t" \
    "s_waitcnt lgkmcnt(0)\n\t" \
    "v_smfmac_f32_16x16x64_bf16 v[244:247], v[240:243], v[184:191], v212\n\t" \
    "v_smfmac_f32_16x16x64_bf16 v[248:251], v[224:227], v[112:119], v212\n\t" \
    "v_smfmac_f32_16x16x64_bf16 v[248:251], v[228:231], v[128:135], v212\n\t" \
    "v_smfmac_f32_16x16x64_bf16 v[248:251], v[232:235], v[152:159], v212\n\t" \
    "v_add_f32 v244, v244, v246\n\t" \
    "v_add_f32 v245, v245, v247\n\t" \
    "v_smfmac_f32_16x16x64_bf16 v[248:251], v[236:239], v[168:175], v212\n\t" \
    "v_cvt_pk_bf16_f32 v244, v244, v245\n\t" \
    "ds_write_b16 v255, v244 offset:512\n\t" \
    "v_smfmac_f32_16x16x64_bf16 v[248:251], v[240:243], v[192:199], v212\n\t" \
    "ds_write_b16_d16_hi v255, v244 offset:528\n\t" \
    "v_smfmac_f32_16x16x64_bf16 v[208:211], v[224:227], v[96:103], v212\n\t" \
    "v_smfmac_f32_16x16x64_bf16 v[208:211], v[228:231], v[216:223], v212\n\t" \
    "v_smfmac_f32_16x16x64_bf16 v[208:211], v[232:235], v[136:143], v212\n\t" \
    "v_add_f32 v248, v248, v250\n\t" \
    "v_add_f32 v249, v249, v251\n\t" \
    "v_smfmac_f32_16x16x64_bf16 v[208:211], v[236:239], v[200:207], v212\n\t" \
    "v_cvt_pk_bf16_f32 v248, v248, v249\n\t" \
    "ds_write_b16 v255, v248 offset:1024\n\t" \
    "v_smfmac_f32_16x16x64_bf16 v[208:211], v[240:243], v[176:183], v212\n\t" \
    "ds_write_b16_d16_hi v255, v248 offset:1040\n\t" \
    "ds_read_b128 v[224:227], v253 offset:0\n\t" \
    "ds_read_b128 v[244:247], v254 offset:57344\n\t" \
    "ds_read_b128 v[228:231], v253 offset:20480\n\t" \
    "ds_read_b128 v[232:235], v253 offset:1024\n\t" \
    "ds_read_b128 v[236:239], v253 offset:21504\n\t" \
    "ds_read_b128 v[248:251], v254 offset:58368\n\t" \
    "s_nop 0\n\t" \
    "v_add_f32 v208, v208, v210\n\t" \
    "v_add_f32 v209, v209, v211\n\t" \
    "v_cvt_pk_bf16_f32 v208, v208, v209\n\t" \
    "ds_write_b16 v255, v208 offset:0\n\t" \
    "ds_write_b16_d16_hi v255, v208 offset:16\n\t" \
    "s_setprio 0\n\t" \
    "s_waitcnt lgkmcnt(0)\n\t" \
    "s_barrier \n\t" \
    LST4 \
    LFLUSH200 \
    "s_add_u32 s40, s40, 1\n\t" \
    "s_cmp_lg_u32 s40, 64\n\t" \
    "s_cbranch_scc1 .Lv31_%=\n\t" \
    "s_branch .Lend%=\n\t" \
    ".Lv2_%=:\n\t" \
    "s_mov_b32 s42, %[xs]\n\t" \
    "ds_read_b128 v[224:227], v253 offset:0\n\t" \
    "ds_read_b128 v[244:247], v254 offset:53248\n\t" \
    "ds_read_b128 v[228:231], v253 offset:20480\n\t" \
    "ds_read_b128 v[232:235], v253 offset:1024\n\t" \
    "ds_read_b128 v[236:239], v253 offset:21504\n\t" \
    "ds_read_b128 v[248:251], v254 offset:54272\n\t" \
    ".Lv21_%=:\n\t" \
    LST7 \
    "s_waitcnt lgkmcnt(5)\n\t" \
    "v_mfma_f32_16x16x32_bf16 v[204:207], v[40:43], v[224:227], 0\n\t" \
    "ds_read_b128 v[240:243], v253 offset:2048\n\t" \
    "v_mfma_f32_16x16x32_bf16 v[208:211], v[48:51], v[224:227], 0\n\t" \
    "s_waitcnt lgkmcnt(5)\n\t" \
    "v_mfma_f32_16x16x32_bf16 v[200:203], v[244:247], v[224:227], 0\n\t" \
    "s_waitcnt lgkmcnt(4)\n\t" \
    "v_mfma_f32_16x16x32_bf16 v[216:219], v[40:43], v[228:231], 0\n\t" \
    "ds_read_b128 v[224:227], v253 offset:22528\n\t" \
    "v_mfma_f32_16x16x32_bf16 v[220:223], v[48:51], v[228:231], 0\n\t" \
    "v_mfma_f32_16x16x32_bf16 v[212:215], v[244:247], v[228:231], 0\n\t" \
    "ds_read_b128 v[244:247], v254 offset:55296\n\t" \
    "s_waitcnt lgkmcnt(5)\n\t" \
    "v_mfma_f32_16x16x32_bf16 v[204:207], v[44:47], v[232:235], v[204:207]\n\t" \
    "ds_read_b128 v[228:231], v253 offset:3072\n\t" \
    "v_mfma_f32_16x16x32_bf16 v[208:211], v[52:55], v[232:235], v[208:211]\n\t" \
    "s_waitcnt lgkmcnt(4)\n\t" \
    "v_mfma_f32_16x16x32_bf16 v[200:203], v[248:251], v[232:235], v[200:203]\n\t" \
    "v_mfma_f32_16x16x32_bf16 v[216:219], v[44:47], v[236:239], v[216:219]\n\t" \
    "ds_read_b128 v[232:235], v253 offset:23552\n\t" \
    "v_mfma_f32_16x16x32_bf16 v[220:223], v[52:55], v[236:239], v[220:223]\n\t" \
    "v_mfma_f32_16x16x32_bf16 v[212:215], v[248:251], v[236:239], v[212:215]\n\t" \
    "ds_read_b128 v[248:251], v254 offset:56320\n\t" \
    "s_waitcnt lgkmcnt(5)\n\t" \
    "v_mfma_f32_16x16x32_bf16 v[204:207], v[64:67], v[240:243], v[204:207]\n\t" \
    "ds_read_b128 v[236:239], v253 offset:4096\n\t" \
    "v_mfma_f32_16x16x32_bf16 v[208:211], v[72:75], v[240:243], v[208:211]\n\t" \
    "v_mfma_f32_16x16x32_bf16 v[200:203], v[56:59], v[240:243], v[200:203]\n\t" \
    "s_waitcnt lgkmcnt(5)\n\t" \
    "v_mfma_f32_16x16x32_bf16 v[216:219], v[64:67], v[224:227], v[216:219]\n\t" \
    "ds_read_b128 v[240:243], v253 offset:24576\n\t" \
    "v_mfma_f32_16x16x32_bf16 v[220:223], v[72:75], v[224:227], v[220:223]\n\t" \
    "v_mfma_f32_16x16x32_bf16 v[212:215], v[56:59], v[224:227], v[212:215]\n\t" \
    "s_waitcnt lgkmcnt(4)\n\t" \
    "v_mfma_f32_16x16x32_bf16 v[204:207], v[68:71], v[228:231], v[204:207]\n\t" \
    "ds_read_b128 v[224:227], v253 offset:5120\n\t" \
    "v_mfma_f32_16x16x32_bf16 v[208:211], v[76:79], v[228:231], v[208:211]\n\t" \
    "v_mfma_f32_16x16x32_bf16 v[200:203], v[60:63], v[228:231], v[200:203]\n\t" \
    "s_waitcnt lgkmcnt(4)\n\t" \
    "v_mfma_f32_16x16x32_bf16 v[216:219], v[68:71], v[232:235], v[216:219]\n\t" \
    "ds_read_b128 v[228:231], v253 offset:25600\n\t" \
    "v_mfma_f32_16x16x32_bf16 v[220:223], v[76:79], v[232:235], v[220:223]\n\t" \
    "v_mfma_f32_16x16x32_bf16 v[212:215], v[60:63], v[232:235], v[212:215]\n\t" \
    "s_waitcnt lgkmcnt(3)\n\t" \
    "v_mfma_f32_16x16x32_bf16 v[204:207], v[80:83], v[236:239], v[204:207]\n\t" \
    "ds_read_b128 v[232:235], v253 offset:6144\n\t" \
    "s_add_u32 s44, s40, 1\n\t" \
    "v_mfma_f32_16x16x32_bf16 v[208:211], v[88:91], v[236:239], v[208:211]\n\t" \
    "s_and_b32 s45, s44, 15\n\t" \
    "v_mfma_f32_16x16x32_bf16 v[200:203], v[244:247], v[236:239], v[200:203]\n\t" \
    "s_cmp_eq_u32 s45, 0\n\t" \
    "s_waitcnt lgkmcnt(3)\n\t" \
    "v_mfma_f32_16x16x32_bf16 v[216:219], v[80:83], v[240:243], v[216:219]\n\t" \
    "ds_read_b128 v[236:239], v253 offset:26624\n\t" \
    "s_cselect_b32 s45, %[cj], %[cs]\n\t" \
    "v_mfma_f32_16x16x32_bf16 v[220:223], v[88:91], v[240:243], v[220:223]\n\t" \
    "s_cmp_eq_u32 s44, 64\n\t" \
    "v_mfma_f32_16x16x32_bf16 v[212:215], v[244:247], v[240:243], v[212:215]\n\t" \
    "ds_read_b128 v[244:247], v254 offset:51200\n\t" \
    "s_cselect_b32 s45, 0, s45\n\t" \
    "s_waitcnt lgkmcnt(4)\n\t" \
    "v_mfma_f32_16x16x32_bf16 v[204:207], v[84:87], v[224:227], v[204:207]\n\t" \
    "ds_read_b128 v[240:243], v253 offset:7168\n\t" \
    "s_add_u32 s42, s42, s45\n\t" \
    "v_mfma_f32_16x16x32_bf16 v[208:211], v[92:95], v[224:227], v[208:211]\n\t" \
    "v_mfma_f32_16x16x32_bf16 v[200:203], v[248:251], v[224:227], v[200:203]\n\t" \
    "s_waitcnt lgkmcnt(4)\n\t" \
    "v_mfma_f32_16x16x32_bf16 v[216:219], v[84:87], v[228:231], v[216:219]\n\t" \
    "ds_read_b128 v[224:227], v253 offset:27648\n\t" \
    "v_mfma_f32_16x16x32_bf16 v[220:223], v[92:95], v[228:231], v[220:223]\n\t" \
    "v_mfma_f32_16x16x32_bf16 v[212:215], v[248:251], v[228:231], v[212:215]\n\t" \
    "ds_read_b128 v[248:251], v254 offset:52224\n\t" \
    "s_waitcnt lgkmcnt(5)\n\t" \
    "v_mfma_f32_16x16x32_bf16 v[204:207], v[104:107], v[232:235], v[204:207]\n\t" \
    "ds_read_b128 v[228:231], v253 offset:8192\n\t" \
    "v_mfma_f32_16x16x32_bf16 v[208:211], v[112:115], v[232:235], v[208:211]\n\t" \
    "v_mfma_f32_16x16x32_bf16 v[200:203], v[96:99], v[232:235], v[200:203]\n\t" \
    "s_waitcnt lgkmcnt(5)\n\t" \
    "v_mfma_f32_16x16x32_bf16 v[216:219], v[104:107], v[236:239], v[216:219]\n\t" \
    "ds_read_b128 v[232:235], v253 offset:28672\n\t" \
    "v_mfma_f32_16x16x32_bf16 v[220:223], v[112:115], v[236:239], v[220:223]\n\t" \
    "v_mfma_f32_16x16x32_bf16 v[212:215], v[96:99], v[236:239], v[212:215]\n\t" \
    "s_waitcnt lgkmcnt(4)\n\t" \
    "v_mfma_f32_16x16x32_bf16 v[204:207], v[108:111], v[240:243], v[204:207]\n\t" \
    "ds_read_b128 v[236:239], v253 offset:9216\n\t" \
    "v_mfma_f32_16x16x32_bf16 v[208:211], v[116:119], v[240:243], v[208:211]\n\t" \
    "v_mfma_f32_16x16x32_bf16 v[200:203], v[100:103], v[240:243], v[200:203]\n\t" \
    "s_waitcnt lgkmcnt(4)\n\t" \
    "v_mfma_f32_16x16x32_bf16 v[216:219], v[108:111], v[224:227], v[216:219]\n\t" \
    "ds_read_b128 v[240:243], v253 offset:29696\n\t" \
    "v_mfma_f32_16x16x32_bf16 v[220:223], v[116:119], v[224:227], v[220:223]\n\t" \
    "v_mfma_f32_16x16x32_bf16 v[212:215], v[100:103], v[224:227], v[212:215]\n\t" \
    "s_waitcnt lgkmcnt(3)\n\t" \
    "v_mfma_f32_16x16x32_bf16 v[204:207], v[0:3], v[228:231], v[204:207]\n\t" \
    "ds_read_b128 v[224:227], v253 offset:10240\n\t" \
    "v_mfma_f32_16x16x32_bf16 v[208:211], v[8:11], v[228:231], v[208:211]\n\t" \
    "v_mfma_f32_16x16x32_bf16 v[200:203], v[244:247], v[228:231], v[200:203]\n\t" \
    "s_waitcnt lgkmcnt(3)\n\t" \
    "v_mfma_f32_16x16x32_bf16 v[216:219], v[0:3], v[232:235], v[216:219]\n\t" \
    "ds_read_b128 v[228:231], v253 offset:30720\n\t" \
    "v_mfma_f32_16x16x32_bf16 v[220:223], v[8:11], v[232:235], v[220:223]\n\t" \
    "v_mfma_f32_16x16x32_bf16 v[212:215], v[244:247], v[232:235], v[212:215]\n\t" \
    "ds_read_b128 v[244:247], v254 offset:57344\n\t" \
    "s_waitcnt lgkmcnt(4)\n\t" \
    "v_mfma_f32_16x16x32_bf16 v[204:207], v[4:7], v[236:239], v[204:207]\n\t" \
    "ds_read_b128 v[232:235], v253 offset:11264\n\t" \
    "v_mfma_f32_16x16x32_bf16 v[208:211], v[12:15], v[236:239], v[208:211]\n\t" \
    "v_mfma_f32_16x16x32_bf16 v[200:203], v[248:251], v[236:239], v[200:203]\n\t" \
    "s_waitcnt lgkmcnt(4)\n\t" \
    "v_mfma_f32_16x16x32_bf16 v[216:219], v[4:7], v[240:243], v[216:219]\n\t" \
    "ds_read_b128 v[236:239], v253 offset:31744\n\t" \
    "v_mfma_f32_16x16x32_bf16 v[220:223], v[12:15], v[240:243], v[220:223]\n\t" \
    "v_mfma_f32_16x16x32_bf16 v[212:215], v[248:251], v[240:243], v[212:215]\n\t" \
    "ds_read_b128 v[248:251], v254 offset:58368\n\t" \
    "s_waitcnt lgkmcnt(5)\n\t" \
    "v_mfma_f32_16x16x32_bf16 v[204:207], v[120:123], v[224:227], v[204:207]\n\t" \
    "ds_read_b128 v[240:243], v253 offset:12288\n\t" \
    "s_and_b32 s44, s40, 3\n\t" \
    "v_mfma_f32_16x16x32_bf16 v[208:211], v[128:131], v[224:227], v[208:211]\n\t" \
    "s_lshl_b32 s44, s44, 17\n\t" \
    "s_waitcnt lgkmcnt(4)\n\t" \
    "v_mfma_f32_16x16x32_bf16 v[200:203], v[244:247], v[224:227], v[200:203]\n\t" \
    "s_add_u32 s44, s44, %[mb]\n\t" \
    "v_mfma_f32_16x16x32_bf16 v[216:219], v[120:123], v[228:231], v[216:219]\n\t" \
    "ds_read_b128 v[224:227], v253 offset:32768\n\t" \
    "s_add_u32 s46, s44, 0x1000\n\t" \
    "v_mfma_f32_16x16x32_bf16 v[220:223], v[128:131], v[228:231], v[220:223]\n\t" \
    "s_add_u32 s47, s44, 0x1c00\n\t" \
    "v_mfma_f32_16x16x32_bf16 v[212:215], v[244:247], v[228:231], v[212:215]\n\t" \
    "ds_read_b128 v[244:247], v254 offset:59392\n\t" \
    "s_waitcnt lgkmcnt(5)\n\t" \
    "v_mfma_f32_16x16x32_bf16 v[204:207], v[124:127], v[232:235], v[204:207]\n\t" \
    "ds_read_b128 v[228:231], v253 offset:13312\n\t" \
    "v_mfma_f32_16x16x32_bf16 v[208:211], v[132:135], v[232:235], v[208:211]\n\t" \
    "s_waitcnt lgkmcnt(4)\n\t" \
    "v_mfma_f32_16x16x32_bf16 v[200:203], v[248:251], v[232:235], v[200:203]\n\t" \
    "v_mfma_f32_16x16x32_bf16 v[216:219], v[124:127], v[236:239], v[216:219]\n\t" \
    "ds_read_b128 v[232:235], v253 offset:33792\n\t" \
    "v_mfma_f32_16x16x32_bf16 v[220:223], v[132:135], v[236:239], v[220:223]\n\t" \
    "v_mfma_f32_16x16x32_bf16 v[212:215], v[248:251], v[236:239], v[212:215]\n\t" \
    "ds_read_b128 v[248:251], v254 offset:60416\n\t" \
    "s_waitcnt lgkmcnt(5)\n\t" \
    "v_mfma_f32_16x16x32_bf16 v[204:207], v[144:147], v[240:243], v[204:207]\n\t" \
    "ds_read_b128 v[236:239], v253 offset:16384\n\t" \
    "v_mfma_f32_16x16x32_bf16 v[208:211], v[152:155], v[240:243], v[208:211]\n\t" \
    "v_mfma_f32_16x16x32_bf16 v[200:203], v[136:139], v[240:243], v[200:203]\n\t" \
    "s_waitcnt lgkmcnt(5)\n\t" \
    "v_mfma_f32_16x16x32_bf16 v[216:219], v[144:147], v[224:227], v[216:219]\n\t" \
    "ds_read_b128 v[240:243], v253 offset:36864\n\t" \
    "v_mfma_f32_16x16x32_bf16 v[220:223], v[152:155], v[224:227], v[220:223]\n\t" \
    "v_mfma_f32_16x16x32_bf16 v[212:215], v[136:139], v[224:227], v[212:215]\n\t" \
    "s_waitcnt lgkmcnt(4)\n\t" \
    "v_mfma_f32_16x16x32_bf16 v[204:207], v[148:151], v[228:231], v[204:207]\n\t" \
    "ds_read_b128 v[224:227], v253 offset:17408\n\t" \
    "v_mfma_f32_16x16x32_bf16 v[208:211], v[156:159], v[228:231], v[208:211]\n\t" \
    "v_mfma_f32_16x16x32_bf16 v[200:203], v[140:143], v[228:231], v[200:203]\n\t" \
    "s_waitcnt lgkmcnt(4)\n\t" \
    "v_mfma_f32_16x16x32_bf16 v[216:219], v[148:151], v[232:235], v[216:219]\n\t" \
    "ds_read_b128 v[228:231], v253 offset:37888\n\t" \
    "v_mfma_f32_16x16x32_bf16 v[220:223], v[156:159], v[232:235], v[220:223]\n\t" \
    "v_mfma_f32_16x16x32_bf16 v[212:215], v[140:143], v[232:235], v[212:215]\n\t" \
    "s_cmp_eq_u32 %[fl], 0\n\t" \
    "s_cbranch_scc1 .Lv22_%=\n\t" \
    "v_and_b32 v252, 0xf0, v253\n\t" \
    "v_lshlrev_b32 v252, 3, v252\n\t" \
    "buffer_load_dword v252, v252, %[rs], %[fl] offen sc1\n\t" \
    ".Lv22_%=:\n\t" \
    "s_waitcnt lgkmcnt(3)\n\t" \
    "v_mfma_f32_16x16x32_bf16 v[204:207], v[160:163], v[236:239], v[204:207]\n\t" \
    "ds_read_b128 v[232:235], v253 offset:14336\n\t" \
    "v_mfma_f32_16x16x32_bf16 v[208:211], v[168:171], v[236:239], v[208:211]\n\t" \
    "v_mfma_f32_16x16x32_bf16 v[200:203], v[244:247], v[236:239], v[200:203]\n\t" \
    "s_waitcnt lgkmcnt(3)\n\t" \
    "v_mfma_f32_16x16x32_bf16 v[216:219], v[160:163], v[240:243], v[216:219]\n\t" \
    "ds_read_b128 v[236:239], v253 offset:15360\n\t" \
    "v_mfma_f32_16x16x32_bf16 v[220:223], v[168:171], v[240:243], v[220:223]\n\t" \
    "v_mfma_f32_16x16x32_bf16 v[212:215], v[244:247], v[240:243], v[212:215]\n\t" \
    "s_waitcnt lgkmcnt(3)\n\t" \
    "v_mfma_f32_16x16x32_bf16 v[204:207], v[164:167], v[224:227], v[204:207]\n\t" \
    "ds_read_b128 v[240:243], v253 offset:18432\n\t" \
    "v_mfma_f32_16x16x32_bf16 v[208:211], v[172:175], v[224:227], v[208:211]\n\t" \
    "v_mfma_f32_16x16x32_bf16 v[200:203], v[248:251], v[224:227], v[200:203]\n\t" \
    "s_waitcnt lgkmcnt(3)\n\t" \
    "v_mfma_f32_16x16x32_bf16 v[216:219], v[164:167], v[228:231], v[216:219]\n\t" \
    "ds_read_b128 v[224:227], v253 offset:19456\n\t" \
    "v_mfma_f32_16x16x32_bf16 v[220:223], v[172:175], v[228:231], v[220:223]\n\t" \
    "v_mfma_f32_16x16x32_bf16 v[212:215], v[248:251], v[228:231], v[212:215]\n\t" \
    "s_waitcnt lgkmcnt(3)\n\t" \
    "v_mfma_f32_16x16x32_bf16 v[204:207], v[24:27], v[232:235], v[204:207]\n\t" \
    "ds_read_b128 v[228:231], v253 offset:34816\n\t" \
    "v_mov_b64 v[244:245], 0\n\t" \
    "v_mfma_f32_16x16x32_bf16 v[208:211], v[32:35], v[232:235], v[208:211]\n\t" \
    "v_mov_b64 v[246:247], 0\n\t" \
    "v_mfma_f32_16x16x32_bf16 v[200:203], v[16:19], v[232:235], v[200:203]\n\t" \
    "v_mov_b64 v[248:249], 0\n\t" \
    "s_waitcnt lgkmcnt(3)\n\t" \
    "v_mfma_f32_16x16x32_bf16 v[204:207], v[28:31], v[236:239], v[204:207]\n\t" \
    "ds_read_b128 v[232:235], v253 offset:35840\n\t" \
    "v_mov_b64 v[250:251], 0\n\t" \
    "v_mfma_f32_16x16x32_bf16 v[208:211], v[36:39], v[236:239], v[208:211]\n\t" \
    "v_mfma_f32_16x16x32_bf16 v[200:203], v[20:23], v[236:239], v[200:203]\n\t" \
    "s_waitcnt lgkmcnt(3)\n\t" \
    "v_mfma_f32_16x16x32_bf16 v[204:207], v[184:187], v[240:243], v[204:207]\n\t" \
    "ds_read_b128 v[236:239], v253 offset:38912\n\t" \
    "v_mfma_f32_16x16x32_bf16 v[208:211], v[192:195], v[240:243], v[208:211]\n\t" \
    "v_mfma_f32_16x16x32_bf16 v[200:203], v[176:179], v[240:243], v[200:203]\n\t" \
    "s_waitcnt lgkmcnt(3)\n\t" \
    "v_mfma_f32_16x16x32_bf16 v[204:207], v[188:191], v[224:227], v[204:207]\n\t" \
    "ds_read_b128 v[240:243], v253 offset:39936\n\t" \
    "v_mfma_f32_16x16x32_bf16 v[208:211], v[196:199], v[224:227], v[208:211]\n\t" \
    "v_mfma_f32_16x16x32_bf16 v[200:203], v[180:183], v[224:227], v[200:203]\n\t" \
    "s_waitcnt lgkmcnt(3)\n\t" \
    "v_mfma_f32_16x16x32_bf16 v[216:219], v[24:27], v[228:231], v[216:219]\n\t" \
    "s_waitcnt lgkmcnt(2)\n\t" \
    "v_mfma_f32_16x16x32_bf16 v[216:219], v[28:31], v[232:235], v[216:219]\n\t" \
    "v_cvt_pk_bf16_f32 v204, v204, v205\n\t" \
    "v_cvt_pk_bf16_f32 v205, v206, v207\n\t" \
    "s_waitcnt lgkmcnt(1)\n\t" \
    "v_mfma_f32_16x16x32_bf16 v[216:219], v[184:187], v[236:239], v[216:219]\n\t" \
    "ds_write_b64 v254, v[204:205] offset:7176\n\t" \
    "v_cvt_pk_bf16_f32 v208, v208, v209\n\t" \
    "s_waitcnt lgkmcnt(1)\n\t" \
    "v_mfma_f32_16x16x32_bf16 v[216:219], v[188:191], v[240:243], v[216:219]\n\t" \
    "v_cvt_pk_bf16_f32 v209, v210, v211\n\t" \
    "ds_write_b64 v254, v[208:209] offset:8200\n\t" \
    "v_mfma_f32_16x16x32_bf16 v[220:223], v[32:35], v[228:231], v[220:223]\n\t" \
    "v_cvt_pk_bf16_f32 v200, v200, v201\n\t" \
    "v_cvt_pk_bf16_f32 v201, v202, v203\n\t" \
    "v_mfma_f32_16x16x32_bf16 v[220:223], v[36:39], v[232:235], v[220:223]\n\t" \
    "ds_write_b64 v254, v[200:201] offset:6152\n\t" \
    "ds_read_b128 v[200:203], v254 offset:51200\n\t" \
    "v_mfma_f32_16x16x32_bf16 v[220:223], v[192:195], v[236:239], v[220:223]\n\t" \
    "ds_read_b128 v[204:207], v254 offset:52224\n\t" \
    "v_mov_b64 v[208:209], 0\n\t" \
    "v_mfma_f32_16x16x32_bf16 v[220:223], v[196:199], v[240:243], v[220:223]\n\t" \
    "v_mov_b64 v[210:211], 0\n\t" \
    "v_cvt_pk_bf16_f32 v216, v216, v217\n\t" \
    "v_mfma_f32_16x16x32_bf16 v[212:215], v[16:19], v[228:231], v[212:215]\n\t" \
    "v_cvt_pk_bf16_f32 v217, v218, v219\n\t" \
    "ds_write_b64 v254, v[216:217] offset:13320\n\t" \
    "v_mfma_f32_16x16x32_bf16 v[212:215], v[20:23], v[232:235], v[212:215]\n\t" \
    "v_mfma_f32_16x16x32_bf16 v[212:215], v[176:179], v[236:239], v[212:215]\n\t" \
    "v_cvt_pk_bf16_f32 v220, v220, v221\n\t" \
    "v_cvt_pk_bf16_f32 v221, v222, v223\n\t" \
    "v_mfma_f32_16x16x32_bf16 v[212:215], v[180:183], v[240:243], v[212:215]\n\t" \
    "ds_write_b64 v254, v[220:221] offset:14344\n\t" \
    "ds_read_b128 v[216:219], v254 offset:53248\n\t" \
    "ds_read_b128 v[220:223], v254 offset:54272\n\t" \
    "s_nop 4\n\t" \
    "v_cvt_pk_bf16_f32 v212, v212, v213\n\t" \
    "v_cvt_pk_bf16_f32 v213, v214, v215\n\t" \
    "ds_write_b64 v254, v[212:213] offset:12296\n\t" \
    LST6 \
    "s_waitcnt vmcnt(0) lgkmcnt(0)\n\t" \
    "s_barrier \n\t" \
    "s_setprio 2\n\t" \
    "s_cmp_eq_u32 %[fl], 0\n\t" \
    "s_cbranch_scc1 .Lv24_%=\n\t" \
    "s_add_u32 s44, s40, 1\n\t" \
    "s_min_u32 s44, s44, 63\n\t" \
    ".Lv23_%=:\n\t" \
    "v_cmp_ge_u32 vcc, s44, v252\n\t" \
    "s_cbranch_vccz .Lv24_%=\n\t" \
    "s_cmp_eq_u32 s41, 0\n\t" \
    "s_cbranch_scc1 .Lv24_%=\n\t" \
    "s_sub_u32 s41, s41, 1\n\t" \
    "s_sleep 2\n\t" \
    "v_and_b32 v252, 0xf0, v253\n\t" \
    "v_lshlrev_b32 v252, 3, v252\n\t" \
    "buffer_load_dword v252, v252, %[rs], %[fl] offen sc1\n\t" \
    "s_waitcnt vmcnt(0)\n\t" \
    "s_branch .Lv23_%=\n\t" \
    ".Lv24_%=:\n\t" \
    "s_movk_i32 s45, 0x2000\n\t" \
    "s_mov_b32 m0, s45\n\t" \
    "s_nop 0\n\t" \
    "buffer_load_dwordx4 v253, %[rs], s42 offen offset:0 sc1 lds\n\t" \
    "buffer_load_dwordx4 v253, %[rs], s42 offen offset:1024 sc1 lds\n\t" \
    "buffer_load_dwordx4 v253, %[rs], s42 offen offset:2048 sc1 lds\n\t" \
    "buffer_load_dwordx4 v253, %[rs], s42 offen offset:3072 sc1 lds\n\t" \
    "s_add_u32 s44, s42, 0x1000\n\t" \
    "s_movk_i32 s45, 0x7000\n\t" \
    "s_mov_b32 m0, s45\n\t" \
    "s_nop 0\n\t" \
    "buffer_load_dwordx4 v253, %[rs], s44 offen offset:0 sc1 lds\n\t" \
    "buffer_load_dwordx4 v253, %[rs], s44 offen offset:1024 sc1 lds\n\t" \
    "buffer_load_dwordx4 v253, %[rs], s44 offen offset:2048 sc1 lds\n\t" \
    "buffer_load_dwordx4 v253, %[rs], s44 offen offset:3072 sc1 lds\n\t" \
    "s_add_u32 s44, s42, 0x8800\n\t" \
    "s_mov_b32 s45, 0x4800\n\t" \
    "s_mov_b32 m0, s45\n\t" \
    "s_nop 0\n\t" \
    "buffer_load_dwordx4 v253, %[rs], s44 offen offset:0 sc1 lds\n\t" \
    "s_add_u32 s44, s44, 0x1000\n\t" \
    "s_mov_b32 s45, 0x9800\n\t" \
    "s_mov_b32 m0, s45\n\t" \
    "s_nop 0\n\t" \
    "buffer_load_dwordx4 v253, %[rs], s44 offen offset:0 sc1 lds\n\t" \
    LST1 \
    "buffer_load_dwordx4 v[224:227], v253, %[rs], s46 offen sc1\n\t" \
    "buffer_load_dwordx4 v[228:231], v253, %[rs], s47 offen sc1\n\t" \
    "s_waitcnt lgkmcnt(0)\n\t" \
    "v_bfe_i32 v212, v253, 5, 1\n\t" \
    "v_and_b32 v212, 0xaaaa, v212\n\t" \
    "v_or_b32 v212, 0x4444, v212\n\t" \
    ".Lv25_%=:\n\t" \
    "s_waitcnt vmcnt(1)\n\t" \
    "v_max_u32 v214, v224, v225\n\t" \
    "v_max3_u32 v214, v226, v227, v214\n\t" \
    "s_waitcnt vmcnt(0)\n\t" \
    "v_max3_u32 v214, v228, v229, v214\n\t" \
    "v_max3_u32 v214, v230, v231, v214\n\t" \
    "v_cmp_eq_u32 vcc, -1, v214\n\t" \
    "s_cbranch_vccz .Lv26_%=\n\t" \
    "v_max_u32 v214, v224, v225\n\t" \
    "v_max3_u32 v214, v226, v227, v214\n\t" \
    "v_cmp_eq_u32 vcc, -1, v214\n\t" \
    "s_cbranch_vccz .Lv27_%=\n\t" \
    "buffer_load_dwordx4 v[224:227], v253, %[rs], s46 offen sc1\n\t" \
    ".Lv27_%=:\n\t" \
    "v_max_u32 v214, v228, v229\n\t" \
    "v_max3_u32 v214, v230, v231, v214\n\t" \
    "v_cmp_eq_u32 vcc, -1, v214\n\t" \
    "s_cbranch_vccz .Lv28_%=\n\t" \
    "buffer_load_dwordx4 v[228:231], v253, %[rs], s47 offen sc1\n\t" \
    ".Lv28_%=:\n\t" \
    "s_cmp_eq_u32 s41, 0\n\t" \
    "s_cbranch_scc1 .Lv26_%=\n\t" \
    "s_sub_u32 s41, s41, 1\n\t" \
    "s_waitcnt vmcnt(0)\n\t" \
    "s_branch .Lv25_%=\n\t" \
    ".Lv26_%=:\n\t" \
    LST2 \
    "ds_write_b128 v253, v[224:227] offset:45056\n\t" \
    "ds_write_b128 v253, v[228:231] offset:48128\n\t" \
    "v_smfmac_f32_16x16x64_bf16 v[244:247], v[224:227], v[0:7], v212\n\t" \
    "v_smfmac_f32_16x16x64_bf16 v[248:251], v[224:227], v[8:15], v212\n\t" \
    "v_smfmac_f32_16x16x64_bf16 v[208:211], v[224:227], v[200:207], v212\n\t" \
    "s_waitcnt lgkmcnt(0)\n\t" \
    "s_barrier \n\t" \
    LST3 \
    "ds_read_b128 v[232:235], v253 offset:40960\n\t" \
    "ds_read_b128 v[236:239], v253 offset:41984\n\t" \
    "ds_read_b128 v[240:243], v253 offset:43008\n\t" \
    "ds_read_b128 v[224:227], v253 offset:44032\n\t" \
    "ds_read_b128 v[200:203], v254 offset:55296\n\t" \
    "ds_read_b128 v[204:207], v254 offset:56320\n\t" \
    "v_smfmac_f32_16x16x64_bf16 v[208:211], v[228:231], v[16:23], v212\n\t" \
    "v_smfmac_f32_16x16x64_bf16 v[244:247], v[228:231], v[24:31], v212\n\t" \
    "v_smfmac_f32_16x16x64_bf16 v[248:251], v[228:231], v[32:39], v212\n\t" \
    "ds_read_b128 v[228:231], v253 offset:46080\n\t" \
    "s_waitcnt lgkmcnt(6)\n\t" \
    "v_smfmac_f32_16x16x64_bf16 v[208:211], v[232:235], v[216:223], v212\n\t" \
    "ds_read_b128 v[216:219], v254 offset:57344\n\t" \
    "ds_read_b128 v[220:223], v254 offset:58368\n\t" \
    "v_smfmac_f32_16x16x64_bf16 v[244:247], v[232:235], v[40:47], v212\n\t" \
    "v_smfmac_f32_16x16x64_bf16 v[248:251], v[232:235], v[48:55], v212\n\t" \
    "ds_read_b128 v[232:235], v253 offset:47104\n\t" \
    "s_waitcnt lgkmcnt(8)\n\t" \
    "v_smfmac_f32_16x16x64_bf16 v[208:211], v[236:239], v[56:63], v212\n\t" \
    "v_smfmac_f32_16x16x64_bf16 v[244:247], v[236:239], v[64:71], v212\n\t" \
    "v_smfmac_f32_16x16x64_bf16 v[248:251], v[236:239], v[72:79], v212\n\t" \
    "ds_read_b128 v[236:239], v253 offset:49152\n\t" \
    "s_waitcnt lgkmcnt(5)\n\t" \
    "v_smfmac_f32_16x16x64_bf16 v[208:211], v[240:243], v[200:207], v212\n\t" \
    "ds_read_b128 v[200:203], v254 offset:59392\n\t" \
    "ds_read_b128 v[204:207], v254 offset:60416\n\t" \
    "v_smfmac_f32_16x16x64_bf16 v[244:247], v[240:243], v[80:87], v212\n\t" \
    "v_smfmac_f32_16x16x64_bf16 v[248:251], v[240:243], v[88:95], v212\n\t" \
    "ds_read_b128 v[240:243], v253 offset:50176\n\t" \
    "v_smfmac_f32_16x16x64_bf16 v[244:247], v[224:227], v[104:111], v212\n\t" \
    "s_waitcnt lgkmcnt(7)\n\t" \
    "v_smfmac_f32_16x16x64_bf16 v[244:247], v[228:231], v[120:127], v212\n\t" \
    "s_waitcnt lgkmcnt(4)\n\t" \
    "v_smfmac_f32_16x16x64_bf16 v[244:247], v[232:235], v[144:151], v212\n\t" \
    "s_waitcnt lgkmcnt(3)\n\t" \
    "v_smfmac_f32_16x16x64_bf16 v[244:247], v[236:239], v[160:167], v212\n\t" \
    "s_waitcnt lgkmcnt(0)\n\t" \
    "v_smfmac_f32_16x16x64_bf16 v[244:247], v[240:243], v[184:191], v212\n\t" \
    "v_smfmac_f32_16x16x64_bf16 v[248:251], v[224:227], v[112:119], v212\n\t" \
    "v_smfmac_f32_16x16x64_bf16 v[248:251], v[228:231], v[128:135], v212\n\t" \
    "v_smfmac_f32_16x16x64_bf16 v[248:251], v[232:235], v[152:159], v212\n\t" \
    "v_add_f32 v244, v244, v246\n\t" \
    "v_add_f32 v245, v245, v247\n\t" \
    "v_smfmac_f32_16x16x64_bf16 v[248:251], v[236:239], v[168:175], v212\n\t" \
    "v_cvt_pk_bf16_f32 v244, v244, v245\n\t" \
    "ds_write_b16 v255, v244 offset:512\n\t" \
    "v_smfmac_f32_16x16x64_bf16 v[248:251], v[240:243], v[192:199], v212\n\t" \
    "ds_write_b16_d16_hi v255, v244 offset:528\n\t" \
    "v_smfmac_f32_16x16x64_bf16 v[208:211], v[224:227], v[96:103], v212\n\t" \
    "v_smfmac_f32_16x16x64_bf16 v[208:211], v[228:231], v[216:223], v212\n\t" \
    "v_smfmac_f32_16x16x64_bf16 v[208:211], v[232:235], v[136:143], v212\n\t" \
    "v_add_f32 v248, v248, v250\n\t" \
    "v_add_f32 v249, v249, v251\n\t" \
    "v_smfmac_f32_16x16x64_bf16 v[208:211], v[236:239], v[200:207], v212\n\t" \
    "v_cvt_pk_bf16_f32 v248, v248, v249\n\t" \
    "ds_write_b16 v255, v248 offset:1024\n\t" \
    "v_smfmac_f32_16x16x64_bf16 v[208:211], v[240:243], v[176:183], v212\n\t" \
    "ds_write_b16_d16_hi v255, v248 offset:1040\n\t" \
    "ds_read_b128 v[224:227], v253 offset:0\n\t" \
    "ds_read_b128 v[244:247], v254 offset:53248\n\t" \
    "ds_read_b128 v[228:231], v253 offset:20480\n\t" \
    "ds_read_b128 v[232:235], v253 offset:1024\n\t" \
    "ds_read_b128 v[236:239], v253 offset:21504\n\t" \
    "ds_read_b128 v[248:251], v254 offset:54272\n\t" \
    "s_nop 0\n\t" \
    "v_add_f32 v208, v208, v210\n\t" \
    "v_add_f32 v209, v209, v211\n\t" \
    "v_cvt_pk_bf16_f32 v208, v208, v209\n\t" \
    "ds_write_b16 v255, v208 offset:0\n\t" \
    "ds_write_b16_d16_hi v255, v208 offset:16\n\t" \
    "s_setprio 0\n\t" \
    "s_waitcnt lgkmcnt(0)\n\t" \
    "s_barrier \n\t" \
    LST4 \
    LFLUSH200 \
    "s_add_u32 s40, s40, 1\n\t" \
    "s_cmp_lg_u32 s40, 64\n\t" \
    "s_cbranch_scc1 .Lv21_%=\n\t" \
    "s_branch .Lend%=\n\t" \
    ".Lv1_%=:\n\t" \
    "s_mov_b32 s42, %[xs]\n\t" \
    "ds_read_b128 v[224:227], v253 offset:0\n\t" \
    "ds_read_b128 v[244:247], v254 offset:40960\n\t" \
    "ds_read_b128 v[228:231], v253 offset:20480\n\t" \
    "ds_read_b128 v[232:235], v253 offset:1024\n\t" \
    "ds_read_b128 v[236:239], v253 offset:21504\n\t" \
    "ds_read_b128 v[248:251], v254 offset:41984\n\t" \
    ".Lv11_%=:\n\t" \
    LST7 \
    "s_waitcnt lgkmcnt(5)\n\t" \
    "v_mfma_f32_16x16x32_bf16 v[204:207], v[64:67], v[224:227], 0\n\t" \
    "ds_read_b128 v[240:243], v253 offset:2048\n\t" \
    "v_mfma_f32_16x16x32_bf16 v[208:211], v[72:75], v[224:227], 0\n\t" \
    "v_mfma_f32_16x16x32_bf16 v[200:203], v[56:59], v[224:227], 0\n\t" \
    "s_waitcnt lgkmcnt(4)\n\t" \
    "v_mfma_f32_16x16x32_bf16 v[216:219], v[64:67], v[228:231], 0\n\t" \
    "ds_read_b128 v[224:227], v253 offset:22528\n\t" \
    "v_mfma_f32_16x16x32_bf16 v[220:223], v[72:75], v[228:231], 0\n\t" \
    "v_mfma_f32_16x16x32_bf16 v[212:215], v[56:59], v[228:231], 0\n\t" \
    "s_waitcnt lgkmcnt(4)\n\t" \
    "v_mfma_f32_16x16x32_bf16 v[204:207], v[68:71], v[232:235], v[204:207]\n\t" \
    "ds_read_b128 v[228:231], v253 offset:3072\n\t" \
    "v_mfma_f32_16x16x32_bf16 v[208:211], v[76:79], v[232:235], v[208:211]\n\t" \
    "v_mfma_f32_16x16x32_bf16 v[200:203], v[60:63], v[232:235], v[200:203]\n\t" \
    "s_waitcnt lgkmcnt(4)\n\t" \
    "v_mfma_f32_16x16x32_bf16 v[216:219], v[68:71], v[236:239], v[216:219]\n\t" \
    "ds_read_b128 v[232:235], v253 offset:23552\n\t" \
    "v_mfma_f32_16x16x32_bf16 v[220:223], v[76:79], v[236:239], v[220:223]\n\t" \
    "v_mfma_f32_16x16x32_bf16 v[212:215], v[60:63], v[236:239], v[212:215]\n\t" \
    "s_waitcnt lgkmcnt(3)\n\t" \
    "v_mfma_f32_16x16x32_bf16 v[204:207], v[104:107], v[240:243], v[204:207]\n\t" \
    "ds_read_b128 v[236:239], v253 offset:4096\n\t" \
    "v_mfma_f32_16x16x32_bf16 v[208:211], v[112:115], v[240:243], v[208:211]\n\t" \
    "v_mfma_f32_16x16x32_bf16 v[200:203], v[96:99], v[240:243], v[200:203]\n\t" \
    "s_waitcnt lgkmcnt(3)\n\t" \
    "v_mfma_f32_16x16x32_bf16 v[216:219], v[104:107], v[224:227], v[216:219]\n\t" \
    "ds_read_b128 v[240:243], v253 offset:24576\n\t" \
    "v_mfma_f32_16x16x32_bf16 v[220:223], v[112:115], v[224:227], v[220:223]\n\t" \
    "v_mfma_f32_16x16x32_bf16 v[212:215], v[96:99], v[224:227], v[212:215]\n\t" \
    "s_waitcnt lgkmcnt(3)\n\t" \
    "v_mfma_f32_16x16x32_bf16 v[204:207], v[108:111], v[228:231], v[204:207]\n\t" \
    "ds_read_b128 v[224:227], v253 offset:5120\n\t" \
    "v_mfma_f32_16x16x32_bf16 v[208:211], v[116:119], v[228:231], v[208:211]\n\t" \
    "v_mfma_f32_16x16x32_bf16 v[200:203], v[100:103], v[228:231], v[200:203]\n\t" \
    "s_waitcnt lgkmcnt(3)\n\t" \
    "v_mfma_f32_16x16x32_bf16 v[216:219], v[108:111], v[232:235], v[216:219]\n\t" \
    "ds_read_b128 v[228:231], v253 offset:25600\n\t" \
    "v_mfma_f32_16x16x32_bf16 v[220:223], v[116:119], v[232:235], v[220:223]\n\t" \
    "v_mfma_f32_16x16x32_bf16 v[212:215], v[100:103], v[232:235], v[212:215]\n\t" \
    "s_waitcnt lgkmcnt(3)\n\t" \
    "v_mfma_f32_16x16x32_bf16 v[204:207], v[0:3], v[236:239], v[204:207]\n\t" \
    "ds_read_b128 v[232:235], v253 offset:6144\n\t" \
    "s_add_u32 s44, s40, 1\n\t" \
    "v_mfma_f32_16x16x32_bf16 v[208:211], v[8:11], v[236:239], v[208:211]\n\t" \
    "s_and_b32 s45, s44, 15\n\t" \
    "v_mfma_f32_16x16x32_bf16 v[200:203], v[244:247], v[236:239], v[200:203]\n\t" \
    "s_cmp_eq_u32 s45, 0\n\t" \
    "s_waitcnt lgkmcnt(3)\n\t" \
    "v_mfma_f32_16x16x32_bf16 v[216:219], v[0:3], v[240:243], v[216:219]\n\t" \
    "ds_read_b128 v[236:239], v253 offset:26624\n\t" \
    "s_cselect_b32 s45, %[cj], %[cs]\n\t" \
    "v_mfma_f32_16x16x32_bf16 v[220:223], v[8:11], v[240:243], v[220:223]\n\t" \
    "s_cmp_eq_u32 s44, 64\n\t" \
    "v_mfma_f32_16x16x32_bf16 v[212:215], v[244:247], v[240:243], v[212:215]\n\t" \
    "ds_read_b128 v[244:247], v254 offset:43008\n\t" \
    "s_cselect_b32 s45, 0, s45\n\t" \
    "s_waitcnt lgkmcnt(4)\n\t" \
    "v_mfma_f32_16x16x32_bf16 v[204:207], v[4:7], v[224:227], v[204:207]\n\t" \
    "ds_read_b128 v[240:243], v253 offset:7168\n\t" \
    "s_add_u32 s42, s42, s45\n\t" \
    "v_mfma_f32_16x16x32_bf16 v[208:211], v[12:15], v[224:227], v[208:211]\n\t" \
    "v_mfma_f32_16x16x32_bf16 v[200:203], v[248:251], v[224:227], v[200:203]\n\t" \
    "s_waitcnt lgkmcnt(4)\n\t" \
    "v_mfma_f32_16x16x32_bf16 v[216:219], v[4:7], v[228:231], v[216:219]\n\t" \
    "ds_read_b128 v[224:227], v253 offset:27648\n\t" \
    "v_mfma_f32_16x16x32_bf16 v[220:223], v[12:15], v[228:231], v[220:223]\n\t" \
    "v_mfma_f32_16x16x32_bf16 v[212:215], v[248:251], v[228:231], v[212:215]\n\t" \
    "ds_read_b128 v[248:251], v254 offset:44032\n\t" \
    "s_waitcnt lgkmcnt(5)\n\t" \
    "v_mfma_f32_16x16x32_bf16 v[204:207], v[40:43], v[232:235], v[204:207]\n\t" \
    "ds_read_b128 v[228:231], v253 offset:8192\n\t" \
    "v_mfma_f32_16x16x32_bf16 v[208:211], v[48:51], v[232:235], v[208:211]\n\t" \
    "s_waitcnt lgkmcnt(4)\n\t" \
    "v_mfma_f32_16x16x32_bf16 v[200:203], v[244:247], v[232:235], v[200:203]\n\t" \
    "v_mfma_f32_16x16x32_bf16 v[216:219], v[40:43], v[236:239], v[216:219]\n\t" \
    "ds_read_b128 v[232:235], v253 offset:28672\n\t" \
    "v_mfma_f32_16x16x32_bf16 v[220:223], v[48:51], v[236:239], v[220:223]\n\t" \
    "v_mfma_f32_16x16x32_bf16 v[212:215], v[244:247], v[236:239], v[212:215]\n\t" \
    "ds_read_b128 v[244:247], v254 offset:45056\n\t" \
    "s_waitcnt lgkmcnt(5)\n\t" \
    "v_mfma_f32_16x16x32_bf16 v[204:207], v[44:47], v[240:243], v[204:207]\n\t" \
    "ds_read_b128 v[236:239], v253 offset:9216\n\t" \
    "v_mfma_f32_16x16x32_bf16 v[208:211], v[52:55], v[240:243], v[208:211]\n\t" \
    "s_waitcnt lgkmcnt(4)\n\t" \
    "v_mfma_f32_16x16x32_bf16 v[200:203], v[248:251], v[240:243], v[200:203]\n\t" \
    "v_mfma_f32_16x16x32_bf16 v[216:219], v[44:47], v[224:227], v[216:219]\n\t" \
    "ds_read_b128 v[240:243], v253 offset:29696\n\t" \
    "v_mfma_f32_16x16x32_bf16 v[220:223], v[52:55], v[224:227], v[220:223]\n\t" \
    "v_mfma_f32_16x16x32_bf16 v[212:215], v[248:251], v[224:227], v[212:215]\n\t" \
    "ds_read_b128 v[248:251], v254 offset:46080\n\t" \
    "s_waitcnt lgkmcnt(5)\n\t" \
    "v_mfma_f32_16x16x32_bf16 v[204:207], v[144:147], v[228:231], v[204:207]\n\t" \
    "ds_read_b128 v[224:227], v253 offset:10240\n\t" \
    "v_mfma_f32_16x16x32_bf16 v[208:211], v[152:155], v[228:231], v[208:211]\n\t" \
    "v_mfma_f32_16x16x32_bf16 v[200:203], v[136:139], v[228:231], v[200:203]\n\t" \
    "s_waitcnt lgkmcnt(5)\n\t" \
    "v_mfma_f32_16x16x32_bf16 v[216:219], v[144:147], v[232:235], v[216:219]\n\t" \
    "ds_read_b128 v[228:231], v253 offset:30720\n\t" \
    "v_mfma_f32_16x16x32_bf16 v[220:223], v[152:155], v[232:235], v[220:223]\n\t" \
    "v_mfma_f32_16x16x32_bf16 v[212:215], v[136:139], v[232:235], v[212:215]\n\t" \
    "s_waitcnt lgkmcnt(4)\n\t" \
    "v_mfma_f32_16x16x32_bf16 v[204:207], v[148:151], v[236:239], v[204:207]\n\t" \
    "ds_read_b128 v[232:235], v253 offset:11264\n\t" \
    "v_mfma_f32_16x16x32_bf16 v[208:211], v[156:159], v[236:239], v[208:211]\n\t" \
    "v_mfma_f32_16x16x32_bf16 v[200:203], v[140:143], v[236:239], v[200:203]\n\t" \
    "s_waitcnt lgkmcnt(4)\n\t" \
    "v_mfma_f32_16x16x32_bf16 v[216:219], v[148:151], v[240:243], v[216:219]\n\t" \
    "ds_read_b128 v[236:239], v253 offset:31744\n\t" \
    "v_mfma_f32_16x16x32_bf16 v[220:223], v[156:159], v[240:243], v[220:223]\n\t" \
    "v_mfma_f32_16x16x32_bf16 v[212:215], v[140:143], v[240:243], v[212:215]\n\t" \
    "s_waitcnt lgkmcnt(3)\n\t" \
    "v_mfma_f32_16x16x32_bf16 v[204:207], v[80:83], v[224:227], v[204:207]\n\t" \
    "ds_read_b128 v[240:243], v253 offset:14336\n\t" \
    "s_and_b32 s44, s40, 3\n\t" \
    "v_mfma_f32_16x16x32_bf16 v[208:211], v[88:91], v[224:227], v[208:211]\n\t" \
    "s_lshl_b32 s44, s44, 17\n\t" \
    "v_mfma_f32_16x16x32_bf16 v[200:203], v[244:247], v[224:227], v[200:203]\n\t" \
    "s_add_u32 s44, s44, %[mb]\n\t" \
    "s_waitcnt lgkmcnt(3)\n\t" \
    "v_mfma_f32_16x16x32_bf16 v[216:219], v[80:83], v[228:231], v[216:219]\n\t" \
    "ds_read_b128 v[224:227], v253 offset:34816\n\t" \
    "s_add_u32 s46, s44, 0x800\n\t" \
    "v_mfma_f32_16x16x32_bf16 v[220:223], v[88:91], v[228:231], v[220:223]\n\t" \
    "s_add_u32 s47, s44, 0x1800\n\t" \
    "v_mfma_f32_16x16x32_bf16 v[212:215], v[244:247], v[228:231], v[212:215]\n\t" \
    "ds_read_b128 v[244:247], v254 offset:47104\n\t" \
    "s_add_u32 s48, s44, 0xc00\n\t" \
    "s_waitcnt lgkmcnt(4)\n\t" \
    "v_mfma_f32_16x16x32_bf16 v[204:207], v[84:87], v[232:235], v[204:207]\n\t" \
    "ds_read_b128 v[228:231], v253 offset:15360\n\t" \
    "v_mfma_f32_16x16x32_bf16 v[208:211], v[92:95], v[232:235], v[208:211]\n\t" \
    "v_mfma_f32_16x16x32_bf16 v[200:203], v[248:251], v[232:235], v[200:203]\n\t" \
    "s_waitcnt lgkmcnt(4)\n\t" \
    "v_mfma_f32_16x16x32_bf16 v[216:219], v[84:87], v[236:239], v[216:219]\n\t" \
    "ds_read_b128 v[232:235], v253 offset:35840\n\t" \
    "v_mfma_f32_16x16x32_bf16 v[220:223], v[92:95], v[236:239], v[220:223]\n\t" \
    "v_mfma_f32_16x16x32_bf16 v[212:215], v[248:251], v[236:239], v[212:215]\n\t" \
    "ds_read_b128 v[248:251], v254 offset:48128\n\t" \
    "s_waitcnt lgkmcnt(5)\n\t" \
    "v_mfma_f32_16x16x32_bf16 v[204:207], v[120:123], v[240:243], v[204:207]\n\t" \
    "ds_read_b128 v[236:239], v253 offset:16384\n\t" \
    "v_mfma_f32_16x16x32_bf16 v[208:211], v[128:131], v[240:243], v[208:211]\n\t" \
    "s_waitcnt lgkmcnt(4)\n\t" \
    "v_mfma_f32_16x16x32_bf16 v[200:203], v[244:247], v[240:243], v[200:203]\n\t" \
    "v_mfma_f32_16x16x32_bf16 v[216:219], v[120:123], v[224:227], v[216:219]\n\t" \
    "ds_read_b128 v[240:243], v253 offset:36864\n\t" \
    "v_mfma_f32_16x16x32_bf16 v[220:223], v[128:131], v[224:227], v[220:223]\n\t" \
    "v_mfma_f32_16x16x32_bf16 v[212:215], v[244:247], v[224:227], v[212:215]\n\t" \
    "ds_read_b128 v[244:247], v254 offset:49152\n\t" \
    "s_waitcnt lgkmcnt(5)\n\t" \
    "v_mfma_f32_16x16x32_bf16 v[204:207], v[124:127], v[228:231], v[204:207]\n\t" \
    "ds_read_b128 v[224:227], v253 offset:17408\n\t" \
    "v_mfma_f32_16x16x32_bf16 v[208:211], v[132:135], v[228:231], v[208:211]\n\t" \
    "s_waitcnt lgkmcnt(4)\n\t" \
    "v_mfma_f32_16x16x32_bf16 v[200:203], v[248:251], v[228:231], v[200:203]\n\t" \
    "v_mfma_f32_16x16x32_bf16 v[216:219], v[124:127], v[232:235], v[216:219]\n\t" \
    "ds_read_b128 v[228:231], v253 offset:37888\n\t" \
    "v_mfma_f32_16x16x32_bf16 v[220:223], v[132:135], v[232:235], v[220:223]\n\t" \
    "v_mfma_f32_16x16x32_bf16 v[212:215], v[248:251], v[232:235], v[212:215]\n\t" \
    "ds_read_b128 v[248:251], v254 offset:50176\n\t" \
    "s_cmp_eq_u32 %[fl], 0\n\t" \
    "s_cbranch_scc1 .Lv12_%=\n\t" \
    "v_and_b32 v252, 0xf0, v253\n\t" \
    "v_lshlrev_b32 v252, 3, v252\n\t" \
    "buffer_load_dword v252, v252, %[rs], %[fl] offen sc1\n\t" \
    ".Lv12_%=:\n\t" \
    "s_waitcnt lgkmcnt(5)\n\t" \
    "v_mfma_f32_16x16x32_bf16 v[204:207], v[160:163], v[236:239], v[204:207]\n\t" \
    "ds_read_b128 v[232:235], v253 offset:12288\n\t" \
    "v_mfma_f32_16x16x32_bf16 v[208:211], v[168:171], v[236:239], v[208:211]\n\t" \
    "s_waitcnt lgkmcnt(4)\n\t" \
    "v_mfma_f32_16x16x32_bf16 v[200:203], v[244:247], v[236:239], v[200:203]\n\t" \
    "v_mfma_f32_16x16x32_bf16 v[216:219], v[160:163], v[240:243], v[216:219]\n\t" \
    "ds_read_b128 v[236:239], v253 offset:13312\n\t" \
    "v_mfma_f32_16x16x32_bf16 v[220:223], v[168:171], v[240:243], v[220:223]\n\t" \
    "v_mfma_f32_16x16x32_bf16 v[212:215], v[244:247], v[240:243], v[212:215]\n\t" \
    "s_waitcnt lgkmcnt(4)\n\t" \
    "v_mfma_f32_16x16x32_bf16 v[204:207], v[164:167], v[224:227], v[204:207]\n\t" \
    "ds_read_b128 v[240:243], v253 offset:18432\n\t" \
    "v_mfma_f32_16x16x32_bf16 v[208:211], v[172:175], v[224:227], v[208:211]\n\t" \
    "s_waitcnt lgkmcnt(3)\n\t" \
    "v_mfma_f32_16x16x32_bf16 v[200:203], v[248:251], v[224:227], v[200:203]\n\t" \
    "v_mfma_f32_16x16x32_bf16 v[216:219], v[164:167], v[228:231], v[216:219]\n\t" \
    "ds_read_b128 v[224:227], v253 offset:19456\n\t" \
    "v_mfma_f32_16x16x32_bf16 v[220:223], v[172:175], v[228:231], v[220:223]\n\t" \
    "v_mfma_f32_16x16x32_bf16 v[212:215], v[248:251], v[228:231], v[212:215]\n\t" \
    "s_waitcnt lgkmcnt(3)\n\t" \
    "v_mfma_f32_16x16x32_bf16 v[204:207], v[24:27], v[232:235], v[204:207]\n\t" \
    "ds_read_b128 v[228:231], v253 offset:32768\n\t" \
    "v_mov_b64 v[244:245], 0\n\t" \
    "v_mfma_f32_16x16x32_bf16 v[208:211], v[32:35], v[232:235], v[208:211]\n\t" \
    "v_mov_b64 v[246:247], 0\n\t" \
    "v_mfma_f32_16x16x32_bf16 v[200:203], v[16:19], v[232:235], v[200:203]\n\t" \
    "v_mov_b64 v[248:249], 0\n\t" \
    "s_waitcnt lgkmcnt(3)\n\t" \
    "v_mfma_f32_16x16x32_bf16 v[204:207], v[28:31], v[236:239], v[204:207]\n\t" \
    "ds_read_b128 v[232:235], v253 offset:33792\n\t" \
    "v_mov_b64 v[250:251], 0\n\t" \
    "v_mfma_f32_16x16x32_bf16 v[208:211], v[36:39], v[236:239], v[208:211]\n\t" \
    "v_mfma_f32_16x16x32_bf16 v[200:203], v[20:23], v[236:239], v[200:203]\n\t" \
    "s_waitcnt lgkmcnt(3)\n\t" \
    "v_mfma_f32_16x16x32_bf16 v[204:207], v[184:187], v[240:243], v[204:207]\n\t" \
    "ds_read_b128 v[236:239], v253 offset:38912\n\t" \
    "v_mfma_f32_16x16x32_bf16 v[208:211], v[192:195], v[240:243], v[208:211]\n\t" \
    "v_mfma_f32_16x16x32_bf16 v[200:203], v[176:179], v[240:243], v[200:203]\n\t" \
    "s_waitcnt lgkmcnt(3)\n\t" \
    "v_mfma_f32_16x16x32_bf16 v[204:207], v[188:191], v[224:227], v[204:207]\n\t" \
    "ds_read_b128 v[240:243], v253 offset:39936\n\t" \
    "v_mfma_f32_16x16x32_bf16 v[208:211], v[196:199], v[224:227], v[208:211]\n\t" \
    "v_mfma_f32_16x16x32_bf16 v[200:203], v[180:183], v[224:227], v[200:203]\n\t" \
    "s_waitcnt lgkmcnt(3)\n\t" \
    "v_mfma_f32_16x16x32_bf16 v[216:219], v[24:27], v[228:231], v[216:219]\n\t" \
    "s_waitcnt lgkmcnt(2)\n\t" \
    "v_mfma_f32_16x16x32_bf16 v[216:219], v[28:31], v[232:235], v[216:219]\n\t" \
    "v_cvt_pk_bf16_f32 v204, v204, v205\n\t" \
    "v_cvt_pk_bf16_f32 v205, v206, v207\n\t" \
    "s_waitcnt lgkmcnt(1)\n\t" \
    "v_mfma_f32_16x16x32_bf16 v[216:219], v[184:187], v[236:239], v[216:219]\n\t" \
    "ds_write_b64 v254, v[204:205] offset:10240\n\t" \
    "v_cvt_pk_bf16_f32 v208, v208, v209\n\t" \
    "s_waitcnt lgkmcnt(1)\n\t" \
    "v_mfma_f32_16x16x32_bf16 v[216:219], v[188:191], v[240:243], v[216:219]\n\t" \
    "v_cvt_pk_bf16_f32 v209, v210, v211\n\t" \
    "ds_write_b64 v254, v[208:209] offset:11264\n\t" \
    "v_mfma_f32_16x16x32_bf16 v[220:223], v[32:35], v[228:231], v[220:223]\n\t" \
    "v_cvt_pk_bf16_f32 v200, v200, v201\n\t" \
    "v_cvt_pk_bf16_f32 v201, v202, v203\n\t" \
    "v_mfma_f32_16x16x32_bf16 v[220:223], v[36:39], v[232:235], v[220:223]\n\t" \
    "ds_write_b64 v254, v[200:201] offset:9216\n\t" \
    "ds_read_b128 v[200:203], v254 offset:40960\n\t" \
    "v_mfma_f32_16x16x32_bf16 v[220:223], v[192:195], v[236:239], v[220:223]\n\t" \
    "ds_read_b128 v[204:207], v254 offset:41984\n\t" \
    "v_mov_b64 v[208:209], 0\n\t" \
    "v_mfma_f32_16x16x32_bf16 v[220:223], v[196:199], v[240:243], v[220:223]\n\t" \
    "v_mov_b64 v[210:211], 0\n\t" \
    "v_cvt_pk_bf16_f32 v216, v216, v217\n\t" \
    "v_mfma_f32_16x16x32_bf16 v[212:215], v[16:19], v[228:231], v[212:215]\n\t" \
    "v_cvt_pk_bf16_f32 v217, v218, v219\n\t" \
    "ds_write_b64 v254, v[216:217] offset:16384\n\t" \
    "v_mfma_f32_16x16x32_bf16 v[212:215], v[20:23], v[232:235], v[212:215]\n\t" \
    "v_mfma_f32_16x16x32_bf16 v[212:215], v[176:179], v[236:239], v[212:215]\n\t" \
    "v_cvt_pk_bf16_f32 v220, v220, v221\n\t" \
    "v_cvt_pk_bf16_f32 v221, v222, v223\n\t" \
    "v_mfma_f32_16x16x32_bf16 v[212:215], v[180:183], v[240:243], v[212:215]\n\t" \
    "ds_write_b64 v254, v[220:221] offset:17408\n\t" \
    "ds_read_b128 v[216:219], v254 offset:43008\n\t" \
    "ds_read_b128 v[220:223], v254 offset:44032\n\t" \
    "s_nop 4\n\t" \
    "v_cvt_pk_bf16_f32 v212, v212, v213\n\t" \
    "v_cvt_pk_bf16_f32 v213, v214, v215\n\t" \
    "ds_write_b64 v254, v[212:213] offset:15360\n\t" \
    LST6 \
    "s_waitcnt vmcnt(0) lgkmcnt(0)\n\t" \
    "s_barrier \n\t" \
    "s_setprio 2\n\t" \
    "s_cmp_eq_u32 %[fl], 0\n\t" \
    "s_cbranch_scc1 .Lv14_%=\n\t" \
    "s_add_u32 s44, s40, 1\n\t" \
    "s_min_u32 s44, s44, 63\n\t" \
    ".Lv13_%=:\n\t" \
    "v_cmp_ge_u32 vcc, s44, v252\n\t" \
    "s_cbranch_vccz .Lv14_%=\n\t" \
    "s_cmp_eq_u32 s41, 0\n\t" \
    "s_cbranch_scc1 .Lv14_%=\n\t" \
    "s_sub_u32 s41, s41, 1\n\t" \
    "s_sleep 2\n\t" \
    "v_and_b32 v252, 0xf0, v253\n\t" \
    "v_lshlrev_b32 v252, 3, v252\n\t" \
    "buffer_load_dword v252, v252, %[rs], %[fl] offen sc1\n\t" \
    "s_waitcnt vmcnt(0)\n\t" \
    "s_branch .Lv13_%=\n\t" \
    ".Lv14_%=:\n\t" \
    "s_movk_i32 s45, 0x1000\n\t" \
    "s_mov_b32 m0, s45\n\t" \
    "s_nop 0\n\t" \
    "buffer_load_dwordx4 v253, %[rs], s42 offen offset:0 sc1 lds\n\t" \
    "buffer_load_dwordx4 v253, %[rs], s42 offen offset:1024 sc1 lds\n\t" \
    "buffer_load_dwordx4 v253, %[rs], s42 offen offset:2048 sc1 lds\n\t" \
    "buffer_load_dwordx4 v253, %[rs], s42 offen offset:3072 sc1 lds\n\t" \
    "s_add_u32 s44, s42, 0x1000\n\t" \
    "s_movk_i32 s45, 0x6000\n\t" \
    "s_mov_b32 m0, s45\n\t" \
    "s_nop 0\n\t" \
    "buffer_load_dwordx4 v253, %[rs], s44 offen offset:0 sc1 lds\n\t" \
    "buffer_load_dwordx4 v253, %[rs], s44 offen offset:1024 sc1 lds\n\t" \
    "buffer_load_dwordx4 v253, %[rs], s44 offen offset:2048 sc1 lds\n\t" \
    "buffer_load_dwordx4 v253, %[rs], s44 offen offset:3072 sc1 lds\n\t" \
    "s_add_u32 s44, s42, 0xc400\n\t" \
    "s_mov_b32 s45, 0x4400\n\t" \
    "s_mov_b32 m0, s45\n\t" \
    "s_nop 0\n\t" \
    "buffer_load_dwordx4 v253, %[rs], s44 offen offset:0 sc1 lds\n\t" \
    "s_add_u32 s44, s44, 0x1000\n\t" \
    "s_mov_b32 s45, 0x9400\n\t" \
    "s_mov_b32 m0, s45\n\t" \
    "s_nop 0\n\t" \
    "buffer_load_dwordx4 v253, %[rs], s44 offen offset:0 sc1 lds\n\t" \
    LST1 \
    "buffer_load_dwordx4 v[224:227], v253, %[rs], s46 offen sc1\n\t" \
    "buffer_load_dwordx4 v[228:231], v253, %[rs], s47 offen sc1\n\t" \
    "buffer_load_dwordx4 v[232:235], v253, %[rs], s48 offen sc1\n\t" \
    "s_waitcnt lgkmcnt(0)\n\t" \
    "v_bfe_i32 v212, v253, 5, 1\n\t" \
    "v_and_b32 v212, 0xaaaa, v212\n\t" \
    "v_or_b32 v212, 0x4444, v212\n\t" \
    ".Lv15_%=:\n\t" \
    "s_waitcnt vmcnt(2)\n\t" \
    "v_max_u32 v214, v224, v225\n\t" \
    "v_max3_u32 v214, v226, v227, v214\n\t" \
    "s_waitcnt vmcnt(1)\n\t" \
    "v_max3_u32 v214, v228, v229, v214\n\t" \
    "v_max3_u32 v214, v230, v231, v214\n\t" \
    "s_waitcnt vmcnt(0)\n\t" \
    "v_max3_u32 v214, v232, v233, v214\n\t" \
    "v_max3_u32 v214, v234, v235, v214\n\t" \
    "v_cmp_eq_u32 vcc, -1, v214\n\t" \
    "s_cbranch_vccz .Lv16_%=\n\t" \
    "v_max_u32 v214, v224, v225\n\t" \
    "v_max3_u32 v214, v226, v227, v214\n\t" \
    "v_cmp_eq_u32 vcc, -1, v214\n\t" \
    "s_cbranch_vccz .Lv17_%=\n\t" \
    "buffer_load_dwordx4 v[224:227], v253, %[rs], s46 offen sc1\n\t" \
    ".Lv17_%=:\n\t" \
    "v_max_u32 v214, v228, v229\n\t" \
    "v_max3_u32 v214, v230, v231, v214\n\t" \
    "v_cmp_eq_u32 vcc, -1, v214\n\t" \
    "s_cbranch_vccz .Lv18_%=\n\t" \
    "buffer_load_dwordx4 v[228:231], v253, %[rs], s47 offen sc1\n\t" \
    ".Lv18_%=:\n\t" \
    "v_max_u32 v214, v232, v233\n\t" \
    "v_max3_u32 v214, v234, v235, v214\n\t" \
    "v_cmp_eq_u32 vcc, -1, v214\n\t" \
    "s_cbranch_vccz .Lv19_%=\n\t" \
    "buffer_load_dwordx4 v[232:235], v253, %[rs], s48 offen sc1\n\t" \
    ".Lv19_%=:\n\t" \
    "s_cmp_eq_u32 s41, 0\n\t" \
    "s_cbranch_scc1 .Lv16_%=\n\t" \
    "s_sub_u32 s41, s41, 1\n\t" \
    "s_waitcnt vmcnt(0)\n\t" \
    "s_branch .Lv15_%=\n\t" \
    ".Lv16_%=:\n\t" \
    LST2 \
    "ds_write_b128 v253, v[224:227] offset:43008\n\t" \
    "ds_write_b128 v253, v[228:231] offset:47104\n\t" \
    "ds_write_b128 v253, v[232:235] offset:44032\n\t" \
    "v_smfmac_f32_16x16x64_bf16 v[244:247], v[224:227], v[0:7], v212\n\t" \
    "v_smfmac_f32_16x16x64_bf16 v[248:251], v[224:227], v[8:15], v212\n\t" \
    "v_smfmac_f32_16x16x64_bf16 v[208:211], v[224:227], v[200:207], v212\n\t" \
    "s_waitcnt lgkmcnt(0)\n\t" \
    "s_barrier \n\t" \
    LST3 \
    "ds_read_b128 v[236:239], v253 offset:40960\n\t" \
    "ds_read_b128 v[240:243], v253 offset:46080\n\t" \
    "ds_read_b128 v[224:227], v253 offset:41984\n\t" \
    "ds_read_b128 v[200:203], v254 offset:45056\n\t" \
    "ds_read_b128 v[204:207], v254 offset:46080\n\t" \
    "v_smfmac_f32_16x16x64_bf16 v[208:211], v[228:231], v[16:23], v212\n\t" \
    "v_smfmac_f32_16x16x64_bf16 v[244:247], v[228:231], v[24:31], v212\n\t" \
    "v_smfmac_f32_16x16x64_bf16 v[248:251], v[228:231], v[32:39], v212\n\t" \
    "ds_read_b128 v[228:231], v253 offset:48128\n\t" \
    "v_smfmac_f32_16x16x64_bf16 v[208:211], v[232:235], v[216:223], v212\n\t" \
    "ds_read_b128 v[216:219], v254 offset:47104\n\t" \
    "ds_read_b128 v[220:223], v254 offset:48128\n\t" \
    "v_smfmac_f32_16x16x64_bf16 v[244:247], v[232:235], v[40:47], v212\n\t" \
    "v_smfmac_f32_16x16x64_bf16 v[248:251], v[232:235], v[48:55], v212\n\t" \
    "ds_read_b128 v[232:235], v253 offset:45056\n\t" \
    "s_waitcnt lgkmcnt(8)\n\t" \
    "v_smfmac_f32_16x16x64_bf16 v[208:211], v[236:239], v[56:63], v212\n\t" \
    "v_smfmac_f32_16x16x64_bf16 v[244:247], v[236:239], v[64:71], v212\n\t" \
    "v_smfmac_f32_16x16x64_bf16 v[248:251], v[236:239], v[72:79], v212\n\t" \
    "ds_read_b128 v[236:239], v253 offset:49152\n\t" \
    "s_waitcnt lgkmcnt(5)\n\t" \
    "v_smfmac_f32_16x16x64_bf16 v[208:211], v[240:243], v[200:207], v212\n\t" \
    "ds_read_b128 v[200:203], v254 offset:49152\n\t" \
    "ds_read_b128 v[204:207], v254 offset:50176\n\t" \
    "v_smfmac_f32_16x16x64_bf16 v[244:247], v[240:243], v[80:87], v212\n\t" \
    "v_smfmac_f32_16x16x64_bf16 v[248:251], v[240:243], v[88:95], v212\n\t" \
    "ds_read_b128 v[240:243], v253 offset:50176\n\t" \
    "v_smfmac_f32_16x16x64_bf16 v[244:247], v[224:227], v[104:111], v212\n\t" \
    "s_waitcnt lgkmcnt(7)\n\t" \
    "v_smfmac_f32_16x16x64_bf16 v[244:247], v[228:231], v[120:127], v212\n\t" \
    "s_waitcnt lgkmcnt(4)\n\t" \
    "v_smfmac_f32_16x16x64_bf16 v[244:247], v[232:235], v[144:151], v212\n\t" \
    "s_waitcnt lgkmcnt(3)\n\t" \
    "v_smfmac_f32_16x16x64_bf16 v[244:247], v[236:239], v[160:167], v212\n\t" \
    "s_waitcnt lgkmcnt(0)\n\t" \
    "v_smfmac_f32_16x16x64_bf16 v[244:247], v[240:243], v[184:191], v212\n\t" \
    "v_smfmac_f32_16x16x64_bf16 v[248:251], v[224:227], v[112:119], v212\n\t" \
    "v_smfmac_f32_16x16x64_bf16 v[248:251], v[228:231], v[128:135], v212\n\t" \
    "v_smfmac_f32_16x16x64_bf16 v[248:251], v[232:235], v[152:159], v212\n\t" \
    "v_add_f32 v244, v244, v246\n\t" \
    "v_add_f32 v245, v245, v247\n\t" \
    "v_smfmac_f32_16x16x64_bf16 v[248:251], v[236:239], v[168:175], v212\n\t" \
    "v_cvt_pk_bf16_f32 v244, v244, v245\n\t" \
    "ds_write_b16 v255, v244 offset:512\n\t" \
    "v_smfmac_f32_16x16x64_bf16 v[248:251], v[240:243], v[192:199], v212\n\t" \
    "ds_write_b16_d16_hi v255, v244 offset:528\n\t" \
    "v_smfmac_f32_16x16x64_bf16 v[208:211], v[224:227], v[96:103], v212\n\t" \
    "v_smfmac_f32_16x16x64_bf16 v[208:211], v[228:231], v[216:223], v212\n\t" \
    "v_smfmac_f32_16x16x64_bf16 v[208:211], v[232:235], v[136:143], v212\n\t" \
    "v_add_f32 v248, v248, v250\n\t" \
    "v_add_f32 v249, v249, v251\n\t" \
    "v_smfmac_f32_16x16x64_bf16 v[208:211], v[236:239], v[200:207], v212\n\t" \
    "v_cvt_pk_bf16_f32 v248, v248, v249\n\t" \
    "ds_write_b16 v255, v248 offset:1024\n\t" \
    "v_smfmac_f32_16x16x64_bf16 v[208:211], v[240:243], v[176:183], v212\n\t" \
    "ds_write_b16_d16_hi v255, v248 offset:1040\n\t" \
    "ds_read_b128 v[224:227], v253 offset:0\n\t" \
    "ds_read_b128 v[244:247], v254 offset:40960\n\t" \
    "ds_read_b128 v[228:231], v253 offset:20480\n\t" \
    "ds_read_b128 v[232:235], v253 offset:1024\n\t" \
    "ds_read_b128 v[236:239], v253 offset:21504\n\t" \
    "ds_read_b128 v[248:251], v254 offset:41984\n\t" \
    "s_nop 0\n\t" \
    "v_add_f32 v208, v208, v210\n\t" \
    "v_add_f32 v209, v209, v211\n\t" \
    "v_cvt_pk_bf16_f32 v208, v208, v209\n\t" \
    "ds_write_b16 v255, v208 offset:0\n\t" \
    "ds_write_b16_d16_hi v255, v208 offset:16\n\t" \
    "s_setprio 0\n\t" \
    "s_waitcnt lgkmcnt(0)\n\t" \
    "s_barrier \n\t" \
    LST4 \
    LFLUSH200 \
    "s_add_u32 s40, s40, 1\n\t" \
    "s_cmp_lg_u32 s40, 64\n\t" \
    "s_cbranch_scc1 .Lv11_%=\n\t" \
    "s_branch .Lend%=\n\t" \
    ".Lv0_%=:\n\t" \
    "s_mov_b32 s42, %[xs]\n\t" \
    "ds_read_b128 v[224:227], v253 offset:0\n\t" \
    "ds_read_b128 v[244:247], v254 offset:30720\n\t" \
    "ds_read_b128 v[228:231], v253 offset:20480\n\t" \
    "ds_read_b128 v[232:235], v253 offset:1024\n\t" \
    "ds_read_b128 v[236:239], v253 offset:21504\n\t" \
    "ds_read_b128 v[248:251], v254 offset:31744\n\t" \
    ".Lv01_%=:\n\t" \
    LST7 \
    "s_waitcnt lgkmcnt(5)\n\t" \
    "v_mfma_f32_16x16x32_bf16 v[204:207], v[0:3], v[224:227], 0\n\t" \
    "ds_read_b128 v[240:243], v253 offset:2048\n\t" \
    "v_mfma_f32_16x16x32_bf16 v[208:211], v[8:11], v[224:227], 0\n\t" \
    "s_waitcnt lgkmcnt(5)\n\t" \
    "v_mfma_f32_16x16x32_bf16 v[200:203], v[244:247], v[224:227], 0\n\t" \
    "s_waitcnt lgkmcnt(4)\n\t" \
    "v_mfma_f32_16x16x32_bf16 v[216:219], v[0:3], v[228:231], 0\n\t" \
    "ds_read_b128 v[224:227], v253 offset:22528\n\t" \
    "v_mfma_f32_16x16x32_bf16 v[220:223], v[8:11], v[228:231], 0\n\t" \
    "v_mfma_f32_16x16x32_bf16 v[212:215], v[244:247], v[228:231], 0\n\t" \
    "ds_read_b128 v[244:247], v254 offset:32768\n\t" \
    "s_waitcnt lgkmcnt(5)\n\t" \
    "v_mfma_f32_16x16x32_bf16 v[204:207], v[4:7], v[232:235], v[204:207]\n\t" \
    "ds_read_b128 v[228:231], v253 offset:3072\n\t" \
    "v_mfma_f32_16x16x32_bf16 v[208:211], v[12:15], v[232:235], v[208:211]\n\t" \
    "s_waitcnt lgkmcnt(4)\n\t" \
    "v_mfma_f32_16x16x32_bf16 v[200:203], v[248:251], v[232:235], v[200:203]\n\t" \
    "v_mfma_f32_16x16x32_bf16 v[216:219], v[4:7], v[236:239], v[216:219]\n\t" \
    "ds_read_b128 v[232:235], v253 offset:23552\n\t" \
    "v_mfma_f32_16x16x32_bf16 v[220:223], v[12:15], v[236:239], v[220:223]\n\t" \
    "v_mfma_f32_16x16x32_bf16 v[212:215], v[248:251], v[236:239], v[212:215]\n\t" \
    "ds_read_b128 v[248:251], v254 offset:33792\n\t" \
    "s_waitcnt lgkmcnt(5)\n\t" \
    "v_mfma_f32_16x16x32_bf16 v[204:207], v[40:43], v[240:243], v[204:207]\n\t" \
    "ds_read_b128 v[236:239], v253 offset:4096\n\t" \
    "v_mfma_f32_16x16x32_bf16 v[208:211], v[48:51], v[240:243], v[208:211]\n\t" \
    "s_waitcnt lgkmcnt(4)\n\t" \
    "v_mfma_f32_16x16x32_bf16 v[200:203], v[244:247], v[240:243], v[200:203]\n\t" \
    "v_mfma_f32_16x16x32_bf16 v[216:219], v[40:43], v[224:227], v[216:219]\n\t" \
    "ds_read_b128 v[240:243], v253 offset:24576\n\t" \
    "v_mfma_f32_16x16x32_bf16 v[220:223], v[48:51], v[224:227], v[220:223]\n\t" \
    "v_mfma_f32_16x16x32_bf16 v[212:215], v[244:247], v[224:227], v[212:215]\n\t" \
    "ds_read_b128 v[244:247], v254 offset:34816\n\t" \
    "s_waitcnt lgkmcnt(5)\n\t" \
    "v_mfma_f32_16x16x32_bf16 v[204:207], v[44:47], v[228:231], v[204:207]\n\t" \
    "ds_read_b128 v[224:227], v253 offset:5120\n\t" \
    "v_mfma_f32_16x16x32_bf16 v[208:211], v[52:55], v[228:231], v[208:211]\n\t" \
    "s_waitcnt lgkmcnt(4)\n\t" \
    "v_mfma_f32_16x16x32_bf16 v[200:203], v[248:251], v[228:231], v[200:203]\n\t" \
    "v_mfma_f32_16x16x32_bf16 v[216:219], v[44:47], v[232:235], v[216:219]\n\t" \
    "ds_read_b128 v[228:231], v253 offset:25600\n\t" \
    "v_mfma_f32_16x16x32_bf16 v[220:223], v[52:55], v[232:235], v[220:223]\n\t" \
    "v_mfma_f32_16x16x32_bf16 v[212:215], v[248:251], v[232:235], v[212:215]\n\t" \
    "ds_read_b128 v[248:251], v254 offset:35840\n\t" \
    "s_waitcnt lgkmcnt(5)\n\t" \
    "v_mfma_f32_16x16x32_bf16 v[204:207], v[80:83], v[236:239], v[204:207]\n\t" \
    "ds_read_b128 v[232:235], v253 offset:6144\n\t" \
    "s_add_u32 s44, s40, 1\n\t" \
    "v_mfma_f32_16x16x32_bf16 v[208:211], v[88:91], v[236:239], v[208:211]\n\t" \
    "s_and_b32 s45, s44, 15\n\t" \
    "s_waitcnt lgkmcnt(4)\n\t" \
    "v_mfma_f32_16x16x32_bf16 v[200:203], v[244:247], v[236:239], v[200:203]\n\t" \
    "s_cmp_eq_u32 s45, 0\n\t" \
    "v_mfma_f32_16x16x32_bf16 v[216:219], v[80:83], v[240:243], v[216:219]\n\t" \
    "ds_read_b128 v[236:239], v253 offset:26624\n\t" \
    "s_cselect_b32 s45, %[cj], %[cs]\n\t" \
    "v_mfma_f32_16x16x32_bf16 v[220:223], v[88:91], v[240:243], v[220:223]\n\t" \
    "s_cmp_eq_u32 s44, 64\n\t" \
    "v_mfma_f32_16x16x32_bf16 v[212:215], v[244:247], v[240:243], v[212:215]\n\t" \
    "ds_read_b128 v[244:247], v254 offset:36864\n\t" \
    "s_cselect_b32 s45, 0, s45\n\t" \
    "s_waitcnt lgkmcnt(5)\n\t" \
    "v_mfma_f32_16x16x32_bf16 v[204:207], v[84:87], v[224:227], v[204:207]\n\t" \
    "ds_read_b128 v[240:243], v253 offset:7168\n\t" \
    "s_add_u32 s42, s42, s45\n\t" \
    "v_mfma_f32_16x16x32_bf16 v[208:211], v[92:95], v[224:227], v[208:211]\n\t" \
    "s_waitcnt lgkmcnt(4)\n\t" \
    "v_mfma_f32_16x16x32_bf16 v[200:203], v[248:251], v[224:227], v[200:203]\n\t" \
    "v_mfma_f32_16x16x32_bf16 v[216:219], v[84:87], v[228:231], v[216:219]\n\t" \
    "ds_read_b128 v[224:227], v253 offset:27648\n\t" \
    "v_mfma_f32_16x16x32_bf16 v[220:223], v[92:95], v[228:231], v[220:223]\n\t" \
    "v_mfma_f32_16x16x32_bf16 v[212:215], v[248:251], v[228:231], v[212:215]\n\t" \
    "ds_read_b128 v[248:251], v254 offset:37888\n\t" \
    "s_waitcnt lgkmcnt(5)\n\t" \
    "v_mfma_f32_16x16x32_bf16 v[204:207], v[120:123], v[232:235], v[204:207]\n\t" \
    "ds_read_b128 v[228:231], v253 offset:8192\n\t" \
    "v_mfma_f32_16x16x32_bf16 v[208:211], v[128:131], v[232:235], v[208:211]\n\t" \
    "s_waitcnt lgkmcnt(4)\n\t" \
    "v_mfma_f32_16x16x32_bf16 v[200:203], v[244:247], v[232:235], v[200:203]\n\t" \
    "v_mfma_f32_16x16x32_bf16 v[216:219], v[120:123], v[236:239], v[216:219]\n\t" \
    "ds_read_b128 v[232:235], v253 offset:28672\n\t" \
    "v_mfma_f32_16x16x32_bf16 v[220:223], v[128:131], v[236:239], v[220:223]\n\t" \
    "v_mfma_f32_16x16x32_bf16 v[212:215], v[244:247], v[236:239], v[212:215]\n\t" \
    "ds_read_b128 v[244:247], v254 offset:38912\n\t" \
    "s_waitcnt lgkmcnt(5)\n\t" \
    "v_mfma_f32_16x16x32_bf16 v[204:207], v[124:127], v[240:243], v[204:207]\n\t" \
    "ds_read_b128 v[236:239], v253 offset:9216\n\t" \
    "v_mfma_f32_16x16x32_bf16 v[208:211], v[132:135], v[240:243], v[208:211]\n\t" \
    "s_waitcnt lgkmcnt(4)\n\t" \
    "v_mfma_f32_16x16x32_bf16 v[200:203], v[248:251], v[240:243], v[200:203]\n\t" \
    "v_mfma_f32_16x16x32_bf16 v[216:219], v[124:127], v[224:227], v[216:219]\n\t" \
    "ds_read_b128 v[240:243], v253 offset:29696\n\t" \
    "v_mfma_f32_16x16x32_bf16 v[220:223], v[132:135], v[224:227], v[220:223]\n\t" \
    "v_mfma_f32_16x16x32_bf16 v[212:215], v[248:251], v[224:227], v[212:215]\n\t" \
    "ds_read_b128 v[248:251], v254 offset:39936\n\t" \
    "s_waitcnt lgkmcnt(5)\n\t" \
    "v_mfma_f32_16x16x32_bf16 v[204:207], v[160:163], v[228:231], v[204:207]\n\t" \
    "ds_read_b128 v[224:227], v253 offset:10240\n\t" \
    "v_mfma_f32_16x16x32_bf16 v[208:211], v[168:171], v[228:231], v[208:211]\n\t" \
    "s_waitcnt lgkmcnt(4)\n\t" \
    "v_mfma_f32_16x16x32_bf16 v[200:203], v[244:247], v[228:231], v[200:203]\n\t" \
    "v_mfma_f32_16x16x32_bf16 v[216:219], v[160:163], v[232:235], v[216:219]\n\t" \
    "ds_read_b128 v[228:231], v253 offset:30720\n\t" \
    "v_mfma_f32_16x16x32_bf16 v[220:223], v[168:171], v[232:235], v[220:223]\n\t" \
    "v_mfma_f32_16x16x32_bf16 v[212:215], v[244:247], v[232:235], v[212:215]\n\t" \
    "s_waitcnt lgkmcnt(4)\n\t" \
    "v_mfma_f32_16x16x32_bf16 v[204:207], v[164:167], v[236:239], v[204:207]\n\t" \
    "ds_read_b128 v[232:235], v253 offset:11264\n\t" \
    "v_mfma_f32_16x16x32_bf16 v[208:211], v[172:175], v[236:239], v[208:211]\n\t" \
    "s_waitcnt lgkmcnt(3)\n\t" \
    "v_mfma_f32_16x16x32_bf16 v[200:203], v[248:251], v[236:239], v[200:203]\n\t" \
    "v_mfma_f32_16x16x32_bf16 v[216:219], v[164:167], v[240:243], v[216:219]\n\t" \
    "ds_read_b128 v[236:239], v253 offset:31744\n\t" \
    "v_mfma_f32_16x16x32_bf16 v[220:223], v[172:175], v[240:243], v[220:223]\n\t" \
    "v_mfma_f32_16x16x32_bf16 v[212:215], v[248:251], v[240:243], v[212:215]\n\t" \
    "s_waitcnt lgkmcnt(3)\n\t" \
    "v_mfma_f32_16x16x32_bf16 v[204:207], v[24:27], v[224:227], v[204:207]\n\t" \
    "ds_read_b128 v[240:243], v253 offset:12288\n\t" \
    "s_and_b32 s44, s40, 3\n\t" \
    "v_mfma_f32_16x16x32_bf16 v[208:211], v[32:35], v[224:227], v[208:211]\n\t" \
    "s_lshl_b32 s44, s44, 17\n\t" \
    "v_mfma_f32_16x16x32_bf16 v[200:203], v[16:19], v[224:227], v[200:203]\n\t" \
    "s_add_u32 s44, s44, %[mb]\n\t" \
    "s_waitcnt lgkmcnt(3)\n\t" \
    "v_mfma_f32_16x16x32_bf16 v[216:219], v[24:27], v[228:231], v[216:219]\n\t" \
    "ds_read_b128 v[224:227], v253 offset:32768\n\t" \
    "s_add_u32 s46, s44, 0x0\n\t" \
    "v_mfma_f32_16x16x32_bf16 v[220:223], v[32:35], v[228:231], v[220:223]\n\t" \
    "s_add_u32 s47, s44, 0x1400\n\t" \
    "v_mfma_f32_16x16x32_bf16 v[212:215], v[16:19], v[228:231], v[212:215]\n\t" \
    "s_add_u32 s48, s44, 0x400\n\t" \
    "s_waitcnt lgkmcnt(3)\n\t" \
    "v_mfma_f32_16x16x32_bf16 v[204:207], v[28:31], v[232:235], v[204:207]\n\t" \
    "ds_read_b128 v[228:231], v253 offset:13312\n\t" \
    "v_mfma_f32_16x16x32_bf16 v[208:211], v[36:39], v[232:235], v[208:211]\n\t" \
    "v_mfma_f32_16x16x32_bf16 v[200:203], v[20:23], v[232:235], v[200:203]\n\t" \
    "s_waitcnt lgkmcnt(3)\n\t" \
    "v_mfma_f32_16x16x32_bf16 v[216:219], v[28:31], v[236:239], v[216:219]\n\t" \
    "ds_read_b128 v[232:235], v253 offset:33792\n\t" \
    "v_mfma_f32_16x16x32_bf16 v[220:223], v[36:39], v[236:239], v[220:223]\n\t" \
    "v_mfma_f32_16x16x32_bf16 v[212:215], v[20:23], v[236:239], v[212:215]\n\t" \
    "s_waitcnt lgkmcnt(3)\n\t" \
    "v_mfma_f32_16x16x32_bf16 v[204:207], v[64:67], v[240:243], v[204:207]\n\t" \
    "ds_read_b128 v[236:239], v253 offset:14336\n\t" \
    "v_mfma_f32_16x16x32_bf16 v[208:211], v[72:75], v[240:243], v[208:211]\n\t" \
    "v_mfma_f32_16x16x32_bf16 v[200:203], v[56:59], v[240:243], v[200:203]\n\t" \
    "s_waitcnt lgkmcnt(3)\n\t" \
    "v_mfma_f32_16x16x32_bf16 v[216:219], v[64:67], v[224:227], v[216:219]\n\t" \
    "ds_read_b128 v[240:243], v253 offset:34816\n\t" \
    "v_mfma_f32_16x16x32_bf16 v[220:223], v[72:75], v[224:227], v[220:223]\n\t" \
    "v_mfma_f32_16x16x32_bf16 v[212:215], v[56:59], v[224:227], v[212:215]\n\t" \
    "s_waitcnt lgkmcnt(3)\n\t" \
    "v_mfma_f32_16x16x32_bf16 v[204:207], v[68:71], v[228:231], v[204:207]\n\t" \
    "ds_read_b128 v[224:227], v253 offset:15360\n\t" \
    "v_mfma_f32_16x16x32_bf16 v[208:211], v[76:79], v[228:231], v[208:211]\n\t" \
    "v_mfma_f32_16x16x32_bf16 v[200:203], v[60:63], v[228:231], v[200:203]\n\t" \
    "s_waitcnt lgkmcnt(3)\n\t" \
    "v_mfma_f32_16x16x32_bf16 v[216:219], v[68:71], v[232:235], v[216:219]\n\t" \
    "ds_read_b128 v[228:231], v253 offset:35840\n\t" \
    "v_mfma_f32_16x16x32_bf16 v[220:223], v[76:79], v[232:235], v[220:223]\n\t" \
    "v_mfma_f32_16x16x32_bf16 v[212:215], v[60:63], v[232:235], v[212:215]\n\t" \
    "s_cmp_eq_u32 %[fl], 0\n\t" \
    "s_cbranch_scc1 .Lv02_%=\n\t" \
    "v_and_b32 v252, 0xf0, v253\n\t" \
    "v_lshlrev_b32 v252, 3, v252\n\t" \
    "buffer_load_dword v252, v252, %[rs], %[fl] offen sc1\n\t" \
    ".Lv02_%=:\n\t" \
    "s_waitcnt lgkmcnt(3)\n\t" \
    "v_mfma_f32_16x16x32_bf16 v[204:207], v[104:107], v[236:239], v[204:207]\n\t" \
    "ds_read_b128 v[232:235], v253 offset:16384\n\t" \
    "v_mfma_f32_16x16x32_bf16 v[208:211], v[112:115], v[236:239], v[208:211]\n\t" \
    "v_mfma_f32_16x16x32_bf16 v[200:203], v[96:99], v[236:239], v[200:203]\n\t" \
    "s_waitcnt lgkmcnt(3)\n\t" \
    "v_mfma_f32_16x16x32_bf16 v[216:219], v[104:107], v[240:243], v[216:219]\n\t" \
    "ds_read_b128 v[236:239], v253 offset:17408\n\t" \
    "v_mfma_f32_16x16x32_bf16 v[220:223], v[112:115], v[240:243], v[220:223]\n\t" \
    "v_mfma_f32_16x16x32_bf16 v[212:215], v[96:99], v[240:243], v[212:215]\n\t" \
    "s_waitcnt lgkmcnt(3)\n\t" \
    "v_mfma_f32_16x16x32_bf16 v[204:207], v[108:111], v[224:227], v[204:207]\n\t" \
    "ds_read_b128 v[240:243], v253 offset:18432\n\t" \
    "v_mfma_f32_16x16x32_bf16 v[208:211], v[116:119], v[224:227], v[208:211]\n\t" \
    "v_mfma_f32_16x16x32_bf16 v[200:203], v[100:103], v[224:227], v[200:203]\n\t" \
    "s_waitcnt lgkmcnt(3)\n\t" \
    "v_mfma_f32_16x16x32_bf16 v[216:219], v[108:111], v[228:231], v[216:219]\n\t" \
    "ds_read_b128 v[224:227], v253 offset:19456\n\t" \
    "v_mfma_f32_16x16x32_bf16 v[220:223], v[116:119], v[228:231], v[220:223]\n\t" \
    "v_mfma_f32_16x16x32_bf16 v[212:215], v[100:103], v[228:231], v[212:215]\n\t" \
    "s_waitcnt lgkmcnt(3)\n\t" \
    "v_mfma_f32_16x16x32_bf16 v[204:207], v[144:147], v[232:235], v[204:207]\n\t" \
    "ds_read_b128 v[228:231], v253 offset:36864\n\t" \
    "v_mov_b64 v[244:245], 0\n\t" \
    "v_mfma_f32_16x16x32_bf16 v[208:211], v[152:155], v[232:235], v[208:211]\n\t" \
    "v_mov_b64 v[246:247], 0\n\t" \
    "v_mfma_f32_16x16x32_bf16 v[200:203], v[136:139], v[232:235], v[200:203]\n\t" \
    "v_mov_b64 v[248:249], 0\n\t" \
    "s_waitcnt lgkmcnt(3)\n\t" \
    "v_mfma_f32_16x16x32_bf16 v[204:207], v[148:151], v[236:239], v[204:207]\n\t" \
    "ds_read_b128 v[232:235], v253 offset:37888\n\t" \
    "v_mov_b64 v[250:251], 0\n\t" \
    "v_mfma_f32_16x16x32_bf16 v[208:211], v[156:159], v[236:239], v[208:211]\n\t" \
    "v_mfma_f32_16x16x32_bf16 v[200:203], v[140:143], v[236:239], v[200:203]\n\t" \
    "s_waitcnt lgkmcnt(3)\n\t" \
    "v_mfma_f32_16x16x32_bf16 v[204:207], v[184:187], v[240:243], v[204:207]\n\t" \
    "ds_read_b128 v[236:239], v253 offset:38912\n\t" \
    "v_mfma_f32_16x16x32_bf16 v[208:211], v[192:195], v[240:243], v[208:211]\n\t" \
    "v_mfma_f32_16x16x32_bf16 v[200:203], v[176:179], v[240:243], v[200:203]\n\t" \
    "s_waitcnt lgkmcnt(3)\n\t" \
    "v_mfma_f32_16x16x32_bf16 v[204:207], v[188:191], v[224:227], v[204:207]\n\t" \
    "ds_read_b128 v[240:243], v253 offset:39936\n\t" \
    "v_mfma_f32_16x16x32_bf16 v[208:211], v[196:199], v[224:227], v[208:211]\n\t" \
    "v_mfma_f32_16x16x32_bf16 v[200:203], v[180:183], v[224:227], v[200:203]\n\t" \
    "s_waitcnt lgkmcnt(3)\n\t" \
    "v_mfma_f32_16x16x32_bf16 v[216:219], v[144:147], v[228:231], v[216:219]\n\t" \
    "s_waitcnt lgkmcnt(2)\n\t" \
    "v_mfma_f32_16x16x32_bf16 v[216:219], v[148:151], v[232:235], v[216:219]\n\t" \
    "v_cvt_pk_bf16_f32 v204, v204, v205\n\t" \
    "v_cvt_pk_bf16_f32 v205, v206, v207\n\t" \
    "s_waitcnt lgkmcnt(1)\n\t" \
    "v_mfma_f32_16x16x32_bf16 v[216:219], v[184:187], v[236:239], v[216:219]\n\t" \
    "ds_write_b64 v254, v[204:205] offset:7168\n\t" \
    "v_cvt_pk_bf16_f32 v208, v208, v209\n\t" \
    "s_waitcnt lgkmcnt(1)\n\t" \
    "v_mfma_f32_16x16x32_bf16 v[216:219], v[188:191], v[240:243], v[216:219]\n\t" \
    "v_cvt_pk_bf16_f32 v209, v210, v211\n\t" \
    "ds_write_b64 v254, v[208:209] offset:8192\n\t" \
    "v_mfma_f32_16x16x32_bf16 v[220:223], v[152:155], v[228:231], v[220:223]\n\t" \
    "v_cvt_pk_bf16_f32 v200, v200, v201\n\t" \
    "v_cvt_pk_bf16_f32 v201, v202, v203\n\t" \
    "v_mfma_f32_16x16x32_bf16 v[220:223], v[156:159], v[232:235], v[220:223]\n\t" \
    "ds_write_b64 v254, v[200:201] offset:6144\n\t" \
    "ds_read_b128 v[200:203], v254 offset:30720\n\t" \
    "v_mfma_f32_16x16x32_bf16 v[220:223], v[192:195], v[236:239], v[220:223]\n\t" \
    "ds_read_b128 v[204:207], v254 offset:31744\n\t" \
    "v_mov_b64 v[208:209], 0\n\t" \
    "v_mfma_f32_16x16x32_bf16 v[220:223], v[196:199], v[240:243], v[220:223]\n\t" \
    "v_mov_b64 v[210:211], 0\n\t" \
    "v_cvt_pk_bf16_f32 v216, v216, v217\n\t" \
    "v_mfma_f32_16x16x32_bf16 v[212:215], v[136:139], v[228:231], v[212:215]\n\t" \
    "v_cvt_pk_bf16_f32 v217, v218, v219\n\t" \
    "ds_write_b64 v254, v[216:217] offset:13312\n\t" \
    "v_mfma_f32_16x16x32_bf16 v[212:215], v[140:143], v[232:235], v[212:215]\n\t" \
    "v_mfma_f32_16x16x32_bf16 v[212:215], v[176:179], v[236:239], v[212:215]\n\t" \
    "v_cvt_pk_bf16_f32 v220, v220, v221\n\t" \
    "v_cvt_pk_bf16_f32 v221, v222, v223\n\t" \
    "v_mfma_f32_16x16x32_bf16 v[212:215], v[180:183], v[240:243], v[212:215]\n\t" \
    "ds_write_b64 v254, v[220:221] offset:14336\n\t" \
    "ds_read_b128 v[216:219], v254 offset:32768\n\t" \
    "ds_read_b128 v[220:223], v254 offset:33792\n\t" \
    "s_nop 4\n\t" \
    "v_cvt_pk_bf16_f32 v212, v212, v213\n\t" \
    "v_cvt_pk_bf16_f32 v213, v214, v215\n\t" \
    "ds_write_b64 v254, v[212:213] offset:12288\n\t" \
    LST6 \
    "s_waitcnt vmcnt(0) lgkmcnt(0)\n\t" \
    "s_barrier \n\t" \
    "s_setprio 2\n\t" \
    "s_cmp_eq_u32 %[fl], 0\n\t" \
    "s_cbranch_scc1 .Lv04_%=\n\t" \
    "s_add_u32 s44, s40, 1\n\t" \
    "s_min_u32 s44, s44, 63\n\t" \
    ".Lv03_%=:\n\t" \
    "v_cmp_ge_u32 vcc, s44, v252\n\t" \
    "s_cbranch_vccz .Lv04_%=\n\t" \
    "s_cmp_eq_u32 s41, 0\n\t" \
    "s_cbranch_scc1 .Lv04_%=\n\t" \
    "s_sub_u32 s41, s41, 1\n\t" \
    "s_sleep 2\n\t" \
    "v_and_b32 v252, 0xf0, v253\n\t" \
    "v_lshlrev_b32 v252, 3, v252\n\t" \
    "buffer_load_dword v252, v252, %[rs], %[fl] offen sc1\n\t" \
    "s_waitcnt vmcnt(0)\n\t" \
    "s_branch .Lv03_%=\n\t" \
    ".Lv04_%=:\n\t" \
    "s_movk_i32 s45, 0x0\n\t" \
    "s_mov_b32 m0, s45\n\t" \
    "s_nop 0\n\t" \
    "buffer_load_dwordx4 v253, %[rs], s42 offen offset:0 sc1 lds\n\t" \
    "buffer_load_dwordx4 v253, %[rs], s42 offen offset:1024 sc1 lds\n\t" \
    "buffer_load_dwordx4 v253, %[rs], s42 offen offset:2048 sc1 lds\n\t" \
    "buffer_load_dwordx4 v253, %[rs], s42 offen offset:3072 sc1 lds\n\t" \
    "s_add_u32 s44, s42, 0x1000\n\t" \
    "s_movk_i32 s45, 0x5000\n\t" \
    "s_mov_b32 m0, s45\n\t" \
    "s_nop 0\n\t" \
    "buffer_load_dwordx4 v253, %[rs], s44 offen offset:0 sc1 lds\n\t" \
    "buffer_load_dwordx4 v253, %[rs], s44 offen offset:1024 sc1 lds\n\t" \
    "buffer_load_dwordx4 v253, %[rs], s44 offen offset:2048 sc1 lds\n\t" \
    "buffer_load_dwordx4 v253, %[rs], s44 offen offset:3072 sc1 lds\n\t" \
    "s_add_u32 s44, s42, 0x10000\n\t" \
    "s_mov_b32 s45, 0x4000\n\t" \
    "s_mov_b32 m0, s45\n\t" \
    "s_nop 0\n\t" \
    "buffer_load_dwordx4 v253, %[rs], s44 offen offset:0 sc1 lds\n\t" \
    "s_add_u32 s44, s44, 0x1000\n\t" \
    "s_mov_b32 s45, 0x9000\n\t" \
    "s_mov_b32 m0, s45\n\t" \
    "s_nop 0\n\t" \
    "buffer_load_dwordx4 v253, %[rs], s44 offen offset:0 sc1 lds\n\t" \
    LST1 \
    "buffer_load_dwordx4 v[224:227], v253, %[rs], s46 offen sc1\n\t" \
    "buffer_load_dwordx4 v[228:231], v253, %[rs], s47 offen sc1\n\t" \
    "buffer_load_dwordx4 v[232:235], v253, %[rs], s48 offen sc1\n\t" \
    "s_waitcnt lgkmcnt(0)\n\t" \
    "v_bfe_i32 v212, v253, 5, 1\n\t" \
    "v_and_b32 v212, 0xaaaa, v212\n\t" \
    "v_or_b32 v212, 0x4444, v212\n\t" \
    ".Lv05_%=:\n\t" \
    "s_waitcnt vmcnt(2)\n\t" \
    "v_max_u32 v214, v224, v225\n\t" \
    "v_max3_u32 v214, v226, v227, v214\n\t" \
    "s_waitcnt vmcnt(1)\n\t" \
    "v_max3_u32 v214, v228, v229, v214\n\t" \
    "v_max3_u32 v214, v230, v231, v214\n\t" \
    "s_waitcnt vmcnt(0)\n\t" \
    "v_max3_u32 v214, v232, v233, v214\n\t" \
    "v_max3_u32 v214, v234, v235, v214\n\t" \
    "v_cmp_eq_u32 vcc, -1, v214\n\t" \
    "s_cbranch_vccz .Lv06_%=\n\t" \
    "v_max_u32 v214, v224, v225\n\t" \
    "v_max3_u32 v214, v226, v227, v214\n\t" \
    "v_cmp_eq_u32 vcc, -1, v214\n\t" \
    "s_cbranch_vccz .Lv07_%=\n\t" \
    "buffer_load_dwordx4 v[224:227], v253, %[rs], s46 offen sc1\n\t" \
    ".Lv07_%=:\n\t" \
    "v_max_u32 v214, v228, v229\n\t" \
    "v_max3_u32 v214, v230, v231, v214\n\t" \
    "v_cmp_eq_u32 vcc, -1, v214\n\t" \
    "s_cbranch_vccz .Lv08_%=\n\t" \
    "buffer_load_dwordx4 v[228:231], v253, %[rs], s47 offen sc1\n\t" \
    ".Lv08_%=:\n\t" \
    "v_max_u32 v214, v232, v233\n\t" \
    "v_max3_u32 v214, v234, v235, v214\n\t" \
    "v_cmp_eq_u32 vcc, -1, v214\n\t" \
    "s_cbranch_vccz .Lv09_%=\n\t" \
    "buffer_load_dwordx4 v[232:235], v253, %[rs], s48 offen sc1\n\t" \
    ".Lv09_%=:\n\t" \
    "s_cmp_eq_u32 s41, 0\n\t" \
    "s_cbranch_scc1 .Lv06_%=\n\t" \
    "s_sub_u32 s41, s41, 1\n\t" \
    "s_waitcnt vmcnt(0)\n\t" \
    "s_branch .Lv05_%=\n\t" \
    ".Lv06_%=:\n\t" \
    LST2 \
    "ds_write_b128 v253, v[224:227] offset:40960\n\t" \
    "ds_write_b128 v253, v[228:231] offset:46080\n\t" \
    "ds_write_b128 v253, v[232:235] offset:41984\n\t" \
    "v_smfmac_f32_16x16x64_bf16 v[244:247], v[224:227], v[0:7], v212\n\t" \
    "v_smfmac_f32_16x16x64_bf16 v[248:251], v[224:227], v[8:15], v212\n\t" \
    "v_smfmac_f32_16x16x64_bf16 v[208:211], v[224:227], v[200:207], v212\n\t" \
    "s_waitcnt lgkmcnt(0)\n\t" \
    "s_barrier \n\t" \
    LST3 \
    "ds_read_b128 v[236:239], v253 offset:47104\n\t" \
    "ds_read_b128 v[240:243], v253 offset:43008\n\t" \
    "ds_read_b128 v[224:227], v253 offset:48128\n\t" \
    "ds_read_b128 v[200:203], v254 offset:34816\n\t" \
    "ds_read_b128 v[204:207], v254 offset:35840\n\t" \
    "v_smfmac_f32_16x16x64_bf16 v[208:211], v[228:231], v[16:23], v212\n\t" \
    "v_smfmac_f32_16x16x64_bf16 v[244:247], v[228:231], v[24:31], v212\n\t" \
    "v_smfmac_f32_16x16x64_bf16 v[248:251], v[228:231], v[32:39], v212\n\t" \
    "ds_read_b128 v[228:231], v253 offset:44032\n\t" \
    "v_smfmac_f32_16x16x64_bf16 v[208:211], v[232:235], v[216:223], v212\n\t" \
    "ds_read_b128 v[216:219], v254 offset:36864\n\t" \
    "ds_read_b128 v[220:223], v254 offset:37888\n\t" \
    "v_smfmac_f32_16x16x64_bf16 v[244:247], v[232:235], v[40:47], v212\n\t" \
    "v_smfmac_f32_16x16x64_bf16 v[248:251], v[232:235], v[48:55], v212\n\t" \
    "ds_read_b128 v[232:235], v253 offset:49152\n\t" \
    "s_waitcnt lgkmcnt(8)\n\t" \
    "v_smfmac_f32_16x16x64_bf16 v[208:211], v[236:239], v[56:63], v212\n\t" \
    "v_smfmac_f32_16x16x64_bf16 v[244:247], v[236:239], v[64:71], v212\n\t" \
    "v_smfmac_f32_16x16x64_bf16 v[248:251], v[236:239], v[72:79], v212\n\t" \
    "ds_read_b128 v[236:239], v253 offset:45056\n\t" \
    "s_waitcnt lgkmcnt(5)\n\t" \
    "v_smfmac_f32_16x16x64_bf16 v[208:211], v[240:243], v[200:207], v212\n\t" \
    "ds_read_b128 v[200:203], v254 offset:38912\n\t" \
    "ds_read_b128 v[204:207], v254 offset:39936\n\t" \
    "v_smfmac_f32_16x16x64_bf16 v[244:247], v[240:243], v[80:87], v212\n\t" \
    "v_smfmac_f32_16x16x64_bf16 v[248:251], v[240:243], v[88:95], v212\n\t" \
    "ds_read_b128 v[240:243], v253 offset:50176\n\t" \
    "v_smfmac_f32_16x16x64_bf16 v[244:247], v[224:227], v[104:111], v212\n\t" \
    "s_waitcnt lgkmcnt(7)\n\t" \
    "v_smfmac_f32_16x16x64_bf16 v[244:247], v[228:231], v[120:127], v212\n\t" \
    "s_waitcnt lgkmcnt(4)\n\t" \
    "v_smfmac_f32_16x16x64_bf16 v[244:247], v[232:235], v[144:151], v212\n\t" \
    "s_waitcnt lgkmcnt(3)\n\t" \
    "v_smfmac_f32_16x16x64_bf16 v[244:247], v[236:239], v[160:167], v212\n\t" \
    "s_waitcnt lgkmcnt(0)\n\t" \
    "v_smfmac_f32_16x16x64_bf16 v[244:247], v[240:243], v[184:191], v212\n\t" \
    "v_smfmac_f32_16x16x64_bf16 v[248:251], v[224:227], v[112:119], v212\n\t" \
    "v_smfmac_f32_16x16x64_bf16 v[248:251], v[228:231], v[128:135], v212\n\t" \
    "v_smfmac_f32_16x16x64_bf16 v[248:251], v[232:235], v[152:159], v212\n\t" \
    "v_add_f32 v244, v244, v246\n\t" \
    "v_add_f32 v245, v245, v247\n\t" \
    "v_smfmac_f32_16x16x64_bf16 v[248:251], v[236:239], v[168:175], v212\n\t" \
    "v_cvt_pk_bf16_f32 v244, v244, v245\n\t" \
    "ds_write_b16 v255, v244 offset:512\n\t" \
    "v_smfmac_f32_16x16x64_bf16 v[248:251], v[240:243], v[192:199], v212\n\t" \
    "ds_write_b16_d16_hi v255, v244 offset:528\n\t" \
    "v_smfmac_f32_16x16x64_bf16 v[208:211], v[224:227], v[96:103], v212\n\t" \
    "v_smfmac_f32_16x16x64_bf16 v[208:211], v[228:231], v[216:223], v212\n\t" \
    "v_smfmac_f32_16x16x64_bf16 v[208:211], v[232:235], v[136:143], v212\n\t" \
    "v_add_f32 v248, v248, v250\n\t" \
    "v_add_f32 v249, v249, v251\n\t" \
    "v_smfmac_f32_16x16x64_bf16 v[208:211], v[236:239], v[200:207], v212\n\t" \
    "v_cvt_pk_bf16_f32 v248, v248, v249\n\t" \
    "ds_write_b16 v255, v248 offset:1024\n\t" \
    "v_smfmac_f32_16x16x64_bf16 v[208:211], v[240:243], v[176:183], v212\n\t" \
    "ds_write_b16_d16_hi v255, v248 offset:1040\n\t" \
    "ds_read_b128 v[224:227], v253 offset:0\n\t" \
    "ds_read_b128 v[244:247], v254 offset:30720\n\t" \
    "ds_read_b128 v[228:231], v253 offset:20480\n\t" \
    "ds_read_b128 v[232:235], v253 offset:1024\n\t" \
    "ds_read_b128 v[236:239], v253 offset:21504\n\t" \
    "ds_read_b128 v[248:251], v254 offset:31744\n\t" \
    "s_nop 0\n\t" \
    "v_add_f32 v208, v208, v210\n\t" \
    "v_add_f32 v209, v209, v211\n\t" \
    "v_cvt_pk_bf16_f32 v208, v208, v209\n\t" \
    "ds_write_b16 v255, v208 offset:0\n\t" \
    "ds_write_b16_d16_hi v255, v208 offset:16\n\t" \
    "s_setprio 0\n\t" \
    "s_waitcnt lgkmcnt(0)\n\t" \
    "s_barrier \n\t" \
    LST4 \
    LFLUSH200 \
    "s_add_u32 s40, s40, 1\n\t" \
    "s_cmp_lg_u32 s40, 64\n\t" \
    "s_cbranch_scc1 .Lv01_%=\n\t" \
    "s_branch .Lend%=\n\t" \
    ".Lend%=:\n\t" \
    "s_waitcnt vmcnt(0) lgkmcnt(0)\n\t" \
    "s_mov_b32 %[bud], s41\n\t"
#define ASM_RCRIT \
    "s_movk_i32 %[n], 0x400\n\t" \
    ".Lfa%=:\n\t" \
    "v_max_u32 v196, v180, v181\n\t" \
    "v_max3_u32 v196, v182, v183, v196\n\t" \
    "v_cmp_eq_u32 vcc, -1, v196\n\t" \
    "s_cbranch_vccz .Lha%=\n\t" \
    "buffer_load_dwordx4 v[180:183], %[ov], %[rs], %[sv] offen sc1\n\t" \
    "s_waitcnt vmcnt(12)\n\t" \
    "v_max_u32 v196, v180, v181\n\t" \
    "v_max3_u32 v196, v182, v183, v196\n\t" \
    "v_cmp_eq_u32 vcc, -1, v196\n\t" \
    "s_cbranch_vccz .Lha%=\n\t" \
    "s_nop 7\n\t" \
    "v_max_u32 v196, v180, v181\n\t" \
    "v_max3_u32 v196, v182, v183, v196\n\t" \
    "v_cmp_eq_u32 vcc, -1, v196\n\t" \
    "s_cbranch_vccz .Lha%=\n\t" \
    "s_nop 7\n\t" \
    "v_max_u32 v196, v180, v181\n\t" \
    "v_max3_u32 v196, v182, v183, v196\n\t" \
    "v_cmp_eq_u32 vcc, -1, v196\n\t" \
    "s_cbranch_vccz .Lha%=\n\t" \
    "s_nop 7\n\t" \
    "v_max_u32 v196, v180, v181\n\t" \
    "v_max3_u32 v196, v182, v183, v196\n\t" \
    "v_cmp_eq_u32 vcc, -1, v196\n\t" \
    "s_cbranch_vccz .Lha%=\n\t" \
    "s_nop 7\n\t" \
    "s_sub_u32 %[n], %[n], 1\n\t" \
    "s_cmp_lg_u32 %[n], 0\n\t" \
    "s_cbranch_scc1 .Lfa%=\n\t" \
    ".Lha%=:\n\t" \
    "s_cmp_eq_u32 %[n], 0\n\t" \
    "s_cbranch_scc1 .Lre%=\n\t" \
    ".Lfb%=:\n\t" \
    "s_mov_b32 %[a], 0\n\t" \
    "v_max_u32 v196, v184, v185\n\t" \
    "v_max3_u32 v196, v186, v187, v196\n\t" \
    "v_cmp_eq_u32 vcc, -1, v196\n\t" \
    "s_cbranch_vccz .Lgb0_%=\n\t" \
    "buffer_load_dwordx4 v[184:187], %[o0], %[rs], %[sp] offen sc1\n\t" \
    "s_mov_b32 %[a], 1\n\t" \
    ".Lgb0_%=:\n\t" \
    "v_max_u32 v196, v188, v189\n\t" \
    "v_max3_u32 v196, v190, v191, v196\n\t" \
    "v_cmp_eq_u32 vcc, -1, v196\n\t" \
    "s_cbranch_vccz .Lgb1_%=\n\t" \
    "buffer_load_dwordx4 v[188:191], %[o1], %[rs], %[sp] offen sc1\n\t" \
    "s_mov_b32 %[a], 1\n\t" \
    ".Lgb1_%=:\n\t" \
    "v_max_u32 v196, v192, v193\n\t" \
    "v_max3_u32 v196, v194, v195, v196\n\t" \
    "v_cmp_eq_u32 vcc, -1, v196\n\t" \
    "s_cbranch_vccz .Lgb2_%=\n\t" \
    "buffer_load_dwordx4 v[192:195], %[o2], %[rs], %[sp] offen sc1\n\t" \
    "s_mov_b32 %[a], 1\n\t" \
    ".Lgb2_%=:\n\t" \
    "s_cmp_eq_u32 %[a], 0\n\t" \
    "s_cbranch_scc1 .Lhb%=\n\t" \
    "s_waitcnt vmcnt(12)\n\t" \
    "v_max_u32 v196, v184, v185\n\t" \
    "v_max3_u32 v196, v186, v187, v196\n\t" \
    "v_max3_u32 v196, v188, v189, v196\n\t" \
    "v_max3_u32 v196, v190, v191, v196\n\t" \
    "v_max3_u32 v196, v192, v193, v196\n\t" \
    "v_max3_u32 v196, v194, v195, v196\n\t" \
    "v_cmp_eq_u32 vcc, -1, v196\n\t" \
    "s_cbranch_vccz .Lhb%=\n\t" \
    "s_nop 7\n\t" \
    "v_max_u32 v196, v184, v185\n\t" \
    "v_max3_u32 v196, v186, v187, v196\n\t" \
    "v_max3_u32 v196, v188, v189, v196\n\t" \
    "v_max3_u32 v196, v190, v191, v196\n\t" \
    "v_max3_u32 v196, v192, v193, v196\n\t" \
    "v_max3_u32 v196, v194, v195, v196\n\t" \
    "v_cmp_eq_u32 vcc, -1, v196\n\t" \
    "s_cbranch_vccz .Lhb%=\n\t" \
    "s_nop 7\n\t" \
    "v_max_u32 v196, v184, v185\n\t" \
    "v_max3_u32 v196, v186, v187, v196\n\t" \
    "v_max3_u32 v196, v188, v189, v196\n\t" \
    "v_max3_u32 v196, v190, v191, v196\n\t" \
    "v_max3_u32 v196, v192, v193, v196\n\t" \
    "v_max3_u32 v196, v194, v195, v196\n\t" \
    "v_cmp_eq_u32 vcc, -1, v196\n\t" \
    "s_cbranch_vccz .Lhb%=\n\t" \
    "s_nop 7\n\t" \
    "v_max_u32 v196, v184, v185\n\t" \
    "v_max3_u32 v196, v186, v187, v196\n\t" \
    "v_max3_u32 v196, v188, v189, v196\n\t" \
    "v_max3_u32 v196, v190, v191, v196\n\t" \
    "v_max3_u32 v196, v192, v193, v196\n\t" \
    "v_max3_u32 v196, v194, v195, v196\n\t" \
    "v_cmp_eq_u32 vcc, -1, v196\n\t" \
    "s_cbranch_vccz .Lhb%=\n\t" \
    "s_nop 7\n\t" \
    "v_max_u32 v196, v184, v185\n\t" \
    "v_max3_u32 v196, v186, v187, v196\n\t" \
    "v_max3_u32 v196, v188, v189, v196\n\t" \
    "v_max3_u32 v196, v190, v191, v196\n\t" \
    "v_max3_u32 v196, v192, v193, v196\n\t" \
    "v_max3_u32 v196, v194, v195, v196\n\t" \
    "v_cmp_eq_u32 vcc, -1, v196\n\t" \
    "s_cbranch_vccz .Lhb%=\n\t" \
    "s_nop 7\n\t" \
    "v_max_u32 v196, v184, v185\n\t" \
    "v_max3_u32 v196, v186, v187, v196\n\t" \
    "v_max3_u32 v196, v188, v189, v196\n\t" \
    "v_max3_u32 v196, v190, v191, v196\n\t" \
    "v_max3_u32 v196, v192, v193, v196\n\t" \
    "v_max3_u32 v196, v194, v195, v196\n\t" \
    "v_cmp_eq_u32 vcc, -1, v196\n\t" \
    "s_cbranch_vccz .Lhb%=\n\t" \
    "s_nop 7\n\t" \
    "v_max_u32 v196, v184, v185\n\t" \
    "v_max3_u32 v196, v186, v187, v196\n\t" \
    "v_max3_u32 v196, v188, v189, v196\n\t" \
    "v_max3_u32 v196, v190, v191, v196\n\t" \
    "v_max3_u32 v196, v192, v193, v196\n\t" \
    "v_max3_u32 v196, v194, v195, v196\n\t" \
    "v_cmp_eq_u32 vcc, -1, v196\n\t" \
    "s_cbranch_vccz .Lhb%=\n\t" \
    "s_nop 7\n\t" \
    "v_max_u32 v196, v184, v185\n\t" \
    "v_max3_u32 v196, v186, v187, v196\n\t" \
    "v_max3_u32 v196, v188, v189, v196\n\t" \
    "v_max3_u32 v196, v190, v191, v196\n\t" \
    "v_max3_u32 v196, v192, v193, v196\n\t" \
    "v_max3_u32 v196, v194, v195, v196\n\t" \
    "v_cmp_eq_u32 vcc, -1, v196\n\t" \
    "s_cbranch_vccz .Lhb%=\n\t" \
    "s_nop 7\n\t" \
    "s_sub_u32 %[n], %[n], 1\n\t" \
    "s_cmp_lg_u32 %[n], 0\n\t" \
    "s_cbranch_scc1 .Lfb%=\n\t" \
    "s_branch .Lre%=\n\t" \
    ".Lhb%=:\n\t" \
    ASM_ST2 \
    "v_mfma_f32_16x16x32_bf16 v[176:179], v[192:195], v[180:183], 0\n\t" \
    "v_smfmac_f32_16x16x64_bf16 v[176:179], v[184:187], v[160:167], %[sidx]\n\t" \
    "v_smfmac_f32_16x16x64_bf16 v[176:179], v[188:191], v[168:175], %[sidx]\n\t" \
    "s_cmp_eq_u32 %[pub], 0\n\t" \
    "s_nop 7\n\t" \
    "v_add_f32 v176, v176, v178\n\t" \
    "v_add_f32 v177, v177, v179\n\t" \
    "ds_write2_b32 %[rw], v176, v177 offset1:80\n\t" \
    "s_waitcnt lgkmcnt(0)\n\t" \
    "s_barrier\n\t" ASM_ST \
    "s_cbranch_scc1 .Lre%=\n\t" \
    "ds_read_b128 v[200:203], %[rd] offset:0\n\t" \
    "ds_read_b128 v[204:207], %[rd] offset:16\n\t" \
    "ds_read_b128 v[208:211], %[rd] offset:32\n\t" \
    "ds_read_b128 v[212:215], %[rd] offset:48\n\t" \
    "s_waitcnt lgkmcnt(2)\n\t" \
    "v_pk_add_f32 v[200:201], v[200:201], v[202:203]\n\t" \
    "v_pk_add_f32 v[204:205], v[204:205], v[206:207]\n\t" \
    "s_waitcnt lgkmcnt(0)\n\t" \
    "v_pk_add_f32 v[208:209], v[208:209], v[210:211]\n\t" \
    "v_pk_add_f32 v[212:213], v[212:213], v[214:215]\n\t" \
    "v_pk_add_f32 v[200:201], v[200:201], v[204:205]\n\t" \
    "v_pk_add_f32 v[208:209], v[208:209], v[212:213]\n\t" \
    "v_pk_add_f32 v[200:201], v[200:201], v[208:209]\n\t" \
    "v_cvt_pk_bf16_f32 v200, v200, v201\n\t" \
    "v_min_u32 v200, -2, v200\n\t" \
    "buffer_store_dword v200, %[oo], %[rs], %[so] offen sc1\n\t" \
    RPUB2 \
    ".Lre%=:\n\t"
#define ASM_RX \
    "s_cmp_eq_u32 %[fin], 0\n\t" \
    "s_cbranch_scc1 .Lx0%=\n\t" \
    "buffer_load_dwordx2 v[250:251], %[xo], %[rd], %[sx] offen sc1\n\t" \
    ".Lx0%=:\n\t" \
    "buffer_load_dwordx4 v[200:203], %[l16], %[rs], %[s0] offen offset:0 sc1\n\t" \
    "buffer_load_dwordx4 v[204:207], %[l16], %[rs], %[s0] offen offset:1024 sc1\n\t" \
    "buffer_load_dwordx4 v[208:211], %[l16], %[rs], %[s0] offen offset:2048 sc1\n\t" \
    "buffer_load_dwordx4 v[212:215], %[l16], %[rs], %[s0] offen offset:3072 sc1\n\t" \
    "buffer_load_dwordx4 v[216:219], %[l16], %[rs], %[s1] offen offset:0 sc1\n\t" \
    "buffer_load_dwordx4 v[220:223], %[l16], %[rs], %[s1] offen offset:1024 sc1\n\t" \
    "buffer_load_dwordx4 v[224:227], %[l16], %[rs], %[s1] offen offset:2048 sc1\n\t" \
    "buffer_load_dwordx4 v[228:231], %[l16], %[rs], %[s1] offen offset:3072 sc1\n\t" \
    "buffer_load_dword v248, %[fo], %[rf], %[fs] offen sc1\n\t" \
    "v_mov_b64 v[232:233], v[160:161]\n\t" \
    "v_mov_b64 v[234:235], v[164:165]\n\t" \
    "v_mov_b64 v[236:237], v[168:169]\n\t" \
    "v_mov_b64 v[238:239], v[172:173]\n\t" \
    "s_movk_i32 %[n], 0x200\n\t" \
    "s_nop 1\n\t" \
    "v_permlane16_swap_b32 v232, v234\n\t" \
    "v_permlane16_swap_b32 v233, v235\n\t" \
    "v_permlane16_swap_b32 v236, v238\n\t" \
    "v_permlane16_swap_b32 v237, v239\n\t" \
    "s_waitcnt vmcnt(8)\n\t" \
    "v_max_u32 v196, v200, v201\n\t" \
    "v_max3_u32 v196, v202, v203, v196\n\t" \
    "s_waitcnt vmcnt(7)\n\t" \
    "v_max3_u32 v196, v204, v205, v196\n\t" \
    "v_max3_u32 v196, v206, v207, v196\n\t" \
    "s_waitcnt vmcnt(6)\n\t" \
    "v_max3_u32 v196, v208, v209, v196\n\t" \
    "v_max3_u32 v196, v210, v211, v196\n\t" \
    "s_waitcnt vmcnt(5)\n\t" \
    "v_max3_u32 v196, v212, v213, v196\n\t" \
    "v_max3_u32 v196, v214, v215, v196\n\t" \
    "s_waitcnt vmcnt(4)\n\t" \
    "v_max3_u32 v196, v216, v217, v196\n\t" \
    "v_max3_u32 v196, v218, v219, v196\n\t" \
    "s_waitcnt vmcnt(3)\n\t" \
    "v_max3_u32 v196, v220, v221, v196\n\t" \
    "v_max3_u32 v196, v222, v223, v196\n\t" \
    "s_waitcnt vmcnt(2)\n\t" \
    "v_max3_u32 v196, v224, v225, v196\n\t" \
    "v_max3_u32 v196, v226, v227, v196\n\t" \
    "s_waitcnt vmcnt(1)\n\t" \
    "v_max3_u32 v196, v228, v229, v196\n\t" \
    "v_max3_u32 v196, v230, v231, v196\n\t" \
    "v_cmp_eq_u32 vcc, -1, v196\n\t" \
    "s_cbranch_vccz .Lxd%=\n\t" \
    ".Lxl%=:\n\t" \
    "s_mov_b32 %[a], 0\n\t" \
    "v_max_u32 v196, v200, v201\n\t" \
    "v_max3_u32 v196, v202, v203, v196\n\t" \
    "v_cmp_eq_u32 vcc, -1, v196\n\t" \
    "s_cbranch_vccz .Lxs0_%=\n\t" \
    "buffer_load_dwordx4 v[200:203], %[l16], %[rs], %[s0] offen offset:0 sc1\n\t" \
    "s_mov_b32 %[a], 1\n\t" \
    ".Lxs0_%=:\n\t" \
    "v_max_u32 v196, v204, v205\n\t" \
    "v_max3_u32 v196, v206, v207, v196\n\t" \
    "v_cmp_eq_u32 vcc, -1, v196\n\t" \
    "s_cbranch_vccz .Lxs1_%=\n\t" \
    "buffer_load_dwordx4 v[204:207], %[l16], %[rs], %[s0] offen offset:1024 sc1\n\t" \
    "s_mov_b32 %[a], 1\n\t" \
    ".Lxs1_%=:\n\t" \
    "v_max_u32 v196, v208, v209\n\t" \
    "v_max3_u32 v196, v210, v211, v196\n\t" \
    "v_cmp_eq_u32 vcc, -1, v196\n\t" \
    "s_cbranch_vccz .Lxs2_%=\n\t" \
    "buffer_load_dwordx4 v[208:211], %[l16], %[rs], %[s0] offen offset:2048 sc1\n\t" \
    "s_mov_b32 %[a], 1\n\t" \
    ".Lxs2_%=:\n\t" \
    "v_max_u32 v196, v212, v213\n\t" \
    "v_max3_u32 v196, v214, v215, v196\n\t" \
    "v_cmp_eq_u32 vcc, -1, v196\n\t" \
    "s_cbranch_vccz .Lxs3_%=\n\t" \
    "buffer_load_dwordx4 v[212:215], %[l16], %[rs], %[s0] offen offset:3072 sc1\n\t" \
    "s_mov_b32 %[a], 1\n\t" \
    ".Lxs3_%=:\n\t" \
    "v_max_u32 v196, v216, v217\n\t" \
    "v_max3_u32 v196, v218, v219, v196\n\t" \
    "v_cmp_eq_u32 vcc, -1, v196\n\t" \
    "s_cbranch_vccz .Lxs4_%=\n\t" \
    "buffer_load_dwordx4 v[216:219], %[l16], %[rs], %[s1] offen offset:0 sc1\n\t" \
    "s_mov_b32 %[a], 1\n\t" \
    ".Lxs4_%=:\n\t" \
    "v_max_u32 v196, v220, v221\n\t" \
    "v_max3_u32 v196, v222, v223, v196\n\t" \
    "v_cmp_eq_u32 vcc, -1, v196\n\t" \
    "s_cbranch_vccz .Lxs5_%=\n\t" \
    "buffer_load_dwordx4 v[220:223], %[l16], %[rs], %[s1] offen offset:1024 sc1\n\t" \
    "s_mov_b32 %[a], 1\n\t" \
    ".Lxs5_%=:\n\t" \
    "v_max_u32 v196, v224, v225\n\t" \
    "v_max3_u32 v196, v226, v227, v196\n\t" \
    "v_cmp_eq_u32 vcc, -1, v196\n\t" \
    "s_cbranch_vccz .Lxs6_%=\n\t" \
    "buffer_load_dwordx4 v[224:227], %[l16], %[rs], %[s1] offen offset:2048 sc1\n\t" \
    "s_mov_b32 %[a], 1\n\t" \
    ".Lxs6_%=:\n\t" \
    "v_max_u32 v196, v228, v229\n\t" \
    "v_max3_u32 v196, v230, v231, v196\n\t" \
    "v_cmp_eq_u32 vcc, -1, v196\n\t" \
    "s_cbranch_vccz .Lxs7_%=\n\t" \
    "buffer_load_dwordx4 v[228:231], %[l16], %[rs], %[s1] offen offset:3072 sc1\n\t" \
    "s_mov_b32 %[a], 1\n\t" \
    ".Lxs7_%=:\n\t" \
    "s_cmp_eq_u32 %[a], 0\n\t" \
    "s_cbranch_scc1 .Lxd%=\n\t" \
    "s_waitcnt vmcnt(0)\n\t" \
    "s_sub_u32 %[n], %[n], 1\n\t" \
    "s_cmp_lg_u32 %[n], 0\n\t" \
    "s_cbranch_scc1 .Lxl%=\n\t" \
    "s_branch .Lxe%=\n\t" \
    ".Lxd%=:\n\t" \
    ASM_ST2 \
    "v_mfma_f32_16x16x32_bf16 v[244:247], v[160:163], v[200:203], 0\n\t" \
    "v_mfma_f32_16x16x32_bf16 v[244:247], v[164:167], v[204:207], v[244:247]\n\t" \
    "v_mfma_f32_16x16x32_bf16 v[244:247], v[168:171], v[208:211], v[244:247]\n\t" \
    "v_mfma_f32_16x16x32_bf16 v[244:247], v[172:175], v[212:215], v[244:247]\n\t" \
    "v_mfma_f32_16x16x32_bf16 v[240:243], v[232:235], v[220:223], 0\n\t" \
    "v_mfma_f32_16x16x32_bf16 v[244:247], v[180:183], v[216:219], v[244:247]\n\t" \
    "v_mfma_f32_16x16x32_bf16 v[240:243], v[236:239], v[224:227], v[240:243]\n\t" \
    "v_mfma_f32_16x16x32_bf16 v[240:243], v[180:183], v[228:231], v[240:243]\n\t" \
    "s_waitcnt vmcnt(0)\n\t" \
    "v_cmp_ge_u32 vcc, %[tn], v248\n\t" \
    "s_mov_b32 %[a], 0\n\t" \
    "s_cbranch_vccnz .Lxv%=\n\t" \
    "buffer_load_dwordx4 v[160:163], %[l16], %[rd], %[sv] offen offset:0 sc1\n\t" \
    "buffer_load_dwordx4 v[164:167], %[l16], %[rd], %[sv] offen offset:1024 sc1\n\t" \
    "buffer_load_dwordx4 v[168:171], %[l16], %[rd], %[sv] offen offset:2048 sc1\n\t" \
    "buffer_load_dwordx4 v[172:175], %[l16], %[rd], %[sv] offen offset:3072 sc1\n\t" \
    "s_mov_b32 %[a], 1\n\t" \
    ".Lxv%=:\n\t" \
    "s_nop 7\n\t" \
    "ds_write_b128 %[pw], v[244:247] offset:8192\n\t" \
    "ds_write_b128 %[pw], v[240:243]\n\t" \
    "s_cmp_eq_u32 %[fin], 0\n\t" \
    "s_waitcnt lgkmcnt(0)\n\t" \
    "s_barrier\n\t" ASM_ST \
    "s_cbranch_scc1 .Lxe%=\n\t" \
    "ds_read_b128 v[200:203], %[pr] offset:0\n\t" \
    "ds_read_b128 v[204:207], %[pr] offset:1024\n\t" \
    "ds_read_b128 v[208:211], %[pr] offset:2048\n\t" \
    "ds_read_b128 v[212:215], %[pr] offset:3072\n\t" \
    "ds_read_b128 v[216:219], %[pr] offset:4096\n\t" \
    "ds_read_b128 v[220:223], %[pr] offset:5120\n\t" \
    "ds_read_b128 v[224:227], %[pr] offset:6144\n\t" \
    "ds_read_b128 v[228:231], %[pr] offset:7168\n\t" \
    "v_lshlrev_b32 v240, 16, v250\n\t" \
    "v_and_b32 v241, 0xffff0000, v250\n\t" \
    "v_lshlrev_b32 v242, 16, v251\n\t" \
    "v_and_b32 v243, 0xffff0000, v251\n\t" \
    "s_waitcnt lgkmcnt(6)\n\t" \
    "v_pk_add_f32 v[200:201], v[200:201], v[204:205]\n\t" \
    "v_pk_add_f32 v[202:203], v[202:203], v[206:207]\n\t" \
    "s_waitcnt lgkmcnt(4)\n\t" \
    "v_pk_add_f32 v[208:209], v[208:209], v[212:213]\n\t" \
    "v_pk_add_f32 v[210:211], v[210:211], v[214:215]\n\t" \
    "v_pk_add_f32 v[200:201], v[200:201], v[208:209]\n\t" \
    "v_pk_add_f32 v[202:203], v[202:203], v[210:211]\n\t" \
    "s_waitcnt lgkmcnt(2)\n\t" \
    "v_pk_add_f32 v[216:217], v[216:217], v[220:221]\n\t" \
    "v_pk_add_f32 v[218:219], v[218:219], v[222:223]\n\t" \
    "s_waitcnt lgkmcnt(0)\n\t" \
    "v_pk_add_f32 v[224:225], v[224:225], v[228:229]\n\t" \
    "v_pk_add_f32 v[226:227], v[226:227], v[230:231]\n\t" \
    "v_pk_add_f32 v[216:217], v[216:217], v[224:225]\n\t" \
    "v_pk_add_f32 v[218:219], v[218:219], v[226:227]\n\t" \
    "v_pk_add_f32 v[200:201], v[200:201], v[216:217]\n\t" \
    "v_pk_add_f32 v[202:203], v[202:203], v[218:219]\n\t" \
    "v_pk_add_f32 v[200:201], v[200:201], v[240:241]\n\t" \
    "v_pk_add_f32 v[202:203], v[202:203], v[242:243]\n\t" \
    "v_cvt_pk_bf16_f32 v200, v200, v201\n\t" \
    "v_cvt_pk_bf16_f32 v201, v202, v203\n\t" \
    "v_min_u32 v200, -2, v200\n\t" \
    "v_min_u32 v201, -2, v201\n\t" \
    "s_add_u32 %[sx], %[sx], 0x1000000\n\t" \
    "buffer_store_dwordx2 v[200:201], %[xo], %[rd], %[sx] offen sc1\n\t" \
    ".Lxe%=:\n\t"
#define ASM_W4_M \
    "buffer_load_dwordx4 v[184:187], %[l16], %[rs], %[s1] offen offset:2048 sc1\n\t" \
    "buffer_load_dwordx4 v[180:183], %[l16], %[rs], %[s1] offen offset:1024 sc1\n\t" \
    "buffer_load_dwordx4 v[176:179], %[l16], %[rs], %[s1] offen offset:0 sc1\n\t" \
    "buffer_load_dwordx4 v[172:175], %[l16], %[rs], %[s0] offen offset:3072 sc1\n\t" \
    "buffer_load_dwordx4 v[168:171], %[l16], %[rs], %[s0] offen offset:2048 sc1\n\t" \
    "buffer_load_dwordx4 v[164:167], %[l16], %[rs], %[s0] offen offset:1024 sc1\n\t" \
    "buffer_load_dwordx4 v[160:163], %[l16], %[rs], %[s0] offen offset:0 sc1\n\t" \
    "buffer_load_dwordx4 v[220:223], %[l16], %[rs], %[s3] offen offset:3072 sc1\n\t" \
    "v_mfma_f32_16x16x32_bf16 v[224:227], v[28:31], v[188:191], 0\n\t" \
    "v_mfma_f32_16x16x32_bf16 v[228:231], v[60:63], v[188:191], 0\n\t" \
    "v_mfma_f32_16x16x32_bf16 v[232:235], v[92:95], v[188:191], 0\n\t" \
    "v_mfma_f32_16x16x32_bf16 v[236:239], v[124:127], v[188:191], 0\n\t" \
    "v_mfma_f32_16x16x32_bf16 v[240:243], v[156:159], v[188:191], 0\n\t" \
    "s_waitcnt vmcnt(7)\n\t" \
    ".Lc06_%=:\n\t" \
    "v_max_u32 v244, v184, v185\n\t" \
    "v_max3_u32 v244, v186, v187, v244\n\t" \
    "v_cmp_eq_u32 vcc, -1, v244\n\t" \
    "s_cbranch_vccnz .Ls06_%=\n\t" \
    ".Lk06_%=:\n\t" \
    "v_mfma_f32_16x16x32_bf16 v[224:227], v[24:27], v[184:187], v[224:227]\n\t" \
    "v_mfma_f32_16x16x32_bf16 v[228:231], v[56:59], v[184:187], v[228:231]\n\t" \
    "v_mfma_f32_16x16x32_bf16 v[232:235], v[88:91], v[184:187], v[232:235]\n\t" \
    "v_mfma_f32_16x16x32_bf16 v[236:239], v[120:123], v[184:187], v[236:239]\n\t" \
    "v_mfma_f32_16x16x32_bf16 v[240:243], v[152:155], v[184:187], v[240:243]\n\t" \
    "s_waitcnt vmcnt(6)\n\t" \
    ".Lc05_%=:\n\t" \
    "v_max_u32 v244, v180, v181\n\t" \
    "v_max3_u32 v244, v182, v183, v244\n\t" \
    "v_cmp_eq_u32 vcc, -1, v244\n\t" \
    "s_cbranch_vccnz .Ls05_%=\n\t" \
    ".Lk05_%=:\n\t" \
    "v_mfma_f32_16x16x32_bf16 v[224:227], v[20:23], v[180:183], v[224:227]\n\t" \
    "v_mfma_f32_16x16x32_bf16 v[228:231], v[52:55], v[180:183], v[228:231]\n\t" \
    "v_mfma_f32_16x16x32_bf16 v[232:235], v[84:87], v[180:183], v[232:235]\n\t" \
    "v_mfma_f32_16x16x32_bf16 v[236:239], v[116:119], v[180:183], v[236:239]\n\t" \
    "v_mfma_f32_16x16x32_bf16 v[240:243], v[148:151], v[180:183], v[240:243]\n\t" \
    "s_waitcnt vmcnt(5)\n\t" \
    ".Lc04_%=:\n\t" \
    "v_max_u32 v244, v176, v177\n\t" \
    "v_max3_u32 v244, v178, v179, v244\n\t" \
    "v_cmp_eq_u32 vcc, -1, v244\n\t" \
    "s_cbranch_vccnz .Ls04_%=\n\t" \
    ".Lk04_%=:\n\t" \
    "v_mfma_f32_16x16x32_bf16 v[224:227], v[16:19], v[176:179], v[224:227]\n\t" \
    "v_mfma_f32_16x16x32_bf16 v[228:231], v[48:51], v[176:179], v[228:231]\n\t" \
    "v_mfma_f32_16x16x32_bf16 v[232:235], v[80:83], v[176:179], v[232:235]\n\t" \
    "v_mfma_f32_16x16x32_bf16 v[236:239], v[112:115], v[176:179], v[236:239]\n\t" \
    "v_mfma_f32_16x16x32_bf16 v[240:243], v[144:147], v[176:179], v[240:243]\n\t" \
    "s_waitcnt vmcnt(4)\n\t" \
    ".Lc03_%=:\n\t" \
    "v_max_u32 v244, v172, v173\n\t" \
    "v_max3_u32 v244, v174, v175, v244\n\t" \
    "v_cmp_eq_u32 vcc, -1, v244\n\t" \
    "s_cbranch_vccnz .Ls03_%=\n\t" \
    ".Lk03_%=:\n\t" \
    "v_mfma_f32_16x16x32_bf16 v[224:227], v[12:15], v[172:175], v[224:227]\n\t" \
    "v_mfma_f32_16x16x32_bf16 v[228:231], v[44:47], v[172:175], v[228:231]\n\t" \
    "v_mfma_f32_16x16x32_bf16 v[232:235], v[76:79], v[172:175], v[232:235]\n\t" \
    "v_mfma_f32_16x16x32_bf16 v[236:239], v[108:111], v[172:175], v[236:239]\n\t" \
    "v_mfma_f32_16x16x32_bf16 v[240:243], v[140:143], v[172:175], v[240:243]\n\t" \
    "s_waitcnt vmcnt(3)\n\t" \
    ".Lc02_%=:\n\t" \
    "v_max_u32 v244, v168, v169\n\t" \
    "v_max3_u32 v244, v170, v171, v244\n\t" \
    "v_cmp_eq_u32 vcc, -1, v244\n\t" \
    "s_cbranch_vccnz .Ls02_%=\n\t" \
    ".Lk02_%=:\n\t" \
    "v_mfma_f32_16x16x32_bf16 v[224:227], v[8:11], v[168:171], v[224:227]\n\t" \
    "v_mfma_f32_16x16x32_bf16 v[228:231], v[40:43], v[168:171], v[228:231]\n\t" \
    "v_mfma_f32_16x16x32_bf16 v[232:235], v[72:75], v[168:171], v[232:235]\n\t" \
    "v_mfma_f32_16x16x32_bf16 v[236:239], v[104:107], v[168:171], v[236:239]\n\t" \
    "v_mfma_f32_16x16x32_bf16 v[240:243], v[136:139], v[168:171], v[240:243]\n\t" \
    "s_waitcnt vmcnt(2)\n\t" \
    ".Lc01_%=:\n\t" \
    "v_max_u32 v244, v164, v165\n\t" \
    "v_max3_u32 v244, v166, v167, v244\n\t" \
    "v_cmp_eq_u32 vcc, -1, v244\n\t" \
    "s_cbranch_vccnz .Ls01_%=\n\t" \
    ".Lk01_%=:\n\t" \
    "v_mfma_f32_16x16x32_bf16 v[224:227], v[4:7], v[164:167], v[224:227]\n\t" \
    "v_mfma_f32_16x16x32_bf16 v[228:231], v[36:39], v[164:167], v[228:231]\n\t" \
    "v_mfma_f32_16x16x32_bf16 v[232:235], v[68:71], v[164:167], v[232:235]\n\t" \
    "v_mfma_f32_16x16x32_bf16 v[236:239], v[100:103], v[164:167], v[236:239]\n\t" \
    "v_mfma_f32_16x16x32_bf16 v[240:243], v[132:135], v[164:167], v[240:243]\n\t" \
    "s_waitcnt vmcnt(1)\n\t" \
    ".Lc00_%=:\n\t" \
    "v_max_u32 v244, v160, v161\n\t" \
    "v_max3_u32 v244, v162, v163, v244\n\t" \
    "v_cmp_eq_u32 vcc, -1, v244\n\t" \
    "s_cbranch_vccnz .Ls00_%=\n\t" \
    ".Lk00_%=:\n\t" \
    "v_mfma_f32_16x16x32_bf16 v[224:227], v[0:3], v[160:163], v[224:227]\n\t" \
    "v_mfma_f32_16x16x32_bf16 v[228:231], v[32:35], v[160:163], v[228:231]\n\t" \
    "v_mfma_f32_16x16x32_bf16 v[232:235], v[64:67], v[160:163], v[232:235]\n\t" \
    "v_mfma_f32_16x16x32_bf16 v[236:239], v[96:99], v[160:163], v[236:239]\n\t" \
    "v_mfma_f32_16x16x32_bf16 v[240:243], v[128:131], v[160:163], v[240:243]\n\t" \
    "s_waitcnt vmcnt(0)\n\t" \
    ".Lc17_%=:\n\t" \
    "v_max_u32 v244, v220, v221\n\t" \
    "v_max3_u32 v244, v222, v223, v244\n\t" \
    "v_cmp_eq_u32 vcc, -1, v244\n\t" \
    "s_cbranch_vccnz .Ls17_%=\n\t" \
    ".Lk17_%=:\n\t" \
    "buffer_load_dwordx4 v[216:219], %[l16], %[rs], %[s3] offen offset:2048 sc1\n\t" \
    "buffer_load_dwordx4 v[212:215], %[l16], %[rs], %[s3] offen offset:1024 sc1\n\t" \
    "buffer_load_dwordx4 v[208:211], %[l16], %[rs], %[s3] offen offset:0 sc1\n\t" \
    "buffer_load_dwordx4 v[204:207], %[l16], %[rs], %[s2] offen offset:3072 sc1\n\t" \
    "buffer_load_dwordx4 v[200:203], %[l16], %[rs], %[s2] offen offset:2048 sc1\n\t" \
    "buffer_load_dwordx4 v[196:199], %[l16], %[rs], %[s2] offen offset:1024 sc1\n\t" \
    "buffer_load_dwordx4 v[192:195], %[l16], %[rs], %[s2] offen offset:0 sc1\n\t" \
    "v_mfma_f32_16x16x32_bf16 v[160:163], v[28:31], v[220:223], 0\n\t" \
    "ds_write_b128 %[ld], v[224:227] offset:0\n\t" \
    "v_mfma_f32_16x16x32_bf16 v[164:167], v[60:63], v[220:223], 0\n\t" \
    "ds_write_b128 %[ld], v[228:231] offset:6144\n\t" \
    "v_mfma_f32_16x16x32_bf16 v[168:171], v[92:95], v[220:223], 0\n\t" \
    "ds_write_b128 %[ld], v[232:235] offset:12288\n\t" \
    "v_mfma_f32_16x16x32_bf16 v[172:175], v[124:127], v[220:223], 0\n\t" \
    "ds_write_b128 %[ld], v[236:239] offset:18432\n\t" \
    "v_mfma_f32_16x16x32_bf16 v[176:179], v[156:159], v[220:223], 0\n\t" \
    "ds_write_b128 %[ld], v[240:243] offset:24576\n\t" \
    "s_waitcnt vmcnt(6)\n\t" \
    ".Lc16_%=:\n\t" \
    "v_max_u32 v244, v216, v217\n\t" \
    "v_max3_u32 v244, v218, v219, v244\n\t" \
    "v_cmp_eq_u32 vcc, -1, v244\n\t" \
    "s_cbranch_vccnz .Ls16_%=\n\t" \
    ".Lk16_%=:\n\t" \
    "v_mfma_f32_16x16x32_bf16 v[160:163], v[24:27], v[216:219], v[160:163]\n\t" \
    "v_mfma_f32_16x16x32_bf16 v[164:167], v[56:59], v[216:219], v[164:167]\n\t" \
    "v_mfma_f32_16x16x32_bf16 v[168:171], v[88:91], v[216:219], v[168:171]\n\t" \
    "v_mfma_f32_16x16x32_bf16 v[172:175], v[120:123], v[216:219], v[172:175]\n\t" \
    "v_mfma_f32_16x16x32_bf16 v[176:179], v[152:155], v[216:219], v[176:179]\n\t" \
    "s_waitcnt vmcnt(5)\n\t" \
    ".Lc15_%=:\n\t" \
    "v_max_u32 v244, v212, v213\n\t" \
    "v_max3_u32 v244, v214, v215, v244\n\t" \
    "v_cmp_eq_u32 vcc, -1, v244\n\t" \
    "s_cbranch_vccnz .Ls15_%=\n\t" \
    ".Lk15_%=:\n\t" \
    "v_mfma_f32_16x16x32_bf16 v[160:163], v[20:23], v[212:215], v[160:163]\n\t" \
    "v_mfma_f32_16x16x32_bf16 v[164:167], v[52:55], v[212:215], v[164:167]\n\t" \
    "v_mfma_f32_16x16x32_bf16 v[168:171], v[84:87], v[212:215], v[168:171]\n\t" \
    "v_mfma_f32_16x16x32_bf16 v[172:175], v[116:119], v[212:215], v[172:175]\n\t" \
    "v_mfma_f32_16x16x32_bf16 v[176:179], v[148:151], v[212:215], v[176:179]\n\t" \
    "s_waitcnt vmcnt(4)\n\t" \
    ".Lc14_%=:\n\t" \
    "v_max_u32 v244, v208, v209\n\t" \
    "v_max3_u32 v244, v210, v211, v244\n\t" \
    "v_cmp_eq_u32 vcc, -1, v244\n\t" \
    "s_cbranch_vccnz .Ls14_%=\n\t" \
    ".Lk14_%=:\n\t" \
    "v_mfma_f32_16x16x32_bf16 v[160:163], v[16:19], v[208:211], v[160:163]\n\t" \
    "v_mfma_f32_16x16x32_bf16 v[164:167], v[48:51], v[208:211], v[164:167]\n\t" \
    "v_mfma_f32_16x16x32_bf16 v[168:171], v[80:83], v[208:211], v[168:171]\n\t" \
    "v_mfma_f32_16x16x32_bf16 v[172:175], v[112:115], v[208:211], v[172:175]\n\t" \
    "v_mfma_f32_16x16x32_bf16 v[176:179], v[144:147], v[208:211], v[176:179]\n\t" \
    "s_waitcnt vmcnt(3)\n\t" \
    ".Lc13_%=:\n\t" \
    "v_max_u32 v244, v204, v205\n\t" \
    "v_max3_u32 v244, v206, v207, v244\n\t" \
    "v_cmp_eq_u32 vcc, -1, v244\n\t" \
    "s_cbranch_vccnz .Ls13_%=\n\t" \
    ".Lk13_%=:\n\t" \
    "v_mfma_f32_16x16x32_bf16 v[160:163], v[12:15], v[204:207], v[160:163]\n\t" \
    "v_mfma_f32_16x16x32_bf16 v[164:167], v[44:47], v[204:207], v[164:167]\n\t" \
    "v_mfma_f32_16x16x32_bf16 v[168:171], v[76:79], v[204:207], v[168:171]\n\t" \
    "v_mfma_f32_16x16x32_bf16 v[172:175], v[108:111], v[204:207], v[172:175]\n\t" \
    "v_mfma_f32_16x16x32_bf16 v[176:179], v[140:143], v[204:207], v[176:179]\n\t" \
    "s_waitcnt vmcnt(2)\n\t" \
    ".Lc12_%=:\n\t" \
    "v_max_u32 v244, v200, v201\n\t" \
    "v_max3_u32 v244, v202, v203, v244\n\t" \
    "v_cmp_eq_u32 vcc, -1, v244\n\t" \
    "s_cbranch_vccnz .Ls12_%=\n\t" \
    ".Lk12_%=:\n\t" \
    "v_mfma_f32_16x16x32_bf16 v[160:163], v[8:11], v[200:203], v[160:163]\n\t" \
    "v_mfma_f32_16x16x32_bf16 v[164:167], v[40:43], v[200:203], v[164:167]\n\t" \
    "v_mfma_f32_16x16x32_bf16 v[168:171], v[72:75], v[200:203], v[168:171]\n\t" \
    "v_mfma_f32_16x16x32_bf16 v[172:175], v[104:107], v[200:203], v[172:175]\n\t" \
    "v_mfma_f32_16x16x32_bf16 v[176:179], v[136:139], v[200:203], v[176:179]\n\t" \
    "s_waitcnt vmcnt(1)\n\t" \
    ".Lc11_%=:\n\t" \
    "v_max_u32 v244, v196, v197\n\t" \
    "v_max3_u32 v244, v198, v199, v244\n\t" \
    "v_cmp_eq_u32 vcc, -1, v244\n\t" \
    "s_cbranch_vccnz .Ls11_%=\n\t" \
    ".Lk11_%=:\n\t" \
    "v_mfma_f32_16x16x32_bf16 v[160:163], v[4:7], v[196:199], v[160:163]\n\t" \
    "v_mfma_f32_16x16x32_bf16 v[164:167], v[36:39], v[196:199], v[164:167]\n\t" \
    "v_mfma_f32_16x16x32_bf16 v[168:171], v[68:71], v[196:199], v[168:171]\n\t" \
    "v_mfma_f32_16x16x32_bf16 v[172:175], v[100:103], v[196:199], v[172:175]\n\t" \
    "v_mfma_f32_16x16x32_bf16 v[176:179], v[132:135], v[196:199], v[176:179]\n\t" \
    "s_waitcnt vmcnt(0)\n\t" \
    ".Lc10_%=:\n\t" \
    "v_max_u32 v244, v192, v193\n\t" \
    "v_max3_u32 v244, v194, v195, v244\n\t" \
    "v_cmp_eq_u32 vcc, -1, v244\n\t" \
    "s_cbranch_vccnz .Ls10_%=\n\t" \
    ".Lk10_%=:\n\t" \
    "v_mfma_f32_16x16x32_bf16 v[160:163], v[0:3], v[192:195], v[160:163]\n\t" \
    "v_mfma_f32_16x16x32_bf16 v[164:167], v[32:35], v[192:195], v[164:167]\n\t" \
    "v_mfma_f32_16x16x32_bf16 v[168:171], v[64:67], v[192:195], v[168:171]\n\t" \
    "v_mfma_f32_16x16x32_bf16 v[172:175], v[96:99], v[192:195], v[172:175]\n\t" \
    "v_mfma_f32_16x16x32_bf16 v[176:179], v[128:131], v[192:195], v[176:179]\n\t" \
    "ds_write_b128 %[ld], v[160:163] offset:30720\n\t" \
    "ds_write_b128 %[ld], v[164:167] offset:36864\n\t" \
    "ds_write_b128 %[ld], v[168:171] offset:43008\n\t" \
    "s_nop 1\n\t" \
    "ds_write_b128 %[ld], v[172:175] offset:49152\n\t" \
    "s_nop 1\n\t" \
    "ds_write_b128 %[ld], v[176:179] offset:55296\n\t" \
    "s_branch .Lme%=\n\t" \
    ".Ls06_%=:\n\t" \
    "s_cmp_eq_u32 %[n], 0\n\t" \
    "s_cbranch_scc1 .Lk06_%=\n\t" \
    "s_sub_u32 %[n], %[n], 1\n\t" \
    "buffer_load_dwordx4 v[184:187], %[l16], %[rs], %[s1] offen offset:2048 sc1\n\t" \
    "s_waitcnt vmcnt(0)\n\t" \
    "s_branch .Lc06_%=\n\t" \
    ".Ls05_%=:\n\t" \
    "s_cmp_eq_u32 %[n], 0\n\t" \
    "s_cbranch_scc1 .Lk05_%=\n\t" \
    "s_sub_u32 %[n], %[n], 1\n\t" \
    "buffer_load_dwordx4 v[180:183], %[l16], %[rs], %[s1] offen offset:1024 sc1\n\t" \
    "s_waitcnt vmcnt(0)\n\t" \
    "s_branch .Lc05_%=\n\t" \
    ".Ls04_%=:\n\t" \
    "s_cmp_eq_u32 %[n], 0\n\t" \
    "s_cbranch_scc1 .Lk04_%=\n\t" \
    "s_sub_u32 %[n], %[n], 1\n\t" \
    "buffer_load_dwordx4 v[176:179], %[l16], %[rs], %[s1] offen offset:0 sc1\n\t" \
    "s_waitcnt vmcnt(0)\n\t" \
    "s_branch .Lc04_%=\n\t" \
    ".Ls03_%=:\n\t" \
    "s_cmp_eq_u32 %[n], 0\n\t" \
    "s_cbranch_scc1 .Lk03_%=\n\t" \
    "s_sub_u32 %[n], %[n], 1\n\t" \
    "buffer_load_dwordx4 v[172:175], %[l16], %[rs], %[s0] offen offset:3072 sc1\n\t" \
    "s_waitcnt vmcnt(0)\n\t" \
    "s_branch .Lc03_%=\n\t" \
    ".Ls02_%=:\n\t" \
    "s_cmp_eq_u32 %[n], 0\n\t" \
    "s_cbranch_scc1 .Lk02_%=\n\t" \
    "s_sub_u32 %[n], %[n], 1\n\t" \
    "buffer_load_dwordx4 v[168:171], %[l16], %[rs], %[s0] offen offset:2048 sc1\n\t" \
    "s_waitcnt vmcnt(0)\n\t" \
    "s_branch .Lc02_%=\n\t" \
    ".Ls01_%=:\n\t" \
    "s_cmp_eq_u32 %[n], 0\n\t" \
    "s_cbranch_scc1 .Lk01_%=\n\t" \
    "s_sub_u32 %[n], %[n], 1\n\t" \
    "buffer_load_dwordx4 v[164:167], %[l16], %[rs], %[s0] offen offset:1024 sc1\n\t" \
    "s_waitcnt vmcnt(0)\n\t" \
    "s_branch .Lc01_%=\n\t" \
    ".Ls00_%=:\n\t" \
    "s_cmp_eq_u32 %[n], 0\n\t" \
    "s_cbranch_scc1 .Lk00_%=\n\t" \
    "s_sub_u32 %[n], %[n], 1\n\t" \
    "buffer_load_dwordx4 v[160:163], %[l16], %[rs], %[s0] offen offset:0 sc1\n\t" \
    "s_waitcnt vmcnt(0)\n\t" \
    "s_branch .Lc00_%=\n\t" \
    ".Ls17_%=:\n\t" \
    "s_cmp_eq_u32 %[n], 0\n\t" \
    "s_cbranch_scc1 .Lk17_%=\n\t" \
    "s_sub_u32 %[n], %[n], 1\n\t" \
    "buffer_load_dwordx4 v[220:223], %[l16], %[rs], %[s3] offen offset:3072 sc1\n\t" \
    "s_waitcnt vmcnt(0)\n\t" \
    "s_branch .Lc17_%=\n\t" \
    ".Ls16_%=:\n\t" \
    "s_cmp_eq_u32 %[n], 0\n\t" \
    "s_cbranch_scc1 .Lk16_%=\n\t" \
    "s_sub_u32 %[n], %[n], 1\n\t" \
    "buffer_load_dwordx4 v[216:219], %[l16], %[rs], %[s3] offen offset:2048 sc1\n\t" \
    "s_waitcnt vmcnt(0)\n\t" \
    "s_branch .Lc16_%=\n\t" \
    ".Ls15_%=:\n\t" \
    "s_cmp_eq_u32 %[n], 0\n\t" \
    "s_cbranch_scc1 .Lk15_%=\n\t" \
    "s_sub_u32 %[n], %[n], 1\n\t" \
    "buffer_load_dwordx4 v[212:215], %[l16], %[rs], %[s3] offen offset:1024 sc1\n\t" \
    "s_waitcnt vmcnt(0)\n\t" \
    "s_branch .Lc15_%=\n\t" \
    ".Ls14_%=:\n\t" \
    "s_cmp_eq_u32 %[n], 0\n\t" \
    "s_cbranch_scc1 .Lk14_%=\n\t" \
    "s_sub_u32 %[n], %[n], 1\n\t" \
    "buffer_load_dwordx4 v[208:211], %[l16], %[rs], %[s3] offen offset:0 sc1\n\t" \
    "s_waitcnt vmcnt(0)\n\t" \
    "s_branch .Lc14_%=\n\t" \
    ".Ls13_%=:\n\t" \
    "s_cmp_eq_u32 %[n], 0\n\t" \
    "s_cbranch_scc1 .Lk13_%=\n\t" \
    "s_sub_u32 %[n], %[n], 1\n\t" \
    "buffer_load_dwordx4 v[204:207], %[l16], %[rs], %[s2] offen offset:3072 sc1\n\t" \
    "s_waitcnt vmcnt(0)\n\t" \
    "s_branch .Lc13_%=\n\t" \
    ".Ls12_%=:\n\t" \
    "s_cmp_eq_u32 %[n], 0\n\t" \
    "s_cbranch_scc1 .Lk12_%=\n\t" \
    "s_sub_u32 %[n], %[n], 1\n\t" \
    "buffer_load_dwordx4 v[200:203], %[l16], %[rs], %[s2] offen offset:2048 sc1\n\t" \
    "s_waitcnt vmcnt(0)\n\t" \
    "s_branch .Lc12_%=\n\t" \
    ".Ls11_%=:\n\t" \
    "s_cmp_eq_u32 %[n], 0\n\t" \
    "s_cbranch_scc1 .Lk11_%=\n\t" \
    "s_sub_u32 %[n], %[n], 1\n\t" \
    "buffer_load_dwordx4 v[196:199], %[l16], %[rs], %[s2] offen offset:1024 sc1\n\t" \
    "s_waitcnt vmcnt(0)\n\t" \
    "s_branch .Lc11_%=\n\t" \
    ".Ls10_%=:\n\t" \
    "s_cmp_eq_u32 %[n], 0\n\t" \
    "s_cbranch_scc1 .Lk10_%=\n\t" \
    "s_sub_u32 %[n], %[n], 1\n\t" \
    "buffer_load_dwordx4 v[192:195], %[l16], %[rs], %[s2] offen offset:0 sc1\n\t" \
    "s_waitcnt vmcnt(0)\n\t" \
    "s_branch .Lc10_%=\n\t" \
    ".Lme%=:\n\t"
#define ASM_POLL5_I \
    "buffer_load_dwordx4 v[232:235], %[l16], %[rs], %[s0] offen offset:0 sc1\n\t" \
    "buffer_load_dwordx4 v[236:239], %[l16], %[rs], %[s1] offen offset:0 sc1\n\t" \
    "buffer_load_dwordx4 v[240:243], %[l16], %[rs], %[s2] offen offset:0 sc1\n\t" \
    "buffer_load_dwordx4 v[244:247], %[l16], %[rs], %[s3] offen offset:0 sc1\n\t" \
    "buffer_load_dwordx4 v[248:251], %[l16], %[rs], %[s4] offen offset:0 sc1\n\t"
#define ASM_POLL5_C \
    "s_movk_i32 %[n], 0x200\n\t" \
    "v_max_u32 v252, v232, v233\n\t" \
    "v_max3_u32 v252, v234, v235, v252\n\t" \
    "v_max3_u32 v252, v236, v237, v252\n\t" \
    "v_max3_u32 v252, v238, v239, v252\n\t" \
    "v_max3_u32 v252, v240, v241, v252\n\t" \
    "v_max3_u32 v252, v242, v243, v252\n\t" \
    "v_max3_u32 v252, v244, v245, v252\n\t" \
    "v_max3_u32 v252, v246, v247, v252\n\t" \
    "v_max3_u32 v252, v248, v249, v252\n\t" \
    "v_max3_u32 v252, v250, v251, v252\n\t" \
    "v_cmp_eq_u32 vcc, -1, v252\n\t" \
    "s_cbranch_vccz .Lhw%=\n\t" \
    ".Lfw%=:\n\t" \
    "s_mov_b32 %[a], 0\n\t" \
    "v_max_u32 v252, v232, v233\n\t" \
    "v_max3_u32 v252, v234, v235, v252\n\t" \
    "v_cmp_eq_u32 vcc, -1, v252\n\t" \
    "s_cbranch_vccz .Lgw0_%=\n\t" \
    "buffer_load_dwordx4 v[232:235], %[l16], %[rs], %[s0] offen sc1\n\t" \
    "s_mov_b32 %[a], 1\n\t" \
    ".Lgw0_%=:\n\t" \
    "v_max_u32 v252, v236, v237\n\t" \
    "v_max3_u32 v252, v238, v239, v252\n\t" \
    "v_cmp_eq_u32 vcc, -1, v252\n\t" \
    "s_cbranch_vccz .Lgw1_%=\n\t" \
    "buffer_load_dwordx4 v[236:239], %[l16], %[rs], %[s1] offen sc1\n\t" \
    "s_mov_b32 %[a], 1\n\t" \
    ".Lgw1_%=:\n\t" \
    "v_max_u32 v252, v240, v241\n\t" \
    "v_max3_u32 v252, v242, v243, v252\n\t" \
    "v_cmp_eq_u32 vcc, -1, v252\n\t" \
    "s_cbranch_vccz .Lgw2_%=\n\t" \
    "buffer_load_dwordx4 v[240:243], %[l16], %[rs], %[s2] offen sc1\n\t" \
    "s_mov_b32 %[a], 1\n\t" \
    ".Lgw2_%=:\n\t" \
    "v_max_u32 v252, v244, v245\n\t" \
    "v_max3_u32 v252, v246, v247, v252\n\t" \
    "v_cmp_eq_u32 vcc, -1, v252\n\t" \
    "s_cbranch_vccz .Lgw3_%=\n\t" \
    "buffer_load_dwordx4 v[244:247], %[l16], %[rs], %[s3] offen sc1\n\t" \
    "s_mov_b32 %[a], 1\n\t" \
    ".Lgw3_%=:\n\t" \
    "v_max_u32 v252, v248, v249\n\t" \
    "v_max3_u32 v252, v250, v251, v252\n\t" \
    "v_cmp_eq_u32 vcc, -1, v252\n\t" \
    "s_cbranch_vccz .Lgw4_%=\n\t" \
    "buffer_load_dwordx4 v[248:251], %[l16], %[rs], %[s4] offen sc1\n\t" \
    "s_mov_b32 %[a], 1\n\t" \
    ".Lgw4_%=:\n\t" \
    "s_cmp_eq_u32 %[a], 0\n\t" \
    "s_cbranch_scc1 .Lhw%=\n\t" \
    "s_waitcnt vmcnt(20)\n\t" \
    "v_max_u32 v252, v232, v233\n\t" \
    "v_max3_u32 v252, v234, v235, v252\n\t" \
    "v_max3_u32 v252, v236, v237, v252\n\t" \
    "v_max3_u32 v252, v238, v239, v252\n\t" \
    "v_max3_u32 v252, v240, v241, v252\n\t" \
    "v_max3_u32 v252, v242, v243, v252\n\t" \
    "v_max3_u32 v252, v244, v245, v252\n\t" \
    "v_max3_u32 v252, v246, v247, v252\n\t" \
    "v_max3_u32 v252, v248, v249, v252\n\t" \
    "v_max3_u32 v252, v250, v251, v252\n\t" \
    "v_cmp_eq_u32 vcc, -1, v252\n\t" \
    "s_cbranch_vccz .Lhw%=\n\t" \
    "s_sleep 1\n\t" \
    "v_max_u32 v252, v232, v233\n\t" \
    "v_max3_u32 v252, v234, v235, v252\n\t" \
    "v_max3_u32 v252, v236, v237, v252\n\t" \
    "v_max3_u32 v252, v238, v239, v252\n\t" \
    "v_max3_u32 v252, v240, v241, v252\n\t" \
    "v_max3_u32 v252, v242, v243, v252\n\t" \
    "v_max3_u32 v252, v244, v245, v252\n\t" \
    "v_max3_u32 v252, v246, v247, v252\n\t" \
    "v_max3_u32 v252, v248, v249, v252\n\t" \
    "v_max3_u32 v252, v250, v251, v252\n\t" \
    "v_cmp_eq_u32 vcc, -1, v252\n\t" \
    "s_cbranch_vccz .Lhw%=\n\t" \
    "s_sleep 1\n\t" \
    "v_max_u32 v252, v232, v233\n\t" \
    "v_max3_u32 v252, v234, v235, v252\n\t" \
    "v_max3_u32 v252, v236, v237, v252\n\t" \
    "v_max3_u32 v252, v238, v239, v252\n\t" \
    "v_max3_u32 v252, v240, v241, v252\n\t" \
    "v_max3_u32 v252, v242, v243, v252\n\t" \
    "v_max3_u32 v252, v244, v245, v252\n\t" \
    "v_max3_u32 v252, v246, v247, v252\n\t" \
    "v_max3_u32 v252, v248, v249, v252\n\t" \
    "v_max3_u32 v252, v250, v251, v252\n\t" \
    "v_cmp_eq_u32 vcc, -1, v252\n\t" \
    "s_cbranch_vccz .Lhw%=\n\t" \
    "s_sleep 1\n\t" \
    "v_max_u32 v252, v232, v233\n\t" \
    "v_max3_u32 v252, v234, v235, v252\n\t" \
    "v_max3_u32 v252, v236, v237, v252\n\t" \
    "v_max3_u32 v252, v238, v239, v252\n\t" \
    "v_max3_u32 v252, v240, v241, v252\n\t" \
    "v_max3_u32 v252, v242, v243, v252\n\t" \
    "v_max3_u32 v252, v244, v245, v252\n\t" \
    "v_max3_u32 v252, v246, v247, v252\n\t" \
    "v_max3_u32 v252, v248, v249, v252\n\t" \
    "v_max3_u32 v252, v250, v251, v252\n\t" \
    "v_cmp_eq_u32 vcc, -1, v252\n\t" \
    "s_cbranch_vccz .Lhw%=\n\t" \
    "s_sleep 1\n\t" \
    "v_max_u32 v252, v232, v233\n\t" \
    "v_max3_u32 v252, v234, v235, v252\n\t" \
    "v_max3_u32 v252, v236, v237, v252\n\t" \
    "v_max3_u32 v252, v238, v239, v252\n\t" \
    "v_max3_u32 v252, v240, v241, v252\n\t" \
    "v_max3_u32 v252, v242, v243, v252\n\t" \
    "v_max3_u32 v252, v244, v245, v252\n\t" \
    "v_max3_u32 v252, v246, v247, v252\n\t" \
    "v_max3_u32 v252, v248, v249, v252\n\t" \
    "v_max3_u32 v252, v250, v251, v252\n\t" \
    "v_cmp_eq_u32 vcc, -1, v252\n\t" \
    "s_cbranch_vccz .Lhw%=\n\t" \
    "s_sleep 1\n\t" \
    "v_max_u32 v252, v232, v233\n\t" \
    "v_max3_u32 v252, v234, v235, v252\n\t" \
    "v_max3_u32 v252, v236, v237, v252\n\t" \
    "v_max3_u32 v252, v238, v239, v252\n\t" \
    "v_max3_u32 v252, v240, v241, v252\n\t" \
    "v_max3_u32 v252, v242, v243, v252\n\t" \
    "v_max3_u32 v252, v244, v245, v252\n\t" \
    "v_max3_u32 v252, v246, v247, v252\n\t" \
    "v_max3_u32 v252, v248, v249, v252\n\t" \
    "v_max3_u32 v252, v250, v251, v252\n\t" \
    "v_cmp_eq_u32 vcc, -1, v252\n\t" \
    "s_cbranch_vccz .Lhw%=\n\t" \
    "s_sleep 1\n\t" \
    "s_sub_u32 %[n], %[n], 1\n\t" \
    "s_cmp_lg_u32 %[n], 0\n\t" \
    "s_cbranch_scc1 .Lfw%=\n\t" \
    ".Lhw%=:\n\t"
__device__ __forceinline__ void poll5_issue(rsrc_t rs, unsigned l16, unsigned base, u32x4 (&v)[5]) {
    asm volatile(ASM_POLL5_I : "+{v[232:235]}"(v[0]), "+{v[236:239]}"(v[1]), "+{v[240:243]}"(v[2]), "+{v[244:247]}"(v[3]), "+{v[248:251]}"(v[4]) : [l16] "v"(l16), [rs] "s"(rs), [s0] "s"(base), [s1] "s"(base + WIN), [s2] "s"(base + 2 * WIN), [s3] "s"(base + 3 * WIN), [s4] "s"(base + 4 * WIN) : "memory");
}
__device__ __forceinline__ void poll5(rsrc_t rs, unsigned l16, unsigned base, u32x4 (&v)[5], const Params& P) {
#pragma unroll 1
    for (int spins = 1023;; spins += 1024) {
        unsigned n, a;
        asm volatile(ASM_POLL5_C : "+{v[232:235]}"(v[0]), "+{v[236:239]}"(v[1]), "+{v[240:243]}"(v[2]), "+{v[244:247]}"(v[3]), "+{v[248:251]}"(v[4]), [n] "=&s"(n), [a] "=&s"(a) : [l16] "v"(l16), [rs] "s"(rs), [s0] "s"(base), [s1] "s"(base + WIN), [s2] "s"(base + 2 * WIN), [s3] "s"(base + 3 * WIN), [s4] "s"(base + 4 * WIN) : "memory", "vcc", "scc", "v252");
        if (n || spin_fail(spins, P)) break;
    }
}
using u32x8 = __attribute__((ext_vector_type(8))) unsigned;
using u32x16 = __attribute__((ext_vector_type(16))) unsigned;
using u32x32 = __attribute__((ext_vector_type(32))) unsigned;
struct WRegs { u32x32 w[6]; u32x8 t; };
#define WREGS(W) "{v[0:31]}"(W.w[0]), "{v[32:63]}"(W.w[1]), "{v[64:95]}"(W.w[2]), "{v[96:127]}"(W.w[3]), "{v[128:159]}"(W.w[4]), "{v[160:191]}"(W.w[5]), "{v[192:199]}"(W.t)
__device__ __forceinline__ u32x32 cat32(u32x8 a, u32x8 b, u32x8 c, u32x8 d) {
    const u32x16 lo = __builtin_shufflevector(a, b, 0, 1, 2, 3, 4, 5, 6, 7, 8, 9, 10, 11, 12, 13, 14, 15);
    const u32x16 hi = __builtin_shufflevector(c, d, 0, 1, 2, 3, 4, 5, 6, 7, 8, 9, 10, 11, 12, 13, 14, 15);
    return __builtin_shufflevector(lo, hi, 0, 1, 2, 3, 4, 5, 6, 7, 8, 9, 10, 11, 12, 13, 14, 15, 16, 17, 18, 19, 20, 21, 22, 23, 24, 25, 26, 27, 28, 29, 30, 31);
}
template <class F>
__device__ __forceinline__ void load_weights(WRegs& W, unsigned wl, F src) {
    u32x8 f[25];
#pragma unroll
    for (int i = 0; i < 10; ++i)
#pragma unroll
        for (int j = 0; j < 3; ++j) {
            const bf16_t* p = src(i, j);
            const u32x4 a = *(const u32x4*)p, b = *(const u32x4*)(p + 512);
            if ((i & 1) || j) f[wi(i, j)] = __builtin_shufflevector(a, b, 0, 1, 2, 3, 4, 5, 6, 7);
            else { lds_st<u32x4>(wl + (i >> 1) * 2048, a); lds_st<u32x4>(wl + (i >> 1) * 2048 + 1024, b); }
        }
#pragma unroll
    for (int k = 0; k < 6; ++k) {
        const u32x16 lo = __builtin_shufflevector(f[4 * k], f[4 * k + 1], 0, 1, 2, 3, 4, 5, 6, 7, 8, 9, 10, 11, 12, 13, 14, 15);
        const u32x16 hi = __builtin_shufflevector(f[4 * k + 2], f[4 * k + 3], 0, 1, 2, 3, 4, 5, 6, 7, 8, 9, 10, 11, 12, 13, 14, 15);
        W.w[k] = __builtin_shufflevector(lo, hi, 0, 1, 2, 3, 4, 5, 6, 7, 8, 9, 10, 11, 12, 13, 14, 15, 16, 17, 18, 19, 20, 21, 22, 23, 24, 25, 26, 27, 28, 29, 30, 31);
    }
    W.t = f[24];
    asm volatile("" : "+{v[0:31]}"(W.w[0]), "+{v[32:63]}"(W.w[1]), "+{v[64:95]}"(W.w[2]), "+{v[96:127]}"(W.w[3]), "+{v[128:159]}"(W.w[4]), "+{v[160:191]}"(W.w[5]), "+{v[192:199]}"(W.t));
}
__device__ __forceinline__ void xn_wait(const Params& P, int seg, int cpy) {
    for (int spins = 0;; ++spins) {
        const unsigned f = __builtin_amdgcn_raw_buffer_load_b32(mkrsrc(lbase(P, 1), RG_L), (lane_id() & 15) * 128, FL_XN + (cpy & 3) * 2048, 16);
        if (__all(f > (unsigned)seg)) break;
        __builtin_amdgcn_s_sleep(4);
        if (spin_fail(spins, P)) break;
    }
    asm volatile("" ::: "memory");
}
__device__ __forceinline__ unsigned c1_dst(int wave, bool ktype) {
    const int ln = lane_id(), c = ln & 15, g = ln >> 4, hf = (wave >> 1) & 1;
    return ktype ? L_KM + (c >> 2) * 256 + (wave & 1) * 128 + g * 32 + hf * 8 + (c & 3) * 2
                 : L_Q + (wave & 1) * 1536 + (c >> 3) * 256 + g * 64 + ((c >> 1) & 1) * 32 + ((c >> 2) & 1) * 8 + hf * 4 + (c & 1) * 2;
}
__device__ __forceinline__ int kblock(int wave, int i) {
    const unsigned long long tab = wave == 0 ? 0x9483726150ull : wave == 1 ? 0x9847150362ull : wave == 2 ? 0x9865321074ull : wave == 3 ? 0x7654321098ull : 0x9876543210ull;
    return (int)(tab >> (4 * i)) & 15;
}
__device__ __forceinline__ void dma1(rsrc_t rs, unsigned l16, unsigned so, unsigned dst) {
    asm volatile("s_mov_b32 m0, %3\n\ts_nop 0\n\tbuffer_load_dwordx4 %0, %1, %2 offen sc1 lds" :: "v"(l16), "s"(rs), "s"(so), "s"(dst) : "memory");
}
#define LOOP_CLOB "memory", "vcc", "scc", "v200", "v201", "v202", "v203", "v204", "v205", "v206", "v207", "v208", "v209", "v210", "v211", "v212", "v213", "v214", "v215", "v216", "v217", "v218", "v219", "v220", "v221", "v222", "v223", "v224", "v225", \
    "v226", "v227", "v228", "v229", "v230", "v231", "v232", "v233", "v234", "v235", "v236", "v237", "v238", "v239", "v240", "v241", "v242", "v243", "v244", "v245", "v246", "v247", "v248", "v249", "v250", "v251", "v252", \
    "s40", "s41", "s42", "s43", "s44", "s45", "s46", "s47", "s48", "s49", "s50", "s51", "s52", "s53", "s54", "s55", "s56", "s57", "s58", "s59", \
    "s64", "s65", "s66", "s67", "s68", "s69", "s70", "s71", "s72", "s73", "s74", "s75", "s76", "s77", "s78", "s79"
__device__ __forceinline__ unsigned UNI(unsigned x) {
    unsigned r;
    asm volatile("s_nop 0\n\tv_readfirstlane_b32 %0, %1\n\ts_nop 4" : "=s"(r) : "v"(x));
    return r;
}
__device__ __forceinline__ void note_timeout(const Params& P, unsigned bud) {
    if (bud == 0) __builtin_amdgcn_raw_buffer_store_b32(1u, mkrsrc(P.ws + FL0, 512), 0x100, 0, 16);
}

__device__ __forceinline__ void s_role(const Params& P, int wave, int L, int h, int cpy) {
    const rsrc_t rr = mkrsrc(lbase(P, L), RG_L);
    WRegs wf;
    unsigned l16 = lane_id() * 16;
    {
        const bf16_t* wsrc = (const bf16_t*)(P.ws + WT_QKV) + ((size_t)((wave >> 2) * 384 + h * 12 + (wave & 3) * 3) * 20) * 512 + l16 / 2;
        load_weights(wf, L_W + wave * 10240 + l16, [&](int i, int j) { return wsrc + (size_t)(j * 20 + 2 * kblock(wave, i)) * 512; });
    }
    const int mt = (wave >> 1) & 1, hf = wave & 1;
    const unsigned xs = D_XN + mt * 4096 + hf * WIN, x1 = (2 - hf) * WIN + hf * 1024, m0 = L_X + mt * 20480 + hf * 4096, m1 = L_X + mt * 20480 + (8 + hf) * 1024;
    if (wave >= 4) {
        if (L) xn_wait(P, 0, cpy);
#pragma unroll
        for (int k = 0; k < 4; ++k) dma1(rr, l16, xs + k * 1024, m0 + k * 1024);
        dma1(rr, l16, xs + x1, m1);
        wait_vm<0>();
    }
    bar();
    const float SCL = 0.07216878364870322f * 1.4426950408889634f;
    const unsigned px = RG_PX + (h >> 2) * WIN;
    const unsigned pb = wave == 0 ? RG_XP + (h / 12) * WIN + (h % 12) * 640 - L_Q : px + (wave == 2 ? (h & 3) * 1024 : 5120 + ((h >> 1) & 1) * 1024 + (h & 1) * 256);
    unsigned b2 = l16 + (wave < 3 ? L_Q : wave == 3 ? L_QX : L_W + wave * 10240), dsto = c1_dst(wave, wave >= 4), bud;
    asm volatile(ASM_S_LOOP : [bud] "=s"(bud), "+{v253}"(l16)
                 : WREGS(wf), "{v254}"(b2), "{v255}"(dsto), [wave] "s"(wave), [rs] "s"(rr), [xs] "s"(xs), [x1] "s"(x1), [m0] "s"(m0), [m1] "s"(m1), [fl] "s"(L ? FL_XN + cpy * 2048 : 0u),
                   [cj] "s"(REGS - 75 * WIN), [cs] "s"(5 * WIN), [qs] "s"(UNI(L_KX + hf * 3072 + mt * 8)), [mb] "s"(RG_XM + cpy * XM_COPY), [pb] "s"(UNI(pb)),
                   [pm] "s"(UNI(px + (wave == 2 ? 4096 : 7168) + (h & 3) * 256)), [scl] "s"(__float_as_uint(SCL)) LST_IN
                 : LOOP_CLOB);
    note_timeout(P, bud);
}

__device__ __forceinline__ void v_role(const Params& P, int wave, int L, int h, int cpy) {
    const rsrc_t rr = mkrsrc(lbase(P, L), RG_L);
    WRegs wf;
    unsigned l16 = lane_id() * 16;
    const int w4 = wave & 3;
    {
        const bf16_t* wv = (const bf16_t*)(P.ws + WT_QKV) + ((size_t)(768 + h * 12 + w4 * 3) * 20) * 512 + l16 / 2;
        const bf16_t* wo = (const bf16_t*)(P.ws + WT_O) + ((size_t)(h * 40 + w4 * 10) * 6) * 512 + l16 / 2;
        load_weights(wf, L_W + wave * 10240 + l16, [&](int i, int j) { return wave < 4 ? wv + (size_t)(j * 20 + 2 * kblock(wave, i)) * 512 : wo + (size_t)(i * 6 + 2 * j) * 512; });
    }
    if (wave < 4) {
        if (L) xn_wait(P, 0, cpy);
#pragma unroll
        for (int k = 0; k < 10; ++k) { const int mt = k / 5, ks = k % 5 < 4 ? wave * 4 + k % 5 : 16 + wave; dma1(rr, l16, D_XN + (ks >> 2) * WIN + mt * 4096 + (ks & 3) * 1024, L_X + mt * 20480 + ks * 1024); }
        wait_vm<0>();
    } else if (wave == 4) lds_st<u32x4>(L_KM + l16, (u32x4){0u, 0u, 0u, 0u});
    bar();
    unsigned b2 = l16 + (wave < 3 ? L_Q : wave == 3 ? L_QX : L_W + wave * 10240), dsto = wave < 4 ? c1_dst(wave, false) : (l16 >> 8) * WIN + (l16 & 255), bud;
    if (wave >= 4) l16 += L_Q;
    asm volatile(ASM_V_LOOP : [bud] "=s"(bud), "+{v253}"(l16)
                 : WREGS(wf), "{v254}"(b2), "{v255}"(dsto), [wave] "s"(wave), [rs] "s"(rr), [xs] "s"(D_XN + w4 * WIN), [fl] "s"(L ? FL_XN + cpy * 2048 : 0u),
                   [cj] "s"(REGS - 75 * WIN), [cs] "s"(5 * WIN), [mb] "s"(RG_XM + cpy * XM_COPY), [vm] "s"(VXD - 1), [vx] "s"(vxb(w4, 0) + (h >> 3) * WIN + (h & 7) * 1024 - L_Q),
                   [fo] "s"(FL_VO + (w4 >> 1) * WIN + ((w4 & 1) * 32 + h) * 128 - L_Q), [vo] "s"(RG_VM + w4 * 10 * WIN + (h >> 2) * 1024 + (h & 3) * 256) LST_IN
                 : LOOP_CLOB);
    note_timeout(P, bud);
}

constexpr unsigned R_RED = 0;
constexpr unsigned R_PART = 8192;
__device__ __forceinline__ void r_role(const Params& P, int wave, int L, int j, int cpy) {
    const int lane = lane_id(), l15 = lane & 15, l4 = lane >> 4;
    const unsigned l16 = lane * 16;
    const rsrc_t rr = mkrsrc(lbase(P, L), RG_L), rd = rr;
    const int sidx = (lane & 2) ? 0xEEEE : 0x4444;
    unsigned offp[3];
    auto hq = [&](int q) { return RG_XP + cpy * XP_COPY + (q / 3) * WIN + (q % 3) * 2560; };
    offp[0] = hq(wave) + (lane >> 5) * 640 + 128 + (lane & 31) * 16;
    offp[1] = offp[0] + 1280;
    offp[2] = (lane & 2) ? OOB : hq(wave) + l4 * 640 + ((l15 >> 2) * 2 + (l15 & 1)) * 16;
    const unsigned arp = (wave < 3 && j < 32 && wave * 1024 + l16 < 2560) ? hq(j >> 2) + wave * 1024 + l16 : OOB;
    const unsigned arx = RG_PX + (j >> 2) * WIN + (j & 3) * 2048 + (wave & 1) * 1024 + l16;
    const unsigned offv = RG_VM + j * WIN + wave * 1024 + l16;
    const unsigned offo = (wave < (int)NCP) ? RG_XM + wave * XM_COPY + (j >> 5) * WIN + (j & 31) * 256 + lane * 4 : OOB;
    const int pubw = 1 - (wave >> 2);
    {
        const int row = l4 * 2 + ((lane >> 2) & 1), col = 16 * j + 4 * (lane & 3) + 2 * ((lane >> 3) & 1);
        __builtin_amdgcn_raw_buffer_store_b32(nosent(*(const unsigned*)(P.mem0 + row * DM + col)), rr, offo, 0, 16);
    }
    auto red_addr = [](int fl) { return R_RED + fl * 80 + (fl >> 4) * 16; };
    const unsigned rw0 = red_addr(l4 * 16 + ((l15 >> 1) & 1) * 8 + (l15 >> 2)) + (l15 & 1) * 4 + (wave ^ (l15 >> 3)) * 8;
    const unsigned rrd = red_addr(lane);
    u32x16 vx;
    const rsrc_t rf = rr;
    const int jq = j / 10;
    const unsigned fo = (lane & 3) * 128, fs = FL_VO + (jq >> 1) * WIN + ((jq & 1) * 32 + wave * 4) * 128, vxo = (unsigned)((j - jq * 10) * 4 + (wave >> 1)) * WIN + (wave & 1) * 4096;
    const unsigned xo = (l4 >> 1) * 256 + l15 * 16 + (l4 & 1) * 8, pw = R_PART + wave * 1024 + l16, pr = R_PART + (wave & 1) * 8192 + l16;
    const int fin = (wave + 2) >> 3;
    asm volatile("" : "={v[160:175]}"(vx));
    auto vx_fetch = [&](int t) {
        for (int spins = 0;; ++spins) {
            const unsigned f = __builtin_amdgcn_raw_buffer_load_b32(rf, fo, fs, 16);
            if (__all(f > (unsigned)t)) break;
            __builtin_amdgcn_s_sleep(2);
            if (spin_fail(spins, P)) break;
        }
        asm volatile("buffer_load_dwordx4 v[160:163], %1, %2, %3 offen sc1\n\tbuffer_load_dwordx4 v[164:167], %1, %2, %3 offen offset:1024 sc1\n\t"
                     "buffer_load_dwordx4 v[168:171], %1, %2, %3 offen offset:2048 sc1\n\tbuffer_load_dwordx4 v[172:175], %1, %2, %3 offen offset:3072 sc1"
                     : "+{v[160:175]}"(vx) : "v"(l16), "s"(rd), "s"(vxb(jq, t) + vxo) : "memory");
    };
    vx_fetch(0);
    STDECL;
    for (int t = 0; t < NSEG; ++t) {
        const unsigned slot = t & (NSLOT - 1);
        ST(0);
        u32x4 vm = sent4(), pm = vm;
        u32x8 pv = __builtin_shufflevector(vm, vm, 0, 1, 2, 3, 0, 1, 2, 3);
#pragma unroll 1
        for (int spins = 1023;; spins += 1024) {
            unsigned n, a;
            ASM_ST_DECL;
            asm volatile(ASM_RCRIT : "+{v[180:183]}"(vm), "+{v[184:191]}"(pv), "+{v[192:195]}"(pm), [n] "=&s"(n), [a] "=&s"(a) ASM_ST_OUT ASM_ST2_OUT
                         : [rs] "s"(rr), [ov] "v"(offv), [sv] "s"(slot * VM_SLOT), [o0] "v"(offp[0]), [o1] "v"(offp[1]), [o2] "v"(offp[2]), [sp] "s"(slot * XP_SLOT),
                           "{v[160:175]}"(vx), [sidx] "v"(sidx), [rw] "v"(rw0), [rd] "v"(rrd), [oo] "v"(offo),
                           [so] "s"(((t + 1) & (NSLOT - 1)) * XM_SLOT), [pub] "s"(pubw * (NSEG - 1 - t))
                         : "memory", "vcc", "scc", "v176", "v177", "v178", "v179", "v196", "v200", "v201", "v202", "v203", "v204", "v205", "v206", "v207", "v208", "v209", "v210", "v211", "v212", "v213", "v214", "v215");
            ASM_ST_SAVE(2); ASM_ST2_SAVE(1);
            if (__builtin_amdgcn_readfirstlane(n)) break;
            if (spin_fail(spins, P)) { bar(); break; }
        }
        ST(3);
        if (wave >= 4) __builtin_amdgcn_s_sleep(RXDEL);
        if ((wave >> 1) == 2 && j < 32 && t >= 2) st128(sent4(), rr, arx, ((t - 2) & (NSLOT - 1)) * PX_SLOT);
        unsigned early;
#pragma unroll 1
        for (int spins = 1023;; spins += 1024) {
            unsigned n, sx = D_XR + segb(t) + (j >> 3) * WIN + (wave & 1) * 4096 + (j & 7) * 512;
            const unsigned pxb = RG_PX + slot * PX_SLOT + wave * WIN;
            ASM_ST_DECL;
            asm volatile(ASM_RX : "+{v[160:175]}"(vx), [n] "=&s"(n), [a] "=&s"(early), [sx] "+s"(sx) ASM_ST_OUT ASM_ST2_OUT
                         : "{v[180:183]}"(vm), "{v[184:191]}"(pv), "{v[192:195]}"(pm), [l16] "v"(l16), [rs] "s"(rr), [s0] "s"(pxb), [s1] "s"(pxb + 4096), [rd] "s"(rd), [xo] "v"(xo),
                           [fo] "v"(fo), [rf] "s"(rf), [fs] "s"(fs), [tn] "s"(t + 1), [sv] "s"(vxb(jq, t + 1) + vxo),
                           [pw] "v"(pw), [pr] "v"(pr), [fin] "s"(fin)
                         : "memory", "vcc", "scc", "v196", "v200", "v201", "v202", "v203", "v204", "v205", "v206", "v207", "v208", "v209", "v210", "v211", "v212", "v213", "v214", "v215", "v216", "v217", "v218", "v219", "v220", "v221", "v222", "v223", "v224", "v225", "v226", "v227", "v228", "v229", "v230", "v231", "v232", "v233", "v234", "v235", "v236", "v237", "v238", "v239", "v240", "v241", "v242", "v243", "v244", "v245", "v246", "v247", "v248", "v249", "v250", "v251");
            ASM_ST_SAVE(4); ASM_ST2_SAVE(5);
            if (__builtin_amdgcn_readfirstlane(n)) break;
            early = 0;
            if (spin_fail(spins, P)) { bar(); break; }
        }
        ST(6);
        st128(sent4(), rr, offv, slot * VM_SLOT);
        __builtin_amdgcn_raw_buffer_store_b32(SENT, rr, offo, slot * XM_SLOT, 16);
        if (wave < 3 && t >= 1) st128(sent4(), rr, arp, ((t - 1) & (NSLOT - 1)) * XP_SLOT);
        if (wave == 5 && t + AH < NSEG) st128(sent4(), rd, (lane >> 5) * 4096 + (lane & 31) * 16, D_XA + segb(t + AH) + (j >> 3) * WIN + (j & 7) * 512);
        if (t + 1 < NSEG && !__builtin_amdgcn_readfirstlane(early)) vx_fetch(t + 1);
        ST(7);
        STFLUSH(t);
    }
}

template <int WHICH>
__device__ __forceinline__ void w_role(const Params& P, int wave, int L, int cu) {
    constexpr int NT = (WHICH == 3) ? 2 : 1;
    constexpr int KS = 20, PW = 5;
    constexpr int N = (WHICH == 3) ? FF : DM;
    constexpr unsigned W_ST = 2 * 49152;
    const int ln = lane_id(), l15 = ln & 15, l4 = ln >> 4;
    const unsigned l16 = ln * 16;
    const rsrc_t rd = mkrsrc(lbase(P, L), RG_L);
    const int nt0 = (cu * 8 + wave) * NT;
    bf16x8 wf[NT][KS];
    {
        const bf16_t* w = (const bf16_t*)(P.ws + (WHICH == 1 ? WT_1 : WHICH == 2 ? WT_2 : WT_3)) + (size_t)nt0 * KS * 512 + ln * 8;
#pragma unroll
        for (int n = 0; n < NT; ++n)
#pragma unroll
            for (int k = 0; k < KS; ++k) wf[n][k] = *(const bf16x8*)(w + (n * KS + k) * 512);
    }
    f32x4 cs[NT];
    if (WHICH != 2) {
        const bf16x8 ones = __builtin_bit_cast(bf16x8, ((u32x4){0x3F803F80u, 0x3F803F80u, 0x3F803F80u, 0x3F803F80u}));
#pragma unroll
        for (int n = 0; n < NT; ++n) {
            cs[n] = (f32x4){0.f, 0.f, 0.f, 0.f};
#pragma unroll
            for (int k = 0; k < KS; ++k) MFMA(wf[n][k], ones, cs[n]);
        }
    }
    const int grp = nt0 / 10, sh = (WHICH == 1) ? ((grp & 1) ? -1 : 1) * ((grp >> 1) + 1) : 0;
    if (WHICH == 1) {
        const int zr = (grp & 1) ? S_ - 1 - l15 : l15;
        unsigned zv;
        asm volatile("v_mov_b32 %0, 0" : "=v"(zv));
        st128(((u32x4){zv, zv, zv, zv}), rd, (l15 <= (grp >> 1) && l4 < 2) ? toff(zr, nt0 * 16 + l4 * 8) : OOB, D_HS);
    }
    constexpr unsigned D_IN = WHICH == 1 ? D_XA : WHICH == 2 ? D_HS : D_X1;
    constexpr unsigned D_OUT = WHICH == 1 ? D_HS : WHICH == 2 ? D_X1 : D_H2;
    u32x4 v[PW] = {};
    poll5_issue(rd, l16, D_IN + wave * 1024, v);
    wait_vm<0>();
    STDECL;
    for (int t = 0; t < NSEG; ++t) {
        const unsigned ab = (t & 1) * 49152u;
        ST(0);
        {
            poll5(rd, l16, D_IN + segb(t) + wave * 1024, v, P);
            ST(1);
#pragma unroll
            for (int k = 0; k < PW; ++k) lds_st<u32x4>(ab + ((wave >> 2) * 20 + (wave & 3) + 4 * k) * 1024 + l16, v[k]);
            if (WHICH != 2) {
                float s = 0.f, q = 0.f;
                const bf16x2 one2 = __builtin_bit_cast(bf16x2, 0x3F803F80u);
#pragma unroll
                for (int k = 0; k < PW; ++k)
#pragma unroll
                    for (int e = 0; e < 4; ++e) {
                        const unsigned u = v[k][e];
                        const bf16x2 x = __builtin_bit_cast(bf16x2, u);
                        s = __builtin_amdgcn_fdot2_f32_bf16(x, one2, s, false);
                        q = __builtin_amdgcn_fdot2_f32_bf16(x, x, q, false);
                    }
                sum2_rows(s, q);
                if (ln < 16) lds_st<f32x2>(W_ST + (t & 1) * 1024 + wave * 128 + ln * 8, (f32x2){s, q});
            }
        }
        bar();
        ST(2);
        u32x2 r1[2], sv[2];
        if (WHICH == 2) {
            const unsigned ro = toff(l15, nt0 * 16 + l4 * 4);
#pragma unroll
            for (int mt = 0; mt < 2; ++mt) { r1[mt] = LD64(rd, ro + mt * 4096, D_XA + segb(t)); sv[mt] = LD64(rd, (mt * 16 + l15) * 8, D_SS + (t >> 5) * WIN + (t & 31) * 256); }
        }
        float mr[2], rs[2];
        f32x2 st[2][4];
        if (WHICH != 2) {
#pragma unroll
            for (int w = 0; w < 8; ++w) st[w >> 2][w & 3] = lds_ld<f32x2>(W_ST + (t & 1) * 1024 + w * 128 + l15 * 8);
        }
        auto stats = [&](int mt) {
            const f32x2 a = (st[mt][0] + st[mt][1]) + (st[mt][2] + st[mt][3]);
            const float mean = a[0] * (1.0f / DM), var = __builtin_fmaxf(a[1] * (1.0f / DM) - mean * mean, 0.f);
            const float m1 = rbf(mean), d1 = ln_den(var), r1i = rcp(d1);
            if (WHICH == 1) {
                const float m2 = rbf((mean - m1) * r1i), d2 = ln_den(var * r1i * r1i);
                rs[mt] = r1i * rcp(d2); mr[mt] = -(m1 + m2 * d1) * rs[mt];
                if (cu == 0 && wave == 0)
                    __builtin_amdgcn_raw_buffer_store_b64((u32x2){__float_as_uint(m1), __float_as_uint(r1i)}, rd, ln < 16 ? (mt * 16 + ln) * 8 : OOB, D_SS + (t >> 5) * WIN + (t & 31) * 256, 16);
            } else { rs[mt] = r1i; mr[mt] = -m1 * r1i; }
            asm volatile("" : "+v"(rs[mt]), "+v"(mr[mt]));
        };
        f32x4 acc[NT][2];
        unsigned o[2][NT][2];
        auto fin = [&](int mt, int n, int hf) {
            float v[2];
#pragma unroll
            for (int e = 0; e < 2; ++e) {
                if (WHICH != 2) v[e] = gelu_tanh(__builtin_fmaf(acc[n][mt][hf * 2 + e], rs[mt], cs[n][hf * 2 + e] * mr[mt]));
                else v[e] = rbf(((e ? hi2f(r1[mt][hf]) : lo2f(r1[mt][hf])) - __uint_as_float(sv[mt][0])) * __uint_as_float(sv[mt][1])) + acc[n][mt][hf * 2 + e];
            }
            o[mt][n][hf] = nosent(pack2(v[0], v[1]));
            asm volatile("" : "+v"(o[mt][n][hf]));
        };
        const unsigned o3 = (unsigned)((l4 & 1) * 512 + (l4 >> 1) * 256 + l15 * 16), c3 = D_H2 + (nt0 >> 3) * WIN + ((nt0 >> 1) & 3) * 1024, b3 = c3 + segh(t);
#pragma unroll
        for (int n = 0; n < NT; ++n) { acc[n][0] = (f32x4){0.f, 0.f, 0.f, 0.f}; acc[n][1] = acc[n][0]; }
        {
            constexpr int RG = 8;
            bf16x8 a[RG];
#pragma unroll
            for (int u = 0; u < RG - 1; ++u) a[u] = lds_ld<bf16x8>(ab + l16 + u * 1024);
#pragma unroll
            for (int u = 0; u < 2 * KS; ++u) {
                if (u + RG - 1 < 2 * KS) a[(u + RG - 1) % RG] = lds_ld<bf16x8>(ab + l16 + (u + RG - 1) * 1024);
                SB();
#pragma unroll
                for (int n = 0; n < NT; ++n) MFMA(wf[n][u % KS], a[u % RG], acc[n][u / KS]);
                if (WHICH != 2 && (u == 4 || u == 12)) stats(u == 12);
                if (u == (WHICH == 3 ? 22 : 6)) poll5_issue(rd, l16, D_IN + segb(t + 1) + wave * 1024, v);
                if (WHICH == 3 && u == 38) wait_vm<0>();
                if (u >= 24 && u % 4 == 0 && (u - 24) / 4 < 2 * NT) fin(0, (u - 24) / 8, ((u - 24) / 4) & 1);
                if (WHICH == 3 && u == 38) st128(merge16((u32x2){o[0][0][0], o[0][0][1]}, (u32x2){o[0][NT - 1][0], o[0][NT - 1][1]}), rd, o3, b3);
                SB();
            }
        }
        ST(3);
#pragma unroll
        for (int k = 0; k < 2 * NT; ++k) fin(1, k >> 1, k & 1);
        if (WHICH == 3) {
            st128(merge16((u32x2){o[1][0][0], o[1][0][1]}, (u32x2){o[1][NT - 1][0], o[1][NT - 1][1]}), rd, o3, b3 + 4096);
            ST(4);
            if (t + AH < NSEG) { st128(sent4(), rd, l16, c3 + segh(t + AH)); st128(sent4(), rd, l16, c3 + segh(t + AH) + 4096); }
        } else {
            wait_vm<0>();
            const int orow = t * SEG + (l4 & 1) * 16 + l15 + sh;
            st128(merge16((u32x2){o[0][0][0], o[0][0][1]}, (u32x2){o[1][0][0], o[1][0][1]}), rd, (unsigned)orow < (unsigned)S_ ? toff(orow, nt0 * 16 + (l4 >> 1) * 8) : OOB, D_OUT);
            ST(4);
            if (t + AH < NSEG) st128(sent4(), rd, orow + AH * SEG < S_ ? toff(orow + AH * SEG, nt0 * 16 + (l4 >> 1) * 8) : OOB, D_OUT);
        }
        STFLUSH(t);
    }
    wait_vm<0>();
    asm volatile("" :: "v"(v[0]), "v"(v[1]), "v"(v[2]), "v"(v[3]), "v"(v[4]));
}

__device__ __forceinline__ void w4_role(const Params& P, int wave, int L, int cu) {
    const int ln = lane_id(), l15 = ln & 15, l4 = ln >> 4;
    const unsigned l16 = ln * 16;
    const rsrc_t rd = mkrsrc(lbase(P, L), RG_L);
    STDECL;
    if (wave < 6) {
        u32x32 w[5];
        {
            const char* ws = P.ws + WT_4 + (size_t)(cu * 5 * 48 + wave * 8) * 1024 + l16;
#pragma unroll
            for (int n = 0; n < 5; ++n) {
                u32x8 f[4];
#pragma unroll
                for (int i = 0; i < 4; ++i) f[i] = __builtin_shufflevector(*(const u32x4*)(ws + n * 49152 + i * 2048), *(const u32x4*)(ws + n * 49152 + i * 2048 + 1024), 0, 1, 2, 3, 4, 5, 6, 7);
                w[n] = cat32(f[0], f[1], f[2], f[3]);
            }
            asm volatile("" : "+{v[0:31]}"(w[0]), "+{v[32:63]}"(w[1]), "+{v[64:95]}"(w[2]), "+{v[96:127]}"(w[3]), "+{v[128:159]}"(w[4]));
        }
        unsigned budget = 1u << 17;
        for (int t = 0; t < NSEG; ++t) {
            ST(0);
            const unsigned gb = D_H2 + segh(t) + wave * 2 * WIN;
            u32x4 can;
            for (int spins = 0;; ++spins) {
                can = LD128(rd, l16, gb + WIN + 3072);
                if (__all(umax4(0u, can) != SENT)) break;
                __builtin_amdgcn_s_sleep(1);
                if (spin_fail(spins, P)) { budget = 0; break; }
            }
            ST(1);
            asm volatile(ASM_W4_M : [n] "+s"(budget)
                         : "{v[188:191]}"(can), "{v[0:31]}"(w[0]), "{v[32:63]}"(w[1]), "{v[64:95]}"(w[2]), "{v[96:127]}"(w[3]), "{v[128:159]}"(w[4]),
                           [ld] "v"(l16 + wave * 1024 + (t & 1) * 61440), [l16] "v"(l16), [rs] "s"(rd), [s0] "s"(gb), [s1] "s"(gb + WIN), [s2] "s"(gb + 4096), [s3] "s"(gb + WIN + 4096)
                         : "memory", "vcc", "scc", "v160", "v161", "v162", "v163", "v164", "v165", "v166", "v167", "v168", "v169", "v170", "v171", "v172", "v173", "v174", "v175", "v176", "v177", "v178", "v179", "v180", "v181", "v182", "v183", "v184", "v185", "v186", "v187", "v192", "v193", "v194", "v195", "v196", "v197", "v198", "v199", "v200", "v201", "v202", "v203", "v204", "v205", "v206", "v207", "v208", "v209", "v210", "v211", "v212", "v213", "v214", "v215", "v216", "v217", "v218", "v219", "v220", "v221", "v222", "v223", "v224", "v225", "v226", "v227", "v228", "v229", "v230", "v231", "v232", "v233", "v234", "v235", "v236", "v237", "v238", "v239", "v240", "v241", "v242", "v243", "v244");
            ST(2);
            bar();
            ST(3);
            STFLUSH(t);
        }
    } else {
        const int mt = wave - 6;
        const rsrc_t rn = mkrsrc(lbase(P, 1), RG_L), rr = rd, ro = mkrsrc(P.out, ACT);
        auto tl = [&](int n) { return (unsigned)((cu * 5 + n) >> 3) * WIN + ((cu * 5 + n) & 7) * 512 + mt * 4096; };
        const unsigned o8 = (l4 >> 1) * 256 + l15 * 16 + (l4 & 1) * 8, o16 = tl(l4 & 1) + (l4 >> 1) * 256 + l15 * 16, o16b = tl(2 + (l4 & 1)) + (l4 >> 1) * 256 + l15 * 16;
        const unsigned q8 = l15 * (DM * 2) + l4 * 8, q16 = l15 * (DM * 2) + (l4 & 1) * 32 + (l4 >> 1) * 16;
        for (int t = 0; t < NSEG; ++t) {
            const unsigned sb = segb(t);
            ST(0);
            f32x4 res[5];
            for (int spins = 0;; ++spins) {
                u32x2 r1[5], r2[5];
                unsigned m = 0;
#pragma unroll
                for (int n = 0; n < 5; ++n) { r1[n] = LD64(rd, o8, D_X1 + sb + tl(n)); r2[n] = LD64(rd, o8, D_XA + sb + tl(n)); }
#pragma unroll
                for (int n = 0; n < 5; ++n) {
                    m = umax4(m, (u32x4){r1[n][0], r1[n][1], r2[n][0], r2[n][1]});
                    res[n] = (f32x4){lo2f(r1[n][0]), hi2f(r1[n][0]), lo2f(r1[n][1]), hi2f(r1[n][1])} + (f32x4){lo2f(r2[n][0]), hi2f(r2[n][0]), lo2f(r2[n][1]), hi2f(r2[n][1])};
                }
                if (__all(m != SENT)) break;
                __builtin_amdgcn_s_sleep(8);
                if (spin_fail(spins * 4 + 3, P)) break;
            }
            asm volatile("" : "+v"(res[0]), "+v"(res[1]), "+v"(res[2]), "+v"(res[3]), "+v"(res[4]));
            ST(1);
            bar();
            ST(2);
            {
                const unsigned la = (t & 1) * 61440 + mt * 30720 + l16;
                f32x4 c[30];
#pragma unroll
                for (int i = 0; i < 30; ++i) c[i] = lds_ld<f32x4>(la + ((i % 5) * 6 + i / 5) * 1024);
#pragma unroll
                for (int i = 0; i < 30; ++i) { res[i % 5] += c[i]; asm volatile("" : "+v"(res[i % 5])); }
            }
            u32x2 o[5];
#pragma unroll
            for (int n = 0; n < 5; ++n) o[n] = (u32x2){pack2(res[n][0], res[n][1]), pack2(res[n][2], res[n][3])};
            if (L) {
                const unsigned ob = (unsigned)(t * 2 + mt) * 20480 + cu * 160;
                st128_<0>(merge16(o[0], o[1]), ro, q16, ob); st128_<0>(merge16(o[2], o[3]), ro, q16, ob + 64);
                __builtin_amdgcn_raw_buffer_store_b64(o[4], ro, q8, ob + 128, 0);
            } else {
                float s = 0.f, q = 0.f;
                const bf16x2 one2 = __builtin_bit_cast(bf16x2, 0x3F803F80u);
#pragma unroll
                for (int e = 0; e < 10; ++e) {
                    const unsigned u = o[e >> 1][e & 1];
                    const bf16x2 x = __builtin_bit_cast(bf16x2, u);
                    s = __builtin_amdgcn_fdot2_f32_bf16(x, one2, s, false);
                    q = __builtin_amdgcn_fdot2_f32_bf16(x, x, q, false);
                }
                sum2_rows(s, q);
                const unsigned st = RG_ST + (unsigned)(t >> 2) * WIN + (unsigned)((t & 3) * 2 + mt) * 1024;
                __builtin_amdgcn_raw_buffer_store_b64((u32x2){nosent(__float_as_uint(s)), nosent(__float_as_uint(q))}, rr, ln < 16 ? ln * 8 : OOB, st + cu * 128, 16);
                ST(3);
                st128(merge16(o[0], o[1]), rn, o16, D_XR + sb); st128(merge16(o[2], o[3]), rn, o16b, D_XR + sb);
                __builtin_amdgcn_raw_buffer_store_b64(o[4], rn, o8, D_XR + sb + tl(4), 16);
                __builtin_amdgcn_s_sleep(W4SL);
                for (int spins = 0;; ++spins) {
                    const u32x2 a = LD64(rr, ln * 8, st), b = LD64(rr, ln * 8 + 512, st);
                    s = __uint_as_float(a[0]) + __uint_as_float(b[0]); q = __uint_as_float(a[1]) + __uint_as_float(b[1]);
                    if (__all(umax4(0u, (u32x4){a[0], a[1], b[0], b[1]}) != SENT)) break;
                    asm volatile("" ::: "memory");
                    if (spin_fail(spins, P)) break;
                }
                ST(4);
                sum2_rows(s, q);
                const float mean = s * (1.0f / DM), var = __builtin_fmaxf(q * (1.0f / DM) - mean * mean, 0.f);
                const float r = rcp(ln_den(var)), mr = -rbf(mean) * r;
#pragma unroll
                for (int n = 0; n < 5; ++n)
                    o[n] = (u32x2){pack2(__builtin_fmaf(res[n][0], r, mr), __builtin_fmaf(res[n][1], r, mr)), pack2(__builtin_fmaf(res[n][2], r, mr), __builtin_fmaf(res[n][3], r, mr))};
                st128(merge16(o[0], o[1]), rn, o16, D_XN + sb); st128(merge16(o[2], o[3]), rn, o16b, D_XN + sb);
                __builtin_amdgcn_raw_buffer_store_b64(o[4], rn, o8, D_XN + sb + tl(4), 16);
                wait_vm<0>();
                st128(((u32x4){(unsigned)t + 1, (unsigned)t + 1, (unsigned)t + 1, (unsigned)t + 1}), rn, ln < 32 ? (ln >> 3) * 2048 + (ln & 7) * 16 : OOB, FL_XN + (cu * 2 + mt) * 128);
            }
            ST(5);
            STFLUSH(t);
        }
    }
}

__device__ __forceinline__ int par_votes(const char* ws) {
    int v = *(const int*)(ws + FL_PAR + (threadIdx.x & 63) * 4);
    for (int o = 32; o; o >>= 1) v += __shfl_xor(v, o);
    return __builtin_amdgcn_readfirstlane(v);
}
__global__ __launch_bounds__(512, 2) void mega_kernel(Params P) {
    const int wg = blockIdx.x;
    {
        const int v = par_votes(P.ws);
        P.par = v > 0 ? 1u : 0u;
    }
    const int wave = __builtin_amdgcn_readfirstlane(threadIdx.x >> 6);
    const int L = (wg >> 2) & 1, x = wg & 3, r = wg >> 3, cpy = x;
    if (r < 8) s_role(P, wave, L, x * 8 + r, cpy);
    else if (r < 16) v_role(P, wave, L, x * 8 + r - 8, cpy);
    else if (r < 26) r_role(P, wave, L, (r - 16) * 4 + x, cpy);
    else {
        const int i = (r - 26) * 4 + x;
        if (i < 5) w_role<1>(P, wave, L, i);
        else if (i < 10) w_role<2>(P, wave, L, i - 5);
        else if (i < 16) w_role<3>(P, wave, L, i - 10);
        else w4_role(P, wave, L, i - 16);
    }
}

__device__ __forceinline__ void xtile_body(int mt, const bf16_t* __restrict__ x, char* __restrict__ ws) {
    __shared__ float red[2][16][17];
    const int r15 = threadIdx.x & 15, slot = threadIdx.x >> 4;
    u32x4 v[5];
    float s = 0.f;
#pragma unroll
    for (int c = 0; c < 5; ++c) {
        v[c] = *(const u32x4*)(x + (size_t)(mt * 16 + r15) * DM + (slot * 5 + c) * 8);
#pragma unroll
        for (int e = 0; e < 4; ++e) s += lo2f(v[c][e]) + hi2f(v[c][e]);
    }
    red[0][slot][r15] = s;
    __syncthreads();
    s = 0.f;
#pragma unroll
    for (int k = 0; k < 16; ++k) s += red[0][k][r15];
    const float mean = s * (1.0f / DM);
    float q = 0.f;
#pragma unroll
    for (int c = 0; c < 5; ++c)
#pragma unroll
        for (int e = 0; e < 4; ++e) { const float a = lo2f(v[c][e]) - mean, b = hi2f(v[c][e]) - mean; q += a * a + b * b; }
    red[1][slot][r15] = q;
    __syncthreads();
    q = 0.f;
#pragma unroll
    for (int k = 0; k < 16; ++k) q += red[1][k][r15];
    const float m1 = rbf(mean), r = 1.0f / ln_den(q * (1.0f / DM));
#pragma unroll
    for (int c = 0; c < 5; ++c) {
        const unsigned o = toff(mt * 16 + r15, (slot * 5 + c) * 8);
        *(u32x4*)(ws + D_XR + o) = v[c];
        u32x4 n;
#pragma unroll
        for (int e = 0; e < 4; ++e) n[e] = pack2(rbf(lo2f(v[c][e]) - m1) * r, rbf(hi2f(v[c][e]) - m1) * r);
        *(u32x4*)(ws + D_XN + o) = n;
    }
}

__global__ __launch_bounds__(256) void cal_kernel(char* ws) {
    const int b = blockIdx.x;
        if (b < 16 && (threadIdx.x & 63) == 0) {
            unsigned xcc;
            asm volatile("s_getreg_b32 %0, hwreg(HW_REG_XCC_ID)" : "=s"(xcc));
            const int v = b * 4 + (threadIdx.x >> 6);
            int sum = 0;
            unsigned prev = 0;
            for (int r = 0; r < 6; ++r) {
                char* p = ws + CAL0 + v * WIN + (v & 7) * 1024 + (r & 1) * 8192;
                unsigned long long a, c;
                asm volatile("s_waitcnt vmcnt(0) lgkmcnt(0)\n\ts_memtime %0\n\ts_waitcnt lgkmcnt(0)\n\tglobal_store_dword %3, %2, off sc1\n\ts_waitcnt vmcnt(0)\n\ts_memtime %1\n\ts_waitcnt lgkmcnt(0)"
                             : "=&s"(a), "=&s"(c) : "v"(r), "v"(p) : "memory");
                const unsigned d = (unsigned)(c - a);
                if ((r & 1) && r > 1) sum += prev > d ? 1 : -1;
                prev = d;
            }
            *(int*)(ws + FL_PAR + v * 4) = (xcc & 4) ? -sum : sum;
        }
}
struct Prep1 { TileJobs J; const bf16_t* x; char* ws; };
__global__ __launch_bounds__(256) void prep1_kernel(Prep1 A) {
    int b = blockIdx.x;
    if (b < 256) {
        const u32x4 s4 = {SENT, SENT, SENT, SENT};
        if (b == 16) for (unsigned i = threadIdx.x; i < 3 * WIN / 16; i += 256) ((u32x4*)(A.ws + FL_VO))[i] = (u32x4){0u, 0u, 0u, 0u};
        if (b == 17 && threadIdx.x < 32) ((u32x4*)(A.ws + FL0))[threadIdx.x] = (u32x4){0u, 0u, 0u, 0u};
        for (unsigned i = b * 256 + threadIdx.x; i < REG / 16; i += 256 * 256) ((u32x4*)A.ws)[i] = s4;
        for (unsigned i = b * 256 + threadIdx.x; i < 4 * 40 * WIN / 16; i += 256 * 256) { const unsigned sl = i / (40 * WIN / 16); ((u32x4*)(A.ws + RG_VM + sl * VM_SLOT))[i - sl * (40 * WIN / 16)] = s4; }
        {
            const int u = b >> 6;
            const unsigned n16 = (AH + 1) * (u == 3 ? 12 : 5) * WIN / 16;
            u32x4* p = (u32x4*)(A.ws + D_XA + u * 4 * REG);
            for (unsigned i = (b & 63) * 256 + threadIdx.x; i < n16; i += 64 * 256) p[i] = s4;
        }
        return;
    }
    b -= 256;
    if (b < S_ / 16) { xtile_body(b, A.x, A.ws + (par_votes(A.ws) > 0 ? 8192 : 0)); return; }
    tile_body(A.J, b - S_ / 16);
}

extern "C" void kernel_launch(void* const* d_in, const int* in_sizes, int n_in, void* d_out, int out_size, void* d_ws,
                              size_t ws_size, hipStream_t stream) {
    char* ws = (char*)d_ws;
    Params P;
    P.par = 0; P.pad = 0; P.ws = ws; P.mem0 = (const bf16_t*)d_in[1]; P.out = (bf16_t*)d_out;
    (void)hipFuncSetAttribute((const void*)mega_kernel, hipFuncAttributeMaxDynamicSharedMemorySize, MEGA_LDS);

    Prep1 A1;
    int blk = 0;
    auto job = [&](int i, int in, unsigned o, int Kd, int Nd, int ksh) { A1.J.j[i] = TileJob{(const bf16_t*)d_in[in], (bf16_t*)(ws + o), i == 3 ? 1 : 0, Nd, ksh, blk}; blk += (Kd / 32) * (Nd / 128); };
    job(0, 2, WT_QKV, DM, HD, 20);
    job(1, 3, WT_QKV + HD * DM * 2, DM, HD, 20);
    job(2, 4, WT_QKV + 2 * HD * DM * 2, DM, HD, 20);
    job(3, 5, WT_O, HD, DM, 6);
    job(4, 8, WT_1, DM, DM, 20);
    job(5, 9, WT_2, DM, DM, 20);
    job(6, 10, WT_3, DM, FF, 20);
    job(7, 11, WT_4, FF, DM, 48);
    A1.x = (const bf16_t*)d_in[0]; A1.ws = ws;
    cal_kernel<<<16, 256, 0, stream>>>(ws);
    prep1_kernel<<<256 + S_ / 16 + blk, 256, 0, stream>>>(A1);
    mega_kernel<<<256, 512, MEGA_LDS, stream>>>(P);
}
```

```cpp
#include <hip/hip_runtime.h>
#include <stdint.h>
#include <type_traits>

typedef unsigned short bf16_t;
using bf16x2 = __attribute__((ext_vector_type(2))) __bf16;
using bf16x8 = __attribute__((ext_vector_type(8))) __bf16;
using bf16x16 = __attribute__((ext_vector_type(16))) __bf16;
using f32x2 = __attribute__((ext_vector_type(2))) float;
using f32x4 = __attribute__((ext_vector_type(4))) float;
using u32x2 = __attribute__((ext_vector_type(2))) unsigned;
using u32x4 = __attribute__((ext_vector_type(4))) unsigned;
typedef __amdgpu_buffer_rsrc_t rsrc_t;

__device__ __forceinline__ unsigned pack2(float a, float b) {
    bf16x2 r = __builtin_convertvector((f32x2){a, b}, bf16x2);
    return __builtin_bit_cast(unsigned, r);
}
__device__ __forceinline__ float rbf(float f) { return __uint_as_float(pack2(f, 0.f) << 16); }
__device__ __forceinline__ float lo2f(unsigned u) { return __uint_as_float(u << 16); }
__device__ __forceinline__ float hi2f(unsigned u) { return __uint_as_float(u & 0xFFFF0000u); }
__device__ __forceinline__ rsrc_t mkrsrc(const void* p, unsigned bytes) {
    return __builtin_amdgcn_make_buffer_rsrc((void*)p, 0, bytes, 0x00020000);
}

constexpr int S_ = 2048, DM = 640, NH = 32, DH = 192, HD = NH * DH  , SEG = 32, NSEG = 64;
constexpr int FF = 1536;

#define NCPY 4
constexpr unsigned NSLOT = 4, NCP = NCPY;
constexpr unsigned WIN = 16384, REG = 2u << 20, REGS = REG + 8192;
constexpr unsigned XP_COPY = 3 * WIN, XP_SLOT = NCP * XP_COPY;
constexpr unsigned XM_COPY = 2 * WIN, XM_SLOT = NCP * XM_COPY;
constexpr unsigned VM_SLOT = REGS;
constexpr unsigned PX_SLOT = 8 * WIN;
constexpr unsigned RG_XM = 0, RG_XP = RG_XM + NSLOT * XM_SLOT, RG_PX = RG_XP + NSLOT * XP_SLOT, RG_ST = RG_PX + NSLOT * PX_SLOT, RG_VM = 2 * REG;
static_assert(RG_ST + 16 * WIN <= REG, "ring region");
constexpr unsigned FL0 = REG, FL_PAR = REG + 4096;
constexpr unsigned FL_VO = RG_VM + 48 * WIN, FL_XN = FL_VO + 2 * WIN, D_SS = FL_VO + 4 * WIN, CAL0 = RG_VM + 64 * WIN;
constexpr unsigned ACT = S_ * DM * 2;
constexpr unsigned D_XR = 6 * REG, D_XN = 10 * REG, D_XA = 14 * REG, D_HS = 18 * REG, D_X1 = 22 * REG, D_H2 = 26 * REG, D_VX = 34 * REG;
__host__ __device__ constexpr unsigned segb(unsigned seg) { return seg * (5 * WIN) + (seg >> 4) * (REGS - 80 * WIN); }
__host__ __device__ constexpr unsigned segh(unsigned seg) { return seg * (12 * WIN) + (seg >> 3) * (REGS - 96 * WIN); }
#define CALN 4
#define RK 3
#define PXROT 5
#define W4SL 6
#define RXDEL 4
#define VXD 16
#define VXAUX 16
__host__ __device__ constexpr unsigned vxb(unsigned q, unsigned t) { return D_VX + q * (VXD / 2 * REG) + (t & (VXD - 1)) * (REG / 2) + (t & 2) * 4096; }
constexpr int AH = 3;
constexpr unsigned RG_L = D_VX + 2 * VXD * REG;

#define SB() __builtin_amdgcn_sched_barrier(0)
#define LAS __attribute__((address_space(3)))
template <class T> __device__ __forceinline__ T lds_ld(unsigned a) { return *(const LAS T*)(a); }
template <class T> __device__ __forceinline__ void lds_st(unsigned a, T v) { *(LAS T*)(a) = v; }
constexpr unsigned OOB = 0x7FFFFF00u;
template <int N> __device__ __forceinline__ void wait_vm() { asm volatile("s_waitcnt vmcnt(%0)" :: "i"(N) : "memory"); }
__device__ __forceinline__ void bar() {
    asm volatile("s_waitcnt lgkmcnt(0)" ::: "memory");
    __builtin_amdgcn_s_barrier();
    asm volatile("" ::: "memory");
}
__device__ __forceinline__ int lane_id() {
    int ln;
    asm volatile("v_mbcnt_lo_u32_b32 %0, -1, 0\n\tv_mbcnt_hi_u32_b32 %0, -1, %0" : "=v"(ln));
    return ln;
}
__device__ __forceinline__ void swap16(unsigned& a, unsigned& b) { asm volatile("s_nop 3\n\tv_permlane16_swap_b32 %0, %1\n\ts_nop 1" : "+v"(a), "+v"(b)); }
__device__ __forceinline__ void swap32(unsigned& a, unsigned& b) { asm volatile("s_nop 3\n\tv_permlane32_swap_b32 %0, %1\n\ts_nop 1" : "+v"(a), "+v"(b)); }
__device__ __forceinline__ float sum_x16(float x) { unsigned a = __float_as_uint(x), b = a; swap16(a, b); return __uint_as_float(a) + __uint_as_float(b); }
__device__ __forceinline__ float sum_x32(float x) { unsigned a = __float_as_uint(x), b = a; swap32(a, b); return __uint_as_float(a) + __uint_as_float(b); }
__device__ __forceinline__ float max_x16(float x) { unsigned a = __float_as_uint(x), b = a; swap16(a, b); return __builtin_fmaxf(__uint_as_float(a), __uint_as_float(b)); }
__device__ __forceinline__ float max_x32(float x) { unsigned a = __float_as_uint(x), b = a; swap32(a, b); return __builtin_fmaxf(__uint_as_float(a), __uint_as_float(b)); }
__device__ __forceinline__ void sum2_rows(float& s, float& q) {
    unsigned a = __float_as_uint(s), b = a, c = __float_as_uint(q), d = c;
    asm volatile("s_nop 2\n\tv_permlane16_swap_b32 %0, %1\n\tv_permlane16_swap_b32 %2, %3\n\tv_add_f32 %0, %0, %1\n\tv_add_f32 %2, %2, %3\n\tv_mov_b32 %1, %0\n\tv_mov_b32 %3, %2\n\t"
                 "s_nop 1\n\tv_permlane32_swap_b32 %0, %1\n\tv_permlane32_swap_b32 %2, %3\n\tv_add_f32 %0, %0, %1\n\tv_add_f32 %2, %2, %3" : "+v"(a), "+v"(b), "+v"(c), "+v"(d));
    s = __uint_as_float(a); q = __uint_as_float(c);
}
__device__ __forceinline__ u32x4 merge16(u32x2 a, u32x2 b) {
    unsigned a0 = a[0], a1 = a[1], b0 = b[0], b1 = b[1];
    asm volatile("s_nop 3\n\tv_permlane16_swap_b32 %0, %2\n\tv_permlane16_swap_b32 %1, %3\n\ts_nop 1" : "+v"(a0), "+v"(a1), "+v"(b0), "+v"(b1));
    return (u32x4){a0, a1, b0, b1};
}
template <int AUXV>
__device__ __forceinline__ void st128_(u32x4 v, rsrc_t r, unsigned voff, unsigned soff) {
    __builtin_amdgcn_raw_buffer_store_b128(v, r, voff, soff, AUXV);
    asm volatile("s_nop 1" :: "v"(v) : "memory");
}
#define st128(V, R, VO_, SO_) st128_<16>(V, R, VO_, SO_)
#define LD128(R, VO_, SO_) __builtin_amdgcn_raw_buffer_load_b128(R, VO_, SO_, 16)
#define LD64(R, VO_, SO_) __builtin_amdgcn_raw_buffer_load_b64(R, VO_, SO_, 16)
#define BF8(X) __builtin_bit_cast(bf16x8, X)
__device__ __forceinline__ float gelu_tanh(float x) {
    const float c0 = -2.0f * 0.7978845608028654f * 1.4426950408889634f, c1 = c0 * 0.044715f;
    const float e = __builtin_amdgcn_exp2f(x * __builtin_fmaf(x * x, c1, c0));
    return x * __builtin_amdgcn_rcpf(1.0f + e);
}
#define MFMA(A_, B_, C_) C_ = __builtin_amdgcn_mfma_f32_16x16x32_bf16(A_, B_, C_, 0, 0, 0)
#define SMF(A_, B_, C_) C_ = __builtin_amdgcn_smfmac_f32_16x16x64_bf16(A_, B_, C_, sidx, 0, 0)
__device__ __forceinline__ float ln_den(float var) { return rbf(__builtin_amdgcn_sqrtf(rbf(rbf(var) + 1e-5f))); }
__device__ __forceinline__ float rcp(float x) { return __builtin_amdgcn_rcpf(x); }

struct Params {
    char* ws;
    const bf16_t *x, *mem0, *wq, *wk, *wv, *wo, *w1, *w2, *w3, *w4;
    bf16_t* out;
    unsigned par, pad;
};

#define RLX_AGENT __ATOMIC_RELAXED, __HIP_MEMORY_SCOPE_AGENT
#define SENT 0xFFFFFFFFu
constexpr int SPIN_CAP = 400000;
__device__ __forceinline__ bool spin_fail(int spins, const Params& P) {
    if ((spins & 1023) == 1023) {
        const rsrc_t rt = mkrsrc(P.ws + FL0, 512);
        unsigned o;
        asm volatile("v_mov_b32 %0, 0x100" : "=v"(o));
        if (__builtin_amdgcn_readfirstlane(__builtin_amdgcn_raw_buffer_load_b32(rt, o, 0, 16)) != 0u) return true;
        if (spins > SPIN_CAP) { __builtin_amdgcn_raw_buffer_store_b32(o, rt, o, 0, 16); return true; }
    }
    return false;
}
__device__ __forceinline__ bool ok4(u32x4 v) { return v[0] != SENT && v[1] != SENT && v[2] != SENT && v[3] != SENT; }
__device__ __forceinline__ unsigned nosent(unsigned d) { return d < 0xFFFFFFFEu ? d : 0xFFFFFFFEu; }
__device__ __forceinline__ u32x4 nosent4(u32x4 v) { return (u32x4){nosent(v[0]), nosent(v[1]), nosent(v[2]), nosent(v[3])}; }
__device__ __forceinline__ unsigned umax3(unsigned a, unsigned b, unsigned c) { const unsigned m = a > b ? a : b; return m > c ? m : c; }
__device__ __forceinline__ unsigned umax4(unsigned m, u32x4 v) { return umax3(umax3(m, v[0], v[1]), v[2], v[3]); }
__device__ __forceinline__ u32x4 sent4() { unsigned sv; asm volatile("v_mov_b32 %0, -1" : "=v"(sv)); return (u32x4){sv, sv, sv, sv}; }
template <int N>
__device__ __forceinline__ void poll(rsrc_t rs, unsigned base, const unsigned (&off)[N], u32x4 (&v)[N], const Params& P) {
    for (int spins = 0;; ++spins) {
        unsigned m = 0;
#pragma unroll
        for (int k = 0; k < N; ++k) v[k] = LD128(rs, off[k], base);
#pragma unroll
        for (int k = 0; k < N; ++k) m = umax4(m, v[k]);
        if (__all(m != SENT)) return;
        asm volatile("" ::: "memory");
        if (spin_fail(spins, P)) return;
    }
}
__device__ __forceinline__ bf16x16 cat16(bf16x8 a, bf16x8 b) {
    return __builtin_shufflevector(a, b, 0, 1, 2, 3, 4, 5, 6, 7, 8, 9, 10, 11, 12, 13, 14, 15);
}
__device__ __forceinline__ bf16x8 half8(bf16x16 v, int h) {
    return h ? __builtin_shufflevector(v, v, 8, 9, 10, 11, 12, 13, 14, 15) : __builtin_shufflevector(v, v, 0, 1, 2, 3, 4, 5, 6, 7);
}
__device__ __forceinline__ bf16x16 lds_frag(unsigned a, unsigned step) { return cat16(lds_ld<bf16x8>(a), lds_ld<bf16x8>(a + step)); }
__device__ __forceinline__ unsigned toff(int row, int col) {
    return segb((unsigned)row >> 5) + (unsigned)(col >> 7) * WIN + (unsigned)(row & 16) * 256u + (unsigned)((col >> 3) & 15) * 256u + (unsigned)(row & 15) * 16u + (unsigned)(col & 7) * 2u;
}
__device__ __forceinline__ char* lbase(const Params& P, int L) { return P.ws + ((P.par ^ L) << 13); }

#define ASM_ST
#define ASM_ST_DECL
#define ASM_ST_OUT
#define ASM_ST_SAVE(k)
#define ASM_STW
#define ASM_ST3
#define ASM_ST3_OUT
#define ASM_ST3_SAVE(k)
#define ASM_ST2
#define ASM_ST2_OUT
#define ASM_ST2_SAVE(k)
#define LST0
#define LST1
#define LST2
#define LST3
#define LST4
#define LST5
#define LST6
#define LST7
#define LSTINIT
#define LFLUSH200
#define LFLUSH212
#define LST_IN
#define STDECL
#define ST(k)
#define STFLUSH(t)

constexpr unsigned L_X = 0;
constexpr unsigned L_MEM = 40960;
constexpr unsigned L_Q = 51200;
constexpr unsigned L_KM = 54272;
constexpr unsigned L_QX = 57344;
constexpr unsigned L_KX = 69632;
constexpr unsigned L_W = 81920;
constexpr int MEGA_LDS = 163840;
constexpr int wi(int i, int j) { return (i >> 1) * 5 + ((i & 1) ? 2 + j : j - 1); }

#define RPUB2
#define ASM_S_LOOP \
    "s_load_dwordx4 s[48:51], %[pp], 0\n\t" \
    "s_mov_b64 s[60:61], %[ws]\n\t" \
    "s_mov_b32 s62, %[sz]\n\t" \
    "s_mov_b32 s63, 0x20000\n\t" \
    "s_mov_b32 s40, 0\n\t" \
    "s_mov_b32 s41, 0x20000\n\t" \
    LSTINIT \
    LDELAY \
    "s_cmp_eq_u32 %[wave], 0\n\t" \
    "s_cbranch_scc1 .Ls0_%=\n\t" \
    "s_cmp_eq_u32 %[wave], 1\n\t" \
    "s_cbranch_scc1 .Ls1_%=\n\t" \
    "s_cmp_eq_u32 %[wave], 2\n\t" \
    "s_cbranch_scc1 .Ls2_%=\n\t" \
    "s_cmp_eq_u32 %[wave], 3\n\t" \
    "s_cbranch_scc1 .Ls3_%=\n\t" \
    ".Ls4_%=:\n\t" \
    "s_mov_b32 s42, %[xs]\n\t" \
    "s_add_u32 m0, %[wm], 0x18000\n\t" \
    "s_mov_b32 s44, 0x0\n\t" \
    "buffer_load_dwordx4 v240, %[wr], s44 offen offset:0 nt lds\n\t" \
    "buffer_load_dwordx4 v241, %[wr], s44 offen offset:1024 nt lds\n\t" \
    "buffer_load_dwordx4 v242, %[wr], s44 offen offset:2048 nt lds\n\t" \
    "s_add_u32 m0, %[wm], 0x1e000\n\t" \
    "s_mov_b32 s44, 0x60000\n\t" \
    "buffer_load_dwordx4 v240, %[wr], s44 offen offset:0 nt lds\n\t" \
    "buffer_load_dwordx4 v241, %[wr], s44 offen offset:1024 nt lds\n\t" \
    "buffer_load_dwordx4 v242, %[wr], s44 offen offset:2048 nt lds\n\t" \
    "s_add_u32 m0, %[wm], 0x0\n\t" \
    "s_mov_b32 s44, 0xc0000\n\t" \
    "buffer_load_dwordx4 v240, %[wr], s44 offen offset:0 nt lds\n\t" \
    "buffer_load_dwordx4 v241, %[wr], s44 offen offset:1024 nt lds\n\t" \
    "buffer_load_dwordx4 v242, %[wr], s44 offen offset:2048 nt lds\n\t" \
    "s_add_u32 m0, %[wm], 0x6000\n\t" \
    "s_mov_b32 s44, 0x120000\n\t" \
    "buffer_load_dwordx4 v240, %[wr], s44 offen offset:0 nt lds\n\t" \
    "buffer_load_dwordx4 v241, %[wr], s44 offen offset:1024 nt lds\n\t" \
    "buffer_load_dwordx4 v242, %[wr], s44 offen offset:2048 nt lds\n\t" \
    "s_add_u32 m0, %[wm], 0xc000\n\t" \
    "s_mov_b32 s44, 0x180000\n\t" \
    "buffer_load_dwordx4 v240, %[wr], s44 offen offset:0 nt lds\n\t" \
    "buffer_load_dwordx4 v241, %[wr], s44 offen offset:1024 nt lds\n\t" \
    "buffer_load_dwordx4 v242, %[wr], s44 offen offset:2048 nt lds\n\t" \
    "s_waitcnt lgkmcnt(0)\n\t" \
    "s_add_u32 s43, s48, s49\n\t" \
    "s_add_u32 s43, s43, s50\n\t" \
    "s_and_b32 s43, s43, 0x2000\n\t" \
    "s_xor_b32 s43, s43, %[lx]\n\t" \
    "s_add_u32 s60, s60, s43\n\t" \
    "s_addc_u32 s61, s61, 0\n\t" \
    "v_add_u32 v244, 0x18000, v243\n\t" \
    "s_waitcnt vmcnt(12) lgkmcnt(0)\n\t" \
    "s_barrier \n\t" \
    "s_add_u32 m0, %[wm], 0x12000\n\t" \
    "s_mov_b32 s44, 0x1e0000\n\t" \
    "buffer_load_dwordx4 v240, %[wr], s44 offen offset:0 nt lds\n\t" \
    "buffer_load_dwordx4 v241, %[wr], s44 offen offset:1024 nt lds\n\t" \
    "buffer_load_dwordx4 v242, %[wr], s44 offen offset:2048 nt lds\n\t" \
    "ds_read_b64_tr_b16 v[200:201], v244 offset:0\n\t" \
    "ds_read_b64_tr_b16 v[202:203], v244 offset:1536\n\t" \
    "ds_read_b64_tr_b16 v[0:1], v244 offset:32\n\t" \
    "ds_read_b64_tr_b16 v[2:3], v244 offset:1568\n\t" \
    "ds_read_b64_tr_b16 v[8:9], v244 offset:64\n\t" \
    "ds_read_b64_tr_b16 v[10:11], v244 offset:1600\n\t" \
    "v_add_u32 v244, 0x1e000, v243\n\t" \
    "s_waitcnt vmcnt(12) lgkmcnt(0)\n\t" \
    "s_barrier \n\t" \
    "s_add_u32 m0, %[wm], 0x18000\n\t" \
    "s_mov_b32 s44, 0x240000\n\t" \
    "buffer_load_dwordx4 v240, %[wr], s44 offen offset:0 nt lds\n\t" \
    "buffer_load_dwordx4 v241, %[wr], s44 offen offset:1024 nt lds\n\t" \
    "buffer_load_dwordx4 v242, %[wr], s44 offen offset:2048 nt lds\n\t" \
    "ds_read_b64_tr_b16 v[204:205], v244 offset:0\n\t" \
    "ds_read_b64_tr_b16 v[206:207], v244 offset:1536\n\t" \
    "ds_read_b64_tr_b16 v[4:5], v244 offset:32\n\t" \
    "ds_read_b64_tr_b16 v[6:7], v244 offset:1568\n\t" \
    "ds_read_b64_tr_b16 v[12:13], v244 offset:64\n\t" \
    "ds_read_b64_tr_b16 v[14:15], v244 offset:1600\n\t" \
    "v_add_u32 v244, 0x0, v243\n\t" \
    "s_waitcnt vmcnt(12) lgkmcnt(0)\n\t" \
    "s_barrier \n\t" \
    "s_add_u32 m0, %[wm], 0x1e000\n\t" \
    "s_mov_b32 s44, 0x2a0000\n\t" \
    "buffer_load_dwordx4 v240, %[wr], s44 offen offset:0 nt lds\n\t" \
    "buffer_load_dwordx4 v241, %[wr], s44 offen offset:1024 nt lds\n\t" \
    "buffer_load_dwordx4 v242, %[wr], s44 offen offset:2048 nt lds\n\t" \
    "ds_read_b64_tr_b16 v[16:17], v244 offset:0\n\t" \
    "ds_read_b64_tr_b16 v[18:19], v244 offset:1536\n\t" \
    "ds_read_b64_tr_b16 v[24:25], v244 offset:32\n\t" \
    "ds_read_b64_tr_b16 v[26:27], v244 offset:1568\n\t" \
    "ds_read_b64_tr_b16 v[32:33], v244 offset:64\n\t" \
    "ds_read_b64_tr_b16 v[34:35], v244 offset:1600\n\t" \
    "v_add_u32 v244, 0x6000, v243\n\t" \
    "s_waitcnt vmcnt(12) lgkmcnt(0)\n\t" \
    "s_barrier \n\t" \
    "s_add_u32 m0, %[wm], 0x0\n\t" \
    "s_mov_b32 s44, 0x300000\n\t" \
    "buffer_load_dwordx4 v240, %[wr], s44 offen offset:0 nt lds\n\t" \
    "buffer_load_dwordx4 v241, %[wr], s44 offen offset:1024 nt lds\n\t" \
    "buffer_load_dwordx4 v242, %[wr], s44 offen offset:2048 nt lds\n\t" \
    "ds_read_b64_tr_b16 v[20:21], v244 offset:0\n\t" \
    "ds_read_b64_tr_b16 v[22:23], v244 offset:1536\n\t" \
    "ds_read_b64_tr_b16 v[28:29], v244 offset:32\n\t" \
    "ds_read_b64_tr_b16 v[30:31], v244 offset:1568\n\t" \
    "ds_read_b64_tr_b16 v[36:37], v244 offset:64\n\t" \
    "ds_read_b64_tr_b16 v[38:39], v244 offset:1600\n\t" \
    "v_add_u32 v244, 0xc000, v243\n\t" \
    "s_waitcnt vmcnt(12) lgkmcnt(0)\n\t" \
    "s_barrier \n\t" \
    "s_add_u32 m0, %[wm], 0x6000\n\t" \
    "s_mov_b32 s44, 0x360000\n\t" \
    "buffer_load_dwordx4 v240, %[wr], s44 offen offset:0 nt lds\n\t" \
    "buffer_load_dwordx4 v241, %[wr], s44 offen offset:1024 nt lds\n\t" \
    "buffer_load_dwordx4 v242, %[wr], s44 offen offset:2048 nt lds\n\t" \
    "ds_read_b64_tr_b16 v[208:209], v244 offset:0\n\t" \
    "ds_read_b64_tr_b16 v[210:211], v244 offset:1536\n\t" \
    "ds_read_b64_tr_b16 v[40:41], v244 offset:32\n\t" \
    "ds_read_b64_tr_b16 v[42:43], v244 offset:1568\n\t" \
    "ds_read_b64_tr_b16 v[48:49], v244 offset:64\n\t" \
    "ds_read_b64_tr_b16 v[50:51], v244 offset:1600\n\t" \
    "v_add_u32 v244, 0x12000, v243\n\t" \
    "s_waitcnt vmcnt(12) lgkmcnt(0)\n\t" \
    "s_barrier \n\t" \
    "s_add_u32 m0, %[wm], 0xc000\n\t" \
    "s_mov_b32 s44, 0x3c0000\n\t" \
    "buffer_load_dwordx4 v240, %[wr], s44 offen offset:0 nt lds\n\t" \
    "buffer_load_dwordx4 v241, %[wr], s44 offen offset:1024 nt lds\n\t" \
    "buffer_load_dwordx4 v242, %[wr], s44 offen offset:2048 nt lds\n\t" \
    "ds_read_b64_tr_b16 v[212:213], v244 offset:0\n\t" \
    "ds_read_b64_tr_b16 v[214:215], v244 offset:1536\n\t" \
    "ds_read_b64_tr_b16 v[44:45], v244 offset:32\n\t" \
    "ds_read_b64_tr_b16 v[46:47], v244 offset:1568\n\t" \
    "ds_read_b64_tr_b16 v[52:53], v244 offset:64\n\t" \
    "ds_read_b64_tr_b16 v[54:55], v244 offset:1600\n\t" \
    "v_add_u32 v244, 0x18000, v243\n\t" \
    "s_waitcnt vmcnt(12) lgkmcnt(0)\n\t" \
    "s_barrier \n\t" \
    "s_add_u32 m0, %[wm], 0x12000\n\t" \
    "s_mov_b32 s44, 0x420000\n\t" \
    "buffer_load_dwordx4 v240, %[wr], s44 offen offset:0 nt lds\n\t" \
    "buffer_load_dwordx4 v241, %[wr], s44 offen offset:1024 nt lds\n\t" \
    "buffer_load_dwordx4 v242, %[wr], s44 offen offset:2048 nt lds\n\t" \
    "ds_read_b64_tr_b16 v[56:57], v244 offset:0\n\t" \
    "ds_read_b64_tr_b16 v[58:59], v244 offset:1536\n\t" \
    "ds_read_b64_tr_b16 v[64:65], v244 offset:32\n\t" \
    "ds_read_b64_tr_b16 v[66:67], v244 offset:1568\n\t" \
    "ds_read_b64_tr_b16 v[72:73], v244 offset:64\n\t" \
    "ds_read_b64_tr_b16 v[74:75], v244 offset:1600\n\t" \
    "v_add_u32 v244, 0x1e000, v243\n\t" \
    "s_waitcnt vmcnt(12) lgkmcnt(0)\n\t" \
    "s_barrier \n\t" \
    "s_add_u32 m0, %[wm], 0x18000\n\t" \
    "s_mov_b32 s44, 0x480000\n\t" \
    "buffer_load_dwordx4 v240, %[wr], s44 offen offset:0 nt lds\n\t" \
    "buffer_load_dwordx4 v241, %[wr], s44 offen offset:1024 nt lds\n\t" \
    "buffer_load_dwordx4 v242, %[wr], s44 offen offset:2048 nt lds\n\t" \
    "ds_read_b64_tr_b16 v[60:61], v244 offset:0\n\t" \
    "ds_read_b64_tr_b16 v[62:63], v244 offset:1536\n\t" \
    "ds_read_b64_tr_b16 v[68:69], v244 offset:32\n\t" \
    "ds_read_b64_tr_b16 v[70:71], v244 offset:1568\n\t" \
    "ds_read_b64_tr_b16 v[76:77], v244 offset:64\n\t" \
    "ds_read_b64_tr_b16 v[78:79], v244 offset:1600\n\t" \
    "v_add_u32 v244, 0x0, v243\n\t" \
    "s_waitcnt vmcnt(12) lgkmcnt(0)\n\t" \
    "s_barrier \n\t" \
    "s_add_u32 m0, %[wm], 0x1e000\n\t" \
    "s_mov_b32 s44, 0x4e0000\n\t" \
    "buffer_load_dwordx4 v240, %[wr], s44 offen offset:0 nt lds\n\t" \
    "buffer_load_dwordx4 v241, %[wr], s44 offen offset:1024 nt lds\n\t" \
    "buffer_load_dwordx4 v242, %[wr], s44 offen offset:2048 nt lds\n\t" \
    "ds_read_b64_tr_b16 v[216:217], v244 offset:0\n\t" \
    "ds_read_b64_tr_b16 v[218:219], v244 offset:1536\n\t" \
    "ds_read_b64_tr_b16 v[80:81], v244 offset:32\n\t" \
    "ds_read_b64_tr_b16 v[82:83], v244 offset:1568\n\t" \
    "ds_read_b64_tr_b16 v[88:89], v244 offset:64\n\t" \
    "ds_read_b64_tr_b16 v[90:91], v244 offset:1600\n\t" \
    "v_add_u32 v244, 0x6000, v243\n\t" \
    "s_waitcnt vmcnt(12) lgkmcnt(0)\n\t" \
    "s_barrier \n\t" \
    "v_and_b32 v252, 0xf0, v253\n\t" \
    "v_lshlrev_b32 v252, 3, v252\n\t" \
    "buffer_load_dword v252, v252, s[60:63], %[fl] offen sc1\n\t" \
    "s_add_u32 m0, %[wm], 0x0\n\t" \
    "s_mov_b32 s44, 0x540000\n\t" \
    "buffer_load_dwordx4 v240, %[wr], s44 offen offset:0 nt lds\n\t" \
    "buffer_load_dwordx4 v241, %[wr], s44 offen offset:1024 nt lds\n\t" \
    "buffer_load_dwordx4 v242, %[wr], s44 offen offset:2048 nt lds\n\t" \
    "ds_read_b64_tr_b16 v[220:221], v244 offset:0\n\t" \
    "ds_read_b64_tr_b16 v[222:223], v244 offset:1536\n\t" \
    "ds_read_b64_tr_b16 v[84:85], v244 offset:32\n\t" \
    "ds_read_b64_tr_b16 v[86:87], v244 offset:1568\n\t" \
    "ds_read_b64_tr_b16 v[92:93], v244 offset:64\n\t" \
    "ds_read_b64_tr_b16 v[94:95], v244 offset:1600\n\t" \
    "v_add_u32 v244, 0xc000, v243\n\t" \
    "s_waitcnt vmcnt(12) lgkmcnt(0)\n\t" \
    "s_barrier \n\t" \
    "s_add_u32 m0, %[wm], 0x6000\n\t" \
    "s_mov_b32 s44, 0x5a0000\n\t" \
    "buffer_load_dwordx4 v240, %[wr], s44 offen offset:0 nt lds\n\t" \
    "buffer_load_dwordx4 v241, %[wr], s44 offen offset:1024 nt lds\n\t" \
    "buffer_load_dwordx4 v242, %[wr], s44 offen offset:2048 nt lds\n\t" \
    "ds_read_b64_tr_b16 v[96:97], v244 offset:0\n\t" \
    "ds_read_b64_tr_b16 v[98:99], v244 offset:1536\n\t" \
    "ds_read_b64_tr_b16 v[104:105], v244 offset:32\n\t" \
    "ds_read_b64_tr_b16 v[106:107], v244 offset:1568\n\t" \
    "ds_read_b64_tr_b16 v[112:113], v244 offset:64\n\t" \
    "ds_read_b64_tr_b16 v[114:115], v244 offset:1600\n\t" \
    "v_add_u32 v244, 0x12000, v243\n\t" \
    "s_waitcnt vmcnt(12) lgkmcnt(0)\n\t" \
    "s_barrier \n\t" \
    "s_add_u32 m0, %[wm], 0xc000\n\t" \
    "s_mov_b32 s44, 0x600000\n\t" \
    "buffer_load_dwordx4 v240, %[wr], s44 offen offset:0 nt lds\n\t" \
    "buffer_load_dwordx4 v241, %[wr], s44 offen offset:1024 nt lds\n\t" \
    "buffer_load_dwordx4 v242, %[wr], s44 offen offset:2048 nt lds\n\t" \
    "ds_read_b64_tr_b16 v[100:101], v244 offset:0\n\t" \
    "ds_read_b64_tr_b16 v[102:103], v244 offset:1536\n\t" \
    "ds_read_b64_tr_b16 v[108:109], v244 offset:32\n\t" \
    "ds_read_b64_tr_b16 v[110:111], v244 offset:1568\n\t" \
    "ds_read_b64_tr_b16 v[116:117], v244 offset:64\n\t" \
    "ds_read_b64_tr_b16 v[118:119], v244 offset:1600\n\t" \
    "v_add_u32 v244, 0x18000, v243\n\t" \
    "s_waitcnt vmcnt(12) lgkmcnt(0)\n\t" \
    "s_barrier \n\t" \
    "s_add_u32 m0, %[wm], 0x12000\n\t" \
    "s_mov_b32 s44, 0x660000\n\t" \
    "buffer_load_dwordx4 v240, %[wr], s44 offen offset:0 nt lds\n\t" \
    "buffer_load_dwordx4 v241, %[wr], s44 offen offset:1024 nt lds\n\t" \
    "buffer_load_dwordx4 v242, %[wr], s44 offen offset:2048 nt lds\n\t" \
    "ds_read_b64_tr_b16 v[224:225], v244 offset:0\n\t" \
    "ds_read_b64_tr_b16 v[226:227], v244 offset:1536\n\t" \
    "ds_read_b64_tr_b16 v[120:121], v244 offset:32\n\t" \
    "ds_read_b64_tr_b16 v[122:123], v244 offset:1568\n\t" \
    "ds_read_b64_tr_b16 v[128:129], v244 offset:64\n\t" \
    "ds_read_b64_tr_b16 v[130:131], v244 offset:1600\n\t" \
    "v_add_u32 v244, 0x1e000, v243\n\t" \
    "s_waitcnt vmcnt(12) lgkmcnt(0)\n\t" \
    "s_barrier \n\t" \
    "s_add_u32 m0, %[wm], 0x18000\n\t" \
    "s_mov_b32 s44, 0x6c0000\n\t" \
    "buffer_load_dwordx4 v240, %[wr], s44 offen offset:0 nt lds\n\t" \
    "buffer_load_dwordx4 v241, %[wr], s44 offen offset:1024 nt lds\n\t" \
    "buffer_load_dwordx4 v242, %[wr], s44 offen offset:2048 nt lds\n\t" \
    "ds_read_b64_tr_b16 v[228:229], v244 offset:0\n\t" \
    "ds_read_b64_tr_b16 v[230:231], v244 offset:1536\n\t" \
    "ds_read_b64_tr_b16 v[124:125], v244 offset:32\n\t" \
    "ds_read_b64_tr_b16 v[126:127], v244 offset:1568\n\t" \
    "ds_read_b64_tr_b16 v[132:133], v244 offset:64\n\t" \
    "ds_read_b64_tr_b16 v[134:135], v244 offset:1600\n\t" \
    "v_add_u32 v244, 0x0, v243\n\t" \
    "s_waitcnt vmcnt(12) lgkmcnt(0)\n\t" \
    "s_barrier \n\t" \
    "s_add_u32 m0, %[wm], 0x1e000\n\t" \
    "s_mov_b32 s44, 0x720000\n\t" \
    "buffer_load_dwordx4 v240, %[wr], s44 offen offset:0 nt lds\n\t" \
    "buffer_load_dwordx4 v241, %[wr], s44 offen offset:1024 nt lds\n\t" \
    "buffer_load_dwordx4 v242, %[wr], s44 offen offset:2048 nt lds\n\t" \
    "ds_read_b64_tr_b16 v[136:137], v244 offset:0\n\t" \
    "ds_read_b64_tr_b16 v[138:139], v244 offset:1536\n\t" \
    "ds_read_b64_tr_b16 v[144:145], v244 offset:32\n\t" \
    "ds_read_b64_tr_b16 v[146:147], v244 offset:1568\n\t" \
    "ds_read_b64_tr_b16 v[152:153], v244 offset:64\n\t" \
    "ds_read_b64_tr_b16 v[154:155], v244 offset:1600\n\t" \
    "v_add_u32 v244, 0x6000, v243\n\t" \
    "s_waitcnt vmcnt(12) lgkmcnt(0)\n\t" \
    "s_barrier \n\t" \
    "ds_read_b64_tr_b16 v[140:141], v244 offset:0\n\t" \
    "ds_read_b64_tr_b16 v[142:143], v244 offset:1536\n\t" \
    "ds_read_b64_tr_b16 v[148:149], v244 offset:32\n\t" \
    "ds_read_b64_tr_b16 v[150:151], v244 offset:1568\n\t" \
    "ds_read_b64_tr_b16 v[156:157], v244 offset:64\n\t" \
    "ds_read_b64_tr_b16 v[158:159], v244 offset:1600\n\t" \
    "v_add_u32 v244, 0xc000, v243\n\t" \
    "s_waitcnt vmcnt(9) lgkmcnt(0)\n\t" \
    "s_barrier \n\t" \
    "ds_read_b64_tr_b16 v[232:233], v244 offset:0\n\t" \
    "ds_read_b64_tr_b16 v[234:235], v244 offset:1536\n\t" \
    "ds_read_b64_tr_b16 v[160:161], v244 offset:32\n\t" \
    "ds_read_b64_tr_b16 v[162:163], v244 offset:1568\n\t" \
    "ds_read_b64_tr_b16 v[168:169], v244 offset:64\n\t" \
    "ds_read_b64_tr_b16 v[170:171], v244 offset:1600\n\t" \
    "s_mov_b32 s43, 0\n\t" \
    "v_cmp_eq_u32 vcc, 0, v252\n\t" \
    "s_cbranch_vccnz .Ls41_%=\n\t" \
    "s_mov_b32 m0, %[m0]\n\t" \
    "s_add_u32 s45, s42, %[x1]\n\t" \
    "buffer_load_dwordx4 v253, s[60:63], s42 offen offset:0 sc1 lds\n\t" \
    "buffer_load_dwordx4 v253, s[60:63], s42 offen offset:1024 sc1 lds\n\t" \
    "buffer_load_dwordx4 v253, s[60:63], s42 offen offset:2048 sc1 lds\n\t" \
    "buffer_load_dwordx4 v253, s[60:63], s42 offen offset:3072 sc1 lds\n\t" \
    "s_mov_b32 m0, %[m1]\n\t" \
    "s_nop 0\n\t" \
    "buffer_load_dwordx4 v253, s[60:63], s45 offen offset:0 sc1 lds\n\t" \
    "buffer_load_dwordx4 v253, s[60:63], s45 offen offset:1024 sc1 lds\n\t" \
    "s_mov_b32 s43, 1\n\t" \
    ".Ls41_%=:\n\t" \
    "v_add_u32 v244, 0x12000, v243\n\t" \
    "s_waitcnt vmcnt(6) lgkmcnt(0)\n\t" \
    "s_barrier \n\t" \
    "ds_read_b64_tr_b16 v[236:237], v244 offset:0\n\t" \
    "ds_read_b64_tr_b16 v[238:239], v244 offset:1536\n\t" \
    "ds_read_b64_tr_b16 v[164:165], v244 offset:32\n\t" \
    "ds_read_b64_tr_b16 v[166:167], v244 offset:1568\n\t" \
    "ds_read_b64_tr_b16 v[172:173], v244 offset:64\n\t" \
    "ds_read_b64_tr_b16 v[174:175], v244 offset:1600\n\t" \
    "v_add_u32 v244, 0x18000, v243\n\t" \
    "s_waitcnt vmcnt(3) lgkmcnt(0)\n\t" \
    "s_barrier \n\t" \
    "ds_read_b64_tr_b16 v[176:177], v244 offset:0\n\t" \
    "ds_read_b64_tr_b16 v[178:179], v244 offset:1536\n\t" \
    "ds_read_b64_tr_b16 v[184:185], v244 offset:32\n\t" \
    "ds_read_b64_tr_b16 v[186:187], v244 offset:1568\n\t" \
    "ds_read_b64_tr_b16 v[192:193], v244 offset:64\n\t" \
    "ds_read_b64_tr_b16 v[194:195], v244 offset:1600\n\t" \
    "v_add_u32 v244, 0x1e000, v243\n\t" \
    "s_waitcnt vmcnt(0) lgkmcnt(0)\n\t" \
    "s_barrier \n\t" \
    "ds_read_b64_tr_b16 v[180:181], v244 offset:0\n\t" \
    "ds_read_b64_tr_b16 v[182:183], v244 offset:1536\n\t" \
    "ds_read_b64_tr_b16 v[188:189], v244 offset:32\n\t" \
    "ds_read_b64_tr_b16 v[190:191], v244 offset:1568\n\t" \
    "ds_read_b64_tr_b16 v[196:197], v244 offset:64\n\t" \
    "ds_read_b64_tr_b16 v[198:199], v244 offset:1600\n\t" \
    "s_waitcnt lgkmcnt(0)\n\t" \
    "s_barrier \n\t" \
    "ds_write_b128 v254, v[200:203] offset:0\n\t" \
    "ds_write_b128 v254, v[204:207] offset:1024\n\t" \
    "ds_write_b128 v254, v[208:211] offset:2048\n\t" \
    "ds_write_b128 v254, v[212:215] offset:3072\n\t" \
    "ds_write_b128 v254, v[216:219] offset:4096\n\t" \
    "ds_write_b128 v254, v[220:223] offset:5120\n\t" \
    "ds_write_b128 v254, v[224:227] offset:6144\n\t" \
    "ds_write_b128 v254, v[228:231] offset:7168\n\t" \
    "ds_write_b128 v254, v[232:235] offset:8192\n\t" \
    "ds_write_b128 v254, v[236:239] offset:9216\n\t" \
    "s_cmp_lg_u32 s43, 0\n\t" \
    "s_cbranch_scc1 .Ls42_%=\n\t" \
    "s_mov_b32 s44, 0\n\t" \
    ".Ls43_%=:\n\t" \
    "v_cmp_ge_u32 vcc, s44, v252\n\t" \
    "s_cbranch_vccz .Ls44_%=\n\t" \
    "s_cmp_eq_u32 s41, 0\n\t" \
    "s_cbranch_scc1 .Ls44_%=\n\t" \
    "s_sub_u32 s41, s41, 1\n\t" \
    "s_sleep 8\n\t" \
    "v_and_b32 v252, 0xf0, v253\n\t" \
    "v_lshlrev_b32 v252, 3, v252\n\t" \
    "buffer_load_dword v252, v252, s[60:63], %[fl] offen sc1\n\t" \
    "s_waitcnt vmcnt(0)\n\t" \
    "s_branch .Ls43_%=\n\t" \
    ".Ls44_%=:\n\t" \
    "s_mov_b32 m0, %[m0]\n\t" \
    "s_add_u32 s45, s42, %[x1]\n\t" \
    "buffer_load_dwordx4 v253, s[60:63], s42 offen offset:0 sc1 lds\n\t" \
    "buffer_load_dwordx4 v253, s[60:63], s42 offen offset:1024 sc1 lds\n\t" \
    "buffer_load_dwordx4 v253, s[60:63], s42 offen offset:2048 sc1 lds\n\t" \
    "buffer_load_dwordx4 v253, s[60:63], s42 offen offset:3072 sc1 lds\n\t" \
    "s_mov_b32 m0, %[m1]\n\t" \
    "s_nop 0\n\t" \
    "buffer_load_dwordx4 v253, s[60:63], s45 offen offset:0 sc1 lds\n\t" \
    "buffer_load_dwordx4 v253, s[60:63], s45 offen offset:1024 sc1 lds\n\t" \
    ".Ls42_%=:\n\t" \
    "s_waitcnt vmcnt(0) lgkmcnt(0)\n\t" \
    "s_barrier \n\t" \
    "ds_read_b128 v[224:227], v253 offset:0\n\t" \
    "ds_read_b128 v[244:247], v254 offset:0\n\t" \
    "ds_read_b128 v[228:231], v253 offset:1024\n\t" \
    "ds_read_b128 v[232:235], v253 offset:20480\n\t" \
    "ds_read_b128 v[236:239], v253 offset:21504\n\t" \
    "ds_read_b128 v[248:251], v254 offset:1024\n\t" \
    ".Ls45_%=:\n\t" \
    "s_cmp_lg_u32 %[wave], 4\n\t" \
    "s_cbranch_scc1 .Ls46_%=\n\t" \
    "s_sleep 9\n\t" \
    ".Ls46_%=:\n\t" \
    "s_waitcnt lgkmcnt(5)\n\t" \
    "v_mfma_f32_16x16x32_bf16 v[204:207], v[0:3], v[224:227], 0\n\t" \
    "s_add_u32 m0, %[m0], 0x3000\n\t" \
    "s_waitcnt lgkmcnt(3)\n\t" \
    "v_mfma_f32_16x16x32_bf16 v[204:207], v[4:7], v[228:231], v[204:207]\n\t" \
    "ds_read_b128 v[240:243], v253 offset:2048\n\t" \
    "s_add_u32 s45, s42, 0xc000\n\t" \
    "v_mfma_f32_16x16x32_bf16 v[208:211], v[8:11], v[224:227], 0\n\t" \
    "buffer_load_dwordx4 v253, s[60:63], s45 offen offset:0 sc1 lds\n\t" \
    "v_mfma_f32_16x16x32_bf16 v[208:211], v[12:15], v[228:231], v[208:211]\n\t" \
    "v_mfma_f32_16x16x32_bf16 v[200:203], v[244:247], v[224:227], 0\n\t" \
    "s_waitcnt lgkmcnt(1)\n\t" \
    "v_mfma_f32_16x16x32_bf16 v[200:203], v[248:251], v[228:231], v[200:203]\n\t" \
    "buffer_load_dwordx4 v253, s[60:63], s45 offen offset:1024 sc1 lds\n\t" \
    "v_mfma_f32_16x16x32_bf16 v[216:219], v[0:3], v[232:235], 0\n\t" \
    "ds_read_b128 v[224:227], v253 offset:3072\n\t" \
    "v_mfma_f32_16x16x32_bf16 v[216:219], v[4:7], v[236:239], v[216:219]\n\t" \
    "ds_read_b128 v[228:231], v253 offset:22528\n\t" \
    "v_mfma_f32_16x16x32_bf16 v[220:223], v[8:11], v[232:235], 0\n\t" \
    "buffer_load_dwordx4 v253, s[60:63], s45 offen offset:2048 sc1 lds\n\t" \
    "v_mfma_f32_16x16x32_bf16 v[220:223], v[12:15], v[236:239], v[220:223]\n\t" \
    "v_mfma_f32_16x16x32_bf16 v[212:215], v[244:247], v[232:235], 0\n\t" \
    "ds_read_b128 v[244:247], v254 offset:2048\n\t" \
    "v_mfma_f32_16x16x32_bf16 v[212:215], v[248:251], v[236:239], v[212:215]\n\t" \
    "ds_read_b128 v[248:251], v254 offset:3072\n\t" \
    "buffer_load_dwordx4 v253, s[60:63], s45 offen offset:3072 sc1 lds\n\t" \
    "s_waitcnt lgkmcnt(4)\n\t" \
    "v_mfma_f32_16x16x32_bf16 v[204:207], v[24:27], v[240:243], v[204:207]\n\t" \
    "ds_read_b128 v[232:235], v253 offset:23552\n\t" \
    "s_waitcnt lgkmcnt(4)\n\t" \
    "v_mfma_f32_16x16x32_bf16 v[204:207], v[28:31], v[224:227], v[204:207]\n\t" \
    "ds_read_b128 v[236:239], v253 offset:4096\n\t" \
    "v_mfma_f32_16x16x32_bf16 v[208:211], v[32:35], v[240:243], v[208:211]\n\t" \
    "v_mfma_f32_16x16x32_bf16 v[208:211], v[36:39], v[224:227], v[208:211]\n\t" \
    "v_mfma_f32_16x16x32_bf16 v[200:203], v[16:19], v[240:243], v[200:203]\n\t" \
    "v_mfma_f32_16x16x32_bf16 v[200:203], v[20:23], v[224:227], v[200:203]\n\t" \
    "s_waitcnt lgkmcnt(4)\n\t" \
    "v_mfma_f32_16x16x32_bf16 v[216:219], v[24:27], v[228:231], v[216:219]\n\t" \
    "ds_read_b128 v[240:243], v253 offset:5120\n\t" \
    "s_waitcnt lgkmcnt(2)\n\t" \
    "v_mfma_f32_16x16x32_bf16 v[216:219], v[28:31], v[232:235], v[216:219]\n\t" \
    "ds_read_b128 v[224:227], v253 offset:24576\n\t" \
    "v_mfma_f32_16x16x32_bf16 v[220:223], v[32:35], v[228:231], v[220:223]\n\t" \
    "v_mfma_f32_16x16x32_bf16 v[220:223], v[36:39], v[232:235], v[220:223]\n\t" \
    "v_mfma_f32_16x16x32_bf16 v[212:215], v[16:19], v[228:231], v[212:215]\n\t" \
    "v_mfma_f32_16x16x32_bf16 v[212:215], v[20:23], v[232:235], v[212:215]\n\t" \
    "s_waitcnt lgkmcnt(2)\n\t" \
    "v_mfma_f32_16x16x32_bf16 v[204:207], v[40:43], v[236:239], v[204:207]\n\t" \
    "ds_read_b128 v[228:231], v253 offset:25600\n\t" \
    "s_waitcnt lgkmcnt(2)\n\t" \
    "v_mfma_f32_16x16x32_bf16 v[204:207], v[44:47], v[240:243], v[204:207]\n\t" \
    "ds_read_b128 v[232:235], v253 offset:6144\n\t" \
    "v_mfma_f32_16x16x32_bf16 v[208:211], v[48:51], v[236:239], v[208:211]\n\t" \
    "v_mfma_f32_16x16x32_bf16 v[208:211], v[52:55], v[240:243], v[208:211]\n\t" \
    "v_mfma_f32_16x16x32_bf16 v[200:203], v[244:247], v[236:239], v[200:203]\n\t" \
    "v_mfma_f32_16x16x32_bf16 v[200:203], v[248:251], v[240:243], v[200:203]\n\t" \
    "s_waitcnt lgkmcnt(2)\n\t" \
    "v_mfma_f32_16x16x32_bf16 v[216:219], v[40:43], v[224:227], v[216:219]\n\t" \
    "ds_read_b128 v[236:239], v253 offset:7168\n\t" \
    "s_waitcnt lgkmcnt(2)\n\t" \
    "v_mfma_f32_16x16x32_bf16 v[216:219], v[44:47], v[228:231], v[216:219]\n\t" \
    "ds_read_b128 v[240:243], v253 offset:26624\n\t" \
    "v_mfma_f32_16x16x32_bf16 v[220:223], v[48:51], v[224:227], v[220:223]\n\t" \
    "v_mfma_f32_16x16x32_bf16 v[220:223], v[52:55], v[228:231], v[220:223]\n\t" \
    "v_mfma_f32_16x16x32_bf16 v[212:215], v[244:247], v[224:227], v[212:215]\n\t" \
    "ds_read_b128 v[244:247], v254 offset:4096\n\t" \
    "v_mfma_f32_16x16x32_bf16 v[212:215], v[248:251], v[228:231], v[212:215]\n\t" \
    "ds_read_b128 v[248:251], v254 offset:5120\n\t" \
    "s_waitcnt lgkmcnt(4)\n\t" \
    "v_mfma_f32_16x16x32_bf16 v[204:207], v[64:67], v[232:235], v[204:207]\n\t" \
    "ds_read_b128 v[224:227], v253 offset:27648\n\t" \
    "s_waitcnt lgkmcnt(4)\n\t" \
    "v_mfma_f32_16x16x32_bf16 v[204:207], v[68:71], v[236:239], v[204:207]\n\t" \
    "ds_read_b128 v[228:231], v253 offset:8192\n\t" \
    "v_mfma_f32_16x16x32_bf16 v[208:211], v[72:75], v[232:235], v[208:211]\n\t" \
    "v_mfma_f32_16x16x32_bf16 v[208:211], v[76:79], v[236:239], v[208:211]\n\t" \
    "v_mfma_f32_16x16x32_bf16 v[200:203], v[56:59], v[232:235], v[200:203]\n\t" \
    "v_mfma_f32_16x16x32_bf16 v[200:203], v[60:63], v[236:239], v[200:203]\n\t" \
    "v_and_b32 v252, 0xf0, v253\n\t" \
    "v_lshlrev_b32 v252, 3, v252\n\t" \
    "buffer_load_dword v252, v252, s[60:63], %[fl] offen sc1\n\t" \
    "s_waitcnt lgkmcnt(4)\n\t" \
    "v_mfma_f32_16x16x32_bf16 v[216:219], v[64:67], v[240:243], v[216:219]\n\t" \
    "ds_read_b128 v[232:235], v253 offset:9216\n\t" \
    "s_waitcnt lgkmcnt(2)\n\t" \
    "v_mfma_f32_16x16x32_bf16 v[216:219], v[68:71], v[224:227], v[216:219]\n\t" \
    "ds_read_b128 v[236:239], v253 offset:28672\n\t" \
    "v_mfma_f32_16x16x32_bf16 v[220:223], v[72:75], v[240:243], v[220:223]\n\t" \
    "v_mfma_f32_16x16x32_bf16 v[220:223], v[76:79], v[224:227], v[220:223]\n\t" \
    "v_mfma_f32_16x16x32_bf16 v[212:215], v[56:59], v[240:243], v[212:215]\n\t" \
    "v_mfma_f32_16x16x32_bf16 v[212:215], v[60:63], v[224:227], v[212:215]\n\t" \
    "s_waitcnt lgkmcnt(2)\n\t" \
    "v_mfma_f32_16x16x32_bf16 v[204:207], v[80:83], v[228:231], v[204:207]\n\t" \
    "ds_read_b128 v[240:243], v253 offset:29696\n\t" \
    "s_waitcnt lgkmcnt(2)\n\t" \
    "v_mfma_f32_16x16x32_bf16 v[204:207], v[84:87], v[232:235], v[204:207]\n\t" \
    "ds_read_b128 v[224:227], v253 offset:10240\n\t" \
    "v_mfma_f32_16x16x32_bf16 v[208:211], v[88:91], v[228:231], v[208:211]\n\t" \
    "v_mfma_f32_16x16x32_bf16 v[208:211], v[92:95], v[232:235], v[208:211]\n\t" \
    "v_mfma_f32_16x16x32_bf16 v[200:203], v[244:247], v[228:231], v[200:203]\n\t" \
    "v_mfma_f32_16x16x32_bf16 v[200:203], v[248:251], v[232:235], v[200:203]\n\t" \
    "s_waitcnt lgkmcnt(2)\n\t" \
    "v_mfma_f32_16x16x32_bf16 v[216:219], v[80:83], v[236:239], v[216:219]\n\t" \
    "ds_read_b128 v[228:231], v253 offset:11264\n\t" \
    "s_add_u32 s44, s40, 1\n\t" \
    "s_waitcnt lgkmcnt(2)\n\t" \
    "v_mfma_f32_16x16x32_bf16 v[216:219], v[84:87], v[240:243], v[216:219]\n\t" \
    "ds_read_b128 v[232:235], v253 offset:30720\n\t" \
    "s_and_b32 s45, s44, 15\n\t" \
    "v_mfma_f32_16x16x32_bf16 v[220:223], v[88:91], v[236:239], v[220:223]\n\t" \
    "s_cmp_eq_u32 s45, 0\n\t" \
    "v_mfma_f32_16x16x32_bf16 v[220:223], v[92:95], v[240:243], v[220:223]\n\t" \
    "s_cselect_b32 s45, %[cj], %[cs]\n\t" \
    "v_mfma_f32_16x16x32_bf16 v[212:215], v[244:247], v[236:239], v[212:215]\n\t" \
    "ds_read_b128 v[244:247], v254 offset:6144\n\t" \
    "s_cmp_eq_u32 s44, 64\n\t" \
    "v_mfma_f32_16x16x32_bf16 v[212:215], v[248:251], v[240:243], v[212:215]\n\t" \
    "ds_read_b128 v[248:251], v254 offset:7168\n\t" \
    "s_cselect_b32 s45, 0, s45\n\t" \
    "ds_read_b128 v[236:239], v253 offset:31744\n\t" \
    "s_waitcnt vmcnt(0) lgkmcnt(0)\n\t" \
    "s_barrier \n\t" \
    LST7 \
    "s_add_u32 s42, s42, s45\n\t" \
    "s_add_u32 s44, s40, 1\n\t" \
    "s_min_u32 s44, s44, 63\n\t" \
    ".Ls47_%=:\n\t" \
    "v_cmp_ge_u32 vcc, s44, v252\n\t" \
    "s_cbranch_vccz .Ls48_%=\n\t" \
    "s_cmp_eq_u32 s41, 0\n\t" \
    "s_cbranch_scc1 .Ls48_%=\n\t" \
    "s_sub_u32 s41, s41, 1\n\t" \
    "s_sleep 2\n\t" \
    "v_and_b32 v252, 0xf0, v253\n\t" \
    "v_lshlrev_b32 v252, 3, v252\n\t" \
    "buffer_load_dword v252, v252, s[60:63], %[fl] offen sc1\n\t" \
    "s_waitcnt vmcnt(0)\n\t" \
    "s_branch .Ls47_%=\n\t" \
    ".Ls48_%=:\n\t" \
    "v_mfma_f32_16x16x32_bf16 v[204:207], v[104:107], v[224:227], v[204:207]\n\t" \
    "s_mov_b32 m0, %[m0]\n\t" \
    "v_mfma_f32_16x16x32_bf16 v[204:207], v[108:111], v[228:231], v[204:207]\n\t" \
    "ds_read_b128 v[240:243], v253 offset:12288\n\t" \
    "s_add_u32 s45, s42, %[x1]\n\t" \
    "v_mfma_f32_16x16x32_bf16 v[208:211], v[112:115], v[224:227], v[208:211]\n\t" \
    "buffer_load_dwordx4 v253, s[60:63], s42 offen offset:0 sc1 lds\n\t" \
    "v_mfma_f32_16x16x32_bf16 v[208:211], v[116:119], v[228:231], v[208:211]\n\t" \
    "v_mfma_f32_16x16x32_bf16 v[200:203], v[96:99], v[224:227], v[200:203]\n\t" \
    "v_mfma_f32_16x16x32_bf16 v[200:203], v[100:103], v[228:231], v[200:203]\n\t" \
    "buffer_load_dwordx4 v253, s[60:63], s42 offen offset:1024 sc1 lds\n\t" \
    "v_mfma_f32_16x16x32_bf16 v[216:219], v[104:107], v[232:235], v[216:219]\n\t" \
    "ds_read_b128 v[224:227], v253 offset:13312\n\t" \
    "v_mfma_f32_16x16x32_bf16 v[216:219], v[108:111], v[236:239], v[216:219]\n\t" \
    "ds_read_b128 v[228:231], v253 offset:32768\n\t" \
    "v_mfma_f32_16x16x32_bf16 v[220:223], v[112:115], v[232:235], v[220:223]\n\t" \
    "buffer_load_dwordx4 v253, s[60:63], s42 offen offset:2048 sc1 lds\n\t" \
    "v_mfma_f32_16x16x32_bf16 v[220:223], v[116:119], v[236:239], v[220:223]\n\t" \
    "v_mfma_f32_16x16x32_bf16 v[212:215], v[96:99], v[232:235], v[212:215]\n\t" \
    "v_mfma_f32_16x16x32_bf16 v[212:215], v[100:103], v[236:239], v[212:215]\n\t" \
    "buffer_load_dwordx4 v253, s[60:63], s42 offen offset:3072 sc1 lds\n\t" \
    "s_waitcnt lgkmcnt(2)\n\t" \
    "v_mfma_f32_16x16x32_bf16 v[204:207], v[120:123], v[240:243], v[204:207]\n\t" \
    "ds_read_b128 v[232:235], v253 offset:33792\n\t" \
    "s_waitcnt lgkmcnt(2)\n\t" \
    "v_mfma_f32_16x16x32_bf16 v[204:207], v[124:127], v[224:227], v[204:207]\n\t" \
    "ds_read_b128 v[236:239], v253 offset:16384\n\t" \
    "v_mfma_f32_16x16x32_bf16 v[208:211], v[128:131], v[240:243], v[208:211]\n\t" \
    "s_mov_b32 m0, %[m1]\n\t" \
    "v_mfma_f32_16x16x32_bf16 v[208:211], v[132:135], v[224:227], v[208:211]\n\t" \
    "s_nop 0\n\t" \
    "v_mfma_f32_16x16x32_bf16 v[200:203], v[244:247], v[240:243], v[200:203]\n\t" \
    "buffer_load_dwordx4 v253, s[60:63], s45 offen offset:0 sc1 lds\n\t" \
    "v_mfma_f32_16x16x32_bf16 v[200:203], v[248:251], v[224:227], v[200:203]\n\t" \
    "s_waitcnt lgkmcnt(2)\n\t" \
    "v_mfma_f32_16x16x32_bf16 v[216:219], v[120:123], v[228:231], v[216:219]\n\t" \
    "ds_read_b128 v[240:243], v253 offset:17408\n\t" \
    "s_waitcnt lgkmcnt(2)\n\t" \
    "v_mfma_f32_16x16x32_bf16 v[216:219], v[124:127], v[232:235], v[216:219]\n\t" \
    "ds_read_b128 v[224:227], v253 offset:36864\n\t" \
    "buffer_load_dwordx4 v253, s[60:63], s45 offen offset:1024 sc1 lds\n\t" \
    "v_mfma_f32_16x16x32_bf16 v[220:223], v[128:131], v[228:231], v[220:223]\n\t" \
    "v_mfma_f32_16x16x32_bf16 v[220:223], v[132:135], v[232:235], v[220:223]\n\t" \
    "v_mfma_f32_16x16x32_bf16 v[212:215], v[244:247], v[228:231], v[212:215]\n\t" \
    "ds_read_b128 v[244:247], v254 offset:8192\n\t" \
    "v_mfma_f32_16x16x32_bf16 v[212:215], v[248:251], v[232:235], v[212:215]\n\t" \
    "ds_read_b128 v[248:251], v254 offset:9216\n\t" \
    "s_waitcnt lgkmcnt(4)\n\t" \
    "v_mfma_f32_16x16x32_bf16 v[204:207], v[160:163], v[236:239], v[204:207]\n\t" \
    "ds_read_b128 v[228:231], v253 offset:37888\n\t" \
    "s_waitcnt lgkmcnt(4)\n\t" \
    "v_mfma_f32_16x16x32_bf16 v[204:207], v[164:167], v[240:243], v[204:207]\n\t" \
    "ds_read_b128 v[232:235], v253 offset:14336\n\t" \
    "v_mfma_f32_16x16x32_bf16 v[208:211], v[168:171], v[236:239], v[208:211]\n\t" \
    "v_mfma_f32_16x16x32_bf16 v[208:211], v[172:175], v[240:243], v[208:211]\n\t" \
    "s_waitcnt lgkmcnt(3)\n\t" \
    "v_mfma_f32_16x16x32_bf16 v[200:203], v[244:247], v[236:239], v[200:203]\n\t" \
    "s_waitcnt lgkmcnt(2)\n\t" \
    "v_mfma_f32_16x16x32_bf16 v[200:203], v[248:251], v[240:243], v[200:203]\n\t" \
    "v_mfma_f32_16x16x32_bf16 v[216:219], v[160:163], v[224:227], v[216:219]\n\t" \
    "ds_read_b128 v[236:239], v253 offset:15360\n\t" \
    "s_waitcnt lgkmcnt(2)\n\t" \
    "v_mfma_f32_16x16x32_bf16 v[216:219], v[164:167], v[228:231], v[216:219]\n\t" \
    "ds_read_b128 v[240:243], v253 offset:18432\n\t" \
    "v_mfma_f32_16x16x32_bf16 v[220:223], v[168:171], v[224:227], v[220:223]\n\t" \
    "v_mfma_f32_16x16x32_bf16 v[220:223], v[172:175], v[228:231], v[220:223]\n\t" \
    "v_mfma_f32_16x16x32_bf16 v[212:215], v[244:247], v[224:227], v[212:215]\n\t" \
    "v_mfma_f32_16x16x32_bf16 v[212:215], v[248:251], v[228:231], v[212:215]\n\t" \
    "s_waitcnt lgkmcnt(2)\n\t" \
    "v_mfma_f32_16x16x32_bf16 v[204:207], v[144:147], v[232:235], v[204:207]\n\t" \
    "ds_read_b128 v[224:227], v253 offset:19456\n\t" \
    "v_mov_b64 v[244:245], 0\n\t" \
    "s_waitcnt lgkmcnt(2)\n\t" \
    "v_mfma_f32_16x16x32_bf16 v[204:207], v[148:151], v[236:239], v[204:207]\n\t" \
    "ds_read_b128 v[228:231], v253 offset:34816\n\t" \
    "v_mov_b64 v[246:247], 0\n\t" \
    "v_mfma_f32_16x16x32_bf16 v[208:211], v[152:155], v[232:235], v[208:211]\n\t" \
    "v_mov_b64 v[248:249], 0\n\t" \
    "v_mfma_f32_16x16x32_bf16 v[208:211], v[156:159], v[236:239], v[208:211]\n\t" \
    "v_mov_b64 v[250:251], 0\n\t" \
    "v_mfma_f32_16x16x32_bf16 v[200:203], v[136:139], v[232:235], v[200:203]\n\t" \
    "v_add_u32 v252, %[qs], v253\n\t" \
    "v_mfma_f32_16x16x32_bf16 v[200:203], v[140:143], v[236:239], v[200:203]\n\t" \
    "s_waitcnt lgkmcnt(2)\n\t" \
    "v_mfma_f32_16x16x32_bf16 v[204:207], v[184:187], v[240:243], v[204:207]\n\t" \
    "ds_read_b128 v[232:235], v253 offset:35840\n\t" \
    "s_waitcnt lgkmcnt(2)\n\t" \
    "v_mfma_f32_16x16x32_bf16 v[204:207], v[188:191], v[224:227], v[204:207]\n\t" \
    "ds_read_b128 v[236:239], v253 offset:38912\n\t" \
    "v_mfma_f32_16x16x32_bf16 v[208:211], v[192:195], v[240:243], v[208:211]\n\t" \
    "v_mfma_f32_16x16x32_bf16 v[208:211], v[196:199], v[224:227], v[208:211]\n\t" \
    "v_mfma_f32_16x16x32_bf16 v[200:203], v[176:179], v[240:243], v[200:203]\n\t" \
    "v_mfma_f32_16x16x32_bf16 v[200:203], v[180:183], v[224:227], v[200:203]\n\t" \
    "ds_read_b128 v[240:243], v253 offset:39936\n\t" \
    "s_waitcnt lgkmcnt(3)\n\t" \
    "v_mfma_f32_16x16x32_bf16 v[216:219], v[144:147], v[228:231], v[216:219]\n\t" \
    "v_add_u32 v224, %[q2], v253\n\t" \
    "s_waitcnt lgkmcnt(2)\n\t" \
    "v_mfma_f32_16x16x32_bf16 v[216:219], v[148:151], v[232:235], v[216:219]\n\t" \
    "v_cvt_pk_bf16_f32 v204, v204, v208\n\t" \
    "v_cvt_pk_bf16_f32 v205, v205, v209\n\t" \
    "s_waitcnt lgkmcnt(1)\n\t" \
    "v_mfma_f32_16x16x32_bf16 v[216:219], v[184:187], v[236:239], v[216:219]\n\t" \
    "v_cvt_pk_bf16_f32 v206, v206, v210\n\t" \
    "v_cvt_pk_bf16_f32 v207, v207, v211\n\t" \
    "s_waitcnt lgkmcnt(0)\n\t" \
    "v_mfma_f32_16x16x32_bf16 v[216:219], v[188:191], v[240:243], v[216:219]\n\t" \
    "ds_write_b128 v224, v[204:207] offset:0\n\t" \
    "v_cvt_pk_bf16_f32 v200, v200, v201\n\t" \
    "v_mfma_f32_16x16x32_bf16 v[220:223], v[152:155], v[228:231], v[220:223]\n\t" \
    "v_cvt_pk_bf16_f32 v201, v202, v203\n\t" \
    "ds_write_b64 v252, v[200:201] offset:0\n\t" \
    "v_mfma_f32_16x16x32_bf16 v[220:223], v[156:159], v[232:235], v[220:223]\n\t" \
    "ds_read_b128 v[200:203], v254 offset:0\n\t" \
    "ds_read_b128 v[204:207], v254 offset:1024\n\t" \
    "v_mfma_f32_16x16x32_bf16 v[220:223], v[192:195], v[236:239], v[220:223]\n\t" \
    "v_mov_b64 v[208:209], 0\n\t" \
    "v_mov_b64 v[210:211], 0\n\t" \
    "v_mfma_f32_16x16x32_bf16 v[220:223], v[196:199], v[240:243], v[220:223]\n\t" \
    "v_mfma_f32_16x16x32_bf16 v[212:215], v[136:139], v[228:231], v[212:215]\n\t" \
    "v_mfma_f32_16x16x32_bf16 v[212:215], v[140:143], v[232:235], v[212:215]\n\t" \
    "v_mfma_f32_16x16x32_bf16 v[212:215], v[176:179], v[236:239], v[212:215]\n\t" \
    "v_cvt_pk_bf16_f32 v216, v216, v220\n\t" \
    "v_cvt_pk_bf16_f32 v217, v217, v221\n\t" \
    "v_mfma_f32_16x16x32_bf16 v[212:215], v[180:183], v[240:243], v[212:215]\n\t" \
    "v_cvt_pk_bf16_f32 v218, v218, v222\n\t" \
    "v_cvt_pk_bf16_f32 v219, v219, v223\n\t" \
    "ds_write_b128 v224, v[216:219] offset:6144\n\t" \
    "ds_read_b128 v[216:219], v254 offset:2048\n\t" \
    "ds_read_b128 v[220:223], v254 offset:3072\n\t" \
    "s_nop 2\n\t" \
    "v_cvt_pk_bf16_f32 v212, v212, v213\n\t" \
    "v_cvt_pk_bf16_f32 v213, v214, v215\n\t" \
    "ds_write_b64 v252, v[212:213] offset:6144\n\t" \
    LST6 \
    "s_waitcnt vmcnt(0) lgkmcnt(0)\n\t" \
    "s_barrier \n\t" \
    LST3 \
    "ds_read_b128 v[224:227], v253 offset:40960\n\t" \
    "ds_read_b128 v[228:231], v253 offset:41984\n\t" \
    "ds_read_b128 v[232:235], v253 offset:43008\n\t" \
    "ds_read_b128 v[236:239], v253 offset:44032\n\t" \
    "ds_read_b128 v[240:243], v253 offset:45056\n\t" \
    LFLUSH212 \
    "v_bfe_i32 v212, v253, 5, 1\n\t" \
    "v_and_b32 v212, 0xaaaa, v212\n\t" \
    "v_or_b32 v212, 0x4444, v212\n\t" \
    "v_and_b32 v213, 0xffff, v255\n\t" \
    "v_lshrrev_b32 v214, 16, v255\n\t" \
    "s_waitcnt lgkmcnt(4)\n\t" \
    "v_smfmac_f32_16x16x64_bf16 v[208:211], v[224:227], v[200:207], v212\n\t" \
    "ds_read_b128 v[200:203], v254 offset:4096\n\t" \
    "ds_read_b128 v[204:207], v254 offset:5120\n\t" \
    "v_smfmac_f32_16x16x64_bf16 v[244:247], v[224:227], v[0:7], v212\n\t" \
    "v_smfmac_f32_16x16x64_bf16 v[248:251], v[224:227], v[8:15], v212\n\t" \
    "ds_read_b128 v[224:227], v253 offset:46080\n\t" \
    "s_waitcnt lgkmcnt(6)\n\t" \
    "v_smfmac_f32_16x16x64_bf16 v[208:211], v[228:231], v[16:23], v212\n\t" \
    "v_smfmac_f32_16x16x64_bf16 v[244:247], v[228:231], v[24:31], v212\n\t" \
    "v_smfmac_f32_16x16x64_bf16 v[248:251], v[228:231], v[32:39], v212\n\t" \
    "ds_read_b128 v[228:231], v253 offset:47104\n\t" \
    "s_waitcnt lgkmcnt(6)\n\t" \
    "v_smfmac_f32_16x16x64_bf16 v[208:211], v[232:235], v[216:223], v212\n\t" \
    "ds_read_b128 v[216:219], v254 offset:6144\n\t" \
    "ds_read_b128 v[220:223], v254 offset:7168\n\t" \
    "v_smfmac_f32_16x16x64_bf16 v[244:247], v[232:235], v[40:47], v212\n\t" \
    "v_smfmac_f32_16x16x64_bf16 v[248:251], v[232:235], v[48:55], v212\n\t" \
    "ds_read_b128 v[232:235], v253 offset:48128\n\t" \
    "s_waitcnt lgkmcnt(8)\n\t" \
    "v_smfmac_f32_16x16x64_bf16 v[208:211], v[236:239], v[56:63], v212\n\t" \
    "v_smfmac_f32_16x16x64_bf16 v[244:247], v[236:239], v[64:71], v212\n\t" \
    "v_smfmac_f32_16x16x64_bf16 v[248:251], v[236:239], v[72:79], v212\n\t" \
    "ds_read_b128 v[236:239], v253 offset:49152\n\t" \
    "s_waitcnt lgkmcnt(6)\n\t" \
    "v_smfmac_f32_16x16x64_bf16 v[208:211], v[240:243], v[200:207], v212\n\t" \
    "ds_read_b128 v[200:203], v254 offset:8192\n\t" \
    "ds_read_b128 v[204:207], v254 offset:9216\n\t" \
    "v_smfmac_f32_16x16x64_bf16 v[244:247], v[240:243], v[80:87], v212\n\t" \
    "v_smfmac_f32_16x16x64_bf16 v[248:251], v[240:243], v[88:95], v212\n\t" \
    "ds_read_b128 v[240:243], v253 offset:50176\n\t" \
    "s_waitcnt lgkmcnt(8)\n\t" \
    "v_smfmac_f32_16x16x64_bf16 v[208:211], v[224:227], v[96:103], v212\n\t" \
    "v_smfmac_f32_16x16x64_bf16 v[244:247], v[224:227], v[104:111], v212\n\t" \
    "v_smfmac_f32_16x16x64_bf16 v[248:251], v[224:227], v[112:119], v212\n\t" \
    "s_waitcnt lgkmcnt(5)\n\t" \
    "v_smfmac_f32_16x16x64_bf16 v[208:211], v[228:231], v[216:223], v212\n\t" \
    "s_waitcnt lgkmcnt(4)\n\t" \
    "v_smfmac_f32_16x16x64_bf16 v[208:211], v[232:235], v[136:143], v212\n\t" \
    "s_waitcnt lgkmcnt(1)\n\t" \
    "v_smfmac_f32_16x16x64_bf16 v[208:211], v[236:239], v[200:207], v212\n\t" \
    "s_waitcnt lgkmcnt(0)\n\t" \
    "v_smfmac_f32_16x16x64_bf16 v[208:211], v[240:243], v[176:183], v212\n\t" \
    "v_smfmac_f32_16x16x64_bf16 v[244:247], v[228:231], v[120:127], v212\n\t" \
    "v_smfmac_f32_16x16x64_bf16 v[244:247], v[232:235], v[144:151], v212\n\t" \
    "v_smfmac_f32_16x16x64_bf16 v[244:247], v[236:239], v[160:167], v212\n\t" \
    "v_add_f32 v208, v208, v210\n\t" \
    "v_add_f32 v209, v209, v211\n\t" \
    "v_cvt_pk_bf16_f32 v208, v208, v209\n\t" \
    "v_smfmac_f32_16x16x64_bf16 v[244:247], v[240:243], v[184:191], v212\n\t" \
    "ds_write_b16 v213, v208 offset:0\n\t" \
    "ds_write_b16_d16_hi v213, v208 offset:16\n\t" \
    "v_smfmac_f32_16x16x64_bf16 v[248:251], v[228:231], v[128:135], v212\n\t" \
    "v_smfmac_f32_16x16x64_bf16 v[248:251], v[232:235], v[152:159], v212\n\t" \
    "v_smfmac_f32_16x16x64_bf16 v[248:251], v[236:239], v[168:175], v212\n\t" \
    "v_add_f32 v244, v244, v246\n\t" \
    "v_add_f32 v245, v245, v247\n\t" \
    "v_smfmac_f32_16x16x64_bf16 v[248:251], v[240:243], v[192:199], v212\n\t" \
    "s_nop 7\n\t" \
    "v_add_f32 v248, v248, v250\n\t" \
    "v_add_f32 v249, v249, v251\n\t" \
    "v_cvt_pk_bf16_f32 v244, v244, v248\n\t" \
    "v_cvt_pk_bf16_f32 v245, v245, v249\n\t" \
    "ds_write2_b32 v214, v244, v245 offset1:4\n\t" \
    "s_waitcnt lgkmcnt(0)\n\t" \
    "s_barrier \n\t" \
    LST4 \
    "ds_read_b128 v[224:227], v253 offset:0\n\t" \
    "ds_read_b128 v[244:247], v254 offset:0\n\t" \
    "ds_read_b128 v[228:231], v253 offset:1024\n\t" \
    "ds_read_b128 v[232:235], v253 offset:20480\n\t" \
    "ds_read_b128 v[236:239], v253 offset:21504\n\t" \
    "ds_read_b128 v[248:251], v254 offset:1024\n\t" \
    "s_add_u32 s40, s40, 1\n\t" \
    "s_cmp_lg_u32 s40, 64\n\t" \
    "s_cbranch_scc1 .Ls45_%=\n\t" \
    "s_branch .Lend%=\n\t" \
    ".Ls3_%=:\n\t" \
    "s_add_u32 m0, %[wm], 0x18000\n\t" \
    "s_mov_b32 s44, 0x0\n\t" \
    "buffer_load_dwordx4 v240, %[wr], s44 offen offset:0 nt lds\n\t" \
    "buffer_load_dwordx4 v241, %[wr], s44 offen offset:1024 nt lds\n\t" \
    "buffer_load_dwordx4 v242, %[wr], s44 offen offset:2048 nt lds\n\t" \
    "s_add_u32 m0, %[wm], 0x1e000\n\t" \
    "s_mov_b32 s44, 0x60000\n\t" \
    "buffer_load_dwordx4 v240, %[wr], s44 offen offset:0 nt lds\n\t" \
    "buffer_load_dwordx4 v241, %[wr], s44 offen offset:1024 nt lds\n\t" \
    "buffer_load_dwordx4 v242, %[wr], s44 offen offset:2048 nt lds\n\t" \
    "s_add_u32 m0, %[wm], 0x0\n\t" \
    "s_mov_b32 s44, 0xc0000\n\t" \
    "buffer_load_dwordx4 v240, %[wr], s44 offen offset:0 nt lds\n\t" \
    "buffer_load_dwordx4 v241, %[wr], s44 offen offset:1024 nt lds\n\t" \
    "buffer_load_dwordx4 v242, %[wr], s44 offen offset:2048 nt lds\n\t" \
    "s_add_u32 m0, %[wm], 0x6000\n\t" \
    "s_mov_b32 s44, 0x120000\n\t" \
    "buffer_load_dwordx4 v240, %[wr], s44 offen offset:0 nt lds\n\t" \
    "buffer_load_dwordx4 v241, %[wr], s44 offen offset:1024 nt lds\n\t" \
    "buffer_load_dwordx4 v242, %[wr], s44 offen offset:2048 nt lds\n\t" \
    "s_add_u32 m0, %[wm], 0xc000\n\t" \
    "s_mov_b32 s44, 0x180000\n\t" \
    "buffer_load_dwordx4 v240, %[wr], s44 offen offset:0 nt lds\n\t" \
    "buffer_load_dwordx4 v241, %[wr], s44 offen offset:1024 nt lds\n\t" \
    "buffer_load_dwordx4 v242, %[wr], s44 offen offset:2048 nt lds\n\t" \
    "s_waitcnt lgkmcnt(0)\n\t" \
    "s_add_u32 s43, s48, s49\n\t" \
    "s_add_u32 s43, s43, s50\n\t" \
    "s_and_b32 s43, s43, 0x2000\n\t" \
    "s_xor_b32 s43, s43, %[lx]\n\t" \
    "s_add_u32 s60, s60, s43\n\t" \
    "s_addc_u32 s61, s61, 0\n\t" \
    "v_add_u32 v244, 0x18000, v243\n\t" \
    "s_waitcnt vmcnt(12) lgkmcnt(0)\n\t" \
    "s_barrier \n\t" \
    "s_add_u32 m0, %[wm], 0x12000\n\t" \
    "s_mov_b32 s44, 0x1e0000\n\t" \
    "buffer_load_dwordx4 v240, %[wr], s44 offen offset:0 nt lds\n\t" \
    "buffer_load_dwordx4 v241, %[wr], s44 offen offset:1024 nt lds\n\t" \
    "buffer_load_dwordx4 v242, %[wr], s44 offen offset:2048 nt lds\n\t" \
    "ds_read_b64_tr_b16 v[208:209], v244 offset:0\n\t" \
    "ds_read_b64_tr_b16 v[210:211], v244 offset:1536\n\t" \
    "ds_read_b64_tr_b16 v[40:41], v244 offset:32\n\t" \
    "ds_read_b64_tr_b16 v[42:43], v244 offset:1568\n\t" \
    "ds_read_b64_tr_b16 v[48:49], v244 offset:64\n\t" \
    "ds_read_b64_tr_b16 v[50:51], v244 offset:1600\n\t" \
    "v_add_u32 v244, 0x1e000, v243\n\t" \
    "s_waitcnt vmcnt(12) lgkmcnt(0)\n\t" \
    "s_barrier \n\t" \
    "s_add_u32 m0, %[wm], 0x18000\n\t" \
    "s_mov_b32 s44, 0x240000\n\t" \
    "buffer_load_dwordx4 v240, %[wr], s44 offen offset:0 nt lds\n\t" \
    "buffer_load_dwordx4 v241, %[wr], s44 offen offset:1024 nt lds\n\t" \
    "buffer_load_dwordx4 v242, %[wr], s44 offen offset:2048 nt lds\n\t" \
    "ds_read_b64_tr_b16 v[212:213], v244 offset:0\n\t" \
    "ds_read_b64_tr_b16 v[214:215], v244 offset:1536\n\t" \
    "ds_read_b64_tr_b16 v[44:45], v244 offset:32\n\t" \
    "ds_read_b64_tr_b16 v[46:47], v244 offset:1568\n\t" \
    "ds_read_b64_tr_b16 v[52:53], v244 offset:64\n\t" \
    "ds_read_b64_tr_b16 v[54:55], v244 offset:1600\n\t" \
    "v_add_u32 v244, 0x0, v243\n\t" \
    "s_waitcnt vmcnt(12) lgkmcnt(0)\n\t" \
    "s_barrier \n\t" \
    "s_add_u32 m0, %[wm], 0x1e000\n\t" \
    "s_mov_b32 s44, 0x2a0000\n\t" \
    "buffer_load_dwordx4 v240, %[wr], s44 offen offset:0 nt lds\n\t" \
    "buffer_load_dwordx4 v241, %[wr], s44 offen offset:1024 nt lds\n\t" \
    "buffer_load_dwordx4 v242, %[wr], s44 offen offset:2048 nt lds\n\t" \
    "ds_read_b64_tr_b16 v[56:57], v244 offset:0\n\t" \
    "ds_read_b64_tr_b16 v[58:59], v244 offset:1536\n\t" \
    "ds_read_b64_tr_b16 v[64:65], v244 offset:32\n\t" \
    "ds_read_b64_tr_b16 v[66:67], v244 offset:1568\n\t" \
    "ds_read_b64_tr_b16 v[72:73], v244 offset:64\n\t" \
    "ds_read_b64_tr_b16 v[74:75], v244 offset:1600\n\t" \
    "v_add_u32 v244, 0x6000, v243\n\t" \
    "s_waitcnt vmcnt(12) lgkmcnt(0)\n\t" \
    "s_barrier \n\t" \
    "s_add_u32 m0, %[wm], 0x0\n\t" \
    "s_mov_b32 s44, 0x300000\n\t" \
    "buffer_load_dwordx4 v240, %[wr], s44 offen offset:0 nt lds\n\t" \
    "buffer_load_dwordx4 v241, %[wr], s44 offen offset:1024 nt lds\n\t" \
    "buffer_load_dwordx4 v242, %[wr], s44 offen offset:2048 nt lds\n\t" \
    "ds_read_b64_tr_b16 v[60:61], v244 offset:0\n\t" \
    "ds_read_b64_tr_b16 v[62:63], v244 offset:1536\n\t" \
    "ds_read_b64_tr_b16 v[68:69], v244 offset:32\n\t" \
    "ds_read_b64_tr_b16 v[70:71], v244 offset:1568\n\t" \
    "ds_read_b64_tr_b16 v[76:77], v244 offset:64\n\t" \
    "ds_read_b64_tr_b16 v[78:79], v244 offset:1600\n\t" \
    "v_add_u32 v244, 0xc000, v243\n\t" \
    "s_waitcnt vmcnt(12) lgkmcnt(0)\n\t" \
    "s_barrier \n\t" \
    "s_add_u32 m0, %[wm], 0x6000\n\t" \
    "s_mov_b32 s44, 0x360000\n\t" \
    "buffer_load_dwordx4 v240, %[wr], s44 offen offset:0 nt lds\n\t" \
    "buffer_load_dwordx4 v241, %[wr], s44 offen offset:1024 nt lds\n\t" \
    "buffer_load_dwordx4 v242, %[wr], s44 offen offset:2048 nt lds\n\t" \
    "ds_read_b64_tr_b16 v[216:217], v244 offset:0\n\t" \
    "ds_read_b64_tr_b16 v[218:219], v244 offset:1536\n\t" \
    "ds_read_b64_tr_b16 v[80:81], v244 offset:32\n\t" \
    "ds_read_b64_tr_b16 v[82:83], v244 offset:1568\n\t" \
    "ds_read_b64_tr_b16 v[88:89], v244 offset:64\n\t" \
    "ds_read_b64_tr_b16 v[90:91], v244 offset:1600\n\t" \
    "v_add_u32 v244, 0x12000, v243\n\t" \
    "s_waitcnt vmcnt(12) lgkmcnt(0)\n\t" \
    "s_barrier \n\t" \
    "s_add_u32 m0, %[wm], 0xc000\n\t" \
    "s_mov_b32 s44, 0x3c0000\n\t" \
    "buffer_load_dwordx4 v240, %[wr], s44 offen offset:0 nt lds\n\t" \
    "buffer_load_dwordx4 v241, %[wr], s44 offen offset:1024 nt lds\n\t" \
    "buffer_load_dwordx4 v242, %[wr], s44 offen offset:2048 nt lds\n\t" \
    "ds_read_b64_tr_b16 v[220:221], v244 offset:0\n\t" \
    "ds_read_b64_tr_b16 v[222:223], v244 offset:1536\n\t" \
    "ds_read_b64_tr_b16 v[84:85], v244 offset:32\n\t" \
    "ds_read_b64_tr_b16 v[86:87], v244 offset:1568\n\t" \
    "ds_read_b64_tr_b16 v[92:93], v244 offset:64\n\t" \
    "ds_read_b64_tr_b16 v[94:95], v244 offset:1600\n\t" \
    "v_add_u32 v244, 0x18000, v243\n\t" \
    "s_waitcnt vmcnt(12) lgkmcnt(0)\n\t" \
    "s_barrier \n\t" \
    "s_add_u32 m0, %[wm], 0x12000\n\t" \
    "s_mov_b32 s44, 0x420000\n\t" \
    "buffer_load_dwordx4 v240, %[wr], s44 offen offset:0 nt lds\n\t" \
    "buffer_load_dwordx4 v241, %[wr], s44 offen offset:1024 nt lds\n\t" \
    "buffer_load_dwordx4 v242, %[wr], s44 offen offset:2048 nt lds\n\t" \
    "ds_read_b64_tr_b16 v[96:97], v244 offset:0\n\t" \
    "ds_read_b64_tr_b16 v[98:99], v244 offset:1536\n\t" \
    "ds_read_b64_tr_b16 v[104:105], v244 offset:32\n\t" \
    "ds_read_b64_tr_b16 v[106:107], v244 offset:1568\n\t" \
    "ds_read_b64_tr_b16 v[112:113], v244 offset:64\n\t" \
    "ds_read_b64_tr_b16 v[114:115], v244 offset:1600\n\t" \
    "v_add_u32 v244, 0x1e000, v243\n\t" \
    "s_waitcnt vmcnt(12) lgkmcnt(0)\n\t" \
    "s_barrier \n\t" \
    "s_add_u32 m0, %[wm], 0x18000\n\t" \
    "s_mov_b32 s44, 0x480000\n\t" \
    "buffer_load_dwordx4 v240, %[wr], s44 offen offset:0 nt lds\n\t" \
    "buffer_load_dwordx4 v241, %[wr], s44 offen offset:1024 nt lds\n\t" \
    "buffer_load_dwordx4 v242, %[wr], s44 offen offset:2048 nt lds\n\t" \
    "ds_read_b64_tr_b16 v[100:101], v244 offset:0\n\t" \
    "ds_read_b64_tr_b16 v[102:103], v244 offset:1536\n\t" \
    "ds_read_b64_tr_b16 v[108:109], v244 offset:32\n\t" \
    "ds_read_b64_tr_b16 v[110:111], v244 offset:1568\n\t" \
    "ds_read_b64_tr_b16 v[116:117], v244 offset:64\n\t" \
    "ds_read_b64_tr_b16 v[118:119], v244 offset:1600\n\t" \
    "v_add_u32 v244, 0x0, v243\n\t" \
    "s_waitcnt vmcnt(12) lgkmcnt(0)\n\t" \
    "s_barrier \n\t" \
    "s_add_u32 m0, %[wm], 0x1e000\n\t" \
    "s_mov_b32 s44, 0x4e0000\n\t" \
    "buffer_load_dwordx4 v240, %[wr], s44 offen offset:0 nt lds\n\t" \
    "buffer_load_dwordx4 v241, %[wr], s44 offen offset:1024 nt lds\n\t" \
    "buffer_load_dwordx4 v242, %[wr], s44 offen offset:2048 nt lds\n\t" \
    "ds_read_b64_tr_b16 v[224:225], v244 offset:0\n\t" \
    "ds_read_b64_tr_b16 v[226:227], v244 offset:1536\n\t" \
    "ds_read_b64_tr_b16 v[120:121], v244 offset:32\n\t" \
    "ds_read_b64_tr_b16 v[122:123], v244 offset:1568\n\t" \
    "ds_read_b64_tr_b16 v[128:129], v244 offset:64\n\t" \
    "ds_read_b64_tr_b16 v[130:131], v244 offset:1600\n\t" \
    "v_add_u32 v244, 0x6000, v243\n\t" \
    "s_waitcnt vmcnt(12) lgkmcnt(0)\n\t" \
    "s_barrier \n\t" \
    "s_add_u32 m0, %[wm], 0x0\n\t" \
    "s_mov_b32 s44, 0x540000\n\t" \
    "buffer_load_dwordx4 v240, %[wr], s44 offen offset:0 nt lds\n\t" \
    "buffer_load_dwordx4 v241, %[wr], s44 offen offset:1024 nt lds\n\t" \
    "buffer_load_dwordx4 v242, %[wr], s44 offen offset:2048 nt lds\n\t" \
    "ds_read_b64_tr_b16 v[228:229], v244 offset:0\n\t" \
    "ds_read_b64_tr_b16 v[230:231], v244 offset:1536\n\t" \
    "ds_read_b64_tr_b16 v[124:125], v244 offset:32\n\t" \
    "ds_read_b64_tr_b16 v[126:127], v244 offset:1568\n\t" \
    "ds_read_b64_tr_b16 v[132:133], v244 offset:64\n\t" \
    "ds_read_b64_tr_b16 v[134:135], v244 offset:1600\n\t" \
    "v_add_u32 v244, 0xc000, v243\n\t" \
    "s_waitcnt vmcnt(12) lgkmcnt(0)\n\t" \
    "s_barrier \n\t" \
    "s_add_u32 m0, %[wm], 0x6000\n\t" \
    "s_mov_b32 s44, 0x5a0000\n\t" \
    "buffer_load_dwordx4 v240, %[wr], s44 offen offset:0 nt lds\n\t" \
    "buffer_load_dwordx4 v241, %[wr], s44 offen offset:1024 nt lds\n\t" \
    "buffer_load_dwordx4 v242, %[wr], s44 offen offset:2048 nt lds\n\t" \
    "ds_read_b64_tr_b16 v[136:137], v244 offset:0\n\t" \
    "ds_read_b64_tr_b16 v[138:139], v244 offset:1536\n\t" \
    "ds_read_b64_tr_b16 v[144:145], v244 offset:32\n\t" \
    "ds_read_b64_tr_b16 v[146:147], v244 offset:1568\n\t" \
    "ds_read_b64_tr_b16 v[152:153], v244 offset:64\n\t" \
    "ds_read_b64_tr_b16 v[154:155], v244 offset:1600\n\t" \
    "v_add_u32 v244, 0x12000, v243\n\t" \
    "s_waitcnt vmcnt(12) lgkmcnt(0)\n\t" \
    "s_barrier \n\t" \
    "s_add_u32 m0, %[wm], 0xc000\n\t" \
    "s_mov_b32 s44, 0x600000\n\t" \
    "buffer_load_dwordx4 v240, %[wr], s44 offen offset:0 nt lds\n\t" \
    "buffer_load_dwordx4 v241, %[wr], s44 offen offset:1024 nt lds\n\t" \
    "buffer_load_dwordx4 v242, %[wr], s44 offen offset:2048 nt lds\n\t" \
    "ds_read_b64_tr_b16 v[140:141], v244 offset:0\n\t" \
    "ds_read_b64_tr_b16 v[142:143], v244 offset:1536\n\t" \
    "ds_read_b64_tr_b16 v[148:149], v244 offset:32\n\t" \
    "ds_read_b64_tr_b16 v[150:151], v244 offset:1568\n\t" \
    "ds_read_b64_tr_b16 v[156:157], v244 offset:64\n\t" \
    "ds_read_b64_tr_b16 v[158:159], v244 offset:1600\n\t" \
    "v_add_u32 v244, 0x18000, v243\n\t" \
    "s_waitcnt vmcnt(12) lgkmcnt(0)\n\t" \
    "s_barrier \n\t" \
    "s_add_u32 m0, %[wm], 0x12000\n\t" \
    "s_mov_b32 s44, 0x660000\n\t" \
    "buffer_load_dwordx4 v240, %[wr], s44 offen offset:0 nt lds\n\t" \
    "buffer_load_dwordx4 v241, %[wr], s44 offen offset:1024 nt lds\n\t" \
    "buffer_load_dwordx4 v242, %[wr], s44 offen offset:2048 nt lds\n\t" \
    "ds_read_b64_tr_b16 v[232:233], v244 offset:0\n\t" \
    "ds_read_b64_tr_b16 v[234:235], v244 offset:1536\n\t" \
    "ds_read_b64_tr_b16 v[160:161], v244 offset:32\n\t" \
    "ds_read_b64_tr_b16 v[162:163], v244 offset:1568\n\t" \
    "ds_read_b64_tr_b16 v[168:169], v244 offset:64\n\t" \
    "ds_read_b64_tr_b16 v[170:171], v244 offset:1600\n\t" \
    "v_add_u32 v244, 0x1e000, v243\n\t" \
    "s_waitcnt vmcnt(12) lgkmcnt(0)\n\t" \
    "s_barrier \n\t" \
    "s_add_u32 m0, %[wm], 0x18000\n\t" \
    "s_mov_b32 s44, 0x6c0000\n\t" \
    "buffer_load_dwordx4 v240, %[wr], s44 offen offset:0 nt lds\n\t" \
    "buffer_load_dwordx4 v241, %[wr], s44 offen offset:1024 nt lds\n\t" \
    "buffer_load_dwordx4 v242, %[wr], s44 offen offset:2048 nt lds\n\t" \
    "ds_read_b64_tr_b16 v[236:237], v244 offset:0\n\t" \
    "ds_read_b64_tr_b16 v[238:239], v244 offset:1536\n\t" \
    "ds_read_b64_tr_b16 v[164:165], v244 offset:32\n\t" \
    "ds_read_b64_tr_b16 v[166:167], v244 offset:1568\n\t" \
    "ds_read_b64_tr_b16 v[172:173], v244 offset:64\n\t" \
    "ds_read_b64_tr_b16 v[174:175], v244 offset:1600\n\t" \
    "v_add_u32 v244, 0x0, v243\n\t" \
    "s_waitcnt vmcnt(12) lgkmcnt(0)\n\t" \
    "s_barrier \n\t" \
    "s_add_u32 m0, %[wm], 0x1e000\n\t" \
    "s_mov_b32 s44, 0x720000\n\t" \
    "buffer_load_dwordx4 v240, %[wr], s44 offen offset:0 nt lds\n\t" \
    "buffer_load_dwordx4 v241, %[wr], s44 offen offset:1024 nt lds\n\t" \
    "buffer_load_dwordx4 v242, %[wr], s44 offen offset:2048 nt lds\n\t" \
    "ds_read_b64_tr_b16 v[176:177], v244 offset:0\n\t" \
    "ds_read_b64_tr_b16 v[178:179], v244 offset:1536\n\t" \
    "ds_read_b64_tr_b16 v[184:185], v244 offset:32\n\t" \
    "ds_read_b64_tr_b16 v[186:187], v244 offset:1568\n\t" \
    "ds_read_b64_tr_b16 v[192:193], v244 offset:64\n\t" \
    "ds_read_b64_tr_b16 v[194:195], v244 offset:1600\n\t" \
    "v_add_u32 v244, 0x6000, v243\n\t" \
    "s_waitcnt vmcnt(12) lgkmcnt(0)\n\t" \
    "s_barrier \n\t" \
    "ds_read_b64_tr_b16 v[180:181], v244 offset:0\n\t" \
    "ds_read_b64_tr_b16 v[182:183], v244 offset:1536\n\t" \
    "ds_read_b64_tr_b16 v[188:189], v244 offset:32\n\t" \
    "ds_read_b64_tr_b16 v[190:191], v244 offset:1568\n\t" \
    "ds_read_b64_tr_b16 v[196:197], v244 offset:64\n\t" \
    "ds_read_b64_tr_b16 v[198:199], v244 offset:1600\n\t" \
    "v_add_u32 v244, 0xc000, v243\n\t" \
    "s_waitcnt vmcnt(9) lgkmcnt(0)\n\t" \
    "s_barrier \n\t" \
    "ds_read_b64_tr_b16 v[200:201], v244 offset:0\n\t" \
    "ds_read_b64_tr_b16 v[202:203], v244 offset:1536\n\t" \
    "ds_read_b64_tr_b16 v[0:1], v244 offset:32\n\t" \
    "ds_read_b64_tr_b16 v[2:3], v244 offset:1568\n\t" \
    "ds_read_b64_tr_b16 v[8:9], v244 offset:64\n\t" \
    "ds_read_b64_tr_b16 v[10:11], v244 offset:1600\n\t" \
    "v_add_u32 v244, 0x12000, v243\n\t" \
    "s_waitcnt vmcnt(6) lgkmcnt(0)\n\t" \
    "s_barrier \n\t" \
    "ds_read_b64_tr_b16 v[204:205], v244 offset:0\n\t" \
    "ds_read_b64_tr_b16 v[206:207], v244 offset:1536\n\t" \
    "ds_read_b64_tr_b16 v[4:5], v244 offset:32\n\t" \
    "ds_read_b64_tr_b16 v[6:7], v244 offset:1568\n\t" \
    "ds_read_b64_tr_b16 v[12:13], v244 offset:64\n\t" \
    "ds_read_b64_tr_b16 v[14:15], v244 offset:1600\n\t" \
    "v_add_u32 v244, 0x18000, v243\n\t" \
    "s_waitcnt vmcnt(3) lgkmcnt(0)\n\t" \
    "s_barrier \n\t" \
    "ds_read_b64_tr_b16 v[16:17], v244 offset:0\n\t" \
    "ds_read_b64_tr_b16 v[18:19], v244 offset:1536\n\t" \
    "ds_read_b64_tr_b16 v[24:25], v244 offset:32\n\t" \
    "ds_read_b64_tr_b16 v[26:27], v244 offset:1568\n\t" \
    "ds_read_b64_tr_b16 v[32:33], v244 offset:64\n\t" \
    "ds_read_b64_tr_b16 v[34:35], v244 offset:1600\n\t" \
    "v_add_u32 v244, 0x1e000, v243\n\t" \
    "s_waitcnt vmcnt(0) lgkmcnt(0)\n\t" \
    "s_barrier \n\t" \
    "ds_read_b64_tr_b16 v[20:21], v244 offset:0\n\t" \
    "ds_read_b64_tr_b16 v[22:23], v244 offset:1536\n\t" \
    "ds_read_b64_tr_b16 v[28:29], v244 offset:32\n\t" \
    "ds_read_b64_tr_b16 v[30:31], v244 offset:1568\n\t" \
    "ds_read_b64_tr_b16 v[36:37], v244 offset:64\n\t" \
    "ds_read_b64_tr_b16 v[38:39], v244 offset:1600\n\t" \
    "s_waitcnt lgkmcnt(0)\n\t" \
    "s_barrier \n\t" \
    "ds_write_b128 v254, v[200:203] offset:55296\n\t" \
    "ds_write_b128 v254, v[204:207] offset:56320\n\t" \
    "ds_write_b128 v254, v[208:211] offset:57344\n\t" \
    "ds_write_b128 v254, v[212:215] offset:58368\n\t" \
    "ds_write_b128 v254, v[216:219] offset:59392\n\t" \
    "ds_write_b128 v254, v[220:223] offset:60416\n\t" \
    "ds_write_b128 v254, v[224:227] offset:61440\n\t" \
    "ds_write_b128 v254, v[228:231] offset:62464\n\t" \
    "ds_write_b128 v254, v[232:235] offset:63488\n\t" \
    "ds_write_b128 v254, v[236:239] offset:64512\n\t" \
    "s_waitcnt vmcnt(0) lgkmcnt(0)\n\t" \
    "s_barrier \n\t" \
    "ds_read_b128 v[208:211], v254 offset:24576\n\t" \
    "ds_read_b128 v[224:227], v253 offset:0\n\t" \
    "ds_read_b128 v[228:231], v253 offset:20480\n\t" \
    "ds_read_b128 v[212:215], v254 offset:25600\n\t" \
    "ds_read_b128 v[232:235], v253 offset:1024\n\t" \
    "ds_read_b128 v[236:239], v253 offset:21504\n\t" \
    "ds_read_b128 v[216:219], v254 offset:26624\n\t" \
    "ds_read_b128 v[240:243], v253 offset:2048\n\t" \
    "ds_read_b128 v[244:247], v253 offset:22528\n\t" \
    "ds_read_b128 v[220:223], v254 offset:27648\n\t" \
    "ds_read_b128 v[248:251], v253 offset:3072\n\t" \
    ".Ls31_%=:\n\t" \
    "s_waitcnt lgkmcnt(9)\n\t" \
    "v_mfma_f32_16x16x32_bf16 v[200:203], v[208:211], v[224:227], 0\n\t" \
    "ds_read_b128 v[224:227], v253 offset:23552\n\t" \
    "s_waitcnt lgkmcnt(9)\n\t" \
    "v_mfma_f32_16x16x32_bf16 v[204:207], v[208:211], v[228:231], 0\n\t" \
    "ds_read_b128 v[228:231], v253 offset:4096\n\t" \
    "ds_read_b128 v[208:211], v254 offset:28672\n\t" \
    "s_waitcnt lgkmcnt(9)\n\t" \
    "v_mfma_f32_16x16x32_bf16 v[200:203], v[212:215], v[232:235], v[200:203]\n\t" \
    "ds_read_b128 v[232:235], v253 offset:24576\n\t" \
    "s_waitcnt lgkmcnt(9)\n\t" \
    "v_mfma_f32_16x16x32_bf16 v[204:207], v[212:215], v[236:239], v[204:207]\n\t" \
    "ds_read_b128 v[236:239], v253 offset:5120\n\t" \
    "ds_read_b128 v[212:215], v254 offset:29696\n\t" \
    "s_waitcnt lgkmcnt(9)\n\t" \
    "v_mfma_f32_16x16x32_bf16 v[200:203], v[216:219], v[240:243], v[200:203]\n\t" \
    "ds_read_b128 v[240:243], v253 offset:25600\n\t" \
    "s_waitcnt lgkmcnt(9)\n\t" \
    "v_mfma_f32_16x16x32_bf16 v[204:207], v[216:219], v[244:247], v[204:207]\n\t" \
    "ds_read_b128 v[244:247], v253 offset:6144\n\t" \
    "ds_read_b128 v[216:219], v254 offset:30720\n\t" \
    "s_waitcnt lgkmcnt(9)\n\t" \
    "v_mfma_f32_16x16x32_bf16 v[200:203], v[220:223], v[248:251], v[200:203]\n\t" \
    "ds_read_b128 v[248:251], v253 offset:26624\n\t" \
    "s_waitcnt lgkmcnt(9)\n\t" \
    "v_mfma_f32_16x16x32_bf16 v[204:207], v[220:223], v[224:227], v[204:207]\n\t" \
    "ds_read_b128 v[224:227], v253 offset:7168\n\t" \
    "ds_read_b128 v[220:223], v254 offset:31744\n\t" \
    "s_waitcnt lgkmcnt(9)\n\t" \
    "v_mfma_f32_16x16x32_bf16 v[200:203], v[208:211], v[228:231], v[200:203]\n\t" \
    "ds_read_b128 v[228:231], v253 offset:27648\n\t" \
    "s_waitcnt lgkmcnt(9)\n\t" \
    "v_mfma_f32_16x16x32_bf16 v[204:207], v[208:211], v[232:235], v[204:207]\n\t" \
    "ds_read_b128 v[232:235], v253 offset:8192\n\t" \
    "ds_read_b128 v[208:211], v254 offset:32768\n\t" \
    "s_waitcnt lgkmcnt(9)\n\t" \
    "v_mfma_f32_16x16x32_bf16 v[200:203], v[212:215], v[236:239], v[200:203]\n\t" \
    "ds_read_b128 v[236:239], v253 offset:28672\n\t" \
    "s_waitcnt lgkmcnt(9)\n\t" \
    "v_mfma_f32_16x16x32_bf16 v[204:207], v[212:215], v[240:243], v[204:207]\n\t" \
    "ds_read_b128 v[240:243], v253 offset:9216\n\t" \
    "ds_read_b128 v[212:215], v254 offset:33792\n\t" \
    "s_waitcnt lgkmcnt(9)\n\t" \
    "v_mfma_f32_16x16x32_bf16 v[200:203], v[216:219], v[244:247], v[200:203]\n\t" \
    "ds_read_b128 v[244:247], v253 offset:29696\n\t" \
    "s_waitcnt lgkmcnt(9)\n\t" \
    "v_mfma_f32_16x16x32_bf16 v[204:207], v[216:219], v[248:251], v[204:207]\n\t" \
    "s_waitcnt lgkmcnt(7)\n\t" \
    "v_mfma_f32_16x16x32_bf16 v[200:203], v[220:223], v[224:227], v[200:203]\n\t" \
    "s_waitcnt lgkmcnt(6)\n\t" \
    "v_mfma_f32_16x16x32_bf16 v[204:207], v[220:223], v[228:231], v[204:207]\n\t" \
    "s_waitcnt lgkmcnt(4)\n\t" \
    "v_mfma_f32_16x16x32_bf16 v[200:203], v[208:211], v[232:235], v[200:203]\n\t" \
    "s_waitcnt lgkmcnt(3)\n\t" \
    "v_mfma_f32_16x16x32_bf16 v[204:207], v[208:211], v[236:239], v[204:207]\n\t" \
    "s_waitcnt lgkmcnt(1)\n\t" \
    "v_mfma_f32_16x16x32_bf16 v[200:203], v[212:215], v[240:243], v[200:203]\n\t" \
    "s_waitcnt lgkmcnt(0)\n\t" \
    "v_mfma_f32_16x16x32_bf16 v[204:207], v[212:215], v[244:247], v[204:207]\n\t" \
    "ds_read_b128 v[224:227], v253 offset:0\n\t" \
    "ds_read_b128 v[244:247], v254 offset:57344\n\t" \
    "ds_read_b128 v[228:231], v253 offset:1024\n\t" \
    "ds_read_b128 v[232:235], v253 offset:20480\n\t" \
    "ds_read_b128 v[236:239], v253 offset:21504\n\t" \
    "ds_read_b128 v[248:251], v254 offset:58368\n\t" \
    "s_nop 1\n\t" \
    "ds_write_b128 v253, v[200:203] offset:40960\n\t" \
    "ds_write_b128 v253, v[204:207] offset:41984\n\t" \
    "s_nop 1\n\t" \
    "s_waitcnt lgkmcnt(7)\n\t" \
    "v_mfma_f32_16x16x32_bf16 v[204:207], v[40:43], v[224:227], 0\n\t" \
    "s_waitcnt lgkmcnt(5)\n\t" \
    "v_mfma_f32_16x16x32_bf16 v[204:207], v[44:47], v[228:231], v[204:207]\n\t" \
    "ds_read_b128 v[240:243], v253 offset:2048\n\t" \
    "v_mfma_f32_16x16x32_bf16 v[208:211], v[48:51], v[224:227], 0\n\t" \
    "v_mfma_f32_16x16x32_bf16 v[208:211], v[52:55], v[228:231], v[208:211]\n\t" \
    "v_mfma_f32_16x16x32_bf16 v[200:203], v[244:247], v[224:227], 0\n\t" \
    "s_waitcnt lgkmcnt(3)\n\t" \
    "v_mfma_f32_16x16x32_bf16 v[200:203], v[248:251], v[228:231], v[200:203]\n\t" \
    "v_mfma_f32_16x16x32_bf16 v[216:219], v[40:43], v[232:235], 0\n\t" \
    "ds_read_b128 v[224:227], v253 offset:3072\n\t" \
    "v_mfma_f32_16x16x32_bf16 v[216:219], v[44:47], v[236:239], v[216:219]\n\t" \
    "ds_read_b128 v[228:231], v253 offset:22528\n\t" \
    "v_mfma_f32_16x16x32_bf16 v[220:223], v[48:51], v[232:235], 0\n\t" \
    "v_mfma_f32_16x16x32_bf16 v[220:223], v[52:55], v[236:239], v[220:223]\n\t" \
    "v_mfma_f32_16x16x32_bf16 v[212:215], v[244:247], v[232:235], 0\n\t" \
    "ds_read_b128 v[244:247], v254 offset:59392\n\t" \
    "v_mfma_f32_16x16x32_bf16 v[212:215], v[248:251], v[236:239], v[212:215]\n\t" \
    "ds_read_b128 v[248:251], v254 offset:60416\n\t" \
    "s_waitcnt lgkmcnt(4)\n\t" \
    "v_mfma_f32_16x16x32_bf16 v[204:207], v[64:67], v[240:243], v[204:207]\n\t" \
    "ds_read_b128 v[232:235], v253 offset:23552\n\t" \
    "s_waitcnt lgkmcnt(4)\n\t" \
    "v_mfma_f32_16x16x32_bf16 v[204:207], v[68:71], v[224:227], v[204:207]\n\t" \
    "ds_read_b128 v[236:239], v253 offset:4096\n\t" \
    "v_mfma_f32_16x16x32_bf16 v[208:211], v[72:75], v[240:243], v[208:211]\n\t" \
    "v_mfma_f32_16x16x32_bf16 v[208:211], v[76:79], v[224:227], v[208:211]\n\t" \
    "v_mfma_f32_16x16x32_bf16 v[200:203], v[56:59], v[240:243], v[200:203]\n\t" \
    "v_mfma_f32_16x16x32_bf16 v[200:203], v[60:63], v[224:227], v[200:203]\n\t" \
    "s_waitcnt lgkmcnt(4)\n\t" \
    "v_mfma_f32_16x16x32_bf16 v[216:219], v[64:67], v[228:231], v[216:219]\n\t" \
    "ds_read_b128 v[240:243], v253 offset:5120\n\t" \
    "s_waitcnt lgkmcnt(2)\n\t" \
    "v_mfma_f32_16x16x32_bf16 v[216:219], v[68:71], v[232:235], v[216:219]\n\t" \
    "ds_read_b128 v[224:227], v253 offset:24576\n\t" \
    "v_mfma_f32_16x16x32_bf16 v[220:223], v[72:75], v[228:231], v[220:223]\n\t" \
    "v_mfma_f32_16x16x32_bf16 v[220:223], v[76:79], v[232:235], v[220:223]\n\t" \
    "v_mfma_f32_16x16x32_bf16 v[212:215], v[56:59], v[228:231], v[212:215]\n\t" \
    "v_mfma_f32_16x16x32_bf16 v[212:215], v[60:63], v[232:235], v[212:215]\n\t" \
    "s_waitcnt lgkmcnt(2)\n\t" \
    "v_mfma_f32_16x16x32_bf16 v[204:207], v[80:83], v[236:239], v[204:207]\n\t" \
    "ds_read_b128 v[228:231], v253 offset:25600\n\t" \
    "s_waitcnt lgkmcnt(2)\n\t" \
    "v_mfma_f32_16x16x32_bf16 v[204:207], v[84:87], v[240:243], v[204:207]\n\t" \
    "ds_read_b128 v[232:235], v253 offset:6144\n\t" \
    "v_mfma_f32_16x16x32_bf16 v[208:211], v[88:91], v[236:239], v[208:211]\n\t" \
    "v_mfma_f32_16x16x32_bf16 v[208:211], v[92:95], v[240:243], v[208:211]\n\t" \
    "v_mfma_f32_16x16x32_bf16 v[200:203], v[244:247], v[236:239], v[200:203]\n\t" \
    "v_mfma_f32_16x16x32_bf16 v[200:203], v[248:251], v[240:243], v[200:203]\n\t" \
    "s_waitcnt lgkmcnt(2)\n\t" \
    "v_mfma_f32_16x16x32_bf16 v[216:219], v[80:83], v[224:227], v[216:219]\n\t" \
    "ds_read_b128 v[236:239], v253 offset:7168\n\t" \
    "s_waitcnt lgkmcnt(2)\n\t" \
    "v_mfma_f32_16x16x32_bf16 v[216:219], v[84:87], v[228:231], v[216:219]\n\t" \
    "ds_read_b128 v[240:243], v253 offset:26624\n\t" \
    "v_mfma_f32_16x16x32_bf16 v[220:223], v[88:91], v[224:227], v[220:223]\n\t" \
    "v_mfma_f32_16x16x32_bf16 v[220:223], v[92:95], v[228:231], v[220:223]\n\t" \
    "v_mfma_f32_16x16x32_bf16 v[212:215], v[244:247], v[224:227], v[212:215]\n\t" \
    "ds_read_b128 v[244:247], v254 offset:61440\n\t" \
    "v_mfma_f32_16x16x32_bf16 v[212:215], v[248:251], v[228:231], v[212:215]\n\t" \
    "ds_read_b128 v[248:251], v254 offset:62464\n\t" \
    "s_waitcnt lgkmcnt(4)\n\t" \
    "v_mfma_f32_16x16x32_bf16 v[204:207], v[104:107], v[232:235], v[204:207]\n\t" \
    "ds_read_b128 v[224:227], v253 offset:27648\n\t" \
    "s_waitcnt lgkmcnt(4)\n\t" \
    "v_mfma_f32_16x16x32_bf16 v[204:207], v[108:111], v[236:239], v[204:207]\n\t" \
    "ds_read_b128 v[228:231], v253 offset:8192\n\t" \
    "v_mfma_f32_16x16x32_bf16 v[208:211], v[112:115], v[232:235], v[208:211]\n\t" \
    "v_mfma_f32_16x16x32_bf16 v[208:211], v[116:119], v[236:239], v[208:211]\n\t" \
    "v_mfma_f32_16x16x32_bf16 v[200:203], v[96:99], v[232:235], v[200:203]\n\t" \
    "v_mfma_f32_16x16x32_bf16 v[200:203], v[100:103], v[236:239], v[200:203]\n\t" \
    "s_waitcnt lgkmcnt(4)\n\t" \
    "v_mfma_f32_16x16x32_bf16 v[216:219], v[104:107], v[240:243], v[216:219]\n\t" \
    "ds_read_b128 v[232:235], v253 offset:9216\n\t" \
    "s_waitcnt lgkmcnt(2)\n\t" \
    "v_mfma_f32_16x16x32_bf16 v[216:219], v[108:111], v[224:227], v[216:219]\n\t" \
    "ds_read_b128 v[236:239], v253 offset:28672\n\t" \
    "v_mfma_f32_16x16x32_bf16 v[220:223], v[112:115], v[240:243], v[220:223]\n\t" \
    "v_mfma_f32_16x16x32_bf16 v[220:223], v[116:119], v[224:227], v[220:223]\n\t" \
    "v_mfma_f32_16x16x32_bf16 v[212:215], v[96:99], v[240:243], v[212:215]\n\t" \
    "v_mfma_f32_16x16x32_bf16 v[212:215], v[100:103], v[224:227], v[212:215]\n\t" \
    "s_waitcnt lgkmcnt(2)\n\t" \
    "v_mfma_f32_16x16x32_bf16 v[204:207], v[120:123], v[228:231], v[204:207]\n\t" \
    "ds_read_b128 v[240:243], v253 offset:29696\n\t" \
    "s_waitcnt lgkmcnt(2)\n\t" \
    "v_mfma_f32_16x16x32_bf16 v[204:207], v[124:127], v[232:235], v[204:207]\n\t" \
    "ds_read_b128 v[224:227], v253 offset:10240\n\t" \
    "v_mfma_f32_16x16x32_bf16 v[208:211], v[128:131], v[228:231], v[208:211]\n\t" \
    "v_mfma_f32_16x16x32_bf16 v[208:211], v[132:135], v[232:235], v[208:211]\n\t" \
    "v_mfma_f32_16x16x32_bf16 v[200:203], v[244:247], v[228:231], v[200:203]\n\t" \
    "v_mfma_f32_16x16x32_bf16 v[200:203], v[248:251], v[232:235], v[200:203]\n\t" \
    "s_waitcnt lgkmcnt(2)\n\t" \
    "v_mfma_f32_16x16x32_bf16 v[216:219], v[120:123], v[236:239], v[216:219]\n\t" \
    "ds_read_b128 v[228:231], v253 offset:11264\n\t" \
    "s_waitcnt lgkmcnt(2)\n\t" \
    "v_mfma_f32_16x16x32_bf16 v[216:219], v[124:127], v[240:243], v[216:219]\n\t" \
    "ds_read_b128 v[232:235], v253 offset:30720\n\t" \
    "v_mfma_f32_16x16x32_bf16 v[220:223], v[128:131], v[236:239], v[220:223]\n\t" \
    "v_mfma_f32_16x16x32_bf16 v[220:223], v[132:135], v[240:243], v[220:223]\n\t" \
    "v_mfma_f32_16x16x32_bf16 v[212:215], v[244:247], v[236:239], v[212:215]\n\t" \
    "ds_read_b128 v[244:247], v254 offset:63488\n\t" \
    "v_mfma_f32_16x16x32_bf16 v[212:215], v[248:251], v[240:243], v[212:215]\n\t" \
    "ds_read_b128 v[248:251], v254 offset:64512\n\t" \
    "ds_read_b128 v[236:239], v253 offset:31744\n\t" \
    "s_waitcnt lgkmcnt(0)\n\t" \
    "s_barrier \n\t" \
    LST7 \
    "v_mfma_f32_16x16x32_bf16 v[204:207], v[144:147], v[224:227], v[204:207]\n\t" \
    "v_mfma_f32_16x16x32_bf16 v[204:207], v[148:151], v[228:231], v[204:207]\n\t" \
    "ds_read_b128 v[240:243], v253 offset:12288\n\t" \
    "v_mfma_f32_16x16x32_bf16 v[208:211], v[152:155], v[224:227], v[208:211]\n\t" \
    "v_mfma_f32_16x16x32_bf16 v[208:211], v[156:159], v[228:231], v[208:211]\n\t" \
    "v_mfma_f32_16x16x32_bf16 v[200:203], v[136:139], v[224:227], v[200:203]\n\t" \
    "v_mfma_f32_16x16x32_bf16 v[200:203], v[140:143], v[228:231], v[200:203]\n\t" \
    "v_mfma_f32_16x16x32_bf16 v[216:219], v[144:147], v[232:235], v[216:219]\n\t" \
    "ds_read_b128 v[224:227], v253 offset:13312\n\t" \
    "v_mfma_f32_16x16x32_bf16 v[216:219], v[148:151], v[236:239], v[216:219]\n\t" \
    "ds_read_b128 v[228:231], v253 offset:32768\n\t" \
    "v_mfma_f32_16x16x32_bf16 v[220:223], v[152:155], v[232:235], v[220:223]\n\t" \
    "v_mfma_f32_16x16x32_bf16 v[220:223], v[156:159], v[236:239], v[220:223]\n\t" \
    "v_mfma_f32_16x16x32_bf16 v[212:215], v[136:139], v[232:235], v[212:215]\n\t" \
    "v_mfma_f32_16x16x32_bf16 v[212:215], v[140:143], v[236:239], v[212:215]\n\t" \
    "s_waitcnt lgkmcnt(2)\n\t" \
    "v_mfma_f32_16x16x32_bf16 v[204:207], v[160:163], v[240:243], v[204:207]\n\t" \
    "ds_read_b128 v[232:235], v253 offset:33792\n\t" \
    "s_and_b32 s44, s40, 3\n\t" \
    "s_waitcnt lgkmcnt(2)\n\t" \
    "v_mfma_f32_16x16x32_bf16 v[204:207], v[164:167], v[224:227], v[204:207]\n\t" \
    "ds_read_b128 v[236:239], v253 offset:16384\n\t" \
    "s_lshl_b32 s44, s44, 17\n\t" \
    "v_mfma_f32_16x16x32_bf16 v[208:211], v[168:171], v[240:243], v[208:211]\n\t" \
    "s_add_u32 s44, s44, %[mb]\n\t" \
    "v_mfma_f32_16x16x32_bf16 v[208:211], v[172:175], v[224:227], v[208:211]\n\t" \
    "s_add_u32 s46, s44, 0x4000\n\t" \
    "v_mfma_f32_16x16x32_bf16 v[200:203], v[244:247], v[240:243], v[200:203]\n\t" \
    "s_add_u32 s47, s44, 0x4400\n\t" \
    "v_mfma_f32_16x16x32_bf16 v[200:203], v[248:251], v[224:227], v[200:203]\n\t" \
    "s_waitcnt lgkmcnt(2)\n\t" \
    "v_mfma_f32_16x16x32_bf16 v[216:219], v[160:163], v[228:231], v[216:219]\n\t" \
    "ds_read_b128 v[240:243], v253 offset:17408\n\t" \
    "s_waitcnt lgkmcnt(2)\n\t" \
    "v_mfma_f32_16x16x32_bf16 v[216:219], v[164:167], v[232:235], v[216:219]\n\t" \
    "ds_read_b128 v[224:227], v253 offset:36864\n\t" \
    "v_mfma_f32_16x16x32_bf16 v[220:223], v[168:171], v[228:231], v[220:223]\n\t" \
    "v_mfma_f32_16x16x32_bf16 v[220:223], v[172:175], v[232:235], v[220:223]\n\t" \
    "v_mfma_f32_16x16x32_bf16 v[212:215], v[244:247], v[228:231], v[212:215]\n\t" \
    "ds_read_b128 v[244:247], v254 offset:55296\n\t" \
    "v_mfma_f32_16x16x32_bf16 v[212:215], v[248:251], v[232:235], v[212:215]\n\t" \
    "ds_read_b128 v[248:251], v254 offset:56320\n\t" \
    "s_waitcnt lgkmcnt(4)\n\t" \
    "v_mfma_f32_16x16x32_bf16 v[204:207], v[0:3], v[236:239], v[204:207]\n\t" \
    "ds_read_b128 v[228:231], v253 offset:37888\n\t" \
    "s_waitcnt lgkmcnt(4)\n\t" \
    "v_mfma_f32_16x16x32_bf16 v[204:207], v[4:7], v[240:243], v[204:207]\n\t" \
    "ds_read_b128 v[232:235], v253 offset:14336\n\t" \
    "v_mfma_f32_16x16x32_bf16 v[208:211], v[8:11], v[236:239], v[208:211]\n\t" \
    "v_mfma_f32_16x16x32_bf16 v[208:211], v[12:15], v[240:243], v[208:211]\n\t" \
    "s_waitcnt lgkmcnt(3)\n\t" \
    "v_mfma_f32_16x16x32_bf16 v[200:203], v[244:247], v[236:239], v[200:203]\n\t" \
    "s_waitcnt lgkmcnt(2)\n\t" \
    "v_mfma_f32_16x16x32_bf16 v[200:203], v[248:251], v[240:243], v[200:203]\n\t" \
    "v_mfma_f32_16x16x32_bf16 v[216:219], v[0:3], v[224:227], v[216:219]\n\t" \
    "ds_read_b128 v[236:239], v253 offset:15360\n\t" \
    "s_waitcnt lgkmcnt(2)\n\t" \
    "v_mfma_f32_16x16x32_bf16 v[216:219], v[4:7], v[228:231], v[216:219]\n\t" \
    "ds_read_b128 v[240:243], v253 offset:18432\n\t" \
    "v_mfma_f32_16x16x32_bf16 v[220:223], v[8:11], v[224:227], v[220:223]\n\t" \
    "v_mfma_f32_16x16x32_bf16 v[220:223], v[12:15], v[228:231], v[220:223]\n\t" \
    "v_mfma_f32_16x16x32_bf16 v[212:215], v[244:247], v[224:227], v[212:215]\n\t" \
    "v_mfma_f32_16x16x32_bf16 v[212:215], v[248:251], v[228:231], v[212:215]\n\t" \
    "s_waitcnt lgkmcnt(2)\n\t" \
    "v_mfma_f32_16x16x32_bf16 v[204:207], v[184:187], v[232:235], v[204:207]\n\t" \
    "ds_read_b128 v[224:227], v253 offset:19456\n\t" \
    "v_mov_b64 v[244:245], 0\n\t" \
    "s_waitcnt lgkmcnt(2)\n\t" \
    "v_mfma_f32_16x16x32_bf16 v[204:207], v[188:191], v[236:239], v[204:207]\n\t" \
    "ds_read_b128 v[228:231], v253 offset:34816\n\t" \
    "v_mov_b64 v[246:247], 0\n\t" \
    "v_mfma_f32_16x16x32_bf16 v[208:211], v[192:195], v[232:235], v[208:211]\n\t" \
    "v_mov_b64 v[248:249], 0\n\t" \
    "v_mfma_f32_16x16x32_bf16 v[208:211], v[196:199], v[236:239], v[208:211]\n\t" \
    "v_mov_b64 v[250:251], 0\n\t" \
    "v_mfma_f32_16x16x32_bf16 v[200:203], v[176:179], v[232:235], v[200:203]\n\t" \
    "v_mfma_f32_16x16x32_bf16 v[200:203], v[180:183], v[236:239], v[200:203]\n\t" \
    "s_waitcnt lgkmcnt(2)\n\t" \
    "v_mfma_f32_16x16x32_bf16 v[204:207], v[24:27], v[240:243], v[204:207]\n\t" \
    "ds_read_b128 v[232:235], v253 offset:35840\n\t" \
    "s_waitcnt lgkmcnt(2)\n\t" \
    "v_mfma_f32_16x16x32_bf16 v[204:207], v[28:31], v[224:227], v[204:207]\n\t" \
    "ds_read_b128 v[236:239], v253 offset:38912\n\t" \
    "v_mfma_f32_16x16x32_bf16 v[208:211], v[32:35], v[240:243], v[208:211]\n\t" \
    "v_mfma_f32_16x16x32_bf16 v[208:211], v[36:39], v[224:227], v[208:211]\n\t" \
    "v_mfma_f32_16x16x32_bf16 v[200:203], v[16:19], v[240:243], v[200:203]\n\t" \
    "v_mfma_f32_16x16x32_bf16 v[200:203], v[20:23], v[224:227], v[200:203]\n\t" \
    "ds_read_b128 v[240:243], v253 offset:39936\n\t" \
    "s_waitcnt lgkmcnt(3)\n\t" \
    "v_mfma_f32_16x16x32_bf16 v[216:219], v[184:187], v[228:231], v[216:219]\n\t" \
    "s_waitcnt lgkmcnt(2)\n\t" \
    "v_mfma_f32_16x16x32_bf16 v[216:219], v[188:191], v[232:235], v[216:219]\n\t" \
    "v_cvt_pk_bf16_f32 v204, v204, v208\n\t" \
    "v_cvt_pk_bf16_f32 v205, v205, v209\n\t" \
    "s_waitcnt lgkmcnt(1)\n\t" \
    "v_mfma_f32_16x16x32_bf16 v[216:219], v[24:27], v[236:239], v[216:219]\n\t" \
    "v_cvt_pk_bf16_f32 v206, v206, v210\n\t" \
    "v_cvt_pk_bf16_f32 v207, v207, v211\n\t" \
    "s_waitcnt lgkmcnt(0)\n\t" \
    "v_mfma_f32_16x16x32_bf16 v[216:219], v[28:31], v[240:243], v[216:219]\n\t" \
    "ds_write_b128 v254, v[204:207] offset:5120\n\t" \
    "v_cvt_pk_bf16_f32 v200, v200, v201\n\t" \
    "v_mfma_f32_16x16x32_bf16 v[220:223], v[192:195], v[228:231], v[220:223]\n\t" \
    "v_cvt_pk_bf16_f32 v201, v202, v203\n\t" \
    "ds_write_b64 v254, v[200:201] offset:1032\n\t" \
    "v_mfma_f32_16x16x32_bf16 v[220:223], v[196:199], v[232:235], v[220:223]\n\t" \
    "ds_read_b128 v[200:203], v254 offset:55296\n\t" \
    "ds_read_b128 v[204:207], v254 offset:56320\n\t" \
    "v_mfma_f32_16x16x32_bf16 v[220:223], v[32:35], v[236:239], v[220:223]\n\t" \
    "v_mov_b64 v[208:209], 0\n\t" \
    "v_mov_b64 v[210:211], 0\n\t" \
    "v_mfma_f32_16x16x32_bf16 v[220:223], v[36:39], v[240:243], v[220:223]\n\t" \
    "v_mfma_f32_16x16x32_bf16 v[212:215], v[176:179], v[228:231], v[212:215]\n\t" \
    "v_mfma_f32_16x16x32_bf16 v[212:215], v[180:183], v[232:235], v[212:215]\n\t" \
    "v_mfma_f32_16x16x32_bf16 v[212:215], v[16:19], v[236:239], v[212:215]\n\t" \
    "v_cvt_pk_bf16_f32 v216, v216, v220\n\t" \
    "v_cvt_pk_bf16_f32 v217, v217, v221\n\t" \
    "v_mfma_f32_16x16x32_bf16 v[212:215], v[20:23], v[240:243], v[212:215]\n\t" \
    "v_cvt_pk_bf16_f32 v218, v218, v222\n\t" \
    "v_cvt_pk_bf16_f32 v219, v219, v223\n\t" \
    "ds_write_b128 v254, v[216:219] offset:11264\n\t" \
    "ds_read_b128 v[216:219], v254 offset:57344\n\t" \
    "ds_read_b128 v[220:223], v254 offset:58368\n\t" \
    "s_nop 2\n\t" \
    "v_cvt_pk_bf16_f32 v212, v212, v213\n\t" \
    "v_cvt_pk_bf16_f32 v213, v214, v215\n\t" \
    "ds_write_b64 v254, v[212:213] offset:7176\n\t" \
    LST6 \
    "s_sleep 8\n\t" \
    "buffer_load_dwordx4 v[224:227], v253, s[60:63], s46 offen sc1\n\t" \
    "buffer_load_dwordx4 v[228:231], v253, s[60:63], s47 offen sc1\n\t" \
    "s_waitcnt lgkmcnt(0)\n\t" \
    LFLUSH212 \
    "v_bfe_i32 v212, v253, 5, 1\n\t" \
    "v_and_b32 v212, 0xaaaa, v212\n\t" \
    "v_or_b32 v212, 0x4444, v212\n\t" \
    "v_and_b32 v213, 0xffff, v255\n\t" \
    "v_lshrrev_b32 v214, 16, v255\n\t" \
    ".Ls32_%=:\n\t" \
    "s_waitcnt vmcnt(1)\n\t" \
    "v_max_u32 v215, v224, v225\n\t" \
    "v_max3_u32 v215, v226, v227, v215\n\t" \
    "s_waitcnt vmcnt(0)\n\t" \
    "v_max3_u32 v215, v228, v229, v215\n\t" \
    "v_max3_u32 v215, v230, v231, v215\n\t" \
    "v_cmp_eq_u32 vcc, -1, v215\n\t" \
    "s_cbranch_vccz .Ls33_%=\n\t" \
    "v_max_u32 v215, v224, v225\n\t" \
    "v_max3_u32 v215, v226, v227, v215\n\t" \
    "v_cmp_eq_u32 vcc, -1, v215\n\t" \
    "s_cbranch_vccz .Ls34_%=\n\t" \
    "buffer_load_dwordx4 v[224:227], v253, s[60:63], s46 offen sc1\n\t" \
    ".Ls34_%=:\n\t" \
    "v_max_u32 v215, v228, v229\n\t" \
    "v_max3_u32 v215, v230, v231, v215\n\t" \
    "v_cmp_eq_u32 vcc, -1, v215\n\t" \
    "s_cbranch_vccz .Ls35_%=\n\t" \
    "buffer_load_dwordx4 v[228:231], v253, s[60:63], s47 offen sc1\n\t" \
    ".Ls35_%=:\n\t" \
    "s_cmp_eq_u32 s41, 0\n\t" \
    "s_cbranch_scc1 .Ls33_%=\n\t" \
    "s_sub_u32 s41, s41, 1\n\t" \
    "s_waitcnt vmcnt(0)\n\t" \
    "s_branch .Ls32_%=\n\t" \
    ".Ls33_%=:\n\t" \
    LST2 \
    "ds_write_b128 v253, v[224:227] offset:49152\n\t" \
    "ds_write_b128 v253, v[228:231] offset:50176\n\t" \
    "v_smfmac_f32_16x16x64_bf16 v[244:247], v[224:227], v[0:7], v212\n\t" \
    "v_smfmac_f32_16x16x64_bf16 v[248:251], v[224:227], v[8:15], v212\n\t" \
    "v_smfmac_f32_16x16x64_bf16 v[208:211], v[224:227], v[200:207], v212\n\t" \
    "s_waitcnt lgkmcnt(0)\n\t" \
    "s_barrier \n\t" \
    LST3 \
    "ds_read_b128 v[232:235], v253 offset:40960\n\t" \
    "ds_read_b128 v[236:239], v253 offset:41984\n\t" \
    "ds_read_b128 v[240:243], v253 offset:43008\n\t" \
    "ds_read_b128 v[224:227], v253 offset:44032\n\t" \
    "ds_read_b128 v[200:203], v254 offset:59392\n\t" \
    "ds_read_b128 v[204:207], v254 offset:60416\n\t" \
    "v_smfmac_f32_16x16x64_bf16 v[208:211], v[228:231], v[16:23], v212\n\t" \
    "v_smfmac_f32_16x16x64_bf16 v[244:247], v[228:231], v[24:31], v212\n\t" \
    "v_smfmac_f32_16x16x64_bf16 v[248:251], v[228:231], v[32:39], v212\n\t" \
    "ds_read_b128 v[228:231], v253 offset:45056\n\t" \
    "s_waitcnt lgkmcnt(6)\n\t" \
    "v_smfmac_f32_16x16x64_bf16 v[208:211], v[232:235], v[216:223], v212\n\t" \
    "ds_read_b128 v[216:219], v254 offset:61440\n\t" \
    "ds_read_b128 v[220:223], v254 offset:62464\n\t" \
    "v_smfmac_f32_16x16x64_bf16 v[244:247], v[232:235], v[40:47], v212\n\t" \
    "v_smfmac_f32_16x16x64_bf16 v[248:251], v[232:235], v[48:55], v212\n\t" \
    "ds_read_b128 v[232:235], v253 offset:46080\n\t" \
    "s_waitcnt lgkmcnt(8)\n\t" \
    "v_smfmac_f32_16x16x64_bf16 v[208:211], v[236:239], v[56:63], v212\n\t" \
    "v_smfmac_f32_16x16x64_bf16 v[244:247], v[236:239], v[64:71], v212\n\t" \
    "v_smfmac_f32_16x16x64_bf16 v[248:251], v[236:239], v[72:79], v212\n\t" \
    "ds_read_b128 v[236:239], v253 offset:47104\n\t" \
    "s_waitcnt lgkmcnt(5)\n\t" \
    "v_smfmac_f32_16x16x64_bf16 v[208:211], v[240:243], v[200:207], v212\n\t" \
    "ds_read_b128 v[200:203], v254 offset:63488\n\t" \
    "ds_read_b128 v[204:207], v254 offset:64512\n\t" \
    "v_smfmac_f32_16x16x64_bf16 v[244:247], v[240:243], v[80:87], v212\n\t" \
    "v_smfmac_f32_16x16x64_bf16 v[248:251], v[240:243], v[88:95], v212\n\t" \
    "ds_read_b128 v[240:243], v253 offset:48128\n\t" \
    "v_smfmac_f32_16x16x64_bf16 v[208:211], v[224:227], v[96:103], v212\n\t" \
    "v_smfmac_f32_16x16x64_bf16 v[244:247], v[224:227], v[104:111], v212\n\t" \
    "v_smfmac_f32_16x16x64_bf16 v[248:251], v[224:227], v[112:119], v212\n\t" \
    "s_waitcnt lgkmcnt(5)\n\t" \
    "v_smfmac_f32_16x16x64_bf16 v[208:211], v[228:231], v[216:223], v212\n\t" \
    "s_waitcnt lgkmcnt(4)\n\t" \
    "v_smfmac_f32_16x16x64_bf16 v[208:211], v[232:235], v[136:143], v212\n\t" \
    "s_waitcnt lgkmcnt(1)\n\t" \
    "v_smfmac_f32_16x16x64_bf16 v[208:211], v[236:239], v[200:207], v212\n\t" \
    "s_waitcnt lgkmcnt(0)\n\t" \
    "v_smfmac_f32_16x16x64_bf16 v[208:211], v[240:243], v[176:183], v212\n\t" \
    "v_smfmac_f32_16x16x64_bf16 v[244:247], v[228:231], v[120:127], v212\n\t" \
    "v_smfmac_f32_16x16x64_bf16 v[244:247], v[232:235], v[144:151], v212\n\t" \
    "v_smfmac_f32_16x16x64_bf16 v[244:247], v[236:239], v[160:167], v212\n\t" \
    "v_add_f32 v208, v208, v210\n\t" \
    "v_add_f32 v209, v209, v211\n\t" \
    "v_cvt_pk_bf16_f32 v208, v208, v209\n\t" \
    "v_smfmac_f32_16x16x64_bf16 v[244:247], v[240:243], v[184:191], v212\n\t" \
    "ds_write_b16 v213, v208 offset:0\n\t" \
    "ds_write_b16_d16_hi v213, v208 offset:16\n\t" \
    "v_smfmac_f32_16x16x64_bf16 v[248:251], v[228:231], v[128:135], v212\n\t" \
    "v_smfmac_f32_16x16x64_bf16 v[248:251], v[232:235], v[152:159], v212\n\t" \
    "v_smfmac_f32_16x16x64_bf16 v[248:251], v[236:239], v[168:175], v212\n\t" \
    "v_add_f32 v244, v244, v246\n\t" \
    "v_add_f32 v245, v245, v247\n\t" \
    "v_smfmac_f32_16x16x64_bf16 v[248:251], v[240:243], v[192:199], v212\n\t" \
    "s_nop 7\n\t" \
    "v_add_f32 v248, v248, v250\n\t" \
    "v_add_f32 v249, v249, v251\n\t" \
    "v_cvt_pk_bf16_f32 v244, v244, v248\n\t" \
    "v_cvt_pk_bf16_f32 v245, v245, v249\n\t" \
    "ds_write2_b32 v214, v244, v245 offset1:4\n\t" \
    "s_sleep 3\n\t" \
    "ds_read_b128 v[208:211], v254 offset:24576\n\t" \
    "ds_read_b128 v[224:227], v253 offset:0\n\t" \
    "ds_read_b128 v[228:231], v253 offset:20480\n\t" \
    "ds_read_b128 v[212:215], v254 offset:25600\n\t" \
    "ds_read_b128 v[232:235], v253 offset:1024\n\t" \
    "ds_read_b128 v[236:239], v253 offset:21504\n\t" \
    "ds_read_b128 v[216:219], v254 offset:26624\n\t" \
    "ds_read_b128 v[240:243], v253 offset:2048\n\t" \
    "ds_read_b128 v[244:247], v253 offset:22528\n\t" \
    "ds_read_b128 v[220:223], v254 offset:27648\n\t" \
    "ds_read_b128 v[248:251], v253 offset:3072\n\t" \
    "s_waitcnt lgkmcnt(0)\n\t" \
    "s_barrier \n\t" \
    LST4 \
    "s_add_u32 s40, s40, 1\n\t" \
    "s_cmp_lg_u32 s40, 64\n\t" \
    "s_cbranch_scc1 .Ls31_%=\n\t" \
    "s_branch .Lend%=\n\t" \
    ".Ls2_%=:\n\t" \
    "s_add_u32 m0, %[wm], 0x18000\n\t" \
    "s_mov_b32 s44, 0x0\n\t" \
    "buffer_load_dwordx4 v240, %[wr], s44 offen offset:0 nt lds\n\t" \
    "buffer_load_dwordx4 v241, %[wr], s44 offen offset:1024 nt lds\n\t" \
    "buffer_load_dwordx4 v242, %[wr], s44 offen offset:2048 nt lds\n\t" \
    "s_add_u32 m0, %[wm], 0x1e000\n\t" \
    "s_mov_b32 s44, 0x60000\n\t" \
    "buffer_load_dwordx4 v240, %[wr], s44 offen offset:0 nt lds\n\t" \
    "buffer_load_dwordx4 v241, %[wr], s44 offen offset:1024 nt lds\n\t" \
    "buffer_load_dwordx4 v242, %[wr], s44 offen offset:2048 nt lds\n\t" \
    "s_add_u32 m0, %[wm], 0x0\n\t" \
    "s_mov_b32 s44, 0xc0000\n\t" \
    "buffer_load_dwordx4 v240, %[wr], s44 offen offset:0 nt lds\n\t" \
    "buffer_load_dwordx4 v241, %[wr], s44 offen offset:1024 nt lds\n\t" \
    "buffer_load_dwordx4 v242, %[wr], s44 offen offset:2048 nt lds\n\t" \
    "s_add_u32 m0, %[wm], 0x6000\n\t" \
    "s_mov_b32 s44, 0x120000\n\t" \
    "buffer_load_dwordx4 v240, %[wr], s44 offen offset:0 nt lds\n\t" \
    "buffer_load_dwordx4 v241, %[wr], s44 offen offset:1024 nt lds\n\t" \
    "buffer_load_dwordx4 v242, %[wr], s44 offen offset:2048 nt lds\n\t" \
    "s_add_u32 m0, %[wm], 0xc000\n\t" \
    "s_mov_b32 s44, 0x180000\n\t" \
    "buffer_load_dwordx4 v240, %[wr], s44 offen offset:0 nt lds\n\t" \
    "buffer_load_dwordx4 v241, %[wr], s44 offen offset:1024 nt lds\n\t" \
    "buffer_load_dwordx4 v242, %[wr], s44 offen offset:2048 nt lds\n\t" \
    "s_waitcnt lgkmcnt(0)\n\t" \
    "s_add_u32 s43, s48, s49\n\t" \
    "s_add_u32 s43, s43, s50\n\t" \
    "s_and_b32 s43, s43, 0x2000\n\t" \
    "s_xor_b32 s43, s43, %[lx]\n\t" \
    "s_add_u32 s60, s60, s43\n\t" \
    "s_addc_u32 s61, s61, 0\n\t" \
    "v_add_u32 v244, 0x18000, v243\n\t" \
    "s_waitcnt vmcnt(12) lgkmcnt(0)\n\t" \
    "s_barrier \n\t" \
    "s_add_u32 m0, %[wm], 0x12000\n\t" \
    "s_mov_b32 s44, 0x1e0000\n\t" \
    "buffer_load_dwordx4 v240, %[wr], s44 offen offset:0 nt lds\n\t" \
    "buffer_load_dwordx4 v241, %[wr], s44 offen offset:1024 nt lds\n\t" \
    "buffer_load_dwordx4 v242, %[wr], s44 offen offset:2048 nt lds\n\t" \
    "ds_read_b64_tr_b16 v[208:209], v244 offset:0\n\t" \
    "ds_read_b64_tr_b16 v[210:211], v244 offset:1536\n\t" \
    "ds_read_b64_tr_b16 v[40:41], v244 offset:32\n\t" \
    "ds_read_b64_tr_b16 v[42:43], v244 offset:1568\n\t" \
    "ds_read_b64_tr_b16 v[48:49], v244 offset:64\n\t" \
    "ds_read_b64_tr_b16 v[50:51], v244 offset:1600\n\t" \
    "v_add_u32 v244, 0x1e000, v243\n\t" \
    "s_waitcnt vmcnt(12) lgkmcnt(0)\n\t" \
    "s_barrier \n\t" \
    "s_add_u32 m0, %[wm], 0x18000\n\t" \
    "s_mov_b32 s44, 0x240000\n\t" \
    "buffer_load_dwordx4 v240, %[wr], s44 offen offset:0 nt lds\n\t" \
    "buffer_load_dwordx4 v241, %[wr], s44 offen offset:1024 nt lds\n\t" \
    "buffer_load_dwordx4 v242, %[wr], s44 offen offset:2048 nt lds\n\t" \
    "ds_read_b64_tr_b16 v[212:213], v244 offset:0\n\t" \
    "ds_read_b64_tr_b16 v[214:215], v244 offset:1536\n\t" \
    "ds_read_b64_tr_b16 v[44:45], v244 offset:32\n\t" \
    "ds_read_b64_tr_b16 v[46:47], v244 offset:1568\n\t" \
    "ds_read_b64_tr_b16 v[52:53], v244 offset:64\n\t" \
    "ds_read_b64_tr_b16 v[54:55], v244 offset:1600\n\t" \
    "v_add_u32 v244, 0x0, v243\n\t" \
    "s_waitcnt vmcnt(12) lgkmcnt(0)\n\t" \
    "s_barrier \n\t" \
    "s_add_u32 m0, %[wm], 0x1e000\n\t" \
    "s_mov_b32 s44, 0x2a0000\n\t" \
    "buffer_load_dwordx4 v240, %[wr], s44 offen offset:0 nt lds\n\t" \
    "buffer_load_dwordx4 v241, %[wr], s44 offen offset:1024 nt lds\n\t" \
    "buffer_load_dwordx4 v242, %[wr], s44 offen offset:2048 nt lds\n\t" \
    "ds_read_b64_tr_b16 v[56:57], v244 offset:0\n\t" \
    "ds_read_b64_tr_b16 v[58:59], v244 offset:1536\n\t" \
    "ds_read_b64_tr_b16 v[64:65], v244 offset:32\n\t" \
    "ds_read_b64_tr_b16 v[66:67], v244 offset:1568\n\t" \
    "ds_read_b64_tr_b16 v[72:73], v244 offset:64\n\t" \
    "ds_read_b64_tr_b16 v[74:75], v244 offset:1600\n\t" \
    "v_add_u32 v244, 0x6000, v243\n\t" \
    "s_waitcnt vmcnt(12) lgkmcnt(0)\n\t" \
    "s_barrier \n\t" \
    "s_add_u32 m0, %[wm], 0x0\n\t" \
    "s_mov_b32 s44, 0x300000\n\t" \
    "buffer_load_dwordx4 v240, %[wr], s44 offen offset:0 nt lds\n\t" \
    "buffer_load_dwordx4 v241, %[wr], s44 offen offset:1024 nt lds\n\t" \
    "buffer_load_dwordx4 v242, %[wr], s44 offen offset:2048 nt lds\n\t" \
    "ds_read_b64_tr_b16 v[60:61], v244 offset:0\n\t" \
    "ds_read_b64_tr_b16 v[62:63], v244 offset:1536\n\t" \
    "ds_read_b64_tr_b16 v[68:69], v244 offset:32\n\t" \
    "ds_read_b64_tr_b16 v[70:71], v244 offset:1568\n\t" \
    "ds_read_b64_tr_b16 v[76:77], v244 offset:64\n\t" \
    "ds_read_b64_tr_b16 v[78:79], v244 offset:1600\n\t" \
    "v_add_u32 v244, 0xc000, v243\n\t" \
    "s_waitcnt vmcnt(12) lgkmcnt(0)\n\t" \
    "s_barrier \n\t" \
    "s_add_u32 m0, %[wm], 0x6000\n\t" \
    "s_mov_b32 s44, 0x360000\n\t" \
    "buffer_load_dwordx4 v240, %[wr], s44 offen offset:0 nt lds\n\t" \
    "buffer_load_dwordx4 v241, %[wr], s44 offen offset:1024 nt lds\n\t" \
    "buffer_load_dwordx4 v242, %[wr], s44 offen offset:2048 nt lds\n\t" \
    "ds_read_b64_tr_b16 v[216:217], v244 offset:0\n\t" \
    "ds_read_b64_tr_b16 v[218:219], v244 offset:1536\n\t" \
    "ds_read_b64_tr_b16 v[80:81], v244 offset:32\n\t" \
    "ds_read_b64_tr_b16 v[82:83], v244 offset:1568\n\t" \
    "ds_read_b64_tr_b16 v[88:89], v244 offset:64\n\t" \
    "ds_read_b64_tr_b16 v[90:91], v244 offset:1600\n\t" \
    "v_add_u32 v244, 0x12000, v243\n\t" \
    "s_waitcnt vmcnt(12) lgkmcnt(0)\n\t" \
    "s_barrier \n\t" \
    "s_add_u32 m0, %[wm], 0xc000\n\t" \
    "s_mov_b32 s44, 0x3c0000\n\t" \
    "buffer_load_dwordx4 v240, %[wr], s44 offen offset:0 nt lds\n\t" \
    "buffer_load_dwordx4 v241, %[wr], s44 offen offset:1024 nt lds\n\t" \
    "buffer_load_dwordx4 v242, %[wr], s44 offen offset:2048 nt lds\n\t" \
    "ds_read_b64_tr_b16 v[220:221], v244 offset:0\n\t" \
    "ds_read_b64_tr_b16 v[222:223], v244 offset:1536\n\t" \
    "ds_read_b64_tr_b16 v[84:85], v244 offset:32\n\t" \
    "ds_read_b64_tr_b16 v[86:87], v244 offset:1568\n\t" \
    "ds_read_b64_tr_b16 v[92:93], v244 offset:64\n\t" \
    "ds_read_b64_tr_b16 v[94:95], v244 offset:1600\n\t" \
    "v_add_u32 v244, 0x18000, v243\n\t" \
    "s_waitcnt vmcnt(12) lgkmcnt(0)\n\t" \
    "s_barrier \n\t" \
    "s_add_u32 m0, %[wm], 0x12000\n\t" \
    "s_mov_b32 s44, 0x420000\n\t" \
    "buffer_load_dwordx4 v240, %[wr], s44 offen offset:0 nt lds\n\t" \
    "buffer_load_dwordx4 v241, %[wr], s44 offen offset:1024 nt lds\n\t" \
    "buffer_load_dwordx4 v242, %[wr], s44 offen offset:2048 nt lds\n\t" \
    "ds_read_b64_tr_b16 v[96:97], v244 offset:0\n\t" \
    "ds_read_b64_tr_b16 v[98:99], v244 offset:1536\n\t" \
    "ds_read_b64_tr_b16 v[104:105], v244 offset:32\n\t" \
    "ds_read_b64_tr_b16 v[106:107], v244 offset:1568\n\t" \
    "ds_read_b64_tr_b16 v[112:113], v244 offset:64\n\t" \
    "ds_read_b64_tr_b16 v[114:115], v244 offset:1600\n\t" \
    "v_add_u32 v244, 0x1e000, v243\n\t" \
    "s_waitcnt vmcnt(12) lgkmcnt(0)\n\t" \
    "s_barrier \n\t" \
    "s_add_u32 m0, %[wm], 0x18000\n\t" \
    "s_mov_b32 s44, 0x480000\n\t" \
    "buffer_load_dwordx4 v240, %[wr], s44 offen offset:0 nt lds\n\t" \
    "buffer_load_dwordx4 v241, %[wr], s44 offen offset:1024 nt lds\n\t" \
    "buffer_load_dwordx4 v242, %[wr], s44 offen offset:2048 nt lds\n\t" \
    "ds_read_b64_tr_b16 v[100:101], v244 offset:0\n\t" \
    "ds_read_b64_tr_b16 v[102:103], v244 offset:1536\n\t" \
    "ds_read_b64_tr_b16 v[108:109], v244 offset:32\n\t" \
    "ds_read_b64_tr_b16 v[110:111], v244 offset:1568\n\t" \
    "ds_read_b64_tr_b16 v[116:117], v244 offset:64\n\t" \
    "ds_read_b64_tr_b16 v[118:119], v244 offset:1600\n\t" \
    "v_add_u32 v244, 0x0, v243\n\t" \
    "s_waitcnt vmcnt(12) lgkmcnt(0)\n\t" \
    "s_barrier \n\t" \
    "s_add_u32 m0, %[wm], 0x1e000\n\t" \
    "s_mov_b32 s44, 0x4e0000\n\t" \
    "buffer_load_dwordx4 v240, %[wr], s44 offen offset:0 nt lds\n\t" \
    "buffer_load_dwordx4 v241, %[wr], s44 offen offset:1024 nt lds\n\t" \
    "buffer_load_dwordx4 v242, %[wr], s44 offen offset:2048 nt lds\n\t" \
    "ds_read_b64_tr_b16 v[200:201], v244 offset:0\n\t" \
    "ds_read_b64_tr_b16 v[202:203], v244 offset:1536\n\t" \
    "ds_read_b64_tr_b16 v[0:1], v244 offset:32\n\t" \
    "ds_read_b64_tr_b16 v[2:3], v244 offset:1568\n\t" \
    "ds_read_b64_tr_b16 v[8:9], v244 offset:64\n\t" \
    "ds_read_b64_tr_b16 v[10:11], v244 offset:1600\n\t" \
    "v_add_u32 v244, 0x6000, v243\n\t" \
    "s_waitcnt vmcnt(12) lgkmcnt(0)\n\t" \
    "s_barrier \n\t" \
    "s_add_u32 m0, %[wm], 0x0\n\t" \
    "s_mov_b32 s44, 0x540000\n\t" \
    "buffer_load_dwordx4 v240, %[wr], s44 offen offset:0 nt lds\n\t" \
    "buffer_load_dwordx4 v241, %[wr], s44 offen offset:1024 nt lds\n\t" \
    "buffer_load_dwordx4 v242, %[wr], s44 offen offset:2048 nt lds\n\t" \
    "ds_read_b64_tr_b16 v[204:205], v244 offset:0\n\t" \
    "ds_read_b64_tr_b16 v[206:207], v244 offset:1536\n\t" \
    "ds_read_b64_tr_b16 v[4:5], v244 offset:32\n\t" \
    "ds_read_b64_tr_b16 v[6:7], v244 offset:1568\n\t" \
    "ds_read_b64_tr_b16 v[12:13], v244 offset:64\n\t" \
    "ds_read_b64_tr_b16 v[14:15], v244 offset:1600\n\t" \
    "v_add_u32 v244, 0xc000, v243\n\t" \
    "s_waitcnt vmcnt(12) lgkmcnt(0)\n\t" \
    "s_barrier \n\t" \
    "s_add_u32 m0, %[wm], 0x6000\n\t" \
    "s_mov_b32 s44, 0x5a0000\n\t" \
    "buffer_load_dwordx4 v240, %[wr], s44 offen offset:0 nt lds\n\t" \
    "buffer_load_dwordx4 v241, %[wr], s44 offen offset:1024 nt lds\n\t" \
    "buffer_load_dwordx4 v242, %[wr], s44 offen offset:2048 nt lds\n\t" \
    "ds_read_b64_tr_b16 v[224:225], v244 offset:0\n\t" \
    "ds_read_b64_tr_b16 v[226:227], v244 offset:1536\n\t" \
    "ds_read_b64_tr_b16 v[120:121], v244 offset:32\n\t" \
    "ds_read_b64_tr_b16 v[122:123], v244 offset:1568\n\t" \
    "ds_read_b64_tr_b16 v[128:129], v244 offset:64\n\t" \
    "ds_read_b64_tr_b16 v[130:131], v244 offset:1600\n\t" \
    "v_add_u32 v244, 0x12000, v243\n\t" \
    "s_waitcnt vmcnt(12) lgkmcnt(0)\n\t" \
    "s_barrier \n\t" \
    "s_add_u32 m0, %[wm], 0xc000\n\t" \
    "s_mov_b32 s44, 0x600000\n\t" \
    "buffer_load_dwordx4 v240, %[wr], s44 offen offset:0 nt lds\n\t" \
    "buffer_load_dwordx4 v241, %[wr], s44 offen offset:1024 nt lds\n\t" \
    "buffer_load_dwordx4 v242, %[wr], s44 offen offset:2048 nt lds\n\t" \
    "ds_read_b64_tr_b16 v[228:229], v244 offset:0\n\t" \
    "ds_read_b64_tr_b16 v[230:231], v244 offset:1536\n\t" \
    "ds_read_b64_tr_b16 v[124:125], v244 offset:32\n\t" \
    "ds_read_b64_tr_b16 v[126:127], v244 offset:1568\n\t" \
    "ds_read_b64_tr_b16 v[132:133], v244 offset:64\n\t" \
    "ds_read_b64_tr_b16 v[134:135], v244 offset:1600\n\t" \
    "v_add_u32 v244, 0x18000, v243\n\t" \
    "s_waitcnt vmcnt(12) lgkmcnt(0)\n\t" \
    "s_barrier \n\t" \
    "s_add_u32 m0, %[wm], 0x12000\n\t" \
    "s_mov_b32 s44, 0x660000\n\t" \
    "buffer_load_dwordx4 v240, %[wr], s44 offen offset:0 nt lds\n\t" \
    "buffer_load_dwordx4 v241, %[wr], s44 offen offset:1024 nt lds\n\t" \
    "buffer_load_dwordx4 v242, %[wr], s44 offen offset:2048 nt lds\n\t" \
    "ds_read_b64_tr_b16 v[136:137], v244 offset:0\n\t" \
    "ds_read_b64_tr_b16 v[138:139], v244 offset:1536\n\t" \
    "ds_read_b64_tr_b16 v[144:145], v244 offset:32\n\t" \
    "ds_read_b64_tr_b16 v[146:147], v244 offset:1568\n\t" \
    "ds_read_b64_tr_b16 v[152:153], v244 offset:64\n\t" \
    "ds_read_b64_tr_b16 v[154:155], v244 offset:1600\n\t" \
    "v_add_u32 v244, 0x1e000, v243\n\t" \
    "s_waitcnt vmcnt(12) lgkmcnt(0)\n\t" \
    "s_barrier \n\t" \
    "s_add_u32 m0, %[wm], 0x18000\n\t" \
    "s_mov_b32 s44, 0x6c0000\n\t" \
    "buffer_load_dwordx4 v240, %[wr], s44 offen offset:0 nt lds\n\t" \
    "buffer_load_dwordx4 v241, %[wr], s44 offen offset:1024 nt lds\n\t" \
    "buffer_load_dwordx4 v242, %[wr], s44 offen offset:2048 nt lds\n\t" \
    "ds_read_b64_tr_b16 v[140:141], v244 offset:0\n\t" \
    "ds_read_b64_tr_b16 v[142:143], v244 offset:1536\n\t" \
    "ds_read_b64_tr_b16 v[148:149], v244 offset:32\n\t" \
    "ds_read_b64_tr_b16 v[150:151], v244 offset:1568\n\t" \
    "ds_read_b64_tr_b16 v[156:157], v244 offset:64\n\t" \
    "ds_read_b64_tr_b16 v[158:159], v244 offset:1600\n\t" \
    "v_add_u32 v244, 0x0, v243\n\t" \
    "s_waitcnt vmcnt(12) lgkmcnt(0)\n\t" \
    "s_barrier \n\t" \
    "s_add_u32 m0, %[wm], 0x1e000\n\t" \
    "s_mov_b32 s44, 0x720000\n\t" \
    "buffer_load_dwordx4 v240, %[wr], s44 offen offset:0 nt lds\n\t" \
    "buffer_load_dwordx4 v241, %[wr], s44 offen offset:1024 nt lds\n\t" \
    "buffer_load_dwordx4 v242, %[wr], s44 offen offset:2048 nt lds\n\t" \
    "ds_read_b64_tr_b16 v[16:17], v244 offset:0\n\t" \
    "ds_read_b64_tr_b16 v[18:19], v244 offset:1536\n\t" \
    "ds_read_b64_tr_b16 v[24:25], v244 offset:32\n\t" \
    "ds_read_b64_tr_b16 v[26:27], v244 offset:1568\n\t" \
    "ds_read_b64_tr_b16 v[32:33], v244 offset:64\n\t" \
    "ds_read_b64_tr_b16 v[34:35], v244 offset:1600\n\t" \
    "v_add_u32 v244, 0x6000, v243\n\t" \
    "s_waitcnt vmcnt(12) lgkmcnt(0)\n\t" \
    "s_barrier \n\t" \
    "ds_read_b64_tr_b16 v[20:21], v244 offset:0\n\t" \
    "ds_read_b64_tr_b16 v[22:23], v244 offset:1536\n\t" \
    "ds_read_b64_tr_b16 v[28:29], v244 offset:32\n\t" \
    "ds_read_b64_tr_b16 v[30:31], v244 offset:1568\n\t" \
    "ds_read_b64_tr_b16 v[36:37], v244 offset:64\n\t" \
    "ds_read_b64_tr_b16 v[38:39], v244 offset:1600\n\t" \
    "v_add_u32 v244, 0xc000, v243\n\t" \
    "s_waitcnt vmcnt(9) lgkmcnt(0)\n\t" \
    "s_barrier \n\t" \
    "ds_read_b64_tr_b16 v[232:233], v244 offset:0\n\t" \
    "ds_read_b64_tr_b16 v[234:235], v244 offset:1536\n\t" \
    "ds_read_b64_tr_b16 v[160:161], v244 offset:32\n\t" \
    "ds_read_b64_tr_b16 v[162:163], v244 offset:1568\n\t" \
    "ds_read_b64_tr_b16 v[168:169], v244 offset:64\n\t" \
    "ds_read_b64_tr_b16 v[170:171], v244 offset:1600\n\t" \
    "v_add_u32 v244, 0x12000, v243\n\t" \
    "s_waitcnt vmcnt(6) lgkmcnt(0)\n\t" \
    "s_barrier \n\t" \
    "ds_read_b64_tr_b16 v[236:237], v244 offset:0\n\t" \
    "ds_read_b64_tr_b16 v[238:239], v244 offset:1536\n\t" \
    "ds_read_b64_tr_b16 v[164:165], v244 offset:32\n\t" \
    "ds_read_b64_tr_b16 v[166:167], v244 offset:1568\n\t" \
    "ds_read_b64_tr_b16 v[172:173], v244 offset:64\n\t" \
    "ds_read_b64_tr_b16 v[174:175], v244 offset:1600\n\t" \
    "v_add_u32 v244, 0x18000, v243\n\t" \
    "s_waitcnt vmcnt(3) lgkmcnt(0)\n\t" \
    "s_barrier \n\t" \
    "ds_read_b64_tr_b16 v[176:177], v244 offset:0\n\t" \
    "ds_read_b64_tr_b16 v[178:179], v244 offset:1536\n\t" \
    "ds_read_b64_tr_b16 v[184:185], v244 offset:32\n\t" \
    "ds_read_b64_tr_b16 v[186:187], v244 offset:1568\n\t" \
    "ds_read_b64_tr_b16 v[192:193], v244 offset:64\n\t" \
    "ds_read_b64_tr_b16 v[194:195], v244 offset:1600\n\t" \
    "v_add_u32 v244, 0x1e000, v243\n\t" \
    "s_waitcnt vmcnt(0) lgkmcnt(0)\n\t" \
    "s_barrier \n\t" \
    "ds_read_b64_tr_b16 v[180:181], v244 offset:0\n\t" \
    "ds_read_b64_tr_b16 v[182:183], v244 offset:1536\n\t" \
    "ds_read_b64_tr_b16 v[188:189], v244 offset:32\n\t" \
    "ds_read_b64_tr_b16 v[190:191], v244 offset:1568\n\t" \
    "ds_read_b64_tr_b16 v[196:197], v244 offset:64\n\t" \
    "ds_read_b64_tr_b16 v[198:199], v244 offset:1600\n\t" \
    "s_waitcnt lgkmcnt(0)\n\t" \
    "s_barrier \n\t" \
    "ds_write_b128 v254, v[200:203] offset:51200\n\t" \
    "ds_write_b128 v254, v[204:207] offset:52224\n\t" \
    "ds_write_b128 v254, v[208:211] offset:53248\n\t" \
    "ds_write_b128 v254, v[212:215] offset:54272\n\t" \
    "ds_write_b128 v254, v[216:219] offset:55296\n\t" \
    "ds_write_b128 v254, v[220:223] offset:56320\n\t" \
    "ds_write_b128 v254, v[224:227] offset:57344\n\t" \
    "ds_write_b128 v254, v[228:231] offset:58368\n\t" \
    "ds_write_b128 v254, v[232:235] offset:59392\n\t" \
    "ds_write_b128 v254, v[236:239] offset:60416\n\t" \
    "s_waitcnt vmcnt(0) lgkmcnt(0)\n\t" \
    "s_barrier \n\t" \
    "ds_read_b128 v[224:227], v253 offset:0\n\t" \
    "ds_read_b128 v[244:247], v254 offset:53248\n\t" \
    "ds_read_b128 v[228:231], v253 offset:1024\n\t" \
    "ds_read_b128 v[232:235], v253 offset:20480\n\t" \
    "ds_read_b128 v[236:239], v253 offset:21504\n\t" \
    "ds_read_b128 v[248:251], v254 offset:54272\n\t" \
    ".Ls21_%=:\n\t" \
    "s_waitcnt lgkmcnt(5)\n\t" \
    "v_mfma_f32_16x16x32_bf16 v[204:207], v[40:43], v[224:227], 0\n\t" \
    "s_waitcnt lgkmcnt(3)\n\t" \
    "v_mfma_f32_16x16x32_bf16 v[204:207], v[44:47], v[228:231], v[204:207]\n\t" \
    "ds_read_b128 v[240:243], v253 offset:2048\n\t" \
    "v_mfma_f32_16x16x32_bf16 v[208:211], v[48:51], v[224:227], 0\n\t" \
    "v_mfma_f32_16x16x32_bf16 v[208:211], v[52:55], v[228:231], v[208:211]\n\t" \
    "v_mfma_f32_16x16x32_bf16 v[200:203], v[244:247], v[224:227], 0\n\t" \
    "s_waitcnt lgkmcnt(1)\n\t" \
    "v_mfma_f32_16x16x32_bf16 v[200:203], v[248:251], v[228:231], v[200:203]\n\t" \
    "v_mfma_f32_16x16x32_bf16 v[216:219], v[40:43], v[232:235], 0\n\t" \
    "ds_read_b128 v[224:227], v253 offset:3072\n\t" \
    "v_mfma_f32_16x16x32_bf16 v[216:219], v[44:47], v[236:239], v[216:219]\n\t" \
    "ds_read_b128 v[228:231], v253 offset:22528\n\t" \
    "v_mfma_f32_16x16x32_bf16 v[220:223], v[48:51], v[232:235], 0\n\t" \
    "v_mfma_f32_16x16x32_bf16 v[220:223], v[52:55], v[236:239], v[220:223]\n\t" \
    "v_mfma_f32_16x16x32_bf16 v[212:215], v[244:247], v[232:235], 0\n\t" \
    "ds_read_b128 v[244:247], v254 offset:55296\n\t" \
    "v_mfma_f32_16x16x32_bf16 v[212:215], v[248:251], v[236:239], v[212:215]\n\t" \
    "ds_read_b128 v[248:251], v254 offset:56320\n\t" \
    "s_waitcnt lgkmcnt(4)\n\t" \
    "v_mfma_f32_16x16x32_bf16 v[204:207], v[64:67], v[240:243], v[204:207]\n\t" \
    "ds_read_b128 v[232:235], v253 offset:23552\n\t" \
    "s_waitcnt lgkmcnt(4)\n\t" \
    "v_mfma_f32_16x16x32_bf16 v[204:207], v[68:71], v[224:227], v[204:207]\n\t" \
    "ds_read_b128 v[236:239], v253 offset:4096\n\t" \
    "v_mfma_f32_16x16x32_bf16 v[208:211], v[72:75], v[240:243], v[208:211]\n\t" \
    "v_mfma_f32_16x16x32_bf16 v[208:211], v[76:79], v[224:227], v[208:211]\n\t" \
    "v_mfma_f32_16x16x32_bf16 v[200:203], v[56:59], v[240:243], v[200:203]\n\t" \
    "v_mfma_f32_16x16x32_bf16 v[200:203], v[60:63], v[224:227], v[200:203]\n\t" \
    "s_waitcnt lgkmcnt(4)\n\t" \
    "v_mfma_f32_16x16x32_bf16 v[216:219], v[64:67], v[228:231], v[216:219]\n\t" \
    "ds_read_b128 v[240:243], v253 offset:5120\n\t" \
    "s_waitcnt lgkmcnt(2)\n\t" \
    "v_mfma_f32_16x16x32_bf16 v[216:219], v[68:71], v[232:235], v[216:219]\n\t" \
    "ds_read_b128 v[224:227], v253 offset:24576\n\t" \
    "v_mfma_f32_16x16x32_bf16 v[220:223], v[72:75], v[228:231], v[220:223]\n\t" \
    "v_mfma_f32_16x16x32_bf16 v[220:223], v[76:79], v[232:235], v[220:223]\n\t" \
    "v_mfma_f32_16x16x32_bf16 v[212:215], v[56:59], v[228:231], v[212:215]\n\t" \
    "v_mfma_f32_16x16x32_bf16 v[212:215], v[60:63], v[232:235], v[212:215]\n\t" \
    "s_waitcnt lgkmcnt(2)\n\t" \
    "v_mfma_f32_16x16x32_bf16 v[204:207], v[80:83], v[236:239], v[204:207]\n\t" \
    "ds_read_b128 v[228:231], v253 offset:25600\n\t" \
    "s_waitcnt lgkmcnt(2)\n\t" \
    "v_mfma_f32_16x16x32_bf16 v[204:207], v[84:87], v[240:243], v[204:207]\n\t" \
    "ds_read_b128 v[232:235], v253 offset:6144\n\t" \
    "v_mfma_f32_16x16x32_bf16 v[208:211], v[88:91], v[236:239], v[208:211]\n\t" \
    "v_mfma_f32_16x16x32_bf16 v[208:211], v[92:95], v[240:243], v[208:211]\n\t" \
    "v_mfma_f32_16x16x32_bf16 v[200:203], v[244:247], v[236:239], v[200:203]\n\t" \
    "v_mfma_f32_16x16x32_bf16 v[200:203], v[248:251], v[240:243], v[200:203]\n\t" \
    "s_waitcnt lgkmcnt(2)\n\t" \
    "v_mfma_f32_16x16x32_bf16 v[216:219], v[80:83], v[224:227], v[216:219]\n\t" \
    "ds_read_b128 v[236:239], v253 offset:7168\n\t" \
    "s_waitcnt lgkmcnt(2)\n\t" \
    "v_mfma_f32_16x16x32_bf16 v[216:219], v[84:87], v[228:231], v[216:219]\n\t" \
    "ds_read_b128 v[240:243], v253 offset:26624\n\t" \
    "v_mfma_f32_16x16x32_bf16 v[220:223], v[88:91], v[224:227], v[220:223]\n\t" \
    "v_mfma_f32_16x16x32_bf16 v[220:223], v[92:95], v[228:231], v[220:223]\n\t" \
    "v_mfma_f32_16x16x32_bf16 v[212:215], v[244:247], v[224:227], v[212:215]\n\t" \
    "ds_read_b128 v[244:247], v254 offset:51200\n\t" \
    "v_mfma_f32_16x16x32_bf16 v[212:215], v[248:251], v[228:231], v[212:215]\n\t" \
    "ds_read_b128 v[248:251], v254 offset:52224\n\t" \
    "s_waitcnt lgkmcnt(4)\n\t" \
    "v_mfma_f32_16x16x32_bf16 v[204:207], v[104:107], v[232:235], v[204:207]\n\t" \
    "ds_read_b128 v[224:227], v253 offset:27648\n\t" \
    "s_waitcnt lgkmcnt(4)\n\t" \
    "v_mfma_f32_16x16x32_bf16 v[204:207], v[108:111], v[236:239], v[204:207]\n\t" \
    "ds_read_b128 v[228:231], v253 offset:8192\n\t" \
    "v_mfma_f32_16x16x32_bf16 v[208:211], v[112:115], v[232:235], v[208:211]\n\t" \
    "v_mfma_f32_16x16x32_bf16 v[208:211], v[116:119], v[236:239], v[208:211]\n\t" \
    "v_mfma_f32_16x16x32_bf16 v[200:203], v[96:99], v[232:235], v[200:203]\n\t" \
    "v_mfma_f32_16x16x32_bf16 v[200:203], v[100:103], v[236:239], v[200:203]\n\t" \
    "s_waitcnt lgkmcnt(4)\n\t" \
    "v_mfma_f32_16x16x32_bf16 v[216:219], v[104:107], v[240:243], v[216:219]\n\t" \
    "ds_read_b128 v[232:235], v253 offset:9216\n\t" \
    "s_waitcnt lgkmcnt(2)\n\t" \
    "v_mfma_f32_16x16x32_bf16 v[216:219], v[108:111], v[224:227], v[216:219]\n\t" \
    "ds_read_b128 v[236:239], v253 offset:28672\n\t" \
    "v_mfma_f32_16x16x32_bf16 v[220:223], v[112:115], v[240:243], v[220:223]\n\t" \
    "v_mfma_f32_16x16x32_bf16 v[220:223], v[116:119], v[224:227], v[220:223]\n\t" \
    "v_mfma_f32_16x16x32_bf16 v[212:215], v[96:99], v[240:243], v[212:215]\n\t" \
    "v_mfma_f32_16x16x32_bf16 v[212:215], v[100:103], v[224:227], v[212:215]\n\t" \
    "s_waitcnt lgkmcnt(2)\n\t" \
    "v_mfma_f32_16x16x32_bf16 v[204:207], v[0:3], v[228:231], v[204:207]\n\t" \
    "ds_read_b128 v[240:243], v253 offset:29696\n\t" \
    "s_waitcnt lgkmcnt(2)\n\t" \
    "v_mfma_f32_16x16x32_bf16 v[204:207], v[4:7], v[232:235], v[204:207]\n\t" \
    "ds_read_b128 v[224:227], v253 offset:10240\n\t" \
    "v_mfma_f32_16x16x32_bf16 v[208:211], v[8:11], v[228:231], v[208:211]\n\t" \
    "v_mfma_f32_16x16x32_bf16 v[208:211], v[12:15], v[232:235], v[208:211]\n\t" \
    "v_mfma_f32_16x16x32_bf16 v[200:203], v[244:247], v[228:231], v[200:203]\n\t" \
    "v_mfma_f32_16x16x32_bf16 v[200:203], v[248:251], v[232:235], v[200:203]\n\t" \
    "s_waitcnt lgkmcnt(2)\n\t" \
    "v_mfma_f32_16x16x32_bf16 v[216:219], v[0:3], v[236:239], v[216:219]\n\t" \
    "ds_read_b128 v[228:231], v253 offset:11264\n\t" \
    "s_waitcnt lgkmcnt(2)\n\t" \
    "v_mfma_f32_16x16x32_bf16 v[216:219], v[4:7], v[240:243], v[216:219]\n\t" \
    "ds_read_b128 v[232:235], v253 offset:30720\n\t" \
    "v_mfma_f32_16x16x32_bf16 v[220:223], v[8:11], v[236:239], v[220:223]\n\t" \
    "v_mfma_f32_16x16x32_bf16 v[220:223], v[12:15], v[240:243], v[220:223]\n\t" \
    "v_mfma_f32_16x16x32_bf16 v[212:215], v[244:247], v[236:239], v[212:215]\n\t" \
    "ds_read_b128 v[244:247], v254 offset:57344\n\t" \
    "v_mfma_f32_16x16x32_bf16 v[212:215], v[248:251], v[240:243], v[212:215]\n\t" \
    "ds_read_b128 v[248:251], v254 offset:58368\n\t" \
    "ds_read_b128 v[236:239], v253 offset:31744\n\t" \
    "s_waitcnt lgkmcnt(0)\n\t" \
    "s_barrier \n\t" \
    LST7 \
    "v_mfma_f32_16x16x32_bf16 v[204:207], v[120:123], v[224:227], v[204:207]\n\t" \
    "v_mfma_f32_16x16x32_bf16 v[204:207], v[124:127], v[228:231], v[204:207]\n\t" \
    "ds_read_b128 v[240:243], v253 offset:12288\n\t" \
    "v_mfma_f32_16x16x32_bf16 v[208:211], v[128:131], v[224:227], v[208:211]\n\t" \
    "v_mfma_f32_16x16x32_bf16 v[208:211], v[132:135], v[228:231], v[208:211]\n\t" \
    "v_mfma_f32_16x16x32_bf16 v[200:203], v[244:247], v[224:227], v[200:203]\n\t" \
    "v_mfma_f32_16x16x32_bf16 v[200:203], v[248:251], v[228:231], v[200:203]\n\t" \
    "v_mfma_f32_16x16x32_bf16 v[216:219], v[120:123], v[232:235], v[216:219]\n\t" \
    "ds_read_b128 v[224:227], v253 offset:13312\n\t" \
    "v_mfma_f32_16x16x32_bf16 v[216:219], v[124:127], v[236:239], v[216:219]\n\t" \
    "ds_read_b128 v[228:231], v253 offset:32768\n\t" \
    "v_mfma_f32_16x16x32_bf16 v[220:223], v[128:131], v[232:235], v[220:223]\n\t" \
    "v_mfma_f32_16x16x32_bf16 v[220:223], v[132:135], v[236:239], v[220:223]\n\t" \
    "v_mfma_f32_16x16x32_bf16 v[212:215], v[244:247], v[232:235], v[212:215]\n\t" \
    "ds_read_b128 v[244:247], v254 offset:59392\n\t" \
    "v_mfma_f32_16x16x32_bf16 v[212:215], v[248:251], v[236:239], v[212:215]\n\t" \
    "ds_read_b128 v[248:251], v254 offset:60416\n\t" \
    "s_waitcnt lgkmcnt(4)\n\t" \
    "v_mfma_f32_16x16x32_bf16 v[204:207], v[144:147], v[240:243], v[204:207]\n\t" \
    "ds_read_b128 v[232:235], v253 offset:33792\n\t" \
    "s_and_b32 s44, s40, 3\n\t" \
    "s_waitcnt lgkmcnt(4)\n\t" \
    "v_mfma_f32_16x16x32_bf16 v[204:207], v[148:151], v[224:227], v[204:207]\n\t" \
    "ds_read_b128 v[236:239], v253 offset:16384\n\t" \
    "s_lshl_b32 s44, s44, 17\n\t" \
    "v_mfma_f32_16x16x32_bf16 v[208:211], v[152:155], v[240:243], v[208:211]\n\t" \
    "s_add_u32 s44, s44, %[mb]\n\t" \
    "v_mfma_f32_16x16x32_bf16 v[208:211], v[156:159], v[224:227], v[208:211]\n\t" \
    "s_add_u32 s46, s44, 0x1000\n\t" \
    "v_mfma_f32_16x16x32_bf16 v[200:203], v[136:139], v[240:243], v[200:203]\n\t" \
    "s_add_u32 s47, s44, 0x1c00\n\t" \
    "v_mfma_f32_16x16x32_bf16 v[200:203], v[140:143], v[224:227], v[200:203]\n\t" \
    "s_waitcnt lgkmcnt(4)\n\t" \
    "v_mfma_f32_16x16x32_bf16 v[216:219], v[144:147], v[228:231], v[216:219]\n\t" \
    "ds_read_b128 v[240:243], v253 offset:17408\n\t" \
    "s_waitcnt lgkmcnt(2)\n\t" \
    "v_mfma_f32_16x16x32_bf16 v[216:219], v[148:151], v[232:235], v[216:219]\n\t" \
    "ds_read_b128 v[224:227], v253 offset:36864\n\t" \
    "v_mfma_f32_16x16x32_bf16 v[220:223], v[152:155], v[228:231], v[220:223]\n\t" \
    "v_mfma_f32_16x16x32_bf16 v[220:223], v[156:159], v[232:235], v[220:223]\n\t" \
    "v_mfma_f32_16x16x32_bf16 v[212:215], v[136:139], v[228:231], v[212:215]\n\t" \
    "v_mfma_f32_16x16x32_bf16 v[212:215], v[140:143], v[232:235], v[212:215]\n\t" \
    "s_waitcnt lgkmcnt(2)\n\t" \
    "v_mfma_f32_16x16x32_bf16 v[204:207], v[160:163], v[236:239], v[204:207]\n\t" \
    "ds_read_b128 v[228:231], v253 offset:37888\n\t" \
    "s_waitcnt lgkmcnt(2)\n\t" \
    "v_mfma_f32_16x16x32_bf16 v[204:207], v[164:167], v[240:243], v[204:207]\n\t" \
    "ds_read_b128 v[232:235], v253 offset:14336\n\t" \
    "v_mfma_f32_16x16x32_bf16 v[208:211], v[168:171], v[236:239], v[208:211]\n\t" \
    "v_mfma_f32_16x16x32_bf16 v[208:211], v[172:175], v[240:243], v[208:211]\n\t" \
    "v_mfma_f32_16x16x32_bf16 v[200:203], v[244:247], v[236:239], v[200:203]\n\t" \
    "v_mfma_f32_16x16x32_bf16 v[200:203], v[248:251], v[240:243], v[200:203]\n\t" \
    "s_waitcnt lgkmcnt(2)\n\t" \
    "v_mfma_f32_16x16x32_bf16 v[216:219], v[160:163], v[224:227], v[216:219]\n\t" \
    "ds_read_b128 v[236:239], v253 offset:15360\n\t" \
    "s_waitcnt lgkmcnt(2)\n\t" \
    "v_mfma_f32_16x16x32_bf16 v[216:219], v[164:167], v[228:231], v[216:219]\n\t" \
    "ds_read_b128 v[240:243], v253 offset:18432\n\t" \
    "v_mfma_f32_16x16x32_bf16 v[220:223], v[168:171], v[224:227], v[220:223]\n\t" \
    "v_mfma_f32_16x16x32_bf16 v[220:223], v[172:175], v[228:231], v[220:223]\n\t" \
    "v_mfma_f32_16x16x32_bf16 v[212:215], v[244:247], v[224:227], v[212:215]\n\t" \
    "v_mfma_f32_16x16x32_bf16 v[212:215], v[248:251], v[228:231], v[212:215]\n\t" \
    "s_waitcnt lgkmcnt(2)\n\t" \
    "v_mfma_f32_16x16x32_bf16 v[204:207], v[24:27], v[232:235], v[204:207]\n\t" \
    "ds_read_b128 v[224:227], v253 offset:19456\n\t" \
    "v_mov_b64 v[244:245], 0\n\t" \
    "s_waitcnt lgkmcnt(2)\n\t" \
    "v_mfma_f32_16x16x32_bf16 v[204:207], v[28:31], v[236:239], v[204:207]\n\t" \
    "ds_read_b128 v[228:231], v253 offset:34816\n\t" \
    "v_mov_b64 v[246:247], 0\n\t" \
    "v_mfma_f32_16x16x32_bf16 v[208:211], v[32:35], v[232:235], v[208:211]\n\t" \
    "v_mov_b64 v[248:249], 0\n\t" \
    "v_mfma_f32_16x16x32_bf16 v[208:211], v[36:39], v[236:239], v[208:211]\n\t" \
    "v_mov_b64 v[250:251], 0\n\t" \
    "v_mfma_f32_16x16x32_bf16 v[200:203], v[16:19], v[232:235], v[200:203]\n\t" \
    "v_mfma_f32_16x16x32_bf16 v[200:203], v[20:23], v[236:239], v[200:203]\n\t" \
    "s_waitcnt lgkmcnt(2)\n\t" \
    "v_mfma_f32_16x16x32_bf16 v[204:207], v[184:187], v[240:243], v[204:207]\n\t" \
    "ds_read_b128 v[232:235], v253 offset:35840\n\t" \
    "s_waitcnt lgkmcnt(2)\n\t" \
    "v_mfma_f32_16x16x32_bf16 v[204:207], v[188:191], v[224:227], v[204:207]\n\t" \
    "ds_read_b128 v[236:239], v253 offset:38912\n\t" \
    "v_mfma_f32_16x16x32_bf16 v[208:211], v[192:195], v[240:243], v[208:211]\n\t" \
    "v_mfma_f32_16x16x32_bf16 v[208:211], v[196:199], v[224:227], v[208:211]\n\t" \
    "v_mfma_f32_16x16x32_bf16 v[200:203], v[176:179], v[240:243], v[200:203]\n\t" \
    "v_mfma_f32_16x16x32_bf16 v[200:203], v[180:183], v[224:227], v[200:203]\n\t" \
    "ds_read_b128 v[240:243], v253 offset:39936\n\t" \
    "s_waitcnt lgkmcnt(3)\n\t" \
    "v_mfma_f32_16x16x32_bf16 v[216:219], v[24:27], v[228:231], v[216:219]\n\t" \
    "s_waitcnt lgkmcnt(2)\n\t" \
    "v_mfma_f32_16x16x32_bf16 v[216:219], v[28:31], v[232:235], v[216:219]\n\t" \
    "v_cvt_pk_bf16_f32 v204, v204, v208\n\t" \
    "v_cvt_pk_bf16_f32 v205, v205, v209\n\t" \
    "s_waitcnt lgkmcnt(1)\n\t" \
    "v_mfma_f32_16x16x32_bf16 v[216:219], v[184:187], v[236:239], v[216:219]\n\t" \
    "v_cvt_pk_bf16_f32 v206, v206, v210\n\t" \
    "v_cvt_pk_bf16_f32 v207, v207, v211\n\t" \
    "s_waitcnt lgkmcnt(0)\n\t" \
    "v_mfma_f32_16x16x32_bf16 v[216:219], v[188:191], v[240:243], v[216:219]\n\t" \
    "ds_write_b128 v254, v[204:207] offset:10240\n\t" \
    "v_cvt_pk_bf16_f32 v200, v200, v201\n\t" \
    "v_mfma_f32_16x16x32_bf16 v[220:223], v[32:35], v[228:231], v[220:223]\n\t" \
    "v_cvt_pk_bf16_f32 v201, v202, v203\n\t" \
    "ds_write_b64 v254, v[200:201] offset:6152\n\t" \
    "v_mfma_f32_16x16x32_bf16 v[220:223], v[36:39], v[232:235], v[220:223]\n\t" \
    "ds_read_b128 v[200:203], v254 offset:51200\n\t" \
    "ds_read_b128 v[204:207], v254 offset:52224\n\t" \
    "v_mfma_f32_16x16x32_bf16 v[220:223], v[192:195], v[236:239], v[220:223]\n\t" \
    "v_mov_b64 v[208:209], 0\n\t" \
    "v_mov_b64 v[210:211], 0\n\t" \
    "v_mfma_f32_16x16x32_bf16 v[220:223], v[196:199], v[240:243], v[220:223]\n\t" \
    "v_mfma_f32_16x16x32_bf16 v[212:215], v[16:19], v[228:231], v[212:215]\n\t" \
    "v_mfma_f32_16x16x32_bf16 v[212:215], v[20:23], v[232:235], v[212:215]\n\t" \
    "v_mfma_f32_16x16x32_bf16 v[212:215], v[176:179], v[236:239], v[212:215]\n\t" \
    "v_cvt_pk_bf16_f32 v216, v216, v220\n\t" \
    "v_cvt_pk_bf16_f32 v217, v217, v221\n\t" \
    "v_mfma_f32_16x16x32_bf16 v[212:215], v[180:183], v[240:243], v[212:215]\n\t" \
    "v_cvt_pk_bf16_f32 v218, v218, v222\n\t" \
    "v_cvt_pk_bf16_f32 v219, v219, v223\n\t" \
    "ds_write_b128 v254, v[216:219] offset:16384\n\t" \
    "ds_read_b128 v[216:219], v254 offset:53248\n\t" \
    "ds_read_b128 v[220:223], v254 offset:54272\n\t" \
    "s_nop 2\n\t" \
    "v_cvt_pk_bf16_f32 v212, v212, v213\n\t" \
    "v_cvt_pk_bf16_f32 v213, v214, v215\n\t" \
    "ds_write_b64 v254, v[212:213] offset:12296\n\t" \
    LST6 \
    "s_sleep 8\n\t" \
    "buffer_load_dwordx4 v[224:227], v253, s[60:63], s46 offen sc1\n\t" \
    "buffer_load_dwordx4 v[228:231], v253, s[60:63], s47 offen sc1\n\t" \
    "s_waitcnt lgkmcnt(0)\n\t" \
    LFLUSH212 \
    "v_bfe_i32 v212, v253, 5, 1\n\t" \
    "v_and_b32 v212, 0xaaaa, v212\n\t" \
    "v_or_b32 v212, 0x4444, v212\n\t" \
    "v_and_b32 v213, 0xffff, v255\n\t" \
    "v_lshrrev_b32 v214, 16, v255\n\t" \
    ".Ls22_%=:\n\t" \
    "s_waitcnt vmcnt(1)\n\t" \
    "v_max_u32 v215, v224, v225\n\t" \
    "v_max3_u32 v215, v226, v227, v215\n\t" \
    "s_waitcnt vmcnt(0)\n\t" \
    "v_max3_u32 v215, v228, v229, v215\n\t" \
    "v_max3_u32 v215, v230, v231, v215\n\t" \
    "v_cmp_eq_u32 vcc, -1, v215\n\t" \
    "s_cbranch_vccz .Ls23_%=\n\t" \
    "v_max_u32 v215, v224, v225\n\t" \
    "v_max3_u32 v215, v226, v227, v215\n\t" \
    "v_cmp_eq_u32 vcc, -1, v215\n\t" \
    "s_cbranch_vccz .Ls24_%=\n\t" \
    "buffer_load_dwordx4 v[224:227], v253, s[60:63], s46 offen sc1\n\t" \
    ".Ls24_%=:\n\t" \
    "v_max_u32 v215, v228, v229\n\t" \
    "v_max3_u32 v215, v230, v231, v215\n\t" \
    "v_cmp_eq_u32 vcc, -1, v215\n\t" \
    "s_cbranch_vccz .Ls25_%=\n\t" \
    "buffer_load_dwordx4 v[228:231], v253, s[60:63], s47 offen sc1\n\t" \
    ".Ls25_%=:\n\t" \
    "s_cmp_eq_u32 s41, 0\n\t" \
    "s_cbranch_scc1 .Ls23_%=\n\t" \
    "s_sub_u32 s41, s41, 1\n\t" \
    "s_waitcnt vmcnt(0)\n\t" \
    "s_branch .Ls22_%=\n\t" \
    ".Ls23_%=:\n\t" \
    LST2 \
    "ds_write_b128 v253, v[224:227] offset:45056\n\t" \
    "ds_write_b128 v253, v[228:231] offset:48128\n\t" \
    "v_smfmac_f32_16x16x64_bf16 v[244:247], v[224:227], v[0:7], v212\n\t" \
    "v_smfmac_f32_16x16x64_bf16 v[248:251], v[224:227], v[8:15], v212\n\t" \
    "v_smfmac_f32_16x16x64_bf16 v[208:211], v[224:227], v[200:207], v212\n\t" \
    "s_waitcnt lgkmcnt(0)\n\t" \
    "s_barrier \n\t" \
    LST3 \
    "ds_read_b128 v[232:235], v253 offset:40960\n\t" \
    "ds_read_b128 v[236:239], v253 offset:41984\n\t" \
    "ds_read_b128 v[240:243], v253 offset:43008\n\t" \
    "ds_read_b128 v[224:227], v253 offset:44032\n\t" \
    "ds_read_b128 v[200:203], v254 offset:55296\n\t" \
    "ds_read_b128 v[204:207], v254 offset:56320\n\t" \
    "v_smfmac_f32_16x16x64_bf16 v[208:211], v[228:231], v[16:23], v212\n\t" \
    "v_smfmac_f32_16x16x64_bf16 v[244:247], v[228:231], v[24:31], v212\n\t" \
    "v_smfmac_f32_16x16x64_bf16 v[248:251], v[228:231], v[32:39], v212\n\t" \
    "ds_read_b128 v[228:231], v253 offset:46080\n\t" \
    "s_waitcnt lgkmcnt(6)\n\t" \
    "v_smfmac_f32_16x16x64_bf16 v[208:211], v[232:235], v[216:223], v212\n\t" \
    "ds_read_b128 v[216:219], v254 offset:57344\n\t" \
    "ds_read_b128 v[220:223], v254 offset:58368\n\t" \
    "v_smfmac_f32_16x16x64_bf16 v[244:247], v[232:235], v[40:47], v212\n\t" \
    "v_smfmac_f32_16x16x64_bf16 v[248:251], v[232:235], v[48:55], v212\n\t" \
    "ds_read_b128 v[232:235], v253 offset:47104\n\t" \
    "s_waitcnt lgkmcnt(8)\n\t" \
    "v_smfmac_f32_16x16x64_bf16 v[208:211], v[236:239], v[56:63], v212\n\t" \
    "v_smfmac_f32_16x16x64_bf16 v[244:247], v[236:239], v[64:71], v212\n\t" \
    "v_smfmac_f32_16x16x64_bf16 v[248:251], v[236:239], v[72:79], v212\n\t" \
    "ds_read_b128 v[236:239], v253 offset:49152\n\t" \
    "s_waitcnt lgkmcnt(5)\n\t" \
    "v_smfmac_f32_16x16x64_bf16 v[208:211], v[240:243], v[200:207], v212\n\t" \
    "ds_read_b128 v[200:203], v254 offset:59392\n\t" \
    "ds_read_b128 v[204:207], v254 offset:60416\n\t" \
    "v_smfmac_f32_16x16x64_bf16 v[244:247], v[240:243], v[80:87], v212\n\t" \
    "v_smfmac_f32_16x16x64_bf16 v[248:251], v[240:243], v[88:95], v212\n\t" \
    "ds_read_b128 v[240:243], v253 offset:50176\n\t" \
    "v_smfmac_f32_16x16x64_bf16 v[208:211], v[224:227], v[96:103], v212\n\t" \
    "v_smfmac_f32_16x16x64_bf16 v[244:247], v[224:227], v[104:111], v212\n\t" \
    "v_smfmac_f32_16x16x64_bf16 v[248:251], v[224:227], v[112:119], v212\n\t" \
    "s_waitcnt lgkmcnt(5)\n\t" \
    "v_smfmac_f32_16x16x64_bf16 v[208:211], v[228:231], v[216:223], v212\n\t" \
    "s_waitcnt lgkmcnt(4)\n\t" \
    "v_smfmac_f32_16x16x64_bf16 v[208:211], v[232:235], v[136:143], v212\n\t" \
    "s_waitcnt lgkmcnt(1)\n\t" \
    "v_smfmac_f32_16x16x64_bf16 v[208:211], v[236:239], v[200:207], v212\n\t" \
    "s_waitcnt lgkmcnt(0)\n\t" \
    "v_smfmac_f32_16x16x64_bf16 v[208:211], v[240:243], v[176:183], v212\n\t" \
    "v_smfmac_f32_16x16x64_bf16 v[244:247], v[228:231], v[120:127], v212\n\t" \
    "v_smfmac_f32_16x16x64_bf16 v[244:247], v[232:235], v[144:151], v212\n\t" \
    "v_smfmac_f32_16x16x64_bf16 v[244:247], v[236:239], v[160:167], v212\n\t" \
    "v_add_f32 v208, v208, v210\n\t" \
    "v_add_f32 v209, v209, v211\n\t" \
    "v_cvt_pk_bf16_f32 v208, v208, v209\n\t" \
    "v_smfmac_f32_16x16x64_bf16 v[244:247], v[240:243], v[184:191], v212\n\t" \
    "ds_write_b16 v213, v208 offset:0\n\t" \
    "ds_write_b16_d16_hi v213, v208 offset:16\n\t" \
    "v_smfmac_f32_16x16x64_bf16 v[248:251], v[228:231], v[128:135], v212\n\t" \
    "v_smfmac_f32_16x16x64_bf16 v[248:251], v[232:235], v[152:159], v212\n\t" \
    "v_smfmac_f32_16x16x64_bf16 v[248:251], v[236:239], v[168:175], v212\n\t" \
    "v_add_f32 v244, v244, v246\n\t" \
    "v_add_f32 v245, v245, v247\n\t" \
    "v_smfmac_f32_16x16x64_bf16 v[248:251], v[240:243], v[192:199], v212\n\t" \
    "s_nop 7\n\t" \
    "v_add_f32 v248, v248, v250\n\t" \
    "v_add_f32 v249, v249, v251\n\t" \
    "v_cvt_pk_bf16_f32 v244, v244, v248\n\t" \
    "v_cvt_pk_bf16_f32 v245, v245, v249\n\t" \
    "ds_write2_b32 v214, v244, v245 offset1:4\n\t" \
    "s_sleep 3\n\t" \
    "ds_read_b128 v[200:203], v254 offset:12288\n\t" \
    "ds_read_b128 v[224:227], v254 offset:24576\n\t" \
    "ds_read_b128 v[204:207], v254 offset:13312\n\t" \
    "ds_read_b128 v[228:231], v254 offset:25600\n\t" \
    "ds_read_b128 v[208:211], v254 offset:14336\n\t" \
    "ds_read_b128 v[232:235], v254 offset:26624\n\t" \
    "ds_read_b128 v[212:215], v254 offset:15360\n\t" \
    "ds_read_b128 v[236:239], v254 offset:27648\n\t" \
    "ds_read_b128 v[216:219], v254 offset:16384\n\t" \
    "ds_read_b128 v[220:223], v254 offset:17408\n\t" \
    "s_and_b32 s44, s40, 3\n\t" \
    "s_lshl_b32 s44, s44, 17\n\t" \
    "s_add_u32 s50, s44, %[pb]\n\t" \
    "s_add_u32 s51, s44, %[pm]\n\t" \
    "s_mov_b32 s52, 0xf0000\n\t" \
    "s_mov_b32 s53, 0xfff00ff\n\t" \
    "s_mov_b32 s54, 0x1f0001\n\t" \
    "s_mov_b32 s55, 0x1fff01ff\n\t" \
    "s_mov_b32 s56, 0x3f0003\n\t" \
    "s_mov_b32 s57, 0x3fff03ff\n\t" \
    "s_mov_b32 s58, 0x7f0007\n\t" \
    "s_mov_b32 s59, 0x7fff07ff\n\t" \
    "s_waitcnt lgkmcnt(0)\n\t" \
    "s_barrier \n\t" \
    LST4 \
    "v_and_b32 v252, 0xffffff7f, v254\n\t" \
    "v_mfma_f32_16x16x32_bf16 v[240:243], v[224:227], v[200:203], 0\n\t" \
    "ds_read_b128 v[224:227], v254 offset:28672\n\t" \
    "v_mfma_f32_16x16x32_bf16 v[240:243], v[228:231], v[204:207], v[240:243]\n\t" \
    "ds_read_b128 v[228:231], v254 offset:29696\n\t" \
    "v_mfma_f32_16x16x32_bf16 v[240:243], v[232:235], v[208:211], v[240:243]\n\t" \
    "ds_read_b128 v[232:235], v254 offset:18432\n\t" \
    "v_mfma_f32_16x16x32_bf16 v[240:243], v[236:239], v[212:215], v[240:243]\n\t" \
    "ds_read_b128 v[236:239], v254 offset:19456\n\t" \
    "s_waitcnt lgkmcnt(3)\n\t" \
    "v_mfma_f32_16x16x32_bf16 v[240:243], v[224:227], v[216:219], v[240:243]\n\t" \
    "ds_read_b128 v[224:227], v254 offset:20480\n\t" \
    "s_waitcnt lgkmcnt(3)\n\t" \
    "v_mfma_f32_16x16x32_bf16 v[240:243], v[228:231], v[220:223], v[240:243]\n\t" \
    "ds_read_b128 v[228:231], v254 offset:21504\n\t" \
    "s_waitcnt lgkmcnt(3)\n\t" \
    "v_mfma_f32_16x16x32_bf16 v[244:247], v[232:235], v[200:203], 0\n\t" \
    "ds_read_b128 v[232:235], v254 offset:22528\n\t" \
    "s_waitcnt lgkmcnt(3)\n\t" \
    "v_mfma_f32_16x16x32_bf16 v[244:247], v[236:239], v[204:207], v[244:247]\n\t" \
    "ds_read_b128 v[236:239], v254 offset:23552\n\t" \
    "s_waitcnt lgkmcnt(3)\n\t" \
    "v_mfma_f32_16x16x32_bf16 v[244:247], v[224:227], v[208:211], v[244:247]\n\t" \
    "ds_read_b128 v[224:227], v252 offset:3072\n\t" \
    "s_waitcnt lgkmcnt(3)\n\t" \
    "v_mfma_f32_16x16x32_bf16 v[244:247], v[228:231], v[212:215], v[244:247]\n\t" \
    "ds_read_b128 v[228:231], v252 offset:3200\n\t" \
    "s_waitcnt lgkmcnt(3)\n\t" \
    "v_mfma_f32_16x16x32_bf16 v[244:247], v[232:235], v[216:219], v[244:247]\n\t" \
    "ds_read_b128 v[232:235], v252 offset:4096\n\t" \
    "s_waitcnt lgkmcnt(3)\n\t" \
    "v_mfma_f32_16x16x32_bf16 v[244:247], v[236:239], v[220:223], v[244:247]\n\t" \
    "ds_read_b128 v[236:239], v252 offset:4224\n\t" \
    "s_waitcnt lgkmcnt(3)\n\t" \
    "v_mfma_f32_16x16x32_bf16 v[248:251], v[224:227], v[200:203], 0\n\t" \
    "ds_read_b128 v[224:227], v252 offset:5120\n\t" \
    "s_waitcnt lgkmcnt(3)\n\t" \
    "v_mfma_f32_16x16x32_bf16 v[248:251], v[228:231], v[204:207], v[248:251]\n\t" \
    "ds_read_b128 v[228:231], v252 offset:5248\n\t" \
    "s_waitcnt lgkmcnt(3)\n\t" \
    "v_mfma_f32_16x16x32_bf16 v[248:251], v[232:235], v[208:211], v[248:251]\n\t" \
    "s_waitcnt lgkmcnt(2)\n\t" \
    "v_mfma_f32_16x16x32_bf16 v[248:251], v[236:239], v[212:215], v[248:251]\n\t" \
    "s_waitcnt lgkmcnt(1)\n\t" \
    "v_mfma_f32_16x16x32_bf16 v[248:251], v[224:227], v[216:219], v[248:251]\n\t" \
    "s_waitcnt lgkmcnt(0)\n\t" \
    "v_mfma_f32_16x16x32_bf16 v[248:251], v[228:231], v[220:223], v[248:251]\n\t" \
    "v_mov_b32 v226, 0xff7fffff\n\t" \
    "v_cndmask_b32 v240, v240, v226, s[52:53]\n\t" \
    "v_cndmask_b32 v241, v241, v226, s[54:55]\n\t" \
    "v_cndmask_b32 v242, v242, v226, s[56:57]\n\t" \
    "v_cndmask_b32 v243, v243, v226, s[58:59]\n\t" \
    "v_max3_f32 v224, v240, v241, v242\n\t" \
    "v_max3_f32 v225, v244, v245, v246\n\t" \
    "v_max3_f32 v224, v224, v243, v247\n\t" \
    "v_lshrrev_b32 v200, 5, v253\n\t" \
    "v_and_b32 v201, 0xf0, v253\n\t" \
    "v_and_or_b32 v200, v200, 8, v201\n\t" \
    "v_max3_f32 v224, v224, v248, v249\n\t" \
    "v_max3_f32 v225, v225, v250, v251\n\t" \
    "v_max_f32 v224, v224, v225\n\t" \
    "v_mov_b32 v225, v224\n\t" \
    "s_nop 1\n\t" \
    "v_permlane16_swap_b32 v224, v225\n\t" \
    "v_max_f32 v224, v224, v225\n\t" \
    "v_mov_b32 v225, v224\n\t" \
    "s_nop 1\n\t" \
    "v_permlane32_swap_b32 v224, v225\n\t" \
    "v_max_f32 v224, v224, v225\n\t" \
    "v_mul_f32 v224, %[scl], v224\n\t" \
    "v_fma_f32 v240, v240, %[scl], -v224\n\t" \
    "v_fma_f32 v241, v241, %[scl], -v224\n\t" \
    "v_exp_f32 v240, v240\n\t" \
    "v_fma_f32 v242, v242, %[scl], -v224\n\t" \
    "v_exp_f32 v241, v241\n\t" \
    "v_fma_f32 v243, v243, %[scl], -v224\n\t" \
    "v_exp_f32 v242, v242\n\t" \
    "v_fma_f32 v244, v244, %[scl], -v224\n\t" \
    "v_exp_f32 v243, v243\n\t" \
    "v_fma_f32 v245, v245, %[scl], -v224\n\t" \
    "v_exp_f32 v244, v244\n\t" \
    "v_fma_f32 v246, v246, %[scl], -v224\n\t" \
    "v_exp_f32 v245, v245\n\t" \
    "v_fma_f32 v247, v247, %[scl], -v224\n\t" \
    "v_exp_f32 v246, v246\n\t" \
    "v_fma_f32 v248, v248, %[scl], -v224\n\t" \
    "v_exp_f32 v247, v247\n\t" \
    "v_fma_f32 v249, v249, %[scl], -v224\n\t" \
    "v_exp_f32 v248, v248\n\t" \
    "v_fma_f32 v250, v250, %[scl], -v224\n\t" \
    "v_exp_f32 v249, v249\n\t" \
    "v_fma_f32 v251, v251, %[scl], -v224\n\t" \
    "v_exp_f32 v250, v250\n\t" \
    "v_exp_f32 v251, v251\n\t" \
    "v_add_f32 v224, v240, v241\n\t" \
    "v_add_f32 v225, v242, v243\n\t" \
    "v_add_f32 v226, v244, v245\n\t" \
    "v_add_f32 v227, v246, v247\n\t" \
    "v_add_f32 v232, v248, v249\n\t" \
    "v_add_f32 v233, v250, v251\n\t" \
    "v_add_f32 v224, v224, v225\n\t" \
    "v_add_f32 v226, v226, v227\n\t" \
    "v_add_f32 v232, v232, v233\n\t" \
    "v_add_f32 v224, v224, v226\n\t" \
    "v_fma_f32 v224, v232, 0.5, v224\n\t" \
    "v_mov_b32 v225, v224\n\t" \
    "s_nop 1\n\t" \
    "v_permlane16_swap_b32 v224, v225\n\t" \
    "v_add_f32 v224, v224, v225\n\t" \
    "v_mov_b32 v225, v224\n\t" \
    "s_nop 1\n\t" \
    "v_permlane32_swap_b32 v224, v225\n\t" \
    "v_add_f32 v224, v224, v225\n\t" \
    "v_rcp_f32 v224, v224\n\t" \
    "s_nop 0\n\t" \
    "v_mul_f32 v240, v240, v224\n\t" \
    "v_mul_f32 v241, v241, v224\n\t" \
    "v_mul_f32 v242, v242, v224\n\t" \
    "v_mul_f32 v243, v243, v224\n\t" \
    "v_mul_f32 v244, v244, v224\n\t" \
    "v_mul_f32 v245, v245, v224\n\t" \
    "v_mul_f32 v246, v246, v224\n\t" \
    "v_mul_f32 v247, v247, v224\n\t" \
    "v_mul_f32 v248, v248, v224\n\t" \
    "v_mul_f32 v249, v249, v224\n\t" \
    "v_mul_f32 v250, v250, v224\n\t" \
    "v_mul_f32 v251, v251, v224\n\t" \
    "v_cvt_pk_bf16_f32 v204, |v244|, |v240|\n\t" \
    "v_cvt_pk_bf16_f32 v205, |v245|, |v241|\n\t" \
    "v_cvt_pk_bf16_f32 v206, |v246|, |v242|\n\t" \
    "v_cvt_pk_bf16_f32 v207, |v247|, |v243|\n\t" \
    "v_cvt_pk_bf16_f32 v208, |v248|, |v249|\n\t" \
    "v_cvt_pk_bf16_f32 v209, |v250|, |v251|\n\t" \
    "buffer_store_dwordx4 v[204:207], v253, s[60:63], s50 offen sc1\n\t" \
    "s_mov_b32 exec_lo, -1\n\t" \
    "s_mov_b32 exec_hi, 0\n\t" \
    "buffer_store_dwordx2 v[208:209], v200, s[60:63], s51 offen sc1\n\t" \
    "s_mov_b32 exec_hi, -1\n\t" \
    LST5 \
    "ds_read_b128 v[224:227], v253 offset:0\n\t" \
    "ds_read_b128 v[244:247], v254 offset:53248\n\t" \
    "ds_read_b128 v[228:231], v253 offset:1024\n\t" \
    "ds_read_b128 v[232:235], v253 offset:20480\n\t" \
    "ds_read_b128 v[236:239], v253 offset:21504\n\t" \
    "ds_read_b128 v[248:251], v254 offset:54272\n\t" \
    "s_add_u32 s40, s40, 1\n\t" \
    "s_cmp_lg_u32 s40, 64\n\t" \
    "s_cbranch_scc1 .Ls21_%=\n\t" \
    "s_branch .Lend%=\n\t" \
    ".Ls1_%=:\n\t" \
    "s_add_u32 m0, %[wm], 0x18000\n\t" \
    "s_mov_b32 s44, 0x0\n\t" \
    "buffer_load_dwordx4 v240, %[wr], s44 offen offset:0 nt lds\n\t" \
    "buffer_load_dwordx4 v241, %[wr], s44 offen offset:1024 nt lds\n\t" \
    "buffer_load_dwordx4 v242, %[wr], s44 offen offset:2048 nt lds\n\t" \
    "s_add_u32 m0, %[wm], 0x1e000\n\t" \
    "s_mov_b32 s44, 0x60000\n\t" \
    "buffer_load_dwordx4 v240, %[wr], s44 offen offset:0 nt lds\n\t" \
    "buffer_load_dwordx4 v241, %[wr], s44 offen offset:1024 nt lds\n\t" \
    "buffer_load_dwordx4 v242, %[wr], s44 offen offset:2048 nt lds\n\t" \
    "s_add_u32 m0, %[wm], 0x0\n\t" \
    "s_mov_b32 s44, 0xc0000\n\t" \
    "buffer_load_dwordx4 v240, %[wr], s44 offen offset:0 nt lds\n\t" \
    "buffer_load_dwordx4 v241, %[wr], s44 offen offset:1024 nt lds\n\t" \
    "buffer_load_dwordx4 v242, %[wr], s44 offen offset:2048 nt lds\n\t" \
    "s_add_u32 m0, %[wm], 0x6000\n\t" \
    "s_mov_b32 s44, 0x120000\n\t" \
    "buffer_load_dwordx4 v240, %[wr], s44 offen offset:0 nt lds\n\t" \
    "buffer_load_dwordx4 v241, %[wr], s44 offen offset:1024 nt lds\n\t" \
    "buffer_load_dwordx4 v242, %[wr], s44 offen offset:2048 nt lds\n\t" \
    "s_add_u32 m0, %[wm], 0xc000\n\t" \
    "s_mov_b32 s44, 0x180000\n\t" \
    "buffer_load_dwordx4 v240, %[wr], s44 offen offset:0 nt lds\n\t" \
    "buffer_load_dwordx4 v241, %[wr], s44 offen offset:1024 nt lds\n\t" \
    "buffer_load_dwordx4 v242, %[wr], s44 offen offset:2048 nt lds\n\t" \
    "s_waitcnt lgkmcnt(0)\n\t" \
    "s_add_u32 s43, s48, s49\n\t" \
    "s_add_u32 s43, s43, s50\n\t" \
    "s_and_b32 s43, s43, 0x2000\n\t" \
    "s_xor_b32 s43, s43, %[lx]\n\t" \
    "s_add_u32 s60, s60, s43\n\t" \
    "s_addc_u32 s61, s61, 0\n\t" \
    "v_add_u32 v244, 0x18000, v243\n\t" \
    "s_waitcnt vmcnt(12) lgkmcnt(0)\n\t" \
    "s_barrier \n\t" \
    "s_add_u32 m0, %[wm], 0x12000\n\t" \
    "s_mov_b32 s44, 0x1e0000\n\t" \
    "buffer_load_dwordx4 v240, %[wr], s44 offen offset:0 nt lds\n\t" \
    "buffer_load_dwordx4 v241, %[wr], s44 offen offset:1024 nt lds\n\t" \
    "buffer_load_dwordx4 v242, %[wr], s44 offen offset:2048 nt lds\n\t" \
    "ds_read_b64_tr_b16 v[56:57], v244 offset:0\n\t" \
    "ds_read_b64_tr_b16 v[58:59], v244 offset:1536\n\t" \
    "ds_read_b64_tr_b16 v[64:65], v244 offset:32\n\t" \
    "ds_read_b64_tr_b16 v[66:67], v244 offset:1568\n\t" \
    "ds_read_b64_tr_b16 v[72:73], v244 offset:64\n\t" \
    "ds_read_b64_tr_b16 v[74:75], v244 offset:1600\n\t" \
    "v_add_u32 v244, 0x1e000, v243\n\t" \
    "s_waitcnt vmcnt(12) lgkmcnt(0)\n\t" \
    "s_barrier \n\t" \
    "s_add_u32 m0, %[wm], 0x18000\n\t" \
    "s_mov_b32 s44, 0x240000\n\t" \
    "buffer_load_dwordx4 v240, %[wr], s44 offen offset:0 nt lds\n\t" \
    "buffer_load_dwordx4 v241, %[wr], s44 offen offset:1024 nt lds\n\t" \
    "buffer_load_dwordx4 v242, %[wr], s44 offen offset:2048 nt lds\n\t" \
    "ds_read_b64_tr_b16 v[60:61], v244 offset:0\n\t" \
    "ds_read_b64_tr_b16 v[62:63], v244 offset:1536\n\t" \
    "ds_read_b64_tr_b16 v[68:69], v244 offset:32\n\t" \
    "ds_read_b64_tr_b16 v[70:71], v244 offset:1568\n\t" \
    "ds_read_b64_tr_b16 v[76:77], v244 offset:64\n\t" \
    "ds_read_b64_tr_b16 v[78:79], v244 offset:1600\n\t" \
    "v_add_u32 v244, 0x0, v243\n\t" \
    "s_waitcnt vmcnt(12) lgkmcnt(0)\n\t" \
    "s_barrier \n\t" \
    "s_add_u32 m0, %[wm], 0x1e000\n\t" \
    "s_mov_b32 s44, 0x2a0000\n\t" \
    "buffer_load_dwordx4 v240, %[wr], s44 offen offset:0 nt lds\n\t" \
    "buffer_load_dwordx4 v241, %[wr], s44 offen offset:1024 nt lds\n\t" \
    "buffer_load_dwordx4 v242, %[wr], s44 offen offset:2048 nt lds\n\t" \
    "ds_read_b64_tr_b16 v[96:97], v244 offset:0\n\t" \
    "ds_read_b64_tr_b16 v[98:99], v244 offset:1536\n\t" \
    "ds_read_b64_tr_b16 v[104:105], v244 offset:32\n\t" \
    "ds_read_b64_tr_b16 v[106:107], v244 offset:1568\n\t" \
    "ds_read_b64_tr_b16 v[112:113], v244 offset:64\n\t" \
    "ds_read_b64_tr_b16 v[114:115], v244 offset:1600\n\t" \
    "v_add_u32 v244, 0x6000, v243\n\t" \
    "s_waitcnt vmcnt(12) lgkmcnt(0)\n\t" \
    "s_barrier \n\t" \
    "s_add_u32 m0, %[wm], 0x0\n\t" \
    "s_mov_b32 s44, 0x300000\n\t" \
    "buffer_load_dwordx4 v240, %[wr], s44 offen offset:0 nt lds\n\t" \
    "buffer_load_dwordx4 v241, %[wr], s44 offen offset:1024 nt lds\n\t" \
    "buffer_load_dwordx4 v242, %[wr], s44 offen offset:2048 nt lds\n\t" \
    "ds_read_b64_tr_b16 v[100:101], v244 offset:0\n\t" \
    "ds_read_b64_tr_b16 v[102:103], v244 offset:1536\n\t" \
    "ds_read_b64_tr_b16 v[108:109], v244 offset:32\n\t" \
    "ds_read_b64_tr_b16 v[110:111], v244 offset:1568\n\t" \
    "ds_read_b64_tr_b16 v[116:117], v244 offset:64\n\t" \
    "ds_read_b64_tr_b16 v[118:119], v244 offset:1600\n\t" \
    "v_add_u32 v244, 0xc000, v243\n\t" \
    "s_waitcnt vmcnt(12) lgkmcnt(0)\n\t" \
    "s_barrier \n\t" \
    "s_add_u32 m0, %[wm], 0x6000\n\t" \
    "s_mov_b32 s44, 0x360000\n\t" \
    "buffer_load_dwordx4 v240, %[wr], s44 offen offset:0 nt lds\n\t" \
    "buffer_load_dwordx4 v241, %[wr], s44 offen offset:1024 nt lds\n\t" \
    "buffer_load_dwordx4 v242, %[wr], s44 offen offset:2048 nt lds\n\t" \
    "ds_read_b64_tr_b16 v[200:201], v244 offset:0\n\t" \
    "ds_read_b64_tr_b16 v[202:203], v244 offset:1536\n\t" \
    "ds_read_b64_tr_b16 v[0:1], v244 offset:32\n\t" \
    "ds_read_b64_tr_b16 v[2:3], v244 offset:1568\n\t" \
    "ds_read_b64_tr_b16 v[8:9], v244 offset:64\n\t" \
    "ds_read_b64_tr_b16 v[10:11], v244 offset:1600\n\t" \
    "v_add_u32 v244, 0x12000, v243\n\t" \
    "s_waitcnt vmcnt(12) lgkmcnt(0)\n\t" \
    "s_barrier \n\t" \
    "s_add_u32 m0, %[wm], 0xc000\n\t" \
    "s_mov_b32 s44, 0x3c0000\n\t" \
    "buffer_load_dwordx4 v240, %[wr], s44 offen offset:0 nt lds\n\t" \
    "buffer_load_dwordx4 v241, %[wr], s44 offen offset:1024 nt lds\n\t" \
    "buffer_load_dwordx4 v242, %[wr], s44 offen offset:2048 nt lds\n\t" \
    "ds_read_b64_tr_b16 v[204:205], v244 offset:0\n\t" \
    "ds_read_b64_tr_b16 v[206:207], v244 offset:1536\n\t" \
    "ds_read_b64_tr_b16 v[4:5], v244 offset:32\n\t" \
    "ds_read_b64_tr_b16 v[6:7], v244 offset:1568\n\t" \
    "ds_read_b64_tr_b16 v[12:13], v244 offset:64\n\t" \
    "ds_read_b64_tr_b16 v[14:15], v244 offset:1600\n\t" \
    "v_add_u32 v244, 0x18000, v243\n\t" \
    "s_waitcnt vmcnt(12) lgkmcnt(0)\n\t" \
    "s_barrier \n\t" \
    "s_add_u32 m0, %[wm], 0x12000\n\t" \
    "s_mov_b32 s44, 0x420000\n\t" \
    "buffer_load_dwordx4 v240, %[wr], s44 offen offset:0 nt lds\n\t" \
    "buffer_load_dwordx4 v241, %[wr], s44 offen offset:1024 nt lds\n\t" \
    "buffer_load_dwordx4 v242, %[wr], s44 offen offset:2048 nt lds\n\t" \
    "ds_read_b64_tr_b16 v[208:209], v244 offset:0\n\t" \
    "ds_read_b64_tr_b16 v[210:211], v244 offset:1536\n\t" \
    "ds_read_b64_tr_b16 v[40:41], v244 offset:32\n\t" \
    "ds_read_b64_tr_b16 v[42:43], v244 offset:1568\n\t" \
    "ds_read_b64_tr_b16 v[48:49], v244 offset:64\n\t" \
    "ds_read_b64_tr_b16 v[50:51], v244 offset:1600\n\t" \
    "v_add_u32 v244, 0x1e000, v243\n\t" \
    "s_waitcnt vmcnt(12) lgkmcnt(0)\n\t" \
    "s_barrier \n\t" \
    "s_add_u32 m0, %[wm], 0x18000\n\t" \
    "s_mov_b32 s44, 0x480000\n\t" \
    "buffer_load_dwordx4 v240, %[wr], s44 offen offset:0 nt lds\n\t" \
    "buffer_load_dwordx4 v241, %[wr], s44 offen offset:1024 nt lds\n\t" \
    "buffer_load_dwordx4 v242, %[wr], s44 offen offset:2048 nt lds\n\t" \
    "ds_read_b64_tr_b16 v[212:213], v244 offset:0\n\t" \
    "ds_read_b64_tr_b16 v[214:215], v244 offset:1536\n\t" \
    "ds_read_b64_tr_b16 v[44:45], v244 offset:32\n\t" \
    "ds_read_b64_tr_b16 v[46:47], v244 offset:1568\n\t" \
    "ds_read_b64_tr_b16 v[52:53], v244 offset:64\n\t" \
    "ds_read_b64_tr_b16 v[54:55], v244 offset:1600\n\t" \
    "v_add_u32 v244, 0x0, v243\n\t" \
    "s_waitcnt vmcnt(12) lgkmcnt(0)\n\t" \
    "s_barrier \n\t" \
    "s_add_u32 m0, %[wm], 0x1e000\n\t" \
    "s_mov_b32 s44, 0x4e0000\n\t" \
    "buffer_load_dwordx4 v240, %[wr], s44 offen offset:0 nt lds\n\t" \
    "buffer_load_dwordx4 v241, %[wr], s44 offen offset:1024 nt lds\n\t" \
    "buffer_load_dwordx4 v242, %[wr], s44 offen offset:2048 nt lds\n\t" \
    "ds_read_b64_tr_b16 v[136:137], v244 offset:0\n\t" \
    "ds_read_b64_tr_b16 v[138:139], v244 offset:1536\n\t" \
    "ds_read_b64_tr_b16 v[144:145], v244 offset:32\n\t" \
    "ds_read_b64_tr_b16 v[146:147], v244 offset:1568\n\t" \
    "ds_read_b64_tr_b16 v[152:153], v244 offset:64\n\t" \
    "ds_read_b64_tr_b16 v[154:155], v244 offset:1600\n\t" \
    "v_add_u32 v244, 0x6000, v243\n\t" \
    "s_waitcnt vmcnt(12) lgkmcnt(0)\n\t" \
    "s_barrier \n\t" \
    "s_add_u32 m0, %[wm], 0x0\n\t" \
    "s_mov_b32 s44, 0x540000\n\t" \
    "buffer_load_dwordx4 v240, %[wr], s44 offen offset:0 nt lds\n\t" \
    "buffer_load_dwordx4 v241, %[wr], s44 offen offset:1024 nt lds\n\t" \
    "buffer_load_dwordx4 v242, %[wr], s44 offen offset:2048 nt lds\n\t" \
    "ds_read_b64_tr_b16 v[140:141], v244 offset:0\n\t" \
    "ds_read_b64_tr_b16 v[142:143], v244 offset:1536\n\t" \
    "ds_read_b64_tr_b16 v[148:149], v244 offset:32\n\t" \
    "ds_read_b64_tr_b16 v[150:151], v244 offset:1568\n\t" \
    "ds_read_b64_tr_b16 v[156:157], v244 offset:64\n\t" \
    "ds_read_b64_tr_b16 v[158:159], v244 offset:1600\n\t" \
    "v_add_u32 v244, 0xc000, v243\n\t" \
    "s_waitcnt vmcnt(12) lgkmcnt(0)\n\t" \
    "s_barrier \n\t" \
    "s_add_u32 m0, %[wm], 0x6000\n\t" \
    "s_mov_b32 s44, 0x5a0000\n\t" \
    "buffer_load_dwordx4 v240, %[wr], s44 offen offset:0 nt lds\n\t" \
    "buffer_load_dwordx4 v241, %[wr], s44 offen offset:1024 nt lds\n\t" \
    "buffer_load_dwordx4 v242, %[wr], s44 offen offset:2048 nt lds\n\t" \
    "ds_read_b64_tr_b16 v[216:217], v244 offset:0\n\t" \
    "ds_read_b64_tr_b16 v[218:219], v244 offset:1536\n\t" \
    "ds_read_b64_tr_b16 v[80:81], v244 offset:32\n\t" \
    "ds_read_b64_tr_b16 v[82:83], v244 offset:1568\n\t" \
    "ds_read_b64_tr_b16 v[88:89], v244 offset:64\n\t" \
    "ds_read_b64_tr_b16 v[90:91], v244 offset:1600\n\t" \
    "v_add_u32 v244, 0x12000, v243\n\t" \
    "s_waitcnt vmcnt(12) lgkmcnt(0)\n\t" \
    "s_barrier \n\t" \
    "s_add_u32 m0, %[wm], 0xc000\n\t" \
    "s_mov_b32 s44, 0x600000\n\t" \
    "buffer_load_dwordx4 v240, %[wr], s44 offen offset:0 nt lds\n\t" \
    "buffer_load_dwordx4 v241, %[wr], s44 offen offset:1024 nt lds\n\t" \
    "buffer_load_dwordx4 v242, %[wr], s44 offen offset:2048 nt lds\n\t" \
    "ds_read_b64_tr_b16 v[220:221], v244 offset:0\n\t" \
    "ds_read_b64_tr_b16 v[222:223], v244 offset:1536\n\t" \
    "ds_read_b64_tr_b16 v[84:85], v244 offset:32\n\t" \
    "ds_read_b64_tr_b16 v[86:87], v244 offset:1568\n\t" \
    "ds_read_b64_tr_b16 v[92:93], v244 offset:64\n\t" \
    "ds_read_b64_tr_b16 v[94:95], v244 offset:1600\n\t" \
    "v_add_u32 v244, 0x18000, v243\n\t" \
    "s_waitcnt vmcnt(12) lgkmcnt(0)\n\t" \
    "s_barrier \n\t" \
    "s_add_u32 m0, %[wm], 0x12000\n\t" \
    "s_mov_b32 s44, 0x660000\n\t" \
    "buffer_load_dwordx4 v240, %[wr], s44 offen offset:0 nt lds\n\t" \
    "buffer_load_dwordx4 v241, %[wr], s44 offen offset:1024 nt lds\n\t" \
    "buffer_load_dwordx4 v242, %[wr], s44 offen offset:2048 nt lds\n\t" \
    "ds_read_b64_tr_b16 v[16:17], v244 offset:0\n\t" \
    "ds_read_b64_tr_b16 v[18:19], v244 offset:1536\n\t" \
    "ds_read_b64_tr_b16 v[24:25], v244 offset:32\n\t" \
    "ds_read_b64_tr_b16 v[26:27], v244 offset:1568\n\t" \
    "ds_read_b64_tr_b16 v[32:33], v244 offset:64\n\t" \
    "ds_read_b64_tr_b16 v[34:35], v244 offset:1600\n\t" \
    "v_add_u32 v244, 0x1e000, v243\n\t" \
    "s_waitcnt vmcnt(12) lgkmcnt(0)\n\t" \
    "s_barrier \n\t" \
    "s_add_u32 m0, %[wm], 0x18000\n\t" \
    "s_mov_b32 s44, 0x6c0000\n\t" \
    "buffer_load_dwordx4 v240, %[wr], s44 offen offset:0 nt lds\n\t" \
    "buffer_load_dwordx4 v241, %[wr], s44 offen offset:1024 nt lds\n\t" \
    "buffer_load_dwordx4 v242, %[wr], s44 offen offset:2048 nt lds\n\t" \
    "ds_read_b64_tr_b16 v[20:21], v244 offset:0\n\t" \
    "ds_read_b64_tr_b16 v[22:23], v244 offset:1536\n\t" \
    "ds_read_b64_tr_b16 v[28:29], v244 offset:32\n\t" \
    "ds_read_b64_tr_b16 v[30:31], v244 offset:1568\n\t" \
    "ds_read_b64_tr_b16 v[36:37], v244 offset:64\n\t" \
    "ds_read_b64_tr_b16 v[38:39], v244 offset:1600\n\t" \
    "v_add_u32 v244, 0x0, v243\n\t" \
    "s_waitcnt vmcnt(12) lgkmcnt(0)\n\t" \
    "s_barrier \n\t" \
    "s_add_u32 m0, %[wm], 0x1e000\n\t" \
    "s_mov_b32 s44, 0x720000\n\t" \
    "buffer_load_dwordx4 v240, %[wr], s44 offen offset:0 nt lds\n\t" \
    "buffer_load_dwordx4 v241, %[wr], s44 offen offset:1024 nt lds\n\t" \
    "buffer_load_dwordx4 v242, %[wr], s44 offen offset:2048 nt lds\n\t" \
    "ds_read_b64_tr_b16 v[224:225], v244 offset:0\n\t" \
    "ds_read_b64_tr_b16 v[226:227], v244 offset:1536\n\t" \
    "ds_read_b64_tr_b16 v[120:121], v244 offset:32\n\t" \
    "ds_read_b64_tr_b16 v[122:123], v244 offset:1568\n\t" \
    "ds_read_b64_tr_b16 v[128:129], v244 offset:64\n\t" \
    "ds_read_b64_tr_b16 v[130:131], v244 offset:1600\n\t" \
    "v_add_u32 v244, 0x6000, v243\n\t" \
    "s_waitcnt vmcnt(12) lgkmcnt(0)\n\t" \
    "s_barrier \n\t" \
    "ds_read_b64_tr_b16 v[228:229], v244 offset:0\n\t" \
    "ds_read_b64_tr_b16 v[230:231], v244 offset:1536\n\t" \
    "ds_read_b64_tr_b16 v[124:125], v244 offset:32\n\t" \
    "ds_read_b64_tr_b16 v[126:127], v244 offset:1568\n\t" \
    "ds_read_b64_tr_b16 v[132:133], v244 offset:64\n\t" \
    "ds_read_b64_tr_b16 v[134:135], v244 offset:1600\n\t" \
    "v_add_u32 v244, 0xc000, v243\n\t" \
    "s_waitcnt vmcnt(9) lgkmcnt(0)\n\t" \
    "s_barrier \n\t" \
    "ds_read_b64_tr_b16 v[232:233], v244 offset:0\n\t" \
    "ds_read_b64_tr_b16 v[234:235], v244 offset:1536\n\t" \
    "ds_read_b64_tr_b16 v[160:161], v244 offset:32\n\t" \
    "ds_read_b64_tr_b16 v[162:163], v244 offset:1568\n\t" \
    "ds_read_b64_tr_b16 v[168:169], v244 offset:64\n\t" \
    "ds_read_b64_tr_b16 v[170:171], v244 offset:1600\n\t" \
    "v_add_u32 v244, 0x12000, v243\n\t" \
    "s_waitcnt vmcnt(6) lgkmcnt(0)\n\t" \
    "s_barrier \n\t" \
    "ds_read_b64_tr_b16 v[236:237], v244 offset:0\n\t" \
    "ds_read_b64_tr_b16 v[238:239], v244 offset:1536\n\t" \
    "ds_read_b64_tr_b16 v[164:165], v244 offset:32\n\t" \
    "ds_read_b64_tr_b16 v[166:167], v244 offset:1568\n\t" \
    "ds_read_b64_tr_b16 v[172:173], v244 offset:64\n\t" \
    "ds_read_b64_tr_b16 v[174:175], v244 offset:1600\n\t" \
    "v_add_u32 v244, 0x18000, v243\n\t" \
    "s_waitcnt vmcnt(3) lgkmcnt(0)\n\t" \
    "s_barrier \n\t" \
    "ds_read_b64_tr_b16 v[176:177], v244 offset:0\n\t" \
    "ds_read_b64_tr_b16 v[178:179], v244 offset:1536\n\t" \
    "ds_read_b64_tr_b16 v[184:185], v244 offset:32\n\t" \
    "ds_read_b64_tr_b16 v[186:187], v244 offset:1568\n\t" \
    "ds_read_b64_tr_b16 v[192:193], v244 offset:64\n\t" \
    "ds_read_b64_tr_b16 v[194:195], v244 offset:1600\n\t" \
    "v_add_u32 v244, 0x1e000, v243\n\t" \
    "s_waitcnt vmcnt(0) lgkmcnt(0)\n\t" \
    "s_barrier \n\t" \
    "ds_read_b64_tr_b16 v[180:181], v244 offset:0\n\t" \
    "ds_read_b64_tr_b16 v[182:183], v244 offset:1536\n\t" \
    "ds_read_b64_tr_b16 v[188:189], v244 offset:32\n\t" \
    "ds_read_b64_tr_b16 v[190:191], v244 offset:1568\n\t" \
    "ds_read_b64_tr_b16 v[196:197], v244 offset:64\n\t" \
    "ds_read_b64_tr_b16 v[198:199], v244 offset:1600\n\t" \
    "s_waitcnt lgkmcnt(0)\n\t" \
    "s_barrier \n\t" \
    "ds_write_b128 v254, v[200:203] offset:40960\n\t" \
    "ds_write_b128 v254, v[204:207] offset:41984\n\t" \
    "ds_write_b128 v254, v[208:211] offset:43008\n\t" \
    "ds_write_b128 v254, v[212:215] offset:44032\n\t" \
    "ds_write_b128 v254, v[216:219] offset:45056\n\t" \
    "ds_write_b128 v254, v[220:223] offset:46080\n\t" \
    "ds_write_b128 v254, v[224:227] offset:47104\n\t" \
    "ds_write_b128 v254, v[228:231] offset:48128\n\t" \
    "ds_write_b128 v254, v[232:235] offset:49152\n\t" \
    "ds_write_b128 v254, v[236:239] offset:50176\n\t" \
    "s_waitcnt vmcnt(0) lgkmcnt(0)\n\t" \
    "s_barrier \n\t" \
    "ds_read_b128 v[224:227], v253 offset:0\n\t" \
    "ds_read_b128 v[244:247], v254 offset:40960\n\t" \
    "ds_read_b128 v[228:231], v253 offset:1024\n\t" \
    "ds_read_b128 v[232:235], v253 offset:20480\n\t" \
    "ds_read_b128 v[236:239], v253 offset:21504\n\t" \
    "ds_read_b128 v[248:251], v254 offset:41984\n\t" \
    ".Ls11_%=:\n\t" \
    "s_waitcnt lgkmcnt(5)\n\t" \
    "v_mfma_f32_16x16x32_bf16 v[204:207], v[64:67], v[224:227], 0\n\t" \
    "s_waitcnt lgkmcnt(3)\n\t" \
    "v_mfma_f32_16x16x32_bf16 v[204:207], v[68:71], v[228:231], v[204:207]\n\t" \
    "ds_read_b128 v[240:243], v253 offset:2048\n\t" \
    "v_mfma_f32_16x16x32_bf16 v[208:211], v[72:75], v[224:227], 0\n\t" \
    "v_mfma_f32_16x16x32_bf16 v[208:211], v[76:79], v[228:231], v[208:211]\n\t" \
    "v_mfma_f32_16x16x32_bf16 v[200:203], v[56:59], v[224:227], 0\n\t" \
    "v_mfma_f32_16x16x32_bf16 v[200:203], v[60:63], v[228:231], v[200:203]\n\t" \
    "s_waitcnt lgkmcnt(3)\n\t" \
    "v_mfma_f32_16x16x32_bf16 v[216:219], v[64:67], v[232:235], 0\n\t" \
    "ds_read_b128 v[224:227], v253 offset:3072\n\t" \
    "s_waitcnt lgkmcnt(3)\n\t" \
    "v_mfma_f32_16x16x32_bf16 v[216:219], v[68:71], v[236:239], v[216:219]\n\t" \
    "ds_read_b128 v[228:231], v253 offset:22528\n\t" \
    "v_mfma_f32_16x16x32_bf16 v[220:223], v[72:75], v[232:235], 0\n\t" \
    "v_mfma_f32_16x16x32_bf16 v[220:223], v[76:79], v[236:239], v[220:223]\n\t" \
    "v_mfma_f32_16x16x32_bf16 v[212:215], v[56:59], v[232:235], 0\n\t" \
    "v_mfma_f32_16x16x32_bf16 v[212:215], v[60:63], v[236:239], v[212:215]\n\t" \
    "s_waitcnt lgkmcnt(2)\n\t" \
    "v_mfma_f32_16x16x32_bf16 v[204:207], v[104:107], v[240:243], v[204:207]\n\t" \
    "ds_read_b128 v[232:235], v253 offset:23552\n\t" \
    "s_waitcnt lgkmcnt(2)\n\t" \
    "v_mfma_f32_16x16x32_bf16 v[204:207], v[108:111], v[224:227], v[204:207]\n\t" \
    "ds_read_b128 v[236:239], v253 offset:4096\n\t" \
    "v_mfma_f32_16x16x32_bf16 v[208:211], v[112:115], v[240:243], v[208:211]\n\t" \
    "v_mfma_f32_16x16x32_bf16 v[208:211], v[116:119], v[224:227], v[208:211]\n\t" \
    "v_mfma_f32_16x16x32_bf16 v[200:203], v[96:99], v[240:243], v[200:203]\n\t" \
    "v_mfma_f32_16x16x32_bf16 v[200:203], v[100:103], v[224:227], v[200:203]\n\t" \
    "s_waitcnt lgkmcnt(2)\n\t" \
    "v_mfma_f32_16x16x32_bf16 v[216:219], v[104:107], v[228:231], v[216:219]\n\t" \
    "ds_read_b128 v[240:243], v253 offset:5120\n\t" \
    "s_waitcnt lgkmcnt(2)\n\t" \
    "v_mfma_f32_16x16x32_bf16 v[216:219], v[108:111], v[232:235], v[216:219]\n\t" \
    "ds_read_b128 v[224:227], v253 offset:24576\n\t" \
    "v_mfma_f32_16x16x32_bf16 v[220:223], v[112:115], v[228:231], v[220:223]\n\t" \
    "v_mfma_f32_16x16x32_bf16 v[220:223], v[116:119], v[232:235], v[220:223]\n\t" \
    "v_mfma_f32_16x16x32_bf16 v[212:215], v[96:99], v[228:231], v[212:215]\n\t" \
    "v_mfma_f32_16x16x32_bf16 v[212:215], v[100:103], v[232:235], v[212:215]\n\t" \
    "s_waitcnt lgkmcnt(2)\n\t" \
    "v_mfma_f32_16x16x32_bf16 v[204:207], v[0:3], v[236:239], v[204:207]\n\t" \
    "ds_read_b128 v[228:231], v253 offset:25600\n\t" \
    "s_waitcnt lgkmcnt(2)\n\t" \
    "v_mfma_f32_16x16x32_bf16 v[204:207], v[4:7], v[240:243], v[204:207]\n\t" \
    "ds_read_b128 v[232:235], v253 offset:6144\n\t" \
    "v_mfma_f32_16x16x32_bf16 v[208:211], v[8:11], v[236:239], v[208:211]\n\t" \
    "v_mfma_f32_16x16x32_bf16 v[208:211], v[12:15], v[240:243], v[208:211]\n\t" \
    "v_mfma_f32_16x16x32_bf16 v[200:203], v[244:247], v[236:239], v[200:203]\n\t" \
    "v_mfma_f32_16x16x32_bf16 v[200:203], v[248:251], v[240:243], v[200:203]\n\t" \
    "s_waitcnt lgkmcnt(2)\n\t" \
    "v_mfma_f32_16x16x32_bf16 v[216:219], v[0:3], v[224:227], v[216:219]\n\t" \
    "ds_read_b128 v[236:239], v253 offset:7168\n\t" \
    "s_waitcnt lgkmcnt(2)\n\t" \
    "v_mfma_f32_16x16x32_bf16 v[216:219], v[4:7], v[228:231], v[216:219]\n\t" \
    "ds_read_b128 v[240:243], v253 offset:26624\n\t" \
    "v_mfma_f32_16x16x32_bf16 v[220:223], v[8:11], v[224:227], v[220:223]\n\t" \
    "v_mfma_f32_16x16x32_bf16 v[220:223], v[12:15], v[228:231], v[220:223]\n\t" \
    "v_mfma_f32_16x16x32_bf16 v[212:215], v[244:247], v[224:227], v[212:215]\n\t" \
    "ds_read_b128 v[244:247], v254 offset:43008\n\t" \
    "v_mfma_f32_16x16x32_bf16 v[212:215], v[248:251], v[228:231], v[212:215]\n\t" \
    "ds_read_b128 v[248:251], v254 offset:44032\n\t" \
    "s_waitcnt lgkmcnt(4)\n\t" \
    "v_mfma_f32_16x16x32_bf16 v[204:207], v[40:43], v[232:235], v[204:207]\n\t" \
    "ds_read_b128 v[224:227], v253 offset:27648\n\t" \
    "s_waitcnt lgkmcnt(4)\n\t" \
    "v_mfma_f32_16x16x32_bf16 v[204:207], v[44:47], v[236:239], v[204:207]\n\t" \
    "ds_read_b128 v[228:231], v253 offset:8192\n\t" \
    "v_mfma_f32_16x16x32_bf16 v[208:211], v[48:51], v[232:235], v[208:211]\n\t" \
    "v_mfma_f32_16x16x32_bf16 v[208:211], v[52:55], v[236:239], v[208:211]\n\t" \
    "s_waitcnt lgkmcnt(3)\n\t" \
    "v_mfma_f32_16x16x32_bf16 v[200:203], v[244:247], v[232:235], v[200:203]\n\t" \
    "s_waitcnt lgkmcnt(2)\n\t" \
    "v_mfma_f32_16x16x32_bf16 v[200:203], v[248:251], v[236:239], v[200:203]\n\t" \
    "v_mfma_f32_16x16x32_bf16 v[216:219], v[40:43], v[240:243], v[216:219]\n\t" \
    "ds_read_b128 v[232:235], v253 offset:9216\n\t" \
    "s_waitcnt lgkmcnt(2)\n\t" \
    "v_mfma_f32_16x16x32_bf16 v[216:219], v[44:47], v[224:227], v[216:219]\n\t" \
    "ds_read_b128 v[236:239], v253 offset:28672\n\t" \
    "v_mfma_f32_16x16x32_bf16 v[220:223], v[48:51], v[240:243], v[220:223]\n\t" \
    "v_mfma_f32_16x16x32_bf16 v[220:223], v[52:55], v[224:227], v[220:223]\n\t" \
    "v_mfma_f32_16x16x32_bf16 v[212:215], v[244:247], v[240:243], v[212:215]\n\t" \
    "ds_read_b128 v[244:247], v254 offset:45056\n\t" \
    "v_mfma_f32_16x16x32_bf16 v[212:215], v[248:251], v[224:227], v[212:215]\n\t" \
    "ds_read_b128 v[248:251], v254 offset:46080\n\t" \
    "s_waitcnt lgkmcnt(4)\n\t" \
    "v_mfma_f32_16x16x32_bf16 v[204:207], v[144:147], v[228:231], v[204:207]\n\t" \
    "ds_read_b128 v[240:243], v253 offset:29696\n\t" \
    "s_waitcnt lgkmcnt(4)\n\t" \
    "v_mfma_f32_16x16x32_bf16 v[204:207], v[148:151], v[232:235], v[204:207]\n\t" \
    "ds_read_b128 v[224:227], v253 offset:10240\n\t" \
    "v_mfma_f32_16x16x32_bf16 v[208:211], v[152:155], v[228:231], v[208:211]\n\t" \
    "v_mfma_f32_16x16x32_bf16 v[208:211], v[156:159], v[232:235], v[208:211]\n\t" \
    "v_mfma_f32_16x16x32_bf16 v[200:203], v[136:139], v[228:231], v[200:203]\n\t" \
    "v_mfma_f32_16x16x32_bf16 v[200:203], v[140:143], v[232:235], v[200:203]\n\t" \
    "s_waitcnt lgkmcnt(4)\n\t" \
    "v_mfma_f32_16x16x32_bf16 v[216:219], v[144:147], v[236:239], v[216:219]\n\t" \
    "ds_read_b128 v[228:231], v253 offset:11264\n\t" \
    "s_waitcnt lgkmcnt(2)\n\t" \
    "v_mfma_f32_16x16x32_bf16 v[216:219], v[148:151], v[240:243], v[216:219]\n\t" \
    "ds_read_b128 v[232:235], v253 offset:30720\n\t" \
    "v_mfma_f32_16x16x32_bf16 v[220:223], v[152:155], v[236:239], v[220:223]\n\t" \
    "v_mfma_f32_16x16x32_bf16 v[220:223], v[156:159], v[240:243], v[220:223]\n\t" \
    "v_mfma_f32_16x16x32_bf16 v[212:215], v[136:139], v[236:239], v[212:215]\n\t" \
    "v_mfma_f32_16x16x32_bf16 v[212:215], v[140:143], v[240:243], v[212:215]\n\t" \
    "ds_read_b128 v[236:239], v253 offset:31744\n\t" \
    "s_waitcnt lgkmcnt(0)\n\t" \
    "s_barrier \n\t" \
    LST7 \
    "v_mfma_f32_16x16x32_bf16 v[204:207], v[80:83], v[224:227], v[204:207]\n\t" \
    "v_mfma_f32_16x16x32_bf16 v[204:207], v[84:87], v[228:231], v[204:207]\n\t" \
    "ds_read_b128 v[240:243], v253 offset:14336\n\t" \
    "v_mfma_f32_16x16x32_bf16 v[208:211], v[88:91], v[224:227], v[208:211]\n\t" \
    "v_mfma_f32_16x16x32_bf16 v[208:211], v[92:95], v[228:231], v[208:211]\n\t" \
    "v_mfma_f32_16x16x32_bf16 v[200:203], v[244:247], v[224:227], v[200:203]\n\t" \
    "v_mfma_f32_16x16x32_bf16 v[200:203], v[248:251], v[228:231], v[200:203]\n\t" \
    "v_mfma_f32_16x16x32_bf16 v[216:219], v[80:83], v[232:235], v[216:219]\n\t" \
    "ds_read_b128 v[224:227], v253 offset:15360\n\t" \
    "v_mfma_f32_16x16x32_bf16 v[216:219], v[84:87], v[236:239], v[216:219]\n\t" \
    "ds_read_b128 v[228:231], v253 offset:34816\n\t" \
    "v_mfma_f32_16x16x32_bf16 v[220:223], v[88:91], v[232:235], v[220:223]\n\t" \
    "v_mfma_f32_16x16x32_bf16 v[220:223], v[92:95], v[236:239], v[220:223]\n\t" \
    "v_mfma_f32_16x16x32_bf16 v[212:215], v[244:247], v[232:235], v[212:215]\n\t" \
    "ds_read_b128 v[244:247], v254 offset:47104\n\t" \
    "v_mfma_f32_16x16x32_bf16 v[212:215], v[248:251], v[236:239], v[212:215]\n\t" \
    "ds_read_b128 v[248:251], v254 offset:48128\n\t" \
    "s_waitcnt lgkmcnt(4)\n\t" \
    "v_mfma_f32_16x16x32_bf16 v[204:207], v[120:123], v[240:243], v[204:207]\n\t" \
    "ds_read_b128 v[232:235], v253 offset:35840\n\t" \
    "s_and_b32 s44, s40, 3\n\t" \
    "s_waitcnt lgkmcnt(4)\n\t" \
    "v_mfma_f32_16x16x32_bf16 v[204:207], v[124:127], v[224:227], v[204:207]\n\t" \
    "ds_read_b128 v[236:239], v253 offset:16384\n\t" \
    "s_lshl_b32 s44, s44, 17\n\t" \
    "v_mfma_f32_16x16x32_bf16 v[208:211], v[128:131], v[240:243], v[208:211]\n\t" \
    "s_add_u32 s44, s44, %[mb]\n\t" \
    "v_mfma_f32_16x16x32_bf16 v[208:211], v[132:135], v[224:227], v[208:211]\n\t" \
    "s_add_u32 s46, s44, 0x800\n\t" \
    "s_waitcnt lgkmcnt(3)\n\t" \
    "v_mfma_f32_16x16x32_bf16 v[200:203], v[244:247], v[240:243], v[200:203]\n\t" \
    "s_add_u32 s47, s44, 0x1800\n\t" \
    "s_waitcnt lgkmcnt(2)\n\t" \
    "v_mfma_f32_16x16x32_bf16 v[200:203], v[248:251], v[224:227], v[200:203]\n\t" \
    "s_add_u32 s48, s44, 0xc00\n\t" \
    "v_mfma_f32_16x16x32_bf16 v[216:219], v[120:123], v[228:231], v[216:219]\n\t" \
    "ds_read_b128 v[240:243], v253 offset:17408\n\t" \
    "s_waitcnt lgkmcnt(2)\n\t" \
    "v_mfma_f32_16x16x32_bf16 v[216:219], v[124:127], v[232:235], v[216:219]\n\t" \
    "ds_read_b128 v[224:227], v253 offset:36864\n\t" \
    "v_mfma_f32_16x16x32_bf16 v[220:223], v[128:131], v[228:231], v[220:223]\n\t" \
    "v_mfma_f32_16x16x32_bf16 v[220:223], v[132:135], v[232:235], v[220:223]\n\t" \
    "v_mfma_f32_16x16x32_bf16 v[212:215], v[244:247], v[228:231], v[212:215]\n\t" \
    "ds_read_b128 v[244:247], v254 offset:49152\n\t" \
    "v_mfma_f32_16x16x32_bf16 v[212:215], v[248:251], v[232:235], v[212:215]\n\t" \
    "ds_read_b128 v[248:251], v254 offset:50176\n\t" \
    "s_waitcnt lgkmcnt(4)\n\t" \
    "v_mfma_f32_16x16x32_bf16 v[204:207], v[160:163], v[236:239], v[204:207]\n\t" \
    "ds_read_b128 v[228:231], v253 offset:37888\n\t" \
    "s_waitcnt lgkmcnt(4)\n\t" \
    "v_mfma_f32_16x16x32_bf16 v[204:207], v[164:167], v[240:243], v[204:207]\n\t" \
    "ds_read_b128 v[232:235], v253 offset:12288\n\t" \
    "v_mfma_f32_16x16x32_bf16 v[208:211], v[168:171], v[236:239], v[208:211]\n\t" \
    "v_mfma_f32_16x16x32_bf16 v[208:211], v[172:175], v[240:243], v[208:211]\n\t" \
    "s_waitcnt lgkmcnt(3)\n\t" \
    "v_mfma_f32_16x16x32_bf16 v[200:203], v[244:247], v[236:239], v[200:203]\n\t" \
    "s_waitcnt lgkmcnt(2)\n\t" \
    "v_mfma_f32_16x16x32_bf16 v[200:203], v[248:251], v[240:243], v[200:203]\n\t" \
    "v_mfma_f32_16x16x32_bf16 v[216:219], v[160:163], v[224:227], v[216:219]\n\t" \
    "ds_read_b128 v[236:239], v253 offset:13312\n\t" \
    "s_waitcnt lgkmcnt(2)\n\t" \
    "v_mfma_f32_16x16x32_bf16 v[216:219], v[164:167], v[228:231], v[216:219]\n\t" \
    "ds_read_b128 v[240:243], v253 offset:18432\n\t" \
    "v_mfma_f32_16x16x32_bf16 v[220:223], v[168:171], v[224:227], v[220:223]\n\t" \
    "v_mfma_f32_16x16x32_bf16 v[220:223], v[172:175], v[228:231], v[220:223]\n\t" \
    "v_mfma_f32_16x16x32_bf16 v[212:215], v[244:247], v[224:227], v[212:215]\n\t" \
    "v_mfma_f32_16x16x32_bf16 v[212:215], v[248:251], v[228:231], v[212:215]\n\t" \
    "s_waitcnt lgkmcnt(2)\n\t" \
    "v_mfma_f32_16x16x32_bf16 v[204:207], v[24:27], v[232:235], v[204:207]\n\t" \
    "ds_read_b128 v[224:227], v253 offset:19456\n\t" \
    "v_mov_b64 v[244:245], 0\n\t" \
    "s_waitcnt lgkmcnt(2)\n\t" \
    "v_mfma_f32_16x16x32_bf16 v[204:207], v[28:31], v[236:239], v[204:207]\n\t" \
    "ds_read_b128 v[228:231], v253 offset:32768\n\t" \
    "v_mov_b64 v[246:247], 0\n\t" \
    "v_mfma_f32_16x16x32_bf16 v[208:211], v[32:35], v[232:235], v[208:211]\n\t" \
    "v_mov_b64 v[248:249], 0\n\t" \
    "v_mfma_f32_16x16x32_bf16 v[208:211], v[36:39], v[236:239], v[208:211]\n\t" \
    "v_mov_b64 v[250:251], 0\n\t" \
    "v_mfma_f32_16x16x32_bf16 v[200:203], v[16:19], v[232:235], v[200:203]\n\t" \
    "v_mfma_f32_16x16x32_bf16 v[200:203], v[20:23], v[236:239], v[200:203]\n\t" \
    "s_waitcnt lgkmcnt(2)\n\t" \
    "v_mfma_f32_16x16x32_bf16 v[204:207], v[184:187], v[240:243], v[204:207]\n\t" \
    "ds_read_b128 v[232:235], v253 offset:33792\n\t" \
    "s_waitcnt lgkmcnt(2)\n\t" \
    "v_mfma_f32_16x16x32_bf16 v[204:207], v[188:191], v[224:227], v[204:207]\n\t" \
    "ds_read_b128 v[236:239], v253 offset:38912\n\t" \
    "v_mfma_f32_16x16x32_bf16 v[208:211], v[192:195], v[240:243], v[208:211]\n\t" \
    "v_mfma_f32_16x16x32_bf16 v[208:211], v[196:199], v[224:227], v[208:211]\n\t" \
    "v_mfma_f32_16x16x32_bf16 v[200:203], v[176:179], v[240:243], v[200:203]\n\t" \
    "v_mfma_f32_16x16x32_bf16 v[200:203], v[180:183], v[224:227], v[200:203]\n\t" \
    "ds_read_b128 v[240:243], v253 offset:39936\n\t" \
    "s_waitcnt lgkmcnt(3)\n\t" \
    "v_mfma_f32_16x16x32_bf16 v[216:219], v[24:27], v[228:231], v[216:219]\n\t" \
    "s_waitcnt lgkmcnt(2)\n\t" \
    "v_mfma_f32_16x16x32_bf16 v[216:219], v[28:31], v[232:235], v[216:219]\n\t" \
    "v_cvt_pk_bf16_f32 v204, v204, v208\n\t" \
    "v_cvt_pk_bf16_f32 v205, v205, v209\n\t" \
    "s_waitcnt lgkmcnt(1)\n\t" \
    "v_mfma_f32_16x16x32_bf16 v[216:219], v[184:187], v[236:239], v[216:219]\n\t" \
    "v_cvt_pk_bf16_f32 v206, v206, v210\n\t" \
    "v_cvt_pk_bf16_f32 v207, v207, v211\n\t" \
    "s_waitcnt lgkmcnt(0)\n\t" \
    "v_mfma_f32_16x16x32_bf16 v[216:219], v[188:191], v[240:243], v[216:219]\n\t" \
    "ds_write_b128 v254, v[204:207] offset:9216\n\t" \
    "v_cvt_pk_bf16_f32 v200, v200, v201\n\t" \
    "v_mfma_f32_16x16x32_bf16 v[220:223], v[32:35], v[228:231], v[220:223]\n\t" \
    "v_cvt_pk_bf16_f32 v201, v202, v203\n\t" \
    "ds_write_b64 v254, v[200:201] offset:7168\n\t" \
    "v_mfma_f32_16x16x32_bf16 v[220:223], v[36:39], v[232:235], v[220:223]\n\t" \
    "ds_read_b128 v[200:203], v254 offset:40960\n\t" \
    "ds_read_b128 v[204:207], v254 offset:41984\n\t" \
    "v_mfma_f32_16x16x32_bf16 v[220:223], v[192:195], v[236:239], v[220:223]\n\t" \
    "v_mov_b64 v[208:209], 0\n\t" \
    "v_mov_b64 v[210:211], 0\n\t" \
    "v_mfma_f32_16x16x32_bf16 v[220:223], v[196:199], v[240:243], v[220:223]\n\t" \
    "v_mfma_f32_16x16x32_bf16 v[212:215], v[16:19], v[228:231], v[212:215]\n\t" \
    "v_mfma_f32_16x16x32_bf16 v[212:215], v[20:23], v[232:235], v[212:215]\n\t" \
    "v_mfma_f32_16x16x32_bf16 v[212:215], v[176:179], v[236:239], v[212:215]\n\t" \
    "v_cvt_pk_bf16_f32 v216, v216, v220\n\t" \
    "v_cvt_pk_bf16_f32 v217, v217, v221\n\t" \
    "v_mfma_f32_16x16x32_bf16 v[212:215], v[180:183], v[240:243], v[212:215]\n\t" \
    "v_cvt_pk_bf16_f32 v218, v218, v222\n\t" \
    "v_cvt_pk_bf16_f32 v219, v219, v223\n\t" \
    "ds_write_b128 v254, v[216:219] offset:15360\n\t" \
    "ds_read_b128 v[216:219], v254 offset:43008\n\t" \
    "ds_read_b128 v[220:223], v254 offset:44032\n\t" \
    "s_nop 2\n\t" \
    "v_cvt_pk_bf16_f32 v212, v212, v213\n\t" \
    "v_cvt_pk_bf16_f32 v213, v214, v215\n\t" \
    "ds_write_b64 v254, v[212:213] offset:13312\n\t" \
    LST6 \
    "s_sleep 8\n\t" \
    "buffer_load_dwordx4 v[224:227], v253, s[60:63], s46 offen sc1\n\t" \
    "buffer_load_dwordx4 v[228:231], v253, s[60:63], s47 offen sc1\n\t" \
    "buffer_load_dwordx4 v[232:235], v253, s[60:63], s48 offen sc1\n\t" \
    "s_waitcnt lgkmcnt(0)\n\t" \
    LFLUSH212 \
    "v_bfe_i32 v212, v253, 5, 1\n\t" \
    "v_and_b32 v212, 0xaaaa, v212\n\t" \
    "v_or_b32 v212, 0x4444, v212\n\t" \
    "v_and_b32 v213, 0xffff, v255\n\t" \
    "v_lshrrev_b32 v214, 16, v255\n\t" \
    ".Ls12_%=:\n\t" \
    "s_waitcnt vmcnt(2)\n\t" \
    "v_max_u32 v215, v224, v225\n\t" \
    "v_max3_u32 v215, v226, v227, v215\n\t" \
    "s_waitcnt vmcnt(1)\n\t" \
    "v_max3_u32 v215, v228, v229, v215\n\t" \
    "v_max3_u32 v215, v230, v231, v215\n\t" \
    "s_waitcnt vmcnt(0)\n\t" \
    "v_max3_u32 v215, v232, v233, v215\n\t" \
    "v_max3_u32 v215, v234, v235, v215\n\t" \
    "v_cmp_eq_u32 vcc, -1, v215\n\t" \
    "s_cbranch_vccz .Ls13_%=\n\t" \
    "v_max_u32 v215, v224, v225\n\t" \
    "v_max3_u32 v215, v226, v227, v215\n\t" \
    "v_cmp_eq_u32 vcc, -1, v215\n\t" \
    "s_cbranch_vccz .Ls14_%=\n\t" \
    "buffer_load_dwordx4 v[224:227], v253, s[60:63], s46 offen sc1\n\t" \
    ".Ls14_%=:\n\t" \
    "v_max_u32 v215, v228, v229\n\t" \
    "v_max3_u32 v215, v230, v231, v215\n\t" \
    "v_cmp_eq_u32 vcc, -1, v215\n\t" \
    "s_cbranch_vccz .Ls15_%=\n\t" \
    "buffer_load_dwordx4 v[228:231], v253, s[60:63], s47 offen sc1\n\t" \
    ".Ls15_%=:\n\t" \
    "v_max_u32 v215, v232, v233\n\t" \
    "v_max3_u32 v215, v234, v235, v215\n\t" \
    "v_cmp_eq_u32 vcc, -1, v215\n\t" \
    "s_cbranch_vccz .Ls16_%=\n\t" \
    "buffer_load_dwordx4 v[232:235], v253, s[60:63], s48 offen sc1\n\t" \
    ".Ls16_%=:\n\t" \
    "s_cmp_eq_u32 s41, 0\n\t" \
    "s_cbranch_scc1 .Ls13_%=\n\t" \
    "s_sub_u32 s41, s41, 1\n\t" \
    "s_waitcnt vmcnt(0)\n\t" \
    "s_branch .Ls12_%=\n\t" \
    ".Ls13_%=:\n\t" \
    LST2 \
    "ds_write_b128 v253, v[224:227] offset:43008\n\t" \
    "ds_write_b128 v253, v[228:231] offset:47104\n\t" \
    "ds_write_b128 v253, v[232:235] offset:44032\n\t" \
    "v_smfmac_f32_16x16x64_bf16 v[244:247], v[224:227], v[0:7], v212\n\t" \
    "v_smfmac_f32_16x16x64_bf16 v[248:251], v[224:227], v[8:15], v212\n\t" \
    "v_smfmac_f32_16x16x64_bf16 v[208:211], v[224:227], v[200:207], v212\n\t" \
    "s_waitcnt lgkmcnt(0)\n\t" \
    "s_barrier \n\t" \
    LST3 \
    "ds_read_b128 v[236:239], v253 offset:40960\n\t" \
    "ds_read_b128 v[240:243], v253 offset:46080\n\t" \
    "ds_read_b128 v[224:227], v253 offset:41984\n\t" \
    "ds_read_b128 v[200:203], v254 offset:45056\n\t" \
    "ds_read_b128 v[204:207], v254 offset:46080\n\t" \
    "v_smfmac_f32_16x16x64_bf16 v[208:211], v[228:231], v[16:23], v212\n\t" \
    "v_smfmac_f32_16x16x64_bf16 v[244:247], v[228:231], v[24:31], v212\n\t" \
    "v_smfmac_f32_16x16x64_bf16 v[248:251], v[228:231], v[32:39], v212\n\t" \
    "ds_read_b128 v[228:231], v253 offset:48128\n\t" \
    "v_smfmac_f32_16x16x64_bf16 v[208:211], v[232:235], v[216:223], v212\n\t" \
    "ds_read_b128 v[216:219], v254 offset:47104\n\t" \
    "ds_read_b128 v[220:223], v254 offset:48128\n\t" \
    "v_smfmac_f32_16x16x64_bf16 v[244:247], v[232:235], v[40:47], v212\n\t" \
    "v_smfmac_f32_16x16x64_bf16 v[248:251], v[232:235], v[48:55], v212\n\t" \
    "ds_read_b128 v[232:235], v253 offset:45056\n\t" \
    "s_waitcnt lgkmcnt(8)\n\t" \
    "v_smfmac_f32_16x16x64_bf16 v[208:211], v[236:239], v[56:63], v212\n\t" \
    "v_smfmac_f32_16x16x64_bf16 v[244:247], v[236:239], v[64:71], v212\n\t" \
    "v_smfmac_f32_16x16x64_bf16 v[248:251], v[236:239], v[72:79], v212\n\t" \
    "ds_read_b128 v[236:239], v253 offset:49152\n\t" \
    "s_waitcnt lgkmcnt(5)\n\t" \
    "v_smfmac_f32_16x16x64_bf16 v[208:211], v[240:243], v[200:207], v212\n\t" \
    "ds_read_b128 v[200:203], v254 offset:49152\n\t" \
    "ds_read_b128 v[204:207], v254 offset:50176\n\t" \
    "v_smfmac_f32_16x16x64_bf16 v[244:247], v[240:243], v[80:87], v212\n\t" \
    "v_smfmac_f32_16x16x64_bf16 v[248:251], v[240:243], v[88:95], v212\n\t" \
    "ds_read_b128 v[240:243], v253 offset:50176\n\t" \
    "v_smfmac_f32_16x16x64_bf16 v[208:211], v[224:227], v[96:103], v212\n\t" \
    "v_smfmac_f32_16x16x64_bf16 v[244:247], v[224:227], v[104:111], v212\n\t" \
    "v_smfmac_f32_16x16x64_bf16 v[248:251], v[224:227], v[112:119], v212\n\t" \
    "s_waitcnt lgkmcnt(5)\n\t" \
    "v_smfmac_f32_16x16x64_bf16 v[208:211], v[228:231], v[216:223], v212\n\t" \
    "s_waitcnt lgkmcnt(4)\n\t" \
    "v_smfmac_f32_16x16x64_bf16 v[208:211], v[232:235], v[136:143], v212\n\t" \
    "s_waitcnt lgkmcnt(1)\n\t" \
    "v_smfmac_f32_16x16x64_bf16 v[208:211], v[236:239], v[200:207], v212\n\t" \
    "s_waitcnt lgkmcnt(0)\n\t" \
    "v_smfmac_f32_16x16x64_bf16 v[208:211], v[240:243], v[176:183], v212\n\t" \
    "v_smfmac_f32_16x16x64_bf16 v[244:247], v[228:231], v[120:127], v212\n\t" \
    "v_smfmac_f32_16x16x64_bf16 v[244:247], v[232:235], v[144:151], v212\n\t" \
    "v_smfmac_f32_16x16x64_bf16 v[244:247], v[236:239], v[160:167], v212\n\t" \
    "v_add_f32 v208, v208, v210\n\t" \
    "v_add_f32 v209, v209, v211\n\t" \
    "v_cvt_pk_bf16_f32 v208, v208, v209\n\t" \
    "v_smfmac_f32_16x16x64_bf16 v[244:247], v[240:243], v[184:191], v212\n\t" \
    "ds_write_b16 v213, v208 offset:0\n\t" \
    "ds_write_b16_d16_hi v213, v208 offset:16\n\t" \
    "v_smfmac_f32_16x16x64_bf16 v[248:251], v[228:231], v[128:135], v212\n\t" \
    "v_smfmac_f32_16x16x64_bf16 v[248:251], v[232:235], v[152:159], v212\n\t" \
    "v_smfmac_f32_16x16x64_bf16 v[248:251], v[236:239], v[168:175], v212\n\t" \
    "v_add_f32 v244, v244, v246\n\t" \
    "v_add_f32 v245, v245, v247\n\t" \
    "v_smfmac_f32_16x16x64_bf16 v[248:251], v[240:243], v[192:199], v212\n\t" \
    "s_nop 7\n\t" \
    "v_add_f32 v248, v248, v250\n\t" \
    "v_add_f32 v249, v249, v251\n\t" \
    "v_cvt_pk_bf16_f32 v244, v244, v248\n\t" \
    "v_cvt_pk_bf16_f32 v245, v245, v249\n\t" \
    "ds_write2_b32 v214, v244, v245 offset1:4\n\t" \
    "s_sleep 3\n\t" \
    "ds_read_b128 v[200:203], v254 offset:6144\n\t" \
    "ds_read_b128 v[224:227], v254 offset:18432\n\t" \
    "ds_read_b128 v[204:207], v254 offset:7168\n\t" \
    "ds_read_b128 v[228:231], v254 offset:19456\n\t" \
    "ds_read_b128 v[208:211], v254 offset:8192\n\t" \
    "ds_read_b128 v[232:235], v254 offset:20480\n\t" \
    "ds_read_b128 v[212:215], v254 offset:9216\n\t" \
    "ds_read_b128 v[236:239], v254 offset:21504\n\t" \
    "ds_read_b128 v[216:219], v254 offset:10240\n\t" \
    "ds_read_b128 v[220:223], v254 offset:11264\n\t" \
    "s_and_b32 s44, s40, 3\n\t" \
    "s_lshl_b32 s44, s44, 17\n\t" \
    "s_add_u32 s50, s44, %[pb]\n\t" \
    "s_add_u32 s51, s44, %[pm]\n\t" \
    "s_mov_b32 s52, 0xf0000\n\t" \
    "s_mov_b32 s53, 0xfff00ff\n\t" \
    "s_mov_b32 s54, 0x1f0001\n\t" \
    "s_mov_b32 s55, 0x1fff01ff\n\t" \
    "s_mov_b32 s56, 0x3f0003\n\t" \
    "s_mov_b32 s57, 0x3fff03ff\n\t" \
    "s_mov_b32 s58, 0x7f0007\n\t" \
    "s_mov_b32 s59, 0x7fff07ff\n\t" \
    "s_waitcnt lgkmcnt(0)\n\t" \
    "s_barrier \n\t" \
    LST4 \
    "v_and_b32 v252, 0xffffff7f, v254\n\t" \
    "v_mfma_f32_16x16x32_bf16 v[240:243], v[224:227], v[200:203], 0\n\t" \
    "ds_read_b128 v[224:227], v254 offset:22528\n\t" \
    "v_mfma_f32_16x16x32_bf16 v[240:243], v[228:231], v[204:207], v[240:243]\n\t" \
    "ds_read_b128 v[228:231], v254 offset:23552\n\t" \
    "v_mfma_f32_16x16x32_bf16 v[240:243], v[232:235], v[208:211], v[240:243]\n\t" \
    "ds_read_b128 v[232:235], v252 offset:3072\n\t" \
    "v_mfma_f32_16x16x32_bf16 v[240:243], v[236:239], v[212:215], v[240:243]\n\t" \
    "ds_read_b128 v[236:239], v252 offset:3200\n\t" \
    "s_waitcnt lgkmcnt(3)\n\t" \
    "v_mfma_f32_16x16x32_bf16 v[240:243], v[224:227], v[216:219], v[240:243]\n\t" \
    "ds_read_b128 v[224:227], v252 offset:4096\n\t" \
    "s_waitcnt lgkmcnt(3)\n\t" \
    "v_mfma_f32_16x16x32_bf16 v[240:243], v[228:231], v[220:223], v[240:243]\n\t" \
    "ds_read_b128 v[228:231], v252 offset:4224\n\t" \
    "s_waitcnt lgkmcnt(3)\n\t" \
    "v_mfma_f32_16x16x32_bf16 v[248:251], v[232:235], v[200:203], 0\n\t" \
    "ds_read_b128 v[232:235], v252 offset:5120\n\t" \
    "s_waitcnt lgkmcnt(3)\n\t" \
    "v_mfma_f32_16x16x32_bf16 v[248:251], v[236:239], v[204:207], v[248:251]\n\t" \
    "ds_read_b128 v[236:239], v252 offset:5248\n\t" \
    "s_waitcnt lgkmcnt(3)\n\t" \
    "v_mfma_f32_16x16x32_bf16 v[248:251], v[224:227], v[208:211], v[248:251]\n\t" \
    "s_waitcnt lgkmcnt(2)\n\t" \
    "v_mfma_f32_16x16x32_bf16 v[248:251], v[228:231], v[212:215], v[248:251]\n\t" \
    "s_waitcnt lgkmcnt(1)\n\t" \
    "v_mfma_f32_16x16x32_bf16 v[248:251], v[232:235], v[216:219], v[248:251]\n\t" \
    "s_waitcnt lgkmcnt(0)\n\t" \
    "v_mfma_f32_16x16x32_bf16 v[248:251], v[236:239], v[220:223], v[248:251]\n\t" \
    "v_mov_b32 v226, 0xff7fffff\n\t" \
    "v_cndmask_b32 v240, v240, v226, s[52:53]\n\t" \
    "v_cndmask_b32 v241, v241, v226, s[54:55]\n\t" \
    "v_cndmask_b32 v242, v242, v226, s[56:57]\n\t" \
    "v_cndmask_b32 v243, v243, v226, s[58:59]\n\t" \
    "v_max3_f32 v224, v240, v241, v242\n\t" \
    "v_mov_b32 v225, v243\n\t" \
    "v_lshrrev_b32 v200, 5, v253\n\t" \
    "v_and_b32 v201, 0xf0, v253\n\t" \
    "v_and_or_b32 v200, v200, 8, v201\n\t" \
    "v_max3_f32 v224, v224, v248, v249\n\t" \
    "v_max3_f32 v225, v225, v250, v251\n\t" \
    "v_max_f32 v224, v224, v225\n\t" \
    "v_mov_b32 v225, v224\n\t" \
    "s_nop 1\n\t" \
    "v_permlane16_swap_b32 v224, v225\n\t" \
    "v_max_f32 v224, v224, v225\n\t" \
    "v_mov_b32 v225, v224\n\t" \
    "s_nop 1\n\t" \
    "v_permlane32_swap_b32 v224, v225\n\t" \
    "v_max_f32 v224, v224, v225\n\t" \
    "v_mul_f32 v224, %[scl], v224\n\t" \
    "v_fma_f32 v240, v240, %[scl], -v224\n\t" \
    "v_fma_f32 v241, v241, %[scl], -v224\n\t" \
    "v_exp_f32 v240, v240\n\t" \
    "v_fma_f32 v242, v242, %[scl], -v224\n\t" \
    "v_exp_f32 v241, v241\n\t" \
    "v_fma_f32 v243, v243, %[scl], -v224\n\t" \
    "v_exp_f32 v242, v242\n\t" \
    "v_fma_f32 v248, v248, %[scl], -v224\n\t" \
    "v_exp_f32 v243, v243\n\t" \
    "v_fma_f32 v249, v249, %[scl], -v224\n\t" \
    "v_exp_f32 v248, v248\n\t" \
    "v_fma_f32 v250, v250, %[scl], -v224\n\t" \
    "v_exp_f32 v249, v249\n\t" \
    "v_fma_f32 v251, v251, %[scl], -v224\n\t" \
    "v_exp_f32 v250, v250\n\t" \
    "v_exp_f32 v251, v251\n\t" \
    "v_add_f32 v224, v240, v241\n\t" \
    "v_add_f32 v225, v242, v243\n\t" \
    "v_add_f32 v232, v248, v249\n\t" \
    "v_add_f32 v233, v250, v251\n\t" \
    "v_add_f32 v224, v224, v225\n\t" \
    "v_add_f32 v232, v232, v233\n\t" \
    "v_fma_f32 v224, v232, 0.5, v224\n\t" \
    "v_mov_b32 v225, v224\n\t" \
    "s_nop 1\n\t" \
    "v_permlane16_swap_b32 v224, v225\n\t" \
    "v_add_f32 v224, v224, v225\n\t" \
    "v_mov_b32 v225, v224\n\t" \
    "s_nop 1\n\t" \
    "v_permlane32_swap_b32 v224, v225\n\t" \
    "v_add_f32 v224, v224, v225\n\t" \
    "v_rcp_f32 v224, v224\n\t" \
    "s_nop 0\n\t" \
    "v_mul_f32 v240, v240, v224\n\t" \
    "v_mul_f32 v241, v241, v224\n\t" \
    "v_mul_f32 v242, v242, v224\n\t" \
    "v_mul_f32 v243, v243, v224\n\t" \
    "v_mul_f32 v248, v248, v224\n\t" \
    "v_mul_f32 v249, v249, v224\n\t" \
    "v_mul_f32 v250, v250, v224\n\t" \
    "v_mul_f32 v251, v251, v224\n\t" \
    "v_cvt_pk_bf16_f32 v204, |v240|, |v241|\n\t" \
    "v_cvt_pk_bf16_f32 v205, |v242|, |v243|\n\t" \
    "v_cvt_pk_bf16_f32 v208, |v248|, |v249|\n\t" \
    "v_cvt_pk_bf16_f32 v209, |v250|, |v251|\n\t" \
    "v_permlane16_swap_b32 v204, v206\n\t" \
    "v_permlane16_swap_b32 v205, v207\n\t" \
    "s_mov_b32 exec_lo, 0xffff\n\t" \
    "s_mov_b32 exec_hi, 0xffff\n\t" \
    "buffer_store_dwordx4 v[204:207], v253, s[60:63], s50 offen sc1\n\t" \
    "s_mov_b32 exec_lo, -1\n\t" \
    "s_mov_b32 exec_hi, 0\n\t" \
    "buffer_store_dwordx2 v[208:209], v200, s[60:63], s51 offen sc1\n\t" \
    "s_mov_b32 exec_hi, -1\n\t" \
    LST5 \
    "ds_read_b128 v[224:227], v253 offset:0\n\t" \
    "ds_read_b128 v[244:247], v254 offset:40960\n\t" \
    "ds_read_b128 v[228:231], v253 offset:1024\n\t" \
    "ds_read_b128 v[232:235], v253 offset:20480\n\t" \
    "ds_read_b128 v[236:239], v253 offset:21504\n\t" \
    "ds_read_b128 v[248:251], v254 offset:41984\n\t" \
    "s_add_u32 s40, s40, 1\n\t" \
    "s_cmp_lg_u32 s40, 64\n\t" \
    "s_cbranch_scc1 .Ls11_%=\n\t" \
    "s_branch .Lend%=\n\t" \
    ".Ls0_%=:\n\t" \
    "s_add_u32 m0, %[wm], 0x18000\n\t" \
    "s_mov_b32 s44, 0x0\n\t" \
    "buffer_load_dwordx4 v240, %[wr], s44 offen offset:0 nt lds\n\t" \
    "buffer_load_dwordx4 v241, %[wr], s44 offen offset:1024 nt lds\n\t" \
    "buffer_load_dwordx4 v242, %[wr], s44 offen offset:2048 nt lds\n\t" \
    "s_add_u32 m0, %[wm], 0x1e000\n\t" \
    "s_mov_b32 s44, 0x60000\n\t" \
    "buffer_load_dwordx4 v240, %[wr], s44 offen offset:0 nt lds\n\t" \
    "buffer_load_dwordx4 v241, %[wr], s44 offen offset:1024 nt lds\n\t" \
    "buffer_load_dwordx4 v242, %[wr], s44 offen offset:2048 nt lds\n\t" \
    "s_add_u32 m0, %[wm], 0x0\n\t" \
    "s_mov_b32 s44, 0xc0000\n\t" \
    "buffer_load_dwordx4 v240, %[wr], s44 offen offset:0 nt lds\n\t" \
    "buffer_load_dwordx4 v241, %[wr], s44 offen offset:1024 nt lds\n\t" \
    "buffer_load_dwordx4 v242, %[wr], s44 offen offset:2048 nt lds\n\t" \
    "s_add_u32 m0, %[wm], 0x6000\n\t" \
    "s_mov_b32 s44, 0x120000\n\t" \
    "buffer_load_dwordx4 v240, %[wr], s44 offen offset:0 nt lds\n\t" \
    "buffer_load_dwordx4 v241, %[wr], s44 offen offset:1024 nt lds\n\t" \
    "buffer_load_dwordx4 v242, %[wr], s44 offen offset:2048 nt lds\n\t" \
    "s_add_u32 m0, %[wm], 0xc000\n\t" \
    "s_mov_b32 s44, 0x180000\n\t" \
    "buffer_load_dwordx4 v240, %[wr], s44 offen offset:0 nt lds\n\t" \
    "buffer_load_dwordx4 v241, %[wr], s44 offen offset:1024 nt lds\n\t" \
    "buffer_load_dwordx4 v242, %[wr], s44 offen offset:2048 nt lds\n\t" \
    "s_waitcnt lgkmcnt(0)\n\t" \
    "s_add_u32 s43, s48, s49\n\t" \
    "s_add_u32 s43, s43, s50\n\t" \
    "s_and_b32 s43, s43, 0x2000\n\t" \
    "s_xor_b32 s43, s43, %[lx]\n\t" \
    "s_add_u32 s60, s60, s43\n\t" \
    "s_addc_u32 s61, s61, 0\n\t" \
    "v_add_u32 v244, 0x18000, v243\n\t" \
    "s_waitcnt vmcnt(12) lgkmcnt(0)\n\t" \
    "s_barrier \n\t" \
    "s_add_u32 m0, %[wm], 0x12000\n\t" \
    "s_mov_b32 s44, 0x1e0000\n\t" \
    "buffer_load_dwordx4 v240, %[wr], s44 offen offset:0 nt lds\n\t" \
    "buffer_load_dwordx4 v241, %[wr], s44 offen offset:1024 nt lds\n\t" \
    "buffer_load_dwordx4 v242, %[wr], s44 offen offset:2048 nt lds\n\t" \
    "ds_read_b64_tr_b16 v[200:201], v244 offset:0\n\t" \
    "ds_read_b64_tr_b16 v[202:203], v244 offset:1536\n\t" \
    "ds_read_b64_tr_b16 v[0:1], v244 offset:32\n\t" \
    "ds_read_b64_tr_b16 v[2:3], v244 offset:1568\n\t" \
    "ds_read_b64_tr_b16 v[8:9], v244 offset:64\n\t" \
    "ds_read_b64_tr_b16 v[10:11], v244 offset:1600\n\t" \
    "v_add_u32 v244, 0x1e000, v243\n\t" \
    "s_waitcnt vmcnt(12) lgkmcnt(0)\n\t" \
    "s_barrier \n\t" \
    "s_add_u32 m0, %[wm], 0x18000\n\t" \
    "s_mov_b32 s44, 0x240000\n\t" \
    "buffer_load_dwordx4 v240, %[wr], s44 offen offset:0 nt lds\n\t" \
    "buffer_load_dwordx4 v241, %[wr], s44 offen offset:1024 nt lds\n\t" \
    "buffer_load_dwordx4 v242, %[wr], s44 offen offset:2048 nt lds\n\t" \
    "ds_read_b64_tr_b16 v[204:205], v244 offset:0\n\t" \
    "ds_read_b64_tr_b16 v[206:207], v244 offset:1536\n\t" \
    "ds_read_b64_tr_b16 v[4:5], v244 offset:32\n\t" \
    "ds_read_b64_tr_b16 v[6:7], v244 offset:1568\n\t" \
    "ds_read_b64_tr_b16 v[12:13], v244 offset:64\n\t" \
    "ds_read_b64_tr_b16 v[14:15], v244 offset:1600\n\t" \
    "v_add_u32 v244, 0x0, v243\n\t" \
    "s_waitcnt vmcnt(12) lgkmcnt(0)\n\t" \
    "s_barrier \n\t" \
    "s_add_u32 m0, %[wm], 0x1e000\n\t" \
    "s_mov_b32 s44, 0x2a0000\n\t" \
    "buffer_load_dwordx4 v240, %[wr], s44 offen offset:0 nt lds\n\t" \
    "buffer_load_dwordx4 v241, %[wr], s44 offen offset:1024 nt lds\n\t" \
    "buffer_load_dwordx4 v242, %[wr], s44 offen offset:2048 nt lds\n\t" \
    "ds_read_b64_tr_b16 v[208:209], v244 offset:0\n\t" \
    "ds_read_b64_tr_b16 v[210:211], v244 offset:1536\n\t" \
    "ds_read_b64_tr_b16 v[40:41], v244 offset:32\n\t" \
    "ds_read_b64_tr_b16 v[42:43], v244 offset:1568\n\t" \
    "ds_read_b64_tr_b16 v[48:49], v244 offset:64\n\t" \
    "ds_read_b64_tr_b16 v[50:51], v244 offset:1600\n\t" \
    "v_add_u32 v244, 0x6000, v243\n\t" \
    "s_waitcnt vmcnt(12) lgkmcnt(0)\n\t" \
    "s_barrier \n\t" \
    "s_add_u32 m0, %[wm], 0x0\n\t" \
    "s_mov_b32 s44, 0x300000\n\t" \
    "buffer_load_dwordx4 v240, %[wr], s44 offen offset:0 nt lds\n\t" \
    "buffer_load_dwordx4 v241, %[wr], s44 offen offset:1024 nt lds\n\t" \
    "buffer_load_dwordx4 v242, %[wr], s44 offen offset:2048 nt lds\n\t" \
    "ds_read_b64_tr_b16 v[212:213], v244 offset:0\n\t" \
    "ds_read_b64_tr_b16 v[214:215], v244 offset:1536\n\t" \
    "ds_read_b64_tr_b16 v[44:45], v244 offset:32\n\t" \
    "ds_read_b64_tr_b16 v[46:47], v244 offset:1568\n\t" \
    "ds_read_b64_tr_b16 v[52:53], v244 offset:64\n\t" \
    "ds_read_b64_tr_b16 v[54:55], v244 offset:1600\n\t" \
    "v_add_u32 v244, 0xc000, v243\n\t" \
    "s_waitcnt vmcnt(12) lgkmcnt(0)\n\t" \
    "s_barrier \n\t" \
    "s_add_u32 m0, %[wm], 0x6000\n\t" \
    "s_mov_b32 s44, 0x360000\n\t" \
    "buffer_load_dwordx4 v240, %[wr], s44 offen offset:0 nt lds\n\t" \
    "buffer_load_dwordx4 v241, %[wr], s44 offen offset:1024 nt lds\n\t" \
    "buffer_load_dwordx4 v242, %[wr], s44 offen offset:2048 nt lds\n\t" \
    "ds_read_b64_tr_b16 v[216:217], v244 offset:0\n\t" \
    "ds_read_b64_tr_b16 v[218:219], v244 offset:1536\n\t" \
    "ds_read_b64_tr_b16 v[80:81], v244 offset:32\n\t" \
    "ds_read_b64_tr_b16 v[82:83], v244 offset:1568\n\t" \
    "ds_read_b64_tr_b16 v[88:89], v244 offset:64\n\t" \
    "ds_read_b64_tr_b16 v[90:91], v244 offset:1600\n\t" \
    "v_add_u32 v244, 0x12000, v243\n\t" \
    "s_waitcnt vmcnt(12) lgkmcnt(0)\n\t" \
    "s_barrier \n\t" \
    "s_add_u32 m0, %[wm], 0xc000\n\t" \
    "s_mov_b32 s44, 0x3c0000\n\t" \
    "buffer_load_dwordx4 v240, %[wr], s44 offen offset:0 nt lds\n\t" \
    "buffer_load_dwordx4 v241, %[wr], s44 offen offset:1024 nt lds\n\t" \
    "buffer_load_dwordx4 v242, %[wr], s44 offen offset:2048 nt lds\n\t" \
    "ds_read_b64_tr_b16 v[220:221], v244 offset:0\n\t" \
    "ds_read_b64_tr_b16 v[222:223], v244 offset:1536\n\t" \
    "ds_read_b64_tr_b16 v[84:85], v244 offset:32\n\t" \
    "ds_read_b64_tr_b16 v[86:87], v244 offset:1568\n\t" \
    "ds_read_b64_tr_b16 v[92:93], v244 offset:64\n\t" \
    "ds_read_b64_tr_b16 v[94:95], v244 offset:1600\n\t" \
    "v_add_u32 v244, 0x18000, v243\n\t" \
    "s_waitcnt vmcnt(12) lgkmcnt(0)\n\t" \
    "s_barrier \n\t" \
    "s_add_u32 m0, %[wm], 0x12000\n\t" \
    "s_mov_b32 s44, 0x420000\n\t" \
    "buffer_load_dwordx4 v240, %[wr], s44 offen offset:0 nt lds\n\t" \
    "buffer_load_dwordx4 v241, %[wr], s44 offen offset:1024 nt lds\n\t" \
    "buffer_load_dwordx4 v242, %[wr], s44 offen offset:2048 nt lds\n\t" \
    "ds_read_b64_tr_b16 v[224:225], v244 offset:0\n\t" \
    "ds_read_b64_tr_b16 v[226:227], v244 offset:1536\n\t" \
    "ds_read_b64_tr_b16 v[120:121], v244 offset:32\n\t" \
    "ds_read_b64_tr_b16 v[122:123], v244 offset:1568\n\t" \
    "ds_read_b64_tr_b16 v[128:129], v244 offset:64\n\t" \
    "ds_read_b64_tr_b16 v[130:131], v244 offset:1600\n\t" \
    "v_add_u32 v244, 0x1e000, v243\n\t" \
    "s_waitcnt vmcnt(12) lgkmcnt(0)\n\t" \
    "s_barrier \n\t" \
    "s_add_u32 m0, %[wm], 0x18000\n\t" \
    "s_mov_b32 s44, 0x480000\n\t" \
    "buffer_load_dwordx4 v240, %[wr], s44 offen offset:0 nt lds\n\t" \
    "buffer_load_dwordx4 v241, %[wr], s44 offen offset:1024 nt lds\n\t" \
    "buffer_load_dwordx4 v242, %[wr], s44 offen offset:2048 nt lds\n\t" \
    "ds_read_b64_tr_b16 v[228:229], v244 offset:0\n\t" \
    "ds_read_b64_tr_b16 v[230:231], v244 offset:1536\n\t" \
    "ds_read_b64_tr_b16 v[124:125], v244 offset:32\n\t" \
    "ds_read_b64_tr_b16 v[126:127], v244 offset:1568\n\t" \
    "ds_read_b64_tr_b16 v[132:133], v244 offset:64\n\t" \
    "ds_read_b64_tr_b16 v[134:135], v244 offset:1600\n\t" \
    "v_add_u32 v244, 0x0, v243\n\t" \
    "s_waitcnt vmcnt(12) lgkmcnt(0)\n\t" \
    "s_barrier \n\t" \
    "s_add_u32 m0, %[wm], 0x1e000\n\t" \
    "s_mov_b32 s44, 0x4e0000\n\t" \
    "buffer_load_dwordx4 v240, %[wr], s44 offen offset:0 nt lds\n\t" \
    "buffer_load_dwordx4 v241, %[wr], s44 offen offset:1024 nt lds\n\t" \
    "buffer_load_dwordx4 v242, %[wr], s44 offen offset:2048 nt lds\n\t" \
    "ds_read_b64_tr_b16 v[232:233], v244 offset:0\n\t" \
    "ds_read_b64_tr_b16 v[234:235], v244 offset:1536\n\t" \
    "ds_read_b64_tr_b16 v[160:161], v244 offset:32\n\t" \
    "ds_read_b64_tr_b16 v[162:163], v244 offset:1568\n\t" \
    "ds_read_b64_tr_b16 v[168:169], v244 offset:64\n\t" \
    "ds_read_b64_tr_b16 v[170:171], v244 offset:1600\n\t" \
    "v_add_u32 v244, 0x6000, v243\n\t" \
    "s_waitcnt vmcnt(12) lgkmcnt(0)\n\t" \
    "s_barrier \n\t" \
    "s_add_u32 m0, %[wm], 0x0\n\t" \
    "s_mov_b32 s44, 0x540000\n\t" \
    "buffer_load_dwordx4 v240, %[wr], s44 offen offset:0 nt lds\n\t" \
    "buffer_load_dwordx4 v241, %[wr], s44 offen offset:1024 nt lds\n\t" \
    "buffer_load_dwordx4 v242, %[wr], s44 offen offset:2048 nt lds\n\t" \
    "ds_read_b64_tr_b16 v[236:237], v244 offset:0\n\t" \
    "ds_read_b64_tr_b16 v[238:239], v244 offset:1536\n\t" \
    "ds_read_b64_tr_b16 v[164:165], v244 offset:32\n\t" \
    "ds_read_b64_tr_b16 v[166:167], v244 offset:1568\n\t" \
    "ds_read_b64_tr_b16 v[172:173], v244 offset:64\n\t" \
    "ds_read_b64_tr_b16 v[174:175], v244 offset:1600\n\t" \
    "v_add_u32 v244, 0xc000, v243\n\t" \
    "s_waitcnt vmcnt(12) lgkmcnt(0)\n\t" \
    "s_barrier \n\t" \
    "s_add_u32 m0, %[wm], 0x6000\n\t" \
    "s_mov_b32 s44, 0x5a0000\n\t" \
    "buffer_load_dwordx4 v240, %[wr], s44 offen offset:0 nt lds\n\t" \
    "buffer_load_dwordx4 v241, %[wr], s44 offen offset:1024 nt lds\n\t" \
    "buffer_load_dwordx4 v242, %[wr], s44 offen offset:2048 nt lds\n\t" \
    "ds_read_b64_tr_b16 v[16:17], v244 offset:0\n\t" \
    "ds_read_b64_tr_b16 v[18:19], v244 offset:1536\n\t" \
    "ds_read_b64_tr_b16 v[24:25], v244 offset:32\n\t" \
    "ds_read_b64_tr_b16 v[26:27], v244 offset:1568\n\t" \
    "ds_read_b64_tr_b16 v[32:33], v244 offset:64\n\t" \
    "ds_read_b64_tr_b16 v[34:35], v244 offset:1600\n\t" \
    "v_add_u32 v244, 0x12000, v243\n\t" \
    "s_waitcnt vmcnt(12) lgkmcnt(0)\n\t" \
    "s_barrier \n\t" \
    "s_add_u32 m0, %[wm], 0xc000\n\t" \
    "s_mov_b32 s44, 0x600000\n\t" \
    "buffer_load_dwordx4 v240, %[wr], s44 offen offset:0 nt lds\n\t" \
    "buffer_load_dwordx4 v241, %[wr], s44 offen offset:1024 nt lds\n\t" \
    "buffer_load_dwordx4 v242, %[wr], s44 offen offset:2048 nt lds\n\t" \
    "ds_read_b64_tr_b16 v[20:21], v244 offset:0\n\t" \
    "ds_read_b64_tr_b16 v[22:23], v244 offset:1536\n\t" \
    "ds_read_b64_tr_b16 v[28:29], v244 offset:32\n\t" \
    "ds_read_b64_tr_b16 v[30:31], v244 offset:1568\n\t" \
    "ds_read_b64_tr_b16 v[36:37], v244 offset:64\n\t" \
    "ds_read_b64_tr_b16 v[38:39], v244 offset:1600\n\t" \
    "v_add_u32 v244, 0x18000, v243\n\t" \
    "s_waitcnt vmcnt(12) lgkmcnt(0)\n\t" \
    "s_barrier \n\t" \
    "s_add_u32 m0, %[wm], 0x12000\n\t" \
    "s_mov_b32 s44, 0x660000\n\t" \
    "buffer_load_dwordx4 v240, %[wr], s44 offen offset:0 nt lds\n\t" \
    "buffer_load_dwordx4 v241, %[wr], s44 offen offset:1024 nt lds\n\t" \
    "buffer_load_dwordx4 v242, %[wr], s44 offen offset:2048 nt lds\n\t" \
    "ds_read_b64_tr_b16 v[56:57], v244 offset:0\n\t" \
    "ds_read_b64_tr_b16 v[58:59], v244 offset:1536\n\t" \
    "ds_read_b64_tr_b16 v[64:65], v244 offset:32\n\t" \
    "ds_read_b64_tr_b16 v[66:67], v244 offset:1568\n\t" \
    "ds_read_b64_tr_b16 v[72:73], v244 offset:64\n\t" \
    "ds_read_b64_tr_b16 v[74:75], v244 offset:1600\n\t" \
    "v_add_u32 v244, 0x1e000, v243\n\t" \
    "s_waitcnt vmcnt(12) lgkmcnt(0)\n\t" \
    "s_barrier \n\t" \
    "s_add_u32 m0, %[wm], 0x18000\n\t" \
    "s_mov_b32 s44, 0x6c0000\n\t" \
    "buffer_load_dwordx4 v240, %[wr], s44 offen offset:0 nt lds\n\t" \
    "buffer_load_dwordx4 v241, %[wr], s44 offen offset:1024 nt lds\n\t" \
    "buffer_load_dwordx4 v242, %[wr], s44 offen offset:2048 nt lds\n\t" \
    "ds_read_b64_tr_b16 v[60:61], v244 offset:0\n\t" \
    "ds_read_b64_tr_b16 v[62:63], v244 offset:1536\n\t" \
    "ds_read_b64_tr_b16 v[68:69], v244 offset:32\n\t" \
    "ds_read_b64_tr_b16 v[70:71], v244 offset:1568\n\t" \
    "ds_read_b64_tr_b16 v[76:77], v244 offset:64\n\t" \
    "ds_read_b64_tr_b16 v[78:79], v244 offset:1600\n\t" \
    "v_add_u32 v244, 0x0, v243\n\t" \
    "s_waitcnt vmcnt(12) lgkmcnt(0)\n\t" \
    "s_barrier \n\t" \
    "s_add_u32 m0, %[wm], 0x1e000\n\t" \
    "s_mov_b32 s44, 0x720000\n\t" \
    "buffer_load_dwordx4 v240, %[wr], s44 offen offset:0 nt lds\n\t" \
    "buffer_load_dwordx4 v241, %[wr], s44 offen offset:1024 nt lds\n\t" \
    "buffer_load_dwordx4 v242, %[wr], s44 offen offset:2048 nt lds\n\t" \
    "ds_read_b64_tr_b16 v[96:97], v244 offset:0\n\t" \
    "ds_read_b64_tr_b16 v[98:99], v244 offset:1536\n\t" \
    "ds_read_b64_tr_b16 v[104:105], v244 offset:32\n\t" \
    "ds_read_b64_tr_b16 v[106:107], v244 offset:1568\n\t" \
    "ds_read_b64_tr_b16 v[112:113], v244 offset:64\n\t" \
    "ds_read_b64_tr_b16 v[114:115], v244 offset:1600\n\t" \
    "v_add_u32 v244, 0x6000, v243\n\t" \
    "s_waitcnt vmcnt(12) lgkmcnt(0)\n\t" \
    "s_barrier \n\t" \
    "ds_read_b64_tr_b16 v[100:101], v244 offset:0\n\t" \
    "ds_read_b64_tr_b16 v[102:103], v244 offset:1536\n\t" \
    "ds_read_b64_tr_b16 v[108:109], v244 offset:32\n\t" \
    "ds_read_b64_tr_b16 v[110:111], v244 offset:1568\n\t" \
    "ds_read_b64_tr_b16 v[116:117], v244 offset:64\n\t" \
    "ds_read_b64_tr_b16 v[118:119], v244 offset:1600\n\t" \
    "v_add_u32 v244, 0xc000, v243\n\t" \
    "s_waitcnt vmcnt(9) lgkmcnt(0)\n\t" \
    "s_barrier \n\t" \
    "ds_read_b64_tr_b16 v[136:137], v244 offset:0\n\t" \
    "ds_read_b64_tr_b16 v[138:139], v244 offset:1536\n\t" \
    "ds_read_b64_tr_b16 v[144:145], v244 offset:32\n\t" \
    "ds_read_b64_tr_b16 v[146:147], v244 offset:1568\n\t" \
    "ds_read_b64_tr_b16 v[152:153], v244 offset:64\n\t" \
    "ds_read_b64_tr_b16 v[154:155], v244 offset:1600\n\t" \
    "v_add_u32 v244, 0x12000, v243\n\t" \
    "s_waitcnt vmcnt(6) lgkmcnt(0)\n\t" \
    "s_barrier \n\t" \
    "ds_read_b64_tr_b16 v[140:141], v244 offset:0\n\t" \
    "ds_read_b64_tr_b16 v[142:143], v244 offset:1536\n\t" \
    "ds_read_b64_tr_b16 v[148:149], v244 offset:32\n\t" \
    "ds_read_b64_tr_b16 v[150:151], v244 offset:1568\n\t" \
    "ds_read_b64_tr_b16 v[156:157], v244 offset:64\n\t" \
    "ds_read_b64_tr_b16 v[158:159], v244 offset:1600\n\t" \
    "v_add_u32 v244, 0x18000, v243\n\t" \
    "s_waitcnt vmcnt(3) lgkmcnt(0)\n\t" \
    "s_barrier \n\t" \
    "ds_read_b64_tr_b16 v[176:177], v244 offset:0\n\t" \
    "ds_read_b64_tr_b16 v[178:179], v244 offset:1536\n\t" \
    "ds_read_b64_tr_b16 v[184:185], v244 offset:32\n\t" \
    "ds_read_b64_tr_b16 v[186:187], v244 offset:1568\n\t" \
    "ds_read_b64_tr_b16 v[192:193], v244 offset:64\n\t" \
    "ds_read_b64_tr_b16 v[194:195], v244 offset:1600\n\t" \
    "v_add_u32 v244, 0x1e000, v243\n\t" \
    "s_waitcnt vmcnt(0) lgkmcnt(0)\n\t" \
    "s_barrier \n\t" \
    "ds_read_b64_tr_b16 v[180:181], v244 offset:0\n\t" \
    "ds_read_b64_tr_b16 v[182:183], v244 offset:1536\n\t" \
    "ds_read_b64_tr_b16 v[188:189], v244 offset:32\n\t" \
    "ds_read_b64_tr_b16 v[190:191], v244 offset:1568\n\t" \
    "ds_read_b64_tr_b16 v[196:197], v244 offset:64\n\t" \
    "ds_read_b64_tr_b16 v[198:199], v244 offset:1600\n\t" \
    "s_waitcnt lgkmcnt(0)\n\t" \
    "s_barrier \n\t" \
    "ds_write_b128 v254, v[200:203] offset:30720\n\t" \
    "ds_write_b128 v254, v[204:207] offset:31744\n\t" \
    "ds_write_b128 v254, v[208:211] offset:32768\n\t" \
    "ds_write_b128 v254, v[212:215] offset:33792\n\t" \
    "ds_write_b128 v254, v[216:219] offset:34816\n\t" \
    "ds_write_b128 v254, v[220:223] offset:35840\n\t" \
    "ds_write_b128 v254, v[224:227] offset:36864\n\t" \
    "ds_write_b128 v254, v[228:231] offset:37888\n\t" \
    "ds_write_b128 v254, v[232:235] offset:38912\n\t" \
    "ds_write_b128 v254, v[236:239] offset:39936\n\t" \
    "s_waitcnt vmcnt(0) lgkmcnt(0)\n\t" \
    "s_barrier \n\t" \
    "ds_read_b128 v[224:227], v253 offset:0\n\t" \
    "ds_read_b128 v[228:231], v253 offset:1024\n\t" \
    "ds_read_b128 v[232:235], v253 offset:20480\n\t" \
    "ds_read_b128 v[236:239], v253 offset:21504\n\t" \
    ".Ls01_%=:\n\t" \
    "s_waitcnt lgkmcnt(3)\n\t" \
    "v_mfma_f32_16x16x32_bf16 v[204:207], v[0:3], v[224:227], 0\n\t" \
    "s_waitcnt lgkmcnt(2)\n\t" \
    "v_mfma_f32_16x16x32_bf16 v[204:207], v[4:7], v[228:231], v[204:207]\n\t" \
    "ds_read_b128 v[240:243], v253 offset:2048\n\t" \
    "v_mfma_f32_16x16x32_bf16 v[208:211], v[8:11], v[224:227], 0\n\t" \
    "v_mfma_f32_16x16x32_bf16 v[208:211], v[12:15], v[228:231], v[208:211]\n\t" \
    "s_waitcnt lgkmcnt(2)\n\t" \
    "v_mfma_f32_16x16x32_bf16 v[216:219], v[0:3], v[232:235], 0\n\t" \
    "ds_read_b128 v[224:227], v253 offset:3072\n\t" \
    "s_waitcnt lgkmcnt(2)\n\t" \
    "v_mfma_f32_16x16x32_bf16 v[216:219], v[4:7], v[236:239], v[216:219]\n\t" \
    "ds_read_b128 v[228:231], v253 offset:22528\n\t" \
    "v_mfma_f32_16x16x32_bf16 v[220:223], v[8:11], v[232:235], 0\n\t" \
    "v_mfma_f32_16x16x32_bf16 v[220:223], v[12:15], v[236:239], v[220:223]\n\t" \
    "s_waitcnt lgkmcnt(2)\n\t" \
    "v_mfma_f32_16x16x32_bf16 v[204:207], v[40:43], v[240:243], v[204:207]\n\t" \
    "ds_read_b128 v[232:235], v253 offset:23552\n\t" \
    "s_waitcnt lgkmcnt(2)\n\t" \
    "v_mfma_f32_16x16x32_bf16 v[204:207], v[44:47], v[224:227], v[204:207]\n\t" \
    "ds_read_b128 v[236:239], v253 offset:4096\n\t" \
    "v_mfma_f32_16x16x32_bf16 v[208:211], v[48:51], v[240:243], v[208:211]\n\t" \
    "v_mfma_f32_16x16x32_bf16 v[208:211], v[52:55], v[224:227], v[208:211]\n\t" \
    "s_waitcnt lgkmcnt(2)\n\t" \
    "v_mfma_f32_16x16x32_bf16 v[216:219], v[40:43], v[228:231], v[216:219]\n\t" \
    "ds_read_b128 v[240:243], v253 offset:5120\n\t" \
    "s_waitcnt lgkmcnt(2)\n\t" \
    "v_mfma_f32_16x16x32_bf16 v[216:219], v[44:47], v[232:235], v[216:219]\n\t" \
    "ds_read_b128 v[224:227], v253 offset:24576\n\t" \
    "v_mfma_f32_16x16x32_bf16 v[220:223], v[48:51], v[228:231], v[220:223]\n\t" \
    "v_mfma_f32_16x16x32_bf16 v[220:223], v[52:55], v[232:235], v[220:223]\n\t" \
    "s_waitcnt lgkmcnt(2)\n\t" \
    "v_mfma_f32_16x16x32_bf16 v[204:207], v[80:83], v[236:239], v[204:207]\n\t" \
    "ds_read_b128 v[228:231], v253 offset:25600\n\t" \
    "s_waitcnt lgkmcnt(2)\n\t" \
    "v_mfma_f32_16x16x32_bf16 v[204:207], v[84:87], v[240:243], v[204:207]\n\t" \
    "ds_read_b128 v[232:235], v253 offset:6144\n\t" \
    "v_mfma_f32_16x16x32_bf16 v[208:211], v[88:91], v[236:239], v[208:211]\n\t" \
    "v_mfma_f32_16x16x32_bf16 v[208:211], v[92:95], v[240:243], v[208:211]\n\t" \
    "s_waitcnt lgkmcnt(2)\n\t" \
    "v_mfma_f32_16x16x32_bf16 v[216:219], v[80:83], v[224:227], v[216:219]\n\t" \
    "ds_read_b128 v[236:239], v253 offset:7168\n\t" \
    "s_waitcnt lgkmcnt(2)\n\t" \
    "v_mfma_f32_16x16x32_bf16 v[216:219], v[84:87], v[228:231], v[216:219]\n\t" \
    "ds_read_b128 v[240:243], v253 offset:26624\n\t" \
    "v_mfma_f32_16x16x32_bf16 v[220:223], v[88:91], v[224:227], v[220:223]\n\t" \
    "v_mfma_f32_16x16x32_bf16 v[220:223], v[92:95], v[228:231], v[220:223]\n\t" \
    "s_waitcnt lgkmcnt(2)\n\t" \
    "v_mfma_f32_16x16x32_bf16 v[204:207], v[120:123], v[232:235], v[204:207]\n\t" \
    "ds_read_b128 v[224:227], v253 offset:27648\n\t" \
    "s_waitcnt lgkmcnt(2)\n\t" \
    "v_mfma_f32_16x16x32_bf16 v[204:207], v[124:127], v[236:239], v[204:207]\n\t" \
    "ds_read_b128 v[228:231], v253 offset:8192\n\t" \
    "v_mfma_f32_16x16x32_bf16 v[208:211], v[128:131], v[232:235], v[208:211]\n\t" \
    "v_mfma_f32_16x16x32_bf16 v[208:211], v[132:135], v[236:239], v[208:211]\n\t" \
    "s_waitcnt lgkmcnt(2)\n\t" \
    "v_mfma_f32_16x16x32_bf16 v[216:219], v[120:123], v[240:243], v[216:219]\n\t" \
    "ds_read_b128 v[232:235], v253 offset:9216\n\t" \
    "s_waitcnt lgkmcnt(2)\n\t" \
    "v_mfma_f32_16x16x32_bf16 v[216:219], v[124:127], v[224:227], v[216:219]\n\t" \
    "ds_read_b128 v[236:239], v253 offset:28672\n\t" \
    "v_mfma_f32_16x16x32_bf16 v[220:223], v[128:131], v[240:243], v[220:223]\n\t" \
    "v_mfma_f32_16x16x32_bf16 v[220:223], v[132:135], v[224:227], v[220:223]\n\t" \
    "s_waitcnt lgkmcnt(2)\n\t" \
    "v_mfma_f32_16x16x32_bf16 v[204:207], v[160:163], v[228:231], v[204:207]\n\t" \
    "ds_read_b128 v[240:243], v253 offset:29696\n\t" \
    "s_waitcnt lgkmcnt(2)\n\t" \
    "v_mfma_f32_16x16x32_bf16 v[204:207], v[164:167], v[232:235], v[204:207]\n\t" \
    "ds_read_b128 v[224:227], v253 offset:10240\n\t" \
    "v_mfma_f32_16x16x32_bf16 v[208:211], v[168:171], v[228:231], v[208:211]\n\t" \
    "v_mfma_f32_16x16x32_bf16 v[208:211], v[172:175], v[232:235], v[208:211]\n\t" \
    "s_waitcnt lgkmcnt(2)\n\t" \
    "v_mfma_f32_16x16x32_bf16 v[216:219], v[160:163], v[236:239], v[216:219]\n\t" \
    "ds_read_b128 v[228:231], v253 offset:11264\n\t" \
    "s_waitcnt lgkmcnt(2)\n\t" \
    "v_mfma_f32_16x16x32_bf16 v[216:219], v[164:167], v[240:243], v[216:219]\n\t" \
    "ds_read_b128 v[232:235], v253 offset:30720\n\t" \
    "v_mfma_f32_16x16x32_bf16 v[220:223], v[168:171], v[236:239], v[220:223]\n\t" \
    "v_mfma_f32_16x16x32_bf16 v[220:223], v[172:175], v[240:243], v[220:223]\n\t" \
    "ds_read_b128 v[236:239], v253 offset:31744\n\t" \
    "s_waitcnt lgkmcnt(0)\n\t" \
    "s_barrier \n\t" \
    LST7 \
    "ds_read_b128 v[200:203], v253 offset:40960\n\t" \
    "ds_read_b128 v[212:215], v253 offset:41984\n\t" \
    "v_mfma_f32_16x16x32_bf16 v[204:207], v[24:27], v[224:227], v[204:207]\n\t" \
    "v_mfma_f32_16x16x32_bf16 v[204:207], v[28:31], v[228:231], v[204:207]\n\t" \
    "ds_read_b128 v[240:243], v253 offset:12288\n\t" \
    "v_mfma_f32_16x16x32_bf16 v[208:211], v[32:35], v[224:227], v[208:211]\n\t" \
    "v_mfma_f32_16x16x32_bf16 v[208:211], v[36:39], v[228:231], v[208:211]\n\t" \
    "s_waitcnt lgkmcnt(2)\n\t" \
    "v_mfma_f32_16x16x32_bf16 v[200:203], v[16:19], v[224:227], v[200:203]\n\t" \
    "v_mfma_f32_16x16x32_bf16 v[200:203], v[20:23], v[228:231], v[200:203]\n\t" \
    "v_mfma_f32_16x16x32_bf16 v[216:219], v[24:27], v[232:235], v[216:219]\n\t" \
    "ds_read_b128 v[224:227], v253 offset:13312\n\t" \
    "v_mfma_f32_16x16x32_bf16 v[216:219], v[28:31], v[236:239], v[216:219]\n\t" \
    "ds_read_b128 v[228:231], v253 offset:32768\n\t" \
    "v_mfma_f32_16x16x32_bf16 v[220:223], v[32:35], v[232:235], v[220:223]\n\t" \
    "v_mfma_f32_16x16x32_bf16 v[220:223], v[36:39], v[236:239], v[220:223]\n\t" \
    "s_waitcnt lgkmcnt(3)\n\t" \
    "v_mfma_f32_16x16x32_bf16 v[212:215], v[16:19], v[232:235], v[212:215]\n\t" \
    "v_mfma_f32_16x16x32_bf16 v[212:215], v[20:23], v[236:239], v[212:215]\n\t" \
    "s_waitcnt lgkmcnt(2)\n\t" \
    "v_mfma_f32_16x16x32_bf16 v[204:207], v[64:67], v[240:243], v[204:207]\n\t" \
    "ds_read_b128 v[232:235], v253 offset:33792\n\t" \
    "s_and_b32 s44, s40, 3\n\t" \
    "s_waitcnt lgkmcnt(2)\n\t" \
    "v_mfma_f32_16x16x32_bf16 v[204:207], v[68:71], v[224:227], v[204:207]\n\t" \
    "ds_read_b128 v[236:239], v253 offset:14336\n\t" \
    "s_lshl_b32 s44, s44, 17\n\t" \
    "v_mfma_f32_16x16x32_bf16 v[208:211], v[72:75], v[240:243], v[208:211]\n\t" \
    "s_add_u32 s44, s44, %[mb]\n\t" \
    "v_mfma_f32_16x16x32_bf16 v[208:211], v[76:79], v[224:227], v[208:211]\n\t" \
    "s_add_u32 s46, s44, 0x0\n\t" \
    "v_mfma_f32_16x16x32_bf16 v[200:203], v[56:59], v[240:243], v[200:203]\n\t" \
    "s_add_u32 s47, s44, 0x1400\n\t" \
    "v_mfma_f32_16x16x32_bf16 v[200:203], v[60:63], v[224:227], v[200:203]\n\t" \
    "s_add_u32 s48, s44, 0x400\n\t" \
    "s_waitcnt lgkmcnt(2)\n\t" \
    "v_mfma_f32_16x16x32_bf16 v[216:219], v[64:67], v[228:231], v[216:219]\n\t" \
    "ds_read_b128 v[240:243], v253 offset:15360\n\t" \
    "s_waitcnt lgkmcnt(2)\n\t" \
    "v_mfma_f32_16x16x32_bf16 v[216:219], v[68:71], v[232:235], v[216:219]\n\t" \
    "ds_read_b128 v[224:227], v253 offset:34816\n\t" \
    "v_mfma_f32_16x16x32_bf16 v[220:223], v[72:75], v[228:231], v[220:223]\n\t" \
    "v_mfma_f32_16x16x32_bf16 v[220:223], v[76:79], v[232:235], v[220:223]\n\t" \
    "v_mfma_f32_16x16x32_bf16 v[212:215], v[56:59], v[228:231], v[212:215]\n\t" \
    "v_mfma_f32_16x16x32_bf16 v[212:215], v[60:63], v[232:235], v[212:215]\n\t" \
    "s_waitcnt lgkmcnt(2)\n\t" \
    "v_mfma_f32_16x16x32_bf16 v[204:207], v[104:107], v[236:239], v[204:207]\n\t" \
    "ds_read_b128 v[228:231], v253 offset:35840\n\t" \
    "s_waitcnt lgkmcnt(2)\n\t" \
    "v_mfma_f32_16x16x32_bf16 v[204:207], v[108:111], v[240:243], v[204:207]\n\t" \
    "ds_read_b128 v[232:235], v253 offset:16384\n\t" \
    "v_mfma_f32_16x16x32_bf16 v[208:211], v[112:115], v[236:239], v[208:211]\n\t" \
    "v_mfma_f32_16x16x32_bf16 v[208:211], v[116:119], v[240:243], v[208:211]\n\t" \
    "v_mfma_f32_16x16x32_bf16 v[200:203], v[96:99], v[236:239], v[200:203]\n\t" \
    "v_mfma_f32_16x16x32_bf16 v[200:203], v[100:103], v[240:243], v[200:203]\n\t" \
    "s_waitcnt lgkmcnt(2)\n\t" \
    "v_mfma_f32_16x16x32_bf16 v[216:219], v[104:107], v[224:227], v[216:219]\n\t" \
    "ds_read_b128 v[236:239], v253 offset:17408\n\t" \
    "s_waitcnt lgkmcnt(2)\n\t" \
    "v_mfma_f32_16x16x32_bf16 v[216:219], v[108:111], v[228:231], v[216:219]\n\t" \
    "ds_read_b128 v[240:243], v253 offset:18432\n\t" \
    "v_mfma_f32_16x16x32_bf16 v[220:223], v[112:115], v[224:227], v[220:223]\n\t" \
    "v_mfma_f32_16x16x32_bf16 v[220:223], v[116:119], v[228:231], v[220:223]\n\t" \
    "v_mfma_f32_16x16x32_bf16 v[212:215], v[96:99], v[224:227], v[212:215]\n\t" \
    "v_mfma_f32_16x16x32_bf16 v[212:215], v[100:103], v[228:231], v[212:215]\n\t" \
    "s_waitcnt lgkmcnt(2)\n\t" \
    "v_mfma_f32_16x16x32_bf16 v[204:207], v[144:147], v[232:235], v[204:207]\n\t" \
    "ds_read_b128 v[224:227], v253 offset:19456\n\t" \
    "v_mov_b64 v[244:245], 0\n\t" \
    "s_waitcnt lgkmcnt(2)\n\t" \
    "v_mfma_f32_16x16x32_bf16 v[204:207], v[148:151], v[236:239], v[204:207]\n\t" \
    "ds_read_b128 v[228:231], v253 offset:36864\n\t" \
    "v_mov_b64 v[246:247], 0\n\t" \
    "v_mfma_f32_16x16x32_bf16 v[208:211], v[152:155], v[232:235], v[208:211]\n\t" \
    "v_mov_b64 v[248:249], 0\n\t" \
    "v_mfma_f32_16x16x32_bf16 v[208:211], v[156:159], v[236:239], v[208:211]\n\t" \
    "v_mov_b64 v[250:251], 0\n\t" \
    "v_mfma_f32_16x16x32_bf16 v[200:203], v[136:139], v[232:235], v[200:203]\n\t" \
    "v_mfma_f32_16x16x32_bf16 v[200:203], v[140:143], v[236:239], v[200:203]\n\t" \
    "s_waitcnt lgkmcnt(2)\n\t" \
    "v_mfma_f32_16x16x32_bf16 v[204:207], v[184:187], v[240:243], v[204:207]\n\t" \
    "ds_read_b128 v[232:235], v253 offset:37888\n\t" \
    "s_waitcnt lgkmcnt(2)\n\t" \
    "v_mfma_f32_16x16x32_bf16 v[204:207], v[188:191], v[224:227], v[204:207]\n\t" \
    "ds_read_b128 v[236:239], v253 offset:38912\n\t" \
    "v_mfma_f32_16x16x32_bf16 v[208:211], v[192:195], v[240:243], v[208:211]\n\t" \
    "v_mfma_f32_16x16x32_bf16 v[208:211], v[196:199], v[224:227], v[208:211]\n\t" \
    "v_mfma_f32_16x16x32_bf16 v[200:203], v[176:179], v[240:243], v[200:203]\n\t" \
    "v_mfma_f32_16x16x32_bf16 v[200:203], v[180:183], v[224:227], v[200:203]\n\t" \
    "ds_read_b128 v[240:243], v253 offset:39936\n\t" \
    "s_waitcnt lgkmcnt(3)\n\t" \
    "v_mfma_f32_16x16x32_bf16 v[216:219], v[144:147], v[228:231], v[216:219]\n\t" \
    "s_waitcnt lgkmcnt(2)\n\t" \
    "v_mfma_f32_16x16x32_bf16 v[216:219], v[148:151], v[232:235], v[216:219]\n\t" \
    "v_cvt_pk_bf16_f32 v204, v204, v208\n\t" \
    "v_cvt_pk_bf16_f32 v205, v205, v209\n\t" \
    "s_waitcnt lgkmcnt(1)\n\t" \
    "v_mfma_f32_16x16x32_bf16 v[216:219], v[184:187], v[236:239], v[216:219]\n\t" \
    "v_cvt_pk_bf16_f32 v206, v206, v210\n\t" \
    "v_cvt_pk_bf16_f32 v207, v207, v211\n\t" \
    "s_waitcnt lgkmcnt(0)\n\t" \
    "v_mfma_f32_16x16x32_bf16 v[216:219], v[188:191], v[240:243], v[216:219]\n\t" \
    "ds_write_b128 v254, v[204:207] offset:8192\n\t" \
    "v_cvt_pk_bf16_f32 v200, v200, v201\n\t" \
    "v_mfma_f32_16x16x32_bf16 v[220:223], v[152:155], v[228:231], v[220:223]\n\t" \
    "v_cvt_pk_bf16_f32 v201, v202, v203\n\t" \
    "ds_write_b64 v254, v[200:201] offset:6144\n\t" \
    "v_mfma_f32_16x16x32_bf16 v[220:223], v[156:159], v[232:235], v[220:223]\n\t" \
    "ds_read_b128 v[200:203], v254 offset:30720\n\t" \
    "ds_read_b128 v[204:207], v254 offset:31744\n\t" \
    "v_mfma_f32_16x16x32_bf16 v[220:223], v[192:195], v[236:239], v[220:223]\n\t" \
    "v_mov_b64 v[208:209], 0\n\t" \
    "v_mov_b64 v[210:211], 0\n\t" \
    "v_mfma_f32_16x16x32_bf16 v[220:223], v[196:199], v[240:243], v[220:223]\n\t" \
    "v_mfma_f32_16x16x32_bf16 v[212:215], v[136:139], v[228:231], v[212:215]\n\t" \
    "v_mfma_f32_16x16x32_bf16 v[212:215], v[140:143], v[232:235], v[212:215]\n\t" \
    "v_mfma_f32_16x16x32_bf16 v[212:215], v[176:179], v[236:239], v[212:215]\n\t" \
    "v_cvt_pk_bf16_f32 v216, v216, v220\n\t" \
    "v_cvt_pk_bf16_f32 v217, v217, v221\n\t" \
    "v_mfma_f32_16x16x32_bf16 v[212:215], v[180:183], v[240:243], v[212:215]\n\t" \
    "v_cvt_pk_bf16_f32 v218, v218, v222\n\t" \
    "v_cvt_pk_bf16_f32 v219, v219, v223\n\t" \
    "ds_write_b128 v254, v[216:219] offset:14336\n\t" \
    "ds_read_b128 v[216:219], v254 offset:32768\n\t" \
    "ds_read_b128 v[220:223], v254 offset:33792\n\t" \
    "s_nop 2\n\t" \
    "v_cvt_pk_bf16_f32 v212, v212, v213\n\t" \
    "v_cvt_pk_bf16_f32 v213, v214, v215\n\t" \
    "ds_write_b64 v254, v[212:213] offset:12288\n\t" \
    LST6 \
    "s_sleep 8\n\t" \
    "buffer_load_dwordx4 v[224:227], v253, s[60:63], s46 offen sc1\n\t" \
    "buffer_load_dwordx4 v[228:231], v253, s[60:63], s47 offen sc1\n\t" \
    "buffer_load_dwordx4 v[232:235], v253, s[60:63], s48 offen sc1\n\t" \
    "s_waitcnt lgkmcnt(0)\n\t" \
    LFLUSH212 \
    "v_bfe_i32 v212, v253, 5, 1\n\t" \
    "v_and_b32 v212, 0xaaaa, v212\n\t" \
    "v_or_b32 v212, 0x4444, v212\n\t" \
    "v_and_b32 v213, 0xffff, v255\n\t" \
    "v_lshrrev_b32 v214, 16, v255\n\t" \
    ".Ls02_%=:\n\t" \
    "s_waitcnt vmcnt(2)\n\t" \
    "v_max_u32 v215, v224, v225\n\t" \
    "v_max3_u32 v215, v226, v227, v215\n\t" \
    "s_waitcnt vmcnt(1)\n\t" \
    "v_max3_u32 v215, v228, v229, v215\n\t" \
    "v_max3_u32 v215, v230, v231, v215\n\t" \
    "s_waitcnt vmcnt(0)\n\t" \
    "v_max3_u32 v215, v232, v233, v215\n\t" \
    "v_max3_u32 v215, v234, v235, v215\n\t" \
    "v_cmp_eq_u32 vcc, -1, v215\n\t" \
    "s_cbranch_vccz .Ls03_%=\n\t" \
    "v_max_u32 v215, v224, v225\n\t" \
    "v_max3_u32 v215, v226, v227, v215\n\t" \
    "v_cmp_eq_u32 vcc, -1, v215\n\t" \
    "s_cbranch_vccz .Ls04_%=\n\t" \
    "buffer_load_dwordx4 v[224:227], v253, s[60:63], s46 offen sc1\n\t" \
    ".Ls04_%=:\n\t" \
    "v_max_u32 v215, v228, v229\n\t" \
    "v_max3_u32 v215, v230, v231, v215\n\t" \
    "v_cmp_eq_u32 vcc, -1, v215\n\t" \
    "s_cbranch_vccz .Ls05_%=\n\t" \
    "buffer_load_dwordx4 v[228:231], v253, s[60:63], s47 offen sc1\n\t" \
    ".Ls05_%=:\n\t" \
    "v_max_u32 v215, v232, v233\n\t" \
    "v_max3_u32 v215, v234, v235, v215\n\t" \
    "v_cmp_eq_u32 vcc, -1, v215\n\t" \
    "s_cbranch_vccz .Ls06_%=\n\t" \
    "buffer_load_dwordx4 v[232:235], v253, s[60:63], s48 offen sc1\n\t" \
    ".Ls06_%=:\n\t" \
    "s_cmp_eq_u32 s41, 0\n\t" \
    "s_cbranch_scc1 .Ls03_%=\n\t" \
    "s_sub_u32 s41, s41, 1\n\t" \
    "s_waitcnt vmcnt(0)\n\t" \
    "s_branch .Ls02_%=\n\t" \
    ".Ls03_%=:\n\t" \
    LST2 \
    "ds_write_b128 v253, v[224:227] offset:40960\n\t" \
    "ds_write_b128 v253, v[228:231] offset:46080\n\t" \
    "ds_write_b128 v253, v[232:235] offset:41984\n\t" \
    "v_smfmac_f32_16x16x64_bf16 v[244:247], v[224:227], v[0:7], v212\n\t" \
    "v_smfmac_f32_16x16x64_bf16 v[248:251], v[224:227], v[8:15], v212\n\t" \
    "v_smfmac_f32_16x16x64_bf16 v[208:211], v[224:227], v[200:207], v212\n\t" \
    "s_waitcnt lgkmcnt(0)\n\t" \
    "s_barrier \n\t" \
    LST3 \
    "ds_read_b128 v[236:239], v253 offset:47104\n\t" \
    "ds_read_b128 v[240:243], v253 offset:43008\n\t" \
    "ds_read_b128 v[224:227], v253 offset:48128\n\t" \
    "ds_read_b128 v[200:203], v254 offset:34816\n\t" \
    "ds_read_b128 v[204:207], v254 offset:35840\n\t" \
    "v_smfmac_f32_16x16x64_bf16 v[208:211], v[228:231], v[16:23], v212\n\t" \
    "v_smfmac_f32_16x16x64_bf16 v[244:247], v[228:231], v[24:31], v212\n\t" \
    "v_smfmac_f32_16x16x64_bf16 v[248:251], v[228:231], v[32:39], v212\n\t" \
    "ds_read_b128 v[228:231], v253 offset:44032\n\t" \
    "v_smfmac_f32_16x16x64_bf16 v[208:211], v[232:235], v[216:223], v212\n\t" \
    "ds_read_b128 v[216:219], v254 offset:36864\n\t" \
    "ds_read_b128 v[220:223], v254 offset:37888\n\t" \
    "v_smfmac_f32_16x16x64_bf16 v[244:247], v[232:235], v[40:47], v212\n\t" \
    "v_smfmac_f32_16x16x64_bf16 v[248:251], v[232:235], v[48:55], v212\n\t" \
    "ds_read_b128 v[232:235], v253 offset:49152\n\t" \
    "s_waitcnt lgkmcnt(8)\n\t" \
    "v_smfmac_f32_16x16x64_bf16 v[208:211], v[236:239], v[56:63], v212\n\t" \
    "v_smfmac_f32_16x16x64_bf16 v[244:247], v[236:239], v[64:71], v212\n\t" \
    "v_smfmac_f32_16x16x64_bf16 v[248:251], v[236:239], v[72:79], v212\n\t" \
    "ds_read_b128 v[236:239], v253 offset:45056\n\t" \
    "s_waitcnt lgkmcnt(5)\n\t" \
    "v_smfmac_f32_16x16x64_bf16 v[208:211], v[240:243], v[200:207], v212\n\t" \
    "ds_read_b128 v[200:203], v254 offset:38912\n\t" \
    "ds_read_b128 v[204:207], v254 offset:39936\n\t" \
    "v_smfmac_f32_16x16x64_bf16 v[244:247], v[240:243], v[80:87], v212\n\t" \
    "v_smfmac_f32_16x16x64_bf16 v[248:251], v[240:243], v[88:95], v212\n\t" \
    "ds_read_b128 v[240:243], v253 offset:50176\n\t" \
    "v_smfmac_f32_16x16x64_bf16 v[208:211], v[224:227], v[96:103], v212\n\t" \
    "v_smfmac_f32_16x16x64_bf16 v[244:247], v[224:227], v[104:111], v212\n\t" \
    "v_smfmac_f32_16x16x64_bf16 v[248:251], v[224:227], v[112:119], v212\n\t" \
    "s_waitcnt lgkmcnt(5)\n\t" \
    "v_smfmac_f32_16x16x64_bf16 v[208:211], v[228:231], v[216:223], v212\n\t" \
    "s_waitcnt lgkmcnt(4)\n\t" \
    "v_smfmac_f32_16x16x64_bf16 v[208:211], v[232:235], v[136:143], v212\n\t" \
    "s_waitcnt lgkmcnt(1)\n\t" \
    "v_smfmac_f32_16x16x64_bf16 v[208:211], v[236:239], v[200:207], v212\n\t" \
    "s_waitcnt lgkmcnt(0)\n\t" \
    "v_smfmac_f32_16x16x64_bf16 v[208:211], v[240:243], v[176:183], v212\n\t" \
    "v_smfmac_f32_16x16x64_bf16 v[244:247], v[228:231], v[120:127], v212\n\t" \
    "v_smfmac_f32_16x16x64_bf16 v[244:247], v[232:235], v[144:151], v212\n\t" \
    "v_smfmac_f32_16x16x64_bf16 v[244:247], v[236:239], v[160:167], v212\n\t" \
    "v_add_f32 v208, v208, v210\n\t" \
    "v_add_f32 v209, v209, v211\n\t" \
    "v_cvt_pk_bf16_f32 v208, v208, v209\n\t" \
    "v_smfmac_f32_16x16x64_bf16 v[244:247], v[240:243], v[184:191], v212\n\t" \
    "ds_write_b16 v213, v208 offset:0\n\t" \
    "ds_write_b16_d16_hi v213, v208 offset:16\n\t" \
    "v_smfmac_f32_16x16x64_bf16 v[248:251], v[228:231], v[128:135], v212\n\t" \
    "v_smfmac_f32_16x16x64_bf16 v[248:251], v[232:235], v[152:159], v212\n\t" \
    "v_smfmac_f32_16x16x64_bf16 v[248:251], v[236:239], v[168:175], v212\n\t" \
    "v_add_f32 v244, v244, v246\n\t" \
    "v_add_f32 v245, v245, v247\n\t" \
    "v_smfmac_f32_16x16x64_bf16 v[248:251], v[240:243], v[192:199], v212\n\t" \
    "s_nop 7\n\t" \
    "v_add_f32 v248, v248, v250\n\t" \
    "v_add_f32 v249, v249, v251\n\t" \
    "v_cvt_pk_bf16_f32 v244, v244, v248\n\t" \
    "v_cvt_pk_bf16_f32 v245, v245, v249\n\t" \
    "ds_write2_b32 v214, v244, v245 offset1:4\n\t" \
    "s_sleep 3\n\t" \
    "ds_read_b128 v[200:203], v254 offset:18432\n\t" \
    "ds_read_b128 v[204:207], v254 offset:19456\n\t" \
    "ds_read_b128 v[214:217], v254 offset:24576\n\t" \
    "ds_read_b128 v[218:221], v254 offset:25600\n\t" \
    "ds_read_b128 v[222:225], v254 offset:20480\n\t" \
    "ds_read_b128 v[226:229], v254 offset:21504\n\t" \
    "ds_read_b128 v[230:233], v254 offset:26624\n\t" \
    "ds_read_b128 v[234:237], v254 offset:27648\n\t" \
    "v_and_b32 v213, 0xffffff7f, v254\n\t" \
    "v_mov_b64 v[242:243], 0\n\t" \
    "v_mov_b64 v[244:245], 0\n\t" \
    "v_mov_b64 v[246:247], 0\n\t" \
    "v_mov_b64 v[248:249], 0\n\t" \
    "v_mov_b64 v[250:251], 0\n\t" \
    "v_mov_b64 v[252:253], 0\n\t" \
    "s_and_b32 s44, s40, 3\n\t" \
    "s_mul_i32 s44, s44, 0x30000\n\t" \
    "s_add_u32 s50, s44, %[pb]\n\t" \
    "s_add_u32 s51, s50, 0xc000\n\t" \
    "s_add_u32 s52, s50, 0x18000\n\t" \
    "s_add_u32 s53, s50, 0x24000\n\t" \
    "s_setprio 3\n\t" \
    "s_waitcnt lgkmcnt(0)\n\t" \
    "s_barrier \n\t" \
    LST4 \
    "ds_read_b128 v[208:211], v254 offset:0\n\t" \
    "ds_read_b128 v[238:241], v254 offset:1024\n\t" \
    "s_waitcnt lgkmcnt(1)\n\t" \
    "v_smfmac_f32_16x16x64_bf16 v[242:245], v[208:211], v[200:207], v212\n\t" \
    "ds_read_b128 v[200:203], v213 offset:3072\n\t" \
    "ds_read_b128 v[204:207], v213 offset:3200\n\t" \
    "v_smfmac_f32_16x16x64_bf16 v[246:249], v[208:211], v[214:221], v212\n\t" \
    "ds_read_b128 v[214:217], v213 offset:4096\n\t" \
    "ds_read_b128 v[218:221], v213 offset:4224\n\t" \
    "s_waitcnt lgkmcnt(4)\n\t" \
    "v_smfmac_f32_16x16x64_bf16 v[242:245], v[238:241], v[222:229], v212\n\t" \
    "ds_read_b128 v[222:225], v254 offset:22528\n\t" \
    "ds_read_b128 v[226:229], v254 offset:23552\n\t" \
    "v_smfmac_f32_16x16x64_bf16 v[246:249], v[238:241], v[230:237], v212\n\t" \
    "ds_read_b128 v[230:233], v254 offset:28672\n\t" \
    "ds_read_b128 v[234:237], v254 offset:29696\n\t" \
    "s_waitcnt lgkmcnt(6)\n\t" \
    "v_smfmac_f32_16x16x64_bf16 v[250:253], v[208:211], v[200:207], v212\n\t" \
    "ds_read_b128 v[208:211], v254 offset:2048\n\t" \
    "ds_read_b128 v[200:203], v213 offset:5120\n\t" \
    "ds_read_b128 v[204:207], v213 offset:5248\n\t" \
    "s_waitcnt lgkmcnt(7)\n\t" \
    "v_smfmac_f32_16x16x64_bf16 v[250:253], v[238:241], v[214:221], v212\n\t" \
    "v_lshrrev_b32 v238, 3, v254\n\t" \
    "v_and_b32 v238, 0x6e, v238\n\t" \
    "v_lshrrev_b32 v239, 16, v255\n\t" \
    "s_waitcnt lgkmcnt(2)\n\t" \
    "v_smfmac_f32_16x16x64_bf16 v[242:245], v[208:211], v[222:229], v212\n\t" \
    "v_smfmac_f32_16x16x64_bf16 v[246:249], v[208:211], v[230:237], v212\n\t" \
    "s_waitcnt lgkmcnt(0)\n\t" \
    "v_smfmac_f32_16x16x64_bf16 v[250:253], v[208:211], v[200:207], v212\n\t" \
    "s_nop 1\n\t" \
    "v_add_f32 v242, v242, v244\n\t" \
    "v_add_f32 v243, v243, v245\n\t" \
    "s_nop 0\n\t" \
    "v_add_f32 v246, v246, v248\n\t" \
    "v_add_f32 v247, v247, v249\n\t" \
    "s_nop 0\n\t" \
    "v_add_f32 v250, v250, v252\n\t" \
    "v_add_f32 v251, v251, v253\n\t" \
    "v_max3_f32 v244, v242, v246, v250\n\t" \
    "v_max3_f32 v245, v243, v247, v251\n\t" \
    "s_nop 0\n\t" \
    "v_max_f32_dpp v244, v244, v244 quad_perm:[1,0,3,2] row_mask:0xf bank_mask:0xf\n\t" \
    "v_max_f32_dpp v245, v245, v245 quad_perm:[1,0,3,2] row_mask:0xf bank_mask:0xf\n\t" \
    "s_nop 0\n\t" \
    "v_max_f32_dpp v244, v244, v244 quad_perm:[2,3,0,1] row_mask:0xf bank_mask:0xf\n\t" \
    "v_max_f32_dpp v245, v245, v245 quad_perm:[2,3,0,1] row_mask:0xf bank_mask:0xf\n\t" \
    "s_nop 0\n\t" \
    "v_max_f32_dpp v244, v244, v244 row_half_mirror row_mask:0xf bank_mask:0xf\n\t" \
    "v_max_f32_dpp v245, v245, v245 row_half_mirror row_mask:0xf bank_mask:0xf\n\t" \
    "s_nop 0\n\t" \
    "v_max_f32_dpp v244, v244, v244 row_mirror row_mask:0xf bank_mask:0xf\n\t" \
    "v_max_f32_dpp v245, v245, v245 row_mirror row_mask:0xf bank_mask:0xf\n\t" \
    "v_mul_f32 v244, %[scl], v244\n\t" \
    "v_mul_f32 v245, %[scl], v245\n\t" \
    "v_fma_f32 v242, v242, %[scl], -v244\n\t" \
    "v_fma_f32 v243, v243, %[scl], -v245\n\t" \
    "v_fma_f32 v246, v246, %[scl], -v244\n\t" \
    "v_fma_f32 v247, v247, %[scl], -v245\n\t" \
    "v_exp_f32 v242, v242\n\t" \
    "v_exp_f32 v243, v243\n\t" \
    "v_fma_f32 v250, v250, %[scl], -v244\n\t" \
    "v_fma_f32 v251, v251, %[scl], -v245\n\t" \
    "v_exp_f32 v246, v246\n\t" \
    "v_exp_f32 v247, v247\n\t" \
    "v_exp_f32 v250, v250\n\t" \
    "v_exp_f32 v251, v251\n\t" \
    "v_add_f32 v244, v242, v246\n\t" \
    "v_add_f32 v245, v243, v247\n\t" \
    "v_fma_f32 v244, v250, 0.5, v244\n\t" \
    "v_fma_f32 v245, v251, 0.5, v245\n\t" \
    "s_nop 0\n\t" \
    "v_add_f32_dpp v244, v244, v244 quad_perm:[1,0,3,2] row_mask:0xf bank_mask:0xf\n\t" \
    "v_add_f32_dpp v245, v245, v245 quad_perm:[1,0,3,2] row_mask:0xf bank_mask:0xf\n\t" \
    "s_nop 0\n\t" \
    "v_add_f32_dpp v244, v244, v244 quad_perm:[2,3,0,1] row_mask:0xf bank_mask:0xf\n\t" \
    "v_add_f32_dpp v245, v245, v245 quad_perm:[2,3,0,1] row_mask:0xf bank_mask:0xf\n\t" \
    "s_nop 0\n\t" \
    "v_add_f32_dpp v244, v244, v244 row_half_mirror row_mask:0xf bank_mask:0xf\n\t" \
    "v_add_f32_dpp v245, v245, v245 row_half_mirror row_mask:0xf bank_mask:0xf\n\t" \
    "s_nop 0\n\t" \
    "v_add_f32_dpp v244, v244, v244 row_mirror row_mask:0xf bank_mask:0xf\n\t" \
    "v_add_f32_dpp v245, v245, v245 row_mirror row_mask:0xf bank_mask:0xf\n\t" \
    "v_rcp_f32 v244, v244\n\t" \
    "v_rcp_f32 v245, v245\n\t" \
    "v_mul_f32 v250, v250, v244\n\t" \
    "v_mul_f32 v251, v251, v245\n\t" \
    "v_mul_f32 v242, v242, v244\n\t" \
    "v_mul_f32 v243, v243, v245\n\t" \
    "v_mul_f32 v246, v246, v244\n\t" \
    "v_mul_f32 v247, v247, v245\n\t" \
    "v_cvt_pk_bf16_f32 v250, |v250|, |v251|\n\t" \
    "ds_write_b16 v238, v250 offset:52096\n\t" \
    "ds_write_b16_d16_hi v238, v250 offset:52112\n\t" \
    "v_cvt_pk_bf16_f32 v242, |v242|, |v246|\n\t" \
    "v_cvt_pk_bf16_f32 v243, |v243|, |v247|\n\t" \
    "ds_write2_b32 v239, v242, v243 offset1:4\n\t" \
    "ds_read_b128 v[200:203], v254 offset:896\n\t" \
    "s_wakeup\n\t" \
    "v_and_b32 v253, 0x3ff, v254\n\t" \
    "s_mov_b32 exec_hi, 0xff\n\t" \
    "s_waitcnt lgkmcnt(0)\n\t" \
    "buffer_store_dwordx4 v[200:203], v254, s[60:63], s50 offen sc1\n\t" \
    "buffer_store_dwordx4 v[200:203], v254, s[60:63], s51 offen sc1\n\t" \
    "buffer_store_dwordx4 v[200:203], v254, s[60:63], s52 offen sc1\n\t" \
    "buffer_store_dwordx4 v[200:203], v254, s[60:63], s53 offen sc1\n\t" \
    "s_mov_b32 exec_hi, -1\n\t" \
    "s_setprio 0\n\t" \
    LST5 \
    "ds_read_b128 v[224:227], v253 offset:0\n\t" \
    "ds_read_b128 v[228:231], v253 offset:1024\n\t" \
    "ds_read_b128 v[232:235], v253 offset:20480\n\t" \
    "ds_read_b128 v[236:239], v253 offset:21504\n\t" \
    "s_add_u32 s40, s40, 1\n\t" \
    "s_cmp_lg_u32 s40, 64\n\t" \
    "s_cbranch_scc1 .Ls01_%=\n\t" \
    "s_branch .Lend%=\n\t" \
    ".Lend%=:\n\t" \
    "s_waitcnt vmcnt(0) lgkmcnt(0)\n\t" \
    "s_mov_b32 %[bud], s41\n\t"
#define ASM_V_LOOP \
    "s_load_dwordx4 s[48:51], %[pp], 0\n\t" \
    "s_mov_b64 s[60:61], %[ws]\n\t" \
    "s_mov_b32 s62, %[sz]\n\t" \
    "s_mov_b32 s63, 0x20000\n\t" \
    "s_mov_b32 s40, 0\n\t" \
    "s_mov_b32 s41, 0x20000\n\t" \
    LSTINIT \
    LDELAY \
    "s_cmp_eq_u32 %[wave], 0\n\t" \
    "s_cbranch_scc1 .Lv0_%=\n\t" \
    "s_cmp_eq_u32 %[wave], 1\n\t" \
    "s_cbranch_scc1 .Lv1_%=\n\t" \
    "s_cmp_eq_u32 %[wave], 2\n\t" \
    "s_cbranch_scc1 .Lv2_%=\n\t" \
    "s_cmp_eq_u32 %[wave], 3\n\t" \
    "s_cbranch_scc1 .Lv3_%=\n\t" \
    ".Lv4_%=:\n\t" \
    "s_add_u32 m0, %[wm], 0x0\n\t" \
    "s_mov_b32 s44, 0x0\n\t" \
    "buffer_load_dwordx4 v240, %[wr], s44 offen offset:0 nt lds\n\t" \
    "buffer_load_dwordx4 v241, %[wr], s44 offen offset:1024 nt lds\n\t" \
    "buffer_load_dwordx4 v242, %[wr], s44 offen offset:2048 nt lds\n\t" \
    "buffer_load_dwordx4 v243, %[wr], s44 offen offset:3072 nt lds\n\t" \
    "s_add_u32 m0, %[wm], 0x1000\n\t" \
    "s_nop 0\n\t" \
    "buffer_load_dwordx4 v244, %[wr], s44 offen nt lds\n\t" \
    "s_add_u32 m0, %[wm], 0x1400\n\t" \
    "s_mov_b32 s44, 0x5000\n\t" \
    "buffer_load_dwordx4 v240, %[wr], s44 offen offset:0 nt lds\n\t" \
    "buffer_load_dwordx4 v241, %[wr], s44 offen offset:1024 nt lds\n\t" \
    "buffer_load_dwordx4 v242, %[wr], s44 offen offset:2048 nt lds\n\t" \
    "buffer_load_dwordx4 v243, %[wr], s44 offen offset:3072 nt lds\n\t" \
    "s_add_u32 m0, %[wm], 0x2400\n\t" \
    "s_nop 0\n\t" \
    "buffer_load_dwordx4 v244, %[wr], s44 offen nt lds\n\t" \
    "s_add_u32 m0, %[wm], 0x2800\n\t" \
    "s_mov_b32 s44, 0xa000\n\t" \
    "buffer_load_dwordx4 v240, %[wr], s44 offen offset:0 nt lds\n\t" \
    "buffer_load_dwordx4 v241, %[wr], s44 offen offset:1024 nt lds\n\t" \
    "buffer_load_dwordx4 v242, %[wr], s44 offen offset:2048 nt lds\n\t" \
    "buffer_load_dwordx4 v243, %[wr], s44 offen offset:3072 nt lds\n\t" \
    "s_add_u32 m0, %[wm], 0x3800\n\t" \
    "s_nop 0\n\t" \
    "buffer_load_dwordx4 v244, %[wr], s44 offen nt lds\n\t" \
    "s_add_u32 m0, %[wm], 0x3c00\n\t" \
    "s_mov_b32 s44, 0xf000\n\t" \
    "buffer_load_dwordx4 v240, %[wr], s44 offen offset:0 nt lds\n\t" \
    "buffer_load_dwordx4 v241, %[wr], s44 offen offset:1024 nt lds\n\t" \
    "buffer_load_dwordx4 v242, %[wr], s44 offen offset:2048 nt lds\n\t" \
    "buffer_load_dwordx4 v243, %[wr], s44 offen offset:3072 nt lds\n\t" \
    "s_add_u32 m0, %[wm], 0x4c00\n\t" \
    "s_nop 0\n\t" \
    "buffer_load_dwordx4 v244, %[wr], s44 offen nt lds\n\t" \
    "s_waitcnt lgkmcnt(0)\n\t" \
    "s_add_u32 s43, s48, s49\n\t" \
    "s_add_u32 s43, s43, s50\n\t" \
    "s_and_b32 s43, s43, 0x2000\n\t" \
    "s_xor_b32 s43, s43, %[lx]\n\t" \
    "s_add_u32 s60, s60, s43\n\t" \
    "s_addc_u32 s61, s61, 0\n\t" \
    "s_barrier \n\t" \
    "s_waitcnt vmcnt(15)\n\t" \
    "ds_read_b64_tr_b16 v[200:201], v245 offset:0\n\t" \
    "ds_read_b64_tr_b16 v[16:17], v245 offset:32\n\t" \
    "ds_read_b64_tr_b16 v[208:209], v245 offset:64\n\t" \
    "ds_read_b64_tr_b16 v[56:57], v245 offset:96\n\t" \
    "ds_read_b64_tr_b16 v[216:217], v245 offset:128\n\t" \
    "ds_read_b64_tr_b16 v[96:97], v245 offset:160\n\t" \
    "ds_read_b64_tr_b16 v[224:225], v245 offset:192\n\t" \
    "ds_read_b64_tr_b16 v[136:137], v245 offset:224\n\t" \
    "ds_read_b64_tr_b16 v[232:233], v245 offset:256\n\t" \
    "ds_read_b64_tr_b16 v[176:177], v245 offset:288\n\t" \
    "s_waitcnt lgkmcnt(0)\n\t" \
    "s_add_u32 m0, %[wm], 0x0\n\t" \
    "s_mov_b32 s44, 0x14000\n\t" \
    "buffer_load_dwordx4 v240, %[wr], s44 offen offset:0 nt lds\n\t" \
    "buffer_load_dwordx4 v241, %[wr], s44 offen offset:1024 nt lds\n\t" \
    "buffer_load_dwordx4 v242, %[wr], s44 offen offset:2048 nt lds\n\t" \
    "buffer_load_dwordx4 v243, %[wr], s44 offen offset:3072 nt lds\n\t" \
    "s_add_u32 m0, %[wm], 0x1000\n\t" \
    "s_nop 0\n\t" \
    "buffer_load_dwordx4 v244, %[wr], s44 offen nt lds\n\t" \
    "s_barrier \n\t" \
    "s_barrier \n\t" \
    "s_waitcnt vmcnt(15)\n\t" \
    "ds_read_b64_tr_b16 v[204:205], v245 offset:5120\n\t" \
    "ds_read_b64_tr_b16 v[20:21], v245 offset:5152\n\t" \
    "ds_read_b64_tr_b16 v[212:213], v245 offset:5184\n\t" \
    "ds_read_b64_tr_b16 v[60:61], v245 offset:5216\n\t" \
    "ds_read_b64_tr_b16 v[220:221], v245 offset:5248\n\t" \
    "ds_read_b64_tr_b16 v[100:101], v245 offset:5280\n\t" \
    "ds_read_b64_tr_b16 v[228:229], v245 offset:5312\n\t" \
    "ds_read_b64_tr_b16 v[140:141], v245 offset:5344\n\t" \
    "ds_read_b64_tr_b16 v[236:237], v245 offset:5376\n\t" \
    "ds_read_b64_tr_b16 v[180:181], v245 offset:5408\n\t" \
    "s_waitcnt lgkmcnt(0)\n\t" \
    "s_add_u32 m0, %[wm], 0x1400\n\t" \
    "s_mov_b32 s44, 0x19000\n\t" \
    "buffer_load_dwordx4 v240, %[wr], s44 offen offset:0 nt lds\n\t" \
    "buffer_load_dwordx4 v241, %[wr], s44 offen offset:1024 nt lds\n\t" \
    "buffer_load_dwordx4 v242, %[wr], s44 offen offset:2048 nt lds\n\t" \
    "buffer_load_dwordx4 v243, %[wr], s44 offen offset:3072 nt lds\n\t" \
    "s_add_u32 m0, %[wm], 0x2400\n\t" \
    "s_nop 0\n\t" \
    "buffer_load_dwordx4 v244, %[wr], s44 offen nt lds\n\t" \
    "s_barrier \n\t" \
    "s_barrier \n\t" \
    "s_waitcnt vmcnt(15)\n\t" \
    "ds_read_b64_tr_b16 v[0:1], v245 offset:10240\n\t" \
    "ds_read_b64_tr_b16 v[24:25], v245 offset:10272\n\t" \
    "ds_read_b64_tr_b16 v[40:41], v245 offset:10304\n\t" \
    "ds_read_b64_tr_b16 v[64:65], v245 offset:10336\n\t" \
    "ds_read_b64_tr_b16 v[80:81], v245 offset:10368\n\t" \
    "ds_read_b64_tr_b16 v[104:105], v245 offset:10400\n\t" \
    "ds_read_b64_tr_b16 v[120:121], v245 offset:10432\n\t" \
    "ds_read_b64_tr_b16 v[144:145], v245 offset:10464\n\t" \
    "ds_read_b64_tr_b16 v[160:161], v245 offset:10496\n\t" \
    "ds_read_b64_tr_b16 v[184:185], v245 offset:10528\n\t" \
    "s_waitcnt lgkmcnt(0)\n\t" \
    "s_add_u32 m0, %[wm], 0x2800\n\t" \
    "s_mov_b32 s44, 0x1e000\n\t" \
    "buffer_load_dwordx4 v240, %[wr], s44 offen offset:0 nt lds\n\t" \
    "buffer_load_dwordx4 v241, %[wr], s44 offen offset:1024 nt lds\n\t" \
    "buffer_load_dwordx4 v242, %[wr], s44 offen offset:2048 nt lds\n\t" \
    "buffer_load_dwordx4 v243, %[wr], s44 offen offset:3072 nt lds\n\t" \
    "s_add_u32 m0, %[wm], 0x3800\n\t" \
    "s_nop 0\n\t" \
    "buffer_load_dwordx4 v244, %[wr], s44 offen nt lds\n\t" \
    "s_barrier \n\t" \
    "s_waitcnt vmcnt(15)\n\t" \
    "ds_read_b64_tr_b16 v[4:5], v245 offset:15360\n\t" \
    "ds_read_b64_tr_b16 v[28:29], v245 offset:15392\n\t" \
    "ds_read_b64_tr_b16 v[44:45], v245 offset:15424\n\t" \
    "ds_read_b64_tr_b16 v[68:69], v245 offset:15456\n\t" \
    "ds_read_b64_tr_b16 v[84:85], v245 offset:15488\n\t" \
    "ds_read_b64_tr_b16 v[108:109], v245 offset:15520\n\t" \
    "ds_read_b64_tr_b16 v[124:125], v245 offset:15552\n\t" \
    "ds_read_b64_tr_b16 v[148:149], v245 offset:15584\n\t" \
    "ds_read_b64_tr_b16 v[164:165], v245 offset:15616\n\t" \
    "ds_read_b64_tr_b16 v[188:189], v245 offset:15648\n\t" \
    "s_waitcnt lgkmcnt(0)\n\t" \
    "s_add_u32 m0, %[wm], 0x3c00\n\t" \
    "s_mov_b32 s44, 0x23000\n\t" \
    "buffer_load_dwordx4 v240, %[wr], s44 offen offset:0 nt lds\n\t" \
    "buffer_load_dwordx4 v241, %[wr], s44 offen offset:1024 nt lds\n\t" \
    "buffer_load_dwordx4 v242, %[wr], s44 offen offset:2048 nt lds\n\t" \
    "buffer_load_dwordx4 v243, %[wr], s44 offen offset:3072 nt lds\n\t" \
    "s_add_u32 m0, %[wm], 0x4c00\n\t" \
    "s_nop 0\n\t" \
    "buffer_load_dwordx4 v244, %[wr], s44 offen nt lds\n\t" \
    "s_barrier \n\t" \
    "s_barrier \n\t" \
    "s_waitcnt vmcnt(15)\n\t" \
    "ds_read_b64_tr_b16 v[8:9], v245 offset:0\n\t" \
    "ds_read_b64_tr_b16 v[32:33], v245 offset:32\n\t" \
    "ds_read_b64_tr_b16 v[48:49], v245 offset:64\n\t" \
    "ds_read_b64_tr_b16 v[72:73], v245 offset:96\n\t" \
    "ds_read_b64_tr_b16 v[88:89], v245 offset:128\n\t" \
    "ds_read_b64_tr_b16 v[112:113], v245 offset:160\n\t" \
    "ds_read_b64_tr_b16 v[128:129], v245 offset:192\n\t" \
    "ds_read_b64_tr_b16 v[152:153], v245 offset:224\n\t" \
    "ds_read_b64_tr_b16 v[168:169], v245 offset:256\n\t" \
    "ds_read_b64_tr_b16 v[192:193], v245 offset:288\n\t" \
    "s_waitcnt lgkmcnt(0)\n\t" \
    "s_add_u32 m0, %[wm], 0x0\n\t" \
    "s_mov_b32 s44, 0x28000\n\t" \
    "buffer_load_dwordx4 v240, %[wr], s44 offen offset:0 nt lds\n\t" \
    "buffer_load_dwordx4 v241, %[wr], s44 offen offset:1024 nt lds\n\t" \
    "buffer_load_dwordx4 v242, %[wr], s44 offen offset:2048 nt lds\n\t" \
    "buffer_load_dwordx4 v243, %[wr], s44 offen offset:3072 nt lds\n\t" \
    "s_add_u32 m0, %[wm], 0x1000\n\t" \
    "s_nop 0\n\t" \
    "buffer_load_dwordx4 v244, %[wr], s44 offen nt lds\n\t" \
    "s_barrier \n\t" \
    "s_barrier \n\t" \
    "s_waitcnt vmcnt(15)\n\t" \
    "ds_read_b64_tr_b16 v[12:13], v245 offset:5120\n\t" \
    "ds_read_b64_tr_b16 v[36:37], v245 offset:5152\n\t" \
    "ds_read_b64_tr_b16 v[52:53], v245 offset:5184\n\t" \
    "ds_read_b64_tr_b16 v[76:77], v245 offset:5216\n\t" \
    "ds_read_b64_tr_b16 v[92:93], v245 offset:5248\n\t" \
    "ds_read_b64_tr_b16 v[116:117], v245 offset:5280\n\t" \
    "ds_read_b64_tr_b16 v[132:133], v245 offset:5312\n\t" \
    "ds_read_b64_tr_b16 v[156:157], v245 offset:5344\n\t" \
    "ds_read_b64_tr_b16 v[172:173], v245 offset:5376\n\t" \
    "ds_read_b64_tr_b16 v[196:197], v245 offset:5408\n\t" \
    "s_waitcnt lgkmcnt(0)\n\t" \
    "s_add_u32 m0, %[wm], 0x1400\n\t" \
    "s_mov_b32 s44, 0x2d000\n\t" \
    "buffer_load_dwordx4 v240, %[wr], s44 offen offset:0 nt lds\n\t" \
    "buffer_load_dwordx4 v241, %[wr], s44 offen offset:1024 nt lds\n\t" \
    "buffer_load_dwordx4 v242, %[wr], s44 offen offset:2048 nt lds\n\t" \
    "buffer_load_dwordx4 v243, %[wr], s44 offen offset:3072 nt lds\n\t" \
    "s_add_u32 m0, %[wm], 0x2400\n\t" \
    "s_nop 0\n\t" \
    "buffer_load_dwordx4 v244, %[wr], s44 offen nt lds\n\t" \
    "s_barrier \n\t" \
    "s_waitcnt vmcnt(15)\n\t" \
    "ds_read_b64_tr_b16 v[202:203], v245 offset:10240\n\t" \
    "ds_read_b64_tr_b16 v[18:19], v245 offset:10272\n\t" \
    "ds_read_b64_tr_b16 v[210:211], v245 offset:10304\n\t" \
    "ds_read_b64_tr_b16 v[58:59], v245 offset:10336\n\t" \
    "ds_read_b64_tr_b16 v[218:219], v245 offset:10368\n\t" \
    "ds_read_b64_tr_b16 v[98:99], v245 offset:10400\n\t" \
    "ds_read_b64_tr_b16 v[226:227], v245 offset:10432\n\t" \
    "ds_read_b64_tr_b16 v[138:139], v245 offset:10464\n\t" \
    "ds_read_b64_tr_b16 v[234:235], v245 offset:10496\n\t" \
    "ds_read_b64_tr_b16 v[178:179], v245 offset:10528\n\t" \
    "s_waitcnt lgkmcnt(0)\n\t" \
    "s_add_u32 m0, %[wm], 0x2800\n\t" \
    "s_mov_b32 s44, 0x32000\n\t" \
    "buffer_load_dwordx4 v240, %[wr], s44 offen offset:0 nt lds\n\t" \
    "buffer_load_dwordx4 v241, %[wr], s44 offen offset:1024 nt lds\n\t" \
    "buffer_load_dwordx4 v242, %[wr], s44 offen offset:2048 nt lds\n\t" \
    "buffer_load_dwordx4 v243, %[wr], s44 offen offset:3072 nt lds\n\t" \
    "s_add_u32 m0, %[wm], 0x3800\n\t" \
    "s_nop 0\n\t" \
    "buffer_load_dwordx4 v244, %[wr], s44 offen nt lds\n\t" \
    "s_barrier \n\t" \
    "s_barrier \n\t" \
    "s_waitcnt vmcnt(15)\n\t" \
    "ds_read_b64_tr_b16 v[206:207], v245 offset:15360\n\t" \
    "ds_read_b64_tr_b16 v[22:23], v245 offset:15392\n\t" \
    "ds_read_b64_tr_b16 v[214:215], v245 offset:15424\n\t" \
    "ds_read_b64_tr_b16 v[62:63], v245 offset:15456\n\t" \
    "ds_read_b64_tr_b16 v[222:223], v245 offset:15488\n\t" \
    "ds_read_b64_tr_b16 v[102:103], v245 offset:15520\n\t" \
    "ds_read_b64_tr_b16 v[230:231], v245 offset:15552\n\t" \
    "ds_read_b64_tr_b16 v[142:143], v245 offset:15584\n\t" \
    "ds_read_b64_tr_b16 v[238:239], v245 offset:15616\n\t" \
    "ds_read_b64_tr_b16 v[182:183], v245 offset:15648\n\t" \
    "s_waitcnt lgkmcnt(0)\n\t" \
    "s_add_u32 m0, %[wm], 0x3c00\n\t" \
    "s_mov_b32 s44, 0x37000\n\t" \
    "buffer_load_dwordx4 v240, %[wr], s44 offen offset:0 nt lds\n\t" \
    "buffer_load_dwordx4 v241, %[wr], s44 offen offset:1024 nt lds\n\t" \
    "buffer_load_dwordx4 v242, %[wr], s44 offen offset:2048 nt lds\n\t" \
    "buffer_load_dwordx4 v243, %[wr], s44 offen offset:3072 nt lds\n\t" \
    "s_add_u32 m0, %[wm], 0x4c00\n\t" \
    "s_nop 0\n\t" \
    "buffer_load_dwordx4 v244, %[wr], s44 offen nt lds\n\t" \
    "s_barrier \n\t" \
    "s_barrier \n\t" \
    "s_waitcnt vmcnt(15)\n\t" \
    "ds_read_b64_tr_b16 v[2:3], v245 offset:0\n\t" \
    "ds_read_b64_tr_b16 v[26:27], v245 offset:32\n\t" \
    "ds_read_b64_tr_b16 v[42:43], v245 offset:64\n\t" \
    "ds_read_b64_tr_b16 v[66:67], v245 offset:96\n\t" \
    "ds_read_b64_tr_b16 v[82:83], v245 offset:128\n\t" \
    "ds_read_b64_tr_b16 v[106:107], v245 offset:160\n\t" \
    "ds_read_b64_tr_b16 v[122:123], v245 offset:192\n\t" \
    "ds_read_b64_tr_b16 v[146:147], v245 offset:224\n\t" \
    "ds_read_b64_tr_b16 v[162:163], v245 offset:256\n\t" \
    "ds_read_b64_tr_b16 v[186:187], v245 offset:288\n\t" \
    "s_barrier \n\t" \
    "s_waitcnt vmcnt(10)\n\t" \
    "ds_read_b64_tr_b16 v[6:7], v245 offset:5120\n\t" \
    "ds_read_b64_tr_b16 v[30:31], v245 offset:5152\n\t" \
    "ds_read_b64_tr_b16 v[46:47], v245 offset:5184\n\t" \
    "ds_read_b64_tr_b16 v[70:71], v245 offset:5216\n\t" \
    "ds_read_b64_tr_b16 v[86:87], v245 offset:5248\n\t" \
    "ds_read_b64_tr_b16 v[110:111], v245 offset:5280\n\t" \
    "ds_read_b64_tr_b16 v[126:127], v245 offset:5312\n\t" \
    "ds_read_b64_tr_b16 v[150:151], v245 offset:5344\n\t" \
    "ds_read_b64_tr_b16 v[166:167], v245 offset:5376\n\t" \
    "ds_read_b64_tr_b16 v[190:191], v245 offset:5408\n\t" \
    "s_barrier \n\t" \
    "s_barrier \n\t" \
    "s_waitcnt vmcnt(5)\n\t" \
    "ds_read_b64_tr_b16 v[10:11], v245 offset:10240\n\t" \
    "ds_read_b64_tr_b16 v[34:35], v245 offset:10272\n\t" \
    "ds_read_b64_tr_b16 v[50:51], v245 offset:10304\n\t" \
    "ds_read_b64_tr_b16 v[74:75], v245 offset:10336\n\t" \
    "ds_read_b64_tr_b16 v[90:91], v245 offset:10368\n\t" \
    "ds_read_b64_tr_b16 v[114:115], v245 offset:10400\n\t" \
    "ds_read_b64_tr_b16 v[130:131], v245 offset:10432\n\t" \
    "ds_read_b64_tr_b16 v[154:155], v245 offset:10464\n\t" \
    "ds_read_b64_tr_b16 v[170:171], v245 offset:10496\n\t" \
    "ds_read_b64_tr_b16 v[194:195], v245 offset:10528\n\t" \
    "s_barrier \n\t" \
    "s_barrier \n\t" \
    "s_waitcnt vmcnt(0)\n\t" \
    "ds_read_b64_tr_b16 v[14:15], v245 offset:15360\n\t" \
    "ds_read_b64_tr_b16 v[38:39], v245 offset:15392\n\t" \
    "ds_read_b64_tr_b16 v[54:55], v245 offset:15424\n\t" \
    "ds_read_b64_tr_b16 v[78:79], v245 offset:15456\n\t" \
    "ds_read_b64_tr_b16 v[94:95], v245 offset:15488\n\t" \
    "ds_read_b64_tr_b16 v[118:119], v245 offset:15520\n\t" \
    "ds_read_b64_tr_b16 v[134:135], v245 offset:15552\n\t" \
    "ds_read_b64_tr_b16 v[158:159], v245 offset:15584\n\t" \
    "ds_read_b64_tr_b16 v[174:175], v245 offset:15616\n\t" \
    "ds_read_b64_tr_b16 v[198:199], v245 offset:15648\n\t" \
    "s_waitcnt lgkmcnt(0)\n\t" \
    "s_barrier \n\t" \
    "ds_write_b128 v254, v[200:203] offset:0\n\t" \
    "ds_write_b128 v254, v[204:207] offset:1024\n\t" \
    "ds_write_b128 v254, v[208:211] offset:2048\n\t" \
    "ds_write_b128 v254, v[212:215] offset:3072\n\t" \
    "ds_write_b128 v254, v[216:219] offset:4096\n\t" \
    "ds_write_b128 v254, v[220:223] offset:5120\n\t" \
    "ds_write_b128 v254, v[224:227] offset:6144\n\t" \
    "ds_write_b128 v254, v[228:231] offset:7168\n\t" \
    "ds_write_b128 v254, v[232:235] offset:8192\n\t" \
    "ds_write_b128 v254, v[236:239] offset:9216\n\t" \
    "s_waitcnt vmcnt(0) lgkmcnt(0)\n\t" \
    "s_barrier \n\t" \
    "v_mov_b64 v[200:201], 0\n\t" \
    "v_mov_b64 v[202:203], 0\n\t" \
    "ds_write_b128 v253, v[200:203] offset:3072\n\t" \
    "ds_write_b128 v253, v[200:203] offset:4096\n\t" \
    "v_bfe_i32 v252, v253, 5, 1\n\t" \
    "v_and_b32 v252, 0xaaaa, v252\n\t" \
    "v_or_b32 v252, 0x4444, v252\n\t" \
    ".Lv41_%=:\n\t" \
    "ds_read_b128 v[248:251], v254 offset:0\n\t" \
    "s_and_b32 s44, s40, %[vm]\n\t" \
    "s_lshl_b32 s44, s44, 20\n\t" \
    "s_and_b32 s45, s40, 2\n\t" \
    "s_lshl_b32 s45, s45, 12\n\t" \
    "s_add_u32 s44, s44, s45\n\t" \
    "s_add_u32 s42, s44, %[vx]\n\t" \
    "s_waitcnt lgkmcnt(0)\n\t" \
    "s_barrier \n\t" \
    LST7 \
    "ds_read_b128 v[224:227], v253 offset:6144\n\t" \
    "ds_read_b128 v[228:231], v253 offset:12288\n\t" \
    "ds_read_b128 v[232:235], v253 offset:8192\n\t" \
    "ds_read_b128 v[236:239], v253 offset:14336\n\t" \
    "ds_read_b128 v[240:243], v253 offset:9216\n\t" \
    "s_waitcnt lgkmcnt(4)\n\t" \
    "v_mfma_f32_16x16x32_bf16 v[200:203], v[224:227], v[248:251], 0\n\t" \
    "ds_read_b128 v[244:247], v253 offset:15360\n\t" \
    "s_waitcnt lgkmcnt(4)\n\t" \
    "v_mfma_f32_16x16x32_bf16 v[204:207], v[228:231], v[248:251], 0\n\t" \
    "ds_read_b128 v[224:227], v253 offset:7168\n\t" \
    "s_waitcnt lgkmcnt(4)\n\t" \
    "v_mfma_f32_16x16x32_bf16 v[200:203], v[232:235], v[0:3], v[200:203]\n\t" \
    "ds_read_b128 v[228:231], v253 offset:13312\n\t" \
    "ds_read_b128 v[248:251], v254 offset:1024\n\t" \
    "s_waitcnt lgkmcnt(5)\n\t" \
    "v_mfma_f32_16x16x32_bf16 v[204:207], v[236:239], v[0:3], v[204:207]\n\t" \
    "ds_read_b128 v[232:235], v253 offset:10240\n\t" \
    "s_waitcnt lgkmcnt(5)\n\t" \
    "v_mfma_f32_16x16x32_bf16 v[200:203], v[240:243], v[4:7], v[200:203]\n\t" \
    "ds_read_b128 v[236:239], v253 offset:16384\n\t" \
    "s_waitcnt lgkmcnt(5)\n\t" \
    "v_mfma_f32_16x16x32_bf16 v[204:207], v[244:247], v[4:7], v[204:207]\n\t" \
    "ds_read_b128 v[240:243], v253 offset:11264\n\t" \
    "s_waitcnt lgkmcnt(3)\n\t" \
    "v_mfma_f32_16x16x32_bf16 v[200:203], v[224:227], v[248:251], v[200:203]\n\t" \
    "ds_read_b128 v[244:247], v253 offset:17408\n\t" \
    "v_mfma_f32_16x16x32_bf16 v[208:211], v[224:227], v[20:23], 0\n\t" \
    "v_mfma_f32_16x16x32_bf16 v[204:207], v[228:231], v[248:251], v[204:207]\n\t" \
    "ds_read_b128 v[224:227], v253 offset:6144\n\t" \
    "v_mfma_f32_16x16x32_bf16 v[212:215], v[228:231], v[20:23], 0\n\t" \
    "s_waitcnt lgkmcnt(4)\n\t" \
    "v_mfma_f32_16x16x32_bf16 v[200:203], v[232:235], v[8:11], v[200:203]\n\t" \
    "ds_read_b128 v[228:231], v253 offset:12288\n\t" \
    "ds_read_b128 v[248:251], v254 offset:2048\n\t" \
    "v_mfma_f32_16x16x32_bf16 v[208:211], v[232:235], v[32:35], v[208:211]\n\t" \
    "s_waitcnt lgkmcnt(5)\n\t" \
    "v_mfma_f32_16x16x32_bf16 v[204:207], v[236:239], v[8:11], v[204:207]\n\t" \
    "ds_read_b128 v[232:235], v253 offset:8192\n\t" \
    "v_mfma_f32_16x16x32_bf16 v[212:215], v[236:239], v[32:35], v[212:215]\n\t" \
    "s_waitcnt lgkmcnt(5)\n\t" \
    "v_mfma_f32_16x16x32_bf16 v[200:203], v[240:243], v[12:15], v[200:203]\n\t" \
    "ds_read_b128 v[236:239], v253 offset:14336\n\t" \
    "v_mfma_f32_16x16x32_bf16 v[208:211], v[240:243], v[36:39], v[208:211]\n\t" \
    "s_waitcnt lgkmcnt(5)\n\t" \
    "v_mfma_f32_16x16x32_bf16 v[204:207], v[244:247], v[12:15], v[204:207]\n\t" \
    "ds_read_b128 v[240:243], v253 offset:9216\n\t" \
    "v_mfma_f32_16x16x32_bf16 v[212:215], v[244:247], v[36:39], v[212:215]\n\t" \
    "s_waitcnt lgkmcnt(3)\n\t" \
    "v_mfma_f32_16x16x32_bf16 v[216:219], v[224:227], v[248:251], 0\n\t" \
    "ds_read_b128 v[244:247], v253 offset:15360\n\t" \
    "v_mfma_f32_16x16x32_bf16 v[208:211], v[224:227], v[16:19], v[208:211]\n\t" \
    "v_mfma_f32_16x16x32_bf16 v[220:223], v[228:231], v[248:251], 0\n\t" \
    "ds_read_b128 v[224:227], v253 offset:7168\n\t" \
    "v_mfma_f32_16x16x32_bf16 v[212:215], v[228:231], v[16:19], v[212:215]\n\t" \
    "v_cvt_pk_bf16_f32 v200, v200, v204\n\t" \
    "s_waitcnt lgkmcnt(4)\n\t" \
    "v_mfma_f32_16x16x32_bf16 v[216:219], v[232:235], v[40:43], v[216:219]\n\t" \
    "ds_read_b128 v[228:231], v253 offset:13312\n\t" \
    "ds_read_b128 v[248:251], v254 offset:3072\n\t" \
    "v_cvt_pk_bf16_f32 v201, v201, v205\n\t" \
    "v_mfma_f32_16x16x32_bf16 v[208:211], v[232:235], v[24:27], v[208:211]\n\t" \
    "v_cvt_pk_bf16_f32 v202, v202, v206\n\t" \
    "s_waitcnt lgkmcnt(5)\n\t" \
    "v_mfma_f32_16x16x32_bf16 v[220:223], v[236:239], v[40:43], v[220:223]\n\t" \
    "ds_read_b128 v[232:235], v253 offset:10240\n\t" \
    "v_cvt_pk_bf16_f32 v203, v203, v207\n\t" \
    "v_mfma_f32_16x16x32_bf16 v[212:215], v[236:239], v[24:27], v[212:215]\n\t" \
    "buffer_store_dwordx4 v[200:203], v253, s[60:63], s42 offen sc1\n\t" \
    "s_waitcnt lgkmcnt(5)\n\t" \
    "v_mfma_f32_16x16x32_bf16 v[216:219], v[240:243], v[44:47], v[216:219]\n\t" \
    "ds_read_b128 v[236:239], v253 offset:16384\n\t" \
    "s_add_u32 s42, s42, 0x10000\n\t" \
    "v_mfma_f32_16x16x32_bf16 v[208:211], v[240:243], v[28:31], v[208:211]\n\t" \
    "s_waitcnt lgkmcnt(5)\n\t" \
    "v_mfma_f32_16x16x32_bf16 v[220:223], v[244:247], v[44:47], v[220:223]\n\t" \
    "ds_read_b128 v[240:243], v253 offset:11264\n\t" \
    "v_mfma_f32_16x16x32_bf16 v[212:215], v[244:247], v[28:31], v[212:215]\n\t" \
    "s_waitcnt lgkmcnt(3)\n\t" \
    "v_mfma_f32_16x16x32_bf16 v[216:219], v[224:227], v[248:251], v[216:219]\n\t" \
    "ds_read_b128 v[244:247], v253 offset:17408\n\t" \
    "v_mfma_f32_16x16x32_bf16 v[200:203], v[224:227], v[60:63], 0\n\t" \
    "v_mfma_f32_16x16x32_bf16 v[220:223], v[228:231], v[248:251], v[220:223]\n\t" \
    "ds_read_b128 v[224:227], v253 offset:6144\n\t" \
    "v_mfma_f32_16x16x32_bf16 v[204:207], v[228:231], v[60:63], 0\n\t" \
    "v_cvt_pk_bf16_f32 v208, v208, v212\n\t" \
    "s_waitcnt lgkmcnt(4)\n\t" \
    "v_mfma_f32_16x16x32_bf16 v[216:219], v[232:235], v[48:51], v[216:219]\n\t" \
    "ds_read_b128 v[228:231], v253 offset:12288\n\t" \
    "ds_read_b128 v[248:251], v254 offset:4096\n\t" \
    "v_cvt_pk_bf16_f32 v209, v209, v213\n\t" \
    "v_mfma_f32_16x16x32_bf16 v[200:203], v[232:235], v[72:75], v[200:203]\n\t" \
    "v_cvt_pk_bf16_f32 v210, v210, v214\n\t" \
    "s_waitcnt lgkmcnt(5)\n\t" \
    "v_mfma_f32_16x16x32_bf16 v[220:223], v[236:239], v[48:51], v[220:223]\n\t" \
    "ds_read_b128 v[232:235], v253 offset:8192\n\t" \
    "v_cvt_pk_bf16_f32 v211, v211, v215\n\t" \
    "v_mfma_f32_16x16x32_bf16 v[204:207], v[236:239], v[72:75], v[204:207]\n\t" \
    "buffer_store_dwordx4 v[208:211], v253, s[60:63], s42 offen sc1\n\t" \
    "s_waitcnt lgkmcnt(5)\n\t" \
    "v_mfma_f32_16x16x32_bf16 v[216:219], v[240:243], v[52:55], v[216:219]\n\t" \
    "ds_read_b128 v[236:239], v253 offset:14336\n\t" \
    "s_add_u32 s42, s42, 0x10000\n\t" \
    "v_mfma_f32_16x16x32_bf16 v[200:203], v[240:243], v[76:79], v[200:203]\n\t" \
    "s_waitcnt lgkmcnt(5)\n\t" \
    "v_mfma_f32_16x16x32_bf16 v[220:223], v[244:247], v[52:55], v[220:223]\n\t" \
    "ds_read_b128 v[240:243], v253 offset:9216\n\t" \
    "v_mfma_f32_16x16x32_bf16 v[204:207], v[244:247], v[76:79], v[204:207]\n\t" \
    "s_waitcnt lgkmcnt(3)\n\t" \
    "v_mfma_f32_16x16x32_bf16 v[208:211], v[224:227], v[248:251], 0\n\t" \
    "ds_read_b128 v[244:247], v253 offset:15360\n\t" \
    "v_mfma_f32_16x16x32_bf16 v[200:203], v[224:227], v[56:59], v[200:203]\n\t" \
    "v_mfma_f32_16x16x32_bf16 v[212:215], v[228:231], v[248:251], 0\n\t" \
    "ds_read_b128 v[224:227], v253 offset:7168\n\t" \
    "v_mfma_f32_16x16x32_bf16 v[204:207], v[228:231], v[56:59], v[204:207]\n\t" \
    "v_cvt_pk_bf16_f32 v216, v216, v220\n\t" \
    "s_waitcnt lgkmcnt(4)\n\t" \
    "v_mfma_f32_16x16x32_bf16 v[208:211], v[232:235], v[80:83], v[208:211]\n\t" \
    "ds_read_b128 v[228:231], v253 offset:13312\n\t" \
    "ds_read_b128 v[248:251], v254 offset:5120\n\t" \
    "v_cvt_pk_bf16_f32 v217, v217, v221\n\t" \
    "v_mfma_f32_16x16x32_bf16 v[200:203], v[232:235], v[64:67], v[200:203]\n\t" \
    "v_cvt_pk_bf16_f32 v218, v218, v222\n\t" \
    "s_waitcnt lgkmcnt(5)\n\t" \
    "v_mfma_f32_16x16x32_bf16 v[212:215], v[236:239], v[80:83], v[212:215]\n\t" \
    "ds_read_b128 v[232:235], v253 offset:10240\n\t" \
    "v_cvt_pk_bf16_f32 v219, v219, v223\n\t" \
    "v_mfma_f32_16x16x32_bf16 v[204:207], v[236:239], v[64:67], v[204:207]\n\t" \
    "buffer_store_dwordx4 v[216:219], v253, s[60:63], s42 offen sc1\n\t" \
    "s_waitcnt lgkmcnt(5)\n\t" \
    "v_mfma_f32_16x16x32_bf16 v[208:211], v[240:243], v[84:87], v[208:211]\n\t" \
    "ds_read_b128 v[236:239], v253 offset:16384\n\t" \
    "s_add_u32 s42, s42, 0x10000\n\t" \
    "v_mfma_f32_16x16x32_bf16 v[200:203], v[240:243], v[68:71], v[200:203]\n\t" \
    "s_waitcnt lgkmcnt(5)\n\t" \
    "v_mfma_f32_16x16x32_bf16 v[212:215], v[244:247], v[84:87], v[212:215]\n\t" \
    "ds_read_b128 v[240:243], v253 offset:11264\n\t" \
    "v_mfma_f32_16x16x32_bf16 v[204:207], v[244:247], v[68:71], v[204:207]\n\t" \
    "s_waitcnt lgkmcnt(3)\n\t" \
    "v_mfma_f32_16x16x32_bf16 v[208:211], v[224:227], v[248:251], v[208:211]\n\t" \
    "ds_read_b128 v[244:247], v253 offset:17408\n\t" \
    "v_mfma_f32_16x16x32_bf16 v[216:219], v[224:227], v[100:103], 0\n\t" \
    "v_mfma_f32_16x16x32_bf16 v[212:215], v[228:231], v[248:251], v[212:215]\n\t" \
    "ds_read_b128 v[224:227], v253 offset:6144\n\t" \
    "v_mfma_f32_16x16x32_bf16 v[220:223], v[228:231], v[100:103], 0\n\t" \
    "v_cvt_pk_bf16_f32 v200, v200, v204\n\t" \
    "s_waitcnt lgkmcnt(4)\n\t" \
    "v_mfma_f32_16x16x32_bf16 v[208:211], v[232:235], v[88:91], v[208:211]\n\t" \
    "ds_read_b128 v[228:231], v253 offset:12288\n\t" \
    "ds_read_b128 v[248:251], v254 offset:6144\n\t" \
    "v_cvt_pk_bf16_f32 v201, v201, v205\n\t" \
    "v_mfma_f32_16x16x32_bf16 v[216:219], v[232:235], v[112:115], v[216:219]\n\t" \
    "v_cvt_pk_bf16_f32 v202, v202, v206\n\t" \
    "s_waitcnt lgkmcnt(5)\n\t" \
    "v_mfma_f32_16x16x32_bf16 v[212:215], v[236:239], v[88:91], v[212:215]\n\t" \
    "ds_read_b128 v[232:235], v253 offset:8192\n\t" \
    "v_cvt_pk_bf16_f32 v203, v203, v207\n\t" \
    "v_mfma_f32_16x16x32_bf16 v[220:223], v[236:239], v[112:115], v[220:223]\n\t" \
    "buffer_store_dwordx4 v[200:203], v253, s[60:63], s42 offen sc1\n\t" \
    "s_waitcnt lgkmcnt(5)\n\t" \
    "v_mfma_f32_16x16x32_bf16 v[208:211], v[240:243], v[92:95], v[208:211]\n\t" \
    "ds_read_b128 v[236:239], v253 offset:14336\n\t" \
    "s_add_u32 s42, s42, 0x10000\n\t" \
    "v_mfma_f32_16x16x32_bf16 v[216:219], v[240:243], v[116:119], v[216:219]\n\t" \
    "s_waitcnt lgkmcnt(5)\n\t" \
    "v_mfma_f32_16x16x32_bf16 v[212:215], v[244:247], v[92:95], v[212:215]\n\t" \
    "ds_read_b128 v[240:243], v253 offset:9216\n\t" \
    "v_mfma_f32_16x16x32_bf16 v[220:223], v[244:247], v[116:119], v[220:223]\n\t" \
    "s_waitcnt lgkmcnt(3)\n\t" \
    "v_mfma_f32_16x16x32_bf16 v[200:203], v[224:227], v[248:251], 0\n\t" \
    "ds_read_b128 v[244:247], v253 offset:15360\n\t" \
    "v_mfma_f32_16x16x32_bf16 v[216:219], v[224:227], v[96:99], v[216:219]\n\t" \
    "v_mfma_f32_16x16x32_bf16 v[204:207], v[228:231], v[248:251], 0\n\t" \
    "ds_read_b128 v[224:227], v253 offset:7168\n\t" \
    "v_mfma_f32_16x16x32_bf16 v[220:223], v[228:231], v[96:99], v[220:223]\n\t" \
    "v_cvt_pk_bf16_f32 v208, v208, v212\n\t" \
    "s_waitcnt lgkmcnt(4)\n\t" \
    "v_mfma_f32_16x16x32_bf16 v[200:203], v[232:235], v[120:123], v[200:203]\n\t" \
    "ds_read_b128 v[228:231], v253 offset:13312\n\t" \
    "ds_read_b128 v[248:251], v254 offset:7168\n\t" \
    "v_cvt_pk_bf16_f32 v209, v209, v213\n\t" \
    "v_mfma_f32_16x16x32_bf16 v[216:219], v[232:235], v[104:107], v[216:219]\n\t" \
    "v_cvt_pk_bf16_f32 v210, v210, v214\n\t" \
    "s_waitcnt lgkmcnt(5)\n\t" \
    "v_mfma_f32_16x16x32_bf16 v[204:207], v[236:239], v[120:123], v[204:207]\n\t" \
    "ds_read_b128 v[232:235], v253 offset:10240\n\t" \
    "v_cvt_pk_bf16_f32 v211, v211, v215\n\t" \
    "v_mfma_f32_16x16x32_bf16 v[220:223], v[236:239], v[104:107], v[220:223]\n\t" \
    "buffer_store_dwordx4 v[208:211], v253, s[60:63], s42 offen sc1\n\t" \
    "s_waitcnt lgkmcnt(5)\n\t" \
    "v_mfma_f32_16x16x32_bf16 v[200:203], v[240:243], v[124:127], v[200:203]\n\t" \
    "ds_read_b128 v[236:239], v253 offset:16384\n\t" \
    "s_add_u32 s42, s42, 0x10000\n\t" \
    "v_mfma_f32_16x16x32_bf16 v[216:219], v[240:243], v[108:111], v[216:219]\n\t" \
    "s_waitcnt lgkmcnt(5)\n\t" \
    "v_mfma_f32_16x16x32_bf16 v[204:207], v[244:247], v[124:127], v[204:207]\n\t" \
    "ds_read_b128 v[240:243], v253 offset:11264\n\t" \
    "v_mfma_f32_16x16x32_bf16 v[220:223], v[244:247], v[108:111], v[220:223]\n\t" \
    "s_waitcnt lgkmcnt(3)\n\t" \
    "v_mfma_f32_16x16x32_bf16 v[200:203], v[224:227], v[248:251], v[200:203]\n\t" \
    "ds_read_b128 v[244:247], v253 offset:17408\n\t" \
    "v_mfma_f32_16x16x32_bf16 v[208:211], v[224:227], v[140:143], 0\n\t" \
    "v_mfma_f32_16x16x32_bf16 v[204:207], v[228:231], v[248:251], v[204:207]\n\t" \
    "ds_read_b128 v[224:227], v253 offset:6144\n\t" \
    "v_mfma_f32_16x16x32_bf16 v[212:215], v[228:231], v[140:143], 0\n\t" \
    "v_cvt_pk_bf16_f32 v216, v216, v220\n\t" \
    "s_waitcnt lgkmcnt(4)\n\t" \
    "v_mfma_f32_16x16x32_bf16 v[200:203], v[232:235], v[128:131], v[200:203]\n\t" \
    "ds_read_b128 v[228:231], v253 offset:12288\n\t" \
    "ds_read_b128 v[248:251], v254 offset:8192\n\t" \
    "v_cvt_pk_bf16_f32 v217, v217, v221\n\t" \
    "v_mfma_f32_16x16x32_bf16 v[208:211], v[232:235], v[152:155], v[208:211]\n\t" \
    "v_cvt_pk_bf16_f32 v218, v218, v222\n\t" \
    "s_waitcnt lgkmcnt(5)\n\t" \
    "v_mfma_f32_16x16x32_bf16 v[204:207], v[236:239], v[128:131], v[204:207]\n\t" \
    "ds_read_b128 v[232:235], v253 offset:8192\n\t" \
    "v_cvt_pk_bf16_f32 v219, v219, v223\n\t" \
    "v_mfma_f32_16x16x32_bf16 v[212:215], v[236:239], v[152:155], v[212:215]\n\t" \
    "buffer_store_dwordx4 v[216:219], v253, s[60:63], s42 offen sc1\n\t" \
    "s_waitcnt lgkmcnt(5)\n\t" \
    "v_mfma_f32_16x16x32_bf16 v[200:203], v[240:243], v[132:135], v[200:203]\n\t" \
    "ds_read_b128 v[236:239], v253 offset:14336\n\t" \
    "s_add_u32 s42, s42, 0x10000\n\t" \
    "v_mfma_f32_16x16x32_bf16 v[208:211], v[240:243], v[156:159], v[208:211]\n\t" \
    "s_waitcnt lgkmcnt(5)\n\t" \
    "v_mfma_f32_16x16x32_bf16 v[204:207], v[244:247], v[132:135], v[204:207]\n\t" \
    "ds_read_b128 v[240:243], v253 offset:9216\n\t" \
    "v_mfma_f32_16x16x32_bf16 v[212:215], v[244:247], v[156:159], v[212:215]\n\t" \
    "s_waitcnt lgkmcnt(3)\n\t" \
    "v_mfma_f32_16x16x32_bf16 v[216:219], v[224:227], v[248:251], 0\n\t" \
    "ds_read_b128 v[244:247], v253 offset:15360\n\t" \
    "v_mfma_f32_16x16x32_bf16 v[208:211], v[224:227], v[136:139], v[208:211]\n\t" \
    "v_mfma_f32_16x16x32_bf16 v[220:223], v[228:231], v[248:251], 0\n\t" \
    "ds_read_b128 v[224:227], v253 offset:7168\n\t" \
    "v_mfma_f32_16x16x32_bf16 v[212:215], v[228:231], v[136:139], v[212:215]\n\t" \
    "v_cvt_pk_bf16_f32 v200, v200, v204\n\t" \
    "s_waitcnt lgkmcnt(4)\n\t" \
    "v_mfma_f32_16x16x32_bf16 v[216:219], v[232:235], v[160:163], v[216:219]\n\t" \
    "ds_read_b128 v[228:231], v253 offset:13312\n\t" \
    "ds_read_b128 v[248:251], v254 offset:9216\n\t" \
    "v_cvt_pk_bf16_f32 v201, v201, v205\n\t" \
    "v_mfma_f32_16x16x32_bf16 v[208:211], v[232:235], v[144:147], v[208:211]\n\t" \
    "v_cvt_pk_bf16_f32 v202, v202, v206\n\t" \
    "s_waitcnt lgkmcnt(5)\n\t" \
    "v_mfma_f32_16x16x32_bf16 v[220:223], v[236:239], v[160:163], v[220:223]\n\t" \
    "ds_read_b128 v[232:235], v253 offset:10240\n\t" \
    "v_cvt_pk_bf16_f32 v203, v203, v207\n\t" \
    "v_mfma_f32_16x16x32_bf16 v[212:215], v[236:239], v[144:147], v[212:215]\n\t" \
    "buffer_store_dwordx4 v[200:203], v253, s[60:63], s42 offen sc1\n\t" \
    "s_waitcnt lgkmcnt(5)\n\t" \
    "v_mfma_f32_16x16x32_bf16 v[216:219], v[240:243], v[164:167], v[216:219]\n\t" \
    "ds_read_b128 v[236:239], v253 offset:16384\n\t" \
    "s_add_u32 s42, s42, 0x10000\n\t" \
    "v_mfma_f32_16x16x32_bf16 v[208:211], v[240:243], v[148:151], v[208:211]\n\t" \
    "s_waitcnt lgkmcnt(5)\n\t" \
    "v_mfma_f32_16x16x32_bf16 v[220:223], v[244:247], v[164:167], v[220:223]\n\t" \
    "ds_read_b128 v[240:243], v253 offset:11264\n\t" \
    "v_mfma_f32_16x16x32_bf16 v[212:215], v[244:247], v[148:151], v[212:215]\n\t" \
    "s_waitcnt lgkmcnt(3)\n\t" \
    "v_mfma_f32_16x16x32_bf16 v[216:219], v[224:227], v[248:251], v[216:219]\n\t" \
    "ds_read_b128 v[244:247], v253 offset:17408\n\t" \
    "v_mfma_f32_16x16x32_bf16 v[200:203], v[224:227], v[180:183], 0\n\t" \
    "v_mfma_f32_16x16x32_bf16 v[220:223], v[228:231], v[248:251], v[220:223]\n\t" \
    "ds_read_b128 v[224:227], v253 offset:6144\n\t" \
    "v_mfma_f32_16x16x32_bf16 v[204:207], v[228:231], v[180:183], 0\n\t" \
    "v_cvt_pk_bf16_f32 v208, v208, v212\n\t" \
    "s_waitcnt lgkmcnt(4)\n\t" \
    "v_mfma_f32_16x16x32_bf16 v[216:219], v[232:235], v[168:171], v[216:219]\n\t" \
    "ds_read_b128 v[228:231], v253 offset:12288\n\t" \
    "v_cvt_pk_bf16_f32 v209, v209, v213\n\t" \
    "v_mfma_f32_16x16x32_bf16 v[200:203], v[232:235], v[192:195], v[200:203]\n\t" \
    "v_cvt_pk_bf16_f32 v210, v210, v214\n\t" \
    "s_waitcnt lgkmcnt(4)\n\t" \
    "v_mfma_f32_16x16x32_bf16 v[220:223], v[236:239], v[168:171], v[220:223]\n\t" \
    "ds_read_b128 v[232:235], v253 offset:8192\n\t" \
    "v_cvt_pk_bf16_f32 v211, v211, v215\n\t" \
    "v_mfma_f32_16x16x32_bf16 v[204:207], v[236:239], v[192:195], v[204:207]\n\t" \
    "buffer_store_dwordx4 v[208:211], v253, s[60:63], s42 offen sc1\n\t" \
    "s_waitcnt lgkmcnt(4)\n\t" \
    "v_mfma_f32_16x16x32_bf16 v[216:219], v[240:243], v[172:175], v[216:219]\n\t" \
    "ds_read_b128 v[236:239], v253 offset:14336\n\t" \
    "s_add_u32 s42, s42, 0x10000\n\t" \
    "v_mfma_f32_16x16x32_bf16 v[200:203], v[240:243], v[196:199], v[200:203]\n\t" \
    "s_waitcnt lgkmcnt(4)\n\t" \
    "v_mfma_f32_16x16x32_bf16 v[220:223], v[244:247], v[172:175], v[220:223]\n\t" \
    "ds_read_b128 v[240:243], v253 offset:9216\n\t" \
    "v_mfma_f32_16x16x32_bf16 v[204:207], v[244:247], v[196:199], v[204:207]\n\t" \
    "s_waitcnt lgkmcnt(4)\n\t" \
    "v_mfma_f32_16x16x32_bf16 v[200:203], v[224:227], v[176:179], v[200:203]\n\t" \
    "ds_read_b128 v[244:247], v253 offset:15360\n\t" \
    "s_waitcnt lgkmcnt(4)\n\t" \
    "v_mfma_f32_16x16x32_bf16 v[204:207], v[228:231], v[176:179], v[204:207]\n\t" \
    "s_waitcnt lgkmcnt(3)\n\t" \
    "v_mfma_f32_16x16x32_bf16 v[200:203], v[232:235], v[184:187], v[200:203]\n\t" \
    "s_waitcnt lgkmcnt(2)\n\t" \
    "v_mfma_f32_16x16x32_bf16 v[204:207], v[236:239], v[184:187], v[204:207]\n\t" \
    "v_cvt_pk_bf16_f32 v216, v216, v220\n\t" \
    "s_waitcnt lgkmcnt(1)\n\t" \
    "v_mfma_f32_16x16x32_bf16 v[200:203], v[240:243], v[188:191], v[200:203]\n\t" \
    "v_cvt_pk_bf16_f32 v217, v217, v221\n\t" \
    "s_waitcnt lgkmcnt(0)\n\t" \
    "v_mfma_f32_16x16x32_bf16 v[204:207], v[244:247], v[188:191], v[204:207]\n\t" \
    "v_cvt_pk_bf16_f32 v218, v218, v222\n\t" \
    "v_cvt_pk_bf16_f32 v219, v219, v223\n\t" \
    "buffer_store_dwordx4 v[216:219], v253, s[60:63], s42 offen sc1\n\t" \
    "s_add_u32 s42, s42, 0x10000\n\t" \
    "s_nop 4\n\t" \
    "v_cvt_pk_bf16_f32 v200, v200, v204\n\t" \
    "v_cvt_pk_bf16_f32 v201, v201, v205\n\t" \
    "v_cvt_pk_bf16_f32 v202, v202, v206\n\t" \
    "v_cvt_pk_bf16_f32 v203, v203, v207\n\t" \
    "buffer_store_dwordx4 v[200:203], v253, s[60:63], s42 offen sc1\n\t" \
    "s_add_u32 s42, s42, 0x10000\n\t" \
    LST0 \
    "s_waitcnt lgkmcnt(0)\n\t" \
    "s_barrier \n\t" \
    LST3 \
    "ds_read_b128 v[224:227], v253 offset:3072\n\t" \
    "ds_read_b128 v[228:231], v253 offset:3072\n\t" \
    "ds_read_b128 v[232:235], v253 offset:3072\n\t" \
    "ds_read_b128 v[236:239], v253 offset:3072\n\t" \
    "ds_read_b128 v[240:243], v253 offset:3072\n\t" \
    "ds_read_b128 v[244:247], v253 offset:3072\n\t" \
    "ds_read_b128 v[248:251], v253 offset:3072\n\t" \
    "s_add_u32 s44, s40, 1\n\t" \
    "s_mov_b32 s45, s44\n\t" \
    "s_waitcnt vmcnt(0)\n\t" \
    "v_mov_b64 v[200:201], s[44:45]\n\t" \
    "v_mov_b64 v[202:203], s[44:45]\n\t" \
    "s_mov_b64 exec, 0xff\n\t" \
    "buffer_store_dwordx4 v[200:203], v253, s[60:63], %[fo] offen sc1\n\t" \
    "s_mov_b64 exec, -1\n\t" \
    LST1 \
    "ds_read_b128 v[220:223], v253 offset:3072\n\t" \
    "ds_read_b128 v[212:215], v254 offset:0\n\t" \
    "ds_read_b128 v[216:219], v254 offset:1024\n\t" \
    "s_and_b32 s44, s40, 3\n\t" \
    "s_mul_i32 s44, s44, 0x202000\n\t" \
    "s_add_u32 s42, s44, %[vo]\n\t" \
    "s_setprio 3\n\t" \
    "s_waitcnt lgkmcnt(0)\n\t" \
    "s_barrier \n\t" \
    LST4 \
    "ds_read_b128 v[204:207], v253 offset:1024\n\t" \
    "ds_read_b128 v[208:211], v253 offset:2048\n\t" \
    "ds_read_b128 v[200:203], v253 offset:0\n\t" \
    "s_waitcnt lgkmcnt(2)\n\t" \
    "v_smfmac_f32_16x16x64_bf16 v[224:227], v[204:207], v[24:31], v252\n\t" \
    "v_smfmac_f32_16x16x64_bf16 v[220:223], v[204:207], v[0:7], v252\n\t" \
    "s_waitcnt lgkmcnt(1)\n\t" \
    "v_smfmac_f32_16x16x64_bf16 v[224:227], v[208:211], v[32:39], v252\n\t" \
    "v_smfmac_f32_16x16x64_bf16 v[220:223], v[208:211], v[8:15], v252\n\t" \
    "s_waitcnt lgkmcnt(0)\n\t" \
    "v_smfmac_f32_16x16x64_bf16 v[224:227], v[200:203], v[16:23], v252\n\t" \
    "v_smfmac_f32_16x16x64_bf16 v[220:223], v[200:203], v[212:219], v252\n\t" \
    "ds_read_b128 v[212:215], v254 offset:2048\n\t" \
    "ds_read_b128 v[216:219], v254 offset:3072\n\t" \
    "v_smfmac_f32_16x16x64_bf16 v[232:235], v[204:207], v[64:71], v252\n\t" \
    "v_smfmac_f32_16x16x64_bf16 v[228:231], v[204:207], v[40:47], v252\n\t" \
    "v_smfmac_f32_16x16x64_bf16 v[232:235], v[208:211], v[72:79], v252\n\t" \
    "v_add_f32 v220, v220, v222\n\t" \
    "v_add_f32 v221, v221, v223\n\t" \
    "v_cvt_pk_bf16_f32 v220, v220, v221\n\t" \
    "v_smfmac_f32_16x16x64_bf16 v[228:231], v[208:211], v[48:55], v252\n\t" \
    "v_min_u32 v220, -2, v220\n\t" \
    "v_add_f32 v224, v224, v226\n\t" \
    "v_add_f32 v225, v225, v227\n\t" \
    "v_smfmac_f32_16x16x64_bf16 v[232:235], v[200:203], v[56:63], v252\n\t" \
    "v_cvt_pk_bf16_f32 v221, v224, v225\n\t" \
    "v_min_u32 v221, -2, v221\n\t" \
    "s_waitcnt lgkmcnt(0)\n\t" \
    "v_smfmac_f32_16x16x64_bf16 v[228:231], v[200:203], v[212:219], v252\n\t" \
    "ds_read_b128 v[212:215], v254 offset:4096\n\t" \
    "ds_read_b128 v[216:219], v254 offset:5120\n\t" \
    "v_smfmac_f32_16x16x64_bf16 v[240:243], v[204:207], v[104:111], v252\n\t" \
    "v_smfmac_f32_16x16x64_bf16 v[236:239], v[204:207], v[80:87], v252\n\t" \
    "v_smfmac_f32_16x16x64_bf16 v[240:243], v[208:211], v[112:119], v252\n\t" \
    "v_add_f32 v228, v228, v230\n\t" \
    "v_add_f32 v229, v229, v231\n\t" \
    "v_cvt_pk_bf16_f32 v222, v228, v229\n\t" \
    "v_smfmac_f32_16x16x64_bf16 v[236:239], v[208:211], v[88:95], v252\n\t" \
    "v_min_u32 v222, -2, v222\n\t" \
    "v_add_f32 v232, v232, v234\n\t" \
    "v_add_f32 v233, v233, v235\n\t" \
    "v_smfmac_f32_16x16x64_bf16 v[240:243], v[200:203], v[96:103], v252\n\t" \
    "v_cvt_pk_bf16_f32 v223, v232, v233\n\t" \
    "v_min_u32 v223, -2, v223\n\t" \
    "v_permlane32_swap_b32 v220, v222\n\t" \
    "s_waitcnt lgkmcnt(0)\n\t" \
    "v_smfmac_f32_16x16x64_bf16 v[236:239], v[200:203], v[212:219], v252\n\t" \
    "ds_read_b128 v[212:215], v254 offset:6144\n\t" \
    "ds_read_b128 v[216:219], v254 offset:7168\n\t" \
    "s_nop 0\n\t" \
    "v_permlane32_swap_b32 v221, v223\n\t" \
    "s_nop 1\n\t" \
    "v_permlane16_swap_b32 v220, v221\n\t" \
    "v_permlane16_swap_b32 v222, v223\n\t" \
    "buffer_store_dwordx4 v[220:223], v255, s[60:63], s42 offen sc1\n\t" \
    "s_add_u32 s42, s42, 0x10000\n\t" \
    "ds_read_b128 v[224:227], v253 offset:3072\n\t" \
    "ds_read_b128 v[228:231], v253 offset:3072\n\t" \
    "v_smfmac_f32_16x16x64_bf16 v[248:251], v[204:207], v[144:151], v252\n\t" \
    "v_smfmac_f32_16x16x64_bf16 v[244:247], v[204:207], v[120:127], v252\n\t" \
    "v_smfmac_f32_16x16x64_bf16 v[248:251], v[208:211], v[152:159], v252\n\t" \
    "v_add_f32 v236, v236, v238\n\t" \
    "v_add_f32 v237, v237, v239\n\t" \
    "v_cvt_pk_bf16_f32 v236, v236, v237\n\t" \
    "v_smfmac_f32_16x16x64_bf16 v[244:247], v[208:211], v[128:135], v252\n\t" \
    "v_min_u32 v236, -2, v236\n\t" \
    "v_add_f32 v240, v240, v242\n\t" \
    "v_add_f32 v241, v241, v243\n\t" \
    "v_smfmac_f32_16x16x64_bf16 v[248:251], v[200:203], v[136:143], v252\n\t" \
    "v_cvt_pk_bf16_f32 v237, v240, v241\n\t" \
    "v_min_u32 v237, -2, v237\n\t" \
    "s_waitcnt lgkmcnt(2)\n\t" \
    "v_smfmac_f32_16x16x64_bf16 v[244:247], v[200:203], v[212:219], v252\n\t" \
    "ds_read_b128 v[212:215], v254 offset:8192\n\t" \
    "ds_read_b128 v[216:219], v254 offset:9216\n\t" \
    "s_waitcnt lgkmcnt(2)\n\t" \
    "v_smfmac_f32_16x16x64_bf16 v[228:231], v[204:207], v[184:191], v252\n\t" \
    "v_smfmac_f32_16x16x64_bf16 v[224:227], v[204:207], v[160:167], v252\n\t" \
    "v_smfmac_f32_16x16x64_bf16 v[228:231], v[208:211], v[192:199], v252\n\t" \
    "v_and_b32 v232, 0x7fff, v255\n\t" \
    "v_lshrrev_b32 v233, 15, v255\n\t" \
    "v_lshl_add_u32 v232, v233, 3, v232\n\t" \
    "v_smfmac_f32_16x16x64_bf16 v[224:227], v[208:211], v[168:175], v252\n\t" \
    "v_add_f32 v244, v244, v246\n\t" \
    "v_add_f32 v245, v245, v247\n\t" \
    "v_cvt_pk_bf16_f32 v238, v244, v245\n\t" \
    "v_smfmac_f32_16x16x64_bf16 v[228:231], v[200:203], v[176:183], v252\n\t" \
    "v_min_u32 v238, -2, v238\n\t" \
    "v_add_f32 v248, v248, v250\n\t" \
    "v_add_f32 v249, v249, v251\n\t" \
    "s_waitcnt lgkmcnt(0)\n\t" \
    "v_smfmac_f32_16x16x64_bf16 v[224:227], v[200:203], v[212:219], v252\n\t" \
    "v_cvt_pk_bf16_f32 v239, v248, v249\n\t" \
    "v_min_u32 v239, -2, v239\n\t" \
    "v_permlane32_swap_b32 v236, v238\n\t" \
    "s_nop 0\n\t" \
    "v_permlane32_swap_b32 v237, v239\n\t" \
    "s_nop 1\n\t" \
    "v_permlane16_swap_b32 v236, v237\n\t" \
    "v_permlane16_swap_b32 v238, v239\n\t" \
    "buffer_store_dwordx4 v[236:239], v255, s[60:63], s42 offen sc1\n\t" \
    "s_add_u32 s42, s42, 0x10000\n\t" \
    "v_add_f32 v228, v228, v230\n\t" \
    "v_add_f32 v229, v229, v231\n\t" \
    "v_add_f32 v224, v224, v226\n\t" \
    "v_add_f32 v225, v225, v227\n\t" \
    "v_cvt_pk_bf16_f32 v229, v228, v229\n\t" \
    "v_cvt_pk_bf16_f32 v228, v224, v225\n\t" \
    "v_min_u32 v229, -2, v229\n\t" \
    "v_min_u32 v228, -2, v228\n\t" \
    "s_nop 1\n\t" \
    "v_permlane16_swap_b32 v228, v229\n\t" \
    "buffer_store_dwordx2 v[228:229], v232, s[60:63], s42 offen sc1\n\t" \
    "s_setprio 0\n\t" \
    LST5 \
    LFLUSH200 \
    "s_add_u32 s40, s40, 1\n\t" \
    "s_cmp_lg_u32 s40, 64\n\t" \
    "s_cbranch_scc1 .Lv41_%=\n\t" \
    "s_branch .Lend%=\n\t" \
    ".Lv3_%=:\n\t" \
    "s_mov_b32 s42, %[xs]\n\t" \
    "s_add_u32 m0, %[wm], 0xc000\n\t" \
    "s_mov_b32 s44, 0x0\n\t" \
    "buffer_load_dwordx4 v240, %[wr], s44 offen offset:0 nt lds\n\t" \
    "buffer_load_dwordx4 v241, %[wr], s44 offen offset:1024 nt lds\n\t" \
    "buffer_load_dwordx4 v242, %[wr], s44 offen offset:2048 nt lds\n\t" \
    "s_add_u32 m0, %[wm], 0xf000\n\t" \
    "s_mov_b32 s44, 0x60000\n\t" \
    "buffer_load_dwordx4 v240, %[wr], s44 offen offset:0 nt lds\n\t" \
    "buffer_load_dwordx4 v241, %[wr], s44 offen offset:1024 nt lds\n\t" \
    "buffer_load_dwordx4 v242, %[wr], s44 offen offset:2048 nt lds\n\t" \
    "s_add_u32 m0, %[wm], 0x0\n\t" \
    "s_mov_b32 s44, 0xc0000\n\t" \
    "buffer_load_dwordx4 v240, %[wr], s44 offen offset:0 nt lds\n\t" \
    "buffer_load_dwordx4 v241, %[wr], s44 offen offset:1024 nt lds\n\t" \
    "buffer_load_dwordx4 v242, %[wr], s44 offen offset:2048 nt lds\n\t" \
    "s_add_u32 m0, %[wm], 0x3000\n\t" \
    "s_mov_b32 s44, 0x120000\n\t" \
    "buffer_load_dwordx4 v240, %[wr], s44 offen offset:0 nt lds\n\t" \
    "buffer_load_dwordx4 v241, %[wr], s44 offen offset:1024 nt lds\n\t" \
    "buffer_load_dwordx4 v242, %[wr], s44 offen offset:2048 nt lds\n\t" \
    "s_add_u32 m0, %[wm], 0x6000\n\t" \
    "s_mov_b32 s44, 0x180000\n\t" \
    "buffer_load_dwordx4 v240, %[wr], s44 offen offset:0 nt lds\n\t" \
    "buffer_load_dwordx4 v241, %[wr], s44 offen offset:1024 nt lds\n\t" \
    "buffer_load_dwordx4 v242, %[wr], s44 offen offset:2048 nt lds\n\t" \
    "s_waitcnt lgkmcnt(0)\n\t" \
    "s_add_u32 s43, s48, s49\n\t" \
    "s_add_u32 s43, s43, s50\n\t" \
    "s_and_b32 s43, s43, 0x2000\n\t" \
    "s_xor_b32 s43, s43, %[lx]\n\t" \
    "s_add_u32 s60, s60, s43\n\t" \
    "s_addc_u32 s61, s61, 0\n\t" \
    "v_add_u32 v244, 0xc000, v243\n\t" \
    "s_waitcnt vmcnt(12) lgkmcnt(0)\n\t" \
    "s_barrier \n\t" \
    "s_add_u32 m0, %[wm], 0x9000\n\t" \
    "s_mov_b32 s44, 0x1e0000\n\t" \
    "buffer_load_dwordx4 v240, %[wr], s44 offen offset:0 nt lds\n\t" \
    "buffer_load_dwordx4 v241, %[wr], s44 offen offset:1024 nt lds\n\t" \
    "buffer_load_dwordx4 v242, %[wr], s44 offen offset:2048 nt lds\n\t" \
    "ds_read_b64_tr_b16 v[208:209], v244 offset:0\n\t" \
    "ds_read_b64_tr_b16 v[210:211], v244 offset:1536\n\t" \
    "ds_read_b64_tr_b16 v[40:41], v244 offset:32\n\t" \
    "ds_read_b64_tr_b16 v[42:43], v244 offset:1568\n\t" \
    "ds_read_b64_tr_b16 v[48:49], v244 offset:64\n\t" \
    "ds_read_b64_tr_b16 v[50:51], v244 offset:1600\n\t" \
    "v_add_u32 v244, 0xf000, v243\n\t" \
    "s_waitcnt vmcnt(12) lgkmcnt(0)\n\t" \
    "s_barrier \n\t" \
    "s_add_u32 m0, %[wm], 0xc000\n\t" \
    "s_mov_b32 s44, 0x240000\n\t" \
    "buffer_load_dwordx4 v240, %[wr], s44 offen offset:0 nt lds\n\t" \
    "buffer_load_dwordx4 v241, %[wr], s44 offen offset:1024 nt lds\n\t" \
    "buffer_load_dwordx4 v242, %[wr], s44 offen offset:2048 nt lds\n\t" \
    "ds_read_b64_tr_b16 v[212:213], v244 offset:0\n\t" \
    "ds_read_b64_tr_b16 v[214:215], v244 offset:1536\n\t" \
    "ds_read_b64_tr_b16 v[44:45], v244 offset:32\n\t" \
    "ds_read_b64_tr_b16 v[46:47], v244 offset:1568\n\t" \
    "ds_read_b64_tr_b16 v[52:53], v244 offset:64\n\t" \
    "ds_read_b64_tr_b16 v[54:55], v244 offset:1600\n\t" \
    "v_add_u32 v244, 0x0, v243\n\t" \
    "s_waitcnt vmcnt(12) lgkmcnt(0)\n\t" \
    "s_barrier \n\t" \
    "s_add_u32 m0, %[wm], 0xf000\n\t" \
    "s_mov_b32 s44, 0x2a0000\n\t" \
    "buffer_load_dwordx4 v240, %[wr], s44 offen offset:0 nt lds\n\t" \
    "buffer_load_dwordx4 v241, %[wr], s44 offen offset:1024 nt lds\n\t" \
    "buffer_load_dwordx4 v242, %[wr], s44 offen offset:2048 nt lds\n\t" \
    "ds_read_b64_tr_b16 v[56:57], v244 offset:0\n\t" \
    "ds_read_b64_tr_b16 v[58:59], v244 offset:1536\n\t" \
    "ds_read_b64_tr_b16 v[64:65], v244 offset:32\n\t" \
    "ds_read_b64_tr_b16 v[66:67], v244 offset:1568\n\t" \
    "ds_read_b64_tr_b16 v[72:73], v244 offset:64\n\t" \
    "ds_read_b64_tr_b16 v[74:75], v244 offset:1600\n\t" \
    "v_add_u32 v244, 0x3000, v243\n\t" \
    "s_waitcnt vmcnt(12) lgkmcnt(0)\n\t" \
    "s_barrier \n\t" \
    "s_add_u32 m0, %[wm], 0x0\n\t" \
    "s_mov_b32 s44, 0x300000\n\t" \
    "buffer_load_dwordx4 v240, %[wr], s44 offen offset:0 nt lds\n\t" \
    "buffer_load_dwordx4 v241, %[wr], s44 offen offset:1024 nt lds\n\t" \
    "buffer_load_dwordx4 v242, %[wr], s44 offen offset:2048 nt lds\n\t" \
    "ds_read_b64_tr_b16 v[60:61], v244 offset:0\n\t" \
    "ds_read_b64_tr_b16 v[62:63], v244 offset:1536\n\t" \
    "ds_read_b64_tr_b16 v[68:69], v244 offset:32\n\t" \
    "ds_read_b64_tr_b16 v[70:71], v244 offset:1568\n\t" \
    "ds_read_b64_tr_b16 v[76:77], v244 offset:64\n\t" \
    "ds_read_b64_tr_b16 v[78:79], v244 offset:1600\n\t" \
    "v_add_u32 v244, 0x6000, v243\n\t" \
    "s_waitcnt vmcnt(12) lgkmcnt(0)\n\t" \
    "s_barrier \n\t" \
    "s_add_u32 m0, %[wm], 0x3000\n\t" \
    "s_mov_b32 s44, 0x360000\n\t" \
    "buffer_load_dwordx4 v240, %[wr], s44 offen offset:0 nt lds\n\t" \
    "buffer_load_dwordx4 v241, %[wr], s44 offen offset:1024 nt lds\n\t" \
    "buffer_load_dwordx4 v242, %[wr], s44 offen offset:2048 nt lds\n\t" \
    "ds_read_b64_tr_b16 v[216:217], v244 offset:0\n\t" \
    "ds_read_b64_tr_b16 v[218:219], v244 offset:1536\n\t" \
    "ds_read_b64_tr_b16 v[80:81], v244 offset:32\n\t" \
    "ds_read_b64_tr_b16 v[82:83], v244 offset:1568\n\t" \
    "ds_read_b64_tr_b16 v[88:89], v244 offset:64\n\t" \
    "ds_read_b64_tr_b16 v[90:91], v244 offset:1600\n\t" \
    "v_add_u32 v244, 0x9000, v243\n\t" \
    "s_waitcnt vmcnt(12) lgkmcnt(0)\n\t" \
    "s_barrier \n\t" \
    "s_add_u32 m0, %[wm], 0x6000\n\t" \
    "s_mov_b32 s44, 0x3c0000\n\t" \
    "buffer_load_dwordx4 v240, %[wr], s44 offen offset:0 nt lds\n\t" \
    "buffer_load_dwordx4 v241, %[wr], s44 offen offset:1024 nt lds\n\t" \
    "buffer_load_dwordx4 v242, %[wr], s44 offen offset:2048 nt lds\n\t" \
    "ds_read_b64_tr_b16 v[220:221], v244 offset:0\n\t" \
    "ds_read_b64_tr_b16 v[222:223], v244 offset:1536\n\t" \
    "ds_read_b64_tr_b16 v[84:85], v244 offset:32\n\t" \
    "ds_read_b64_tr_b16 v[86:87], v244 offset:1568\n\t" \
    "ds_read_b64_tr_b16 v[92:93], v244 offset:64\n\t" \
    "ds_read_b64_tr_b16 v[94:95], v244 offset:1600\n\t" \
    "v_add_u32 v244, 0xc000, v243\n\t" \
    "s_waitcnt vmcnt(12) lgkmcnt(0)\n\t" \
    "s_barrier \n\t" \
    "s_add_u32 m0, %[wm], 0x9000\n\t" \
    "s_mov_b32 s44, 0x420000\n\t" \
    "buffer_load_dwordx4 v240, %[wr], s44 offen offset:0 nt lds\n\t" \
    "buffer_load_dwordx4 v241, %[wr], s44 offen offset:1024 nt lds\n\t" \
    "buffer_load_dwordx4 v242, %[wr], s44 offen offset:2048 nt lds\n\t" \
    "ds_read_b64_tr_b16 v[96:97], v244 offset:0\n\t" \
    "ds_read_b64_tr_b16 v[98:99], v244 offset:1536\n\t" \
    "ds_read_b64_tr_b16 v[104:105], v244 offset:32\n\t" \
    "ds_read_b64_tr_b16 v[106:107], v244 offset:1568\n\t" \
    "ds_read_b64_tr_b16 v[112:113], v244 offset:64\n\t" \
    "ds_read_b64_tr_b16 v[114:115], v244 offset:1600\n\t" \
    "v_add_u32 v244, 0xf000, v243\n\t" \
    "s_waitcnt vmcnt(12) lgkmcnt(0)\n\t" \
    "s_barrier \n\t" \
    "s_add_u32 m0, %[wm], 0xc000\n\t" \
    "s_mov_b32 s44, 0x480000\n\t" \
    "buffer_load_dwordx4 v240, %[wr], s44 offen offset:0 nt lds\n\t" \
    "buffer_load_dwordx4 v241, %[wr], s44 offen offset:1024 nt lds\n\t" \
    "buffer_load_dwordx4 v242, %[wr], s44 offen offset:2048 nt lds\n\t" \
    "ds_read_b64_tr_b16 v[100:101], v244 offset:0\n\t" \
    "ds_read_b64_tr_b16 v[102:103], v244 offset:1536\n\t" \
    "ds_read_b64_tr_b16 v[108:109], v244 offset:32\n\t" \
    "ds_read_b64_tr_b16 v[110:111], v244 offset:1568\n\t" \
    "ds_read_b64_tr_b16 v[116:117], v244 offset:64\n\t" \
    "ds_read_b64_tr_b16 v[118:119], v244 offset:1600\n\t" \
    "v_add_u32 v244, 0x0, v243\n\t" \
    "s_waitcnt vmcnt(12) lgkmcnt(0)\n\t" \
    "s_barrier \n\t" \
    "s_add_u32 m0, %[wm], 0xf000\n\t" \
    "s_mov_b32 s44, 0x4e0000\n\t" \
    "buffer_load_dwordx4 v240, %[wr], s44 offen offset:0 nt lds\n\t" \
    "buffer_load_dwordx4 v241, %[wr], s44 offen offset:1024 nt lds\n\t" \
    "buffer_load_dwordx4 v242, %[wr], s44 offen offset:2048 nt lds\n\t" \
    "ds_read_b64_tr_b16 v[224:225], v244 offset:0\n\t" \
    "ds_read_b64_tr_b16 v[226:227], v244 offset:1536\n\t" \
    "ds_read_b64_tr_b16 v[120:121], v244 offset:32\n\t" \
    "ds_read_b64_tr_b16 v[122:123], v244 offset:1568\n\t" \
    "ds_read_b64_tr_b16 v[128:129], v244 offset:64\n\t" \
    "ds_read_b64_tr_b16 v[130:131], v244 offset:1600\n\t" \
    "v_add_u32 v244, 0x3000, v243\n\t" \
    "s_waitcnt vmcnt(12) lgkmcnt(0)\n\t" \
    "s_barrier \n\t" \
    "v_and_b32 v252, 0xf0, v253\n\t" \
    "v_lshlrev_b32 v252, 3, v252\n\t" \
    "buffer_load_dword v252, v252, s[60:63], %[fl] offen sc1\n\t" \
    "s_add_u32 m0, %[wm], 0x0\n\t" \
    "s_mov_b32 s44, 0x540000\n\t" \
    "buffer_load_dwordx4 v240, %[wr], s44 offen offset:0 nt lds\n\t" \
    "buffer_load_dwordx4 v241, %[wr], s44 offen offset:1024 nt lds\n\t" \
    "buffer_load_dwordx4 v242, %[wr], s44 offen offset:2048 nt lds\n\t" \
    "ds_read_b64_tr_b16 v[228:229], v244 offset:0\n\t" \
    "ds_read_b64_tr_b16 v[230:231], v244 offset:1536\n\t" \
    "ds_read_b64_tr_b16 v[124:125], v244 offset:32\n\t" \
    "ds_read_b64_tr_b16 v[126:127], v244 offset:1568\n\t" \
    "ds_read_b64_tr_b16 v[132:133], v244 offset:64\n\t" \
    "ds_read_b64_tr_b16 v[134:135], v244 offset:1600\n\t" \
    "v_add_u32 v244, 0x6000, v243\n\t" \
    "s_waitcnt vmcnt(12) lgkmcnt(0)\n\t" \
    "s_barrier \n\t" \
    "s_add_u32 m0, %[wm], 0x3000\n\t" \
    "s_mov_b32 s44, 0x5a0000\n\t" \
    "buffer_load_dwordx4 v240, %[wr], s44 offen offset:0 nt lds\n\t" \
    "buffer_load_dwordx4 v241, %[wr], s44 offen offset:1024 nt lds\n\t" \
    "buffer_load_dwordx4 v242, %[wr], s44 offen offset:2048 nt lds\n\t" \
    "ds_read_b64_tr_b16 v[136:137], v244 offset:0\n\t" \
    "ds_read_b64_tr_b16 v[138:139], v244 offset:1536\n\t" \
    "ds_read_b64_tr_b16 v[144:145], v244 offset:32\n\t" \
    "ds_read_b64_tr_b16 v[146:147], v244 offset:1568\n\t" \
    "ds_read_b64_tr_b16 v[152:153], v244 offset:64\n\t" \
    "ds_read_b64_tr_b16 v[154:155], v244 offset:1600\n\t" \
    "v_add_u32 v244, 0x9000, v243\n\t" \
    "s_waitcnt vmcnt(12) lgkmcnt(0)\n\t" \
    "s_barrier \n\t" \
    "s_add_u32 m0, %[wm], 0x6000\n\t" \
    "s_mov_b32 s44, 0x600000\n\t" \
    "buffer_load_dwordx4 v240, %[wr], s44 offen offset:0 nt lds\n\t" \
    "buffer_load_dwordx4 v241, %[wr], s44 offen offset:1024 nt lds\n\t" \
    "buffer_load_dwordx4 v242, %[wr], s44 offen offset:2048 nt lds\n\t" \
    "ds_read_b64_tr_b16 v[140:141], v244 offset:0\n\t" \
    "ds_read_b64_tr_b16 v[142:143], v244 offset:1536\n\t" \
    "ds_read_b64_tr_b16 v[148:149], v244 offset:32\n\t" \
    "ds_read_b64_tr_b16 v[150:151], v244 offset:1568\n\t" \
    "ds_read_b64_tr_b16 v[156:157], v244 offset:64\n\t" \
    "ds_read_b64_tr_b16 v[158:159], v244 offset:1600\n\t" \
    "v_add_u32 v244, 0xc000, v243\n\t" \
    "s_waitcnt vmcnt(12) lgkmcnt(0)\n\t" \
    "s_barrier \n\t" \
    "s_add_u32 m0, %[wm], 0x9000\n\t" \
    "s_mov_b32 s44, 0x660000\n\t" \
    "buffer_load_dwordx4 v240, %[wr], s44 offen offset:0 nt lds\n\t" \
    "buffer_load_dwordx4 v241, %[wr], s44 offen offset:1024 nt lds\n\t" \
    "buffer_load_dwordx4 v242, %[wr], s44 offen offset:2048 nt lds\n\t" \
    "ds_read_b64_tr_b16 v[232:233], v244 offset:0\n\t" \
    "ds_read_b64_tr_b16 v[234:235], v244 offset:1536\n\t" \
    "ds_read_b64_tr_b16 v[160:161], v244 offset:32\n\t" \
    "ds_read_b64_tr_b16 v[162:163], v244 offset:1568\n\t" \
    "ds_read_b64_tr_b16 v[168:169], v244 offset:64\n\t" \
    "ds_read_b64_tr_b16 v[170:171], v244 offset:1600\n\t" \
    "v_add_u32 v244, 0xf000, v243\n\t" \
    "s_waitcnt vmcnt(12) lgkmcnt(0)\n\t" \
    "s_barrier \n\t" \
    "s_add_u32 m0, %[wm], 0xc000\n\t" \
    "s_mov_b32 s44, 0x6c0000\n\t" \
    "buffer_load_dwordx4 v240, %[wr], s44 offen offset:0 nt lds\n\t" \
    "buffer_load_dwordx4 v241, %[wr], s44 offen offset:1024 nt lds\n\t" \
    "buffer_load_dwordx4 v242, %[wr], s44 offen offset:2048 nt lds\n\t" \
    "ds_read_b64_tr_b16 v[236:237], v244 offset:0\n\t" \
    "ds_read_b64_tr_b16 v[238:239], v244 offset:1536\n\t" \
    "ds_read_b64_tr_b16 v[164:165], v244 offset:32\n\t" \
    "ds_read_b64_tr_b16 v[166:167], v244 offset:1568\n\t" \
    "ds_read_b64_tr_b16 v[172:173], v244 offset:64\n\t" \
    "ds_read_b64_tr_b16 v[174:175], v244 offset:1600\n\t" \
    "v_add_u32 v244, 0x0, v243\n\t" \
    "s_waitcnt vmcnt(12) lgkmcnt(0)\n\t" \
    "s_barrier \n\t" \
    "s_add_u32 m0, %[wm], 0xf000\n\t" \
    "s_mov_b32 s44, 0x720000\n\t" \
    "buffer_load_dwordx4 v240, %[wr], s44 offen offset:0 nt lds\n\t" \
    "buffer_load_dwordx4 v241, %[wr], s44 offen offset:1024 nt lds\n\t" \
    "buffer_load_dwordx4 v242, %[wr], s44 offen offset:2048 nt lds\n\t" \
    "ds_read_b64_tr_b16 v[176:177], v244 offset:0\n\t" \
    "ds_read_b64_tr_b16 v[178:179], v244 offset:1536\n\t" \
    "ds_read_b64_tr_b16 v[184:185], v244 offset:32\n\t" \
    "ds_read_b64_tr_b16 v[186:187], v244 offset:1568\n\t" \
    "ds_read_b64_tr_b16 v[192:193], v244 offset:64\n\t" \
    "ds_read_b64_tr_b16 v[194:195], v244 offset:1600\n\t" \
    "v_add_u32 v244, 0x3000, v243\n\t" \
    "s_waitcnt vmcnt(12) lgkmcnt(0)\n\t" \
    "s_barrier \n\t" \
    "ds_read_b64_tr_b16 v[180:181], v244 offset:0\n\t" \
    "ds_read_b64_tr_b16 v[182:183], v244 offset:1536\n\t" \
    "ds_read_b64_tr_b16 v[188:189], v244 offset:32\n\t" \
    "ds_read_b64_tr_b16 v[190:191], v244 offset:1568\n\t" \
    "ds_read_b64_tr_b16 v[196:197], v244 offset:64\n\t" \
    "ds_read_b64_tr_b16 v[198:199], v244 offset:1600\n\t" \
    "v_add_u32 v244, 0x6000, v243\n\t" \
    "s_waitcnt vmcnt(9) lgkmcnt(0)\n\t" \
    "s_barrier \n\t" \
    "ds_read_b64_tr_b16 v[200:201], v244 offset:0\n\t" \
    "ds_read_b64_tr_b16 v[202:203], v244 offset:1536\n\t" \
    "ds_read_b64_tr_b16 v[0:1], v244 offset:32\n\t" \
    "ds_read_b64_tr_b16 v[2:3], v244 offset:1568\n\t" \
    "ds_read_b64_tr_b16 v[8:9], v244 offset:64\n\t" \
    "ds_read_b64_tr_b16 v[10:11], v244 offset:1600\n\t" \
    "v_add_u32 v244, 0x9000, v243\n\t" \
    "s_waitcnt vmcnt(6) lgkmcnt(0)\n\t" \
    "s_barrier \n\t" \
    "ds_read_b64_tr_b16 v[204:205], v244 offset:0\n\t" \
    "ds_read_b64_tr_b16 v[206:207], v244 offset:1536\n\t" \
    "ds_read_b64_tr_b16 v[4:5], v244 offset:32\n\t" \
    "ds_read_b64_tr_b16 v[6:7], v244 offset:1568\n\t" \
    "ds_read_b64_tr_b16 v[12:13], v244 offset:64\n\t" \
    "ds_read_b64_tr_b16 v[14:15], v244 offset:1600\n\t" \
    "v_add_u32 v244, 0xc000, v243\n\t" \
    "s_waitcnt vmcnt(3) lgkmcnt(0)\n\t" \
    "s_barrier \n\t" \
    "ds_read_b64_tr_b16 v[16:17], v244 offset:0\n\t" \
    "ds_read_b64_tr_b16 v[18:19], v244 offset:1536\n\t" \
    "ds_read_b64_tr_b16 v[24:25], v244 offset:32\n\t" \
    "ds_read_b64_tr_b16 v[26:27], v244 offset:1568\n\t" \
    "ds_read_b64_tr_b16 v[32:33], v244 offset:64\n\t" \
    "ds_read_b64_tr_b16 v[34:35], v244 offset:1600\n\t" \
    "s_mov_b32 s43, 0\n\t" \
    "v_cmp_eq_u32 vcc, 0, v252\n\t" \
    "s_cbranch_vccnz .Lv31_%=\n\t" \
    "s_movk_i32 s45, 0x3000\n\t" \
    "s_mov_b32 m0, s45\n\t" \
    "s_nop 0\n\t" \
    "buffer_load_dwordx4 v253, s[60:63], s42 offen offset:0 sc1 lds\n\t" \
    "buffer_load_dwordx4 v253, s[60:63], s42 offen offset:1024 sc1 lds\n\t" \
    "buffer_load_dwordx4 v253, s[60:63], s42 offen offset:2048 sc1 lds\n\t" \
    "buffer_load_dwordx4 v253, s[60:63], s42 offen offset:3072 sc1 lds\n\t" \
    "s_add_u32 s44, s42, 0x1000\n\t" \
    "s_mov_b32 s45, 0x8000\n\t" \
    "s_mov_b32 m0, s45\n\t" \
    "s_nop 0\n\t" \
    "buffer_load_dwordx4 v253, s[60:63], s44 offen offset:0 sc1 lds\n\t" \
    "buffer_load_dwordx4 v253, s[60:63], s44 offen offset:1024 sc1 lds\n\t" \
    "buffer_load_dwordx4 v253, s[60:63], s44 offen offset:2048 sc1 lds\n\t" \
    "buffer_load_dwordx4 v253, s[60:63], s44 offen offset:3072 sc1 lds\n\t" \
    "s_add_u32 s44, s42, 0x4c00\n\t" \
    "s_mov_b32 s45, 0x4c00\n\t" \
    "s_mov_b32 m0, s45\n\t" \
    "s_nop 0\n\t" \
    "buffer_load_dwordx4 v253, s[60:63], s44 offen offset:0 sc1 lds\n\t" \
    "s_add_u32 s44, s44, 0x1000\n\t" \
    "s_mov_b32 s45, 0x9c00\n\t" \
    "s_mov_b32 m0, s45\n\t" \
    "s_nop 0\n\t" \
    "buffer_load_dwordx4 v253, s[60:63], s44 offen offset:0 sc1 lds\n\t" \
    "s_mov_b32 s43, 1\n\t" \
    ".Lv31_%=:\n\t" \
    "v_add_u32 v244, 0xf000, v243\n\t" \
    "s_waitcnt vmcnt(0) lgkmcnt(0)\n\t" \
    "s_barrier \n\t" \
    "ds_read_b64_tr_b16 v[20:21], v244 offset:0\n\t" \
    "ds_read_b64_tr_b16 v[22:23], v244 offset:1536\n\t" \
    "ds_read_b64_tr_b16 v[28:29], v244 offset:32\n\t" \
    "ds_read_b64_tr_b16 v[30:31], v244 offset:1568\n\t" \
    "ds_read_b64_tr_b16 v[36:37], v244 offset:64\n\t" \
    "ds_read_b64_tr_b16 v[38:39], v244 offset:1600\n\t" \
    "s_waitcnt lgkmcnt(0)\n\t" \
    "s_barrier \n\t" \
    "ds_write_b128 v254, v[200:203] offset:55296\n\t" \
    "ds_write_b128 v254, v[204:207] offset:56320\n\t" \
    "ds_write_b128 v254, v[208:211] offset:57344\n\t" \
    "ds_write_b128 v254, v[212:215] offset:58368\n\t" \
    "ds_write_b128 v254, v[216:219] offset:59392\n\t" \
    "ds_write_b128 v254, v[220:223] offset:60416\n\t" \
    "ds_write_b128 v254, v[224:227] offset:61440\n\t" \
    "ds_write_b128 v254, v[228:231] offset:62464\n\t" \
    "ds_write_b128 v254, v[232:235] offset:63488\n\t" \
    "ds_write_b128 v254, v[236:239] offset:64512\n\t" \
    "s_cmp_lg_u32 s43, 0\n\t" \
    "s_cbranch_scc1 .Lv32_%=\n\t" \
    "s_mov_b32 s44, 0\n\t" \
    ".Lv33_%=:\n\t" \
    "v_cmp_ge_u32 vcc, s44, v252\n\t" \
    "s_cbranch_vccz .Lv34_%=\n\t" \
    "s_cmp_eq_u32 s41, 0\n\t" \
    "s_cbranch_scc1 .Lv34_%=\n\t" \
    "s_sub_u32 s41, s41, 1\n\t" \
    "s_sleep 8\n\t" \
    "v_and_b32 v252, 0xf0, v253\n\t" \
    "v_lshlrev_b32 v252, 3, v252\n\t" \
    "buffer_load_dword v252, v252, s[60:63], %[fl] offen sc1\n\t" \
    "s_waitcnt vmcnt(0)\n\t" \
    "s_branch .Lv33_%=\n\t" \
    ".Lv34_%=:\n\t" \
    "s_movk_i32 s45, 0x3000\n\t" \
    "s_mov_b32 m0, s45\n\t" \
    "s_nop 0\n\t" \
    "buffer_load_dwordx4 v253, s[60:63], s42 offen offset:0 sc1 lds\n\t" \
    "buffer_load_dwordx4 v253, s[60:63], s42 offen offset:1024 sc1 lds\n\t" \
    "buffer_load_dwordx4 v253, s[60:63], s42 offen offset:2048 sc1 lds\n\t" \
    "buffer_load_dwordx4 v253, s[60:63], s42 offen offset:3072 sc1 lds\n\t" \
    "s_add_u32 s44, s42, 0x1000\n\t" \
    "s_mov_b32 s45, 0x8000\n\t" \
    "s_mov_b32 m0, s45\n\t" \
    "s_nop 0\n\t" \
    "buffer_load_dwordx4 v253, s[60:63], s44 offen offset:0 sc1 lds\n\t" \
    "buffer_load_dwordx4 v253, s[60:63], s44 offen offset:1024 sc1 lds\n\t" \
    "buffer_load_dwordx4 v253, s[60:63], s44 offen offset:2048 sc1 lds\n\t" \
    "buffer_load_dwordx4 v253, s[60:63], s44 offen offset:3072 sc1 lds\n\t" \
    "s_add_u32 s44, s42, 0x4c00\n\t" \
    "s_mov_b32 s45, 0x4c00\n\t" \
    "s_mov_b32 m0, s45\n\t" \
    "s_nop 0\n\t" \
    "buffer_load_dwordx4 v253, s[60:63], s44 offen offset:0 sc1 lds\n\t" \
    "s_add_u32 s44, s44, 0x1000\n\t" \
    "s_mov_b32 s45, 0x9c00\n\t" \
    "s_mov_b32 m0, s45\n\t" \
    "s_nop 0\n\t" \
    "buffer_load_dwordx4 v253, s[60:63], s44 offen offset:0 sc1 lds\n\t" \
    ".Lv32_%=:\n\t" \
    "s_waitcnt vmcnt(0) lgkmcnt(0)\n\t" \
    "s_barrier \n\t" \
    "ds_read_b128 v[224:227], v253 offset:0\n\t" \
    "ds_read_b128 v[244:247], v254 offset:57344\n\t" \
    "ds_read_b128 v[228:231], v253 offset:1024\n\t" \
    "ds_read_b128 v[232:235], v253 offset:20480\n\t" \
    "ds_read_b128 v[236:239], v253 offset:21504\n\t" \
    "ds_read_b128 v[248:251], v254 offset:58368\n\t" \
    ".Lv35_%=:\n\t" \
    LST7 \
    "s_waitcnt lgkmcnt(5)\n\t" \
    "v_mfma_f32_16x16x32_bf16 v[204:207], v[40:43], v[224:227], 0\n\t" \
    "s_waitcnt lgkmcnt(3)\n\t" \
    "v_mfma_f32_16x16x32_bf16 v[204:207], v[44:47], v[228:231], v[204:207]\n\t" \
    "ds_read_b128 v[240:243], v253 offset:2048\n\t" \
    "v_mfma_f32_16x16x32_bf16 v[208:211], v[48:51], v[224:227], 0\n\t" \
    "v_mfma_f32_16x16x32_bf16 v[208:211], v[52:55], v[228:231], v[208:211]\n\t" \
    "v_mfma_f32_16x16x32_bf16 v[200:203], v[244:247], v[224:227], 0\n\t" \
    "s_waitcnt lgkmcnt(1)\n\t" \
    "v_mfma_f32_16x16x32_bf16 v[200:203], v[248:251], v[228:231], v[200:203]\n\t" \
    "v_mfma_f32_16x16x32_bf16 v[216:219], v[40:43], v[232:235], 0\n\t" \
    "ds_read_b128 v[224:227], v253 offset:3072\n\t" \
    "v_mfma_f32_16x16x32_bf16 v[216:219], v[44:47], v[236:239], v[216:219]\n\t" \
    "ds_read_b128 v[228:231], v253 offset:22528\n\t" \
    "v_mfma_f32_16x16x32_bf16 v[220:223], v[48:51], v[232:235], 0\n\t" \
    "v_mfma_f32_16x16x32_bf16 v[220:223], v[52:55], v[236:239], v[220:223]\n\t" \
    "v_mfma_f32_16x16x32_bf16 v[212:215], v[244:247], v[232:235], 0\n\t" \
    "ds_read_b128 v[244:247], v254 offset:59392\n\t" \
    "v_mfma_f32_16x16x32_bf16 v[212:215], v[248:251], v[236:239], v[212:215]\n\t" \
    "ds_read_b128 v[248:251], v254 offset:60416\n\t" \
    "s_waitcnt lgkmcnt(4)\n\t" \
    "v_mfma_f32_16x16x32_bf16 v[204:207], v[64:67], v[240:243], v[204:207]\n\t" \
    "ds_read_b128 v[232:235], v253 offset:23552\n\t" \
    "s_waitcnt lgkmcnt(4)\n\t" \
    "v_mfma_f32_16x16x32_bf16 v[204:207], v[68:71], v[224:227], v[204:207]\n\t" \
    "ds_read_b128 v[236:239], v253 offset:4096\n\t" \
    "v_mfma_f32_16x16x32_bf16 v[208:211], v[72:75], v[240:243], v[208:211]\n\t" \
    "v_mfma_f32_16x16x32_bf16 v[208:211], v[76:79], v[224:227], v[208:211]\n\t" \
    "v_mfma_f32_16x16x32_bf16 v[200:203], v[56:59], v[240:243], v[200:203]\n\t" \
    "v_mfma_f32_16x16x32_bf16 v[200:203], v[60:63], v[224:227], v[200:203]\n\t" \
    "s_waitcnt lgkmcnt(4)\n\t" \
    "v_mfma_f32_16x16x32_bf16 v[216:219], v[64:67], v[228:231], v[216:219]\n\t" \
    "ds_read_b128 v[240:243], v253 offset:5120\n\t" \
    "s_waitcnt lgkmcnt(2)\n\t" \
    "v_mfma_f32_16x16x32_bf16 v[216:219], v[68:71], v[232:235], v[216:219]\n\t" \
    "ds_read_b128 v[224:227], v253 offset:24576\n\t" \
    "v_mfma_f32_16x16x32_bf16 v[220:223], v[72:75], v[228:231], v[220:223]\n\t" \
    "v_mfma_f32_16x16x32_bf16 v[220:223], v[76:79], v[232:235], v[220:223]\n\t" \
    "v_mfma_f32_16x16x32_bf16 v[212:215], v[56:59], v[228:231], v[212:215]\n\t" \
    "v_mfma_f32_16x16x32_bf16 v[212:215], v[60:63], v[232:235], v[212:215]\n\t" \
    "s_waitcnt lgkmcnt(2)\n\t" \
    "v_mfma_f32_16x16x32_bf16 v[204:207], v[80:83], v[236:239], v[204:207]\n\t" \
    "ds_read_b128 v[228:231], v253 offset:25600\n\t" \
    "s_add_u32 s44, s40, 1\n\t" \
    "s_waitcnt lgkmcnt(2)\n\t" \
    "v_mfma_f32_16x16x32_bf16 v[204:207], v[84:87], v[240:243], v[204:207]\n\t" \
    "ds_read_b128 v[232:235], v253 offset:6144\n\t" \
    "s_and_b32 s45, s44, 15\n\t" \
    "v_mfma_f32_16x16x32_bf16 v[208:211], v[88:91], v[236:239], v[208:211]\n\t" \
    "s_cmp_eq_u32 s45, 0\n\t" \
    "v_mfma_f32_16x16x32_bf16 v[208:211], v[92:95], v[240:243], v[208:211]\n\t" \
    "s_cselect_b32 s45, %[cj], %[cs]\n\t" \
    "v_mfma_f32_16x16x32_bf16 v[200:203], v[244:247], v[236:239], v[200:203]\n\t" \
    "s_cmp_eq_u32 s44, 64\n\t" \
    "v_mfma_f32_16x16x32_bf16 v[200:203], v[248:251], v[240:243], v[200:203]\n\t" \
    "s_cselect_b32 s45, 0, s45\n\t" \
    "s_waitcnt lgkmcnt(2)\n\t" \
    "v_mfma_f32_16x16x32_bf16 v[216:219], v[80:83], v[224:227], v[216:219]\n\t" \
    "ds_read_b128 v[236:239], v253 offset:7168\n\t" \
    "s_add_u32 s42, s42, s45\n\t" \
    "s_waitcnt lgkmcnt(2)\n\t" \
    "v_mfma_f32_16x16x32_bf16 v[216:219], v[84:87], v[228:231], v[216:219]\n\t" \
    "ds_read_b128 v[240:243], v253 offset:26624\n\t" \
    "v_mfma_f32_16x16x32_bf16 v[220:223], v[88:91], v[224:227], v[220:223]\n\t" \
    "v_mfma_f32_16x16x32_bf16 v[220:223], v[92:95], v[228:231], v[220:223]\n\t" \
    "v_mfma_f32_16x16x32_bf16 v[212:215], v[244:247], v[224:227], v[212:215]\n\t" \
    "ds_read_b128 v[244:247], v254 offset:61440\n\t" \
    "v_mfma_f32_16x16x32_bf16 v[212:215], v[248:251], v[228:231], v[212:215]\n\t" \
    "ds_read_b128 v[248:251], v254 offset:62464\n\t" \
    "s_waitcnt lgkmcnt(4)\n\t" \
    "v_mfma_f32_16x16x32_bf16 v[204:207], v[104:107], v[232:235], v[204:207]\n\t" \
    "ds_read_b128 v[224:227], v253 offset:27648\n\t" \
    "s_waitcnt lgkmcnt(4)\n\t" \
    "v_mfma_f32_16x16x32_bf16 v[204:207], v[108:111], v[236:239], v[204:207]\n\t" \
    "ds_read_b128 v[228:231], v253 offset:8192\n\t" \
    "v_mfma_f32_16x16x32_bf16 v[208:211], v[112:115], v[232:235], v[208:211]\n\t" \
    "v_mfma_f32_16x16x32_bf16 v[208:211], v[116:119], v[236:239], v[208:211]\n\t" \
    "v_mfma_f32_16x16x32_bf16 v[200:203], v[96:99], v[232:235], v[200:203]\n\t" \
    "v_mfma_f32_16x16x32_bf16 v[200:203], v[100:103], v[236:239], v[200:203]\n\t" \
    "s_waitcnt lgkmcnt(4)\n\t" \
    "v_mfma_f32_16x16x32_bf16 v[216:219], v[104:107], v[240:243], v[216:219]\n\t" \
    "ds_read_b128 v[232:235], v253 offset:9216\n\t" \
    "s_waitcnt lgkmcnt(2)\n\t" \
    "v_mfma_f32_16x16x32_bf16 v[216:219], v[108:111], v[224:227], v[216:219]\n\t" \
    "ds_read_b128 v[236:239], v253 offset:28672\n\t" \
    "v_mfma_f32_16x16x32_bf16 v[220:223], v[112:115], v[240:243], v[220:223]\n\t" \
    "v_mfma_f32_16x16x32_bf16 v[220:223], v[116:119], v[224:227], v[220:223]\n\t" \
    "v_mfma_f32_16x16x32_bf16 v[212:215], v[96:99], v[240:243], v[212:215]\n\t" \
    "v_mfma_f32_16x16x32_bf16 v[212:215], v[100:103], v[224:227], v[212:215]\n\t" \
    "s_waitcnt lgkmcnt(2)\n\t" \
    "v_mfma_f32_16x16x32_bf16 v[204:207], v[120:123], v[228:231], v[204:207]\n\t" \
    "ds_read_b128 v[240:243], v253 offset:29696\n\t" \
    "s_waitcnt lgkmcnt(2)\n\t" \
    "v_mfma_f32_16x16x32_bf16 v[204:207], v[124:127], v[232:235], v[204:207]\n\t" \
    "ds_read_b128 v[224:227], v253 offset:10240\n\t" \
    "v_mfma_f32_16x16x32_bf16 v[208:211], v[128:131], v[228:231], v[208:211]\n\t" \
    "v_mfma_f32_16x16x32_bf16 v[208:211], v[132:135], v[232:235], v[208:211]\n\t" \
    "v_mfma_f32_16x16x32_bf16 v[200:203], v[244:247], v[228:231], v[200:203]\n\t" \
    "v_mfma_f32_16x16x32_bf16 v[200:203], v[248:251], v[232:235], v[200:203]\n\t" \
    "s_waitcnt lgkmcnt(2)\n\t" \
    "v_mfma_f32_16x16x32_bf16 v[216:219], v[120:123], v[236:239], v[216:219]\n\t" \
    "ds_read_b128 v[228:231], v253 offset:11264\n\t" \
    "s_waitcnt lgkmcnt(2)\n\t" \
    "v_mfma_f32_16x16x32_bf16 v[216:219], v[124:127], v[240:243], v[216:219]\n\t" \
    "ds_read_b128 v[232:235], v253 offset:30720\n\t" \
    "v_mfma_f32_16x16x32_bf16 v[220:223], v[128:131], v[236:239], v[220:223]\n\t" \
    "v_mfma_f32_16x16x32_bf16 v[220:223], v[132:135], v[240:243], v[220:223]\n\t" \
    "v_mfma_f32_16x16x32_bf16 v[212:215], v[244:247], v[236:239], v[212:215]\n\t" \
    "ds_read_b128 v[244:247], v254 offset:63488\n\t" \
    "v_mfma_f32_16x16x32_bf16 v[212:215], v[248:251], v[240:243], v[212:215]\n\t" \
    "ds_read_b128 v[248:251], v254 offset:64512\n\t" \
    "s_waitcnt lgkmcnt(4)\n\t" \
    "v_mfma_f32_16x16x32_bf16 v[204:207], v[144:147], v[224:227], v[204:207]\n\t" \
    "ds_read_b128 v[236:239], v253 offset:31744\n\t" \
    "s_and_b32 s44, s40, 3\n\t" \
    "s_waitcnt lgkmcnt(4)\n\t" \
    "v_mfma_f32_16x16x32_bf16 v[204:207], v[148:151], v[228:231], v[204:207]\n\t" \
    "ds_read_b128 v[240:243], v253 offset:12288\n\t" \
    "s_lshl_b32 s44, s44, 17\n\t" \
    "v_mfma_f32_16x16x32_bf16 v[208:211], v[152:155], v[224:227], v[208:211]\n\t" \
    "s_add_u32 s44, s44, %[mb]\n\t" \
    "v_mfma_f32_16x16x32_bf16 v[208:211], v[156:159], v[228:231], v[208:211]\n\t" \
    "s_add_u32 s46, s44, 0x4000\n\t" \
    "v_mfma_f32_16x16x32_bf16 v[200:203], v[136:139], v[224:227], v[200:203]\n\t" \
    "s_add_u32 s47, s44, 0x4400\n\t" \
    "v_mfma_f32_16x16x32_bf16 v[200:203], v[140:143], v[228:231], v[200:203]\n\t" \
    "s_waitcnt lgkmcnt(4)\n\t" \
    "v_mfma_f32_16x16x32_bf16 v[216:219], v[144:147], v[232:235], v[216:219]\n\t" \
    "ds_read_b128 v[224:227], v253 offset:13312\n\t" \
    "s_waitcnt lgkmcnt(2)\n\t" \
    "v_mfma_f32_16x16x32_bf16 v[216:219], v[148:151], v[236:239], v[216:219]\n\t" \
    "ds_read_b128 v[228:231], v253 offset:32768\n\t" \
    "v_mfma_f32_16x16x32_bf16 v[220:223], v[152:155], v[232:235], v[220:223]\n\t" \
    "v_mfma_f32_16x16x32_bf16 v[220:223], v[156:159], v[236:239], v[220:223]\n\t" \
    "v_mfma_f32_16x16x32_bf16 v[212:215], v[136:139], v[232:235], v[212:215]\n\t" \
    "v_mfma_f32_16x16x32_bf16 v[212:215], v[140:143], v[236:239], v[212:215]\n\t" \
    "s_waitcnt lgkmcnt(2)\n\t" \
    "v_mfma_f32_16x16x32_bf16 v[204:207], v[160:163], v[240:243], v[204:207]\n\t" \
    "ds_read_b128 v[232:235], v253 offset:33792\n\t" \
    "s_waitcnt lgkmcnt(2)\n\t" \
    "v_mfma_f32_16x16x32_bf16 v[204:207], v[164:167], v[224:227], v[204:207]\n\t" \
    "ds_read_b128 v[236:239], v253 offset:16384\n\t" \
    "v_mfma_f32_16x16x32_bf16 v[208:211], v[168:171], v[240:243], v[208:211]\n\t" \
    "v_mfma_f32_16x16x32_bf16 v[208:211], v[172:175], v[224:227], v[208:211]\n\t" \
    "v_mfma_f32_16x16x32_bf16 v[200:203], v[244:247], v[240:243], v[200:203]\n\t" \
    "v_mfma_f32_16x16x32_bf16 v[200:203], v[248:251], v[224:227], v[200:203]\n\t" \
    "s_waitcnt lgkmcnt(2)\n\t" \
    "v_mfma_f32_16x16x32_bf16 v[216:219], v[160:163], v[228:231], v[216:219]\n\t" \
    "ds_read_b128 v[240:243], v253 offset:17408\n\t" \
    "s_waitcnt lgkmcnt(2)\n\t" \
    "v_mfma_f32_16x16x32_bf16 v[216:219], v[164:167], v[232:235], v[216:219]\n\t" \
    "ds_read_b128 v[224:227], v253 offset:36864\n\t" \
    "v_mfma_f32_16x16x32_bf16 v[220:223], v[168:171], v[228:231], v[220:223]\n\t" \
    "v_mfma_f32_16x16x32_bf16 v[220:223], v[172:175], v[232:235], v[220:223]\n\t" \
    "v_mfma_f32_16x16x32_bf16 v[212:215], v[244:247], v[228:231], v[212:215]\n\t" \
    "ds_read_b128 v[244:247], v254 offset:55296\n\t" \
    "v_mfma_f32_16x16x32_bf16 v[212:215], v[248:251], v[232:235], v[212:215]\n\t" \
    "ds_read_b128 v[248:251], v254 offset:56320\n\t" \
    "v_and_b32 v252, 0xf0, v253\n\t" \
    "v_lshlrev_b32 v252, 3, v252\n\t" \
    "buffer_load_dword v252, v252, s[60:63], %[fl] offen sc1\n\t" \
    "s_waitcnt lgkmcnt(4)\n\t" \
    "v_mfma_f32_16x16x32_bf16 v[204:207], v[0:3], v[236:239], v[204:207]\n\t" \
    "ds_read_b128 v[228:231], v253 offset:37888\n\t" \
    "s_waitcnt lgkmcnt(4)\n\t" \
    "v_mfma_f32_16x16x32_bf16 v[204:207], v[4:7], v[240:243], v[204:207]\n\t" \
    "ds_read_b128 v[232:235], v253 offset:14336\n\t" \
    "v_mfma_f32_16x16x32_bf16 v[208:211], v[8:11], v[236:239], v[208:211]\n\t" \
    "v_mfma_f32_16x16x32_bf16 v[208:211], v[12:15], v[240:243], v[208:211]\n\t" \
    "s_waitcnt lgkmcnt(3)\n\t" \
    "v_mfma_f32_16x16x32_bf16 v[200:203], v[244:247], v[236:239], v[200:203]\n\t" \
    "s_waitcnt lgkmcnt(2)\n\t" \
    "v_mfma_f32_16x16x32_bf16 v[200:203], v[248:251], v[240:243], v[200:203]\n\t" \
    "v_mfma_f32_16x16x32_bf16 v[216:219], v[0:3], v[224:227], v[216:219]\n\t" \
    "ds_read_b128 v[236:239], v253 offset:15360\n\t" \
    "s_waitcnt lgkmcnt(2)\n\t" \
    "v_mfma_f32_16x16x32_bf16 v[216:219], v[4:7], v[228:231], v[216:219]\n\t" \
    "ds_read_b128 v[240:243], v253 offset:18432\n\t" \
    "v_mfma_f32_16x16x32_bf16 v[220:223], v[8:11], v[224:227], v[220:223]\n\t" \
    "v_mfma_f32_16x16x32_bf16 v[220:223], v[12:15], v[228:231], v[220:223]\n\t" \
    "v_mfma_f32_16x16x32_bf16 v[212:215], v[244:247], v[224:227], v[212:215]\n\t" \
    "v_mfma_f32_16x16x32_bf16 v[212:215], v[248:251], v[228:231], v[212:215]\n\t" \
    "s_waitcnt lgkmcnt(2)\n\t" \
    "v_mfma_f32_16x16x32_bf16 v[204:207], v[184:187], v[232:235], v[204:207]\n\t" \
    "ds_read_b128 v[224:227], v253 offset:19456\n\t" \
    "v_mov_b64 v[244:245], 0\n\t" \
    "s_waitcnt lgkmcnt(2)\n\t" \
    "v_mfma_f32_16x16x32_bf16 v[204:207], v[188:191], v[236:239], v[204:207]\n\t" \
    "ds_read_b128 v[228:231], v253 offset:34816\n\t" \
    "v_mov_b64 v[246:247], 0\n\t" \
    "v_mfma_f32_16x16x32_bf16 v[208:211], v[192:195], v[232:235], v[208:211]\n\t" \
    "v_mov_b64 v[248:249], 0\n\t" \
    "v_mfma_f32_16x16x32_bf16 v[208:211], v[196:199], v[236:239], v[208:211]\n\t" \
    "v_mov_b64 v[250:251], 0\n\t" \
    "v_mfma_f32_16x16x32_bf16 v[200:203], v[176:179], v[232:235], v[200:203]\n\t" \
    "v_mfma_f32_16x16x32_bf16 v[200:203], v[180:183], v[236:239], v[200:203]\n\t" \
    "s_waitcnt lgkmcnt(2)\n\t" \
    "v_mfma_f32_16x16x32_bf16 v[204:207], v[24:27], v[240:243], v[204:207]\n\t" \
    "ds_read_b128 v[232:235], v253 offset:35840\n\t" \
    "s_waitcnt lgkmcnt(2)\n\t" \
    "v_mfma_f32_16x16x32_bf16 v[204:207], v[28:31], v[224:227], v[204:207]\n\t" \
    "ds_read_b128 v[236:239], v253 offset:38912\n\t" \
    "v_mfma_f32_16x16x32_bf16 v[208:211], v[32:35], v[240:243], v[208:211]\n\t" \
    "v_mfma_f32_16x16x32_bf16 v[208:211], v[36:39], v[224:227], v[208:211]\n\t" \
    "v_mfma_f32_16x16x32_bf16 v[200:203], v[16:19], v[240:243], v[200:203]\n\t" \
    "v_mfma_f32_16x16x32_bf16 v[200:203], v[20:23], v[224:227], v[200:203]\n\t" \
    "ds_read_b128 v[240:243], v253 offset:39936\n\t" \
    "s_waitcnt lgkmcnt(3)\n\t" \
    "v_mfma_f32_16x16x32_bf16 v[216:219], v[184:187], v[228:231], v[216:219]\n\t" \
    "s_waitcnt lgkmcnt(2)\n\t" \
    "v_mfma_f32_16x16x32_bf16 v[216:219], v[188:191], v[232:235], v[216:219]\n\t" \
    "v_cvt_pk_bf16_f32 v204, v204, v205\n\t" \
    "v_cvt_pk_bf16_f32 v205, v206, v207\n\t" \
    "s_waitcnt lgkmcnt(1)\n\t" \
    "v_mfma_f32_16x16x32_bf16 v[216:219], v[24:27], v[236:239], v[216:219]\n\t" \
    "ds_write_b64 v254, v[204:205] offset:4104\n\t" \
    "v_cvt_pk_bf16_f32 v208, v208, v209\n\t" \
    "s_waitcnt lgkmcnt(1)\n\t" \
    "v_mfma_f32_16x16x32_bf16 v[216:219], v[28:31], v[240:243], v[216:219]\n\t" \
    "v_cvt_pk_bf16_f32 v209, v210, v211\n\t" \
    "ds_write_b64 v254, v[208:209] offset:5128\n\t" \
    "v_mfma_f32_16x16x32_bf16 v[220:223], v[192:195], v[228:231], v[220:223]\n\t" \
    "v_cvt_pk_bf16_f32 v200, v200, v201\n\t" \
    "v_cvt_pk_bf16_f32 v201, v202, v203\n\t" \
    "v_mfma_f32_16x16x32_bf16 v[220:223], v[196:199], v[232:235], v[220:223]\n\t" \
    "ds_write_b64 v254, v[200:201] offset:3080\n\t" \
    "ds_read_b128 v[200:203], v254 offset:55296\n\t" \
    "v_mfma_f32_16x16x32_bf16 v[220:223], v[32:35], v[236:239], v[220:223]\n\t" \
    "ds_read_b128 v[204:207], v254 offset:56320\n\t" \
    "v_mov_b64 v[208:209], 0\n\t" \
    "v_mfma_f32_16x16x32_bf16 v[220:223], v[36:39], v[240:243], v[220:223]\n\t" \
    "v_mov_b64 v[210:211], 0\n\t" \
    "v_cvt_pk_bf16_f32 v216, v216, v217\n\t" \
    "v_mfma_f32_16x16x32_bf16 v[212:215], v[176:179], v[228:231], v[212:215]\n\t" \
    "v_cvt_pk_bf16_f32 v217, v218, v219\n\t" \
    "ds_write_b64 v254, v[216:217] offset:10248\n\t" \
    "v_mfma_f32_16x16x32_bf16 v[212:215], v[180:183], v[232:235], v[212:215]\n\t" \
    "v_mfma_f32_16x16x32_bf16 v[212:215], v[16:19], v[236:239], v[212:215]\n\t" \
    "v_cvt_pk_bf16_f32 v220, v220, v221\n\t" \
    "v_cvt_pk_bf16_f32 v221, v222, v223\n\t" \
    "v_mfma_f32_16x16x32_bf16 v[212:215], v[20:23], v[240:243], v[212:215]\n\t" \
    "ds_write_b64 v254, v[220:221] offset:11272\n\t" \
    "ds_read_b128 v[216:219], v254 offset:57344\n\t" \
    "ds_read_b128 v[220:223], v254 offset:58368\n\t" \
    "s_nop 4\n\t" \
    "v_cvt_pk_bf16_f32 v212, v212, v213\n\t" \
    "v_cvt_pk_bf16_f32 v213, v214, v215\n\t" \
    "ds_write_b64 v254, v[212:213] offset:9224\n\t" \
    LST6 \
    "s_waitcnt vmcnt(0) lgkmcnt(0)\n\t" \
    "s_barrier \n\t" \
    "s_setprio 2\n\t" \
    "s_add_u32 s44, s40, 1\n\t" \
    "s_min_u32 s44, s44, 63\n\t" \
    ".Lv36_%=:\n\t" \
    "v_cmp_ge_u32 vcc, s44, v252\n\t" \
    "s_cbranch_vccz .Lv37_%=\n\t" \
    "s_cmp_eq_u32 s41, 0\n\t" \
    "s_cbranch_scc1 .Lv37_%=\n\t" \
    "s_sub_u32 s41, s41, 1\n\t" \
    "s_sleep 2\n\t" \
    "v_and_b32 v252, 0xf0, v253\n\t" \
    "v_lshlrev_b32 v252, 3, v252\n\t" \
    "buffer_load_dword v252, v252, s[60:63], %[fl] offen sc1\n\t" \
    "s_waitcnt vmcnt(0)\n\t" \
    "s_branch .Lv36_%=\n\t" \
    ".Lv37_%=:\n\t" \
    "s_movk_i32 s45, 0x3000\n\t" \
    "s_mov_b32 m0, s45\n\t" \
    "s_nop 0\n\t" \
    "buffer_load_dwordx4 v253, s[60:63], s42 offen offset:0 sc1 lds\n\t" \
    "buffer_load_dwordx4 v253, s[60:63], s42 offen offset:1024 sc1 lds\n\t" \
    "buffer_load_dwordx4 v253, s[60:63], s42 offen offset:2048 sc1 lds\n\t" \
    "buffer_load_dwordx4 v253, s[60:63], s42 offen offset:3072 sc1 lds\n\t" \
    "s_add_u32 s44, s42, 0x1000\n\t" \
    "s_mov_b32 s45, 0x8000\n\t" \
    "s_mov_b32 m0, s45\n\t" \
    "s_nop 0\n\t" \
    "buffer_load_dwordx4 v253, s[60:63], s44 offen offset:0 sc1 lds\n\t" \
    "buffer_load_dwordx4 v253, s[60:63], s44 offen offset:1024 sc1 lds\n\t" \
    "buffer_load_dwordx4 v253, s[60:63], s44 offen offset:2048 sc1 lds\n\t" \
    "buffer_load_dwordx4 v253, s[60:63], s44 offen offset:3072 sc1 lds\n\t" \
    "s_add_u32 s44, s42, 0x4c00\n\t" \
    "s_mov_b32 s45, 0x4c00\n\t" \
    "s_mov_b32 m0, s45\n\t" \
    "s_nop 0\n\t" \
    "buffer_load_dwordx4 v253, s[60:63], s44 offen offset:0 sc1 lds\n\t" \
    "s_add_u32 s44, s44, 0x1000\n\t" \
    "s_mov_b32 s45, 0x9c00\n\t" \
    "s_mov_b32 m0, s45\n\t" \
    "s_nop 0\n\t" \
    "buffer_load_dwordx4 v253, s[60:63], s44 offen offset:0 sc1 lds\n\t" \
    LST1 \
    "s_sleep 18\n\t" \
    "buffer_load_dwordx4 v[224:227], v253, s[60:63], s46 offen sc1\n\t" \
    "buffer_load_dwordx4 v[228:231], v253, s[60:63], s47 offen sc1\n\t" \
    "s_waitcnt lgkmcnt(0)\n\t" \
    LFLUSH212 \
    "v_bfe_i32 v212, v253, 5, 1\n\t" \
    "v_and_b32 v212, 0xaaaa, v212\n\t" \
    "v_or_b32 v212, 0x4444, v212\n\t" \
    "v_and_b32 v213, 0xffff, v255\n\t" \
    "v_lshrrev_b32 v214, 16, v255\n\t" \
    ".Lv38_%=:\n\t" \
    "s_waitcnt vmcnt(1)\n\t" \
    "v_max_u32 v215, v224, v225\n\t" \
    "v_max3_u32 v215, v226, v227, v215\n\t" \
    "s_waitcnt vmcnt(0)\n\t" \
    "v_max3_u32 v215, v228, v229, v215\n\t" \
    "v_max3_u32 v215, v230, v231, v215\n\t" \
    "v_cmp_eq_u32 vcc, -1, v215\n\t" \
    "s_cbranch_vccz .Lv39_%=\n\t" \
    "v_max_u32 v215, v224, v225\n\t" \
    "v_max3_u32 v215, v226, v227, v215\n\t" \
    "v_cmp_eq_u32 vcc, -1, v215\n\t" \
    "s_cbranch_vccz .Lv310_%=\n\t" \
    "buffer_load_dwordx4 v[224:227], v253, s[60:63], s46 offen sc1\n\t" \
    ".Lv310_%=:\n\t" \
    "v_max_u32 v215, v228, v229\n\t" \
    "v_max3_u32 v215, v230, v231, v215\n\t" \
    "v_cmp_eq_u32 vcc, -1, v215\n\t" \
    "s_cbranch_vccz .Lv311_%=\n\t" \
    "buffer_load_dwordx4 v[228:231], v253, s[60:63], s47 offen sc1\n\t" \
    ".Lv311_%=:\n\t" \
    "s_cmp_eq_u32 s41, 0\n\t" \
    "s_cbranch_scc1 .Lv39_%=\n\t" \
    "s_sub_u32 s41, s41, 1\n\t" \
    "s_waitcnt vmcnt(0)\n\t" \
    "s_branch .Lv38_%=\n\t" \
    ".Lv39_%=:\n\t" \
    LST2 \
    "ds_write_b128 v253, v[224:227] offset:49152\n\t" \
    "ds_write_b128 v253, v[228:231] offset:50176\n\t" \
    "v_smfmac_f32_16x16x64_bf16 v[244:247], v[224:227], v[0:7], v212\n\t" \
    "v_smfmac_f32_16x16x64_bf16 v[248:251], v[224:227], v[8:15], v212\n\t" \
    "v_smfmac_f32_16x16x64_bf16 v[208:211], v[224:227], v[200:207], v212\n\t" \
    "s_waitcnt lgkmcnt(0)\n\t" \
    "s_barrier \n\t" \
    LST3 \
    "ds_read_b128 v[232:235], v253 offset:40960\n\t" \
    "ds_read_b128 v[236:239], v253 offset:41984\n\t" \
    "ds_read_b128 v[240:243], v253 offset:43008\n\t" \
    "ds_read_b128 v[224:227], v253 offset:44032\n\t" \
    "ds_read_b128 v[200:203], v254 offset:59392\n\t" \
    "ds_read_b128 v[204:207], v254 offset:60416\n\t" \
    "v_smfmac_f32_16x16x64_bf16 v[208:211], v[228:231], v[16:23], v212\n\t" \
    "v_smfmac_f32_16x16x64_bf16 v[244:247], v[228:231], v[24:31], v212\n\t" \
    "v_smfmac_f32_16x16x64_bf16 v[248:251], v[228:231], v[32:39], v212\n\t" \
    "ds_read_b128 v[228:231], v253 offset:45056\n\t" \
    "s_waitcnt lgkmcnt(6)\n\t" \
    "v_smfmac_f32_16x16x64_bf16 v[208:211], v[232:235], v[216:223], v212\n\t" \
    "ds_read_b128 v[216:219], v254 offset:61440\n\t" \
    "ds_read_b128 v[220:223], v254 offset:62464\n\t" \
    "v_smfmac_f32_16x16x64_bf16 v[244:247], v[232:235], v[40:47], v212\n\t" \
    "v_smfmac_f32_16x16x64_bf16 v[248:251], v[232:235], v[48:55], v212\n\t" \
    "ds_read_b128 v[232:235], v253 offset:46080\n\t" \
    "s_waitcnt lgkmcnt(8)\n\t" \
    "v_smfmac_f32_16x16x64_bf16 v[208:211], v[236:239], v[56:63], v212\n\t" \
    "v_smfmac_f32_16x16x64_bf16 v[244:247], v[236:239], v[64:71], v212\n\t" \
    "v_smfmac_f32_16x16x64_bf16 v[248:251], v[236:239], v[72:79], v212\n\t" \
    "ds_read_b128 v[236:239], v253 offset:47104\n\t" \
    "s_waitcnt lgkmcnt(5)\n\t" \
    "v_smfmac_f32_16x16x64_bf16 v[208:211], v[240:243], v[200:207], v212\n\t" \
    "ds_read_b128 v[200:203], v254 offset:63488\n\t" \
    "ds_read_b128 v[204:207], v254 offset:64512\n\t" \
    "v_smfmac_f32_16x16x64_bf16 v[244:247], v[240:243], v[80:87], v212\n\t" \
    "v_smfmac_f32_16x16x64_bf16 v[248:251], v[240:243], v[88:95], v212\n\t" \
    "ds_read_b128 v[240:243], v253 offset:48128\n\t" \
    "v_smfmac_f32_16x16x64_bf16 v[208:211], v[224:227], v[96:103], v212\n\t" \
    "v_smfmac_f32_16x16x64_bf16 v[244:247], v[224:227], v[104:111], v212\n\t" \
    "v_smfmac_f32_16x16x64_bf16 v[248:251], v[224:227], v[112:119], v212\n\t" \
    "s_waitcnt lgkmcnt(7)\n\t" \
    "v_smfmac_f32_16x16x64_bf16 v[244:247], v[228:231], v[120:127], v212\n\t" \
    "s_waitcnt lgkmcnt(4)\n\t" \
    "v_smfmac_f32_16x16x64_bf16 v[244:247], v[232:235], v[144:151], v212\n\t" \
    "s_waitcnt lgkmcnt(3)\n\t" \
    "v_smfmac_f32_16x16x64_bf16 v[244:247], v[236:239], v[160:167], v212\n\t" \
    "s_waitcnt lgkmcnt(0)\n\t" \
    "v_smfmac_f32_16x16x64_bf16 v[244:247], v[240:243], v[184:191], v212\n\t" \
    "v_smfmac_f32_16x16x64_bf16 v[248:251], v[228:231], v[128:135], v212\n\t" \
    "v_smfmac_f32_16x16x64_bf16 v[248:251], v[232:235], v[152:159], v212\n\t" \
    "v_smfmac_f32_16x16x64_bf16 v[248:251], v[236:239], v[168:175], v212\n\t" \
    "v_add_f32 v244, v244, v246\n\t" \
    "v_add_f32 v245, v245, v247\n\t" \
    "v_cvt_pk_bf16_f32 v244, v244, v245\n\t" \
    "v_smfmac_f32_16x16x64_bf16 v[248:251], v[240:243], v[192:199], v212\n\t" \
    "ds_write_b16 v213, v244 offset:512\n\t" \
    "ds_write_b16_d16_hi v213, v244 offset:528\n\t" \
    "v_smfmac_f32_16x16x64_bf16 v[208:211], v[228:231], v[216:223], v212\n\t" \
    "v_smfmac_f32_16x16x64_bf16 v[208:211], v[232:235], v[136:143], v212\n\t" \
    "v_smfmac_f32_16x16x64_bf16 v[208:211], v[236:239], v[200:207], v212\n\t" \
    "v_add_f32 v248, v248, v250\n\t" \
    "v_add_f32 v249, v249, v251\n\t" \
    "v_cvt_pk_bf16_f32 v248, v248, v249\n\t" \
    "v_smfmac_f32_16x16x64_bf16 v[208:211], v[240:243], v[176:183], v212\n\t" \
    "ds_write_b16 v213, v248 offset:1024\n\t" \
    "ds_write_b16_d16_hi v213, v248 offset:1040\n\t" \
    "s_nop 5\n\t" \
    "v_add_f32 v208, v208, v210\n\t" \
    "v_add_f32 v209, v209, v211\n\t" \
    "v_cvt_pk_bf16_f32 v208, v208, v209\n\t" \
    "ds_write_b16 v213, v208 offset:0\n\t" \
    "ds_write_b16_d16_hi v213, v208 offset:16\n\t" \
    "s_setprio 0\n\t" \
    "s_waitcnt lgkmcnt(0)\n\t" \
    "s_barrier \n\t" \
    LST4 \
    "ds_read_b128 v[224:227], v253 offset:0\n\t" \
    "ds_read_b128 v[244:247], v254 offset:57344\n\t" \
    "ds_read_b128 v[228:231], v253 offset:1024\n\t" \
    "ds_read_b128 v[232:235], v253 offset:20480\n\t" \
    "ds_read_b128 v[236:239], v253 offset:21504\n\t" \
    "ds_read_b128 v[248:251], v254 offset:58368\n\t" \
    "s_add_u32 s40, s40, 1\n\t" \
    "s_cmp_lg_u32 s40, 64\n\t" \
    "s_cbranch_scc1 .Lv35_%=\n\t" \
    "s_branch .Lend%=\n\t" \
    ".Lv2_%=:\n\t" \
    "s_mov_b32 s42, %[xs]\n\t" \
    "s_add_u32 m0, %[wm], 0xc000\n\t" \
    "s_mov_b32 s44, 0x0\n\t" \
    "buffer_load_dwordx4 v240, %[wr], s44 offen offset:0 nt lds\n\t" \
    "buffer_load_dwordx4 v241, %[wr], s44 offen offset:1024 nt lds\n\t" \
    "buffer_load_dwordx4 v242, %[wr], s44 offen offset:2048 nt lds\n\t" \
    "s_add_u32 m0, %[wm], 0xf000\n\t" \
    "s_mov_b32 s44, 0x60000\n\t" \
    "buffer_load_dwordx4 v240, %[wr], s44 offen offset:0 nt lds\n\t" \
    "buffer_load_dwordx4 v241, %[wr], s44 offen offset:1024 nt lds\n\t" \
    "buffer_load_dwordx4 v242, %[wr], s44 offen offset:2048 nt lds\n\t" \
    "s_add_u32 m0, %[wm], 0x0\n\t" \
    "s_mov_b32 s44, 0xc0000\n\t" \
    "buffer_load_dwordx4 v240, %[wr], s44 offen offset:0 nt lds\n\t" \
    "buffer_load_dwordx4 v241, %[wr], s44 offen offset:1024 nt lds\n\t" \
    "buffer_load_dwordx4 v242, %[wr], s44 offen offset:2048 nt lds\n\t" \
    "s_add_u32 m0, %[wm], 0x3000\n\t" \
    "s_mov_b32 s44, 0x120000\n\t" \
    "buffer_load_dwordx4 v240, %[wr], s44 offen offset:0 nt lds\n\t" \
    "buffer_load_dwordx4 v241, %[wr], s44 offen offset:1024 nt lds\n\t" \
    "buffer_load_dwordx4 v242, %[wr], s44 offen offset:2048 nt lds\n\t" \
    "s_add_u32 m0, %[wm], 0x6000\n\t" \
    "s_mov_b32 s44, 0x180000\n\t" \
    "buffer_load_dwordx4 v240, %[wr], s44 offen offset:0 nt lds\n\t" \
    "buffer_load_dwordx4 v241, %[wr], s44 offen offset:1024 nt lds\n\t" \
    "buffer_load_dwordx4 v242, %[wr], s44 offen offset:2048 nt lds\n\t" \
    "s_waitcnt lgkmcnt(0)\n\t" \
    "s_add_u32 s43, s48, s49\n\t" \
    "s_add_u32 s43, s43, s50\n\t" \
    "s_and_b32 s43, s43, 0x2000\n\t" \
    "s_xor_b32 s43, s43, %[lx]\n\t" \
    "s_add_u32 s60, s60, s43\n\t" \
    "s_addc_u32 s61, s61, 0\n\t" \
    "v_add_u32 v244, 0xc000, v243\n\t" \
    "s_waitcnt vmcnt(12) lgkmcnt(0)\n\t" \
    "s_barrier \n\t" \
    "s_add_u32 m0, %[wm], 0x9000\n\t" \
    "s_mov_b32 s44, 0x1e0000\n\t" \
    "buffer_load_dwordx4 v240, %[wr], s44 offen offset:0 nt lds\n\t" \
    "buffer_load_dwordx4 v241, %[wr], s44 offen offset:1024 nt lds\n\t" \
    "buffer_load_dwordx4 v242, %[wr], s44 offen offset:2048 nt lds\n\t" \
    "ds_read_b64_tr_b16 v[208:209], v244 offset:0\n\t" \
    "ds_read_b64_tr_b16 v[210:211], v244 offset:1536\n\t" \
    "ds_read_b64_tr_b16 v[40:41], v244 offset:32\n\t" \
    "ds_read_b64_tr_b16 v[42:43], v244 offset:1568\n\t" \
    "ds_read_b64_tr_b16 v[48:49], v244 offset:64\n\t" \
    "ds_read_b64_tr_b16 v[50:51], v244 offset:1600\n\t" \
    "v_add_u32 v244, 0xf000, v243\n\t" \
    "s_waitcnt vmcnt(12) lgkmcnt(0)\n\t" \
    "s_barrier \n\t" \
    "s_add_u32 m0, %[wm], 0xc000\n\t" \
    "s_mov_b32 s44, 0x240000\n\t" \
    "buffer_load_dwordx4 v240, %[wr], s44 offen offset:0 nt lds\n\t" \
    "buffer_load_dwordx4 v241, %[wr], s44 offen offset:1024 nt lds\n\t" \
    "buffer_load_dwordx4 v242, %[wr], s44 offen offset:2048 nt lds\n\t" \
    "ds_read_b64_tr_b16 v[212:213], v244 offset:0\n\t" \
    "ds_read_b64_tr_b16 v[214:215], v244 offset:1536\n\t" \
    "ds_read_b64_tr_b16 v[44:45], v244 offset:32\n\t" \
    "ds_read_b64_tr_b16 v[46:47], v244 offset:1568\n\t" \
    "ds_read_b64_tr_b16 v[52:53], v244 offset:64\n\t" \
    "ds_read_b64_tr_b16 v[54:55], v244 offset:1600\n\t" \
    "v_add_u32 v244, 0x0, v243\n\t" \
    "s_waitcnt vmcnt(12) lgkmcnt(0)\n\t" \
    "s_barrier \n\t" \
    "s_add_u32 m0, %[wm], 0xf000\n\t" \
    "s_mov_b32 s44, 0x2a0000\n\t" \
    "buffer_load_dwordx4 v240, %[wr], s44 offen offset:0 nt lds\n\t" \
    "buffer_load_dwordx4 v241, %[wr], s44 offen offset:1024 nt lds\n\t" \
    "buffer_load_dwordx4 v242, %[wr], s44 offen offset:2048 nt lds\n\t" \
    "ds_read_b64_tr_b16 v[56:57], v244 offset:0\n\t" \
    "ds_read_b64_tr_b16 v[58:59], v244 offset:1536\n\t" \
    "ds_read_b64_tr_b16 v[64:65], v244 offset:32\n\t" \
    "ds_read_b64_tr_b16 v[66:67], v244 offset:1568\n\t" \
    "ds_read_b64_tr_b16 v[72:73], v244 offset:64\n\t" \
    "ds_read_b64_tr_b16 v[74:75], v244 offset:1600\n\t" \
    "v_add_u32 v244, 0x3000, v243\n\t" \
    "s_waitcnt vmcnt(12) lgkmcnt(0)\n\t" \
    "s_barrier \n\t" \
    "s_add_u32 m0, %[wm], 0x0\n\t" \
    "s_mov_b32 s44, 0x300000\n\t" \
    "buffer_load_dwordx4 v240, %[wr], s44 offen offset:0 nt lds\n\t" \
    "buffer_load_dwordx4 v241, %[wr], s44 offen offset:1024 nt lds\n\t" \
    "buffer_load_dwordx4 v242, %[wr], s44 offen offset:2048 nt lds\n\t" \
    "ds_read_b64_tr_b16 v[60:61], v244 offset:0\n\t" \
    "ds_read_b64_tr_b16 v[62:63], v244 offset:1536\n\t" \
    "ds_read_b64_tr_b16 v[68:69], v244 offset:32\n\t" \
    "ds_read_b64_tr_b16 v[70:71], v244 offset:1568\n\t" \
    "ds_read_b64_tr_b16 v[76:77], v244 offset:64\n\t" \
    "ds_read_b64_tr_b16 v[78:79], v244 offset:1600\n\t" \
    "v_add_u32 v244, 0x6000, v243\n\t" \
    "s_waitcnt vmcnt(12) lgkmcnt(0)\n\t" \
    "s_barrier \n\t" \
    "s_add_u32 m0, %[wm], 0x3000\n\t" \
    "s_mov_b32 s44, 0x360000\n\t" \
    "buffer_load_dwordx4 v240, %[wr], s44 offen offset:0 nt lds\n\t" \
    "buffer_load_dwordx4 v241, %[wr], s44 offen offset:1024 nt lds\n\t" \
    "buffer_load_dwordx4 v242, %[wr], s44 offen offset:2048 nt lds\n\t" \
    "ds_read_b64_tr_b16 v[216:217], v244 offset:0\n\t" \
    "ds_read_b64_tr_b16 v[218:219], v244 offset:1536\n\t" \
    "ds_read_b64_tr_b16 v[80:81], v244 offset:32\n\t" \
    "ds_read_b64_tr_b16 v[82:83], v244 offset:1568\n\t" \
    "ds_read_b64_tr_b16 v[88:89], v244 offset:64\n\t" \
    "ds_read_b64_tr_b16 v[90:91], v244 offset:1600\n\t" \
    "v_add_u32 v244, 0x9000, v243\n\t" \
    "s_waitcnt vmcnt(12) lgkmcnt(0)\n\t" \
    "s_barrier \n\t" \
    "s_add_u32 m0, %[wm], 0x6000\n\t" \
    "s_mov_b32 s44, 0x3c0000\n\t" \
    "buffer_load_dwordx4 v240, %[wr], s44 offen offset:0 nt lds\n\t" \
    "buffer_load_dwordx4 v241, %[wr], s44 offen offset:1024 nt lds\n\t" \
    "buffer_load_dwordx4 v242, %[wr], s44 offen offset:2048 nt lds\n\t" \
    "ds_read_b64_tr_b16 v[220:221], v244 offset:0\n\t" \
    "ds_read_b64_tr_b16 v[222:223], v244 offset:1536\n\t" \
    "ds_read_b64_tr_b16 v[84:85], v244 offset:32\n\t" \
    "ds_read_b64_tr_b16 v[86:87], v244 offset:1568\n\t" \
    "ds_read_b64_tr_b16 v[92:93], v244 offset:64\n\t" \
    "ds_read_b64_tr_b16 v[94:95], v244 offset:1600\n\t" \
    "v_add_u32 v244, 0xc000, v243\n\t" \
    "s_waitcnt vmcnt(12) lgkmcnt(0)\n\t" \
    "s_barrier \n\t" \
    "s_add_u32 m0, %[wm], 0x9000\n\t" \
    "s_mov_b32 s44, 0x420000\n\t" \
    "buffer_load_dwordx4 v240, %[wr], s44 offen offset:0 nt lds\n\t" \
    "buffer_load_dwordx4 v241, %[wr], s44 offen offset:1024 nt lds\n\t" \
    "buffer_load_dwordx4 v242, %[wr], s44 offen offset:2048 nt lds\n\t" \
    "ds_read_b64_tr_b16 v[96:97], v244 offset:0\n\t" \
    "ds_read_b64_tr_b16 v[98:99], v244 offset:1536\n\t" \
    "ds_read_b64_tr_b16 v[104:105], v244 offset:32\n\t" \
    "ds_read_b64_tr_b16 v[106:107], v244 offset:1568\n\t" \
    "ds_read_b64_tr_b16 v[112:113], v244 offset:64\n\t" \
    "ds_read_b64_tr_b16 v[114:115], v244 offset:1600\n\t" \
    "v_add_u32 v244, 0xf000, v243\n\t" \
    "s_waitcnt vmcnt(12) lgkmcnt(0)\n\t" \
    "s_barrier \n\t" \
    "s_add_u32 m0, %[wm], 0xc000\n\t" \
    "s_mov_b32 s44, 0x480000\n\t" \
    "buffer_load_dwordx4 v240, %[wr], s44 offen offset:0 nt lds\n\t" \
    "buffer_load_dwordx4 v241, %[wr], s44 offen offset:1024 nt lds\n\t" \
    "buffer_load_dwordx4 v242, %[wr], s44 offen offset:2048 nt lds\n\t" \
    "ds_read_b64_tr_b16 v[100:101], v244 offset:0\n\t" \
    "ds_read_b64_tr_b16 v[102:103], v244 offset:1536\n\t" \
    "ds_read_b64_tr_b16 v[108:109], v244 offset:32\n\t" \
    "ds_read_b64_tr_b16 v[110:111], v244 offset:1568\n\t" \
    "ds_read_b64_tr_b16 v[116:117], v244 offset:64\n\t" \
    "ds_read_b64_tr_b16 v[118:119], v244 offset:1600\n\t" \
    "v_add_u32 v244, 0x0, v243\n\t" \
    "s_waitcnt vmcnt(12) lgkmcnt(0)\n\t" \
    "s_barrier \n\t" \
    "s_add_u32 m0, %[wm], 0xf000\n\t" \
    "s_mov_b32 s44, 0x4e0000\n\t" \
    "buffer_load_dwordx4 v240, %[wr], s44 offen offset:0 nt lds\n\t" \
    "buffer_load_dwordx4 v241, %[wr], s44 offen offset:1024 nt lds\n\t" \
    "buffer_load_dwordx4 v242, %[wr], s44 offen offset:2048 nt lds\n\t" \
    "ds_read_b64_tr_b16 v[200:201], v244 offset:0\n\t" \
    "ds_read_b64_tr_b16 v[202:203], v244 offset:1536\n\t" \
    "ds_read_b64_tr_b16 v[0:1], v244 offset:32\n\t" \
    "ds_read_b64_tr_b16 v[2:3], v244 offset:1568\n\t" \
    "ds_read_b64_tr_b16 v[8:9], v244 offset:64\n\t" \
    "ds_read_b64_tr_b16 v[10:11], v244 offset:1600\n\t" \
    "v_add_u32 v244, 0x3000, v243\n\t" \
    "s_waitcnt vmcnt(12) lgkmcnt(0)\n\t" \
    "s_barrier \n\t" \
    "v_and_b32 v252, 0xf0, v253\n\t" \
    "v_lshlrev_b32 v252, 3, v252\n\t" \
    "buffer_load_dword v252, v252, s[60:63], %[fl] offen sc1\n\t" \
    "s_add_u32 m0, %[wm], 0x0\n\t" \
    "s_mov_b32 s44, 0x540000\n\t" \
    "buffer_load_dwordx4 v240, %[wr], s44 offen offset:0 nt lds\n\t" \
    "buffer_load_dwordx4 v241, %[wr], s44 offen offset:1024 nt lds\n\t" \
    "buffer_load_dwordx4 v242, %[wr], s44 offen offset:2048 nt lds\n\t" \
    "ds_read_b64_tr_b16 v[204:205], v244 offset:0\n\t" \
    "ds_read_b64_tr_b16 v[206:207], v244 offset:1536\n\t" \
    "ds_read_b64_tr_b16 v[4:5], v244 offset:32\n\t" \
    "ds_read_b64_tr_b16 v[6:7], v244 offset:1568\n\t" \
    "ds_read_b64_tr_b16 v[12:13], v244 offset:64\n\t" \
    "ds_read_b64_tr_b16 v[14:15], v244 offset:1600\n\t" \
    "v_add_u32 v244, 0x6000, v243\n\t" \
    "s_waitcnt vmcnt(12) lgkmcnt(0)\n\t" \
    "s_barrier \n\t" \
    "s_add_u32 m0, %[wm], 0x3000\n\t" \
    "s_mov_b32 s44, 0x5a0000\n\t" \
    "buffer_load_dwordx4 v240, %[wr], s44 offen offset:0 nt lds\n\t" \
    "buffer_load_dwordx4 v241, %[wr], s44 offen offset:1024 nt lds\n\t" \
    "buffer_load_dwordx4 v242, %[wr], s44 offen offset:2048 nt lds\n\t" \
    "ds_read_b64_tr_b16 v[224:225], v244 offset:0\n\t" \
    "ds_read_b64_tr_b16 v[226:227], v244 offset:1536\n\t" \
    "ds_read_b64_tr_b16 v[120:121], v244 offset:32\n\t" \
    "ds_read_b64_tr_b16 v[122:123], v244 offset:1568\n\t" \
    "ds_read_b64_tr_b16 v[128:129], v244 offset:64\n\t" \
    "ds_read_b64_tr_b16 v[130:131], v244 offset:1600\n\t" \
    "v_add_u32 v244, 0x9000, v243\n\t" \
    "s_waitcnt vmcnt(12) lgkmcnt(0)\n\t" \
    "s_barrier \n\t" \
    "s_add_u32 m0, %[wm], 0x6000\n\t" \
    "s_mov_b32 s44, 0x600000\n\t" \
    "buffer_load_dwordx4 v240, %[wr], s44 offen offset:0 nt lds\n\t" \
    "buffer_load_dwordx4 v241, %[wr], s44 offen offset:1024 nt lds\n\t" \
    "buffer_load_dwordx4 v242, %[wr], s44 offen offset:2048 nt lds\n\t" \
    "ds_read_b64_tr_b16 v[228:229], v244 offset:0\n\t" \
    "ds_read_b64_tr_b16 v[230:231], v244 offset:1536\n\t" \
    "ds_read_b64_tr_b16 v[124:125], v244 offset:32\n\t" \
    "ds_read_b64_tr_b16 v[126:127], v244 offset:1568\n\t" \
    "ds_read_b64_tr_b16 v[132:133], v244 offset:64\n\t" \
    "ds_read_b64_tr_b16 v[134:135], v244 offset:1600\n\t" \
    "v_add_u32 v244, 0xc000, v243\n\t" \
    "s_waitcnt vmcnt(12) lgkmcnt(0)\n\t" \
    "s_barrier \n\t" \
    "s_add_u32 m0, %[wm], 0x9000\n\t" \
    "s_mov_b32 s44, 0x660000\n\t" \
    "buffer_load_dwordx4 v240, %[wr], s44 offen offset:0 nt lds\n\t" \
    "buffer_load_dwordx4 v241, %[wr], s44 offen offset:1024 nt lds\n\t" \
    "buffer_load_dwordx4 v242, %[wr], s44 offen offset:2048 nt lds\n\t" \
    "ds_read_b64_tr_b16 v[136:137], v244 offset:0\n\t" \
    "ds_read_b64_tr_b16 v[138:139], v244 offset:1536\n\t" \
    "ds_read_b64_tr_b16 v[144:145], v244 offset:32\n\t" \
    "ds_read_b64_tr_b16 v[146:147], v244 offset:1568\n\t" \
    "ds_read_b64_tr_b16 v[152:153], v244 offset:64\n\t" \
    "ds_read_b64_tr_b16 v[154:155], v244 offset:1600\n\t" \
    "v_add_u32 v244, 0xf000, v243\n\t" \
    "s_waitcnt vmcnt(12) lgkmcnt(0)\n\t" \
    "s_barrier \n\t" \
    "s_add_u32 m0, %[wm], 0xc000\n\t" \
    "s_mov_b32 s44, 0x6c0000\n\t" \
    "buffer_load_dwordx4 v240, %[wr], s44 offen offset:0 nt lds\n\t" \
    "buffer_load_dwordx4 v241, %[wr], s44 offen offset:1024 nt lds\n\t" \
    "buffer_load_dwordx4 v242, %[wr], s44 offen offset:2048 nt lds\n\t" \
    "ds_read_b64_tr_b16 v[140:141], v244 offset:0\n\t" \
    "ds_read_b64_tr_b16 v[142:143], v244 offset:1536\n\t" \
    "ds_read_b64_tr_b16 v[148:149], v244 offset:32\n\t" \
    "ds_read_b64_tr_b16 v[150:151], v244 offset:1568\n\t" \
    "ds_read_b64_tr_b16 v[156:157], v244 offset:64\n\t" \
    "ds_read_b64_tr_b16 v[158:159], v244 offset:1600\n\t" \
    "v_add_u32 v244, 0x0, v243\n\t" \
    "s_waitcnt vmcnt(12) lgkmcnt(0)\n\t" \
    "s_barrier \n\t" \
    "s_add_u32 m0, %[wm], 0xf000\n\t" \
    "s_mov_b32 s44, 0x720000\n\t" \
    "buffer_load_dwordx4 v240, %[wr], s44 offen offset:0 nt lds\n\t" \
    "buffer_load_dwordx4 v241, %[wr], s44 offen offset:1024 nt lds\n\t" \
    "buffer_load_dwordx4 v242, %[wr], s44 offen offset:2048 nt lds\n\t" \
    "ds_read_b64_tr_b16 v[16:17], v244 offset:0\n\t" \
    "ds_read_b64_tr_b16 v[18:19], v244 offset:1536\n\t" \
    "ds_read_b64_tr_b16 v[24:25], v244 offset:32\n\t" \
    "ds_read_b64_tr_b16 v[26:27], v244 offset:1568\n\t" \
    "ds_read_b64_tr_b16 v[32:33], v244 offset:64\n\t" \
    "ds_read_b64_tr_b16 v[34:35], v244 offset:1600\n\t" \
    "v_add_u32 v244, 0x3000, v243\n\t" \
    "s_waitcnt vmcnt(12) lgkmcnt(0)\n\t" \
    "s_barrier \n\t" \
    "ds_read_b64_tr_b16 v[20:21], v244 offset:0\n\t" \
    "ds_read_b64_tr_b16 v[22:23], v244 offset:1536\n\t" \
    "ds_read_b64_tr_b16 v[28:29], v244 offset:32\n\t" \
    "ds_read_b64_tr_b16 v[30:31], v244 offset:1568\n\t" \
    "ds_read_b64_tr_b16 v[36:37], v244 offset:64\n\t" \
    "ds_read_b64_tr_b16 v[38:39], v244 offset:1600\n\t" \
    "v_add_u32 v244, 0x6000, v243\n\t" \
    "s_waitcnt vmcnt(9) lgkmcnt(0)\n\t" \
    "s_barrier \n\t" \
    "ds_read_b64_tr_b16 v[232:233], v244 offset:0\n\t" \
    "ds_read_b64_tr_b16 v[234:235], v244 offset:1536\n\t" \
    "ds_read_b64_tr_b16 v[160:161], v244 offset:32\n\t" \
    "ds_read_b64_tr_b16 v[162:163], v244 offset:1568\n\t" \
    "ds_read_b64_tr_b16 v[168:169], v244 offset:64\n\t" \
    "ds_read_b64_tr_b16 v[170:171], v244 offset:1600\n\t" \
    "v_add_u32 v244, 0x9000, v243\n\t" \
    "s_waitcnt vmcnt(6) lgkmcnt(0)\n\t" \
    "s_barrier \n\t" \
    "ds_read_b64_tr_b16 v[236:237], v244 offset:0\n\t" \
    "ds_read_b64_tr_b16 v[238:239], v244 offset:1536\n\t" \
    "ds_read_b64_tr_b16 v[164:165], v244 offset:32\n\t" \
    "ds_read_b64_tr_b16 v[166:167], v244 offset:1568\n\t" \
    "ds_read_b64_tr_b16 v[172:173], v244 offset:64\n\t" \
    "ds_read_b64_tr_b16 v[174:175], v244 offset:1600\n\t" \
    "v_add_u32 v244, 0xc000, v243\n\t" \
    "s_waitcnt vmcnt(3) lgkmcnt(0)\n\t" \
    "s_barrier \n\t" \
    "ds_read_b64_tr_b16 v[176:177], v244 offset:0\n\t" \
    "ds_read_b64_tr_b16 v[178:179], v244 offset:1536\n\t" \
    "ds_read_b64_tr_b16 v[184:185], v244 offset:32\n\t" \
    "ds_read_b64_tr_b16 v[186:187], v244 offset:1568\n\t" \
    "ds_read_b64_tr_b16 v[192:193], v244 offset:64\n\t" \
    "ds_read_b64_tr_b16 v[194:195], v244 offset:1600\n\t" \
    "s_mov_b32 s43, 0\n\t" \
    "v_cmp_eq_u32 vcc, 0, v252\n\t" \
    "s_cbranch_vccnz .Lv21_%=\n\t" \
    "s_movk_i32 s45, 0x2000\n\t" \
    "s_mov_b32 m0, s45\n\t" \
    "s_nop 0\n\t" \
    "buffer_load_dwordx4 v253, s[60:63], s42 offen offset:0 sc1 lds\n\t" \
    "buffer_load_dwordx4 v253, s[60:63], s42 offen offset:1024 sc1 lds\n\t" \
    "buffer_load_dwordx4 v253, s[60:63], s42 offen offset:2048 sc1 lds\n\t" \
    "buffer_load_dwordx4 v253, s[60:63], s42 offen offset:3072 sc1 lds\n\t" \
    "s_add_u32 s44, s42, 0x1000\n\t" \
    "s_movk_i32 s45, 0x7000\n\t" \
    "s_mov_b32 m0, s45\n\t" \
    "s_nop 0\n\t" \
    "buffer_load_dwordx4 v253, s[60:63], s44 offen offset:0 sc1 lds\n\t" \
    "buffer_load_dwordx4 v253, s[60:63], s44 offen offset:1024 sc1 lds\n\t" \
    "buffer_load_dwordx4 v253, s[60:63], s44 offen offset:2048 sc1 lds\n\t" \
    "buffer_load_dwordx4 v253, s[60:63], s44 offen offset:3072 sc1 lds\n\t" \
    "s_add_u32 s44, s42, 0x8800\n\t" \
    "s_mov_b32 s45, 0x4800\n\t" \
    "s_mov_b32 m0, s45\n\t" \
    "s_nop 0\n\t" \
    "buffer_load_dwordx4 v253, s[60:63], s44 offen offset:0 sc1 lds\n\t" \
    "s_add_u32 s44, s44, 0x1000\n\t" \
    "s_mov_b32 s45, 0x9800\n\t" \
    "s_mov_b32 m0, s45\n\t" \
    "s_nop 0\n\t" \
    "buffer_load_dwordx4 v253, s[60:63], s44 offen offset:0 sc1 lds\n\t" \
    "s_mov_b32 s43, 1\n\t" \
    ".Lv21_%=:\n\t" \
    "v_add_u32 v244, 0xf000, v243\n\t" \
    "s_waitcnt vmcnt(0) lgkmcnt(0)\n\t" \
    "s_barrier \n\t" \
    "ds_read_b64_tr_b16 v[180:181], v244 offset:0\n\t" \
    "ds_read_b64_tr_b16 v[182:183], v244 offset:1536\n\t" \
    "ds_read_b64_tr_b16 v[188:189], v244 offset:32\n\t" \
    "ds_read_b64_tr_b16 v[190:191], v244 offset:1568\n\t" \
    "ds_read_b64_tr_b16 v[196:197], v244 offset:64\n\t" \
    "ds_read_b64_tr_b16 v[198:199], v244 offset:1600\n\t" \
    "s_waitcnt lgkmcnt(0)\n\t" \
    "s_barrier \n\t" \
    "ds_write_b128 v254, v[200:203] offset:51200\n\t" \
    "ds_write_b128 v254, v[204:207] offset:52224\n\t" \
    "ds_write_b128 v254, v[208:211] offset:53248\n\t" \
    "ds_write_b128 v254, v[212:215] offset:54272\n\t" \
    "ds_write_b128 v254, v[216:219] offset:55296\n\t" \
    "ds_write_b128 v254, v[220:223] offset:56320\n\t" \
    "ds_write_b128 v254, v[224:227] offset:57344\n\t" \
    "ds_write_b128 v254, v[228:231] offset:58368\n\t" \
    "ds_write_b128 v254, v[232:235] offset:59392\n\t" \
    "ds_write_b128 v254, v[236:239] offset:60416\n\t" \
    "s_cmp_lg_u32 s43, 0\n\t" \
    "s_cbranch_scc1 .Lv22_%=\n\t" \
    "s_mov_b32 s44, 0\n\t" \
    ".Lv23_%=:\n\t" \
    "v_cmp_ge_u32 vcc, s44, v252\n\t" \
    "s_cbranch_vccz .Lv24_%=\n\t" \
    "s_cmp_eq_u32 s41, 0\n\t" \
    "s_cbranch_scc1 .Lv24_%=\n\t" \
    "s_sub_u32 s41, s41, 1\n\t" \
    "s_sleep 8\n\t" \
    "v_and_b32 v252, 0xf0, v253\n\t" \
    "v_lshlrev_b32 v252, 3, v252\n\t" \
    "buffer_load_dword v252, v252, s[60:63], %[fl] offen sc1\n\t" \
    "s_waitcnt vmcnt(0)\n\t" \
    "s_branch .Lv23_%=\n\t" \
    ".Lv24_%=:\n\t" \
    "s_movk_i32 s45, 0x2000\n\t" \
    "s_mov_b32 m0, s45\n\t" \
    "s_nop 0\n\t" \
    "buffer_load_dwordx4 v253, s[60:63], s42 offen offset:0 sc1 lds\n\t" \
    "buffer_load_dwordx4 v253, s[60:63], s42 offen offset:1024 sc1 lds\n\t" \
    "buffer_load_dwordx4 v253, s[60:63], s42 offen offset:2048 sc1 lds\n\t" \
    "buffer_load_dwordx4 v253, s[60:63], s42 offen offset:3072 sc1 lds\n\t" \
    "s_add_u32 s44, s42, 0x1000\n\t" \
    "s_movk_i32 s45, 0x7000\n\t" \
    "s_mov_b32 m0, s45\n\t" \
    "s_nop 0\n\t" \
    "buffer_load_dwordx4 v253, s[60:63], s44 offen offset:0 sc1 lds\n\t" \
    "buffer_load_dwordx4 v253, s[60:63], s44 offen offset:1024 sc1 lds\n\t" \
    "buffer_load_dwordx4 v253, s[60:63], s44 offen offset:2048 sc1 lds\n\t" \
    "buffer_load_dwordx4 v253, s[60:63], s44 offen offset:3072 sc1 lds\n\t" \
    "s_add_u32 s44, s42, 0x8800\n\t" \
    "s_mov_b32 s45, 0x4800\n\t" \
    "s_mov_b32 m0, s45\n\t" \
    "s_nop 0\n\t" \
    "buffer_load_dwordx4 v253, s[60:63], s44 offen offset:0 sc1 lds\n\t" \
    "s_add_u32 s44, s44, 0x1000\n\t" \
    "s_mov_b32 s45, 0x9800\n\t" \
    "s_mov_b32 m0, s45\n\t" \
    "s_nop 0\n\t" \
    "buffer_load_dwordx4 v253, s[60:63], s44 offen offset:0 sc1 lds\n\t" \
    ".Lv22_%=:\n\t" \
    "s_waitcnt vmcnt(0) lgkmcnt(0)\n\t" \
    "s_barrier \n\t" \
    "ds_read_b128 v[224:227], v253 offset:0\n\t" \
    "ds_read_b128 v[244:247], v254 offset:53248\n\t" \
    "ds_read_b128 v[228:231], v253 offset:1024\n\t" \
    "ds_read_b128 v[232:235], v253 offset:20480\n\t" \
    "ds_read_b128 v[236:239], v253 offset:21504\n\t" \
    "ds_read_b128 v[248:251], v254 offset:54272\n\t" \
    ".Lv25_%=:\n\t" \
    LST7 \
    "s_waitcnt lgkmcnt(5)\n\t" \
    "v_mfma_f32_16x16x32_bf16 v[204:207], v[40:43], v[224:227], 0\n\t" \
    "s_waitcnt lgkmcnt(3)\n\t" \
    "v_mfma_f32_16x16x32_bf16 v[204:207], v[44:47], v[228:231], v[204:207]\n\t" \
    "ds_read_b128 v[240:243], v253 offset:2048\n\t" \
    "v_mfma_f32_16x16x32_bf16 v[208:211], v[48:51], v[224:227], 0\n\t" \
    "v_mfma_f32_16x16x32_bf16 v[208:211], v[52:55], v[228:231], v[208:211]\n\t" \
    "v_mfma_f32_16x16x32_bf16 v[200:203], v[244:247], v[224:227], 0\n\t" \
    "s_waitcnt lgkmcnt(1)\n\t" \
    "v_mfma_f32_16x16x32_bf16 v[200:203], v[248:251], v[228:231], v[200:203]\n\t" \
    "v_mfma_f32_16x16x32_bf16 v[216:219], v[40:43], v[232:235], 0\n\t" \
    "ds_read_b128 v[224:227], v253 offset:3072\n\t" \
    "v_mfma_f32_16x16x32_bf16 v[216:219], v[44:47], v[236:239], v[216:219]\n\t" \
    "ds_read_b128 v[228:231], v253 offset:22528\n\t" \
    "v_mfma_f32_16x16x32_bf16 v[220:223], v[48:51], v[232:235], 0\n\t" \
    "v_mfma_f32_16x16x32_bf16 v[220:223], v[52:55], v[236:239], v[220:223]\n\t" \
    "v_mfma_f32_16x16x32_bf16 v[212:215], v[244:247], v[232:235], 0\n\t" \
    "ds_read_b128 v[244:247], v254 offset:55296\n\t" \
    "v_mfma_f32_16x16x32_bf16 v[212:215], v[248:251], v[236:239], v[212:215]\n\t" \
    "ds_read_b128 v[248:251], v254 offset:56320\n\t" \
    "s_waitcnt lgkmcnt(4)\n\t" \
    "v_mfma_f32_16x16x32_bf16 v[204:207], v[64:67], v[240:243], v[204:207]\n\t" \
    "ds_read_b128 v[232:235], v253 offset:23552\n\t" \
    "s_waitcnt lgkmcnt(4)\n\t" \
    "v_mfma_f32_16x16x32_bf16 v[204:207], v[68:71], v[224:227], v[204:207]\n\t" \
    "ds_read_b128 v[236:239], v253 offset:4096\n\t" \
    "v_mfma_f32_16x16x32_bf16 v[208:211], v[72:75], v[240:243], v[208:211]\n\t" \
    "v_mfma_f32_16x16x32_bf16 v[208:211], v[76:79], v[224:227], v[208:211]\n\t" \
    "v_mfma_f32_16x16x32_bf16 v[200:203], v[56:59], v[240:243], v[200:203]\n\t" \
    "v_mfma_f32_16x16x32_bf16 v[200:203], v[60:63], v[224:227], v[200:203]\n\t" \
    "s_waitcnt lgkmcnt(4)\n\t" \
    "v_mfma_f32_16x16x32_bf16 v[216:219], v[64:67], v[228:231], v[216:219]\n\t" \
    "ds_read_b128 v[240:243], v253 offset:5120\n\t" \
    "s_waitcnt lgkmcnt(2)\n\t" \
    "v_mfma_f32_16x16x32_bf16 v[216:219], v[68:71], v[232:235], v[216:219]\n\t" \
    "ds_read_b128 v[224:227], v253 offset:24576\n\t" \
    "v_mfma_f32_16x16x32_bf16 v[220:223], v[72:75], v[228:231], v[220:223]\n\t" \
    "v_mfma_f32_16x16x32_bf16 v[220:223], v[76:79], v[232:235], v[220:223]\n\t" \
    "v_mfma_f32_16x16x32_bf16 v[212:215], v[56:59], v[228:231], v[212:215]\n\t" \
    "v_mfma_f32_16x16x32_bf16 v[212:215], v[60:63], v[232:235], v[212:215]\n\t" \
    "s_waitcnt lgkmcnt(2)\n\t" \
    "v_mfma_f32_16x16x32_bf16 v[204:207], v[80:83], v[236:239], v[204:207]\n\t" \
    "ds_read_b128 v[228:231], v253 offset:25600\n\t" \
    "s_add_u32 s44, s40, 1\n\t" \
    "s_waitcnt lgkmcnt(2)\n\t" \
    "v_mfma_f32_16x16x32_bf16 v[204:207], v[84:87], v[240:243], v[204:207]\n\t" \
    "ds_read_b128 v[232:235], v253 offset:6144\n\t" \
    "s_and_b32 s45, s44, 15\n\t" \
    "v_mfma_f32_16x16x32_bf16 v[208:211], v[88:91], v[236:239], v[208:211]\n\t" \
    "s_cmp_eq_u32 s45, 0\n\t" \
    "v_mfma_f32_16x16x32_bf16 v[208:211], v[92:95], v[240:243], v[208:211]\n\t" \
    "s_cselect_b32 s45, %[cj], %[cs]\n\t" \
    "v_mfma_f32_16x16x32_bf16 v[200:203], v[244:247], v[236:239], v[200:203]\n\t" \
    "s_cmp_eq_u32 s44, 64\n\t" \
    "v_mfma_f32_16x16x32_bf16 v[200:203], v[248:251], v[240:243], v[200:203]\n\t" \
    "s_cselect_b32 s45, 0, s45\n\t" \
    "s_waitcnt lgkmcnt(2)\n\t" \
    "v_mfma_f32_16x16x32_bf16 v[216:219], v[80:83], v[224:227], v[216:219]\n\t" \
    "ds_read_b128 v[236:239], v253 offset:7168\n\t" \
    "s_add_u32 s42, s42, s45\n\t" \
    "s_waitcnt lgkmcnt(2)\n\t" \
    "v_mfma_f32_16x16x32_bf16 v[216:219], v[84:87], v[228:231], v[216:219]\n\t" \
    "ds_read_b128 v[240:243], v253 offset:26624\n\t" \
    "v_mfma_f32_16x16x32_bf16 v[220:223], v[88:91], v[224:227], v[220:223]\n\t" \
    "v_mfma_f32_16x16x32_bf16 v[220:223], v[92:95], v[228:231], v[220:223]\n\t" \
    "v_mfma_f32_16x16x32_bf16 v[212:215], v[244:247], v[224:227], v[212:215]\n\t" \
    "ds_read_b128 v[244:247], v254 offset:51200\n\t" \
    "v_mfma_f32_16x16x32_bf16 v[212:215], v[248:251], v[228:231], v[212:215]\n\t" \
    "ds_read_b128 v[248:251], v254 offset:52224\n\t" \
    "s_waitcnt lgkmcnt(4)\n\t" \
    "v_mfma_f32_16x16x32_bf16 v[204:207], v[104:107], v[232:235], v[204:207]\n\t" \
    "ds_read_b128 v[224:227], v253 offset:27648\n\t" \
    "s_waitcnt lgkmcnt(4)\n\t" \
    "v_mfma_f32_16x16x32_bf16 v[204:207], v[108:111], v[236:239], v[204:207]\n\t" \
    "ds_read_b128 v[228:231], v253 offset:8192\n\t" \
    "v_mfma_f32_16x16x32_bf16 v[208:211], v[112:115], v[232:235], v[208:211]\n\t" \
    "v_mfma_f32_16x16x32_bf16 v[208:211], v[116:119], v[236:239], v[208:211]\n\t" \
    "v_mfma_f32_16x16x32_bf16 v[200:203], v[96:99], v[232:235], v[200:203]\n\t" \
    "v_mfma_f32_16x16x32_bf16 v[200:203], v[100:103], v[236:239], v[200:203]\n\t" \
    "s_waitcnt lgkmcnt(4)\n\t" \
    "v_mfma_f32_16x16x32_bf16 v[216:219], v[104:107], v[240:243], v[216:219]\n\t" \
    "ds_read_b128 v[232:235], v253 offset:9216\n\t" \
    "s_waitcnt lgkmcnt(2)\n\t" \
    "v_mfma_f32_16x16x32_bf16 v[216:219], v[108:111], v[224:227], v[216:219]\n\t" \
    "ds_read_b128 v[236:239], v253 offset:28672\n\t" \
    "v_mfma_f32_16x16x32_bf16 v[220:223], v[112:115], v[240:243], v[220:223]\n\t" \
    "v_mfma_f32_16x16x32_bf16 v[220:223], v[116:119], v[224:227], v[220:223]\n\t" \
    "v_mfma_f32_16x16x32_bf16 v[212:215], v[96:99], v[240:243], v[212:215]\n\t" \
    "v_mfma_f32_16x16x32_bf16 v[212:215], v[100:103], v[224:227], v[212:215]\n\t" \
    "s_waitcnt lgkmcnt(2)\n\t" \
    "v_mfma_f32_16x16x32_bf16 v[204:207], v[0:3], v[228:231], v[204:207]\n\t" \
    "ds_read_b128 v[240:243], v253 offset:29696\n\t" \
    "s_waitcnt lgkmcnt(2)\n\t" \
    "v_mfma_f32_16x16x32_bf16 v[204:207], v[4:7], v[232:235], v[204:207]\n\t" \
    "ds_read_b128 v[224:227], v253 offset:10240\n\t" \
    "v_mfma_f32_16x16x32_bf16 v[208:211], v[8:11], v[228:231], v[208:211]\n\t" \
    "v_mfma_f32_16x16x32_bf16 v[208:211], v[12:15], v[232:235], v[208:211]\n\t" \
    "v_mfma_f32_16x16x32_bf16 v[200:203], v[244:247], v[228:231], v[200:203]\n\t" \
    "v_mfma_f32_16x16x32_bf16 v[200:203], v[248:251], v[232:235], v[200:203]\n\t" \
    "s_waitcnt lgkmcnt(2)\n\t" \
    "v_mfma_f32_16x16x32_bf16 v[216:219], v[0:3], v[236:239], v[216:219]\n\t" \
    "ds_read_b128 v[228:231], v253 offset:11264\n\t" \
    "s_waitcnt lgkmcnt(2)\n\t" \
    "v_mfma_f32_16x16x32_bf16 v[216:219], v[4:7], v[240:243], v[216:219]\n\t" \
    "ds_read_b128 v[232:235], v253 offset:30720\n\t" \
    "v_mfma_f32_16x16x32_bf16 v[220:223], v[8:11], v[236:239], v[220:223]\n\t" \
    "v_mfma_f32_16x16x32_bf16 v[220:223], v[12:15], v[240:243], v[220:223]\n\t" \
    "v_mfma_f32_16x16x32_bf16 v[212:215], v[244:247], v[236:239], v[212:215]\n\t" \
    "ds_read_b128 v[244:247], v254 offset:57344\n\t" \
    "v_mfma_f32_16x16x32_bf16 v[212:215], v[248:251], v[240:243], v[212:215]\n\t" \
    "ds_read_b128 v[248:251], v254 offset:58368\n\t" \
    "s_waitcnt lgkmcnt(4)\n\t" \
    "v_mfma_f32_16x16x32_bf16 v[204:207], v[120:123], v[224:227], v[204:207]\n\t" \
    "ds_read_b128 v[236:239], v253 offset:31744\n\t" \
    "s_and_b32 s44, s40, 3\n\t" \
    "s_waitcnt lgkmcnt(4)\n\t" \
    "v_mfma_f32_16x16x32_bf16 v[204:207], v[124:127], v[228:231], v[204:207]\n\t" \
    "ds_read_b128 v[240:243], v253 offset:12288\n\t" \
    "s_lshl_b32 s44, s44, 17\n\t" \
    "v_mfma_f32_16x16x32_bf16 v[208:211], v[128:131], v[224:227], v[208:211]\n\t" \
    "s_add_u32 s44, s44, %[mb]\n\t" \
    "v_mfma_f32_16x16x32_bf16 v[208:211], v[132:135], v[228:231], v[208:211]\n\t" \
    "s_add_u32 s46, s44, 0x1000\n\t" \
    "s_waitcnt lgkmcnt(3)\n\t" \
    "v_mfma_f32_16x16x32_bf16 v[200:203], v[244:247], v[224:227], v[200:203]\n\t" \
    "s_add_u32 s47, s44, 0x1c00\n\t" \
    "s_waitcnt lgkmcnt(2)\n\t" \
    "v_mfma_f32_16x16x32_bf16 v[200:203], v[248:251], v[228:231], v[200:203]\n\t" \
    "v_mfma_f32_16x16x32_bf16 v[216:219], v[120:123], v[232:235], v[216:219]\n\t" \
    "ds_read_b128 v[224:227], v253 offset:13312\n\t" \
    "s_waitcnt lgkmcnt(2)\n\t" \
    "v_mfma_f32_16x16x32_bf16 v[216:219], v[124:127], v[236:239], v[216:219]\n\t" \
    "ds_read_b128 v[228:231], v253 offset:32768\n\t" \
    "v_mfma_f32_16x16x32_bf16 v[220:223], v[128:131], v[232:235], v[220:223]\n\t" \
    "v_mfma_f32_16x16x32_bf16 v[220:223], v[132:135], v[236:239], v[220:223]\n\t" \
    "v_mfma_f32_16x16x32_bf16 v[212:215], v[244:247], v[232:235], v[212:215]\n\t" \
    "ds_read_b128 v[244:247], v254 offset:59392\n\t" \
    "v_mfma_f32_16x16x32_bf16 v[212:215], v[248:251], v[236:239], v[212:215]\n\t" \
    "ds_read_b128 v[248:251], v254 offset:60416\n\t" \
    "s_waitcnt lgkmcnt(4)\n\t" \
    "v_mfma_f32_16x16x32_bf16 v[204:207], v[144:147], v[240:243], v[204:207]\n\t" \
    "ds_read_b128 v[232:235], v253 offset:33792\n\t" \
    "s_waitcnt lgkmcnt(4)\n\t" \
    "v_mfma_f32_16x16x32_bf16 v[204:207], v[148:151], v[224:227], v[204:207]\n\t" \
    "ds_read_b128 v[236:239], v253 offset:16384\n\t" \
    "v_mfma_f32_16x16x32_bf16 v[208:211], v[152:155], v[240:243], v[208:211]\n\t" \
    "v_mfma_f32_16x16x32_bf16 v[208:211], v[156:159], v[224:227], v[208:211]\n\t" \
    "v_mfma_f32_16x16x32_bf16 v[200:203], v[136:139], v[240:243], v[200:203]\n\t" \
    "v_mfma_f32_16x16x32_bf16 v[200:203], v[140:143], v[224:227], v[200:203]\n\t" \
    "s_waitcnt lgkmcnt(4)\n\t" \
    "v_mfma_f32_16x16x32_bf16 v[216:219], v[144:147], v[228:231], v[216:219]\n\t" \
    "ds_read_b128 v[240:243], v253 offset:17408\n\t" \
    "s_waitcnt lgkmcnt(2)\n\t" \
    "v_mfma_f32_16x16x32_bf16 v[216:219], v[148:151], v[232:235], v[216:219]\n\t" \
    "ds_read_b128 v[224:227], v253 offset:36864\n\t" \
    "v_mfma_f32_16x16x32_bf16 v[220:223], v[152:155], v[228:231], v[220:223]\n\t" \
    "v_mfma_f32_16x16x32_bf16 v[220:223], v[156:159], v[232:235], v[220:223]\n\t" \
    "v_mfma_f32_16x16x32_bf16 v[212:215], v[136:139], v[228:231], v[212:215]\n\t" \
    "v_mfma_f32_16x16x32_bf16 v[212:215], v[140:143], v[232:235], v[212:215]\n\t" \
    "v_and_b32 v252, 0xf0, v253\n\t" \
    "v_lshlrev_b32 v252, 3, v252\n\t" \
    "buffer_load_dword v252, v252, s[60:63], %[fl] offen sc1\n\t" \
    "s_waitcnt lgkmcnt(2)\n\t" \
    "v_mfma_f32_16x16x32_bf16 v[204:207], v[160:163], v[236:239], v[204:207]\n\t" \
    "ds_read_b128 v[228:231], v253 offset:37888\n\t" \
    "s_waitcnt lgkmcnt(2)\n\t" \
    "v_mfma_f32_16x16x32_bf16 v[204:207], v[164:167], v[240:243], v[204:207]\n\t" \
    "ds_read_b128 v[232:235], v253 offset:14336\n\t" \
    "v_mfma_f32_16x16x32_bf16 v[208:211], v[168:171], v[236:239], v[208:211]\n\t" \
    "v_mfma_f32_16x16x32_bf16 v[208:211], v[172:175], v[240:243], v[208:211]\n\t" \
    "v_mfma_f32_16x16x32_bf16 v[200:203], v[244:247], v[236:239], v[200:203]\n\t" \
    "v_mfma_f32_16x16x32_bf16 v[200:203], v[248:251], v[240:243], v[200:203]\n\t" \
    "s_waitcnt lgkmcnt(2)\n\t" \
    "v_mfma_f32_16x16x32_bf16 v[216:219], v[160:163], v[224:227], v[216:219]\n\t" \
    "ds_read_b128 v[236:239], v253 offset:15360\n\t" \
    "s_waitcnt lgkmcnt(2)\n\t" \
    "v_mfma_f32_16x16x32_bf16 v[216:219], v[164:167], v[228:231], v[216:219]\n\t" \
    "ds_read_b128 v[240:243], v253 offset:18432\n\t" \
    "v_mfma_f32_16x16x32_bf16 v[220:223], v[168:171], v[224:227], v[220:223]\n\t" \
    "v_mfma_f32_16x16x32_bf16 v[220:223], v[172:175], v[228:231], v[220:223]\n\t" \
    "v_mfma_f32_16x16x32_bf16 v[212:215], v[244:247], v[224:227], v[212:215]\n\t" \
    "v_mfma_f32_16x16x32_bf16 v[212:215], v[248:251], v[228:231], v[212:215]\n\t" \
    "s_waitcnt lgkmcnt(2)\n\t" \
    "v_mfma_f32_16x16x32_bf16 v[204:207], v[24:27], v[232:235], v[204:207]\n\t" \
    "ds_read_b128 v[224:227], v253 offset:19456\n\t" \
    "v_mov_b64 v[244:245], 0\n\t" \
    "s_waitcnt lgkmcnt(2)\n\t" \
    "v_mfma_f32_16x16x32_bf16 v[204:207], v[28:31], v[236:239], v[204:207]\n\t" \
    "ds_read_b128 v[228:231], v253 offset:34816\n\t" \
    "v_mov_b64 v[246:247], 0\n\t" \
    "v_mfma_f32_16x16x32_bf16 v[208:211], v[32:35], v[232:235], v[208:211]\n\t" \
    "v_mov_b64 v[248:249], 0\n\t" \
    "v_mfma_f32_16x16x32_bf16 v[208:211], v[36:39], v[236:239], v[208:211]\n\t" \
    "v_mov_b64 v[250:251], 0\n\t" \
    "v_mfma_f32_16x16x32_bf16 v[200:203], v[16:19], v[232:235], v[200:203]\n\t" \
    "v_mfma_f32_16x16x32_bf16 v[200:203], v[20:23], v[236:239], v[200:203]\n\t" \
    "s_waitcnt lgkmcnt(2)\n\t" \
    "v_mfma_f32_16x16x32_bf16 v[204:207], v[184:187], v[240:243], v[204:207]\n\t" \
    "ds_read_b128 v[232:235], v253 offset:35840\n\t" \
    "s_waitcnt lgkmcnt(2)\n\t" \
    "v_mfma_f32_16x16x32_bf16 v[204:207], v[188:191], v[224:227], v[204:207]\n\t" \
    "ds_read_b128 v[236:239], v253 offset:38912\n\t" \
    "v_mfma_f32_16x16x32_bf16 v[208:211], v[192:195], v[240:243], v[208:211]\n\t" \
    "v_mfma_f32_16x16x32_bf16 v[208:211], v[196:199], v[224:227], v[208:211]\n\t" \
    "v_mfma_f32_16x16x32_bf16 v[200:203], v[176:179], v[240:243], v[200:203]\n\t" \
    "v_mfma_f32_16x16x32_bf16 v[200:203], v[180:183], v[224:227], v[200:203]\n\t" \
    "ds_read_b128 v[240:243], v253 offset:39936\n\t" \
    "s_waitcnt lgkmcnt(3)\n\t" \
    "v_mfma_f32_16x16x32_bf16 v[216:219], v[24:27], v[228:231], v[216:219]\n\t" \
    "s_waitcnt lgkmcnt(2)\n\t" \
    "v_mfma_f32_16x16x32_bf16 v[216:219], v[28:31], v[232:235], v[216:219]\n\t" \
    "v_cvt_pk_bf16_f32 v204, v204, v205\n\t" \
    "v_cvt_pk_bf16_f32 v205, v206, v207\n\t" \
    "s_waitcnt lgkmcnt(1)\n\t" \
    "v_mfma_f32_16x16x32_bf16 v[216:219], v[184:187], v[236:239], v[216:219]\n\t" \
    "ds_write_b64 v254, v[204:205] offset:7176\n\t" \
    "v_cvt_pk_bf16_f32 v208, v208, v209\n\t" \
    "s_waitcnt lgkmcnt(1)\n\t" \
    "v_mfma_f32_16x16x32_bf16 v[216:219], v[188:191], v[240:243], v[216:219]\n\t" \
    "v_cvt_pk_bf16_f32 v209, v210, v211\n\t" \
    "ds_write_b64 v254, v[208:209] offset:8200\n\t" \
    "v_mfma_f32_16x16x32_bf16 v[220:223], v[32:35], v[228:231], v[220:223]\n\t" \
    "v_cvt_pk_bf16_f32 v200, v200, v201\n\t" \
    "v_cvt_pk_bf16_f32 v201, v202, v203\n\t" \
    "v_mfma_f32_16x16x32_bf16 v[220:223], v[36:39], v[232:235], v[220:223]\n\t" \
    "ds_write_b64 v254, v[200:201] offset:6152\n\t" \
    "ds_read_b128 v[200:203], v254 offset:51200\n\t" \
    "v_mfma_f32_16x16x32_bf16 v[220:223], v[192:195], v[236:239], v[220:223]\n\t" \
    "ds_read_b128 v[204:207], v254 offset:52224\n\t" \
    "v_mov_b64 v[208:209], 0\n\t" \
    "v_mfma_f32_16x16x32_bf16 v[220:223], v[196:199], v[240:243], v[220:223]\n\t" \
    "v_mov_b64 v[210:211], 0\n\t" \
    "v_cvt_pk_bf16_f32 v216, v216, v217\n\t" \
    "v_mfma_f32_16x16x32_bf16 v[212:215], v[16:19], v[228:231], v[212:215]\n\t" \
    "v_cvt_pk_bf16_f32 v217, v218, v219\n\t" \
    "ds_write_b64 v254, v[216:217] offset:13320\n\t" \
    "v_mfma_f32_16x16x32_bf16 v[212:215], v[20:23], v[232:235], v[212:215]\n\t" \
    "v_mfma_f32_16x16x32_bf16 v[212:215], v[176:179], v[236:239], v[212:215]\n\t" \
    "v_cvt_pk_bf16_f32 v220, v220, v221\n\t" \
    "v_cvt_pk_bf16_f32 v221, v222, v223\n\t" \
    "v_mfma_f32_16x16x32_bf16 v[212:215], v[180:183], v[240:243], v[212:215]\n\t" \
    "ds_write_b64 v254, v[220:221] offset:14344\n\t" \
    "ds_read_b128 v[216:219], v254 offset:53248\n\t" \
    "ds_read_b128 v[220:223], v254 offset:54272\n\t" \
    "s_nop 4\n\t" \
    "v_cvt_pk_bf16_f32 v212, v212, v213\n\t" \
    "v_cvt_pk_bf16_f32 v213, v214, v215\n\t" \
    "ds_write_b64 v254, v[212:213] offset:12296\n\t" \
    LST6 \
    "s_waitcnt vmcnt(0) lgkmcnt(0)\n\t" \
    "s_barrier \n\t" \
    "s_setprio 2\n\t" \
    "s_add_u32 s44, s40, 1\n\t" \
    "s_min_u32 s44, s44, 63\n\t" \
    ".Lv26_%=:\n\t" \
    "v_cmp_ge_u32 vcc, s44, v252\n\t" \
    "s_cbranch_vccz .Lv27_%=\n\t" \
    "s_cmp_eq_u32 s41, 0\n\t" \
    "s_cbranch_scc1 .Lv27_%=\n\t" \
    "s_sub_u32 s41, s41, 1\n\t" \
    "s_sleep 2\n\t" \
    "v_and_b32 v252, 0xf0, v253\n\t" \
    "v_lshlrev_b32 v252, 3, v252\n\t" \
    "buffer_load_dword v252, v252, s[60:63], %[fl] offen sc1\n\t" \
    "s_waitcnt vmcnt(0)\n\t" \
    "s_branch .Lv26_%=\n\t" \
    ".Lv27_%=:\n\t" \
    "s_movk_i32 s45, 0x2000\n\t" \
    "s_mov_b32 m0, s45\n\t" \
    "s_nop 0\n\t" \
    "buffer_load_dwordx4 v253, s[60:63], s42 offen offset:0 sc1 lds\n\t" \
    "buffer_load_dwordx4 v253, s[60:63], s42 offen offset:1024 sc1 lds\n\t" \
    "buffer_load_dwordx4 v253, s[60:63], s42 offen offset:2048 sc1 lds\n\t" \
    "buffer_load_dwordx4 v253, s[60:63], s42 offen offset:3072 sc1 lds\n\t" \
    "s_add_u32 s44, s42, 0x1000\n\t" \
    "s_movk_i32 s45, 0x7000\n\t" \
    "s_mov_b32 m0, s45\n\t" \
    "s_nop 0\n\t" \
    "buffer_load_dwordx4 v253, s[60:63], s44 offen offset:0 sc1 lds\n\t" \
    "buffer_load_dwordx4 v253, s[60:63], s44 offen offset:1024 sc1 lds\n\t" \
    "buffer_load_dwordx4 v253, s[60:63], s44 offen offset:2048 sc1 lds\n\t" \
    "buffer_load_dwordx4 v253, s[60:63], s44 offen offset:3072 sc1 lds\n\t" \
    "s_add_u32 s44, s42, 0x8800\n\t" \
    "s_mov_b32 s45, 0x4800\n\t" \
    "s_mov_b32 m0, s45\n\t" \
    "s_nop 0\n\t" \
    "buffer_load_dwordx4 v253, s[60:63], s44 offen offset:0 sc1 lds\n\t" \
    "s_add_u32 s44, s44, 0x1000\n\t" \
    "s_mov_b32 s45, 0x9800\n\t" \
    "s_mov_b32 m0, s45\n\t" \
    "s_nop 0\n\t" \
    "buffer_load_dwordx4 v253, s[60:63], s44 offen offset:0 sc1 lds\n\t" \
    LST1 \
    "s_sleep 18\n\t" \
    "buffer_load_dwordx4 v[224:227], v253, s[60:63], s46 offen sc1\n\t" \
    "buffer_load_dwordx4 v[228:231], v253, s[60:63], s47 offen sc1\n\t" \
    "s_waitcnt lgkmcnt(0)\n\t" \
    LFLUSH212 \
    "v_bfe_i32 v212, v253, 5, 1\n\t" \
    "v_and_b32 v212, 0xaaaa, v212\n\t" \
    "v_or_b32 v212, 0x4444, v212\n\t" \
    "v_and_b32 v213, 0xffff, v255\n\t" \
    "v_lshrrev_b32 v214, 16, v255\n\t" \
    ".Lv28_%=:\n\t" \
    "s_waitcnt vmcnt(1)\n\t" \
    "v_max_u32 v215, v224, v225\n\t" \
    "v_max3_u32 v215, v226, v227, v215\n\t" \
    "s_waitcnt vmcnt(0)\n\t" \
    "v_max3_u32 v215, v228, v229, v215\n\t" \
    "v_max3_u32 v215, v230, v231, v215\n\t" \
    "v_cmp_eq_u32 vcc, -1, v215\n\t" \
    "s_cbranch_vccz .Lv29_%=\n\t" \
    "v_max_u32 v215, v224, v225\n\t" \
    "v_max3_u32 v215, v226, v227, v215\n\t" \
    "v_cmp_eq_u32 vcc, -1, v215\n\t" \
    "s_cbranch_vccz .Lv210_%=\n\t" \
    "buffer_load_dwordx4 v[224:227], v253, s[60:63], s46 offen sc1\n\t" \
    ".Lv210_%=:\n\t" \
    "v_max_u32 v215, v228, v229\n\t" \
    "v_max3_u32 v215, v230, v231, v215\n\t" \
    "v_cmp_eq_u32 vcc, -1, v215\n\t" \
    "s_cbranch_vccz .Lv211_%=\n\t" \
    "buffer_load_dwordx4 v[228:231], v253, s[60:63], s47 offen sc1\n\t" \
    ".Lv211_%=:\n\t" \
    "s_cmp_eq_u32 s41, 0\n\t" \
    "s_cbranch_scc1 .Lv29_%=\n\t" \
    "s_sub_u32 s41, s41, 1\n\t" \
    "s_waitcnt vmcnt(0)\n\t" \
    "s_branch .Lv28_%=\n\t" \
    ".Lv29_%=:\n\t" \
    LST2 \
    "ds_write_b128 v253, v[224:227] offset:45056\n\t" \
    "ds_write_b128 v253, v[228:231] offset:48128\n\t" \
    "v_smfmac_f32_16x16x64_bf16 v[244:247], v[224:227], v[0:7], v212\n\t" \
    "v_smfmac_f32_16x16x64_bf16 v[248:251], v[224:227], v[8:15], v212\n\t" \
    "v_smfmac_f32_16x16x64_bf16 v[208:211], v[224:227], v[200:207], v212\n\t" \
    "s_waitcnt lgkmcnt(0)\n\t" \
    "s_barrier \n\t" \
    LST3 \
    "ds_read_b128 v[232:235], v253 offset:40960\n\t" \
    "ds_read_b128 v[236:239], v253 offset:41984\n\t" \
    "ds_read_b128 v[240:243], v253 offset:43008\n\t" \
    "ds_read_b128 v[224:227], v253 offset:44032\n\t" \
    "ds_read_b128 v[200:203], v254 offset:55296\n\t" \
    "ds_read_b128 v[204:207], v254 offset:56320\n\t" \
    "v_smfmac_f32_16x16x64_bf16 v[208:211], v[228:231], v[16:23], v212\n\t" \
    "v_smfmac_f32_16x16x64_bf16 v[244:247], v[228:231], v[24:31], v212\n\t" \
    "v_smfmac_f32_16x16x64_bf16 v[248:251], v[228:231], v[32:39], v212\n\t" \
    "ds_read_b128 v[228:231], v253 offset:46080\n\t" \
    "s_waitcnt lgkmcnt(6)\n\t" \
    "v_smfmac_f32_16x16x64_bf16 v[208:211], v[232:235], v[216:223], v212\n\t" \
    "ds_read_b128 v[216:219], v254 offset:57344\n\t" \
    "ds_read_b128 v[220:223], v254 offset:58368\n\t" \
    "v_smfmac_f32_16x16x64_bf16 v[244:247], v[232:235], v[40:47], v212\n\t" \
    "v_smfmac_f32_16x16x64_bf16 v[248:251], v[232:235], v[48:55], v212\n\t" \
    "ds_read_b128 v[232:235], v253 offset:47104\n\t" \
    "s_waitcnt lgkmcnt(8)\n\t" \
    "v_smfmac_f32_16x16x64_bf16 v[208:211], v[236:239], v[56:63], v212\n\t" \
    "v_smfmac_f32_16x16x64_bf16 v[244:247], v[236:239], v[64:71], v212\n\t" \
    "v_smfmac_f32_16x16x64_bf16 v[248:251], v[236:239], v[72:79], v212\n\t" \
    "ds_read_b128 v[236:239], v253 offset:49152\n\t" \
    "s_waitcnt lgkmcnt(5)\n\t" \
    "v_smfmac_f32_16x16x64_bf16 v[208:211], v[240:243], v[200:207], v212\n\t" \
    "ds_read_b128 v[200:203], v254 offset:59392\n\t" \
    "ds_read_b128 v[204:207], v254 offset:60416\n\t" \
    "v_smfmac_f32_16x16x64_bf16 v[244:247], v[240:243], v[80:87], v212\n\t" \
    "v_smfmac_f32_16x16x64_bf16 v[248:251], v[240:243], v[88:95], v212\n\t" \
    "ds_read_b128 v[240:243], v253 offset:50176\n\t" \
    "v_smfmac_f32_16x16x64_bf16 v[208:211], v[224:227], v[96:103], v212\n\t" \
    "v_smfmac_f32_16x16x64_bf16 v[244:247], v[224:227], v[104:111], v212\n\t" \
    "v_smfmac_f32_16x16x64_bf16 v[248:251], v[224:227], v[112:119], v212\n\t" \
    "s_waitcnt lgkmcnt(7)\n\t" \
    "v_smfmac_f32_16x16x64_bf16 v[244:247], v[228:231], v[120:127], v212\n\t" \
    "s_waitcnt lgkmcnt(4)\n\t" \
    "v_smfmac_f32_16x16x64_bf16 v[244:247], v[232:235], v[144:151], v212\n\t" \
    "s_waitcnt lgkmcnt(3)\n\t" \
    "v_smfmac_f32_16x16x64_bf16 v[244:247], v[236:239], v[160:167], v212\n\t" \
    "s_waitcnt lgkmcnt(0)\n\t" \
    "v_smfmac_f32_16x16x64_bf16 v[244:247], v[240:243], v[184:191], v212\n\t" \
    "v_smfmac_f32_16x16x64_bf16 v[248:251], v[228:231], v[128:135], v212\n\t" \
    "v_smfmac_f32_16x16x64_bf16 v[248:251], v[232:235], v[152:159], v212\n\t" \
    "v_smfmac_f32_16x16x64_bf16 v[248:251], v[236:239], v[168:175], v212\n\t" \
    "v_add_f32 v244, v244, v246\n\t" \
    "v_add_f32 v245, v245, v247\n\t" \
    "v_cvt_pk_bf16_f32 v244, v244, v245\n\t" \
    "v_smfmac_f32_16x16x64_bf16 v[248:251], v[240:243], v[192:199], v212\n\t" \
    "ds_write_b16 v213, v244 offset:512\n\t" \
    "ds_write_b16_d16_hi v213, v244 offset:528\n\t" \
    "v_smfmac_f32_16x16x64_bf16 v[208:211], v[228:231], v[216:223], v212\n\t" \
    "v_smfmac_f32_16x16x64_bf16 v[208:211], v[232:235], v[136:143], v212\n\t" \
    "v_smfmac_f32_16x16x64_bf16 v[208:211], v[236:239], v[200:207], v212\n\t" \
    "v_add_f32 v248, v248, v250\n\t" \
    "v_add_f32 v249, v249, v251\n\t" \
    "v_cvt_pk_bf16_f32 v248, v248, v249\n\t" \
    "v_smfmac_f32_16x16x64_bf16 v[208:211], v[240:243], v[176:183], v212\n\t" \
    "ds_write_b16 v213, v248 offset:1024\n\t" \
    "ds_write_b16_d16_hi v213, v248 offset:1040\n\t" \
    "s_nop 5\n\t" \
    "v_add_f32 v208, v208, v210\n\t" \
    "v_add_f32 v209, v209, v211\n\t" \
    "v_cvt_pk_bf16_f32 v208, v208, v209\n\t" \
    "ds_write_b16 v213, v208 offset:0\n\t" \
    "ds_write_b16_d16_hi v213, v208 offset:16\n\t" \
    "s_setprio 0\n\t" \
    "s_waitcnt lgkmcnt(0)\n\t" \
    "s_barrier \n\t" \
    LST4 \
    "ds_read_b128 v[224:227], v253 offset:0\n\t" \
    "ds_read_b128 v[244:247], v254 offset:53248\n\t" \
    "ds_read_b128 v[228:231], v253 offset:1024\n\t" \
    "ds_read_b128 v[232:235], v253 offset:20480\n\t" \
    "ds_read_b128 v[236:239], v253 offset:21504\n\t" \
    "ds_read_b128 v[248:251], v254 offset:54272\n\t" \
    "s_add_u32 s40, s40, 1\n\t" \
    "s_cmp_lg_u32 s40, 64\n\t" \
    "s_cbranch_scc1 .Lv25_%=\n\t" \
    "s_branch .Lend%=\n\t" \
    ".Lv1_%=:\n\t" \
    "s_mov_b32 s42, %[xs]\n\t" \
    "s_add_u32 m0, %[wm], 0xc000\n\t" \
    "s_mov_b32 s44, 0x0\n\t" \
    "buffer_load_dwordx4 v240, %[wr], s44 offen offset:0 nt lds\n\t" \
    "buffer_load_dwordx4 v241, %[wr], s44 offen offset:1024 nt lds\n\t" \
    "buffer_load_dwordx4 v242, %[wr], s44 offen offset:2048 nt lds\n\t" \
    "s_add_u32 m0, %[wm], 0xf000\n\t" \
    "s_mov_b32 s44, 0x60000\n\t" \
    "buffer_load_dwordx4 v240, %[wr], s44 offen offset:0 nt lds\n\t" \
    "buffer_load_dwordx4 v241, %[wr], s44 offen offset:1024 nt lds\n\t" \
    "buffer_load_dwordx4 v242, %[wr], s44 offen offset:2048 nt lds\n\t" \
    "s_add_u32 m0, %[wm], 0x0\n\t" \
    "s_mov_b32 s44, 0xc0000\n\t" \
    "buffer_load_dwordx4 v240, %[wr], s44 offen offset:0 nt lds\n\t" \
    "buffer_load_dwordx4 v241, %[wr], s44 offen offset:1024 nt lds\n\t" \
    "buffer_load_dwordx4 v242, %[wr], s44 offen offset:2048 nt lds\n\t" \
    "s_add_u32 m0, %[wm], 0x3000\n\t" \
    "s_mov_b32 s44, 0x120000\n\t" \
    "buffer_load_dwordx4 v240, %[wr], s44 offen offset:0 nt lds\n\t" \
    "buffer_load_dwordx4 v241, %[wr], s44 offen offset:1024 nt lds\n\t" \
    "buffer_load_dwordx4 v242, %[wr], s44 offen offset:2048 nt lds\n\t" \
    "s_add_u32 m0, %[wm], 0x6000\n\t" \
    "s_mov_b32 s44, 0x180000\n\t" \
    "buffer_load_dwordx4 v240, %[wr], s44 offen offset:0 nt lds\n\t" \
    "buffer_load_dwordx4 v241, %[wr], s44 offen offset:1024 nt lds\n\t" \
    "buffer_load_dwordx4 v242, %[wr], s44 offen offset:2048 nt lds\n\t" \
    "s_waitcnt lgkmcnt(0)\n\t" \
    "s_add_u32 s43, s48, s49\n\t" \
    "s_add_u32 s43, s43, s50\n\t" \
    "s_and_b32 s43, s43, 0x2000\n\t" \
    "s_xor_b32 s43, s43, %[lx]\n\t" \
    "s_add_u32 s60, s60, s43\n\t" \
    "s_addc_u32 s61, s61, 0\n\t" \
    "v_add_u32 v244, 0xc000, v243\n\t" \
    "s_waitcnt vmcnt(12) lgkmcnt(0)\n\t" \
    "s_barrier \n\t" \
    "s_add_u32 m0, %[wm], 0x9000\n\t" \
    "s_mov_b32 s44, 0x1e0000\n\t" \
    "buffer_load_dwordx4 v240, %[wr], s44 offen offset:0 nt lds\n\t" \
    "buffer_load_dwordx4 v241, %[wr], s44 offen offset:1024 nt lds\n\t" \
    "buffer_load_dwordx4 v242, %[wr], s44 offen offset:2048 nt lds\n\t" \
    "ds_read_b64_tr_b16 v[56:57], v244 offset:0\n\t" \
    "ds_read_b64_tr_b16 v[58:59], v244 offset:1536\n\t" \
    "ds_read_b64_tr_b16 v[64:65], v244 offset:32\n\t" \
    "ds_read_b64_tr_b16 v[66:67], v244 offset:1568\n\t" \
    "ds_read_b64_tr_b16 v[72:73], v244 offset:64\n\t" \
    "ds_read_b64_tr_b16 v[74:75], v244 offset:1600\n\t" \
    "v_add_u32 v244, 0xf000, v243\n\t" \
    "s_waitcnt vmcnt(12) lgkmcnt(0)\n\t" \
    "s_barrier \n\t" \
    "s_add_u32 m0, %[wm], 0xc000\n\t" \
    "s_mov_b32 s44, 0x240000\n\t" \
    "buffer_load_dwordx4 v240, %[wr], s44 offen offset:0 nt lds\n\t" \
    "buffer_load_dwordx4 v241, %[wr], s44 offen offset:1024 nt lds\n\t" \
    "buffer_load_dwordx4 v242, %[wr], s44 offen offset:2048 nt lds\n\t" \
    "ds_read_b64_tr_b16 v[60:61], v244 offset:0\n\t" \
    "ds_read_b64_tr_b16 v[62:63], v244 offset:1536\n\t" \
    "ds_read_b64_tr_b16 v[68:69], v244 offset:32\n\t" \
    "ds_read_b64_tr_b16 v[70:71], v244 offset:1568\n\t" \
    "ds_read_b64_tr_b16 v[76:77], v244 offset:64\n\t" \
    "ds_read_b64_tr_b16 v[78:79], v244 offset:1600\n\t" \
    "v_add_u32 v244, 0x0, v243\n\t" \
    "s_waitcnt vmcnt(12) lgkmcnt(0)\n\t" \
    "s_barrier \n\t" \
    "s_add_u32 m0, %[wm], 0xf000\n\t" \
    "s_mov_b32 s44, 0x2a0000\n\t" \
    "buffer_load_dwordx4 v240, %[wr], s44 offen offset:0 nt lds\n\t" \
    "buffer_load_dwordx4 v241, %[wr], s44 offen offset:1024 nt lds\n\t" \
    "buffer_load_dwordx4 v242, %[wr], s44 offen offset:2048 nt lds\n\t" \
    "ds_read_b64_tr_b16 v[96:97], v244 offset:0\n\t" \
    "ds_read_b64_tr_b16 v[98:99], v244 offset:1536\n\t" \
    "ds_read_b64_tr_b16 v[104:105], v244 offset:32\n\t" \
    "ds_read_b64_tr_b16 v[106:107], v244 offset:1568\n\t" \
    "ds_read_b64_tr_b16 v[112:113], v244 offset:64\n\t" \
    "ds_read_b64_tr_b16 v[114:115], v244 offset:1600\n\t" \
    "v_add_u32 v244, 0x3000, v243\n\t" \
    "s_waitcnt vmcnt(12) lgkmcnt(0)\n\t" \
    "s_barrier \n\t" \
    "s_add_u32 m0, %[wm], 0x0\n\t" \
    "s_mov_b32 s44, 0x300000\n\t" \
    "buffer_load_dwordx4 v240, %[wr], s44 offen offset:0 nt lds\n\t" \
    "buffer_load_dwordx4 v241, %[wr], s44 offen offset:1024 nt lds\n\t" \
    "buffer_load_dwordx4 v242, %[wr], s44 offen offset:2048 nt lds\n\t" \
    "ds_read_b64_tr_b16 v[100:101], v244 offset:0\n\t" \
    "ds_read_b64_tr_b16 v[102:103], v244 offset:1536\n\t" \
    "ds_read_b64_tr_b16 v[108:109], v244 offset:32\n\t" \
    "ds_read_b64_tr_b16 v[110:111], v244 offset:1568\n\t" \
    "ds_read_b64_tr_b16 v[116:117], v244 offset:64\n\t" \
    "ds_read_b64_tr_b16 v[118:119], v244 offset:1600\n\t" \
    "v_add_u32 v244, 0x6000, v243\n\t" \
    "s_waitcnt vmcnt(12) lgkmcnt(0)\n\t" \
    "s_barrier \n\t" \
    "s_add_u32 m0, %[wm], 0x3000\n\t" \
    "s_mov_b32 s44, 0x360000\n\t" \
    "buffer_load_dwordx4 v240, %[wr], s44 offen offset:0 nt lds\n\t" \
    "buffer_load_dwordx4 v241, %[wr], s44 offen offset:1024 nt lds\n\t" \
    "buffer_load_dwordx4 v242, %[wr], s44 offen offset:2048 nt lds\n\t" \
    "ds_read_b64_tr_b16 v[200:201], v244 offset:0\n\t" \
    "ds_read_b64_tr_b16 v[202:203], v244 offset:1536\n\t" \
    "ds_read_b64_tr_b16 v[0:1], v244 offset:32\n\t" \
    "ds_read_b64_tr_b16 v[2:3], v244 offset:1568\n\t" \
    "ds_read_b64_tr_b16 v[8:9], v244 offset:64\n\t" \
    "ds_read_b64_tr_b16 v[10:11], v244 offset:1600\n\t" \
    "v_add_u32 v244, 0x9000, v243\n\t" \
    "s_waitcnt vmcnt(12) lgkmcnt(0)\n\t" \
    "s_barrier \n\t" \
    "s_add_u32 m0, %[wm], 0x6000\n\t" \
    "s_mov_b32 s44, 0x3c0000\n\t" \
    "buffer_load_dwordx4 v240, %[wr], s44 offen offset:0 nt lds\n\t" \
    "buffer_load_dwordx4 v241, %[wr], s44 offen offset:1024 nt lds\n\t" \
    "buffer_load_dwordx4 v242, %[wr], s44 offen offset:2048 nt lds\n\t" \
    "ds_read_b64_tr_b16 v[204:205], v244 offset:0\n\t" \
    "ds_read_b64_tr_b16 v[206:207], v244 offset:1536\n\t" \
    "ds_read_b64_tr_b16 v[4:5], v244 offset:32\n\t" \
    "ds_read_b64_tr_b16 v[6:7], v244 offset:1568\n\t" \
    "ds_read_b64_tr_b16 v[12:13], v244 offset:64\n\t" \
    "ds_read_b64_tr_b16 v[14:15], v244 offset:1600\n\t" \
    "v_add_u32 v244, 0xc000, v243\n\t" \
    "s_waitcnt vmcnt(12) lgkmcnt(0)\n\t" \
    "s_barrier \n\t" \
    "s_add_u32 m0, %[wm], 0x9000\n\t" \
    "s_mov_b32 s44, 0x420000\n\t" \
    "buffer_load_dwordx4 v240, %[wr], s44 offen offset:0 nt lds\n\t" \
    "buffer_load_dwordx4 v241, %[wr], s44 offen offset:1024 nt lds\n\t" \
    "buffer_load_dwordx4 v242, %[wr], s44 offen offset:2048 nt lds\n\t" \
    "ds_read_b64_tr_b16 v[208:209], v244 offset:0\n\t" \
    "ds_read_b64_tr_b16 v[210:211], v244 offset:1536\n\t" \
    "ds_read_b64_tr_b16 v[40:41], v244 offset:32\n\t" \
    "ds_read_b64_tr_b16 v[42:43], v244 offset:1568\n\t" \
    "ds_read_b64_tr_b16 v[48:49], v244 offset:64\n\t" \
    "ds_read_b64_tr_b16 v[50:51], v244 offset:1600\n\t" \
    "v_add_u32 v244, 0xf000, v243\n\t" \
    "s_waitcnt vmcnt(12) lgkmcnt(0)\n\t" \
    "s_barrier \n\t" \
    "s_add_u32 m0, %[wm], 0xc000\n\t" \
    "s_mov_b32 s44, 0x480000\n\t" \
    "buffer_load_dwordx4 v240, %[wr], s44 offen offset:0 nt lds\n\t" \
    "buffer_load_dwordx4 v241, %[wr], s44 offen offset:1024 nt lds\n\t" \
    "buffer_load_dwordx4 v242, %[wr], s44 offen offset:2048 nt lds\n\t" \
    "ds_read_b64_tr_b16 v[212:213], v244 offset:0\n\t" \
    "ds_read_b64_tr_b16 v[214:215], v244 offset:1536\n\t" \
    "ds_read_b64_tr_b16 v[44:45], v244 offset:32\n\t" \
    "ds_read_b64_tr_b16 v[46:47], v244 offset:1568\n\t" \
    "ds_read_b64_tr_b16 v[52:53], v244 offset:64\n\t" \
    "ds_read_b64_tr_b16 v[54:55], v244 offset:1600\n\t" \
    "v_add_u32 v244, 0x0, v243\n\t" \
    "s_waitcnt vmcnt(12) lgkmcnt(0)\n\t" \
    "s_barrier \n\t" \
    "s_add_u32 m0, %[wm], 0xf000\n\t" \
    "s_mov_b32 s44, 0x4e0000\n\t" \
    "buffer_load_dwordx4 v240, %[wr], s44 offen offset:0 nt lds\n\t" \
    "buffer_load_dwordx4 v241, %[wr], s44 offen offset:1024 nt lds\n\t" \
    "buffer_load_dwordx4 v242, %[wr], s44 offen offset:2048 nt lds\n\t" \
    "ds_read_b64_tr_b16 v[136:137], v244 offset:0\n\t" \
    "ds_read_b64_tr_b16 v[138:139], v244 offset:1536\n\t" \
    "ds_read_b64_tr_b16 v[144:145], v244 offset:32\n\t" \
    "ds_read_b64_tr_b16 v[146:147], v244 offset:1568\n\t" \
    "ds_read_b64_tr_b16 v[152:153], v244 offset:64\n\t" \
    "ds_read_b64_tr_b16 v[154:155], v244 offset:1600\n\t" \
    "v_add_u32 v244, 0x3000, v243\n\t" \
    "s_waitcnt vmcnt(12) lgkmcnt(0)\n\t" \
    "s_barrier \n\t" \
    "v_and_b32 v252, 0xf0, v253\n\t" \
    "v_lshlrev_b32 v252, 3, v252\n\t" \
    "buffer_load_dword v252, v252, s[60:63], %[fl] offen sc1\n\t" \
    "s_add_u32 m0, %[wm], 0x0\n\t" \
    "s_mov_b32 s44, 0x540000\n\t" \
    "buffer_load_dwordx4 v240, %[wr], s44 offen offset:0 nt lds\n\t" \
    "buffer_load_dwordx4 v241, %[wr], s44 offen offset:1024 nt lds\n\t" \
    "buffer_load_dwordx4 v242, %[wr], s44 offen offset:2048 nt lds\n\t" \
    "ds_read_b64_tr_b16 v[140:141], v244 offset:0\n\t" \
    "ds_read_b64_tr_b16 v[142:143], v244 offset:1536\n\t" \
    "ds_read_b64_tr_b16 v[148:149], v244 offset:32\n\t" \
    "ds_read_b64_tr_b16 v[150:151], v244 offset:1568\n\t" \
    "ds_read_b64_tr_b16 v[156:157], v244 offset:64\n\t" \
    "ds_read_b64_tr_b16 v[158:159], v244 offset:1600\n\t" \
    "v_add_u32 v244, 0x6000, v243\n\t" \
    "s_waitcnt vmcnt(12) lgkmcnt(0)\n\t" \
    "s_barrier \n\t" \
    "s_add_u32 m0, %[wm], 0x3000\n\t" \
    "s_mov_b32 s44, 0x5a0000\n\t" \
    "buffer_load_dwordx4 v240, %[wr], s44 offen offset:0 nt lds\n\t" \
    "buffer_load_dwordx4 v241, %[wr], s44 offen offset:1024 nt lds\n\t" \
    "buffer_load_dwordx4 v242, %[wr], s44 offen offset:2048 nt lds\n\t" \
    "ds_read_b64_tr_b16 v[216:217], v244 offset:0\n\t" \
    "ds_read_b64_tr_b16 v[218:219], v244 offset:1536\n\t" \
    "ds_read_b64_tr_b16 v[80:81], v244 offset:32\n\t" \
    "ds_read_b64_tr_b16 v[82:83], v244 offset:1568\n\t" \
    "ds_read_b64_tr_b16 v[88:89], v244 offset:64\n\t" \
    "ds_read_b64_tr_b16 v[90:91], v244 offset:1600\n\t" \
    "v_add_u32 v244, 0x9000, v243\n\t" \
    "s_waitcnt vmcnt(12) lgkmcnt(0)\n\t" \
    "s_barrier \n\t" \
    "s_add_u32 m0, %[wm], 0x6000\n\t" \
    "s_mov_b32 s44, 0x600000\n\t" \
    "buffer_load_dwordx4 v240, %[wr], s44 offen offset:0 nt lds\n\t" \
    "buffer_load_dwordx4 v241, %[wr], s44 offen offset:1024 nt lds\n\t" \
    "buffer_load_dwordx4 v242, %[wr], s44 offen offset:2048 nt lds\n\t" \
    "ds_read_b64_tr_b16 v[220:221], v244 offset:0\n\t" \
    "ds_read_b64_tr_b16 v[222:223], v244 offset:1536\n\t" \
    "ds_read_b64_tr_b16 v[84:85], v244 offset:32\n\t" \
    "ds_read_b64_tr_b16 v[86:87], v244 offset:1568\n\t" \
    "ds_read_b64_tr_b16 v[92:93], v244 offset:64\n\t" \
    "ds_read_b64_tr_b16 v[94:95], v244 offset:1600\n\t" \
    "v_add_u32 v244, 0xc000, v243\n\t" \
    "s_waitcnt vmcnt(12) lgkmcnt(0)\n\t" \
    "s_barrier \n\t" \
    "s_add_u32 m0, %[wm], 0x9000\n\t" \
    "s_mov_b32 s44, 0x660000\n\t" \
    "buffer_load_dwordx4 v240, %[wr], s44 offen offset:0 nt lds\n\t" \
    "buffer_load_dwordx4 v241, %[wr], s44 offen offset:1024 nt lds\n\t" \
    "buffer_load_dwordx4 v242, %[wr], s44 offen offset:2048 nt lds\n\t" \
    "ds_read_b64_tr_b16 v[16:17], v244 offset:0\n\t" \
    "ds_read_b64_tr_b16 v[18:19], v244 offset:1536\n\t" \
    "ds_read_b64_tr_b16 v[24:25], v244 offset:32\n\t" \
    "ds_read_b64_tr_b16 v[26:27], v244 offset:1568\n\t" \
    "ds_read_b64_tr_b16 v[32:33], v244 offset:64\n\t" \
    "ds_read_b64_tr_b16 v[34:35], v244 offset:1600\n\t" \
    "v_add_u32 v244, 0xf000, v243\n\t" \
    "s_waitcnt vmcnt(12) lgkmcnt(0)\n\t" \
    "s_barrier \n\t" \
    "s_add_u32 m0, %[wm], 0xc000\n\t" \
    "s_mov_b32 s44, 0x6c0000\n\t" \
    "buffer_load_dwordx4 v240, %[wr], s44 offen offset:0 nt lds\n\t" \
    "buffer_load_dwordx4 v241, %[wr], s44 offen offset:1024 nt lds\n\t" \
    "buffer_load_dwordx4 v242, %[wr], s44 offen offset:2048 nt lds\n\t" \
    "ds_read_b64_tr_b16 v[20:21], v244 offset:0\n\t" \
    "ds_read_b64_tr_b16 v[22:23], v244 offset:1536\n\t" \
    "ds_read_b64_tr_b16 v[28:29], v244 offset:32\n\t" \
    "ds_read_b64_tr_b16 v[30:31], v244 offset:1568\n\t" \
    "ds_read_b64_tr_b16 v[36:37], v244 offset:64\n\t" \
    "ds_read_b64_tr_b16 v[38:39], v244 offset:1600\n\t" \
    "v_add_u32 v244, 0x0, v243\n\t" \
    "s_waitcnt vmcnt(12) lgkmcnt(0)\n\t" \
    "s_barrier \n\t" \
    "s_add_u32 m0, %[wm], 0xf000\n\t" \
    "s_mov_b32 s44, 0x720000\n\t" \
    "buffer_load_dwordx4 v240, %[wr], s44 offen offset:0 nt lds\n\t" \
    "buffer_load_dwordx4 v241, %[wr], s44 offen offset:1024 nt lds\n\t" \
    "buffer_load_dwordx4 v242, %[wr], s44 offen offset:2048 nt lds\n\t" \
    "ds_read_b64_tr_b16 v[224:225], v244 offset:0\n\t" \
    "ds_read_b64_tr_b16 v[226:227], v244 offset:1536\n\t" \
    "ds_read_b64_tr_b16 v[120:121], v244 offset:32\n\t" \
    "ds_read_b64_tr_b16 v[122:123], v244 offset:1568\n\t" \
    "ds_read_b64_tr_b16 v[128:129], v244 offset:64\n\t" \
    "ds_read_b64_tr_b16 v[130:131], v244 offset:1600\n\t" \
    "v_add_u32 v244, 0x3000, v243\n\t" \
    "s_waitcnt vmcnt(12) lgkmcnt(0)\n\t" \
    "s_barrier \n\t" \
    "ds_read_b64_tr_b16 v[228:229], v244 offset:0\n\t" \
    "ds_read_b64_tr_b16 v[230:231], v244 offset:1536\n\t" \
    "ds_read_b64_tr_b16 v[124:125], v244 offset:32\n\t" \
    "ds_read_b64_tr_b16 v[126:127], v244 offset:1568\n\t" \
    "ds_read_b64_tr_b16 v[132:133], v244 offset:64\n\t" \
    "ds_read_b64_tr_b16 v[134:135], v244 offset:1600\n\t" \
    "v_add_u32 v244, 0x6000, v243\n\t" \
    "s_waitcnt vmcnt(9) lgkmcnt(0)\n\t" \
    "s_barrier \n\t" \
    "ds_read_b64_tr_b16 v[232:233], v244 offset:0\n\t" \
    "ds_read_b64_tr_b16 v[234:235], v244 offset:1536\n\t" \
    "ds_read_b64_tr_b16 v[160:161], v244 offset:32\n\t" \
    "ds_read_b64_tr_b16 v[162:163], v244 offset:1568\n\t" \
    "ds_read_b64_tr_b16 v[168:169], v244 offset:64\n\t" \
    "ds_read_b64_tr_b16 v[170:171], v244 offset:1600\n\t" \
    "v_add_u32 v244, 0x9000, v243\n\t" \
    "s_waitcnt vmcnt(6) lgkmcnt(0)\n\t" \
    "s_barrier \n\t" \
    "ds_read_b64_tr_b16 v[236:237], v244 offset:0\n\t" \
    "ds_read_b64_tr_b16 v[238:239], v244 offset:1536\n\t" \
    "ds_read_b64_tr_b16 v[164:165], v244 offset:32\n\t" \
    "ds_read_b64_tr_b16 v[166:167], v244 offset:1568\n\t" \
    "ds_read_b64_tr_b16 v[172:173], v244 offset:64\n\t" \
    "ds_read_b64_tr_b16 v[174:175], v244 offset:1600\n\t" \
    "v_add_u32 v244, 0xc000, v243\n\t" \
    "s_waitcnt vmcnt(3) lgkmcnt(0)\n\t" \
    "s_barrier \n\t" \
    "ds_read_b64_tr_b16 v[176:177], v244 offset:0\n\t" \
    "ds_read_b64_tr_b16 v[178:179], v244 offset:1536\n\t" \
    "ds_read_b64_tr_b16 v[184:185], v244 offset:32\n\t" \
    "ds_read_b64_tr_b16 v[186:187], v244 offset:1568\n\t" \
    "ds_read_b64_tr_b16 v[192:193], v244 offset:64\n\t" \
    "ds_read_b64_tr_b16 v[194:195], v244 offset:1600\n\t" \
    "s_mov_b32 s43, 0\n\t" \
    "v_cmp_eq_u32 vcc, 0, v252\n\t" \
    "s_cbranch_vccnz .Lv11_%=\n\t" \
    "s_movk_i32 s45, 0x1000\n\t" \
    "s_mov_b32 m0, s45\n\t" \
    "s_nop 0\n\t" \
    "buffer_load_dwordx4 v253, s[60:63], s42 offen offset:0 sc1 lds\n\t" \
    "buffer_load_dwordx4 v253, s[60:63], s42 offen offset:1024 sc1 lds\n\t" \
    "buffer_load_dwordx4 v253, s[60:63], s42 offen offset:2048 sc1 lds\n\t" \
    "buffer_load_dwordx4 v253, s[60:63], s42 offen offset:3072 sc1 lds\n\t" \
    "s_add_u32 s44, s42, 0x1000\n\t" \
    "s_movk_i32 s45, 0x6000\n\t" \
    "s_mov_b32 m0, s45\n\t" \
    "s_nop 0\n\t" \
    "buffer_load_dwordx4 v253, s[60:63], s44 offen offset:0 sc1 lds\n\t" \
    "buffer_load_dwordx4 v253, s[60:63], s44 offen offset:1024 sc1 lds\n\t" \
    "buffer_load_dwordx4 v253, s[60:63], s44 offen offset:2048 sc1 lds\n\t" \
    "buffer_load_dwordx4 v253, s[60:63], s44 offen offset:3072 sc1 lds\n\t" \
    "s_add_u32 s44, s42, 0xc400\n\t" \
    "s_mov_b32 s45, 0x4400\n\t" \
    "s_mov_b32 m0, s45\n\t" \
    "s_nop 0\n\t" \
    "buffer_load_dwordx4 v253, s[60:63], s44 offen offset:0 sc1 lds\n\t" \
    "s_add_u32 s44, s44, 0x1000\n\t" \
    "s_mov_b32 s45, 0x9400\n\t" \
    "s_mov_b32 m0, s45\n\t" \
    "s_nop 0\n\t" \
    "buffer_load_dwordx4 v253, s[60:63], s44 offen offset:0 sc1 lds\n\t" \
    "s_mov_b32 s43, 1\n\t" \
    ".Lv11_%=:\n\t" \
    "v_add_u32 v244, 0xf000, v243\n\t" \
    "s_waitcnt vmcnt(0) lgkmcnt(0)\n\t" \
    "s_barrier \n\t" \
    "ds_read_b64_tr_b16 v[180:181], v244 offset:0\n\t" \
    "ds_read_b64_tr_b16 v[182:183], v244 offset:1536\n\t" \
    "ds_read_b64_tr_b16 v[188:189], v244 offset:32\n\t" \
    "ds_read_b64_tr_b16 v[190:191], v244 offset:1568\n\t" \
    "ds_read_b64_tr_b16 v[196:197], v244 offset:64\n\t" \
    "ds_read_b64_tr_b16 v[198:199], v244 offset:1600\n\t" \
    "s_waitcnt lgkmcnt(0)\n\t" \
    "s_barrier \n\t" \
    "ds_write_b128 v254, v[200:203] offset:40960\n\t" \
    "ds_write_b128 v254, v[204:207] offset:41984\n\t" \
    "ds_write_b128 v254, v[208:211] offset:43008\n\t" \
    "ds_write_b128 v254, v[212:215] offset:44032\n\t" \
    "ds_write_b128 v254, v[216:219] offset:45056\n\t" \
    "ds_write_b128 v254, v[220:223] offset:46080\n\t" \
    "ds_write_b128 v254, v[224:227] offset:47104\n\t" \
    "ds_write_b128 v254, v[228:231] offset:48128\n\t" \
    "ds_write_b128 v254, v[232:235] offset:49152\n\t" \
    "ds_write_b128 v254, v[236:239] offset:50176\n\t" \
    "s_cmp_lg_u32 s43, 0\n\t" \
    "s_cbranch_scc1 .Lv12_%=\n\t" \
    "s_mov_b32 s44, 0\n\t" \
    ".Lv13_%=:\n\t" \
    "v_cmp_ge_u32 vcc, s44, v252\n\t" \
    "s_cbranch_vccz .Lv14_%=\n\t" \
    "s_cmp_eq_u32 s41, 0\n\t" \
    "s_cbranch_scc1 .Lv14_%=\n\t" \
    "s_sub_u32 s41, s41, 1\n\t" \
    "s_sleep 8\n\t" \
    "v_and_b32 v252, 0xf0, v253\n\t" \
    "v_lshlrev_b32 v252, 3, v252\n\t" \
    "buffer_load_dword v252, v252, s[60:63], %[fl] offen sc1\n\t" \
    "s_waitcnt vmcnt(0)\n\t" \
    "s_branch .Lv13_%=\n\t" \
    ".Lv14_%=:\n\t" \
    "s_movk_i32 s45, 0x1000\n\t" \
    "s_mov_b32 m0, s45\n\t" \
    "s_nop 0\n\t" \
    "buffer_load_dwordx4 v253, s[60:63], s42 offen offset:0 sc1 lds\n\t" \
    "buffer_load_dwordx4 v253, s[60:63], s42 offen offset:1024 sc1 lds\n\t" \
    "buffer_load_dwordx4 v253, s[60:63], s42 offen offset:2048 sc1 lds\n\t" \
    "buffer_load_dwordx4 v253, s[60:63], s42 offen offset:3072 sc1 lds\n\t" \
    "s_add_u32 s44, s42, 0x1000\n\t" \
    "s_movk_i32 s45, 0x6000\n\t" \
    "s_mov_b32 m0, s45\n\t" \
    "s_nop 0\n\t" \
    "buffer_load_dwordx4 v253, s[60:63], s44 offen offset:0 sc1 lds\n\t" \
    "buffer_load_dwordx4 v253, s[60:63], s44 offen offset:1024 sc1 lds\n\t" \
    "buffer_load_dwordx4 v253, s[60:63], s44 offen offset:2048 sc1 lds\n\t" \
    "buffer_load_dwordx4 v253, s[60:63], s44 offen offset:3072 sc1 lds\n\t" \
    "s_add_u32 s44, s42, 0xc400\n\t" \
    "s_mov_b32 s45, 0x4400\n\t" \
    "s_mov_b32 m0, s45\n\t" \
    "s_nop 0\n\t" \
    "buffer_load_dwordx4 v253, s[60:63], s44 offen offset:0 sc1 lds\n\t" \
    "s_add_u32 s44, s44, 0x1000\n\t" \
    "s_mov_b32 s45, 0x9400\n\t" \
    "s_mov_b32 m0, s45\n\t" \
    "s_nop 0\n\t" \
    "buffer_load_dwordx4 v253, s[60:63], s44 offen offset:0 sc1 lds\n\t" \
    ".Lv12_%=:\n\t" \
    "s_waitcnt vmcnt(0) lgkmcnt(0)\n\t" \
    "s_barrier \n\t" \
    "ds_read_b128 v[224:227], v253 offset:0\n\t" \
    "ds_read_b128 v[244:247], v254 offset:40960\n\t" \
    "ds_read_b128 v[228:231], v253 offset:1024\n\t" \
    "ds_read_b128 v[232:235], v253 offset:20480\n\t" \
    "ds_read_b128 v[236:239], v253 offset:21504\n\t" \
    "ds_read_b128 v[248:251], v254 offset:41984\n\t" \
    ".Lv15_%=:\n\t" \
    LST7 \
    "s_waitcnt lgkmcnt(5)\n\t" \
    "v_mfma_f32_16x16x32_bf16 v[204:207], v[64:67], v[224:227], 0\n\t" \
    "s_waitcnt lgkmcnt(3)\n\t" \
    "v_mfma_f32_16x16x32_bf16 v[204:207], v[68:71], v[228:231], v[204:207]\n\t" \
    "ds_read_b128 v[240:243], v253 offset:2048\n\t" \
    "v_mfma_f32_16x16x32_bf16 v[208:211], v[72:75], v[224:227], 0\n\t" \
    "v_mfma_f32_16x16x32_bf16 v[208:211], v[76:79], v[228:231], v[208:211]\n\t" \
    "v_mfma_f32_16x16x32_bf16 v[200:203], v[56:59], v[224:227], 0\n\t" \
    "v_mfma_f32_16x16x32_bf16 v[200:203], v[60:63], v[228:231], v[200:203]\n\t" \
    "s_waitcnt lgkmcnt(3)\n\t" \
    "v_mfma_f32_16x16x32_bf16 v[216:219], v[64:67], v[232:235], 0\n\t" \
    "ds_read_b128 v[224:227], v253 offset:3072\n\t" \
    "s_waitcnt lgkmcnt(3)\n\t" \
    "v_mfma_f32_16x16x32_bf16 v[216:219], v[68:71], v[236:239], v[216:219]\n\t" \
    "ds_read_b128 v[228:231], v253 offset:22528\n\t" \
    "v_mfma_f32_16x16x32_bf16 v[220:223], v[72:75], v[232:235], 0\n\t" \
    "v_mfma_f32_16x16x32_bf16 v[220:223], v[76:79], v[236:239], v[220:223]\n\t" \
    "v_mfma_f32_16x16x32_bf16 v[212:215], v[56:59], v[232:235], 0\n\t" \
    "v_mfma_f32_16x16x32_bf16 v[212:215], v[60:63], v[236:239], v[212:215]\n\t" \
    "s_waitcnt lgkmcnt(2)\n\t" \
    "v_mfma_f32_16x16x32_bf16 v[204:207], v[104:107], v[240:243], v[204:207]\n\t" \
    "ds_read_b128 v[232:235], v253 offset:23552\n\t" \
    "s_waitcnt lgkmcnt(2)\n\t" \
    "v_mfma_f32_16x16x32_bf16 v[204:207], v[108:111], v[224:227], v[204:207]\n\t" \
    "ds_read_b128 v[236:239], v253 offset:4096\n\t" \
    "v_mfma_f32_16x16x32_bf16 v[208:211], v[112:115], v[240:243], v[208:211]\n\t" \
    "v_mfma_f32_16x16x32_bf16 v[208:211], v[116:119], v[224:227], v[208:211]\n\t" \
    "v_mfma_f32_16x16x32_bf16 v[200:203], v[96:99], v[240:243], v[200:203]\n\t" \
    "v_mfma_f32_16x16x32_bf16 v[200:203], v[100:103], v[224:227], v[200:203]\n\t" \
    "s_waitcnt lgkmcnt(2)\n\t" \
    "v_mfma_f32_16x16x32_bf16 v[216:219], v[104:107], v[228:231], v[216:219]\n\t" \
    "ds_read_b128 v[240:243], v253 offset:5120\n\t" \
    "s_waitcnt lgkmcnt(2)\n\t" \
    "v_mfma_f32_16x16x32_bf16 v[216:219], v[108:111], v[232:235], v[216:219]\n\t" \
    "ds_read_b128 v[224:227], v253 offset:24576\n\t" \
    "v_mfma_f32_16x16x32_bf16 v[220:223], v[112:115], v[228:231], v[220:223]\n\t" \
    "v_mfma_f32_16x16x32_bf16 v[220:223], v[116:119], v[232:235], v[220:223]\n\t" \
    "v_mfma_f32_16x16x32_bf16 v[212:215], v[96:99], v[228:231], v[212:215]\n\t" \
    "v_mfma_f32_16x16x32_bf16 v[212:215], v[100:103], v[232:235], v[212:215]\n\t" \
    "s_waitcnt lgkmcnt(2)\n\t" \
    "v_mfma_f32_16x16x32_bf16 v[204:207], v[0:3], v[236:239], v[204:207]\n\t" \
    "ds_read_b128 v[228:231], v253 offset:25600\n\t" \
    "s_add_u32 s44, s40, 1\n\t" \
    "s_waitcnt lgkmcnt(2)\n\t" \
    "v_mfma_f32_16x16x32_bf16 v[204:207], v[4:7], v[240:243], v[204:207]\n\t" \
    "ds_read_b128 v[232:235], v253 offset:6144\n\t" \
    "s_and_b32 s45, s44, 15\n\t" \
    "v_mfma_f32_16x16x32_bf16 v[208:211], v[8:11], v[236:239], v[208:211]\n\t" \
    "s_cmp_eq_u32 s45, 0\n\t" \
    "v_mfma_f32_16x16x32_bf16 v[208:211], v[12:15], v[240:243], v[208:211]\n\t" \
    "s_cselect_b32 s45, %[cj], %[cs]\n\t" \
    "v_mfma_f32_16x16x32_bf16 v[200:203], v[244:247], v[236:239], v[200:203]\n\t" \
    "s_cmp_eq_u32 s44, 64\n\t" \
    "v_mfma_f32_16x16x32_bf16 v[200:203], v[248:251], v[240:243], v[200:203]\n\t" \
    "s_cselect_b32 s45, 0, s45\n\t" \
    "s_waitcnt lgkmcnt(2)\n\t" \
    "v_mfma_f32_16x16x32_bf16 v[216:219], v[0:3], v[224:227], v[216:219]\n\t" \
    "ds_read_b128 v[236:239], v253 offset:7168\n\t" \
    "s_add_u32 s42, s42, s45\n\t" \
    "s_waitcnt lgkmcnt(2)\n\t" \
    "v_mfma_f32_16x16x32_bf16 v[216:219], v[4:7], v[228:231], v[216:219]\n\t" \
    "ds_read_b128 v[240:243], v253 offset:26624\n\t" \
    "v_mfma_f32_16x16x32_bf16 v[220:223], v[8:11], v[224:227], v[220:223]\n\t" \
    "v_mfma_f32_16x16x32_bf16 v[220:223], v[12:15], v[228:231], v[220:223]\n\t" \
    "v_mfma_f32_16x16x32_bf16 v[212:215], v[244:247], v[224:227], v[212:215]\n\t" \
    "ds_read_b128 v[244:247], v254 offset:43008\n\t" \
    "v_mfma_f32_16x16x32_bf16 v[212:215], v[248:251], v[228:231], v[212:215]\n\t" \
    "ds_read_b128 v[248:251], v254 offset:44032\n\t" \
    "s_waitcnt lgkmcnt(4)\n\t" \
    "v_mfma_f32_16x16x32_bf16 v[204:207], v[40:43], v[232:235], v[204:207]\n\t" \
    "ds_read_b128 v[224:227], v253 offset:27648\n\t" \
    "s_waitcnt lgkmcnt(4)\n\t" \
    "v_mfma_f32_16x16x32_bf16 v[204:207], v[44:47], v[236:239], v[204:207]\n\t" \
    "ds_read_b128 v[228:231], v253 offset:8192\n\t" \
    "v_mfma_f32_16x16x32_bf16 v[208:211], v[48:51], v[232:235], v[208:211]\n\t" \
    "v_mfma_f32_16x16x32_bf16 v[208:211], v[52:55], v[236:239], v[208:211]\n\t" \
    "s_waitcnt lgkmcnt(3)\n\t" \
    "v_mfma_f32_16x16x32_bf16 v[200:203], v[244:247], v[232:235], v[200:203]\n\t" \
    "s_waitcnt lgkmcnt(2)\n\t" \
    "v_mfma_f32_16x16x32_bf16 v[200:203], v[248:251], v[236:239], v[200:203]\n\t" \
    "v_mfma_f32_16x16x32_bf16 v[216:219], v[40:43], v[240:243], v[216:219]\n\t" \
    "ds_read_b128 v[232:235], v253 offset:9216\n\t" \
    "s_waitcnt lgkmcnt(2)\n\t" \
    "v_mfma_f32_16x16x32_bf16 v[216:219], v[44:47], v[224:227], v[216:219]\n\t" \
    "ds_read_b128 v[236:239], v253 offset:28672\n\t" \
    "v_mfma_f32_16x16x32_bf16 v[220:223], v[48:51], v[240:243], v[220:223]\n\t" \
    "v_mfma_f32_16x16x32_bf16 v[220:223], v[52:55], v[224:227], v[220:223]\n\t" \
    "v_mfma_f32_16x16x32_bf16 v[212:215], v[244:247], v[240:243], v[212:215]\n\t" \
    "ds_read_b128 v[244:247], v254 offset:45056\n\t" \
    "v_mfma_f32_16x16x32_bf16 v[212:215], v[248:251], v[224:227], v[212:215]\n\t" \
    "ds_read_b128 v[248:251], v254 offset:46080\n\t" \
    "s_waitcnt lgkmcnt(4)\n\t" \
    "v_mfma_f32_16x16x32_bf16 v[204:207], v[144:147], v[228:231], v[204:207]\n\t" \
    "ds_read_b128 v[240:243], v253 offset:29696\n\t" \
    "s_waitcnt lgkmcnt(4)\n\t" \
    "v_mfma_f32_16x16x32_bf16 v[204:207], v[148:151], v[232:235], v[204:207]\n\t" \
    "ds_read_b128 v[224:227], v253 offset:10240\n\t" \
    "v_mfma_f32_16x16x32_bf16 v[208:211], v[152:155], v[228:231], v[208:211]\n\t" \
    "v_mfma_f32_16x16x32_bf16 v[208:211], v[156:159], v[232:235], v[208:211]\n\t" \
    "v_mfma_f32_16x16x32_bf16 v[200:203], v[136:139], v[228:231], v[200:203]\n\t" \
    "v_mfma_f32_16x16x32_bf16 v[200:203], v[140:143], v[232:235], v[200:203]\n\t" \
    "s_waitcnt lgkmcnt(4)\n\t" \
    "v_mfma_f32_16x16x32_bf16 v[216:219], v[144:147], v[236:239], v[216:219]\n\t" \
    "ds_read_b128 v[228:231], v253 offset:11264\n\t" \
    "s_waitcnt lgkmcnt(2)\n\t" \
    "v_mfma_f32_16x16x32_bf16 v[216:219], v[148:151], v[240:243], v[216:219]\n\t" \
    "ds_read_b128 v[232:235], v253 offset:30720\n\t" \
    "v_mfma_f32_16x16x32_bf16 v[220:223], v[152:155], v[236:239], v[220:223]\n\t" \
    "v_mfma_f32_16x16x32_bf16 v[220:223], v[156:159], v[240:243], v[220:223]\n\t" \
    "v_mfma_f32_16x16x32_bf16 v[212:215], v[136:139], v[236:239], v[212:215]\n\t" \
    "v_mfma_f32_16x16x32_bf16 v[212:215], v[140:143], v[240:243], v[212:215]\n\t" \
    "s_waitcnt lgkmcnt(2)\n\t" \
    "v_mfma_f32_16x16x32_bf16 v[204:207], v[80:83], v[224:227], v[204:207]\n\t" \
    "ds_read_b128 v[236:239], v253 offset:31744\n\t" \
    "s_and_b32 s44, s40, 3\n\t" \
    "s_waitcnt lgkmcnt(2)\n\t" \
    "v_mfma_f32_16x16x32_bf16 v[204:207], v[84:87], v[228:231], v[204:207]\n\t" \
    "ds_read_b128 v[240:243], v253 offset:14336\n\t" \
    "s_lshl_b32 s44, s44, 17\n\t" \
    "v_mfma_f32_16x16x32_bf16 v[208:211], v[88:91], v[224:227], v[208:211]\n\t" \
    "s_add_u32 s44, s44, %[mb]\n\t" \
    "v_mfma_f32_16x16x32_bf16 v[208:211], v[92:95], v[228:231], v[208:211]\n\t" \
    "s_add_u32 s46, s44, 0x800\n\t" \
    "v_mfma_f32_16x16x32_bf16 v[200:203], v[244:247], v[224:227], v[200:203]\n\t" \
    "s_add_u32 s47, s44, 0x1800\n\t" \
    "v_mfma_f32_16x16x32_bf16 v[200:203], v[248:251], v[228:231], v[200:203]\n\t" \
    "s_add_u32 s48, s44, 0xc00\n\t" \
    "s_waitcnt lgkmcnt(2)\n\t" \
    "v_mfma_f32_16x16x32_bf16 v[216:219], v[80:83], v[232:235], v[216:219]\n\t" \
    "ds_read_b128 v[224:227], v253 offset:15360\n\t" \
    "s_waitcnt lgkmcnt(2)\n\t" \
    "v_mfma_f32_16x16x32_bf16 v[216:219], v[84:87], v[236:239], v[216:219]\n\t" \
    "ds_read_b128 v[228:231], v253 offset:34816\n\t" \
    "v_mfma_f32_16x16x32_bf16 v[220:223], v[88:91], v[232:235], v[220:223]\n\t" \
    "v_mfma_f32_16x16x32_bf16 v[220:223], v[92:95], v[236:239], v[220:223]\n\t" \
    "v_mfma_f32_16x16x32_bf16 v[212:215], v[244:247], v[232:235], v[212:215]\n\t" \
    "ds_read_b128 v[244:247], v254 offset:47104\n\t" \
    "v_mfma_f32_16x16x32_bf16 v[212:215], v[248:251], v[236:239], v[212:215]\n\t" \
    "ds_read_b128 v[248:251], v254 offset:48128\n\t" \
    "s_waitcnt lgkmcnt(4)\n\t" \
    "v_mfma_f32_16x16x32_bf16 v[204:207], v[120:123], v[240:243], v[204:207]\n\t" \
    "ds_read_b128 v[232:235], v253 offset:35840\n\t" \
    "s_waitcnt lgkmcnt(4)\n\t" \
    "v_mfma_f32_16x16x32_bf16 v[204:207], v[124:127], v[224:227], v[204:207]\n\t" \
    "ds_read_b128 v[236:239], v253 offset:16384\n\t" \
    "v_mfma_f32_16x16x32_bf16 v[208:211], v[128:131], v[240:243], v[208:211]\n\t" \
    "v_mfma_f32_16x16x32_bf16 v[208:211], v[132:135], v[224:227], v[208:211]\n\t" \
    "s_waitcnt lgkmcnt(3)\n\t" \
    "v_mfma_f32_16x16x32_bf16 v[200:203], v[244:247], v[240:243], v[200:203]\n\t" \
    "s_waitcnt lgkmcnt(2)\n\t" \
    "v_mfma_f32_16x16x32_bf16 v[200:203], v[248:251], v[224:227], v[200:203]\n\t" \
    "v_mfma_f32_16x16x32_bf16 v[216:219], v[120:123], v[228:231], v[216:219]\n\t" \
    "ds_read_b128 v[240:243], v253 offset:17408\n\t" \
    "s_waitcnt lgkmcnt(2)\n\t" \
    "v_mfma_f32_16x16x32_bf16 v[216:219], v[124:127], v[232:235], v[216:219]\n\t" \
    "ds_read_b128 v[224:227], v253 offset:36864\n\t" \
    "v_mfma_f32_16x16x32_bf16 v[220:223], v[128:131], v[228:231], v[220:223]\n\t" \
    "v_mfma_f32_16x16x32_bf16 v[220:223], v[132:135], v[232:235], v[220:223]\n\t" \
    "v_mfma_f32_16x16x32_bf16 v[212:215], v[244:247], v[228:231], v[212:215]\n\t" \
    "ds_read_b128 v[244:247], v254 offset:49152\n\t" \
    "v_mfma_f32_16x16x32_bf16 v[212:215], v[248:251], v[232:235], v[212:215]\n\t" \
    "ds_read_b128 v[248:251], v254 offset:50176\n\t" \
    "v_and_b32 v252, 0xf0, v253\n\t" \
    "v_lshlrev_b32 v252, 3, v252\n\t" \
    "buffer_load_dword v252, v252, s[60:63], %[fl] offen sc1\n\t" \
    "s_waitcnt lgkmcnt(4)\n\t" \
    "v_mfma_f32_16x16x32_bf16 v[204:207], v[160:163], v[236:239], v[204:207]\n\t" \
    "ds_read_b128 v[228:231], v253 offset:37888\n\t" \
    "s_waitcnt lgkmcnt(4)\n\t" \
    "v_mfma_f32_16x16x32_bf16 v[204:207], v[164:167], v[240:243], v[204:207]\n\t" \
    "ds_read_b128 v[232:235], v253 offset:12288\n\t" \
    "v_mfma_f32_16x16x32_bf16 v[208:211], v[168:171], v[236:239], v[208:211]\n\t" \
    "v_mfma_f32_16x16x32_bf16 v[208:211], v[172:175], v[240:243], v[208:211]\n\t" \
    "s_waitcnt lgkmcnt(3)\n\t" \
    "v_mfma_f32_16x16x32_bf16 v[200:203], v[244:247], v[236:239], v[200:203]\n\t" \
    "s_waitcnt lgkmcnt(2)\n\t" \
    "v_mfma_f32_16x16x32_bf16 v[200:203], v[248:251], v[240:243], v[200:203]\n\t" \
    "v_mfma_f32_16x16x32_bf16 v[216:219], v[160:163], v[224:227], v[216:219]\n\t" \
    "ds_read_b128 v[236:239], v253 offset:13312\n\t" \
    "s_waitcnt lgkmcnt(2)\n\t" \
    "v_mfma_f32_16x16x32_bf16 v[216:219], v[164:167], v[228:231], v[216:219]\n\t" \
    "ds_read_b128 v[240:243], v253 offset:18432\n\t" \
    "v_mfma_f32_16x16x32_bf16 v[220:223], v[168:171], v[224:227], v[220:223]\n\t" \
    "v_mfma_f32_16x16x32_bf16 v[220:223], v[172:175], v[228:231], v[220:223]\n\t" \
    "v_mfma_f32_16x16x32_bf16 v[212:215], v[244:247], v[224:227], v[212:215]\n\t" \
    "v_mfma_f32_16x16x32_bf16 v[212:215], v[248:251], v[228:231], v[212:215]\n\t" \
    "s_waitcnt lgkmcnt(2)\n\t" \
    "v_mfma_f32_16x16x32_bf16 v[204:207], v[24:27], v[232:235], v[204:207]\n\t" \
    "ds_read_b128 v[224:227], v253 offset:19456\n\t" \
    "v_mov_b64 v[244:245], 0\n\t" \
    "s_waitcnt lgkmcnt(2)\n\t" \
    "v_mfma_f32_16x16x32_bf16 v[204:207], v[28:31], v[236:239], v[204:207]\n\t" \
    "ds_read_b128 v[228:231], v253 offset:32768\n\t" \
    "v_mov_b64 v[246:247], 0\n\t" \
    "v_mfma_f32_16x16x32_bf16 v[208:211], v[32:35], v[232:235], v[208:211]\n\t" \
    "v_mov_b64 v[248:249], 0\n\t" \
    "v_mfma_f32_16x16x32_bf16 v[208:211], v[36:39], v[236:239], v[208:211]\n\t" \
    "v_mov_b64 v[250:251], 0\n\t" \
    "v_mfma_f32_16x16x32_bf16 v[200:203], v[16:19], v[232:235], v[200:203]\n\t" \
    "v_mfma_f32_16x16x32_bf16 v[200:203], v[20:23], v[236:239], v[200:203]\n\t" \
    "s_waitcnt lgkmcnt(2)\n\t" \
    "v_mfma_f32_16x16x32_bf16 v[204:207], v[184:187], v[240:243], v[204:207]\n\t" \
    "ds_read_b128 v[232:235], v253 offset:33792\n\t" \
    "s_waitcnt lgkmcnt(2)\n\t" \
    "v_mfma_f32_16x16x32_bf16 v[204:207], v[188:191], v[224:227], v[204:207]\n\t" \
    "ds_read_b128 v[236:239], v253 offset:38912\n\t" \
    "v_mfma_f32_16x16x32_bf16 v[208:211], v[192:195], v[240:243], v[208:211]\n\t" \
    "v_mfma_f32_16x16x32_bf16 v[208:211], v[196:199], v[224:227], v[208:211]\n\t" \
    "v_mfma_f32_16x16x32_bf16 v[200:203], v[176:179], v[240:243], v[200:203]\n\t" \
    "v_mfma_f32_16x16x32_bf16 v[200:203], v[180:183], v[224:227], v[200:203]\n\t" \
    "ds_read_b128 v[240:243], v253 offset:39936\n\t" \
    "s_waitcnt lgkmcnt(3)\n\t" \
    "v_mfma_f32_16x16x32_bf16 v[216:219], v[24:27], v[228:231], v[216:219]\n\t" \
    "s_waitcnt lgkmcnt(2)\n\t" \
    "v_mfma_f32_16x16x32_bf16 v[216:219], v[28:31], v[232:235], v[216:219]\n\t" \
    "v_cvt_pk_bf16_f32 v204, v204, v205\n\t" \
    "v_cvt_pk_bf16_f32 v205, v206, v207\n\t" \
    "s_waitcnt lgkmcnt(1)\n\t" \
    "v_mfma_f32_16x16x32_bf16 v[216:219], v[184:187], v[236:239], v[216:219]\n\t" \
    "ds_write_b64 v254, v[204:205] offset:10240\n\t" \
    "v_cvt_pk_bf16_f32 v208, v208, v209\n\t" \
    "s_waitcnt lgkmcnt(1)\n\t" \
    "v_mfma_f32_16x16x32_bf16 v[216:219], v[188:191], v[240:243], v[216:219]\n\t" \
    "v_cvt_pk_bf16_f32 v209, v210, v211\n\t" \
    "ds_write_b64 v254, v[208:209] offset:11264\n\t" \
    "v_mfma_f32_16x16x32_bf16 v[220:223], v[32:35], v[228:231], v[220:223]\n\t" \
    "v_cvt_pk_bf16_f32 v200, v200, v201\n\t" \
    "v_cvt_pk_bf16_f32 v201, v202, v203\n\t" \
    "v_mfma_f32_16x16x32_bf16 v[220:223], v[36:39], v[232:235], v[220:223]\n\t" \
    "ds_write_b64 v254, v[200:201] offset:9216\n\t" \
    "ds_read_b128 v[200:203], v254 offset:40960\n\t" \
    "v_mfma_f32_16x16x32_bf16 v[220:223], v[192:195], v[236:239], v[220:223]\n\t" \
    "ds_read_b128 v[204:207], v254 offset:41984\n\t" \
    "v_mov_b64 v[208:209], 0\n\t" \
    "v_mfma_f32_16x16x32_bf16 v[220:223], v[196:199], v[240:243], v[220:223]\n\t" \
    "v_mov_b64 v[210:211], 0\n\t" \
    "v_cvt_pk_bf16_f32 v216, v216, v217\n\t" \
    "v_mfma_f32_16x16x32_bf16 v[212:215], v[16:19], v[228:231], v[212:215]\n\t" \
    "v_cvt_pk_bf16_f32 v217, v218, v219\n\t" \
    "ds_write_b64 v254, v[216:217] offset:16384\n\t" \
    "v_mfma_f32_16x16x32_bf16 v[212:215], v[20:23], v[232:235], v[212:215]\n\t" \
    "v_mfma_f32_16x16x32_bf16 v[212:215], v[176:179], v[236:239], v[212:215]\n\t" \
    "v_cvt_pk_bf16_f32 v220, v220, v221\n\t" \
    "v_cvt_pk_bf16_f32 v221, v222, v223\n\t" \
    "v_mfma_f32_16x16x32_bf16 v[212:215], v[180:183], v[240:243], v[212:215]\n\t" \
    "ds_write_b64 v254, v[220:221] offset:17408\n\t" \
    "ds_read_b128 v[216:219], v254 offset:43008\n\t" \
    "ds_read_b128 v[220:223], v254 offset:44032\n\t" \
    "s_nop 4\n\t" \
    "v_cvt_pk_bf16_f32 v212, v212, v213\n\t" \
    "v_cvt_pk_bf16_f32 v213, v214, v215\n\t" \
    "ds_write_b64 v254, v[212:213] offset:15360\n\t" \
    LST6 \
    "s_waitcnt vmcnt(0) lgkmcnt(0)\n\t" \
    "s_barrier \n\t" \
    "s_setprio 2\n\t" \
    "s_add_u32 s44, s40, 1\n\t" \
    "s_min_u32 s44, s44, 63\n\t" \
    ".Lv16_%=:\n\t" \
    "v_cmp_ge_u32 vcc, s44, v252\n\t" \
    "s_cbranch_vccz .Lv17_%=\n\t" \
    "s_cmp_eq_u32 s41, 0\n\t" \
    "s_cbranch_scc1 .Lv17_%=\n\t" \
    "s_sub_u32 s41, s41, 1\n\t" \
    "s_sleep 2\n\t" \
    "v_and_b32 v252, 0xf0, v253\n\t" \
    "v_lshlrev_b32 v252, 3, v252\n\t" \
    "buffer_load_dword v252, v252, s[60:63], %[fl] offen sc1\n\t" \
    "s_waitcnt vmcnt(0)\n\t" \
    "s_branch .Lv16_%=\n\t" \
    ".Lv17_%=:\n\t" \
    "s_movk_i32 s45, 0x1000\n\t" \
    "s_mov_b32 m0, s45\n\t" \
    "s_nop 0\n\t" \
    "buffer_load_dwordx4 v253, s[60:63], s42 offen offset:0 sc1 lds\n\t" \
    "buffer_load_dwordx4 v253, s[60:63], s42 offen offset:1024 sc1 lds\n\t" \
    "buffer_load_dwordx4 v253, s[60:63], s42 offen offset:2048 sc1 lds\n\t" \
    "buffer_load_dwordx4 v253, s[60:63], s42 offen offset:3072 sc1 lds\n\t" \
    "s_add_u32 s44, s42, 0x1000\n\t" \
    "s_movk_i32 s45, 0x6000\n\t" \
    "s_mov_b32 m0, s45\n\t" \
    "s_nop 0\n\t" \
    "buffer_load_dwordx4 v253, s[60:63], s44 offen offset:0 sc1 lds\n\t" \
    "buffer_load_dwordx4 v253, s[60:63], s44 offen offset:1024 sc1 lds\n\t" \
    "buffer_load_dwordx4 v253, s[60:63], s44 offen offset:2048 sc1 lds\n\t" \
    "buffer_load_dwordx4 v253, s[60:63], s44 offen offset:3072 sc1 lds\n\t" \
    "s_add_u32 s44, s42, 0xc400\n\t" \
    "s_mov_b32 s45, 0x4400\n\t" \
    "s_mov_b32 m0, s45\n\t" \
    "s_nop 0\n\t" \
    "buffer_load_dwordx4 v253, s[60:63], s44 offen offset:0 sc1 lds\n\t" \
    "s_add_u32 s44, s44, 0x1000\n\t" \
    "s_mov_b32 s45, 0x9400\n\t" \
    "s_mov_b32 m0, s45\n\t" \
    "s_nop 0\n\t" \
    "buffer_load_dwordx4 v253, s[60:63], s44 offen offset:0 sc1 lds\n\t" \
    LST1 \
    "s_sleep 18\n\t" \
    "buffer_load_dwordx4 v[224:227], v253, s[60:63], s46 offen sc1\n\t" \
    "buffer_load_dwordx4 v[228:231], v253, s[60:63], s47 offen sc1\n\t" \
    "buffer_load_dwordx4 v[232:235], v253, s[60:63], s48 offen sc1\n\t" \
    "s_waitcnt lgkmcnt(0)\n\t" \
    LFLUSH212 \
    "v_bfe_i32 v212, v253, 5, 1\n\t" \
    "v_and_b32 v212, 0xaaaa, v212\n\t" \
    "v_or_b32 v212, 0x4444, v212\n\t" \
    "v_and_b32 v213, 0xffff, v255\n\t" \
    "v_lshrrev_b32 v214, 16, v255\n\t" \
    ".Lv18_%=:\n\t" \
    "s_waitcnt vmcnt(2)\n\t" \
    "v_max_u32 v215, v224, v225\n\t" \
    "v_max3_u32 v215, v226, v227, v215\n\t" \
    "s_waitcnt vmcnt(1)\n\t" \
    "v_max3_u32 v215, v228, v229, v215\n\t" \
    "v_max3_u32 v215, v230, v231, v215\n\t" \
    "s_waitcnt vmcnt(0)\n\t" \
    "v_max3_u32 v215, v232, v233, v215\n\t" \
    "v_max3_u32 v215, v234, v235, v215\n\t" \
    "v_cmp_eq_u32 vcc, -1, v215\n\t" \
    "s_cbranch_vccz .Lv19_%=\n\t" \
    "v_max_u32 v215, v224, v225\n\t" \
    "v_max3_u32 v215, v226, v227, v215\n\t" \
    "v_cmp_eq_u32 vcc, -1, v215\n\t" \
    "s_cbranch_vccz .Lv110_%=\n\t" \
    "buffer_load_dwordx4 v[224:227], v253, s[60:63], s46 offen sc1\n\t" \
    ".Lv110_%=:\n\t" \
    "v_max_u32 v215, v228, v229\n\t" \
    "v_max3_u32 v215, v230, v231, v215\n\t" \
    "v_cmp_eq_u32 vcc, -1, v215\n\t" \
    "s_cbranch_vccz .Lv111_%=\n\t" \
    "buffer_load_dwordx4 v[228:231], v253, s[60:63], s47 offen sc1\n\t" \
    ".Lv111_%=:\n\t" \
    "v_max_u32 v215, v232, v233\n\t" \
    "v_max3_u32 v215, v234, v235, v215\n\t" \
    "v_cmp_eq_u32 vcc, -1, v215\n\t" \
    "s_cbranch_vccz .Lv112_%=\n\t" \
    "buffer_load_dwordx4 v[232:235], v253, s[60:63], s48 offen sc1\n\t" \
    ".Lv112_%=:\n\t" \
    "s_cmp_eq_u32 s41, 0\n\t" \
    "s_cbranch_scc1 .Lv19_%=\n\t" \
    "s_sub_u32 s41, s41, 1\n\t" \
    "s_waitcnt vmcnt(0)\n\t" \
    "s_branch .Lv18_%=\n\t" \
    ".Lv19_%=:\n\t" \
    LST2 \
    "ds_write_b128 v253, v[224:227] offset:43008\n\t" \
    "ds_write_b128 v253, v[228:231] offset:47104\n\t" \
    "ds_write_b128 v253, v[232:235] offset:44032\n\t" \
    "v_smfmac_f32_16x16x64_bf16 v[244:247], v[224:227], v[0:7], v212\n\t" \
    "v_smfmac_f32_16x16x64_bf16 v[248:251], v[224:227], v[8:15], v212\n\t" \
    "v_smfmac_f32_16x16x64_bf16 v[208:211], v[224:227], v[200:207], v212\n\t" \
    "s_waitcnt lgkmcnt(0)\n\t" \
    "s_barrier \n\t" \
    LST3 \
    "ds_read_b128 v[236:239], v253 offset:40960\n\t" \
    "ds_read_b128 v[240:243], v253 offset:46080\n\t" \
    "ds_read_b128 v[224:227], v253 offset:41984\n\t" \
    "ds_read_b128 v[200:203], v254 offset:45056\n\t" \
    "ds_read_b128 v[204:207], v254 offset:46080\n\t" \
    "v_smfmac_f32_16x16x64_bf16 v[208:211], v[228:231], v[16:23], v212\n\t" \
    "v_smfmac_f32_16x16x64_bf16 v[244:247], v[228:231], v[24:31], v212\n\t" \
    "v_smfmac_f32_16x16x64_bf16 v[248:251], v[228:231], v[32:39], v212\n\t" \
    "ds_read_b128 v[228:231], v253 offset:48128\n\t" \
    "v_smfmac_f32_16x16x64_bf16 v[208:211], v[232:235], v[216:223], v212\n\t" \
    "ds_read_b128 v[216:219], v254 offset:47104\n\t" \
    "ds_read_b128 v[220:223], v254 offset:48128\n\t" \
    "v_smfmac_f32_16x16x64_bf16 v[244:247], v[232:235], v[40:47], v212\n\t" \
    "v_smfmac_f32_16x16x64_bf16 v[248:251], v[232:235], v[48:55], v212\n\t" \
    "ds_read_b128 v[232:235], v253 offset:45056\n\t" \
    "s_waitcnt lgkmcnt(8)\n\t" \
    "v_smfmac_f32_16x16x64_bf16 v[208:211], v[236:239], v[56:63], v212\n\t" \
    "v_smfmac_f32_16x16x64_bf16 v[244:247], v[236:239], v[64:71], v212\n\t" \
    "v_smfmac_f32_16x16x64_bf16 v[248:251], v[236:239], v[72:79], v212\n\t" \
    "ds_read_b128 v[236:239], v253 offset:49152\n\t" \
    "s_waitcnt lgkmcnt(5)\n\t" \
    "v_smfmac_f32_16x16x64_bf16 v[208:211], v[240:243], v[200:207], v212\n\t" \
    "ds_read_b128 v[200:203], v254 offset:49152\n\t" \
    "ds_read_b128 v[204:207], v254 offset:50176\n\t" \
    "v_smfmac_f32_16x16x64_bf16 v[244:247], v[240:243], v[80:87], v212\n\t" \
    "v_smfmac_f32_16x16x64_bf16 v[248:251], v[240:243], v[88:95], v212\n\t" \
    "ds_read_b128 v[240:243], v253 offset:50176\n\t" \
    "v_smfmac_f32_16x16x64_bf16 v[208:211], v[224:227], v[96:103], v212\n\t" \
    "v_smfmac_f32_16x16x64_bf16 v[244:247], v[224:227], v[104:111], v212\n\t" \
    "v_smfmac_f32_16x16x64_bf16 v[248:251], v[224:227], v[112:119], v212\n\t" \
    "s_waitcnt lgkmcnt(7)\n\t" \
    "v_smfmac_f32_16x16x64_bf16 v[244:247], v[228:231], v[120:127], v212\n\t" \
    "s_waitcnt lgkmcnt(4)\n\t" \
    "v_smfmac_f32_16x16x64_bf16 v[244:247], v[232:235], v[144:151], v212\n\t" \
    "s_waitcnt lgkmcnt(3)\n\t" \
    "v_smfmac_f32_16x16x64_bf16 v[244:247], v[236:239], v[160:167], v212\n\t" \
    "s_waitcnt lgkmcnt(0)\n\t" \
    "v_smfmac_f32_16x16x64_bf16 v[244:247], v[240:243], v[184:191], v212\n\t" \
    "v_smfmac_f32_16x16x64_bf16 v[248:251], v[228:231], v[128:135], v212\n\t" \
    "v_smfmac_f32_16x16x64_bf16 v[248:251], v[232:235], v[152:159], v212\n\t" \
    "v_smfmac_f32_16x16x64_bf16 v[248:251], v[236:239], v[168:175], v212\n\t" \
    "v_add_f32 v244, v244, v246\n\t" \
    "v_add_f32 v245, v245, v247\n\t" \
    "v_cvt_pk_bf16_f32 v244, v244, v245\n\t" \
    "v_smfmac_f32_16x16x64_bf16 v[248:251], v[240:243], v[192:199], v212\n\t" \
    "ds_write_b16 v213, v244 offset:512\n\t" \
    "ds_write_b16_d16_hi v213, v244 offset:528\n\t" \
    "v_smfmac_f32_16x16x64_bf16 v[208:211], v[228:231], v[216:223], v212\n\t" \
    "v_smfmac_f32_16x16x64_bf16 v[208:211], v[232:235], v[136:143], v212\n\t" \
    "v_smfmac_f32_16x16x64_bf16 v[208:211], v[236:239], v[200:207], v212\n\t" \
    "v_add_f32 v248, v248, v250\n\t" \
    "v_add_f32 v249, v249, v251\n\t" \
    "v_cvt_pk_bf16_f32 v248, v248, v249\n\t" \
    "v_smfmac_f32_16x16x64_bf16 v[208:211], v[240:243], v[176:183], v212\n\t" \
    "ds_write_b16 v213, v248 offset:1024\n\t" \
    "ds_write_b16_d16_hi v213, v248 offset:1040\n\t" \
    "s_nop 5\n\t" \
    "v_add_f32 v208, v208, v210\n\t" \
    "v_add_f32 v209, v209, v211\n\t" \
    "v_cvt_pk_bf16_f32 v208, v208, v209\n\t" \
    "ds_write_b16 v213, v208 offset:0\n\t" \
    "ds_write_b16_d16_hi v213, v208 offset:16\n\t" \
    "s_setprio 0\n\t" \
    "s_waitcnt lgkmcnt(0)\n\t" \
    "s_barrier \n\t" \
    LST4 \
    "ds_read_b128 v[224:227], v253 offset:0\n\t" \
    "ds_read_b128 v[244:247], v254 offset:40960\n\t" \
    "ds_read_b128 v[228:231], v253 offset:1024\n\t" \
    "ds_read_b128 v[232:235], v253 offset:20480\n\t" \
    "ds_read_b128 v[236:239], v253 offset:21504\n\t" \
    "ds_read_b128 v[248:251], v254 offset:41984\n\t" \
    "s_add_u32 s40, s40, 1\n\t" \
    "s_cmp_lg_u32 s40, 64\n\t" \
    "s_cbranch_scc1 .Lv15_%=\n\t" \
    "s_branch .Lend%=\n\t" \
    ".Lv0_%=:\n\t" \
    "s_mov_b32 s42, %[xs]\n\t" \
    "s_add_u32 m0, %[wm], 0xc000\n\t" \
    "s_mov_b32 s44, 0x0\n\t" \
    "buffer_load_dwordx4 v240, %[wr], s44 offen offset:0 nt lds\n\t" \
    "buffer_load_dwordx4 v241, %[wr], s44 offen offset:1024 nt lds\n\t" \
    "buffer_load_dwordx4 v242, %[wr], s44 offen offset:2048 nt lds\n\t" \
    "s_add_u32 m0, %[wm], 0xf000\n\t" \
    "s_mov_b32 s44, 0x60000\n\t" \
    "buffer_load_dwordx4 v240, %[wr], s44 offen offset:0 nt lds\n\t" \
    "buffer_load_dwordx4 v241, %[wr], s44 offen offset:1024 nt lds\n\t" \
    "buffer_load_dwordx4 v242, %[wr], s44 offen offset:2048 nt lds\n\t" \
    "s_add_u32 m0, %[wm], 0x0\n\t" \
    "s_mov_b32 s44, 0xc0000\n\t" \
    "buffer_load_dwordx4 v240, %[wr], s44 offen offset:0 nt lds\n\t" \
    "buffer_load_dwordx4 v241, %[wr], s44 offen offset:1024 nt lds\n\t" \
    "buffer_load_dwordx4 v242, %[wr], s44 offen offset:2048 nt lds\n\t" \
    "s_add_u32 m0, %[wm], 0x3000\n\t" \
    "s_mov_b32 s44, 0x120000\n\t" \
    "buffer_load_dwordx4 v240, %[wr], s44 offen offset:0 nt lds\n\t" \
    "buffer_load_dwordx4 v241, %[wr], s44 offen offset:1024 nt lds\n\t" \
    "buffer_load_dwordx4 v242, %[wr], s44 offen offset:2048 nt lds\n\t" \
    "s_add_u32 m0, %[wm], 0x6000\n\t" \
    "s_mov_b32 s44, 0x180000\n\t" \
    "buffer_load_dwordx4 v240, %[wr], s44 offen offset:0 nt lds\n\t" \
    "buffer_load_dwordx4 v241, %[wr], s44 offen offset:1024 nt lds\n\t" \
    "buffer_load_dwordx4 v242, %[wr], s44 offen offset:2048 nt lds\n\t" \
    "s_waitcnt lgkmcnt(0)\n\t" \
    "s_add_u32 s43, s48, s49\n\t" \
    "s_add_u32 s43, s43, s50\n\t" \
    "s_and_b32 s43, s43, 0x2000\n\t" \
    "s_xor_b32 s43, s43, %[lx]\n\t" \
    "s_add_u32 s60, s60, s43\n\t" \
    "s_addc_u32 s61, s61, 0\n\t" \
    "v_add_u32 v244, 0xc000, v243\n\t" \
    "s_waitcnt vmcnt(12) lgkmcnt(0)\n\t" \
    "s_barrier \n\t" \
    "s_add_u32 m0, %[wm], 0x9000\n\t" \
    "s_mov_b32 s44, 0x1e0000\n\t" \
    "buffer_load_dwordx4 v240, %[wr], s44 offen offset:0 nt lds\n\t" \
    "buffer_load_dwordx4 v241, %[wr], s44 offen offset:1024 nt lds\n\t" \
    "buffer_load_dwordx4 v242, %[wr], s44 offen offset:2048 nt lds\n\t" \
    "ds_read_b64_tr_b16 v[200:201], v244 offset:0\n\t" \
    "ds_read_b64_tr_b16 v[202:203], v244 offset:1536\n\t" \
    "ds_read_b64_tr_b16 v[0:1], v244 offset:32\n\t" \
    "ds_read_b64_tr_b16 v[2:3], v244 offset:1568\n\t" \
    "ds_read_b64_tr_b16 v[8:9], v244 offset:64\n\t" \
    "ds_read_b64_tr_b16 v[10:11], v244 offset:1600\n\t" \
    "v_add_u32 v244, 0xf000, v243\n\t" \
    "s_waitcnt vmcnt(12) lgkmcnt(0)\n\t" \
    "s_barrier \n\t" \
    "s_add_u32 m0, %[wm], 0xc000\n\t" \
    "s_mov_b32 s44, 0x240000\n\t" \
    "buffer_load_dwordx4 v240, %[wr], s44 offen offset:0 nt lds\n\t" \
    "buffer_load_dwordx4 v241, %[wr], s44 offen offset:1024 nt lds\n\t" \
    "buffer_load_dwordx4 v242, %[wr], s44 offen offset:2048 nt lds\n\t" \
    "ds_read_b64_tr_b16 v[204:205], v244 offset:0\n\t" \
    "ds_read_b64_tr_b16 v[206:207], v244 offset:1536\n\t" \
    "ds_read_b64_tr_b16 v[4:5], v244 offset:32\n\t" \
    "ds_read_b64_tr_b16 v[6:7], v244 offset:1568\n\t" \
    "ds_read_b64_tr_b16 v[12:13], v244 offset:64\n\t" \
    "ds_read_b64_tr_b16 v[14:15], v244 offset:1600\n\t" \
    "v_add_u32 v244, 0x0, v243\n\t" \
    "s_waitcnt vmcnt(12) lgkmcnt(0)\n\t" \
    "s_barrier \n\t" \
    "s_add_u32 m0, %[wm], 0xf000\n\t" \
    "s_mov_b32 s44, 0x2a0000\n\t" \
    "buffer_load_dwordx4 v240, %[wr], s44 offen offset:0 nt lds\n\t" \
    "buffer_load_dwordx4 v241, %[wr], s44 offen offset:1024 nt lds\n\t" \
    "buffer_load_dwordx4 v242, %[wr], s44 offen offset:2048 nt lds\n\t" \
    "ds_read_b64_tr_b16 v[208:209], v244 offset:0\n\t" \
    "ds_read_b64_tr_b16 v[210:211], v244 offset:1536\n\t" \
    "ds_read_b64_tr_b16 v[40:41], v244 offset:32\n\t" \
    "ds_read_b64_tr_b16 v[42:43], v244 offset:1568\n\t" \
    "ds_read_b64_tr_b16 v[48:49], v244 offset:64\n\t" \
    "ds_read_b64_tr_b16 v[50:51], v244 offset:1600\n\t" \
    "v_add_u32 v244, 0x3000, v243\n\t" \
    "s_waitcnt vmcnt(12) lgkmcnt(0)\n\t" \
    "s_barrier \n\t" \
    "s_add_u32 m0, %[wm], 0x0\n\t" \
    "s_mov_b32 s44, 0x300000\n\t" \
    "buffer_load_dwordx4 v240, %[wr], s44 offen offset:0 nt lds\n\t" \
    "buffer_load_dwordx4 v241, %[wr], s44 offen offset:1024 nt lds\n\t" \
    "buffer_load_dwordx4 v242, %[wr], s44 offen offset:2048 nt lds\n\t" \
    "ds_read_b64_tr_b16 v[212:213], v244 offset:0\n\t" \
    "ds_read_b64_tr_b16 v[214:215], v244 offset:1536\n\t" \
    "ds_read_b64_tr_b16 v[44:45], v244 offset:32\n\t" \
    "ds_read_b64_tr_b16 v[46:47], v244 offset:1568\n\t" \
    "ds_read_b64_tr_b16 v[52:53], v244 offset:64\n\t" \
    "ds_read_b64_tr_b16 v[54:55], v244 offset:1600\n\t" \
    "v_add_u32 v244, 0x6000, v243\n\t" \
    "s_waitcnt vmcnt(12) lgkmcnt(0)\n\t" \
    "s_barrier \n\t" \
    "s_add_u32 m0, %[wm], 0x3000\n\t" \
    "s_mov_b32 s44, 0x360000\n\t" \
    "buffer_load_dwordx4 v240, %[wr], s44 offen offset:0 nt lds\n\t" \
    "buffer_load_dwordx4 v241, %[wr], s44 offen offset:1024 nt lds\n\t" \
    "buffer_load_dwordx4 v242, %[wr], s44 offen offset:2048 nt lds\n\t" \
    "ds_read_b64_tr_b16 v[216:217], v244 offset:0\n\t" \
    "ds_read_b64_tr_b16 v[218:219], v244 offset:1536\n\t" \
    "ds_read_b64_tr_b16 v[80:81], v244 offset:32\n\t" \
    "ds_read_b64_tr_b16 v[82:83], v244 offset:1568\n\t" \
    "ds_read_b64_tr_b16 v[88:89], v244 offset:64\n\t" \
    "ds_read_b64_tr_b16 v[90:91], v244 offset:1600\n\t" \
    "v_add_u32 v244, 0x9000, v243\n\t" \
    "s_waitcnt vmcnt(12) lgkmcnt(0)\n\t" \
    "s_barrier \n\t" \
    "s_add_u32 m0, %[wm], 0x6000\n\t" \
    "s_mov_b32 s44, 0x3c0000\n\t" \
    "buffer_load_dwordx4 v240, %[wr], s44 offen offset:0 nt lds\n\t" \
    "buffer_load_dwordx4 v241, %[wr], s44 offen offset:1024 nt lds\n\t" \
    "buffer_load_dwordx4 v242, %[wr], s44 offen offset:2048 nt lds\n\t" \
    "ds_read_b64_tr_b16 v[220:221], v244 offset:0\n\t" \
    "ds_read_b64_tr_b16 v[222:223], v244 offset:1536\n\t" \
    "ds_read_b64_tr_b16 v[84:85], v244 offset:32\n\t" \
    "ds_read_b64_tr_b16 v[86:87], v244 offset:1568\n\t" \
    "ds_read_b64_tr_b16 v[92:93], v244 offset:64\n\t" \
    "ds_read_b64_tr_b16 v[94:95], v244 offset:1600\n\t" \
    "v_add_u32 v244, 0xc000, v243\n\t" \
    "s_waitcnt vmcnt(12) lgkmcnt(0)\n\t" \
    "s_barrier \n\t" \
    "s_add_u32 m0, %[wm], 0x9000\n\t" \
    "s_mov_b32 s44, 0x420000\n\t" \
    "buffer_load_dwordx4 v240, %[wr], s44 offen offset:0 nt lds\n\t" \
    "buffer_load_dwordx4 v241, %[wr], s44 offen offset:1024 nt lds\n\t" \
    "buffer_load_dwordx4 v242, %[wr], s44 offen offset:2048 nt lds\n\t" \
    "ds_read_b64_tr_b16 v[224:225], v244 offset:0\n\t" \
    "ds_read_b64_tr_b16 v[226:227], v244 offset:1536\n\t" \
    "ds_read_b64_tr_b16 v[120:121], v244 offset:32\n\t" \
    "ds_read_b64_tr_b16 v[122:123], v244 offset:1568\n\t" \
    "ds_read_b64_tr_b16 v[128:129], v244 offset:64\n\t" \
    "ds_read_b64_tr_b16 v[130:131], v244 offset:1600\n\t" \
    "v_add_u32 v244, 0xf000, v243\n\t" \
    "s_waitcnt vmcnt(12) lgkmcnt(0)\n\t" \
    "s_barrier \n\t" \
    "s_add_u32 m0, %[wm], 0xc000\n\t" \
    "s_mov_b32 s44, 0x480000\n\t" \
    "buffer_load_dwordx4 v240, %[wr], s44 offen offset:0 nt lds\n\t" \
    "buffer_load_dwordx4 v241, %[wr], s44 offen offset:1024 nt lds\n\t" \
    "buffer_load_dwordx4 v242, %[wr], s44 offen offset:2048 nt lds\n\t" \
    "ds_read_b64_tr_b16 v[228:229], v244 offset:0\n\t" \
    "ds_read_b64_tr_b16 v[230:231], v244 offset:1536\n\t" \
    "ds_read_b64_tr_b16 v[124:125], v244 offset:32\n\t" \
    "ds_read_b64_tr_b16 v[126:127], v244 offset:1568\n\t" \
    "ds_read_b64_tr_b16 v[132:133], v244 offset:64\n\t" \
    "ds_read_b64_tr_b16 v[134:135], v244 offset:1600\n\t" \
    "v_add_u32 v244, 0x0, v243\n\t" \
    "s_waitcnt vmcnt(12) lgkmcnt(0)\n\t" \
    "s_barrier \n\t" \
    "s_add_u32 m0, %[wm], 0xf000\n\t" \
    "s_mov_b32 s44, 0x4e0000\n\t" \
    "buffer_load_dwordx4 v240, %[wr], s44 offen offset:0 nt lds\n\t" \
    "buffer_load_dwordx4 v241, %[wr], s44 offen offset:1024 nt lds\n\t" \
    "buffer_load_dwordx4 v242, %[wr], s44 offen offset:2048 nt lds\n\t" \
    "ds_read_b64_tr_b16 v[232:233], v244 offset:0\n\t" \
    "ds_read_b64_tr_b16 v[234:235], v244 offset:1536\n\t" \
    "ds_read_b64_tr_b16 v[160:161], v244 offset:32\n\t" \
    "ds_read_b64_tr_b16 v[162:163], v244 offset:1568\n\t" \
    "ds_read_b64_tr_b16 v[168:169], v244 offset:64\n\t" \
    "ds_read_b64_tr_b16 v[170:171], v244 offset:1600\n\t" \
    "v_add_u32 v244, 0x3000, v243\n\t" \
    "s_waitcnt vmcnt(12) lgkmcnt(0)\n\t" \
    "s_barrier \n\t" \
    "v_and_b32 v252, 0xf0, v253\n\t" \
    "v_lshlrev_b32 v252, 3, v252\n\t" \
    "buffer_load_dword v252, v252, s[60:63], %[fl] offen sc1\n\t" \
    "s_add_u32 m0, %[wm], 0x0\n\t" \
    "s_mov_b32 s44, 0x540000\n\t" \
    "buffer_load_dwordx4 v240, %[wr], s44 offen offset:0 nt lds\n\t" \
    "buffer_load_dwordx4 v241, %[wr], s44 offen offset:1024 nt lds\n\t" \
    "buffer_load_dwordx4 v242, %[wr], s44 offen offset:2048 nt lds\n\t" \
    "ds_read_b64_tr_b16 v[236:237], v244 offset:0\n\t" \
    "ds_read_b64_tr_b16 v[238:239], v244 offset:1536\n\t" \
    "ds_read_b64_tr_b16 v[164:165], v244 offset:32\n\t" \
    "ds_read_b64_tr_b16 v[166:167], v244 offset:1568\n\t" \
    "ds_read_b64_tr_b16 v[172:173], v244 offset:64\n\t" \
    "ds_read_b64_tr_b16 v[174:175], v244 offset:1600\n\t" \
    "v_add_u32 v244, 0x6000, v243\n\t" \
    "s_waitcnt vmcnt(12) lgkmcnt(0)\n\t" \
    "s_barrier \n\t" \
    "s_add_u32 m0, %[wm], 0x3000\n\t" \
    "s_mov_b32 s44, 0x5a0000\n\t" \
    "buffer_load_dwordx4 v240, %[wr], s44 offen offset:0 nt lds\n\t" \
    "buffer_load_dwordx4 v241, %[wr], s44 offen offset:1024 nt lds\n\t" \
    "buffer_load_dwordx4 v242, %[wr], s44 offen offset:2048 nt lds\n\t" \
    "ds_read_b64_tr_b16 v[16:17], v244 offset:0\n\t" \
    "ds_read_b64_tr_b16 v[18:19], v244 offset:1536\n\t" \
    "ds_read_b64_tr_b16 v[24:25], v244 offset:32\n\t" \
    "ds_read_b64_tr_b16 v[26:27], v244 offset:1568\n\t" \
    "ds_read_b64_tr_b16 v[32:33], v244 offset:64\n\t" \
    "ds_read_b64_tr_b16 v[34:35], v244 offset:1600\n\t" \
    "v_add_u32 v244, 0x9000, v243\n\t" \
    "s_waitcnt vmcnt(12) lgkmcnt(0)\n\t" \
    "s_barrier \n\t" \
    "s_add_u32 m0, %[wm], 0x6000\n\t" \
    "s_mov_b32 s44, 0x600000\n\t" \
    "buffer_load_dwordx4 v240, %[wr], s44 offen offset:0 nt lds\n\t" \
    "buffer_load_dwordx4 v241, %[wr], s44 offen offset:1024 nt lds\n\t" \
    "buffer_load_dwordx4 v242, %[wr], s44 offen offset:2048 nt lds\n\t" \
    "ds_read_b64_tr_b16 v[20:21], v244 offset:0\n\t" \
    "ds_read_b64_tr_b16 v[22:23], v244 offset:1536\n\t" \
    "ds_read_b64_tr_b16 v[28:29], v244 offset:32\n\t" \
    "ds_read_b64_tr_b16 v[30:31], v244 offset:1568\n\t" \
    "ds_read_b64_tr_b16 v[36:37], v244 offset:64\n\t" \
    "ds_read_b64_tr_b16 v[38:39], v244 offset:1600\n\t" \
    "v_add_u32 v244, 0xc000, v243\n\t" \
    "s_waitcnt vmcnt(12) lgkmcnt(0)\n\t" \
    "s_barrier \n\t" \
    "s_add_u32 m0, %[wm], 0x9000\n\t" \
    "s_mov_b32 s44, 0x660000\n\t" \
    "buffer_load_dwordx4 v240, %[wr], s44 offen offset:0 nt lds\n\t" \
    "buffer_load_dwordx4 v241, %[wr], s44 offen offset:1024 nt lds\n\t" \
    "buffer_load_dwordx4 v242, %[wr], s44 offen offset:2048 nt lds\n\t" \
    "ds_read_b64_tr_b16 v[56:57], v244 offset:0\n\t" \
    "ds_read_b64_tr_b16 v[58:59], v244 offset:1536\n\t" \
    "ds_read_b64_tr_b16 v[64:65], v244 offset:32\n\t" \
    "ds_read_b64_tr_b16 v[66:67], v244 offset:1568\n\t" \
    "ds_read_b64_tr_b16 v[72:73], v244 offset:64\n\t" \
    "ds_read_b64_tr_b16 v[74:75], v244 offset:1600\n\t" \
    "v_add_u32 v244, 0xf000, v243\n\t" \
    "s_waitcnt vmcnt(12) lgkmcnt(0)\n\t" \
    "s_barrier \n\t" \
    "s_add_u32 m0, %[wm], 0xc000\n\t" \
    "s_mov_b32 s44, 0x6c0000\n\t" \
    "buffer_load_dwordx4 v240, %[wr], s44 offen offset:0 nt lds\n\t" \
    "buffer_load_dwordx4 v241, %[wr], s44 offen offset:1024 nt lds\n\t" \
    "buffer_load_dwordx4 v242, %[wr], s44 offen offset:2048 nt lds\n\t" \
    "ds_read_b64_tr_b16 v[60:61], v244 offset:0\n\t" \
    "ds_read_b64_tr_b16 v[62:63], v244 offset:1536\n\t" \
    "ds_read_b64_tr_b16 v[68:69], v244 offset:32\n\t" \
    "ds_read_b64_tr_b16 v[70:71], v244 offset:1568\n\t" \
    "ds_read_b64_tr_b16 v[76:77], v244 offset:64\n\t" \
    "ds_read_b64_tr_b16 v[78:79], v244 offset:1600\n\t" \
    "v_add_u32 v244, 0x0, v243\n\t" \
    "s_waitcnt vmcnt(12) lgkmcnt(0)\n\t" \
    "s_barrier \n\t" \
    "s_add_u32 m0, %[wm], 0xf000\n\t" \
    "s_mov_b32 s44, 0x720000\n\t" \
    "buffer_load_dwordx4 v240, %[wr], s44 offen offset:0 nt lds\n\t" \
    "buffer_load_dwordx4 v241, %[wr], s44 offen offset:1024 nt lds\n\t" \
    "buffer_load_dwordx4 v242, %[wr], s44 offen offset:2048 nt lds\n\t" \
    "ds_read_b64_tr_b16 v[96:97], v244 offset:0\n\t" \
    "ds_read_b64_tr_b16 v[98:99], v244 offset:1536\n\t" \
    "ds_read_b64_tr_b16 v[104:105], v244 offset:32\n\t" \
    "ds_read_b64_tr_b16 v[106:107], v244 offset:1568\n\t" \
    "ds_read_b64_tr_b16 v[112:113], v244 offset:64\n\t" \
    "ds_read_b64_tr_b16 v[114:115], v244 offset:1600\n\t" \
    "v_add_u32 v244, 0x3000, v243\n\t" \
    "s_waitcnt vmcnt(12) lgkmcnt(0)\n\t" \
    "s_barrier \n\t" \
    "ds_read_b64_tr_b16 v[100:101], v244 offset:0\n\t" \
    "ds_read_b64_tr_b16 v[102:103], v244 offset:1536\n\t" \
    "ds_read_b64_tr_b16 v[108:109], v244 offset:32\n\t" \
    "ds_read_b64_tr_b16 v[110:111], v244 offset:1568\n\t" \
    "ds_read_b64_tr_b16 v[116:117], v244 offset:64\n\t" \
    "ds_read_b64_tr_b16 v[118:119], v244 offset:1600\n\t" \
    "v_add_u32 v244, 0x6000, v243\n\t" \
    "s_waitcnt vmcnt(9) lgkmcnt(0)\n\t" \
    "s_barrier \n\t" \
    "ds_read_b64_tr_b16 v[136:137], v244 offset:0\n\t" \
    "ds_read_b64_tr_b16 v[138:139], v244 offset:1536\n\t" \
    "ds_read_b64_tr_b16 v[144:145], v244 offset:32\n\t" \
    "ds_read_b64_tr_b16 v[146:147], v244 offset:1568\n\t" \
    "ds_read_b64_tr_b16 v[152:153], v244 offset:64\n\t" \
    "ds_read_b64_tr_b16 v[154:155], v244 offset:1600\n\t" \
    "v_add_u32 v244, 0x9000, v243\n\t" \
    "s_waitcnt vmcnt(6) lgkmcnt(0)\n\t" \
    "s_barrier \n\t" \
    "ds_read_b64_tr_b16 v[140:141], v244 offset:0\n\t" \
    "ds_read_b64_tr_b16 v[142:143], v244 offset:1536\n\t" \
    "ds_read_b64_tr_b16 v[148:149], v244 offset:32\n\t" \
    "ds_read_b64_tr_b16 v[150:151], v244 offset:1568\n\t" \
    "ds_read_b64_tr_b16 v[156:157], v244 offset:64\n\t" \
    "ds_read_b64_tr_b16 v[158:159], v244 offset:1600\n\t" \
    "v_add_u32 v244, 0xc000, v243\n\t" \
    "s_waitcnt vmcnt(3) lgkmcnt(0)\n\t" \
    "s_barrier \n\t" \
    "ds_read_b64_tr_b16 v[176:177], v244 offset:0\n\t" \
    "ds_read_b64_tr_b16 v[178:179], v244 offset:1536\n\t" \
    "ds_read_b64_tr_b16 v[184:185], v244 offset:32\n\t" \
    "ds_read_b64_tr_b16 v[186:187], v244 offset:1568\n\t" \
    "ds_read_b64_tr_b16 v[192:193], v244 offset:64\n\t" \
    "ds_read_b64_tr_b16 v[194:195], v244 offset:1600\n\t" \
    "s_mov_b32 s43, 0\n\t" \
    "v_cmp_eq_u32 vcc, 0, v252\n\t" \
    "s_cbranch_vccnz .Lv01_%=\n\t" \
    "s_movk_i32 s45, 0x0\n\t" \
    "s_mov_b32 m0, s45\n\t" \
    "s_nop 0\n\t" \
    "buffer_load_dwordx4 v253, s[60:63], s42 offen offset:0 sc1 lds\n\t" \
    "buffer_load_dwordx4 v253, s[60:63], s42 offen offset:1024 sc1 lds\n\t" \
    "buffer_load_dwordx4 v253, s[60:63], s42 offen offset:2048 sc1 lds\n\t" \
    "buffer_load_dwordx4 v253, s[60:63], s42 offen offset:3072 sc1 lds\n\t" \
    "s_add_u32 s44, s42, 0x1000\n\t" \
    "s_movk_i32 s45, 0x5000\n\t" \
    "s_mov_b32 m0, s45\n\t" \
    "s_nop 0\n\t" \
    "buffer_load_dwordx4 v253, s[60:63], s44 offen offset:0 sc1 lds\n\t" \
    "buffer_load_dwordx4 v253, s[60:63], s44 offen offset:1024 sc1 lds\n\t" \
    "buffer_load_dwordx4 v253, s[60:63], s44 offen offset:2048 sc1 lds\n\t" \
    "buffer_load_dwordx4 v253, s[60:63], s44 offen offset:3072 sc1 lds\n\t" \
    "s_add_u32 s44, s42, 0x10000\n\t" \
    "s_mov_b32 s45, 0x4000\n\t" \
    "s_mov_b32 m0, s45\n\t" \
    "s_nop 0\n\t" \
    "buffer_load_dwordx4 v253, s[60:63], s44 offen offset:0 sc1 lds\n\t" \
    "s_add_u32 s44, s44, 0x1000\n\t" \
    "s_mov_b32 s45, 0x9000\n\t" \
    "s_mov_b32 m0, s45\n\t" \
    "s_nop 0\n\t" \
    "buffer_load_dwordx4 v253, s[60:63], s44 offen offset:0 sc1 lds\n\t" \
    "s_mov_b32 s43, 1\n\t" \
    ".Lv01_%=:\n\t" \
    "v_add_u32 v244, 0xf000, v243\n\t" \
    "s_waitcnt vmcnt(0) lgkmcnt(0)\n\t" \
    "s_barrier \n\t" \
    "ds_read_b64_tr_b16 v[180:181], v244 offset:0\n\t" \
    "ds_read_b64_tr_b16 v[182:183], v244 offset:1536\n\t" \
    "ds_read_b64_tr_b16 v[188:189], v244 offset:32\n\t" \
    "ds_read_b64_tr_b16 v[190:191], v244 offset:1568\n\t" \
    "ds_read_b64_tr_b16 v[196:197], v244 offset:64\n\t" \
    "ds_read_b64_tr_b16 v[198:199], v244 offset:1600\n\t" \
    "s_waitcnt lgkmcnt(0)\n\t" \
    "s_barrier \n\t" \
    "ds_write_b128 v254, v[200:203] offset:30720\n\t" \
    "ds_write_b128 v254, v[204:207] offset:31744\n\t" \
    "ds_write_b128 v254, v[208:211] offset:32768\n\t" \
    "ds_write_b128 v254, v[212:215] offset:33792\n\t" \
    "ds_write_b128 v254, v[216:219] offset:34816\n\t" \
    "ds_write_b128 v254, v[220:223] offset:35840\n\t" \
    "ds_write_b128 v254, v[224:227] offset:36864\n\t" \
    "ds_write_b128 v254, v[228:231] offset:37888\n\t" \
    "ds_write_b128 v254, v[232:235] offset:38912\n\t" \
    "ds_write_b128 v254, v[236:239] offset:39936\n\t" \
    "s_cmp_lg_u32 s43, 0\n\t" \
    "s_cbranch_scc1 .Lv02_%=\n\t" \
    "s_mov_b32 s44, 0\n\t" \
    ".Lv03_%=:\n\t" \
    "v_cmp_ge_u32 vcc, s44, v252\n\t" \
    "s_cbranch_vccz .Lv04_%=\n\t" \
    "s_cmp_eq_u32 s41, 0\n\t" \
    "s_cbranch_scc1 .Lv04_%=\n\t" \
    "s_sub_u32 s41, s41, 1\n\t" \
    "s_sleep 8\n\t" \
    "v_and_b32 v252, 0xf0, v253\n\t" \
    "v_lshlrev_b32 v252, 3, v252\n\t" \
    "buffer_load_dword v252, v252, s[60:63], %[fl] offen sc1\n\t" \
    "s_waitcnt vmcnt(0)\n\t" \
    "s_branch .Lv03_%=\n\t" \
    ".Lv04_%=:\n\t" \
    "s_movk_i32 s45, 0x0\n\t" \
    "s_mov_b32 m0, s45\n\t" \
    "s_nop 0\n\t" \
    "buffer_load_dwordx4 v253, s[60:63], s42 offen offset:0 sc1 lds\n\t" \
    "buffer_load_dwordx4 v253, s[60:63], s42 offen offset:1024 sc1 lds\n\t" \
    "buffer_load_dwordx4 v253, s[60:63], s42 offen offset:2048 sc1 lds\n\t" \
    "buffer_load_dwordx4 v253, s[60:63], s42 offen offset:3072 sc1 lds\n\t" \
    "s_add_u32 s44, s42, 0x1000\n\t" \
    "s_movk_i32 s45, 0x5000\n\t" \
    "s_mov_b32 m0, s45\n\t" \
    "s_nop 0\n\t" \
    "buffer_load_dwordx4 v253, s[60:63], s44 offen offset:0 sc1 lds\n\t" \
    "buffer_load_dwordx4 v253, s[60:63], s44 offen offset:1024 sc1 lds\n\t" \
    "buffer_load_dwordx4 v253, s[60:63], s44 offen offset:2048 sc1 lds\n\t" \
    "buffer_load_dwordx4 v253, s[60:63], s44 offen offset:3072 sc1 lds\n\t" \
    "s_add_u32 s44, s42, 0x10000\n\t" \
    "s_mov_b32 s45, 0x4000\n\t" \
    "s_mov_b32 m0, s45\n\t" \
    "s_nop 0\n\t" \
    "buffer_load_dwordx4 v253, s[60:63], s44 offen offset:0 sc1 lds\n\t" \
    "s_add_u32 s44, s44, 0x1000\n\t" \
    "s_mov_b32 s45, 0x9000\n\t" \
    "s_mov_b32 m0, s45\n\t" \
    "s_nop 0\n\t" \
    "buffer_load_dwordx4 v253, s[60:63], s44 offen offset:0 sc1 lds\n\t" \
    ".Lv02_%=:\n\t" \
    "s_waitcnt vmcnt(0) lgkmcnt(0)\n\t" \
    "s_barrier \n\t" \
    "ds_read_b128 v[224:227], v253 offset:0\n\t" \
    "ds_read_b128 v[244:247], v254 offset:30720\n\t" \
    "ds_read_b128 v[228:231], v253 offset:1024\n\t" \
    "ds_read_b128 v[232:235], v253 offset:20480\n\t" \
    "ds_read_b128 v[236:239], v253 offset:21504\n\t" \
    "ds_read_b128 v[248:251], v254 offset:31744\n\t" \
    ".Lv05_%=:\n\t" \
    LST7 \
    "s_waitcnt lgkmcnt(5)\n\t" \
    "v_mfma_f32_16x16x32_bf16 v[204:207], v[0:3], v[224:227], 0\n\t" \
    "s_waitcnt lgkmcnt(3)\n\t" \
    "v_mfma_f32_16x16x32_bf16 v[204:207], v[4:7], v[228:231], v[204:207]\n\t" \
    "ds_read_b128 v[240:243], v253 offset:2048\n\t" \
    "v_mfma_f32_16x16x32_bf16 v[208:211], v[8:11], v[224:227], 0\n\t" \
    "v_mfma_f32_16x16x32_bf16 v[208:211], v[12:15], v[228:231], v[208:211]\n\t" \
    "v_mfma_f32_16x16x32_bf16 v[200:203], v[244:247], v[224:227], 0\n\t" \
    "s_waitcnt lgkmcnt(1)\n\t" \
    "v_mfma_f32_16x16x32_bf16 v[200:203], v[248:251], v[228:231], v[200:203]\n\t" \
    "v_mfma_f32_16x16x32_bf16 v[216:219], v[0:3], v[232:235], 0\n\t" \
    "ds_read_b128 v[224:227], v253 offset:3072\n\t" \
    "v_mfma_f32_16x16x32_bf16 v[216:219], v[4:7], v[236:239], v[216:219]\n\t" \
    "ds_read_b128 v[228:231], v253 offset:22528\n\t" \
    "v_mfma_f32_16x16x32_bf16 v[220:223], v[8:11], v[232:235], 0\n\t" \
    "v_mfma_f32_16x16x32_bf16 v[220:223], v[12:15], v[236:239], v[220:223]\n\t" \
    "v_mfma_f32_16x16x32_bf16 v[212:215], v[244:247], v[232:235], 0\n\t" \
    "ds_read_b128 v[244:247], v254 offset:32768\n\t" \
    "v_mfma_f32_16x16x32_bf16 v[212:215], v[248:251], v[236:239], v[212:215]\n\t" \
    "ds_read_b128 v[248:251], v254 offset:33792\n\t" \
    "s_waitcnt lgkmcnt(4)\n\t" \
    "v_mfma_f32_16x16x32_bf16 v[204:207], v[40:43], v[240:243], v[204:207]\n\t" \
    "ds_read_b128 v[232:235], v253 offset:23552\n\t" \
    "s_waitcnt lgkmcnt(4)\n\t" \
    "v_mfma_f32_16x16x32_bf16 v[204:207], v[44:47], v[224:227], v[204:207]\n\t" \
    "ds_read_b128 v[236:239], v253 offset:4096\n\t" \
    "v_mfma_f32_16x16x32_bf16 v[208:211], v[48:51], v[240:243], v[208:211]\n\t" \
    "v_mfma_f32_16x16x32_bf16 v[208:211], v[52:55], v[224:227], v[208:211]\n\t" \
    "s_waitcnt lgkmcnt(3)\n\t" \
    "v_mfma_f32_16x16x32_bf16 v[200:203], v[244:247], v[240:243], v[200:203]\n\t" \
    "s_waitcnt lgkmcnt(2)\n\t" \
    "v_mfma_f32_16x16x32_bf16 v[200:203], v[248:251], v[224:227], v[200:203]\n\t" \
    "v_mfma_f32_16x16x32_bf16 v[216:219], v[40:43], v[228:231], v[216:219]\n\t" \
    "ds_read_b128 v[240:243], v253 offset:5120\n\t" \
    "s_waitcnt lgkmcnt(2)\n\t" \
    "v_mfma_f32_16x16x32_bf16 v[216:219], v[44:47], v[232:235], v[216:219]\n\t" \
    "ds_read_b128 v[224:227], v253 offset:24576\n\t" \
    "v_mfma_f32_16x16x32_bf16 v[220:223], v[48:51], v[228:231], v[220:223]\n\t" \
    "v_mfma_f32_16x16x32_bf16 v[220:223], v[52:55], v[232:235], v[220:223]\n\t" \
    "v_mfma_f32_16x16x32_bf16 v[212:215], v[244:247], v[228:231], v[212:215]\n\t" \
    "ds_read_b128 v[244:247], v254 offset:34816\n\t" \
    "v_mfma_f32_16x16x32_bf16 v[212:215], v[248:251], v[232:235], v[212:215]\n\t" \
    "ds_read_b128 v[248:251], v254 offset:35840\n\t" \
    "s_waitcnt lgkmcnt(4)\n\t" \
    "v_mfma_f32_16x16x32_bf16 v[204:207], v[80:83], v[236:239], v[204:207]\n\t" \
    "ds_read_b128 v[228:231], v253 offset:25600\n\t" \
    "s_add_u32 s44, s40, 1\n\t" \
    "s_waitcnt lgkmcnt(4)\n\t" \
    "v_mfma_f32_16x16x32_bf16 v[204:207], v[84:87], v[240:243], v[204:207]\n\t" \
    "ds_read_b128 v[232:235], v253 offset:6144\n\t" \
    "s_and_b32 s45, s44, 15\n\t" \
    "v_mfma_f32_16x16x32_bf16 v[208:211], v[88:91], v[236:239], v[208:211]\n\t" \
    "s_cmp_eq_u32 s45, 0\n\t" \
    "v_mfma_f32_16x16x32_bf16 v[208:211], v[92:95], v[240:243], v[208:211]\n\t" \
    "s_cselect_b32 s45, %[cj], %[cs]\n\t" \
    "s_waitcnt lgkmcnt(3)\n\t" \
    "v_mfma_f32_16x16x32_bf16 v[200:203], v[244:247], v[236:239], v[200:203]\n\t" \
    "s_cmp_eq_u32 s44, 64\n\t" \
    "s_waitcnt lgkmcnt(2)\n\t" \
    "v_mfma_f32_16x16x32_bf16 v[200:203], v[248:251], v[240:243], v[200:203]\n\t" \
    "s_cselect_b32 s45, 0, s45\n\t" \
    "v_mfma_f32_16x16x32_bf16 v[216:219], v[80:83], v[224:227], v[216:219]\n\t" \
    "ds_read_b128 v[236:239], v253 offset:7168\n\t" \
    "s_add_u32 s42, s42, s45\n\t" \
    "s_waitcnt lgkmcnt(2)\n\t" \
    "v_mfma_f32_16x16x32_bf16 v[216:219], v[84:87], v[228:231], v[216:219]\n\t" \
    "ds_read_b128 v[240:243], v253 offset:26624\n\t" \
    "v_mfma_f32_16x16x32_bf16 v[220:223], v[88:91], v[224:227], v[220:223]\n\t" \
    "v_mfma_f32_16x16x32_bf16 v[220:223], v[92:95], v[228:231], v[220:223]\n\t" \
    "v_mfma_f32_16x16x32_bf16 v[212:215], v[244:247], v[224:227], v[212:215]\n\t" \
    "ds_read_b128 v[244:247], v254 offset:36864\n\t" \
    "v_mfma_f32_16x16x32_bf16 v[212:215], v[248:251], v[228:231], v[212:215]\n\t" \
    "ds_read_b128 v[248:251], v254 offset:37888\n\t" \
    "s_waitcnt lgkmcnt(4)\n\t" \
    "v_mfma_f32_16x16x32_bf16 v[204:207], v[120:123], v[232:235], v[204:207]\n\t" \
    "ds_read_b128 v[224:227], v253 offset:27648\n\t" \
    "s_waitcnt lgkmcnt(4)\n\t" \
    "v_mfma_f32_16x16x32_bf16 v[204:207], v[124:127], v[236:239], v[204:207]\n\t" \
    "ds_read_b128 v[228:231], v253 offset:8192\n\t" \
    "v_mfma_f32_16x16x32_bf16 v[208:211], v[128:131], v[232:235], v[208:211]\n\t" \
    "v_mfma_f32_16x16x32_bf16 v[208:211], v[132:135], v[236:239], v[208:211]\n\t" \
    "s_waitcnt lgkmcnt(3)\n\t" \
    "v_mfma_f32_16x16x32_bf16 v[200:203], v[244:247], v[232:235], v[200:203]\n\t" \
    "s_waitcnt lgkmcnt(2)\n\t" \
    "v_mfma_f32_16x16x32_bf16 v[200:203], v[248:251], v[236:239], v[200:203]\n\t" \
    "v_mfma_f32_16x16x32_bf16 v[216:219], v[120:123], v[240:243], v[216:219]\n\t" \
    "ds_read_b128 v[232:235], v253 offset:9216\n\t" \
    "s_waitcnt lgkmcnt(2)\n\t" \
    "v_mfma_f32_16x16x32_bf16 v[216:219], v[124:127], v[224:227], v[216:219]\n\t" \
    "ds_read_b128 v[236:239], v253 offset:28672\n\t" \
    "v_mfma_f32_16x16x32_bf16 v[220:223], v[128:131], v[240:243], v[220:223]\n\t" \
    "v_mfma_f32_16x16x32_bf16 v[220:223], v[132:135], v[224:227], v[220:223]\n\t" \
    "v_mfma_f32_16x16x32_bf16 v[212:215], v[244:247], v[240:243], v[212:215]\n\t" \
    "ds_read_b128 v[244:247], v254 offset:38912\n\t" \
    "v_mfma_f32_16x16x32_bf16 v[212:215], v[248:251], v[224:227], v[212:215]\n\t" \
    "ds_read_b128 v[248:251], v254 offset:39936\n\t" \
    "s_waitcnt lgkmcnt(4)\n\t" \
    "v_mfma_f32_16x16x32_bf16 v[204:207], v[160:163], v[228:231], v[204:207]\n\t" \
    "ds_read_b128 v[240:243], v253 offset:29696\n\t" \
    "s_waitcnt lgkmcnt(4)\n\t" \
    "v_mfma_f32_16x16x32_bf16 v[204:207], v[164:167], v[232:235], v[204:207]\n\t" \
    "ds_read_b128 v[224:227], v253 offset:10240\n\t" \
    "v_mfma_f32_16x16x32_bf16 v[208:211], v[168:171], v[228:231], v[208:211]\n\t" \
    "v_mfma_f32_16x16x32_bf16 v[208:211], v[172:175], v[232:235], v[208:211]\n\t" \
    "s_waitcnt lgkmcnt(3)\n\t" \
    "v_mfma_f32_16x16x32_bf16 v[200:203], v[244:247], v[228:231], v[200:203]\n\t" \
    "s_waitcnt lgkmcnt(2)\n\t" \
    "v_mfma_f32_16x16x32_bf16 v[200:203], v[248:251], v[232:235], v[200:203]\n\t" \
    "v_mfma_f32_16x16x32_bf16 v[216:219], v[160:163], v[236:239], v[216:219]\n\t" \
    "ds_read_b128 v[228:231], v253 offset:11264\n\t" \
    "s_waitcnt lgkmcnt(2)\n\t" \
    "v_mfma_f32_16x16x32_bf16 v[216:219], v[164:167], v[240:243], v[216:219]\n\t" \
    "ds_read_b128 v[232:235], v253 offset:30720\n\t" \
    "v_mfma_f32_16x16x32_bf16 v[220:223], v[168:171], v[236:239], v[220:223]\n\t" \
    "v_mfma_f32_16x16x32_bf16 v[220:223], v[172:175], v[240:243], v[220:223]\n\t" \
    "v_mfma_f32_16x16x32_bf16 v[212:215], v[244:247], v[236:239], v[212:215]\n\t" \
    "v_mfma_f32_16x16x32_bf16 v[212:215], v[248:251], v[240:243], v[212:215]\n\t" \
    "s_waitcnt lgkmcnt(2)\n\t" \
    "v_mfma_f32_16x16x32_bf16 v[204:207], v[24:27], v[224:227], v[204:207]\n\t" \
    "ds_read_b128 v[236:239], v253 offset:31744\n\t" \
    "s_and_b32 s44, s40, 3\n\t" \
    "s_waitcnt lgkmcnt(2)\n\t" \
    "v_mfma_f32_16x16x32_bf16 v[204:207], v[28:31], v[228:231], v[204:207]\n\t" \
    "ds_read_b128 v[240:243], v253 offset:12288\n\t" \
    "s_lshl_b32 s44, s44, 17\n\t" \
    "v_mfma_f32_16x16x32_bf16 v[208:211], v[32:35], v[224:227], v[208:211]\n\t" \
    "s_add_u32 s44, s44, %[mb]\n\t" \
    "v_mfma_f32_16x16x32_bf16 v[208:211], v[36:39], v[228:231], v[208:211]\n\t" \
    "s_add_u32 s46, s44, 0x0\n\t" \
    "v_mfma_f32_16x16x32_bf16 v[200:203], v[16:19], v[224:227], v[200:203]\n\t" \
    "s_add_u32 s47, s44, 0x1400\n\t" \
    "v_mfma_f32_16x16x32_bf16 v[200:203], v[20:23], v[228:231], v[200:203]\n\t" \
    "s_add_u32 s48, s44, 0x400\n\t" \
    "s_waitcnt lgkmcnt(2)\n\t" \
    "v_mfma_f32_16x16x32_bf16 v[216:219], v[24:27], v[232:235], v[216:219]\n\t" \
    "ds_read_b128 v[224:227], v253 offset:13312\n\t" \
    "s_waitcnt lgkmcnt(2)\n\t" \
    "v_mfma_f32_16x16x32_bf16 v[216:219], v[28:31], v[236:239], v[216:219]\n\t" \
    "ds_read_b128 v[228:231], v253 offset:32768\n\t" \
    "v_mfma_f32_16x16x32_bf16 v[220:223], v[32:35], v[232:235], v[220:223]\n\t" \
    "v_mfma_f32_16x16x32_bf16 v[220:223], v[36:39], v[236:239], v[220:223]\n\t" \
    "v_mfma_f32_16x16x32_bf16 v[212:215], v[16:19], v[232:235], v[212:215]\n\t" \
    "v_mfma_f32_16x16x32_bf16 v[212:215], v[20:23], v[236:239], v[212:215]\n\t" \
    "s_waitcnt lgkmcnt(2)\n\t" \
    "v_mfma_f32_16x16x32_bf16 v[204:207], v[64:67], v[240:243], v[204:207]\n\t" \
    "ds_read_b128 v[232:235], v253 offset:33792\n\t" \
    "s_waitcnt lgkmcnt(2)\n\t" \
    "v_mfma_f32_16x16x32_bf16 v[204:207], v[68:71], v[224:227], v[204:207]\n\t" \
    "ds_read_b128 v[236:239], v253 offset:14336\n\t" \
    "v_mfma_f32_16x16x32_bf16 v[208:211], v[72:75], v[240:243], v[208:211]\n\t" \
    "v_mfma_f32_16x16x32_bf16 v[208:211], v[76:79], v[224:227], v[208:211]\n\t" \
    "v_mfma_f32_16x16x32_bf16 v[200:203], v[56:59], v[240:243], v[200:203]\n\t" \
    "v_mfma_f32_16x16x32_bf16 v[200:203], v[60:63], v[224:227], v[200:203]\n\t" \
    "s_waitcnt lgkmcnt(2)\n\t" \
    "v_mfma_f32_16x16x32_bf16 v[216:219], v[64:67], v[228:231], v[216:219]\n\t" \
    "ds_read_b128 v[240:243], v253 offset:15360\n\t" \
    "s_waitcnt lgkmcnt(2)\n\t" \
    "v_mfma_f32_16x16x32_bf16 v[216:219], v[68:71], v[232:235], v[216:219]\n\t" \
    "ds_read_b128 v[224:227], v253 offset:34816\n\t" \
    "v_mfma_f32_16x16x32_bf16 v[220:223], v[72:75], v[228:231], v[220:223]\n\t" \
    "v_mfma_f32_16x16x32_bf16 v[220:223], v[76:79], v[232:235], v[220:223]\n\t" \
    "v_mfma_f32_16x16x32_bf16 v[212:215], v[56:59], v[228:231], v[212:215]\n\t" \
    "v_mfma_f32_16x16x32_bf16 v[212:215], v[60:63], v[232:235], v[212:215]\n\t" \
    "v_and_b32 v252, 0xf0, v253\n\t" \
    "v_lshlrev_b32 v252, 3, v252\n\t" \
    "buffer_load_dword v252, v252, s[60:63], %[fl] offen sc1\n\t" \
    "s_waitcnt lgkmcnt(2)\n\t" \
    "v_mfma_f32_16x16x32_bf16 v[204:207], v[104:107], v[236:239], v[204:207]\n\t" \
    "ds_read_b128 v[228:231], v253 offset:35840\n\t" \
    "s_waitcnt lgkmcnt(2)\n\t" \
    "v_mfma_f32_16x16x32_bf16 v[204:207], v[108:111], v[240:243], v[204:207]\n\t" \
    "ds_read_b128 v[232:235], v253 offset:16384\n\t" \
    "v_mfma_f32_16x16x32_bf16 v[208:211], v[112:115], v[236:239], v[208:211]\n\t" \
    "v_mfma_f32_16x16x32_bf16 v[208:211], v[116:119], v[240:243], v[208:211]\n\t" \
    "v_mfma_f32_16x16x32_bf16 v[200:203], v[96:99], v[236:239], v[200:203]\n\t" \
    "v_mfma_f32_16x16x32_bf16 v[200:203], v[100:103], v[240:243], v[200:203]\n\t" \
    "s_waitcnt lgkmcnt(2)\n\t" \
    "v_mfma_f32_16x16x32_bf16 v[216:219], v[104:107], v[224:227], v[216:219]\n\t" \
    "ds_read_b128 v[236:239], v253 offset:17408\n\t" \
    "s_waitcnt lgkmcnt(2)\n\t" \
    "v_mfma_f32_16x16x32_bf16 v[216:219], v[108:111], v[228:231], v[216:219]\n\t" \
    "ds_read_b128 v[240:243], v253 offset:18432\n\t" \
    "v_mfma_f32_16x16x32_bf16 v[220:223], v[112:115], v[224:227], v[220:223]\n\t" \
    "v_mfma_f32_16x16x32_bf16 v[220:223], v[116:119], v[228:231], v[220:223]\n\t" \
    "v_mfma_f32_16x16x32_bf16 v[212:215], v[96:99], v[224:227], v[212:215]\n\t" \
    "v_mfma_f32_16x16x32_bf16 v[212:215], v[100:103], v[228:231], v[212:215]\n\t" \
    "s_waitcnt lgkmcnt(2)\n\t" \
    "v_mfma_f32_16x16x32_bf16 v[204:207], v[144:147], v[232:235], v[204:207]\n\t" \
    "ds_read_b128 v[224:227], v253 offset:19456\n\t" \
    "v_mov_b64 v[244:245], 0\n\t" \
    "s_waitcnt lgkmcnt(2)\n\t" \
    "v_mfma_f32_16x16x32_bf16 v[204:207], v[148:151], v[236:239], v[204:207]\n\t" \
    "ds_read_b128 v[228:231], v253 offset:36864\n\t" \
    "v_mov_b64 v[246:247], 0\n\t" \
    "v_mfma_f32_16x16x32_bf16 v[208:211], v[152:155], v[232:235], v[208:211]\n\t" \
    "v_mov_b64 v[248:249], 0\n\t" \
    "v_mfma_f32_16x16x32_bf16 v[208:211], v[156:159], v[236:239], v[208:211]\n\t" \
    "v_mov_b64 v[250:251], 0\n\t" \
    "v_mfma_f32_16x16x32_bf16 v[200:203], v[136:139], v[232:235], v[200:203]\n\t" \
    "v_mfma_f32_16x16x32_bf16 v[200:203], v[140:143], v[236:239], v[200:203]\n\t" \
    "s_waitcnt lgkmcnt(2)\n\t" \
    "v_mfma_f32_16x16x32_bf16 v[204:207], v[184:187], v[240:243], v[204:207]\n\t" \
    "ds_read_b128 v[232:235], v253 offset:37888\n\t" \
    "s_waitcnt lgkmcnt(2)\n\t" \
    "v_mfma_f32_16x16x32_bf16 v[204:207], v[188:191], v[224:227], v[204:207]\n\t" \
    "ds_read_b128 v[236:239], v253 offset:38912\n\t" \
    "v_mfma_f32_16x16x32_bf16 v[208:211], v[192:195], v[240:243], v[208:211]\n\t" \
    "v_mfma_f32_16x16x32_bf16 v[208:211], v[196:199], v[224:227], v[208:211]\n\t" \
    "v_mfma_f32_16x16x32_bf16 v[200:203], v[176:179], v[240:243], v[200:203]\n\t" \
    "v_mfma_f32_16x16x32_bf16 v[200:203], v[180:183], v[224:227], v[200:203]\n\t" \
    "ds_read_b128 v[240:243], v253 offset:39936\n\t" \
    "s_waitcnt lgkmcnt(3)\n\t" \
    "v_mfma_f32_16x16x32_bf16 v[216:219], v[144:147], v[228:231], v[216:219]\n\t" \
    "s_waitcnt lgkmcnt(2)\n\t" \
    "v_mfma_f32_16x16x32_bf16 v[216:219], v[148:151], v[232:235], v[216:219]\n\t" \
    "v_cvt_pk_bf16_f32 v204, v204, v205\n\t" \
    "v_cvt_pk_bf16_f32 v205, v206, v207\n\t" \
    "s_waitcnt lgkmcnt(1)\n\t" \
    "v_mfma_f32_16x16x32_bf16 v[216:219], v[184:187], v[236:239], v[216:219]\n\t" \
    "ds_write_b64 v254, v[204:205] offset:7168\n\t" \
    "v_cvt_pk_bf16_f32 v208, v208, v209\n\t" \
    "s_waitcnt lgkmcnt(1)\n\t" \
    "v_mfma_f32_16x16x32_bf16 v[216:219], v[188:191], v[240:243], v[216:219]\n\t" \
    "v_cvt_pk_bf16_f32 v209, v210, v211\n\t" \
    "ds_write_b64 v254, v[208:209] offset:8192\n\t" \
    "v_mfma_f32_16x16x32_bf16 v[220:223], v[152:155], v[228:231], v[220:223]\n\t" \
    "v_cvt_pk_bf16_f32 v200, v200, v201\n\t" \
    "v_cvt_pk_bf16_f32 v201, v202, v203\n\t" \
    "v_mfma_f32_16x16x32_bf16 v[220:223], v[156:159], v[232:235], v[220:223]\n\t" \
    "ds_write_b64 v254, v[200:201] offset:6144\n\t" \
    "ds_read_b128 v[200:203], v254 offset:30720\n\t" \
    "v_mfma_f32_16x16x32_bf16 v[220:223], v[192:195], v[236:239], v[220:223]\n\t" \
    "ds_read_b128 v[204:207], v254 offset:31744\n\t" \
    "v_mov_b64 v[208:209], 0\n\t" \
    "v_mfma_f32_16x16x32_bf16 v[220:223], v[196:199], v[240:243], v[220:223]\n\t" \
    "v_mov_b64 v[210:211], 0\n\t" \
    "v_cvt_pk_bf16_f32 v216, v216, v217\n\t" \
    "v_mfma_f32_16x16x32_bf16 v[212:215], v[136:139], v[228:231], v[212:215]\n\t" \
    "v_cvt_pk_bf16_f32 v217, v218, v219\n\t" \
    "ds_write_b64 v254, v[216:217] offset:13312\n\t" \
    "v_mfma_f32_16x16x32_bf16 v[212:215], v[140:143], v[232:235], v[212:215]\n\t" \
    "v_mfma_f32_16x16x32_bf16 v[212:215], v[176:179], v[236:239], v[212:215]\n\t" \
    "v_cvt_pk_bf16_f32 v220, v220, v221\n\t" \
    "v_cvt_pk_bf16_f32 v221, v222, v223\n\t" \
    "v_mfma_f32_16x16x32_bf16 v[212:215], v[180:183], v[240:243], v[212:215]\n\t" \
    "ds_write_b64 v254, v[220:221] offset:14336\n\t" \
    "ds_read_b128 v[216:219], v254 offset:32768\n\t" \
    "ds_read_b128 v[220:223], v254 offset:33792\n\t" \
    "s_nop 4\n\t" \
    "v_cvt_pk_bf16_f32 v212, v212, v213\n\t" \
    "v_cvt_pk_bf16_f32 v213, v214, v215\n\t" \
    "ds_write_b64 v254, v[212:213] offset:12288\n\t" \
    LST6 \
    "s_waitcnt vmcnt(0) lgkmcnt(0)\n\t" \
    "s_barrier \n\t" \
    "s_setprio 2\n\t" \
    "s_add_u32 s44, s40, 1\n\t" \
    "s_min_u32 s44, s44, 63\n\t" \
    ".Lv06_%=:\n\t" \
    "v_cmp_ge_u32 vcc, s44, v252\n\t" \
    "s_cbranch_vccz .Lv07_%=\n\t" \
    "s_cmp_eq_u32 s41, 0\n\t" \
    "s_cbranch_scc1 .Lv07_%=\n\t" \
    "s_sub_u32 s41, s41, 1\n\t" \
    "s_sleep 2\n\t" \
    "v_and_b32 v252, 0xf0, v253\n\t" \
    "v_lshlrev_b32 v252, 3, v252\n\t" \
    "buffer_load_dword v252, v252, s[60:63], %[fl] offen sc1\n\t" \
    "s_waitcnt vmcnt(0)\n\t" \
    "s_branch .Lv06_%=\n\t" \
    ".Lv07_%=:\n\t" \
    "s_movk_i32 s45, 0x0\n\t" \
    "s_mov_b32 m0, s45\n\t" \
    "s_nop 0\n\t" \
    "buffer_load_dwordx4 v253, s[60:63], s42 offen offset:0 sc1 lds\n\t" \
    "buffer_load_dwordx4 v253, s[60:63], s42 offen offset:1024 sc1 lds\n\t" \
    "buffer_load_dwordx4 v253, s[60:63], s42 offen offset:2048 sc1 lds\n\t" \
    "buffer_load_dwordx4 v253, s[60:63], s42 offen offset:3072 sc1 lds\n\t" \
    "s_add_u32 s44, s42, 0x1000\n\t" \
    "s_movk_i32 s45, 0x5000\n\t" \
    "s_mov_b32 m0, s45\n\t" \
    "s_nop 0\n\t" \
    "buffer_load_dwordx4 v253, s[60:63], s44 offen offset:0 sc1 lds\n\t" \
    "buffer_load_dwordx4 v253, s[60:63], s44 offen offset:1024 sc1 lds\n\t" \
    "buffer_load_dwordx4 v253, s[60:63], s44 offen offset:2048 sc1 lds\n\t" \
    "buffer_load_dwordx4 v253, s[60:63], s44 offen offset:3072 sc1 lds\n\t" \
    "s_add_u32 s44, s42, 0x10000\n\t" \
    "s_mov_b32 s45, 0x4000\n\t" \
    "s_mov_b32 m0, s45\n\t" \
    "s_nop 0\n\t" \
    "buffer_load_dwordx4 v253, s[60:63], s44 offen offset:0 sc1 lds\n\t" \
    "s_add_u32 s44, s44, 0x1000\n\t" \
    "s_mov_b32 s45, 0x9000\n\t" \
    "s_mov_b32 m0, s45\n\t" \
    "s_nop 0\n\t" \
    "buffer_load_dwordx4 v253, s[60:63], s44 offen offset:0 sc1 lds\n\t" \
    LST1 \
    "s_sleep 18\n\t" \
    "buffer_load_dwordx4 v[224:227], v253, s[60:63], s46 offen sc1\n\t" \
    "buffer_load_dwordx4 v[228:231], v253, s[60:63], s47 offen sc1\n\t" \
    "buffer_load_dwordx4 v[232:235], v253, s[60:63], s48 offen sc1\n\t" \
    "s_waitcnt lgkmcnt(0)\n\t" \
    LFLUSH212 \
    "v_bfe_i32 v212, v253, 5, 1\n\t" \
    "v_and_b32 v212, 0xaaaa, v212\n\t" \
    "v_or_b32 v212, 0x4444, v212\n\t" \
    "v_and_b32 v213, 0xffff, v255\n\t" \
    "v_lshrrev_b32 v214, 16, v255\n\t" \
    ".Lv08_%=:\n\t" \
    "s_waitcnt vmcnt(2)\n\t" \
    "v_max_u32 v215, v224, v225\n\t" \
    "v_max3_u32 v215, v226, v227, v215\n\t" \
    "s_waitcnt vmcnt(1)\n\t" \
    "v_max3_u32 v215, v228, v229, v215\n\t" \
    "v_max3_u32 v215, v230, v231, v215\n\t" \
    "s_waitcnt vmcnt(0)\n\t" \
    "v_max3_u32 v215, v232, v233, v215\n\t" \
    "v_max3_u32 v215, v234, v235, v215\n\t" \
    "v_cmp_eq_u32 vcc, -1, v215\n\t" \
    "s_cbranch_vccz .Lv09_%=\n\t" \
    "v_max_u32 v215, v224, v225\n\t" \
    "v_max3_u32 v215, v226, v227, v215\n\t" \
    "v_cmp_eq_u32 vcc, -1, v215\n\t" \
    "s_cbranch_vccz .Lv010_%=\n\t" \
    "buffer_load_dwordx4 v[224:227], v253, s[60:63], s46 offen sc1\n\t" \
    ".Lv010_%=:\n\t" \
    "v_max_u32 v215, v228, v229\n\t" \
    "v_max3_u32 v215, v230, v231, v215\n\t" \
    "v_cmp_eq_u32 vcc, -1, v215\n\t" \
    "s_cbranch_vccz .Lv011_%=\n\t" \
    "buffer_load_dwordx4 v[228:231], v253, s[60:63], s47 offen sc1\n\t" \
    ".Lv011_%=:\n\t" \
    "v_max_u32 v215, v232, v233\n\t" \
    "v_max3_u32 v215, v234, v235, v215\n\t" \
    "v_cmp_eq_u32 vcc, -1, v215\n\t" \
    "s_cbranch_vccz .Lv012_%=\n\t" \
    "buffer_load_dwordx4 v[232:235], v253, s[60:63], s48 offen sc1\n\t" \
    ".Lv012_%=:\n\t" \
    "s_cmp_eq_u32 s41, 0\n\t" \
    "s_cbranch_scc1 .Lv09_%=\n\t" \
    "s_sub_u32 s41, s41, 1\n\t" \
    "s_waitcnt vmcnt(0)\n\t" \
    "s_branch .Lv08_%=\n\t" \
    ".Lv09_%=:\n\t" \
    LST2 \
    "ds_write_b128 v253, v[224:227] offset:40960\n\t" \
    "ds_write_b128 v253, v[228:231] offset:46080\n\t" \
    "ds_write_b128 v253, v[232:235] offset:41984\n\t" \
    "v_smfmac_f32_16x16x64_bf16 v[244:247], v[224:227], v[0:7], v212\n\t" \
    "v_smfmac_f32_16x16x64_bf16 v[248:251], v[224:227], v[8:15], v212\n\t" \
    "v_smfmac_f32_16x16x64_bf16 v[208:211], v[224:227], v[200:207], v212\n\t" \
    "s_waitcnt lgkmcnt(0)\n\t" \
    "s_barrier \n\t" \
    LST3 \
    "ds_read_b128 v[236:239], v253 offset:47104\n\t" \
    "ds_read_b128 v[240:243], v253 offset:43008\n\t" \
    "ds_read_b128 v[224:227], v253 offset:48128\n\t" \
    "ds_read_b128 v[200:203], v254 offset:34816\n\t" \
    "ds_read_b128 v[204:207], v254 offset:35840\n\t" \
    "v_smfmac_f32_16x16x64_bf16 v[208:211], v[228:231], v[16:23], v212\n\t" \
    "v_smfmac_f32_16x16x64_bf16 v[244:247], v[228:231], v[24:31], v212\n\t" \
    "v_smfmac_f32_16x16x64_bf16 v[248:251], v[228:231], v[32:39], v212\n\t" \
    "ds_read_b128 v[228:231], v253 offset:44032\n\t" \
    "v_smfmac_f32_16x16x64_bf16 v[208:211], v[232:235], v[216:223], v212\n\t" \
    "ds_read_b128 v[216:219], v254 offset:36864\n\t" \
    "ds_read_b128 v[220:223], v254 offset:37888\n\t" \
    "v_smfmac_f32_16x16x64_bf16 v[244:247], v[232:235], v[40:47], v212\n\t" \
    "v_smfmac_f32_16x16x64_bf16 v[248:251], v[232:235], v[48:55], v212\n\t" \
    "ds_read_b128 v[232:235], v253 offset:49152\n\t" \
    "s_waitcnt lgkmcnt(8)\n\t" \
    "v_smfmac_f32_16x16x64_bf16 v[208:211], v[236:239], v[56:63], v212\n\t" \
    "v_smfmac_f32_16x16x64_bf16 v[244:247], v[236:239], v[64:71], v212\n\t" \
    "v_smfmac_f32_16x16x64_bf16 v[248:251], v[236:239], v[72:79], v212\n\t" \
    "ds_read_b128 v[236:239], v253 offset:45056\n\t" \
    "s_waitcnt lgkmcnt(5)\n\t" \
    "v_smfmac_f32_16x16x64_bf16 v[208:211], v[240:243], v[200:207], v212\n\t" \
    "ds_read_b128 v[200:203], v254 offset:38912\n\t" \
    "ds_read_b128 v[204:207], v254 offset:39936\n\t" \
    "v_smfmac_f32_16x16x64_bf16 v[244:247], v[240:243], v[80:87], v212\n\t" \
    "v_smfmac_f32_16x16x64_bf16 v[248:251], v[240:243], v[88:95], v212\n\t" \
    "ds_read_b128 v[240:243], v253 offset:50176\n\t" \
    "v_smfmac_f32_16x16x64_bf16 v[208:211], v[224:227], v[96:103], v212\n\t" \
    "v_smfmac_f32_16x16x64_bf16 v[244:247], v[224:227], v[104:111], v212\n\t" \
    "v_smfmac_f32_16x16x64_bf16 v[248:251], v[224:227], v[112:119], v212\n\t" \
    "s_waitcnt lgkmcnt(7)\n\t" \
    "v_smfmac_f32_16x16x64_bf16 v[244:247], v[228:231], v[120:127], v212\n\t" \
    "s_waitcnt lgkmcnt(4)\n\t" \
    "v_smfmac_f32_16x16x64_bf16 v[244:247], v[232:235], v[144:151], v212\n\t" \
    "s_waitcnt lgkmcnt(3)\n\t" \
    "v_smfmac_f32_16x16x64_bf16 v[244:247], v[236:239], v[160:167], v212\n\t" \
    "s_waitcnt lgkmcnt(0)\n\t" \
    "v_smfmac_f32_16x16x64_bf16 v[244:247], v[240:243], v[184:191], v212\n\t" \
    "v_smfmac_f32_16x16x64_bf16 v[248:251], v[228:231], v[128:135], v212\n\t" \
    "v_smfmac_f32_16x16x64_bf16 v[248:251], v[232:235], v[152:159], v212\n\t" \
    "v_smfmac_f32_16x16x64_bf16 v[248:251], v[236:239], v[168:175], v212\n\t" \
    "v_add_f32 v244, v244, v246\n\t" \
    "v_add_f32 v245, v245, v247\n\t" \
    "v_cvt_pk_bf16_f32 v244, v244, v245\n\t" \
    "v_smfmac_f32_16x16x64_bf16 v[248:251], v[240:243], v[192:199], v212\n\t" \
    "ds_write_b16 v213, v244 offset:512\n\t" \
    "ds_write_b16_d16_hi v213, v244 offset:528\n\t" \
    "v_smfmac_f32_16x16x64_bf16 v[208:211], v[228:231], v[216:223], v212\n\t" \
    "v_smfmac_f32_16x16x64_bf16 v[208:211], v[232:235], v[136:143], v212\n\t" \
    "v_smfmac_f32_16x16x64_bf16 v[208:211], v[236:239], v[200:207], v212\n\t" \
    "v_add_f32 v248, v248, v250\n\t" \
    "v_add_f32 v249, v249, v251\n\t" \
    "v_cvt_pk_bf16_f32 v248, v248, v249\n\t" \
    "v_smfmac_f32_16x16x64_bf16 v[208:211], v[240:243], v[176:183], v212\n\t" \
    "ds_write_b16 v213, v248 offset:1024\n\t" \
    "ds_write_b16_d16_hi v213, v248 offset:1040\n\t" \
    "s_nop 5\n\t" \
    "v_add_f32 v208, v208, v210\n\t" \
    "v_add_f32 v209, v209, v211\n\t" \
    "v_cvt_pk_bf16_f32 v208, v208, v209\n\t" \
    "ds_write_b16 v213, v208 offset:0\n\t" \
    "ds_write_b16_d16_hi v213, v208 offset:16\n\t" \
    "s_setprio 0\n\t" \
    "s_waitcnt lgkmcnt(0)\n\t" \
    "s_barrier \n\t" \
    LST4 \
    "ds_read_b128 v[224:227], v253 offset:0\n\t" \
    "ds_read_b128 v[244:247], v254 offset:30720\n\t" \
    "ds_read_b128 v[228:231], v253 offset:1024\n\t" \
    "ds_read_b128 v[232:235], v253 offset:20480\n\t" \
    "ds_read_b128 v[236:239], v253 offset:21504\n\t" \
    "ds_read_b128 v[248:251], v254 offset:31744\n\t" \
    "s_add_u32 s40, s40, 1\n\t" \
    "s_cmp_lg_u32 s40, 64\n\t" \
    "s_cbranch_scc1 .Lv05_%=\n\t" \
    "s_branch .Lend%=\n\t" \
    ".Lend%=:\n\t" \
    "s_waitcnt vmcnt(0) lgkmcnt(0)\n\t" \
    "s_mov_b32 %[bud], s41\n\t"
#define ASM_RCRIT \
    "s_movk_i32 %[n], 0x400\n\t" \
    ".Lfa%=:\n\t" \
    "v_max_u32 v196, v180, v181\n\t" \
    "v_max3_u32 v196, v182, v183, v196\n\t" \
    "v_cmp_eq_u32 vcc, -1, v196\n\t" \
    "s_cbranch_vccz .Lha%=\n\t" \
    "buffer_load_dwordx4 v[180:183], %[ov], %[rs], %[sv] offen sc1\n\t" \
    "s_waitcnt vmcnt(12)\n\t" \
    "v_max_u32 v196, v180, v181\n\t" \
    "v_max3_u32 v196, v182, v183, v196\n\t" \
    "v_cmp_eq_u32 vcc, -1, v196\n\t" \
    "s_cbranch_vccz .Lha%=\n\t" \
    "s_nop 7\n\t" \
    "v_max_u32 v196, v180, v181\n\t" \
    "v_max3_u32 v196, v182, v183, v196\n\t" \
    "v_cmp_eq_u32 vcc, -1, v196\n\t" \
    "s_cbranch_vccz .Lha%=\n\t" \
    "s_nop 7\n\t" \
    "v_max_u32 v196, v180, v181\n\t" \
    "v_max3_u32 v196, v182, v183, v196\n\t" \
    "v_cmp_eq_u32 vcc, -1, v196\n\t" \
    "s_cbranch_vccz .Lha%=\n\t" \
    "s_nop 7\n\t" \
    "v_max_u32 v196, v180, v181\n\t" \
    "v_max3_u32 v196, v182, v183, v196\n\t" \
    "v_cmp_eq_u32 vcc, -1, v196\n\t" \
    "s_cbranch_vccz .Lha%=\n\t" \
    "s_nop 7\n\t" \
    "s_sub_u32 %[n], %[n], 1\n\t" \
    "s_cmp_lg_u32 %[n], 0\n\t" \
    "s_cbranch_scc1 .Lfa%=\n\t" \
    ".Lha%=:\n\t" \
    "s_cmp_eq_u32 %[n], 0\n\t" \
    "s_cbranch_scc1 .Lre%=\n\t" \
    ASM_ST3 \
    ".Lfb%=:\n\t" \
    "s_mov_b32 %[a], 0\n\t" \
    "v_max_u32 v196, v184, v185\n\t" \
    "v_max3_u32 v196, v186, v187, v196\n\t" \
    "v_cmp_eq_u32 vcc, -1, v196\n\t" \
    "s_cbranch_vccz .Lgb0_%=\n\t" \
    "buffer_load_dwordx4 v[184:187], %[o0], %[rs], %[sp] offen sc1\n\t" \
    "s_mov_b32 %[a], 1\n\t" \
    ".Lgb0_%=:\n\t" \
    "v_max_u32 v196, v188, v189\n\t" \
    "v_max3_u32 v196, v190, v191, v196\n\t" \
    "v_cmp_eq_u32 vcc, -1, v196\n\t" \
    "s_cbranch_vccz .Lgb1_%=\n\t" \
    "buffer_load_dwordx4 v[188:191], %[o1], %[rs], %[sp] offen sc1\n\t" \
    "s_mov_b32 %[a], 1\n\t" \
    ".Lgb1_%=:\n\t" \
    "v_max_u32 v196, v192, v193\n\t" \
    "v_max3_u32 v196, v194, v195, v196\n\t" \
    "v_cmp_eq_u32 vcc, -1, v196\n\t" \
    "s_cbranch_vccz .Lgb2_%=\n\t" \
    "buffer_load_dwordx4 v[192:195], %[o2], %[rs], %[sp] offen sc1\n\t" \
    "s_mov_b32 %[a], 1\n\t" \
    ".Lgb2_%=:\n\t" \
    "s_cmp_eq_u32 %[a], 0\n\t" \
    "s_cbranch_scc1 .Lhb%=\n\t" \
    "s_waitcnt vmcnt(12)\n\t" \
    "v_max_u32 v196, v184, v185\n\t" \
    "v_max3_u32 v196, v186, v187, v196\n\t" \
    "v_max3_u32 v196, v188, v189, v196\n\t" \
    "v_max3_u32 v196, v190, v191, v196\n\t" \
    "v_max3_u32 v196, v192, v193, v196\n\t" \
    "v_max3_u32 v196, v194, v195, v196\n\t" \
    "v_cmp_eq_u32 vcc, -1, v196\n\t" \
    "s_cbranch_vccz .Lhb%=\n\t" \
    "s_nop 7\n\t" \
    "v_max_u32 v196, v184, v185\n\t" \
    "v_max3_u32 v196, v186, v187, v196\n\t" \
    "v_max3_u32 v196, v188, v189, v196\n\t" \
    "v_max3_u32 v196, v190, v191, v196\n\t" \
    "v_max3_u32 v196, v192, v193, v196\n\t" \
    "v_max3_u32 v196, v194, v195, v196\n\t" \
    "v_cmp_eq_u32 vcc, -1, v196\n\t" \
    "s_cbranch_vccz .Lhb%=\n\t" \
    "s_nop 7\n\t" \
    "v_max_u32 v196, v184, v185\n\t" \
    "v_max3_u32 v196, v186, v187, v196\n\t" \
    "v_max3_u32 v196, v188, v189, v196\n\t" \
    "v_max3_u32 v196, v190, v191, v196\n\t" \
    "v_max3_u32 v196, v192, v193, v196\n\t" \
    "v_max3_u32 v196, v194, v195, v196\n\t" \
    "v_cmp_eq_u32 vcc, -1, v196\n\t" \
    "s_cbranch_vccz .Lhb%=\n\t" \
    "s_nop 7\n\t" \
    "v_max_u32 v196, v184, v185\n\t" \
    "v_max3_u32 v196, v186, v187, v196\n\t" \
    "v_max3_u32 v196, v188, v189, v196\n\t" \
    "v_max3_u32 v196, v190, v191, v196\n\t" \
    "v_max3_u32 v196, v192, v193, v196\n\t" \
    "v_max3_u32 v196, v194, v195, v196\n\t" \
    "v_cmp_eq_u32 vcc, -1, v196\n\t" \
    "s_cbranch_vccz .Lhb%=\n\t" \
    "s_nop 7\n\t" \
    "v_max_u32 v196, v184, v185\n\t" \
    "v_max3_u32 v196, v186, v187, v196\n\t" \
    "v_max3_u32 v196, v188, v189, v196\n\t" \
    "v_max3_u32 v196, v190, v191, v196\n\t" \
    "v_max3_u32 v196, v192, v193, v196\n\t" \
    "v_max3_u32 v196, v194, v195, v196\n\t" \
    "v_cmp_eq_u32 vcc, -1, v196\n\t" \
    "s_cbranch_vccz .Lhb%=\n\t" \
    "s_nop 7\n\t" \
    "v_max_u32 v196, v184, v185\n\t" \
    "v_max3_u32 v196, v186, v187, v196\n\t" \
    "v_max3_u32 v196, v188, v189, v196\n\t" \
    "v_max3_u32 v196, v190, v191, v196\n\t" \
    "v_max3_u32 v196, v192, v193, v196\n\t" \
    "v_max3_u32 v196, v194, v195, v196\n\t" \
    "v_cmp_eq_u32 vcc, -1, v196\n\t" \
    "s_cbranch_vccz .Lhb%=\n\t" \
    "s_nop 7\n\t" \
    "v_max_u32 v196, v184, v185\n\t" \
    "v_max3_u32 v196, v186, v187, v196\n\t" \
    "v_max3_u32 v196, v188, v189, v196\n\t" \
    "v_max3_u32 v196, v190, v191, v196\n\t" \
    "v_max3_u32 v196, v192, v193, v196\n\t" \
    "v_max3_u32 v196, v194, v195, v196\n\t" \
    "v_cmp_eq_u32 vcc, -1, v196\n\t" \
    "s_cbranch_vccz .Lhb%=\n\t" \
    "s_nop 7\n\t" \
    "v_max_u32 v196, v184, v185\n\t" \
    "v_max3_u32 v196, v186, v187, v196\n\t" \
    "v_max3_u32 v196, v188, v189, v196\n\t" \
    "v_max3_u32 v196, v190, v191, v196\n\t" \
    "v_max3_u32 v196, v192, v193, v196\n\t" \
    "v_max3_u32 v196, v194, v195, v196\n\t" \
    "v_cmp_eq_u32 vcc, -1, v196\n\t" \
    "s_cbranch_vccz .Lhb%=\n\t" \
    "s_nop 7\n\t" \
    "s_sub_u32 %[n], %[n], 1\n\t" \
    "s_cmp_lg_u32 %[n], 0\n\t" \
    "s_cbranch_scc1 .Lfb%=\n\t" \
    "s_branch .Lre%=\n\t" \
    ".Lhb%=:\n\t" \
    ASM_ST2 \
    "v_mfma_f32_16x16x32_bf16 v[176:179], v[192:195], v[180:183], 0\n\t" \
    "v_smfmac_f32_16x16x64_bf16 v[176:179], v[184:187], v[160:167], %[sidx]\n\t" \
    "v_smfmac_f32_16x16x64_bf16 v[176:179], v[188:191], v[168:175], %[sidx]\n\t" \
    "s_cmp_eq_u32 %[pub], 0\n\t" \
    "s_nop 7\n\t" \
    "v_add_f32 v176, v176, v178\n\t" \
    "v_add_f32 v177, v177, v179\n\t" \
    "ds_write2_b32 %[rw], v176, v177 offset1:80\n\t" \
    "s_waitcnt lgkmcnt(0)\n\t" \
    "s_barrier\n\t" ASM_ST \
    "s_cbranch_scc1 .Lre%=\n\t" \
    "ds_read_b128 v[200:203], %[rd] offset:0\n\t" \
    "ds_read_b128 v[204:207], %[rd] offset:16\n\t" \
    "ds_read_b128 v[208:211], %[rd] offset:32\n\t" \
    "ds_read_b128 v[212:215], %[rd] offset:48\n\t" \
    "s_waitcnt lgkmcnt(2)\n\t" \
    "v_pk_add_f32 v[200:201], v[200:201], v[202:203]\n\t" \
    "v_pk_add_f32 v[204:205], v[204:205], v[206:207]\n\t" \
    "s_waitcnt lgkmcnt(0)\n\t" \
    "v_pk_add_f32 v[208:209], v[208:209], v[210:211]\n\t" \
    "v_pk_add_f32 v[212:213], v[212:213], v[214:215]\n\t" \
    "v_pk_add_f32 v[200:201], v[200:201], v[204:205]\n\t" \
    "v_pk_add_f32 v[208:209], v[208:209], v[212:213]\n\t" \
    "v_pk_add_f32 v[200:201], v[200:201], v[208:209]\n\t" \
    "v_cvt_pk_bf16_f32 v200, v200, v201\n\t" \
    "v_min_u32 v200, -2, v200\n\t" \
    "buffer_store_dword v200, %[oo], %[rs], %[so] offen sc1\n\t" \
    ".Lre%=:\n\t" \
    ASM_STW
#define ASM_RX \
    "s_cmp_eq_u32 %[fin], 0\n\t" \
    "s_cbranch_scc1 .Lx0%=\n\t" \
    "buffer_load_dwordx2 v[250:251], %[xo], %[rd], %[sx] offen sc1\n\t" \
    ".Lx0%=:\n\t" \
    "buffer_load_dwordx4 v[200:203], %[l16], %[rs], %[s0] offen offset:0 sc1\n\t" \
    "buffer_load_dwordx4 v[204:207], %[l16], %[rs], %[s0] offen offset:1024 sc1\n\t" \
    "buffer_load_dwordx4 v[208:211], %[l16], %[rs], %[s0] offen offset:2048 sc1\n\t" \
    "buffer_load_dwordx4 v[212:215], %[l16], %[rs], %[s0] offen offset:3072 sc1\n\t" \
    "buffer_load_dwordx4 v[216:219], %[l16], %[rs], %[s1] offen offset:0 sc1\n\t" \
    "buffer_load_dwordx4 v[220:223], %[l16], %[rs], %[s1] offen offset:1024 sc1\n\t" \
    "buffer_load_dwordx4 v[224:227], %[l16], %[rs], %[s1] offen offset:2048 sc1\n\t" \
    "buffer_load_dwordx4 v[228:231], %[l16], %[rs], %[s1] offen offset:3072 sc1\n\t" \
    "buffer_load_dword v248, %[fo], %[rf], %[fs] offen sc1\n\t" \
    "s_mov_b32 %[a], 0x05040100\n\t" \
    "v_perm_b32 v232, v161, v160, %[a]\n\t" \
    "v_perm_b32 v233, v163, v162, %[a]\n\t" \
    "v_perm_b32 v234, v165, v164, %[a]\n\t" \
    "v_perm_b32 v235, v167, v166, %[a]\n\t" \
    "v_perm_b32 v236, v169, v168, %[a]\n\t" \
    "v_perm_b32 v237, v171, v170, %[a]\n\t" \
    "v_perm_b32 v238, v173, v172, %[a]\n\t" \
    "v_perm_b32 v239, v175, v174, %[a]\n\t" \
    "s_movk_i32 %[n], 0x200\n\t" \
    "s_nop 1\n\t" \
    "v_permlane16_swap_b32 v232, v234\n\t" \
    "v_permlane16_swap_b32 v233, v235\n\t" \
    "v_permlane16_swap_b32 v236, v238\n\t" \
    "v_permlane16_swap_b32 v237, v239\n\t" \
    "s_waitcnt vmcnt(8)\n\t" \
    "v_max_u32 v196, v200, v201\n\t" \
    "v_max3_u32 v196, v202, v203, v196\n\t" \
    "s_waitcnt vmcnt(7)\n\t" \
    "v_max3_u32 v196, v204, v205, v196\n\t" \
    "v_max3_u32 v196, v206, v207, v196\n\t" \
    "s_waitcnt vmcnt(6)\n\t" \
    "v_max3_u32 v196, v208, v209, v196\n\t" \
    "v_max3_u32 v196, v210, v211, v196\n\t" \
    "s_waitcnt vmcnt(5)\n\t" \
    "v_max3_u32 v196, v212, v213, v196\n\t" \
    "v_max3_u32 v196, v214, v215, v196\n\t" \
    "s_waitcnt vmcnt(4)\n\t" \
    "v_max3_u32 v196, v216, v217, v196\n\t" \
    "v_max3_u32 v196, v218, v219, v196\n\t" \
    "s_waitcnt vmcnt(3)\n\t" \
    "v_max3_u32 v196, v220, v221, v196\n\t" \
    "v_max3_u32 v196, v222, v223, v196\n\t" \
    "s_waitcnt vmcnt(2)\n\t" \
    "v_max3_u32 v196, v224, v225, v196\n\t" \
    "v_max3_u32 v196, v226, v227, v196\n\t" \
    "s_waitcnt vmcnt(1)\n\t" \
    "v_max3_u32 v196, v228, v229, v196\n\t" \
    "v_max3_u32 v196, v230, v231, v196\n\t" \
    "v_cmp_eq_u32 vcc, -1, v196\n\t" \
    "s_cbranch_vccz .Lxd%=\n\t" \
    ".Lxl%=:\n\t" \
    "s_mov_b32 %[a], 0\n\t" \
    "v_max_u32 v196, v200, v201\n\t" \
    "v_max3_u32 v196, v202, v203, v196\n\t" \
    "v_cmp_eq_u32 vcc, -1, v196\n\t" \
    "s_cbranch_vccz .Lxs0_%=\n\t" \
    "buffer_load_dwordx4 v[200:203], %[l16], %[rs], %[s0] offen offset:0 sc1\n\t" \
    "s_mov_b32 %[a], 1\n\t" \
    ".Lxs0_%=:\n\t" \
    "v_max_u32 v196, v204, v205\n\t" \
    "v_max3_u32 v196, v206, v207, v196\n\t" \
    "v_cmp_eq_u32 vcc, -1, v196\n\t" \
    "s_cbranch_vccz .Lxs1_%=\n\t" \
    "buffer_load_dwordx4 v[204:207], %[l16], %[rs], %[s0] offen offset:1024 sc1\n\t" \
    "s_mov_b32 %[a], 1\n\t" \
    ".Lxs1_%=:\n\t" \
    "v_max_u32 v196, v208, v209\n\t" \
    "v_max3_u32 v196, v210, v211, v196\n\t" \
    "v_cmp_eq_u32 vcc, -1, v196\n\t" \
    "s_cbranch_vccz .Lxs2_%=\n\t" \
    "buffer_load_dwordx4 v[208:211], %[l16], %[rs], %[s0] offen offset:2048 sc1\n\t" \
    "s_mov_b32 %[a], 1\n\t" \
    ".Lxs2_%=:\n\t" \
    "v_max_u32 v196, v212, v213\n\t" \
    "v_max3_u32 v196, v214, v215, v196\n\t" \
    "v_cmp_eq_u32 vcc, -1, v196\n\t" \
    "s_cbranch_vccz .Lxs3_%=\n\t" \
    "buffer_load_dwordx4 v[212:215], %[l16], %[rs], %[s0] offen offset:3072 sc1\n\t" \
    "s_mov_b32 %[a], 1\n\t" \
    ".Lxs3_%=:\n\t" \
    "v_max_u32 v196, v216, v217\n\t" \
    "v_max3_u32 v196, v218, v219, v196\n\t" \
    "v_cmp_eq_u32 vcc, -1, v196\n\t" \
    "s_cbranch_vccz .Lxs4_%=\n\t" \
    "buffer_load_dwordx4 v[216:219], %[l16], %[rs], %[s1] offen offset:0 sc1\n\t" \
    "s_mov_b32 %[a], 1\n\t" \
    ".Lxs4_%=:\n\t" \
    "v_max_u32 v196, v220, v221\n\t" \
    "v_max3_u32 v196, v222, v223, v196\n\t" \
    "v_cmp_eq_u32 vcc, -1, v196\n\t" \
    "s_cbranch_vccz .Lxs5_%=\n\t" \
    "buffer_load_dwordx4 v[220:223], %[l16], %[rs], %[s1] offen offset:1024 sc1\n\t" \
    "s_mov_b32 %[a], 1\n\t" \
    ".Lxs5_%=:\n\t" \
    "v_max_u32 v196, v224, v225\n\t" \
    "v_max3_u32 v196, v226, v227, v196\n\t" \
    "v_cmp_eq_u32 vcc, -1, v196\n\t" \
    "s_cbranch_vccz .Lxs6_%=\n\t" \
    "buffer_load_dwordx4 v[224:227], %[l16], %[rs], %[s1] offen offset:2048 sc1\n\t" \
    "s_mov_b32 %[a], 1\n\t" \
    ".Lxs6_%=:\n\t" \
    "v_max_u32 v196, v228, v229\n\t" \
    "v_max3_u32 v196, v230, v231, v196\n\t" \
    "v_cmp_eq_u32 vcc, -1, v196\n\t" \
    "s_cbranch_vccz .Lxs7_%=\n\t" \
    "buffer_load_dwordx4 v[228:231], %[l16], %[rs], %[s1] offen offset:3072 sc1\n\t" \
    "s_mov_b32 %[a], 1\n\t" \
    ".Lxs7_%=:\n\t" \
    "s_cmp_eq_u32 %[a], 0\n\t" \
    "s_cbranch_scc1 .Lxd%=\n\t" \
    "s_waitcnt vmcnt(0)\n\t" \
    "s_sub_u32 %[n], %[n], 1\n\t" \
    "s_cmp_lg_u32 %[n], 0\n\t" \
    "s_cbranch_scc1 .Lxl%=\n\t" \
    "s_branch .Lxe%=\n\t" \
    ".Lxd%=:\n\t" \
    ASM_ST2 \
    "v_mfma_f32_16x16x32_bf16 v[244:247], v[160:163], v[200:203], 0\n\t" \
    "v_mfma_f32_16x16x32_bf16 v[244:247], v[164:167], v[204:207], v[244:247]\n\t" \
    "v_mfma_f32_16x16x32_bf16 v[244:247], v[168:171], v[208:211], v[244:247]\n\t" \
    "v_mfma_f32_16x16x32_bf16 v[244:247], v[172:175], v[212:215], v[244:247]\n\t" \
    "v_mfma_f32_16x16x32_bf16 v[240:243], v[232:235], v[220:223], 0\n\t" \
    "v_mfma_f32_16x16x32_bf16 v[244:247], v[180:183], v[216:219], v[244:247]\n\t" \
    "v_mfma_f32_16x16x32_bf16 v[240:243], v[236:239], v[224:227], v[240:243]\n\t" \
    "v_mfma_f32_16x16x32_bf16 v[240:243], v[180:183], v[228:231], v[240:243]\n\t" \
    "s_waitcnt vmcnt(0)\n\t" \
    "v_cmp_ge_u32 vcc, %[tn], v248\n\t" \
    "s_mov_b32 %[a], 0\n\t" \
    "s_cbranch_vccnz .Lxv%=\n\t" \
    "buffer_load_dwordx4 v[160:163], %[l16], %[rd], %[sv] offen offset:0 sc1\n\t" \
    "buffer_load_dwordx4 v[164:167], %[l16], %[rd], %[sv] offen offset:1024 sc1\n\t" \
    "buffer_load_dwordx4 v[168:171], %[l16], %[rd], %[sv] offen offset:2048 sc1\n\t" \
    "buffer_load_dwordx4 v[172:175], %[l16], %[rd], %[sv] offen offset:3072 sc1\n\t" \
    "s_mov_b32 %[a], 1\n\t" \
    ".Lxv%=:\n\t" \
    "s_nop 7\n\t" \
    "ds_write_b128 %[pw], v[244:247] offset:8192\n\t" \
    "ds_write_b128 %[pw], v[240:243]\n\t" \
    "s_waitcnt lgkmcnt(0)\n\t" \
    "s_barrier\n\t" ASM_ST \
    "s_cmp_eq_u32 %[fin], 0\n\t" \
    "s_cbranch_scc1 .Lxe%=\n\t" \
    "ds_read_b128 v[200:203], %[pr] offset:0\n\t" \
    "ds_read_b128 v[204:207], %[pr] offset:1024\n\t" \
    "ds_read_b128 v[208:211], %[pr] offset:2048\n\t" \
    "ds_read_b128 v[212:215], %[pr] offset:3072\n\t" \
    "ds_read_b128 v[216:219], %[pr] offset:4096\n\t" \
    "ds_read_b128 v[220:223], %[pr] offset:5120\n\t" \
    "ds_read_b128 v[224:227], %[pr] offset:6144\n\t" \
    "ds_read_b128 v[228:231], %[pr] offset:7168\n\t" \
    "v_lshlrev_b32 v240, 16, v250\n\t" \
    "v_and_b32 v241, 0xffff0000, v250\n\t" \
    "v_lshlrev_b32 v242, 16, v251\n\t" \
    "v_and_b32 v243, 0xffff0000, v251\n\t" \
    "s_waitcnt lgkmcnt(6)\n\t" \
    "v_pk_add_f32 v[200:201], v[200:201], v[204:205]\n\t" \
    "v_pk_add_f32 v[202:203], v[202:203], v[206:207]\n\t" \
    "s_waitcnt lgkmcnt(4)\n\t" \
    "v_pk_add_f32 v[208:209], v[208:209], v[212:213]\n\t" \
    "v_pk_add_f32 v[210:211], v[210:211], v[214:215]\n\t" \
    "v_pk_add_f32 v[200:201], v[200:201], v[208:209]\n\t" \
    "v_pk_add_f32 v[202:203], v[202:203], v[210:211]\n\t" \
    "s_waitcnt lgkmcnt(2)\n\t" \
    "v_pk_add_f32 v[216:217], v[216:217], v[220:221]\n\t" \
    "v_pk_add_f32 v[218:219], v[218:219], v[222:223]\n\t" \
    "s_waitcnt lgkmcnt(0)\n\t" \
    "v_pk_add_f32 v[224:225], v[224:225], v[228:229]\n\t" \
    "v_pk_add_f32 v[226:227], v[226:227], v[230:231]\n\t" \
    "v_pk_add_f32 v[216:217], v[216:217], v[224:225]\n\t" \
    "v_pk_add_f32 v[218:219], v[218:219], v[226:227]\n\t" \
    "v_pk_add_f32 v[200:201], v[200:201], v[216:217]\n\t" \
    "v_pk_add_f32 v[202:203], v[202:203], v[218:219]\n\t" \
    "v_pk_add_f32 v[200:201], v[200:201], v[240:241]\n\t" \
    "v_pk_add_f32 v[202:203], v[202:203], v[242:243]\n\t" \
    "v_cvt_pk_bf16_f32 v200, v200, v201\n\t" \
    "v_cvt_pk_bf16_f32 v201, v202, v203\n\t" \
    "v_min_u32 v200, -2, v200\n\t" \
    "v_min_u32 v201, -2, v201\n\t" \
    "s_add_u32 %[sx], %[sx], 0x1000000\n\t" \
    "buffer_store_dwordx2 v[200:201], %[xo], %[rd], %[sx] offen sc1\n\t" \
    ".Lxe%=:\n\t" \
    ASM_STW
#define ASM_W4_M \
    "buffer_load_dwordx4 v[184:187], %[l16], %[rs], %[s1] offen offset:2048 sc1\n\t" \
    "buffer_load_dwordx4 v[180:183], %[l16], %[rs], %[s1] offen offset:1024 sc1\n\t" \
    "buffer_load_dwordx4 v[176:179], %[l16], %[rs], %[s1] offen offset:0 sc1\n\t" \
    "buffer_load_dwordx4 v[172:175], %[l16], %[rs], %[s0] offen offset:3072 sc1\n\t" \
    "buffer_load_dwordx4 v[168:171], %[l16], %[rs], %[s0] offen offset:2048 sc1\n\t" \
    "buffer_load_dwordx4 v[164:167], %[l16], %[rs], %[s0] offen offset:1024 sc1\n\t" \
    "buffer_load_dwordx4 v[160:163], %[l16], %[rs], %[s0] offen offset:0 sc1\n\t" \
    "buffer_load_dwordx4 v[220:223], %[l16], %[rs], %[s3] offen offset:3072 sc1\n\t" \
    "v_mfma_f32_16x16x32_bf16 v[224:227], v[28:31], v[188:191], 0\n\t" \
    "v_mfma_f32_16x16x32_bf16 v[228:231], v[60:63], v[188:191], 0\n\t" \
    "v_mfma_f32_16x16x32_bf16 v[232:235], v[92:95], v[188:191], 0\n\t" \
    "v_mfma_f32_16x16x32_bf16 v[236:239], v[124:127], v[188:191], 0\n\t" \
    "v_mfma_f32_16x16x32_bf16 v[240:243], v[156:159], v[188:191], 0\n\t" \
    "s_waitcnt vmcnt(7)\n\t" \
    ".Lc06_%=:\n\t" \
    "v_max_u32 v244, v184, v185\n\t" \
    "v_max3_u32 v244, v186, v187, v244\n\t" \
    "v_cmp_eq_u32 vcc, -1, v244\n\t" \
    "s_cbranch_vccnz .Ls06_%=\n\t" \
    ".Lk06_%=:\n\t" \
    "v_mfma_f32_16x16x32_bf16 v[224:227], v[24:27], v[184:187], v[224:227]\n\t" \
    "v_mfma_f32_16x16x32_bf16 v[228:231], v[56:59], v[184:187], v[228:231]\n\t" \
    "v_mfma_f32_16x16x32_bf16 v[232:235], v[88:91], v[184:187], v[232:235]\n\t" \
    "v_mfma_f32_16x16x32_bf16 v[236:239], v[120:123], v[184:187], v[236:239]\n\t" \
    "v_mfma_f32_16x16x32_bf16 v[240:243], v[152:155], v[184:187], v[240:243]\n\t" \
    "s_waitcnt vmcnt(6)\n\t" \
    ".Lc05_%=:\n\t" \
    "v_max_u32 v244, v180, v181\n\t" \
    "v_max3_u32 v244, v182, v183, v244\n\t" \
    "v_cmp_eq_u32 vcc, -1, v244\n\t" \
    "s_cbranch_vccnz .Ls05_%=\n\t" \
    ".Lk05_%=:\n\t" \
    "v_mfma_f32_16x16x32_bf16 v[224:227], v[20:23], v[180:183], v[224:227]\n\t" \
    "v_mfma_f32_16x16x32_bf16 v[228:231], v[52:55], v[180:183], v[228:231]\n\t" \
    "v_mfma_f32_16x16x32_bf16 v[232:235], v[84:87], v[180:183], v[232:235]\n\t" \
    "v_mfma_f32_16x16x32_bf16 v[236:239], v[116:119], v[180:183], v[236:239]\n\t" \
    "v_mfma_f32_16x16x32_bf16 v[240:243], v[148:151], v[180:183], v[240:243]\n\t" \
    "s_waitcnt vmcnt(5)\n\t" \
    ".Lc04_%=:\n\t" \
    "v_max_u32 v244, v176, v177\n\t" \
    "v_max3_u32 v244, v178, v179, v244\n\t" \
    "v_cmp_eq_u32 vcc, -1, v244\n\t" \
    "s_cbranch_vccnz .Ls04_%=\n\t" \
    ".Lk04_%=:\n\t" \
    "v_mfma_f32_16x16x32_bf16 v[224:227], v[16:19], v[176:179], v[224:227]\n\t" \
    "v_mfma_f32_16x16x32_bf16 v[228:231], v[48:51], v[176:179], v[228:231]\n\t" \
    "v_mfma_f32_16x16x32_bf16 v[232:235], v[80:83], v[176:179], v[232:235]\n\t" \
    "v_mfma_f32_16x16x32_bf16 v[236:239], v[112:115], v[176:179], v[236:239]\n\t" \
    "v_mfma_f32_16x16x32_bf16 v[240:243], v[144:147], v[176:179], v[240:243]\n\t" \
    "s_waitcnt vmcnt(4)\n\t" \
    ".Lc03_%=:\n\t" \
    "v_max_u32 v244, v172, v173\n\t" \
    "v_max3_u32 v244, v174, v175, v244\n\t" \
    "v_cmp_eq_u32 vcc, -1, v244\n\t" \
    "s_cbranch_vccnz .Ls03_%=\n\t" \
    ".Lk03_%=:\n\t" \
    "v_mfma_f32_16x16x32_bf16 v[224:227], v[12:15], v[172:175], v[224:227]\n\t" \
    "v_mfma_f32_16x16x32_bf16 v[228:231], v[44:47], v[172:175], v[228:231]\n\t" \
    "v_mfma_f32_16x16x32_bf16 v[232:235], v[76:79], v[172:175], v[232:235]\n\t" \
    "v_mfma_f32_16x16x32_bf16 v[236:239], v[108:111], v[172:175], v[236:239]\n\t" \
    "v_mfma_f32_16x16x32_bf16 v[240:243], v[140:143], v[172:175], v[240:243]\n\t" \
    "s_waitcnt vmcnt(3)\n\t" \
    ".Lc02_%=:\n\t" \
    "v_max_u32 v244, v168, v169\n\t" \
    "v_max3_u32 v244, v170, v171, v244\n\t" \
    "v_cmp_eq_u32 vcc, -1, v244\n\t" \
    "s_cbranch_vccnz .Ls02_%=\n\t" \
    ".Lk02_%=:\n\t" \
    "v_mfma_f32_16x16x32_bf16 v[224:227], v[8:11], v[168:171], v[224:227]\n\t" \
    "v_mfma_f32_16x16x32_bf16 v[228:231], v[40:43], v[168:171], v[228:231]\n\t" \
    "v_mfma_f32_16x16x32_bf16 v[232:235], v[72:75], v[168:171], v[232:235]\n\t" \
    "v_mfma_f32_16x16x32_bf16 v[236:239], v[104:107], v[168:171], v[236:239]\n\t" \
    "v_mfma_f32_16x16x32_bf16 v[240:243], v[136:139], v[168:171], v[240:243]\n\t" \
    "s_waitcnt vmcnt(2)\n\t" \
    ".Lc01_%=:\n\t" \
    "v_max_u32 v244, v164, v165\n\t" \
    "v_max3_u32 v244, v166, v167, v244\n\t" \
    "v_cmp_eq_u32 vcc, -1, v244\n\t" \
    "s_cbranch_vccnz .Ls01_%=\n\t" \
    ".Lk01_%=:\n\t" \
    "v_mfma_f32_16x16x32_bf16 v[224:227], v[4:7], v[164:167], v[224:227]\n\t" \
    "v_mfma_f32_16x16x32_bf16 v[228:231], v[36:39], v[164:167], v[228:231]\n\t" \
    "v_mfma_f32_16x16x32_bf16 v[232:235], v[68:71], v[164:167], v[232:235]\n\t" \
    "v_mfma_f32_16x16x32_bf16 v[236:239], v[100:103], v[164:167], v[236:239]\n\t" \
    "v_mfma_f32_16x16x32_bf16 v[240:243], v[132:135], v[164:167], v[240:243]\n\t" \
    "s_waitcnt vmcnt(1)\n\t" \
    ".Lc00_%=:\n\t" \
    "v_max_u32 v244, v160, v161\n\t" \
    "v_max3_u32 v244, v162, v163, v244\n\t" \
    "v_cmp_eq_u32 vcc, -1, v244\n\t" \
    "s_cbranch_vccnz .Ls00_%=\n\t" \
    ".Lk00_%=:\n\t" \
    "v_mfma_f32_16x16x32_bf16 v[224:227], v[0:3], v[160:163], v[224:227]\n\t" \
    "v_mfma_f32_16x16x32_bf16 v[228:231], v[32:35], v[160:163], v[228:231]\n\t" \
    "v_mfma_f32_16x16x32_bf16 v[232:235], v[64:67], v[160:163], v[232:235]\n\t" \
    "v_mfma_f32_16x16x32_bf16 v[236:239], v[96:99], v[160:163], v[236:239]\n\t" \
    "v_mfma_f32_16x16x32_bf16 v[240:243], v[128:131], v[160:163], v[240:243]\n\t" \
    "s_waitcnt vmcnt(0)\n\t" \
    ".Lc17_%=:\n\t" \
    "v_max_u32 v244, v220, v221\n\t" \
    "v_max3_u32 v244, v222, v223, v244\n\t" \
    "v_cmp_eq_u32 vcc, -1, v244\n\t" \
    "s_cbranch_vccnz .Ls17_%=\n\t" \
    ".Lk17_%=:\n\t" \
    "buffer_load_dwordx4 v[216:219], %[l16], %[rs], %[s3] offen offset:2048 sc1\n\t" \
    "buffer_load_dwordx4 v[212:215], %[l16], %[rs], %[s3] offen offset:1024 sc1\n\t" \
    "buffer_load_dwordx4 v[208:211], %[l16], %[rs], %[s3] offen offset:0 sc1\n\t" \
    "buffer_load_dwordx4 v[204:207], %[l16], %[rs], %[s2] offen offset:3072 sc1\n\t" \
    "buffer_load_dwordx4 v[200:203], %[l16], %[rs], %[s2] offen offset:2048 sc1\n\t" \
    "buffer_load_dwordx4 v[196:199], %[l16], %[rs], %[s2] offen offset:1024 sc1\n\t" \
    "buffer_load_dwordx4 v[192:195], %[l16], %[rs], %[s2] offen offset:0 sc1\n\t" \
    "v_mfma_f32_16x16x32_bf16 v[160:163], v[28:31], v[220:223], 0\n\t" \
    "ds_write_b128 %[ld], v[224:227] offset:0\n\t" \
    "v_mfma_f32_16x16x32_bf16 v[164:167], v[60:63], v[220:223], 0\n\t" \
    "ds_write_b128 %[ld], v[228:231] offset:6144\n\t" \
    "v_mfma_f32_16x16x32_bf16 v[168:171], v[92:95], v[220:223], 0\n\t" \
    "ds_write_b128 %[ld], v[232:235] offset:12288\n\t" \
    "v_mfma_f32_16x16x32_bf16 v[172:175], v[124:127], v[220:223], 0\n\t" \
    "ds_write_b128 %[ld], v[236:239] offset:18432\n\t" \
    "v_mfma_f32_16x16x32_bf16 v[176:179], v[156:159], v[220:223], 0\n\t" \
    "ds_write_b128 %[ld], v[240:243] offset:24576\n\t" \
    "s_waitcnt vmcnt(6)\n\t" \
    ".Lc16_%=:\n\t" \
    "v_max_u32 v244, v216, v217\n\t" \
    "v_max3_u32 v244, v218, v219, v244\n\t" \
    "v_cmp_eq_u32 vcc, -1, v244\n\t" \
    "s_cbranch_vccnz .Ls16_%=\n\t" \
    ".Lk16_%=:\n\t" \
    "v_mfma_f32_16x16x32_bf16 v[160:163], v[24:27], v[216:219], v[160:163]\n\t" \
    "v_mfma_f32_16x16x32_bf16 v[164:167], v[56:59], v[216:219], v[164:167]\n\t" \
    "v_mfma_f32_16x16x32_bf16 v[168:171], v[88:91], v[216:219], v[168:171]\n\t" \
    "v_mfma_f32_16x16x32_bf16 v[172:175], v[120:123], v[216:219], v[172:175]\n\t" \
    "v_mfma_f32_16x16x32_bf16 v[176:179], v[152:155], v[216:219], v[176:179]\n\t" \
    "s_waitcnt vmcnt(5)\n\t" \
    ".Lc15_%=:\n\t" \
    "v_max_u32 v244, v212, v213\n\t" \
    "v_max3_u32 v244, v214, v215, v244\n\t" \
    "v_cmp_eq_u32 vcc, -1, v244\n\t" \
    "s_cbranch_vccnz .Ls15_%=\n\t" \
    ".Lk15_%=:\n\t" \
    "v_mfma_f32_16x16x32_bf16 v[160:163], v[20:23], v[212:215], v[160:163]\n\t" \
    "v_mfma_f32_16x16x32_bf16 v[164:167], v[52:55], v[212:215], v[164:167]\n\t" \
    "v_mfma_f32_16x16x32_bf16 v[168:171], v[84:87], v[212:215], v[168:171]\n\t" \
    "v_mfma_f32_16x16x32_bf16 v[172:175], v[116:119], v[212:215], v[172:175]\n\t" \
    "v_mfma_f32_16x16x32_bf16 v[176:179], v[148:151], v[212:215], v[176:179]\n\t" \
    "s_waitcnt vmcnt(4)\n\t" \
    ".Lc14_%=:\n\t" \
    "v_max_u32 v244, v208, v209\n\t" \
    "v_max3_u32 v244, v210, v211, v244\n\t" \
    "v_cmp_eq_u32 vcc, -1, v244\n\t" \
    "s_cbranch_vccnz .Ls14_%=\n\t" \
    ".Lk14_%=:\n\t" \
    "v_mfma_f32_16x16x32_bf16 v[160:163], v[16:19], v[208:211], v[160:163]\n\t" \
    "v_mfma_f32_16x16x32_bf16 v[164:167], v[48:51], v[208:211], v[164:167]\n\t" \
    "v_mfma_f32_16x16x32_bf16 v[168:171], v[80:83], v[208:211], v[168:171]\n\t" \
    "v_mfma_f32_16x16x32_bf16 v[172:175], v[112:115], v[208:211], v[172:175]\n\t" \
    "v_mfma_f32_16x16x32_bf16 v[176:179], v[144:147], v[208:211], v[176:179]\n\t" \
    "s_waitcnt vmcnt(3)\n\t" \
    ".Lc13_%=:\n\t" \
    "v_max_u32 v244, v204, v205\n\t" \
    "v_max3_u32 v244, v206, v207, v244\n\t" \
    "v_cmp_eq_u32 vcc, -1, v244\n\t" \
    "s_cbranch_vccnz .Ls13_%=\n\t" \
    ".Lk13_%=:\n\t" \
    "v_mfma_f32_16x16x32_bf16 v[160:163], v[12:15], v[204:207], v[160:163]\n\t" \
    "v_mfma_f32_16x16x32_bf16 v[164:167], v[44:47], v[204:207], v[164:167]\n\t" \
    "v_mfma_f32_16x16x32_bf16 v[168:171], v[76:79], v[204:207], v[168:171]\n\t" \
    "v_mfma_f32_16x16x32_bf16 v[172:175], v[108:111], v[204:207], v[172:175]\n\t" \
    "v_mfma_f32_16x16x32_bf16 v[176:179], v[140:143], v[204:207], v[176:179]\n\t" \
    "s_waitcnt vmcnt(2)\n\t" \
    ".Lc12_%=:\n\t" \
    "v_max_u32 v244, v200, v201\n\t" \
    "v_max3_u32 v244, v202, v203, v244\n\t" \
    "v_cmp_eq_u32 vcc, -1, v244\n\t" \
    "s_cbranch_vccnz .Ls12_%=\n\t" \
    ".Lk12_%=:\n\t" \
    "v_mfma_f32_16x16x32_bf16 v[160:163], v[8:11], v[200:203], v[160:163]\n\t" \
    "v_mfma_f32_16x16x32_bf16 v[164:167], v[40:43], v[200:203], v[164:167]\n\t" \
    "v_mfma_f32_16x16x32_bf16 v[168:171], v[72:75], v[200:203], v[168:171]\n\t" \
    "v_mfma_f32_16x16x32_bf16 v[172:175], v[104:107], v[200:203], v[172:175]\n\t" \
    "v_mfma_f32_16x16x32_bf16 v[176:179], v[136:139], v[200:203], v[176:179]\n\t" \
    "s_waitcnt vmcnt(1)\n\t" \
    ".Lc11_%=:\n\t" \
    "v_max_u32 v244, v196, v197\n\t" \
    "v_max3_u32 v244, v198, v199, v244\n\t" \
    "v_cmp_eq_u32 vcc, -1, v244\n\t" \
    "s_cbranch_vccnz .Ls11_%=\n\t" \
    ".Lk11_%=:\n\t" \
    "v_mfma_f32_16x16x32_bf16 v[160:163], v[4:7], v[196:199], v[160:163]\n\t" \
    "v_mfma_f32_16x16x32_bf16 v[164:167], v[36:39], v[196:199], v[164:167]\n\t" \
    "v_mfma_f32_16x16x32_bf16 v[168:171], v[68:71], v[196:199], v[168:171]\n\t" \
    "v_mfma_f32_16x16x32_bf16 v[172:175], v[100:103], v[196:199], v[172:175]\n\t" \
    "v_mfma_f32_16x16x32_bf16 v[176:179], v[132:135], v[196:199], v[176:179]\n\t" \
    "s_waitcnt vmcnt(0)\n\t" \
    ".Lc10_%=:\n\t" \
    "v_max_u32 v244, v192, v193\n\t" \
    "v_max3_u32 v244, v194, v195, v244\n\t" \
    "v_cmp_eq_u32 vcc, -1, v244\n\t" \
    "s_cbranch_vccnz .Ls10_%=\n\t" \
    ".Lk10_%=:\n\t" \
    "v_mfma_f32_16x16x32_bf16 v[160:163], v[0:3], v[192:195], v[160:163]\n\t" \
    "v_mfma_f32_16x16x32_bf16 v[164:167], v[32:35], v[192:195], v[164:167]\n\t" \
    "v_mfma_f32_16x16x32_bf16 v[168:171], v[64:67], v[192:195], v[168:171]\n\t" \
    "v_mfma_f32_16x16x32_bf16 v[172:175], v[96:99], v[192:195], v[172:175]\n\t" \
    "v_mfma_f32_16x16x32_bf16 v[176:179], v[128:131], v[192:195], v[176:179]\n\t" \
    "ds_write_b128 %[ld], v[160:163] offset:30720\n\t" \
    "ds_write_b128 %[ld], v[164:167] offset:36864\n\t" \
    "ds_write_b128 %[ld], v[168:171] offset:43008\n\t" \
    "s_nop 1\n\t" \
    "ds_write_b128 %[ld], v[172:175] offset:49152\n\t" \
    "s_nop 1\n\t" \
    "ds_write_b128 %[ld], v[176:179] offset:55296\n\t" \
    "s_branch .Lme%=\n\t" \
    ".Ls06_%=:\n\t" \
    "s_cmp_eq_u32 %[n], 0\n\t" \
    "s_cbranch_scc1 .Lk06_%=\n\t" \
    "s_sub_u32 %[n], %[n], 1\n\t" \
    "buffer_load_dwordx4 v[184:187], %[l16], %[rs], %[s1] offen offset:2048 sc1\n\t" \
    "s_waitcnt vmcnt(0)\n\t" \
    "s_branch .Lc06_%=\n\t" \
    ".Ls05_%=:\n\t" \
    "s_cmp_eq_u32 %[n], 0\n\t" \
    "s_cbranch_scc1 .Lk05_%=\n\t" \
    "s_sub_u32 %[n], %[n], 1\n\t" \
    "buffer_load_dwordx4 v[180:183], %[l16], %[rs], %[s1] offen offset:1024 sc1\n\t" \
    "s_waitcnt vmcnt(0)\n\t" \
    "s_branch .Lc05_%=\n\t" \
    ".Ls04_%=:\n\t" \
    "s_cmp_eq_u32 %[n], 0\n\t" \
    "s_cbranch_scc1 .Lk04_%=\n\t" \
    "s_sub_u32 %[n], %[n], 1\n\t" \
    "buffer_load_dwordx4 v[176:179], %[l16], %[rs], %[s1] offen offset:0 sc1\n\t" \
    "s_waitcnt vmcnt(0)\n\t" \
    "s_branch .Lc04_%=\n\t" \
    ".Ls03_%=:\n\t" \
    "s_cmp_eq_u32 %[n], 0\n\t" \
    "s_cbranch_scc1 .Lk03_%=\n\t" \
    "s_sub_u32 %[n], %[n], 1\n\t" \
    "buffer_load_dwordx4 v[172:175], %[l16], %[rs], %[s0] offen offset:3072 sc1\n\t" \
    "s_waitcnt vmcnt(0)\n\t" \
    "s_branch .Lc03_%=\n\t" \
    ".Ls02_%=:\n\t" \
    "s_cmp_eq_u32 %[n], 0\n\t" \
    "s_cbranch_scc1 .Lk02_%=\n\t" \
    "s_sub_u32 %[n], %[n], 1\n\t" \
    "buffer_load_dwordx4 v[168:171], %[l16], %[rs], %[s0] offen offset:2048 sc1\n\t" \
    "s_waitcnt vmcnt(0)\n\t" \
    "s_branch .Lc02_%=\n\t" \
    ".Ls01_%=:\n\t" \
    "s_cmp_eq_u32 %[n], 0\n\t" \
    "s_cbranch_scc1 .Lk01_%=\n\t" \
    "s_sub_u32 %[n], %[n], 1\n\t" \
    "buffer_load_dwordx4 v[164:167], %[l16], %[rs], %[s0] offen offset:1024 sc1\n\t" \
    "s_waitcnt vmcnt(0)\n\t" \
    "s_branch .Lc01_%=\n\t" \
    ".Ls00_%=:\n\t" \
    "s_cmp_eq_u32 %[n], 0\n\t" \
    "s_cbranch_scc1 .Lk00_%=\n\t" \
    "s_sub_u32 %[n], %[n], 1\n\t" \
    "buffer_load_dwordx4 v[160:163], %[l16], %[rs], %[s0] offen offset:0 sc1\n\t" \
    "s_waitcnt vmcnt(0)\n\t" \
    "s_branch .Lc00_%=\n\t" \
    ".Ls17_%=:\n\t" \
    "s_cmp_eq_u32 %[n], 0\n\t" \
    "s_cbranch_scc1 .Lk17_%=\n\t" \
    "s_sub_u32 %[n], %[n], 1\n\t" \
    "buffer_load_dwordx4 v[220:223], %[l16], %[rs], %[s3] offen offset:3072 sc1\n\t" \
    "s_waitcnt vmcnt(0)\n\t" \
    "s_branch .Lc17_%=\n\t" \
    ".Ls16_%=:\n\t" \
    "s_cmp_eq_u32 %[n], 0\n\t" \
    "s_cbranch_scc1 .Lk16_%=\n\t" \
    "s_sub_u32 %[n], %[n], 1\n\t" \
    "buffer_load_dwordx4 v[216:219], %[l16], %[rs], %[s3] offen offset:2048 sc1\n\t" \
    "s_waitcnt vmcnt(0)\n\t" \
    "s_branch .Lc16_%=\n\t" \
    ".Ls15_%=:\n\t" \
    "s_cmp_eq_u32 %[n], 0\n\t" \
    "s_cbranch_scc1 .Lk15_%=\n\t" \
    "s_sub_u32 %[n], %[n], 1\n\t" \
    "buffer_load_dwordx4 v[212:215], %[l16], %[rs], %[s3] offen offset:1024 sc1\n\t" \
    "s_waitcnt vmcnt(0)\n\t" \
    "s_branch .Lc15_%=\n\t" \
    ".Ls14_%=:\n\t" \
    "s_cmp_eq_u32 %[n], 0\n\t" \
    "s_cbranch_scc1 .Lk14_%=\n\t" \
    "s_sub_u32 %[n], %[n], 1\n\t" \
    "buffer_load_dwordx4 v[208:211], %[l16], %[rs], %[s3] offen offset:0 sc1\n\t" \
    "s_waitcnt vmcnt(0)\n\t" \
    "s_branch .Lc14_%=\n\t" \
    ".Ls13_%=:\n\t" \
    "s_cmp_eq_u32 %[n], 0\n\t" \
    "s_cbranch_scc1 .Lk13_%=\n\t" \
    "s_sub_u32 %[n], %[n], 1\n\t" \
    "buffer_load_dwordx4 v[204:207], %[l16], %[rs], %[s2] offen offset:3072 sc1\n\t" \
    "s_waitcnt vmcnt(0)\n\t" \
    "s_branch .Lc13_%=\n\t" \
    ".Ls12_%=:\n\t" \
    "s_cmp_eq_u32 %[n], 0\n\t" \
    "s_cbranch_scc1 .Lk12_%=\n\t" \
    "s_sub_u32 %[n], %[n], 1\n\t" \
    "buffer_load_dwordx4 v[200:203], %[l16], %[rs], %[s2] offen offset:2048 sc1\n\t" \
    "s_waitcnt vmcnt(0)\n\t" \
    "s_branch .Lc12_%=\n\t" \
    ".Ls11_%=:\n\t" \
    "s_cmp_eq_u32 %[n], 0\n\t" \
    "s_cbranch_scc1 .Lk11_%=\n\t" \
    "s_sub_u32 %[n], %[n], 1\n\t" \
    "buffer_load_dwordx4 v[196:199], %[l16], %[rs], %[s2] offen offset:1024 sc1\n\t" \
    "s_waitcnt vmcnt(0)\n\t" \
    "s_branch .Lc11_%=\n\t" \
    ".Ls10_%=:\n\t" \
    "s_cmp_eq_u32 %[n], 0\n\t" \
    "s_cbranch_scc1 .Lk10_%=\n\t" \
    "s_sub_u32 %[n], %[n], 1\n\t" \
    "buffer_load_dwordx4 v[192:195], %[l16], %[rs], %[s2] offen offset:0 sc1\n\t" \
    "s_waitcnt vmcnt(0)\n\t" \
    "s_branch .Lc10_%=\n\t" \
    ".Lme%=:\n\t"
#define ASM_POLL5_I \
    "buffer_load_dwordx4 v[232:235], %[l16], %[rs], %[s0] offen offset:0 sc1\n\t" \
    "buffer_load_dwordx4 v[236:239], %[l16], %[rs], %[s1] offen offset:0 sc1\n\t" \
    "buffer_load_dwordx4 v[240:243], %[l16], %[rs], %[s2] offen offset:0 sc1\n\t" \
    "buffer_load_dwordx4 v[244:247], %[l16], %[rs], %[s3] offen offset:0 sc1\n\t" \
    "buffer_load_dwordx4 v[248:251], %[l16], %[rs], %[s4] offen offset:0 sc1\n\t"
#define ASM_POLL5_C \
    "s_movk_i32 %[n], 0x200\n\t" \
    "v_max_u32 v252, v232, v233\n\t" \
    "v_max3_u32 v252, v234, v235, v252\n\t" \
    "v_max3_u32 v252, v236, v237, v252\n\t" \
    "v_max3_u32 v252, v238, v239, v252\n\t" \
    "v_max3_u32 v252, v240, v241, v252\n\t" \
    "v_max3_u32 v252, v242, v243, v252\n\t" \
    "v_max3_u32 v252, v244, v245, v252\n\t" \
    "v_max3_u32 v252, v246, v247, v252\n\t" \
    "v_max3_u32 v252, v248, v249, v252\n\t" \
    "v_max3_u32 v252, v250, v251, v252\n\t" \
    "v_cmp_eq_u32 vcc, -1, v252\n\t" \
    "s_cbranch_vccz .Lhw%=\n\t" \
    ".Lfw%=:\n\t" \
    "s_mov_b32 %[a], 0\n\t" \
    "v_max_u32 v252, v232, v233\n\t" \
    "v_max3_u32 v252, v234, v235, v252\n\t" \
    "v_cmp_eq_u32 vcc, -1, v252\n\t" \
    "s_cbranch_vccz .Lgw0_%=\n\t" \
    "buffer_load_dwordx4 v[232:235], %[l16], %[rs], %[s0] offen sc1\n\t" \
    "s_mov_b32 %[a], 1\n\t" \
    ".Lgw0_%=:\n\t" \
    "v_max_u32 v252, v236, v237\n\t" \
    "v_max3_u32 v252, v238, v239, v252\n\t" \
    "v_cmp_eq_u32 vcc, -1, v252\n\t" \
    "s_cbranch_vccz .Lgw1_%=\n\t" \
    "buffer_load_dwordx4 v[236:239], %[l16], %[rs], %[s1] offen sc1\n\t" \
    "s_mov_b32 %[a], 1\n\t" \
    ".Lgw1_%=:\n\t" \
    "v_max_u32 v252, v240, v241\n\t" \
    "v_max3_u32 v252, v242, v243, v252\n\t" \
    "v_cmp_eq_u32 vcc, -1, v252\n\t" \
    "s_cbranch_vccz .Lgw2_%=\n\t" \
    "buffer_load_dwordx4 v[240:243], %[l16], %[rs], %[s2] offen sc1\n\t" \
    "s_mov_b32 %[a], 1\n\t" \
    ".Lgw2_%=:\n\t" \
    "v_max_u32 v252, v244, v245\n\t" \
    "v_max3_u32 v252, v246, v247, v252\n\t" \
    "v_cmp_eq_u32 vcc, -1, v252\n\t" \
    "s_cbranch_vccz .Lgw3_%=\n\t" \
    "buffer_load_dwordx4 v[244:247], %[l16], %[rs], %[s3] offen sc1\n\t" \
    "s_mov_b32 %[a], 1\n\t" \
    ".Lgw3_%=:\n\t" \
    "v_max_u32 v252, v248, v249\n\t" \
    "v_max3_u32 v252, v250, v251, v252\n\t" \
    "v_cmp_eq_u32 vcc, -1, v252\n\t" \
    "s_cbranch_vccz .Lgw4_%=\n\t" \
    "buffer_load_dwordx4 v[248:251], %[l16], %[rs], %[s4] offen sc1\n\t" \
    "s_mov_b32 %[a], 1\n\t" \
    ".Lgw4_%=:\n\t" \
    "s_cmp_eq_u32 %[a], 0\n\t" \
    "s_cbranch_scc1 .Lhw%=\n\t" \
    "s_waitcnt vmcnt(20)\n\t" \
    "v_max_u32 v252, v232, v233\n\t" \
    "v_max3_u32 v252, v234, v235, v252\n\t" \
    "v_max3_u32 v252, v236, v237, v252\n\t" \
    "v_max3_u32 v252, v238, v239, v252\n\t" \
    "v_max3_u32 v252, v240, v241, v252\n\t" \
    "v_max3_u32 v252, v242, v243, v252\n\t" \
    "v_max3_u32 v252, v244, v245, v252\n\t" \
    "v_max3_u32 v252, v246, v247, v252\n\t" \
    "v_max3_u32 v252, v248, v249, v252\n\t" \
    "v_max3_u32 v252, v250, v251, v252\n\t" \
    "v_cmp_eq_u32 vcc, -1, v252\n\t" \
    "s_cbranch_vccz .Lhw%=\n\t" \
    "s_sleep 1\n\t" \
    "v_max_u32 v252, v232, v233\n\t" \
    "v_max3_u32 v252, v234, v235, v252\n\t" \
    "v_max3_u32 v252, v236, v237, v252\n\t" \
    "v_max3_u32 v252, v238, v239, v252\n\t" \
    "v_max3_u32 v252, v240, v241, v252\n\t" \
    "v_max3_u32 v252, v242, v243, v252\n\t" \
    "v_max3_u32 v252, v244, v245, v252\n\t" \
    "v_max3_u32 v252, v246, v247, v252\n\t" \
    "v_max3_u32 v252, v248, v249, v252\n\t" \
    "v_max3_u32 v252, v250, v251, v252\n\t" \
    "v_cmp_eq_u32 vcc, -1, v252\n\t" \
    "s_cbranch_vccz .Lhw%=\n\t" \
    "s_sleep 1\n\t" \
    "v_max_u32 v252, v232, v233\n\t" \
    "v_max3_u32 v252, v234, v235, v252\n\t" \
    "v_max3_u32 v252, v236, v237, v252\n\t" \
    "v_max3_u32 v252, v238, v239, v252\n\t" \
    "v_max3_u32 v252, v240, v241, v252\n\t" \
    "v_max3_u32 v252, v242, v243, v252\n\t" \
    "v_max3_u32 v252, v244, v245, v252\n\t" \
    "v_max3_u32 v252, v246, v247, v252\n\t" \
    "v_max3_u32 v252, v248, v249, v252\n\t" \
    "v_max3_u32 v252, v250, v251, v252\n\t" \
    "v_cmp_eq_u32 vcc, -1, v252\n\t" \
    "s_cbranch_vccz .Lhw%=\n\t" \
    "s_sleep 1\n\t" \
    "v_max_u32 v252, v232, v233\n\t" \
    "v_max3_u32 v252, v234, v235, v252\n\t" \
    "v_max3_u32 v252, v236, v237, v252\n\t" \
    "v_max3_u32 v252, v238, v239, v252\n\t" \
    "v_max3_u32 v252, v240, v241, v252\n\t" \
    "v_max3_u32 v252, v242, v243, v252\n\t" \
    "v_max3_u32 v252, v244, v245, v252\n\t" \
    "v_max3_u32 v252, v246, v247, v252\n\t" \
    "v_max3_u32 v252, v248, v249, v252\n\t" \
    "v_max3_u32 v252, v250, v251, v252\n\t" \
    "v_cmp_eq_u32 vcc, -1, v252\n\t" \
    "s_cbranch_vccz .Lhw%=\n\t" \
    "s_sleep 1\n\t" \
    "v_max_u32 v252, v232, v233\n\t" \
    "v_max3_u32 v252, v234, v235, v252\n\t" \
    "v_max3_u32 v252, v236, v237, v252\n\t" \
    "v_max3_u32 v252, v238, v239, v252\n\t" \
    "v_max3_u32 v252, v240, v241, v252\n\t" \
    "v_max3_u32 v252, v242, v243, v252\n\t" \
    "v_max3_u32 v252, v244, v245, v252\n\t" \
    "v_max3_u32 v252, v246, v247, v252\n\t" \
    "v_max3_u32 v252, v248, v249, v252\n\t" \
    "v_max3_u32 v252, v250, v251, v252\n\t" \
    "v_cmp_eq_u32 vcc, -1, v252\n\t" \
    "s_cbranch_vccz .Lhw%=\n\t" \
    "s_sleep 1\n\t" \
    "v_max_u32 v252, v232, v233\n\t" \
    "v_max3_u32 v252, v234, v235, v252\n\t" \
    "v_max3_u32 v252, v236, v237, v252\n\t" \
    "v_max3_u32 v252, v238, v239, v252\n\t" \
    "v_max3_u32 v252, v240, v241, v252\n\t" \
    "v_max3_u32 v252, v242, v243, v252\n\t" \
    "v_max3_u32 v252, v244, v245, v252\n\t" \
    "v_max3_u32 v252, v246, v247, v252\n\t" \
    "v_max3_u32 v252, v248, v249, v252\n\t" \
    "v_max3_u32 v252, v250, v251, v252\n\t" \
    "v_cmp_eq_u32 vcc, -1, v252\n\t" \
    "s_cbranch_vccz .Lhw%=\n\t" \
    "s_sleep 1\n\t" \
    "s_sub_u32 %[n], %[n], 1\n\t" \
    "s_cmp_lg_u32 %[n], 0\n\t" \
    "s_cbranch_scc1 .Lfw%=\n\t" \
    ".Lhw%=:\n\t"
__device__ __forceinline__ void poll5_issue(rsrc_t rs, unsigned l16, unsigned base, u32x4 (&v)[5]) {
    asm volatile(ASM_POLL5_I : "+{v[232:235]}"(v[0]), "+{v[236:239]}"(v[1]), "+{v[240:243]}"(v[2]), "+{v[244:247]}"(v[3]), "+{v[248:251]}"(v[4]) : [l16] "v"(l16), [rs] "s"(rs), [s0] "s"(base), [s1] "s"(base + WIN), [s2] "s"(base + 2 * WIN), [s3] "s"(base + 3 * WIN), [s4] "s"(base + 4 * WIN) : "memory");
}
__device__ __forceinline__ void poll5(rsrc_t rs, unsigned l16, unsigned base, u32x4 (&v)[5], const Params& P) {
#pragma unroll 1
    for (int spins = 1023;; spins += 1024) {
        unsigned n, a;
        asm volatile(ASM_POLL5_C : "+{v[232:235]}"(v[0]), "+{v[236:239]}"(v[1]), "+{v[240:243]}"(v[2]), "+{v[244:247]}"(v[3]), "+{v[248:251]}"(v[4]), [n] "=&s"(n), [a] "=&s"(a) : [l16] "v"(l16), [rs] "s"(rs), [s0] "s"(base), [s1] "s"(base + WIN), [s2] "s"(base + 2 * WIN), [s3] "s"(base + 3 * WIN), [s4] "s"(base + 4 * WIN) : "memory", "vcc", "scc", "v252");
        if (n || spin_fail(spins, P)) break;
    }
}
using u32x8 = __attribute__((ext_vector_type(8))) unsigned;
using u32x16 = __attribute__((ext_vector_type(16))) unsigned;
using u32x32 = __attribute__((ext_vector_type(32))) unsigned;
__device__ __forceinline__ u32x32 cat32(u32x8 a, u32x8 b, u32x8 c, u32x8 d) {
    const u32x16 lo = __builtin_shufflevector(a, b, 0, 1, 2, 3, 4, 5, 6, 7, 8, 9, 10, 11, 12, 13, 14, 15);
    const u32x16 hi = __builtin_shufflevector(c, d, 0, 1, 2, 3, 4, 5, 6, 7, 8, 9, 10, 11, 12, 13, 14, 15);
    return __builtin_shufflevector(lo, hi, 0, 1, 2, 3, 4, 5, 6, 7, 8, 9, 10, 11, 12, 13, 14, 15, 16, 17, 18, 19, 20, 21, 22, 23, 24, 25, 26, 27, 28, 29, 30, 31);
}
__device__ __forceinline__ unsigned c1_dst(int wave, int kind) {
    const int ln = lane_id(), c = ln & 15, g = ln >> 4, hf = (wave >> 1) & 1;
    if (kind == 2) {
        const unsigned b = L_KM + (c >> 2) * 256 + (wave & 1) * 128 + g * 32;
        return (b + hf * 8 + (c & 3) * 2) | (b + 1024 + hf * 1024 + (c & 3) * 4) << 16;
    }
    const unsigned b = L_Q + (c >> 3) * 256 + g * 64 + ((c >> 2) & 1) * 8;
    return (b + (wave & 1) * (kind ? 512 : 1536) + ((c >> 1) & 1) * 32 + hf * 4 + (c & 1) * 2) | (b + 1024 + (wave & 3) * 512 + (c & 1) * 32 + ((c >> 1) & 1) * 4) << 16;
}
__device__ __forceinline__ void dma1(rsrc_t rs, unsigned vo, unsigned so, unsigned dst) {
    asm volatile("s_mov_b32 m0, %3\n\ts_nop 0\n\tbuffer_load_dwordx4 %0, %1, %2 offen nt lds" :: "v"(vo), "s"(rs), "s"(so), "s"(dst) : "memory");
}
using s16x4 = __attribute__((ext_vector_type(4))) short;
__device__ __forceinline__ u32x4 tr_frag(unsigned a, unsigned step) {
    const u32x2 x = __builtin_bit_cast(u32x2, __builtin_amdgcn_ds_read_tr16_b64_v4i16((LAS s16x4*)(size_t)a)), y = __builtin_bit_cast(u32x2, __builtin_amdgcn_ds_read_tr16_b64_v4i16((LAS s16x4*)(size_t)(a + step)));
    return (u32x4){x[0], x[1], y[0], y[1]};
}
#define LDELAY
#define LOOP_CLOB "memory", "vcc", "scc", "v0", "v1", "v2", "v3", "v4", "v5", "v6", "v7", "v8", "v9", "v10", "v11", "v12", "v13", "v14", "v15", "v16", "v17", "v18", "v19", "v20", "v21", "v22", "v23", "v24", "v25", "v26", "v27", "v28", "v29", "v30", "v31", "v32", "v33", "v34", "v35", "v36", "v37", "v38", "v39", "v40", "v41", "v42", "v43", "v44", "v45", "v46", "v47", "v48", "v49", "v50", "v51", "v52", "v53", "v54", "v55", "v56", "v57", "v58", "v59", "v60", "v61", "v62", "v63", "v64", "v65", "v66", "v67", "v68", "v69", "v70", "v71", "v72", "v73", "v74", "v75", "v76", "v77", "v78", "v79", "v80", "v81", "v82", "v83", "v84", "v85", "v86", "v87", "v88", "v89", "v90", "v91", "v92", "v93", "v94", "v95", "v96", "v97", "v98", "v99", "v100", "v101", "v102", "v103", "v104", "v105", "v106", "v107", "v108", "v109", "v110", "v111", "v112", "v113", "v114", "v115", "v116", "v117", "v118", "v119", "v120", "v121", "v122", "v123", "v124", "v125", "v126", "v127", "v128", "v129", "v130", "v131", "v132", "v133", "v134", "v135", "v136", "v137", "v138", "v139", "v140", "v141", "v142", "v143", "v144", "v145", "v146", "v147", "v148", "v149", "v150", "v151", "v152", "v153", "v154", "v155", "v156", "v157", "v158", "v159", "v160", "v161", "v162", "v163", "v164", "v165", "v166", "v167", "v168", "v169", "v170", "v171", "v172", "v173", "v174", "v175", "v176", "v177", "v178", "v179", "v180", "v181", "v182", "v183", "v184", "v185", "v186", "v187", "v188", "v189", "v190", "v191", "v192", "v193", "v194", "v195", "v196", "v197", "v198", "v199", "v200", "v201", "v202", "v203", "v204", "v205", "v206", "v207", "v208", "v209", "v210", "v211", "v212", "v213", "v214", "v215", "v216", "v217", "v218", "v219", "v220", "v221", "v222", "v223", "v224", "v225", "v226", "v227", "v228", "v229", "v230", "v231", "v232", "v233", "v234", "v235", "v236", "v237", "v238", "v239", "v246", "v247", "v248", "v249", "v250", "v251", "v252", "s40", "s41", "s42", "s43", "s44", "s45", "s46", "s47", "s48", "s49", "s50", "s51", "s52", "s53", "s54", "s55", "s56", "s57", "s58", "s59", "s60", "s61", "s62", "s63", "s64", "s65", "s66", "s67", "s68", "s69", "s70", "s71", "s72", "s73", "s74", "s75", "s76", "s77", "s78", "s79"
__device__ __forceinline__ unsigned UNI(unsigned x) {
    unsigned r;
    asm volatile("s_nop 0\n\tv_readfirstlane_b32 %0, %1\n\ts_nop 4" : "=s"(r) : "v"(x));
    return r;
}
__device__ __forceinline__ void note_timeout(const Params& P, unsigned bud) {
    if (bud == 0) __builtin_amdgcn_raw_buffer_store_b32(1u, mkrsrc(P.ws + FL0, 512), 0x100, 0, 16);
}

#define LOOP_IO [bud] "=s"(bud), "+{v253}"(l16), "+{v240}"(a[0]), "+{v241}"(a[1]), "+{v242}"(a[2]), "+{v243}"(a[3]), "+{v244}"(a[4]), "+{v245}"(a[5])
#define LOOP_IN "{v254}"(b2), "{v255}"(dsto), [wave] "s"(wave), [ws] "s"(P.ws), [pp] "s"(P.ws + FL_PAR), [sz] "s"(RG_L), [lx] "s"(L << 13), [wr] "s"(wr), [wm] "s"(wm), [fl] "s"(FL_XN + cpy * 2048), \
                [cj] "s"(REGS - 75 * WIN), [cs] "s"(5 * WIN), [mb] "s"(RG_XM + cpy * XM_COPY)
__device__ __forceinline__ void qkv_addr(unsigned (&a)[6], int w4, int ln, unsigned base) {
#pragma unroll
    for (int k = 0; k < 3; ++k) { const unsigned u = (w4 * 3 + k) * 64 + ln; a[k] = (u / 24) * (HD * 2) + (u % 24) * 16 - k * 1024; }
    a[3] = base + ((ln >> 4) * 8 + ((ln >> 2) & 3)) * 384 + (ln & 3) * 8 + w4 * 96;
    a[4] = a[5] = 0;
}
__device__ __forceinline__ void s_role(const Params& P, int wave, int L, int h, int cpy) {
    const int ln = lane_id();
    unsigned l16 = ln * 16, a[6];
    qkv_addr(a, wave & 3, ln, (wave >> 2) * 12288);
    const rsrc_t wr = mkrsrc((wave < 4 ? P.wq : P.wk) + h * DH, (DM * HD - h * DH) * 2);
    const unsigned wm = UNI(wave * 3072);
    const int mt = (wave >> 1) & 1, hf = wave & 1;
    const unsigned xs = D_XN + mt * 4096 + hf * WIN, x1 = (2 - hf) * WIN + hf * 2048, m0 = L_X + mt * 20480 + hf * 4096, m1 = L_X + mt * 20480 + 8192 + hf * 2048;
    const float SCL = 0.07216878364870322f * 1.4426950408889634f;
    const unsigned px = RG_PX + (h >> 2) * WIN;
    const unsigned pb = wave == 0 ? RG_XP + (h / 12) * WIN + (h % 12) * 640 - L_Q : px + (wave == 2 ? (h & 3) * 1024 : 5120 + ((h >> 1) & 1) * 1024 + (h & 1) * 256);
    unsigned b2 = l16 + (wave < 3 ? L_Q : wave == 3 ? L_QX : L_W + wave * 10240), dsto = c1_dst(wave, 1 + (wave >> 2)), bud;
    asm volatile(ASM_S_LOOP : LOOP_IO
                 : LOOP_IN, [xs] "s"(xs), [x1] "s"(x1), [m0] "s"(m0), [m1] "s"(m1), [qs] "s"(UNI(L_KX + hf * 1024 + mt * 8)), [q2] "s"(UNI(L_KX + 2048 + (wave & 3) * 1024)), [pb] "s"(UNI(pb)),
                   [pm] "s"(UNI(px + (wave == 2 ? 4096 : 7168) + (h & 3) * 256)), [scl] "s"(__float_as_uint(SCL)) LST_IN
                 : LOOP_CLOB);
    note_timeout(P, bud);
}

__device__ __forceinline__ void v_role(const Params& P, int wave, int L, int h, int cpy) {
    const int ln = lane_id(), w4 = wave & 3;
    unsigned l16 = ln * 16, a[6];
    if (wave < 4) qkv_addr(a, w4, ln, 0);
    else {
#pragma unroll
        for (int k = 0; k < 5; ++k) { const unsigned u = k * 64 + ln; a[k] = (u / 20) * (DM * 2) + (u % 20) * 16 - (k & 3) * 1024; }
        a[5] = L_W + w4 * 20480 + (ln >> 2) * 320 + (ln & 3) * 8;
    }
    const unsigned wo = wave < 4 ? h * DH : h * DH * DM + w4 * 160;
    const rsrc_t wr = mkrsrc((wave < 4 ? P.wv : P.wo) + wo, (DM * HD - wo) * 2);
    const unsigned wm = UNI(wave < 4 ? wave * 3072 : L_W + w4 * 20480);
    unsigned b2 = l16 + (wave < 3 ? L_Q : wave == 3 ? L_QX : L_W + wave * 10240), dsto = wave < 4 ? c1_dst(wave, 0) : (l16 >> 8) * WIN + (l16 & 255), bud;
    if (wave >= 4) l16 += L_Q;
    asm volatile(ASM_V_LOOP : LOOP_IO
                 : LOOP_IN, [xs] "s"(D_XN + w4 * WIN), [vm] "s"(VXD - 1), [vx] "s"(vxb(w4, 0) + (h >> 3) * WIN + (h & 7) * 1024 - L_Q),
                   [fo] "s"(FL_VO + (w4 >> 1) * WIN + ((w4 & 1) * 32 + h) * 128 - L_Q), [vo] "s"(RG_VM + w4 * 10 * WIN + (h >> 2) * 1024 + (h & 3) * 256) LST_IN
                 : LOOP_CLOB);
    note_timeout(P, bud);
}

#define L1DEL 4
#define W1DEL 3
__device__ __forceinline__ void l1_delay(int n = L1DEL) { for (int i = 0; i < n; ++i) __builtin_amdgcn_s_sleep(127); }
__device__ __forceinline__ void x_tile(const Params& P, rsrc_t rr, int tile) {
    const int ln = lane_id(), l15 = ln & 15, l4 = ln >> 4;
    u32x4 v[20];
    float s = 0.f, q = 0.f;
#pragma unroll
    for (int c = 0; c < 20; ++c) v[c] = *(const u32x4*)(P.x + (size_t)(tile * 16 + l15) * DM + c * 32 + l4 * 8);
#pragma unroll
    for (int c = 0; c < 20; ++c)
#pragma unroll
        for (int e = 0; e < 4; ++e) s += lo2f(v[c][e]) + hi2f(v[c][e]);
    sum2_rows(s, q);
#pragma unroll
    for (int c = 0; c < 20; ++c) asm volatile("" : "+v"(v[c]));
    const float mean = s * (1.0f / DM);
#pragma unroll
    for (int c = 0; c < 20; ++c)
#pragma unroll
        for (int e = 0; e < 4; ++e) { const float a = lo2f(v[c][e]) - mean, b = hi2f(v[c][e]) - mean; q += a * a + b * b; }
    sum2_rows(s, q);
#pragma unroll
    for (int c = 0; c < 20; ++c) asm volatile("" : "+v"(v[c]));
    const float m1 = rbf(mean), r = 1.0f / ln_den(q * (1.0f / DM));
    const unsigned sb = segb(tile >> 1) + (tile & 1) * 4096;
#pragma unroll
    for (int c = 0; c < 20; ++c) {
        u32x4 n;
#pragma unroll
        for (int e = 0; e < 4; ++e) n[e] = pack2(rbf(lo2f(v[c][e]) - m1) * r, rbf(hi2f(v[c][e]) - m1) * r);
        st128(n, rr, ln * 16 + (c & 3) * 1024, D_XN + sb + (c >> 2) * WIN);
        st128(v[c], rr, ln * 16 + (c & 3) * 1024, D_XR + sb + (c >> 2) * WIN);
    }
}
__device__ __forceinline__ void wait_go(const Params& P, rsrc_t rd, int k) {
    for (int spins = 0;; ++spins) {
        if (__builtin_amdgcn_readfirstlane(__builtin_amdgcn_raw_buffer_load_b32(rd, 0, FL_VO + k * 128, 16))) break;
        __builtin_amdgcn_s_sleep(16);
        if (spin_fail(spins * 4 + 3, P)) break;
    }
    asm volatile("" ::: "memory");
}

constexpr unsigned R_RED = 0;
constexpr unsigned R_PART = 8192;
__device__ __forceinline__ void r_role(const Params& P, int wave, int L, int j, int cpy) {
    const int lane = lane_id(), l15 = lane & 15, l4 = lane >> 4;
    const unsigned l16 = lane * 16;
    const rsrc_t rr = mkrsrc(lbase(P, L), RG_L), rd = rr;
    const int sidx = (lane & 2) ? 0xEEEE : 0x4444;
    unsigned offp[3];
    auto hq = [&](int q) { return RG_XP + cpy * XP_COPY + (q / 3) * WIN + (q % 3) * 2560; };
#define ROT 1
    const int hg = (wave + ROT * j) & 7;
    offp[0] = hq(hg) + (lane >> 5) * 640 + 128 + (lane & 31) * 16;
    offp[1] = offp[0] + 1280;
    offp[2] = (lane & 2) ? OOB : hq(hg) + l4 * 640 + ((l15 >> 2) * 2 + (l15 & 1)) * 16;
    const int j10 = j % 10;
    const unsigned arp = (wave < 3 && j10 < 8 && wave * 1024 + l16 < 2560) ? hq(j10) + (j / 10 - cpy) * XP_COPY + wave * 1024 + l16 : OOB;
    const unsigned arx = RG_PX + (j >> 2) * WIN + (j & 3) * 2048 + (wave & 1) * 1024 + l16;
    const unsigned offv = RG_VM + j * WIN + hg * 1024 + l16;
    const unsigned offo = (wave < (int)NCP) ? RG_XM + wave * XM_COPY + (j >> 5) * WIN + (j & 31) * 256 + lane * 4 : OOB;
    const int pubw = 1 - (wave >> 2);
    {
        if (L) {
            const rsrc_t r0 = mkrsrc(lbase(P, 0), RG_L);
            const unsigned fo0 = lane < 32 ? (lane >> 3) * 2048 + (lane & 7) * 16 : OOB;
            if (wave || j >= 16) l1_delay();
            if (j < 16) {
                x_tile(P, r0, wave * 16 + j);
                wait_vm<0>();
                if (wave == 0) st128(sent4() & 8u, r0, fo0, FL_XN + j * 128);
                wait_vm<0>();
            }
            bar();
            if (wave == 0 && j < 16) st128(sent4(), r0, fo0, FL_XN + j * 128);
        }
        const u32x4 s4 = sent4();
#pragma unroll
        for (int k = 0; k < (int)NSLOT; ++k) {
            st128(s4, rr, offv, k * VM_SLOT);
            if (k) __builtin_amdgcn_raw_buffer_store_b32(SENT, rr, offo, k * XM_SLOT, 16);
            if (wave < 3) st128(s4, rr, arp, k * XP_SLOT);
            if ((wave >> 1) == 2 && j < 32) st128(s4, rr, arx, k * PX_SLOT);
        }
        for (int u = j; u < 124; u += 40) st128(s4, rr, wave * 1024 + l16, u < 108 ? D_XA + (u < 60 ? u / 20 : 3) * 4 * REG + (u < 60 ? u % 20 : u - 60) * WIN : RG_ST + (u - 108) * WIN);
        wait_vm<0>();
        bar();
        const int row = l4 * 2 + ((lane >> 2) & 1), col = 16 * j + 4 * (lane & 3) + 2 * ((lane >> 3) & 1);
        __builtin_amdgcn_raw_buffer_store_b32(nosent(*(const unsigned*)(P.mem0 + row * DM + col)), rr, offo, 0, 16);
    }
    auto red_addr = [](int fl) { return R_RED + fl * 80 + (fl >> 4) * 16; };
    const unsigned rw0 = red_addr(l4 * 16 + ((l15 >> 1) & 1) * 8 + (l15 >> 2)) + (l15 & 1) * 4 + (hg ^ (l15 >> 3)) * 8;
    const unsigned rrd = red_addr(lane);
    u32x16 vx;
    const rsrc_t rf = rr;
    const int jq = j / 10;
    const unsigned fo = (lane & 3) * 128, fs = FL_VO + (jq >> 1) * WIN + ((jq & 1) * 32 + hg * 4) * 128, vxo = (unsigned)((j - jq * 10) * 4 + (hg >> 1)) * WIN + (hg & 1) * 4096;
    const unsigned xo = (l4 >> 1) * 256 + l15 * 16 + (l4 & 1) * 8, pw = R_PART + hg * 1024 + l16, pr = R_PART + (wave & 1) * 8192 + l16;
    const int fin = (wave + 2) >> 3;
    asm volatile("" : "={v[160:175]}"(vx));
    auto vx_fetch = [&](int t) {
        for (int spins = 0;; ++spins) {
            const unsigned f = __builtin_amdgcn_raw_buffer_load_b32(rf, fo, fs, 16);
            if (__all(f > (unsigned)t)) break;
            __builtin_amdgcn_s_sleep(2);
            if (spin_fail(spins, P)) break;
        }
        asm volatile("buffer_load_dwordx4 v[160:163], %1, %2, %3 offen sc1\n\tbuffer_load_dwordx4 v[164:167], %1, %2, %3 offen offset:1024 sc1\n\t"
                     "buffer_load_dwordx4 v[168:171], %1, %2, %3 offen offset:2048 sc1\n\tbuffer_load_dwordx4 v[172:175], %1, %2, %3 offen offset:3072 sc1"
                     : "+{v[160:175]}"(vx) : "v"(l16), "s"(rd), "s"(vxb(jq, t) + vxo) : "memory");
    };
    vx_fetch(0);
    unsigned tb = 0;
    STDECL;
    for (int t = 0; t < NSEG; ++t) {
        const unsigned slot = t & (NSLOT - 1);
        ST(0);
        u32x4 vm = sent4(), pm = vm;
        u32x8 pv = __builtin_shufflevector(vm, vm, 0, 1, 2, 3, 0, 1, 2, 3);
#pragma unroll 1
        for (int spins = 1023;; spins += 1024) {
            unsigned n, a;
            ASM_ST_DECL;
            asm volatile(ASM_RCRIT : "+{v[180:183]}"(vm), "+{v[184:191]}"(pv), "+{v[192:195]}"(pm), [n] "=&s"(n), [a] "=&s"(a) ASM_ST_OUT ASM_ST2_OUT ASM_ST3_OUT
                         : [rs] "s"(rr), [ov] "v"(offv), [sv] "s"(slot * VM_SLOT), [o0] "v"(offp[0]), [o1] "v"(offp[1]), [o2] "v"(offp[2]), [sp] "s"(slot * XP_SLOT),
                           "{v[160:175]}"(vx), [sidx] "v"(sidx), [rw] "v"(rw0), [rd] "v"(rrd), [oo] "v"(offo),
                           [so] "s"(((t + 1) & (NSLOT - 1)) * XM_SLOT), [pub] "s"(pubw * (NSEG - 1 - t))
                         : "memory", "vcc", "scc", "v176", "v177", "v178", "v179", "v196", "v200", "v201", "v202", "v203", "v204", "v205", "v206", "v207", "v208", "v209", "v210", "v211", "v212", "v213", "v214", "v215");
            ASM_ST_SAVE(2); ASM_ST2_SAVE(1); ASM_ST3_SAVE(7);
            if (__builtin_amdgcn_readfirstlane(n)) break;
            if (spin_fail(spins, P)) { bar(); break; }
        }
        ST(3);
        if (wave >= 4) __builtin_amdgcn_s_sleep(RXDEL);
        if ((wave >> 1) == 2 && j < 32 && t >= 2) st128(sent4(), rr, arx, ((t - 2) & (NSLOT - 1)) * PX_SLOT);
        unsigned early;
#pragma unroll 1
        for (int spins = 1023;; spins += 1024) {
            unsigned n, sx = D_XR + segb(t) + (j >> 3) * WIN + (wave & 1) * 4096 + (j & 7) * 512;
            const unsigned pxb = RG_PX + slot * PX_SLOT + hg * WIN;
            ASM_ST_DECL;
            asm volatile(ASM_RX : "+{v[160:175]}"(vx), [n] "=&s"(n), [a] "=&s"(early), [sx] "+s"(sx) ASM_ST_OUT ASM_ST2_OUT
                         : "{v[180:183]}"(vm), "{v[184:191]}"(pv), "{v[192:195]}"(pm), [l16] "v"(l16), [rs] "s"(rr), [s0] "s"(pxb), [s1] "s"(pxb + 4096), [rd] "s"(rd), [xo] "v"(xo),
                           [fo] "v"(fo), [rf] "s"(rf), [fs] "s"(fs), [tn] "s"(t + 1), [sv] "s"(vxb(jq, t + 1) + vxo),
                           [pw] "v"(pw), [pr] "v"(pr), [fin] "s"(fin)
                         : "memory", "vcc", "scc", "v196", "v200", "v201", "v202", "v203", "v204", "v205", "v206", "v207", "v208", "v209", "v210", "v211", "v212", "v213", "v214", "v215", "v216", "v217", "v218", "v219", "v220", "v221", "v222", "v223", "v224", "v225", "v226", "v227", "v228", "v229", "v230", "v231", "v232", "v233", "v234", "v235", "v236", "v237", "v238", "v239", "v240", "v241", "v242", "v243", "v244", "v245", "v246", "v247", "v248", "v249", "v250", "v251");
            ASM_ST_SAVE(4); ASM_ST2_SAVE(5);
            if (__builtin_amdgcn_readfirstlane(n)) break;
            early = 0;
            if (spin_fail(spins, P)) { bar(); break; }
        }
        STFLUSH(t);
        st128(sent4(), rr, offv, slot * VM_SLOT);
        __builtin_amdgcn_raw_buffer_store_b32(SENT, rr, offo, slot * XM_SLOT, 16);
        if (wave < 3 && t >= 1) st128(sent4(), rr, arp, ((t - 1) & (NSLOT - 1)) * XP_SLOT);
        if (wave == 5 && t + AH < NSEG) st128(sent4(), rd, (lane >> 5) * 4096 + (lane & 31) * 16, D_XA + segb(t + AH) + (j >> 3) * WIN + (j & 7) * 512);
        if (t + 1 < NSEG && !__builtin_amdgcn_readfirstlane(early)) vx_fetch(t + 1);
    }
}

template <int WHICH>
__device__ __forceinline__ void w_role(const Params& P, int wave, int L, int cu) {
    constexpr int NT = (WHICH == 3) ? 2 : 1;
    constexpr int KS = 20, PW = 5;
    constexpr int N = (WHICH == 3) ? FF : DM;
    constexpr unsigned W_ST = 2 * 49152;
    const int ln = lane_id(), l15 = ln & 15, l4 = ln >> 4;
    const unsigned l16 = ln * 16;
    const rsrc_t rd = mkrsrc(lbase(P, L), RG_L);
    const int nt0 = (cu * 8 + wave) * NT;
    bf16x8 wf[NT][KS];
    if (!L) l1_delay(WHICH == 1 ? W1DEL : L1DEL);
    {
        constexpr int RB = NT * 256, RPI = 1024 / RB;
        const rsrc_t wr = mkrsrc((WHICH == 1 ? P.w1 : WHICH == 2 ? P.w2 : P.w3) + cu * (RB / 2), DM * N * 2 - cu * RB);
        const unsigned vo = (l16 / RB) * (N * 2) + (l16 % RB), ta = (l4 * 8 + ((ln >> 2) & 3)) * RB + (ln & 3) * 8 + wave * NT * 32;
#pragma unroll
        for (int r = 0; r < NT; ++r) {
            if (r) bar();
            for (int i = 0; i < 20; ++i) dma1(wr, vo, (unsigned)((r * 160 + i * 8 + wave) * RPI) * (N * 2), (i * 8 + wave) * 1024);
            wait_vm<0>();
            bar();
#pragma unroll
            for (int k = 0; k < KS / NT; ++k)
#pragma unroll
                for (int n = 0; n < NT; ++n) wf[n][r * (KS / NT) + k] = BF8(tr_frag(ta + k * 32 * RB + n * 32, 4 * RB));
        }
        bar();
    }
    wait_go(P, rd, WHICH * 8 + cu);
    f32x4 cs[NT];
    if (WHICH != 2) {
        const bf16x8 ones = __builtin_bit_cast(bf16x8, ((u32x4){0x3F803F80u, 0x3F803F80u, 0x3F803F80u, 0x3F803F80u}));
#pragma unroll
        for (int n = 0; n < NT; ++n) {
            cs[n] = (f32x4){0.f, 0.f, 0.f, 0.f};
#pragma unroll
            for (int k = 0; k < KS; ++k) MFMA(wf[n][k], ones, cs[n]);
        }
    }
    const int grp = nt0 / 10, sh = (WHICH == 1) ? ((grp & 1) ? -1 : 1) * ((grp >> 1) + 1) : 0;
    if (WHICH == 1) {
        const int zr = (grp & 1) ? S_ - 1 - l15 : l15;
        unsigned zv;
        asm volatile("v_mov_b32 %0, 0" : "=v"(zv));
        st128(((u32x4){zv, zv, zv, zv}), rd, (l15 <= (grp >> 1) && l4 < 2) ? toff(zr, nt0 * 16 + l4 * 8) : OOB, D_HS);
    }
    constexpr unsigned D_IN = WHICH == 1 ? D_XA : WHICH == 2 ? D_HS : D_X1;
    constexpr unsigned D_OUT = WHICH == 1 ? D_HS : WHICH == 2 ? D_X1 : D_H2;
    u32x4 v[PW] = {};
    poll5_issue(rd, l16, D_IN + wave * 1024, v);
    wait_vm<0>();
    STDECL;
    for (int t = 0; t < NSEG; ++t) {
        const unsigned ab = (t & 1) * 49152u;
        ST(0);
        {
            poll5(rd, l16, D_IN + segb(t) + wave * 1024, v, P);
            ST(1);
#pragma unroll
            for (int k = 0; k < PW; ++k) lds_st<u32x4>(ab + ((wave >> 2) * 20 + (wave & 3) + 4 * k) * 1024 + l16, v[k]);
            if (WHICH != 2) {
                float s = 0.f, q = 0.f;
                const bf16x2 one2 = __builtin_bit_cast(bf16x2, 0x3F803F80u);
#pragma unroll
                for (int k = 0; k < PW; ++k)
#pragma unroll
                    for (int e = 0; e < 4; ++e) {
                        const unsigned u = v[k][e];
                        const bf16x2 x = __builtin_bit_cast(bf16x2, u);
                        s = __builtin_amdgcn_fdot2_f32_bf16(x, one2, s, false);
                        q = __builtin_amdgcn_fdot2_f32_bf16(x, x, q, false);
                    }
                sum2_rows(s, q);
                if (ln < 16) lds_st<f32x2>(W_ST + (t & 1) * 1024 + wave * 128 + ln * 8, (f32x2){s, q});
            }
        }
        bar();
        ST(2);
        u32x2 r1[2], sv[2];
        if (WHICH == 2) {
            const unsigned ro = toff(l15, nt0 * 16 + l4 * 4);
#pragma unroll
            for (int mt = 0; mt < 2; ++mt) { r1[mt] = LD64(rd, ro + mt * 4096, D_XA + segb(t)); sv[mt] = LD64(rd, (mt * 16 + l15) * 8, D_SS + (t >> 5) * WIN + (t & 31) * 256); }
        }
        float mr[2], rs[2];
        f32x2 st[2][4];
        if (WHICH != 2) {
#pragma unroll
            for (int w = 0; w < 8; ++w) st[w >> 2][w & 3] = lds_ld<f32x2>(W_ST + (t & 1) * 1024 + w * 128 + l15 * 8);
        }
        auto stats = [&](int mt) {
            float a[2];
#pragma unroll
            for (int e = 0; e < 2; ++e) {
                float x0 = st[mt][0][e], x1 = st[mt][1][e], x2 = st[mt][2][e], x3 = st[mt][3][e];
                asm("" : "+v"(x0), "+v"(x1), "+v"(x2), "+v"(x3));
                x0 += x1;
                asm("" : "+v"(x0));
                a[e] = x0 + (x2 + x3);
                asm("" : "+v"(a[e]));
            }
            float mean = a[0] * (1.0f / DM);
            asm("" : "+v"(mean));
            const float var = __builtin_fmaxf(a[1] * (1.0f / DM) - mean * mean, 0.f);
            const float m1 = rbf(mean), d1 = ln_den(var), r1i = rcp(d1);
            if (WHICH == 1) {
                const float m2 = rbf((mean - m1) * r1i), d2 = ln_den(var * r1i * r1i);
                rs[mt] = r1i * rcp(d2); mr[mt] = -(m1 + m2 * d1) * rs[mt];
                if (cu == 0 && wave == 0)
                    __builtin_amdgcn_raw_buffer_store_b64((u32x2){__float_as_uint(m1), __float_as_uint(r1i)}, rd, ln < 16 ? (mt * 16 + ln) * 8 : OOB, D_SS + (t >> 5) * WIN + (t & 31) * 256, 16);
            } else { rs[mt] = r1i; mr[mt] = -m1 * r1i; }
            asm volatile("" : "+v"(rs[mt]), "+v"(mr[mt]));
        };
        f32x4 acc[NT][2];
        unsigned o[2][NT][2];
        auto fin = [&](int mt, int n, int hf) {
            float v[2];
#pragma unroll
            for (int e = 0; e < 2; ++e) {
                if (WHICH != 2) v[e] = gelu_tanh(__builtin_fmaf(acc[n][mt][hf * 2 + e], rs[mt], cs[n][hf * 2 + e] * mr[mt]));
                else v[e] = rbf(((e ? hi2f(r1[mt][hf]) : lo2f(r1[mt][hf])) - __uint_as_float(sv[mt][0])) * __uint_as_float(sv[mt][1])) + acc[n][mt][hf * 2 + e];
            }
            asm("" : "+v"(v[0]));
            asm("" : "+v"(v[1]));
            o[mt][n][hf] = nosent(pack2(v[0], v[1]));
            asm volatile("" : "+v"(o[mt][n][hf]));
        };
        const unsigned o3 = (unsigned)((l4 & 1) * 512 + (l4 >> 1) * 256 + l15 * 16), c3 = D_H2 + (nt0 >> 3) * WIN + ((nt0 >> 1) & 3) * 1024, b3 = c3 + segh(t);
#pragma unroll
        for (int n = 0; n < NT; ++n) { acc[n][0] = (f32x4){0.f, 0.f, 0.f, 0.f}; acc[n][1] = acc[n][0]; }
        {
            constexpr int RG = 8;
            bf16x8 a[RG];
#pragma unroll
            for (int u = 0; u < RG - 1; ++u) a[u] = lds_ld<bf16x8>(ab + l16 + u * 1024);
#pragma unroll
            for (int u = 0; u < 2 * KS; ++u) {
                if (u + RG - 1 < 2 * KS) a[(u + RG - 1) % RG] = lds_ld<bf16x8>(ab + l16 + (u + RG - 1) * 1024);
                SB();
#pragma unroll
                for (int n = 0; n < NT; ++n) MFMA(wf[n][u % KS], a[u % RG], acc[n][u / KS]);
                if (WHICH != 2 && (u == 4 || u == 12)) stats(u == 12);
                if (u == (WHICH == 3 ? 22 : 6)) poll5_issue(rd, l16, D_IN + segb(t + 1) + wave * 1024, v);
                if (WHICH == 3 && u == 38) wait_vm<0>();
                if (u >= 24 && u % 4 == 0 && (u - 24) / 4 < 2 * NT) fin(0, (u - 24) / 8, ((u - 24) / 4) & 1);
                if (WHICH == 3 && u == 38) st128(merge16((u32x2){o[0][0][0], o[0][0][1]}, (u32x2){o[0][NT - 1][0], o[0][NT - 1][1]}), rd, o3, b3);
                SB();
            }
        }
        ST(3);
#pragma unroll
        for (int k = 0; k < 2 * NT; ++k) fin(1, k >> 1, k & 1);
        if (WHICH == 3) {
            st128(merge16((u32x2){o[1][0][0], o[1][0][1]}, (u32x2){o[1][NT - 1][0], o[1][NT - 1][1]}), rd, o3, b3 + 4096);
            ST(4);
            if (t + AH < NSEG) { st128(sent4(), rd, l16, c3 + segh(t + AH)); st128(sent4(), rd, l16, c3 + segh(t + AH) + 4096); }
        } else {
            wait_vm<0>();
            const int orow = t * SEG + (l4 & 1) * 16 + l15 + sh;
            st128(merge16((u32x2){o[0][0][0], o[0][0][1]}, (u32x2){o[1][0][0], o[1][0][1]}), rd, (unsigned)orow < (unsigned)S_ ? toff(orow, nt0 * 16 + (l4 >> 1) * 8) : OOB, D_OUT);
            ST(4);
            if (t + AH < NSEG) st128(sent4(), rd, orow + AH * SEG < S_ ? toff(orow + AH * SEG, nt0 * 16 + (l4 >> 1) * 8) : OOB, D_OUT);
        }
        STFLUSH(t);
    }
    wait_vm<0>();
    asm volatile("" :: "v"(v[0]), "v"(v[1]), "v"(v[2]), "v"(v[3]), "v"(v[4]));
}

__device__ __forceinline__ void w4_role(const Params& P, int wave, int L, int cu) {
    const int ln = lane_id(), l15 = ln & 15, l4 = ln >> 4;
    const unsigned l16 = ln * 16;
    const rsrc_t rd = mkrsrc(lbase(P, L), RG_L);
    STDECL;
    if (!L) l1_delay();
    if (wave >= 6) wait_go(P, rd, 32 + cu);
    if (wave < 6) {
        u32x32 w[5];
        {
            const rsrc_t wr = mkrsrc(P.w4 + wave * 256 * DM + cu * 80, (FF * DM - wave * 256 * DM - cu * 80) * 2);
            const unsigned ta = wave * 20480 + (l4 * 8 + ((ln >> 2) & 3)) * 160 + (ln & 3) * 8;
            u32x4 f[5][8];
#pragma unroll
            for (int r = 0; r < 2; ++r) {
#pragma unroll
                for (int i = 0; i < 5; ++i) {
                    const unsigned u = i * 64 + ln, vo = (u / 10) * (DM * 2) + (u % 10) * 16;
#pragma unroll
                    for (int k = 0; k < 4; ++k) dma1(wr, vo, (r * 128 + k * 32) * (DM * 2), wave * 20480 + k * 5120 + i * 1024);
                }
                wait_vm<0>();
#pragma unroll
                for (int k = 0; k < 4; ++k)
#pragma unroll
                    for (int n = 0; n < 5; ++n) f[n][r * 4 + k] = tr_frag(ta + k * 5120 + n * 32, 640);
                asm volatile("s_waitcnt lgkmcnt(0)" ::: "memory");
                SB();
            }
#pragma unroll
            for (int n = 0; n < 5; ++n) {
                u32x8 g[4];
#pragma unroll
                for (int i = 0; i < 4; ++i) g[i] = __builtin_shufflevector(f[n][2 * i], f[n][2 * i + 1], 0, 1, 2, 3, 4, 5, 6, 7);
                w[n] = cat32(g[0], g[1], g[2], g[3]);
            }
            asm volatile("" : "+{v[0:31]}"(w[0]), "+{v[32:63]}"(w[1]), "+{v[64:95]}"(w[2]), "+{v[96:127]}"(w[3]), "+{v[128:159]}"(w[4]));
        }
        wait_go(P, rd, 32 + cu);
        bar();
        unsigned budget = 1u << 17;
        for (int t = 0; t < NSEG; ++t) {
            ST(0);
            const unsigned gb = D_H2 + segh(t) + wave * 2 * WIN;
            u32x4 can;
            for (int spins = 0;; ++spins) {
                can = LD128(rd, l16, gb + WIN + 3072);
                if (__all(umax4(0u, can) != SENT)) break;
                __builtin_amdgcn_s_sleep(1);
                if (spin_fail(spins, P)) { budget = 0; break; }
            }
            ST(1);
            asm volatile(ASM_W4_M : [n] "+s"(budget)
                         : "{v[188:191]}"(can), "{v[0:31]}"(w[0]), "{v[32:63]}"(w[1]), "{v[64:95]}"(w[2]), "{v[96:127]}"(w[3]), "{v[128:159]}"(w[4]),
                           [ld] "v"(l16 + wave * 1024 + (t & 1) * 61440), [l16] "v"(l16), [rs] "s"(rd), [s0] "s"(gb), [s1] "s"(gb + WIN), [s2] "s"(gb + 4096), [s3] "s"(gb + WIN + 4096)
                         : "memory", "vcc", "scc", "v160", "v161", "v162", "v163", "v164", "v165", "v166", "v167", "v168", "v169", "v170", "v171", "v172", "v173", "v174", "v175", "v176", "v177", "v178", "v179", "v180", "v181", "v182", "v183", "v184", "v185", "v186", "v187", "v192", "v193", "v194", "v195", "v196", "v197", "v198", "v199", "v200", "v201", "v202", "v203", "v204", "v205", "v206", "v207", "v208", "v209", "v210", "v211", "v212", "v213", "v214", "v215", "v216", "v217", "v218", "v219", "v220", "v221", "v222", "v223", "v224", "v225", "v226", "v227", "v228", "v229", "v230", "v231", "v232", "v233", "v234", "v235", "v236", "v237", "v238", "v239", "v240", "v241", "v242", "v243", "v244");
            ST(2);
            bar();
            ST(3);
            STFLUSH(t);
        }
    } else {
        const int mt = wave - 6;
        bar();
        const rsrc_t rn = mkrsrc(lbase(P, 1), RG_L), rr = rd, ro = mkrsrc(P.out, ACT);
        auto tl = [&](int n) { return (unsigned)((cu * 5 + n) >> 3) * WIN + ((cu * 5 + n) & 7) * 512 + mt * 4096; };
        const unsigned o8 = (l4 >> 1) * 256 + l15 * 16 + (l4 & 1) * 8, o16 = tl(l4 & 1) + (l4 >> 1) * 256 + l15 * 16, o16b = tl(2 + (l4 & 1)) + (l4 >> 1) * 256 + l15 * 16;
        const unsigned q8 = l15 * (DM * 2) + l4 * 8, q16 = l15 * (DM * 2) + (l4 & 1) * 32 + (l4 >> 1) * 16;
        for (int t = 0; t < NSEG; ++t) {
            const unsigned sb = segb(t);
            ST(0);
            f32x4 res[5];
            for (int spins = 0;; ++spins) {
                u32x2 r1[5], r2[5];
                unsigned m = 0;
#pragma unroll
                for (int n = 0; n < 5; ++n) { r1[n] = LD64(rd, o8, D_X1 + sb + tl(n)); r2[n] = LD64(rd, o8, D_XA + sb + tl(n)); }
#pragma unroll
                for (int n = 0; n < 5; ++n) {
                    m = umax4(m, (u32x4){r1[n][0], r1[n][1], r2[n][0], r2[n][1]});
                    res[n] = (f32x4){lo2f(r1[n][0]), hi2f(r1[n][0]), lo2f(r1[n][1]), hi2f(r1[n][1])} + (f32x4){lo2f(r2[n][0]), hi2f(r2[n][0]), lo2f(r2[n][1]), hi2f(r2[n][1])};
                }
                if (__all(m != SENT)) break;
                __builtin_amdgcn_s_sleep(8);
                if (spin_fail(spins * 4 + 3, P)) break;
            }
            asm volatile("" : "+v"(res[0]), "+v"(res[1]), "+v"(res[2]), "+v"(res[3]), "+v"(res[4]));
            ST(1);
            bar();
            ST(2);
            {
                const unsigned la = (t & 1) * 61440 + mt * 30720 + l16;
                f32x4 c[30];
#pragma unroll
                for (int i = 0; i < 30; ++i) c[i] = lds_ld<f32x4>(la + ((i % 5) * 6 + i / 5) * 1024);
#pragma unroll
                for (int i = 0; i < 30; ++i) { res[i % 5] += c[i]; asm volatile("" : "+v"(res[i % 5])); }
            }
            u32x2 o[5];
#pragma unroll
            for (int n = 0; n < 5; ++n) o[n] = (u32x2){pack2(res[n][0], res[n][1]), pack2(res[n][2], res[n][3])};
            if (L) {
                const unsigned ob = (unsigned)(t * 2 + mt) * 20480 + cu * 160;
                st128_<0>(merge16(o[0], o[1]), ro, q16, ob); st128_<0>(merge16(o[2], o[3]), ro, q16, ob + 64);
                __builtin_amdgcn_raw_buffer_store_b64(o[4], ro, q8, ob + 128, 0);
            } else {
                float s = 0.f, q = 0.f;
                const bf16x2 one2 = __builtin_bit_cast(bf16x2, 0x3F803F80u);
#pragma unroll
                for (int e = 0; e < 10; ++e) {
                    const unsigned u = o[e >> 1][e & 1];
                    const bf16x2 x = __builtin_bit_cast(bf16x2, u);
                    s = __builtin_amdgcn_fdot2_f32_bf16(x, one2, s, false);
                    q = __builtin_amdgcn_fdot2_f32_bf16(x, x, q, false);
                }
                sum2_rows(s, q);
                const unsigned st = RG_ST + (unsigned)(t >> 2) * WIN + (unsigned)((t & 3) * 2 + mt) * 1024;
                __builtin_amdgcn_raw_buffer_store_b64((u32x2){nosent(__float_as_uint(s)), nosent(__float_as_uint(q))}, rr, ln < 16 ? ln * 8 : OOB, st + cu * 128, 16);
                ST(3);
                st128(merge16(o[0], o[1]), rn, o16, D_XR + sb); st128(merge16(o[2], o[3]), rn, o16b, D_XR + sb);
                __builtin_amdgcn_raw_buffer_store_b64(o[4], rn, o8, D_XR + sb + tl(4), 16);
                __builtin_amdgcn_s_sleep(W4SL);
                for (int spins = 0;; ++spins) {
                    const u32x2 a = LD64(rr, ln * 8, st), b = LD64(rr, ln * 8 + 512, st);
                    s = __uint_as_float(a[0]) + __uint_as_float(b[0]); q = __uint_as_float(a[1]) + __uint_as_float(b[1]);
                    if (__all(umax4(0u, (u32x4){a[0], a[1], b[0], b[1]}) != SENT)) break;
                    asm volatile("" ::: "memory");
                    if (spin_fail(spins, P)) break;
                }
                ST(4);
                sum2_rows(s, q);
                const float mean = s * (1.0f / DM), var = __builtin_fmaxf(q * (1.0f / DM) - mean * mean, 0.f);
                const float r = rcp(ln_den(var)), mr = -rbf(mean) * r;
#pragma unroll
                for (int n = 0; n < 5; ++n)
                    o[n] = (u32x2){pack2(__builtin_fmaf(res[n][0], r, mr), __builtin_fmaf(res[n][1], r, mr)), pack2(__builtin_fmaf(res[n][2], r, mr), __builtin_fmaf(res[n][3], r, mr))};
                st128(merge16(o[0], o[1]), rn, o16, D_XN + sb); st128(merge16(o[2], o[3]), rn, o16b, D_XN + sb);
                __builtin_amdgcn_raw_buffer_store_b64(o[4], rn, o8, D_XN + sb + tl(4), 16);
                wait_vm<0>();
                st128(((u32x4){(unsigned)t + 1, (unsigned)t + 1, (unsigned)t + 1, (unsigned)t + 1}), rn, ln < 32 ? (ln >> 3) * 2048 + (ln & 7) * 16 : OOB, FL_XN + (cu * 2 + mt) * 128);
            }
            ST(5);
            STFLUSH(t);
        }
    }
}

__global__ __launch_bounds__(512, 2) void mega_kernel(Params P) {
    const int wg = blockIdx.x;
    const int wave = __builtin_amdgcn_readfirstlane(threadIdx.x >> 6);
    const int L = (wg >> 2) & 1, x = wg & 3, r = wg >> 3, cpy = x;
    const int nr = (x & 1) ? 10 + RK : 10 - RK, j = (x >> 1) * 20 + (x & 1) * (10 - RK) + r - 16, i0 = (x >> 1) * 12 + (x & 1) * (6 + RK) + r - 16 - nr;
    const int i = (i0 * 5) % 24;
    if (L && (r < 16 || r - 16 >= nr)) l1_delay();
    if (r < 8) s_role(P, wave, L, x * 8 + r, cpy);
    else if (r < 16) v_role(P, wave, L, x * 8 + r - 8, cpy);
    else if (const u32x4 pv = *(const u32x4*)(P.ws + FL_PAR); P.par = (pv[0] + pv[1] + pv[2]) >> 13, r - 16 < nr) r_role(P, wave, L, j, cpy);
    else {
        if (i < 5) w_role<1>(P, wave, L, i);
        else if (i < 10) w_role<2>(P, wave, L, i - 5);
        else if (i < 16) w_role<3>(P, wave, L, i - 10);
        else w4_role(P, wave, L, i - 16);
    }
}

__global__ __launch_bounds__(1024) void init_kernel(char* ws) {
    const int b = blockIdx.x, tid = threadIdx.x;
    if (b < 3) {
        if (tid) return;
        char* p = ws + CAL0 + b * WIN;
        asm volatile("global_store_dword %0, %2, off sc1\n\tglobal_store_dword %1, %2, off sc1" :: "v"(p), "v"(p + 8192), "v"(b) : "memory");
        unsigned m[2] = {~0u, ~0u}, xcc;
#pragma unroll
        for (int r = 0; r < CALN; ++r) {
            unsigned long long a, c;
            asm volatile("s_waitcnt vmcnt(0) lgkmcnt(0)\n\ts_memtime %0\n\ts_waitcnt lgkmcnt(0)\n\tglobal_store_dword %3, %2, off sc1\n\ts_waitcnt vmcnt(0)\n\ts_memtime %1\n\ts_waitcnt lgkmcnt(0)"
                         : "=&s"(a), "=&s"(c) : "v"(r), "v"(p + (r & 1) * 8192) : "memory");
            const unsigned d = (unsigned)(c - a);
            m[r & 1] = d < m[r & 1] ? d : m[r & 1];
        }
        asm volatile("s_getreg_b32 %0, hwreg(HW_REG_XCC_ID)" : "=s"(xcc));
        *(unsigned*)(ws + FL_PAR + b * 4) = ((m[0] > m[1]) != ((xcc & 4) != 0)) ? 4096u : 0u;
    } else if (b < 6) ((u32x4*)(ws + FL_VO + (b - 3) * WIN))[tid] = (u32x4){0u, 0u, 0u, 0u};
    else if (b < 14) ((u32x4*)(ws + RG_XM + (b - 6) * WIN))[tid] = (u32x4){SENT, SENT, SENT, SENT};
    else if (tid == 0) *(unsigned*)(ws + FL0 + 256) = 0u;
}

extern "C" void kernel_launch(void* const* d_in, const int* in_sizes, int n_in, void* d_out, int out_size, void* d_ws,
                              size_t ws_size, hipStream_t stream) {
    char* ws = (char*)d_ws + (-(size_t)d_ws & (REG - 1));
    Params P;
    P.par = 0; P.pad = 0; P.ws = ws; P.out = (bf16_t*)d_out;
    P.x = (const bf16_t*)d_in[0]; P.mem0 = (const bf16_t*)d_in[1]; P.wq = (const bf16_t*)d_in[2]; P.wk = (const bf16_t*)d_in[3]; P.wv = (const bf16_t*)d_in[4]; P.wo = (const bf16_t*)d_in[5];
    P.w1 = (const bf16_t*)d_in[8]; P.w2 = (const bf16_t*)d_in[9]; P.w3 = (const bf16_t*)d_in[10]; P.w4 = (const bf16_t*)d_in[11];
    (void)hipFuncSetAttribute((const void*)mega_kernel, hipFuncAttributeMaxDynamicSharedMemorySize, MEGA_LDS);

    init_kernel<<<16, 1024, 0, stream>>>(ws);
    mega_kernel<<<256, 512, MEGA_LDS, stream>>>(P);
}
```

```cpp
#include <hip/hip_runtime.h>
#include <stdint.h>
#include <type_traits>

typedef unsigned short bf16_t;
using bf16x2 = __attribute__((ext_vector_type(2))) __bf16;
using bf16x8 = __attribute__((ext_vector_type(8))) __bf16;
using bf16x16 = __attribute__((ext_vector_type(16))) __bf16;
using f32x2 = __attribute__((ext_vector_type(2))) float;
using f32x4 = __attribute__((ext_vector_type(4))) float;
using u32x2 = __attribute__((ext_vector_type(2))) unsigned;
using u32x4 = __attribute__((ext_vector_type(4))) unsigned;
typedef __amdgpu_buffer_rsrc_t rsrc_t;

__device__ __forceinline__ unsigned pack2(float a, float b) {
    bf16x2 r = __builtin_convertvector((f32x2){a, b}, bf16x2);
    return __builtin_bit_cast(unsigned, r);
}
__device__ __forceinline__ float rbf(float f) { return __uint_as_float(pack2(f, 0.f) << 16); }
__device__ __forceinline__ float lo2f(unsigned u) { return __uint_as_float(u << 16); }
__device__ __forceinline__ float hi2f(unsigned u) { return __uint_as_float(u & 0xFFFF0000u); }
__device__ __forceinline__ rsrc_t mkrsrc(const void* p, unsigned bytes) {
    return __builtin_amdgcn_make_buffer_rsrc((void*)p, 0, bytes, 0x00020000);
}

constexpr int S_ = 2048, DM = 640, NH = 32, DH = 192, HD = NH * DH  , SEG = 32, NSEG = 64;
constexpr int FF = 1536;

#define NCPY 4
constexpr unsigned NSLOT = 4, NCP = NCPY;
constexpr unsigned WIN = 16384, REG = 2u << 20, REGS = REG + 8192;
constexpr unsigned XP_COPY = 3 * WIN, XP_SLOT = NCP * XP_COPY;
constexpr unsigned XM_COPY = 2 * WIN, XM_SLOT = NCP * XM_COPY;
constexpr unsigned VM_SLOT = REGS;
constexpr unsigned PX_SLOT = 8 * WIN;
constexpr unsigned RG_XM = 0, RG_XP = RG_XM + NSLOT * XM_SLOT, RG_PX = RG_XP + NSLOT * XP_SLOT, RG_ST = RG_PX + NSLOT * PX_SLOT, RG_VM = 2 * REG;
static_assert(RG_ST + 16 * WIN <= REG, "ring region");
constexpr unsigned FL0 = REG, FL_PAR = REG + 4096, FL_MB = REG + 65536;
constexpr unsigned FL_VO = RG_VM + 48 * WIN, FL_XN = FL_VO + 2 * WIN, D_SS = FL_VO + 4 * WIN, CAL0 = RG_VM + 64 * WIN;
constexpr unsigned ACT = S_ * DM * 2;
constexpr unsigned D_XR = 6 * REG, D_XN = 10 * REG, D_XA = 14 * REG, D_HS = 18 * REG, D_X1 = 22 * REG, D_H2 = 26 * REG, D_VX = 34 * REG;
__host__ __device__ constexpr unsigned segb(unsigned seg) { return seg * (5 * WIN) + (seg >> 4) * (REGS - 80 * WIN); }
__host__ __device__ constexpr unsigned segh(unsigned seg) { return seg * (12 * WIN) + (seg >> 3) * (REGS - 96 * WIN); }
#define CALN 4
#define RK 3
#define PXROT 5
#define W4SL 6
#define RXDEL 4
#define VXD 4
#define VXAUX 16
__host__ __device__ constexpr unsigned vxb(unsigned q, unsigned t) { return D_VX + q * (VXD / 2 * REG) + (t & (VXD - 1)) * (REG / 2) + (t & 2) * 4096; }
constexpr int AH = 3;
constexpr unsigned RG_L = D_VX + 2 * VXD * REG;

#define SB() __builtin_amdgcn_sched_barrier(0)
#define LAS __attribute__((address_space(3)))
template <class T> __device__ __forceinline__ T lds_ld(unsigned a) { return *(const LAS T*)(a); }
template <class T> __device__ __forceinline__ void lds_st(unsigned a, T v) { *(LAS T*)(a) = v; }
constexpr unsigned OOB = 0x7FFFFF00u;
template <int N> __device__ __forceinline__ void wait_vm() { asm volatile("s_waitcnt vmcnt(%0)" :: "i"(N) : "memory"); }
__device__ __forceinline__ void bar() {
    asm volatile("s_waitcnt lgkmcnt(0)" ::: "memory");
    __builtin_amdgcn_s_barrier();
    asm volatile("" ::: "memory");
}
__device__ __forceinline__ int lane_id() {
    int ln;
    asm volatile("v_mbcnt_lo_u32_b32 %0, -1, 0\n\tv_mbcnt_hi_u32_b32 %0, -1, %0" : "=v"(ln));
    return ln;
}
__device__ __forceinline__ void swap16(unsigned& a, unsigned& b) { asm volatile("s_nop 3\n\tv_permlane16_swap_b32 %0, %1\n\ts_nop 1" : "+v"(a), "+v"(b)); }
__device__ __forceinline__ void swap32(unsigned& a, unsigned& b) { asm volatile("s_nop 3\n\tv_permlane32_swap_b32 %0, %1\n\ts_nop 1" : "+v"(a), "+v"(b)); }
__device__ __forceinline__ float sum_x16(float x) { unsigned a = __float_as_uint(x), b = a; swap16(a, b); return __uint_as_float(a) + __uint_as_float(b); }
__device__ __forceinline__ float sum_x32(float x) { unsigned a = __float_as_uint(x), b = a; swap32(a, b); return __uint_as_float(a) + __uint_as_float(b); }
__device__ __forceinline__ float max_x16(float x) { unsigned a = __float_as_uint(x), b = a; swap16(a, b); return __builtin_fmaxf(__uint_as_float(a), __uint_as_float(b)); }
__device__ __forceinline__ float max_x32(float x) { unsigned a = __float_as_uint(x), b = a; swap32(a, b); return __builtin_fmaxf(__uint_as_float(a), __uint_as_float(b)); }
__device__ __forceinline__ void sum2_rows(float& s, float& q) {
    unsigned a = __float_as_uint(s), b = a, c = __float_as_uint(q), d = c;
    asm volatile("s_nop 2\n\tv_permlane16_swap_b32 %0, %1\n\tv_permlane16_swap_b32 %2, %3\n\tv_add_f32 %0, %0, %1\n\tv_add_f32 %2, %2, %3\n\tv_mov_b32 %1, %0\n\tv_mov_b32 %3, %2\n\t"
                 "s_nop 1\n\tv_permlane32_swap_b32 %0, %1\n\tv_permlane32_swap_b32 %2, %3\n\tv_add_f32 %0, %0, %1\n\tv_add_f32 %2, %2, %3" : "+v"(a), "+v"(b), "+v"(c), "+v"(d));
    s = __uint_as_float(a); q = __uint_as_float(c);
}
__device__ __forceinline__ u32x4 merge16(u32x2 a, u32x2 b) {
    unsigned a0 = a[0], a1 = a[1], b0 = b[0], b1 = b[1];
    asm volatile("s_nop 3\n\tv_permlane16_swap_b32 %0, %2\n\tv_permlane16_swap_b32 %1, %3\n\ts_nop 1" : "+v"(a0), "+v"(a1), "+v"(b0), "+v"(b1));
    return (u32x4){a0, a1, b0, b1};
}
template <int AUXV>
__device__ __forceinline__ void st128_(u32x4 v, rsrc_t r, unsigned voff, unsigned soff) {
    __builtin_amdgcn_raw_buffer_store_b128(v, r, voff, soff, AUXV);
    asm volatile("s_nop 1" :: "v"(v) : "memory");
}
#define st128(V, R, VO_, SO_) st128_<16>(V, R, VO_, SO_)
#define LD128(R, VO_, SO_) __builtin_amdgcn_raw_buffer_load_b128(R, VO_, SO_, 16)
#define LD64(R, VO_, SO_) __builtin_amdgcn_raw_buffer_load_b64(R, VO_, SO_, 16)
#define BF8(X) __builtin_bit_cast(bf16x8, X)
__device__ __forceinline__ float gelu_tanh(float x) {
    const float c0 = -2.0f * 0.7978845608028654f * 1.4426950408889634f, c1 = c0 * 0.044715f;
    const float e = __builtin_amdgcn_exp2f(x * __builtin_fmaf(x * x, c1, c0));
    return x * __builtin_amdgcn_rcpf(1.0f + e);
}
#define MFMA(A_, B_, C_) C_ = __builtin_amdgcn_mfma_f32_16x16x32_bf16(A_, B_, C_, 0, 0, 0)
#define SMF(A_, B_, C_) C_ = __builtin_amdgcn_smfmac_f32_16x16x64_bf16(A_, B_, C_, sidx, 0, 0)
__device__ __forceinline__ float ln_den(float var) { return rbf(__builtin_amdgcn_sqrtf(rbf(rbf(var) + 1e-5f))); }
__device__ __forceinline__ float rcp(float x) { return __builtin_amdgcn_rcpf(x); }

struct Params {
    char* ws;
    const bf16_t *x, *mem0, *wq, *wk, *wv, *wo, *w1, *w2, *w3, *w4;
    bf16_t* out;
    unsigned par, pad;
};

#define RLX_AGENT __ATOMIC_RELAXED, __HIP_MEMORY_SCOPE_AGENT
#define SENT 0xFFFFFFFFu
constexpr int SPIN_CAP = 400000;
__device__ __forceinline__ bool spin_fail(int spins, const Params& P) {
    if ((spins & 1023) == 1023) {
        const rsrc_t rt = mkrsrc(P.ws + FL0, 512);
        unsigned o;
        asm volatile("v_mov_b32 %0, 0x100" : "=v"(o));
        if (__builtin_amdgcn_readfirstlane(__builtin_amdgcn_raw_buffer_load_b32(rt, o, 0, 16)) != 0u) return true;
        if (spins > SPIN_CAP) { __builtin_amdgcn_raw_buffer_store_b32(o, rt, o, 0, 16); return true; }
    }
    return false;
}
__device__ __forceinline__ bool ok4(u32x4 v) { return v[0] != SENT && v[1] != SENT && v[2] != SENT && v[3] != SENT; }
__device__ __forceinline__ unsigned nosent(unsigned d) { return d < 0xFFFFFFFEu ? d : 0xFFFFFFFEu; }
__device__ __forceinline__ u32x4 nosent4(u32x4 v) { return (u32x4){nosent(v[0]), nosent(v[1]), nosent(v[2]), nosent(v[3])}; }
__device__ __forceinline__ unsigned umax3(unsigned a, unsigned b, unsigned c) { const unsigned m = a > b ? a : b; return m > c ? m : c; }
__device__ __forceinline__ unsigned umax4(unsigned m, u32x4 v) { return umax3(umax3(m, v[0], v[1]), v[2], v[3]); }
__device__ __forceinline__ u32x4 sent4() { unsigned sv; asm volatile("v_mov_b32 %0, -1" : "=v"(sv)); return (u32x4){sv, sv, sv, sv}; }
template <int N>
__device__ __forceinline__ void poll(rsrc_t rs, unsigned base, const unsigned (&off)[N], u32x4 (&v)[N], const Params& P) {
    for (int spins = 0;; ++spins) {
        unsigned m = 0;
#pragma unroll
        for (int k = 0; k < N; ++k) v[k] = LD128(rs, off[k], base);
#pragma unroll
        for (int k = 0; k < N; ++k) m = umax4(m, v[k]);
        if (__all(m != SENT)) return;
        asm volatile("" ::: "memory");
        if (spin_fail(spins, P)) return;
    }
}
__device__ __forceinline__ bf16x16 cat16(bf16x8 a, bf16x8 b) {
    return __builtin_shufflevector(a, b, 0, 1, 2, 3, 4, 5, 6, 7, 8, 9, 10, 11, 12, 13, 14, 15);
}
__device__ __forceinline__ bf16x8 half8(bf16x16 v, int h) {
    return h ? __builtin_shufflevector(v, v, 8, 9, 10, 11, 12, 13, 14, 15) : __builtin_shufflevector(v, v, 0, 1, 2, 3, 4, 5, 6, 7);
}
__device__ __forceinline__ bf16x16 lds_frag(unsigned a, unsigned step) { return cat16(lds_ld<bf16x8>(a), lds_ld<bf16x8>(a + step)); }
__device__ __forceinline__ unsigned toff(int row, int col) {
    return segb((unsigned)row >> 5) + (unsigned)(col >> 7) * WIN + (unsigned)(row & 16) * 256u + (unsigned)((col >> 3) & 15) * 256u + (unsigned)(row & 15) * 16u + (unsigned)(col & 7) * 2u;
}
__device__ __forceinline__ char* lbase(const Params& P, int L) { return P.ws + ((P.par ^ L) << 13); }

#define ASM_ST
#define ASM_ST_DECL
#define ASM_ST_OUT
#define ASM_ST_SAVE(k)
#define ASM_STW
#define ASM_ST3
#define ASM_ST3_OUT
#define ASM_ST3_SAVE(k)
#define ASM_ST2
#define ASM_ST2_OUT
#define ASM_ST2_SAVE(k)
#define LST0
#define LST1
#define LST2
#define LST3
#define LST4
#define LST5
#define LST6
#define LST7
#define LSTINIT
#define LFLUSH200
#define LFLUSH212
#define LFLUSHW
#define LST_INW
#define LST_IN
#define STDECL
#define ST(k)
#define STFLUSH(t)

constexpr unsigned L_X = 0;
constexpr unsigned L_MEM = 40960;
constexpr unsigned L_Q = 51200;
constexpr unsigned L_KM = 54272;
constexpr unsigned L_QX = 57344;
constexpr unsigned L_KX = 69632;
constexpr unsigned L_W = 81920;
constexpr int MEGA_LDS = 163840;
constexpr int wi(int i, int j) { return (i >> 1) * 5 + ((i & 1) ? 2 + j : j - 1); }

#define RPUB2
#define ASM_S_LOOP \
    "s_mov_b64 s[60:61], %[ws]\n\t" \
    "s_mov_b32 s62, %[sz]\n\t" \
    "s_mov_b32 s63, 0x20000\n\t" \
    "s_mov_b32 s40, 0\n\t" \
    "s_mov_b32 s41, 0x20000\n\t" \
    LSTINIT \
    LDELAY \
    "s_cmp_eq_u32 %[wave], 0\n\t" \
    "s_cbranch_scc1 .Ls0_%=\n\t" \
    "s_cmp_eq_u32 %[wave], 1\n\t" \
    "s_cbranch_scc1 .Ls1_%=\n\t" \
    "s_cmp_eq_u32 %[wave], 2\n\t" \
    "s_cbranch_scc1 .Ls2_%=\n\t" \
    "s_cmp_eq_u32 %[wave], 3\n\t" \
    "s_cbranch_scc1 .Ls3_%=\n\t" \
    ".Ls4_%=:\n\t" \
    "s_mov_b32 s42, %[xs]\n\t" \
    "s_add_u32 m0, %[wm], 0x18000\n\t" \
    "s_mov_b32 s44, 0x0\n\t" \
    "buffer_load_dwordx4 v240, %[wr], s44 offen offset:0 nt lds\n\t" \
    "buffer_load_dwordx4 v241, %[wr], s44 offen offset:1024 nt lds\n\t" \
    "buffer_load_dwordx4 v242, %[wr], s44 offen offset:2048 nt lds\n\t" \
    "s_add_u32 m0, %[wm], 0x1e000\n\t" \
    "s_mov_b32 s44, 0x60000\n\t" \
    "buffer_load_dwordx4 v240, %[wr], s44 offen offset:0 nt lds\n\t" \
    "buffer_load_dwordx4 v241, %[wr], s44 offen offset:1024 nt lds\n\t" \
    "buffer_load_dwordx4 v242, %[wr], s44 offen offset:2048 nt lds\n\t" \
    "s_add_u32 m0, %[wm], 0x0\n\t" \
    "s_mov_b32 s44, 0xc0000\n\t" \
    "buffer_load_dwordx4 v240, %[wr], s44 offen offset:0 nt lds\n\t" \
    "buffer_load_dwordx4 v241, %[wr], s44 offen offset:1024 nt lds\n\t" \
    "buffer_load_dwordx4 v242, %[wr], s44 offen offset:2048 nt lds\n\t" \
    "s_add_u32 m0, %[wm], 0x6000\n\t" \
    "s_mov_b32 s44, 0x120000\n\t" \
    "buffer_load_dwordx4 v240, %[wr], s44 offen offset:0 nt lds\n\t" \
    "buffer_load_dwordx4 v241, %[wr], s44 offen offset:1024 nt lds\n\t" \
    "buffer_load_dwordx4 v242, %[wr], s44 offen offset:2048 nt lds\n\t" \
    "s_add_u32 m0, %[wm], 0xc000\n\t" \
    "s_mov_b32 s44, 0x180000\n\t" \
    "buffer_load_dwordx4 v240, %[wr], s44 offen offset:0 nt lds\n\t" \
    "buffer_load_dwordx4 v241, %[wr], s44 offen offset:1024 nt lds\n\t" \
    "buffer_load_dwordx4 v242, %[wr], s44 offen offset:2048 nt lds\n\t" \
    "v_add_u32 v244, 0x18000, v243\n\t" \
    "s_waitcnt vmcnt(12) lgkmcnt(0)\n\t" \
    "s_barrier \n\t" \
    "s_add_u32 m0, %[wm], 0x12000\n\t" \
    "s_mov_b32 s44, 0x1e0000\n\t" \
    "buffer_load_dwordx4 v240, %[wr], s44 offen offset:0 nt lds\n\t" \
    "buffer_load_dwordx4 v241, %[wr], s44 offen offset:1024 nt lds\n\t" \
    "buffer_load_dwordx4 v242, %[wr], s44 offen offset:2048 nt lds\n\t" \
    "ds_read_b64_tr_b16 v[200:201], v244 offset:0\n\t" \
    "ds_read_b64_tr_b16 v[202:203], v244 offset:1536\n\t" \
    "ds_read_b64_tr_b16 v[0:1], v244 offset:32\n\t" \
    "ds_read_b64_tr_b16 v[2:3], v244 offset:1568\n\t" \
    "ds_read_b64_tr_b16 v[8:9], v244 offset:64\n\t" \
    "ds_read_b64_tr_b16 v[10:11], v244 offset:1600\n\t" \
    "v_add_u32 v244, 0x1e000, v243\n\t" \
    "s_waitcnt vmcnt(12) lgkmcnt(0)\n\t" \
    "s_barrier \n\t" \
    "s_add_u32 m0, %[wm], 0x18000\n\t" \
    "s_mov_b32 s44, 0x240000\n\t" \
    "buffer_load_dwordx4 v240, %[wr], s44 offen offset:0 nt lds\n\t" \
    "buffer_load_dwordx4 v241, %[wr], s44 offen offset:1024 nt lds\n\t" \
    "buffer_load_dwordx4 v242, %[wr], s44 offen offset:2048 nt lds\n\t" \
    "ds_read_b64_tr_b16 v[204:205], v244 offset:0\n\t" \
    "ds_read_b64_tr_b16 v[206:207], v244 offset:1536\n\t" \
    "ds_read_b64_tr_b16 v[4:5], v244 offset:32\n\t" \
    "ds_read_b64_tr_b16 v[6:7], v244 offset:1568\n\t" \
    "ds_read_b64_tr_b16 v[12:13], v244 offset:64\n\t" \
    "ds_read_b64_tr_b16 v[14:15], v244 offset:1600\n\t" \
    "v_add_u32 v244, 0x0, v243\n\t" \
    "s_waitcnt vmcnt(12) lgkmcnt(0)\n\t" \
    "s_barrier \n\t" \
    "s_add_u32 m0, %[wm], 0x1e000\n\t" \
    "s_mov_b32 s44, 0x2a0000\n\t" \
    "buffer_load_dwordx4 v240, %[wr], s44 offen offset:0 nt lds\n\t" \
    "buffer_load_dwordx4 v241, %[wr], s44 offen offset:1024 nt lds\n\t" \
    "buffer_load_dwordx4 v242, %[wr], s44 offen offset:2048 nt lds\n\t" \
    "ds_read_b64_tr_b16 v[16:17], v244 offset:0\n\t" \
    "ds_read_b64_tr_b16 v[18:19], v244 offset:1536\n\t" \
    "ds_read_b64_tr_b16 v[24:25], v244 offset:32\n\t" \
    "ds_read_b64_tr_b16 v[26:27], v244 offset:1568\n\t" \
    "ds_read_b64_tr_b16 v[32:33], v244 offset:64\n\t" \
    "ds_read_b64_tr_b16 v[34:35], v244 offset:1600\n\t" \
    "v_add_u32 v244, 0x6000, v243\n\t" \
    "s_waitcnt vmcnt(12) lgkmcnt(0)\n\t" \
    "s_barrier \n\t" \
    "s_add_u32 m0, %[wm], 0x0\n\t" \
    "s_mov_b32 s44, 0x300000\n\t" \
    "buffer_load_dwordx4 v240, %[wr], s44 offen offset:0 nt lds\n\t" \
    "buffer_load_dwordx4 v241, %[wr], s44 offen offset:1024 nt lds\n\t" \
    "buffer_load_dwordx4 v242, %[wr], s44 offen offset:2048 nt lds\n\t" \
    "ds_read_b64_tr_b16 v[20:21], v244 offset:0\n\t" \
    "ds_read_b64_tr_b16 v[22:23], v244 offset:1536\n\t" \
    "ds_read_b64_tr_b16 v[28:29], v244 offset:32\n\t" \
    "ds_read_b64_tr_b16 v[30:31], v244 offset:1568\n\t" \
    "ds_read_b64_tr_b16 v[36:37], v244 offset:64\n\t" \
    "ds_read_b64_tr_b16 v[38:39], v244 offset:1600\n\t" \
    "v_add_u32 v244, 0xc000, v243\n\t" \
    "s_waitcnt vmcnt(12) lgkmcnt(0)\n\t" \
    "s_barrier \n\t" \
    "s_add_u32 m0, %[wm], 0x6000\n\t" \
    "s_mov_b32 s44, 0x360000\n\t" \
    "buffer_load_dwordx4 v240, %[wr], s44 offen offset:0 nt lds\n\t" \
    "buffer_load_dwordx4 v241, %[wr], s44 offen offset:1024 nt lds\n\t" \
    "buffer_load_dwordx4 v242, %[wr], s44 offen offset:2048 nt lds\n\t" \
    "ds_read_b64_tr_b16 v[208:209], v244 offset:0\n\t" \
    "ds_read_b64_tr_b16 v[210:211], v244 offset:1536\n\t" \
    "ds_read_b64_tr_b16 v[40:41], v244 offset:32\n\t" \
    "ds_read_b64_tr_b16 v[42:43], v244 offset:1568\n\t" \
    "ds_read_b64_tr_b16 v[48:49], v244 offset:64\n\t" \
    "ds_read_b64_tr_b16 v[50:51], v244 offset:1600\n\t" \
    "v_add_u32 v244, 0x12000, v243\n\t" \
    "s_waitcnt vmcnt(12) lgkmcnt(0)\n\t" \
    "s_barrier \n\t" \
    "s_add_u32 m0, %[wm], 0xc000\n\t" \
    "s_mov_b32 s44, 0x3c0000\n\t" \
    "buffer_load_dwordx4 v240, %[wr], s44 offen offset:0 nt lds\n\t" \
    "buffer_load_dwordx4 v241, %[wr], s44 offen offset:1024 nt lds\n\t" \
    "buffer_load_dwordx4 v242, %[wr], s44 offen offset:2048 nt lds\n\t" \
    "ds_read_b64_tr_b16 v[212:213], v244 offset:0\n\t" \
    "ds_read_b64_tr_b16 v[214:215], v244 offset:1536\n\t" \
    "ds_read_b64_tr_b16 v[44:45], v244 offset:32\n\t" \
    "ds_read_b64_tr_b16 v[46:47], v244 offset:1568\n\t" \
    "ds_read_b64_tr_b16 v[52:53], v244 offset:64\n\t" \
    "ds_read_b64_tr_b16 v[54:55], v244 offset:1600\n\t" \
    "v_add_u32 v244, 0x18000, v243\n\t" \
    "s_waitcnt vmcnt(12) lgkmcnt(0)\n\t" \
    "s_barrier \n\t" \
    "v_mov_b32 v246, 0\n\t" \
    "buffer_load_dword v246, v246, s[60:63], %[mo] offen sc1\n\t" \
    "s_add_u32 m0, %[wm], 0x12000\n\t" \
    "s_mov_b32 s44, 0x420000\n\t" \
    "buffer_load_dwordx4 v240, %[wr], s44 offen offset:0 nt lds\n\t" \
    "buffer_load_dwordx4 v241, %[wr], s44 offen offset:1024 nt lds\n\t" \
    "buffer_load_dwordx4 v242, %[wr], s44 offen offset:2048 nt lds\n\t" \
    "ds_read_b64_tr_b16 v[56:57], v244 offset:0\n\t" \
    "ds_read_b64_tr_b16 v[58:59], v244 offset:1536\n\t" \
    "ds_read_b64_tr_b16 v[64:65], v244 offset:32\n\t" \
    "ds_read_b64_tr_b16 v[66:67], v244 offset:1568\n\t" \
    "ds_read_b64_tr_b16 v[72:73], v244 offset:64\n\t" \
    "ds_read_b64_tr_b16 v[74:75], v244 offset:1600\n\t" \
    "v_add_u32 v244, 0x1e000, v243\n\t" \
    "s_waitcnt vmcnt(12) lgkmcnt(0)\n\t" \
    "s_barrier \n\t" \
    "s_add_u32 m0, %[wm], 0x18000\n\t" \
    "s_mov_b32 s44, 0x480000\n\t" \
    "buffer_load_dwordx4 v240, %[wr], s44 offen offset:0 nt lds\n\t" \
    "buffer_load_dwordx4 v241, %[wr], s44 offen offset:1024 nt lds\n\t" \
    "buffer_load_dwordx4 v242, %[wr], s44 offen offset:2048 nt lds\n\t" \
    "ds_read_b64_tr_b16 v[60:61], v244 offset:0\n\t" \
    "ds_read_b64_tr_b16 v[62:63], v244 offset:1536\n\t" \
    "ds_read_b64_tr_b16 v[68:69], v244 offset:32\n\t" \
    "ds_read_b64_tr_b16 v[70:71], v244 offset:1568\n\t" \
    "ds_read_b64_tr_b16 v[76:77], v244 offset:64\n\t" \
    "ds_read_b64_tr_b16 v[78:79], v244 offset:1600\n\t" \
    "v_add_u32 v244, 0x0, v243\n\t" \
    "s_waitcnt vmcnt(12) lgkmcnt(0)\n\t" \
    "s_barrier \n\t" \
    "s_add_u32 m0, %[wm], 0x1e000\n\t" \
    "s_mov_b32 s44, 0x4e0000\n\t" \
    "buffer_load_dwordx4 v240, %[wr], s44 offen offset:0 nt lds\n\t" \
    "buffer_load_dwordx4 v241, %[wr], s44 offen offset:1024 nt lds\n\t" \
    "buffer_load_dwordx4 v242, %[wr], s44 offen offset:2048 nt lds\n\t" \
    "ds_read_b64_tr_b16 v[216:217], v244 offset:0\n\t" \
    "ds_read_b64_tr_b16 v[218:219], v244 offset:1536\n\t" \
    "ds_read_b64_tr_b16 v[80:81], v244 offset:32\n\t" \
    "ds_read_b64_tr_b16 v[82:83], v244 offset:1568\n\t" \
    "ds_read_b64_tr_b16 v[88:89], v244 offset:64\n\t" \
    "ds_read_b64_tr_b16 v[90:91], v244 offset:1600\n\t" \
    "v_add_u32 v244, 0x6000, v243\n\t" \
    "s_waitcnt vmcnt(12) lgkmcnt(0)\n\t" \
    "s_barrier \n\t" \
    "s_add_u32 m0, %[wm], 0x0\n\t" \
    "s_mov_b32 s44, 0x540000\n\t" \
    "buffer_load_dwordx4 v240, %[wr], s44 offen offset:0 nt lds\n\t" \
    "buffer_load_dwordx4 v241, %[wr], s44 offen offset:1024 nt lds\n\t" \
    "buffer_load_dwordx4 v242, %[wr], s44 offen offset:2048 nt lds\n\t" \
    "ds_read_b64_tr_b16 v[220:221], v244 offset:0\n\t" \
    "ds_read_b64_tr_b16 v[222:223], v244 offset:1536\n\t" \
    "ds_read_b64_tr_b16 v[84:85], v244 offset:32\n\t" \
    "ds_read_b64_tr_b16 v[86:87], v244 offset:1568\n\t" \
    "ds_read_b64_tr_b16 v[92:93], v244 offset:64\n\t" \
    "ds_read_b64_tr_b16 v[94:95], v244 offset:1600\n\t" \
    "v_add_u32 v244, 0xc000, v243\n\t" \
    "s_waitcnt vmcnt(12) lgkmcnt(0)\n\t" \
    "s_barrier \n\t" \
    "s_add_u32 m0, %[wm], 0x6000\n\t" \
    "s_mov_b32 s44, 0x5a0000\n\t" \
    "buffer_load_dwordx4 v240, %[wr], s44 offen offset:0 nt lds\n\t" \
    "buffer_load_dwordx4 v241, %[wr], s44 offen offset:1024 nt lds\n\t" \
    "buffer_load_dwordx4 v242, %[wr], s44 offen offset:2048 nt lds\n\t" \
    "ds_read_b64_tr_b16 v[96:97], v244 offset:0\n\t" \
    "ds_read_b64_tr_b16 v[98:99], v244 offset:1536\n\t" \
    "ds_read_b64_tr_b16 v[104:105], v244 offset:32\n\t" \
    "ds_read_b64_tr_b16 v[106:107], v244 offset:1568\n\t" \
    "ds_read_b64_tr_b16 v[112:113], v244 offset:64\n\t" \
    "ds_read_b64_tr_b16 v[114:115], v244 offset:1600\n\t" \
    "v_add_u32 v244, 0x12000, v243\n\t" \
    "s_waitcnt vmcnt(12) lgkmcnt(0)\n\t" \
    "s_barrier \n\t" \
    ".Ls41_%=:\n\t" \
    "v_readfirstlane_b32 s43, v246\n\t" \
    "s_nop 0\n\t" \
    "s_cmp_lg_u32 s43, -1\n\t" \
    "s_cbranch_scc1 .Ls42_%=\n\t" \
    "s_cmp_eq_u32 s41, 0\n\t" \
    "s_cbranch_scc1 .Ls42_%=\n\t" \
    "s_sub_u32 s41, s41, 1\n\t" \
    "s_sleep 4\n\t" \
    "v_mov_b32 v246, 0\n\t" \
    "buffer_load_dword v246, v246, s[60:63], %[mo] offen sc1\n\t" \
    "s_waitcnt vmcnt(0)\n\t" \
    "s_branch .Ls41_%=\n\t" \
    ".Ls42_%=:\n\t" \
    "v_mov_b32 v246, 0x8000\n\t" \
    "s_mov_b64 exec, 1\n\t" \
    "buffer_store_dword v246, v246, s[60:63], %[mo] offen sc1\n\t" \
    "s_mov_b64 exec, -1\n\t" \
    "s_and_b32 s43, s43, 0x2000\n\t" \
    "s_xor_b32 s43, s43, %[lx]\n\t" \
    "s_add_u32 s60, s60, s43\n\t" \
    "s_addc_u32 s61, s61, 0\n\t" \
    "v_and_b32 v252, 0xf0, v253\n\t" \
    "v_lshlrev_b32 v252, 3, v252\n\t" \
    "buffer_load_dword v252, v252, s[60:63], %[fl] offen sc1\n\t" \
    "s_add_u32 m0, %[wm], 0xc000\n\t" \
    "s_mov_b32 s44, 0x600000\n\t" \
    "buffer_load_dwordx4 v240, %[wr], s44 offen offset:0 nt lds\n\t" \
    "buffer_load_dwordx4 v241, %[wr], s44 offen offset:1024 nt lds\n\t" \
    "buffer_load_dwordx4 v242, %[wr], s44 offen offset:2048 nt lds\n\t" \
    "ds_read_b64_tr_b16 v[100:101], v244 offset:0\n\t" \
    "ds_read_b64_tr_b16 v[102:103], v244 offset:1536\n\t" \
    "ds_read_b64_tr_b16 v[108:109], v244 offset:32\n\t" \
    "ds_read_b64_tr_b16 v[110:111], v244 offset:1568\n\t" \
    "ds_read_b64_tr_b16 v[116:117], v244 offset:64\n\t" \
    "ds_read_b64_tr_b16 v[118:119], v244 offset:1600\n\t" \
    "v_add_u32 v244, 0x18000, v243\n\t" \
    "s_waitcnt vmcnt(12) lgkmcnt(0)\n\t" \
    "s_barrier \n\t" \
    "s_add_u32 m0, %[wm], 0x12000\n\t" \
    "s_mov_b32 s44, 0x660000\n\t" \
    "buffer_load_dwordx4 v240, %[wr], s44 offen offset:0 nt lds\n\t" \
    "buffer_load_dwordx4 v241, %[wr], s44 offen offset:1024 nt lds\n\t" \
    "buffer_load_dwordx4 v242, %[wr], s44 offen offset:2048 nt lds\n\t" \
    "ds_read_b64_tr_b16 v[224:225], v244 offset:0\n\t" \
    "ds_read_b64_tr_b16 v[226:227], v244 offset:1536\n\t" \
    "ds_read_b64_tr_b16 v[120:121], v244 offset:32\n\t" \
    "ds_read_b64_tr_b16 v[122:123], v244 offset:1568\n\t" \
    "ds_read_b64_tr_b16 v[128:129], v244 offset:64\n\t" \
    "ds_read_b64_tr_b16 v[130:131], v244 offset:1600\n\t" \
    "v_add_u32 v244, 0x1e000, v243\n\t" \
    "s_waitcnt vmcnt(12) lgkmcnt(0)\n\t" \
    "s_barrier \n\t" \
    "s_add_u32 m0, %[wm], 0x18000\n\t" \
    "s_mov_b32 s44, 0x6c0000\n\t" \
    "buffer_load_dwordx4 v240, %[wr], s44 offen offset:0 nt lds\n\t" \
    "buffer_load_dwordx4 v241, %[wr], s44 offen offset:1024 nt lds\n\t" \
    "buffer_load_dwordx4 v242, %[wr], s44 offen offset:2048 nt lds\n\t" \
    "ds_read_b64_tr_b16 v[228:229], v244 offset:0\n\t" \
    "ds_read_b64_tr_b16 v[230:231], v244 offset:1536\n\t" \
    "ds_read_b64_tr_b16 v[124:125], v244 offset:32\n\t" \
    "ds_read_b64_tr_b16 v[126:127], v244 offset:1568\n\t" \
    "ds_read_b64_tr_b16 v[132:133], v244 offset:64\n\t" \
    "ds_read_b64_tr_b16 v[134:135], v244 offset:1600\n\t" \
    "v_add_u32 v244, 0x0, v243\n\t" \
    "s_waitcnt vmcnt(12) lgkmcnt(0)\n\t" \
    "s_barrier \n\t" \
    "s_add_u32 m0, %[wm], 0x1e000\n\t" \
    "s_mov_b32 s44, 0x720000\n\t" \
    "buffer_load_dwordx4 v240, %[wr], s44 offen offset:0 nt lds\n\t" \
    "buffer_load_dwordx4 v241, %[wr], s44 offen offset:1024 nt lds\n\t" \
    "buffer_load_dwordx4 v242, %[wr], s44 offen offset:2048 nt lds\n\t" \
    "ds_read_b64_tr_b16 v[136:137], v244 offset:0\n\t" \
    "ds_read_b64_tr_b16 v[138:139], v244 offset:1536\n\t" \
    "ds_read_b64_tr_b16 v[144:145], v244 offset:32\n\t" \
    "ds_read_b64_tr_b16 v[146:147], v244 offset:1568\n\t" \
    "ds_read_b64_tr_b16 v[152:153], v244 offset:64\n\t" \
    "ds_read_b64_tr_b16 v[154:155], v244 offset:1600\n\t" \
    "v_add_u32 v244, 0x6000, v243\n\t" \
    "s_waitcnt vmcnt(12) lgkmcnt(0)\n\t" \
    "s_barrier \n\t" \
    "ds_read_b64_tr_b16 v[140:141], v244 offset:0\n\t" \
    "ds_read_b64_tr_b16 v[142:143], v244 offset:1536\n\t" \
    "ds_read_b64_tr_b16 v[148:149], v244 offset:32\n\t" \
    "ds_read_b64_tr_b16 v[150:151], v244 offset:1568\n\t" \
    "ds_read_b64_tr_b16 v[156:157], v244 offset:64\n\t" \
    "ds_read_b64_tr_b16 v[158:159], v244 offset:1600\n\t" \
    "v_add_u32 v244, 0xc000, v243\n\t" \
    "s_waitcnt vmcnt(9) lgkmcnt(0)\n\t" \
    "s_barrier \n\t" \
    "ds_read_b64_tr_b16 v[232:233], v244 offset:0\n\t" \
    "ds_read_b64_tr_b16 v[234:235], v244 offset:1536\n\t" \
    "ds_read_b64_tr_b16 v[160:161], v244 offset:32\n\t" \
    "ds_read_b64_tr_b16 v[162:163], v244 offset:1568\n\t" \
    "ds_read_b64_tr_b16 v[168:169], v244 offset:64\n\t" \
    "ds_read_b64_tr_b16 v[170:171], v244 offset:1600\n\t" \
    "s_mov_b32 s43, 0\n\t" \
    "v_cmp_eq_u32 vcc, 0, v252\n\t" \
    "s_cbranch_vccnz .Ls43_%=\n\t" \
    "s_mov_b32 m0, %[m0]\n\t" \
    "s_add_u32 s45, s42, %[x1]\n\t" \
    "buffer_load_dwordx4 v253, s[60:63], s42 offen offset:0 sc1 lds\n\t" \
    "buffer_load_dwordx4 v253, s[60:63], s42 offen offset:1024 sc1 lds\n\t" \
    "buffer_load_dwordx4 v253, s[60:63], s42 offen offset:2048 sc1 lds\n\t" \
    "buffer_load_dwordx4 v253, s[60:63], s42 offen offset:3072 sc1 lds\n\t" \
    "s_mov_b32 m0, %[m1]\n\t" \
    "s_nop 0\n\t" \
    "buffer_load_dwordx4 v253, s[60:63], s45 offen offset:0 sc1 lds\n\t" \
    "buffer_load_dwordx4 v253, s[60:63], s45 offen offset:1024 sc1 lds\n\t" \
    "s_mov_b32 s43, 1\n\t" \
    ".Ls43_%=:\n\t" \
    "v_add_u32 v244, 0x12000, v243\n\t" \
    "s_waitcnt vmcnt(6) lgkmcnt(0)\n\t" \
    "s_barrier \n\t" \
    "ds_read_b64_tr_b16 v[236:237], v244 offset:0\n\t" \
    "ds_read_b64_tr_b16 v[238:239], v244 offset:1536\n\t" \
    "ds_read_b64_tr_b16 v[164:165], v244 offset:32\n\t" \
    "ds_read_b64_tr_b16 v[166:167], v244 offset:1568\n\t" \
    "ds_read_b64_tr_b16 v[172:173], v244 offset:64\n\t" \
    "ds_read_b64_tr_b16 v[174:175], v244 offset:1600\n\t" \
    "v_add_u32 v244, 0x18000, v243\n\t" \
    "s_waitcnt vmcnt(3) lgkmcnt(0)\n\t" \
    "s_barrier \n\t" \
    "ds_read_b64_tr_b16 v[176:177], v244 offset:0\n\t" \
    "ds_read_b64_tr_b16 v[178:179], v244 offset:1536\n\t" \
    "ds_read_b64_tr_b16 v[184:185], v244 offset:32\n\t" \
    "ds_read_b64_tr_b16 v[186:187], v244 offset:1568\n\t" \
    "ds_read_b64_tr_b16 v[192:193], v244 offset:64\n\t" \
    "ds_read_b64_tr_b16 v[194:195], v244 offset:1600\n\t" \
    "v_add_u32 v244, 0x1e000, v243\n\t" \
    "s_waitcnt vmcnt(0) lgkmcnt(0)\n\t" \
    "s_barrier \n\t" \
    "ds_read_b64_tr_b16 v[180:181], v244 offset:0\n\t" \
    "ds_read_b64_tr_b16 v[182:183], v244 offset:1536\n\t" \
    "ds_read_b64_tr_b16 v[188:189], v244 offset:32\n\t" \
    "ds_read_b64_tr_b16 v[190:191], v244 offset:1568\n\t" \
    "ds_read_b64_tr_b16 v[196:197], v244 offset:64\n\t" \
    "ds_read_b64_tr_b16 v[198:199], v244 offset:1600\n\t" \
    "s_waitcnt lgkmcnt(0)\n\t" \
    "s_barrier \n\t" \
    LST0 \
    "ds_write_b128 v254, v[200:203] offset:0\n\t" \
    "ds_write_b128 v254, v[204:207] offset:1024\n\t" \
    "ds_write_b128 v254, v[208:211] offset:2048\n\t" \
    "ds_write_b128 v254, v[212:215] offset:3072\n\t" \
    "ds_write_b128 v254, v[216:219] offset:4096\n\t" \
    "ds_write_b128 v254, v[220:223] offset:5120\n\t" \
    "ds_write_b128 v254, v[224:227] offset:6144\n\t" \
    "ds_write_b128 v254, v[228:231] offset:7168\n\t" \
    "ds_write_b128 v254, v[232:235] offset:8192\n\t" \
    "ds_write_b128 v254, v[236:239] offset:9216\n\t" \
    "s_cmp_lg_u32 s43, 0\n\t" \
    "s_cbranch_scc1 .Ls44_%=\n\t" \
    "s_mov_b32 s44, 0\n\t" \
    ".Ls45_%=:\n\t" \
    "v_cmp_ge_u32 vcc, s44, v252\n\t" \
    "s_cbranch_vccz .Ls46_%=\n\t" \
    "s_cmp_eq_u32 s41, 0\n\t" \
    "s_cbranch_scc1 .Ls46_%=\n\t" \
    "s_sub_u32 s41, s41, 1\n\t" \
    "s_sleep 8\n\t" \
    "v_and_b32 v252, 0xf0, v253\n\t" \
    "v_lshlrev_b32 v252, 3, v252\n\t" \
    "buffer_load_dword v252, v252, s[60:63], %[fl] offen sc1\n\t" \
    "s_waitcnt vmcnt(0)\n\t" \
    "s_branch .Ls45_%=\n\t" \
    ".Ls46_%=:\n\t" \
    "s_mov_b32 m0, %[m0]\n\t" \
    "s_add_u32 s45, s42, %[x1]\n\t" \
    "buffer_load_dwordx4 v253, s[60:63], s42 offen offset:0 sc1 lds\n\t" \
    "buffer_load_dwordx4 v253, s[60:63], s42 offen offset:1024 sc1 lds\n\t" \
    "buffer_load_dwordx4 v253, s[60:63], s42 offen offset:2048 sc1 lds\n\t" \
    "buffer_load_dwordx4 v253, s[60:63], s42 offen offset:3072 sc1 lds\n\t" \
    "s_mov_b32 m0, %[m1]\n\t" \
    "s_nop 0\n\t" \
    "buffer_load_dwordx4 v253, s[60:63], s45 offen offset:0 sc1 lds\n\t" \
    "buffer_load_dwordx4 v253, s[60:63], s45 offen offset:1024 sc1 lds\n\t" \
    ".Ls44_%=:\n\t" \
    "s_waitcnt vmcnt(0) lgkmcnt(0)\n\t" \
    "s_barrier \n\t" \
    "ds_read_b128 v[224:227], v253 offset:0\n\t" \
    "ds_read_b128 v[244:247], v254 offset:0\n\t" \
    "ds_read_b128 v[228:231], v253 offset:1024\n\t" \
    "ds_read_b128 v[232:235], v253 offset:20480\n\t" \
    "ds_read_b128 v[236:239], v253 offset:21504\n\t" \
    "ds_read_b128 v[248:251], v254 offset:1024\n\t" \
    ".Ls47_%=:\n\t" \
    "s_cmp_lg_u32 %[wave], 4\n\t" \
    "s_cbranch_scc1 .Ls48_%=\n\t" \
    "s_sleep 9\n\t" \
    ".Ls48_%=:\n\t" \
    "s_waitcnt lgkmcnt(5)\n\t" \
    "v_mfma_f32_16x16x32_bf16 v[204:207], v[0:3], v[224:227], 0\n\t" \
    "s_add_u32 m0, %[m0], 0x3000\n\t" \
    "s_waitcnt lgkmcnt(3)\n\t" \
    "v_mfma_f32_16x16x32_bf16 v[204:207], v[4:7], v[228:231], v[204:207]\n\t" \
    "ds_read_b128 v[240:243], v253 offset:2048\n\t" \
    "s_add_u32 s45, s42, 0xc000\n\t" \
    "v_mfma_f32_16x16x32_bf16 v[208:211], v[8:11], v[224:227], 0\n\t" \
    "buffer_load_dwordx4 v253, s[60:63], s45 offen offset:0 sc1 lds\n\t" \
    "v_mfma_f32_16x16x32_bf16 v[208:211], v[12:15], v[228:231], v[208:211]\n\t" \
    "v_mfma_f32_16x16x32_bf16 v[200:203], v[244:247], v[224:227], 0\n\t" \
    "s_waitcnt lgkmcnt(1)\n\t" \
    "v_mfma_f32_16x16x32_bf16 v[200:203], v[248:251], v[228:231], v[200:203]\n\t" \
    "buffer_load_dwordx4 v253, s[60:63], s45 offen offset:1024 sc1 lds\n\t" \
    "v_mfma_f32_16x16x32_bf16 v[216:219], v[0:3], v[232:235], 0\n\t" \
    "ds_read_b128 v[224:227], v253 offset:3072\n\t" \
    "v_mfma_f32_16x16x32_bf16 v[216:219], v[4:7], v[236:239], v[216:219]\n\t" \
    "ds_read_b128 v[228:231], v253 offset:22528\n\t" \
    "v_mfma_f32_16x16x32_bf16 v[220:223], v[8:11], v[232:235], 0\n\t" \
    "buffer_load_dwordx4 v253, s[60:63], s45 offen offset:2048 sc1 lds\n\t" \
    "v_mfma_f32_16x16x32_bf16 v[220:223], v[12:15], v[236:239], v[220:223]\n\t" \
    "v_mfma_f32_16x16x32_bf16 v[212:215], v[244:247], v[232:235], 0\n\t" \
    "ds_read_b128 v[244:247], v254 offset:2048\n\t" \
    "v_mfma_f32_16x16x32_bf16 v[212:215], v[248:251], v[236:239], v[212:215]\n\t" \
    "ds_read_b128 v[248:251], v254 offset:3072\n\t" \
    "buffer_load_dwordx4 v253, s[60:63], s45 offen offset:3072 sc1 lds\n\t" \
    "s_waitcnt lgkmcnt(4)\n\t" \
    "v_mfma_f32_16x16x32_bf16 v[204:207], v[24:27], v[240:243], v[204:207]\n\t" \
    "ds_read_b128 v[232:235], v253 offset:23552\n\t" \
    "s_waitcnt lgkmcnt(4)\n\t" \
    "v_mfma_f32_16x16x32_bf16 v[204:207], v[28:31], v[224:227], v[204:207]\n\t" \
    "ds_read_b128 v[236:239], v253 offset:4096\n\t" \
    "v_mfma_f32_16x16x32_bf16 v[208:211], v[32:35], v[240:243], v[208:211]\n\t" \
    "v_mfma_f32_16x16x32_bf16 v[208:211], v[36:39], v[224:227], v[208:211]\n\t" \
    "v_mfma_f32_16x16x32_bf16 v[200:203], v[16:19], v[240:243], v[200:203]\n\t" \
    "v_mfma_f32_16x16x32_bf16 v[200:203], v[20:23], v[224:227], v[200:203]\n\t" \
    "s_waitcnt lgkmcnt(4)\n\t" \
    "v_mfma_f32_16x16x32_bf16 v[216:219], v[24:27], v[228:231], v[216:219]\n\t" \
    "ds_read_b128 v[240:243], v253 offset:5120\n\t" \
    "s_waitcnt lgkmcnt(2)\n\t" \
    "v_mfma_f32_16x16x32_bf16 v[216:219], v[28:31], v[232:235], v[216:219]\n\t" \
    "ds_read_b128 v[224:227], v253 offset:24576\n\t" \
    "v_mfma_f32_16x16x32_bf16 v[220:223], v[32:35], v[228:231], v[220:223]\n\t" \
    "v_mfma_f32_16x16x32_bf16 v[220:223], v[36:39], v[232:235], v[220:223]\n\t" \
    "v_mfma_f32_16x16x32_bf16 v[212:215], v[16:19], v[228:231], v[212:215]\n\t" \
    "v_mfma_f32_16x16x32_bf16 v[212:215], v[20:23], v[232:235], v[212:215]\n\t" \
    "s_waitcnt lgkmcnt(2)\n\t" \
    "v_mfma_f32_16x16x32_bf16 v[204:207], v[40:43], v[236:239], v[204:207]\n\t" \
    "ds_read_b128 v[228:231], v253 offset:25600\n\t" \
    "s_waitcnt lgkmcnt(2)\n\t" \
    "v_mfma_f32_16x16x32_bf16 v[204:207], v[44:47], v[240:243], v[204:207]\n\t" \
    "ds_read_b128 v[232:235], v253 offset:6144\n\t" \
    "v_mfma_f32_16x16x32_bf16 v[208:211], v[48:51], v[236:239], v[208:211]\n\t" \
    "v_mfma_f32_16x16x32_bf16 v[208:211], v[52:55], v[240:243], v[208:211]\n\t" \
    "v_mfma_f32_16x16x32_bf16 v[200:203], v[244:247], v[236:239], v[200:203]\n\t" \
    "v_mfma_f32_16x16x32_bf16 v[200:203], v[248:251], v[240:243], v[200:203]\n\t" \
    "s_waitcnt lgkmcnt(2)\n\t" \
    "v_mfma_f32_16x16x32_bf16 v[216:219], v[40:43], v[224:227], v[216:219]\n\t" \
    "ds_read_b128 v[236:239], v253 offset:7168\n\t" \
    "s_waitcnt lgkmcnt(2)\n\t" \
    "v_mfma_f32_16x16x32_bf16 v[216:219], v[44:47], v[228:231], v[216:219]\n\t" \
    "ds_read_b128 v[240:243], v253 offset:26624\n\t" \
    "v_mfma_f32_16x16x32_bf16 v[220:223], v[48:51], v[224:227], v[220:223]\n\t" \
    "v_mfma_f32_16x16x32_bf16 v[220:223], v[52:55], v[228:231], v[220:223]\n\t" \
    "v_mfma_f32_16x16x32_bf16 v[212:215], v[244:247], v[224:227], v[212:215]\n\t" \
    "ds_read_b128 v[244:247], v254 offset:4096\n\t" \
    "v_mfma_f32_16x16x32_bf16 v[212:215], v[248:251], v[228:231], v[212:215]\n\t" \
    "ds_read_b128 v[248:251], v254 offset:5120\n\t" \
    "s_waitcnt lgkmcnt(4)\n\t" \
    "v_mfma_f32_16x16x32_bf16 v[204:207], v[64:67], v[232:235], v[204:207]\n\t" \
    "ds_read_b128 v[224:227], v253 offset:27648\n\t" \
    "s_waitcnt lgkmcnt(4)\n\t" \
    "v_mfma_f32_16x16x32_bf16 v[204:207], v[68:71], v[236:239], v[204:207]\n\t" \
    "ds_read_b128 v[228:231], v253 offset:8192\n\t" \
    "v_mfma_f32_16x16x32_bf16 v[208:211], v[72:75], v[232:235], v[208:211]\n\t" \
    "v_mfma_f32_16x16x32_bf16 v[208:211], v[76:79], v[236:239], v[208:211]\n\t" \
    "v_mfma_f32_16x16x32_bf16 v[200:203], v[56:59], v[232:235], v[200:203]\n\t" \
    "v_mfma_f32_16x16x32_bf16 v[200:203], v[60:63], v[236:239], v[200:203]\n\t" \
    "v_and_b32 v252, 0xf0, v253\n\t" \
    "v_lshlrev_b32 v252, 3, v252\n\t" \
    "buffer_load_dword v252, v252, s[60:63], %[fl] offen sc1\n\t" \
    "s_waitcnt lgkmcnt(4)\n\t" \
    "v_mfma_f32_16x16x32_bf16 v[216:219], v[64:67], v[240:243], v[216:219]\n\t" \
    "ds_read_b128 v[232:235], v253 offset:9216\n\t" \
    "s_waitcnt lgkmcnt(2)\n\t" \
    "v_mfma_f32_16x16x32_bf16 v[216:219], v[68:71], v[224:227], v[216:219]\n\t" \
    "ds_read_b128 v[236:239], v253 offset:28672\n\t" \
    "v_mfma_f32_16x16x32_bf16 v[220:223], v[72:75], v[240:243], v[220:223]\n\t" \
    "v_mfma_f32_16x16x32_bf16 v[220:223], v[76:79], v[224:227], v[220:223]\n\t" \
    "v_mfma_f32_16x16x32_bf16 v[212:215], v[56:59], v[240:243], v[212:215]\n\t" \
    "v_mfma_f32_16x16x32_bf16 v[212:215], v[60:63], v[224:227], v[212:215]\n\t" \
    "s_waitcnt lgkmcnt(2)\n\t" \
    "v_mfma_f32_16x16x32_bf16 v[204:207], v[80:83], v[228:231], v[204:207]\n\t" \
    "ds_read_b128 v[240:243], v253 offset:29696\n\t" \
    "s_waitcnt lgkmcnt(2)\n\t" \
    "v_mfma_f32_16x16x32_bf16 v[204:207], v[84:87], v[232:235], v[204:207]\n\t" \
    "ds_read_b128 v[224:227], v253 offset:10240\n\t" \
    "v_mfma_f32_16x16x32_bf16 v[208:211], v[88:91], v[228:231], v[208:211]\n\t" \
    "v_mfma_f32_16x16x32_bf16 v[208:211], v[92:95], v[232:235], v[208:211]\n\t" \
    "v_mfma_f32_16x16x32_bf16 v[200:203], v[244:247], v[228:231], v[200:203]\n\t" \
    "v_mfma_f32_16x16x32_bf16 v[200:203], v[248:251], v[232:235], v[200:203]\n\t" \
    "s_waitcnt lgkmcnt(2)\n\t" \
    "v_mfma_f32_16x16x32_bf16 v[216:219], v[80:83], v[236:239], v[216:219]\n\t" \
    "ds_read_b128 v[228:231], v253 offset:11264\n\t" \
    "s_add_u32 s44, s40, 1\n\t" \
    "s_waitcnt lgkmcnt(2)\n\t" \
    "v_mfma_f32_16x16x32_bf16 v[216:219], v[84:87], v[240:243], v[216:219]\n\t" \
    "ds_read_b128 v[232:235], v253 offset:30720\n\t" \
    "s_and_b32 s45, s44, 15\n\t" \
    "v_mfma_f32_16x16x32_bf16 v[220:223], v[88:91], v[236:239], v[220:223]\n\t" \
    "s_cmp_eq_u32 s45, 0\n\t" \
    "v_mfma_f32_16x16x32_bf16 v[220:223], v[92:95], v[240:243], v[220:223]\n\t" \
    "s_cselect_b32 s45, %[cj], %[cs]\n\t" \
    "v_mfma_f32_16x16x32_bf16 v[212:215], v[244:247], v[236:239], v[212:215]\n\t" \
    "ds_read_b128 v[244:247], v254 offset:6144\n\t" \
    "s_cmp_eq_u32 s44, 64\n\t" \
    "v_mfma_f32_16x16x32_bf16 v[212:215], v[248:251], v[240:243], v[212:215]\n\t" \
    "ds_read_b128 v[248:251], v254 offset:7168\n\t" \
    "s_cselect_b32 s45, 0, s45\n\t" \
    "ds_read_b128 v[236:239], v253 offset:31744\n\t" \
    "s_waitcnt vmcnt(0) lgkmcnt(0)\n\t" \
    "s_barrier \n\t" \
    LST7 \
    "s_add_u32 s42, s42, s45\n\t" \
    "s_add_u32 s44, s40, 1\n\t" \
    "s_min_u32 s44, s44, 63\n\t" \
    ".Ls49_%=:\n\t" \
    "v_cmp_ge_u32 vcc, s44, v252\n\t" \
    "s_cbranch_vccz .Ls410_%=\n\t" \
    "s_cmp_eq_u32 s41, 0\n\t" \
    "s_cbranch_scc1 .Ls410_%=\n\t" \
    "s_sub_u32 s41, s41, 1\n\t" \
    "s_sleep 2\n\t" \
    "v_and_b32 v252, 0xf0, v253\n\t" \
    "v_lshlrev_b32 v252, 3, v252\n\t" \
    "buffer_load_dword v252, v252, s[60:63], %[fl] offen sc1\n\t" \
    "s_waitcnt vmcnt(0)\n\t" \
    "s_branch .Ls49_%=\n\t" \
    ".Ls410_%=:\n\t" \
    "v_mfma_f32_16x16x32_bf16 v[204:207], v[104:107], v[224:227], v[204:207]\n\t" \
    "s_mov_b32 m0, %[m0]\n\t" \
    "v_mfma_f32_16x16x32_bf16 v[204:207], v[108:111], v[228:231], v[204:207]\n\t" \
    "ds_read_b128 v[240:243], v253 offset:12288\n\t" \
    "s_add_u32 s45, s42, %[x1]\n\t" \
    "v_mfma_f32_16x16x32_bf16 v[208:211], v[112:115], v[224:227], v[208:211]\n\t" \
    "buffer_load_dwordx4 v253, s[60:63], s42 offen offset:0 sc1 lds\n\t" \
    "v_mfma_f32_16x16x32_bf16 v[208:211], v[116:119], v[228:231], v[208:211]\n\t" \
    "v_mfma_f32_16x16x32_bf16 v[200:203], v[96:99], v[224:227], v[200:203]\n\t" \
    "v_mfma_f32_16x16x32_bf16 v[200:203], v[100:103], v[228:231], v[200:203]\n\t" \
    "buffer_load_dwordx4 v253, s[60:63], s42 offen offset:1024 sc1 lds\n\t" \
    "v_mfma_f32_16x16x32_bf16 v[216:219], v[104:107], v[232:235], v[216:219]\n\t" \
    "ds_read_b128 v[224:227], v253 offset:13312\n\t" \
    "v_mfma_f32_16x16x32_bf16 v[216:219], v[108:111], v[236:239], v[216:219]\n\t" \
    "ds_read_b128 v[228:231], v253 offset:32768\n\t" \
    "v_mfma_f32_16x16x32_bf16 v[220:223], v[112:115], v[232:235], v[220:223]\n\t" \
    "buffer_load_dwordx4 v253, s[60:63], s42 offen offset:2048 sc1 lds\n\t" \
    "v_mfma_f32_16x16x32_bf16 v[220:223], v[116:119], v[236:239], v[220:223]\n\t" \
    "v_mfma_f32_16x16x32_bf16 v[212:215], v[96:99], v[232:235], v[212:215]\n\t" \
    "v_mfma_f32_16x16x32_bf16 v[212:215], v[100:103], v[236:239], v[212:215]\n\t" \
    "buffer_load_dwordx4 v253, s[60:63], s42 offen offset:3072 sc1 lds\n\t" \
    "s_waitcnt lgkmcnt(2)\n\t" \
    "v_mfma_f32_16x16x32_bf16 v[204:207], v[120:123], v[240:243], v[204:207]\n\t" \
    "ds_read_b128 v[232:235], v253 offset:33792\n\t" \
    "s_waitcnt lgkmcnt(2)\n\t" \
    "v_mfma_f32_16x16x32_bf16 v[204:207], v[124:127], v[224:227], v[204:207]\n\t" \
    "ds_read_b128 v[236:239], v253 offset:16384\n\t" \
    "v_mfma_f32_16x16x32_bf16 v[208:211], v[128:131], v[240:243], v[208:211]\n\t" \
    "s_mov_b32 m0, %[m1]\n\t" \
    "v_mfma_f32_16x16x32_bf16 v[208:211], v[132:135], v[224:227], v[208:211]\n\t" \
    "s_nop 0\n\t" \
    "v_mfma_f32_16x16x32_bf16 v[200:203], v[244:247], v[240:243], v[200:203]\n\t" \
    "buffer_load_dwordx4 v253, s[60:63], s45 offen offset:0 sc1 lds\n\t" \
    "v_mfma_f32_16x16x32_bf16 v[200:203], v[248:251], v[224:227], v[200:203]\n\t" \
    "s_waitcnt lgkmcnt(2)\n\t" \
    "v_mfma_f32_16x16x32_bf16 v[216:219], v[120:123], v[228:231], v[216:219]\n\t" \
    "ds_read_b128 v[240:243], v253 offset:17408\n\t" \
    "s_waitcnt lgkmcnt(2)\n\t" \
    "v_mfma_f32_16x16x32_bf16 v[216:219], v[124:127], v[232:235], v[216:219]\n\t" \
    "ds_read_b128 v[224:227], v253 offset:36864\n\t" \
    "buffer_load_dwordx4 v253, s[60:63], s45 offen offset:1024 sc1 lds\n\t" \
    "v_mfma_f32_16x16x32_bf16 v[220:223], v[128:131], v[228:231], v[220:223]\n\t" \
    "v_mfma_f32_16x16x32_bf16 v[220:223], v[132:135], v[232:235], v[220:223]\n\t" \
    "v_mfma_f32_16x16x32_bf16 v[212:215], v[244:247], v[228:231], v[212:215]\n\t" \
    "ds_read_b128 v[244:247], v254 offset:8192\n\t" \
    "v_mfma_f32_16x16x32_bf16 v[212:215], v[248:251], v[232:235], v[212:215]\n\t" \
    "ds_read_b128 v[248:251], v254 offset:9216\n\t" \
    "s_waitcnt lgkmcnt(4)\n\t" \
    "v_mfma_f32_16x16x32_bf16 v[204:207], v[160:163], v[236:239], v[204:207]\n\t" \
    "ds_read_b128 v[228:231], v253 offset:37888\n\t" \
    "s_waitcnt lgkmcnt(4)\n\t" \
    "v_mfma_f32_16x16x32_bf16 v[204:207], v[164:167], v[240:243], v[204:207]\n\t" \
    "ds_read_b128 v[232:235], v253 offset:14336\n\t" \
    "v_mfma_f32_16x16x32_bf16 v[208:211], v[168:171], v[236:239], v[208:211]\n\t" \
    "v_mfma_f32_16x16x32_bf16 v[208:211], v[172:175], v[240:243], v[208:211]\n\t" \
    "s_waitcnt lgkmcnt(3)\n\t" \
    "v_mfma_f32_16x16x32_bf16 v[200:203], v[244:247], v[236:239], v[200:203]\n\t" \
    "s_waitcnt lgkmcnt(2)\n\t" \
    "v_mfma_f32_16x16x32_bf16 v[200:203], v[248:251], v[240:243], v[200:203]\n\t" \
    "v_mfma_f32_16x16x32_bf16 v[216:219], v[160:163], v[224:227], v[216:219]\n\t" \
    "ds_read_b128 v[236:239], v253 offset:15360\n\t" \
    "s_waitcnt lgkmcnt(2)\n\t" \
    "v_mfma_f32_16x16x32_bf16 v[216:219], v[164:167], v[228:231], v[216:219]\n\t" \
    "ds_read_b128 v[240:243], v253 offset:18432\n\t" \
    "v_mfma_f32_16x16x32_bf16 v[220:223], v[168:171], v[224:227], v[220:223]\n\t" \
    "v_mfma_f32_16x16x32_bf16 v[220:223], v[172:175], v[228:231], v[220:223]\n\t" \
    "v_mfma_f32_16x16x32_bf16 v[212:215], v[244:247], v[224:227], v[212:215]\n\t" \
    "v_mfma_f32_16x16x32_bf16 v[212:215], v[248:251], v[228:231], v[212:215]\n\t" \
    "s_waitcnt lgkmcnt(2)\n\t" \
    "v_mfma_f32_16x16x32_bf16 v[204:207], v[144:147], v[232:235], v[204:207]\n\t" \
    "ds_read_b128 v[224:227], v253 offset:19456\n\t" \
    "v_mov_b64 v[244:245], 0\n\t" \
    "s_waitcnt lgkmcnt(2)\n\t" \
    "v_mfma_f32_16x16x32_bf16 v[204:207], v[148:151], v[236:239], v[204:207]\n\t" \
    "ds_read_b128 v[228:231], v253 offset:34816\n\t" \
    "v_mov_b64 v[246:247], 0\n\t" \
    "v_mfma_f32_16x16x32_bf16 v[208:211], v[152:155], v[232:235], v[208:211]\n\t" \
    "v_mov_b64 v[248:249], 0\n\t" \
    "v_mfma_f32_16x16x32_bf16 v[208:211], v[156:159], v[236:239], v[208:211]\n\t" \
    "v_mov_b64 v[250:251], 0\n\t" \
    "v_mfma_f32_16x16x32_bf16 v[200:203], v[136:139], v[232:235], v[200:203]\n\t" \
    "v_add_u32 v252, %[qs], v253\n\t" \
    "v_mfma_f32_16x16x32_bf16 v[200:203], v[140:143], v[236:239], v[200:203]\n\t" \
    "s_waitcnt lgkmcnt(2)\n\t" \
    "v_mfma_f32_16x16x32_bf16 v[204:207], v[184:187], v[240:243], v[204:207]\n\t" \
    "ds_read_b128 v[232:235], v253 offset:35840\n\t" \
    "s_waitcnt lgkmcnt(2)\n\t" \
    "v_mfma_f32_16x16x32_bf16 v[204:207], v[188:191], v[224:227], v[204:207]\n\t" \
    "ds_read_b128 v[236:239], v253 offset:38912\n\t" \
    "v_mfma_f32_16x16x32_bf16 v[208:211], v[192:195], v[240:243], v[208:211]\n\t" \
    "v_mfma_f32_16x16x32_bf16 v[208:211], v[196:199], v[224:227], v[208:211]\n\t" \
    "v_mfma_f32_16x16x32_bf16 v[200:203], v[176:179], v[240:243], v[200:203]\n\t" \
    "v_mfma_f32_16x16x32_bf16 v[200:203], v[180:183], v[224:227], v[200:203]\n\t" \
    "ds_read_b128 v[240:243], v253 offset:39936\n\t" \
    "s_waitcnt lgkmcnt(3)\n\t" \
    "v_mfma_f32_16x16x32_bf16 v[216:219], v[144:147], v[228:231], v[216:219]\n\t" \
    "v_add_u32 v224, %[q2], v253\n\t" \
    "s_waitcnt lgkmcnt(2)\n\t" \
    "v_mfma_f32_16x16x32_bf16 v[216:219], v[148:151], v[232:235], v[216:219]\n\t" \
    "v_cvt_pk_bf16_f32 v204, v204, v208\n\t" \
    "v_cvt_pk_bf16_f32 v205, v205, v209\n\t" \
    "s_waitcnt lgkmcnt(1)\n\t" \
    "v_mfma_f32_16x16x32_bf16 v[216:219], v[184:187], v[236:239], v[216:219]\n\t" \
    "v_cvt_pk_bf16_f32 v206, v206, v210\n\t" \
    "v_cvt_pk_bf16_f32 v207, v207, v211\n\t" \
    "s_waitcnt lgkmcnt(0)\n\t" \
    "v_mfma_f32_16x16x32_bf16 v[216:219], v[188:191], v[240:243], v[216:219]\n\t" \
    "ds_write_b128 v224, v[204:207] offset:0\n\t" \
    "v_cvt_pk_bf16_f32 v200, v200, v201\n\t" \
    "v_mfma_f32_16x16x32_bf16 v[220:223], v[152:155], v[228:231], v[220:223]\n\t" \
    "v_cvt_pk_bf16_f32 v201, v202, v203\n\t" \
    "ds_write_b64 v252, v[200:201] offset:0\n\t" \
    "v_mfma_f32_16x16x32_bf16 v[220:223], v[156:159], v[232:235], v[220:223]\n\t" \
    "ds_read_b128 v[200:203], v254 offset:0\n\t" \
    "ds_read_b128 v[204:207], v254 offset:1024\n\t" \
    "v_mfma_f32_16x16x32_bf16 v[220:223], v[192:195], v[236:239], v[220:223]\n\t" \
    "v_mov_b64 v[208:209], 0\n\t" \
    "v_mov_b64 v[210:211], 0\n\t" \
    "v_mfma_f32_16x16x32_bf16 v[220:223], v[196:199], v[240:243], v[220:223]\n\t" \
    "v_mfma_f32_16x16x32_bf16 v[212:215], v[136:139], v[228:231], v[212:215]\n\t" \
    "v_mfma_f32_16x16x32_bf16 v[212:215], v[140:143], v[232:235], v[212:215]\n\t" \
    "v_mfma_f32_16x16x32_bf16 v[212:215], v[176:179], v[236:239], v[212:215]\n\t" \
    "v_cvt_pk_bf16_f32 v216, v216, v220\n\t" \
    "v_cvt_pk_bf16_f32 v217, v217, v221\n\t" \
    "v_mfma_f32_16x16x32_bf16 v[212:215], v[180:183], v[240:243], v[212:215]\n\t" \
    "v_cvt_pk_bf16_f32 v218, v218, v222\n\t" \
    "v_cvt_pk_bf16_f32 v219, v219, v223\n\t" \
    "ds_write_b128 v224, v[216:219] offset:6144\n\t" \
    "ds_read_b128 v[216:219], v254 offset:2048\n\t" \
    "ds_read_b128 v[220:223], v254 offset:3072\n\t" \
    "s_nop 2\n\t" \
    "v_cvt_pk_bf16_f32 v212, v212, v213\n\t" \
    "v_cvt_pk_bf16_f32 v213, v214, v215\n\t" \
    "ds_write_b64 v252, v[212:213] offset:6144\n\t" \
    LST6 \
    "s_waitcnt vmcnt(0) lgkmcnt(0)\n\t" \
    "s_barrier \n\t" \
    LST3 \
    "ds_read_b128 v[224:227], v253 offset:40960\n\t" \
    "ds_read_b128 v[228:231], v253 offset:41984\n\t" \
    "ds_read_b128 v[232:235], v253 offset:43008\n\t" \
    "ds_read_b128 v[236:239], v253 offset:44032\n\t" \
    "ds_read_b128 v[240:243], v253 offset:45056\n\t" \
    LFLUSH212 \
    "v_bfe_i32 v212, v253, 5, 1\n\t" \
    "v_and_b32 v212, 0xaaaa, v212\n\t" \
    "v_or_b32 v212, 0x4444, v212\n\t" \
    "v_and_b32 v213, 0xffff, v255\n\t" \
    "v_lshrrev_b32 v214, 16, v255\n\t" \
    "s_waitcnt lgkmcnt(4)\n\t" \
    "v_smfmac_f32_16x16x64_bf16 v[208:211], v[224:227], v[200:207], v212\n\t" \
    "ds_read_b128 v[200:203], v254 offset:4096\n\t" \
    "ds_read_b128 v[204:207], v254 offset:5120\n\t" \
    "v_smfmac_f32_16x16x64_bf16 v[244:247], v[224:227], v[0:7], v212\n\t" \
    "v_smfmac_f32_16x16x64_bf16 v[248:251], v[224:227], v[8:15], v212\n\t" \
    "ds_read_b128 v[224:227], v253 offset:46080\n\t" \
    "s_waitcnt lgkmcnt(6)\n\t" \
    "v_smfmac_f32_16x16x64_bf16 v[208:211], v[228:231], v[16:23], v212\n\t" \
    "v_smfmac_f32_16x16x64_bf16 v[244:247], v[228:231], v[24:31], v212\n\t" \
    "v_smfmac_f32_16x16x64_bf16 v[248:251], v[228:231], v[32:39], v212\n\t" \
    "ds_read_b128 v[228:231], v253 offset:47104\n\t" \
    "s_waitcnt lgkmcnt(6)\n\t" \
    "v_smfmac_f32_16x16x64_bf16 v[208:211], v[232:235], v[216:223], v212\n\t" \
    "ds_read_b128 v[216:219], v254 offset:6144\n\t" \
    "ds_read_b128 v[220:223], v254 offset:7168\n\t" \
    "v_smfmac_f32_16x16x64_bf16 v[244:247], v[232:235], v[40:47], v212\n\t" \
    "v_smfmac_f32_16x16x64_bf16 v[248:251], v[232:235], v[48:55], v212\n\t" \
    "ds_read_b128 v[232:235], v253 offset:48128\n\t" \
    "s_waitcnt lgkmcnt(8)\n\t" \
    "v_smfmac_f32_16x16x64_bf16 v[208:211], v[236:239], v[56:63], v212\n\t" \
    "v_smfmac_f32_16x16x64_bf16 v[244:247], v[236:239], v[64:71], v212\n\t" \
    "v_smfmac_f32_16x16x64_bf16 v[248:251], v[236:239], v[72:79], v212\n\t" \
    "ds_read_b128 v[236:239], v253 offset:49152\n\t" \
    "s_waitcnt lgkmcnt(6)\n\t" \
    "v_smfmac_f32_16x16x64_bf16 v[208:211], v[240:243], v[200:207], v212\n\t" \
    "ds_read_b128 v[200:203], v254 offset:8192\n\t" \
    "ds_read_b128 v[204:207], v254 offset:9216\n\t" \
    "v_smfmac_f32_16x16x64_bf16 v[244:247], v[240:243], v[80:87], v212\n\t" \
    "v_smfmac_f32_16x16x64_bf16 v[248:251], v[240:243], v[88:95], v212\n\t" \
    "ds_read_b128 v[240:243], v253 offset:50176\n\t" \
    "s_waitcnt lgkmcnt(8)\n\t" \
    "v_smfmac_f32_16x16x64_bf16 v[208:211], v[224:227], v[96:103], v212\n\t" \
    "v_smfmac_f32_16x16x64_bf16 v[244:247], v[224:227], v[104:111], v212\n\t" \
    "v_smfmac_f32_16x16x64_bf16 v[248:251], v[224:227], v[112:119], v212\n\t" \
    "s_waitcnt lgkmcnt(5)\n\t" \
    "v_smfmac_f32_16x16x64_bf16 v[208:211], v[228:231], v[216:223], v212\n\t" \
    "s_waitcnt lgkmcnt(4)\n\t" \
    "v_smfmac_f32_16x16x64_bf16 v[208:211], v[232:235], v[136:143], v212\n\t" \
    "s_waitcnt lgkmcnt(1)\n\t" \
    "v_smfmac_f32_16x16x64_bf16 v[208:211], v[236:239], v[200:207], v212\n\t" \
    "s_waitcnt lgkmcnt(0)\n\t" \
    "v_smfmac_f32_16x16x64_bf16 v[208:211], v[240:243], v[176:183], v212\n\t" \
    "v_smfmac_f32_16x16x64_bf16 v[244:247], v[228:231], v[120:127], v212\n\t" \
    "v_smfmac_f32_16x16x64_bf16 v[244:247], v[232:235], v[144:151], v212\n\t" \
    "v_smfmac_f32_16x16x64_bf16 v[244:247], v[236:239], v[160:167], v212\n\t" \
    "v_add_f32 v208, v208, v210\n\t" \
    "v_add_f32 v209, v209, v211\n\t" \
    "v_cvt_pk_bf16_f32 v208, v208, v209\n\t" \
    "v_smfmac_f32_16x16x64_bf16 v[244:247], v[240:243], v[184:191], v212\n\t" \
    "ds_write_b16 v213, v208 offset:0\n\t" \
    "ds_write_b16_d16_hi v213, v208 offset:16\n\t" \
    "v_smfmac_f32_16x16x64_bf16 v[248:251], v[228:231], v[128:135], v212\n\t" \
    "v_smfmac_f32_16x16x64_bf16 v[248:251], v[232:235], v[152:159], v212\n\t" \
    "v_smfmac_f32_16x16x64_bf16 v[248:251], v[236:239], v[168:175], v212\n\t" \
    "v_add_f32 v244, v244, v246\n\t" \
    "v_add_f32 v245, v245, v247\n\t" \
    "v_smfmac_f32_16x16x64_bf16 v[248:251], v[240:243], v[192:199], v212\n\t" \
    "s_nop 7\n\t" \
    "v_add_f32 v248, v248, v250\n\t" \
    "v_add_f32 v249, v249, v251\n\t" \
    "v_cvt_pk_bf16_f32 v244, v244, v248\n\t" \
    "v_cvt_pk_bf16_f32 v245, v245, v249\n\t" \
    "ds_write2_b32 v214, v244, v245 offset1:4\n\t" \
    "s_waitcnt lgkmcnt(0)\n\t" \
    "s_barrier \n\t" \
    LST4 \
    "ds_read_b128 v[224:227], v253 offset:0\n\t" \
    "ds_read_b128 v[244:247], v254 offset:0\n\t" \
    "ds_read_b128 v[228:231], v253 offset:1024\n\t" \
    "ds_read_b128 v[232:235], v253 offset:20480\n\t" \
    "ds_read_b128 v[236:239], v253 offset:21504\n\t" \
    "ds_read_b128 v[248:251], v254 offset:1024\n\t" \
    "s_add_u32 s40, s40, 1\n\t" \
    "s_cmp_lg_u32 s40, 64\n\t" \
    "s_cbranch_scc1 .Ls47_%=\n\t" \
    "s_branch .Lend%=\n\t" \
    ".Ls3_%=:\n\t" \
    "s_add_u32 m0, %[wm], 0x18000\n\t" \
    "s_mov_b32 s44, 0x0\n\t" \
    "buffer_load_dwordx4 v240, %[wr], s44 offen offset:0 nt lds\n\t" \
    "buffer_load_dwordx4 v241, %[wr], s44 offen offset:1024 nt lds\n\t" \
    "buffer_load_dwordx4 v242, %[wr], s44 offen offset:2048 nt lds\n\t" \
    "s_add_u32 m0, %[wm], 0x1e000\n\t" \
    "s_mov_b32 s44, 0x60000\n\t" \
    "buffer_load_dwordx4 v240, %[wr], s44 offen offset:0 nt lds\n\t" \
    "buffer_load_dwordx4 v241, %[wr], s44 offen offset:1024 nt lds\n\t" \
    "buffer_load_dwordx4 v242, %[wr], s44 offen offset:2048 nt lds\n\t" \
    "s_add_u32 m0, %[wm], 0x0\n\t" \
    "s_mov_b32 s44, 0xc0000\n\t" \
    "buffer_load_dwordx4 v240, %[wr], s44 offen offset:0 nt lds\n\t" \
    "buffer_load_dwordx4 v241, %[wr], s44 offen offset:1024 nt lds\n\t" \
    "buffer_load_dwordx4 v242, %[wr], s44 offen offset:2048 nt lds\n\t" \
    "s_add_u32 m0, %[wm], 0x6000\n\t" \
    "s_mov_b32 s44, 0x120000\n\t" \
    "buffer_load_dwordx4 v240, %[wr], s44 offen offset:0 nt lds\n\t" \
    "buffer_load_dwordx4 v241, %[wr], s44 offen offset:1024 nt lds\n\t" \
    "buffer_load_dwordx4 v242, %[wr], s44 offen offset:2048 nt lds\n\t" \
    "s_add_u32 m0, %[wm], 0xc000\n\t" \
    "s_mov_b32 s44, 0x180000\n\t" \
    "buffer_load_dwordx4 v240, %[wr], s44 offen offset:0 nt lds\n\t" \
    "buffer_load_dwordx4 v241, %[wr], s44 offen offset:1024 nt lds\n\t" \
    "buffer_load_dwordx4 v242, %[wr], s44 offen offset:2048 nt lds\n\t" \
    "v_add_u32 v244, 0x18000, v243\n\t" \
    "s_waitcnt vmcnt(12) lgkmcnt(0)\n\t" \
    "s_barrier \n\t" \
    "s_add_u32 m0, %[wm], 0x12000\n\t" \
    "s_mov_b32 s44, 0x1e0000\n\t" \
    "buffer_load_dwordx4 v240, %[wr], s44 offen offset:0 nt lds\n\t" \
    "buffer_load_dwordx4 v241, %[wr], s44 offen offset:1024 nt lds\n\t" \
    "buffer_load_dwordx4 v242, %[wr], s44 offen offset:2048 nt lds\n\t" \
    "ds_read_b64_tr_b16 v[208:209], v244 offset:0\n\t" \
    "ds_read_b64_tr_b16 v[210:211], v244 offset:1536\n\t" \
    "ds_read_b64_tr_b16 v[40:41], v244 offset:32\n\t" \
    "ds_read_b64_tr_b16 v[42:43], v244 offset:1568\n\t" \
    "ds_read_b64_tr_b16 v[48:49], v244 offset:64\n\t" \
    "ds_read_b64_tr_b16 v[50:51], v244 offset:1600\n\t" \
    "v_add_u32 v244, 0x1e000, v243\n\t" \
    "s_waitcnt vmcnt(12) lgkmcnt(0)\n\t" \
    "s_barrier \n\t" \
    "s_add_u32 m0, %[wm], 0x18000\n\t" \
    "s_mov_b32 s44, 0x240000\n\t" \
    "buffer_load_dwordx4 v240, %[wr], s44 offen offset:0 nt lds\n\t" \
    "buffer_load_dwordx4 v241, %[wr], s44 offen offset:1024 nt lds\n\t" \
    "buffer_load_dwordx4 v242, %[wr], s44 offen offset:2048 nt lds\n\t" \
    "ds_read_b64_tr_b16 v[212:213], v244 offset:0\n\t" \
    "ds_read_b64_tr_b16 v[214:215], v244 offset:1536\n\t" \
    "ds_read_b64_tr_b16 v[44:45], v244 offset:32\n\t" \
    "ds_read_b64_tr_b16 v[46:47], v244 offset:1568\n\t" \
    "ds_read_b64_tr_b16 v[52:53], v244 offset:64\n\t" \
    "ds_read_b64_tr_b16 v[54:55], v244 offset:1600\n\t" \
    "v_add_u32 v244, 0x0, v243\n\t" \
    "s_waitcnt vmcnt(12) lgkmcnt(0)\n\t" \
    "s_barrier \n\t" \
    "s_add_u32 m0, %[wm], 0x1e000\n\t" \
    "s_mov_b32 s44, 0x2a0000\n\t" \
    "buffer_load_dwordx4 v240, %[wr], s44 offen offset:0 nt lds\n\t" \
    "buffer_load_dwordx4 v241, %[wr], s44 offen offset:1024 nt lds\n\t" \
    "buffer_load_dwordx4 v242, %[wr], s44 offen offset:2048 nt lds\n\t" \
    "ds_read_b64_tr_b16 v[56:57], v244 offset:0\n\t" \
    "ds_read_b64_tr_b16 v[58:59], v244 offset:1536\n\t" \
    "ds_read_b64_tr_b16 v[64:65], v244 offset:32\n\t" \
    "ds_read_b64_tr_b16 v[66:67], v244 offset:1568\n\t" \
    "ds_read_b64_tr_b16 v[72:73], v244 offset:64\n\t" \
    "ds_read_b64_tr_b16 v[74:75], v244 offset:1600\n\t" \
    "v_add_u32 v244, 0x6000, v243\n\t" \
    "s_waitcnt vmcnt(12) lgkmcnt(0)\n\t" \
    "s_barrier \n\t" \
    "s_add_u32 m0, %[wm], 0x0\n\t" \
    "s_mov_b32 s44, 0x300000\n\t" \
    "buffer_load_dwordx4 v240, %[wr], s44 offen offset:0 nt lds\n\t" \
    "buffer_load_dwordx4 v241, %[wr], s44 offen offset:1024 nt lds\n\t" \
    "buffer_load_dwordx4 v242, %[wr], s44 offen offset:2048 nt lds\n\t" \
    "ds_read_b64_tr_b16 v[60:61], v244 offset:0\n\t" \
    "ds_read_b64_tr_b16 v[62:63], v244 offset:1536\n\t" \
    "ds_read_b64_tr_b16 v[68:69], v244 offset:32\n\t" \
    "ds_read_b64_tr_b16 v[70:71], v244 offset:1568\n\t" \
    "ds_read_b64_tr_b16 v[76:77], v244 offset:64\n\t" \
    "ds_read_b64_tr_b16 v[78:79], v244 offset:1600\n\t" \
    "v_add_u32 v244, 0xc000, v243\n\t" \
    "s_waitcnt vmcnt(12) lgkmcnt(0)\n\t" \
    "s_barrier \n\t" \
    "s_add_u32 m0, %[wm], 0x6000\n\t" \
    "s_mov_b32 s44, 0x360000\n\t" \
    "buffer_load_dwordx4 v240, %[wr], s44 offen offset:0 nt lds\n\t" \
    "buffer_load_dwordx4 v241, %[wr], s44 offen offset:1024 nt lds\n\t" \
    "buffer_load_dwordx4 v242, %[wr], s44 offen offset:2048 nt lds\n\t" \
    "ds_read_b64_tr_b16 v[216:217], v244 offset:0\n\t" \
    "ds_read_b64_tr_b16 v[218:219], v244 offset:1536\n\t" \
    "ds_read_b64_tr_b16 v[80:81], v244 offset:32\n\t" \
    "ds_read_b64_tr_b16 v[82:83], v244 offset:1568\n\t" \
    "ds_read_b64_tr_b16 v[88:89], v244 offset:64\n\t" \
    "ds_read_b64_tr_b16 v[90:91], v244 offset:1600\n\t" \
    "v_add_u32 v244, 0x12000, v243\n\t" \
    "s_waitcnt vmcnt(12) lgkmcnt(0)\n\t" \
    "s_barrier \n\t" \
    "s_add_u32 m0, %[wm], 0xc000\n\t" \
    "s_mov_b32 s44, 0x3c0000\n\t" \
    "buffer_load_dwordx4 v240, %[wr], s44 offen offset:0 nt lds\n\t" \
    "buffer_load_dwordx4 v241, %[wr], s44 offen offset:1024 nt lds\n\t" \
    "buffer_load_dwordx4 v242, %[wr], s44 offen offset:2048 nt lds\n\t" \
    "ds_read_b64_tr_b16 v[220:221], v244 offset:0\n\t" \
    "ds_read_b64_tr_b16 v[222:223], v244 offset:1536\n\t" \
    "ds_read_b64_tr_b16 v[84:85], v244 offset:32\n\t" \
    "ds_read_b64_tr_b16 v[86:87], v244 offset:1568\n\t" \
    "ds_read_b64_tr_b16 v[92:93], v244 offset:64\n\t" \
    "ds_read_b64_tr_b16 v[94:95], v244 offset:1600\n\t" \
    "v_add_u32 v244, 0x18000, v243\n\t" \
    "s_waitcnt vmcnt(12) lgkmcnt(0)\n\t" \
    "s_barrier \n\t" \
    "v_mov_b32 v246, 0\n\t" \
    "buffer_load_dword v246, v246, s[60:63], %[mo] offen sc1\n\t" \
    "s_add_u32 m0, %[wm], 0x12000\n\t" \
    "s_mov_b32 s44, 0x420000\n\t" \
    "buffer_load_dwordx4 v240, %[wr], s44 offen offset:0 nt lds\n\t" \
    "buffer_load_dwordx4 v241, %[wr], s44 offen offset:1024 nt lds\n\t" \
    "buffer_load_dwordx4 v242, %[wr], s44 offen offset:2048 nt lds\n\t" \
    "ds_read_b64_tr_b16 v[96:97], v244 offset:0\n\t" \
    "ds_read_b64_tr_b16 v[98:99], v244 offset:1536\n\t" \
    "ds_read_b64_tr_b16 v[104:105], v244 offset:32\n\t" \
    "ds_read_b64_tr_b16 v[106:107], v244 offset:1568\n\t" \
    "ds_read_b64_tr_b16 v[112:113], v244 offset:64\n\t" \
    "ds_read_b64_tr_b16 v[114:115], v244 offset:1600\n\t" \
    "v_add_u32 v244, 0x1e000, v243\n\t" \
    "s_waitcnt vmcnt(12) lgkmcnt(0)\n\t" \
    "s_barrier \n\t" \
    "s_add_u32 m0, %[wm], 0x18000\n\t" \
    "s_mov_b32 s44, 0x480000\n\t" \
    "buffer_load_dwordx4 v240, %[wr], s44 offen offset:0 nt lds\n\t" \
    "buffer_load_dwordx4 v241, %[wr], s44 offen offset:1024 nt lds\n\t" \
    "buffer_load_dwordx4 v242, %[wr], s44 offen offset:2048 nt lds\n\t" \
    "ds_read_b64_tr_b16 v[100:101], v244 offset:0\n\t" \
    "ds_read_b64_tr_b16 v[102:103], v244 offset:1536\n\t" \
    "ds_read_b64_tr_b16 v[108:109], v244 offset:32\n\t" \
    "ds_read_b64_tr_b16 v[110:111], v244 offset:1568\n\t" \
    "ds_read_b64_tr_b16 v[116:117], v244 offset:64\n\t" \
    "ds_read_b64_tr_b16 v[118:119], v244 offset:1600\n\t" \
    "v_add_u32 v244, 0x0, v243\n\t" \
    "s_waitcnt vmcnt(12) lgkmcnt(0)\n\t" \
    "s_barrier \n\t" \
    "s_add_u32 m0, %[wm], 0x1e000\n\t" \
    "s_mov_b32 s44, 0x4e0000\n\t" \
    "buffer_load_dwordx4 v240, %[wr], s44 offen offset:0 nt lds\n\t" \
    "buffer_load_dwordx4 v241, %[wr], s44 offen offset:1024 nt lds\n\t" \
    "buffer_load_dwordx4 v242, %[wr], s44 offen offset:2048 nt lds\n\t" \
    "ds_read_b64_tr_b16 v[224:225], v244 offset:0\n\t" \
    "ds_read_b64_tr_b16 v[226:227], v244 offset:1536\n\t" \
    "ds_read_b64_tr_b16 v[120:121], v244 offset:32\n\t" \
    "ds_read_b64_tr_b16 v[122:123], v244 offset:1568\n\t" \
    "ds_read_b64_tr_b16 v[128:129], v244 offset:64\n\t" \
    "ds_read_b64_tr_b16 v[130:131], v244 offset:1600\n\t" \
    "v_add_u32 v244, 0x6000, v243\n\t" \
    "s_waitcnt vmcnt(12) lgkmcnt(0)\n\t" \
    "s_barrier \n\t" \
    "s_add_u32 m0, %[wm], 0x0\n\t" \
    "s_mov_b32 s44, 0x540000\n\t" \
    "buffer_load_dwordx4 v240, %[wr], s44 offen offset:0 nt lds\n\t" \
    "buffer_load_dwordx4 v241, %[wr], s44 offen offset:1024 nt lds\n\t" \
    "buffer_load_dwordx4 v242, %[wr], s44 offen offset:2048 nt lds\n\t" \
    "ds_read_b64_tr_b16 v[228:229], v244 offset:0\n\t" \
    "ds_read_b64_tr_b16 v[230:231], v244 offset:1536\n\t" \
    "ds_read_b64_tr_b16 v[124:125], v244 offset:32\n\t" \
    "ds_read_b64_tr_b16 v[126:127], v244 offset:1568\n\t" \
    "ds_read_b64_tr_b16 v[132:133], v244 offset:64\n\t" \
    "ds_read_b64_tr_b16 v[134:135], v244 offset:1600\n\t" \
    "v_add_u32 v244, 0xc000, v243\n\t" \
    "s_waitcnt vmcnt(12) lgkmcnt(0)\n\t" \
    "s_barrier \n\t" \
    "s_add_u32 m0, %[wm], 0x6000\n\t" \
    "s_mov_b32 s44, 0x5a0000\n\t" \
    "buffer_load_dwordx4 v240, %[wr], s44 offen offset:0 nt lds\n\t" \
    "buffer_load_dwordx4 v241, %[wr], s44 offen offset:1024 nt lds\n\t" \
    "buffer_load_dwordx4 v242, %[wr], s44 offen offset:2048 nt lds\n\t" \
    "ds_read_b64_tr_b16 v[136:137], v244 offset:0\n\t" \
    "ds_read_b64_tr_b16 v[138:139], v244 offset:1536\n\t" \
    "ds_read_b64_tr_b16 v[144:145], v244 offset:32\n\t" \
    "ds_read_b64_tr_b16 v[146:147], v244 offset:1568\n\t" \
    "ds_read_b64_tr_b16 v[152:153], v244 offset:64\n\t" \
    "ds_read_b64_tr_b16 v[154:155], v244 offset:1600\n\t" \
    "v_add_u32 v244, 0x12000, v243\n\t" \
    "s_waitcnt vmcnt(12) lgkmcnt(0)\n\t" \
    "s_barrier \n\t" \
    ".Ls31_%=:\n\t" \
    "v_readfirstlane_b32 s43, v246\n\t" \
    "s_nop 0\n\t" \
    "s_cmp_lg_u32 s43, -1\n\t" \
    "s_cbranch_scc1 .Ls32_%=\n\t" \
    "s_cmp_eq_u32 s41, 0\n\t" \
    "s_cbranch_scc1 .Ls32_%=\n\t" \
    "s_sub_u32 s41, s41, 1\n\t" \
    "s_sleep 4\n\t" \
    "v_mov_b32 v246, 0\n\t" \
    "buffer_load_dword v246, v246, s[60:63], %[mo] offen sc1\n\t" \
    "s_waitcnt vmcnt(0)\n\t" \
    "s_branch .Ls31_%=\n\t" \
    ".Ls32_%=:\n\t" \
    "v_mov_b32 v246, 0x8000\n\t" \
    "s_mov_b64 exec, 1\n\t" \
    "buffer_store_dword v246, v246, s[60:63], %[mo] offen sc1\n\t" \
    "s_mov_b64 exec, -1\n\t" \
    "s_and_b32 s43, s43, 0x2000\n\t" \
    "s_xor_b32 s43, s43, %[lx]\n\t" \
    "s_add_u32 s60, s60, s43\n\t" \
    "s_addc_u32 s61, s61, 0\n\t" \
    "s_add_u32 m0, %[wm], 0xc000\n\t" \
    "s_mov_b32 s44, 0x600000\n\t" \
    "buffer_load_dwordx4 v240, %[wr], s44 offen offset:0 nt lds\n\t" \
    "buffer_load_dwordx4 v241, %[wr], s44 offen offset:1024 nt lds\n\t" \
    "buffer_load_dwordx4 v242, %[wr], s44 offen offset:2048 nt lds\n\t" \
    "ds_read_b64_tr_b16 v[140:141], v244 offset:0\n\t" \
    "ds_read_b64_tr_b16 v[142:143], v244 offset:1536\n\t" \
    "ds_read_b64_tr_b16 v[148:149], v244 offset:32\n\t" \
    "ds_read_b64_tr_b16 v[150:151], v244 offset:1568\n\t" \
    "ds_read_b64_tr_b16 v[156:157], v244 offset:64\n\t" \
    "ds_read_b64_tr_b16 v[158:159], v244 offset:1600\n\t" \
    "v_add_u32 v244, 0x18000, v243\n\t" \
    "s_waitcnt vmcnt(12) lgkmcnt(0)\n\t" \
    "s_barrier \n\t" \
    "s_add_u32 m0, %[wm], 0x12000\n\t" \
    "s_mov_b32 s44, 0x660000\n\t" \
    "buffer_load_dwordx4 v240, %[wr], s44 offen offset:0 nt lds\n\t" \
    "buffer_load_dwordx4 v241, %[wr], s44 offen offset:1024 nt lds\n\t" \
    "buffer_load_dwordx4 v242, %[wr], s44 offen offset:2048 nt lds\n\t" \
    "ds_read_b64_tr_b16 v[232:233], v244 offset:0\n\t" \
    "ds_read_b64_tr_b16 v[234:235], v244 offset:1536\n\t" \
    "ds_read_b64_tr_b16 v[160:161], v244 offset:32\n\t" \
    "ds_read_b64_tr_b16 v[162:163], v244 offset:1568\n\t" \
    "ds_read_b64_tr_b16 v[168:169], v244 offset:64\n\t" \
    "ds_read_b64_tr_b16 v[170:171], v244 offset:1600\n\t" \
    "v_add_u32 v244, 0x1e000, v243\n\t" \
    "s_waitcnt vmcnt(12) lgkmcnt(0)\n\t" \
    "s_barrier \n\t" \
    "s_add_u32 m0, %[wm], 0x18000\n\t" \
    "s_mov_b32 s44, 0x6c0000\n\t" \
    "buffer_load_dwordx4 v240, %[wr], s44 offen offset:0 nt lds\n\t" \
    "buffer_load_dwordx4 v241, %[wr], s44 offen offset:1024 nt lds\n\t" \
    "buffer_load_dwordx4 v242, %[wr], s44 offen offset:2048 nt lds\n\t" \
    "ds_read_b64_tr_b16 v[236:237], v244 offset:0\n\t" \
    "ds_read_b64_tr_b16 v[238:239], v244 offset:1536\n\t" \
    "ds_read_b64_tr_b16 v[164:165], v244 offset:32\n\t" \
    "ds_read_b64_tr_b16 v[166:167], v244 offset:1568\n\t" \
    "ds_read_b64_tr_b16 v[172:173], v244 offset:64\n\t" \
    "ds_read_b64_tr_b16 v[174:175], v244 offset:1600\n\t" \
    "v_add_u32 v244, 0x0, v243\n\t" \
    "s_waitcnt vmcnt(12) lgkmcnt(0)\n\t" \
    "s_barrier \n\t" \
    "s_add_u32 m0, %[wm], 0x1e000\n\t" \
    "s_mov_b32 s44, 0x720000\n\t" \
    "buffer_load_dwordx4 v240, %[wr], s44 offen offset:0 nt lds\n\t" \
    "buffer_load_dwordx4 v241, %[wr], s44 offen offset:1024 nt lds\n\t" \
    "buffer_load_dwordx4 v242, %[wr], s44 offen offset:2048 nt lds\n\t" \
    "ds_read_b64_tr_b16 v[176:177], v244 offset:0\n\t" \
    "ds_read_b64_tr_b16 v[178:179], v244 offset:1536\n\t" \
    "ds_read_b64_tr_b16 v[184:185], v244 offset:32\n\t" \
    "ds_read_b64_tr_b16 v[186:187], v244 offset:1568\n\t" \
    "ds_read_b64_tr_b16 v[192:193], v244 offset:64\n\t" \
    "ds_read_b64_tr_b16 v[194:195], v244 offset:1600\n\t" \
    "v_add_u32 v244, 0x6000, v243\n\t" \
    "s_waitcnt vmcnt(12) lgkmcnt(0)\n\t" \
    "s_barrier \n\t" \
    "ds_read_b64_tr_b16 v[180:181], v244 offset:0\n\t" \
    "ds_read_b64_tr_b16 v[182:183], v244 offset:1536\n\t" \
    "ds_read_b64_tr_b16 v[188:189], v244 offset:32\n\t" \
    "ds_read_b64_tr_b16 v[190:191], v244 offset:1568\n\t" \
    "ds_read_b64_tr_b16 v[196:197], v244 offset:64\n\t" \
    "ds_read_b64_tr_b16 v[198:199], v244 offset:1600\n\t" \
    "v_add_u32 v244, 0xc000, v243\n\t" \
    "s_waitcnt vmcnt(9) lgkmcnt(0)\n\t" \
    "s_barrier \n\t" \
    "ds_read_b64_tr_b16 v[200:201], v244 offset:0\n\t" \
    "ds_read_b64_tr_b16 v[202:203], v244 offset:1536\n\t" \
    "ds_read_b64_tr_b16 v[0:1], v244 offset:32\n\t" \
    "ds_read_b64_tr_b16 v[2:3], v244 offset:1568\n\t" \
    "ds_read_b64_tr_b16 v[8:9], v244 offset:64\n\t" \
    "ds_read_b64_tr_b16 v[10:11], v244 offset:1600\n\t" \
    "v_add_u32 v244, 0x12000, v243\n\t" \
    "s_waitcnt vmcnt(6) lgkmcnt(0)\n\t" \
    "s_barrier \n\t" \
    "ds_read_b64_tr_b16 v[204:205], v244 offset:0\n\t" \
    "ds_read_b64_tr_b16 v[206:207], v244 offset:1536\n\t" \
    "ds_read_b64_tr_b16 v[4:5], v244 offset:32\n\t" \
    "ds_read_b64_tr_b16 v[6:7], v244 offset:1568\n\t" \
    "ds_read_b64_tr_b16 v[12:13], v244 offset:64\n\t" \
    "ds_read_b64_tr_b16 v[14:15], v244 offset:1600\n\t" \
    "v_add_u32 v244, 0x18000, v243\n\t" \
    "s_waitcnt vmcnt(3) lgkmcnt(0)\n\t" \
    "s_barrier \n\t" \
    "ds_read_b64_tr_b16 v[16:17], v244 offset:0\n\t" \
    "ds_read_b64_tr_b16 v[18:19], v244 offset:1536\n\t" \
    "ds_read_b64_tr_b16 v[24:25], v244 offset:32\n\t" \
    "ds_read_b64_tr_b16 v[26:27], v244 offset:1568\n\t" \
    "ds_read_b64_tr_b16 v[32:33], v244 offset:64\n\t" \
    "ds_read_b64_tr_b16 v[34:35], v244 offset:1600\n\t" \
    "v_add_u32 v244, 0x1e000, v243\n\t" \
    "s_waitcnt vmcnt(0) lgkmcnt(0)\n\t" \
    "s_barrier \n\t" \
    "ds_read_b64_tr_b16 v[20:21], v244 offset:0\n\t" \
    "ds_read_b64_tr_b16 v[22:23], v244 offset:1536\n\t" \
    "ds_read_b64_tr_b16 v[28:29], v244 offset:32\n\t" \
    "ds_read_b64_tr_b16 v[30:31], v244 offset:1568\n\t" \
    "ds_read_b64_tr_b16 v[36:37], v244 offset:64\n\t" \
    "ds_read_b64_tr_b16 v[38:39], v244 offset:1600\n\t" \
    "s_waitcnt lgkmcnt(0)\n\t" \
    "s_barrier \n\t" \
    LST0 \
    "ds_write_b128 v254, v[200:203] offset:55296\n\t" \
    "ds_write_b128 v254, v[204:207] offset:56320\n\t" \
    "ds_write_b128 v254, v[208:211] offset:57344\n\t" \
    "ds_write_b128 v254, v[212:215] offset:58368\n\t" \
    "ds_write_b128 v254, v[216:219] offset:59392\n\t" \
    "ds_write_b128 v254, v[220:223] offset:60416\n\t" \
    "ds_write_b128 v254, v[224:227] offset:61440\n\t" \
    "ds_write_b128 v254, v[228:231] offset:62464\n\t" \
    "ds_write_b128 v254, v[232:235] offset:63488\n\t" \
    "ds_write_b128 v254, v[236:239] offset:64512\n\t" \
    "s_waitcnt vmcnt(0) lgkmcnt(0)\n\t" \
    "s_barrier \n\t" \
    "ds_read_b128 v[208:211], v254 offset:24576\n\t" \
    "ds_read_b128 v[224:227], v253 offset:0\n\t" \
    "ds_read_b128 v[228:231], v253 offset:20480\n\t" \
    "ds_read_b128 v[212:215], v254 offset:25600\n\t" \
    "ds_read_b128 v[232:235], v253 offset:1024\n\t" \
    "ds_read_b128 v[236:239], v253 offset:21504\n\t" \
    "ds_read_b128 v[216:219], v254 offset:26624\n\t" \
    "ds_read_b128 v[240:243], v253 offset:2048\n\t" \
    "ds_read_b128 v[244:247], v253 offset:22528\n\t" \
    "ds_read_b128 v[220:223], v254 offset:27648\n\t" \
    "ds_read_b128 v[248:251], v253 offset:3072\n\t" \
    ".Ls33_%=:\n\t" \
    "s_waitcnt lgkmcnt(9)\n\t" \
    "v_mfma_f32_16x16x32_bf16 v[200:203], v[208:211], v[224:227], 0\n\t" \
    "ds_read_b128 v[224:227], v253 offset:23552\n\t" \
    "s_waitcnt lgkmcnt(9)\n\t" \
    "v_mfma_f32_16x16x32_bf16 v[204:207], v[208:211], v[228:231], 0\n\t" \
    "ds_read_b128 v[228:231], v253 offset:4096\n\t" \
    "ds_read_b128 v[208:211], v254 offset:28672\n\t" \
    "s_waitcnt lgkmcnt(9)\n\t" \
    "v_mfma_f32_16x16x32_bf16 v[200:203], v[212:215], v[232:235], v[200:203]\n\t" \
    "ds_read_b128 v[232:235], v253 offset:24576\n\t" \
    "s_waitcnt lgkmcnt(9)\n\t" \
    "v_mfma_f32_16x16x32_bf16 v[204:207], v[212:215], v[236:239], v[204:207]\n\t" \
    "ds_read_b128 v[236:239], v253 offset:5120\n\t" \
    "ds_read_b128 v[212:215], v254 offset:29696\n\t" \
    "s_waitcnt lgkmcnt(9)\n\t" \
    "v_mfma_f32_16x16x32_bf16 v[200:203], v[216:219], v[240:243], v[200:203]\n\t" \
    "ds_read_b128 v[240:243], v253 offset:25600\n\t" \
    "s_waitcnt lgkmcnt(9)\n\t" \
    "v_mfma_f32_16x16x32_bf16 v[204:207], v[216:219], v[244:247], v[204:207]\n\t" \
    "ds_read_b128 v[244:247], v253 offset:6144\n\t" \
    "ds_read_b128 v[216:219], v254 offset:30720\n\t" \
    "s_waitcnt lgkmcnt(9)\n\t" \
    "v_mfma_f32_16x16x32_bf16 v[200:203], v[220:223], v[248:251], v[200:203]\n\t" \
    "ds_read_b128 v[248:251], v253 offset:26624\n\t" \
    "s_waitcnt lgkmcnt(9)\n\t" \
    "v_mfma_f32_16x16x32_bf16 v[204:207], v[220:223], v[224:227], v[204:207]\n\t" \
    "ds_read_b128 v[224:227], v253 offset:7168\n\t" \
    "ds_read_b128 v[220:223], v254 offset:31744\n\t" \
    "s_waitcnt lgkmcnt(9)\n\t" \
    "v_mfma_f32_16x16x32_bf16 v[200:203], v[208:211], v[228:231], v[200:203]\n\t" \
    "ds_read_b128 v[228:231], v253 offset:27648\n\t" \
    "s_waitcnt lgkmcnt(9)\n\t" \
    "v_mfma_f32_16x16x32_bf16 v[204:207], v[208:211], v[232:235], v[204:207]\n\t" \
    "ds_read_b128 v[232:235], v253 offset:8192\n\t" \
    "ds_read_b128 v[208:211], v254 offset:32768\n\t" \
    "s_waitcnt lgkmcnt(9)\n\t" \
    "v_mfma_f32_16x16x32_bf16 v[200:203], v[212:215], v[236:239], v[200:203]\n\t" \
    "ds_read_b128 v[236:239], v253 offset:28672\n\t" \
    "s_waitcnt lgkmcnt(9)\n\t" \
    "v_mfma_f32_16x16x32_bf16 v[204:207], v[212:215], v[240:243], v[204:207]\n\t" \
    "ds_read_b128 v[240:243], v253 offset:9216\n\t" \
    "ds_read_b128 v[212:215], v254 offset:33792\n\t" \
    "s_waitcnt lgkmcnt(9)\n\t" \
    "v_mfma_f32_16x16x32_bf16 v[200:203], v[216:219], v[244:247], v[200:203]\n\t" \
    "ds_read_b128 v[244:247], v253 offset:29696\n\t" \
    "s_waitcnt lgkmcnt(9)\n\t" \
    "v_mfma_f32_16x16x32_bf16 v[204:207], v[216:219], v[248:251], v[204:207]\n\t" \
    "s_waitcnt lgkmcnt(7)\n\t" \
    "v_mfma_f32_16x16x32_bf16 v[200:203], v[220:223], v[224:227], v[200:203]\n\t" \
    "s_waitcnt lgkmcnt(6)\n\t" \
    "v_mfma_f32_16x16x32_bf16 v[204:207], v[220:223], v[228:231], v[204:207]\n\t" \
    "s_waitcnt lgkmcnt(4)\n\t" \
    "v_mfma_f32_16x16x32_bf16 v[200:203], v[208:211], v[232:235], v[200:203]\n\t" \
    "s_waitcnt lgkmcnt(3)\n\t" \
    "v_mfma_f32_16x16x32_bf16 v[204:207], v[208:211], v[236:239], v[204:207]\n\t" \
    "s_waitcnt lgkmcnt(1)\n\t" \
    "v_mfma_f32_16x16x32_bf16 v[200:203], v[212:215], v[240:243], v[200:203]\n\t" \
    "s_waitcnt lgkmcnt(0)\n\t" \
    "v_mfma_f32_16x16x32_bf16 v[204:207], v[212:215], v[244:247], v[204:207]\n\t" \
    "ds_read_b128 v[224:227], v253 offset:0\n\t" \
    "ds_read_b128 v[244:247], v254 offset:57344\n\t" \
    "ds_read_b128 v[228:231], v253 offset:1024\n\t" \
    "ds_read_b128 v[232:235], v253 offset:20480\n\t" \
    "ds_read_b128 v[236:239], v253 offset:21504\n\t" \
    "ds_read_b128 v[248:251], v254 offset:58368\n\t" \
    "s_nop 1\n\t" \
    "ds_write_b128 v253, v[200:203] offset:40960\n\t" \
    "ds_write_b128 v253, v[204:207] offset:41984\n\t" \
    "s_nop 1\n\t" \
    "s_waitcnt lgkmcnt(7)\n\t" \
    "v_mfma_f32_16x16x32_bf16 v[204:207], v[40:43], v[224:227], 0\n\t" \
    "s_waitcnt lgkmcnt(5)\n\t" \
    "v_mfma_f32_16x16x32_bf16 v[204:207], v[44:47], v[228:231], v[204:207]\n\t" \
    "ds_read_b128 v[240:243], v253 offset:2048\n\t" \
    "v_mfma_f32_16x16x32_bf16 v[208:211], v[48:51], v[224:227], 0\n\t" \
    "v_mfma_f32_16x16x32_bf16 v[208:211], v[52:55], v[228:231], v[208:211]\n\t" \
    "v_mfma_f32_16x16x32_bf16 v[200:203], v[244:247], v[224:227], 0\n\t" \
    "s_waitcnt lgkmcnt(3)\n\t" \
    "v_mfma_f32_16x16x32_bf16 v[200:203], v[248:251], v[228:231], v[200:203]\n\t" \
    "v_mfma_f32_16x16x32_bf16 v[216:219], v[40:43], v[232:235], 0\n\t" \
    "ds_read_b128 v[224:227], v253 offset:3072\n\t" \
    "v_mfma_f32_16x16x32_bf16 v[216:219], v[44:47], v[236:239], v[216:219]\n\t" \
    "ds_read_b128 v[228:231], v253 offset:22528\n\t" \
    "v_mfma_f32_16x16x32_bf16 v[220:223], v[48:51], v[232:235], 0\n\t" \
    "v_mfma_f32_16x16x32_bf16 v[220:223], v[52:55], v[236:239], v[220:223]\n\t" \
    "v_mfma_f32_16x16x32_bf16 v[212:215], v[244:247], v[232:235], 0\n\t" \
    "ds_read_b128 v[244:247], v254 offset:59392\n\t" \
    "v_mfma_f32_16x16x32_bf16 v[212:215], v[248:251], v[236:239], v[212:215]\n\t" \
    "ds_read_b128 v[248:251], v254 offset:60416\n\t" \
    "s_waitcnt lgkmcnt(4)\n\t" \
    "v_mfma_f32_16x16x32_bf16 v[204:207], v[64:67], v[240:243], v[204:207]\n\t" \
    "ds_read_b128 v[232:235], v253 offset:23552\n\t" \
    "s_waitcnt lgkmcnt(4)\n\t" \
    "v_mfma_f32_16x16x32_bf16 v[204:207], v[68:71], v[224:227], v[204:207]\n\t" \
    "ds_read_b128 v[236:239], v253 offset:4096\n\t" \
    "v_mfma_f32_16x16x32_bf16 v[208:211], v[72:75], v[240:243], v[208:211]\n\t" \
    "v_mfma_f32_16x16x32_bf16 v[208:211], v[76:79], v[224:227], v[208:211]\n\t" \
    "v_mfma_f32_16x16x32_bf16 v[200:203], v[56:59], v[240:243], v[200:203]\n\t" \
    "v_mfma_f32_16x16x32_bf16 v[200:203], v[60:63], v[224:227], v[200:203]\n\t" \
    "s_waitcnt lgkmcnt(4)\n\t" \
    "v_mfma_f32_16x16x32_bf16 v[216:219], v[64:67], v[228:231], v[216:219]\n\t" \
    "ds_read_b128 v[240:243], v253 offset:5120\n\t" \
    "s_waitcnt lgkmcnt(2)\n\t" \
    "v_mfma_f32_16x16x32_bf16 v[216:219], v[68:71], v[232:235], v[216:219]\n\t" \
    "ds_read_b128 v[224:227], v253 offset:24576\n\t" \
    "v_mfma_f32_16x16x32_bf16 v[220:223], v[72:75], v[228:231], v[220:223]\n\t" \
    "v_mfma_f32_16x16x32_bf16 v[220:223], v[76:79], v[232:235], v[220:223]\n\t" \
    "v_mfma_f32_16x16x32_bf16 v[212:215], v[56:59], v[228:231], v[212:215]\n\t" \
    "v_mfma_f32_16x16x32_bf16 v[212:215], v[60:63], v[232:235], v[212:215]\n\t" \
    "s_waitcnt lgkmcnt(2)\n\t" \
    "v_mfma_f32_16x16x32_bf16 v[204:207], v[80:83], v[236:239], v[204:207]\n\t" \
    "ds_read_b128 v[228:231], v253 offset:25600\n\t" \
    "s_waitcnt lgkmcnt(2)\n\t" \
    "v_mfma_f32_16x16x32_bf16 v[204:207], v[84:87], v[240:243], v[204:207]\n\t" \
    "ds_read_b128 v[232:235], v253 offset:6144\n\t" \
    "v_mfma_f32_16x16x32_bf16 v[208:211], v[88:91], v[236:239], v[208:211]\n\t" \
    "v_mfma_f32_16x16x32_bf16 v[208:211], v[92:95], v[240:243], v[208:211]\n\t" \
    "v_mfma_f32_16x16x32_bf16 v[200:203], v[244:247], v[236:239], v[200:203]\n\t" \
    "v_mfma_f32_16x16x32_bf16 v[200:203], v[248:251], v[240:243], v[200:203]\n\t" \
    "s_waitcnt lgkmcnt(2)\n\t" \
    "v_mfma_f32_16x16x32_bf16 v[216:219], v[80:83], v[224:227], v[216:219]\n\t" \
    "ds_read_b128 v[236:239], v253 offset:7168\n\t" \
    "s_waitcnt lgkmcnt(2)\n\t" \
    "v_mfma_f32_16x16x32_bf16 v[216:219], v[84:87], v[228:231], v[216:219]\n\t" \
    "ds_read_b128 v[240:243], v253 offset:26624\n\t" \
    "v_mfma_f32_16x16x32_bf16 v[220:223], v[88:91], v[224:227], v[220:223]\n\t" \
    "v_mfma_f32_16x16x32_bf16 v[220:223], v[92:95], v[228:231], v[220:223]\n\t" \
    "v_mfma_f32_16x16x32_bf16 v[212:215], v[244:247], v[224:227], v[212:215]\n\t" \
    "ds_read_b128 v[244:247], v254 offset:61440\n\t" \
    "v_mfma_f32_16x16x32_bf16 v[212:215], v[248:251], v[228:231], v[212:215]\n\t" \
    "ds_read_b128 v[248:251], v254 offset:62464\n\t" \
    "s_waitcnt lgkmcnt(4)\n\t" \
    "v_mfma_f32_16x16x32_bf16 v[204:207], v[104:107], v[232:235], v[204:207]\n\t" \
    "ds_read_b128 v[224:227], v253 offset:27648\n\t" \
    "s_waitcnt lgkmcnt(4)\n\t" \
    "v_mfma_f32_16x16x32_bf16 v[204:207], v[108:111], v[236:239], v[204:207]\n\t" \
    "ds_read_b128 v[228:231], v253 offset:8192\n\t" \
    "v_mfma_f32_16x16x32_bf16 v[208:211], v[112:115], v[232:235], v[208:211]\n\t" \
    "v_mfma_f32_16x16x32_bf16 v[208:211], v[116:119], v[236:239], v[208:211]\n\t" \
    "v_mfma_f32_16x16x32_bf16 v[200:203], v[96:99], v[232:235], v[200:203]\n\t" \
    "v_mfma_f32_16x16x32_bf16 v[200:203], v[100:103], v[236:239], v[200:203]\n\t" \
    "s_waitcnt lgkmcnt(4)\n\t" \
    "v_mfma_f32_16x16x32_bf16 v[216:219], v[104:107], v[240:243], v[216:219]\n\t" \
    "ds_read_b128 v[232:235], v253 offset:9216\n\t" \
    "s_waitcnt lgkmcnt(2)\n\t" \
    "v_mfma_f32_16x16x32_bf16 v[216:219], v[108:111], v[224:227], v[216:219]\n\t" \
    "ds_read_b128 v[236:239], v253 offset:28672\n\t" \
    "v_mfma_f32_16x16x32_bf16 v[220:223], v[112:115], v[240:243], v[220:223]\n\t" \
    "v_mfma_f32_16x16x32_bf16 v[220:223], v[116:119], v[224:227], v[220:223]\n\t" \
    "v_mfma_f32_16x16x32_bf16 v[212:215], v[96:99], v[240:243], v[212:215]\n\t" \
    "v_mfma_f32_16x16x32_bf16 v[212:215], v[100:103], v[224:227], v[212:215]\n\t" \
    "s_waitcnt lgkmcnt(2)\n\t" \
    "v_mfma_f32_16x16x32_bf16 v[204:207], v[120:123], v[228:231], v[204:207]\n\t" \
    "ds_read_b128 v[240:243], v253 offset:29696\n\t" \
    "s_waitcnt lgkmcnt(2)\n\t" \
    "v_mfma_f32_16x16x32_bf16 v[204:207], v[124:127], v[232:235], v[204:207]\n\t" \
    "ds_read_b128 v[224:227], v253 offset:10240\n\t" \
    "v_mfma_f32_16x16x32_bf16 v[208:211], v[128:131], v[228:231], v[208:211]\n\t" \
    "v_mfma_f32_16x16x32_bf16 v[208:211], v[132:135], v[232:235], v[208:211]\n\t" \
    "v_mfma_f32_16x16x32_bf16 v[200:203], v[244:247], v[228:231], v[200:203]\n\t" \
    "v_mfma_f32_16x16x32_bf16 v[200:203], v[248:251], v[232:235], v[200:203]\n\t" \
    "s_waitcnt lgkmcnt(2)\n\t" \
    "v_mfma_f32_16x16x32_bf16 v[216:219], v[120:123], v[236:239], v[216:219]\n\t" \
    "ds_read_b128 v[228:231], v253 offset:11264\n\t" \
    "s_waitcnt lgkmcnt(2)\n\t" \
    "v_mfma_f32_16x16x32_bf16 v[216:219], v[124:127], v[240:243], v[216:219]\n\t" \
    "ds_read_b128 v[232:235], v253 offset:30720\n\t" \
    "v_mfma_f32_16x16x32_bf16 v[220:223], v[128:131], v[236:239], v[220:223]\n\t" \
    "v_mfma_f32_16x16x32_bf16 v[220:223], v[132:135], v[240:243], v[220:223]\n\t" \
    "v_mfma_f32_16x16x32_bf16 v[212:215], v[244:247], v[236:239], v[212:215]\n\t" \
    "ds_read_b128 v[244:247], v254 offset:63488\n\t" \
    "v_mfma_f32_16x16x32_bf16 v[212:215], v[248:251], v[240:243], v[212:215]\n\t" \
    "ds_read_b128 v[248:251], v254 offset:64512\n\t" \
    "ds_read_b128 v[236:239], v253 offset:31744\n\t" \
    "s_waitcnt lgkmcnt(0)\n\t" \
    "s_barrier \n\t" \
    LST7 \
    "v_mfma_f32_16x16x32_bf16 v[204:207], v[144:147], v[224:227], v[204:207]\n\t" \
    "v_mfma_f32_16x16x32_bf16 v[204:207], v[148:151], v[228:231], v[204:207]\n\t" \
    "ds_read_b128 v[240:243], v253 offset:12288\n\t" \
    "v_mfma_f32_16x16x32_bf16 v[208:211], v[152:155], v[224:227], v[208:211]\n\t" \
    "v_mfma_f32_16x16x32_bf16 v[208:211], v[156:159], v[228:231], v[208:211]\n\t" \
    "v_mfma_f32_16x16x32_bf16 v[200:203], v[136:139], v[224:227], v[200:203]\n\t" \
    "v_mfma_f32_16x16x32_bf16 v[200:203], v[140:143], v[228:231], v[200:203]\n\t" \
    "v_mfma_f32_16x16x32_bf16 v[216:219], v[144:147], v[232:235], v[216:219]\n\t" \
    "ds_read_b128 v[224:227], v253 offset:13312\n\t" \
    "v_mfma_f32_16x16x32_bf16 v[216:219], v[148:151], v[236:239], v[216:219]\n\t" \
    "ds_read_b128 v[228:231], v253 offset:32768\n\t" \
    "v_mfma_f32_16x16x32_bf16 v[220:223], v[152:155], v[232:235], v[220:223]\n\t" \
    "v_mfma_f32_16x16x32_bf16 v[220:223], v[156:159], v[236:239], v[220:223]\n\t" \
    "v_mfma_f32_16x16x32_bf16 v[212:215], v[136:139], v[232:235], v[212:215]\n\t" \
    "v_mfma_f32_16x16x32_bf16 v[212:215], v[140:143], v[236:239], v[212:215]\n\t" \
    "s_waitcnt lgkmcnt(2)\n\t" \
    "v_mfma_f32_16x16x32_bf16 v[204:207], v[160:163], v[240:243], v[204:207]\n\t" \
    "ds_read_b128 v[232:235], v253 offset:33792\n\t" \
    "s_and_b32 s44, s40, 3\n\t" \
    "s_waitcnt lgkmcnt(2)\n\t" \
    "v_mfma_f32_16x16x32_bf16 v[204:207], v[164:167], v[224:227], v[204:207]\n\t" \
    "ds_read_b128 v[236:239], v253 offset:16384\n\t" \
    "s_lshl_b32 s44, s44, 17\n\t" \
    "v_mfma_f32_16x16x32_bf16 v[208:211], v[168:171], v[240:243], v[208:211]\n\t" \
    "s_add_u32 s44, s44, %[mb]\n\t" \
    "v_mfma_f32_16x16x32_bf16 v[208:211], v[172:175], v[224:227], v[208:211]\n\t" \
    "s_add_u32 s46, s44, 0x4000\n\t" \
    "v_mfma_f32_16x16x32_bf16 v[200:203], v[244:247], v[240:243], v[200:203]\n\t" \
    "s_add_u32 s47, s44, 0x4400\n\t" \
    "v_mfma_f32_16x16x32_bf16 v[200:203], v[248:251], v[224:227], v[200:203]\n\t" \
    "s_waitcnt lgkmcnt(2)\n\t" \
    "v_mfma_f32_16x16x32_bf16 v[216:219], v[160:163], v[228:231], v[216:219]\n\t" \
    "ds_read_b128 v[240:243], v253 offset:17408\n\t" \
    "s_waitcnt lgkmcnt(2)\n\t" \
    "v_mfma_f32_16x16x32_bf16 v[216:219], v[164:167], v[232:235], v[216:219]\n\t" \
    "ds_read_b128 v[224:227], v253 offset:36864\n\t" \
    "v_mfma_f32_16x16x32_bf16 v[220:223], v[168:171], v[228:231], v[220:223]\n\t" \
    "v_mfma_f32_16x16x32_bf16 v[220:223], v[172:175], v[232:235], v[220:223]\n\t" \
    "v_mfma_f32_16x16x32_bf16 v[212:215], v[244:247], v[228:231], v[212:215]\n\t" \
    "ds_read_b128 v[244:247], v254 offset:55296\n\t" \
    "v_mfma_f32_16x16x32_bf16 v[212:215], v[248:251], v[232:235], v[212:215]\n\t" \
    "ds_read_b128 v[248:251], v254 offset:56320\n\t" \
    "s_waitcnt lgkmcnt(4)\n\t" \
    "v_mfma_f32_16x16x32_bf16 v[204:207], v[0:3], v[236:239], v[204:207]\n\t" \
    "ds_read_b128 v[228:231], v253 offset:37888\n\t" \
    "s_waitcnt lgkmcnt(4)\n\t" \
    "v_mfma_f32_16x16x32_bf16 v[204:207], v[4:7], v[240:243], v[204:207]\n\t" \
    "ds_read_b128 v[232:235], v253 offset:14336\n\t" \
    "v_mfma_f32_16x16x32_bf16 v[208:211], v[8:11], v[236:239], v[208:211]\n\t" \
    "v_mfma_f32_16x16x32_bf16 v[208:211], v[12:15], v[240:243], v[208:211]\n\t" \
    "s_waitcnt lgkmcnt(3)\n\t" \
    "v_mfma_f32_16x16x32_bf16 v[200:203], v[244:247], v[236:239], v[200:203]\n\t" \
    "s_waitcnt lgkmcnt(2)\n\t" \
    "v_mfma_f32_16x16x32_bf16 v[200:203], v[248:251], v[240:243], v[200:203]\n\t" \
    "v_mfma_f32_16x16x32_bf16 v[216:219], v[0:3], v[224:227], v[216:219]\n\t" \
    "ds_read_b128 v[236:239], v253 offset:15360\n\t" \
    "s_waitcnt lgkmcnt(2)\n\t" \
    "v_mfma_f32_16x16x32_bf16 v[216:219], v[4:7], v[228:231], v[216:219]\n\t" \
    "ds_read_b128 v[240:243], v253 offset:18432\n\t" \
    "v_mfma_f32_16x16x32_bf16 v[220:223], v[8:11], v[224:227], v[220:223]\n\t" \
    "v_mfma_f32_16x16x32_bf16 v[220:223], v[12:15], v[228:231], v[220:223]\n\t" \
    "v_mfma_f32_16x16x32_bf16 v[212:215], v[244:247], v[224:227], v[212:215]\n\t" \
    "v_mfma_f32_16x16x32_bf16 v[212:215], v[248:251], v[228:231], v[212:215]\n\t" \
    "s_waitcnt lgkmcnt(2)\n\t" \
    "v_mfma_f32_16x16x32_bf16 v[204:207], v[184:187], v[232:235], v[204:207]\n\t" \
    "ds_read_b128 v[224:227], v253 offset:19456\n\t" \
    "v_mov_b64 v[244:245], 0\n\t" \
    "s_waitcnt lgkmcnt(2)\n\t" \
    "v_mfma_f32_16x16x32_bf16 v[204:207], v[188:191], v[236:239], v[204:207]\n\t" \
    "ds_read_b128 v[228:231], v253 offset:34816\n\t" \
    "v_mov_b64 v[246:247], 0\n\t" \
    "v_mfma_f32_16x16x32_bf16 v[208:211], v[192:195], v[232:235], v[208:211]\n\t" \
    "v_mov_b64 v[248:249], 0\n\t" \
    "v_mfma_f32_16x16x32_bf16 v[208:211], v[196:199], v[236:239], v[208:211]\n\t" \
    "v_mov_b64 v[250:251], 0\n\t" \
    "v_mfma_f32_16x16x32_bf16 v[200:203], v[176:179], v[232:235], v[200:203]\n\t" \
    "v_mfma_f32_16x16x32_bf16 v[200:203], v[180:183], v[236:239], v[200:203]\n\t" \
    "s_waitcnt lgkmcnt(2)\n\t" \
    "v_mfma_f32_16x16x32_bf16 v[204:207], v[24:27], v[240:243], v[204:207]\n\t" \
    "ds_read_b128 v[232:235], v253 offset:35840\n\t" \
    "s_waitcnt lgkmcnt(2)\n\t" \
    "v_mfma_f32_16x16x32_bf16 v[204:207], v[28:31], v[224:227], v[204:207]\n\t" \
    "ds_read_b128 v[236:239], v253 offset:38912\n\t" \
    "v_mfma_f32_16x16x32_bf16 v[208:211], v[32:35], v[240:243], v[208:211]\n\t" \
    "v_mfma_f32_16x16x32_bf16 v[208:211], v[36:39], v[224:227], v[208:211]\n\t" \
    "v_mfma_f32_16x16x32_bf16 v[200:203], v[16:19], v[240:243], v[200:203]\n\t" \
    "v_mfma_f32_16x16x32_bf16 v[200:203], v[20:23], v[224:227], v[200:203]\n\t" \
    "ds_read_b128 v[240:243], v253 offset:39936\n\t" \
    "s_waitcnt lgkmcnt(3)\n\t" \
    "v_mfma_f32_16x16x32_bf16 v[216:219], v[184:187], v[228:231], v[216:219]\n\t" \
    "s_waitcnt lgkmcnt(2)\n\t" \
    "v_mfma_f32_16x16x32_bf16 v[216:219], v[188:191], v[232:235], v[216:219]\n\t" \
    "v_cvt_pk_bf16_f32 v204, v204, v208\n\t" \
    "v_cvt_pk_bf16_f32 v205, v205, v209\n\t" \
    "s_waitcnt lgkmcnt(1)\n\t" \
    "v_mfma_f32_16x16x32_bf16 v[216:219], v[24:27], v[236:239], v[216:219]\n\t" \
    "v_cvt_pk_bf16_f32 v206, v206, v210\n\t" \
    "v_cvt_pk_bf16_f32 v207, v207, v211\n\t" \
    "s_waitcnt lgkmcnt(0)\n\t" \
    "v_mfma_f32_16x16x32_bf16 v[216:219], v[28:31], v[240:243], v[216:219]\n\t" \
    "ds_write_b128 v254, v[204:207] offset:5120\n\t" \
    "v_cvt_pk_bf16_f32 v200, v200, v201\n\t" \
    "v_mfma_f32_16x16x32_bf16 v[220:223], v[192:195], v[228:231], v[220:223]\n\t" \
    "v_cvt_pk_bf16_f32 v201, v202, v203\n\t" \
    "ds_write_b64 v254, v[200:201] offset:1032\n\t" \
    "v_mfma_f32_16x16x32_bf16 v[220:223], v[196:199], v[232:235], v[220:223]\n\t" \
    "ds_read_b128 v[200:203], v254 offset:55296\n\t" \
    "ds_read_b128 v[204:207], v254 offset:56320\n\t" \
    "v_mfma_f32_16x16x32_bf16 v[220:223], v[32:35], v[236:239], v[220:223]\n\t" \
    "v_mov_b64 v[208:209], 0\n\t" \
    "v_mov_b64 v[210:211], 0\n\t" \
    "v_mfma_f32_16x16x32_bf16 v[220:223], v[36:39], v[240:243], v[220:223]\n\t" \
    "v_mfma_f32_16x16x32_bf16 v[212:215], v[176:179], v[228:231], v[212:215]\n\t" \
    "v_mfma_f32_16x16x32_bf16 v[212:215], v[180:183], v[232:235], v[212:215]\n\t" \
    "v_mfma_f32_16x16x32_bf16 v[212:215], v[16:19], v[236:239], v[212:215]\n\t" \
    "v_cvt_pk_bf16_f32 v216, v216, v220\n\t" \
    "v_cvt_pk_bf16_f32 v217, v217, v221\n\t" \
    "v_mfma_f32_16x16x32_bf16 v[212:215], v[20:23], v[240:243], v[212:215]\n\t" \
    "v_cvt_pk_bf16_f32 v218, v218, v222\n\t" \
    "v_cvt_pk_bf16_f32 v219, v219, v223\n\t" \
    "ds_write_b128 v254, v[216:219] offset:11264\n\t" \
    "ds_read_b128 v[216:219], v254 offset:57344\n\t" \
    "ds_read_b128 v[220:223], v254 offset:58368\n\t" \
    "s_nop 2\n\t" \
    "v_cvt_pk_bf16_f32 v212, v212, v213\n\t" \
    "v_cvt_pk_bf16_f32 v213, v214, v215\n\t" \
    "ds_write_b64 v254, v[212:213] offset:7176\n\t" \
    LST6 \
    "s_sleep 8\n\t" \
    "buffer_load_dwordx4 v[224:227], v253, s[60:63], s46 offen sc1\n\t" \
    "buffer_load_dwordx4 v[228:231], v253, s[60:63], s47 offen sc1\n\t" \
    "s_waitcnt lgkmcnt(0)\n\t" \
    LFLUSH212 \
    "v_bfe_i32 v212, v253, 5, 1\n\t" \
    "v_and_b32 v212, 0xaaaa, v212\n\t" \
    "v_or_b32 v212, 0x4444, v212\n\t" \
    "v_and_b32 v213, 0xffff, v255\n\t" \
    "v_lshrrev_b32 v214, 16, v255\n\t" \
    ".Ls34_%=:\n\t" \
    "s_waitcnt vmcnt(1)\n\t" \
    "v_max_u32 v215, v224, v225\n\t" \
    "v_max3_u32 v215, v226, v227, v215\n\t" \
    "s_waitcnt vmcnt(0)\n\t" \
    "v_max3_u32 v215, v228, v229, v215\n\t" \
    "v_max3_u32 v215, v230, v231, v215\n\t" \
    "v_cmp_eq_u32 vcc, -1, v215\n\t" \
    "s_cbranch_vccz .Ls35_%=\n\t" \
    "v_max_u32 v215, v224, v225\n\t" \
    "v_max3_u32 v215, v226, v227, v215\n\t" \
    "v_cmp_eq_u32 vcc, -1, v215\n\t" \
    "s_cbranch_vccz .Ls36_%=\n\t" \
    "buffer_load_dwordx4 v[224:227], v253, s[60:63], s46 offen sc1\n\t" \
    ".Ls36_%=:\n\t" \
    "v_max_u32 v215, v228, v229\n\t" \
    "v_max3_u32 v215, v230, v231, v215\n\t" \
    "v_cmp_eq_u32 vcc, -1, v215\n\t" \
    "s_cbranch_vccz .Ls37_%=\n\t" \
    "buffer_load_dwordx4 v[228:231], v253, s[60:63], s47 offen sc1\n\t" \
    ".Ls37_%=:\n\t" \
    "s_cmp_eq_u32 s41, 0\n\t" \
    "s_cbranch_scc1 .Ls35_%=\n\t" \
    "s_sub_u32 s41, s41, 1\n\t" \
    "s_waitcnt vmcnt(0)\n\t" \
    "s_branch .Ls34_%=\n\t" \
    ".Ls35_%=:\n\t" \
    LST2 \
    "ds_write_b128 v253, v[224:227] offset:49152\n\t" \
    "ds_write_b128 v253, v[228:231] offset:50176\n\t" \
    "v_smfmac_f32_16x16x64_bf16 v[244:247], v[224:227], v[0:7], v212\n\t" \
    "v_smfmac_f32_16x16x64_bf16 v[248:251], v[224:227], v[8:15], v212\n\t" \
    "v_smfmac_f32_16x16x64_bf16 v[208:211], v[224:227], v[200:207], v212\n\t" \
    "s_waitcnt lgkmcnt(0)\n\t" \
    "s_barrier \n\t" \
    LST3 \
    "ds_read_b128 v[232:235], v253 offset:40960\n\t" \
    "ds_read_b128 v[236:239], v253 offset:41984\n\t" \
    "ds_read_b128 v[240:243], v253 offset:43008\n\t" \
    "ds_read_b128 v[224:227], v253 offset:44032\n\t" \
    "ds_read_b128 v[200:203], v254 offset:59392\n\t" \
    "ds_read_b128 v[204:207], v254 offset:60416\n\t" \
    "v_smfmac_f32_16x16x64_bf16 v[208:211], v[228:231], v[16:23], v212\n\t" \
    "v_smfmac_f32_16x16x64_bf16 v[244:247], v[228:231], v[24:31], v212\n\t" \
    "v_smfmac_f32_16x16x64_bf16 v[248:251], v[228:231], v[32:39], v212\n\t" \
    "ds_read_b128 v[228:231], v253 offset:45056\n\t" \
    "s_waitcnt lgkmcnt(6)\n\t" \
    "v_smfmac_f32_16x16x64_bf16 v[208:211], v[232:235], v[216:223], v212\n\t" \
    "ds_read_b128 v[216:219], v254 offset:61440\n\t" \
    "ds_read_b128 v[220:223], v254 offset:62464\n\t" \
    "v_smfmac_f32_16x16x64_bf16 v[244:247], v[232:235], v[40:47], v212\n\t" \
    "v_smfmac_f32_16x16x64_bf16 v[248:251], v[232:235], v[48:55], v212\n\t" \
    "ds_read_b128 v[232:235], v253 offset:46080\n\t" \
    "s_waitcnt lgkmcnt(8)\n\t" \
    "v_smfmac_f32_16x16x64_bf16 v[208:211], v[236:239], v[56:63], v212\n\t" \
    "v_smfmac_f32_16x16x64_bf16 v[244:247], v[236:239], v[64:71], v212\n\t" \
    "v_smfmac_f32_16x16x64_bf16 v[248:251], v[236:239], v[72:79], v212\n\t" \
    "ds_read_b128 v[236:239], v253 offset:47104\n\t" \
    "s_waitcnt lgkmcnt(5)\n\t" \
    "v_smfmac_f32_16x16x64_bf16 v[208:211], v[240:243], v[200:207], v212\n\t" \
    "ds_read_b128 v[200:203], v254 offset:63488\n\t" \
    "ds_read_b128 v[204:207], v254 offset:64512\n\t" \
    "v_smfmac_f32_16x16x64_bf16 v[244:247], v[240:243], v[80:87], v212\n\t" \
    "v_smfmac_f32_16x16x64_bf16 v[248:251], v[240:243], v[88:95], v212\n\t" \
    "ds_read_b128 v[240:243], v253 offset:48128\n\t" \
    "v_smfmac_f32_16x16x64_bf16 v[208:211], v[224:227], v[96:103], v212\n\t" \
    "v_smfmac_f32_16x16x64_bf16 v[244:247], v[224:227], v[104:111], v212\n\t" \
    "v_smfmac_f32_16x16x64_bf16 v[248:251], v[224:227], v[112:119], v212\n\t" \
    "s_waitcnt lgkmcnt(5)\n\t" \
    "v_smfmac_f32_16x16x64_bf16 v[208:211], v[228:231], v[216:223], v212\n\t" \
    "s_waitcnt lgkmcnt(4)\n\t" \
    "v_smfmac_f32_16x16x64_bf16 v[208:211], v[232:235], v[136:143], v212\n\t" \
    "s_waitcnt lgkmcnt(1)\n\t" \
    "v_smfmac_f32_16x16x64_bf16 v[208:211], v[236:239], v[200:207], v212\n\t" \
    "s_waitcnt lgkmcnt(0)\n\t" \
    "v_smfmac_f32_16x16x64_bf16 v[208:211], v[240:243], v[176:183], v212\n\t" \
    "v_smfmac_f32_16x16x64_bf16 v[244:247], v[228:231], v[120:127], v212\n\t" \
    "v_smfmac_f32_16x16x64_bf16 v[244:247], v[232:235], v[144:151], v212\n\t" \
    "v_smfmac_f32_16x16x64_bf16 v[244:247], v[236:239], v[160:167], v212\n\t" \
    "v_add_f32 v208, v208, v210\n\t" \
    "v_add_f32 v209, v209, v211\n\t" \
    "v_cvt_pk_bf16_f32 v208, v208, v209\n\t" \
    "v_smfmac_f32_16x16x64_bf16 v[244:247], v[240:243], v[184:191], v212\n\t" \
    "ds_write_b16 v213, v208 offset:0\n\t" \
    "ds_write_b16_d16_hi v213, v208 offset:16\n\t" \
    "v_smfmac_f32_16x16x64_bf16 v[248:251], v[228:231], v[128:135], v212\n\t" \
    "v_smfmac_f32_16x16x64_bf16 v[248:251], v[232:235], v[152:159], v212\n\t" \
    "v_smfmac_f32_16x16x64_bf16 v[248:251], v[236:239], v[168:175], v212\n\t" \
    "v_add_f32 v244, v244, v246\n\t" \
    "v_add_f32 v245, v245, v247\n\t" \
    "v_smfmac_f32_16x16x64_bf16 v[248:251], v[240:243], v[192:199], v212\n\t" \
    "s_nop 7\n\t" \
    "v_add_f32 v248, v248, v250\n\t" \
    "v_add_f32 v249, v249, v251\n\t" \
    "v_cvt_pk_bf16_f32 v244, v244, v248\n\t" \
    "v_cvt_pk_bf16_f32 v245, v245, v249\n\t" \
    "ds_write2_b32 v214, v244, v245 offset1:4\n\t" \
    "s_sleep 3\n\t" \
    "ds_read_b128 v[208:211], v254 offset:24576\n\t" \
    "ds_read_b128 v[224:227], v253 offset:0\n\t" \
    "ds_read_b128 v[228:231], v253 offset:20480\n\t" \
    "ds_read_b128 v[212:215], v254 offset:25600\n\t" \
    "ds_read_b128 v[232:235], v253 offset:1024\n\t" \
    "ds_read_b128 v[236:239], v253 offset:21504\n\t" \
    "ds_read_b128 v[216:219], v254 offset:26624\n\t" \
    "ds_read_b128 v[240:243], v253 offset:2048\n\t" \
    "ds_read_b128 v[244:247], v253 offset:22528\n\t" \
    "ds_read_b128 v[220:223], v254 offset:27648\n\t" \
    "ds_read_b128 v[248:251], v253 offset:3072\n\t" \
    "s_waitcnt lgkmcnt(0)\n\t" \
    "s_barrier \n\t" \
    LST4 \
    "s_add_u32 s40, s40, 1\n\t" \
    "s_cmp_lg_u32 s40, 64\n\t" \
    "s_cbranch_scc1 .Ls33_%=\n\t" \
    "s_branch .Lend%=\n\t" \
    ".Ls2_%=:\n\t" \
    "s_add_u32 m0, %[wm], 0x18000\n\t" \
    "s_mov_b32 s44, 0x0\n\t" \
    "buffer_load_dwordx4 v240, %[wr], s44 offen offset:0 nt lds\n\t" \
    "buffer_load_dwordx4 v241, %[wr], s44 offen offset:1024 nt lds\n\t" \
    "buffer_load_dwordx4 v242, %[wr], s44 offen offset:2048 nt lds\n\t" \
    "s_add_u32 m0, %[wm], 0x1e000\n\t" \
    "s_mov_b32 s44, 0x60000\n\t" \
    "buffer_load_dwordx4 v240, %[wr], s44 offen offset:0 nt lds\n\t" \
    "buffer_load_dwordx4 v241, %[wr], s44 offen offset:1024 nt lds\n\t" \
    "buffer_load_dwordx4 v242, %[wr], s44 offen offset:2048 nt lds\n\t" \
    "s_add_u32 m0, %[wm], 0x0\n\t" \
    "s_mov_b32 s44, 0xc0000\n\t" \
    "buffer_load_dwordx4 v240, %[wr], s44 offen offset:0 nt lds\n\t" \
    "buffer_load_dwordx4 v241, %[wr], s44 offen offset:1024 nt lds\n\t" \
    "buffer_load_dwordx4 v242, %[wr], s44 offen offset:2048 nt lds\n\t" \
    "s_add_u32 m0, %[wm], 0x6000\n\t" \
    "s_mov_b32 s44, 0x120000\n\t" \
    "buffer_load_dwordx4 v240, %[wr], s44 offen offset:0 nt lds\n\t" \
    "buffer_load_dwordx4 v241, %[wr], s44 offen offset:1024 nt lds\n\t" \
    "buffer_load_dwordx4 v242, %[wr], s44 offen offset:2048 nt lds\n\t" \
    "s_add_u32 m0, %[wm], 0xc000\n\t" \
    "s_mov_b32 s44, 0x180000\n\t" \
    "buffer_load_dwordx4 v240, %[wr], s44 offen offset:0 nt lds\n\t" \
    "buffer_load_dwordx4 v241, %[wr], s44 offen offset:1024 nt lds\n\t" \
    "buffer_load_dwordx4 v242, %[wr], s44 offen offset:2048 nt lds\n\t" \
    "v_add_u32 v244, 0x18000, v243\n\t" \
    "s_waitcnt vmcnt(12) lgkmcnt(0)\n\t" \
    "s_barrier \n\t" \
    "s_add_u32 m0, %[wm], 0x12000\n\t" \
    "s_mov_b32 s44, 0x1e0000\n\t" \
    "buffer_load_dwordx4 v240, %[wr], s44 offen offset:0 nt lds\n\t" \
    "buffer_load_dwordx4 v241, %[wr], s44 offen offset:1024 nt lds\n\t" \
    "buffer_load_dwordx4 v242, %[wr], s44 offen offset:2048 nt lds\n\t" \
    "ds_read_b64_tr_b16 v[208:209], v244 offset:0\n\t" \
    "ds_read_b64_tr_b16 v[210:211], v244 offset:1536\n\t" \
    "ds_read_b64_tr_b16 v[40:41], v244 offset:32\n\t" \
    "ds_read_b64_tr_b16 v[42:43], v244 offset:1568\n\t" \
    "ds_read_b64_tr_b16 v[48:49], v244 offset:64\n\t" \
    "ds_read_b64_tr_b16 v[50:51], v244 offset:1600\n\t" \
    "v_add_u32 v244, 0x1e000, v243\n\t" \
    "s_waitcnt vmcnt(12) lgkmcnt(0)\n\t" \
    "s_barrier \n\t" \
    "s_add_u32 m0, %[wm], 0x18000\n\t" \
    "s_mov_b32 s44, 0x240000\n\t" \
    "buffer_load_dwordx4 v240, %[wr], s44 offen offset:0 nt lds\n\t" \
    "buffer_load_dwordx4 v241, %[wr], s44 offen offset:1024 nt lds\n\t" \
    "buffer_load_dwordx4 v242, %[wr], s44 offen offset:2048 nt lds\n\t" \
    "ds_read_b64_tr_b16 v[212:213], v244 offset:0\n\t" \
    "ds_read_b64_tr_b16 v[214:215], v244 offset:1536\n\t" \
    "ds_read_b64_tr_b16 v[44:45], v244 offset:32\n\t" \
    "ds_read_b64_tr_b16 v[46:47], v244 offset:1568\n\t" \
    "ds_read_b64_tr_b16 v[52:53], v244 offset:64\n\t" \
    "ds_read_b64_tr_b16 v[54:55], v244 offset:1600\n\t" \
    "v_add_u32 v244, 0x0, v243\n\t" \
    "s_waitcnt vmcnt(12) lgkmcnt(0)\n\t" \
    "s_barrier \n\t" \
    "s_add_u32 m0, %[wm], 0x1e000\n\t" \
    "s_mov_b32 s44, 0x2a0000\n\t" \
    "buffer_load_dwordx4 v240, %[wr], s44 offen offset:0 nt lds\n\t" \
    "buffer_load_dwordx4 v241, %[wr], s44 offen offset:1024 nt lds\n\t" \
    "buffer_load_dwordx4 v242, %[wr], s44 offen offset:2048 nt lds\n\t" \
    "ds_read_b64_tr_b16 v[56:57], v244 offset:0\n\t" \
    "ds_read_b64_tr_b16 v[58:59], v244 offset:1536\n\t" \
    "ds_read_b64_tr_b16 v[64:65], v244 offset:32\n\t" \
    "ds_read_b64_tr_b16 v[66:67], v244 offset:1568\n\t" \
    "ds_read_b64_tr_b16 v[72:73], v244 offset:64\n\t" \
    "ds_read_b64_tr_b16 v[74:75], v244 offset:1600\n\t" \
    "v_add_u32 v244, 0x6000, v243\n\t" \
    "s_waitcnt vmcnt(12) lgkmcnt(0)\n\t" \
    "s_barrier \n\t" \
    "s_add_u32 m0, %[wm], 0x0\n\t" \
    "s_mov_b32 s44, 0x300000\n\t" \
    "buffer_load_dwordx4 v240, %[wr], s44 offen offset:0 nt lds\n\t" \
    "buffer_load_dwordx4 v241, %[wr], s44 offen offset:1024 nt lds\n\t" \
    "buffer_load_dwordx4 v242, %[wr], s44 offen offset:2048 nt lds\n\t" \
    "ds_read_b64_tr_b16 v[60:61], v244 offset:0\n\t" \
    "ds_read_b64_tr_b16 v[62:63], v244 offset:1536\n\t" \
    "ds_read_b64_tr_b16 v[68:69], v244 offset:32\n\t" \
    "ds_read_b64_tr_b16 v[70:71], v244 offset:1568\n\t" \
    "ds_read_b64_tr_b16 v[76:77], v244 offset:64\n\t" \
    "ds_read_b64_tr_b16 v[78:79], v244 offset:1600\n\t" \
    "v_add_u32 v244, 0xc000, v243\n\t" \
    "s_waitcnt vmcnt(12) lgkmcnt(0)\n\t" \
    "s_barrier \n\t" \
    "s_add_u32 m0, %[wm], 0x6000\n\t" \
    "s_mov_b32 s44, 0x360000\n\t" \
    "buffer_load_dwordx4 v240, %[wr], s44 offen offset:0 nt lds\n\t" \
    "buffer_load_dwordx4 v241, %[wr], s44 offen offset:1024 nt lds\n\t" \
    "buffer_load_dwordx4 v242, %[wr], s44 offen offset:2048 nt lds\n\t" \
    "ds_read_b64_tr_b16 v[216:217], v244 offset:0\n\t" \
    "ds_read_b64_tr_b16 v[218:219], v244 offset:1536\n\t" \
    "ds_read_b64_tr_b16 v[80:81], v244 offset:32\n\t" \
    "ds_read_b64_tr_b16 v[82:83], v244 offset:1568\n\t" \
    "ds_read_b64_tr_b16 v[88:89], v244 offset:64\n\t" \
    "ds_read_b64_tr_b16 v[90:91], v244 offset:1600\n\t" \
    "v_add_u32 v244, 0x12000, v243\n\t" \
    "s_waitcnt vmcnt(12) lgkmcnt(0)\n\t" \
    "s_barrier \n\t" \
    "s_add_u32 m0, %[wm], 0xc000\n\t" \
    "s_mov_b32 s44, 0x3c0000\n\t" \
    "buffer_load_dwordx4 v240, %[wr], s44 offen offset:0 nt lds\n\t" \
    "buffer_load_dwordx4 v241, %[wr], s44 offen offset:1024 nt lds\n\t" \
    "buffer_load_dwordx4 v242, %[wr], s44 offen offset:2048 nt lds\n\t" \
    "ds_read_b64_tr_b16 v[220:221], v244 offset:0\n\t" \
    "ds_read_b64_tr_b16 v[222:223], v244 offset:1536\n\t" \
    "ds_read_b64_tr_b16 v[84:85], v244 offset:32\n\t" \
    "ds_read_b64_tr_b16 v[86:87], v244 offset:1568\n\t" \
    "ds_read_b64_tr_b16 v[92:93], v244 offset:64\n\t" \
    "ds_read_b64_tr_b16 v[94:95], v244 offset:1600\n\t" \
    "v_add_u32 v244, 0x18000, v243\n\t" \
    "s_waitcnt vmcnt(12) lgkmcnt(0)\n\t" \
    "s_barrier \n\t" \
    "v_mov_b32 v246, 0\n\t" \
    "buffer_load_dword v246, v246, s[60:63], %[mo] offen sc1\n\t" \
    "s_add_u32 m0, %[wm], 0x12000\n\t" \
    "s_mov_b32 s44, 0x420000\n\t" \
    "buffer_load_dwordx4 v240, %[wr], s44 offen offset:0 nt lds\n\t" \
    "buffer_load_dwordx4 v241, %[wr], s44 offen offset:1024 nt lds\n\t" \
    "buffer_load_dwordx4 v242, %[wr], s44 offen offset:2048 nt lds\n\t" \
    "ds_read_b64_tr_b16 v[96:97], v244 offset:0\n\t" \
    "ds_read_b64_tr_b16 v[98:99], v244 offset:1536\n\t" \
    "ds_read_b64_tr_b16 v[104:105], v244 offset:32\n\t" \
    "ds_read_b64_tr_b16 v[106:107], v244 offset:1568\n\t" \
    "ds_read_b64_tr_b16 v[112:113], v244 offset:64\n\t" \
    "ds_read_b64_tr_b16 v[114:115], v244 offset:1600\n\t" \
    "v_add_u32 v244, 0x1e000, v243\n\t" \
    "s_waitcnt vmcnt(12) lgkmcnt(0)\n\t" \
    "s_barrier \n\t" \
    "s_add_u32 m0, %[wm], 0x18000\n\t" \
    "s_mov_b32 s44, 0x480000\n\t" \
    "buffer_load_dwordx4 v240, %[wr], s44 offen offset:0 nt lds\n\t" \
    "buffer_load_dwordx4 v241, %[wr], s44 offen offset:1024 nt lds\n\t" \
    "buffer_load_dwordx4 v242, %[wr], s44 offen offset:2048 nt lds\n\t" \
    "ds_read_b64_tr_b16 v[100:101], v244 offset:0\n\t" \
    "ds_read_b64_tr_b16 v[102:103], v244 offset:1536\n\t" \
    "ds_read_b64_tr_b16 v[108:109], v244 offset:32\n\t" \
    "ds_read_b64_tr_b16 v[110:111], v244 offset:1568\n\t" \
    "ds_read_b64_tr_b16 v[116:117], v244 offset:64\n\t" \
    "ds_read_b64_tr_b16 v[118:119], v244 offset:1600\n\t" \
    "v_add_u32 v244, 0x0, v243\n\t" \
    "s_waitcnt vmcnt(12) lgkmcnt(0)\n\t" \
    "s_barrier \n\t" \
    "s_add_u32 m0, %[wm], 0x1e000\n\t" \
    "s_mov_b32 s44, 0x4e0000\n\t" \
    "buffer_load_dwordx4 v240, %[wr], s44 offen offset:0 nt lds\n\t" \
    "buffer_load_dwordx4 v241, %[wr], s44 offen offset:1024 nt lds\n\t" \
    "buffer_load_dwordx4 v242, %[wr], s44 offen offset:2048 nt lds\n\t" \
    "ds_read_b64_tr_b16 v[200:201], v244 offset:0\n\t" \
    "ds_read_b64_tr_b16 v[202:203], v244 offset:1536\n\t" \
    "ds_read_b64_tr_b16 v[0:1], v244 offset:32\n\t" \
    "ds_read_b64_tr_b16 v[2:3], v244 offset:1568\n\t" \
    "ds_read_b64_tr_b16 v[8:9], v244 offset:64\n\t" \
    "ds_read_b64_tr_b16 v[10:11], v244 offset:1600\n\t" \
    "v_add_u32 v244, 0x6000, v243\n\t" \
    "s_waitcnt vmcnt(12) lgkmcnt(0)\n\t" \
    "s_barrier \n\t" \
    "s_add_u32 m0, %[wm], 0x0\n\t" \
    "s_mov_b32 s44, 0x540000\n\t" \
    "buffer_load_dwordx4 v240, %[wr], s44 offen offset:0 nt lds\n\t" \
    "buffer_load_dwordx4 v241, %[wr], s44 offen offset:1024 nt lds\n\t" \
    "buffer_load_dwordx4 v242, %[wr], s44 offen offset:2048 nt lds\n\t" \
    "ds_read_b64_tr_b16 v[204:205], v244 offset:0\n\t" \
    "ds_read_b64_tr_b16 v[206:207], v244 offset:1536\n\t" \
    "ds_read_b64_tr_b16 v[4:5], v244 offset:32\n\t" \
    "ds_read_b64_tr_b16 v[6:7], v244 offset:1568\n\t" \
    "ds_read_b64_tr_b16 v[12:13], v244 offset:64\n\t" \
    "ds_read_b64_tr_b16 v[14:15], v244 offset:1600\n\t" \
    "v_add_u32 v244, 0xc000, v243\n\t" \
    "s_waitcnt vmcnt(12) lgkmcnt(0)\n\t" \
    "s_barrier \n\t" \
    "s_add_u32 m0, %[wm], 0x6000\n\t" \
    "s_mov_b32 s44, 0x5a0000\n\t" \
    "buffer_load_dwordx4 v240, %[wr], s44 offen offset:0 nt lds\n\t" \
    "buffer_load_dwordx4 v241, %[wr], s44 offen offset:1024 nt lds\n\t" \
    "buffer_load_dwordx4 v242, %[wr], s44 offen offset:2048 nt lds\n\t" \
    "ds_read_b64_tr_b16 v[224:225], v244 offset:0\n\t" \
    "ds_read_b64_tr_b16 v[226:227], v244 offset:1536\n\t" \
    "ds_read_b64_tr_b16 v[120:121], v244 offset:32\n\t" \
    "ds_read_b64_tr_b16 v[122:123], v244 offset:1568\n\t" \
    "ds_read_b64_tr_b16 v[128:129], v244 offset:64\n\t" \
    "ds_read_b64_tr_b16 v[130:131], v244 offset:1600\n\t" \
    "v_add_u32 v244, 0x12000, v243\n\t" \
    "s_waitcnt vmcnt(12) lgkmcnt(0)\n\t" \
    "s_barrier \n\t" \
    ".Ls21_%=:\n\t" \
    "v_readfirstlane_b32 s43, v246\n\t" \
    "s_nop 0\n\t" \
    "s_cmp_lg_u32 s43, -1\n\t" \
    "s_cbranch_scc1 .Ls22_%=\n\t" \
    "s_cmp_eq_u32 s41, 0\n\t" \
    "s_cbranch_scc1 .Ls22_%=\n\t" \
    "s_sub_u32 s41, s41, 1\n\t" \
    "s_sleep 4\n\t" \
    "v_mov_b32 v246, 0\n\t" \
    "buffer_load_dword v246, v246, s[60:63], %[mo] offen sc1\n\t" \
    "s_waitcnt vmcnt(0)\n\t" \
    "s_branch .Ls21_%=\n\t" \
    ".Ls22_%=:\n\t" \
    "v_mov_b32 v246, 0x8000\n\t" \
    "s_mov_b64 exec, 1\n\t" \
    "buffer_store_dword v246, v246, s[60:63], %[mo] offen sc1\n\t" \
    "s_mov_b64 exec, -1\n\t" \
    "s_and_b32 s43, s43, 0x2000\n\t" \
    "s_xor_b32 s43, s43, %[lx]\n\t" \
    "s_add_u32 s60, s60, s43\n\t" \
    "s_addc_u32 s61, s61, 0\n\t" \
    "s_add_u32 m0, %[wm], 0xc000\n\t" \
    "s_mov_b32 s44, 0x600000\n\t" \
    "buffer_load_dwordx4 v240, %[wr], s44 offen offset:0 nt lds\n\t" \
    "buffer_load_dwordx4 v241, %[wr], s44 offen offset:1024 nt lds\n\t" \
    "buffer_load_dwordx4 v242, %[wr], s44 offen offset:2048 nt lds\n\t" \
    "ds_read_b64_tr_b16 v[228:229], v244 offset:0\n\t" \
    "ds_read_b64_tr_b16 v[230:231], v244 offset:1536\n\t" \
    "ds_read_b64_tr_b16 v[124:125], v244 offset:32\n\t" \
    "ds_read_b64_tr_b16 v[126:127], v244 offset:1568\n\t" \
    "ds_read_b64_tr_b16 v[132:133], v244 offset:64\n\t" \
    "ds_read_b64_tr_b16 v[134:135], v244 offset:1600\n\t" \
    "v_add_u32 v244, 0x18000, v243\n\t" \
    "s_waitcnt vmcnt(12) lgkmcnt(0)\n\t" \
    "s_barrier \n\t" \
    "s_add_u32 m0, %[wm], 0x12000\n\t" \
    "s_mov_b32 s44, 0x660000\n\t" \
    "buffer_load_dwordx4 v240, %[wr], s44 offen offset:0 nt lds\n\t" \
    "buffer_load_dwordx4 v241, %[wr], s44 offen offset:1024 nt lds\n\t" \
    "buffer_load_dwordx4 v242, %[wr], s44 offen offset:2048 nt lds\n\t" \
    "ds_read_b64_tr_b16 v[136:137], v244 offset:0\n\t" \
    "ds_read_b64_tr_b16 v[138:139], v244 offset:1536\n\t" \
    "ds_read_b64_tr_b16 v[144:145], v244 offset:32\n\t" \
    "ds_read_b64_tr_b16 v[146:147], v244 offset:1568\n\t" \
    "ds_read_b64_tr_b16 v[152:153], v244 offset:64\n\t" \
    "ds_read_b64_tr_b16 v[154:155], v244 offset:1600\n\t" \
    "v_add_u32 v244, 0x1e000, v243\n\t" \
    "s_waitcnt vmcnt(12) lgkmcnt(0)\n\t" \
    "s_barrier \n\t" \
    "s_add_u32 m0, %[wm], 0x18000\n\t" \
    "s_mov_b32 s44, 0x6c0000\n\t" \
    "buffer_load_dwordx4 v240, %[wr], s44 offen offset:0 nt lds\n\t" \
    "buffer_load_dwordx4 v241, %[wr], s44 offen offset:1024 nt lds\n\t" \
    "buffer_load_dwordx4 v242, %[wr], s44 offen offset:2048 nt lds\n\t" \
    "ds_read_b64_tr_b16 v[140:141], v244 offset:0\n\t" \
    "ds_read_b64_tr_b16 v[142:143], v244 offset:1536\n\t" \
    "ds_read_b64_tr_b16 v[148:149], v244 offset:32\n\t" \
    "ds_read_b64_tr_b16 v[150:151], v244 offset:1568\n\t" \
    "ds_read_b64_tr_b16 v[156:157], v244 offset:64\n\t" \
    "ds_read_b64_tr_b16 v[158:159], v244 offset:1600\n\t" \
    "v_add_u32 v244, 0x0, v243\n\t" \
    "s_waitcnt vmcnt(12) lgkmcnt(0)\n\t" \
    "s_barrier \n\t" \
    "s_add_u32 m0, %[wm], 0x1e000\n\t" \
    "s_mov_b32 s44, 0x720000\n\t" \
    "buffer_load_dwordx4 v240, %[wr], s44 offen offset:0 nt lds\n\t" \
    "buffer_load_dwordx4 v241, %[wr], s44 offen offset:1024 nt lds\n\t" \
    "buffer_load_dwordx4 v242, %[wr], s44 offen offset:2048 nt lds\n\t" \
    "ds_read_b64_tr_b16 v[16:17], v244 offset:0\n\t" \
    "ds_read_b64_tr_b16 v[18:19], v244 offset:1536\n\t" \
    "ds_read_b64_tr_b16 v[24:25], v244 offset:32\n\t" \
    "ds_read_b64_tr_b16 v[26:27], v244 offset:1568\n\t" \
    "ds_read_b64_tr_b16 v[32:33], v244 offset:64\n\t" \
    "ds_read_b64_tr_b16 v[34:35], v244 offset:1600\n\t" \
    "v_add_u32 v244, 0x6000, v243\n\t" \
    "s_waitcnt vmcnt(12) lgkmcnt(0)\n\t" \
    "s_barrier \n\t" \
    "ds_read_b64_tr_b16 v[20:21], v244 offset:0\n\t" \
    "ds_read_b64_tr_b16 v[22:23], v244 offset:1536\n\t" \
    "ds_read_b64_tr_b16 v[28:29], v244 offset:32\n\t" \
    "ds_read_b64_tr_b16 v[30:31], v244 offset:1568\n\t" \
    "ds_read_b64_tr_b16 v[36:37], v244 offset:64\n\t" \
    "ds_read_b64_tr_b16 v[38:39], v244 offset:1600\n\t" \
    "v_add_u32 v244, 0xc000, v243\n\t" \
    "s_waitcnt vmcnt(9) lgkmcnt(0)\n\t" \
    "s_barrier \n\t" \
    "ds_read_b64_tr_b16 v[232:233], v244 offset:0\n\t" \
    "ds_read_b64_tr_b16 v[234:235], v244 offset:1536\n\t" \
    "ds_read_b64_tr_b16 v[160:161], v244 offset:32\n\t" \
    "ds_read_b64_tr_b16 v[162:163], v244 offset:1568\n\t" \
    "ds_read_b64_tr_b16 v[168:169], v244 offset:64\n\t" \
    "ds_read_b64_tr_b16 v[170:171], v244 offset:1600\n\t" \
    "v_add_u32 v244, 0x12000, v243\n\t" \
    "s_waitcnt vmcnt(6) lgkmcnt(0)\n\t" \
    "s_barrier \n\t" \
    "ds_read_b64_tr_b16 v[236:237], v244 offset:0\n\t" \
    "ds_read_b64_tr_b16 v[238:239], v244 offset:1536\n\t" \
    "ds_read_b64_tr_b16 v[164:165], v244 offset:32\n\t" \
    "ds_read_b64_tr_b16 v[166:167], v244 offset:1568\n\t" \
    "ds_read_b64_tr_b16 v[172:173], v244 offset:64\n\t" \
    "ds_read_b64_tr_b16 v[174:175], v244 offset:1600\n\t" \
    "v_add_u32 v244, 0x18000, v243\n\t" \
    "s_waitcnt vmcnt(3) lgkmcnt(0)\n\t" \
    "s_barrier \n\t" \
    "ds_read_b64_tr_b16 v[176:177], v244 offset:0\n\t" \
    "ds_read_b64_tr_b16 v[178:179], v244 offset:1536\n\t" \
    "ds_read_b64_tr_b16 v[184:185], v244 offset:32\n\t" \
    "ds_read_b64_tr_b16 v[186:187], v244 offset:1568\n\t" \
    "ds_read_b64_tr_b16 v[192:193], v244 offset:64\n\t" \
    "ds_read_b64_tr_b16 v[194:195], v244 offset:1600\n\t" \
    "v_add_u32 v244, 0x1e000, v243\n\t" \
    "s_waitcnt vmcnt(0) lgkmcnt(0)\n\t" \
    "s_barrier \n\t" \
    "ds_read_b64_tr_b16 v[180:181], v244 offset:0\n\t" \
    "ds_read_b64_tr_b16 v[182:183], v244 offset:1536\n\t" \
    "ds_read_b64_tr_b16 v[188:189], v244 offset:32\n\t" \
    "ds_read_b64_tr_b16 v[190:191], v244 offset:1568\n\t" \
    "ds_read_b64_tr_b16 v[196:197], v244 offset:64\n\t" \
    "ds_read_b64_tr_b16 v[198:199], v244 offset:1600\n\t" \
    "s_waitcnt lgkmcnt(0)\n\t" \
    "s_barrier \n\t" \
    LST0 \
    "ds_write_b128 v254, v[200:203] offset:51200\n\t" \
    "ds_write_b128 v254, v[204:207] offset:52224\n\t" \
    "ds_write_b128 v254, v[208:211] offset:53248\n\t" \
    "ds_write_b128 v254, v[212:215] offset:54272\n\t" \
    "ds_write_b128 v254, v[216:219] offset:55296\n\t" \
    "ds_write_b128 v254, v[220:223] offset:56320\n\t" \
    "ds_write_b128 v254, v[224:227] offset:57344\n\t" \
    "ds_write_b128 v254, v[228:231] offset:58368\n\t" \
    "ds_write_b128 v254, v[232:235] offset:59392\n\t" \
    "ds_write_b128 v254, v[236:239] offset:60416\n\t" \
    "s_waitcnt vmcnt(0) lgkmcnt(0)\n\t" \
    "s_barrier \n\t" \
    "ds_read_b128 v[224:227], v253 offset:0\n\t" \
    "ds_read_b128 v[244:247], v254 offset:53248\n\t" \
    "ds_read_b128 v[228:231], v253 offset:1024\n\t" \
    "ds_read_b128 v[232:235], v253 offset:20480\n\t" \
    "ds_read_b128 v[236:239], v253 offset:21504\n\t" \
    "ds_read_b128 v[248:251], v254 offset:54272\n\t" \
    ".Ls23_%=:\n\t" \
    "s_waitcnt lgkmcnt(5)\n\t" \
    "v_mfma_f32_16x16x32_bf16 v[204:207], v[40:43], v[224:227], 0\n\t" \
    "s_waitcnt lgkmcnt(3)\n\t" \
    "v_mfma_f32_16x16x32_bf16 v[204:207], v[44:47], v[228:231], v[204:207]\n\t" \
    "ds_read_b128 v[240:243], v253 offset:2048\n\t" \
    "v_mfma_f32_16x16x32_bf16 v[208:211], v[48:51], v[224:227], 0\n\t" \
    "v_mfma_f32_16x16x32_bf16 v[208:211], v[52:55], v[228:231], v[208:211]\n\t" \
    "v_mfma_f32_16x16x32_bf16 v[200:203], v[244:247], v[224:227], 0\n\t" \
    "s_waitcnt lgkmcnt(1)\n\t" \
    "v_mfma_f32_16x16x32_bf16 v[200:203], v[248:251], v[228:231], v[200:203]\n\t" \
    "v_mfma_f32_16x16x32_bf16 v[216:219], v[40:43], v[232:235], 0\n\t" \
    "ds_read_b128 v[224:227], v253 offset:3072\n\t" \
    "v_mfma_f32_16x16x32_bf16 v[216:219], v[44:47], v[236:239], v[216:219]\n\t" \
    "ds_read_b128 v[228:231], v253 offset:22528\n\t" \
    "v_mfma_f32_16x16x32_bf16 v[220:223], v[48:51], v[232:235], 0\n\t" \
    "v_mfma_f32_16x16x32_bf16 v[220:223], v[52:55], v[236:239], v[220:223]\n\t" \
    "v_mfma_f32_16x16x32_bf16 v[212:215], v[244:247], v[232:235], 0\n\t" \
    "ds_read_b128 v[244:247], v254 offset:55296\n\t" \
    "v_mfma_f32_16x16x32_bf16 v[212:215], v[248:251], v[236:239], v[212:215]\n\t" \
    "ds_read_b128 v[248:251], v254 offset:56320\n\t" \
    "s_waitcnt lgkmcnt(4)\n\t" \
    "v_mfma_f32_16x16x32_bf16 v[204:207], v[64:67], v[240:243], v[204:207]\n\t" \
    "ds_read_b128 v[232:235], v253 offset:23552\n\t" \
    "s_waitcnt lgkmcnt(4)\n\t" \
    "v_mfma_f32_16x16x32_bf16 v[204:207], v[68:71], v[224:227], v[204:207]\n\t" \
    "ds_read_b128 v[236:239], v253 offset:4096\n\t" \
    "v_mfma_f32_16x16x32_bf16 v[208:211], v[72:75], v[240:243], v[208:211]\n\t" \
    "v_mfma_f32_16x16x32_bf16 v[208:211], v[76:79], v[224:227], v[208:211]\n\t" \
    "v_mfma_f32_16x16x32_bf16 v[200:203], v[56:59], v[240:243], v[200:203]\n\t" \
    "v_mfma_f32_16x16x32_bf16 v[200:203], v[60:63], v[224:227], v[200:203]\n\t" \
    "s_waitcnt lgkmcnt(4)\n\t" \
    "v_mfma_f32_16x16x32_bf16 v[216:219], v[64:67], v[228:231], v[216:219]\n\t" \
    "ds_read_b128 v[240:243], v253 offset:5120\n\t" \
    "s_waitcnt lgkmcnt(2)\n\t" \
    "v_mfma_f32_16x16x32_bf16 v[216:219], v[68:71], v[232:235], v[216:219]\n\t" \
    "ds_read_b128 v[224:227], v253 offset:24576\n\t" \
    "v_mfma_f32_16x16x32_bf16 v[220:223], v[72:75], v[228:231], v[220:223]\n\t" \
    "v_mfma_f32_16x16x32_bf16 v[220:223], v[76:79], v[232:235], v[220:223]\n\t" \
    "v_mfma_f32_16x16x32_bf16 v[212:215], v[56:59], v[228:231], v[212:215]\n\t" \
    "v_mfma_f32_16x16x32_bf16 v[212:215], v[60:63], v[232:235], v[212:215]\n\t" \
    "s_waitcnt lgkmcnt(2)\n\t" \
    "v_mfma_f32_16x16x32_bf16 v[204:207], v[80:83], v[236:239], v[204:207]\n\t" \
    "ds_read_b128 v[228:231], v253 offset:25600\n\t" \
    "s_waitcnt lgkmcnt(2)\n\t" \
    "v_mfma_f32_16x16x32_bf16 v[204:207], v[84:87], v[240:243], v[204:207]\n\t" \
    "ds_read_b128 v[232:235], v253 offset:6144\n\t" \
    "v_mfma_f32_16x16x32_bf16 v[208:211], v[88:91], v[236:239], v[208:211]\n\t" \
    "v_mfma_f32_16x16x32_bf16 v[208:211], v[92:95], v[240:243], v[208:211]\n\t" \
    "v_mfma_f32_16x16x32_bf16 v[200:203], v[244:247], v[236:239], v[200:203]\n\t" \
    "v_mfma_f32_16x16x32_bf16 v[200:203], v[248:251], v[240:243], v[200:203]\n\t" \
    "s_waitcnt lgkmcnt(2)\n\t" \
    "v_mfma_f32_16x16x32_bf16 v[216:219], v[80:83], v[224:227], v[216:219]\n\t" \
    "ds_read_b128 v[236:239], v253 offset:7168\n\t" \
    "s_waitcnt lgkmcnt(2)\n\t" \
    "v_mfma_f32_16x16x32_bf16 v[216:219], v[84:87], v[228:231], v[216:219]\n\t" \
    "ds_read_b128 v[240:243], v253 offset:26624\n\t" \
    "v_mfma_f32_16x16x32_bf16 v[220:223], v[88:91], v[224:227], v[220:223]\n\t" \
    "v_mfma_f32_16x16x32_bf16 v[220:223], v[92:95], v[228:231], v[220:223]\n\t" \
    "v_mfma_f32_16x16x32_bf16 v[212:215], v[244:247], v[224:227], v[212:215]\n\t" \
    "ds_read_b128 v[244:247], v254 offset:51200\n\t" \
    "v_mfma_f32_16x16x32_bf16 v[212:215], v[248:251], v[228:231], v[212:215]\n\t" \
    "ds_read_b128 v[248:251], v254 offset:52224\n\t" \
    "s_waitcnt lgkmcnt(4)\n\t" \
    "v_mfma_f32_16x16x32_bf16 v[204:207], v[104:107], v[232:235], v[204:207]\n\t" \
    "ds_read_b128 v[224:227], v253 offset:27648\n\t" \
    "s_waitcnt lgkmcnt(4)\n\t" \
    "v_mfma_f32_16x16x32_bf16 v[204:207], v[108:111], v[236:239], v[204:207]\n\t" \
    "ds_read_b128 v[228:231], v253 offset:8192\n\t" \
    "v_mfma_f32_16x16x32_bf16 v[208:211], v[112:115], v[232:235], v[208:211]\n\t" \
    "v_mfma_f32_16x16x32_bf16 v[208:211], v[116:119], v[236:239], v[208:211]\n\t" \
    "v_mfma_f32_16x16x32_bf16 v[200:203], v[96:99], v[232:235], v[200:203]\n\t" \
    "v_mfma_f32_16x16x32_bf16 v[200:203], v[100:103], v[236:239], v[200:203]\n\t" \
    "s_waitcnt lgkmcnt(4)\n\t" \
    "v_mfma_f32_16x16x32_bf16 v[216:219], v[104:107], v[240:243], v[216:219]\n\t" \
    "ds_read_b128 v[232:235], v253 offset:9216\n\t" \
    "s_waitcnt lgkmcnt(2)\n\t" \
    "v_mfma_f32_16x16x32_bf16 v[216:219], v[108:111], v[224:227], v[216:219]\n\t" \
    "ds_read_b128 v[236:239], v253 offset:28672\n\t" \
    "v_mfma_f32_16x16x32_bf16 v[220:223], v[112:115], v[240:243], v[220:223]\n\t" \
    "v_mfma_f32_16x16x32_bf16 v[220:223], v[116:119], v[224:227], v[220:223]\n\t" \
    "v_mfma_f32_16x16x32_bf16 v[212:215], v[96:99], v[240:243], v[212:215]\n\t" \
    "v_mfma_f32_16x16x32_bf16 v[212:215], v[100:103], v[224:227], v[212:215]\n\t" \
    "s_waitcnt lgkmcnt(2)\n\t" \
    "v_mfma_f32_16x16x32_bf16 v[204:207], v[0:3], v[228:231], v[204:207]\n\t" \
    "ds_read_b128 v[240:243], v253 offset:29696\n\t" \
    "s_waitcnt lgkmcnt(2)\n\t" \
    "v_mfma_f32_16x16x32_bf16 v[204:207], v[4:7], v[232:235], v[204:207]\n\t" \
    "ds_read_b128 v[224:227], v253 offset:10240\n\t" \
    "v_mfma_f32_16x16x32_bf16 v[208:211], v[8:11], v[228:231], v[208:211]\n\t" \
    "v_mfma_f32_16x16x32_bf16 v[208:211], v[12:15], v[232:235], v[208:211]\n\t" \
    "v_mfma_f32_16x16x32_bf16 v[200:203], v[244:247], v[228:231], v[200:203]\n\t" \
    "v_mfma_f32_16x16x32_bf16 v[200:203], v[248:251], v[232:235], v[200:203]\n\t" \
    "s_waitcnt lgkmcnt(2)\n\t" \
    "v_mfma_f32_16x16x32_bf16 v[216:219], v[0:3], v[236:239], v[216:219]\n\t" \
    "ds_read_b128 v[228:231], v253 offset:11264\n\t" \
    "s_waitcnt lgkmcnt(2)\n\t" \
    "v_mfma_f32_16x16x32_bf16 v[216:219], v[4:7], v[240:243], v[216:219]\n\t" \
    "ds_read_b128 v[232:235], v253 offset:30720\n\t" \
    "v_mfma_f32_16x16x32_bf16 v[220:223], v[8:11], v[236:239], v[220:223]\n\t" \
    "v_mfma_f32_16x16x32_bf16 v[220:223], v[12:15], v[240:243], v[220:223]\n\t" \
    "v_mfma_f32_16x16x32_bf16 v[212:215], v[244:247], v[236:239], v[212:215]\n\t" \
    "ds_read_b128 v[244:247], v254 offset:57344\n\t" \
    "v_mfma_f32_16x16x32_bf16 v[212:215], v[248:251], v[240:243], v[212:215]\n\t" \
    "ds_read_b128 v[248:251], v254 offset:58368\n\t" \
    "ds_read_b128 v[236:239], v253 offset:31744\n\t" \
    "s_waitcnt lgkmcnt(0)\n\t" \
    "s_barrier \n\t" \
    LST7 \
    "v_mfma_f32_16x16x32_bf16 v[204:207], v[120:123], v[224:227], v[204:207]\n\t" \
    "v_mfma_f32_16x16x32_bf16 v[204:207], v[124:127], v[228:231], v[204:207]\n\t" \
    "ds_read_b128 v[240:243], v253 offset:12288\n\t" \
    "v_mfma_f32_16x16x32_bf16 v[208:211], v[128:131], v[224:227], v[208:211]\n\t" \
    "v_mfma_f32_16x16x32_bf16 v[208:211], v[132:135], v[228:231], v[208:211]\n\t" \
    "v_mfma_f32_16x16x32_bf16 v[200:203], v[244:247], v[224:227], v[200:203]\n\t" \
    "v_mfma_f32_16x16x32_bf16 v[200:203], v[248:251], v[228:231], v[200:203]\n\t" \
    "v_mfma_f32_16x16x32_bf16 v[216:219], v[120:123], v[232:235], v[216:219]\n\t" \
    "ds_read_b128 v[224:227], v253 offset:13312\n\t" \
    "v_mfma_f32_16x16x32_bf16 v[216:219], v[124:127], v[236:239], v[216:219]\n\t" \
    "ds_read_b128 v[228:231], v253 offset:32768\n\t" \
    "v_mfma_f32_16x16x32_bf16 v[220:223], v[128:131], v[232:235], v[220:223]\n\t" \
    "v_mfma_f32_16x16x32_bf16 v[220:223], v[132:135], v[236:239], v[220:223]\n\t" \
    "v_mfma_f32_16x16x32_bf16 v[212:215], v[244:247], v[232:235], v[212:215]\n\t" \
    "ds_read_b128 v[244:247], v254 offset:59392\n\t" \
    "v_mfma_f32_16x16x32_bf16 v[212:215], v[248:251], v[236:239], v[212:215]\n\t" \
    "ds_read_b128 v[248:251], v254 offset:60416\n\t" \
    "s_waitcnt lgkmcnt(4)\n\t" \
    "v_mfma_f32_16x16x32_bf16 v[204:207], v[144:147], v[240:243], v[204:207]\n\t" \
    "ds_read_b128 v[232:235], v253 offset:33792\n\t" \
    "s_and_b32 s44, s40, 3\n\t" \
    "s_waitcnt lgkmcnt(4)\n\t" \
    "v_mfma_f32_16x16x32_bf16 v[204:207], v[148:151], v[224:227], v[204:207]\n\t" \
    "ds_read_b128 v[236:239], v253 offset:16384\n\t" \
    "s_lshl_b32 s44, s44, 17\n\t" \
    "v_mfma_f32_16x16x32_bf16 v[208:211], v[152:155], v[240:243], v[208:211]\n\t" \
    "s_add_u32 s44, s44, %[mb]\n\t" \
    "v_mfma_f32_16x16x32_bf16 v[208:211], v[156:159], v[224:227], v[208:211]\n\t" \
    "s_add_u32 s46, s44, 0x1000\n\t" \
    "v_mfma_f32_16x16x32_bf16 v[200:203], v[136:139], v[240:243], v[200:203]\n\t" \
    "s_add_u32 s47, s44, 0x1c00\n\t" \
    "v_mfma_f32_16x16x32_bf16 v[200:203], v[140:143], v[224:227], v[200:203]\n\t" \
    "s_waitcnt lgkmcnt(4)\n\t" \
    "v_mfma_f32_16x16x32_bf16 v[216:219], v[144:147], v[228:231], v[216:219]\n\t" \
    "ds_read_b128 v[240:243], v253 offset:17408\n\t" \
    "s_waitcnt lgkmcnt(2)\n\t" \
    "v_mfma_f32_16x16x32_bf16 v[216:219], v[148:151], v[232:235], v[216:219]\n\t" \
    "ds_read_b128 v[224:227], v253 offset:36864\n\t" \
    "v_mfma_f32_16x16x32_bf16 v[220:223], v[152:155], v[228:231], v[220:223]\n\t" \
    "v_mfma_f32_16x16x32_bf16 v[220:223], v[156:159], v[232:235], v[220:223]\n\t" \
    "v_mfma_f32_16x16x32_bf16 v[212:215], v[136:139], v[228:231], v[212:215]\n\t" \
    "v_mfma_f32_16x16x32_bf16 v[212:215], v[140:143], v[232:235], v[212:215]\n\t" \
    "s_waitcnt lgkmcnt(2)\n\t" \
    "v_mfma_f32_16x16x32_bf16 v[204:207], v[160:163], v[236:239], v[204:207]\n\t" \
    "ds_read_b128 v[228:231], v253 offset:37888\n\t" \
    "s_waitcnt lgkmcnt(2)\n\t" \
    "v_mfma_f32_16x16x32_bf16 v[204:207], v[164:167], v[240:243], v[204:207]\n\t" \
    "ds_read_b128 v[232:235], v253 offset:14336\n\t" \
    "v_mfma_f32_16x16x32_bf16 v[208:211], v[168:171], v[236:239], v[208:211]\n\t" \
    "v_mfma_f32_16x16x32_bf16 v[208:211], v[172:175], v[240:243], v[208:211]\n\t" \
    "v_mfma_f32_16x16x32_bf16 v[200:203], v[244:247], v[236:239], v[200:203]\n\t" \
    "v_mfma_f32_16x16x32_bf16 v[200:203], v[248:251], v[240:243], v[200:203]\n\t" \
    "s_waitcnt lgkmcnt(2)\n\t" \
    "v_mfma_f32_16x16x32_bf16 v[216:219], v[160:163], v[224:227], v[216:219]\n\t" \
    "ds_read_b128 v[236:239], v253 offset:15360\n\t" \
    "s_waitcnt lgkmcnt(2)\n\t" \
    "v_mfma_f32_16x16x32_bf16 v[216:219], v[164:167], v[228:231], v[216:219]\n\t" \
    "ds_read_b128 v[240:243], v253 offset:18432\n\t" \
    "v_mfma_f32_16x16x32_bf16 v[220:223], v[168:171], v[224:227], v[220:223]\n\t" \
    "v_mfma_f32_16x16x32_bf16 v[220:223], v[172:175], v[228:231], v[220:223]\n\t" \
    "v_mfma_f32_16x16x32_bf16 v[212:215], v[244:247], v[224:227], v[212:215]\n\t" \
    "v_mfma_f32_16x16x32_bf16 v[212:215], v[248:251], v[228:231], v[212:215]\n\t" \
    "s_waitcnt lgkmcnt(2)\n\t" \
    "v_mfma_f32_16x16x32_bf16 v[204:207], v[24:27], v[232:235], v[204:207]\n\t" \
    "ds_read_b128 v[224:227], v253 offset:19456\n\t" \
    "v_mov_b64 v[244:245], 0\n\t" \
    "s_waitcnt lgkmcnt(2)\n\t" \
    "v_mfma_f32_16x16x32_bf16 v[204:207], v[28:31], v[236:239], v[204:207]\n\t" \
    "ds_read_b128 v[228:231], v253 offset:34816\n\t" \
    "v_mov_b64 v[246:247], 0\n\t" \
    "v_mfma_f32_16x16x32_bf16 v[208:211], v[32:35], v[232:235], v[208:211]\n\t" \
    "v_mov_b64 v[248:249], 0\n\t" \
    "v_mfma_f32_16x16x32_bf16 v[208:211], v[36:39], v[236:239], v[208:211]\n\t" \
    "v_mov_b64 v[250:251], 0\n\t" \
    "v_mfma_f32_16x16x32_bf16 v[200:203], v[16:19], v[232:235], v[200:203]\n\t" \
    "v_mfma_f32_16x16x32_bf16 v[200:203], v[20:23], v[236:239], v[200:203]\n\t" \
    "s_waitcnt lgkmcnt(2)\n\t" \
    "v_mfma_f32_16x16x32_bf16 v[204:207], v[184:187], v[240:243], v[204:207]\n\t" \
    "ds_read_b128 v[232:235], v253 offset:35840\n\t" \
    "s_waitcnt lgkmcnt(2)\n\t" \
    "v_mfma_f32_16x16x32_bf16 v[204:207], v[188:191], v[224:227], v[204:207]\n\t" \
    "ds_read_b128 v[236:239], v253 offset:38912\n\t" \
    "v_mfma_f32_16x16x32_bf16 v[208:211], v[192:195], v[240:243], v[208:211]\n\t" \
    "v_mfma_f32_16x16x32_bf16 v[208:211], v[196:199], v[224:227], v[208:211]\n\t" \
    "v_mfma_f32_16x16x32_bf16 v[200:203], v[176:179], v[240:243], v[200:203]\n\t" \
    "v_mfma_f32_16x16x32_bf16 v[200:203], v[180:183], v[224:227], v[200:203]\n\t" \
    "ds_read_b128 v[240:243], v253 offset:39936\n\t" \
    "s_waitcnt lgkmcnt(3)\n\t" \
    "v_mfma_f32_16x16x32_bf16 v[216:219], v[24:27], v[228:231], v[216:219]\n\t" \
    "s_waitcnt lgkmcnt(2)\n\t" \
    "v_mfma_f32_16x16x32_bf16 v[216:219], v[28:31], v[232:235], v[216:219]\n\t" \
    "v_cvt_pk_bf16_f32 v204, v204, v208\n\t" \
    "v_cvt_pk_bf16_f32 v205, v205, v209\n\t" \
    "s_waitcnt lgkmcnt(1)\n\t" \
    "v_mfma_f32_16x16x32_bf16 v[216:219], v[184:187], v[236:239], v[216:219]\n\t" \
    "v_cvt_pk_bf16_f32 v206, v206, v210\n\t" \
    "v_cvt_pk_bf16_f32 v207, v207, v211\n\t" \
    "s_waitcnt lgkmcnt(0)\n\t" \
    "v_mfma_f32_16x16x32_bf16 v[216:219], v[188:191], v[240:243], v[216:219]\n\t" \
    "ds_write_b128 v254, v[204:207] offset:10240\n\t" \
    "v_cvt_pk_bf16_f32 v200, v200, v201\n\t" \
    "v_mfma_f32_16x16x32_bf16 v[220:223], v[32:35], v[228:231], v[220:223]\n\t" \
    "v_cvt_pk_bf16_f32 v201, v202, v203\n\t" \
    "ds_write_b64 v254, v[200:201] offset:6152\n\t" \
    "v_mfma_f32_16x16x32_bf16 v[220:223], v[36:39], v[232:235], v[220:223]\n\t" \
    "ds_read_b128 v[200:203], v254 offset:51200\n\t" \
    "ds_read_b128 v[204:207], v254 offset:52224\n\t" \
    "v_mfma_f32_16x16x32_bf16 v[220:223], v[192:195], v[236:239], v[220:223]\n\t" \
    "v_mov_b64 v[208:209], 0\n\t" \
    "v_mov_b64 v[210:211], 0\n\t" \
    "v_mfma_f32_16x16x32_bf16 v[220:223], v[196:199], v[240:243], v[220:223]\n\t" \
    "v_mfma_f32_16x16x32_bf16 v[212:215], v[16:19], v[228:231], v[212:215]\n\t" \
    "v_mfma_f32_16x16x32_bf16 v[212:215], v[20:23], v[232:235], v[212:215]\n\t" \
    "v_mfma_f32_16x16x32_bf16 v[212:215], v[176:179], v[236:239], v[212:215]\n\t" \
    "v_cvt_pk_bf16_f32 v216, v216, v220\n\t" \
    "v_cvt_pk_bf16_f32 v217, v217, v221\n\t" \
    "v_mfma_f32_16x16x32_bf16 v[212:215], v[180:183], v[240:243], v[212:215]\n\t" \
    "v_cvt_pk_bf16_f32 v218, v218, v222\n\t" \
    "v_cvt_pk_bf16_f32 v219, v219, v223\n\t" \
    "ds_write_b128 v254, v[216:219] offset:16384\n\t" \
    "ds_read_b128 v[216:219], v254 offset:53248\n\t" \
    "ds_read_b128 v[220:223], v254 offset:54272\n\t" \
    "s_nop 2\n\t" \
    "v_cvt_pk_bf16_f32 v212, v212, v213\n\t" \
    "v_cvt_pk_bf16_f32 v213, v214, v215\n\t" \
    "ds_write_b64 v254, v[212:213] offset:12296\n\t" \
    LST6 \
    "s_sleep 8\n\t" \
    "buffer_load_dwordx4 v[224:227], v253, s[60:63], s46 offen sc1\n\t" \
    "buffer_load_dwordx4 v[228:231], v253, s[60:63], s47 offen sc1\n\t" \
    "s_waitcnt lgkmcnt(0)\n\t" \
    LFLUSH212 \
    "v_bfe_i32 v212, v253, 5, 1\n\t" \
    "v_and_b32 v212, 0xaaaa, v212\n\t" \
    "v_or_b32 v212, 0x4444, v212\n\t" \
    "v_and_b32 v213, 0xffff, v255\n\t" \
    "v_lshrrev_b32 v214, 16, v255\n\t" \
    ".Ls24_%=:\n\t" \
    "s_waitcnt vmcnt(1)\n\t" \
    "v_max_u32 v215, v224, v225\n\t" \
    "v_max3_u32 v215, v226, v227, v215\n\t" \
    "s_waitcnt vmcnt(0)\n\t" \
    "v_max3_u32 v215, v228, v229, v215\n\t" \
    "v_max3_u32 v215, v230, v231, v215\n\t" \
    "v_cmp_eq_u32 vcc, -1, v215\n\t" \
    "s_cbranch_vccz .Ls25_%=\n\t" \
    "v_max_u32 v215, v224, v225\n\t" \
    "v_max3_u32 v215, v226, v227, v215\n\t" \
    "v_cmp_eq_u32 vcc, -1, v215\n\t" \
    "s_cbranch_vccz .Ls26_%=\n\t" \
    "buffer_load_dwordx4 v[224:227], v253, s[60:63], s46 offen sc1\n\t" \
    ".Ls26_%=:\n\t" \
    "v_max_u32 v215, v228, v229\n\t" \
    "v_max3_u32 v215, v230, v231, v215\n\t" \
    "v_cmp_eq_u32 vcc, -1, v215\n\t" \
    "s_cbranch_vccz .Ls27_%=\n\t" \
    "buffer_load_dwordx4 v[228:231], v253, s[60:63], s47 offen sc1\n\t" \
    ".Ls27_%=:\n\t" \
    "s_cmp_eq_u32 s41, 0\n\t" \
    "s_cbranch_scc1 .Ls25_%=\n\t" \
    "s_sub_u32 s41, s41, 1\n\t" \
    "s_waitcnt vmcnt(0)\n\t" \
    "s_branch .Ls24_%=\n\t" \
    ".Ls25_%=:\n\t" \
    LST2 \
    "ds_write_b128 v253, v[224:227] offset:45056\n\t" \
    "ds_write_b128 v253, v[228:231] offset:48128\n\t" \
    "v_smfmac_f32_16x16x64_bf16 v[244:247], v[224:227], v[0:7], v212\n\t" \
    "v_smfmac_f32_16x16x64_bf16 v[248:251], v[224:227], v[8:15], v212\n\t" \
    "v_smfmac_f32_16x16x64_bf16 v[208:211], v[224:227], v[200:207], v212\n\t" \
    "s_waitcnt lgkmcnt(0)\n\t" \
    "s_barrier \n\t" \
    LST3 \
    "ds_read_b128 v[232:235], v253 offset:40960\n\t" \
    "ds_read_b128 v[236:239], v253 offset:41984\n\t" \
    "ds_read_b128 v[240:243], v253 offset:43008\n\t" \
    "ds_read_b128 v[224:227], v253 offset:44032\n\t" \
    "ds_read_b128 v[200:203], v254 offset:55296\n\t" \
    "ds_read_b128 v[204:207], v254 offset:56320\n\t" \
    "v_smfmac_f32_16x16x64_bf16 v[208:211], v[228:231], v[16:23], v212\n\t" \
    "v_smfmac_f32_16x16x64_bf16 v[244:247], v[228:231], v[24:31], v212\n\t" \
    "v_smfmac_f32_16x16x64_bf16 v[248:251], v[228:231], v[32:39], v212\n\t" \
    "ds_read_b128 v[228:231], v253 offset:46080\n\t" \
    "s_waitcnt lgkmcnt(6)\n\t" \
    "v_smfmac_f32_16x16x64_bf16 v[208:211], v[232:235], v[216:223], v212\n\t" \
    "ds_read_b128 v[216:219], v254 offset:57344\n\t" \
    "ds_read_b128 v[220:223], v254 offset:58368\n\t" \
    "v_smfmac_f32_16x16x64_bf16 v[244:247], v[232:235], v[40:47], v212\n\t" \
    "v_smfmac_f32_16x16x64_bf16 v[248:251], v[232:235], v[48:55], v212\n\t" \
    "ds_read_b128 v[232:235], v253 offset:47104\n\t" \
    "s_waitcnt lgkmcnt(8)\n\t" \
    "v_smfmac_f32_16x16x64_bf16 v[208:211], v[236:239], v[56:63], v212\n\t" \
    "v_smfmac_f32_16x16x64_bf16 v[244:247], v[236:239], v[64:71], v212\n\t" \
    "v_smfmac_f32_16x16x64_bf16 v[248:251], v[236:239], v[72:79], v212\n\t" \
    "ds_read_b128 v[236:239], v253 offset:49152\n\t" \
    "s_waitcnt lgkmcnt(5)\n\t" \
    "v_smfmac_f32_16x16x64_bf16 v[208:211], v[240:243], v[200:207], v212\n\t" \
    "ds_read_b128 v[200:203], v254 offset:59392\n\t" \
    "ds_read_b128 v[204:207], v254 offset:60416\n\t" \
    "v_smfmac_f32_16x16x64_bf16 v[244:247], v[240:243], v[80:87], v212\n\t" \
    "v_smfmac_f32_16x16x64_bf16 v[248:251], v[240:243], v[88:95], v212\n\t" \
    "ds_read_b128 v[240:243], v253 offset:50176\n\t" \
    "v_smfmac_f32_16x16x64_bf16 v[208:211], v[224:227], v[96:103], v212\n\t" \
    "v_smfmac_f32_16x16x64_bf16 v[244:247], v[224:227], v[104:111], v212\n\t" \
    "v_smfmac_f32_16x16x64_bf16 v[248:251], v[224:227], v[112:119], v212\n\t" \
    "s_waitcnt lgkmcnt(5)\n\t" \
    "v_smfmac_f32_16x16x64_bf16 v[208:211], v[228:231], v[216:223], v212\n\t" \
    "s_waitcnt lgkmcnt(4)\n\t" \
    "v_smfmac_f32_16x16x64_bf16 v[208:211], v[232:235], v[136:143], v212\n\t" \
    "s_waitcnt lgkmcnt(1)\n\t" \
    "v_smfmac_f32_16x16x64_bf16 v[208:211], v[236:239], v[200:207], v212\n\t" \
    "s_waitcnt lgkmcnt(0)\n\t" \
    "v_smfmac_f32_16x16x64_bf16 v[208:211], v[240:243], v[176:183], v212\n\t" \
    "v_smfmac_f32_16x16x64_bf16 v[244:247], v[228:231], v[120:127], v212\n\t" \
    "v_smfmac_f32_16x16x64_bf16 v[244:247], v[232:235], v[144:151], v212\n\t" \
    "v_smfmac_f32_16x16x64_bf16 v[244:247], v[236:239], v[160:167], v212\n\t" \
    "v_add_f32 v208, v208, v210\n\t" \
    "v_add_f32 v209, v209, v211\n\t" \
    "v_cvt_pk_bf16_f32 v208, v208, v209\n\t" \
    "v_smfmac_f32_16x16x64_bf16 v[244:247], v[240:243], v[184:191], v212\n\t" \
    "ds_write_b16 v213, v208 offset:0\n\t" \
    "ds_write_b16_d16_hi v213, v208 offset:16\n\t" \
    "v_smfmac_f32_16x16x64_bf16 v[248:251], v[228:231], v[128:135], v212\n\t" \
    "v_smfmac_f32_16x16x64_bf16 v[248:251], v[232:235], v[152:159], v212\n\t" \
    "v_smfmac_f32_16x16x64_bf16 v[248:251], v[236:239], v[168:175], v212\n\t" \
    "v_add_f32 v244, v244, v246\n\t" \
    "v_add_f32 v245, v245, v247\n\t" \
    "v_smfmac_f32_16x16x64_bf16 v[248:251], v[240:243], v[192:199], v212\n\t" \
    "s_nop 7\n\t" \
    "v_add_f32 v248, v248, v250\n\t" \
    "v_add_f32 v249, v249, v251\n\t" \
    "v_cvt_pk_bf16_f32 v244, v244, v248\n\t" \
    "v_cvt_pk_bf16_f32 v245, v245, v249\n\t" \
    "ds_write2_b32 v214, v244, v245 offset1:4\n\t" \
    "s_sleep 3\n\t" \
    "ds_read_b128 v[200:203], v254 offset:12288\n\t" \
    "ds_read_b128 v[224:227], v254 offset:24576\n\t" \
    "ds_read_b128 v[204:207], v254 offset:13312\n\t" \
    "ds_read_b128 v[228:231], v254 offset:25600\n\t" \
    "ds_read_b128 v[208:211], v254 offset:14336\n\t" \
    "ds_read_b128 v[232:235], v254 offset:26624\n\t" \
    "ds_read_b128 v[212:215], v254 offset:15360\n\t" \
    "ds_read_b128 v[236:239], v254 offset:27648\n\t" \
    "ds_read_b128 v[216:219], v254 offset:16384\n\t" \
    "ds_read_b128 v[220:223], v254 offset:17408\n\t" \
    "s_and_b32 s44, s40, 3\n\t" \
    "s_lshl_b32 s44, s44, 17\n\t" \
    "s_add_u32 s50, s44, %[pb]\n\t" \
    "s_add_u32 s51, s44, %[pm]\n\t" \
    "s_mov_b32 s52, 0xf0000\n\t" \
    "s_mov_b32 s53, 0xfff00ff\n\t" \
    "s_mov_b32 s54, 0x1f0001\n\t" \
    "s_mov_b32 s55, 0x1fff01ff\n\t" \
    "s_mov_b32 s56, 0x3f0003\n\t" \
    "s_mov_b32 s57, 0x3fff03ff\n\t" \
    "s_mov_b32 s58, 0x7f0007\n\t" \
    "s_mov_b32 s59, 0x7fff07ff\n\t" \
    "s_waitcnt lgkmcnt(0)\n\t" \
    "s_barrier \n\t" \
    LST4 \
    "v_and_b32 v252, 0xffffff7f, v254\n\t" \
    "v_mfma_f32_16x16x32_bf16 v[240:243], v[224:227], v[200:203], 0\n\t" \
    "ds_read_b128 v[224:227], v254 offset:28672\n\t" \
    "v_mfma_f32_16x16x32_bf16 v[240:243], v[228:231], v[204:207], v[240:243]\n\t" \
    "ds_read_b128 v[228:231], v254 offset:29696\n\t" \
    "v_mfma_f32_16x16x32_bf16 v[240:243], v[232:235], v[208:211], v[240:243]\n\t" \
    "ds_read_b128 v[232:235], v254 offset:18432\n\t" \
    "v_mfma_f32_16x16x32_bf16 v[240:243], v[236:239], v[212:215], v[240:243]\n\t" \
    "ds_read_b128 v[236:239], v254 offset:19456\n\t" \
    "s_waitcnt lgkmcnt(3)\n\t" \
    "v_mfma_f32_16x16x32_bf16 v[240:243], v[224:227], v[216:219], v[240:243]\n\t" \
    "ds_read_b128 v[224:227], v254 offset:20480\n\t" \
    "s_waitcnt lgkmcnt(3)\n\t" \
    "v_mfma_f32_16x16x32_bf16 v[240:243], v[228:231], v[220:223], v[240:243]\n\t" \
    "ds_read_b128 v[228:231], v254 offset:21504\n\t" \
    "s_waitcnt lgkmcnt(3)\n\t" \
    "v_mfma_f32_16x16x32_bf16 v[244:247], v[232:235], v[200:203], 0\n\t" \
    "ds_read_b128 v[232:235], v254 offset:22528\n\t" \
    "s_waitcnt lgkmcnt(3)\n\t" \
    "v_mfma_f32_16x16x32_bf16 v[244:247], v[236:239], v[204:207], v[244:247]\n\t" \
    "ds_read_b128 v[236:239], v254 offset:23552\n\t" \
    "s_waitcnt lgkmcnt(3)\n\t" \
    "v_mfma_f32_16x16x32_bf16 v[244:247], v[224:227], v[208:211], v[244:247]\n\t" \
    "ds_read_b128 v[224:227], v252 offset:3072\n\t" \
    "s_waitcnt lgkmcnt(3)\n\t" \
    "v_mfma_f32_16x16x32_bf16 v[244:247], v[228:231], v[212:215], v[244:247]\n\t" \
    "ds_read_b128 v[228:231], v252 offset:3200\n\t" \
    "s_waitcnt lgkmcnt(3)\n\t" \
    "v_mfma_f32_16x16x32_bf16 v[244:247], v[232:235], v[216:219], v[244:247]\n\t" \
    "ds_read_b128 v[232:235], v252 offset:4096\n\t" \
    "s_waitcnt lgkmcnt(3)\n\t" \
    "v_mfma_f32_16x16x32_bf16 v[244:247], v[236:239], v[220:223], v[244:247]\n\t" \
    "ds_read_b128 v[236:239], v252 offset:4224\n\t" \
    "s_waitcnt lgkmcnt(3)\n\t" \
    "v_mfma_f32_16x16x32_bf16 v[248:251], v[224:227], v[200:203], 0\n\t" \
    "ds_read_b128 v[224:227], v252 offset:5120\n\t" \
    "s_waitcnt lgkmcnt(3)\n\t" \
    "v_mfma_f32_16x16x32_bf16 v[248:251], v[228:231], v[204:207], v[248:251]\n\t" \
    "ds_read_b128 v[228:231], v252 offset:5248\n\t" \
    "s_waitcnt lgkmcnt(3)\n\t" \
    "v_mfma_f32_16x16x32_bf16 v[248:251], v[232:235], v[208:211], v[248:251]\n\t" \
    "s_waitcnt lgkmcnt(2)\n\t" \
    "v_mfma_f32_16x16x32_bf16 v[248:251], v[236:239], v[212:215], v[248:251]\n\t" \
    "s_waitcnt lgkmcnt(1)\n\t" \
    "v_mfma_f32_16x16x32_bf16 v[248:251], v[224:227], v[216:219], v[248:251]\n\t" \
    "s_waitcnt lgkmcnt(0)\n\t" \
    "v_mfma_f32_16x16x32_bf16 v[248:251], v[228:231], v[220:223], v[248:251]\n\t" \
    "v_mov_b32 v226, 0xff7fffff\n\t" \
    "v_cndmask_b32 v240, v240, v226, s[52:53]\n\t" \
    "v_cndmask_b32 v241, v241, v226, s[54:55]\n\t" \
    "v_cndmask_b32 v242, v242, v226, s[56:57]\n\t" \
    "v_cndmask_b32 v243, v243, v226, s[58:59]\n\t" \
    "v_max3_f32 v224, v240, v241, v242\n\t" \
    "v_max3_f32 v225, v244, v245, v246\n\t" \
    "v_max3_f32 v224, v224, v243, v247\n\t" \
    "v_lshrrev_b32 v200, 5, v253\n\t" \
    "v_and_b32 v201, 0xf0, v253\n\t" \
    "v_and_or_b32 v200, v200, 8, v201\n\t" \
    "v_max3_f32 v224, v224, v248, v249\n\t" \
    "v_max3_f32 v225, v225, v250, v251\n\t" \
    "v_max_f32 v224, v224, v225\n\t" \
    "v_mov_b32 v225, v224\n\t" \
    "s_nop 1\n\t" \
    "v_permlane16_swap_b32 v224, v225\n\t" \
    "v_max_f32 v224, v224, v225\n\t" \
    "v_mov_b32 v225, v224\n\t" \
    "s_nop 1\n\t" \
    "v_permlane32_swap_b32 v224, v225\n\t" \
    "v_max_f32 v224, v224, v225\n\t" \
    "v_mul_f32 v224, %[scl], v224\n\t" \
    "v_fma_f32 v240, v240, %[scl], -v224\n\t" \
    "v_fma_f32 v241, v241, %[scl], -v224\n\t" \
    "v_exp_f32 v240, v240\n\t" \
    "v_fma_f32 v242, v242, %[scl], -v224\n\t" \
    "v_exp_f32 v241, v241\n\t" \
    "v_fma_f32 v243, v243, %[scl], -v224\n\t" \
    "v_exp_f32 v242, v242\n\t" \
    "v_fma_f32 v244, v244, %[scl], -v224\n\t" \
    "v_exp_f32 v243, v243\n\t" \
    "v_fma_f32 v245, v245, %[scl], -v224\n\t" \
    "v_exp_f32 v244, v244\n\t" \
    "v_fma_f32 v246, v246, %[scl], -v224\n\t" \
    "v_exp_f32 v245, v245\n\t" \
    "v_fma_f32 v247, v247, %[scl], -v224\n\t" \
    "v_exp_f32 v246, v246\n\t" \
    "v_fma_f32 v248, v248, %[scl], -v224\n\t" \
    "v_exp_f32 v247, v247\n\t" \
    "v_fma_f32 v249, v249, %[scl], -v224\n\t" \
    "v_exp_f32 v248, v248\n\t" \
    "v_fma_f32 v250, v250, %[scl], -v224\n\t" \
    "v_exp_f32 v249, v249\n\t" \
    "v_fma_f32 v251, v251, %[scl], -v224\n\t" \
    "v_exp_f32 v250, v250\n\t" \
    "v_exp_f32 v251, v251\n\t" \
    "v_add_f32 v224, v240, v241\n\t" \
    "v_add_f32 v225, v242, v243\n\t" \
    "v_add_f32 v226, v244, v245\n\t" \
    "v_add_f32 v227, v246, v247\n\t" \
    "v_add_f32 v232, v248, v249\n\t" \
    "v_add_f32 v233, v250, v251\n\t" \
    "v_add_f32 v224, v224, v225\n\t" \
    "v_add_f32 v226, v226, v227\n\t" \
    "v_add_f32 v232, v232, v233\n\t" \
    "v_add_f32 v224, v224, v226\n\t" \
    "v_fma_f32 v224, v232, 0.5, v224\n\t" \
    "v_mov_b32 v225, v224\n\t" \
    "s_nop 1\n\t" \
    "v_permlane16_swap_b32 v224, v225\n\t" \
    "v_add_f32 v224, v224, v225\n\t" \
    "v_mov_b32 v225, v224\n\t" \
    "s_nop 1\n\t" \
    "v_permlane32_swap_b32 v224, v225\n\t" \
    "v_add_f32 v224, v224, v225\n\t" \
    "v_rcp_f32 v224, v224\n\t" \
    "s_nop 0\n\t" \
    "v_mul_f32 v240, v240, v224\n\t" \
    "v_mul_f32 v241, v241, v224\n\t" \
    "v_mul_f32 v242, v242, v224\n\t" \
    "v_mul_f32 v243, v243, v224\n\t" \
    "v_mul_f32 v244, v244, v224\n\t" \
    "v_mul_f32 v245, v245, v224\n\t" \
    "v_mul_f32 v246, v246, v224\n\t" \
    "v_mul_f32 v247, v247, v224\n\t" \
    "v_mul_f32 v248, v248, v224\n\t" \
    "v_mul_f32 v249, v249, v224\n\t" \
    "v_mul_f32 v250, v250, v224\n\t" \
    "v_mul_f32 v251, v251, v224\n\t" \
    "v_cvt_pk_bf16_f32 v204, |v244|, |v240|\n\t" \
    "v_cvt_pk_bf16_f32 v205, |v245|, |v241|\n\t" \
    "v_cvt_pk_bf16_f32 v206, |v246|, |v242|\n\t" \
    "v_cvt_pk_bf16_f32 v207, |v247|, |v243|\n\t" \
    "v_cvt_pk_bf16_f32 v208, |v248|, |v249|\n\t" \
    "v_cvt_pk_bf16_f32 v209, |v250|, |v251|\n\t" \
    "buffer_store_dwordx4 v[204:207], v253, s[60:63], s50 offen sc1\n\t" \
    "s_mov_b32 exec_lo, -1\n\t" \
    "s_mov_b32 exec_hi, 0\n\t" \
    "buffer_store_dwordx2 v[208:209], v200, s[60:63], s51 offen sc1\n\t" \
    "s_mov_b32 exec_hi, -1\n\t" \
    LST5 \
    "ds_read_b128 v[224:227], v253 offset:0\n\t" \
    "ds_read_b128 v[244:247], v254 offset:53248\n\t" \
    "ds_read_b128 v[228:231], v253 offset:1024\n\t" \
    "ds_read_b128 v[232:235], v253 offset:20480\n\t" \
    "ds_read_b128 v[236:239], v253 offset:21504\n\t" \
    "ds_read_b128 v[248:251], v254 offset:54272\n\t" \
    "s_add_u32 s40, s40, 1\n\t" \
    "s_cmp_lg_u32 s40, 64\n\t" \
    "s_cbranch_scc1 .Ls23_%=\n\t" \
    "s_branch .Lend%=\n\t" \
    ".Ls1_%=:\n\t" \
    "s_add_u32 m0, %[wm], 0x18000\n\t" \
    "s_mov_b32 s44, 0x0\n\t" \
    "buffer_load_dwordx4 v240, %[wr], s44 offen offset:0 nt lds\n\t" \
    "buffer_load_dwordx4 v241, %[wr], s44 offen offset:1024 nt lds\n\t" \
    "buffer_load_dwordx4 v242, %[wr], s44 offen offset:2048 nt lds\n\t" \
    "s_add_u32 m0, %[wm], 0x1e000\n\t" \
    "s_mov_b32 s44, 0x60000\n\t" \
    "buffer_load_dwordx4 v240, %[wr], s44 offen offset:0 nt lds\n\t" \
    "buffer_load_dwordx4 v241, %[wr], s44 offen offset:1024 nt lds\n\t" \
    "buffer_load_dwordx4 v242, %[wr], s44 offen offset:2048 nt lds\n\t" \
    "s_add_u32 m0, %[wm], 0x0\n\t" \
    "s_mov_b32 s44, 0xc0000\n\t" \
    "buffer_load_dwordx4 v240, %[wr], s44 offen offset:0 nt lds\n\t" \
    "buffer_load_dwordx4 v241, %[wr], s44 offen offset:1024 nt lds\n\t" \
    "buffer_load_dwordx4 v242, %[wr], s44 offen offset:2048 nt lds\n\t" \
    "s_add_u32 m0, %[wm], 0x6000\n\t" \
    "s_mov_b32 s44, 0x120000\n\t" \
    "buffer_load_dwordx4 v240, %[wr], s44 offen offset:0 nt lds\n\t" \
    "buffer_load_dwordx4 v241, %[wr], s44 offen offset:1024 nt lds\n\t" \
    "buffer_load_dwordx4 v242, %[wr], s44 offen offset:2048 nt lds\n\t" \
    "s_add_u32 m0, %[wm], 0xc000\n\t" \
    "s_mov_b32 s44, 0x180000\n\t" \
    "buffer_load_dwordx4 v240, %[wr], s44 offen offset:0 nt lds\n\t" \
    "buffer_load_dwordx4 v241, %[wr], s44 offen offset:1024 nt lds\n\t" \
    "buffer_load_dwordx4 v242, %[wr], s44 offen offset:2048 nt lds\n\t" \
    "v_add_u32 v244, 0x18000, v243\n\t" \
    "s_waitcnt vmcnt(12) lgkmcnt(0)\n\t" \
    "s_barrier \n\t" \
    "s_add_u32 m0, %[wm], 0x12000\n\t" \
    "s_mov_b32 s44, 0x1e0000\n\t" \
    "buffer_load_dwordx4 v240, %[wr], s44 offen offset:0 nt lds\n\t" \
    "buffer_load_dwordx4 v241, %[wr], s44 offen offset:1024 nt lds\n\t" \
    "buffer_load_dwordx4 v242, %[wr], s44 offen offset:2048 nt lds\n\t" \
    "ds_read_b64_tr_b16 v[56:57], v244 offset:0\n\t" \
    "ds_read_b64_tr_b16 v[58:59], v244 offset:1536\n\t" \
    "ds_read_b64_tr_b16 v[64:65], v244 offset:32\n\t" \
    "ds_read_b64_tr_b16 v[66:67], v244 offset:1568\n\t" \
    "ds_read_b64_tr_b16 v[72:73], v244 offset:64\n\t" \
    "ds_read_b64_tr_b16 v[74:75], v244 offset:1600\n\t" \
    "v_add_u32 v244, 0x1e000, v243\n\t" \
    "s_waitcnt vmcnt(12) lgkmcnt(0)\n\t" \
    "s_barrier \n\t" \
    "s_add_u32 m0, %[wm], 0x18000\n\t" \
    "s_mov_b32 s44, 0x240000\n\t" \
    "buffer_load_dwordx4 v240, %[wr], s44 offen offset:0 nt lds\n\t" \
    "buffer_load_dwordx4 v241, %[wr], s44 offen offset:1024 nt lds\n\t" \
    "buffer_load_dwordx4 v242, %[wr], s44 offen offset:2048 nt lds\n\t" \
    "ds_read_b64_tr_b16 v[60:61], v244 offset:0\n\t" \
    "ds_read_b64_tr_b16 v[62:63], v244 offset:1536\n\t" \
    "ds_read_b64_tr_b16 v[68:69], v244 offset:32\n\t" \
    "ds_read_b64_tr_b16 v[70:71], v244 offset:1568\n\t" \
    "ds_read_b64_tr_b16 v[76:77], v244 offset:64\n\t" \
    "ds_read_b64_tr_b16 v[78:79], v244 offset:1600\n\t" \
    "v_add_u32 v244, 0x0, v243\n\t" \
    "s_waitcnt vmcnt(12) lgkmcnt(0)\n\t" \
    "s_barrier \n\t" \
    "s_add_u32 m0, %[wm], 0x1e000\n\t" \
    "s_mov_b32 s44, 0x2a0000\n\t" \
    "buffer_load_dwordx4 v240, %[wr], s44 offen offset:0 nt lds\n\t" \
    "buffer_load_dwordx4 v241, %[wr], s44 offen offset:1024 nt lds\n\t" \
    "buffer_load_dwordx4 v242, %[wr], s44 offen offset:2048 nt lds\n\t" \
    "ds_read_b64_tr_b16 v[96:97], v244 offset:0\n\t" \
    "ds_read_b64_tr_b16 v[98:99], v244 offset:1536\n\t" \
    "ds_read_b64_tr_b16 v[104:105], v244 offset:32\n\t" \
    "ds_read_b64_tr_b16 v[106:107], v244 offset:1568\n\t" \
    "ds_read_b64_tr_b16 v[112:113], v244 offset:64\n\t" \
    "ds_read_b64_tr_b16 v[114:115], v244 offset:1600\n\t" \
    "v_add_u32 v244, 0x6000, v243\n\t" \
    "s_waitcnt vmcnt(12) lgkmcnt(0)\n\t" \
    "s_barrier \n\t" \
    "s_add_u32 m0, %[wm], 0x0\n\t" \
    "s_mov_b32 s44, 0x300000\n\t" \
    "buffer_load_dwordx4 v240, %[wr], s44 offen offset:0 nt lds\n\t" \
    "buffer_load_dwordx4 v241, %[wr], s44 offen offset:1024 nt lds\n\t" \
    "buffer_load_dwordx4 v242, %[wr], s44 offen offset:2048 nt lds\n\t" \
    "ds_read_b64_tr_b16 v[100:101], v244 offset:0\n\t" \
    "ds_read_b64_tr_b16 v[102:103], v244 offset:1536\n\t" \
    "ds_read_b64_tr_b16 v[108:109], v244 offset:32\n\t" \
    "ds_read_b64_tr_b16 v[110:111], v244 offset:1568\n\t" \
    "ds_read_b64_tr_b16 v[116:117], v244 offset:64\n\t" \
    "ds_read_b64_tr_b16 v[118:119], v244 offset:1600\n\t" \
    "v_add_u32 v244, 0xc000, v243\n\t" \
    "s_waitcnt vmcnt(12) lgkmcnt(0)\n\t" \
    "s_barrier \n\t" \
    "s_add_u32 m0, %[wm], 0x6000\n\t" \
    "s_mov_b32 s44, 0x360000\n\t" \
    "buffer_load_dwordx4 v240, %[wr], s44 offen offset:0 nt lds\n\t" \
    "buffer_load_dwordx4 v241, %[wr], s44 offen offset:1024 nt lds\n\t" \
    "buffer_load_dwordx4 v242, %[wr], s44 offen offset:2048 nt lds\n\t" \
    "ds_read_b64_tr_b16 v[200:201], v244 offset:0\n\t" \
    "ds_read_b64_tr_b16 v[202:203], v244 offset:1536\n\t" \
    "ds_read_b64_tr_b16 v[0:1], v244 offset:32\n\t" \
    "ds_read_b64_tr_b16 v[2:3], v244 offset:1568\n\t" \
    "ds_read_b64_tr_b16 v[8:9], v244 offset:64\n\t" \
    "ds_read_b64_tr_b16 v[10:11], v244 offset:1600\n\t" \
    "v_add_u32 v244, 0x12000, v243\n\t" \
    "s_waitcnt vmcnt(12) lgkmcnt(0)\n\t" \
    "s_barrier \n\t" \
    "s_add_u32 m0, %[wm], 0xc000\n\t" \
    "s_mov_b32 s44, 0x3c0000\n\t" \
    "buffer_load_dwordx4 v240, %[wr], s44 offen offset:0 nt lds\n\t" \
    "buffer_load_dwordx4 v241, %[wr], s44 offen offset:1024 nt lds\n\t" \
    "buffer_load_dwordx4 v242, %[wr], s44 offen offset:2048 nt lds\n\t" \
    "ds_read_b64_tr_b16 v[204:205], v244 offset:0\n\t" \
    "ds_read_b64_tr_b16 v[206:207], v244 offset:1536\n\t" \
    "ds_read_b64_tr_b16 v[4:5], v244 offset:32\n\t" \
    "ds_read_b64_tr_b16 v[6:7], v244 offset:1568\n\t" \
    "ds_read_b64_tr_b16 v[12:13], v244 offset:64\n\t" \
    "ds_read_b64_tr_b16 v[14:15], v244 offset:1600\n\t" \
    "v_add_u32 v244, 0x18000, v243\n\t" \
    "s_waitcnt vmcnt(12) lgkmcnt(0)\n\t" \
    "s_barrier \n\t" \
    "v_mov_b32 v246, 0\n\t" \
    "buffer_load_dword v246, v246, s[60:63], %[mo] offen sc1\n\t" \
    "s_add_u32 m0, %[wm], 0x12000\n\t" \
    "s_mov_b32 s44, 0x420000\n\t" \
    "buffer_load_dwordx4 v240, %[wr], s44 offen offset:0 nt lds\n\t" \
    "buffer_load_dwordx4 v241, %[wr], s44 offen offset:1024 nt lds\n\t" \
    "buffer_load_dwordx4 v242, %[wr], s44 offen offset:2048 nt lds\n\t" \
    "ds_read_b64_tr_b16 v[208:209], v244 offset:0\n\t" \
    "ds_read_b64_tr_b16 v[210:211], v244 offset:1536\n\t" \
    "ds_read_b64_tr_b16 v[40:41], v244 offset:32\n\t" \
    "ds_read_b64_tr_b16 v[42:43], v244 offset:1568\n\t" \
    "ds_read_b64_tr_b16 v[48:49], v244 offset:64\n\t" \
    "ds_read_b64_tr_b16 v[50:51], v244 offset:1600\n\t" \
    "v_add_u32 v244, 0x1e000, v243\n\t" \
    "s_waitcnt vmcnt(12) lgkmcnt(0)\n\t" \
    "s_barrier \n\t" \
    "s_add_u32 m0, %[wm], 0x18000\n\t" \
    "s_mov_b32 s44, 0x480000\n\t" \
    "buffer_load_dwordx4 v240, %[wr], s44 offen offset:0 nt lds\n\t" \
    "buffer_load_dwordx4 v241, %[wr], s44 offen offset:1024 nt lds\n\t" \
    "buffer_load_dwordx4 v242, %[wr], s44 offen offset:2048 nt lds\n\t" \
    "ds_read_b64_tr_b16 v[212:213], v244 offset:0\n\t" \
    "ds_read_b64_tr_b16 v[214:215], v244 offset:1536\n\t" \
    "ds_read_b64_tr_b16 v[44:45], v244 offset:32\n\t" \
    "ds_read_b64_tr_b16 v[46:47], v244 offset:1568\n\t" \
    "ds_read_b64_tr_b16 v[52:53], v244 offset:64\n\t" \
    "ds_read_b64_tr_b16 v[54:55], v244 offset:1600\n\t" \
    "v_add_u32 v244, 0x0, v243\n\t" \
    "s_waitcnt vmcnt(12) lgkmcnt(0)\n\t" \
    "s_barrier \n\t" \
    "s_add_u32 m0, %[wm], 0x1e000\n\t" \
    "s_mov_b32 s44, 0x4e0000\n\t" \
    "buffer_load_dwordx4 v240, %[wr], s44 offen offset:0 nt lds\n\t" \
    "buffer_load_dwordx4 v241, %[wr], s44 offen offset:1024 nt lds\n\t" \
    "buffer_load_dwordx4 v242, %[wr], s44 offen offset:2048 nt lds\n\t" \
    "ds_read_b64_tr_b16 v[136:137], v244 offset:0\n\t" \
    "ds_read_b64_tr_b16 v[138:139], v244 offset:1536\n\t" \
    "ds_read_b64_tr_b16 v[144:145], v244 offset:32\n\t" \
    "ds_read_b64_tr_b16 v[146:147], v244 offset:1568\n\t" \
    "ds_read_b64_tr_b16 v[152:153], v244 offset:64\n\t" \
    "ds_read_b64_tr_b16 v[154:155], v244 offset:1600\n\t" \
    "v_add_u32 v244, 0x6000, v243\n\t" \
    "s_waitcnt vmcnt(12) lgkmcnt(0)\n\t" \
    "s_barrier \n\t" \
    "s_add_u32 m0, %[wm], 0x0\n\t" \
    "s_mov_b32 s44, 0x540000\n\t" \
    "buffer_load_dwordx4 v240, %[wr], s44 offen offset:0 nt lds\n\t" \
    "buffer_load_dwordx4 v241, %[wr], s44 offen offset:1024 nt lds\n\t" \
    "buffer_load_dwordx4 v242, %[wr], s44 offen offset:2048 nt lds\n\t" \
    "ds_read_b64_tr_b16 v[140:141], v244 offset:0\n\t" \
    "ds_read_b64_tr_b16 v[142:143], v244 offset:1536\n\t" \
    "ds_read_b64_tr_b16 v[148:149], v244 offset:32\n\t" \
    "ds_read_b64_tr_b16 v[150:151], v244 offset:1568\n\t" \
    "ds_read_b64_tr_b16 v[156:157], v244 offset:64\n\t" \
    "ds_read_b64_tr_b16 v[158:159], v244 offset:1600\n\t" \
    "v_add_u32 v244, 0xc000, v243\n\t" \
    "s_waitcnt vmcnt(12) lgkmcnt(0)\n\t" \
    "s_barrier \n\t" \
    "s_add_u32 m0, %[wm], 0x6000\n\t" \
    "s_mov_b32 s44, 0x5a0000\n\t" \
    "buffer_load_dwordx4 v240, %[wr], s44 offen offset:0 nt lds\n\t" \
    "buffer_load_dwordx4 v241, %[wr], s44 offen offset:1024 nt lds\n\t" \
    "buffer_load_dwordx4 v242, %[wr], s44 offen offset:2048 nt lds\n\t" \
    "ds_read_b64_tr_b16 v[216:217], v244 offset:0\n\t" \
    "ds_read_b64_tr_b16 v[218:219], v244 offset:1536\n\t" \
    "ds_read_b64_tr_b16 v[80:81], v244 offset:32\n\t" \
    "ds_read_b64_tr_b16 v[82:83], v244 offset:1568\n\t" \
    "ds_read_b64_tr_b16 v[88:89], v244 offset:64\n\t" \
    "ds_read_b64_tr_b16 v[90:91], v244 offset:1600\n\t" \
    "v_add_u32 v244, 0x12000, v243\n\t" \
    "s_waitcnt vmcnt(12) lgkmcnt(0)\n\t" \
    "s_barrier \n\t" \
    ".Ls11_%=:\n\t" \
    "v_readfirstlane_b32 s43, v246\n\t" \
    "s_nop 0\n\t" \
    "s_cmp_lg_u32 s43, -1\n\t" \
    "s_cbranch_scc1 .Ls12_%=\n\t" \
    "s_cmp_eq_u32 s41, 0\n\t" \
    "s_cbranch_scc1 .Ls12_%=\n\t" \
    "s_sub_u32 s41, s41, 1\n\t" \
    "s_sleep 4\n\t" \
    "v_mov_b32 v246, 0\n\t" \
    "buffer_load_dword v246, v246, s[60:63], %[mo] offen sc1\n\t" \
    "s_waitcnt vmcnt(0)\n\t" \
    "s_branch .Ls11_%=\n\t" \
    ".Ls12_%=:\n\t" \
    "v_mov_b32 v246, 0x8000\n\t" \
    "s_mov_b64 exec, 1\n\t" \
    "buffer_store_dword v246, v246, s[60:63], %[mo] offen sc1\n\t" \
    "s_mov_b64 exec, -1\n\t" \
    "s_and_b32 s43, s43, 0x2000\n\t" \
    "s_xor_b32 s43, s43, %[lx]\n\t" \
    "s_add_u32 s60, s60, s43\n\t" \
    "s_addc_u32 s61, s61, 0\n\t" \
    "s_add_u32 m0, %[wm], 0xc000\n\t" \
    "s_mov_b32 s44, 0x600000\n\t" \
    "buffer_load_dwordx4 v240, %[wr], s44 offen offset:0 nt lds\n\t" \
    "buffer_load_dwordx4 v241, %[wr], s44 offen offset:1024 nt lds\n\t" \
    "buffer_load_dwordx4 v242, %[wr], s44 offen offset:2048 nt lds\n\t" \
    "ds_read_b64_tr_b16 v[220:221], v244 offset:0\n\t" \
    "ds_read_b64_tr_b16 v[222:223], v244 offset:1536\n\t" \
    "ds_read_b64_tr_b16 v[84:85], v244 offset:32\n\t" \
    "ds_read_b64_tr_b16 v[86:87], v244 offset:1568\n\t" \
    "ds_read_b64_tr_b16 v[92:93], v244 offset:64\n\t" \
    "ds_read_b64_tr_b16 v[94:95], v244 offset:1600\n\t" \
    "v_add_u32 v244, 0x18000, v243\n\t" \
    "s_waitcnt vmcnt(12) lgkmcnt(0)\n\t" \
    "s_barrier \n\t" \
    "s_add_u32 m0, %[wm], 0x12000\n\t" \
    "s_mov_b32 s44, 0x660000\n\t" \
    "buffer_load_dwordx4 v240, %[wr], s44 offen offset:0 nt lds\n\t" \
    "buffer_load_dwordx4 v241, %[wr], s44 offen offset:1024 nt lds\n\t" \
    "buffer_load_dwordx4 v242, %[wr], s44 offen offset:2048 nt lds\n\t" \
    "ds_read_b64_tr_b16 v[16:17], v244 offset:0\n\t" \
    "ds_read_b64_tr_b16 v[18:19], v244 offset:1536\n\t" \
    "ds_read_b64_tr_b16 v[24:25], v244 offset:32\n\t" \
    "ds_read_b64_tr_b16 v[26:27], v244 offset:1568\n\t" \
    "ds_read_b64_tr_b16 v[32:33], v244 offset:64\n\t" \
    "ds_read_b64_tr_b16 v[34:35], v244 offset:1600\n\t" \
    "v_add_u32 v244, 0x1e000, v243\n\t" \
    "s_waitcnt vmcnt(12) lgkmcnt(0)\n\t" \
    "s_barrier \n\t" \
    "s_add_u32 m0, %[wm], 0x18000\n\t" \
    "s_mov_b32 s44, 0x6c0000\n\t" \
    "buffer_load_dwordx4 v240, %[wr], s44 offen offset:0 nt lds\n\t" \
    "buffer_load_dwordx4 v241, %[wr], s44 offen offset:1024 nt lds\n\t" \
    "buffer_load_dwordx4 v242, %[wr], s44 offen offset:2048 nt lds\n\t" \
    "ds_read_b64_tr_b16 v[20:21], v244 offset:0\n\t" \
    "ds_read_b64_tr_b16 v[22:23], v244 offset:1536\n\t" \
    "ds_read_b64_tr_b16 v[28:29], v244 offset:32\n\t" \
    "ds_read_b64_tr_b16 v[30:31], v244 offset:1568\n\t" \
    "ds_read_b64_tr_b16 v[36:37], v244 offset:64\n\t" \
    "ds_read_b64_tr_b16 v[38:39], v244 offset:1600\n\t" \
    "v_add_u32 v244, 0x0, v243\n\t" \
    "s_waitcnt vmcnt(12) lgkmcnt(0)\n\t" \
    "s_barrier \n\t" \
    "s_add_u32 m0, %[wm], 0x1e000\n\t" \
    "s_mov_b32 s44, 0x720000\n\t" \
    "buffer_load_dwordx4 v240, %[wr], s44 offen offset:0 nt lds\n\t" \
    "buffer_load_dwordx4 v241, %[wr], s44 offen offset:1024 nt lds\n\t" \
    "buffer_load_dwordx4 v242, %[wr], s44 offen offset:2048 nt lds\n\t" \
    "ds_read_b64_tr_b16 v[224:225], v244 offset:0\n\t" \
    "ds_read_b64_tr_b16 v[226:227], v244 offset:1536\n\t" \
    "ds_read_b64_tr_b16 v[120:121], v244 offset:32\n\t" \
    "ds_read_b64_tr_b16 v[122:123], v244 offset:1568\n\t" \
    "ds_read_b64_tr_b16 v[128:129], v244 offset:64\n\t" \
    "ds_read_b64_tr_b16 v[130:131], v244 offset:1600\n\t" \
    "v_add_u32 v244, 0x6000, v243\n\t" \
    "s_waitcnt vmcnt(12) lgkmcnt(0)\n\t" \
    "s_barrier \n\t" \
    "ds_read_b64_tr_b16 v[228:229], v244 offset:0\n\t" \
    "ds_read_b64_tr_b16 v[230:231], v244 offset:1536\n\t" \
    "ds_read_b64_tr_b16 v[124:125], v244 offset:32\n\t" \
    "ds_read_b64_tr_b16 v[126:127], v244 offset:1568\n\t" \
    "ds_read_b64_tr_b16 v[132:133], v244 offset:64\n\t" \
    "ds_read_b64_tr_b16 v[134:135], v244 offset:1600\n\t" \
    "v_add_u32 v244, 0xc000, v243\n\t" \
    "s_waitcnt vmcnt(9) lgkmcnt(0)\n\t" \
    "s_barrier \n\t" \
    "ds_read_b64_tr_b16 v[232:233], v244 offset:0\n\t" \
    "ds_read_b64_tr_b16 v[234:235], v244 offset:1536\n\t" \
    "ds_read_b64_tr_b16 v[160:161], v244 offset:32\n\t" \
    "ds_read_b64_tr_b16 v[162:163], v244 offset:1568\n\t" \
    "ds_read_b64_tr_b16 v[168:169], v244 offset:64\n\t" \
    "ds_read_b64_tr_b16 v[170:171], v244 offset:1600\n\t" \
    "v_add_u32 v244, 0x12000, v243\n\t" \
    "s_waitcnt vmcnt(6) lgkmcnt(0)\n\t" \
    "s_barrier \n\t" \
    "ds_read_b64_tr_b16 v[236:237], v244 offset:0\n\t" \
    "ds_read_b64_tr_b16 v[238:239], v244 offset:1536\n\t" \
    "ds_read_b64_tr_b16 v[164:165], v244 offset:32\n\t" \
    "ds_read_b64_tr_b16 v[166:167], v244 offset:1568\n\t" \
    "ds_read_b64_tr_b16 v[172:173], v244 offset:64\n\t" \
    "ds_read_b64_tr_b16 v[174:175], v244 offset:1600\n\t" \
    "v_add_u32 v244, 0x18000, v243\n\t" \
    "s_waitcnt vmcnt(3) lgkmcnt(0)\n\t" \
    "s_barrier \n\t" \
    "ds_read_b64_tr_b16 v[176:177], v244 offset:0\n\t" \
    "ds_read_b64_tr_b16 v[178:179], v244 offset:1536\n\t" \
    "ds_read_b64_tr_b16 v[184:185], v244 offset:32\n\t" \
    "ds_read_b64_tr_b16 v[186:187], v244 offset:1568\n\t" \
    "ds_read_b64_tr_b16 v[192:193], v244 offset:64\n\t" \
    "ds_read_b64_tr_b16 v[194:195], v244 offset:1600\n\t" \
    "v_add_u32 v244, 0x1e000, v243\n\t" \
    "s_waitcnt vmcnt(0) lgkmcnt(0)\n\t" \
    "s_barrier \n\t" \
    "ds_read_b64_tr_b16 v[180:181], v244 offset:0\n\t" \
    "ds_read_b64_tr_b16 v[182:183], v244 offset:1536\n\t" \
    "ds_read_b64_tr_b16 v[188:189], v244 offset:32\n\t" \
    "ds_read_b64_tr_b16 v[190:191], v244 offset:1568\n\t" \
    "ds_read_b64_tr_b16 v[196:197], v244 offset:64\n\t" \
    "ds_read_b64_tr_b16 v[198:199], v244 offset:1600\n\t" \
    "s_waitcnt lgkmcnt(0)\n\t" \
    "s_barrier \n\t" \
    LST0 \
    "ds_write_b128 v254, v[200:203] offset:40960\n\t" \
    "ds_write_b128 v254, v[204:207] offset:41984\n\t" \
    "ds_write_b128 v254, v[208:211] offset:43008\n\t" \
    "ds_write_b128 v254, v[212:215] offset:44032\n\t" \
    "ds_write_b128 v254, v[216:219] offset:45056\n\t" \
    "ds_write_b128 v254, v[220:223] offset:46080\n\t" \
    "ds_write_b128 v254, v[224:227] offset:47104\n\t" \
    "ds_write_b128 v254, v[228:231] offset:48128\n\t" \
    "ds_write_b128 v254, v[232:235] offset:49152\n\t" \
    "ds_write_b128 v254, v[236:239] offset:50176\n\t" \
    "s_waitcnt vmcnt(0) lgkmcnt(0)\n\t" \
    "s_barrier \n\t" \
    "ds_read_b128 v[224:227], v253 offset:0\n\t" \
    "ds_read_b128 v[244:247], v254 offset:40960\n\t" \
    "ds_read_b128 v[228:231], v253 offset:1024\n\t" \
    "ds_read_b128 v[232:235], v253 offset:20480\n\t" \
    "ds_read_b128 v[236:239], v253 offset:21504\n\t" \
    "ds_read_b128 v[248:251], v254 offset:41984\n\t" \
    ".Ls13_%=:\n\t" \
    "s_waitcnt lgkmcnt(5)\n\t" \
    "v_mfma_f32_16x16x32_bf16 v[204:207], v[64:67], v[224:227], 0\n\t" \
    "s_waitcnt lgkmcnt(3)\n\t" \
    "v_mfma_f32_16x16x32_bf16 v[204:207], v[68:71], v[228:231], v[204:207]\n\t" \
    "ds_read_b128 v[240:243], v253 offset:2048\n\t" \
    "v_mfma_f32_16x16x32_bf16 v[208:211], v[72:75], v[224:227], 0\n\t" \
    "v_mfma_f32_16x16x32_bf16 v[208:211], v[76:79], v[228:231], v[208:211]\n\t" \
    "v_mfma_f32_16x16x32_bf16 v[200:203], v[56:59], v[224:227], 0\n\t" \
    "v_mfma_f32_16x16x32_bf16 v[200:203], v[60:63], v[228:231], v[200:203]\n\t" \
    "s_waitcnt lgkmcnt(3)\n\t" \
    "v_mfma_f32_16x16x32_bf16 v[216:219], v[64:67], v[232:235], 0\n\t" \
    "ds_read_b128 v[224:227], v253 offset:3072\n\t" \
    "s_waitcnt lgkmcnt(3)\n\t" \
    "v_mfma_f32_16x16x32_bf16 v[216:219], v[68:71], v[236:239], v[216:219]\n\t" \
    "ds_read_b128 v[228:231], v253 offset:22528\n\t" \
    "v_mfma_f32_16x16x32_bf16 v[220:223], v[72:75], v[232:235], 0\n\t" \
    "v_mfma_f32_16x16x32_bf16 v[220:223], v[76:79], v[236:239], v[220:223]\n\t" \
    "v_mfma_f32_16x16x32_bf16 v[212:215], v[56:59], v[232:235], 0\n\t" \
    "v_mfma_f32_16x16x32_bf16 v[212:215], v[60:63], v[236:239], v[212:215]\n\t" \
    "s_waitcnt lgkmcnt(2)\n\t" \
    "v_mfma_f32_16x16x32_bf16 v[204:207], v[104:107], v[240:243], v[204:207]\n\t" \
    "ds_read_b128 v[232:235], v253 offset:23552\n\t" \
    "s_waitcnt lgkmcnt(2)\n\t" \
    "v_mfma_f32_16x16x32_bf16 v[204:207], v[108:111], v[224:227], v[204:207]\n\t" \
    "ds_read_b128 v[236:239], v253 offset:4096\n\t" \
    "v_mfma_f32_16x16x32_bf16 v[208:211], v[112:115], v[240:243], v[208:211]\n\t" \
    "v_mfma_f32_16x16x32_bf16 v[208:211], v[116:119], v[224:227], v[208:211]\n\t" \
    "v_mfma_f32_16x16x32_bf16 v[200:203], v[96:99], v[240:243], v[200:203]\n\t" \
    "v_mfma_f32_16x16x32_bf16 v[200:203], v[100:103], v[224:227], v[200:203]\n\t" \
    "s_waitcnt lgkmcnt(2)\n\t" \
    "v_mfma_f32_16x16x32_bf16 v[216:219], v[104:107], v[228:231], v[216:219]\n\t" \
    "ds_read_b128 v[240:243], v253 offset:5120\n\t" \
    "s_waitcnt lgkmcnt(2)\n\t" \
    "v_mfma_f32_16x16x32_bf16 v[216:219], v[108:111], v[232:235], v[216:219]\n\t" \
    "ds_read_b128 v[224:227], v253 offset:24576\n\t" \
    "v_mfma_f32_16x16x32_bf16 v[220:223], v[112:115], v[228:231], v[220:223]\n\t" \
    "v_mfma_f32_16x16x32_bf16 v[220:223], v[116:119], v[232:235], v[220:223]\n\t" \
    "v_mfma_f32_16x16x32_bf16 v[212:215], v[96:99], v[228:231], v[212:215]\n\t" \
    "v_mfma_f32_16x16x32_bf16 v[212:215], v[100:103], v[232:235], v[212:215]\n\t" \
    "s_waitcnt lgkmcnt(2)\n\t" \
    "v_mfma_f32_16x16x32_bf16 v[204:207], v[0:3], v[236:239], v[204:207]\n\t" \
    "ds_read_b128 v[228:231], v253 offset:25600\n\t" \
    "s_waitcnt lgkmcnt(2)\n\t" \
    "v_mfma_f32_16x16x32_bf16 v[204:207], v[4:7], v[240:243], v[204:207]\n\t" \
    "ds_read_b128 v[232:235], v253 offset:6144\n\t" \
    "v_mfma_f32_16x16x32_bf16 v[208:211], v[8:11], v[236:239], v[208:211]\n\t" \
    "v_mfma_f32_16x16x32_bf16 v[208:211], v[12:15], v[240:243], v[208:211]\n\t" \
    "v_mfma_f32_16x16x32_bf16 v[200:203], v[244:247], v[236:239], v[200:203]\n\t" \
    "v_mfma_f32_16x16x32_bf16 v[200:203], v[248:251], v[240:243], v[200:203]\n\t" \
    "s_waitcnt lgkmcnt(2)\n\t" \
    "v_mfma_f32_16x16x32_bf16 v[216:219], v[0:3], v[224:227], v[216:219]\n\t" \
    "ds_read_b128 v[236:239], v253 offset:7168\n\t" \
    "s_waitcnt lgkmcnt(2)\n\t" \
    "v_mfma_f32_16x16x32_bf16 v[216:219], v[4:7], v[228:231], v[216:219]\n\t" \
    "ds_read_b128 v[240:243], v253 offset:26624\n\t" \
    "v_mfma_f32_16x16x32_bf16 v[220:223], v[8:11], v[224:227], v[220:223]\n\t" \
    "v_mfma_f32_16x16x32_bf16 v[220:223], v[12:15], v[228:231], v[220:223]\n\t" \
    "v_mfma_f32_16x16x32_bf16 v[212:215], v[244:247], v[224:227], v[212:215]\n\t" \
    "ds_read_b128 v[244:247], v254 offset:43008\n\t" \
    "v_mfma_f32_16x16x32_bf16 v[212:215], v[248:251], v[228:231], v[212:215]\n\t" \
    "ds_read_b128 v[248:251], v254 offset:44032\n\t" \
    "s_waitcnt lgkmcnt(4)\n\t" \
    "v_mfma_f32_16x16x32_bf16 v[204:207], v[40:43], v[232:235], v[204:207]\n\t" \
    "ds_read_b128 v[224:227], v253 offset:27648\n\t" \
    "s_waitcnt lgkmcnt(4)\n\t" \
    "v_mfma_f32_16x16x32_bf16 v[204:207], v[44:47], v[236:239], v[204:207]\n\t" \
    "ds_read_b128 v[228:231], v253 offset:8192\n\t" \
    "v_mfma_f32_16x16x32_bf16 v[208:211], v[48:51], v[232:235], v[208:211]\n\t" \
    "v_mfma_f32_16x16x32_bf16 v[208:211], v[52:55], v[236:239], v[208:211]\n\t" \
    "s_waitcnt lgkmcnt(3)\n\t" \
    "v_mfma_f32_16x16x32_bf16 v[200:203], v[244:247], v[232:235], v[200:203]\n\t" \
    "s_waitcnt lgkmcnt(2)\n\t" \
    "v_mfma_f32_16x16x32_bf16 v[200:203], v[248:251], v[236:239], v[200:203]\n\t" \
    "v_mfma_f32_16x16x32_bf16 v[216:219], v[40:43], v[240:243], v[216:219]\n\t" \
    "ds_read_b128 v[232:235], v253 offset:9216\n\t" \
    "s_waitcnt lgkmcnt(2)\n\t" \
    "v_mfma_f32_16x16x32_bf16 v[216:219], v[44:47], v[224:227], v[216:219]\n\t" \
    "ds_read_b128 v[236:239], v253 offset:28672\n\t" \
    "v_mfma_f32_16x16x32_bf16 v[220:223], v[48:51], v[240:243], v[220:223]\n\t" \
    "v_mfma_f32_16x16x32_bf16 v[220:223], v[52:55], v[224:227], v[220:223]\n\t" \
    "v_mfma_f32_16x16x32_bf16 v[212:215], v[244:247], v[240:243], v[212:215]\n\t" \
    "ds_read_b128 v[244:247], v254 offset:45056\n\t" \
    "v_mfma_f32_16x16x32_bf16 v[212:215], v[248:251], v[224:227], v[212:215]\n\t" \
    "ds_read_b128 v[248:251], v254 offset:46080\n\t" \
    "s_waitcnt lgkmcnt(4)\n\t" \
    "v_mfma_f32_16x16x32_bf16 v[204:207], v[144:147], v[228:231], v[204:207]\n\t" \
    "ds_read_b128 v[240:243], v253 offset:29696\n\t" \
    "s_waitcnt lgkmcnt(4)\n\t" \
    "v_mfma_f32_16x16x32_bf16 v[204:207], v[148:151], v[232:235], v[204:207]\n\t" \
    "ds_read_b128 v[224:227], v253 offset:10240\n\t" \
    "v_mfma_f32_16x16x32_bf16 v[208:211], v[152:155], v[228:231], v[208:211]\n\t" \
    "v_mfma_f32_16x16x32_bf16 v[208:211], v[156:159], v[232:235], v[208:211]\n\t" \
    "v_mfma_f32_16x16x32_bf16 v[200:203], v[136:139], v[228:231], v[200:203]\n\t" \
    "v_mfma_f32_16x16x32_bf16 v[200:203], v[140:143], v[232:235], v[200:203]\n\t" \
    "s_waitcnt lgkmcnt(4)\n\t" \
    "v_mfma_f32_16x16x32_bf16 v[216:219], v[144:147], v[236:239], v[216:219]\n\t" \
    "ds_read_b128 v[228:231], v253 offset:11264\n\t" \
    "s_waitcnt lgkmcnt(2)\n\t" \
    "v_mfma_f32_16x16x32_bf16 v[216:219], v[148:151], v[240:243], v[216:219]\n\t" \
    "ds_read_b128 v[232:235], v253 offset:30720\n\t" \
    "v_mfma_f32_16x16x32_bf16 v[220:223], v[152:155], v[236:239], v[220:223]\n\t" \
    "v_mfma_f32_16x16x32_bf16 v[220:223], v[156:159], v[240:243], v[220:223]\n\t" \
    "v_mfma_f32_16x16x32_bf16 v[212:215], v[136:139], v[236:239], v[212:215]\n\t" \
    "v_mfma_f32_16x16x32_bf16 v[212:215], v[140:143], v[240:243], v[212:215]\n\t" \
    "ds_read_b128 v[236:239], v253 offset:31744\n\t" \
    "s_waitcnt lgkmcnt(0)\n\t" \
    "s_barrier \n\t" \
    LST7 \
    "v_mfma_f32_16x16x32_bf16 v[204:207], v[80:83], v[224:227], v[204:207]\n\t" \
    "v_mfma_f32_16x16x32_bf16 v[204:207], v[84:87], v[228:231], v[204:207]\n\t" \
    "ds_read_b128 v[240:243], v253 offset:14336\n\t" \
    "v_mfma_f32_16x16x32_bf16 v[208:211], v[88:91], v[224:227], v[208:211]\n\t" \
    "v_mfma_f32_16x16x32_bf16 v[208:211], v[92:95], v[228:231], v[208:211]\n\t" \
    "v_mfma_f32_16x16x32_bf16 v[200:203], v[244:247], v[224:227], v[200:203]\n\t" \
    "v_mfma_f32_16x16x32_bf16 v[200:203], v[248:251], v[228:231], v[200:203]\n\t" \
    "v_mfma_f32_16x16x32_bf16 v[216:219], v[80:83], v[232:235], v[216:219]\n\t" \
    "ds_read_b128 v[224:227], v253 offset:15360\n\t" \
    "v_mfma_f32_16x16x32_bf16 v[216:219], v[84:87], v[236:239], v[216:219]\n\t" \
    "ds_read_b128 v[228:231], v253 offset:34816\n\t" \
    "v_mfma_f32_16x16x32_bf16 v[220:223], v[88:91], v[232:235], v[220:223]\n\t" \
    "v_mfma_f32_16x16x32_bf16 v[220:223], v[92:95], v[236:239], v[220:223]\n\t" \
    "v_mfma_f32_16x16x32_bf16 v[212:215], v[244:247], v[232:235], v[212:215]\n\t" \
    "ds_read_b128 v[244:247], v254 offset:47104\n\t" \
    "v_mfma_f32_16x16x32_bf16 v[212:215], v[248:251], v[236:239], v[212:215]\n\t" \
    "ds_read_b128 v[248:251], v254 offset:48128\n\t" \
    "s_waitcnt lgkmcnt(4)\n\t" \
    "v_mfma_f32_16x16x32_bf16 v[204:207], v[120:123], v[240:243], v[204:207]\n\t" \
    "ds_read_b128 v[232:235], v253 offset:35840\n\t" \
    "s_and_b32 s44, s40, 3\n\t" \
    "s_waitcnt lgkmcnt(4)\n\t" \
    "v_mfma_f32_16x16x32_bf16 v[204:207], v[124:127], v[224:227], v[204:207]\n\t" \
    "ds_read_b128 v[236:239], v253 offset:16384\n\t" \
    "s_lshl_b32 s44, s44, 17\n\t" \
    "v_mfma_f32_16x16x32_bf16 v[208:211], v[128:131], v[240:243], v[208:211]\n\t" \
    "s_add_u32 s44, s44, %[mb]\n\t" \
    "v_mfma_f32_16x16x32_bf16 v[208:211], v[132:135], v[224:227], v[208:211]\n\t" \
    "s_add_u32 s46, s44, 0x800\n\t" \
    "s_waitcnt lgkmcnt(3)\n\t" \
    "v_mfma_f32_16x16x32_bf16 v[200:203], v[244:247], v[240:243], v[200:203]\n\t" \
    "s_add_u32 s47, s44, 0x1800\n\t" \
    "s_waitcnt lgkmcnt(2)\n\t" \
    "v_mfma_f32_16x16x32_bf16 v[200:203], v[248:251], v[224:227], v[200:203]\n\t" \
    "s_add_u32 s48, s44, 0xc00\n\t" \
    "v_mfma_f32_16x16x32_bf16 v[216:219], v[120:123], v[228:231], v[216:219]\n\t" \
    "ds_read_b128 v[240:243], v253 offset:17408\n\t" \
    "s_waitcnt lgkmcnt(2)\n\t" \
    "v_mfma_f32_16x16x32_bf16 v[216:219], v[124:127], v[232:235], v[216:219]\n\t" \
    "ds_read_b128 v[224:227], v253 offset:36864\n\t" \
    "v_mfma_f32_16x16x32_bf16 v[220:223], v[128:131], v[228:231], v[220:223]\n\t" \
    "v_mfma_f32_16x16x32_bf16 v[220:223], v[132:135], v[232:235], v[220:223]\n\t" \
    "v_mfma_f32_16x16x32_bf16 v[212:215], v[244:247], v[228:231], v[212:215]\n\t" \
    "ds_read_b128 v[244:247], v254 offset:49152\n\t" \
    "v_mfma_f32_16x16x32_bf16 v[212:215], v[248:251], v[232:235], v[212:215]\n\t" \
    "ds_read_b128 v[248:251], v254 offset:50176\n\t" \
    "s_waitcnt lgkmcnt(4)\n\t" \
    "v_mfma_f32_16x16x32_bf16 v[204:207], v[160:163], v[236:239], v[204:207]\n\t" \
    "ds_read_b128 v[228:231], v253 offset:37888\n\t" \
    "s_waitcnt lgkmcnt(4)\n\t" \
    "v_mfma_f32_16x16x32_bf16 v[204:207], v[164:167], v[240:243], v[204:207]\n\t" \
    "ds_read_b128 v[232:235], v253 offset:12288\n\t" \
    "v_mfma_f32_16x16x32_bf16 v[208:211], v[168:171], v[236:239], v[208:211]\n\t" \
    "v_mfma_f32_16x16x32_bf16 v[208:211], v[172:175], v[240:243], v[208:211]\n\t" \
    "s_waitcnt lgkmcnt(3)\n\t" \
    "v_mfma_f32_16x16x32_bf16 v[200:203], v[244:247], v[236:239], v[200:203]\n\t" \
    "s_waitcnt lgkmcnt(2)\n\t" \
    "v_mfma_f32_16x16x32_bf16 v[200:203], v[248:251], v[240:243], v[200:203]\n\t" \
    "v_mfma_f32_16x16x32_bf16 v[216:219], v[160:163], v[224:227], v[216:219]\n\t" \
    "ds_read_b128 v[236:239], v253 offset:13312\n\t" \
    "s_waitcnt lgkmcnt(2)\n\t" \
    "v_mfma_f32_16x16x32_bf16 v[216:219], v[164:167], v[228:231], v[216:219]\n\t" \
    "ds_read_b128 v[240:243], v253 offset:18432\n\t" \
    "v_mfma_f32_16x16x32_bf16 v[220:223], v[168:171], v[224:227], v[220:223]\n\t" \
    "v_mfma_f32_16x16x32_bf16 v[220:223], v[172:175], v[228:231], v[220:223]\n\t" \
    "v_mfma_f32_16x16x32_bf16 v[212:215], v[244:247], v[224:227], v[212:215]\n\t" \
    "v_mfma_f32_16x16x32_bf16 v[212:215], v[248:251], v[228:231], v[212:215]\n\t" \
    "s_waitcnt lgkmcnt(2)\n\t" \
    "v_mfma_f32_16x16x32_bf16 v[204:207], v[24:27], v[232:235], v[204:207]\n\t" \
    "ds_read_b128 v[224:227], v253 offset:19456\n\t" \
    "v_mov_b64 v[244:245], 0\n\t" \
    "s_waitcnt lgkmcnt(2)\n\t" \
    "v_mfma_f32_16x16x32_bf16 v[204:207], v[28:31], v[236:239], v[204:207]\n\t" \
    "ds_read_b128 v[228:231], v253 offset:32768\n\t" \
    "v_mov_b64 v[246:247], 0\n\t" \
    "v_mfma_f32_16x16x32_bf16 v[208:211], v[32:35], v[232:235], v[208:211]\n\t" \
    "v_mov_b64 v[248:249], 0\n\t" \
    "v_mfma_f32_16x16x32_bf16 v[208:211], v[36:39], v[236:239], v[208:211]\n\t" \
    "v_mov_b64 v[250:251], 0\n\t" \
    "v_mfma_f32_16x16x32_bf16 v[200:203], v[16:19], v[232:235], v[200:203]\n\t" \
    "v_mfma_f32_16x16x32_bf16 v[200:203], v[20:23], v[236:239], v[200:203]\n\t" \
    "s_waitcnt lgkmcnt(2)\n\t" \
    "v_mfma_f32_16x16x32_bf16 v[204:207], v[184:187], v[240:243], v[204:207]\n\t" \
    "ds_read_b128 v[232:235], v253 offset:33792\n\t" \
    "s_waitcnt lgkmcnt(2)\n\t" \
    "v_mfma_f32_16x16x32_bf16 v[204:207], v[188:191], v[224:227], v[204:207]\n\t" \
    "ds_read_b128 v[236:239], v253 offset:38912\n\t" \
    "v_mfma_f32_16x16x32_bf16 v[208:211], v[192:195], v[240:243], v[208:211]\n\t" \
    "v_mfma_f32_16x16x32_bf16 v[208:211], v[196:199], v[224:227], v[208:211]\n\t" \
    "v_mfma_f32_16x16x32_bf16 v[200:203], v[176:179], v[240:243], v[200:203]\n\t" \
    "v_mfma_f32_16x16x32_bf16 v[200:203], v[180:183], v[224:227], v[200:203]\n\t" \
    "ds_read_b128 v[240:243], v253 offset:39936\n\t" \
    "s_waitcnt lgkmcnt(3)\n\t" \
    "v_mfma_f32_16x16x32_bf16 v[216:219], v[24:27], v[228:231], v[216:219]\n\t" \
    "s_waitcnt lgkmcnt(2)\n\t" \
    "v_mfma_f32_16x16x32_bf16 v[216:219], v[28:31], v[232:235], v[216:219]\n\t" \
    "v_cvt_pk_bf16_f32 v204, v204, v208\n\t" \
    "v_cvt_pk_bf16_f32 v205, v205, v209\n\t" \
    "s_waitcnt lgkmcnt(1)\n\t" \
    "v_mfma_f32_16x16x32_bf16 v[216:219], v[184:187], v[236:239], v[216:219]\n\t" \
    "v_cvt_pk_bf16_f32 v206, v206, v210\n\t" \
    "v_cvt_pk_bf16_f32 v207, v207, v211\n\t" \
    "s_waitcnt lgkmcnt(0)\n\t" \
    "v_mfma_f32_16x16x32_bf16 v[216:219], v[188:191], v[240:243], v[216:219]\n\t" \
    "ds_write_b128 v254, v[204:207] offset:9216\n\t" \
    "v_cvt_pk_bf16_f32 v200, v200, v201\n\t" \
    "v_mfma_f32_16x16x32_bf16 v[220:223], v[32:35], v[228:231], v[220:223]\n\t" \
    "v_cvt_pk_bf16_f32 v201, v202, v203\n\t" \
    "ds_write_b64 v254, v[200:201] offset:7168\n\t" \
    "v_mfma_f32_16x16x32_bf16 v[220:223], v[36:39], v[232:235], v[220:223]\n\t" \
    "ds_read_b128 v[200:203], v254 offset:40960\n\t" \
    "ds_read_b128 v[204:207], v254 offset:41984\n\t" \
    "v_mfma_f32_16x16x32_bf16 v[220:223], v[192:195], v[236:239], v[220:223]\n\t" \
    "v_mov_b64 v[208:209], 0\n\t" \
    "v_mov_b64 v[210:211], 0\n\t" \
    "v_mfma_f32_16x16x32_bf16 v[220:223], v[196:199], v[240:243], v[220:223]\n\t" \
    "v_mfma_f32_16x16x32_bf16 v[212:215], v[16:19], v[228:231], v[212:215]\n\t" \
    "v_mfma_f32_16x16x32_bf16 v[212:215], v[20:23], v[232:235], v[212:215]\n\t" \
    "v_mfma_f32_16x16x32_bf16 v[212:215], v[176:179], v[236:239], v[212:215]\n\t" \
    "v_cvt_pk_bf16_f32 v216, v216, v220\n\t" \
    "v_cvt_pk_bf16_f32 v217, v217, v221\n\t" \
    "v_mfma_f32_16x16x32_bf16 v[212:215], v[180:183], v[240:243], v[212:215]\n\t" \
    "v_cvt_pk_bf16_f32 v218, v218, v222\n\t" \
    "v_cvt_pk_bf16_f32 v219, v219, v223\n\t" \
    "ds_write_b128 v254, v[216:219] offset:15360\n\t" \
    "ds_read_b128 v[216:219], v254 offset:43008\n\t" \
    "ds_read_b128 v[220:223], v254 offset:44032\n\t" \
    "s_nop 2\n\t" \
    "v_cvt_pk_bf16_f32 v212, v212, v213\n\t" \
    "v_cvt_pk_bf16_f32 v213, v214, v215\n\t" \
    "ds_write_b64 v254, v[212:213] offset:13312\n\t" \
    LST6 \
    "s_sleep 8\n\t" \
    "buffer_load_dwordx4 v[224:227], v253, s[60:63], s46 offen sc1\n\t" \
    "buffer_load_dwordx4 v[228:231], v253, s[60:63], s47 offen sc1\n\t" \
    "buffer_load_dwordx4 v[232:235], v253, s[60:63], s48 offen sc1\n\t" \
    "s_waitcnt lgkmcnt(0)\n\t" \
    LFLUSH212 \
    "v_bfe_i32 v212, v253, 5, 1\n\t" \
    "v_and_b32 v212, 0xaaaa, v212\n\t" \
    "v_or_b32 v212, 0x4444, v212\n\t" \
    "v_and_b32 v213, 0xffff, v255\n\t" \
    "v_lshrrev_b32 v214, 16, v255\n\t" \
    ".Ls14_%=:\n\t" \
    "s_waitcnt vmcnt(2)\n\t" \
    "v_max_u32 v215, v224, v225\n\t" \
    "v_max3_u32 v215, v226, v227, v215\n\t" \
    "s_waitcnt vmcnt(1)\n\t" \
    "v_max3_u32 v215, v228, v229, v215\n\t" \
    "v_max3_u32 v215, v230, v231, v215\n\t" \
    "s_waitcnt vmcnt(0)\n\t" \
    "v_max3_u32 v215, v232, v233, v215\n\t" \
    "v_max3_u32 v215, v234, v235, v215\n\t" \
    "v_cmp_eq_u32 vcc, -1, v215\n\t" \
    "s_cbranch_vccz .Ls15_%=\n\t" \
    "v_max_u32 v215, v224, v225\n\t" \
    "v_max3_u32 v215, v226, v227, v215\n\t" \
    "v_cmp_eq_u32 vcc, -1, v215\n\t" \
    "s_cbranch_vccz .Ls16_%=\n\t" \
    "buffer_load_dwordx4 v[224:227], v253, s[60:63], s46 offen sc1\n\t" \
    ".Ls16_%=:\n\t" \
    "v_max_u32 v215, v228, v229\n\t" \
    "v_max3_u32 v215, v230, v231, v215\n\t" \
    "v_cmp_eq_u32 vcc, -1, v215\n\t" \
    "s_cbranch_vccz .Ls17_%=\n\t" \
    "buffer_load_dwordx4 v[228:231], v253, s[60:63], s47 offen sc1\n\t" \
    ".Ls17_%=:\n\t" \
    "v_max_u32 v215, v232, v233\n\t" \
    "v_max3_u32 v215, v234, v235, v215\n\t" \
    "v_cmp_eq_u32 vcc, -1, v215\n\t" \
    "s_cbranch_vccz .Ls18_%=\n\t" \
    "buffer_load_dwordx4 v[232:235], v253, s[60:63], s48 offen sc1\n\t" \
    ".Ls18_%=:\n\t" \
    "s_cmp_eq_u32 s41, 0\n\t" \
    "s_cbranch_scc1 .Ls15_%=\n\t" \
    "s_sub_u32 s41, s41, 1\n\t" \
    "s_waitcnt vmcnt(0)\n\t" \
    "s_branch .Ls14_%=\n\t" \
    ".Ls15_%=:\n\t" \
    LST2 \
    "ds_write_b128 v253, v[224:227] offset:43008\n\t" \
    "ds_write_b128 v253, v[228:231] offset:47104\n\t" \
    "ds_write_b128 v253, v[232:235] offset:44032\n\t" \
    "v_smfmac_f32_16x16x64_bf16 v[244:247], v[224:227], v[0:7], v212\n\t" \
    "v_smfmac_f32_16x16x64_bf16 v[248:251], v[224:227], v[8:15], v212\n\t" \
    "v_smfmac_f32_16x16x64_bf16 v[208:211], v[224:227], v[200:207], v212\n\t" \
    "s_waitcnt lgkmcnt(0)\n\t" \
    "s_barrier \n\t" \
    LST3 \
    "ds_read_b128 v[236:239], v253 offset:40960\n\t" \
    "ds_read_b128 v[240:243], v253 offset:46080\n\t" \
    "ds_read_b128 v[224:227], v253 offset:41984\n\t" \
    "ds_read_b128 v[200:203], v254 offset:45056\n\t" \
    "ds_read_b128 v[204:207], v254 offset:46080\n\t" \
    "v_smfmac_f32_16x16x64_bf16 v[208:211], v[228:231], v[16:23], v212\n\t" \
    "v_smfmac_f32_16x16x64_bf16 v[244:247], v[228:231], v[24:31], v212\n\t" \
    "v_smfmac_f32_16x16x64_bf16 v[248:251], v[228:231], v[32:39], v212\n\t" \
    "ds_read_b128 v[228:231], v253 offset:48128\n\t" \
    "v_smfmac_f32_16x16x64_bf16 v[208:211], v[232:235], v[216:223], v212\n\t" \
    "ds_read_b128 v[216:219], v254 offset:47104\n\t" \
    "ds_read_b128 v[220:223], v254 offset:48128\n\t" \
    "v_smfmac_f32_16x16x64_bf16 v[244:247], v[232:235], v[40:47], v212\n\t" \
    "v_smfmac_f32_16x16x64_bf16 v[248:251], v[232:235], v[48:55], v212\n\t" \
    "ds_read_b128 v[232:235], v253 offset:45056\n\t" \
    "s_waitcnt lgkmcnt(8)\n\t" \
    "v_smfmac_f32_16x16x64_bf16 v[208:211], v[236:239], v[56:63], v212\n\t" \
    "v_smfmac_f32_16x16x64_bf16 v[244:247], v[236:239], v[64:71], v212\n\t" \
    "v_smfmac_f32_16x16x64_bf16 v[248:251], v[236:239], v[72:79], v212\n\t" \
    "ds_read_b128 v[236:239], v253 offset:49152\n\t" \
    "s_waitcnt lgkmcnt(5)\n\t" \
    "v_smfmac_f32_16x16x64_bf16 v[208:211], v[240:243], v[200:207], v212\n\t" \
    "ds_read_b128 v[200:203], v254 offset:49152\n\t" \
    "ds_read_b128 v[204:207], v254 offset:50176\n\t" \
    "v_smfmac_f32_16x16x64_bf16 v[244:247], v[240:243], v[80:87], v212\n\t" \
    "v_smfmac_f32_16x16x64_bf16 v[248:251], v[240:243], v[88:95], v212\n\t" \
    "ds_read_b128 v[240:243], v253 offset:50176\n\t" \
    "v_smfmac_f32_16x16x64_bf16 v[208:211], v[224:227], v[96:103], v212\n\t" \
    "v_smfmac_f32_16x16x64_bf16 v[244:247], v[224:227], v[104:111], v212\n\t" \
    "v_smfmac_f32_16x16x64_bf16 v[248:251], v[224:227], v[112:119], v212\n\t" \
    "s_waitcnt lgkmcnt(5)\n\t" \
    "v_smfmac_f32_16x16x64_bf16 v[208:211], v[228:231], v[216:223], v212\n\t" \
    "s_waitcnt lgkmcnt(4)\n\t" \
    "v_smfmac_f32_16x16x64_bf16 v[208:211], v[232:235], v[136:143], v212\n\t" \
    "s_waitcnt lgkmcnt(1)\n\t" \
    "v_smfmac_f32_16x16x64_bf16 v[208:211], v[236:239], v[200:207], v212\n\t" \
    "s_waitcnt lgkmcnt(0)\n\t" \
    "v_smfmac_f32_16x16x64_bf16 v[208:211], v[240:243], v[176:183], v212\n\t" \
    "v_smfmac_f32_16x16x64_bf16 v[244:247], v[228:231], v[120:127], v212\n\t" \
    "v_smfmac_f32_16x16x64_bf16 v[244:247], v[232:235], v[144:151], v212\n\t" \
    "v_smfmac_f32_16x16x64_bf16 v[244:247], v[236:239], v[160:167], v212\n\t" \
    "v_add_f32 v208, v208, v210\n\t" \
    "v_add_f32 v209, v209, v211\n\t" \
    "v_cvt_pk_bf16_f32 v208, v208, v209\n\t" \
    "v_smfmac_f32_16x16x64_bf16 v[244:247], v[240:243], v[184:191], v212\n\t" \
    "ds_write_b16 v213, v208 offset:0\n\t" \
    "ds_write_b16_d16_hi v213, v208 offset:16\n\t" \
    "v_smfmac_f32_16x16x64_bf16 v[248:251], v[228:231], v[128:135], v212\n\t" \
    "v_smfmac_f32_16x16x64_bf16 v[248:251], v[232:235], v[152:159], v212\n\t" \
    "v_smfmac_f32_16x16x64_bf16 v[248:251], v[236:239], v[168:175], v212\n\t" \
    "v_add_f32 v244, v244, v246\n\t" \
    "v_add_f32 v245, v245, v247\n\t" \
    "v_smfmac_f32_16x16x64_bf16 v[248:251], v[240:243], v[192:199], v212\n\t" \
    "s_nop 7\n\t" \
    "v_add_f32 v248, v248, v250\n\t" \
    "v_add_f32 v249, v249, v251\n\t" \
    "v_cvt_pk_bf16_f32 v244, v244, v248\n\t" \
    "v_cvt_pk_bf16_f32 v245, v245, v249\n\t" \
    "ds_write2_b32 v214, v244, v245 offset1:4\n\t" \
    "s_sleep 3\n\t" \
    "ds_read_b128 v[200:203], v254 offset:6144\n\t" \
    "ds_read_b128 v[224:227], v254 offset:18432\n\t" \
    "ds_read_b128 v[204:207], v254 offset:7168\n\t" \
    "ds_read_b128 v[228:231], v254 offset:19456\n\t" \
    "ds_read_b128 v[208:211], v254 offset:8192\n\t" \
    "ds_read_b128 v[232:235], v254 offset:20480\n\t" \
    "ds_read_b128 v[212:215], v254 offset:9216\n\t" \
    "ds_read_b128 v[236:239], v254 offset:21504\n\t" \
    "ds_read_b128 v[216:219], v254 offset:10240\n\t" \
    "ds_read_b128 v[220:223], v254 offset:11264\n\t" \
    "s_and_b32 s44, s40, 3\n\t" \
    "s_lshl_b32 s44, s44, 17\n\t" \
    "s_add_u32 s50, s44, %[pb]\n\t" \
    "s_add_u32 s51, s44, %[pm]\n\t" \
    "s_mov_b32 s52, 0xf0000\n\t" \
    "s_mov_b32 s53, 0xfff00ff\n\t" \
    "s_mov_b32 s54, 0x1f0001\n\t" \
    "s_mov_b32 s55, 0x1fff01ff\n\t" \
    "s_mov_b32 s56, 0x3f0003\n\t" \
    "s_mov_b32 s57, 0x3fff03ff\n\t" \
    "s_mov_b32 s58, 0x7f0007\n\t" \
    "s_mov_b32 s59, 0x7fff07ff\n\t" \
    "s_waitcnt lgkmcnt(0)\n\t" \
    "s_barrier \n\t" \
    LST4 \
    "v_and_b32 v252, 0xffffff7f, v254\n\t" \
    "v_mfma_f32_16x16x32_bf16 v[240:243], v[224:227], v[200:203], 0\n\t" \
    "ds_read_b128 v[224:227], v254 offset:22528\n\t" \
    "v_mfma_f32_16x16x32_bf16 v[240:243], v[228:231], v[204:207], v[240:243]\n\t" \
    "ds_read_b128 v[228:231], v254 offset:23552\n\t" \
    "v_mfma_f32_16x16x32_bf16 v[240:243], v[232:235], v[208:211], v[240:243]\n\t" \
    "ds_read_b128 v[232:235], v252 offset:3072\n\t" \
    "v_mfma_f32_16x16x32_bf16 v[240:243], v[236:239], v[212:215], v[240:243]\n\t" \
    "ds_read_b128 v[236:239], v252 offset:3200\n\t" \
    "s_waitcnt lgkmcnt(3)\n\t" \
    "v_mfma_f32_16x16x32_bf16 v[240:243], v[224:227], v[216:219], v[240:243]\n\t" \
    "ds_read_b128 v[224:227], v252 offset:4096\n\t" \
    "s_waitcnt lgkmcnt(3)\n\t" \
    "v_mfma_f32_16x16x32_bf16 v[240:243], v[228:231], v[220:223], v[240:243]\n\t" \
    "ds_read_b128 v[228:231], v252 offset:4224\n\t" \
    "s_waitcnt lgkmcnt(3)\n\t" \
    "v_mfma_f32_16x16x32_bf16 v[248:251], v[232:235], v[200:203], 0\n\t" \
    "ds_read_b128 v[232:235], v252 offset:5120\n\t" \
    "s_waitcnt lgkmcnt(3)\n\t" \
    "v_mfma_f32_16x16x32_bf16 v[248:251], v[236:239], v[204:207], v[248:251]\n\t" \
    "ds_read_b128 v[236:239], v252 offset:5248\n\t" \
    "s_waitcnt lgkmcnt(3)\n\t" \
    "v_mfma_f32_16x16x32_bf16 v[248:251], v[224:227], v[208:211], v[248:251]\n\t" \
    "s_waitcnt lgkmcnt(2)\n\t" \
    "v_mfma_f32_16x16x32_bf16 v[248:251], v[228:231], v[212:215], v[248:251]\n\t" \
    "s_waitcnt lgkmcnt(1)\n\t" \
    "v_mfma_f32_16x16x32_bf16 v[248:251], v[232:235], v[216:219], v[248:251]\n\t" \
    "s_waitcnt lgkmcnt(0)\n\t" \
    "v_mfma_f32_16x16x32_bf16 v[248:251], v[236:239], v[220:223], v[248:251]\n\t" \
    "v_mov_b32 v226, 0xff7fffff\n\t" \
    "v_cndmask_b32 v240, v240, v226, s[52:53]\n\t" \
    "v_cndmask_b32 v241, v241, v226, s[54:55]\n\t" \
    "v_cndmask_b32 v242, v242, v226, s[56:57]\n\t" \
    "v_cndmask_b32 v243, v243, v226, s[58:59]\n\t" \
    "v_max3_f32 v224, v240, v241, v242\n\t" \
    "v_mov_b32 v225, v243\n\t" \
    "v_lshrrev_b32 v200, 5, v253\n\t" \
    "v_and_b32 v201, 0xf0, v253\n\t" \
    "v_and_or_b32 v200, v200, 8, v201\n\t" \
    "v_max3_f32 v224, v224, v248, v249\n\t" \
    "v_max3_f32 v225, v225, v250, v251\n\t" \
    "v_max_f32 v224, v224, v225\n\t" \
    "v_mov_b32 v225, v224\n\t" \
    "s_nop 1\n\t" \
    "v_permlane16_swap_b32 v224, v225\n\t" \
    "v_max_f32 v224, v224, v225\n\t" \
    "v_mov_b32 v225, v224\n\t" \
    "s_nop 1\n\t" \
    "v_permlane32_swap_b32 v224, v225\n\t" \
    "v_max_f32 v224, v224, v225\n\t" \
    "v_mul_f32 v224, %[scl], v224\n\t" \
    "v_fma_f32 v240, v240, %[scl], -v224\n\t" \
    "v_fma_f32 v241, v241, %[scl], -v224\n\t" \
    "v_exp_f32 v240, v240\n\t" \
    "v_fma_f32 v242, v242, %[scl], -v224\n\t" \
    "v_exp_f32 v241, v241\n\t" \
    "v_fma_f32 v243, v243, %[scl], -v224\n\t" \
    "v_exp_f32 v242, v242\n\t" \
    "v_fma_f32 v248, v248, %[scl], -v224\n\t" \
    "v_exp_f32 v243, v243\n\t" \
    "v_fma_f32 v249, v249, %[scl], -v224\n\t" \
    "v_exp_f32 v248, v248\n\t" \
    "v_fma_f32 v250, v250, %[scl], -v224\n\t" \
    "v_exp_f32 v249, v249\n\t" \
    "v_fma_f32 v251, v251, %[scl], -v224\n\t" \
    "v_exp_f32 v250, v250\n\t" \
    "v_exp_f32 v251, v251\n\t" \
    "v_add_f32 v224, v240, v241\n\t" \
    "v_add_f32 v225, v242, v243\n\t" \
    "v_add_f32 v232, v248, v249\n\t" \
    "v_add_f32 v233, v250, v251\n\t" \
    "v_add_f32 v224, v224, v225\n\t" \
    "v_add_f32 v232, v232, v233\n\t" \
    "v_fma_f32 v224, v232, 0.5, v224\n\t" \
    "v_mov_b32 v225, v224\n\t" \
    "s_nop 1\n\t" \
    "v_permlane16_swap_b32 v224, v225\n\t" \
    "v_add_f32 v224, v224, v225\n\t" \
    "v_mov_b32 v225, v224\n\t" \
    "s_nop 1\n\t" \
    "v_permlane32_swap_b32 v224, v225\n\t" \
    "v_add_f32 v224, v224, v225\n\t" \
    "v_rcp_f32 v224, v224\n\t" \
    "s_nop 0\n\t" \
    "v_mul_f32 v240, v240, v224\n\t" \
    "v_mul_f32 v241, v241, v224\n\t" \
    "v_mul_f32 v242, v242, v224\n\t" \
    "v_mul_f32 v243, v243, v224\n\t" \
    "v_mul_f32 v248, v248, v224\n\t" \
    "v_mul_f32 v249, v249, v224\n\t" \
    "v_mul_f32 v250, v250, v224\n\t" \
    "v_mul_f32 v251, v251, v224\n\t" \
    "v_cvt_pk_bf16_f32 v204, |v240|, |v241|\n\t" \
    "v_cvt_pk_bf16_f32 v205, |v242|, |v243|\n\t" \
    "v_cvt_pk_bf16_f32 v208, |v248|, |v249|\n\t" \
    "v_cvt_pk_bf16_f32 v209, |v250|, |v251|\n\t" \
    "v_permlane16_swap_b32 v204, v206\n\t" \
    "v_permlane16_swap_b32 v205, v207\n\t" \
    "s_mov_b32 exec_lo, 0xffff\n\t" \
    "s_mov_b32 exec_hi, 0xffff\n\t" \
    "buffer_store_dwordx4 v[204:207], v253, s[60:63], s50 offen sc1\n\t" \
    "s_mov_b32 exec_lo, -1\n\t" \
    "s_mov_b32 exec_hi, 0\n\t" \
    "buffer_store_dwordx2 v[208:209], v200, s[60:63], s51 offen sc1\n\t" \
    "s_mov_b32 exec_hi, -1\n\t" \
    LST5 \
    "ds_read_b128 v[224:227], v253 offset:0\n\t" \
    "ds_read_b128 v[244:247], v254 offset:40960\n\t" \
    "ds_read_b128 v[228:231], v253 offset:1024\n\t" \
    "ds_read_b128 v[232:235], v253 offset:20480\n\t" \
    "ds_read_b128 v[236:239], v253 offset:21504\n\t" \
    "ds_read_b128 v[248:251], v254 offset:41984\n\t" \
    "s_add_u32 s40, s40, 1\n\t" \
    "s_cmp_lg_u32 s40, 64\n\t" \
    "s_cbranch_scc1 .Ls13_%=\n\t" \
    "s_branch .Lend%=\n\t" \
    ".Ls0_%=:\n\t" \
    "s_add_u32 m0, %[wm], 0x18000\n\t" \
    "s_mov_b32 s44, 0x0\n\t" \
    "buffer_load_dwordx4 v240, %[wr], s44 offen offset:0 nt lds\n\t" \
    "buffer_load_dwordx4 v241, %[wr], s44 offen offset:1024 nt lds\n\t" \
    "buffer_load_dwordx4 v242, %[wr], s44 offen offset:2048 nt lds\n\t" \
    "s_add_u32 m0, %[wm], 0x1e000\n\t" \
    "s_mov_b32 s44, 0x60000\n\t" \
    "buffer_load_dwordx4 v240, %[wr], s44 offen offset:0 nt lds\n\t" \
    "buffer_load_dwordx4 v241, %[wr], s44 offen offset:1024 nt lds\n\t" \
    "buffer_load_dwordx4 v242, %[wr], s44 offen offset:2048 nt lds\n\t" \
    "s_add_u32 m0, %[wm], 0x0\n\t" \
    "s_mov_b32 s44, 0xc0000\n\t" \
    "buffer_load_dwordx4 v240, %[wr], s44 offen offset:0 nt lds\n\t" \
    "buffer_load_dwordx4 v241, %[wr], s44 offen offset:1024 nt lds\n\t" \
    "buffer_load_dwordx4 v242, %[wr], s44 offen offset:2048 nt lds\n\t" \
    "s_add_u32 m0, %[wm], 0x6000\n\t" \
    "s_mov_b32 s44, 0x120000\n\t" \
    "buffer_load_dwordx4 v240, %[wr], s44 offen offset:0 nt lds\n\t" \
    "buffer_load_dwordx4 v241, %[wr], s44 offen offset:1024 nt lds\n\t" \
    "buffer_load_dwordx4 v242, %[wr], s44 offen offset:2048 nt lds\n\t" \
    "s_add_u32 m0, %[wm], 0xc000\n\t" \
    "s_mov_b32 s44, 0x180000\n\t" \
    "buffer_load_dwordx4 v240, %[wr], s44 offen offset:0 nt lds\n\t" \
    "buffer_load_dwordx4 v241, %[wr], s44 offen offset:1024 nt lds\n\t" \
    "buffer_load_dwordx4 v242, %[wr], s44 offen offset:2048 nt lds\n\t" \
    "v_add_u32 v244, 0x18000, v243\n\t" \
    "s_waitcnt vmcnt(12) lgkmcnt(0)\n\t" \
    "s_barrier \n\t" \
    "s_add_u32 m0, %[wm], 0x12000\n\t" \
    "s_mov_b32 s44, 0x1e0000\n\t" \
    "buffer_load_dwordx4 v240, %[wr], s44 offen offset:0 nt lds\n\t" \
    "buffer_load_dwordx4 v241, %[wr], s44 offen offset:1024 nt lds\n\t" \
    "buffer_load_dwordx4 v242, %[wr], s44 offen offset:2048 nt lds\n\t" \
    "ds_read_b64_tr_b16 v[200:201], v244 offset:0\n\t" \
    "ds_read_b64_tr_b16 v[202:203], v244 offset:1536\n\t" \
    "ds_read_b64_tr_b16 v[0:1], v244 offset:32\n\t" \
    "ds_read_b64_tr_b16 v[2:3], v244 offset:1568\n\t" \
    "ds_read_b64_tr_b16 v[8:9], v244 offset:64\n\t" \
    "ds_read_b64_tr_b16 v[10:11], v244 offset:1600\n\t" \
    "v_add_u32 v244, 0x1e000, v243\n\t" \
    "s_waitcnt vmcnt(12) lgkmcnt(0)\n\t" \
    "s_barrier \n\t" \
    "s_add_u32 m0, %[wm], 0x18000\n\t" \
    "s_mov_b32 s44, 0x240000\n\t" \
    "buffer_load_dwordx4 v240, %[wr], s44 offen offset:0 nt lds\n\t" \
    "buffer_load_dwordx4 v241, %[wr], s44 offen offset:1024 nt lds\n\t" \
    "buffer_load_dwordx4 v242, %[wr], s44 offen offset:2048 nt lds\n\t" \
    "ds_read_b64_tr_b16 v[204:205], v244 offset:0\n\t" \
    "ds_read_b64_tr_b16 v[206:207], v244 offset:1536\n\t" \
    "ds_read_b64_tr_b16 v[4:5], v244 offset:32\n\t" \
    "ds_read_b64_tr_b16 v[6:7], v244 offset:1568\n\t" \
    "ds_read_b64_tr_b16 v[12:13], v244 offset:64\n\t" \
    "ds_read_b64_tr_b16 v[14:15], v244 offset:1600\n\t" \
    "v_add_u32 v244, 0x0, v243\n\t" \
    "s_waitcnt vmcnt(12) lgkmcnt(0)\n\t" \
    "s_barrier \n\t" \
    "s_add_u32 m0, %[wm], 0x1e000\n\t" \
    "s_mov_b32 s44, 0x2a0000\n\t" \
    "buffer_load_dwordx4 v240, %[wr], s44 offen offset:0 nt lds\n\t" \
    "buffer_load_dwordx4 v241, %[wr], s44 offen offset:1024 nt lds\n\t" \
    "buffer_load_dwordx4 v242, %[wr], s44 offen offset:2048 nt lds\n\t" \
    "ds_read_b64_tr_b16 v[208:209], v244 offset:0\n\t" \
    "ds_read_b64_tr_b16 v[210:211], v244 offset:1536\n\t" \
    "ds_read_b64_tr_b16 v[40:41], v244 offset:32\n\t" \
    "ds_read_b64_tr_b16 v[42:43], v244 offset:1568\n\t" \
    "ds_read_b64_tr_b16 v[48:49], v244 offset:64\n\t" \
    "ds_read_b64_tr_b16 v[50:51], v244 offset:1600\n\t" \
    "v_add_u32 v244, 0x6000, v243\n\t" \
    "s_waitcnt vmcnt(12) lgkmcnt(0)\n\t" \
    "s_barrier \n\t" \
    "s_add_u32 m0, %[wm], 0x0\n\t" \
    "s_mov_b32 s44, 0x300000\n\t" \
    "buffer_load_dwordx4 v240, %[wr], s44 offen offset:0 nt lds\n\t" \
    "buffer_load_dwordx4 v241, %[wr], s44 offen offset:1024 nt lds\n\t" \
    "buffer_load_dwordx4 v242, %[wr], s44 offen offset:2048 nt lds\n\t" \
    "ds_read_b64_tr_b16 v[212:213], v244 offset:0\n\t" \
    "ds_read_b64_tr_b16 v[214:215], v244 offset:1536\n\t" \
    "ds_read_b64_tr_b16 v[44:45], v244 offset:32\n\t" \
    "ds_read_b64_tr_b16 v[46:47], v244 offset:1568\n\t" \
    "ds_read_b64_tr_b16 v[52:53], v244 offset:64\n\t" \
    "ds_read_b64_tr_b16 v[54:55], v244 offset:1600\n\t" \
    "v_add_u32 v244, 0xc000, v243\n\t" \
    "s_waitcnt vmcnt(12) lgkmcnt(0)\n\t" \
    "s_barrier \n\t" \
    "s_add_u32 m0, %[wm], 0x6000\n\t" \
    "s_mov_b32 s44, 0x360000\n\t" \
    "buffer_load_dwordx4 v240, %[wr], s44 offen offset:0 nt lds\n\t" \
    "buffer_load_dwordx4 v241, %[wr], s44 offen offset:1024 nt lds\n\t" \
    "buffer_load_dwordx4 v242, %[wr], s44 offen offset:2048 nt lds\n\t" \
    "ds_read_b64_tr_b16 v[216:217], v244 offset:0\n\t" \
    "ds_read_b64_tr_b16 v[218:219], v244 offset:1536\n\t" \
    "ds_read_b64_tr_b16 v[80:81], v244 offset:32\n\t" \
    "ds_read_b64_tr_b16 v[82:83], v244 offset:1568\n\t" \
    "ds_read_b64_tr_b16 v[88:89], v244 offset:64\n\t" \
    "ds_read_b64_tr_b16 v[90:91], v244 offset:1600\n\t" \
    "v_add_u32 v244, 0x12000, v243\n\t" \
    "s_waitcnt vmcnt(12) lgkmcnt(0)\n\t" \
    "s_barrier \n\t" \
    "s_add_u32 m0, %[wm], 0xc000\n\t" \
    "s_mov_b32 s44, 0x3c0000\n\t" \
    "buffer_load_dwordx4 v240, %[wr], s44 offen offset:0 nt lds\n\t" \
    "buffer_load_dwordx4 v241, %[wr], s44 offen offset:1024 nt lds\n\t" \
    "buffer_load_dwordx4 v242, %[wr], s44 offen offset:2048 nt lds\n\t" \
    "ds_read_b64_tr_b16 v[220:221], v244 offset:0\n\t" \
    "ds_read_b64_tr_b16 v[222:223], v244 offset:1536\n\t" \
    "ds_read_b64_tr_b16 v[84:85], v244 offset:32\n\t" \
    "ds_read_b64_tr_b16 v[86:87], v244 offset:1568\n\t" \
    "ds_read_b64_tr_b16 v[92:93], v244 offset:64\n\t" \
    "ds_read_b64_tr_b16 v[94:95], v244 offset:1600\n\t" \
    "v_add_u32 v244, 0x18000, v243\n\t" \
    "s_waitcnt vmcnt(12) lgkmcnt(0)\n\t" \
    "s_barrier \n\t" \
    "v_mov_b32 v246, 0\n\t" \
    "buffer_load_dword v246, v246, s[60:63], %[mo] offen sc1\n\t" \
    "s_add_u32 m0, %[wm], 0x12000\n\t" \
    "s_mov_b32 s44, 0x420000\n\t" \
    "buffer_load_dwordx4 v240, %[wr], s44 offen offset:0 nt lds\n\t" \
    "buffer_load_dwordx4 v241, %[wr], s44 offen offset:1024 nt lds\n\t" \
    "buffer_load_dwordx4 v242, %[wr], s44 offen offset:2048 nt lds\n\t" \
    "ds_read_b64_tr_b16 v[224:225], v244 offset:0\n\t" \
    "ds_read_b64_tr_b16 v[226:227], v244 offset:1536\n\t" \
    "ds_read_b64_tr_b16 v[120:121], v244 offset:32\n\t" \
    "ds_read_b64_tr_b16 v[122:123], v244 offset:1568\n\t" \
    "ds_read_b64_tr_b16 v[128:129], v244 offset:64\n\t" \
    "ds_read_b64_tr_b16 v[130:131], v244 offset:1600\n\t" \
    "v_add_u32 v244, 0x1e000, v243\n\t" \
    "s_waitcnt vmcnt(12) lgkmcnt(0)\n\t" \
    "s_barrier \n\t" \
    "s_add_u32 m0, %[wm], 0x18000\n\t" \
    "s_mov_b32 s44, 0x480000\n\t" \
    "buffer_load_dwordx4 v240, %[wr], s44 offen offset:0 nt lds\n\t" \
    "buffer_load_dwordx4 v241, %[wr], s44 offen offset:1024 nt lds\n\t" \
    "buffer_load_dwordx4 v242, %[wr], s44 offen offset:2048 nt lds\n\t" \
    "ds_read_b64_tr_b16 v[228:229], v244 offset:0\n\t" \
    "ds_read_b64_tr_b16 v[230:231], v244 offset:1536\n\t" \
    "ds_read_b64_tr_b16 v[124:125], v244 offset:32\n\t" \
    "ds_read_b64_tr_b16 v[126:127], v244 offset:1568\n\t" \
    "ds_read_b64_tr_b16 v[132:133], v244 offset:64\n\t" \
    "ds_read_b64_tr_b16 v[134:135], v244 offset:1600\n\t" \
    "v_add_u32 v244, 0x0, v243\n\t" \
    "s_waitcnt vmcnt(12) lgkmcnt(0)\n\t" \
    "s_barrier \n\t" \
    "s_add_u32 m0, %[wm], 0x1e000\n\t" \
    "s_mov_b32 s44, 0x4e0000\n\t" \
    "buffer_load_dwordx4 v240, %[wr], s44 offen offset:0 nt lds\n\t" \
    "buffer_load_dwordx4 v241, %[wr], s44 offen offset:1024 nt lds\n\t" \
    "buffer_load_dwordx4 v242, %[wr], s44 offen offset:2048 nt lds\n\t" \
    "ds_read_b64_tr_b16 v[232:233], v244 offset:0\n\t" \
    "ds_read_b64_tr_b16 v[234:235], v244 offset:1536\n\t" \
    "ds_read_b64_tr_b16 v[160:161], v244 offset:32\n\t" \
    "ds_read_b64_tr_b16 v[162:163], v244 offset:1568\n\t" \
    "ds_read_b64_tr_b16 v[168:169], v244 offset:64\n\t" \
    "ds_read_b64_tr_b16 v[170:171], v244 offset:1600\n\t" \
    "v_add_u32 v244, 0x6000, v243\n\t" \
    "s_waitcnt vmcnt(12) lgkmcnt(0)\n\t" \
    "s_barrier \n\t" \
    "s_add_u32 m0, %[wm], 0x0\n\t" \
    "s_mov_b32 s44, 0x540000\n\t" \
    "buffer_load_dwordx4 v240, %[wr], s44 offen offset:0 nt lds\n\t" \
    "buffer_load_dwordx4 v241, %[wr], s44 offen offset:1024 nt lds\n\t" \
    "buffer_load_dwordx4 v242, %[wr], s44 offen offset:2048 nt lds\n\t" \
    "ds_read_b64_tr_b16 v[236:237], v244 offset:0\n\t" \
    "ds_read_b64_tr_b16 v[238:239], v244 offset:1536\n\t" \
    "ds_read_b64_tr_b16 v[164:165], v244 offset:32\n\t" \
    "ds_read_b64_tr_b16 v[166:167], v244 offset:1568\n\t" \
    "ds_read_b64_tr_b16 v[172:173], v244 offset:64\n\t" \
    "ds_read_b64_tr_b16 v[174:175], v244 offset:1600\n\t" \
    "v_add_u32 v244, 0xc000, v243\n\t" \
    "s_waitcnt vmcnt(12) lgkmcnt(0)\n\t" \
    "s_barrier \n\t" \
    "s_add_u32 m0, %[wm], 0x6000\n\t" \
    "s_mov_b32 s44, 0x5a0000\n\t" \
    "buffer_load_dwordx4 v240, %[wr], s44 offen offset:0 nt lds\n\t" \
    "buffer_load_dwordx4 v241, %[wr], s44 offen offset:1024 nt lds\n\t" \
    "buffer_load_dwordx4 v242, %[wr], s44 offen offset:2048 nt lds\n\t" \
    "ds_read_b64_tr_b16 v[16:17], v244 offset:0\n\t" \
    "ds_read_b64_tr_b16 v[18:19], v244 offset:1536\n\t" \
    "ds_read_b64_tr_b16 v[24:25], v244 offset:32\n\t" \
    "ds_read_b64_tr_b16 v[26:27], v244 offset:1568\n\t" \
    "ds_read_b64_tr_b16 v[32:33], v244 offset:64\n\t" \
    "ds_read_b64_tr_b16 v[34:35], v244 offset:1600\n\t" \
    "v_add_u32 v244, 0x12000, v243\n\t" \
    "s_waitcnt vmcnt(12) lgkmcnt(0)\n\t" \
    "s_barrier \n\t" \
    ".Ls01_%=:\n\t" \
    "v_readfirstlane_b32 s43, v246\n\t" \
    "s_nop 0\n\t" \
    "s_cmp_lg_u32 s43, -1\n\t" \
    "s_cbranch_scc1 .Ls02_%=\n\t" \
    "s_cmp_eq_u32 s41, 0\n\t" \
    "s_cbranch_scc1 .Ls02_%=\n\t" \
    "s_sub_u32 s41, s41, 1\n\t" \
    "s_sleep 4\n\t" \
    "v_mov_b32 v246, 0\n\t" \
    "buffer_load_dword v246, v246, s[60:63], %[mo] offen sc1\n\t" \
    "s_waitcnt vmcnt(0)\n\t" \
    "s_branch .Ls01_%=\n\t" \
    ".Ls02_%=:\n\t" \
    "v_mov_b32 v246, 0x8000\n\t" \
    "s_mov_b64 exec, 1\n\t" \
    "buffer_store_dword v246, v246, s[60:63], %[mo] offen sc1\n\t" \
    "s_mov_b64 exec, -1\n\t" \
    "s_and_b32 s43, s43, 0x2000\n\t" \
    "s_xor_b32 s43, s43, %[lx]\n\t" \
    "s_add_u32 s60, s60, s43\n\t" \
    "s_addc_u32 s61, s61, 0\n\t" \
    "s_add_u32 m0, %[wm], 0xc000\n\t" \
    "s_mov_b32 s44, 0x600000\n\t" \
    "buffer_load_dwordx4 v240, %[wr], s44 offen offset:0 nt lds\n\t" \
    "buffer_load_dwordx4 v241, %[wr], s44 offen offset:1024 nt lds\n\t" \
    "buffer_load_dwordx4 v242, %[wr], s44 offen offset:2048 nt lds\n\t" \
    "ds_read_b64_tr_b16 v[20:21], v244 offset:0\n\t" \
    "ds_read_b64_tr_b16 v[22:23], v244 offset:1536\n\t" \
    "ds_read_b64_tr_b16 v[28:29], v244 offset:32\n\t" \
    "ds_read_b64_tr_b16 v[30:31], v244 offset:1568\n\t" \
    "ds_read_b64_tr_b16 v[36:37], v244 offset:64\n\t" \
    "ds_read_b64_tr_b16 v[38:39], v244 offset:1600\n\t" \
    "v_add_u32 v244, 0x18000, v243\n\t" \
    "s_waitcnt vmcnt(12) lgkmcnt(0)\n\t" \
    "s_barrier \n\t" \
    "s_add_u32 m0, %[wm], 0x12000\n\t" \
    "s_mov_b32 s44, 0x660000\n\t" \
    "buffer_load_dwordx4 v240, %[wr], s44 offen offset:0 nt lds\n\t" \
    "buffer_load_dwordx4 v241, %[wr], s44 offen offset:1024 nt lds\n\t" \
    "buffer_load_dwordx4 v242, %[wr], s44 offen offset:2048 nt lds\n\t" \
    "ds_read_b64_tr_b16 v[56:57], v244 offset:0\n\t" \
    "ds_read_b64_tr_b16 v[58:59], v244 offset:1536\n\t" \
    "ds_read_b64_tr_b16 v[64:65], v244 offset:32\n\t" \
    "ds_read_b64_tr_b16 v[66:67], v244 offset:1568\n\t" \
    "ds_read_b64_tr_b16 v[72:73], v244 offset:64\n\t" \
    "ds_read_b64_tr_b16 v[74:75], v244 offset:1600\n\t" \
    "v_add_u32 v244, 0x1e000, v243\n\t" \
    "s_waitcnt vmcnt(12) lgkmcnt(0)\n\t" \
    "s_barrier \n\t" \
    "s_add_u32 m0, %[wm], 0x18000\n\t" \
    "s_mov_b32 s44, 0x6c0000\n\t" \
    "buffer_load_dwordx4 v240, %[wr], s44 offen offset:0 nt lds\n\t" \
    "buffer_load_dwordx4 v241, %[wr], s44 offen offset:1024 nt lds\n\t" \
    "buffer_load_dwordx4 v242, %[wr], s44 offen offset:2048 nt lds\n\t" \
    "ds_read_b64_tr_b16 v[60:61], v244 offset:0\n\t" \
    "ds_read_b64_tr_b16 v[62:63], v244 offset:1536\n\t" \
    "ds_read_b64_tr_b16 v[68:69], v244 offset:32\n\t" \
    "ds_read_b64_tr_b16 v[70:71], v244 offset:1568\n\t" \
    "ds_read_b64_tr_b16 v[76:77], v244 offset:64\n\t" \
    "ds_read_b64_tr_b16 v[78:79], v244 offset:1600\n\t" \
    "v_add_u32 v244, 0x0, v243\n\t" \
    "s_waitcnt vmcnt(12) lgkmcnt(0)\n\t" \
    "s_barrier \n\t" \
    "s_add_u32 m0, %[wm], 0x1e000\n\t" \
    "s_mov_b32 s44, 0x720000\n\t" \
    "buffer_load_dwordx4 v240, %[wr], s44 offen offset:0 nt lds\n\t" \
    "buffer_load_dwordx4 v241, %[wr], s44 offen offset:1024 nt lds\n\t" \
    "buffer_load_dwordx4 v242, %[wr], s44 offen offset:2048 nt lds\n\t" \
    "ds_read_b64_tr_b16 v[96:97], v244 offset:0\n\t" \
    "ds_read_b64_tr_b16 v[98:99], v244 offset:1536\n\t" \
    "ds_read_b64_tr_b16 v[104:105], v244 offset:32\n\t" \
    "ds_read_b64_tr_b16 v[106:107], v244 offset:1568\n\t" \
    "ds_read_b64_tr_b16 v[112:113], v244 offset:64\n\t" \
    "ds_read_b64_tr_b16 v[114:115], v244 offset:1600\n\t" \
    "v_add_u32 v244, 0x6000, v243\n\t" \
    "s_waitcnt vmcnt(12) lgkmcnt(0)\n\t" \
    "s_barrier \n\t" \
    "ds_read_b64_tr_b16 v[100:101], v244 offset:0\n\t" \
    "ds_read_b64_tr_b16 v[102:103], v244 offset:1536\n\t" \
    "ds_read_b64_tr_b16 v[108:109], v244 offset:32\n\t" \
    "ds_read_b64_tr_b16 v[110:111], v244 offset:1568\n\t" \
    "ds_read_b64_tr_b16 v[116:117], v244 offset:64\n\t" \
    "ds_read_b64_tr_b16 v[118:119], v244 offset:1600\n\t" \
    "v_add_u32 v244, 0xc000, v243\n\t" \
    "s_waitcnt vmcnt(9) lgkmcnt(0)\n\t" \
    "s_barrier \n\t" \
    "ds_read_b64_tr_b16 v[136:137], v244 offset:0\n\t" \
    "ds_read_b64_tr_b16 v[138:139], v244 offset:1536\n\t" \
    "ds_read_b64_tr_b16 v[144:145], v244 offset:32\n\t" \
    "ds_read_b64_tr_b16 v[146:147], v244 offset:1568\n\t" \
    "ds_read_b64_tr_b16 v[152:153], v244 offset:64\n\t" \
    "ds_read_b64_tr_b16 v[154:155], v244 offset:1600\n\t" \
    "v_add_u32 v244, 0x12000, v243\n\t" \
    "s_waitcnt vmcnt(6) lgkmcnt(0)\n\t" \
    "s_barrier \n\t" \
    "ds_read_b64_tr_b16 v[140:141], v244 offset:0\n\t" \
    "ds_read_b64_tr_b16 v[142:143], v244 offset:1536\n\t" \
    "ds_read_b64_tr_b16 v[148:149], v244 offset:32\n\t" \
    "ds_read_b64_tr_b16 v[150:151], v244 offset:1568\n\t" \
    "ds_read_b64_tr_b16 v[156:157], v244 offset:64\n\t" \
    "ds_read_b64_tr_b16 v[158:159], v244 offset:1600\n\t" \
    "v_add_u32 v244, 0x18000, v243\n\t" \
    "s_waitcnt vmcnt(3) lgkmcnt(0)\n\t" \
    "s_barrier \n\t" \
    "ds_read_b64_tr_b16 v[176:177], v244 offset:0\n\t" \
    "ds_read_b64_tr_b16 v[178:179], v244 offset:1536\n\t" \
    "ds_read_b64_tr_b16 v[184:185], v244 offset:32\n\t" \
    "ds_read_b64_tr_b16 v[186:187], v244 offset:1568\n\t" \
    "ds_read_b64_tr_b16 v[192:193], v244 offset:64\n\t" \
    "ds_read_b64_tr_b16 v[194:195], v244 offset:1600\n\t" \
    "v_add_u32 v244, 0x1e000, v243\n\t" \
    "s_waitcnt vmcnt(0) lgkmcnt(0)\n\t" \
    "s_barrier \n\t" \
    "ds_read_b64_tr_b16 v[180:181], v244 offset:0\n\t" \
    "ds_read_b64_tr_b16 v[182:183], v244 offset:1536\n\t" \
    "ds_read_b64_tr_b16 v[188:189], v244 offset:32\n\t" \
    "ds_read_b64_tr_b16 v[190:191], v244 offset:1568\n\t" \
    "ds_read_b64_tr_b16 v[196:197], v244 offset:64\n\t" \
    "ds_read_b64_tr_b16 v[198:199], v244 offset:1600\n\t" \
    "s_waitcnt lgkmcnt(0)\n\t" \
    "s_barrier \n\t" \
    LST0 \
    "ds_write_b128 v254, v[200:203] offset:30720\n\t" \
    "ds_write_b128 v254, v[204:207] offset:31744\n\t" \
    "ds_write_b128 v254, v[208:211] offset:32768\n\t" \
    "ds_write_b128 v254, v[212:215] offset:33792\n\t" \
    "ds_write_b128 v254, v[216:219] offset:34816\n\t" \
    "ds_write_b128 v254, v[220:223] offset:35840\n\t" \
    "ds_write_b128 v254, v[224:227] offset:36864\n\t" \
    "ds_write_b128 v254, v[228:231] offset:37888\n\t" \
    "ds_write_b128 v254, v[232:235] offset:38912\n\t" \
    "ds_write_b128 v254, v[236:239] offset:39936\n\t" \
    "s_waitcnt vmcnt(0) lgkmcnt(0)\n\t" \
    "s_barrier \n\t" \
    "ds_read_b128 v[224:227], v253 offset:0\n\t" \
    "ds_read_b128 v[228:231], v253 offset:1024\n\t" \
    "ds_read_b128 v[232:235], v253 offset:20480\n\t" \
    "ds_read_b128 v[236:239], v253 offset:21504\n\t" \
    ".Ls03_%=:\n\t" \
    "s_waitcnt lgkmcnt(3)\n\t" \
    "v_mfma_f32_16x16x32_bf16 v[204:207], v[0:3], v[224:227], 0\n\t" \
    "s_waitcnt lgkmcnt(2)\n\t" \
    "v_mfma_f32_16x16x32_bf16 v[204:207], v[4:7], v[228:231], v[204:207]\n\t" \
    "ds_read_b128 v[240:243], v253 offset:2048\n\t" \
    "v_mfma_f32_16x16x32_bf16 v[208:211], v[8:11], v[224:227], 0\n\t" \
    "v_mfma_f32_16x16x32_bf16 v[208:211], v[12:15], v[228:231], v[208:211]\n\t" \
    "s_waitcnt lgkmcnt(2)\n\t" \
    "v_mfma_f32_16x16x32_bf16 v[216:219], v[0:3], v[232:235], 0\n\t" \
    "ds_read_b128 v[224:227], v253 offset:3072\n\t" \
    "s_waitcnt lgkmcnt(2)\n\t" \
    "v_mfma_f32_16x16x32_bf16 v[216:219], v[4:7], v[236:239], v[216:219]\n\t" \
    "ds_read_b128 v[228:231], v253 offset:22528\n\t" \
    "v_mfma_f32_16x16x32_bf16 v[220:223], v[8:11], v[232:235], 0\n\t" \
    "v_mfma_f32_16x16x32_bf16 v[220:223], v[12:15], v[236:239], v[220:223]\n\t" \
    "s_waitcnt lgkmcnt(2)\n\t" \
    "v_mfma_f32_16x16x32_bf16 v[204:207], v[40:43], v[240:243], v[204:207]\n\t" \
    "ds_read_b128 v[232:235], v253 offset:23552\n\t" \
    "s_waitcnt lgkmcnt(2)\n\t" \
    "v_mfma_f32_16x16x32_bf16 v[204:207], v[44:47], v[224:227], v[204:207]\n\t" \
    "ds_read_b128 v[236:239], v253 offset:4096\n\t" \
    "v_mfma_f32_16x16x32_bf16 v[208:211], v[48:51], v[240:243], v[208:211]\n\t" \
    "v_mfma_f32_16x16x32_bf16 v[208:211], v[52:55], v[224:227], v[208:211]\n\t" \
    "s_waitcnt lgkmcnt(2)\n\t" \
    "v_mfma_f32_16x16x32_bf16 v[216:219], v[40:43], v[228:231], v[216:219]\n\t" \
    "ds_read_b128 v[240:243], v253 offset:5120\n\t" \
    "s_waitcnt lgkmcnt(2)\n\t" \
    "v_mfma_f32_16x16x32_bf16 v[216:219], v[44:47], v[232:235], v[216:219]\n\t" \
    "ds_read_b128 v[224:227], v253 offset:24576\n\t" \
    "v_mfma_f32_16x16x32_bf16 v[220:223], v[48:51], v[228:231], v[220:223]\n\t" \
    "v_mfma_f32_16x16x32_bf16 v[220:223], v[52:55], v[232:235], v[220:223]\n\t" \
    "s_waitcnt lgkmcnt(2)\n\t" \
    "v_mfma_f32_16x16x32_bf16 v[204:207], v[80:83], v[236:239], v[204:207]\n\t" \
    "ds_read_b128 v[228:231], v253 offset:25600\n\t" \
    "s_waitcnt lgkmcnt(2)\n\t" \
    "v_mfma_f32_16x16x32_bf16 v[204:207], v[84:87], v[240:243], v[204:207]\n\t" \
    "ds_read_b128 v[232:235], v253 offset:6144\n\t" \
    "v_mfma_f32_16x16x32_bf16 v[208:211], v[88:91], v[236:239], v[208:211]\n\t" \
    "v_mfma_f32_16x16x32_bf16 v[208:211], v[92:95], v[240:243], v[208:211]\n\t" \
    "s_waitcnt lgkmcnt(2)\n\t" \
    "v_mfma_f32_16x16x32_bf16 v[216:219], v[80:83], v[224:227], v[216:219]\n\t" \
    "ds_read_b128 v[236:239], v253 offset:7168\n\t" \
    "s_waitcnt lgkmcnt(2)\n\t" \
    "v_mfma_f32_16x16x32_bf16 v[216:219], v[84:87], v[228:231], v[216:219]\n\t" \
    "ds_read_b128 v[240:243], v253 offset:26624\n\t" \
    "v_mfma_f32_16x16x32_bf16 v[220:223], v[88:91], v[224:227], v[220:223]\n\t" \
    "v_mfma_f32_16x16x32_bf16 v[220:223], v[92:95], v[228:231], v[220:223]\n\t" \
    "s_waitcnt lgkmcnt(2)\n\t" \
    "v_mfma_f32_16x16x32_bf16 v[204:207], v[120:123], v[232:235], v[204:207]\n\t" \
    "ds_read_b128 v[224:227], v253 offset:27648\n\t" \
    "s_waitcnt lgkmcnt(2)\n\t" \
    "v_mfma_f32_16x16x32_bf16 v[204:207], v[124:127], v[236:239], v[204:207]\n\t" \
    "ds_read_b128 v[228:231], v253 offset:8192\n\t" \
    "v_mfma_f32_16x16x32_bf16 v[208:211], v[128:131], v[232:235], v[208:211]\n\t" \
    "v_mfma_f32_16x16x32_bf16 v[208:211], v[132:135], v[236:239], v[208:211]\n\t" \
    "s_waitcnt lgkmcnt(2)\n\t" \
    "v_mfma_f32_16x16x32_bf16 v[216:219], v[120:123], v[240:243], v[216:219]\n\t" \
    "ds_read_b128 v[232:235], v253 offset:9216\n\t" \
    "s_waitcnt lgkmcnt(2)\n\t" \
    "v_mfma_f32_16x16x32_bf16 v[216:219], v[124:127], v[224:227], v[216:219]\n\t" \
    "ds_read_b128 v[236:239], v253 offset:28672\n\t" \
    "v_mfma_f32_16x16x32_bf16 v[220:223], v[128:131], v[240:243], v[220:223]\n\t" \
    "v_mfma_f32_16x16x32_bf16 v[220:223], v[132:135], v[224:227], v[220:223]\n\t" \
    "s_waitcnt lgkmcnt(2)\n\t" \
    "v_mfma_f32_16x16x32_bf16 v[204:207], v[160:163], v[228:231], v[204:207]\n\t" \
    "ds_read_b128 v[240:243], v253 offset:29696\n\t" \
    "s_waitcnt lgkmcnt(2)\n\t" \
    "v_mfma_f32_16x16x32_bf16 v[204:207], v[164:167], v[232:235], v[204:207]\n\t" \
    "ds_read_b128 v[224:227], v253 offset:10240\n\t" \
    "v_mfma_f32_16x16x32_bf16 v[208:211], v[168:171], v[228:231], v[208:211]\n\t" \
    "v_mfma_f32_16x16x32_bf16 v[208:211], v[172:175], v[232:235], v[208:211]\n\t" \
    "s_waitcnt lgkmcnt(2)\n\t" \
    "v_mfma_f32_16x16x32_bf16 v[216:219], v[160:163], v[236:239], v[216:219]\n\t" \
    "ds_read_b128 v[228:231], v253 offset:11264\n\t" \
    "s_waitcnt lgkmcnt(2)\n\t" \
    "v_mfma_f32_16x16x32_bf16 v[216:219], v[164:167], v[240:243], v[216:219]\n\t" \
    "ds_read_b128 v[232:235], v253 offset:30720\n\t" \
    "v_mfma_f32_16x16x32_bf16 v[220:223], v[168:171], v[236:239], v[220:223]\n\t" \
    "v_mfma_f32_16x16x32_bf16 v[220:223], v[172:175], v[240:243], v[220:223]\n\t" \
    "ds_read_b128 v[236:239], v253 offset:31744\n\t" \
    "s_waitcnt lgkmcnt(0)\n\t" \
    "s_barrier \n\t" \
    LST7 \
    "ds_read_b128 v[200:203], v253 offset:40960\n\t" \
    "ds_read_b128 v[212:215], v253 offset:41984\n\t" \
    "v_mfma_f32_16x16x32_bf16 v[204:207], v[24:27], v[224:227], v[204:207]\n\t" \
    "v_mfma_f32_16x16x32_bf16 v[204:207], v[28:31], v[228:231], v[204:207]\n\t" \
    "ds_read_b128 v[240:243], v253 offset:12288\n\t" \
    "v_mfma_f32_16x16x32_bf16 v[208:211], v[32:35], v[224:227], v[208:211]\n\t" \
    "v_mfma_f32_16x16x32_bf16 v[208:211], v[36:39], v[228:231], v[208:211]\n\t" \
    "s_waitcnt lgkmcnt(2)\n\t" \
    "v_mfma_f32_16x16x32_bf16 v[200:203], v[16:19], v[224:227], v[200:203]\n\t" \
    "v_mfma_f32_16x16x32_bf16 v[200:203], v[20:23], v[228:231], v[200:203]\n\t" \
    "v_mfma_f32_16x16x32_bf16 v[216:219], v[24:27], v[232:235], v[216:219]\n\t" \
    "ds_read_b128 v[224:227], v253 offset:13312\n\t" \
    "v_mfma_f32_16x16x32_bf16 v[216:219], v[28:31], v[236:239], v[216:219]\n\t" \
    "ds_read_b128 v[228:231], v253 offset:32768\n\t" \
    "v_mfma_f32_16x16x32_bf16 v[220:223], v[32:35], v[232:235], v[220:223]\n\t" \
    "v_mfma_f32_16x16x32_bf16 v[220:223], v[36:39], v[236:239], v[220:223]\n\t" \
    "s_waitcnt lgkmcnt(3)\n\t" \
    "v_mfma_f32_16x16x32_bf16 v[212:215], v[16:19], v[232:235], v[212:215]\n\t" \
    "v_mfma_f32_16x16x32_bf16 v[212:215], v[20:23], v[236:239], v[212:215]\n\t" \
    "s_waitcnt lgkmcnt(2)\n\t" \
    "v_mfma_f32_16x16x32_bf16 v[204:207], v[64:67], v[240:243], v[204:207]\n\t" \
    "ds_read_b128 v[232:235], v253 offset:33792\n\t" \
    "s_and_b32 s44, s40, 3\n\t" \
    "s_waitcnt lgkmcnt(2)\n\t" \
    "v_mfma_f32_16x16x32_bf16 v[204:207], v[68:71], v[224:227], v[204:207]\n\t" \
    "ds_read_b128 v[236:239], v253 offset:14336\n\t" \
    "s_lshl_b32 s44, s44, 17\n\t" \
    "v_mfma_f32_16x16x32_bf16 v[208:211], v[72:75], v[240:243], v[208:211]\n\t" \
    "s_add_u32 s44, s44, %[mb]\n\t" \
    "v_mfma_f32_16x16x32_bf16 v[208:211], v[76:79], v[224:227], v[208:211]\n\t" \
    "s_add_u32 s46, s44, 0x0\n\t" \
    "v_mfma_f32_16x16x32_bf16 v[200:203], v[56:59], v[240:243], v[200:203]\n\t" \
    "s_add_u32 s47, s44, 0x1400\n\t" \
    "v_mfma_f32_16x16x32_bf16 v[200:203], v[60:63], v[224:227], v[200:203]\n\t" \
    "s_add_u32 s48, s44, 0x400\n\t" \
    "s_waitcnt lgkmcnt(2)\n\t" \
    "v_mfma_f32_16x16x32_bf16 v[216:219], v[64:67], v[228:231], v[216:219]\n\t" \
    "ds_read_b128 v[240:243], v253 offset:15360\n\t" \
    "s_waitcnt lgkmcnt(2)\n\t" \
    "v_mfma_f32_16x16x32_bf16 v[216:219], v[68:71], v[232:235], v[216:219]\n\t" \
    "ds_read_b128 v[224:227], v253 offset:34816\n\t" \
    "v_mfma_f32_16x16x32_bf16 v[220:223], v[72:75], v[228:231], v[220:223]\n\t" \
    "v_mfma_f32_16x16x32_bf16 v[220:223], v[76:79], v[232:235], v[220:223]\n\t" \
    "v_mfma_f32_16x16x32_bf16 v[212:215], v[56:59], v[228:231], v[212:215]\n\t" \
    "v_mfma_f32_16x16x32_bf16 v[212:215], v[60:63], v[232:235], v[212:215]\n\t" \
    "s_waitcnt lgkmcnt(2)\n\t" \
    "v_mfma_f32_16x16x32_bf16 v[204:207], v[104:107], v[236:239], v[204:207]\n\t" \
    "ds_read_b128 v[228:231], v253 offset:35840\n\t" \
    "s_waitcnt lgkmcnt(2)\n\t" \
    "v_mfma_f32_16x16x32_bf16 v[204:207], v[108:111], v[240:243], v[204:207]\n\t" \
    "ds_read_b128 v[232:235], v253 offset:16384\n\t" \
    "v_mfma_f32_16x16x32_bf16 v[208:211], v[112:115], v[236:239], v[208:211]\n\t" \
    "v_mfma_f32_16x16x32_bf16 v[208:211], v[116:119], v[240:243], v[208:211]\n\t" \
    "v_mfma_f32_16x16x32_bf16 v[200:203], v[96:99], v[236:239], v[200:203]\n\t" \
    "v_mfma_f32_16x16x32_bf16 v[200:203], v[100:103], v[240:243], v[200:203]\n\t" \
    "s_waitcnt lgkmcnt(2)\n\t" \
    "v_mfma_f32_16x16x32_bf16 v[216:219], v[104:107], v[224:227], v[216:219]\n\t" \
    "ds_read_b128 v[236:239], v253 offset:17408\n\t" \
    "s_waitcnt lgkmcnt(2)\n\t" \
    "v_mfma_f32_16x16x32_bf16 v[216:219], v[108:111], v[228:231], v[216:219]\n\t" \
    "ds_read_b128 v[240:243], v253 offset:18432\n\t" \
    "v_mfma_f32_16x16x32_bf16 v[220:223], v[112:115], v[224:227], v[220:223]\n\t" \
    "v_mfma_f32_16x16x32_bf16 v[220:223], v[116:119], v[228:231], v[220:223]\n\t" \
    "v_mfma_f32_16x16x32_bf16 v[212:215], v[96:99], v[224:227], v[212:215]\n\t" \
    "v_mfma_f32_16x16x32_bf16 v[212:215], v[100:103], v[228:231], v[212:215]\n\t" \
    "s_waitcnt lgkmcnt(2)\n\t" \
    "v_mfma_f32_16x16x32_bf16 v[204:207], v[144:147], v[232:235], v[204:207]\n\t" \
    "ds_read_b128 v[224:227], v253 offset:19456\n\t" \
    "v_mov_b64 v[244:245], 0\n\t" \
    "s_waitcnt lgkmcnt(2)\n\t" \
    "v_mfma_f32_16x16x32_bf16 v[204:207], v[148:151], v[236:239], v[204:207]\n\t" \
    "ds_read_b128 v[228:231], v253 offset:36864\n\t" \
    "v_mov_b64 v[246:247], 0\n\t" \
    "v_mfma_f32_16x16x32_bf16 v[208:211], v[152:155], v[232:235], v[208:211]\n\t" \
    "v_mov_b64 v[248:249], 0\n\t" \
    "v_mfma_f32_16x16x32_bf16 v[208:211], v[156:159], v[236:239], v[208:211]\n\t" \
    "v_mov_b64 v[250:251], 0\n\t" \
    "v_mfma_f32_16x16x32_bf16 v[200:203], v[136:139], v[232:235], v[200:203]\n\t" \
    "v_mfma_f32_16x16x32_bf16 v[200:203], v[140:143], v[236:239], v[200:203]\n\t" \
    "s_waitcnt lgkmcnt(2)\n\t" \
    "v_mfma_f32_16x16x32_bf16 v[204:207], v[184:187], v[240:243], v[204:207]\n\t" \
    "ds_read_b128 v[232:235], v253 offset:37888\n\t" \
    "s_waitcnt lgkmcnt(2)\n\t" \
    "v_mfma_f32_16x16x32_bf16 v[204:207], v[188:191], v[224:227], v[204:207]\n\t" \
    "ds_read_b128 v[236:239], v253 offset:38912\n\t" \
    "v_mfma_f32_16x16x32_bf16 v[208:211], v[192:195], v[240:243], v[208:211]\n\t" \
    "v_mfma_f32_16x16x32_bf16 v[208:211], v[196:199], v[224:227], v[208:211]\n\t" \
    "v_mfma_f32_16x16x32_bf16 v[200:203], v[176:179], v[240:243], v[200:203]\n\t" \
    "v_mfma_f32_16x16x32_bf16 v[200:203], v[180:183], v[224:227], v[200:203]\n\t" \
    "ds_read_b128 v[240:243], v253 offset:39936\n\t" \
    "s_waitcnt lgkmcnt(3)\n\t" \
    "v_mfma_f32_16x16x32_bf16 v[216:219], v[144:147], v[228:231], v[216:219]\n\t" \
    "s_waitcnt lgkmcnt(2)\n\t" \
    "v_mfma_f32_16x16x32_bf16 v[216:219], v[148:151], v[232:235], v[216:219]\n\t" \
    "v_cvt_pk_bf16_f32 v204, v204, v208\n\t" \
    "v_cvt_pk_bf16_f32 v205, v205, v209\n\t" \
    "s_waitcnt lgkmcnt(1)\n\t" \
    "v_mfma_f32_16x16x32_bf16 v[216:219], v[184:187], v[236:239], v[216:219]\n\t" \
    "v_cvt_pk_bf16_f32 v206, v206, v210\n\t" \
    "v_cvt_pk_bf16_f32 v207, v207, v211\n\t" \
    "s_waitcnt lgkmcnt(0)\n\t" \
    "v_mfma_f32_16x16x32_bf16 v[216:219], v[188:191], v[240:243], v[216:219]\n\t" \
    "ds_write_b128 v254, v[204:207] offset:8192\n\t" \
    "v_cvt_pk_bf16_f32 v200, v200, v201\n\t" \
    "v_mfma_f32_16x16x32_bf16 v[220:223], v[152:155], v[228:231], v[220:223]\n\t" \
    "v_cvt_pk_bf16_f32 v201, v202, v203\n\t" \
    "ds_write_b64 v254, v[200:201] offset:6144\n\t" \
    "v_mfma_f32_16x16x32_bf16 v[220:223], v[156:159], v[232:235], v[220:223]\n\t" \
    "ds_read_b128 v[200:203], v254 offset:30720\n\t" \
    "ds_read_b128 v[204:207], v254 offset:31744\n\t" \
    "v_mfma_f32_16x16x32_bf16 v[220:223], v[192:195], v[236:239], v[220:223]\n\t" \
    "v_mov_b64 v[208:209], 0\n\t" \
    "v_mov_b64 v[210:211], 0\n\t" \
    "v_mfma_f32_16x16x32_bf16 v[220:223], v[196:199], v[240:243], v[220:223]\n\t" \
    "v_mfma_f32_16x16x32_bf16 v[212:215], v[136:139], v[228:231], v[212:215]\n\t" \
    "v_mfma_f32_16x16x32_bf16 v[212:215], v[140:143], v[232:235], v[212:215]\n\t" \
    "v_mfma_f32_16x16x32_bf16 v[212:215], v[176:179], v[236:239], v[212:215]\n\t" \
    "v_cvt_pk_bf16_f32 v216, v216, v220\n\t" \
    "v_cvt_pk_bf16_f32 v217, v217, v221\n\t" \
    "v_mfma_f32_16x16x32_bf16 v[212:215], v[180:183], v[240:243], v[212:215]\n\t" \
    "v_cvt_pk_bf16_f32 v218, v218, v222\n\t" \
    "v_cvt_pk_bf16_f32 v219, v219, v223\n\t" \
    "ds_write_b128 v254, v[216:219] offset:14336\n\t" \
    "ds_read_b128 v[216:219], v254 offset:32768\n\t" \
    "ds_read_b128 v[220:223], v254 offset:33792\n\t" \
    "s_nop 2\n\t" \
    "v_cvt_pk_bf16_f32 v212, v212, v213\n\t" \
    "v_cvt_pk_bf16_f32 v213, v214, v215\n\t" \
    "ds_write_b64 v254, v[212:213] offset:12288\n\t" \
    LST6 \
    "s_sleep 8\n\t" \
    "buffer_load_dwordx4 v[224:227], v253, s[60:63], s46 offen sc1\n\t" \
    "buffer_load_dwordx4 v[228:231], v253, s[60:63], s47 offen sc1\n\t" \
    "buffer_load_dwordx4 v[232:235], v253, s[60:63], s48 offen sc1\n\t" \
    "s_waitcnt lgkmcnt(0)\n\t" \
    LFLUSH212 \
    "v_bfe_i32 v212, v253, 5, 1\n\t" \
    "v_and_b32 v212, 0xaaaa, v212\n\t" \
    "v_or_b32 v212, 0x4444, v212\n\t" \
    "v_and_b32 v213, 0xffff, v255\n\t" \
    "v_lshrrev_b32 v214, 16, v255\n\t" \
    ".Ls04_%=:\n\t" \
    "s_waitcnt vmcnt(2)\n\t" \
    "v_max_u32 v215, v224, v225\n\t" \
    "v_max3_u32 v215, v226, v227, v215\n\t" \
    "s_waitcnt vmcnt(1)\n\t" \
    "v_max3_u32 v215, v228, v229, v215\n\t" \
    "v_max3_u32 v215, v230, v231, v215\n\t" \
    "s_waitcnt vmcnt(0)\n\t" \
    "v_max3_u32 v215, v232, v233, v215\n\t" \
    "v_max3_u32 v215, v234, v235, v215\n\t" \
    "v_cmp_eq_u32 vcc, -1, v215\n\t" \
    "s_cbranch_vccz .Ls05_%=\n\t" \
    "v_max_u32 v215, v224, v225\n\t" \
    "v_max3_u32 v215, v226, v227, v215\n\t" \
    "v_cmp_eq_u32 vcc, -1, v215\n\t" \
    "s_cbranch_vccz .Ls06_%=\n\t" \
    "buffer_load_dwordx4 v[224:227], v253, s[60:63], s46 offen sc1\n\t" \
    ".Ls06_%=:\n\t" \
    "v_max_u32 v215, v228, v229\n\t" \
    "v_max3_u32 v215, v230, v231, v215\n\t" \
    "v_cmp_eq_u32 vcc, -1, v215\n\t" \
    "s_cbranch_vccz .Ls07_%=\n\t" \
    "buffer_load_dwordx4 v[228:231], v253, s[60:63], s47 offen sc1\n\t" \
    ".Ls07_%=:\n\t" \
    "v_max_u32 v215, v232, v233\n\t" \
    "v_max3_u32 v215, v234, v235, v215\n\t" \
    "v_cmp_eq_u32 vcc, -1, v215\n\t" \
    "s_cbranch_vccz .Ls08_%=\n\t" \
    "buffer_load_dwordx4 v[232:235], v253, s[60:63], s48 offen sc1\n\t" \
    ".Ls08_%=:\n\t" \
    "s_cmp_eq_u32 s41, 0\n\t" \
    "s_cbranch_scc1 .Ls05_%=\n\t" \
    "s_sub_u32 s41, s41, 1\n\t" \
    "s_waitcnt vmcnt(0)\n\t" \
    "s_branch .Ls04_%=\n\t" \
    ".Ls05_%=:\n\t" \
    LST2 \
    "ds_write_b128 v253, v[224:227] offset:40960\n\t" \
    "ds_write_b128 v253, v[228:231] offset:46080\n\t" \
    "ds_write_b128 v253, v[232:235] offset:41984\n\t" \
    "v_smfmac_f32_16x16x64_bf16 v[244:247], v[224:227], v[0:7], v212\n\t" \
    "v_smfmac_f32_16x16x64_bf16 v[248:251], v[224:227], v[8:15], v212\n\t" \
    "v_smfmac_f32_16x16x64_bf16 v[208:211], v[224:227], v[200:207], v212\n\t" \
    "s_waitcnt lgkmcnt(0)\n\t" \
    "s_barrier \n\t" \
    LST3 \
    "ds_read_b128 v[236:239], v253 offset:47104\n\t" \
    "ds_read_b128 v[240:243], v253 offset:43008\n\t" \
    "ds_read_b128 v[224:227], v253 offset:48128\n\t" \
    "ds_read_b128 v[200:203], v254 offset:34816\n\t" \
    "ds_read_b128 v[204:207], v254 offset:35840\n\t" \
    "v_smfmac_f32_16x16x64_bf16 v[208:211], v[228:231], v[16:23], v212\n\t" \
    "v_smfmac_f32_16x16x64_bf16 v[244:247], v[228:231], v[24:31], v212\n\t" \
    "v_smfmac_f32_16x16x64_bf16 v[248:251], v[228:231], v[32:39], v212\n\t" \
    "ds_read_b128 v[228:231], v253 offset:44032\n\t" \
    "v_smfmac_f32_16x16x64_bf16 v[208:211], v[232:235], v[216:223], v212\n\t" \
    "ds_read_b128 v[216:219], v254 offset:36864\n\t" \
    "ds_read_b128 v[220:223], v254 offset:37888\n\t" \
    "v_smfmac_f32_16x16x64_bf16 v[244:247], v[232:235], v[40:47], v212\n\t" \
    "v_smfmac_f32_16x16x64_bf16 v[248:251], v[232:235], v[48:55], v212\n\t" \
    "ds_read_b128 v[232:235], v253 offset:49152\n\t" \
    "s_waitcnt lgkmcnt(8)\n\t" \
    "v_smfmac_f32_16x16x64_bf16 v[208:211], v[236:239], v[56:63], v212\n\t" \
    "v_smfmac_f32_16x16x64_bf16 v[244:247], v[236:239], v[64:71], v212\n\t" \
    "v_smfmac_f32_16x16x64_bf16 v[248:251], v[236:239], v[72:79], v212\n\t" \
    "ds_read_b128 v[236:239], v253 offset:45056\n\t" \
    "s_waitcnt lgkmcnt(5)\n\t" \
    "v_smfmac_f32_16x16x64_bf16 v[208:211], v[240:243], v[200:207], v212\n\t" \
    "ds_read_b128 v[200:203], v254 offset:38912\n\t" \
    "ds_read_b128 v[204:207], v254 offset:39936\n\t" \
    "v_smfmac_f32_16x16x64_bf16 v[244:247], v[240:243], v[80:87], v212\n\t" \
    "v_smfmac_f32_16x16x64_bf16 v[248:251], v[240:243], v[88:95], v212\n\t" \
    "ds_read_b128 v[240:243], v253 offset:50176\n\t" \
    "v_smfmac_f32_16x16x64_bf16 v[208:211], v[224:227], v[96:103], v212\n\t" \
    "v_smfmac_f32_16x16x64_bf16 v[244:247], v[224:227], v[104:111], v212\n\t" \
    "v_smfmac_f32_16x16x64_bf16 v[248:251], v[224:227], v[112:119], v212\n\t" \
    "s_waitcnt lgkmcnt(5)\n\t" \
    "v_smfmac_f32_16x16x64_bf16 v[208:211], v[228:231], v[216:223], v212\n\t" \
    "s_waitcnt lgkmcnt(4)\n\t" \
    "v_smfmac_f32_16x16x64_bf16 v[208:211], v[232:235], v[136:143], v212\n\t" \
    "s_waitcnt lgkmcnt(1)\n\t" \
    "v_smfmac_f32_16x16x64_bf16 v[208:211], v[236:239], v[200:207], v212\n\t" \
    "s_waitcnt lgkmcnt(0)\n\t" \
    "v_smfmac_f32_16x16x64_bf16 v[208:211], v[240:243], v[176:183], v212\n\t" \
    "v_smfmac_f32_16x16x64_bf16 v[244:247], v[228:231], v[120:127], v212\n\t" \
    "v_smfmac_f32_16x16x64_bf16 v[244:247], v[232:235], v[144:151], v212\n\t" \
    "v_smfmac_f32_16x16x64_bf16 v[244:247], v[236:239], v[160:167], v212\n\t" \
    "v_add_f32 v208, v208, v210\n\t" \
    "v_add_f32 v209, v209, v211\n\t" \
    "v_cvt_pk_bf16_f32 v208, v208, v209\n\t" \
    "v_smfmac_f32_16x16x64_bf16 v[244:247], v[240:243], v[184:191], v212\n\t" \
    "ds_write_b16 v213, v208 offset:0\n\t" \
    "ds_write_b16_d16_hi v213, v208 offset:16\n\t" \
    "v_smfmac_f32_16x16x64_bf16 v[248:251], v[228:231], v[128:135], v212\n\t" \
    "v_smfmac_f32_16x16x64_bf16 v[248:251], v[232:235], v[152:159], v212\n\t" \
    "v_smfmac_f32_16x16x64_bf16 v[248:251], v[236:239], v[168:175], v212\n\t" \
    "v_add_f32 v244, v244, v246\n\t" \
    "v_add_f32 v245, v245, v247\n\t" \
    "v_smfmac_f32_16x16x64_bf16 v[248:251], v[240:243], v[192:199], v212\n\t" \
    "s_nop 7\n\t" \
    "v_add_f32 v248, v248, v250\n\t" \
    "v_add_f32 v249, v249, v251\n\t" \
    "v_cvt_pk_bf16_f32 v244, v244, v248\n\t" \
    "v_cvt_pk_bf16_f32 v245, v245, v249\n\t" \
    "ds_write2_b32 v214, v244, v245 offset1:4\n\t" \
    "s_sleep 3\n\t" \
    "ds_read_b128 v[200:203], v254 offset:18432\n\t" \
    "ds_read_b128 v[204:207], v254 offset:19456\n\t" \
    "ds_read_b128 v[214:217], v254 offset:24576\n\t" \
    "ds_read_b128 v[218:221], v254 offset:25600\n\t" \
    "ds_read_b128 v[222:225], v254 offset:20480\n\t" \
    "ds_read_b128 v[226:229], v254 offset:21504\n\t" \
    "ds_read_b128 v[230:233], v254 offset:26624\n\t" \
    "ds_read_b128 v[234:237], v254 offset:27648\n\t" \
    "v_and_b32 v213, 0xffffff7f, v254\n\t" \
    "v_mov_b64 v[242:243], 0\n\t" \
    "v_mov_b64 v[244:245], 0\n\t" \
    "v_mov_b64 v[246:247], 0\n\t" \
    "v_mov_b64 v[248:249], 0\n\t" \
    "v_mov_b64 v[250:251], 0\n\t" \
    "v_mov_b64 v[252:253], 0\n\t" \
    "s_and_b32 s44, s40, 3\n\t" \
    "s_mul_i32 s44, s44, 0x30000\n\t" \
    "s_add_u32 s50, s44, %[pb]\n\t" \
    "s_add_u32 s51, s50, 0xc000\n\t" \
    "s_add_u32 s52, s50, 0x18000\n\t" \
    "s_add_u32 s53, s50, 0x24000\n\t" \
    "s_setprio 3\n\t" \
    "s_waitcnt lgkmcnt(0)\n\t" \
    "s_barrier \n\t" \
    LST4 \
    "ds_read_b128 v[208:211], v254 offset:0\n\t" \
    "ds_read_b128 v[238:241], v254 offset:1024\n\t" \
    "s_waitcnt lgkmcnt(1)\n\t" \
    "v_smfmac_f32_16x16x64_bf16 v[242:245], v[208:211], v[200:207], v212\n\t" \
    "ds_read_b128 v[200:203], v213 offset:3072\n\t" \
    "ds_read_b128 v[204:207], v213 offset:3200\n\t" \
    "v_smfmac_f32_16x16x64_bf16 v[246:249], v[208:211], v[214:221], v212\n\t" \
    "ds_read_b128 v[214:217], v213 offset:4096\n\t" \
    "ds_read_b128 v[218:221], v213 offset:4224\n\t" \
    "s_waitcnt lgkmcnt(4)\n\t" \
    "v_smfmac_f32_16x16x64_bf16 v[242:245], v[238:241], v[222:229], v212\n\t" \
    "ds_read_b128 v[222:225], v254 offset:22528\n\t" \
    "ds_read_b128 v[226:229], v254 offset:23552\n\t" \
    "v_smfmac_f32_16x16x64_bf16 v[246:249], v[238:241], v[230:237], v212\n\t" \
    "ds_read_b128 v[230:233], v254 offset:28672\n\t" \
    "ds_read_b128 v[234:237], v254 offset:29696\n\t" \
    "s_waitcnt lgkmcnt(6)\n\t" \
    "v_smfmac_f32_16x16x64_bf16 v[250:253], v[208:211], v[200:207], v212\n\t" \
    "ds_read_b128 v[208:211], v254 offset:2048\n\t" \
    "ds_read_b128 v[200:203], v213 offset:5120\n\t" \
    "ds_read_b128 v[204:207], v213 offset:5248\n\t" \
    "s_waitcnt lgkmcnt(7)\n\t" \
    "v_smfmac_f32_16x16x64_bf16 v[250:253], v[238:241], v[214:221], v212\n\t" \
    "v_lshrrev_b32 v238, 3, v254\n\t" \
    "v_and_b32 v238, 0x6e, v238\n\t" \
    "v_lshrrev_b32 v239, 16, v255\n\t" \
    "s_waitcnt lgkmcnt(2)\n\t" \
    "v_smfmac_f32_16x16x64_bf16 v[242:245], v[208:211], v[222:229], v212\n\t" \
    "v_smfmac_f32_16x16x64_bf16 v[246:249], v[208:211], v[230:237], v212\n\t" \
    "s_waitcnt lgkmcnt(0)\n\t" \
    "v_smfmac_f32_16x16x64_bf16 v[250:253], v[208:211], v[200:207], v212\n\t" \
    "s_nop 1\n\t" \
    "v_add_f32 v242, v242, v244\n\t" \
    "v_add_f32 v243, v243, v245\n\t" \
    "s_nop 0\n\t" \
    "v_add_f32 v246, v246, v248\n\t" \
    "v_add_f32 v247, v247, v249\n\t" \
    "s_nop 0\n\t" \
    "v_add_f32 v250, v250, v252\n\t" \
    "v_add_f32 v251, v251, v253\n\t" \
    "v_max3_f32 v244, v242, v246, v250\n\t" \
    "v_max3_f32 v245, v243, v247, v251\n\t" \
    "s_nop 0\n\t" \
    "v_max_f32_dpp v244, v244, v244 quad_perm:[1,0,3,2] row_mask:0xf bank_mask:0xf\n\t" \
    "v_max_f32_dpp v245, v245, v245 quad_perm:[1,0,3,2] row_mask:0xf bank_mask:0xf\n\t" \
    "s_nop 0\n\t" \
    "v_max_f32_dpp v244, v244, v244 quad_perm:[2,3,0,1] row_mask:0xf bank_mask:0xf\n\t" \
    "v_max_f32_dpp v245, v245, v245 quad_perm:[2,3,0,1] row_mask:0xf bank_mask:0xf\n\t" \
    "s_nop 0\n\t" \
    "v_max_f32_dpp v244, v244, v244 row_half_mirror row_mask:0xf bank_mask:0xf\n\t" \
    "v_max_f32_dpp v245, v245, v245 row_half_mirror row_mask:0xf bank_mask:0xf\n\t" \
    "s_nop 0\n\t" \
    "v_max_f32_dpp v244, v244, v244 row_mirror row_mask:0xf bank_mask:0xf\n\t" \
    "v_max_f32_dpp v245, v245, v245 row_mirror row_mask:0xf bank_mask:0xf\n\t" \
    "v_mul_f32 v244, %[scl], v244\n\t" \
    "v_mul_f32 v245, %[scl], v245\n\t" \
    "v_fma_f32 v242, v242, %[scl], -v244\n\t" \
    "v_fma_f32 v243, v243, %[scl], -v245\n\t" \
    "v_fma_f32 v246, v246, %[scl], -v244\n\t" \
    "v_fma_f32 v247, v247, %[scl], -v245\n\t" \
    "v_exp_f32 v242, v242\n\t" \
    "v_exp_f32 v243, v243\n\t" \
    "v_fma_f32 v250, v250, %[scl], -v244\n\t" \
    "v_fma_f32 v251, v251, %[scl], -v245\n\t" \
    "v_exp_f32 v246, v246\n\t" \
    "v_exp_f32 v247, v247\n\t" \
    "v_exp_f32 v250, v250\n\t" \
    "v_exp_f32 v251, v251\n\t" \
    "v_add_f32 v244, v242, v246\n\t" \
    "v_add_f32 v245, v243, v247\n\t" \
    "v_fma_f32 v244, v250, 0.5, v244\n\t" \
    "v_fma_f32 v245, v251, 0.5, v245\n\t" \
    "s_nop 0\n\t" \
    "v_add_f32_dpp v244, v244, v244 quad_perm:[1,0,3,2] row_mask:0xf bank_mask:0xf\n\t" \
    "v_add_f32_dpp v245, v245, v245 quad_perm:[1,0,3,2] row_mask:0xf bank_mask:0xf\n\t" \
    "s_nop 0\n\t" \
    "v_add_f32_dpp v244, v244, v244 quad_perm:[2,3,0,1] row_mask:0xf bank_mask:0xf\n\t" \
    "v_add_f32_dpp v245, v245, v245 quad_perm:[2,3,0,1] row_mask:0xf bank_mask:0xf\n\t" \
    "s_nop 0\n\t" \
    "v_add_f32_dpp v244, v244, v244 row_half_mirror row_mask:0xf bank_mask:0xf\n\t" \
    "v_add_f32_dpp v245, v245, v245 row_half_mirror row_mask:0xf bank_mask:0xf\n\t" \
    "s_nop 0\n\t" \
    "v_add_f32_dpp v244, v244, v244 row_mirror row_mask:0xf bank_mask:0xf\n\t" \
    "v_add_f32_dpp v245, v245, v245 row_mirror row_mask:0xf bank_mask:0xf\n\t" \
    "v_rcp_f32 v244, v244\n\t" \
    "v_rcp_f32 v245, v245\n\t" \
    "v_mul_f32 v250, v250, v244\n\t" \
    "v_mul_f32 v251, v251, v245\n\t" \
    "v_mul_f32 v242, v242, v244\n\t" \
    "v_mul_f32 v243, v243, v245\n\t" \
    "v_mul_f32 v246, v246, v244\n\t" \
    "v_mul_f32 v247, v247, v245\n\t" \
    "v_cvt_pk_bf16_f32 v250, |v250|, |v251|\n\t" \
    "ds_write_b16 v238, v250 offset:52096\n\t" \
    "ds_write_b16_d16_hi v238, v250 offset:52112\n\t" \
    "v_cvt_pk_bf16_f32 v242, |v242|, |v246|\n\t" \
    "v_cvt_pk_bf16_f32 v243, |v243|, |v247|\n\t" \
    "ds_write2_b32 v239, v242, v243 offset1:4\n\t" \
    "ds_read_b128 v[200:203], v254 offset:896\n\t" \
    "s_wakeup\n\t" \
    "v_and_b32 v253, 0x3ff, v254\n\t" \
    "s_mov_b32 exec_hi, 0xff\n\t" \
    "s_waitcnt lgkmcnt(0)\n\t" \
    "buffer_store_dwordx4 v[200:203], v254, s[60:63], s50 offen sc1\n\t" \
    "buffer_store_dwordx4 v[200:203], v254, s[60:63], s51 offen sc1\n\t" \
    "buffer_store_dwordx4 v[200:203], v254, s[60:63], s52 offen sc1\n\t" \
    "buffer_store_dwordx4 v[200:203], v254, s[60:63], s53 offen sc1\n\t" \
    "s_mov_b32 exec_hi, -1\n\t" \
    "s_setprio 0\n\t" \
    LST5 \
    "ds_read_b128 v[224:227], v253 offset:0\n\t" \
    "ds_read_b128 v[228:231], v253 offset:1024\n\t" \
    "ds_read_b128 v[232:235], v253 offset:20480\n\t" \
    "ds_read_b128 v[236:239], v253 offset:21504\n\t" \
    "s_add_u32 s40, s40, 1\n\t" \
    "s_cmp_lg_u32 s40, 64\n\t" \
    "s_cbranch_scc1 .Ls03_%=\n\t" \
    "s_branch .Lend%=\n\t" \
    ".Lend%=:\n\t" \
    "s_waitcnt vmcnt(0) lgkmcnt(0)\n\t" \
    "s_mov_b32 %[bud], s41\n\t"
#define ASM_V_LOOP \
    "s_mov_b64 s[60:61], %[ws]\n\t" \
    "s_mov_b32 s62, %[sz]\n\t" \
    "s_mov_b32 s63, 0x20000\n\t" \
    "s_mov_b32 s40, 0\n\t" \
    "s_mov_b32 s41, 0x20000\n\t" \
    LSTINIT \
    LDELAY \
    "s_cmp_eq_u32 %[wave], 0\n\t" \
    "s_cbranch_scc1 .Lv0_%=\n\t" \
    "s_cmp_eq_u32 %[wave], 1\n\t" \
    "s_cbranch_scc1 .Lv1_%=\n\t" \
    "s_cmp_eq_u32 %[wave], 2\n\t" \
    "s_cbranch_scc1 .Lv2_%=\n\t" \
    "s_cmp_eq_u32 %[wave], 3\n\t" \
    "s_cbranch_scc1 .Lv3_%=\n\t" \
    ".Lv4_%=:\n\t" \
    "s_add_u32 m0, %[wm], 0x0\n\t" \
    "s_mov_b32 s44, 0x0\n\t" \
    "buffer_load_dwordx4 v240, %[wr], s44 offen offset:0 nt lds\n\t" \
    "buffer_load_dwordx4 v241, %[wr], s44 offen offset:1024 nt lds\n\t" \
    "buffer_load_dwordx4 v242, %[wr], s44 offen offset:2048 nt lds\n\t" \
    "buffer_load_dwordx4 v243, %[wr], s44 offen offset:3072 nt lds\n\t" \
    "s_add_u32 m0, %[wm], 0x1000\n\t" \
    "s_nop 0\n\t" \
    "buffer_load_dwordx4 v244, %[wr], s44 offen nt lds\n\t" \
    "s_add_u32 m0, %[wm], 0x1400\n\t" \
    "s_mov_b32 s44, 0x5000\n\t" \
    "buffer_load_dwordx4 v240, %[wr], s44 offen offset:0 nt lds\n\t" \
    "buffer_load_dwordx4 v241, %[wr], s44 offen offset:1024 nt lds\n\t" \
    "buffer_load_dwordx4 v242, %[wr], s44 offen offset:2048 nt lds\n\t" \
    "buffer_load_dwordx4 v243, %[wr], s44 offen offset:3072 nt lds\n\t" \
    "s_add_u32 m0, %[wm], 0x2400\n\t" \
    "s_nop 0\n\t" \
    "buffer_load_dwordx4 v244, %[wr], s44 offen nt lds\n\t" \
    "s_add_u32 m0, %[wm], 0x2800\n\t" \
    "s_mov_b32 s44, 0xa000\n\t" \
    "buffer_load_dwordx4 v240, %[wr], s44 offen offset:0 nt lds\n\t" \
    "buffer_load_dwordx4 v241, %[wr], s44 offen offset:1024 nt lds\n\t" \
    "buffer_load_dwordx4 v242, %[wr], s44 offen offset:2048 nt lds\n\t" \
    "buffer_load_dwordx4 v243, %[wr], s44 offen offset:3072 nt lds\n\t" \
    "s_add_u32 m0, %[wm], 0x3800\n\t" \
    "s_nop 0\n\t" \
    "buffer_load_dwordx4 v244, %[wr], s44 offen nt lds\n\t" \
    "s_add_u32 m0, %[wm], 0x3c00\n\t" \
    "s_mov_b32 s44, 0xf000\n\t" \
    "buffer_load_dwordx4 v240, %[wr], s44 offen offset:0 nt lds\n\t" \
    "buffer_load_dwordx4 v241, %[wr], s44 offen offset:1024 nt lds\n\t" \
    "buffer_load_dwordx4 v242, %[wr], s44 offen offset:2048 nt lds\n\t" \
    "buffer_load_dwordx4 v243, %[wr], s44 offen offset:3072 nt lds\n\t" \
    "s_add_u32 m0, %[wm], 0x4c00\n\t" \
    "s_nop 0\n\t" \
    "buffer_load_dwordx4 v244, %[wr], s44 offen nt lds\n\t" \
    "s_barrier \n\t" \
    "s_waitcnt vmcnt(15)\n\t" \
    "ds_read_b64_tr_b16 v[200:201], v245 offset:0\n\t" \
    "ds_read_b64_tr_b16 v[16:17], v245 offset:32\n\t" \
    "ds_read_b64_tr_b16 v[208:209], v245 offset:64\n\t" \
    "ds_read_b64_tr_b16 v[56:57], v245 offset:96\n\t" \
    "ds_read_b64_tr_b16 v[216:217], v245 offset:128\n\t" \
    "ds_read_b64_tr_b16 v[96:97], v245 offset:160\n\t" \
    "ds_read_b64_tr_b16 v[224:225], v245 offset:192\n\t" \
    "ds_read_b64_tr_b16 v[136:137], v245 offset:224\n\t" \
    "ds_read_b64_tr_b16 v[232:233], v245 offset:256\n\t" \
    "ds_read_b64_tr_b16 v[176:177], v245 offset:288\n\t" \
    "s_waitcnt lgkmcnt(0)\n\t" \
    "s_add_u32 m0, %[wm], 0x0\n\t" \
    "s_mov_b32 s44, 0x14000\n\t" \
    "buffer_load_dwordx4 v240, %[wr], s44 offen offset:0 nt lds\n\t" \
    "buffer_load_dwordx4 v241, %[wr], s44 offen offset:1024 nt lds\n\t" \
    "buffer_load_dwordx4 v242, %[wr], s44 offen offset:2048 nt lds\n\t" \
    "buffer_load_dwordx4 v243, %[wr], s44 offen offset:3072 nt lds\n\t" \
    "s_add_u32 m0, %[wm], 0x1000\n\t" \
    "s_nop 0\n\t" \
    "buffer_load_dwordx4 v244, %[wr], s44 offen nt lds\n\t" \
    "s_barrier \n\t" \
    "s_barrier \n\t" \
    "s_waitcnt vmcnt(15)\n\t" \
    "ds_read_b64_tr_b16 v[204:205], v245 offset:5120\n\t" \
    "ds_read_b64_tr_b16 v[20:21], v245 offset:5152\n\t" \
    "ds_read_b64_tr_b16 v[212:213], v245 offset:5184\n\t" \
    "ds_read_b64_tr_b16 v[60:61], v245 offset:5216\n\t" \
    "ds_read_b64_tr_b16 v[220:221], v245 offset:5248\n\t" \
    "ds_read_b64_tr_b16 v[100:101], v245 offset:5280\n\t" \
    "ds_read_b64_tr_b16 v[228:229], v245 offset:5312\n\t" \
    "ds_read_b64_tr_b16 v[140:141], v245 offset:5344\n\t" \
    "ds_read_b64_tr_b16 v[236:237], v245 offset:5376\n\t" \
    "ds_read_b64_tr_b16 v[180:181], v245 offset:5408\n\t" \
    "s_waitcnt lgkmcnt(0)\n\t" \
    "s_add_u32 m0, %[wm], 0x1400\n\t" \
    "s_mov_b32 s44, 0x19000\n\t" \
    "buffer_load_dwordx4 v240, %[wr], s44 offen offset:0 nt lds\n\t" \
    "buffer_load_dwordx4 v241, %[wr], s44 offen offset:1024 nt lds\n\t" \
    "buffer_load_dwordx4 v242, %[wr], s44 offen offset:2048 nt lds\n\t" \
    "buffer_load_dwordx4 v243, %[wr], s44 offen offset:3072 nt lds\n\t" \
    "s_add_u32 m0, %[wm], 0x2400\n\t" \
    "s_nop 0\n\t" \
    "buffer_load_dwordx4 v244, %[wr], s44 offen nt lds\n\t" \
    "s_barrier \n\t" \
    "s_barrier \n\t" \
    "s_waitcnt vmcnt(15)\n\t" \
    "ds_read_b64_tr_b16 v[0:1], v245 offset:10240\n\t" \
    "ds_read_b64_tr_b16 v[24:25], v245 offset:10272\n\t" \
    "ds_read_b64_tr_b16 v[40:41], v245 offset:10304\n\t" \
    "ds_read_b64_tr_b16 v[64:65], v245 offset:10336\n\t" \
    "ds_read_b64_tr_b16 v[80:81], v245 offset:10368\n\t" \
    "ds_read_b64_tr_b16 v[104:105], v245 offset:10400\n\t" \
    "ds_read_b64_tr_b16 v[120:121], v245 offset:10432\n\t" \
    "ds_read_b64_tr_b16 v[144:145], v245 offset:10464\n\t" \
    "ds_read_b64_tr_b16 v[160:161], v245 offset:10496\n\t" \
    "ds_read_b64_tr_b16 v[184:185], v245 offset:10528\n\t" \
    "s_waitcnt lgkmcnt(0)\n\t" \
    "s_add_u32 m0, %[wm], 0x2800\n\t" \
    "s_mov_b32 s44, 0x1e000\n\t" \
    "buffer_load_dwordx4 v240, %[wr], s44 offen offset:0 nt lds\n\t" \
    "buffer_load_dwordx4 v241, %[wr], s44 offen offset:1024 nt lds\n\t" \
    "buffer_load_dwordx4 v242, %[wr], s44 offen offset:2048 nt lds\n\t" \
    "buffer_load_dwordx4 v243, %[wr], s44 offen offset:3072 nt lds\n\t" \
    "s_add_u32 m0, %[wm], 0x3800\n\t" \
    "s_nop 0\n\t" \
    "buffer_load_dwordx4 v244, %[wr], s44 offen nt lds\n\t" \
    "s_barrier \n\t" \
    "s_waitcnt vmcnt(15)\n\t" \
    "ds_read_b64_tr_b16 v[4:5], v245 offset:15360\n\t" \
    "ds_read_b64_tr_b16 v[28:29], v245 offset:15392\n\t" \
    "ds_read_b64_tr_b16 v[44:45], v245 offset:15424\n\t" \
    "ds_read_b64_tr_b16 v[68:69], v245 offset:15456\n\t" \
    "ds_read_b64_tr_b16 v[84:85], v245 offset:15488\n\t" \
    "ds_read_b64_tr_b16 v[108:109], v245 offset:15520\n\t" \
    "ds_read_b64_tr_b16 v[124:125], v245 offset:15552\n\t" \
    "ds_read_b64_tr_b16 v[148:149], v245 offset:15584\n\t" \
    "ds_read_b64_tr_b16 v[164:165], v245 offset:15616\n\t" \
    "ds_read_b64_tr_b16 v[188:189], v245 offset:15648\n\t" \
    "s_waitcnt lgkmcnt(0)\n\t" \
    "s_add_u32 m0, %[wm], 0x3c00\n\t" \
    "s_mov_b32 s44, 0x23000\n\t" \
    "buffer_load_dwordx4 v240, %[wr], s44 offen offset:0 nt lds\n\t" \
    "buffer_load_dwordx4 v241, %[wr], s44 offen offset:1024 nt lds\n\t" \
    "buffer_load_dwordx4 v242, %[wr], s44 offen offset:2048 nt lds\n\t" \
    "buffer_load_dwordx4 v243, %[wr], s44 offen offset:3072 nt lds\n\t" \
    "s_add_u32 m0, %[wm], 0x4c00\n\t" \
    "s_nop 0\n\t" \
    "buffer_load_dwordx4 v244, %[wr], s44 offen nt lds\n\t" \
    "s_barrier \n\t" \
    "s_barrier \n\t" \
    "s_waitcnt vmcnt(15)\n\t" \
    "ds_read_b64_tr_b16 v[8:9], v245 offset:0\n\t" \
    "ds_read_b64_tr_b16 v[32:33], v245 offset:32\n\t" \
    "ds_read_b64_tr_b16 v[48:49], v245 offset:64\n\t" \
    "ds_read_b64_tr_b16 v[72:73], v245 offset:96\n\t" \
    "ds_read_b64_tr_b16 v[88:89], v245 offset:128\n\t" \
    "ds_read_b64_tr_b16 v[112:113], v245 offset:160\n\t" \
    "ds_read_b64_tr_b16 v[128:129], v245 offset:192\n\t" \
    "ds_read_b64_tr_b16 v[152:153], v245 offset:224\n\t" \
    "ds_read_b64_tr_b16 v[168:169], v245 offset:256\n\t" \
    "ds_read_b64_tr_b16 v[192:193], v245 offset:288\n\t" \
    "v_mov_b32 v246, 0\n\t" \
    "buffer_load_dword v246, v246, s[60:63], %[mo] offen sc1\n\t" \
    "s_waitcnt lgkmcnt(0)\n\t" \
    "s_add_u32 m0, %[wm], 0x0\n\t" \
    "s_mov_b32 s44, 0x28000\n\t" \
    "buffer_load_dwordx4 v240, %[wr], s44 offen offset:0 nt lds\n\t" \
    "buffer_load_dwordx4 v241, %[wr], s44 offen offset:1024 nt lds\n\t" \
    "buffer_load_dwordx4 v242, %[wr], s44 offen offset:2048 nt lds\n\t" \
    "buffer_load_dwordx4 v243, %[wr], s44 offen offset:3072 nt lds\n\t" \
    "s_add_u32 m0, %[wm], 0x1000\n\t" \
    "s_nop 0\n\t" \
    "buffer_load_dwordx4 v244, %[wr], s44 offen nt lds\n\t" \
    "s_barrier \n\t" \
    "s_barrier \n\t" \
    "s_waitcnt vmcnt(15)\n\t" \
    "ds_read_b64_tr_b16 v[12:13], v245 offset:5120\n\t" \
    "ds_read_b64_tr_b16 v[36:37], v245 offset:5152\n\t" \
    "ds_read_b64_tr_b16 v[52:53], v245 offset:5184\n\t" \
    "ds_read_b64_tr_b16 v[76:77], v245 offset:5216\n\t" \
    "ds_read_b64_tr_b16 v[92:93], v245 offset:5248\n\t" \
    "ds_read_b64_tr_b16 v[116:117], v245 offset:5280\n\t" \
    "ds_read_b64_tr_b16 v[132:133], v245 offset:5312\n\t" \
    "ds_read_b64_tr_b16 v[156:157], v245 offset:5344\n\t" \
    "ds_read_b64_tr_b16 v[172:173], v245 offset:5376\n\t" \
    "ds_read_b64_tr_b16 v[196:197], v245 offset:5408\n\t" \
    "s_waitcnt lgkmcnt(0)\n\t" \
    "s_add_u32 m0, %[wm], 0x1400\n\t" \
    "s_mov_b32 s44, 0x2d000\n\t" \
    "buffer_load_dwordx4 v240, %[wr], s44 offen offset:0 nt lds\n\t" \
    "buffer_load_dwordx4 v241, %[wr], s44 offen offset:1024 nt lds\n\t" \
    "buffer_load_dwordx4 v242, %[wr], s44 offen offset:2048 nt lds\n\t" \
    "buffer_load_dwordx4 v243, %[wr], s44 offen offset:3072 nt lds\n\t" \
    "s_add_u32 m0, %[wm], 0x2400\n\t" \
    "s_nop 0\n\t" \
    "buffer_load_dwordx4 v244, %[wr], s44 offen nt lds\n\t" \
    "s_barrier \n\t" \
    "s_waitcnt vmcnt(15)\n\t" \
    "ds_read_b64_tr_b16 v[202:203], v245 offset:10240\n\t" \
    "ds_read_b64_tr_b16 v[18:19], v245 offset:10272\n\t" \
    "ds_read_b64_tr_b16 v[210:211], v245 offset:10304\n\t" \
    "ds_read_b64_tr_b16 v[58:59], v245 offset:10336\n\t" \
    "ds_read_b64_tr_b16 v[218:219], v245 offset:10368\n\t" \
    "ds_read_b64_tr_b16 v[98:99], v245 offset:10400\n\t" \
    "ds_read_b64_tr_b16 v[226:227], v245 offset:10432\n\t" \
    "ds_read_b64_tr_b16 v[138:139], v245 offset:10464\n\t" \
    "ds_read_b64_tr_b16 v[234:235], v245 offset:10496\n\t" \
    "ds_read_b64_tr_b16 v[178:179], v245 offset:10528\n\t" \
    "s_waitcnt lgkmcnt(0)\n\t" \
    "s_add_u32 m0, %[wm], 0x2800\n\t" \
    "s_mov_b32 s44, 0x32000\n\t" \
    "buffer_load_dwordx4 v240, %[wr], s44 offen offset:0 nt lds\n\t" \
    "buffer_load_dwordx4 v241, %[wr], s44 offen offset:1024 nt lds\n\t" \
    "buffer_load_dwordx4 v242, %[wr], s44 offen offset:2048 nt lds\n\t" \
    "buffer_load_dwordx4 v243, %[wr], s44 offen offset:3072 nt lds\n\t" \
    "s_add_u32 m0, %[wm], 0x3800\n\t" \
    "s_nop 0\n\t" \
    "buffer_load_dwordx4 v244, %[wr], s44 offen nt lds\n\t" \
    "s_barrier \n\t" \
    "s_barrier \n\t" \
    "s_waitcnt vmcnt(15)\n\t" \
    "ds_read_b64_tr_b16 v[206:207], v245 offset:15360\n\t" \
    "ds_read_b64_tr_b16 v[22:23], v245 offset:15392\n\t" \
    "ds_read_b64_tr_b16 v[214:215], v245 offset:15424\n\t" \
    "ds_read_b64_tr_b16 v[62:63], v245 offset:15456\n\t" \
    "ds_read_b64_tr_b16 v[222:223], v245 offset:15488\n\t" \
    "ds_read_b64_tr_b16 v[102:103], v245 offset:15520\n\t" \
    "ds_read_b64_tr_b16 v[230:231], v245 offset:15552\n\t" \
    "ds_read_b64_tr_b16 v[142:143], v245 offset:15584\n\t" \
    "ds_read_b64_tr_b16 v[238:239], v245 offset:15616\n\t" \
    "ds_read_b64_tr_b16 v[182:183], v245 offset:15648\n\t" \
    "s_waitcnt lgkmcnt(0)\n\t" \
    "s_add_u32 m0, %[wm], 0x3c00\n\t" \
    "s_mov_b32 s44, 0x37000\n\t" \
    "buffer_load_dwordx4 v240, %[wr], s44 offen offset:0 nt lds\n\t" \
    "buffer_load_dwordx4 v241, %[wr], s44 offen offset:1024 nt lds\n\t" \
    "buffer_load_dwordx4 v242, %[wr], s44 offen offset:2048 nt lds\n\t" \
    "buffer_load_dwordx4 v243, %[wr], s44 offen offset:3072 nt lds\n\t" \
    "s_add_u32 m0, %[wm], 0x4c00\n\t" \
    "s_nop 0\n\t" \
    "buffer_load_dwordx4 v244, %[wr], s44 offen nt lds\n\t" \
    "s_barrier \n\t" \
    "s_barrier \n\t" \
    "s_waitcnt vmcnt(15)\n\t" \
    "ds_read_b64_tr_b16 v[2:3], v245 offset:0\n\t" \
    "ds_read_b64_tr_b16 v[26:27], v245 offset:32\n\t" \
    "ds_read_b64_tr_b16 v[42:43], v245 offset:64\n\t" \
    "ds_read_b64_tr_b16 v[66:67], v245 offset:96\n\t" \
    "ds_read_b64_tr_b16 v[82:83], v245 offset:128\n\t" \
    "ds_read_b64_tr_b16 v[106:107], v245 offset:160\n\t" \
    "ds_read_b64_tr_b16 v[122:123], v245 offset:192\n\t" \
    "ds_read_b64_tr_b16 v[146:147], v245 offset:224\n\t" \
    "ds_read_b64_tr_b16 v[162:163], v245 offset:256\n\t" \
    "ds_read_b64_tr_b16 v[186:187], v245 offset:288\n\t" \
    ".Lv41_%=:\n\t" \
    "v_readfirstlane_b32 s43, v246\n\t" \
    "s_nop 0\n\t" \
    "s_cmp_lg_u32 s43, -1\n\t" \
    "s_cbranch_scc1 .Lv42_%=\n\t" \
    "s_cmp_eq_u32 s41, 0\n\t" \
    "s_cbranch_scc1 .Lv42_%=\n\t" \
    "s_sub_u32 s41, s41, 1\n\t" \
    "s_sleep 4\n\t" \
    "v_mov_b32 v246, 0\n\t" \
    "buffer_load_dword v246, v246, s[60:63], %[mo] offen sc1\n\t" \
    "s_waitcnt vmcnt(0)\n\t" \
    "s_branch .Lv41_%=\n\t" \
    ".Lv42_%=:\n\t" \
    "v_mov_b32 v246, 0x8000\n\t" \
    "s_mov_b64 exec, 1\n\t" \
    "buffer_store_dword v246, v246, s[60:63], %[mo] offen sc1\n\t" \
    "s_mov_b64 exec, -1\n\t" \
    "s_and_b32 s43, s43, 0x2000\n\t" \
    "s_xor_b32 s43, s43, %[lx]\n\t" \
    "s_add_u32 s60, s60, s43\n\t" \
    "s_addc_u32 s61, s61, 0\n\t" \
    "s_barrier \n\t" \
    "s_waitcnt vmcnt(10)\n\t" \
    "ds_read_b64_tr_b16 v[6:7], v245 offset:5120\n\t" \
    "ds_read_b64_tr_b16 v[30:31], v245 offset:5152\n\t" \
    "ds_read_b64_tr_b16 v[46:47], v245 offset:5184\n\t" \
    "ds_read_b64_tr_b16 v[70:71], v245 offset:5216\n\t" \
    "ds_read_b64_tr_b16 v[86:87], v245 offset:5248\n\t" \
    "ds_read_b64_tr_b16 v[110:111], v245 offset:5280\n\t" \
    "ds_read_b64_tr_b16 v[126:127], v245 offset:5312\n\t" \
    "ds_read_b64_tr_b16 v[150:151], v245 offset:5344\n\t" \
    "ds_read_b64_tr_b16 v[166:167], v245 offset:5376\n\t" \
    "ds_read_b64_tr_b16 v[190:191], v245 offset:5408\n\t" \
    "s_barrier \n\t" \
    "s_barrier \n\t" \
    "s_waitcnt vmcnt(5)\n\t" \
    "ds_read_b64_tr_b16 v[10:11], v245 offset:10240\n\t" \
    "ds_read_b64_tr_b16 v[34:35], v245 offset:10272\n\t" \
    "ds_read_b64_tr_b16 v[50:51], v245 offset:10304\n\t" \
    "ds_read_b64_tr_b16 v[74:75], v245 offset:10336\n\t" \
    "ds_read_b64_tr_b16 v[90:91], v245 offset:10368\n\t" \
    "ds_read_b64_tr_b16 v[114:115], v245 offset:10400\n\t" \
    "ds_read_b64_tr_b16 v[130:131], v245 offset:10432\n\t" \
    "ds_read_b64_tr_b16 v[154:155], v245 offset:10464\n\t" \
    "ds_read_b64_tr_b16 v[170:171], v245 offset:10496\n\t" \
    "ds_read_b64_tr_b16 v[194:195], v245 offset:10528\n\t" \
    "s_barrier \n\t" \
    "s_barrier \n\t" \
    "s_waitcnt vmcnt(0)\n\t" \
    "ds_read_b64_tr_b16 v[14:15], v245 offset:15360\n\t" \
    "ds_read_b64_tr_b16 v[38:39], v245 offset:15392\n\t" \
    "ds_read_b64_tr_b16 v[54:55], v245 offset:15424\n\t" \
    "ds_read_b64_tr_b16 v[78:79], v245 offset:15456\n\t" \
    "ds_read_b64_tr_b16 v[94:95], v245 offset:15488\n\t" \
    "ds_read_b64_tr_b16 v[118:119], v245 offset:15520\n\t" \
    "ds_read_b64_tr_b16 v[134:135], v245 offset:15552\n\t" \
    "ds_read_b64_tr_b16 v[158:159], v245 offset:15584\n\t" \
    "ds_read_b64_tr_b16 v[174:175], v245 offset:15616\n\t" \
    "ds_read_b64_tr_b16 v[198:199], v245 offset:15648\n\t" \
    "s_waitcnt lgkmcnt(0)\n\t" \
    "s_barrier \n\t" \
    LST0 \
    "ds_write_b128 v254, v[200:203] offset:0\n\t" \
    "ds_write_b128 v254, v[204:207] offset:1024\n\t" \
    "ds_write_b128 v254, v[208:211] offset:2048\n\t" \
    "ds_write_b128 v254, v[212:215] offset:3072\n\t" \
    "ds_write_b128 v254, v[216:219] offset:4096\n\t" \
    "ds_write_b128 v254, v[220:223] offset:5120\n\t" \
    "ds_write_b128 v254, v[224:227] offset:6144\n\t" \
    "ds_write_b128 v254, v[228:231] offset:7168\n\t" \
    "ds_write_b128 v254, v[232:235] offset:8192\n\t" \
    "ds_write_b128 v254, v[236:239] offset:9216\n\t" \
    "s_waitcnt vmcnt(0) lgkmcnt(0)\n\t" \
    "s_barrier \n\t" \
    "v_mov_b64 v[200:201], 0\n\t" \
    "v_mov_b64 v[202:203], 0\n\t" \
    "ds_write_b128 v253, v[200:203] offset:3072\n\t" \
    "ds_write_b128 v253, v[200:203] offset:4096\n\t" \
    "v_bfe_i32 v252, v253, 5, 1\n\t" \
    "v_and_b32 v252, 0xaaaa, v252\n\t" \
    "v_or_b32 v252, 0x4444, v252\n\t" \
    ".Lv43_%=:\n\t" \
    "ds_read_b128 v[248:251], v254 offset:0\n\t" \
    "s_and_b32 s44, s40, %[vm]\n\t" \
    "s_lshl_b32 s44, s44, 20\n\t" \
    "s_and_b32 s45, s40, 2\n\t" \
    "s_lshl_b32 s45, s45, 12\n\t" \
    "s_add_u32 s44, s44, s45\n\t" \
    "s_add_u32 s42, s44, %[vx]\n\t" \
    "s_waitcnt lgkmcnt(0)\n\t" \
    "s_barrier \n\t" \
    LST7 \
    "ds_read_b128 v[224:227], v253 offset:6144\n\t" \
    "ds_read_b128 v[228:231], v253 offset:12288\n\t" \
    "ds_read_b128 v[232:235], v253 offset:8192\n\t" \
    "ds_read_b128 v[236:239], v253 offset:14336\n\t" \
    "ds_read_b128 v[240:243], v253 offset:9216\n\t" \
    "s_waitcnt lgkmcnt(4)\n\t" \
    "v_mfma_f32_16x16x32_bf16 v[200:203], v[224:227], v[248:251], 0\n\t" \
    "ds_read_b128 v[244:247], v253 offset:15360\n\t" \
    "s_waitcnt lgkmcnt(4)\n\t" \
    "v_mfma_f32_16x16x32_bf16 v[204:207], v[228:231], v[248:251], 0\n\t" \
    "ds_read_b128 v[224:227], v253 offset:7168\n\t" \
    "s_waitcnt lgkmcnt(4)\n\t" \
    "v_mfma_f32_16x16x32_bf16 v[200:203], v[232:235], v[0:3], v[200:203]\n\t" \
    "ds_read_b128 v[228:231], v253 offset:13312\n\t" \
    "ds_read_b128 v[248:251], v254 offset:1024\n\t" \
    "s_waitcnt lgkmcnt(5)\n\t" \
    "v_mfma_f32_16x16x32_bf16 v[204:207], v[236:239], v[0:3], v[204:207]\n\t" \
    "ds_read_b128 v[232:235], v253 offset:10240\n\t" \
    "s_waitcnt lgkmcnt(5)\n\t" \
    "v_mfma_f32_16x16x32_bf16 v[200:203], v[240:243], v[4:7], v[200:203]\n\t" \
    "ds_read_b128 v[236:239], v253 offset:16384\n\t" \
    "s_waitcnt lgkmcnt(5)\n\t" \
    "v_mfma_f32_16x16x32_bf16 v[204:207], v[244:247], v[4:7], v[204:207]\n\t" \
    "ds_read_b128 v[240:243], v253 offset:11264\n\t" \
    "s_waitcnt lgkmcnt(3)\n\t" \
    "v_mfma_f32_16x16x32_bf16 v[200:203], v[224:227], v[248:251], v[200:203]\n\t" \
    "ds_read_b128 v[244:247], v253 offset:17408\n\t" \
    "v_mfma_f32_16x16x32_bf16 v[208:211], v[224:227], v[20:23], 0\n\t" \
    "v_mfma_f32_16x16x32_bf16 v[204:207], v[228:231], v[248:251], v[204:207]\n\t" \
    "ds_read_b128 v[224:227], v253 offset:6144\n\t" \
    "v_mfma_f32_16x16x32_bf16 v[212:215], v[228:231], v[20:23], 0\n\t" \
    "s_waitcnt lgkmcnt(4)\n\t" \
    "v_mfma_f32_16x16x32_bf16 v[200:203], v[232:235], v[8:11], v[200:203]\n\t" \
    "ds_read_b128 v[228:231], v253 offset:12288\n\t" \
    "ds_read_b128 v[248:251], v254 offset:2048\n\t" \
    "v_mfma_f32_16x16x32_bf16 v[208:211], v[232:235], v[32:35], v[208:211]\n\t" \
    "s_waitcnt lgkmcnt(5)\n\t" \
    "v_mfma_f32_16x16x32_bf16 v[204:207], v[236:239], v[8:11], v[204:207]\n\t" \
    "ds_read_b128 v[232:235], v253 offset:8192\n\t" \
    "v_mfma_f32_16x16x32_bf16 v[212:215], v[236:239], v[32:35], v[212:215]\n\t" \
    "s_waitcnt lgkmcnt(5)\n\t" \
    "v_mfma_f32_16x16x32_bf16 v[200:203], v[240:243], v[12:15], v[200:203]\n\t" \
    "ds_read_b128 v[236:239], v253 offset:14336\n\t" \
    "v_mfma_f32_16x16x32_bf16 v[208:211], v[240:243], v[36:39], v[208:211]\n\t" \
    "s_waitcnt lgkmcnt(5)\n\t" \
    "v_mfma_f32_16x16x32_bf16 v[204:207], v[244:247], v[12:15], v[204:207]\n\t" \
    "ds_read_b128 v[240:243], v253 offset:9216\n\t" \
    "v_mfma_f32_16x16x32_bf16 v[212:215], v[244:247], v[36:39], v[212:215]\n\t" \
    "s_waitcnt lgkmcnt(3)\n\t" \
    "v_mfma_f32_16x16x32_bf16 v[216:219], v[224:227], v[248:251], 0\n\t" \
    "ds_read_b128 v[244:247], v253 offset:15360\n\t" \
    "v_mfma_f32_16x16x32_bf16 v[208:211], v[224:227], v[16:19], v[208:211]\n\t" \
    "v_mfma_f32_16x16x32_bf16 v[220:223], v[228:231], v[248:251], 0\n\t" \
    "ds_read_b128 v[224:227], v253 offset:7168\n\t" \
    "v_mfma_f32_16x16x32_bf16 v[212:215], v[228:231], v[16:19], v[212:215]\n\t" \
    "v_cvt_pk_bf16_f32 v200, v200, v204\n\t" \
    "s_waitcnt lgkmcnt(4)\n\t" \
    "v_mfma_f32_16x16x32_bf16 v[216:219], v[232:235], v[40:43], v[216:219]\n\t" \
    "ds_read_b128 v[228:231], v253 offset:13312\n\t" \
    "ds_read_b128 v[248:251], v254 offset:3072\n\t" \
    "v_cvt_pk_bf16_f32 v201, v201, v205\n\t" \
    "v_mfma_f32_16x16x32_bf16 v[208:211], v[232:235], v[24:27], v[208:211]\n\t" \
    "v_cvt_pk_bf16_f32 v202, v202, v206\n\t" \
    "s_waitcnt lgkmcnt(5)\n\t" \
    "v_mfma_f32_16x16x32_bf16 v[220:223], v[236:239], v[40:43], v[220:223]\n\t" \
    "ds_read_b128 v[232:235], v253 offset:10240\n\t" \
    "v_cvt_pk_bf16_f32 v203, v203, v207\n\t" \
    "v_mfma_f32_16x16x32_bf16 v[212:215], v[236:239], v[24:27], v[212:215]\n\t" \
    "buffer_store_dwordx4 v[200:203], v253, s[60:63], s42 offen sc1\n\t" \
    "s_waitcnt lgkmcnt(5)\n\t" \
    "v_mfma_f32_16x16x32_bf16 v[216:219], v[240:243], v[44:47], v[216:219]\n\t" \
    "ds_read_b128 v[236:239], v253 offset:16384\n\t" \
    "s_add_u32 s42, s42, 0x10000\n\t" \
    "v_mfma_f32_16x16x32_bf16 v[208:211], v[240:243], v[28:31], v[208:211]\n\t" \
    "s_waitcnt lgkmcnt(5)\n\t" \
    "v_mfma_f32_16x16x32_bf16 v[220:223], v[244:247], v[44:47], v[220:223]\n\t" \
    "ds_read_b128 v[240:243], v253 offset:11264\n\t" \
    "v_mfma_f32_16x16x32_bf16 v[212:215], v[244:247], v[28:31], v[212:215]\n\t" \
    "s_waitcnt lgkmcnt(3)\n\t" \
    "v_mfma_f32_16x16x32_bf16 v[216:219], v[224:227], v[248:251], v[216:219]\n\t" \
    "ds_read_b128 v[244:247], v253 offset:17408\n\t" \
    "v_mfma_f32_16x16x32_bf16 v[200:203], v[224:227], v[60:63], 0\n\t" \
    "v_mfma_f32_16x16x32_bf16 v[220:223], v[228:231], v[248:251], v[220:223]\n\t" \
    "ds_read_b128 v[224:227], v253 offset:6144\n\t" \
    "v_mfma_f32_16x16x32_bf16 v[204:207], v[228:231], v[60:63], 0\n\t" \
    "v_cvt_pk_bf16_f32 v208, v208, v212\n\t" \
    "s_waitcnt lgkmcnt(4)\n\t" \
    "v_mfma_f32_16x16x32_bf16 v[216:219], v[232:235], v[48:51], v[216:219]\n\t" \
    "ds_read_b128 v[228:231], v253 offset:12288\n\t" \
    "ds_read_b128 v[248:251], v254 offset:4096\n\t" \
    "v_cvt_pk_bf16_f32 v209, v209, v213\n\t" \
    "v_mfma_f32_16x16x32_bf16 v[200:203], v[232:235], v[72:75], v[200:203]\n\t" \
    "v_cvt_pk_bf16_f32 v210, v210, v214\n\t" \
    "s_waitcnt lgkmcnt(5)\n\t" \
    "v_mfma_f32_16x16x32_bf16 v[220:223], v[236:239], v[48:51], v[220:223]\n\t" \
    "ds_read_b128 v[232:235], v253 offset:8192\n\t" \
    "v_cvt_pk_bf16_f32 v211, v211, v215\n\t" \
    "v_mfma_f32_16x16x32_bf16 v[204:207], v[236:239], v[72:75], v[204:207]\n\t" \
    "buffer_store_dwordx4 v[208:211], v253, s[60:63], s42 offen sc1\n\t" \
    "s_waitcnt lgkmcnt(5)\n\t" \
    "v_mfma_f32_16x16x32_bf16 v[216:219], v[240:243], v[52:55], v[216:219]\n\t" \
    "ds_read_b128 v[236:239], v253 offset:14336\n\t" \
    "s_add_u32 s42, s42, 0x10000\n\t" \
    "v_mfma_f32_16x16x32_bf16 v[200:203], v[240:243], v[76:79], v[200:203]\n\t" \
    "s_waitcnt lgkmcnt(5)\n\t" \
    "v_mfma_f32_16x16x32_bf16 v[220:223], v[244:247], v[52:55], v[220:223]\n\t" \
    "ds_read_b128 v[240:243], v253 offset:9216\n\t" \
    "v_mfma_f32_16x16x32_bf16 v[204:207], v[244:247], v[76:79], v[204:207]\n\t" \
    "s_waitcnt lgkmcnt(3)\n\t" \
    "v_mfma_f32_16x16x32_bf16 v[208:211], v[224:227], v[248:251], 0\n\t" \
    "ds_read_b128 v[244:247], v253 offset:15360\n\t" \
    "v_mfma_f32_16x16x32_bf16 v[200:203], v[224:227], v[56:59], v[200:203]\n\t" \
    "v_mfma_f32_16x16x32_bf16 v[212:215], v[228:231], v[248:251], 0\n\t" \
    "ds_read_b128 v[224:227], v253 offset:7168\n\t" \
    "v_mfma_f32_16x16x32_bf16 v[204:207], v[228:231], v[56:59], v[204:207]\n\t" \
    "v_cvt_pk_bf16_f32 v216, v216, v220\n\t" \
    "s_waitcnt lgkmcnt(4)\n\t" \
    "v_mfma_f32_16x16x32_bf16 v[208:211], v[232:235], v[80:83], v[208:211]\n\t" \
    "ds_read_b128 v[228:231], v253 offset:13312\n\t" \
    "ds_read_b128 v[248:251], v254 offset:5120\n\t" \
    "v_cvt_pk_bf16_f32 v217, v217, v221\n\t" \
    "v_mfma_f32_16x16x32_bf16 v[200:203], v[232:235], v[64:67], v[200:203]\n\t" \
    "v_cvt_pk_bf16_f32 v218, v218, v222\n\t" \
    "s_waitcnt lgkmcnt(5)\n\t" \
    "v_mfma_f32_16x16x32_bf16 v[212:215], v[236:239], v[80:83], v[212:215]\n\t" \
    "ds_read_b128 v[232:235], v253 offset:10240\n\t" \
    "v_cvt_pk_bf16_f32 v219, v219, v223\n\t" \
    "v_mfma_f32_16x16x32_bf16 v[204:207], v[236:239], v[64:67], v[204:207]\n\t" \
    "buffer_store_dwordx4 v[216:219], v253, s[60:63], s42 offen sc1\n\t" \
    "s_waitcnt lgkmcnt(5)\n\t" \
    "v_mfma_f32_16x16x32_bf16 v[208:211], v[240:243], v[84:87], v[208:211]\n\t" \
    "ds_read_b128 v[236:239], v253 offset:16384\n\t" \
    "s_add_u32 s42, s42, 0x10000\n\t" \
    "v_mfma_f32_16x16x32_bf16 v[200:203], v[240:243], v[68:71], v[200:203]\n\t" \
    "s_waitcnt lgkmcnt(5)\n\t" \
    "v_mfma_f32_16x16x32_bf16 v[212:215], v[244:247], v[84:87], v[212:215]\n\t" \
    "ds_read_b128 v[240:243], v253 offset:11264\n\t" \
    "v_mfma_f32_16x16x32_bf16 v[204:207], v[244:247], v[68:71], v[204:207]\n\t" \
    "s_waitcnt lgkmcnt(3)\n\t" \
    "v_mfma_f32_16x16x32_bf16 v[208:211], v[224:227], v[248:251], v[208:211]\n\t" \
    "ds_read_b128 v[244:247], v253 offset:17408\n\t" \
    "v_mfma_f32_16x16x32_bf16 v[216:219], v[224:227], v[100:103], 0\n\t" \
    "v_mfma_f32_16x16x32_bf16 v[212:215], v[228:231], v[248:251], v[212:215]\n\t" \
    "ds_read_b128 v[224:227], v253 offset:6144\n\t" \
    "v_mfma_f32_16x16x32_bf16 v[220:223], v[228:231], v[100:103], 0\n\t" \
    "v_cvt_pk_bf16_f32 v200, v200, v204\n\t" \
    "s_waitcnt lgkmcnt(4)\n\t" \
    "v_mfma_f32_16x16x32_bf16 v[208:211], v[232:235], v[88:91], v[208:211]\n\t" \
    "ds_read_b128 v[228:231], v253 offset:12288\n\t" \
    "ds_read_b128 v[248:251], v254 offset:6144\n\t" \
    "v_cvt_pk_bf16_f32 v201, v201, v205\n\t" \
    "v_mfma_f32_16x16x32_bf16 v[216:219], v[232:235], v[112:115], v[216:219]\n\t" \
    "v_cvt_pk_bf16_f32 v202, v202, v206\n\t" \
    "s_waitcnt lgkmcnt(5)\n\t" \
    "v_mfma_f32_16x16x32_bf16 v[212:215], v[236:239], v[88:91], v[212:215]\n\t" \
    "ds_read_b128 v[232:235], v253 offset:8192\n\t" \
    "v_cvt_pk_bf16_f32 v203, v203, v207\n\t" \
    "v_mfma_f32_16x16x32_bf16 v[220:223], v[236:239], v[112:115], v[220:223]\n\t" \
    "buffer_store_dwordx4 v[200:203], v253, s[60:63], s42 offen sc1\n\t" \
    "s_waitcnt lgkmcnt(5)\n\t" \
    "v_mfma_f32_16x16x32_bf16 v[208:211], v[240:243], v[92:95], v[208:211]\n\t" \
    "ds_read_b128 v[236:239], v253 offset:14336\n\t" \
    "s_add_u32 s42, s42, 0x10000\n\t" \
    "v_mfma_f32_16x16x32_bf16 v[216:219], v[240:243], v[116:119], v[216:219]\n\t" \
    "s_waitcnt lgkmcnt(5)\n\t" \
    "v_mfma_f32_16x16x32_bf16 v[212:215], v[244:247], v[92:95], v[212:215]\n\t" \
    "ds_read_b128 v[240:243], v253 offset:9216\n\t" \
    "v_mfma_f32_16x16x32_bf16 v[220:223], v[244:247], v[116:119], v[220:223]\n\t" \
    "s_waitcnt lgkmcnt(3)\n\t" \
    "v_mfma_f32_16x16x32_bf16 v[200:203], v[224:227], v[248:251], 0\n\t" \
    "ds_read_b128 v[244:247], v253 offset:15360\n\t" \
    "v_mfma_f32_16x16x32_bf16 v[216:219], v[224:227], v[96:99], v[216:219]\n\t" \
    "v_mfma_f32_16x16x32_bf16 v[204:207], v[228:231], v[248:251], 0\n\t" \
    "ds_read_b128 v[224:227], v253 offset:7168\n\t" \
    "v_mfma_f32_16x16x32_bf16 v[220:223], v[228:231], v[96:99], v[220:223]\n\t" \
    "v_cvt_pk_bf16_f32 v208, v208, v212\n\t" \
    "s_waitcnt lgkmcnt(4)\n\t" \
    "v_mfma_f32_16x16x32_bf16 v[200:203], v[232:235], v[120:123], v[200:203]\n\t" \
    "ds_read_b128 v[228:231], v253 offset:13312\n\t" \
    "ds_read_b128 v[248:251], v254 offset:7168\n\t" \
    "v_cvt_pk_bf16_f32 v209, v209, v213\n\t" \
    "v_mfma_f32_16x16x32_bf16 v[216:219], v[232:235], v[104:107], v[216:219]\n\t" \
    "v_cvt_pk_bf16_f32 v210, v210, v214\n\t" \
    "s_waitcnt lgkmcnt(5)\n\t" \
    "v_mfma_f32_16x16x32_bf16 v[204:207], v[236:239], v[120:123], v[204:207]\n\t" \
    "ds_read_b128 v[232:235], v253 offset:10240\n\t" \
    "v_cvt_pk_bf16_f32 v211, v211, v215\n\t" \
    "v_mfma_f32_16x16x32_bf16 v[220:223], v[236:239], v[104:107], v[220:223]\n\t" \
    "buffer_store_dwordx4 v[208:211], v253, s[60:63], s42 offen sc1\n\t" \
    "s_waitcnt lgkmcnt(5)\n\t" \
    "v_mfma_f32_16x16x32_bf16 v[200:203], v[240:243], v[124:127], v[200:203]\n\t" \
    "ds_read_b128 v[236:239], v253 offset:16384\n\t" \
    "s_add_u32 s42, s42, 0x10000\n\t" \
    "v_mfma_f32_16x16x32_bf16 v[216:219], v[240:243], v[108:111], v[216:219]\n\t" \
    "s_waitcnt lgkmcnt(5)\n\t" \
    "v_mfma_f32_16x16x32_bf16 v[204:207], v[244:247], v[124:127], v[204:207]\n\t" \
    "ds_read_b128 v[240:243], v253 offset:11264\n\t" \
    "v_mfma_f32_16x16x32_bf16 v[220:223], v[244:247], v[108:111], v[220:223]\n\t" \
    "s_waitcnt lgkmcnt(3)\n\t" \
    "v_mfma_f32_16x16x32_bf16 v[200:203], v[224:227], v[248:251], v[200:203]\n\t" \
    "ds_read_b128 v[244:247], v253 offset:17408\n\t" \
    "v_mfma_f32_16x16x32_bf16 v[208:211], v[224:227], v[140:143], 0\n\t" \
    "v_mfma_f32_16x16x32_bf16 v[204:207], v[228:231], v[248:251], v[204:207]\n\t" \
    "ds_read_b128 v[224:227], v253 offset:6144\n\t" \
    "v_mfma_f32_16x16x32_bf16 v[212:215], v[228:231], v[140:143], 0\n\t" \
    "v_cvt_pk_bf16_f32 v216, v216, v220\n\t" \
    "s_waitcnt lgkmcnt(4)\n\t" \
    "v_mfma_f32_16x16x32_bf16 v[200:203], v[232:235], v[128:131], v[200:203]\n\t" \
    "ds_read_b128 v[228:231], v253 offset:12288\n\t" \
    "ds_read_b128 v[248:251], v254 offset:8192\n\t" \
    "v_cvt_pk_bf16_f32 v217, v217, v221\n\t" \
    "v_mfma_f32_16x16x32_bf16 v[208:211], v[232:235], v[152:155], v[208:211]\n\t" \
    "v_cvt_pk_bf16_f32 v218, v218, v222\n\t" \
    "s_waitcnt lgkmcnt(5)\n\t" \
    "v_mfma_f32_16x16x32_bf16 v[204:207], v[236:239], v[128:131], v[204:207]\n\t" \
    "ds_read_b128 v[232:235], v253 offset:8192\n\t" \
    "v_cvt_pk_bf16_f32 v219, v219, v223\n\t" \
    "v_mfma_f32_16x16x32_bf16 v[212:215], v[236:239], v[152:155], v[212:215]\n\t" \
    "buffer_store_dwordx4 v[216:219], v253, s[60:63], s42 offen sc1\n\t" \
    "s_waitcnt lgkmcnt(5)\n\t" \
    "v_mfma_f32_16x16x32_bf16 v[200:203], v[240:243], v[132:135], v[200:203]\n\t" \
    "ds_read_b128 v[236:239], v253 offset:14336\n\t" \
    "s_add_u32 s42, s42, 0x10000\n\t" \
    "v_mfma_f32_16x16x32_bf16 v[208:211], v[240:243], v[156:159], v[208:211]\n\t" \
    "s_waitcnt lgkmcnt(5)\n\t" \
    "v_mfma_f32_16x16x32_bf16 v[204:207], v[244:247], v[132:135], v[204:207]\n\t" \
    "ds_read_b128 v[240:243], v253 offset:9216\n\t" \
    "v_mfma_f32_16x16x32_bf16 v[212:215], v[244:247], v[156:159], v[212:215]\n\t" \
    "s_waitcnt lgkmcnt(3)\n\t" \
    "v_mfma_f32_16x16x32_bf16 v[216:219], v[224:227], v[248:251], 0\n\t" \
    "ds_read_b128 v[244:247], v253 offset:15360\n\t" \
    "v_mfma_f32_16x16x32_bf16 v[208:211], v[224:227], v[136:139], v[208:211]\n\t" \
    "v_mfma_f32_16x16x32_bf16 v[220:223], v[228:231], v[248:251], 0\n\t" \
    "ds_read_b128 v[224:227], v253 offset:7168\n\t" \
    "v_mfma_f32_16x16x32_bf16 v[212:215], v[228:231], v[136:139], v[212:215]\n\t" \
    "v_cvt_pk_bf16_f32 v200, v200, v204\n\t" \
    "s_waitcnt lgkmcnt(4)\n\t" \
    "v_mfma_f32_16x16x32_bf16 v[216:219], v[232:235], v[160:163], v[216:219]\n\t" \
    "ds_read_b128 v[228:231], v253 offset:13312\n\t" \
    "ds_read_b128 v[248:251], v254 offset:9216\n\t" \
    "v_cvt_pk_bf16_f32 v201, v201, v205\n\t" \
    "v_mfma_f32_16x16x32_bf16 v[208:211], v[232:235], v[144:147], v[208:211]\n\t" \
    "v_cvt_pk_bf16_f32 v202, v202, v206\n\t" \
    "s_waitcnt lgkmcnt(5)\n\t" \
    "v_mfma_f32_16x16x32_bf16 v[220:223], v[236:239], v[160:163], v[220:223]\n\t" \
    "ds_read_b128 v[232:235], v253 offset:10240\n\t" \
    "v_cvt_pk_bf16_f32 v203, v203, v207\n\t" \
    "v_mfma_f32_16x16x32_bf16 v[212:215], v[236:239], v[144:147], v[212:215]\n\t" \
    "buffer_store_dwordx4 v[200:203], v253, s[60:63], s42 offen sc1\n\t" \
    "s_waitcnt lgkmcnt(5)\n\t" \
    "v_mfma_f32_16x16x32_bf16 v[216:219], v[240:243], v[164:167], v[216:219]\n\t" \
    "ds_read_b128 v[236:239], v253 offset:16384\n\t" \
    "s_add_u32 s42, s42, 0x10000\n\t" \
    "v_mfma_f32_16x16x32_bf16 v[208:211], v[240:243], v[148:151], v[208:211]\n\t" \
    "s_waitcnt lgkmcnt(5)\n\t" \
    "v_mfma_f32_16x16x32_bf16 v[220:223], v[244:247], v[164:167], v[220:223]\n\t" \
    "ds_read_b128 v[240:243], v253 offset:11264\n\t" \
    "v_mfma_f32_16x16x32_bf16 v[212:215], v[244:247], v[148:151], v[212:215]\n\t" \
    "s_waitcnt lgkmcnt(3)\n\t" \
    "v_mfma_f32_16x16x32_bf16 v[216:219], v[224:227], v[248:251], v[216:219]\n\t" \
    "ds_read_b128 v[244:247], v253 offset:17408\n\t" \
    "v_mfma_f32_16x16x32_bf16 v[200:203], v[224:227], v[180:183], 0\n\t" \
    "v_mfma_f32_16x16x32_bf16 v[220:223], v[228:231], v[248:251], v[220:223]\n\t" \
    "ds_read_b128 v[224:227], v253 offset:6144\n\t" \
    "v_mfma_f32_16x16x32_bf16 v[204:207], v[228:231], v[180:183], 0\n\t" \
    "v_cvt_pk_bf16_f32 v208, v208, v212\n\t" \
    "s_waitcnt lgkmcnt(4)\n\t" \
    "v_mfma_f32_16x16x32_bf16 v[216:219], v[232:235], v[168:171], v[216:219]\n\t" \
    "ds_read_b128 v[228:231], v253 offset:12288\n\t" \
    "v_cvt_pk_bf16_f32 v209, v209, v213\n\t" \
    "v_mfma_f32_16x16x32_bf16 v[200:203], v[232:235], v[192:195], v[200:203]\n\t" \
    "v_cvt_pk_bf16_f32 v210, v210, v214\n\t" \
    "s_waitcnt lgkmcnt(4)\n\t" \
    "v_mfma_f32_16x16x32_bf16 v[220:223], v[236:239], v[168:171], v[220:223]\n\t" \
    "ds_read_b128 v[232:235], v253 offset:8192\n\t" \
    "v_cvt_pk_bf16_f32 v211, v211, v215\n\t" \
    "v_mfma_f32_16x16x32_bf16 v[204:207], v[236:239], v[192:195], v[204:207]\n\t" \
    "buffer_store_dwordx4 v[208:211], v253, s[60:63], s42 offen sc1\n\t" \
    "s_waitcnt lgkmcnt(4)\n\t" \
    "v_mfma_f32_16x16x32_bf16 v[216:219], v[240:243], v[172:175], v[216:219]\n\t" \
    "ds_read_b128 v[236:239], v253 offset:14336\n\t" \
    "s_add_u32 s42, s42, 0x10000\n\t" \
    "v_mfma_f32_16x16x32_bf16 v[200:203], v[240:243], v[196:199], v[200:203]\n\t" \
    "s_waitcnt lgkmcnt(4)\n\t" \
    "v_mfma_f32_16x16x32_bf16 v[220:223], v[244:247], v[172:175], v[220:223]\n\t" \
    "ds_read_b128 v[240:243], v253 offset:9216\n\t" \
    "v_mfma_f32_16x16x32_bf16 v[204:207], v[244:247], v[196:199], v[204:207]\n\t" \
    "s_waitcnt lgkmcnt(4)\n\t" \
    "v_mfma_f32_16x16x32_bf16 v[200:203], v[224:227], v[176:179], v[200:203]\n\t" \
    "ds_read_b128 v[244:247], v253 offset:15360\n\t" \
    "s_waitcnt lgkmcnt(4)\n\t" \
    "v_mfma_f32_16x16x32_bf16 v[204:207], v[228:231], v[176:179], v[204:207]\n\t" \
    "s_waitcnt lgkmcnt(3)\n\t" \
    "v_mfma_f32_16x16x32_bf16 v[200:203], v[232:235], v[184:187], v[200:203]\n\t" \
    "s_waitcnt lgkmcnt(2)\n\t" \
    "v_mfma_f32_16x16x32_bf16 v[204:207], v[236:239], v[184:187], v[204:207]\n\t" \
    "v_cvt_pk_bf16_f32 v216, v216, v220\n\t" \
    "s_waitcnt lgkmcnt(1)\n\t" \
    "v_mfma_f32_16x16x32_bf16 v[200:203], v[240:243], v[188:191], v[200:203]\n\t" \
    "v_cvt_pk_bf16_f32 v217, v217, v221\n\t" \
    "s_waitcnt lgkmcnt(0)\n\t" \
    "v_mfma_f32_16x16x32_bf16 v[204:207], v[244:247], v[188:191], v[204:207]\n\t" \
    "v_cvt_pk_bf16_f32 v218, v218, v222\n\t" \
    "v_cvt_pk_bf16_f32 v219, v219, v223\n\t" \
    "buffer_store_dwordx4 v[216:219], v253, s[60:63], s42 offen sc1\n\t" \
    "s_add_u32 s42, s42, 0x10000\n\t" \
    "s_nop 4\n\t" \
    "v_cvt_pk_bf16_f32 v200, v200, v204\n\t" \
    "v_cvt_pk_bf16_f32 v201, v201, v205\n\t" \
    "v_cvt_pk_bf16_f32 v202, v202, v206\n\t" \
    "v_cvt_pk_bf16_f32 v203, v203, v207\n\t" \
    "buffer_store_dwordx4 v[200:203], v253, s[60:63], s42 offen sc1\n\t" \
    "s_add_u32 s42, s42, 0x10000\n\t" \
    LST0 \
    "s_waitcnt lgkmcnt(0)\n\t" \
    "s_barrier \n\t" \
    LST3 \
    "ds_read_b128 v[224:227], v253 offset:3072\n\t" \
    "ds_read_b128 v[228:231], v253 offset:3072\n\t" \
    "ds_read_b128 v[232:235], v253 offset:3072\n\t" \
    "ds_read_b128 v[236:239], v253 offset:3072\n\t" \
    "ds_read_b128 v[240:243], v253 offset:3072\n\t" \
    "ds_read_b128 v[244:247], v253 offset:3072\n\t" \
    "ds_read_b128 v[248:251], v253 offset:3072\n\t" \
    "s_add_u32 s44, s40, 1\n\t" \
    "s_mov_b32 s45, s44\n\t" \
    "s_waitcnt vmcnt(0)\n\t" \
    "v_mov_b64 v[200:201], s[44:45]\n\t" \
    "v_mov_b64 v[202:203], s[44:45]\n\t" \
    "s_mov_b64 exec, 0xff\n\t" \
    "buffer_store_dwordx4 v[200:203], v253, s[60:63], %[fo] offen sc1\n\t" \
    "s_mov_b64 exec, -1\n\t" \
    LST1 \
    "ds_read_b128 v[220:223], v253 offset:3072\n\t" \
    "ds_read_b128 v[212:215], v254 offset:0\n\t" \
    "ds_read_b128 v[216:219], v254 offset:1024\n\t" \
    "s_and_b32 s44, s40, 3\n\t" \
    "s_mul_i32 s44, s44, 0x202000\n\t" \
    "s_add_u32 s42, s44, %[vo]\n\t" \
    "s_setprio 3\n\t" \
    "s_waitcnt lgkmcnt(0)\n\t" \
    "s_barrier \n\t" \
    LST4 \
    "ds_read_b128 v[204:207], v253 offset:1024\n\t" \
    "ds_read_b128 v[208:211], v253 offset:2048\n\t" \
    "ds_read_b128 v[200:203], v253 offset:0\n\t" \
    "s_waitcnt lgkmcnt(2)\n\t" \
    "v_smfmac_f32_16x16x64_bf16 v[224:227], v[204:207], v[24:31], v252\n\t" \
    "v_smfmac_f32_16x16x64_bf16 v[220:223], v[204:207], v[0:7], v252\n\t" \
    "s_waitcnt lgkmcnt(1)\n\t" \
    "v_smfmac_f32_16x16x64_bf16 v[224:227], v[208:211], v[32:39], v252\n\t" \
    "v_smfmac_f32_16x16x64_bf16 v[220:223], v[208:211], v[8:15], v252\n\t" \
    "s_waitcnt lgkmcnt(0)\n\t" \
    "v_smfmac_f32_16x16x64_bf16 v[224:227], v[200:203], v[16:23], v252\n\t" \
    "v_smfmac_f32_16x16x64_bf16 v[220:223], v[200:203], v[212:219], v252\n\t" \
    "ds_read_b128 v[212:215], v254 offset:2048\n\t" \
    "ds_read_b128 v[216:219], v254 offset:3072\n\t" \
    "v_smfmac_f32_16x16x64_bf16 v[232:235], v[204:207], v[64:71], v252\n\t" \
    "v_smfmac_f32_16x16x64_bf16 v[228:231], v[204:207], v[40:47], v252\n\t" \
    "v_smfmac_f32_16x16x64_bf16 v[232:235], v[208:211], v[72:79], v252\n\t" \
    "v_add_f32 v220, v220, v222\n\t" \
    "v_add_f32 v221, v221, v223\n\t" \
    "v_cvt_pk_bf16_f32 v220, v220, v221\n\t" \
    "v_smfmac_f32_16x16x64_bf16 v[228:231], v[208:211], v[48:55], v252\n\t" \
    "v_min_u32 v220, -2, v220\n\t" \
    "v_add_f32 v224, v224, v226\n\t" \
    "v_add_f32 v225, v225, v227\n\t" \
    "v_smfmac_f32_16x16x64_bf16 v[232:235], v[200:203], v[56:63], v252\n\t" \
    "v_cvt_pk_bf16_f32 v221, v224, v225\n\t" \
    "v_min_u32 v221, -2, v221\n\t" \
    "s_waitcnt lgkmcnt(0)\n\t" \
    "v_smfmac_f32_16x16x64_bf16 v[228:231], v[200:203], v[212:219], v252\n\t" \
    "ds_read_b128 v[212:215], v254 offset:4096\n\t" \
    "ds_read_b128 v[216:219], v254 offset:5120\n\t" \
    "v_smfmac_f32_16x16x64_bf16 v[240:243], v[204:207], v[104:111], v252\n\t" \
    "v_smfmac_f32_16x16x64_bf16 v[236:239], v[204:207], v[80:87], v252\n\t" \
    "v_smfmac_f32_16x16x64_bf16 v[240:243], v[208:211], v[112:119], v252\n\t" \
    "v_add_f32 v228, v228, v230\n\t" \
    "v_add_f32 v229, v229, v231\n\t" \
    "v_cvt_pk_bf16_f32 v222, v228, v229\n\t" \
    "v_smfmac_f32_16x16x64_bf16 v[236:239], v[208:211], v[88:95], v252\n\t" \
    "v_min_u32 v222, -2, v222\n\t" \
    "v_add_f32 v232, v232, v234\n\t" \
    "v_add_f32 v233, v233, v235\n\t" \
    "v_smfmac_f32_16x16x64_bf16 v[240:243], v[200:203], v[96:103], v252\n\t" \
    "v_cvt_pk_bf16_f32 v223, v232, v233\n\t" \
    "v_min_u32 v223, -2, v223\n\t" \
    "v_permlane32_swap_b32 v220, v222\n\t" \
    "s_waitcnt lgkmcnt(0)\n\t" \
    "v_smfmac_f32_16x16x64_bf16 v[236:239], v[200:203], v[212:219], v252\n\t" \
    "ds_read_b128 v[212:215], v254 offset:6144\n\t" \
    "ds_read_b128 v[216:219], v254 offset:7168\n\t" \
    "s_nop 0\n\t" \
    "v_permlane32_swap_b32 v221, v223\n\t" \
    "s_nop 1\n\t" \
    "v_permlane16_swap_b32 v220, v221\n\t" \
    "v_permlane16_swap_b32 v222, v223\n\t" \
    "buffer_store_dwordx4 v[220:223], v255, s[60:63], s42 offen sc1\n\t" \
    "s_add_u32 s42, s42, 0x10000\n\t" \
    "ds_read_b128 v[224:227], v253 offset:3072\n\t" \
    "ds_read_b128 v[228:231], v253 offset:3072\n\t" \
    "v_smfmac_f32_16x16x64_bf16 v[248:251], v[204:207], v[144:151], v252\n\t" \
    "v_smfmac_f32_16x16x64_bf16 v[244:247], v[204:207], v[120:127], v252\n\t" \
    "v_smfmac_f32_16x16x64_bf16 v[248:251], v[208:211], v[152:159], v252\n\t" \
    "v_add_f32 v236, v236, v238\n\t" \
    "v_add_f32 v237, v237, v239\n\t" \
    "v_cvt_pk_bf16_f32 v236, v236, v237\n\t" \
    "v_smfmac_f32_16x16x64_bf16 v[244:247], v[208:211], v[128:135], v252\n\t" \
    "v_min_u32 v236, -2, v236\n\t" \
    "v_add_f32 v240, v240, v242\n\t" \
    "v_add_f32 v241, v241, v243\n\t" \
    "v_smfmac_f32_16x16x64_bf16 v[248:251], v[200:203], v[136:143], v252\n\t" \
    "v_cvt_pk_bf16_f32 v237, v240, v241\n\t" \
    "v_min_u32 v237, -2, v237\n\t" \
    "s_waitcnt lgkmcnt(2)\n\t" \
    "v_smfmac_f32_16x16x64_bf16 v[244:247], v[200:203], v[212:219], v252\n\t" \
    "ds_read_b128 v[212:215], v254 offset:8192\n\t" \
    "ds_read_b128 v[216:219], v254 offset:9216\n\t" \
    "s_waitcnt lgkmcnt(2)\n\t" \
    "v_smfmac_f32_16x16x64_bf16 v[228:231], v[204:207], v[184:191], v252\n\t" \
    "v_smfmac_f32_16x16x64_bf16 v[224:227], v[204:207], v[160:167], v252\n\t" \
    "v_smfmac_f32_16x16x64_bf16 v[228:231], v[208:211], v[192:199], v252\n\t" \
    "v_and_b32 v232, 0x7fff, v255\n\t" \
    "v_lshrrev_b32 v233, 15, v255\n\t" \
    "v_lshl_add_u32 v232, v233, 3, v232\n\t" \
    "v_smfmac_f32_16x16x64_bf16 v[224:227], v[208:211], v[168:175], v252\n\t" \
    "v_add_f32 v244, v244, v246\n\t" \
    "v_add_f32 v245, v245, v247\n\t" \
    "v_cvt_pk_bf16_f32 v238, v244, v245\n\t" \
    "v_smfmac_f32_16x16x64_bf16 v[228:231], v[200:203], v[176:183], v252\n\t" \
    "v_min_u32 v238, -2, v238\n\t" \
    "v_add_f32 v248, v248, v250\n\t" \
    "v_add_f32 v249, v249, v251\n\t" \
    "s_waitcnt lgkmcnt(0)\n\t" \
    "v_smfmac_f32_16x16x64_bf16 v[224:227], v[200:203], v[212:219], v252\n\t" \
    "v_cvt_pk_bf16_f32 v239, v248, v249\n\t" \
    "v_min_u32 v239, -2, v239\n\t" \
    "v_permlane32_swap_b32 v236, v238\n\t" \
    "s_nop 0\n\t" \
    "v_permlane32_swap_b32 v237, v239\n\t" \
    "s_nop 1\n\t" \
    "v_permlane16_swap_b32 v236, v237\n\t" \
    "v_permlane16_swap_b32 v238, v239\n\t" \
    "buffer_store_dwordx4 v[236:239], v255, s[60:63], s42 offen sc1\n\t" \
    "s_add_u32 s42, s42, 0x10000\n\t" \
    "v_add_f32 v228, v228, v230\n\t" \
    "v_add_f32 v229, v229, v231\n\t" \
    "v_add_f32 v224, v224, v226\n\t" \
    "v_add_f32 v225, v225, v227\n\t" \
    "v_cvt_pk_bf16_f32 v229, v228, v229\n\t" \
    "v_cvt_pk_bf16_f32 v228, v224, v225\n\t" \
    "v_min_u32 v229, -2, v229\n\t" \
    "v_min_u32 v228, -2, v228\n\t" \
    "s_nop 1\n\t" \
    "v_permlane16_swap_b32 v228, v229\n\t" \
    "buffer_store_dwordx2 v[228:229], v232, s[60:63], s42 offen sc1\n\t" \
    "s_setprio 0\n\t" \
    LST5 \
    LFLUSH200 \
    "s_add_u32 s40, s40, 1\n\t" \
    "s_cmp_lg_u32 s40, 64\n\t" \
    "s_cbranch_scc1 .Lv43_%=\n\t" \
    "s_branch .Lend%=\n\t" \
    ".Lv3_%=:\n\t" \
    "s_mov_b32 s42, %[xs]\n\t" \
    "s_add_u32 m0, %[wm], 0xc000\n\t" \
    "s_mov_b32 s44, 0x0\n\t" \
    "buffer_load_dwordx4 v240, %[wr], s44 offen offset:0 nt lds\n\t" \
    "buffer_load_dwordx4 v241, %[wr], s44 offen offset:1024 nt lds\n\t" \
    "buffer_load_dwordx4 v242, %[wr], s44 offen offset:2048 nt lds\n\t" \
    "s_add_u32 m0, %[wm], 0xf000\n\t" \
    "s_mov_b32 s44, 0x60000\n\t" \
    "buffer_load_dwordx4 v240, %[wr], s44 offen offset:0 nt lds\n\t" \
    "buffer_load_dwordx4 v241, %[wr], s44 offen offset:1024 nt lds\n\t" \
    "buffer_load_dwordx4 v242, %[wr], s44 offen offset:2048 nt lds\n\t" \
    "s_add_u32 m0, %[wm], 0x0\n\t" \
    "s_mov_b32 s44, 0xc0000\n\t" \
    "buffer_load_dwordx4 v240, %[wr], s44 offen offset:0 nt lds\n\t" \
    "buffer_load_dwordx4 v241, %[wr], s44 offen offset:1024 nt lds\n\t" \
    "buffer_load_dwordx4 v242, %[wr], s44 offen offset:2048 nt lds\n\t" \
    "s_add_u32 m0, %[wm], 0x3000\n\t" \
    "s_mov_b32 s44, 0x120000\n\t" \
    "buffer_load_dwordx4 v240, %[wr], s44 offen offset:0 nt lds\n\t" \
    "buffer_load_dwordx4 v241, %[wr], s44 offen offset:1024 nt lds\n\t" \
    "buffer_load_dwordx4 v242, %[wr], s44 offen offset:2048 nt lds\n\t" \
    "s_add_u32 m0, %[wm], 0x6000\n\t" \
    "s_mov_b32 s44, 0x180000\n\t" \
    "buffer_load_dwordx4 v240, %[wr], s44 offen offset:0 nt lds\n\t" \
    "buffer_load_dwordx4 v241, %[wr], s44 offen offset:1024 nt lds\n\t" \
    "buffer_load_dwordx4 v242, %[wr], s44 offen offset:2048 nt lds\n\t" \
    "v_add_u32 v244, 0xc000, v243\n\t" \
    "s_waitcnt vmcnt(12) lgkmcnt(0)\n\t" \
    "s_barrier \n\t" \
    "s_add_u32 m0, %[wm], 0x9000\n\t" \
    "s_mov_b32 s44, 0x1e0000\n\t" \
    "buffer_load_dwordx4 v240, %[wr], s44 offen offset:0 nt lds\n\t" \
    "buffer_load_dwordx4 v241, %[wr], s44 offen offset:1024 nt lds\n\t" \
    "buffer_load_dwordx4 v242, %[wr], s44 offen offset:2048 nt lds\n\t" \
    "ds_read_b64_tr_b16 v[208:209], v244 offset:0\n\t" \
    "ds_read_b64_tr_b16 v[210:211], v244 offset:1536\n\t" \
    "ds_read_b64_tr_b16 v[40:41], v244 offset:32\n\t" \
    "ds_read_b64_tr_b16 v[42:43], v244 offset:1568\n\t" \
    "ds_read_b64_tr_b16 v[48:49], v244 offset:64\n\t" \
    "ds_read_b64_tr_b16 v[50:51], v244 offset:1600\n\t" \
    "v_add_u32 v244, 0xf000, v243\n\t" \
    "s_waitcnt vmcnt(12) lgkmcnt(0)\n\t" \
    "s_barrier \n\t" \
    "s_add_u32 m0, %[wm], 0xc000\n\t" \
    "s_mov_b32 s44, 0x240000\n\t" \
    "buffer_load_dwordx4 v240, %[wr], s44 offen offset:0 nt lds\n\t" \
    "buffer_load_dwordx4 v241, %[wr], s44 offen offset:1024 nt lds\n\t" \
    "buffer_load_dwordx4 v242, %[wr], s44 offen offset:2048 nt lds\n\t" \
    "ds_read_b64_tr_b16 v[212:213], v244 offset:0\n\t" \
    "ds_read_b64_tr_b16 v[214:215], v244 offset:1536\n\t" \
    "ds_read_b64_tr_b16 v[44:45], v244 offset:32\n\t" \
    "ds_read_b64_tr_b16 v[46:47], v244 offset:1568\n\t" \
    "ds_read_b64_tr_b16 v[52:53], v244 offset:64\n\t" \
    "ds_read_b64_tr_b16 v[54:55], v244 offset:1600\n\t" \
    "v_add_u32 v244, 0x0, v243\n\t" \
    "s_waitcnt vmcnt(12) lgkmcnt(0)\n\t" \
    "s_barrier \n\t" \
    "s_add_u32 m0, %[wm], 0xf000\n\t" \
    "s_mov_b32 s44, 0x2a0000\n\t" \
    "buffer_load_dwordx4 v240, %[wr], s44 offen offset:0 nt lds\n\t" \
    "buffer_load_dwordx4 v241, %[wr], s44 offen offset:1024 nt lds\n\t" \
    "buffer_load_dwordx4 v242, %[wr], s44 offen offset:2048 nt lds\n\t" \
    "ds_read_b64_tr_b16 v[56:57], v244 offset:0\n\t" \
    "ds_read_b64_tr_b16 v[58:59], v244 offset:1536\n\t" \
    "ds_read_b64_tr_b16 v[64:65], v244 offset:32\n\t" \
    "ds_read_b64_tr_b16 v[66:67], v244 offset:1568\n\t" \
    "ds_read_b64_tr_b16 v[72:73], v244 offset:64\n\t" \
    "ds_read_b64_tr_b16 v[74:75], v244 offset:1600\n\t" \
    "v_add_u32 v244, 0x3000, v243\n\t" \
    "s_waitcnt vmcnt(12) lgkmcnt(0)\n\t" \
    "s_barrier \n\t" \
    "s_add_u32 m0, %[wm], 0x0\n\t" \
    "s_mov_b32 s44, 0x300000\n\t" \
    "buffer_load_dwordx4 v240, %[wr], s44 offen offset:0 nt lds\n\t" \
    "buffer_load_dwordx4 v241, %[wr], s44 offen offset:1024 nt lds\n\t" \
    "buffer_load_dwordx4 v242, %[wr], s44 offen offset:2048 nt lds\n\t" \
    "ds_read_b64_tr_b16 v[60:61], v244 offset:0\n\t" \
    "ds_read_b64_tr_b16 v[62:63], v244 offset:1536\n\t" \
    "ds_read_b64_tr_b16 v[68:69], v244 offset:32\n\t" \
    "ds_read_b64_tr_b16 v[70:71], v244 offset:1568\n\t" \
    "ds_read_b64_tr_b16 v[76:77], v244 offset:64\n\t" \
    "ds_read_b64_tr_b16 v[78:79], v244 offset:1600\n\t" \
    "v_add_u32 v244, 0x6000, v243\n\t" \
    "s_waitcnt vmcnt(12) lgkmcnt(0)\n\t" \
    "s_barrier \n\t" \
    "s_add_u32 m0, %[wm], 0x3000\n\t" \
    "s_mov_b32 s44, 0x360000\n\t" \
    "buffer_load_dwordx4 v240, %[wr], s44 offen offset:0 nt lds\n\t" \
    "buffer_load_dwordx4 v241, %[wr], s44 offen offset:1024 nt lds\n\t" \
    "buffer_load_dwordx4 v242, %[wr], s44 offen offset:2048 nt lds\n\t" \
    "ds_read_b64_tr_b16 v[216:217], v244 offset:0\n\t" \
    "ds_read_b64_tr_b16 v[218:219], v244 offset:1536\n\t" \
    "ds_read_b64_tr_b16 v[80:81], v244 offset:32\n\t" \
    "ds_read_b64_tr_b16 v[82:83], v244 offset:1568\n\t" \
    "ds_read_b64_tr_b16 v[88:89], v244 offset:64\n\t" \
    "ds_read_b64_tr_b16 v[90:91], v244 offset:1600\n\t" \
    "v_add_u32 v244, 0x9000, v243\n\t" \
    "s_waitcnt vmcnt(12) lgkmcnt(0)\n\t" \
    "s_barrier \n\t" \
    "s_add_u32 m0, %[wm], 0x6000\n\t" \
    "s_mov_b32 s44, 0x3c0000\n\t" \
    "buffer_load_dwordx4 v240, %[wr], s44 offen offset:0 nt lds\n\t" \
    "buffer_load_dwordx4 v241, %[wr], s44 offen offset:1024 nt lds\n\t" \
    "buffer_load_dwordx4 v242, %[wr], s44 offen offset:2048 nt lds\n\t" \
    "ds_read_b64_tr_b16 v[220:221], v244 offset:0\n\t" \
    "ds_read_b64_tr_b16 v[222:223], v244 offset:1536\n\t" \
    "ds_read_b64_tr_b16 v[84:85], v244 offset:32\n\t" \
    "ds_read_b64_tr_b16 v[86:87], v244 offset:1568\n\t" \
    "ds_read_b64_tr_b16 v[92:93], v244 offset:64\n\t" \
    "ds_read_b64_tr_b16 v[94:95], v244 offset:1600\n\t" \
    "v_add_u32 v244, 0xc000, v243\n\t" \
    "s_waitcnt vmcnt(12) lgkmcnt(0)\n\t" \
    "s_barrier \n\t" \
    "v_mov_b32 v246, 0\n\t" \
    "buffer_load_dword v246, v246, s[60:63], %[mo] offen sc1\n\t" \
    "s_add_u32 m0, %[wm], 0x9000\n\t" \
    "s_mov_b32 s44, 0x420000\n\t" \
    "buffer_load_dwordx4 v240, %[wr], s44 offen offset:0 nt lds\n\t" \
    "buffer_load_dwordx4 v241, %[wr], s44 offen offset:1024 nt lds\n\t" \
    "buffer_load_dwordx4 v242, %[wr], s44 offen offset:2048 nt lds\n\t" \
    "ds_read_b64_tr_b16 v[96:97], v244 offset:0\n\t" \
    "ds_read_b64_tr_b16 v[98:99], v244 offset:1536\n\t" \
    "ds_read_b64_tr_b16 v[104:105], v244 offset:32\n\t" \
    "ds_read_b64_tr_b16 v[106:107], v244 offset:1568\n\t" \
    "ds_read_b64_tr_b16 v[112:113], v244 offset:64\n\t" \
    "ds_read_b64_tr_b16 v[114:115], v244 offset:1600\n\t" \
    "v_add_u32 v244, 0xf000, v243\n\t" \
    "s_waitcnt vmcnt(12) lgkmcnt(0)\n\t" \
    "s_barrier \n\t" \
    "s_add_u32 m0, %[wm], 0xc000\n\t" \
    "s_mov_b32 s44, 0x480000\n\t" \
    "buffer_load_dwordx4 v240, %[wr], s44 offen offset:0 nt lds\n\t" \
    "buffer_load_dwordx4 v241, %[wr], s44 offen offset:1024 nt lds\n\t" \
    "buffer_load_dwordx4 v242, %[wr], s44 offen offset:2048 nt lds\n\t" \
    "ds_read_b64_tr_b16 v[100:101], v244 offset:0\n\t" \
    "ds_read_b64_tr_b16 v[102:103], v244 offset:1536\n\t" \
    "ds_read_b64_tr_b16 v[108:109], v244 offset:32\n\t" \
    "ds_read_b64_tr_b16 v[110:111], v244 offset:1568\n\t" \
    "ds_read_b64_tr_b16 v[116:117], v244 offset:64\n\t" \
    "ds_read_b64_tr_b16 v[118:119], v244 offset:1600\n\t" \
    "v_add_u32 v244, 0x0, v243\n\t" \
    "s_waitcnt vmcnt(12) lgkmcnt(0)\n\t" \
    "s_barrier \n\t" \
    "s_add_u32 m0, %[wm], 0xf000\n\t" \
    "s_mov_b32 s44, 0x4e0000\n\t" \
    "buffer_load_dwordx4 v240, %[wr], s44 offen offset:0 nt lds\n\t" \
    "buffer_load_dwordx4 v241, %[wr], s44 offen offset:1024 nt lds\n\t" \
    "buffer_load_dwordx4 v242, %[wr], s44 offen offset:2048 nt lds\n\t" \
    "ds_read_b64_tr_b16 v[224:225], v244 offset:0\n\t" \
    "ds_read_b64_tr_b16 v[226:227], v244 offset:1536\n\t" \
    "ds_read_b64_tr_b16 v[120:121], v244 offset:32\n\t" \
    "ds_read_b64_tr_b16 v[122:123], v244 offset:1568\n\t" \
    "ds_read_b64_tr_b16 v[128:129], v244 offset:64\n\t" \
    "ds_read_b64_tr_b16 v[130:131], v244 offset:1600\n\t" \
    "v_add_u32 v244, 0x3000, v243\n\t" \
    "s_waitcnt vmcnt(12) lgkmcnt(0)\n\t" \
    "s_barrier \n\t" \
    "s_add_u32 m0, %[wm], 0x0\n\t" \
    "s_mov_b32 s44, 0x540000\n\t" \
    "buffer_load_dwordx4 v240, %[wr], s44 offen offset:0 nt lds\n\t" \
    "buffer_load_dwordx4 v241, %[wr], s44 offen offset:1024 nt lds\n\t" \
    "buffer_load_dwordx4 v242, %[wr], s44 offen offset:2048 nt lds\n\t" \
    "ds_read_b64_tr_b16 v[228:229], v244 offset:0\n\t" \
    "ds_read_b64_tr_b16 v[230:231], v244 offset:1536\n\t" \
    "ds_read_b64_tr_b16 v[124:125], v244 offset:32\n\t" \
    "ds_read_b64_tr_b16 v[126:127], v244 offset:1568\n\t" \
    "ds_read_b64_tr_b16 v[132:133], v244 offset:64\n\t" \
    "ds_read_b64_tr_b16 v[134:135], v244 offset:1600\n\t" \
    "v_add_u32 v244, 0x6000, v243\n\t" \
    "s_waitcnt vmcnt(12) lgkmcnt(0)\n\t" \
    "s_barrier \n\t" \
    "s_add_u32 m0, %[wm], 0x3000\n\t" \
    "s_mov_b32 s44, 0x5a0000\n\t" \
    "buffer_load_dwordx4 v240, %[wr], s44 offen offset:0 nt lds\n\t" \
    "buffer_load_dwordx4 v241, %[wr], s44 offen offset:1024 nt lds\n\t" \
    "buffer_load_dwordx4 v242, %[wr], s44 offen offset:2048 nt lds\n\t" \
    "ds_read_b64_tr_b16 v[136:137], v244 offset:0\n\t" \
    "ds_read_b64_tr_b16 v[138:139], v244 offset:1536\n\t" \
    "ds_read_b64_tr_b16 v[144:145], v244 offset:32\n\t" \
    "ds_read_b64_tr_b16 v[146:147], v244 offset:1568\n\t" \
    "ds_read_b64_tr_b16 v[152:153], v244 offset:64\n\t" \
    "ds_read_b64_tr_b16 v[154:155], v244 offset:1600\n\t" \
    "v_add_u32 v244, 0x9000, v243\n\t" \
    "s_waitcnt vmcnt(12) lgkmcnt(0)\n\t" \
    "s_barrier \n\t" \
    ".Lv31_%=:\n\t" \
    "v_readfirstlane_b32 s43, v246\n\t" \
    "s_nop 0\n\t" \
    "s_cmp_lg_u32 s43, -1\n\t" \
    "s_cbranch_scc1 .Lv32_%=\n\t" \
    "s_cmp_eq_u32 s41, 0\n\t" \
    "s_cbranch_scc1 .Lv32_%=\n\t" \
    "s_sub_u32 s41, s41, 1\n\t" \
    "s_sleep 4\n\t" \
    "v_mov_b32 v246, 0\n\t" \
    "buffer_load_dword v246, v246, s[60:63], %[mo] offen sc1\n\t" \
    "s_waitcnt vmcnt(0)\n\t" \
    "s_branch .Lv31_%=\n\t" \
    ".Lv32_%=:\n\t" \
    "v_mov_b32 v246, 0x8000\n\t" \
    "s_mov_b64 exec, 1\n\t" \
    "buffer_store_dword v246, v246, s[60:63], %[mo] offen sc1\n\t" \
    "s_mov_b64 exec, -1\n\t" \
    "s_and_b32 s43, s43, 0x2000\n\t" \
    "s_xor_b32 s43, s43, %[lx]\n\t" \
    "s_add_u32 s60, s60, s43\n\t" \
    "s_addc_u32 s61, s61, 0\n\t" \
    "v_and_b32 v252, 0xf0, v253\n\t" \
    "v_lshlrev_b32 v252, 3, v252\n\t" \
    "buffer_load_dword v252, v252, s[60:63], %[fl] offen sc1\n\t" \
    "s_add_u32 m0, %[wm], 0x6000\n\t" \
    "s_mov_b32 s44, 0x600000\n\t" \
    "buffer_load_dwordx4 v240, %[wr], s44 offen offset:0 nt lds\n\t" \
    "buffer_load_dwordx4 v241, %[wr], s44 offen offset:1024 nt lds\n\t" \
    "buffer_load_dwordx4 v242, %[wr], s44 offen offset:2048 nt lds\n\t" \
    "ds_read_b64_tr_b16 v[140:141], v244 offset:0\n\t" \
    "ds_read_b64_tr_b16 v[142:143], v244 offset:1536\n\t" \
    "ds_read_b64_tr_b16 v[148:149], v244 offset:32\n\t" \
    "ds_read_b64_tr_b16 v[150:151], v244 offset:1568\n\t" \
    "ds_read_b64_tr_b16 v[156:157], v244 offset:64\n\t" \
    "ds_read_b64_tr_b16 v[158:159], v244 offset:1600\n\t" \
    "v_add_u32 v244, 0xc000, v243\n\t" \
    "s_waitcnt vmcnt(12) lgkmcnt(0)\n\t" \
    "s_barrier \n\t" \
    "s_add_u32 m0, %[wm], 0x9000\n\t" \
    "s_mov_b32 s44, 0x660000\n\t" \
    "buffer_load_dwordx4 v240, %[wr], s44 offen offset:0 nt lds\n\t" \
    "buffer_load_dwordx4 v241, %[wr], s44 offen offset:1024 nt lds\n\t" \
    "buffer_load_dwordx4 v242, %[wr], s44 offen offset:2048 nt lds\n\t" \
    "ds_read_b64_tr_b16 v[232:233], v244 offset:0\n\t" \
    "ds_read_b64_tr_b16 v[234:235], v244 offset:1536\n\t" \
    "ds_read_b64_tr_b16 v[160:161], v244 offset:32\n\t" \
    "ds_read_b64_tr_b16 v[162:163], v244 offset:1568\n\t" \
    "ds_read_b64_tr_b16 v[168:169], v244 offset:64\n\t" \
    "ds_read_b64_tr_b16 v[170:171], v244 offset:1600\n\t" \
    "v_add_u32 v244, 0xf000, v243\n\t" \
    "s_waitcnt vmcnt(12) lgkmcnt(0)\n\t" \
    "s_barrier \n\t" \
    "s_add_u32 m0, %[wm], 0xc000\n\t" \
    "s_mov_b32 s44, 0x6c0000\n\t" \
    "buffer_load_dwordx4 v240, %[wr], s44 offen offset:0 nt lds\n\t" \
    "buffer_load_dwordx4 v241, %[wr], s44 offen offset:1024 nt lds\n\t" \
    "buffer_load_dwordx4 v242, %[wr], s44 offen offset:2048 nt lds\n\t" \
    "ds_read_b64_tr_b16 v[236:237], v244 offset:0\n\t" \
    "ds_read_b64_tr_b16 v[238:239], v244 offset:1536\n\t" \
    "ds_read_b64_tr_b16 v[164:165], v244 offset:32\n\t" \
    "ds_read_b64_tr_b16 v[166:167], v244 offset:1568\n\t" \
    "ds_read_b64_tr_b16 v[172:173], v244 offset:64\n\t" \
    "ds_read_b64_tr_b16 v[174:175], v244 offset:1600\n\t" \
    "v_add_u32 v244, 0x0, v243\n\t" \
    "s_waitcnt vmcnt(12) lgkmcnt(0)\n\t" \
    "s_barrier \n\t" \
    "s_add_u32 m0, %[wm], 0xf000\n\t" \
    "s_mov_b32 s44, 0x720000\n\t" \
    "buffer_load_dwordx4 v240, %[wr], s44 offen offset:0 nt lds\n\t" \
    "buffer_load_dwordx4 v241, %[wr], s44 offen offset:1024 nt lds\n\t" \
    "buffer_load_dwordx4 v242, %[wr], s44 offen offset:2048 nt lds\n\t" \
    "ds_read_b64_tr_b16 v[176:177], v244 offset:0\n\t" \
    "ds_read_b64_tr_b16 v[178:179], v244 offset:1536\n\t" \
    "ds_read_b64_tr_b16 v[184:185], v244 offset:32\n\t" \
    "ds_read_b64_tr_b16 v[186:187], v244 offset:1568\n\t" \
    "ds_read_b64_tr_b16 v[192:193], v244 offset:64\n\t" \
    "ds_read_b64_tr_b16 v[194:195], v244 offset:1600\n\t" \
    "v_add_u32 v244, 0x3000, v243\n\t" \
    "s_waitcnt vmcnt(12) lgkmcnt(0)\n\t" \
    "s_barrier \n\t" \
    "ds_read_b64_tr_b16 v[180:181], v244 offset:0\n\t" \
    "ds_read_b64_tr_b16 v[182:183], v244 offset:1536\n\t" \
    "ds_read_b64_tr_b16 v[188:189], v244 offset:32\n\t" \
    "ds_read_b64_tr_b16 v[190:191], v244 offset:1568\n\t" \
    "ds_read_b64_tr_b16 v[196:197], v244 offset:64\n\t" \
    "ds_read_b64_tr_b16 v[198:199], v244 offset:1600\n\t" \
    "v_add_u32 v244, 0x6000, v243\n\t" \
    "s_waitcnt vmcnt(9) lgkmcnt(0)\n\t" \
    "s_barrier \n\t" \
    "ds_read_b64_tr_b16 v[200:201], v244 offset:0\n\t" \
    "ds_read_b64_tr_b16 v[202:203], v244 offset:1536\n\t" \
    "ds_read_b64_tr_b16 v[0:1], v244 offset:32\n\t" \
    "ds_read_b64_tr_b16 v[2:3], v244 offset:1568\n\t" \
    "ds_read_b64_tr_b16 v[8:9], v244 offset:64\n\t" \
    "ds_read_b64_tr_b16 v[10:11], v244 offset:1600\n\t" \
    "v_add_u32 v244, 0x9000, v243\n\t" \
    "s_waitcnt vmcnt(6) lgkmcnt(0)\n\t" \
    "s_barrier \n\t" \
    "ds_read_b64_tr_b16 v[204:205], v244 offset:0\n\t" \
    "ds_read_b64_tr_b16 v[206:207], v244 offset:1536\n\t" \
    "ds_read_b64_tr_b16 v[4:5], v244 offset:32\n\t" \
    "ds_read_b64_tr_b16 v[6:7], v244 offset:1568\n\t" \
    "ds_read_b64_tr_b16 v[12:13], v244 offset:64\n\t" \
    "ds_read_b64_tr_b16 v[14:15], v244 offset:1600\n\t" \
    "v_add_u32 v244, 0xc000, v243\n\t" \
    "s_waitcnt vmcnt(3) lgkmcnt(0)\n\t" \
    "s_barrier \n\t" \
    "ds_read_b64_tr_b16 v[16:17], v244 offset:0\n\t" \
    "ds_read_b64_tr_b16 v[18:19], v244 offset:1536\n\t" \
    "ds_read_b64_tr_b16 v[24:25], v244 offset:32\n\t" \
    "ds_read_b64_tr_b16 v[26:27], v244 offset:1568\n\t" \
    "ds_read_b64_tr_b16 v[32:33], v244 offset:64\n\t" \
    "ds_read_b64_tr_b16 v[34:35], v244 offset:1600\n\t" \
    "s_mov_b32 s43, 0\n\t" \
    "v_cmp_eq_u32 vcc, 0, v252\n\t" \
    "s_cbranch_vccnz .Lv33_%=\n\t" \
    "s_movk_i32 s45, 0x3000\n\t" \
    "s_mov_b32 m0, s45\n\t" \
    "s_nop 0\n\t" \
    "buffer_load_dwordx4 v253, s[60:63], s42 offen offset:0 sc1 lds\n\t" \
    "buffer_load_dwordx4 v253, s[60:63], s42 offen offset:1024 sc1 lds\n\t" \
    "buffer_load_dwordx4 v253, s[60:63], s42 offen offset:2048 sc1 lds\n\t" \
    "buffer_load_dwordx4 v253, s[60:63], s42 offen offset:3072 sc1 lds\n\t" \
    "s_add_u32 s44, s42, 0x1000\n\t" \
    "s_mov_b32 s45, 0x8000\n\t" \
    "s_mov_b32 m0, s45\n\t" \
    "s_nop 0\n\t" \
    "buffer_load_dwordx4 v253, s[60:63], s44 offen offset:0 sc1 lds\n\t" \
    "buffer_load_dwordx4 v253, s[60:63], s44 offen offset:1024 sc1 lds\n\t" \
    "buffer_load_dwordx4 v253, s[60:63], s44 offen offset:2048 sc1 lds\n\t" \
    "buffer_load_dwordx4 v253, s[60:63], s44 offen offset:3072 sc1 lds\n\t" \
    "s_add_u32 s44, s42, 0x4c00\n\t" \
    "s_mov_b32 s45, 0x4c00\n\t" \
    "s_mov_b32 m0, s45\n\t" \
    "s_nop 0\n\t" \
    "buffer_load_dwordx4 v253, s[60:63], s44 offen offset:0 sc1 lds\n\t" \
    "s_add_u32 s44, s44, 0x1000\n\t" \
    "s_mov_b32 s45, 0x9c00\n\t" \
    "s_mov_b32 m0, s45\n\t" \
    "s_nop 0\n\t" \
    "buffer_load_dwordx4 v253, s[60:63], s44 offen offset:0 sc1 lds\n\t" \
    "s_mov_b32 s43, 1\n\t" \
    ".Lv33_%=:\n\t" \
    "v_add_u32 v244, 0xf000, v243\n\t" \
    "s_waitcnt vmcnt(0) lgkmcnt(0)\n\t" \
    "s_barrier \n\t" \
    "ds_read_b64_tr_b16 v[20:21], v244 offset:0\n\t" \
    "ds_read_b64_tr_b16 v[22:23], v244 offset:1536\n\t" \
    "ds_read_b64_tr_b16 v[28:29], v244 offset:32\n\t" \
    "ds_read_b64_tr_b16 v[30:31], v244 offset:1568\n\t" \
    "ds_read_b64_tr_b16 v[36:37], v244 offset:64\n\t" \
    "ds_read_b64_tr_b16 v[38:39], v244 offset:1600\n\t" \
    "s_waitcnt lgkmcnt(0)\n\t" \
    "s_barrier \n\t" \
    LST0 \
    "ds_write_b128 v254, v[200:203] offset:55296\n\t" \
    "ds_write_b128 v254, v[204:207] offset:56320\n\t" \
    "ds_write_b128 v254, v[208:211] offset:57344\n\t" \
    "ds_write_b128 v254, v[212:215] offset:58368\n\t" \
    "ds_write_b128 v254, v[216:219] offset:59392\n\t" \
    "ds_write_b128 v254, v[220:223] offset:60416\n\t" \
    "ds_write_b128 v254, v[224:227] offset:61440\n\t" \
    "ds_write_b128 v254, v[228:231] offset:62464\n\t" \
    "ds_write_b128 v254, v[232:235] offset:63488\n\t" \
    "ds_write_b128 v254, v[236:239] offset:64512\n\t" \
    "s_cmp_lg_u32 s43, 0\n\t" \
    "s_cbranch_scc1 .Lv34_%=\n\t" \
    "s_mov_b32 s44, 0\n\t" \
    ".Lv35_%=:\n\t" \
    "v_cmp_ge_u32 vcc, s44, v252\n\t" \
    "s_cbranch_vccz .Lv36_%=\n\t" \
    "s_cmp_eq_u32 s41, 0\n\t" \
    "s_cbranch_scc1 .Lv36_%=\n\t" \
    "s_sub_u32 s41, s41, 1\n\t" \
    "s_sleep 8\n\t" \
    "v_and_b32 v252, 0xf0, v253\n\t" \
    "v_lshlrev_b32 v252, 3, v252\n\t" \
    "buffer_load_dword v252, v252, s[60:63], %[fl] offen sc1\n\t" \
    "s_waitcnt vmcnt(0)\n\t" \
    "s_branch .Lv35_%=\n\t" \
    ".Lv36_%=:\n\t" \
    "s_movk_i32 s45, 0x3000\n\t" \
    "s_mov_b32 m0, s45\n\t" \
    "s_nop 0\n\t" \
    "buffer_load_dwordx4 v253, s[60:63], s42 offen offset:0 sc1 lds\n\t" \
    "buffer_load_dwordx4 v253, s[60:63], s42 offen offset:1024 sc1 lds\n\t" \
    "buffer_load_dwordx4 v253, s[60:63], s42 offen offset:2048 sc1 lds\n\t" \
    "buffer_load_dwordx4 v253, s[60:63], s42 offen offset:3072 sc1 lds\n\t" \
    "s_add_u32 s44, s42, 0x1000\n\t" \
    "s_mov_b32 s45, 0x8000\n\t" \
    "s_mov_b32 m0, s45\n\t" \
    "s_nop 0\n\t" \
    "buffer_load_dwordx4 v253, s[60:63], s44 offen offset:0 sc1 lds\n\t" \
    "buffer_load_dwordx4 v253, s[60:63], s44 offen offset:1024 sc1 lds\n\t" \
    "buffer_load_dwordx4 v253, s[60:63], s44 offen offset:2048 sc1 lds\n\t" \
    "buffer_load_dwordx4 v253, s[60:63], s44 offen offset:3072 sc1 lds\n\t" \
    "s_add_u32 s44, s42, 0x4c00\n\t" \
    "s_mov_b32 s45, 0x4c00\n\t" \
    "s_mov_b32 m0, s45\n\t" \
    "s_nop 0\n\t" \
    "buffer_load_dwordx4 v253, s[60:63], s44 offen offset:0 sc1 lds\n\t" \
    "s_add_u32 s44, s44, 0x1000\n\t" \
    "s_mov_b32 s45, 0x9c00\n\t" \
    "s_mov_b32 m0, s45\n\t" \
    "s_nop 0\n\t" \
    "buffer_load_dwordx4 v253, s[60:63], s44 offen offset:0 sc1 lds\n\t" \
    ".Lv34_%=:\n\t" \
    "s_waitcnt vmcnt(0) lgkmcnt(0)\n\t" \
    "s_barrier \n\t" \
    "ds_read_b128 v[224:227], v253 offset:0\n\t" \
    "ds_read_b128 v[244:247], v254 offset:57344\n\t" \
    "ds_read_b128 v[228:231], v253 offset:1024\n\t" \
    "ds_read_b128 v[232:235], v253 offset:20480\n\t" \
    "ds_read_b128 v[236:239], v253 offset:21504\n\t" \
    "ds_read_b128 v[248:251], v254 offset:58368\n\t" \
    ".Lv37_%=:\n\t" \
    LST7 \
    "s_waitcnt lgkmcnt(5)\n\t" \
    "v_mfma_f32_16x16x32_bf16 v[204:207], v[40:43], v[224:227], 0\n\t" \
    "s_waitcnt lgkmcnt(3)\n\t" \
    "v_mfma_f32_16x16x32_bf16 v[204:207], v[44:47], v[228:231], v[204:207]\n\t" \
    "ds_read_b128 v[240:243], v253 offset:2048\n\t" \
    "v_mfma_f32_16x16x32_bf16 v[208:211], v[48:51], v[224:227], 0\n\t" \
    "v_mfma_f32_16x16x32_bf16 v[208:211], v[52:55], v[228:231], v[208:211]\n\t" \
    "v_mfma_f32_16x16x32_bf16 v[200:203], v[244:247], v[224:227], 0\n\t" \
    "s_waitcnt lgkmcnt(1)\n\t" \
    "v_mfma_f32_16x16x32_bf16 v[200:203], v[248:251], v[228:231], v[200:203]\n\t" \
    "v_mfma_f32_16x16x32_bf16 v[216:219], v[40:43], v[232:235], 0\n\t" \
    "ds_read_b128 v[224:227], v253 offset:3072\n\t" \
    "v_mfma_f32_16x16x32_bf16 v[216:219], v[44:47], v[236:239], v[216:219]\n\t" \
    "ds_read_b128 v[228:231], v253 offset:22528\n\t" \
    "v_mfma_f32_16x16x32_bf16 v[220:223], v[48:51], v[232:235], 0\n\t" \
    "v_mfma_f32_16x16x32_bf16 v[220:223], v[52:55], v[236:239], v[220:223]\n\t" \
    "v_mfma_f32_16x16x32_bf16 v[212:215], v[244:247], v[232:235], 0\n\t" \
    "ds_read_b128 v[244:247], v254 offset:59392\n\t" \
    "v_mfma_f32_16x16x32_bf16 v[212:215], v[248:251], v[236:239], v[212:215]\n\t" \
    "ds_read_b128 v[248:251], v254 offset:60416\n\t" \
    "s_waitcnt lgkmcnt(4)\n\t" \
    "v_mfma_f32_16x16x32_bf16 v[204:207], v[64:67], v[240:243], v[204:207]\n\t" \
    "ds_read_b128 v[232:235], v253 offset:23552\n\t" \
    "s_waitcnt lgkmcnt(4)\n\t" \
    "v_mfma_f32_16x16x32_bf16 v[204:207], v[68:71], v[224:227], v[204:207]\n\t" \
    "ds_read_b128 v[236:239], v253 offset:4096\n\t" \
    "v_mfma_f32_16x16x32_bf16 v[208:211], v[72:75], v[240:243], v[208:211]\n\t" \
    "v_mfma_f32_16x16x32_bf16 v[208:211], v[76:79], v[224:227], v[208:211]\n\t" \
    "v_mfma_f32_16x16x32_bf16 v[200:203], v[56:59], v[240:243], v[200:203]\n\t" \
    "v_mfma_f32_16x16x32_bf16 v[200:203], v[60:63], v[224:227], v[200:203]\n\t" \
    "s_waitcnt lgkmcnt(4)\n\t" \
    "v_mfma_f32_16x16x32_bf16 v[216:219], v[64:67], v[228:231], v[216:219]\n\t" \
    "ds_read_b128 v[240:243], v253 offset:5120\n\t" \
    "s_waitcnt lgkmcnt(2)\n\t" \
    "v_mfma_f32_16x16x32_bf16 v[216:219], v[68:71], v[232:235], v[216:219]\n\t" \
    "ds_read_b128 v[224:227], v253 offset:24576\n\t" \
    "v_mfma_f32_16x16x32_bf16 v[220:223], v[72:75], v[228:231], v[220:223]\n\t" \
    "v_mfma_f32_16x16x32_bf16 v[220:223], v[76:79], v[232:235], v[220:223]\n\t" \
    "v_mfma_f32_16x16x32_bf16 v[212:215], v[56:59], v[228:231], v[212:215]\n\t" \
    "v_mfma_f32_16x16x32_bf16 v[212:215], v[60:63], v[232:235], v[212:215]\n\t" \
    "s_waitcnt lgkmcnt(2)\n\t" \
    "v_mfma_f32_16x16x32_bf16 v[204:207], v[80:83], v[236:239], v[204:207]\n\t" \
    "ds_read_b128 v[228:231], v253 offset:25600\n\t" \
    "s_add_u32 s44, s40, 1\n\t" \
    "s_waitcnt lgkmcnt(2)\n\t" \
    "v_mfma_f32_16x16x32_bf16 v[204:207], v[84:87], v[240:243], v[204:207]\n\t" \
    "ds_read_b128 v[232:235], v253 offset:6144\n\t" \
    "s_and_b32 s45, s44, 15\n\t" \
    "v_mfma_f32_16x16x32_bf16 v[208:211], v[88:91], v[236:239], v[208:211]\n\t" \
    "s_cmp_eq_u32 s45, 0\n\t" \
    "v_mfma_f32_16x16x32_bf16 v[208:211], v[92:95], v[240:243], v[208:211]\n\t" \
    "s_cselect_b32 s45, %[cj], %[cs]\n\t" \
    "v_mfma_f32_16x16x32_bf16 v[200:203], v[244:247], v[236:239], v[200:203]\n\t" \
    "s_cmp_eq_u32 s44, 64\n\t" \
    "v_mfma_f32_16x16x32_bf16 v[200:203], v[248:251], v[240:243], v[200:203]\n\t" \
    "s_cselect_b32 s45, 0, s45\n\t" \
    "s_waitcnt lgkmcnt(2)\n\t" \
    "v_mfma_f32_16x16x32_bf16 v[216:219], v[80:83], v[224:227], v[216:219]\n\t" \
    "ds_read_b128 v[236:239], v253 offset:7168\n\t" \
    "s_add_u32 s42, s42, s45\n\t" \
    "s_waitcnt lgkmcnt(2)\n\t" \
    "v_mfma_f32_16x16x32_bf16 v[216:219], v[84:87], v[228:231], v[216:219]\n\t" \
    "ds_read_b128 v[240:243], v253 offset:26624\n\t" \
    "v_mfma_f32_16x16x32_bf16 v[220:223], v[88:91], v[224:227], v[220:223]\n\t" \
    "v_mfma_f32_16x16x32_bf16 v[220:223], v[92:95], v[228:231], v[220:223]\n\t" \
    "v_mfma_f32_16x16x32_bf16 v[212:215], v[244:247], v[224:227], v[212:215]\n\t" \
    "ds_read_b128 v[244:247], v254 offset:61440\n\t" \
    "v_mfma_f32_16x16x32_bf16 v[212:215], v[248:251], v[228:231], v[212:215]\n\t" \
    "ds_read_b128 v[248:251], v254 offset:62464\n\t" \
    "s_waitcnt lgkmcnt(4)\n\t" \
    "v_mfma_f32_16x16x32_bf16 v[204:207], v[104:107], v[232:235], v[204:207]\n\t" \
    "ds_read_b128 v[224:227], v253 offset:27648\n\t" \
    "s_waitcnt lgkmcnt(4)\n\t" \
    "v_mfma_f32_16x16x32_bf16 v[204:207], v[108:111], v[236:239], v[204:207]\n\t" \
    "ds_read_b128 v[228:231], v253 offset:8192\n\t" \
    "v_mfma_f32_16x16x32_bf16 v[208:211], v[112:115], v[232:235], v[208:211]\n\t" \
    "v_mfma_f32_16x16x32_bf16 v[208:211], v[116:119], v[236:239], v[208:211]\n\t" \
    "v_mfma_f32_16x16x32_bf16 v[200:203], v[96:99], v[232:235], v[200:203]\n\t" \
    "v_mfma_f32_16x16x32_bf16 v[200:203], v[100:103], v[236:239], v[200:203]\n\t" \
    "s_waitcnt lgkmcnt(4)\n\t" \
    "v_mfma_f32_16x16x32_bf16 v[216:219], v[104:107], v[240:243], v[216:219]\n\t" \
    "ds_read_b128 v[232:235], v253 offset:9216\n\t" \
    "s_waitcnt lgkmcnt(2)\n\t" \
    "v_mfma_f32_16x16x32_bf16 v[216:219], v[108:111], v[224:227], v[216:219]\n\t" \
    "ds_read_b128 v[236:239], v253 offset:28672\n\t" \
    "v_mfma_f32_16x16x32_bf16 v[220:223], v[112:115], v[240:243], v[220:223]\n\t" \
    "v_mfma_f32_16x16x32_bf16 v[220:223], v[116:119], v[224:227], v[220:223]\n\t" \
    "v_mfma_f32_16x16x32_bf16 v[212:215], v[96:99], v[240:243], v[212:215]\n\t" \
    "v_mfma_f32_16x16x32_bf16 v[212:215], v[100:103], v[224:227], v[212:215]\n\t" \
    "s_waitcnt lgkmcnt(2)\n\t" \
    "v_mfma_f32_16x16x32_bf16 v[204:207], v[120:123], v[228:231], v[204:207]\n\t" \
    "ds_read_b128 v[240:243], v253 offset:29696\n\t" \
    "s_waitcnt lgkmcnt(2)\n\t" \
    "v_mfma_f32_16x16x32_bf16 v[204:207], v[124:127], v[232:235], v[204:207]\n\t" \
    "ds_read_b128 v[224:227], v253 offset:10240\n\t" \
    "v_mfma_f32_16x16x32_bf16 v[208:211], v[128:131], v[228:231], v[208:211]\n\t" \
    "v_mfma_f32_16x16x32_bf16 v[208:211], v[132:135], v[232:235], v[208:211]\n\t" \
    "v_mfma_f32_16x16x32_bf16 v[200:203], v[244:247], v[228:231], v[200:203]\n\t" \
    "v_mfma_f32_16x16x32_bf16 v[200:203], v[248:251], v[232:235], v[200:203]\n\t" \
    "s_waitcnt lgkmcnt(2)\n\t" \
    "v_mfma_f32_16x16x32_bf16 v[216:219], v[120:123], v[236:239], v[216:219]\n\t" \
    "ds_read_b128 v[228:231], v253 offset:11264\n\t" \
    "s_waitcnt lgkmcnt(2)\n\t" \
    "v_mfma_f32_16x16x32_bf16 v[216:219], v[124:127], v[240:243], v[216:219]\n\t" \
    "ds_read_b128 v[232:235], v253 offset:30720\n\t" \
    "v_mfma_f32_16x16x32_bf16 v[220:223], v[128:131], v[236:239], v[220:223]\n\t" \
    "v_mfma_f32_16x16x32_bf16 v[220:223], v[132:135], v[240:243], v[220:223]\n\t" \
    "v_mfma_f32_16x16x32_bf16 v[212:215], v[244:247], v[236:239], v[212:215]\n\t" \
    "ds_read_b128 v[244:247], v254 offset:63488\n\t" \
    "v_mfma_f32_16x16x32_bf16 v[212:215], v[248:251], v[240:243], v[212:215]\n\t" \
    "ds_read_b128 v[248:251], v254 offset:64512\n\t" \
    "s_waitcnt lgkmcnt(4)\n\t" \
    "v_mfma_f32_16x16x32_bf16 v[204:207], v[144:147], v[224:227], v[204:207]\n\t" \
    "ds_read_b128 v[236:239], v253 offset:31744\n\t" \
    "s_and_b32 s44, s40, 3\n\t" \
    "s_waitcnt lgkmcnt(4)\n\t" \
    "v_mfma_f32_16x16x32_bf16 v[204:207], v[148:151], v[228:231], v[204:207]\n\t" \
    "ds_read_b128 v[240:243], v253 offset:12288\n\t" \
    "s_lshl_b32 s44, s44, 17\n\t" \
    "v_mfma_f32_16x16x32_bf16 v[208:211], v[152:155], v[224:227], v[208:211]\n\t" \
    "s_add_u32 s44, s44, %[mb]\n\t" \
    "v_mfma_f32_16x16x32_bf16 v[208:211], v[156:159], v[228:231], v[208:211]\n\t" \
    "s_add_u32 s46, s44, 0x4000\n\t" \
    "v_mfma_f32_16x16x32_bf16 v[200:203], v[136:139], v[224:227], v[200:203]\n\t" \
    "s_add_u32 s47, s44, 0x4400\n\t" \
    "v_mfma_f32_16x16x32_bf16 v[200:203], v[140:143], v[228:231], v[200:203]\n\t" \
    "s_waitcnt lgkmcnt(4)\n\t" \
    "v_mfma_f32_16x16x32_bf16 v[216:219], v[144:147], v[232:235], v[216:219]\n\t" \
    "ds_read_b128 v[224:227], v253 offset:13312\n\t" \
    "s_waitcnt lgkmcnt(2)\n\t" \
    "v_mfma_f32_16x16x32_bf16 v[216:219], v[148:151], v[236:239], v[216:219]\n\t" \
    "ds_read_b128 v[228:231], v253 offset:32768\n\t" \
    "v_mfma_f32_16x16x32_bf16 v[220:223], v[152:155], v[232:235], v[220:223]\n\t" \
    "v_mfma_f32_16x16x32_bf16 v[220:223], v[156:159], v[236:239], v[220:223]\n\t" \
    "v_mfma_f32_16x16x32_bf16 v[212:215], v[136:139], v[232:235], v[212:215]\n\t" \
    "v_mfma_f32_16x16x32_bf16 v[212:215], v[140:143], v[236:239], v[212:215]\n\t" \
    "s_waitcnt lgkmcnt(2)\n\t" \
    "v_mfma_f32_16x16x32_bf16 v[204:207], v[160:163], v[240:243], v[204:207]\n\t" \
    "ds_read_b128 v[232:235], v253 offset:33792\n\t" \
    "s_waitcnt lgkmcnt(2)\n\t" \
    "v_mfma_f32_16x16x32_bf16 v[204:207], v[164:167], v[224:227], v[204:207]\n\t" \
    "ds_read_b128 v[236:239], v253 offset:16384\n\t" \
    "v_mfma_f32_16x16x32_bf16 v[208:211], v[168:171], v[240:243], v[208:211]\n\t" \
    "v_mfma_f32_16x16x32_bf16 v[208:211], v[172:175], v[224:227], v[208:211]\n\t" \
    "v_mfma_f32_16x16x32_bf16 v[200:203], v[244:247], v[240:243], v[200:203]\n\t" \
    "v_mfma_f32_16x16x32_bf16 v[200:203], v[248:251], v[224:227], v[200:203]\n\t" \
    "s_waitcnt lgkmcnt(2)\n\t" \
    "v_mfma_f32_16x16x32_bf16 v[216:219], v[160:163], v[228:231], v[216:219]\n\t" \
    "ds_read_b128 v[240:243], v253 offset:17408\n\t" \
    "s_waitcnt lgkmcnt(2)\n\t" \
    "v_mfma_f32_16x16x32_bf16 v[216:219], v[164:167], v[232:235], v[216:219]\n\t" \
    "ds_read_b128 v[224:227], v253 offset:36864\n\t" \
    "v_mfma_f32_16x16x32_bf16 v[220:223], v[168:171], v[228:231], v[220:223]\n\t" \
    "v_mfma_f32_16x16x32_bf16 v[220:223], v[172:175], v[232:235], v[220:223]\n\t" \
    "v_mfma_f32_16x16x32_bf16 v[212:215], v[244:247], v[228:231], v[212:215]\n\t" \
    "ds_read_b128 v[244:247], v254 offset:55296\n\t" \
    "v_mfma_f32_16x16x32_bf16 v[212:215], v[248:251], v[232:235], v[212:215]\n\t" \
    "ds_read_b128 v[248:251], v254 offset:56320\n\t" \
    "v_and_b32 v252, 0xf0, v253\n\t" \
    "v_lshlrev_b32 v252, 3, v252\n\t" \
    "buffer_load_dword v252, v252, s[60:63], %[fl] offen sc1\n\t" \
    "s_waitcnt lgkmcnt(4)\n\t" \
    "v_mfma_f32_16x16x32_bf16 v[204:207], v[0:3], v[236:239], v[204:207]\n\t" \
    "ds_read_b128 v[228:231], v253 offset:37888\n\t" \
    "s_waitcnt lgkmcnt(4)\n\t" \
    "v_mfma_f32_16x16x32_bf16 v[204:207], v[4:7], v[240:243], v[204:207]\n\t" \
    "ds_read_b128 v[232:235], v253 offset:14336\n\t" \
    "v_mfma_f32_16x16x32_bf16 v[208:211], v[8:11], v[236:239], v[208:211]\n\t" \
    "v_mfma_f32_16x16x32_bf16 v[208:211], v[12:15], v[240:243], v[208:211]\n\t" \
    "s_waitcnt lgkmcnt(3)\n\t" \
    "v_mfma_f32_16x16x32_bf16 v[200:203], v[244:247], v[236:239], v[200:203]\n\t" \
    "s_waitcnt lgkmcnt(2)\n\t" \
    "v_mfma_f32_16x16x32_bf16 v[200:203], v[248:251], v[240:243], v[200:203]\n\t" \
    "v_mfma_f32_16x16x32_bf16 v[216:219], v[0:3], v[224:227], v[216:219]\n\t" \
    "ds_read_b128 v[236:239], v253 offset:15360\n\t" \
    "s_waitcnt lgkmcnt(2)\n\t" \
    "v_mfma_f32_16x16x32_bf16 v[216:219], v[4:7], v[228:231], v[216:219]\n\t" \
    "ds_read_b128 v[240:243], v253 offset:18432\n\t" \
    "v_mfma_f32_16x16x32_bf16 v[220:223], v[8:11], v[224:227], v[220:223]\n\t" \
    "v_mfma_f32_16x16x32_bf16 v[220:223], v[12:15], v[228:231], v[220:223]\n\t" \
    "v_mfma_f32_16x16x32_bf16 v[212:215], v[244:247], v[224:227], v[212:215]\n\t" \
    "v_mfma_f32_16x16x32_bf16 v[212:215], v[248:251], v[228:231], v[212:215]\n\t" \
    "s_waitcnt lgkmcnt(2)\n\t" \
    "v_mfma_f32_16x16x32_bf16 v[204:207], v[184:187], v[232:235], v[204:207]\n\t" \
    "ds_read_b128 v[224:227], v253 offset:19456\n\t" \
    "v_mov_b64 v[244:245], 0\n\t" \
    "s_waitcnt lgkmcnt(2)\n\t" \
    "v_mfma_f32_16x16x32_bf16 v[204:207], v[188:191], v[236:239], v[204:207]\n\t" \
    "ds_read_b128 v[228:231], v253 offset:34816\n\t" \
    "v_mov_b64 v[246:247], 0\n\t" \
    "v_mfma_f32_16x16x32_bf16 v[208:211], v[192:195], v[232:235], v[208:211]\n\t" \
    "v_mov_b64 v[248:249], 0\n\t" \
    "v_mfma_f32_16x16x32_bf16 v[208:211], v[196:199], v[236:239], v[208:211]\n\t" \
    "v_mov_b64 v[250:251], 0\n\t" \
    "v_mfma_f32_16x16x32_bf16 v[200:203], v[176:179], v[232:235], v[200:203]\n\t" \
    "v_mfma_f32_16x16x32_bf16 v[200:203], v[180:183], v[236:239], v[200:203]\n\t" \
    "s_waitcnt lgkmcnt(2)\n\t" \
    "v_mfma_f32_16x16x32_bf16 v[204:207], v[24:27], v[240:243], v[204:207]\n\t" \
    "ds_read_b128 v[232:235], v253 offset:35840\n\t" \
    "s_waitcnt lgkmcnt(2)\n\t" \
    "v_mfma_f32_16x16x32_bf16 v[204:207], v[28:31], v[224:227], v[204:207]\n\t" \
    "ds_read_b128 v[236:239], v253 offset:38912\n\t" \
    "v_mfma_f32_16x16x32_bf16 v[208:211], v[32:35], v[240:243], v[208:211]\n\t" \
    "v_mfma_f32_16x16x32_bf16 v[208:211], v[36:39], v[224:227], v[208:211]\n\t" \
    "v_mfma_f32_16x16x32_bf16 v[200:203], v[16:19], v[240:243], v[200:203]\n\t" \
    "v_mfma_f32_16x16x32_bf16 v[200:203], v[20:23], v[224:227], v[200:203]\n\t" \
    "ds_read_b128 v[240:243], v253 offset:39936\n\t" \
    "s_waitcnt lgkmcnt(3)\n\t" \
    "v_mfma_f32_16x16x32_bf16 v[216:219], v[184:187], v[228:231], v[216:219]\n\t" \
    "s_waitcnt lgkmcnt(2)\n\t" \
    "v_mfma_f32_16x16x32_bf16 v[216:219], v[188:191], v[232:235], v[216:219]\n\t" \
    "v_cvt_pk_bf16_f32 v204, v204, v205\n\t" \
    "v_cvt_pk_bf16_f32 v205, v206, v207\n\t" \
    "s_waitcnt lgkmcnt(1)\n\t" \
    "v_mfma_f32_16x16x32_bf16 v[216:219], v[24:27], v[236:239], v[216:219]\n\t" \
    "ds_write_b64 v254, v[204:205] offset:4104\n\t" \
    "v_cvt_pk_bf16_f32 v208, v208, v209\n\t" \
    "s_waitcnt lgkmcnt(1)\n\t" \
    "v_mfma_f32_16x16x32_bf16 v[216:219], v[28:31], v[240:243], v[216:219]\n\t" \
    "v_cvt_pk_bf16_f32 v209, v210, v211\n\t" \
    "ds_write_b64 v254, v[208:209] offset:5128\n\t" \
    "v_mfma_f32_16x16x32_bf16 v[220:223], v[192:195], v[228:231], v[220:223]\n\t" \
    "v_cvt_pk_bf16_f32 v200, v200, v201\n\t" \
    "v_cvt_pk_bf16_f32 v201, v202, v203\n\t" \
    "v_mfma_f32_16x16x32_bf16 v[220:223], v[196:199], v[232:235], v[220:223]\n\t" \
    "ds_write_b64 v254, v[200:201] offset:3080\n\t" \
    "ds_read_b128 v[200:203], v254 offset:55296\n\t" \
    "v_mfma_f32_16x16x32_bf16 v[220:223], v[32:35], v[236:239], v[220:223]\n\t" \
    "ds_read_b128 v[204:207], v254 offset:56320\n\t" \
    "v_mov_b64 v[208:209], 0\n\t" \
    "v_mfma_f32_16x16x32_bf16 v[220:223], v[36:39], v[240:243], v[220:223]\n\t" \
    "v_mov_b64 v[210:211], 0\n\t" \
    "v_cvt_pk_bf16_f32 v216, v216, v217\n\t" \
    "v_mfma_f32_16x16x32_bf16 v[212:215], v[176:179], v[228:231], v[212:215]\n\t" \
    "v_cvt_pk_bf16_f32 v217, v218, v219\n\t" \
    "ds_write_b64 v254, v[216:217] offset:10248\n\t" \
    "v_mfma_f32_16x16x32_bf16 v[212:215], v[180:183], v[232:235], v[212:215]\n\t" \
    "v_mfma_f32_16x16x32_bf16 v[212:215], v[16:19], v[236:239], v[212:215]\n\t" \
    "v_cvt_pk_bf16_f32 v220, v220, v221\n\t" \
    "v_cvt_pk_bf16_f32 v221, v222, v223\n\t" \
    "v_mfma_f32_16x16x32_bf16 v[212:215], v[20:23], v[240:243], v[212:215]\n\t" \
    "ds_write_b64 v254, v[220:221] offset:11272\n\t" \
    "ds_read_b128 v[216:219], v254 offset:57344\n\t" \
    "ds_read_b128 v[220:223], v254 offset:58368\n\t" \
    "s_nop 4\n\t" \
    "v_cvt_pk_bf16_f32 v212, v212, v213\n\t" \
    "v_cvt_pk_bf16_f32 v213, v214, v215\n\t" \
    "ds_write_b64 v254, v[212:213] offset:9224\n\t" \
    LST6 \
    "s_waitcnt vmcnt(0) lgkmcnt(0)\n\t" \
    "s_barrier \n\t" \
    "s_setprio 2\n\t" \
    "s_add_u32 s44, s40, 1\n\t" \
    "s_min_u32 s44, s44, 63\n\t" \
    ".Lv38_%=:\n\t" \
    "v_cmp_ge_u32 vcc, s44, v252\n\t" \
    "s_cbranch_vccz .Lv39_%=\n\t" \
    "s_cmp_eq_u32 s41, 0\n\t" \
    "s_cbranch_scc1 .Lv39_%=\n\t" \
    "s_sub_u32 s41, s41, 1\n\t" \
    "s_sleep 2\n\t" \
    "v_and_b32 v252, 0xf0, v253\n\t" \
    "v_lshlrev_b32 v252, 3, v252\n\t" \
    "buffer_load_dword v252, v252, s[60:63], %[fl] offen sc1\n\t" \
    "s_waitcnt vmcnt(0)\n\t" \
    "s_branch .Lv38_%=\n\t" \
    ".Lv39_%=:\n\t" \
    "s_movk_i32 s45, 0x3000\n\t" \
    "s_mov_b32 m0, s45\n\t" \
    "s_nop 0\n\t" \
    "buffer_load_dwordx4 v253, s[60:63], s42 offen offset:0 sc1 lds\n\t" \
    "buffer_load_dwordx4 v253, s[60:63], s42 offen offset:1024 sc1 lds\n\t" \
    "buffer_load_dwordx4 v253, s[60:63], s42 offen offset:2048 sc1 lds\n\t" \
    "buffer_load_dwordx4 v253, s[60:63], s42 offen offset:3072 sc1 lds\n\t" \
    "s_add_u32 s44, s42, 0x1000\n\t" \
    "s_mov_b32 s45, 0x8000\n\t" \
    "s_mov_b32 m0, s45\n\t" \
    "s_nop 0\n\t" \
    "buffer_load_dwordx4 v253, s[60:63], s44 offen offset:0 sc1 lds\n\t" \
    "buffer_load_dwordx4 v253, s[60:63], s44 offen offset:1024 sc1 lds\n\t" \
    "buffer_load_dwordx4 v253, s[60:63], s44 offen offset:2048 sc1 lds\n\t" \
    "buffer_load_dwordx4 v253, s[60:63], s44 offen offset:3072 sc1 lds\n\t" \
    "s_add_u32 s44, s42, 0x4c00\n\t" \
    "s_mov_b32 s45, 0x4c00\n\t" \
    "s_mov_b32 m0, s45\n\t" \
    "s_nop 0\n\t" \
    "buffer_load_dwordx4 v253, s[60:63], s44 offen offset:0 sc1 lds\n\t" \
    "s_add_u32 s44, s44, 0x1000\n\t" \
    "s_mov_b32 s45, 0x9c00\n\t" \
    "s_mov_b32 m0, s45\n\t" \
    "s_nop 0\n\t" \
    "buffer_load_dwordx4 v253, s[60:63], s44 offen offset:0 sc1 lds\n\t" \
    LST1 \
    "s_sleep 18\n\t" \
    "buffer_load_dwordx4 v[224:227], v253, s[60:63], s46 offen sc1\n\t" \
    "buffer_load_dwordx4 v[228:231], v253, s[60:63], s47 offen sc1\n\t" \
    "s_waitcnt lgkmcnt(0)\n\t" \
    LFLUSH212 \
    "v_bfe_i32 v212, v253, 5, 1\n\t" \
    "v_and_b32 v212, 0xaaaa, v212\n\t" \
    "v_or_b32 v212, 0x4444, v212\n\t" \
    "v_and_b32 v213, 0xffff, v255\n\t" \
    "v_lshrrev_b32 v214, 16, v255\n\t" \
    ".Lv310_%=:\n\t" \
    "s_waitcnt vmcnt(1)\n\t" \
    "v_max_u32 v215, v224, v225\n\t" \
    "v_max3_u32 v215, v226, v227, v215\n\t" \
    "s_waitcnt vmcnt(0)\n\t" \
    "v_max3_u32 v215, v228, v229, v215\n\t" \
    "v_max3_u32 v215, v230, v231, v215\n\t" \
    "v_cmp_eq_u32 vcc, -1, v215\n\t" \
    "s_cbranch_vccz .Lv311_%=\n\t" \
    "v_max_u32 v215, v224, v225\n\t" \
    "v_max3_u32 v215, v226, v227, v215\n\t" \
    "v_cmp_eq_u32 vcc, -1, v215\n\t" \
    "s_cbranch_vccz .Lv312_%=\n\t" \
    "buffer_load_dwordx4 v[224:227], v253, s[60:63], s46 offen sc1\n\t" \
    ".Lv312_%=:\n\t" \
    "v_max_u32 v215, v228, v229\n\t" \
    "v_max3_u32 v215, v230, v231, v215\n\t" \
    "v_cmp_eq_u32 vcc, -1, v215\n\t" \
    "s_cbranch_vccz .Lv313_%=\n\t" \
    "buffer_load_dwordx4 v[228:231], v253, s[60:63], s47 offen sc1\n\t" \
    ".Lv313_%=:\n\t" \
    "s_cmp_eq_u32 s41, 0\n\t" \
    "s_cbranch_scc1 .Lv311_%=\n\t" \
    "s_sub_u32 s41, s41, 1\n\t" \
    "s_waitcnt vmcnt(0)\n\t" \
    "s_branch .Lv310_%=\n\t" \
    ".Lv311_%=:\n\t" \
    LST2 \
    "ds_write_b128 v253, v[224:227] offset:49152\n\t" \
    "ds_write_b128 v253, v[228:231] offset:50176\n\t" \
    "v_smfmac_f32_16x16x64_bf16 v[244:247], v[224:227], v[0:7], v212\n\t" \
    "v_smfmac_f32_16x16x64_bf16 v[248:251], v[224:227], v[8:15], v212\n\t" \
    "v_smfmac_f32_16x16x64_bf16 v[208:211], v[224:227], v[200:207], v212\n\t" \
    "s_waitcnt lgkmcnt(0)\n\t" \
    "s_barrier \n\t" \
    LST3 \
    "ds_read_b128 v[232:235], v253 offset:40960\n\t" \
    "ds_read_b128 v[236:239], v253 offset:41984\n\t" \
    "ds_read_b128 v[240:243], v253 offset:43008\n\t" \
    "ds_read_b128 v[224:227], v253 offset:44032\n\t" \
    "ds_read_b128 v[200:203], v254 offset:59392\n\t" \
    "ds_read_b128 v[204:207], v254 offset:60416\n\t" \
    "v_smfmac_f32_16x16x64_bf16 v[208:211], v[228:231], v[16:23], v212\n\t" \
    "v_smfmac_f32_16x16x64_bf16 v[244:247], v[228:231], v[24:31], v212\n\t" \
    "v_smfmac_f32_16x16x64_bf16 v[248:251], v[228:231], v[32:39], v212\n\t" \
    "ds_read_b128 v[228:231], v253 offset:45056\n\t" \
    "s_waitcnt lgkmcnt(6)\n\t" \
    "v_smfmac_f32_16x16x64_bf16 v[208:211], v[232:235], v[216:223], v212\n\t" \
    "ds_read_b128 v[216:219], v254 offset:61440\n\t" \
    "ds_read_b128 v[220:223], v254 offset:62464\n\t" \
    "v_smfmac_f32_16x16x64_bf16 v[244:247], v[232:235], v[40:47], v212\n\t" \
    "v_smfmac_f32_16x16x64_bf16 v[248:251], v[232:235], v[48:55], v212\n\t" \
    "ds_read_b128 v[232:235], v253 offset:46080\n\t" \
    "s_waitcnt lgkmcnt(8)\n\t" \
    "v_smfmac_f32_16x16x64_bf16 v[208:211], v[236:239], v[56:63], v212\n\t" \
    "v_smfmac_f32_16x16x64_bf16 v[244:247], v[236:239], v[64:71], v212\n\t" \
    "v_smfmac_f32_16x16x64_bf16 v[248:251], v[236:239], v[72:79], v212\n\t" \
    "ds_read_b128 v[236:239], v253 offset:47104\n\t" \
    "s_waitcnt lgkmcnt(5)\n\t" \
    "v_smfmac_f32_16x16x64_bf16 v[208:211], v[240:243], v[200:207], v212\n\t" \
    "ds_read_b128 v[200:203], v254 offset:63488\n\t" \
    "ds_read_b128 v[204:207], v254 offset:64512\n\t" \
    "v_smfmac_f32_16x16x64_bf16 v[244:247], v[240:243], v[80:87], v212\n\t" \
    "v_smfmac_f32_16x16x64_bf16 v[248:251], v[240:243], v[88:95], v212\n\t" \
    "ds_read_b128 v[240:243], v253 offset:48128\n\t" \
    "v_smfmac_f32_16x16x64_bf16 v[208:211], v[224:227], v[96:103], v212\n\t" \
    "v_smfmac_f32_16x16x64_bf16 v[244:247], v[224:227], v[104:111], v212\n\t" \
    "v_smfmac_f32_16x16x64_bf16 v[248:251], v[224:227], v[112:119], v212\n\t" \
    "s_waitcnt lgkmcnt(7)\n\t" \
    "v_smfmac_f32_16x16x64_bf16 v[244:247], v[228:231], v[120:127], v212\n\t" \
    "s_waitcnt lgkmcnt(4)\n\t" \
    "v_smfmac_f32_16x16x64_bf16 v[244:247], v[232:235], v[144:151], v212\n\t" \
    "s_waitcnt lgkmcnt(3)\n\t" \
    "v_smfmac_f32_16x16x64_bf16 v[244:247], v[236:239], v[160:167], v212\n\t" \
    "s_waitcnt lgkmcnt(0)\n\t" \
    "v_smfmac_f32_16x16x64_bf16 v[244:247], v[240:243], v[184:191], v212\n\t" \
    "v_smfmac_f32_16x16x64_bf16 v[248:251], v[228:231], v[128:135], v212\n\t" \
    "v_smfmac_f32_16x16x64_bf16 v[248:251], v[232:235], v[152:159], v212\n\t" \
    "v_smfmac_f32_16x16x64_bf16 v[248:251], v[236:239], v[168:175], v212\n\t" \
    "v_add_f32 v244, v244, v246\n\t" \
    "v_add_f32 v245, v245, v247\n\t" \
    "v_cvt_pk_bf16_f32 v244, v244, v245\n\t" \
    "v_smfmac_f32_16x16x64_bf16 v[248:251], v[240:243], v[192:199], v212\n\t" \
    "ds_write_b16 v213, v244 offset:512\n\t" \
    "ds_write_b16_d16_hi v213, v244 offset:528\n\t" \
    "v_smfmac_f32_16x16x64_bf16 v[208:211], v[228:231], v[216:223], v212\n\t" \
    "v_smfmac_f32_16x16x64_bf16 v[208:211], v[232:235], v[136:143], v212\n\t" \
    "v_smfmac_f32_16x16x64_bf16 v[208:211], v[236:239], v[200:207], v212\n\t" \
    "v_add_f32 v248, v248, v250\n\t" \
    "v_add_f32 v249, v249, v251\n\t" \
    "v_cvt_pk_bf16_f32 v248, v248, v249\n\t" \
    "v_smfmac_f32_16x16x64_bf16 v[208:211], v[240:243], v[176:183], v212\n\t" \
    "ds_write_b16 v213, v248 offset:1024\n\t" \
    "ds_write_b16_d16_hi v213, v248 offset:1040\n\t" \
    "s_nop 5\n\t" \
    "v_add_f32 v208, v208, v210\n\t" \
    "v_add_f32 v209, v209, v211\n\t" \
    "v_cvt_pk_bf16_f32 v208, v208, v209\n\t" \
    "ds_write_b16 v213, v208 offset:0\n\t" \
    "ds_write_b16_d16_hi v213, v208 offset:16\n\t" \
    "s_setprio 0\n\t" \
    "s_waitcnt lgkmcnt(0)\n\t" \
    "s_barrier \n\t" \
    LST4 \
    "ds_read_b128 v[224:227], v253 offset:0\n\t" \
    "ds_read_b128 v[244:247], v254 offset:57344\n\t" \
    "ds_read_b128 v[228:231], v253 offset:1024\n\t" \
    "ds_read_b128 v[232:235], v253 offset:20480\n\t" \
    "ds_read_b128 v[236:239], v253 offset:21504\n\t" \
    "ds_read_b128 v[248:251], v254 offset:58368\n\t" \
    "s_add_u32 s40, s40, 1\n\t" \
    "s_cmp_lg_u32 s40, 64\n\t" \
    "s_cbranch_scc1 .Lv37_%=\n\t" \
    "s_branch .Lend%=\n\t" \
    ".Lv2_%=:\n\t" \
    "s_mov_b32 s42, %[xs]\n\t" \
    "s_add_u32 m0, %[wm], 0xc000\n\t" \
    "s_mov_b32 s44, 0x0\n\t" \
    "buffer_load_dwordx4 v240, %[wr], s44 offen offset:0 nt lds\n\t" \
    "buffer_load_dwordx4 v241, %[wr], s44 offen offset:1024 nt lds\n\t" \
    "buffer_load_dwordx4 v242, %[wr], s44 offen offset:2048 nt lds\n\t" \
    "s_add_u32 m0, %[wm], 0xf000\n\t" \
    "s_mov_b32 s44, 0x60000\n\t" \
    "buffer_load_dwordx4 v240, %[wr], s44 offen offset:0 nt lds\n\t" \
    "buffer_load_dwordx4 v241, %[wr], s44 offen offset:1024 nt lds\n\t" \
    "buffer_load_dwordx4 v242, %[wr], s44 offen offset:2048 nt lds\n\t" \
    "s_add_u32 m0, %[wm], 0x0\n\t" \
    "s_mov_b32 s44, 0xc0000\n\t" \
    "buffer_load_dwordx4 v240, %[wr], s44 offen offset:0 nt lds\n\t" \
    "buffer_load_dwordx4 v241, %[wr], s44 offen offset:1024 nt lds\n\t" \
    "buffer_load_dwordx4 v242, %[wr], s44 offen offset:2048 nt lds\n\t" \
    "s_add_u32 m0, %[wm], 0x3000\n\t" \
    "s_mov_b32 s44, 0x120000\n\t" \
    "buffer_load_dwordx4 v240, %[wr], s44 offen offset:0 nt lds\n\t" \
    "buffer_load_dwordx4 v241, %[wr], s44 offen offset:1024 nt lds\n\t" \
    "buffer_load_dwordx4 v242, %[wr], s44 offen offset:2048 nt lds\n\t" \
    "s_add_u32 m0, %[wm], 0x6000\n\t" \
    "s_mov_b32 s44, 0x180000\n\t" \
    "buffer_load_dwordx4 v240, %[wr], s44 offen offset:0 nt lds\n\t" \
    "buffer_load_dwordx4 v241, %[wr], s44 offen offset:1024 nt lds\n\t" \
    "buffer_load_dwordx4 v242, %[wr], s44 offen offset:2048 nt lds\n\t" \
    "v_add_u32 v244, 0xc000, v243\n\t" \
    "s_waitcnt vmcnt(12) lgkmcnt(0)\n\t" \
    "s_barrier \n\t" \
    "s_add_u32 m0, %[wm], 0x9000\n\t" \
    "s_mov_b32 s44, 0x1e0000\n\t" \
    "buffer_load_dwordx4 v240, %[wr], s44 offen offset:0 nt lds\n\t" \
    "buffer_load_dwordx4 v241, %[wr], s44 offen offset:1024 nt lds\n\t" \
    "buffer_load_dwordx4 v242, %[wr], s44 offen offset:2048 nt lds\n\t" \
    "ds_read_b64_tr_b16 v[208:209], v244 offset:0\n\t" \
    "ds_read_b64_tr_b16 v[210:211], v244 offset:1536\n\t" \
    "ds_read_b64_tr_b16 v[40:41], v244 offset:32\n\t" \
    "ds_read_b64_tr_b16 v[42:43], v244 offset:1568\n\t" \
    "ds_read_b64_tr_b16 v[48:49], v244 offset:64\n\t" \
    "ds_read_b64_tr_b16 v[50:51], v244 offset:1600\n\t" \
    "v_add_u32 v244, 0xf000, v243\n\t" \
    "s_waitcnt vmcnt(12) lgkmcnt(0)\n\t" \
    "s_barrier \n\t" \
    "s_add_u32 m0, %[wm], 0xc000\n\t" \
    "s_mov_b32 s44, 0x240000\n\t" \
    "buffer_load_dwordx4 v240, %[wr], s44 offen offset:0 nt lds\n\t" \
    "buffer_load_dwordx4 v241, %[wr], s44 offen offset:1024 nt lds\n\t" \
    "buffer_load_dwordx4 v242, %[wr], s44 offen offset:2048 nt lds\n\t" \
    "ds_read_b64_tr_b16 v[212:213], v244 offset:0\n\t" \
    "ds_read_b64_tr_b16 v[214:215], v244 offset:1536\n\t" \
    "ds_read_b64_tr_b16 v[44:45], v244 offset:32\n\t" \
    "ds_read_b64_tr_b16 v[46:47], v244 offset:1568\n\t" \
    "ds_read_b64_tr_b16 v[52:53], v244 offset:64\n\t" \
    "ds_read_b64_tr_b16 v[54:55], v244 offset:1600\n\t" \
    "v_add_u32 v244, 0x0, v243\n\t" \
    "s_waitcnt vmcnt(12) lgkmcnt(0)\n\t" \
    "s_barrier \n\t" \
    "s_add_u32 m0, %[wm], 0xf000\n\t" \
    "s_mov_b32 s44, 0x2a0000\n\t" \
    "buffer_load_dwordx4 v240, %[wr], s44 offen offset:0 nt lds\n\t" \
    "buffer_load_dwordx4 v241, %[wr], s44 offen offset:1024 nt lds\n\t" \
    "buffer_load_dwordx4 v242, %[wr], s44 offen offset:2048 nt lds\n\t" \
    "ds_read_b64_tr_b16 v[56:57], v244 offset:0\n\t" \
    "ds_read_b64_tr_b16 v[58:59], v244 offset:1536\n\t" \
    "ds_read_b64_tr_b16 v[64:65], v244 offset:32\n\t" \
    "ds_read_b64_tr_b16 v[66:67], v244 offset:1568\n\t" \
    "ds_read_b64_tr_b16 v[72:73], v244 offset:64\n\t" \
    "ds_read_b64_tr_b16 v[74:75], v244 offset:1600\n\t" \
    "v_add_u32 v244, 0x3000, v243\n\t" \
    "s_waitcnt vmcnt(12) lgkmcnt(0)\n\t" \
    "s_barrier \n\t" \
    "s_add_u32 m0, %[wm], 0x0\n\t" \
    "s_mov_b32 s44, 0x300000\n\t" \
    "buffer_load_dwordx4 v240, %[wr], s44 offen offset:0 nt lds\n\t" \
    "buffer_load_dwordx4 v241, %[wr], s44 offen offset:1024 nt lds\n\t" \
    "buffer_load_dwordx4 v242, %[wr], s44 offen offset:2048 nt lds\n\t" \
    "ds_read_b64_tr_b16 v[60:61], v244 offset:0\n\t" \
    "ds_read_b64_tr_b16 v[62:63], v244 offset:1536\n\t" \
    "ds_read_b64_tr_b16 v[68:69], v244 offset:32\n\t" \
    "ds_read_b64_tr_b16 v[70:71], v244 offset:1568\n\t" \
    "ds_read_b64_tr_b16 v[76:77], v244 offset:64\n\t" \
    "ds_read_b64_tr_b16 v[78:79], v244 offset:1600\n\t" \
    "v_add_u32 v244, 0x6000, v243\n\t" \
    "s_waitcnt vmcnt(12) lgkmcnt(0)\n\t" \
    "s_barrier \n\t" \
    "s_add_u32 m0, %[wm], 0x3000\n\t" \
    "s_mov_b32 s44, 0x360000\n\t" \
    "buffer_load_dwordx4 v240, %[wr], s44 offen offset:0 nt lds\n\t" \
    "buffer_load_dwordx4 v241, %[wr], s44 offen offset:1024 nt lds\n\t" \
    "buffer_load_dwordx4 v242, %[wr], s44 offen offset:2048 nt lds\n\t" \
    "ds_read_b64_tr_b16 v[216:217], v244 offset:0\n\t" \
    "ds_read_b64_tr_b16 v[218:219], v244 offset:1536\n\t" \
    "ds_read_b64_tr_b16 v[80:81], v244 offset:32\n\t" \
    "ds_read_b64_tr_b16 v[82:83], v244 offset:1568\n\t" \
    "ds_read_b64_tr_b16 v[88:89], v244 offset:64\n\t" \
    "ds_read_b64_tr_b16 v[90:91], v244 offset:1600\n\t" \
    "v_add_u32 v244, 0x9000, v243\n\t" \
    "s_waitcnt vmcnt(12) lgkmcnt(0)\n\t" \
    "s_barrier \n\t" \
    "s_add_u32 m0, %[wm], 0x6000\n\t" \
    "s_mov_b32 s44, 0x3c0000\n\t" \
    "buffer_load_dwordx4 v240, %[wr], s44 offen offset:0 nt lds\n\t" \
    "buffer_load_dwordx4 v241, %[wr], s44 offen offset:1024 nt lds\n\t" \
    "buffer_load_dwordx4 v242, %[wr], s44 offen offset:2048 nt lds\n\t" \
    "ds_read_b64_tr_b16 v[220:221], v244 offset:0\n\t" \
    "ds_read_b64_tr_b16 v[222:223], v244 offset:1536\n\t" \
    "ds_read_b64_tr_b16 v[84:85], v244 offset:32\n\t" \
    "ds_read_b64_tr_b16 v[86:87], v244 offset:1568\n\t" \
    "ds_read_b64_tr_b16 v[92:93], v244 offset:64\n\t" \
    "ds_read_b64_tr_b16 v[94:95], v244 offset:1600\n\t" \
    "v_add_u32 v244, 0xc000, v243\n\t" \
    "s_waitcnt vmcnt(12) lgkmcnt(0)\n\t" \
    "s_barrier \n\t" \
    "v_mov_b32 v246, 0\n\t" \
    "buffer_load_dword v246, v246, s[60:63], %[mo] offen sc1\n\t" \
    "s_add_u32 m0, %[wm], 0x9000\n\t" \
    "s_mov_b32 s44, 0x420000\n\t" \
    "buffer_load_dwordx4 v240, %[wr], s44 offen offset:0 nt lds\n\t" \
    "buffer_load_dwordx4 v241, %[wr], s44 offen offset:1024 nt lds\n\t" \
    "buffer_load_dwordx4 v242, %[wr], s44 offen offset:2048 nt lds\n\t" \
    "ds_read_b64_tr_b16 v[96:97], v244 offset:0\n\t" \
    "ds_read_b64_tr_b16 v[98:99], v244 offset:1536\n\t" \
    "ds_read_b64_tr_b16 v[104:105], v244 offset:32\n\t" \
    "ds_read_b64_tr_b16 v[106:107], v244 offset:1568\n\t" \
    "ds_read_b64_tr_b16 v[112:113], v244 offset:64\n\t" \
    "ds_read_b64_tr_b16 v[114:115], v244 offset:1600\n\t" \
    "v_add_u32 v244, 0xf000, v243\n\t" \
    "s_waitcnt vmcnt(12) lgkmcnt(0)\n\t" \
    "s_barrier \n\t" \
    "s_add_u32 m0, %[wm], 0xc000\n\t" \
    "s_mov_b32 s44, 0x480000\n\t" \
    "buffer_load_dwordx4 v240, %[wr], s44 offen offset:0 nt lds\n\t" \
    "buffer_load_dwordx4 v241, %[wr], s44 offen offset:1024 nt lds\n\t" \
    "buffer_load_dwordx4 v242, %[wr], s44 offen offset:2048 nt lds\n\t" \
    "ds_read_b64_tr_b16 v[100:101], v244 offset:0\n\t" \
    "ds_read_b64_tr_b16 v[102:103], v244 offset:1536\n\t" \
    "ds_read_b64_tr_b16 v[108:109], v244 offset:32\n\t" \
    "ds_read_b64_tr_b16 v[110:111], v244 offset:1568\n\t" \
    "ds_read_b64_tr_b16 v[116:117], v244 offset:64\n\t" \
    "ds_read_b64_tr_b16 v[118:119], v244 offset:1600\n\t" \
    "v_add_u32 v244, 0x0, v243\n\t" \
    "s_waitcnt vmcnt(12) lgkmcnt(0)\n\t" \
    "s_barrier \n\t" \
    "s_add_u32 m0, %[wm], 0xf000\n\t" \
    "s_mov_b32 s44, 0x4e0000\n\t" \
    "buffer_load_dwordx4 v240, %[wr], s44 offen offset:0 nt lds\n\t" \
    "buffer_load_dwordx4 v241, %[wr], s44 offen offset:1024 nt lds\n\t" \
    "buffer_load_dwordx4 v242, %[wr], s44 offen offset:2048 nt lds\n\t" \
    "ds_read_b64_tr_b16 v[200:201], v244 offset:0\n\t" \
    "ds_read_b64_tr_b16 v[202:203], v244 offset:1536\n\t" \
    "ds_read_b64_tr_b16 v[0:1], v244 offset:32\n\t" \
    "ds_read_b64_tr_b16 v[2:3], v244 offset:1568\n\t" \
    "ds_read_b64_tr_b16 v[8:9], v244 offset:64\n\t" \
    "ds_read_b64_tr_b16 v[10:11], v244 offset:1600\n\t" \
    "v_add_u32 v244, 0x3000, v243\n\t" \
    "s_waitcnt vmcnt(12) lgkmcnt(0)\n\t" \
    "s_barrier \n\t" \
    "s_add_u32 m0, %[wm], 0x0\n\t" \
    "s_mov_b32 s44, 0x540000\n\t" \
    "buffer_load_dwordx4 v240, %[wr], s44 offen offset:0 nt lds\n\t" \
    "buffer_load_dwordx4 v241, %[wr], s44 offen offset:1024 nt lds\n\t" \
    "buffer_load_dwordx4 v242, %[wr], s44 offen offset:2048 nt lds\n\t" \
    "ds_read_b64_tr_b16 v[204:205], v244 offset:0\n\t" \
    "ds_read_b64_tr_b16 v[206:207], v244 offset:1536\n\t" \
    "ds_read_b64_tr_b16 v[4:5], v244 offset:32\n\t" \
    "ds_read_b64_tr_b16 v[6:7], v244 offset:1568\n\t" \
    "ds_read_b64_tr_b16 v[12:13], v244 offset:64\n\t" \
    "ds_read_b64_tr_b16 v[14:15], v244 offset:1600\n\t" \
    "v_add_u32 v244, 0x6000, v243\n\t" \
    "s_waitcnt vmcnt(12) lgkmcnt(0)\n\t" \
    "s_barrier \n\t" \
    "s_add_u32 m0, %[wm], 0x3000\n\t" \
    "s_mov_b32 s44, 0x5a0000\n\t" \
    "buffer_load_dwordx4 v240, %[wr], s44 offen offset:0 nt lds\n\t" \
    "buffer_load_dwordx4 v241, %[wr], s44 offen offset:1024 nt lds\n\t" \
    "buffer_load_dwordx4 v242, %[wr], s44 offen offset:2048 nt lds\n\t" \
    "ds_read_b64_tr_b16 v[224:225], v244 offset:0\n\t" \
    "ds_read_b64_tr_b16 v[226:227], v244 offset:1536\n\t" \
    "ds_read_b64_tr_b16 v[120:121], v244 offset:32\n\t" \
    "ds_read_b64_tr_b16 v[122:123], v244 offset:1568\n\t" \
    "ds_read_b64_tr_b16 v[128:129], v244 offset:64\n\t" \
    "ds_read_b64_tr_b16 v[130:131], v244 offset:1600\n\t" \
    "v_add_u32 v244, 0x9000, v243\n\t" \
    "s_waitcnt vmcnt(12) lgkmcnt(0)\n\t" \
    "s_barrier \n\t" \
    ".Lv21_%=:\n\t" \
    "v_readfirstlane_b32 s43, v246\n\t" \
    "s_nop 0\n\t" \
    "s_cmp_lg_u32 s43, -1\n\t" \
    "s_cbranch_scc1 .Lv22_%=\n\t" \
    "s_cmp_eq_u32 s41, 0\n\t" \
    "s_cbranch_scc1 .Lv22_%=\n\t" \
    "s_sub_u32 s41, s41, 1\n\t" \
    "s_sleep 4\n\t" \
    "v_mov_b32 v246, 0\n\t" \
    "buffer_load_dword v246, v246, s[60:63], %[mo] offen sc1\n\t" \
    "s_waitcnt vmcnt(0)\n\t" \
    "s_branch .Lv21_%=\n\t" \
    ".Lv22_%=:\n\t" \
    "v_mov_b32 v246, 0x8000\n\t" \
    "s_mov_b64 exec, 1\n\t" \
    "buffer_store_dword v246, v246, s[60:63], %[mo] offen sc1\n\t" \
    "s_mov_b64 exec, -1\n\t" \
    "s_and_b32 s43, s43, 0x2000\n\t" \
    "s_xor_b32 s43, s43, %[lx]\n\t" \
    "s_add_u32 s60, s60, s43\n\t" \
    "s_addc_u32 s61, s61, 0\n\t" \
    "v_and_b32 v252, 0xf0, v253\n\t" \
    "v_lshlrev_b32 v252, 3, v252\n\t" \
    "buffer_load_dword v252, v252, s[60:63], %[fl] offen sc1\n\t" \
    "s_add_u32 m0, %[wm], 0x6000\n\t" \
    "s_mov_b32 s44, 0x600000\n\t" \
    "buffer_load_dwordx4 v240, %[wr], s44 offen offset:0 nt lds\n\t" \
    "buffer_load_dwordx4 v241, %[wr], s44 offen offset:1024 nt lds\n\t" \
    "buffer_load_dwordx4 v242, %[wr], s44 offen offset:2048 nt lds\n\t" \
    "ds_read_b64_tr_b16 v[228:229], v244 offset:0\n\t" \
    "ds_read_b64_tr_b16 v[230:231], v244 offset:1536\n\t" \
    "ds_read_b64_tr_b16 v[124:125], v244 offset:32\n\t" \
    "ds_read_b64_tr_b16 v[126:127], v244 offset:1568\n\t" \
    "ds_read_b64_tr_b16 v[132:133], v244 offset:64\n\t" \
    "ds_read_b64_tr_b16 v[134:135], v244 offset:1600\n\t" \
    "v_add_u32 v244, 0xc000, v243\n\t" \
    "s_waitcnt vmcnt(12) lgkmcnt(0)\n\t" \
    "s_barrier \n\t" \
    "s_add_u32 m0, %[wm], 0x9000\n\t" \
    "s_mov_b32 s44, 0x660000\n\t" \
    "buffer_load_dwordx4 v240, %[wr], s44 offen offset:0 nt lds\n\t" \
    "buffer_load_dwordx4 v241, %[wr], s44 offen offset:1024 nt lds\n\t" \
    "buffer_load_dwordx4 v242, %[wr], s44 offen offset:2048 nt lds\n\t" \
    "ds_read_b64_tr_b16 v[136:137], v244 offset:0\n\t" \
    "ds_read_b64_tr_b16 v[138:139], v244 offset:1536\n\t" \
    "ds_read_b64_tr_b16 v[144:145], v244 offset:32\n\t" \
    "ds_read_b64_tr_b16 v[146:147], v244 offset:1568\n\t" \
    "ds_read_b64_tr_b16 v[152:153], v244 offset:64\n\t" \
    "ds_read_b64_tr_b16 v[154:155], v244 offset:1600\n\t" \
    "v_add_u32 v244, 0xf000, v243\n\t" \
    "s_waitcnt vmcnt(12) lgkmcnt(0)\n\t" \
    "s_barrier \n\t" \
    "s_add_u32 m0, %[wm], 0xc000\n\t" \
    "s_mov_b32 s44, 0x6c0000\n\t" \
    "buffer_load_dwordx4 v240, %[wr], s44 offen offset:0 nt lds\n\t" \
    "buffer_load_dwordx4 v241, %[wr], s44 offen offset:1024 nt lds\n\t" \
    "buffer_load_dwordx4 v242, %[wr], s44 offen offset:2048 nt lds\n\t" \
    "ds_read_b64_tr_b16 v[140:141], v244 offset:0\n\t" \
    "ds_read_b64_tr_b16 v[142:143], v244 offset:1536\n\t" \
    "ds_read_b64_tr_b16 v[148:149], v244 offset:32\n\t" \
    "ds_read_b64_tr_b16 v[150:151], v244 offset:1568\n\t" \
    "ds_read_b64_tr_b16 v[156:157], v244 offset:64\n\t" \
    "ds_read_b64_tr_b16 v[158:159], v244 offset:1600\n\t" \
    "v_add_u32 v244, 0x0, v243\n\t" \
    "s_waitcnt vmcnt(12) lgkmcnt(0)\n\t" \
    "s_barrier \n\t" \
    "s_add_u32 m0, %[wm], 0xf000\n\t" \
    "s_mov_b32 s44, 0x720000\n\t" \
    "buffer_load_dwordx4 v240, %[wr], s44 offen offset:0 nt lds\n\t" \
    "buffer_load_dwordx4 v241, %[wr], s44 offen offset:1024 nt lds\n\t" \
    "buffer_load_dwordx4 v242, %[wr], s44 offen offset:2048 nt lds\n\t" \
    "ds_read_b64_tr_b16 v[16:17], v244 offset:0\n\t" \
    "ds_read_b64_tr_b16 v[18:19], v244 offset:1536\n\t" \
    "ds_read_b64_tr_b16 v[24:25], v244 offset:32\n\t" \
    "ds_read_b64_tr_b16 v[26:27], v244 offset:1568\n\t" \
    "ds_read_b64_tr_b16 v[32:33], v244 offset:64\n\t" \
    "ds_read_b64_tr_b16 v[34:35], v244 offset:1600\n\t" \
    "v_add_u32 v244, 0x3000, v243\n\t" \
    "s_waitcnt vmcnt(12) lgkmcnt(0)\n\t" \
    "s_barrier \n\t" \
    "ds_read_b64_tr_b16 v[20:21], v244 offset:0\n\t" \
    "ds_read_b64_tr_b16 v[22:23], v244 offset:1536\n\t" \
    "ds_read_b64_tr_b16 v[28:29], v244 offset:32\n\t" \
    "ds_read_b64_tr_b16 v[30:31], v244 offset:1568\n\t" \
    "ds_read_b64_tr_b16 v[36:37], v244 offset:64\n\t" \
    "ds_read_b64_tr_b16 v[38:39], v244 offset:1600\n\t" \
    "v_add_u32 v244, 0x6000, v243\n\t" \
    "s_waitcnt vmcnt(9) lgkmcnt(0)\n\t" \
    "s_barrier \n\t" \
    "ds_read_b64_tr_b16 v[232:233], v244 offset:0\n\t" \
    "ds_read_b64_tr_b16 v[234:235], v244 offset:1536\n\t" \
    "ds_read_b64_tr_b16 v[160:161], v244 offset:32\n\t" \
    "ds_read_b64_tr_b16 v[162:163], v244 offset:1568\n\t" \
    "ds_read_b64_tr_b16 v[168:169], v244 offset:64\n\t" \
    "ds_read_b64_tr_b16 v[170:171], v244 offset:1600\n\t" \
    "v_add_u32 v244, 0x9000, v243\n\t" \
    "s_waitcnt vmcnt(6) lgkmcnt(0)\n\t" \
    "s_barrier \n\t" \
    "ds_read_b64_tr_b16 v[236:237], v244 offset:0\n\t" \
    "ds_read_b64_tr_b16 v[238:239], v244 offset:1536\n\t" \
    "ds_read_b64_tr_b16 v[164:165], v244 offset:32\n\t" \
    "ds_read_b64_tr_b16 v[166:167], v244 offset:1568\n\t" \
    "ds_read_b64_tr_b16 v[172:173], v244 offset:64\n\t" \
    "ds_read_b64_tr_b16 v[174:175], v244 offset:1600\n\t" \
    "v_add_u32 v244, 0xc000, v243\n\t" \
    "s_waitcnt vmcnt(3) lgkmcnt(0)\n\t" \
    "s_barrier \n\t" \
    "ds_read_b64_tr_b16 v[176:177], v244 offset:0\n\t" \
    "ds_read_b64_tr_b16 v[178:179], v244 offset:1536\n\t" \
    "ds_read_b64_tr_b16 v[184:185], v244 offset:32\n\t" \
    "ds_read_b64_tr_b16 v[186:187], v244 offset:1568\n\t" \
    "ds_read_b64_tr_b16 v[192:193], v244 offset:64\n\t" \
    "ds_read_b64_tr_b16 v[194:195], v244 offset:1600\n\t" \
    "s_mov_b32 s43, 0\n\t" \
    "v_cmp_eq_u32 vcc, 0, v252\n\t" \
    "s_cbranch_vccnz .Lv23_%=\n\t" \
    "s_movk_i32 s45, 0x2000\n\t" \
    "s_mov_b32 m0, s45\n\t" \
    "s_nop 0\n\t" \
    "buffer_load_dwordx4 v253, s[60:63], s42 offen offset:0 sc1 lds\n\t" \
    "buffer_load_dwordx4 v253, s[60:63], s42 offen offset:1024 sc1 lds\n\t" \
    "buffer_load_dwordx4 v253, s[60:63], s42 offen offset:2048 sc1 lds\n\t" \
    "buffer_load_dwordx4 v253, s[60:63], s42 offen offset:3072 sc1 lds\n\t" \
    "s_add_u32 s44, s42, 0x1000\n\t" \
    "s_movk_i32 s45, 0x7000\n\t" \
    "s_mov_b32 m0, s45\n\t" \
    "s_nop 0\n\t" \
    "buffer_load_dwordx4 v253, s[60:63], s44 offen offset:0 sc1 lds\n\t" \
    "buffer_load_dwordx4 v253, s[60:63], s44 offen offset:1024 sc1 lds\n\t" \
    "buffer_load_dwordx4 v253, s[60:63], s44 offen offset:2048 sc1 lds\n\t" \
    "buffer_load_dwordx4 v253, s[60:63], s44 offen offset:3072 sc1 lds\n\t" \
    "s_add_u32 s44, s42, 0x8800\n\t" \
    "s_mov_b32 s45, 0x4800\n\t" \
    "s_mov_b32 m0, s45\n\t" \
    "s_nop 0\n\t" \
    "buffer_load_dwordx4 v253, s[60:63], s44 offen offset:0 sc1 lds\n\t" \
    "s_add_u32 s44, s44, 0x1000\n\t" \
    "s_mov_b32 s45, 0x9800\n\t" \
    "s_mov_b32 m0, s45\n\t" \
    "s_nop 0\n\t" \
    "buffer_load_dwordx4 v253, s[60:63], s44 offen offset:0 sc1 lds\n\t" \
    "s_mov_b32 s43, 1\n\t" \
    ".Lv23_%=:\n\t" \
    "v_add_u32 v244, 0xf000, v243\n\t" \
    "s_waitcnt vmcnt(0) lgkmcnt(0)\n\t" \
    "s_barrier \n\t" \
    "ds_read_b64_tr_b16 v[180:181], v244 offset:0\n\t" \
    "ds_read_b64_tr_b16 v[182:183], v244 offset:1536\n\t" \
    "ds_read_b64_tr_b16 v[188:189], v244 offset:32\n\t" \
    "ds_read_b64_tr_b16 v[190:191], v244 offset:1568\n\t" \
    "ds_read_b64_tr_b16 v[196:197], v244 offset:64\n\t" \
    "ds_read_b64_tr_b16 v[198:199], v244 offset:1600\n\t" \
    "s_waitcnt lgkmcnt(0)\n\t" \
    "s_barrier \n\t" \
    LST0 \
    "ds_write_b128 v254, v[200:203] offset:51200\n\t" \
    "ds_write_b128 v254, v[204:207] offset:52224\n\t" \
    "ds_write_b128 v254, v[208:211] offset:53248\n\t" \
    "ds_write_b128 v254, v[212:215] offset:54272\n\t" \
    "ds_write_b128 v254, v[216:219] offset:55296\n\t" \
    "ds_write_b128 v254, v[220:223] offset:56320\n\t" \
    "ds_write_b128 v254, v[224:227] offset:57344\n\t" \
    "ds_write_b128 v254, v[228:231] offset:58368\n\t" \
    "ds_write_b128 v254, v[232:235] offset:59392\n\t" \
    "ds_write_b128 v254, v[236:239] offset:60416\n\t" \
    "s_cmp_lg_u32 s43, 0\n\t" \
    "s_cbranch_scc1 .Lv24_%=\n\t" \
    "s_mov_b32 s44, 0\n\t" \
    ".Lv25_%=:\n\t" \
    "v_cmp_ge_u32 vcc, s44, v252\n\t" \
    "s_cbranch_vccz .Lv26_%=\n\t" \
    "s_cmp_eq_u32 s41, 0\n\t" \
    "s_cbranch_scc1 .Lv26_%=\n\t" \
    "s_sub_u32 s41, s41, 1\n\t" \
    "s_sleep 8\n\t" \
    "v_and_b32 v252, 0xf0, v253\n\t" \
    "v_lshlrev_b32 v252, 3, v252\n\t" \
    "buffer_load_dword v252, v252, s[60:63], %[fl] offen sc1\n\t" \
    "s_waitcnt vmcnt(0)\n\t" \
    "s_branch .Lv25_%=\n\t" \
    ".Lv26_%=:\n\t" \
    "s_movk_i32 s45, 0x2000\n\t" \
    "s_mov_b32 m0, s45\n\t" \
    "s_nop 0\n\t" \
    "buffer_load_dwordx4 v253, s[60:63], s42 offen offset:0 sc1 lds\n\t" \
    "buffer_load_dwordx4 v253, s[60:63], s42 offen offset:1024 sc1 lds\n\t" \
    "buffer_load_dwordx4 v253, s[60:63], s42 offen offset:2048 sc1 lds\n\t" \
    "buffer_load_dwordx4 v253, s[60:63], s42 offen offset:3072 sc1 lds\n\t" \
    "s_add_u32 s44, s42, 0x1000\n\t" \
    "s_movk_i32 s45, 0x7000\n\t" \
    "s_mov_b32 m0, s45\n\t" \
    "s_nop 0\n\t" \
    "buffer_load_dwordx4 v253, s[60:63], s44 offen offset:0 sc1 lds\n\t" \
    "buffer_load_dwordx4 v253, s[60:63], s44 offen offset:1024 sc1 lds\n\t" \
    "buffer_load_dwordx4 v253, s[60:63], s44 offen offset:2048 sc1 lds\n\t" \
    "buffer_load_dwordx4 v253, s[60:63], s44 offen offset:3072 sc1 lds\n\t" \
    "s_add_u32 s44, s42, 0x8800\n\t" \
    "s_mov_b32 s45, 0x4800\n\t" \
    "s_mov_b32 m0, s45\n\t" \
    "s_nop 0\n\t" \
    "buffer_load_dwordx4 v253, s[60:63], s44 offen offset:0 sc1 lds\n\t" \
    "s_add_u32 s44, s44, 0x1000\n\t" \
    "s_mov_b32 s45, 0x9800\n\t" \
    "s_mov_b32 m0, s45\n\t" \
    "s_nop 0\n\t" \
    "buffer_load_dwordx4 v253, s[60:63], s44 offen offset:0 sc1 lds\n\t" \
    ".Lv24_%=:\n\t" \
    "s_waitcnt vmcnt(0) lgkmcnt(0)\n\t" \
    "s_barrier \n\t" \
    "ds_read_b128 v[224:227], v253 offset:0\n\t" \
    "ds_read_b128 v[244:247], v254 offset:53248\n\t" \
    "ds_read_b128 v[228:231], v253 offset:1024\n\t" \
    "ds_read_b128 v[232:235], v253 offset:20480\n\t" \
    "ds_read_b128 v[236:239], v253 offset:21504\n\t" \
    "ds_read_b128 v[248:251], v254 offset:54272\n\t" \
    ".Lv27_%=:\n\t" \
    LST7 \
    "s_waitcnt lgkmcnt(5)\n\t" \
    "v_mfma_f32_16x16x32_bf16 v[204:207], v[40:43], v[224:227], 0\n\t" \
    "s_waitcnt lgkmcnt(3)\n\t" \
    "v_mfma_f32_16x16x32_bf16 v[204:207], v[44:47], v[228:231], v[204:207]\n\t" \
    "ds_read_b128 v[240:243], v253 offset:2048\n\t" \
    "v_mfma_f32_16x16x32_bf16 v[208:211], v[48:51], v[224:227], 0\n\t" \
    "v_mfma_f32_16x16x32_bf16 v[208:211], v[52:55], v[228:231], v[208:211]\n\t" \
    "v_mfma_f32_16x16x32_bf16 v[200:203], v[244:247], v[224:227], 0\n\t" \
    "s_waitcnt lgkmcnt(1)\n\t" \
    "v_mfma_f32_16x16x32_bf16 v[200:203], v[248:251], v[228:231], v[200:203]\n\t" \
    "v_mfma_f32_16x16x32_bf16 v[216:219], v[40:43], v[232:235], 0\n\t" \
    "ds_read_b128 v[224:227], v253 offset:3072\n\t" \
    "v_mfma_f32_16x16x32_bf16 v[216:219], v[44:47], v[236:239], v[216:219]\n\t" \
    "ds_read_b128 v[228:231], v253 offset:22528\n\t" \
    "v_mfma_f32_16x16x32_bf16 v[220:223], v[48:51], v[232:235], 0\n\t" \
    "v_mfma_f32_16x16x32_bf16 v[220:223], v[52:55], v[236:239], v[220:223]\n\t" \
    "v_mfma_f32_16x16x32_bf16 v[212:215], v[244:247], v[232:235], 0\n\t" \
    "ds_read_b128 v[244:247], v254 offset:55296\n\t" \
    "v_mfma_f32_16x16x32_bf16 v[212:215], v[248:251], v[236:239], v[212:215]\n\t" \
    "ds_read_b128 v[248:251], v254 offset:56320\n\t" \
    "s_waitcnt lgkmcnt(4)\n\t" \
    "v_mfma_f32_16x16x32_bf16 v[204:207], v[64:67], v[240:243], v[204:207]\n\t" \
    "ds_read_b128 v[232:235], v253 offset:23552\n\t" \
    "s_waitcnt lgkmcnt(4)\n\t" \
    "v_mfma_f32_16x16x32_bf16 v[204:207], v[68:71], v[224:227], v[204:207]\n\t" \
    "ds_read_b128 v[236:239], v253 offset:4096\n\t" \
    "v_mfma_f32_16x16x32_bf16 v[208:211], v[72:75], v[240:243], v[208:211]\n\t" \
    "v_mfma_f32_16x16x32_bf16 v[208:211], v[76:79], v[224:227], v[208:211]\n\t" \
    "v_mfma_f32_16x16x32_bf16 v[200:203], v[56:59], v[240:243], v[200:203]\n\t" \
    "v_mfma_f32_16x16x32_bf16 v[200:203], v[60:63], v[224:227], v[200:203]\n\t" \
    "s_waitcnt lgkmcnt(4)\n\t" \
    "v_mfma_f32_16x16x32_bf16 v[216:219], v[64:67], v[228:231], v[216:219]\n\t" \
    "ds_read_b128 v[240:243], v253 offset:5120\n\t" \
    "s_waitcnt lgkmcnt(2)\n\t" \
    "v_mfma_f32_16x16x32_bf16 v[216:219], v[68:71], v[232:235], v[216:219]\n\t" \
    "ds_read_b128 v[224:227], v253 offset:24576\n\t" \
    "v_mfma_f32_16x16x32_bf16 v[220:223], v[72:75], v[228:231], v[220:223]\n\t" \
    "v_mfma_f32_16x16x32_bf16 v[220:223], v[76:79], v[232:235], v[220:223]\n\t" \
    "v_mfma_f32_16x16x32_bf16 v[212:215], v[56:59], v[228:231], v[212:215]\n\t" \
    "v_mfma_f32_16x16x32_bf16 v[212:215], v[60:63], v[232:235], v[212:215]\n\t" \
    "s_waitcnt lgkmcnt(2)\n\t" \
    "v_mfma_f32_16x16x32_bf16 v[204:207], v[80:83], v[236:239], v[204:207]\n\t" \
    "ds_read_b128 v[228:231], v253 offset:25600\n\t" \
    "s_add_u32 s44, s40, 1\n\t" \
    "s_waitcnt lgkmcnt(2)\n\t" \
    "v_mfma_f32_16x16x32_bf16 v[204:207], v[84:87], v[240:243], v[204:207]\n\t" \
    "ds_read_b128 v[232:235], v253 offset:6144\n\t" \
    "s_and_b32 s45, s44, 15\n\t" \
    "v_mfma_f32_16x16x32_bf16 v[208:211], v[88:91], v[236:239], v[208:211]\n\t" \
    "s_cmp_eq_u32 s45, 0\n\t" \
    "v_mfma_f32_16x16x32_bf16 v[208:211], v[92:95], v[240:243], v[208:211]\n\t" \
    "s_cselect_b32 s45, %[cj], %[cs]\n\t" \
    "v_mfma_f32_16x16x32_bf16 v[200:203], v[244:247], v[236:239], v[200:203]\n\t" \
    "s_cmp_eq_u32 s44, 64\n\t" \
    "v_mfma_f32_16x16x32_bf16 v[200:203], v[248:251], v[240:243], v[200:203]\n\t" \
    "s_cselect_b32 s45, 0, s45\n\t" \
    "s_waitcnt lgkmcnt(2)\n\t" \
    "v_mfma_f32_16x16x32_bf16 v[216:219], v[80:83], v[224:227], v[216:219]\n\t" \
    "ds_read_b128 v[236:239], v253 offset:7168\n\t" \
    "s_add_u32 s42, s42, s45\n\t" \
    "s_waitcnt lgkmcnt(2)\n\t" \
    "v_mfma_f32_16x16x32_bf16 v[216:219], v[84:87], v[228:231], v[216:219]\n\t" \
    "ds_read_b128 v[240:243], v253 offset:26624\n\t" \
    "v_mfma_f32_16x16x32_bf16 v[220:223], v[88:91], v[224:227], v[220:223]\n\t" \
    "v_mfma_f32_16x16x32_bf16 v[220:223], v[92:95], v[228:231], v[220:223]\n\t" \
    "v_mfma_f32_16x16x32_bf16 v[212:215], v[244:247], v[224:227], v[212:215]\n\t" \
    "ds_read_b128 v[244:247], v254 offset:51200\n\t" \
    "v_mfma_f32_16x16x32_bf16 v[212:215], v[248:251], v[228:231], v[212:215]\n\t" \
    "ds_read_b128 v[248:251], v254 offset:52224\n\t" \
    "s_waitcnt lgkmcnt(4)\n\t" \
    "v_mfma_f32_16x16x32_bf16 v[204:207], v[104:107], v[232:235], v[204:207]\n\t" \
    "ds_read_b128 v[224:227], v253 offset:27648\n\t" \
    "s_waitcnt lgkmcnt(4)\n\t" \
    "v_mfma_f32_16x16x32_bf16 v[204:207], v[108:111], v[236:239], v[204:207]\n\t" \
    "ds_read_b128 v[228:231], v253 offset:8192\n\t" \
    "v_mfma_f32_16x16x32_bf16 v[208:211], v[112:115], v[232:235], v[208:211]\n\t" \
    "v_mfma_f32_16x16x32_bf16 v[208:211], v[116:119], v[236:239], v[208:211]\n\t" \
    "v_mfma_f32_16x16x32_bf16 v[200:203], v[96:99], v[232:235], v[200:203]\n\t" \
    "v_mfma_f32_16x16x32_bf16 v[200:203], v[100:103], v[236:239], v[200:203]\n\t" \
    "s_waitcnt lgkmcnt(4)\n\t" \
    "v_mfma_f32_16x16x32_bf16 v[216:219], v[104:107], v[240:243], v[216:219]\n\t" \
    "ds_read_b128 v[232:235], v253 offset:9216\n\t" \
    "s_waitcnt lgkmcnt(2)\n\t" \
    "v_mfma_f32_16x16x32_bf16 v[216:219], v[108:111], v[224:227], v[216:219]\n\t" \
    "ds_read_b128 v[236:239], v253 offset:28672\n\t" \
    "v_mfma_f32_16x16x32_bf16 v[220:223], v[112:115], v[240:243], v[220:223]\n\t" \
    "v_mfma_f32_16x16x32_bf16 v[220:223], v[116:119], v[224:227], v[220:223]\n\t" \
    "v_mfma_f32_16x16x32_bf16 v[212:215], v[96:99], v[240:243], v[212:215]\n\t" \
    "v_mfma_f32_16x16x32_bf16 v[212:215], v[100:103], v[224:227], v[212:215]\n\t" \
    "s_waitcnt lgkmcnt(2)\n\t" \
    "v_mfma_f32_16x16x32_bf16 v[204:207], v[0:3], v[228:231], v[204:207]\n\t" \
    "ds_read_b128 v[240:243], v253 offset:29696\n\t" \
    "s_waitcnt lgkmcnt(2)\n\t" \
    "v_mfma_f32_16x16x32_bf16 v[204:207], v[4:7], v[232:235], v[204:207]\n\t" \
    "ds_read_b128 v[224:227], v253 offset:10240\n\t" \
    "v_mfma_f32_16x16x32_bf16 v[208:211], v[8:11], v[228:231], v[208:211]\n\t" \
    "v_mfma_f32_16x16x32_bf16 v[208:211], v[12:15], v[232:235], v[208:211]\n\t" \
    "v_mfma_f32_16x16x32_bf16 v[200:203], v[244:247], v[228:231], v[200:203]\n\t" \
    "v_mfma_f32_16x16x32_bf16 v[200:203], v[248:251], v[232:235], v[200:203]\n\t" \
    "s_waitcnt lgkmcnt(2)\n\t" \
    "v_mfma_f32_16x16x32_bf16 v[216:219], v[0:3], v[236:239], v[216:219]\n\t" \
    "ds_read_b128 v[228:231], v253 offset:11264\n\t" \
    "s_waitcnt lgkmcnt(2)\n\t" \
    "v_mfma_f32_16x16x32_bf16 v[216:219], v[4:7], v[240:243], v[216:219]\n\t" \
    "ds_read_b128 v[232:235], v253 offset:30720\n\t" \
    "v_mfma_f32_16x16x32_bf16 v[220:223], v[8:11], v[236:239], v[220:223]\n\t" \
    "v_mfma_f32_16x16x32_bf16 v[220:223], v[12:15], v[240:243], v[220:223]\n\t" \
    "v_mfma_f32_16x16x32_bf16 v[212:215], v[244:247], v[236:239], v[212:215]\n\t" \
    "ds_read_b128 v[244:247], v254 offset:57344\n\t" \
    "v_mfma_f32_16x16x32_bf16 v[212:215], v[248:251], v[240:243], v[212:215]\n\t" \
    "ds_read_b128 v[248:251], v254 offset:58368\n\t" \
    "s_waitcnt lgkmcnt(4)\n\t" \
    "v_mfma_f32_16x16x32_bf16 v[204:207], v[120:123], v[224:227], v[204:207]\n\t" \
    "ds_read_b128 v[236:239], v253 offset:31744\n\t" \
    "s_and_b32 s44, s40, 3\n\t" \
    "s_waitcnt lgkmcnt(4)\n\t" \
    "v_mfma_f32_16x16x32_bf16 v[204:207], v[124:127], v[228:231], v[204:207]\n\t" \
    "ds_read_b128 v[240:243], v253 offset:12288\n\t" \
    "s_lshl_b32 s44, s44, 17\n\t" \
    "v_mfma_f32_16x16x32_bf16 v[208:211], v[128:131], v[224:227], v[208:211]\n\t" \
    "s_add_u32 s44, s44, %[mb]\n\t" \
    "v_mfma_f32_16x16x32_bf16 v[208:211], v[132:135], v[228:231], v[208:211]\n\t" \
    "s_add_u32 s46, s44, 0x1000\n\t" \
    "s_waitcnt lgkmcnt(3)\n\t" \
    "v_mfma_f32_16x16x32_bf16 v[200:203], v[244:247], v[224:227], v[200:203]\n\t" \
    "s_add_u32 s47, s44, 0x1c00\n\t" \
    "s_waitcnt lgkmcnt(2)\n\t" \
    "v_mfma_f32_16x16x32_bf16 v[200:203], v[248:251], v[228:231], v[200:203]\n\t" \
    "v_mfma_f32_16x16x32_bf16 v[216:219], v[120:123], v[232:235], v[216:219]\n\t" \
    "ds_read_b128 v[224:227], v253 offset:13312\n\t" \
    "s_waitcnt lgkmcnt(2)\n\t" \
    "v_mfma_f32_16x16x32_bf16 v[216:219], v[124:127], v[236:239], v[216:219]\n\t" \
    "ds_read_b128 v[228:231], v253 offset:32768\n\t" \
    "v_mfma_f32_16x16x32_bf16 v[220:223], v[128:131], v[232:235], v[220:223]\n\t" \
    "v_mfma_f32_16x16x32_bf16 v[220:223], v[132:135], v[236:239], v[220:223]\n\t" \
    "v_mfma_f32_16x16x32_bf16 v[212:215], v[244:247], v[232:235], v[212:215]\n\t" \
    "ds_read_b128 v[244:247], v254 offset:59392\n\t" \
    "v_mfma_f32_16x16x32_bf16 v[212:215], v[248:251], v[236:239], v[212:215]\n\t" \
    "ds_read_b128 v[248:251], v254 offset:60416\n\t" \
    "s_waitcnt lgkmcnt(4)\n\t" \
    "v_mfma_f32_16x16x32_bf16 v[204:207], v[144:147], v[240:243], v[204:207]\n\t" \
    "ds_read_b128 v[232:235], v253 offset:33792\n\t" \
    "s_waitcnt lgkmcnt(4)\n\t" \
    "v_mfma_f32_16x16x32_bf16 v[204:207], v[148:151], v[224:227], v[204:207]\n\t" \
    "ds_read_b128 v[236:239], v253 offset:16384\n\t" \
    "v_mfma_f32_16x16x32_bf16 v[208:211], v[152:155], v[240:243], v[208:211]\n\t" \
    "v_mfma_f32_16x16x32_bf16 v[208:211], v[156:159], v[224:227], v[208:211]\n\t" \
    "v_mfma_f32_16x16x32_bf16 v[200:203], v[136:139], v[240:243], v[200:203]\n\t" \
    "v_mfma_f32_16x16x32_bf16 v[200:203], v[140:143], v[224:227], v[200:203]\n\t" \
    "s_waitcnt lgkmcnt(4)\n\t" \
    "v_mfma_f32_16x16x32_bf16 v[216:219], v[144:147], v[228:231], v[216:219]\n\t" \
    "ds_read_b128 v[240:243], v253 offset:17408\n\t" \
    "s_waitcnt lgkmcnt(2)\n\t" \
    "v_mfma_f32_16x16x32_bf16 v[216:219], v[148:151], v[232:235], v[216:219]\n\t" \
    "ds_read_b128 v[224:227], v253 offset:36864\n\t" \
    "v_mfma_f32_16x16x32_bf16 v[220:223], v[152:155], v[228:231], v[220:223]\n\t" \
    "v_mfma_f32_16x16x32_bf16 v[220:223], v[156:159], v[232:235], v[220:223]\n\t" \
    "v_mfma_f32_16x16x32_bf16 v[212:215], v[136:139], v[228:231], v[212:215]\n\t" \
    "v_mfma_f32_16x16x32_bf16 v[212:215], v[140:143], v[232:235], v[212:215]\n\t" \
    "v_and_b32 v252, 0xf0, v253\n\t" \
    "v_lshlrev_b32 v252, 3, v252\n\t" \
    "buffer_load_dword v252, v252, s[60:63], %[fl] offen sc1\n\t" \
    "s_waitcnt lgkmcnt(2)\n\t" \
    "v_mfma_f32_16x16x32_bf16 v[204:207], v[160:163], v[236:239], v[204:207]\n\t" \
    "ds_read_b128 v[228:231], v253 offset:37888\n\t" \
    "s_waitcnt lgkmcnt(2)\n\t" \
    "v_mfma_f32_16x16x32_bf16 v[204:207], v[164:167], v[240:243], v[204:207]\n\t" \
    "ds_read_b128 v[232:235], v253 offset:14336\n\t" \
    "v_mfma_f32_16x16x32_bf16 v[208:211], v[168:171], v[236:239], v[208:211]\n\t" \
    "v_mfma_f32_16x16x32_bf16 v[208:211], v[172:175], v[240:243], v[208:211]\n\t" \
    "v_mfma_f32_16x16x32_bf16 v[200:203], v[244:247], v[236:239], v[200:203]\n\t" \
    "v_mfma_f32_16x16x32_bf16 v[200:203], v[248:251], v[240:243], v[200:203]\n\t" \
    "s_waitcnt lgkmcnt(2)\n\t" \
    "v_mfma_f32_16x16x32_bf16 v[216:219], v[160:163], v[224:227], v[216:219]\n\t" \
    "ds_read_b128 v[236:239], v253 offset:15360\n\t" \
    "s_waitcnt lgkmcnt(2)\n\t" \
    "v_mfma_f32_16x16x32_bf16 v[216:219], v[164:167], v[228:231], v[216:219]\n\t" \
    "ds_read_b128 v[240:243], v253 offset:18432\n\t" \
    "v_mfma_f32_16x16x32_bf16 v[220:223], v[168:171], v[224:227], v[220:223]\n\t" \
    "v_mfma_f32_16x16x32_bf16 v[220:223], v[172:175], v[228:231], v[220:223]\n\t" \
    "v_mfma_f32_16x16x32_bf16 v[212:215], v[244:247], v[224:227], v[212:215]\n\t" \
    "v_mfma_f32_16x16x32_bf16 v[212:215], v[248:251], v[228:231], v[212:215]\n\t" \
    "s_waitcnt lgkmcnt(2)\n\t" \
    "v_mfma_f32_16x16x32_bf16 v[204:207], v[24:27], v[232:235], v[204:207]\n\t" \
    "ds_read_b128 v[224:227], v253 offset:19456\n\t" \
    "v_mov_b64 v[244:245], 0\n\t" \
    "s_waitcnt lgkmcnt(2)\n\t" \
    "v_mfma_f32_16x16x32_bf16 v[204:207], v[28:31], v[236:239], v[204:207]\n\t" \
    "ds_read_b128 v[228:231], v253 offset:34816\n\t" \
    "v_mov_b64 v[246:247], 0\n\t" \
    "v_mfma_f32_16x16x32_bf16 v[208:211], v[32:35], v[232:235], v[208:211]\n\t" \
    "v_mov_b64 v[248:249], 0\n\t" \
    "v_mfma_f32_16x16x32_bf16 v[208:211], v[36:39], v[236:239], v[208:211]\n\t" \
    "v_mov_b64 v[250:251], 0\n\t" \
    "v_mfma_f32_16x16x32_bf16 v[200:203], v[16:19], v[232:235], v[200:203]\n\t" \
    "v_mfma_f32_16x16x32_bf16 v[200:203], v[20:23], v[236:239], v[200:203]\n\t" \
    "s_waitcnt lgkmcnt(2)\n\t" \
    "v_mfma_f32_16x16x32_bf16 v[204:207], v[184:187], v[240:243], v[204:207]\n\t" \
    "ds_read_b128 v[232:235], v253 offset:35840\n\t" \
    "s_waitcnt lgkmcnt(2)\n\t" \
    "v_mfma_f32_16x16x32_bf16 v[204:207], v[188:191], v[224:227], v[204:207]\n\t" \
    "ds_read_b128 v[236:239], v253 offset:38912\n\t" \
    "v_mfma_f32_16x16x32_bf16 v[208:211], v[192:195], v[240:243], v[208:211]\n\t" \
    "v_mfma_f32_16x16x32_bf16 v[208:211], v[196:199], v[224:227], v[208:211]\n\t" \
    "v_mfma_f32_16x16x32_bf16 v[200:203], v[176:179], v[240:243], v[200:203]\n\t" \
    "v_mfma_f32_16x16x32_bf16 v[200:203], v[180:183], v[224:227], v[200:203]\n\t" \
    "ds_read_b128 v[240:243], v253 offset:39936\n\t" \
    "s_waitcnt lgkmcnt(3)\n\t" \
    "v_mfma_f32_16x16x32_bf16 v[216:219], v[24:27], v[228:231], v[216:219]\n\t" \
    "s_waitcnt lgkmcnt(2)\n\t" \
    "v_mfma_f32_16x16x32_bf16 v[216:219], v[28:31], v[232:235], v[216:219]\n\t" \
    "v_cvt_pk_bf16_f32 v204, v204, v205\n\t" \
    "v_cvt_pk_bf16_f32 v205, v206, v207\n\t" \
    "s_waitcnt lgkmcnt(1)\n\t" \
    "v_mfma_f32_16x16x32_bf16 v[216:219], v[184:187], v[236:239], v[216:219]\n\t" \
    "ds_write_b64 v254, v[204:205] offset:7176\n\t" \
    "v_cvt_pk_bf16_f32 v208, v208, v209\n\t" \
    "s_waitcnt lgkmcnt(1)\n\t" \
    "v_mfma_f32_16x16x32_bf16 v[216:219], v[188:191], v[240:243], v[216:219]\n\t" \
    "v_cvt_pk_bf16_f32 v209, v210, v211\n\t" \
    "ds_write_b64 v254, v[208:209] offset:8200\n\t" \
    "v_mfma_f32_16x16x32_bf16 v[220:223], v[32:35], v[228:231], v[220:223]\n\t" \
    "v_cvt_pk_bf16_f32 v200, v200, v201\n\t" \
    "v_cvt_pk_bf16_f32 v201, v202, v203\n\t" \
    "v_mfma_f32_16x16x32_bf16 v[220:223], v[36:39], v[232:235], v[220:223]\n\t" \
    "ds_write_b64 v254, v[200:201] offset:6152\n\t" \
    "ds_read_b128 v[200:203], v254 offset:51200\n\t" \
    "v_mfma_f32_16x16x32_bf16 v[220:223], v[192:195], v[236:239], v[220:223]\n\t" \
    "ds_read_b128 v[204:207], v254 offset:52224\n\t" \
    "v_mov_b64 v[208:209], 0\n\t" \
    "v_mfma_f32_16x16x32_bf16 v[220:223], v[196:199], v[240:243], v[220:223]\n\t" \
    "v_mov_b64 v[210:211], 0\n\t" \
    "v_cvt_pk_bf16_f32 v216, v216, v217\n\t" \
    "v_mfma_f32_16x16x32_bf16 v[212:215], v[16:19], v[228:231], v[212:215]\n\t" \
    "v_cvt_pk_bf16_f32 v217, v218, v219\n\t" \
    "ds_write_b64 v254, v[216:217] offset:13320\n\t" \
    "v_mfma_f32_16x16x32_bf16 v[212:215], v[20:23], v[232:235], v[212:215]\n\t" \
    "v_mfma_f32_16x16x32_bf16 v[212:215], v[176:179], v[236:239], v[212:215]\n\t" \
    "v_cvt_pk_bf16_f32 v220, v220, v221\n\t" \
    "v_cvt_pk_bf16_f32 v221, v222, v223\n\t" \
    "v_mfma_f32_16x16x32_bf16 v[212:215], v[180:183], v[240:243], v[212:215]\n\t" \
    "ds_write_b64 v254, v[220:221] offset:14344\n\t" \
    "ds_read_b128 v[216:219], v254 offset:53248\n\t" \
    "ds_read_b128 v[220:223], v254 offset:54272\n\t" \
    "s_nop 4\n\t" \
    "v_cvt_pk_bf16_f32 v212, v212, v213\n\t" \
    "v_cvt_pk_bf16_f32 v213, v214, v215\n\t" \
    "ds_write_b64 v254, v[212:213] offset:12296\n\t" \
    LST6 \
    "s_waitcnt vmcnt(0) lgkmcnt(0)\n\t" \
    "s_barrier \n\t" \
    "s_setprio 2\n\t" \
    "s_add_u32 s44, s40, 1\n\t" \
    "s_min_u32 s44, s44, 63\n\t" \
    ".Lv28_%=:\n\t" \
    "v_cmp_ge_u32 vcc, s44, v252\n\t" \
    "s_cbranch_vccz .Lv29_%=\n\t" \
    "s_cmp_eq_u32 s41, 0\n\t" \
    "s_cbranch_scc1 .Lv29_%=\n\t" \
    "s_sub_u32 s41, s41, 1\n\t" \
    "s_sleep 2\n\t" \
    "v_and_b32 v252, 0xf0, v253\n\t" \
    "v_lshlrev_b32 v252, 3, v252\n\t" \
    "buffer_load_dword v252, v252, s[60:63], %[fl] offen sc1\n\t" \
    "s_waitcnt vmcnt(0)\n\t" \
    "s_branch .Lv28_%=\n\t" \
    ".Lv29_%=:\n\t" \
    "s_movk_i32 s45, 0x2000\n\t" \
    "s_mov_b32 m0, s45\n\t" \
    "s_nop 0\n\t" \
    "buffer_load_dwordx4 v253, s[60:63], s42 offen offset:0 sc1 lds\n\t" \
    "buffer_load_dwordx4 v253, s[60:63], s42 offen offset:1024 sc1 lds\n\t" \
    "buffer_load_dwordx4 v253, s[60:63], s42 offen offset:2048 sc1 lds\n\t" \
    "buffer_load_dwordx4 v253, s[60:63], s42 offen offset:3072 sc1 lds\n\t" \
    "s_add_u32 s44, s42, 0x1000\n\t" \
    "s_movk_i32 s45, 0x7000\n\t" \
    "s_mov_b32 m0, s45\n\t" \
    "s_nop 0\n\t" \
    "buffer_load_dwordx4 v253, s[60:63], s44 offen offset:0 sc1 lds\n\t" \
    "buffer_load_dwordx4 v253, s[60:63], s44 offen offset:1024 sc1 lds\n\t" \
    "buffer_load_dwordx4 v253, s[60:63], s44 offen offset:2048 sc1 lds\n\t" \
    "buffer_load_dwordx4 v253, s[60:63], s44 offen offset:3072 sc1 lds\n\t" \
    "s_add_u32 s44, s42, 0x8800\n\t" \
    "s_mov_b32 s45, 0x4800\n\t" \
    "s_mov_b32 m0, s45\n\t" \
    "s_nop 0\n\t" \
    "buffer_load_dwordx4 v253, s[60:63], s44 offen offset:0 sc1 lds\n\t" \
    "s_add_u32 s44, s44, 0x1000\n\t" \
    "s_mov_b32 s45, 0x9800\n\t" \
    "s_mov_b32 m0, s45\n\t" \
    "s_nop 0\n\t" \
    "buffer_load_dwordx4 v253, s[60:63], s44 offen offset:0 sc1 lds\n\t" \
    LST1 \
    "s_sleep 18\n\t" \
    "buffer_load_dwordx4 v[224:227], v253, s[60:63], s46 offen sc1\n\t" \
    "buffer_load_dwordx4 v[228:231], v253, s[60:63], s47 offen sc1\n\t" \
    "s_waitcnt lgkmcnt(0)\n\t" \
    LFLUSH212 \
    "v_bfe_i32 v212, v253, 5, 1\n\t" \
    "v_and_b32 v212, 0xaaaa, v212\n\t" \
    "v_or_b32 v212, 0x4444, v212\n\t" \
    "v_and_b32 v213, 0xffff, v255\n\t" \
    "v_lshrrev_b32 v214, 16, v255\n\t" \
    ".Lv210_%=:\n\t" \
    "s_waitcnt vmcnt(1)\n\t" \
    "v_max_u32 v215, v224, v225\n\t" \
    "v_max3_u32 v215, v226, v227, v215\n\t" \
    "s_waitcnt vmcnt(0)\n\t" \
    "v_max3_u32 v215, v228, v229, v215\n\t" \
    "v_max3_u32 v215, v230, v231, v215\n\t" \
    "v_cmp_eq_u32 vcc, -1, v215\n\t" \
    "s_cbranch_vccz .Lv211_%=\n\t" \
    "v_max_u32 v215, v224, v225\n\t" \
    "v_max3_u32 v215, v226, v227, v215\n\t" \
    "v_cmp_eq_u32 vcc, -1, v215\n\t" \
    "s_cbranch_vccz .Lv212_%=\n\t" \
    "buffer_load_dwordx4 v[224:227], v253, s[60:63], s46 offen sc1\n\t" \
    ".Lv212_%=:\n\t" \
    "v_max_u32 v215, v228, v229\n\t" \
    "v_max3_u32 v215, v230, v231, v215\n\t" \
    "v_cmp_eq_u32 vcc, -1, v215\n\t" \
    "s_cbranch_vccz .Lv213_%=\n\t" \
    "buffer_load_dwordx4 v[228:231], v253, s[60:63], s47 offen sc1\n\t" \
    ".Lv213_%=:\n\t" \
    "s_cmp_eq_u32 s41, 0\n\t" \
    "s_cbranch_scc1 .Lv211_%=\n\t" \
    "s_sub_u32 s41, s41, 1\n\t" \
    "s_waitcnt vmcnt(0)\n\t" \
    "s_branch .Lv210_%=\n\t" \
    ".Lv211_%=:\n\t" \
    LST2 \
    "ds_write_b128 v253, v[224:227] offset:45056\n\t" \
    "ds_write_b128 v253, v[228:231] offset:48128\n\t" \
    "v_smfmac_f32_16x16x64_bf16 v[244:247], v[224:227], v[0:7], v212\n\t" \
    "v_smfmac_f32_16x16x64_bf16 v[248:251], v[224:227], v[8:15], v212\n\t" \
    "v_smfmac_f32_16x16x64_bf16 v[208:211], v[224:227], v[200:207], v212\n\t" \
    "s_waitcnt lgkmcnt(0)\n\t" \
    "s_barrier \n\t" \
    LST3 \
    "ds_read_b128 v[232:235], v253 offset:40960\n\t" \
    "ds_read_b128 v[236:239], v253 offset:41984\n\t" \
    "ds_read_b128 v[240:243], v253 offset:43008\n\t" \
    "ds_read_b128 v[224:227], v253 offset:44032\n\t" \
    "ds_read_b128 v[200:203], v254 offset:55296\n\t" \
    "ds_read_b128 v[204:207], v254 offset:56320\n\t" \
    "v_smfmac_f32_16x16x64_bf16 v[208:211], v[228:231], v[16:23], v212\n\t" \
    "v_smfmac_f32_16x16x64_bf16 v[244:247], v[228:231], v[24:31], v212\n\t" \
    "v_smfmac_f32_16x16x64_bf16 v[248:251], v[228:231], v[32:39], v212\n\t" \
    "ds_read_b128 v[228:231], v253 offset:46080\n\t" \
    "s_waitcnt lgkmcnt(6)\n\t" \
    "v_smfmac_f32_16x16x64_bf16 v[208:211], v[232:235], v[216:223], v212\n\t" \
    "ds_read_b128 v[216:219], v254 offset:57344\n\t" \
    "ds_read_b128 v[220:223], v254 offset:58368\n\t" \
    "v_smfmac_f32_16x16x64_bf16 v[244:247], v[232:235], v[40:47], v212\n\t" \
    "v_smfmac_f32_16x16x64_bf16 v[248:251], v[232:235], v[48:55], v212\n\t" \
    "ds_read_b128 v[232:235], v253 offset:47104\n\t" \
    "s_waitcnt lgkmcnt(8)\n\t" \
    "v_smfmac_f32_16x16x64_bf16 v[208:211], v[236:239], v[56:63], v212\n\t" \
    "v_smfmac_f32_16x16x64_bf16 v[244:247], v[236:239], v[64:71], v212\n\t" \
    "v_smfmac_f32_16x16x64_bf16 v[248:251], v[236:239], v[72:79], v212\n\t" \
    "ds_read_b128 v[236:239], v253 offset:49152\n\t" \
    "s_waitcnt lgkmcnt(5)\n\t" \
    "v_smfmac_f32_16x16x64_bf16 v[208:211], v[240:243], v[200:207], v212\n\t" \
    "ds_read_b128 v[200:203], v254 offset:59392\n\t" \
    "ds_read_b128 v[204:207], v254 offset:60416\n\t" \
    "v_smfmac_f32_16x16x64_bf16 v[244:247], v[240:243], v[80:87], v212\n\t" \
    "v_smfmac_f32_16x16x64_bf16 v[248:251], v[240:243], v[88:95], v212\n\t" \
    "ds_read_b128 v[240:243], v253 offset:50176\n\t" \
    "v_smfmac_f32_16x16x64_bf16 v[208:211], v[224:227], v[96:103], v212\n\t" \
    "v_smfmac_f32_16x16x64_bf16 v[244:247], v[224:227], v[104:111], v212\n\t" \
    "v_smfmac_f32_16x16x64_bf16 v[248:251], v[224:227], v[112:119], v212\n\t" \
    "s_waitcnt lgkmcnt(7)\n\t" \
    "v_smfmac_f32_16x16x64_bf16 v[244:247], v[228:231], v[120:127], v212\n\t" \
    "s_waitcnt lgkmcnt(4)\n\t" \
    "v_smfmac_f32_16x16x64_bf16 v[244:247], v[232:235], v[144:151], v212\n\t" \
    "s_waitcnt lgkmcnt(3)\n\t" \
    "v_smfmac_f32_16x16x64_bf16 v[244:247], v[236:239], v[160:167], v212\n\t" \
    "s_waitcnt lgkmcnt(0)\n\t" \
    "v_smfmac_f32_16x16x64_bf16 v[244:247], v[240:243], v[184:191], v212\n\t" \
    "v_smfmac_f32_16x16x64_bf16 v[248:251], v[228:231], v[128:135], v212\n\t" \
    "v_smfmac_f32_16x16x64_bf16 v[248:251], v[232:235], v[152:159], v212\n\t" \
    "v_smfmac_f32_16x16x64_bf16 v[248:251], v[236:239], v[168:175], v212\n\t" \
    "v_add_f32 v244, v244, v246\n\t" \
    "v_add_f32 v245, v245, v247\n\t" \
    "v_cvt_pk_bf16_f32 v244, v244, v245\n\t" \
    "v_smfmac_f32_16x16x64_bf16 v[248:251], v[240:243], v[192:199], v212\n\t" \
    "ds_write_b16 v213, v244 offset:512\n\t" \
    "ds_write_b16_d16_hi v213, v244 offset:528\n\t" \
    "v_smfmac_f32_16x16x64_bf16 v[208:211], v[228:231], v[216:223], v212\n\t" \
    "v_smfmac_f32_16x16x64_bf16 v[208:211], v[232:235], v[136:143], v212\n\t" \
    "v_smfmac_f32_16x16x64_bf16 v[208:211], v[236:239], v[200:207], v212\n\t" \
    "v_add_f32 v248, v248, v250\n\t" \
    "v_add_f32 v249, v249, v251\n\t" \
    "v_cvt_pk_bf16_f32 v248, v248, v249\n\t" \
    "v_smfmac_f32_16x16x64_bf16 v[208:211], v[240:243], v[176:183], v212\n\t" \
    "ds_write_b16 v213, v248 offset:1024\n\t" \
    "ds_write_b16_d16_hi v213, v248 offset:1040\n\t" \
    "s_nop 5\n\t" \
    "v_add_f32 v208, v208, v210\n\t" \
    "v_add_f32 v209, v209, v211\n\t" \
    "v_cvt_pk_bf16_f32 v208, v208, v209\n\t" \
    "ds_write_b16 v213, v208 offset:0\n\t" \
    "ds_write_b16_d16_hi v213, v208 offset:16\n\t" \
    "s_setprio 0\n\t" \
    "s_waitcnt lgkmcnt(0)\n\t" \
    "s_barrier \n\t" \
    LST4 \
    "ds_read_b128 v[224:227], v253 offset:0\n\t" \
    "ds_read_b128 v[244:247], v254 offset:53248\n\t" \
    "ds_read_b128 v[228:231], v253 offset:1024\n\t" \
    "ds_read_b128 v[232:235], v253 offset:20480\n\t" \
    "ds_read_b128 v[236:239], v253 offset:21504\n\t" \
    "ds_read_b128 v[248:251], v254 offset:54272\n\t" \
    "s_add_u32 s40, s40, 1\n\t" \
    "s_cmp_lg_u32 s40, 64\n\t" \
    "s_cbranch_scc1 .Lv27_%=\n\t" \
    "s_branch .Lend%=\n\t" \
    ".Lv1_%=:\n\t" \
    "s_mov_b32 s42, %[xs]\n\t" \
    "s_add_u32 m0, %[wm], 0xc000\n\t" \
    "s_mov_b32 s44, 0x0\n\t" \
    "buffer_load_dwordx4 v240, %[wr], s44 offen offset:0 nt lds\n\t" \
    "buffer_load_dwordx4 v241, %[wr], s44 offen offset:1024 nt lds\n\t" \
    "buffer_load_dwordx4 v242, %[wr], s44 offen offset:2048 nt lds\n\t" \
    "s_add_u32 m0, %[wm], 0xf000\n\t" \
    "s_mov_b32 s44, 0x60000\n\t" \
    "buffer_load_dwordx4 v240, %[wr], s44 offen offset:0 nt lds\n\t" \
    "buffer_load_dwordx4 v241, %[wr], s44 offen offset:1024 nt lds\n\t" \
    "buffer_load_dwordx4 v242, %[wr], s44 offen offset:2048 nt lds\n\t" \
    "s_add_u32 m0, %[wm], 0x0\n\t" \
    "s_mov_b32 s44, 0xc0000\n\t" \
    "buffer_load_dwordx4 v240, %[wr], s44 offen offset:0 nt lds\n\t" \
    "buffer_load_dwordx4 v241, %[wr], s44 offen offset:1024 nt lds\n\t" \
    "buffer_load_dwordx4 v242, %[wr], s44 offen offset:2048 nt lds\n\t" \
    "s_add_u32 m0, %[wm], 0x3000\n\t" \
    "s_mov_b32 s44, 0x120000\n\t" \
    "buffer_load_dwordx4 v240, %[wr], s44 offen offset:0 nt lds\n\t" \
    "buffer_load_dwordx4 v241, %[wr], s44 offen offset:1024 nt lds\n\t" \
    "buffer_load_dwordx4 v242, %[wr], s44 offen offset:2048 nt lds\n\t" \
    "s_add_u32 m0, %[wm], 0x6000\n\t" \
    "s_mov_b32 s44, 0x180000\n\t" \
    "buffer_load_dwordx4 v240, %[wr], s44 offen offset:0 nt lds\n\t" \
    "buffer_load_dwordx4 v241, %[wr], s44 offen offset:1024 nt lds\n\t" \
    "buffer_load_dwordx4 v242, %[wr], s44 offen offset:2048 nt lds\n\t" \
    "v_add_u32 v244, 0xc000, v243\n\t" \
    "s_waitcnt vmcnt(12) lgkmcnt(0)\n\t" \
    "s_barrier \n\t" \
    "s_add_u32 m0, %[wm], 0x9000\n\t" \
    "s_mov_b32 s44, 0x1e0000\n\t" \
    "buffer_load_dwordx4 v240, %[wr], s44 offen offset:0 nt lds\n\t" \
    "buffer_load_dwordx4 v241, %[wr], s44 offen offset:1024 nt lds\n\t" \
    "buffer_load_dwordx4 v242, %[wr], s44 offen offset:2048 nt lds\n\t" \
    "ds_read_b64_tr_b16 v[56:57], v244 offset:0\n\t" \
    "ds_read_b64_tr_b16 v[58:59], v244 offset:1536\n\t" \
    "ds_read_b64_tr_b16 v[64:65], v244 offset:32\n\t" \
    "ds_read_b64_tr_b16 v[66:67], v244 offset:1568\n\t" \
    "ds_read_b64_tr_b16 v[72:73], v244 offset:64\n\t" \
    "ds_read_b64_tr_b16 v[74:75], v244 offset:1600\n\t" \
    "v_add_u32 v244, 0xf000, v243\n\t" \
    "s_waitcnt vmcnt(12) lgkmcnt(0)\n\t" \
    "s_barrier \n\t" \
    "s_add_u32 m0, %[wm], 0xc000\n\t" \
    "s_mov_b32 s44, 0x240000\n\t" \
    "buffer_load_dwordx4 v240, %[wr], s44 offen offset:0 nt lds\n\t" \
    "buffer_load_dwordx4 v241, %[wr], s44 offen offset:1024 nt lds\n\t" \
    "buffer_load_dwordx4 v242, %[wr], s44 offen offset:2048 nt lds\n\t" \
    "ds_read_b64_tr_b16 v[60:61], v244 offset:0\n\t" \
    "ds_read_b64_tr_b16 v[62:63], v244 offset:1536\n\t" \
    "ds_read_b64_tr_b16 v[68:69], v244 offset:32\n\t" \
    "ds_read_b64_tr_b16 v[70:71], v244 offset:1568\n\t" \
    "ds_read_b64_tr_b16 v[76:77], v244 offset:64\n\t" \
    "ds_read_b64_tr_b16 v[78:79], v244 offset:1600\n\t" \
    "v_add_u32 v244, 0x0, v243\n\t" \
    "s_waitcnt vmcnt(12) lgkmcnt(0)\n\t" \
    "s_barrier \n\t" \
    "s_add_u32 m0, %[wm], 0xf000\n\t" \
    "s_mov_b32 s44, 0x2a0000\n\t" \
    "buffer_load_dwordx4 v240, %[wr], s44 offen offset:0 nt lds\n\t" \
    "buffer_load_dwordx4 v241, %[wr], s44 offen offset:1024 nt lds\n\t" \
    "buffer_load_dwordx4 v242, %[wr], s44 offen offset:2048 nt lds\n\t" \
    "ds_read_b64_tr_b16 v[96:97], v244 offset:0\n\t" \
    "ds_read_b64_tr_b16 v[98:99], v244 offset:1536\n\t" \
    "ds_read_b64_tr_b16 v[104:105], v244 offset:32\n\t" \
    "ds_read_b64_tr_b16 v[106:107], v244 offset:1568\n\t" \
    "ds_read_b64_tr_b16 v[112:113], v244 offset:64\n\t" \
    "ds_read_b64_tr_b16 v[114:115], v244 offset:1600\n\t" \
    "v_add_u32 v244, 0x3000, v243\n\t" \
    "s_waitcnt vmcnt(12) lgkmcnt(0)\n\t" \
    "s_barrier \n\t" \
    "s_add_u32 m0, %[wm], 0x0\n\t" \
    "s_mov_b32 s44, 0x300000\n\t" \
    "buffer_load_dwordx4 v240, %[wr], s44 offen offset:0 nt lds\n\t" \
    "buffer_load_dwordx4 v241, %[wr], s44 offen offset:1024 nt lds\n\t" \
    "buffer_load_dwordx4 v242, %[wr], s44 offen offset:2048 nt lds\n\t" \
    "ds_read_b64_tr_b16 v[100:101], v244 offset:0\n\t" \
    "ds_read_b64_tr_b16 v[102:103], v244 offset:1536\n\t" \
    "ds_read_b64_tr_b16 v[108:109], v244 offset:32\n\t" \
    "ds_read_b64_tr_b16 v[110:111], v244 offset:1568\n\t" \
    "ds_read_b64_tr_b16 v[116:117], v244 offset:64\n\t" \
    "ds_read_b64_tr_b16 v[118:119], v244 offset:1600\n\t" \
    "v_add_u32 v244, 0x6000, v243\n\t" \
    "s_waitcnt vmcnt(12) lgkmcnt(0)\n\t" \
    "s_barrier \n\t" \
    "s_add_u32 m0, %[wm], 0x3000\n\t" \
    "s_mov_b32 s44, 0x360000\n\t" \
    "buffer_load_dwordx4 v240, %[wr], s44 offen offset:0 nt lds\n\t" \
    "buffer_load_dwordx4 v241, %[wr], s44 offen offset:1024 nt lds\n\t" \
    "buffer_load_dwordx4 v242, %[wr], s44 offen offset:2048 nt lds\n\t" \
    "ds_read_b64_tr_b16 v[200:201], v244 offset:0\n\t" \
    "ds_read_b64_tr_b16 v[202:203], v244 offset:1536\n\t" \
    "ds_read_b64_tr_b16 v[0:1], v244 offset:32\n\t" \
    "ds_read_b64_tr_b16 v[2:3], v244 offset:1568\n\t" \
    "ds_read_b64_tr_b16 v[8:9], v244 offset:64\n\t" \
    "ds_read_b64_tr_b16 v[10:11], v244 offset:1600\n\t" \
    "v_add_u32 v244, 0x9000, v243\n\t" \
    "s_waitcnt vmcnt(12) lgkmcnt(0)\n\t" \
    "s_barrier \n\t" \
    "s_add_u32 m0, %[wm], 0x6000\n\t" \
    "s_mov_b32 s44, 0x3c0000\n\t" \
    "buffer_load_dwordx4 v240, %[wr], s44 offen offset:0 nt lds\n\t" \
    "buffer_load_dwordx4 v241, %[wr], s44 offen offset:1024 nt lds\n\t" \
    "buffer_load_dwordx4 v242, %[wr], s44 offen offset:2048 nt lds\n\t" \
    "ds_read_b64_tr_b16 v[204:205], v244 offset:0\n\t" \
    "ds_read_b64_tr_b16 v[206:207], v244 offset:1536\n\t" \
    "ds_read_b64_tr_b16 v[4:5], v244 offset:32\n\t" \
    "ds_read_b64_tr_b16 v[6:7], v244 offset:1568\n\t" \
    "ds_read_b64_tr_b16 v[12:13], v244 offset:64\n\t" \
    "ds_read_b64_tr_b16 v[14:15], v244 offset:1600\n\t" \
    "v_add_u32 v244, 0xc000, v243\n\t" \
    "s_waitcnt vmcnt(12) lgkmcnt(0)\n\t" \
    "s_barrier \n\t" \
    "v_mov_b32 v246, 0\n\t" \
    "buffer_load_dword v246, v246, s[60:63], %[mo] offen sc1\n\t" \
    "s_add_u32 m0, %[wm], 0x9000\n\t" \
    "s_mov_b32 s44, 0x420000\n\t" \
    "buffer_load_dwordx4 v240, %[wr], s44 offen offset:0 nt lds\n\t" \
    "buffer_load_dwordx4 v241, %[wr], s44 offen offset:1024 nt lds\n\t" \
    "buffer_load_dwordx4 v242, %[wr], s44 offen offset:2048 nt lds\n\t" \
    "ds_read_b64_tr_b16 v[208:209], v244 offset:0\n\t" \
    "ds_read_b64_tr_b16 v[210:211], v244 offset:1536\n\t" \
    "ds_read_b64_tr_b16 v[40:41], v244 offset:32\n\t" \
    "ds_read_b64_tr_b16 v[42:43], v244 offset:1568\n\t" \
    "ds_read_b64_tr_b16 v[48:49], v244 offset:64\n\t" \
    "ds_read_b64_tr_b16 v[50:51], v244 offset:1600\n\t" \
    "v_add_u32 v244, 0xf000, v243\n\t" \
    "s_waitcnt vmcnt(12) lgkmcnt(0)\n\t" \
    "s_barrier \n\t" \
    "s_add_u32 m0, %[wm], 0xc000\n\t" \
    "s_mov_b32 s44, 0x480000\n\t" \
    "buffer_load_dwordx4 v240, %[wr], s44 offen offset:0 nt lds\n\t" \
    "buffer_load_dwordx4 v241, %[wr], s44 offen offset:1024 nt lds\n\t" \
    "buffer_load_dwordx4 v242, %[wr], s44 offen offset:2048 nt lds\n\t" \
    "ds_read_b64_tr_b16 v[212:213], v244 offset:0\n\t" \
    "ds_read_b64_tr_b16 v[214:215], v244 offset:1536\n\t" \
    "ds_read_b64_tr_b16 v[44:45], v244 offset:32\n\t" \
    "ds_read_b64_tr_b16 v[46:47], v244 offset:1568\n\t" \
    "ds_read_b64_tr_b16 v[52:53], v244 offset:64\n\t" \
    "ds_read_b64_tr_b16 v[54:55], v244 offset:1600\n\t" \
    "v_add_u32 v244, 0x0, v243\n\t" \
    "s_waitcnt vmcnt(12) lgkmcnt(0)\n\t" \
    "s_barrier \n\t" \
    "s_add_u32 m0, %[wm], 0xf000\n\t" \
    "s_mov_b32 s44, 0x4e0000\n\t" \
    "buffer_load_dwordx4 v240, %[wr], s44 offen offset:0 nt lds\n\t" \
    "buffer_load_dwordx4 v241, %[wr], s44 offen offset:1024 nt lds\n\t" \
    "buffer_load_dwordx4 v242, %[wr], s44 offen offset:2048 nt lds\n\t" \
    "ds_read_b64_tr_b16 v[136:137], v244 offset:0\n\t" \
    "ds_read_b64_tr_b16 v[138:139], v244 offset:1536\n\t" \
    "ds_read_b64_tr_b16 v[144:145], v244 offset:32\n\t" \
    "ds_read_b64_tr_b16 v[146:147], v244 offset:1568\n\t" \
    "ds_read_b64_tr_b16 v[152:153], v244 offset:64\n\t" \
    "ds_read_b64_tr_b16 v[154:155], v244 offset:1600\n\t" \
    "v_add_u32 v244, 0x3000, v243\n\t" \
    "s_waitcnt vmcnt(12) lgkmcnt(0)\n\t" \
    "s_barrier \n\t" \
    "s_add_u32 m0, %[wm], 0x0\n\t" \
    "s_mov_b32 s44, 0x540000\n\t" \
    "buffer_load_dwordx4 v240, %[wr], s44 offen offset:0 nt lds\n\t" \
    "buffer_load_dwordx4 v241, %[wr], s44 offen offset:1024 nt lds\n\t" \
    "buffer_load_dwordx4 v242, %[wr], s44 offen offset:2048 nt lds\n\t" \
    "ds_read_b64_tr_b16 v[140:141], v244 offset:0\n\t" \
    "ds_read_b64_tr_b16 v[142:143], v244 offset:1536\n\t" \
    "ds_read_b64_tr_b16 v[148:149], v244 offset:32\n\t" \
    "ds_read_b64_tr_b16 v[150:151], v244 offset:1568\n\t" \
    "ds_read_b64_tr_b16 v[156:157], v244 offset:64\n\t" \
    "ds_read_b64_tr_b16 v[158:159], v244 offset:1600\n\t" \
    "v_add_u32 v244, 0x6000, v243\n\t" \
    "s_waitcnt vmcnt(12) lgkmcnt(0)\n\t" \
    "s_barrier \n\t" \
    "s_add_u32 m0, %[wm], 0x3000\n\t" \
    "s_mov_b32 s44, 0x5a0000\n\t" \
    "buffer_load_dwordx4 v240, %[wr], s44 offen offset:0 nt lds\n\t" \
    "buffer_load_dwordx4 v241, %[wr], s44 offen offset:1024 nt lds\n\t" \
    "buffer_load_dwordx4 v242, %[wr], s44 offen offset:2048 nt lds\n\t" \
    "ds_read_b64_tr_b16 v[216:217], v244 offset:0\n\t" \
    "ds_read_b64_tr_b16 v[218:219], v244 offset:1536\n\t" \
    "ds_read_b64_tr_b16 v[80:81], v244 offset:32\n\t" \
    "ds_read_b64_tr_b16 v[82:83], v244 offset:1568\n\t" \
    "ds_read_b64_tr_b16 v[88:89], v244 offset:64\n\t" \
    "ds_read_b64_tr_b16 v[90:91], v244 offset:1600\n\t" \
    "v_add_u32 v244, 0x9000, v243\n\t" \
    "s_waitcnt vmcnt(12) lgkmcnt(0)\n\t" \
    "s_barrier \n\t" \
    ".Lv11_%=:\n\t" \
    "v_readfirstlane_b32 s43, v246\n\t" \
    "s_nop 0\n\t" \
    "s_cmp_lg_u32 s43, -1\n\t" \
    "s_cbranch_scc1 .Lv12_%=\n\t" \
    "s_cmp_eq_u32 s41, 0\n\t" \
    "s_cbranch_scc1 .Lv12_%=\n\t" \
    "s_sub_u32 s41, s41, 1\n\t" \
    "s_sleep 4\n\t" \
    "v_mov_b32 v246, 0\n\t" \
    "buffer_load_dword v246, v246, s[60:63], %[mo] offen sc1\n\t" \
    "s_waitcnt vmcnt(0)\n\t" \
    "s_branch .Lv11_%=\n\t" \
    ".Lv12_%=:\n\t" \
    "v_mov_b32 v246, 0x8000\n\t" \
    "s_mov_b64 exec, 1\n\t" \
    "buffer_store_dword v246, v246, s[60:63], %[mo] offen sc1\n\t" \
    "s_mov_b64 exec, -1\n\t" \
    "s_and_b32 s43, s43, 0x2000\n\t" \
    "s_xor_b32 s43, s43, %[lx]\n\t" \
    "s_add_u32 s60, s60, s43\n\t" \
    "s_addc_u32 s61, s61, 0\n\t" \
    "v_and_b32 v252, 0xf0, v253\n\t" \
    "v_lshlrev_b32 v252, 3, v252\n\t" \
    "buffer_load_dword v252, v252, s[60:63], %[fl] offen sc1\n\t" \
    "s_add_u32 m0, %[wm], 0x6000\n\t" \
    "s_mov_b32 s44, 0x600000\n\t" \
    "buffer_load_dwordx4 v240, %[wr], s44 offen offset:0 nt lds\n\t" \
    "buffer_load_dwordx4 v241, %[wr], s44 offen offset:1024 nt lds\n\t" \
    "buffer_load_dwordx4 v242, %[wr], s44 offen offset:2048 nt lds\n\t" \
    "ds_read_b64_tr_b16 v[220:221], v244 offset:0\n\t" \
    "ds_read_b64_tr_b16 v[222:223], v244 offset:1536\n\t" \
    "ds_read_b64_tr_b16 v[84:85], v244 offset:32\n\t" \
    "ds_read_b64_tr_b16 v[86:87], v244 offset:1568\n\t" \
    "ds_read_b64_tr_b16 v[92:93], v244 offset:64\n\t" \
    "ds_read_b64_tr_b16 v[94:95], v244 offset:1600\n\t" \
    "v_add_u32 v244, 0xc000, v243\n\t" \
    "s_waitcnt vmcnt(12) lgkmcnt(0)\n\t" \
    "s_barrier \n\t" \
    "s_add_u32 m0, %[wm], 0x9000\n\t" \
    "s_mov_b32 s44, 0x660000\n\t" \
    "buffer_load_dwordx4 v240, %[wr], s44 offen offset:0 nt lds\n\t" \
    "buffer_load_dwordx4 v241, %[wr], s44 offen offset:1024 nt lds\n\t" \
    "buffer_load_dwordx4 v242, %[wr], s44 offen offset:2048 nt lds\n\t" \
    "ds_read_b64_tr_b16 v[16:17], v244 offset:0\n\t" \
    "ds_read_b64_tr_b16 v[18:19], v244 offset:1536\n\t" \
    "ds_read_b64_tr_b16 v[24:25], v244 offset:32\n\t" \
    "ds_read_b64_tr_b16 v[26:27], v244 offset:1568\n\t" \
    "ds_read_b64_tr_b16 v[32:33], v244 offset:64\n\t" \
    "ds_read_b64_tr_b16 v[34:35], v244 offset:1600\n\t" \
    "v_add_u32 v244, 0xf000, v243\n\t" \
    "s_waitcnt vmcnt(12) lgkmcnt(0)\n\t" \
    "s_barrier \n\t" \
    "s_add_u32 m0, %[wm], 0xc000\n\t" \
    "s_mov_b32 s44, 0x6c0000\n\t" \
    "buffer_load_dwordx4 v240, %[wr], s44 offen offset:0 nt lds\n\t" \
    "buffer_load_dwordx4 v241, %[wr], s44 offen offset:1024 nt lds\n\t" \
    "buffer_load_dwordx4 v242, %[wr], s44 offen offset:2048 nt lds\n\t" \
    "ds_read_b64_tr_b16 v[20:21], v244 offset:0\n\t" \
    "ds_read_b64_tr_b16 v[22:23], v244 offset:1536\n\t" \
    "ds_read_b64_tr_b16 v[28:29], v244 offset:32\n\t" \
    "ds_read_b64_tr_b16 v[30:31], v244 offset:1568\n\t" \
    "ds_read_b64_tr_b16 v[36:37], v244 offset:64\n\t" \
    "ds_read_b64_tr_b16 v[38:39], v244 offset:1600\n\t" \
    "v_add_u32 v244, 0x0, v243\n\t" \
    "s_waitcnt vmcnt(12) lgkmcnt(0)\n\t" \
    "s_barrier \n\t" \
    "s_add_u32 m0, %[wm], 0xf000\n\t" \
    "s_mov_b32 s44, 0x720000\n\t" \
    "buffer_load_dwordx4 v240, %[wr], s44 offen offset:0 nt lds\n\t" \
    "buffer_load_dwordx4 v241, %[wr], s44 offen offset:1024 nt lds\n\t" \
    "buffer_load_dwordx4 v242, %[wr], s44 offen offset:2048 nt lds\n\t" \
    "ds_read_b64_tr_b16 v[224:225], v244 offset:0\n\t" \
    "ds_read_b64_tr_b16 v[226:227], v244 offset:1536\n\t" \
    "ds_read_b64_tr_b16 v[120:121], v244 offset:32\n\t" \
    "ds_read_b64_tr_b16 v[122:123], v244 offset:1568\n\t" \
    "ds_read_b64_tr_b16 v[128:129], v244 offset:64\n\t" \
    "ds_read_b64_tr_b16 v[130:131], v244 offset:1600\n\t" \
    "v_add_u32 v244, 0x3000, v243\n\t" \
    "s_waitcnt vmcnt(12) lgkmcnt(0)\n\t" \
    "s_barrier \n\t" \
    "ds_read_b64_tr_b16 v[228:229], v244 offset:0\n\t" \
    "ds_read_b64_tr_b16 v[230:231], v244 offset:1536\n\t" \
    "ds_read_b64_tr_b16 v[124:125], v244 offset:32\n\t" \
    "ds_read_b64_tr_b16 v[126:127], v244 offset:1568\n\t" \
    "ds_read_b64_tr_b16 v[132:133], v244 offset:64\n\t" \
    "ds_read_b64_tr_b16 v[134:135], v244 offset:1600\n\t" \
    "v_add_u32 v244, 0x6000, v243\n\t" \
    "s_waitcnt vmcnt(9) lgkmcnt(0)\n\t" \
    "s_barrier \n\t" \
    "ds_read_b64_tr_b16 v[232:233], v244 offset:0\n\t" \
    "ds_read_b64_tr_b16 v[234:235], v244 offset:1536\n\t" \
    "ds_read_b64_tr_b16 v[160:161], v244 offset:32\n\t" \
    "ds_read_b64_tr_b16 v[162:163], v244 offset:1568\n\t" \
    "ds_read_b64_tr_b16 v[168:169], v244 offset:64\n\t" \
    "ds_read_b64_tr_b16 v[170:171], v244 offset:1600\n\t" \
    "v_add_u32 v244, 0x9000, v243\n\t" \
    "s_waitcnt vmcnt(6) lgkmcnt(0)\n\t" \
    "s_barrier \n\t" \
    "ds_read_b64_tr_b16 v[236:237], v244 offset:0\n\t" \
    "ds_read_b64_tr_b16 v[238:239], v244 offset:1536\n\t" \
    "ds_read_b64_tr_b16 v[164:165], v244 offset:32\n\t" \
    "ds_read_b64_tr_b16 v[166:167], v244 offset:1568\n\t" \
    "ds_read_b64_tr_b16 v[172:173], v244 offset:64\n\t" \
    "ds_read_b64_tr_b16 v[174:175], v244 offset:1600\n\t" \
    "v_add_u32 v244, 0xc000, v243\n\t" \
    "s_waitcnt vmcnt(3) lgkmcnt(0)\n\t" \
    "s_barrier \n\t" \
    "ds_read_b64_tr_b16 v[176:177], v244 offset:0\n\t" \
    "ds_read_b64_tr_b16 v[178:179], v244 offset:1536\n\t" \
    "ds_read_b64_tr_b16 v[184:185], v244 offset:32\n\t" \
    "ds_read_b64_tr_b16 v[186:187], v244 offset:1568\n\t" \
    "ds_read_b64_tr_b16 v[192:193], v244 offset:64\n\t" \
    "ds_read_b64_tr_b16 v[194:195], v244 offset:1600\n\t" \
    "s_mov_b32 s43, 0\n\t" \
    "v_cmp_eq_u32 vcc, 0, v252\n\t" \
    "s_cbranch_vccnz .Lv13_%=\n\t" \
    "s_movk_i32 s45, 0x1000\n\t" \
    "s_mov_b32 m0, s45\n\t" \
    "s_nop 0\n\t" \
    "buffer_load_dwordx4 v253, s[60:63], s42 offen offset:0 sc1 lds\n\t" \
    "buffer_load_dwordx4 v253, s[60:63], s42 offen offset:1024 sc1 lds\n\t" \
    "buffer_load_dwordx4 v253, s[60:63], s42 offen offset:2048 sc1 lds\n\t" \
    "buffer_load_dwordx4 v253, s[60:63], s42 offen offset:3072 sc1 lds\n\t" \
    "s_add_u32 s44, s42, 0x1000\n\t" \
    "s_movk_i32 s45, 0x6000\n\t" \
    "s_mov_b32 m0, s45\n\t" \
    "s_nop 0\n\t" \
    "buffer_load_dwordx4 v253, s[60:63], s44 offen offset:0 sc1 lds\n\t" \
    "buffer_load_dwordx4 v253, s[60:63], s44 offen offset:1024 sc1 lds\n\t" \
    "buffer_load_dwordx4 v253, s[60:63], s44 offen offset:2048 sc1 lds\n\t" \
    "buffer_load_dwordx4 v253, s[60:63], s44 offen offset:3072 sc1 lds\n\t" \
    "s_add_u32 s44, s42, 0xc400\n\t" \
    "s_mov_b32 s45, 0x4400\n\t" \
    "s_mov_b32 m0, s45\n\t" \
    "s_nop 0\n\t" \
    "buffer_load_dwordx4 v253, s[60:63], s44 offen offset:0 sc1 lds\n\t" \
    "s_add_u32 s44, s44, 0x1000\n\t" \
    "s_mov_b32 s45, 0x9400\n\t" \
    "s_mov_b32 m0, s45\n\t" \
    "s_nop 0\n\t" \
    "buffer_load_dwordx4 v253, s[60:63], s44 offen offset:0 sc1 lds\n\t" \
    "s_mov_b32 s43, 1\n\t" \
    ".Lv13_%=:\n\t" \
    "v_add_u32 v244, 0xf000, v243\n\t" \
    "s_waitcnt vmcnt(0) lgkmcnt(0)\n\t" \
    "s_barrier \n\t" \
    "ds_read_b64_tr_b16 v[180:181], v244 offset:0\n\t" \
    "ds_read_b64_tr_b16 v[182:183], v244 offset:1536\n\t" \
    "ds_read_b64_tr_b16 v[188:189], v244 offset:32\n\t" \
    "ds_read_b64_tr_b16 v[190:191], v244 offset:1568\n\t" \
    "ds_read_b64_tr_b16 v[196:197], v244 offset:64\n\t" \
    "ds_read_b64_tr_b16 v[198:199], v244 offset:1600\n\t" \
    "s_waitcnt lgkmcnt(0)\n\t" \
    "s_barrier \n\t" \
    LST0 \
    "ds_write_b128 v254, v[200:203] offset:40960\n\t" \
    "ds_write_b128 v254, v[204:207] offset:41984\n\t" \
    "ds_write_b128 v254, v[208:211] offset:43008\n\t" \
    "ds_write_b128 v254, v[212:215] offset:44032\n\t" \
    "ds_write_b128 v254, v[216:219] offset:45056\n\t" \
    "ds_write_b128 v254, v[220:223] offset:46080\n\t" \
    "ds_write_b128 v254, v[224:227] offset:47104\n\t" \
    "ds_write_b128 v254, v[228:231] offset:48128\n\t" \
    "ds_write_b128 v254, v[232:235] offset:49152\n\t" \
    "ds_write_b128 v254, v[236:239] offset:50176\n\t" \
    "s_cmp_lg_u32 s43, 0\n\t" \
    "s_cbranch_scc1 .Lv14_%=\n\t" \
    "s_mov_b32 s44, 0\n\t" \
    ".Lv15_%=:\n\t" \
    "v_cmp_ge_u32 vcc, s44, v252\n\t" \
    "s_cbranch_vccz .Lv16_%=\n\t" \
    "s_cmp_eq_u32 s41, 0\n\t" \
    "s_cbranch_scc1 .Lv16_%=\n\t" \
    "s_sub_u32 s41, s41, 1\n\t" \
    "s_sleep 8\n\t" \
    "v_and_b32 v252, 0xf0, v253\n\t" \
    "v_lshlrev_b32 v252, 3, v252\n\t" \
    "buffer_load_dword v252, v252, s[60:63], %[fl] offen sc1\n\t" \
    "s_waitcnt vmcnt(0)\n\t" \
    "s_branch .Lv15_%=\n\t" \
    ".Lv16_%=:\n\t" \
    "s_movk_i32 s45, 0x1000\n\t" \
    "s_mov_b32 m0, s45\n\t" \
    "s_nop 0\n\t" \
    "buffer_load_dwordx4 v253, s[60:63], s42 offen offset:0 sc1 lds\n\t" \
    "buffer_load_dwordx4 v253, s[60:63], s42 offen offset:1024 sc1 lds\n\t" \
    "buffer_load_dwordx4 v253, s[60:63], s42 offen offset:2048 sc1 lds\n\t" \
    "buffer_load_dwordx4 v253, s[60:63], s42 offen offset:3072 sc1 lds\n\t" \
    "s_add_u32 s44, s42, 0x1000\n\t" \
    "s_movk_i32 s45, 0x6000\n\t" \
    "s_mov_b32 m0, s45\n\t" \
    "s_nop 0\n\t" \
    "buffer_load_dwordx4 v253, s[60:63], s44 offen offset:0 sc1 lds\n\t" \
    "buffer_load_dwordx4 v253, s[60:63], s44 offen offset:1024 sc1 lds\n\t" \
    "buffer_load_dwordx4 v253, s[60:63], s44 offen offset:2048 sc1 lds\n\t" \
    "buffer_load_dwordx4 v253, s[60:63], s44 offen offset:3072 sc1 lds\n\t" \
    "s_add_u32 s44, s42, 0xc400\n\t" \
    "s_mov_b32 s45, 0x4400\n\t" \
    "s_mov_b32 m0, s45\n\t" \
    "s_nop 0\n\t" \
    "buffer_load_dwordx4 v253, s[60:63], s44 offen offset:0 sc1 lds\n\t" \
    "s_add_u32 s44, s44, 0x1000\n\t" \
    "s_mov_b32 s45, 0x9400\n\t" \
    "s_mov_b32 m0, s45\n\t" \
    "s_nop 0\n\t" \
    "buffer_load_dwordx4 v253, s[60:63], s44 offen offset:0 sc1 lds\n\t" \
    ".Lv14_%=:\n\t" \
    "s_waitcnt vmcnt(0) lgkmcnt(0)\n\t" \
    "s_barrier \n\t" \
    "ds_read_b128 v[224:227], v253 offset:0\n\t" \
    "ds_read_b128 v[244:247], v254 offset:40960\n\t" \
    "ds_read_b128 v[228:231], v253 offset:1024\n\t" \
    "ds_read_b128 v[232:235], v253 offset:20480\n\t" \
    "ds_read_b128 v[236:239], v253 offset:21504\n\t" \
    "ds_read_b128 v[248:251], v254 offset:41984\n\t" \
    ".Lv17_%=:\n\t" \
    LST7 \
    "s_waitcnt lgkmcnt(5)\n\t" \
    "v_mfma_f32_16x16x32_bf16 v[204:207], v[64:67], v[224:227], 0\n\t" \
    "s_waitcnt lgkmcnt(3)\n\t" \
    "v_mfma_f32_16x16x32_bf16 v[204:207], v[68:71], v[228:231], v[204:207]\n\t" \
    "ds_read_b128 v[240:243], v253 offset:2048\n\t" \
    "v_mfma_f32_16x16x32_bf16 v[208:211], v[72:75], v[224:227], 0\n\t" \
    "v_mfma_f32_16x16x32_bf16 v[208:211], v[76:79], v[228:231], v[208:211]\n\t" \
    "v_mfma_f32_16x16x32_bf16 v[200:203], v[56:59], v[224:227], 0\n\t" \
    "v_mfma_f32_16x16x32_bf16 v[200:203], v[60:63], v[228:231], v[200:203]\n\t" \
    "s_waitcnt lgkmcnt(3)\n\t" \
    "v_mfma_f32_16x16x32_bf16 v[216:219], v[64:67], v[232:235], 0\n\t" \
    "ds_read_b128 v[224:227], v253 offset:3072\n\t" \
    "s_waitcnt lgkmcnt(3)\n\t" \
    "v_mfma_f32_16x16x32_bf16 v[216:219], v[68:71], v[236:239], v[216:219]\n\t" \
    "ds_read_b128 v[228:231], v253 offset:22528\n\t" \
    "v_mfma_f32_16x16x32_bf16 v[220:223], v[72:75], v[232:235], 0\n\t" \
    "v_mfma_f32_16x16x32_bf16 v[220:223], v[76:79], v[236:239], v[220:223]\n\t" \
    "v_mfma_f32_16x16x32_bf16 v[212:215], v[56:59], v[232:235], 0\n\t" \
    "v_mfma_f32_16x16x32_bf16 v[212:215], v[60:63], v[236:239], v[212:215]\n\t" \
    "s_waitcnt lgkmcnt(2)\n\t" \
    "v_mfma_f32_16x16x32_bf16 v[204:207], v[104:107], v[240:243], v[204:207]\n\t" \
    "ds_read_b128 v[232:235], v253 offset:23552\n\t" \
    "s_waitcnt lgkmcnt(2)\n\t" \
    "v_mfma_f32_16x16x32_bf16 v[204:207], v[108:111], v[224:227], v[204:207]\n\t" \
    "ds_read_b128 v[236:239], v253 offset:4096\n\t" \
    "v_mfma_f32_16x16x32_bf16 v[208:211], v[112:115], v[240:243], v[208:211]\n\t" \
    "v_mfma_f32_16x16x32_bf16 v[208:211], v[116:119], v[224:227], v[208:211]\n\t" \
    "v_mfma_f32_16x16x32_bf16 v[200:203], v[96:99], v[240:243], v[200:203]\n\t" \
    "v_mfma_f32_16x16x32_bf16 v[200:203], v[100:103], v[224:227], v[200:203]\n\t" \
    "s_waitcnt lgkmcnt(2)\n\t" \
    "v_mfma_f32_16x16x32_bf16 v[216:219], v[104:107], v[228:231], v[216:219]\n\t" \
    "ds_read_b128 v[240:243], v253 offset:5120\n\t" \
    "s_waitcnt lgkmcnt(2)\n\t" \
    "v_mfma_f32_16x16x32_bf16 v[216:219], v[108:111], v[232:235], v[216:219]\n\t" \
    "ds_read_b128 v[224:227], v253 offset:24576\n\t" \
    "v_mfma_f32_16x16x32_bf16 v[220:223], v[112:115], v[228:231], v[220:223]\n\t" \
    "v_mfma_f32_16x16x32_bf16 v[220:223], v[116:119], v[232:235], v[220:223]\n\t" \
    "v_mfma_f32_16x16x32_bf16 v[212:215], v[96:99], v[228:231], v[212:215]\n\t" \
    "v_mfma_f32_16x16x32_bf16 v[212:215], v[100:103], v[232:235], v[212:215]\n\t" \
    "s_waitcnt lgkmcnt(2)\n\t" \
    "v_mfma_f32_16x16x32_bf16 v[204:207], v[0:3], v[236:239], v[204:207]\n\t" \
    "ds_read_b128 v[228:231], v253 offset:25600\n\t" \
    "s_add_u32 s44, s40, 1\n\t" \
    "s_waitcnt lgkmcnt(2)\n\t" \
    "v_mfma_f32_16x16x32_bf16 v[204:207], v[4:7], v[240:243], v[204:207]\n\t" \
    "ds_read_b128 v[232:235], v253 offset:6144\n\t" \
    "s_and_b32 s45, s44, 15\n\t" \
    "v_mfma_f32_16x16x32_bf16 v[208:211], v[8:11], v[236:239], v[208:211]\n\t" \
    "s_cmp_eq_u32 s45, 0\n\t" \
    "v_mfma_f32_16x16x32_bf16 v[208:211], v[12:15], v[240:243], v[208:211]\n\t" \
    "s_cselect_b32 s45, %[cj], %[cs]\n\t" \
    "v_mfma_f32_16x16x32_bf16 v[200:203], v[244:247], v[236:239], v[200:203]\n\t" \
    "s_cmp_eq_u32 s44, 64\n\t" \
    "v_mfma_f32_16x16x32_bf16 v[200:203], v[248:251], v[240:243], v[200:203]\n\t" \
    "s_cselect_b32 s45, 0, s45\n\t" \
    "s_waitcnt lgkmcnt(2)\n\t" \
    "v_mfma_f32_16x16x32_bf16 v[216:219], v[0:3], v[224:227], v[216:219]\n\t" \
    "ds_read_b128 v[236:239], v253 offset:7168\n\t" \
    "s_add_u32 s42, s42, s45\n\t" \
    "s_waitcnt lgkmcnt(2)\n\t" \
    "v_mfma_f32_16x16x32_bf16 v[216:219], v[4:7], v[228:231], v[216:219]\n\t" \
    "ds_read_b128 v[240:243], v253 offset:26624\n\t" \
    "v_mfma_f32_16x16x32_bf16 v[220:223], v[8:11], v[224:227], v[220:223]\n\t" \
    "v_mfma_f32_16x16x32_bf16 v[220:223], v[12:15], v[228:231], v[220:223]\n\t" \
    "v_mfma_f32_16x16x32_bf16 v[212:215], v[244:247], v[224:227], v[212:215]\n\t" \
    "ds_read_b128 v[244:247], v254 offset:43008\n\t" \
    "v_mfma_f32_16x16x32_bf16 v[212:215], v[248:251], v[228:231], v[212:215]\n\t" \
    "ds_read_b128 v[248:251], v254 offset:44032\n\t" \
    "s_waitcnt lgkmcnt(4)\n\t" \
    "v_mfma_f32_16x16x32_bf16 v[204:207], v[40:43], v[232:235], v[204:207]\n\t" \
    "ds_read_b128 v[224:227], v253 offset:27648\n\t" \
    "s_waitcnt lgkmcnt(4)\n\t" \
    "v_mfma_f32_16x16x32_bf16 v[204:207], v[44:47], v[236:239], v[204:207]\n\t" \
    "ds_read_b128 v[228:231], v253 offset:8192\n\t" \
    "v_mfma_f32_16x16x32_bf16 v[208:211], v[48:51], v[232:235], v[208:211]\n\t" \
    "v_mfma_f32_16x16x32_bf16 v[208:211], v[52:55], v[236:239], v[208:211]\n\t" \
    "s_waitcnt lgkmcnt(3)\n\t" \
    "v_mfma_f32_16x16x32_bf16 v[200:203], v[244:247], v[232:235], v[200:203]\n\t" \
    "s_waitcnt lgkmcnt(2)\n\t" \
    "v_mfma_f32_16x16x32_bf16 v[200:203], v[248:251], v[236:239], v[200:203]\n\t" \
    "v_mfma_f32_16x16x32_bf16 v[216:219], v[40:43], v[240:243], v[216:219]\n\t" \
    "ds_read_b128 v[232:235], v253 offset:9216\n\t" \
    "s_waitcnt lgkmcnt(2)\n\t" \
    "v_mfma_f32_16x16x32_bf16 v[216:219], v[44:47], v[224:227], v[216:219]\n\t" \
    "ds_read_b128 v[236:239], v253 offset:28672\n\t" \
    "v_mfma_f32_16x16x32_bf16 v[220:223], v[48:51], v[240:243], v[220:223]\n\t" \
    "v_mfma_f32_16x16x32_bf16 v[220:223], v[52:55], v[224:227], v[220:223]\n\t" \
    "v_mfma_f32_16x16x32_bf16 v[212:215], v[244:247], v[240:243], v[212:215]\n\t" \
    "ds_read_b128 v[244:247], v254 offset:45056\n\t" \
    "v_mfma_f32_16x16x32_bf16 v[212:215], v[248:251], v[224:227], v[212:215]\n\t" \
    "ds_read_b128 v[248:251], v254 offset:46080\n\t" \
    "s_waitcnt lgkmcnt(4)\n\t" \
    "v_mfma_f32_16x16x32_bf16 v[204:207], v[144:147], v[228:231], v[204:207]\n\t" \
    "ds_read_b128 v[240:243], v253 offset:29696\n\t" \
    "s_waitcnt lgkmcnt(4)\n\t" \
    "v_mfma_f32_16x16x32_bf16 v[204:207], v[148:151], v[232:235], v[204:207]\n\t" \
    "ds_read_b128 v[224:227], v253 offset:10240\n\t" \
    "v_mfma_f32_16x16x32_bf16 v[208:211], v[152:155], v[228:231], v[208:211]\n\t" \
    "v_mfma_f32_16x16x32_bf16 v[208:211], v[156:159], v[232:235], v[208:211]\n\t" \
    "v_mfma_f32_16x16x32_bf16 v[200:203], v[136:139], v[228:231], v[200:203]\n\t" \
    "v_mfma_f32_16x16x32_bf16 v[200:203], v[140:143], v[232:235], v[200:203]\n\t" \
    "s_waitcnt lgkmcnt(4)\n\t" \
    "v_mfma_f32_16x16x32_bf16 v[216:219], v[144:147], v[236:239], v[216:219]\n\t" \
    "ds_read_b128 v[228:231], v253 offset:11264\n\t" \
    "s_waitcnt lgkmcnt(2)\n\t" \
    "v_mfma_f32_16x16x32_bf16 v[216:219], v[148:151], v[240:243], v[216:219]\n\t" \
    "ds_read_b128 v[232:235], v253 offset:30720\n\t" \
    "v_mfma_f32_16x16x32_bf16 v[220:223], v[152:155], v[236:239], v[220:223]\n\t" \
    "v_mfma_f32_16x16x32_bf16 v[220:223], v[156:159], v[240:243], v[220:223]\n\t" \
    "v_mfma_f32_16x16x32_bf16 v[212:215], v[136:139], v[236:239], v[212:215]\n\t" \
    "v_mfma_f32_16x16x32_bf16 v[212:215], v[140:143], v[240:243], v[212:215]\n\t" \
    "s_waitcnt lgkmcnt(2)\n\t" \
    "v_mfma_f32_16x16x32_bf16 v[204:207], v[80:83], v[224:227], v[204:207]\n\t" \
    "ds_read_b128 v[236:239], v253 offset:31744\n\t" \
    "s_and_b32 s44, s40, 3\n\t" \
    "s_waitcnt lgkmcnt(2)\n\t" \
    "v_mfma_f32_16x16x32_bf16 v[204:207], v[84:87], v[228:231], v[204:207]\n\t" \
    "ds_read_b128 v[240:243], v253 offset:14336\n\t" \
    "s_lshl_b32 s44, s44, 17\n\t" \
    "v_mfma_f32_16x16x32_bf16 v[208:211], v[88:91], v[224:227], v[208:211]\n\t" \
    "s_add_u32 s44, s44, %[mb]\n\t" \
    "v_mfma_f32_16x16x32_bf16 v[208:211], v[92:95], v[228:231], v[208:211]\n\t" \
    "s_add_u32 s46, s44, 0x800\n\t" \
    "v_mfma_f32_16x16x32_bf16 v[200:203], v[244:247], v[224:227], v[200:203]\n\t" \
    "s_add_u32 s47, s44, 0x1800\n\t" \
    "v_mfma_f32_16x16x32_bf16 v[200:203], v[248:251], v[228:231], v[200:203]\n\t" \
    "s_add_u32 s48, s44, 0xc00\n\t" \
    "s_waitcnt lgkmcnt(2)\n\t" \
    "v_mfma_f32_16x16x32_bf16 v[216:219], v[80:83], v[232:235], v[216:219]\n\t" \
    "ds_read_b128 v[224:227], v253 offset:15360\n\t" \
    "s_waitcnt lgkmcnt(2)\n\t" \
    "v_mfma_f32_16x16x32_bf16 v[216:219], v[84:87], v[236:239], v[216:219]\n\t" \
    "ds_read_b128 v[228:231], v253 offset:34816\n\t" \
    "v_mfma_f32_16x16x32_bf16 v[220:223], v[88:91], v[232:235], v[220:223]\n\t" \
    "v_mfma_f32_16x16x32_bf16 v[220:223], v[92:95], v[236:239], v[220:223]\n\t" \
    "v_mfma_f32_16x16x32_bf16 v[212:215], v[244:247], v[232:235], v[212:215]\n\t" \
    "ds_read_b128 v[244:247], v254 offset:47104\n\t" \
    "v_mfma_f32_16x16x32_bf16 v[212:215], v[248:251], v[236:239], v[212:215]\n\t" \
    "ds_read_b128 v[248:251], v254 offset:48128\n\t" \
    "s_waitcnt lgkmcnt(4)\n\t" \
    "v_mfma_f32_16x16x32_bf16 v[204:207], v[120:123], v[240:243], v[204:207]\n\t" \
    "ds_read_b128 v[232:235], v253 offset:35840\n\t" \
    "s_waitcnt lgkmcnt(4)\n\t" \
    "v_mfma_f32_16x16x32_bf16 v[204:207], v[124:127], v[224:227], v[204:207]\n\t" \
    "ds_read_b128 v[236:239], v253 offset:16384\n\t" \
    "v_mfma_f32_16x16x32_bf16 v[208:211], v[128:131], v[240:243], v[208:211]\n\t" \
    "v_mfma_f32_16x16x32_bf16 v[208:211], v[132:135], v[224:227], v[208:211]\n\t" \
    "s_waitcnt lgkmcnt(3)\n\t" \
    "v_mfma_f32_16x16x32_bf16 v[200:203], v[244:247], v[240:243], v[200:203]\n\t" \
    "s_waitcnt lgkmcnt(2)\n\t" \
    "v_mfma_f32_16x16x32_bf16 v[200:203], v[248:251], v[224:227], v[200:203]\n\t" \
    "v_mfma_f32_16x16x32_bf16 v[216:219], v[120:123], v[228:231], v[216:219]\n\t" \
    "ds_read_b128 v[240:243], v253 offset:17408\n\t" \
    "s_waitcnt lgkmcnt(2)\n\t" \
    "v_mfma_f32_16x16x32_bf16 v[216:219], v[124:127], v[232:235], v[216:219]\n\t" \
    "ds_read_b128 v[224:227], v253 offset:36864\n\t" \
    "v_mfma_f32_16x16x32_bf16 v[220:223], v[128:131], v[228:231], v[220:223]\n\t" \
    "v_mfma_f32_16x16x32_bf16 v[220:223], v[132:135], v[232:235], v[220:223]\n\t" \
    "v_mfma_f32_16x16x32_bf16 v[212:215], v[244:247], v[228:231], v[212:215]\n\t" \
    "ds_read_b128 v[244:247], v254 offset:49152\n\t" \
    "v_mfma_f32_16x16x32_bf16 v[212:215], v[248:251], v[232:235], v[212:215]\n\t" \
    "ds_read_b128 v[248:251], v254 offset:50176\n\t" \
    "v_and_b32 v252, 0xf0, v253\n\t" \
    "v_lshlrev_b32 v252, 3, v252\n\t" \
    "buffer_load_dword v252, v252, s[60:63], %[fl] offen sc1\n\t" \
    "s_waitcnt lgkmcnt(4)\n\t" \
    "v_mfma_f32_16x16x32_bf16 v[204:207], v[160:163], v[236:239], v[204:207]\n\t" \
    "ds_read_b128 v[228:231], v253 offset:37888\n\t" \
    "s_waitcnt lgkmcnt(4)\n\t" \
    "v_mfma_f32_16x16x32_bf16 v[204:207], v[164:167], v[240:243], v[204:207]\n\t" \
    "ds_read_b128 v[232:235], v253 offset:12288\n\t" \
    "v_mfma_f32_16x16x32_bf16 v[208:211], v[168:171], v[236:239], v[208:211]\n\t" \
    "v_mfma_f32_16x16x32_bf16 v[208:211], v[172:175], v[240:243], v[208:211]\n\t" \
    "s_waitcnt lgkmcnt(3)\n\t" \
    "v_mfma_f32_16x16x32_bf16 v[200:203], v[244:247], v[236:239], v[200:203]\n\t" \
    "s_waitcnt lgkmcnt(2)\n\t" \
    "v_mfma_f32_16x16x32_bf16 v[200:203], v[248:251], v[240:243], v[200:203]\n\t" \
    "v_mfma_f32_16x16x32_bf16 v[216:219], v[160:163], v[224:227], v[216:219]\n\t" \
    "ds_read_b128 v[236:239], v253 offset:13312\n\t" \
    "s_waitcnt lgkmcnt(2)\n\t" \
    "v_mfma_f32_16x16x32_bf16 v[216:219], v[164:167], v[228:231], v[216:219]\n\t" \
    "ds_read_b128 v[240:243], v253 offset:18432\n\t" \
    "v_mfma_f32_16x16x32_bf16 v[220:223], v[168:171], v[224:227], v[220:223]\n\t" \
    "v_mfma_f32_16x16x32_bf16 v[220:223], v[172:175], v[228:231], v[220:223]\n\t" \
    "v_mfma_f32_16x16x32_bf16 v[212:215], v[244:247], v[224:227], v[212:215]\n\t" \
    "v_mfma_f32_16x16x32_bf16 v[212:215], v[248:251], v[228:231], v[212:215]\n\t" \
    "s_waitcnt lgkmcnt(2)\n\t" \
    "v_mfma_f32_16x16x32_bf16 v[204:207], v[24:27], v[232:235], v[204:207]\n\t" \
    "ds_read_b128 v[224:227], v253 offset:19456\n\t" \
    "v_mov_b64 v[244:245], 0\n\t" \
    "s_waitcnt lgkmcnt(2)\n\t" \
    "v_mfma_f32_16x16x32_bf16 v[204:207], v[28:31], v[236:239], v[204:207]\n\t" \
    "ds_read_b128 v[228:231], v253 offset:32768\n\t" \
    "v_mov_b64 v[246:247], 0\n\t" \
    "v_mfma_f32_16x16x32_bf16 v[208:211], v[32:35], v[232:235], v[208:211]\n\t" \
    "v_mov_b64 v[248:249], 0\n\t" \
    "v_mfma_f32_16x16x32_bf16 v[208:211], v[36:39], v[236:239], v[208:211]\n\t" \
    "v_mov_b64 v[250:251], 0\n\t" \
    "v_mfma_f32_16x16x32_bf16 v[200:203], v[16:19], v[232:235], v[200:203]\n\t" \
    "v_mfma_f32_16x16x32_bf16 v[200:203], v[20:23], v[236:239], v[200:203]\n\t" \
    "s_waitcnt lgkmcnt(2)\n\t" \
    "v_mfma_f32_16x16x32_bf16 v[204:207], v[184:187], v[240:243], v[204:207]\n\t" \
    "ds_read_b128 v[232:235], v253 offset:33792\n\t" \
    "s_waitcnt lgkmcnt(2)\n\t" \
    "v_mfma_f32_16x16x32_bf16 v[204:207], v[188:191], v[224:227], v[204:207]\n\t" \
    "ds_read_b128 v[236:239], v253 offset:38912\n\t" \
    "v_mfma_f32_16x16x32_bf16 v[208:211], v[192:195], v[240:243], v[208:211]\n\t" \
    "v_mfma_f32_16x16x32_bf16 v[208:211], v[196:199], v[224:227], v[208:211]\n\t" \
    "v_mfma_f32_16x16x32_bf16 v[200:203], v[176:179], v[240:243], v[200:203]\n\t" \
    "v_mfma_f32_16x16x32_bf16 v[200:203], v[180:183], v[224:227], v[200:203]\n\t" \
    "ds_read_b128 v[240:243], v253 offset:39936\n\t" \
    "s_waitcnt lgkmcnt(3)\n\t" \
    "v_mfma_f32_16x16x32_bf16 v[216:219], v[24:27], v[228:231], v[216:219]\n\t" \
    "s_waitcnt lgkmcnt(2)\n\t" \
    "v_mfma_f32_16x16x32_bf16 v[216:219], v[28:31], v[232:235], v[216:219]\n\t" \
    "v_cvt_pk_bf16_f32 v204, v204, v205\n\t" \
    "v_cvt_pk_bf16_f32 v205, v206, v207\n\t" \
    "s_waitcnt lgkmcnt(1)\n\t" \
    "v_mfma_f32_16x16x32_bf16 v[216:219], v[184:187], v[236:239], v[216:219]\n\t" \
    "ds_write_b64 v254, v[204:205] offset:10240\n\t" \
    "v_cvt_pk_bf16_f32 v208, v208, v209\n\t" \
    "s_waitcnt lgkmcnt(1)\n\t" \
    "v_mfma_f32_16x16x32_bf16 v[216:219], v[188:191], v[240:243], v[216:219]\n\t" \
    "v_cvt_pk_bf16_f32 v209, v210, v211\n\t" \
    "ds_write_b64 v254, v[208:209] offset:11264\n\t" \
    "v_mfma_f32_16x16x32_bf16 v[220:223], v[32:35], v[228:231], v[220:223]\n\t" \
    "v_cvt_pk_bf16_f32 v200, v200, v201\n\t" \
    "v_cvt_pk_bf16_f32 v201, v202, v203\n\t" \
    "v_mfma_f32_16x16x32_bf16 v[220:223], v[36:39], v[232:235], v[220:223]\n\t" \
    "ds_write_b64 v254, v[200:201] offset:9216\n\t" \
    "ds_read_b128 v[200:203], v254 offset:40960\n\t" \
    "v_mfma_f32_16x16x32_bf16 v[220:223], v[192:195], v[236:239], v[220:223]\n\t" \
    "ds_read_b128 v[204:207], v254 offset:41984\n\t" \
    "v_mov_b64 v[208:209], 0\n\t" \
    "v_mfma_f32_16x16x32_bf16 v[220:223], v[196:199], v[240:243], v[220:223]\n\t" \
    "v_mov_b64 v[210:211], 0\n\t" \
    "v_cvt_pk_bf16_f32 v216, v216, v217\n\t" \
    "v_mfma_f32_16x16x32_bf16 v[212:215], v[16:19], v[228:231], v[212:215]\n\t" \
    "v_cvt_pk_bf16_f32 v217, v218, v219\n\t" \
    "ds_write_b64 v254, v[216:217] offset:16384\n\t" \
    "v_mfma_f32_16x16x32_bf16 v[212:215], v[20:23], v[232:235], v[212:215]\n\t" \
    "v_mfma_f32_16x16x32_bf16 v[212:215], v[176:179], v[236:239], v[212:215]\n\t" \
    "v_cvt_pk_bf16_f32 v220, v220, v221\n\t" \
    "v_cvt_pk_bf16_f32 v221, v222, v223\n\t" \
    "v_mfma_f32_16x16x32_bf16 v[212:215], v[180:183], v[240:243], v[212:215]\n\t" \
    "ds_write_b64 v254, v[220:221] offset:17408\n\t" \
    "ds_read_b128 v[216:219], v254 offset:43008\n\t" \
    "ds_read_b128 v[220:223], v254 offset:44032\n\t" \
    "s_nop 4\n\t" \
    "v_cvt_pk_bf16_f32 v212, v212, v213\n\t" \
    "v_cvt_pk_bf16_f32 v213, v214, v215\n\t" \
    "ds_write_b64 v254, v[212:213] offset:15360\n\t" \
    LST6 \
    "s_waitcnt vmcnt(0) lgkmcnt(0)\n\t" \
    "s_barrier \n\t" \
    "s_setprio 2\n\t" \
    "s_add_u32 s44, s40, 1\n\t" \
    "s_min_u32 s44, s44, 63\n\t" \
    ".Lv18_%=:\n\t" \
    "v_cmp_ge_u32 vcc, s44, v252\n\t" \
    "s_cbranch_vccz .Lv19_%=\n\t" \
    "s_cmp_eq_u32 s41, 0\n\t" \
    "s_cbranch_scc1 .Lv19_%=\n\t" \
    "s_sub_u32 s41, s41, 1\n\t" \
    "s_sleep 2\n\t" \
    "v_and_b32 v252, 0xf0, v253\n\t" \
    "v_lshlrev_b32 v252, 3, v252\n\t" \
    "buffer_load_dword v252, v252, s[60:63], %[fl] offen sc1\n\t" \
    "s_waitcnt vmcnt(0)\n\t" \
    "s_branch .Lv18_%=\n\t" \
    ".Lv19_%=:\n\t" \
    "s_movk_i32 s45, 0x1000\n\t" \
    "s_mov_b32 m0, s45\n\t" \
    "s_nop 0\n\t" \
    "buffer_load_dwordx4 v253, s[60:63], s42 offen offset:0 sc1 lds\n\t" \
    "buffer_load_dwordx4 v253, s[60:63], s42 offen offset:1024 sc1 lds\n\t" \
    "buffer_load_dwordx4 v253, s[60:63], s42 offen offset:2048 sc1 lds\n\t" \
    "buffer_load_dwordx4 v253, s[60:63], s42 offen offset:3072 sc1 lds\n\t" \
    "s_add_u32 s44, s42, 0x1000\n\t" \
    "s_movk_i32 s45, 0x6000\n\t" \
    "s_mov_b32 m0, s45\n\t" \
    "s_nop 0\n\t" \
    "buffer_load_dwordx4 v253, s[60:63], s44 offen offset:0 sc1 lds\n\t" \
    "buffer_load_dwordx4 v253, s[60:63], s44 offen offset:1024 sc1 lds\n\t" \
    "buffer_load_dwordx4 v253, s[60:63], s44 offen offset:2048 sc1 lds\n\t" \
    "buffer_load_dwordx4 v253, s[60:63], s44 offen offset:3072 sc1 lds\n\t" \
    "s_add_u32 s44, s42, 0xc400\n\t" \
    "s_mov_b32 s45, 0x4400\n\t" \
    "s_mov_b32 m0, s45\n\t" \
    "s_nop 0\n\t" \
    "buffer_load_dwordx4 v253, s[60:63], s44 offen offset:0 sc1 lds\n\t" \
    "s_add_u32 s44, s44, 0x1000\n\t" \
    "s_mov_b32 s45, 0x9400\n\t" \
    "s_mov_b32 m0, s45\n\t" \
    "s_nop 0\n\t" \
    "buffer_load_dwordx4 v253, s[60:63], s44 offen offset:0 sc1 lds\n\t" \
    LST1 \
    "s_sleep 18\n\t" \
    "buffer_load_dwordx4 v[224:227], v253, s[60:63], s46 offen sc1\n\t" \
    "buffer_load_dwordx4 v[228:231], v253, s[60:63], s47 offen sc1\n\t" \
    "buffer_load_dwordx4 v[232:235], v253, s[60:63], s48 offen sc1\n\t" \
    "s_waitcnt lgkmcnt(0)\n\t" \
    LFLUSH212 \
    "v_bfe_i32 v212, v253, 5, 1\n\t" \
    "v_and_b32 v212, 0xaaaa, v212\n\t" \
    "v_or_b32 v212, 0x4444, v212\n\t" \
    "v_and_b32 v213, 0xffff, v255\n\t" \
    "v_lshrrev_b32 v214, 16, v255\n\t" \
    ".Lv110_%=:\n\t" \
    "s_waitcnt vmcnt(2)\n\t" \
    "v_max_u32 v215, v224, v225\n\t" \
    "v_max3_u32 v215, v226, v227, v215\n\t" \
    "s_waitcnt vmcnt(1)\n\t" \
    "v_max3_u32 v215, v228, v229, v215\n\t" \
    "v_max3_u32 v215, v230, v231, v215\n\t" \
    "s_waitcnt vmcnt(0)\n\t" \
    "v_max3_u32 v215, v232, v233, v215\n\t" \
    "v_max3_u32 v215, v234, v235, v215\n\t" \
    "v_cmp_eq_u32 vcc, -1, v215\n\t" \
    "s_cbranch_vccz .Lv111_%=\n\t" \
    "v_max_u32 v215, v224, v225\n\t" \
    "v_max3_u32 v215, v226, v227, v215\n\t" \
    "v_cmp_eq_u32 vcc, -1, v215\n\t" \
    "s_cbranch_vccz .Lv112_%=\n\t" \
    "buffer_load_dwordx4 v[224:227], v253, s[60:63], s46 offen sc1\n\t" \
    ".Lv112_%=:\n\t" \
    "v_max_u32 v215, v228, v229\n\t" \
    "v_max3_u32 v215, v230, v231, v215\n\t" \
    "v_cmp_eq_u32 vcc, -1, v215\n\t" \
    "s_cbranch_vccz .Lv113_%=\n\t" \
    "buffer_load_dwordx4 v[228:231], v253, s[60:63], s47 offen sc1\n\t" \
    ".Lv113_%=:\n\t" \
    "v_max_u32 v215, v232, v233\n\t" \
    "v_max3_u32 v215, v234, v235, v215\n\t" \
    "v_cmp_eq_u32 vcc, -1, v215\n\t" \
    "s_cbranch_vccz .Lv114_%=\n\t" \
    "buffer_load_dwordx4 v[232:235], v253, s[60:63], s48 offen sc1\n\t" \
    ".Lv114_%=:\n\t" \
    "s_cmp_eq_u32 s41, 0\n\t" \
    "s_cbranch_scc1 .Lv111_%=\n\t" \
    "s_sub_u32 s41, s41, 1\n\t" \
    "s_waitcnt vmcnt(0)\n\t" \
    "s_branch .Lv110_%=\n\t" \
    ".Lv111_%=:\n\t" \
    LST2 \
    "ds_write_b128 v253, v[224:227] offset:43008\n\t" \
    "ds_write_b128 v253, v[228:231] offset:47104\n\t" \
    "ds_write_b128 v253, v[232:235] offset:44032\n\t" \
    "v_smfmac_f32_16x16x64_bf16 v[244:247], v[224:227], v[0:7], v212\n\t" \
    "v_smfmac_f32_16x16x64_bf16 v[248:251], v[224:227], v[8:15], v212\n\t" \
    "v_smfmac_f32_16x16x64_bf16 v[208:211], v[224:227], v[200:207], v212\n\t" \
    "s_waitcnt lgkmcnt(0)\n\t" \
    "s_barrier \n\t" \
    LST3 \
    "ds_read_b128 v[236:239], v253 offset:40960\n\t" \
    "ds_read_b128 v[240:243], v253 offset:46080\n\t" \
    "ds_read_b128 v[224:227], v253 offset:41984\n\t" \
    "ds_read_b128 v[200:203], v254 offset:45056\n\t" \
    "ds_read_b128 v[204:207], v254 offset:46080\n\t" \
    "v_smfmac_f32_16x16x64_bf16 v[208:211], v[228:231], v[16:23], v212\n\t" \
    "v_smfmac_f32_16x16x64_bf16 v[244:247], v[228:231], v[24:31], v212\n\t" \
    "v_smfmac_f32_16x16x64_bf16 v[248:251], v[228:231], v[32:39], v212\n\t" \
    "ds_read_b128 v[228:231], v253 offset:48128\n\t" \
    "v_smfmac_f32_16x16x64_bf16 v[208:211], v[232:235], v[216:223], v212\n\t" \
    "ds_read_b128 v[216:219], v254 offset:47104\n\t" \
    "ds_read_b128 v[220:223], v254 offset:48128\n\t" \
    "v_smfmac_f32_16x16x64_bf16 v[244:247], v[232:235], v[40:47], v212\n\t" \
    "v_smfmac_f32_16x16x64_bf16 v[248:251], v[232:235], v[48:55], v212\n\t" \
    "ds_read_b128 v[232:235], v253 offset:45056\n\t" \
    "s_waitcnt lgkmcnt(8)\n\t" \
    "v_smfmac_f32_16x16x64_bf16 v[208:211], v[236:239], v[56:63], v212\n\t" \
    "v_smfmac_f32_16x16x64_bf16 v[244:247], v[236:239], v[64:71], v212\n\t" \
    "v_smfmac_f32_16x16x64_bf16 v[248:251], v[236:239], v[72:79], v212\n\t" \
    "ds_read_b128 v[236:239], v253 offset:49152\n\t" \
    "s_waitcnt lgkmcnt(5)\n\t" \
    "v_smfmac_f32_16x16x64_bf16 v[208:211], v[240:243], v[200:207], v212\n\t" \
    "ds_read_b128 v[200:203], v254 offset:49152\n\t" \
    "ds_read_b128 v[204:207], v254 offset:50176\n\t" \
    "v_smfmac_f32_16x16x64_bf16 v[244:247], v[240:243], v[80:87], v212\n\t" \
    "v_smfmac_f32_16x16x64_bf16 v[248:251], v[240:243], v[88:95], v212\n\t" \
    "ds_read_b128 v[240:243], v253 offset:50176\n\t" \
    "v_smfmac_f32_16x16x64_bf16 v[208:211], v[224:227], v[96:103], v212\n\t" \
    "v_smfmac_f32_16x16x64_bf16 v[244:247], v[224:227], v[104:111], v212\n\t" \
    "v_smfmac_f32_16x16x64_bf16 v[248:251], v[224:227], v[112:119], v212\n\t" \
    "s_waitcnt lgkmcnt(7)\n\t" \
    "v_smfmac_f32_16x16x64_bf16 v[244:247], v[228:231], v[120:127], v212\n\t" \
    "s_waitcnt lgkmcnt(4)\n\t" \
    "v_smfmac_f32_16x16x64_bf16 v[244:247], v[232:235], v[144:151], v212\n\t" \
    "s_waitcnt lgkmcnt(3)\n\t" \
    "v_smfmac_f32_16x16x64_bf16 v[244:247], v[236:239], v[160:167], v212\n\t" \
    "s_waitcnt lgkmcnt(0)\n\t" \
    "v_smfmac_f32_16x16x64_bf16 v[244:247], v[240:243], v[184:191], v212\n\t" \
    "v_smfmac_f32_16x16x64_bf16 v[248:251], v[228:231], v[128:135], v212\n\t" \
    "v_smfmac_f32_16x16x64_bf16 v[248:251], v[232:235], v[152:159], v212\n\t" \
    "v_smfmac_f32_16x16x64_bf16 v[248:251], v[236:239], v[168:175], v212\n\t" \
    "v_add_f32 v244, v244, v246\n\t" \
    "v_add_f32 v245, v245, v247\n\t" \
    "v_cvt_pk_bf16_f32 v244, v244, v245\n\t" \
    "v_smfmac_f32_16x16x64_bf16 v[248:251], v[240:243], v[192:199], v212\n\t" \
    "ds_write_b16 v213, v244 offset:512\n\t" \
    "ds_write_b16_d16_hi v213, v244 offset:528\n\t" \
    "v_smfmac_f32_16x16x64_bf16 v[208:211], v[228:231], v[216:223], v212\n\t" \
    "v_smfmac_f32_16x16x64_bf16 v[208:211], v[232:235], v[136:143], v212\n\t" \
    "v_smfmac_f32_16x16x64_bf16 v[208:211], v[236:239], v[200:207], v212\n\t" \
    "v_add_f32 v248, v248, v250\n\t" \
    "v_add_f32 v249, v249, v251\n\t" \
    "v_cvt_pk_bf16_f32 v248, v248, v249\n\t" \
    "v_smfmac_f32_16x16x64_bf16 v[208:211], v[240:243], v[176:183], v212\n\t" \
    "ds_write_b16 v213, v248 offset:1024\n\t" \
    "ds_write_b16_d16_hi v213, v248 offset:1040\n\t" \
    "s_nop 5\n\t" \
    "v_add_f32 v208, v208, v210\n\t" \
    "v_add_f32 v209, v209, v211\n\t" \
    "v_cvt_pk_bf16_f32 v208, v208, v209\n\t" \
    "ds_write_b16 v213, v208 offset:0\n\t" \
    "ds_write_b16_d16_hi v213, v208 offset:16\n\t" \
    "s_setprio 0\n\t" \
    "s_waitcnt lgkmcnt(0)\n\t" \
    "s_barrier \n\t" \
    LST4 \
    "ds_read_b128 v[224:227], v253 offset:0\n\t" \
    "ds_read_b128 v[244:247], v254 offset:40960\n\t" \
    "ds_read_b128 v[228:231], v253 offset:1024\n\t" \
    "ds_read_b128 v[232:235], v253 offset:20480\n\t" \
    "ds_read_b128 v[236:239], v253 offset:21504\n\t" \
    "ds_read_b128 v[248:251], v254 offset:41984\n\t" \
    "s_add_u32 s40, s40, 1\n\t" \
    "s_cmp_lg_u32 s40, 64\n\t" \
    "s_cbranch_scc1 .Lv17_%=\n\t" \
    "s_branch .Lend%=\n\t" \
    ".Lv0_%=:\n\t" \
    "s_mov_b32 s42, %[xs]\n\t" \
    "s_add_u32 m0, %[wm], 0xc000\n\t" \
    "s_mov_b32 s44, 0x0\n\t" \
    "buffer_load_dwordx4 v240, %[wr], s44 offen offset:0 nt lds\n\t" \
    "buffer_load_dwordx4 v241, %[wr], s44 offen offset:1024 nt lds\n\t" \
    "buffer_load_dwordx4 v242, %[wr], s44 offen offset:2048 nt lds\n\t" \
    "s_add_u32 m0, %[wm], 0xf000\n\t" \
    "s_mov_b32 s44, 0x60000\n\t" \
    "buffer_load_dwordx4 v240, %[wr], s44 offen offset:0 nt lds\n\t" \
    "buffer_load_dwordx4 v241, %[wr], s44 offen offset:1024 nt lds\n\t" \
    "buffer_load_dwordx4 v242, %[wr], s44 offen offset:2048 nt lds\n\t" \
    "s_add_u32 m0, %[wm], 0x0\n\t" \
    "s_mov_b32 s44, 0xc0000\n\t" \
    "buffer_load_dwordx4 v240, %[wr], s44 offen offset:0 nt lds\n\t" \
    "buffer_load_dwordx4 v241, %[wr], s44 offen offset:1024 nt lds\n\t" \
    "buffer_load_dwordx4 v242, %[wr], s44 offen offset:2048 nt lds\n\t" \
    "s_add_u32 m0, %[wm], 0x3000\n\t" \
    "s_mov_b32 s44, 0x120000\n\t" \
    "buffer_load_dwordx4 v240, %[wr], s44 offen offset:0 nt lds\n\t" \
    "buffer_load_dwordx4 v241, %[wr], s44 offen offset:1024 nt lds\n\t" \
    "buffer_load_dwordx4 v242, %[wr], s44 offen offset:2048 nt lds\n\t" \
    "s_add_u32 m0, %[wm], 0x6000\n\t" \
    "s_mov_b32 s44, 0x180000\n\t" \
    "buffer_load_dwordx4 v240, %[wr], s44 offen offset:0 nt lds\n\t" \
    "buffer_load_dwordx4 v241, %[wr], s44 offen offset:1024 nt lds\n\t" \
    "buffer_load_dwordx4 v242, %[wr], s44 offen offset:2048 nt lds\n\t" \
    "v_add_u32 v244, 0xc000, v243\n\t" \
    "s_waitcnt vmcnt(12) lgkmcnt(0)\n\t" \
    "s_barrier \n\t" \
    "s_add_u32 m0, %[wm], 0x9000\n\t" \
    "s_mov_b32 s44, 0x1e0000\n\t" \
    "buffer_load_dwordx4 v240, %[wr], s44 offen offset:0 nt lds\n\t" \
    "buffer_load_dwordx4 v241, %[wr], s44 offen offset:1024 nt lds\n\t" \
    "buffer_load_dwordx4 v242, %[wr], s44 offen offset:2048 nt lds\n\t" \
    "ds_read_b64_tr_b16 v[200:201], v244 offset:0\n\t" \
    "ds_read_b64_tr_b16 v[202:203], v244 offset:1536\n\t" \
    "ds_read_b64_tr_b16 v[0:1], v244 offset:32\n\t" \
    "ds_read_b64_tr_b16 v[2:3], v244 offset:1568\n\t" \
    "ds_read_b64_tr_b16 v[8:9], v244 offset:64\n\t" \
    "ds_read_b64_tr_b16 v[10:11], v244 offset:1600\n\t" \
    "v_add_u32 v244, 0xf000, v243\n\t" \
    "s_waitcnt vmcnt(12) lgkmcnt(0)\n\t" \
    "s_barrier \n\t" \
    "s_add_u32 m0, %[wm], 0xc000\n\t" \
    "s_mov_b32 s44, 0x240000\n\t" \
    "buffer_load_dwordx4 v240, %[wr], s44 offen offset:0 nt lds\n\t" \
    "buffer_load_dwordx4 v241, %[wr], s44 offen offset:1024 nt lds\n\t" \
    "buffer_load_dwordx4 v242, %[wr], s44 offen offset:2048 nt lds\n\t" \
    "ds_read_b64_tr_b16 v[204:205], v244 offset:0\n\t" \
    "ds_read_b64_tr_b16 v[206:207], v244 offset:1536\n\t" \
    "ds_read_b64_tr_b16 v[4:5], v244 offset:32\n\t" \
    "ds_read_b64_tr_b16 v[6:7], v244 offset:1568\n\t" \
    "ds_read_b64_tr_b16 v[12:13], v244 offset:64\n\t" \
    "ds_read_b64_tr_b16 v[14:15], v244 offset:1600\n\t" \
    "v_add_u32 v244, 0x0, v243\n\t" \
    "s_waitcnt vmcnt(12) lgkmcnt(0)\n\t" \
    "s_barrier \n\t" \
    "s_add_u32 m0, %[wm], 0xf000\n\t" \
    "s_mov_b32 s44, 0x2a0000\n\t" \
    "buffer_load_dwordx4 v240, %[wr], s44 offen offset:0 nt lds\n\t" \
    "buffer_load_dwordx4 v241, %[wr], s44 offen offset:1024 nt lds\n\t" \
    "buffer_load_dwordx4 v242, %[wr], s44 offen offset:2048 nt lds\n\t" \
    "ds_read_b64_tr_b16 v[208:209], v244 offset:0\n\t" \
    "ds_read_b64_tr_b16 v[210:211], v244 offset:1536\n\t" \
    "ds_read_b64_tr_b16 v[40:41], v244 offset:32\n\t" \
    "ds_read_b64_tr_b16 v[42:43], v244 offset:1568\n\t" \
    "ds_read_b64_tr_b16 v[48:49], v244 offset:64\n\t" \
    "ds_read_b64_tr_b16 v[50:51], v244 offset:1600\n\t" \
    "v_add_u32 v244, 0x3000, v243\n\t" \
    "s_waitcnt vmcnt(12) lgkmcnt(0)\n\t" \
    "s_barrier \n\t" \
    "s_add_u32 m0, %[wm], 0x0\n\t" \
    "s_mov_b32 s44, 0x300000\n\t" \
    "buffer_load_dwordx4 v240, %[wr], s44 offen offset:0 nt lds\n\t" \
    "buffer_load_dwordx4 v241, %[wr], s44 offen offset:1024 nt lds\n\t" \
    "buffer_load_dwordx4 v242, %[wr], s44 offen offset:2048 nt lds\n\t" \
    "ds_read_b64_tr_b16 v[212:213], v244 offset:0\n\t" \
    "ds_read_b64_tr_b16 v[214:215], v244 offset:1536\n\t" \
    "ds_read_b64_tr_b16 v[44:45], v244 offset:32\n\t" \
    "ds_read_b64_tr_b16 v[46:47], v244 offset:1568\n\t" \
    "ds_read_b64_tr_b16 v[52:53], v244 offset:64\n\t" \
    "ds_read_b64_tr_b16 v[54:55], v244 offset:1600\n\t" \
    "v_add_u32 v244, 0x6000, v243\n\t" \
    "s_waitcnt vmcnt(12) lgkmcnt(0)\n\t" \
    "s_barrier \n\t" \
    "s_add_u32 m0, %[wm], 0x3000\n\t" \
    "s_mov_b32 s44, 0x360000\n\t" \
    "buffer_load_dwordx4 v240, %[wr], s44 offen offset:0 nt lds\n\t" \
    "buffer_load_dwordx4 v241, %[wr], s44 offen offset:1024 nt lds\n\t" \
    "buffer_load_dwordx4 v242, %[wr], s44 offen offset:2048 nt lds\n\t" \
    "ds_read_b64_tr_b16 v[216:217], v244 offset:0\n\t" \
    "ds_read_b64_tr_b16 v[218:219], v244 offset:1536\n\t" \
    "ds_read_b64_tr_b16 v[80:81], v244 offset:32\n\t" \
    "ds_read_b64_tr_b16 v[82:83], v244 offset:1568\n\t" \
    "ds_read_b64_tr_b16 v[88:89], v244 offset:64\n\t" \
    "ds_read_b64_tr_b16 v[90:91], v244 offset:1600\n\t" \
    "v_add_u32 v244, 0x9000, v243\n\t" \
    "s_waitcnt vmcnt(12) lgkmcnt(0)\n\t" \
    "s_barrier \n\t" \
    "s_add_u32 m0, %[wm], 0x6000\n\t" \
    "s_mov_b32 s44, 0x3c0000\n\t" \
    "buffer_load_dwordx4 v240, %[wr], s44 offen offset:0 nt lds\n\t" \
    "buffer_load_dwordx4 v241, %[wr], s44 offen offset:1024 nt lds\n\t" \
    "buffer_load_dwordx4 v242, %[wr], s44 offen offset:2048 nt lds\n\t" \
    "ds_read_b64_tr_b16 v[220:221], v244 offset:0\n\t" \
    "ds_read_b64_tr_b16 v[222:223], v244 offset:1536\n\t" \
    "ds_read_b64_tr_b16 v[84:85], v244 offset:32\n\t" \
    "ds_read_b64_tr_b16 v[86:87], v244 offset:1568\n\t" \
    "ds_read_b64_tr_b16 v[92:93], v244 offset:64\n\t" \
    "ds_read_b64_tr_b16 v[94:95], v244 offset:1600\n\t" \
    "v_add_u32 v244, 0xc000, v243\n\t" \
    "s_waitcnt vmcnt(12) lgkmcnt(0)\n\t" \
    "s_barrier \n\t" \
    "v_mov_b32 v246, 0\n\t" \
    "buffer_load_dword v246, v246, s[60:63], %[mo] offen sc1\n\t" \
    "s_add_u32 m0, %[wm], 0x9000\n\t" \
    "s_mov_b32 s44, 0x420000\n\t" \
    "buffer_load_dwordx4 v240, %[wr], s44 offen offset:0 nt lds\n\t" \
    "buffer_load_dwordx4 v241, %[wr], s44 offen offset:1024 nt lds\n\t" \
    "buffer_load_dwordx4 v242, %[wr], s44 offen offset:2048 nt lds\n\t" \
    "ds_read_b64_tr_b16 v[224:225], v244 offset:0\n\t" \
    "ds_read_b64_tr_b16 v[226:227], v244 offset:1536\n\t" \
    "ds_read_b64_tr_b16 v[120:121], v244 offset:32\n\t" \
    "ds_read_b64_tr_b16 v[122:123], v244 offset:1568\n\t" \
    "ds_read_b64_tr_b16 v[128:129], v244 offset:64\n\t" \
    "ds_read_b64_tr_b16 v[130:131], v244 offset:1600\n\t" \
    "v_add_u32 v244, 0xf000, v243\n\t" \
    "s_waitcnt vmcnt(12) lgkmcnt(0)\n\t" \
    "s_barrier \n\t" \
    "s_add_u32 m0, %[wm], 0xc000\n\t" \
    "s_mov_b32 s44, 0x480000\n\t" \
    "buffer_load_dwordx4 v240, %[wr], s44 offen offset:0 nt lds\n\t" \
    "buffer_load_dwordx4 v241, %[wr], s44 offen offset:1024 nt lds\n\t" \
    "buffer_load_dwordx4 v242, %[wr], s44 offen offset:2048 nt lds\n\t" \
    "ds_read_b64_tr_b16 v[228:229], v244 offset:0\n\t" \
    "ds_read_b64_tr_b16 v[230:231], v244 offset:1536\n\t" \
    "ds_read_b64_tr_b16 v[124:125], v244 offset:32\n\t" \
    "ds_read_b64_tr_b16 v[126:127], v244 offset:1568\n\t" \
    "ds_read_b64_tr_b16 v[132:133], v244 offset:64\n\t" \
    "ds_read_b64_tr_b16 v[134:135], v244 offset:1600\n\t" \
    "v_add_u32 v244, 0x0, v243\n\t" \
    "s_waitcnt vmcnt(12) lgkmcnt(0)\n\t" \
    "s_barrier \n\t" \
    "s_add_u32 m0, %[wm], 0xf000\n\t" \
    "s_mov_b32 s44, 0x4e0000\n\t" \
    "buffer_load_dwordx4 v240, %[wr], s44 offen offset:0 nt lds\n\t" \
    "buffer_load_dwordx4 v241, %[wr], s44 offen offset:1024 nt lds\n\t" \
    "buffer_load_dwordx4 v242, %[wr], s44 offen offset:2048 nt lds\n\t" \
    "ds_read_b64_tr_b16 v[232:233], v244 offset:0\n\t" \
    "ds_read_b64_tr_b16 v[234:235], v244 offset:1536\n\t" \
    "ds_read_b64_tr_b16 v[160:161], v244 offset:32\n\t" \
    "ds_read_b64_tr_b16 v[162:163], v244 offset:1568\n\t" \
    "ds_read_b64_tr_b16 v[168:169], v244 offset:64\n\t" \
    "ds_read_b64_tr_b16 v[170:171], v244 offset:1600\n\t" \
    "v_add_u32 v244, 0x3000, v243\n\t" \
    "s_waitcnt vmcnt(12) lgkmcnt(0)\n\t" \
    "s_barrier \n\t" \
    "s_add_u32 m0, %[wm], 0x0\n\t" \
    "s_mov_b32 s44, 0x540000\n\t" \
    "buffer_load_dwordx4 v240, %[wr], s44 offen offset:0 nt lds\n\t" \
    "buffer_load_dwordx4 v241, %[wr], s44 offen offset:1024 nt lds\n\t" \
    "buffer_load_dwordx4 v242, %[wr], s44 offen offset:2048 nt lds\n\t" \
    "ds_read_b64_tr_b16 v[236:237], v244 offset:0\n\t" \
    "ds_read_b64_tr_b16 v[238:239], v244 offset:1536\n\t" \
    "ds_read_b64_tr_b16 v[164:165], v244 offset:32\n\t" \
    "ds_read_b64_tr_b16 v[166:167], v244 offset:1568\n\t" \
    "ds_read_b64_tr_b16 v[172:173], v244 offset:64\n\t" \
    "ds_read_b64_tr_b16 v[174:175], v244 offset:1600\n\t" \
    "v_add_u32 v244, 0x6000, v243\n\t" \
    "s_waitcnt vmcnt(12) lgkmcnt(0)\n\t" \
    "s_barrier \n\t" \
    "s_add_u32 m0, %[wm], 0x3000\n\t" \
    "s_mov_b32 s44, 0x5a0000\n\t" \
    "buffer_load_dwordx4 v240, %[wr], s44 offen offset:0 nt lds\n\t" \
    "buffer_load_dwordx4 v241, %[wr], s44 offen offset:1024 nt lds\n\t" \
    "buffer_load_dwordx4 v242, %[wr], s44 offen offset:2048 nt lds\n\t" \
    "ds_read_b64_tr_b16 v[16:17], v244 offset:0\n\t" \
    "ds_read_b64_tr_b16 v[18:19], v244 offset:1536\n\t" \
    "ds_read_b64_tr_b16 v[24:25], v244 offset:32\n\t" \
    "ds_read_b64_tr_b16 v[26:27], v244 offset:1568\n\t" \
    "ds_read_b64_tr_b16 v[32:33], v244 offset:64\n\t" \
    "ds_read_b64_tr_b16 v[34:35], v244 offset:1600\n\t" \
    "v_add_u32 v244, 0x9000, v243\n\t" \
    "s_waitcnt vmcnt(12) lgkmcnt(0)\n\t" \
    "s_barrier \n\t" \
    ".Lv01_%=:\n\t" \
    "v_readfirstlane_b32 s43, v246\n\t" \
    "s_nop 0\n\t" \
    "s_cmp_lg_u32 s43, -1\n\t" \
    "s_cbranch_scc1 .Lv02_%=\n\t" \
    "s_cmp_eq_u32 s41, 0\n\t" \
    "s_cbranch_scc1 .Lv02_%=\n\t" \
    "s_sub_u32 s41, s41, 1\n\t" \
    "s_sleep 4\n\t" \
    "v_mov_b32 v246, 0\n\t" \
    "buffer_load_dword v246, v246, s[60:63], %[mo] offen sc1\n\t" \
    "s_waitcnt vmcnt(0)\n\t" \
    "s_branch .Lv01_%=\n\t" \
    ".Lv02_%=:\n\t" \
    "v_mov_b32 v246, 0x8000\n\t" \
    "s_mov_b64 exec, 1\n\t" \
    "buffer_store_dword v246, v246, s[60:63], %[mo] offen sc1\n\t" \
    "s_mov_b64 exec, -1\n\t" \
    "s_and_b32 s43, s43, 0x2000\n\t" \
    "s_xor_b32 s43, s43, %[lx]\n\t" \
    "s_add_u32 s60, s60, s43\n\t" \
    "s_addc_u32 s61, s61, 0\n\t" \
    "v_and_b32 v252, 0xf0, v253\n\t" \
    "v_lshlrev_b32 v252, 3, v252\n\t" \
    "buffer_load_dword v252, v252, s[60:63], %[fl] offen sc1\n\t" \
    "s_add_u32 m0, %[wm], 0x6000\n\t" \
    "s_mov_b32 s44, 0x600000\n\t" \
    "buffer_load_dwordx4 v240, %[wr], s44 offen offset:0 nt lds\n\t" \
    "buffer_load_dwordx4 v241, %[wr], s44 offen offset:1024 nt lds\n\t" \
    "buffer_load_dwordx4 v242, %[wr], s44 offen offset:2048 nt lds\n\t" \
    "ds_read_b64_tr_b16 v[20:21], v244 offset:0\n\t" \
    "ds_read_b64_tr_b16 v[22:23], v244 offset:1536\n\t" \
    "ds_read_b64_tr_b16 v[28:29], v244 offset:32\n\t" \
    "ds_read_b64_tr_b16 v[30:31], v244 offset:1568\n\t" \
    "ds_read_b64_tr_b16 v[36:37], v244 offset:64\n\t" \
    "ds_read_b64_tr_b16 v[38:39], v244 offset:1600\n\t" \
    "v_add_u32 v244, 0xc000, v243\n\t" \
    "s_waitcnt vmcnt(12) lgkmcnt(0)\n\t" \
    "s_barrier \n\t" \
    "s_add_u32 m0, %[wm], 0x9000\n\t" \
    "s_mov_b32 s44, 0x660000\n\t" \
    "buffer_load_dwordx4 v240, %[wr], s44 offen offset:0 nt lds\n\t" \
    "buffer_load_dwordx4 v241, %[wr], s44 offen offset:1024 nt lds\n\t" \
    "buffer_load_dwordx4 v242, %[wr], s44 offen offset:2048 nt lds\n\t" \
    "ds_read_b64_tr_b16 v[56:57], v244 offset:0\n\t" \
    "ds_read_b64_tr_b16 v[58:59], v244 offset:1536\n\t" \
    "ds_read_b64_tr_b16 v[64:65], v244 offset:32\n\t" \
    "ds_read_b64_tr_b16 v[66:67], v244 offset:1568\n\t" \
    "ds_read_b64_tr_b16 v[72:73], v244 offset:64\n\t" \
    "ds_read_b64_tr_b16 v[74:75], v244 offset:1600\n\t" \
    "v_add_u32 v244, 0xf000, v243\n\t" \
    "s_waitcnt vmcnt(12) lgkmcnt(0)\n\t" \
    "s_barrier \n\t" \
    "s_add_u32 m0, %[wm], 0xc000\n\t" \
    "s_mov_b32 s44, 0x6c0000\n\t" \
    "buffer_load_dwordx4 v240, %[wr], s44 offen offset:0 nt lds\n\t" \
    "buffer_load_dwordx4 v241, %[wr], s44 offen offset:1024 nt lds\n\t" \
    "buffer_load_dwordx4 v242, %[wr], s44 offen offset:2048 nt lds\n\t" \
    "ds_read_b64_tr_b16 v[60:61], v244 offset:0\n\t" \
    "ds_read_b64_tr_b16 v[62:63], v244 offset:1536\n\t" \
    "ds_read_b64_tr_b16 v[68:69], v244 offset:32\n\t" \
    "ds_read_b64_tr_b16 v[70:71], v244 offset:1568\n\t" \
    "ds_read_b64_tr_b16 v[76:77], v244 offset:64\n\t" \
    "ds_read_b64_tr_b16 v[78:79], v244 offset:1600\n\t" \
    "v_add_u32 v244, 0x0, v243\n\t" \
    "s_waitcnt vmcnt(12) lgkmcnt(0)\n\t" \
    "s_barrier \n\t" \
    "s_add_u32 m0, %[wm], 0xf000\n\t" \
    "s_mov_b32 s44, 0x720000\n\t" \
    "buffer_load_dwordx4 v240, %[wr], s44 offen offset:0 nt lds\n\t" \
    "buffer_load_dwordx4 v241, %[wr], s44 offen offset:1024 nt lds\n\t" \
    "buffer_load_dwordx4 v242, %[wr], s44 offen offset:2048 nt lds\n\t" \
    "ds_read_b64_tr_b16 v[96:97], v244 offset:0\n\t" \
    "ds_read_b64_tr_b16 v[98:99], v244 offset:1536\n\t" \
    "ds_read_b64_tr_b16 v[104:105], v244 offset:32\n\t" \
    "ds_read_b64_tr_b16 v[106:107], v244 offset:1568\n\t" \
    "ds_read_b64_tr_b16 v[112:113], v244 offset:64\n\t" \
    "ds_read_b64_tr_b16 v[114:115], v244 offset:1600\n\t" \
    "v_add_u32 v244, 0x3000, v243\n\t" \
    "s_waitcnt vmcnt(12) lgkmcnt(0)\n\t" \
    "s_barrier \n\t" \
    "ds_read_b64_tr_b16 v[100:101], v244 offset:0\n\t" \
    "ds_read_b64_tr_b16 v[102:103], v244 offset:1536\n\t" \
    "ds_read_b64_tr_b16 v[108:109], v244 offset:32\n\t" \
    "ds_read_b64_tr_b16 v[110:111], v244 offset:1568\n\t" \
    "ds_read_b64_tr_b16 v[116:117], v244 offset:64\n\t" \
    "ds_read_b64_tr_b16 v[118:119], v244 offset:1600\n\t" \
    "v_add_u32 v244, 0x6000, v243\n\t" \
    "s_waitcnt vmcnt(9) lgkmcnt(0)\n\t" \
    "s_barrier \n\t" \
    "ds_read_b64_tr_b16 v[136:137], v244 offset:0\n\t" \
    "ds_read_b64_tr_b16 v[138:139], v244 offset:1536\n\t" \
    "ds_read_b64_tr_b16 v[144:145], v244 offset:32\n\t" \
    "ds_read_b64_tr_b16 v[146:147], v244 offset:1568\n\t" \
    "ds_read_b64_tr_b16 v[152:153], v244 offset:64\n\t" \
    "ds_read_b64_tr_b16 v[154:155], v244 offset:1600\n\t" \
    "v_add_u32 v244, 0x9000, v243\n\t" \
    "s_waitcnt vmcnt(6) lgkmcnt(0)\n\t" \
    "s_barrier \n\t" \
    "ds_read_b64_tr_b16 v[140:141], v244 offset:0\n\t" \
    "ds_read_b64_tr_b16 v[142:143], v244 offset:1536\n\t" \
    "ds_read_b64_tr_b16 v[148:149], v244 offset:32\n\t" \
    "ds_read_b64_tr_b16 v[150:151], v244 offset:1568\n\t" \
    "ds_read_b64_tr_b16 v[156:157], v244 offset:64\n\t" \
    "ds_read_b64_tr_b16 v[158:159], v244 offset:1600\n\t" \
    "v_add_u32 v244, 0xc000, v243\n\t" \
    "s_waitcnt vmcnt(3) lgkmcnt(0)\n\t" \
    "s_barrier \n\t" \
    "ds_read_b64_tr_b16 v[176:177], v244 offset:0\n\t" \
    "ds_read_b64_tr_b16 v[178:179], v244 offset:1536\n\t" \
    "ds_read_b64_tr_b16 v[184:185], v244 offset:32\n\t" \
    "ds_read_b64_tr_b16 v[186:187], v244 offset:1568\n\t" \
    "ds_read_b64_tr_b16 v[192:193], v244 offset:64\n\t" \
    "ds_read_b64_tr_b16 v[194:195], v244 offset:1600\n\t" \
    "s_mov_b32 s43, 0\n\t" \
    "v_cmp_eq_u32 vcc, 0, v252\n\t" \
    "s_cbranch_vccnz .Lv03_%=\n\t" \
    "s_movk_i32 s45, 0x0\n\t" \
    "s_mov_b32 m0, s45\n\t" \
    "s_nop 0\n\t" \
    "buffer_load_dwordx4 v253, s[60:63], s42 offen offset:0 sc1 lds\n\t" \
    "buffer_load_dwordx4 v253, s[60:63], s42 offen offset:1024 sc1 lds\n\t" \
    "buffer_load_dwordx4 v253, s[60:63], s42 offen offset:2048 sc1 lds\n\t" \
    "buffer_load_dwordx4 v253, s[60:63], s42 offen offset:3072 sc1 lds\n\t" \
    "s_add_u32 s44, s42, 0x1000\n\t" \
    "s_movk_i32 s45, 0x5000\n\t" \
    "s_mov_b32 m0, s45\n\t" \
    "s_nop 0\n\t" \
    "buffer_load_dwordx4 v253, s[60:63], s44 offen offset:0 sc1 lds\n\t" \
    "buffer_load_dwordx4 v253, s[60:63], s44 offen offset:1024 sc1 lds\n\t" \
    "buffer_load_dwordx4 v253, s[60:63], s44 offen offset:2048 sc1 lds\n\t" \
    "buffer_load_dwordx4 v253, s[60:63], s44 offen offset:3072 sc1 lds\n\t" \
    "s_add_u32 s44, s42, 0x10000\n\t" \
    "s_mov_b32 s45, 0x4000\n\t" \
    "s_mov_b32 m0, s45\n\t" \
    "s_nop 0\n\t" \
    "buffer_load_dwordx4 v253, s[60:63], s44 offen offset:0 sc1 lds\n\t" \
    "s_add_u32 s44, s44, 0x1000\n\t" \
    "s_mov_b32 s45, 0x9000\n\t" \
    "s_mov_b32 m0, s45\n\t" \
    "s_nop 0\n\t" \
    "buffer_load_dwordx4 v253, s[60:63], s44 offen offset:0 sc1 lds\n\t" \
    "s_mov_b32 s43, 1\n\t" \
    ".Lv03_%=:\n\t" \
    "v_add_u32 v244, 0xf000, v243\n\t" \
    "s_waitcnt vmcnt(0) lgkmcnt(0)\n\t" \
    "s_barrier \n\t" \
    "ds_read_b64_tr_b16 v[180:181], v244 offset:0\n\t" \
    "ds_read_b64_tr_b16 v[182:183], v244 offset:1536\n\t" \
    "ds_read_b64_tr_b16 v[188:189], v244 offset:32\n\t" \
    "ds_read_b64_tr_b16 v[190:191], v244 offset:1568\n\t" \
    "ds_read_b64_tr_b16 v[196:197], v244 offset:64\n\t" \
    "ds_read_b64_tr_b16 v[198:199], v244 offset:1600\n\t" \
    "s_waitcnt lgkmcnt(0)\n\t" \
    "s_barrier \n\t" \
    LST0 \
    "ds_write_b128 v254, v[200:203] offset:30720\n\t" \
    "ds_write_b128 v254, v[204:207] offset:31744\n\t" \
    "ds_write_b128 v254, v[208:211] offset:32768\n\t" \
    "ds_write_b128 v254, v[212:215] offset:33792\n\t" \
    "ds_write_b128 v254, v[216:219] offset:34816\n\t" \
    "ds_write_b128 v254, v[220:223] offset:35840\n\t" \
    "ds_write_b128 v254, v[224:227] offset:36864\n\t" \
    "ds_write_b128 v254, v[228:231] offset:37888\n\t" \
    "ds_write_b128 v254, v[232:235] offset:38912\n\t" \
    "ds_write_b128 v254, v[236:239] offset:39936\n\t" \
    "s_cmp_lg_u32 s43, 0\n\t" \
    "s_cbranch_scc1 .Lv04_%=\n\t" \
    "s_mov_b32 s44, 0\n\t" \
    ".Lv05_%=:\n\t" \
    "v_cmp_ge_u32 vcc, s44, v252\n\t" \
    "s_cbranch_vccz .Lv06_%=\n\t" \
    "s_cmp_eq_u32 s41, 0\n\t" \
    "s_cbranch_scc1 .Lv06_%=\n\t" \
    "s_sub_u32 s41, s41, 1\n\t" \
    "s_sleep 8\n\t" \
    "v_and_b32 v252, 0xf0, v253\n\t" \
    "v_lshlrev_b32 v252, 3, v252\n\t" \
    "buffer_load_dword v252, v252, s[60:63], %[fl] offen sc1\n\t" \
    "s_waitcnt vmcnt(0)\n\t" \
    "s_branch .Lv05_%=\n\t" \
    ".Lv06_%=:\n\t" \
    "s_movk_i32 s45, 0x0\n\t" \
    "s_mov_b32 m0, s45\n\t" \
    "s_nop 0\n\t" \
    "buffer_load_dwordx4 v253, s[60:63], s42 offen offset:0 sc1 lds\n\t" \
    "buffer_load_dwordx4 v253, s[60:63], s42 offen offset:1024 sc1 lds\n\t" \
    "buffer_load_dwordx4 v253, s[60:63], s42 offen offset:2048 sc1 lds\n\t" \
    "buffer_load_dwordx4 v253, s[60:63], s42 offen offset:3072 sc1 lds\n\t" \
    "s_add_u32 s44, s42, 0x1000\n\t" \
    "s_movk_i32 s45, 0x5000\n\t" \
    "s_mov_b32 m0, s45\n\t" \
    "s_nop 0\n\t" \
    "buffer_load_dwordx4 v253, s[60:63], s44 offen offset:0 sc1 lds\n\t" \
    "buffer_load_dwordx4 v253, s[60:63], s44 offen offset:1024 sc1 lds\n\t" \
    "buffer_load_dwordx4 v253, s[60:63], s44 offen offset:2048 sc1 lds\n\t" \
    "buffer_load_dwordx4 v253, s[60:63], s44 offen offset:3072 sc1 lds\n\t" \
    "s_add_u32 s44, s42, 0x10000\n\t" \
    "s_mov_b32 s45, 0x4000\n\t" \
    "s_mov_b32 m0, s45\n\t" \
    "s_nop 0\n\t" \
    "buffer_load_dwordx4 v253, s[60:63], s44 offen offset:0 sc1 lds\n\t" \
    "s_add_u32 s44, s44, 0x1000\n\t" \
    "s_mov_b32 s45, 0x9000\n\t" \
    "s_mov_b32 m0, s45\n\t" \
    "s_nop 0\n\t" \
    "buffer_load_dwordx4 v253, s[60:63], s44 offen offset:0 sc1 lds\n\t" \
    ".Lv04_%=:\n\t" \
    "s_waitcnt vmcnt(0) lgkmcnt(0)\n\t" \
    "s_barrier \n\t" \
    "ds_read_b128 v[224:227], v253 offset:0\n\t" \
    "ds_read_b128 v[244:247], v254 offset:30720\n\t" \
    "ds_read_b128 v[228:231], v253 offset:1024\n\t" \
    "ds_read_b128 v[232:235], v253 offset:20480\n\t" \
    "ds_read_b128 v[236:239], v253 offset:21504\n\t" \
    "ds_read_b128 v[248:251], v254 offset:31744\n\t" \
    ".Lv07_%=:\n\t" \
    LST7 \
    "s_waitcnt lgkmcnt(5)\n\t" \
    "v_mfma_f32_16x16x32_bf16 v[204:207], v[0:3], v[224:227], 0\n\t" \
    "s_waitcnt lgkmcnt(3)\n\t" \
    "v_mfma_f32_16x16x32_bf16 v[204:207], v[4:7], v[228:231], v[204:207]\n\t" \
    "ds_read_b128 v[240:243], v253 offset:2048\n\t" \
    "v_mfma_f32_16x16x32_bf16 v[208:211], v[8:11], v[224:227], 0\n\t" \
    "v_mfma_f32_16x16x32_bf16 v[208:211], v[12:15], v[228:231], v[208:211]\n\t" \
    "v_mfma_f32_16x16x32_bf16 v[200:203], v[244:247], v[224:227], 0\n\t" \
    "s_waitcnt lgkmcnt(1)\n\t" \
    "v_mfma_f32_16x16x32_bf16 v[200:203], v[248:251], v[228:231], v[200:203]\n\t" \
    "v_mfma_f32_16x16x32_bf16 v[216:219], v[0:3], v[232:235], 0\n\t" \
    "ds_read_b128 v[224:227], v253 offset:3072\n\t" \
    "v_mfma_f32_16x16x32_bf16 v[216:219], v[4:7], v[236:239], v[216:219]\n\t" \
    "ds_read_b128 v[228:231], v253 offset:22528\n\t" \
    "v_mfma_f32_16x16x32_bf16 v[220:223], v[8:11], v[232:235], 0\n\t" \
    "v_mfma_f32_16x16x32_bf16 v[220:223], v[12:15], v[236:239], v[220:223]\n\t" \
    "v_mfma_f32_16x16x32_bf16 v[212:215], v[244:247], v[232:235], 0\n\t" \
    "ds_read_b128 v[244:247], v254 offset:32768\n\t" \
    "v_mfma_f32_16x16x32_bf16 v[212:215], v[248:251], v[236:239], v[212:215]\n\t" \
    "ds_read_b128 v[248:251], v254 offset:33792\n\t" \
    "s_waitcnt lgkmcnt(4)\n\t" \
    "v_mfma_f32_16x16x32_bf16 v[204:207], v[40:43], v[240:243], v[204:207]\n\t" \
    "ds_read_b128 v[232:235], v253 offset:23552\n\t" \
    "s_waitcnt lgkmcnt(4)\n\t" \
    "v_mfma_f32_16x16x32_bf16 v[204:207], v[44:47], v[224:227], v[204:207]\n\t" \
    "ds_read_b128 v[236:239], v253 offset:4096\n\t" \
    "v_mfma_f32_16x16x32_bf16 v[208:211], v[48:51], v[240:243], v[208:211]\n\t" \
    "v_mfma_f32_16x16x32_bf16 v[208:211], v[52:55], v[224:227], v[208:211]\n\t" \
    "s_waitcnt lgkmcnt(3)\n\t" \
    "v_mfma_f32_16x16x32_bf16 v[200:203], v[244:247], v[240:243], v[200:203]\n\t" \
    "s_waitcnt lgkmcnt(2)\n\t" \
    "v_mfma_f32_16x16x32_bf16 v[200:203], v[248:251], v[224:227], v[200:203]\n\t" \
    "v_mfma_f32_16x16x32_bf16 v[216:219], v[40:43], v[228:231], v[216:219]\n\t" \
    "ds_read_b128 v[240:243], v253 offset:5120\n\t" \
    "s_waitcnt lgkmcnt(2)\n\t" \
    "v_mfma_f32_16x16x32_bf16 v[216:219], v[44:47], v[232:235], v[216:219]\n\t" \
    "ds_read_b128 v[224:227], v253 offset:24576\n\t" \
    "v_mfma_f32_16x16x32_bf16 v[220:223], v[48:51], v[228:231], v[220:223]\n\t" \
    "v_mfma_f32_16x16x32_bf16 v[220:223], v[52:55], v[232:235], v[220:223]\n\t" \
    "v_mfma_f32_16x16x32_bf16 v[212:215], v[244:247], v[228:231], v[212:215]\n\t" \
    "ds_read_b128 v[244:247], v254 offset:34816\n\t" \
    "v_mfma_f32_16x16x32_bf16 v[212:215], v[248:251], v[232:235], v[212:215]\n\t" \
    "ds_read_b128 v[248:251], v254 offset:35840\n\t" \
    "s_waitcnt lgkmcnt(4)\n\t" \
    "v_mfma_f32_16x16x32_bf16 v[204:207], v[80:83], v[236:239], v[204:207]\n\t" \
    "ds_read_b128 v[228:231], v253 offset:25600\n\t" \
    "s_add_u32 s44, s40, 1\n\t" \
    "s_waitcnt lgkmcnt(4)\n\t" \
    "v_mfma_f32_16x16x32_bf16 v[204:207], v[84:87], v[240:243], v[204:207]\n\t" \
    "ds_read_b128 v[232:235], v253 offset:6144\n\t" \
    "s_and_b32 s45, s44, 15\n\t" \
    "v_mfma_f32_16x16x32_bf16 v[208:211], v[88:91], v[236:239], v[208:211]\n\t" \
    "s_cmp_eq_u32 s45, 0\n\t" \
    "v_mfma_f32_16x16x32_bf16 v[208:211], v[92:95], v[240:243], v[208:211]\n\t" \
    "s_cselect_b32 s45, %[cj], %[cs]\n\t" \
    "s_waitcnt lgkmcnt(3)\n\t" \
    "v_mfma_f32_16x16x32_bf16 v[200:203], v[244:247], v[236:239], v[200:203]\n\t" \
    "s_cmp_eq_u32 s44, 64\n\t" \
    "s_waitcnt lgkmcnt(2)\n\t" \
    "v_mfma_f32_16x16x32_bf16 v[200:203], v[248:251], v[240:243], v[200:203]\n\t" \
    "s_cselect_b32 s45, 0, s45\n\t" \
    "v_mfma_f32_16x16x32_bf16 v[216:219], v[80:83], v[224:227], v[216:219]\n\t" \
    "ds_read_b128 v[236:239], v253 offset:7168\n\t" \
    "s_add_u32 s42, s42, s45\n\t" \
    "s_waitcnt lgkmcnt(2)\n\t" \
    "v_mfma_f32_16x16x32_bf16 v[216:219], v[84:87], v[228:231], v[216:219]\n\t" \
    "ds_read_b128 v[240:243], v253 offset:26624\n\t" \
    "v_mfma_f32_16x16x32_bf16 v[220:223], v[88:91], v[224:227], v[220:223]\n\t" \
    "v_mfma_f32_16x16x32_bf16 v[220:223], v[92:95], v[228:231], v[220:223]\n\t" \
    "v_mfma_f32_16x16x32_bf16 v[212:215], v[244:247], v[224:227], v[212:215]\n\t" \
    "ds_read_b128 v[244:247], v254 offset:36864\n\t" \
    "v_mfma_f32_16x16x32_bf16 v[212:215], v[248:251], v[228:231], v[212:215]\n\t" \
    "ds_read_b128 v[248:251], v254 offset:37888\n\t" \
    "s_waitcnt lgkmcnt(4)\n\t" \
    "v_mfma_f32_16x16x32_bf16 v[204:207], v[120:123], v[232:235], v[204:207]\n\t" \
    "ds_read_b128 v[224:227], v253 offset:27648\n\t" \
    "s_waitcnt lgkmcnt(4)\n\t" \
    "v_mfma_f32_16x16x32_bf16 v[204:207], v[124:127], v[236:239], v[204:207]\n\t" \
    "ds_read_b128 v[228:231], v253 offset:8192\n\t" \
    "v_mfma_f32_16x16x32_bf16 v[208:211], v[128:131], v[232:235], v[208:211]\n\t" \
    "v_mfma_f32_16x16x32_bf16 v[208:211], v[132:135], v[236:239], v[208:211]\n\t" \
    "s_waitcnt lgkmcnt(3)\n\t" \
    "v_mfma_f32_16x16x32_bf16 v[200:203], v[244:247], v[232:235], v[200:203]\n\t" \
    "s_waitcnt lgkmcnt(2)\n\t" \
    "v_mfma_f32_16x16x32_bf16 v[200:203], v[248:251], v[236:239], v[200:203]\n\t" \
    "v_mfma_f32_16x16x32_bf16 v[216:219], v[120:123], v[240:243], v[216:219]\n\t" \
    "ds_read_b128 v[232:235], v253 offset:9216\n\t" \
    "s_waitcnt lgkmcnt(2)\n\t" \
    "v_mfma_f32_16x16x32_bf16 v[216:219], v[124:127], v[224:227], v[216:219]\n\t" \
    "ds_read_b128 v[236:239], v253 offset:28672\n\t" \
    "v_mfma_f32_16x16x32_bf16 v[220:223], v[128:131], v[240:243], v[220:223]\n\t" \
    "v_mfma_f32_16x16x32_bf16 v[220:223], v[132:135], v[224:227], v[220:223]\n\t" \
    "v_mfma_f32_16x16x32_bf16 v[212:215], v[244:247], v[240:243], v[212:215]\n\t" \
    "ds_read_b128 v[244:247], v254 offset:38912\n\t" \
    "v_mfma_f32_16x16x32_bf16 v[212:215], v[248:251], v[224:227], v[212:215]\n\t" \
    "ds_read_b128 v[248:251], v254 offset:39936\n\t" \
    "s_waitcnt lgkmcnt(4)\n\t" \
    "v_mfma_f32_16x16x32_bf16 v[204:207], v[160:163], v[228:231], v[204:207]\n\t" \
    "ds_read_b128 v[240:243], v253 offset:29696\n\t" \
    "s_waitcnt lgkmcnt(4)\n\t" \
    "v_mfma_f32_16x16x32_bf16 v[204:207], v[164:167], v[232:235], v[204:207]\n\t" \
    "ds_read_b128 v[224:227], v253 offset:10240\n\t" \
    "v_mfma_f32_16x16x32_bf16 v[208:211], v[168:171], v[228:231], v[208:211]\n\t" \
    "v_mfma_f32_16x16x32_bf16 v[208:211], v[172:175], v[232:235], v[208:211]\n\t" \
    "s_waitcnt lgkmcnt(3)\n\t" \
    "v_mfma_f32_16x16x32_bf16 v[200:203], v[244:247], v[228:231], v[200:203]\n\t" \
    "s_waitcnt lgkmcnt(2)\n\t" \
    "v_mfma_f32_16x16x32_bf16 v[200:203], v[248:251], v[232:235], v[200:203]\n\t" \
    "v_mfma_f32_16x16x32_bf16 v[216:219], v[160:163], v[236:239], v[216:219]\n\t" \
    "ds_read_b128 v[228:231], v253 offset:11264\n\t" \
    "s_waitcnt lgkmcnt(2)\n\t" \
    "v_mfma_f32_16x16x32_bf16 v[216:219], v[164:167], v[240:243], v[216:219]\n\t" \
    "ds_read_b128 v[232:235], v253 offset:30720\n\t" \
    "v_mfma_f32_16x16x32_bf16 v[220:223], v[168:171], v[236:239], v[220:223]\n\t" \
    "v_mfma_f32_16x16x32_bf16 v[220:223], v[172:175], v[240:243], v[220:223]\n\t" \
    "v_mfma_f32_16x16x32_bf16 v[212:215], v[244:247], v[236:239], v[212:215]\n\t" \
    "v_mfma_f32_16x16x32_bf16 v[212:215], v[248:251], v[240:243], v[212:215]\n\t" \
    "s_waitcnt lgkmcnt(2)\n\t" \
    "v_mfma_f32_16x16x32_bf16 v[204:207], v[24:27], v[224:227], v[204:207]\n\t" \
    "ds_read_b128 v[236:239], v253 offset:31744\n\t" \
    "s_and_b32 s44, s40, 3\n\t" \
    "s_waitcnt lgkmcnt(2)\n\t" \
    "v_mfma_f32_16x16x32_bf16 v[204:207], v[28:31], v[228:231], v[204:207]\n\t" \
    "ds_read_b128 v[240:243], v253 offset:12288\n\t" \
    "s_lshl_b32 s44, s44, 17\n\t" \
    "v_mfma_f32_16x16x32_bf16 v[208:211], v[32:35], v[224:227], v[208:211]\n\t" \
    "s_add_u32 s44, s44, %[mb]\n\t" \
    "v_mfma_f32_16x16x32_bf16 v[208:211], v[36:39], v[228:231], v[208:211]\n\t" \
    "s_add_u32 s46, s44, 0x0\n\t" \
    "v_mfma_f32_16x16x32_bf16 v[200:203], v[16:19], v[224:227], v[200:203]\n\t" \
    "s_add_u32 s47, s44, 0x1400\n\t" \
    "v_mfma_f32_16x16x32_bf16 v[200:203], v[20:23], v[228:231], v[200:203]\n\t" \
    "s_add_u32 s48, s44, 0x400\n\t" \
    "s_waitcnt lgkmcnt(2)\n\t" \
    "v_mfma_f32_16x16x32_bf16 v[216:219], v[24:27], v[232:235], v[216:219]\n\t" \
    "ds_read_b128 v[224:227], v253 offset:13312\n\t" \
    "s_waitcnt lgkmcnt(2)\n\t" \
    "v_mfma_f32_16x16x32_bf16 v[216:219], v[28:31], v[236:239], v[216:219]\n\t" \
    "ds_read_b128 v[228:231], v253 offset:32768\n\t" \
    "v_mfma_f32_16x16x32_bf16 v[220:223], v[32:35], v[232:235], v[220:223]\n\t" \
    "v_mfma_f32_16x16x32_bf16 v[220:223], v[36:39], v[236:239], v[220:223]\n\t" \
    "v_mfma_f32_16x16x32_bf16 v[212:215], v[16:19], v[232:235], v[212:215]\n\t" \
    "v_mfma_f32_16x16x32_bf16 v[212:215], v[20:23], v[236:239], v[212:215]\n\t" \
    "s_waitcnt lgkmcnt(2)\n\t" \
    "v_mfma_f32_16x16x32_bf16 v[204:207], v[64:67], v[240:243], v[204:207]\n\t" \
    "ds_read_b128 v[232:235], v253 offset:33792\n\t" \
    "s_waitcnt lgkmcnt(2)\n\t" \
    "v_mfma_f32_16x16x32_bf16 v[204:207], v[68:71], v[224:227], v[204:207]\n\t" \
    "ds_read_b128 v[236:239], v253 offset:14336\n\t" \
    "v_mfma_f32_16x16x32_bf16 v[208:211], v[72:75], v[240:243], v[208:211]\n\t" \
    "v_mfma_f32_16x16x32_bf16 v[208:211], v[76:79], v[224:227], v[208:211]\n\t" \
    "v_mfma_f32_16x16x32_bf16 v[200:203], v[56:59], v[240:243], v[200:203]\n\t" \
    "v_mfma_f32_16x16x32_bf16 v[200:203], v[60:63], v[224:227], v[200:203]\n\t" \
    "s_waitcnt lgkmcnt(2)\n\t" \
    "v_mfma_f32_16x16x32_bf16 v[216:219], v[64:67], v[228:231], v[216:219]\n\t" \
    "ds_read_b128 v[240:243], v253 offset:15360\n\t" \
    "s_waitcnt lgkmcnt(2)\n\t" \
    "v_mfma_f32_16x16x32_bf16 v[216:219], v[68:71], v[232:235], v[216:219]\n\t" \
    "ds_read_b128 v[224:227], v253 offset:34816\n\t" \
    "v_mfma_f32_16x16x32_bf16 v[220:223], v[72:75], v[228:231], v[220:223]\n\t" \
    "v_mfma_f32_16x16x32_bf16 v[220:223], v[76:79], v[232:235], v[220:223]\n\t" \
    "v_mfma_f32_16x16x32_bf16 v[212:215], v[56:59], v[228:231], v[212:215]\n\t" \
    "v_mfma_f32_16x16x32_bf16 v[212:215], v[60:63], v[232:235], v[212:215]\n\t" \
    "v_and_b32 v252, 0xf0, v253\n\t" \
    "v_lshlrev_b32 v252, 3, v252\n\t" \
    "buffer_load_dword v252, v252, s[60:63], %[fl] offen sc1\n\t" \
    "s_waitcnt lgkmcnt(2)\n\t" \
    "v_mfma_f32_16x16x32_bf16 v[204:207], v[104:107], v[236:239], v[204:207]\n\t" \
    "ds_read_b128 v[228:231], v253 offset:35840\n\t" \
    "s_waitcnt lgkmcnt(2)\n\t" \
    "v_mfma_f32_16x16x32_bf16 v[204:207], v[108:111], v[240:243], v[204:207]\n\t" \
    "ds_read_b128 v[232:235], v253 offset:16384\n\t" \
    "v_mfma_f32_16x16x32_bf16 v[208:211], v[112:115], v[236:239], v[208:211]\n\t" \
    "v_mfma_f32_16x16x32_bf16 v[208:211], v[116:119], v[240:243], v[208:211]\n\t" \
    "v_mfma_f32_16x16x32_bf16 v[200:203], v[96:99], v[236:239], v[200:203]\n\t" \
    "v_mfma_f32_16x16x32_bf16 v[200:203], v[100:103], v[240:243], v[200:203]\n\t" \
    "s_waitcnt lgkmcnt(2)\n\t" \
    "v_mfma_f32_16x16x32_bf16 v[216:219], v[104:107], v[224:227], v[216:219]\n\t" \
    "ds_read_b128 v[236:239], v253 offset:17408\n\t" \
    "s_waitcnt lgkmcnt(2)\n\t" \
    "v_mfma_f32_16x16x32_bf16 v[216:219], v[108:111], v[228:231], v[216:219]\n\t" \
    "ds_read_b128 v[240:243], v253 offset:18432\n\t" \
    "v_mfma_f32_16x16x32_bf16 v[220:223], v[112:115], v[224:227], v[220:223]\n\t" \
    "v_mfma_f32_16x16x32_bf16 v[220:223], v[116:119], v[228:231], v[220:223]\n\t" \
    "v_mfma_f32_16x16x32_bf16 v[212:215], v[96:99], v[224:227], v[212:215]\n\t" \
    "v_mfma_f32_16x16x32_bf16 v[212:215], v[100:103], v[228:231], v[212:215]\n\t" \
    "s_waitcnt lgkmcnt(2)\n\t" \
    "v_mfma_f32_16x16x32_bf16 v[204:207], v[144:147], v[232:235], v[204:207]\n\t" \
    "ds_read_b128 v[224:227], v253 offset:19456\n\t" \
    "v_mov_b64 v[244:245], 0\n\t" \
    "s_waitcnt lgkmcnt(2)\n\t" \
    "v_mfma_f32_16x16x32_bf16 v[204:207], v[148:151], v[236:239], v[204:207]\n\t" \
    "ds_read_b128 v[228:231], v253 offset:36864\n\t" \
    "v_mov_b64 v[246:247], 0\n\t" \
    "v_mfma_f32_16x16x32_bf16 v[208:211], v[152:155], v[232:235], v[208:211]\n\t" \
    "v_mov_b64 v[248:249], 0\n\t" \
    "v_mfma_f32_16x16x32_bf16 v[208:211], v[156:159], v[236:239], v[208:211]\n\t" \
    "v_mov_b64 v[250:251], 0\n\t" \
    "v_mfma_f32_16x16x32_bf16 v[200:203], v[136:139], v[232:235], v[200:203]\n\t" \
    "v_mfma_f32_16x16x32_bf16 v[200:203], v[140:143], v[236:239], v[200:203]\n\t" \
    "s_waitcnt lgkmcnt(2)\n\t" \
    "v_mfma_f32_16x16x32_bf16 v[204:207], v[184:187], v[240:243], v[204:207]\n\t" \
    "ds_read_b128 v[232:235], v253 offset:37888\n\t" \
    "s_waitcnt lgkmcnt(2)\n\t" \
    "v_mfma_f32_16x16x32_bf16 v[204:207], v[188:191], v[224:227], v[204:207]\n\t" \
    "ds_read_b128 v[236:239], v253 offset:38912\n\t" \
    "v_mfma_f32_16x16x32_bf16 v[208:211], v[192:195], v[240:243], v[208:211]\n\t" \
    "v_mfma_f32_16x16x32_bf16 v[208:211], v[196:199], v[224:227], v[208:211]\n\t" \
    "v_mfma_f32_16x16x32_bf16 v[200:203], v[176:179], v[240:243], v[200:203]\n\t" \
    "v_mfma_f32_16x16x32_bf16 v[200:203], v[180:183], v[224:227], v[200:203]\n\t" \
    "ds_read_b128 v[240:243], v253 offset:39936\n\t" \
    "s_waitcnt lgkmcnt(3)\n\t" \
    "v_mfma_f32_16x16x32_bf16 v[216:219], v[144:147], v[228:231], v[216:219]\n\t" \
    "s_waitcnt lgkmcnt(2)\n\t" \
    "v_mfma_f32_16x16x32_bf16 v[216:219], v[148:151], v[232:235], v[216:219]\n\t" \
    "v_cvt_pk_bf16_f32 v204, v204, v205\n\t" \
    "v_cvt_pk_bf16_f32 v205, v206, v207\n\t" \
    "s_waitcnt lgkmcnt(1)\n\t" \
    "v_mfma_f32_16x16x32_bf16 v[216:219], v[184:187], v[236:239], v[216:219]\n\t" \
    "ds_write_b64 v254, v[204:205] offset:7168\n\t" \
    "v_cvt_pk_bf16_f32 v208, v208, v209\n\t" \
    "s_waitcnt lgkmcnt(1)\n\t" \
    "v_mfma_f32_16x16x32_bf16 v[216:219], v[188:191], v[240:243], v[216:219]\n\t" \
    "v_cvt_pk_bf16_f32 v209, v210, v211\n\t" \
    "ds_write_b64 v254, v[208:209] offset:8192\n\t" \
    "v_mfma_f32_16x16x32_bf16 v[220:223], v[152:155], v[228:231], v[220:223]\n\t" \
    "v_cvt_pk_bf16_f32 v200, v200, v201\n\t" \
    "v_cvt_pk_bf16_f32 v201, v202, v203\n\t" \
    "v_mfma_f32_16x16x32_bf16 v[220:223], v[156:159], v[232:235], v[220:223]\n\t" \
    "ds_write_b64 v254, v[200:201] offset:6144\n\t" \
    "ds_read_b128 v[200:203], v254 offset:30720\n\t" \
    "v_mfma_f32_16x16x32_bf16 v[220:223], v[192:195], v[236:239], v[220:223]\n\t" \
    "ds_read_b128 v[204:207], v254 offset:31744\n\t" \
    "v_mov_b64 v[208:209], 0\n\t" \
    "v_mfma_f32_16x16x32_bf16 v[220:223], v[196:199], v[240:243], v[220:223]\n\t" \
    "v_mov_b64 v[210:211], 0\n\t" \
    "v_cvt_pk_bf16_f32 v216, v216, v217\n\t" \
    "v_mfma_f32_16x16x32_bf16 v[212:215], v[136:139], v[228:231], v[212:215]\n\t" \
    "v_cvt_pk_bf16_f32 v217, v218, v219\n\t" \
    "ds_write_b64 v254, v[216:217] offset:13312\n\t" \
    "v_mfma_f32_16x16x32_bf16 v[212:215], v[140:143], v[232:235], v[212:215]\n\t" \
    "v_mfma_f32_16x16x32_bf16 v[212:215], v[176:179], v[236:239], v[212:215]\n\t" \
    "v_cvt_pk_bf16_f32 v220, v220, v221\n\t" \
    "v_cvt_pk_bf16_f32 v221, v222, v223\n\t" \
    "v_mfma_f32_16x16x32_bf16 v[212:215], v[180:183], v[240:243], v[212:215]\n\t" \
    "ds_write_b64 v254, v[220:221] offset:14336\n\t" \
    "ds_read_b128 v[216:219], v254 offset:32768\n\t" \
    "ds_read_b128 v[220:223], v254 offset:33792\n\t" \
    "s_nop 4\n\t" \
    "v_cvt_pk_bf16_f32 v212, v212, v213\n\t" \
    "v_cvt_pk_bf16_f32 v213, v214, v215\n\t" \
    "ds_write_b64 v254, v[212:213] offset:12288\n\t" \
    LST6 \
    "s_waitcnt vmcnt(0) lgkmcnt(0)\n\t" \
    "s_barrier \n\t" \
    "s_setprio 2\n\t" \
    "s_add_u32 s44, s40, 1\n\t" \
    "s_min_u32 s44, s44, 63\n\t" \
    ".Lv08_%=:\n\t" \
    "v_cmp_ge_u32 vcc, s44, v252\n\t" \
    "s_cbranch_vccz .Lv09_%=\n\t" \
    "s_cmp_eq_u32 s41, 0\n\t" \
    "s_cbranch_scc1 .Lv09_%=\n\t" \
    "s_sub_u32 s41, s41, 1\n\t" \
    "s_sleep 2\n\t" \
    "v_and_b32 v252, 0xf0, v253\n\t" \
    "v_lshlrev_b32 v252, 3, v252\n\t" \
    "buffer_load_dword v252, v252, s[60:63], %[fl] offen sc1\n\t" \
    "s_waitcnt vmcnt(0)\n\t" \
    "s_branch .Lv08_%=\n\t" \
    ".Lv09_%=:\n\t" \
    "s_movk_i32 s45, 0x0\n\t" \
    "s_mov_b32 m0, s45\n\t" \
    "s_nop 0\n\t" \
    "buffer_load_dwordx4 v253, s[60:63], s42 offen offset:0 sc1 lds\n\t" \
    "buffer_load_dwordx4 v253, s[60:63], s42 offen offset:1024 sc1 lds\n\t" \
    "buffer_load_dwordx4 v253, s[60:63], s42 offen offset:2048 sc1 lds\n\t" \
    "buffer_load_dwordx4 v253, s[60:63], s42 offen offset:3072 sc1 lds\n\t" \
    "s_add_u32 s44, s42, 0x1000\n\t" \
    "s_movk_i32 s45, 0x5000\n\t" \
    "s_mov_b32 m0, s45\n\t" \
    "s_nop 0\n\t" \
    "buffer_load_dwordx4 v253, s[60:63], s44 offen offset:0 sc1 lds\n\t" \
    "buffer_load_dwordx4 v253, s[60:63], s44 offen offset:1024 sc1 lds\n\t" \
    "buffer_load_dwordx4 v253, s[60:63], s44 offen offset:2048 sc1 lds\n\t" \
    "buffer_load_dwordx4 v253, s[60:63], s44 offen offset:3072 sc1 lds\n\t" \
    "s_add_u32 s44, s42, 0x10000\n\t" \
    "s_mov_b32 s45, 0x4000\n\t" \
    "s_mov_b32 m0, s45\n\t" \
    "s_nop 0\n\t" \
    "buffer_load_dwordx4 v253, s[60:63], s44 offen offset:0 sc1 lds\n\t" \
    "s_add_u32 s44, s44, 0x1000\n\t" \
    "s_mov_b32 s45, 0x9000\n\t" \
    "s_mov_b32 m0, s45\n\t" \
    "s_nop 0\n\t" \
    "buffer_load_dwordx4 v253, s[60:63], s44 offen offset:0 sc1 lds\n\t" \
    LST1 \
    "s_sleep 18\n\t" \
    "buffer_load_dwordx4 v[224:227], v253, s[60:63], s46 offen sc1\n\t" \
    "buffer_load_dwordx4 v[228:231], v253, s[60:63], s47 offen sc1\n\t" \
    "buffer_load_dwordx4 v[232:235], v253, s[60:63], s48 offen sc1\n\t" \
    "s_waitcnt lgkmcnt(0)\n\t" \
    LFLUSH212 \
    "v_bfe_i32 v212, v253, 5, 1\n\t" \
    "v_and_b32 v212, 0xaaaa, v212\n\t" \
    "v_or_b32 v212, 0x4444, v212\n\t" \
    "v_and_b32 v213, 0xffff, v255\n\t" \
    "v_lshrrev_b32 v214, 16, v255\n\t" \
    ".Lv010_%=:\n\t" \
    "s_waitcnt vmcnt(2)\n\t" \
    "v_max_u32 v215, v224, v225\n\t" \
    "v_max3_u32 v215, v226, v227, v215\n\t" \
    "s_waitcnt vmcnt(1)\n\t" \
    "v_max3_u32 v215, v228, v229, v215\n\t" \
    "v_max3_u32 v215, v230, v231, v215\n\t" \
    "s_waitcnt vmcnt(0)\n\t" \
    "v_max3_u32 v215, v232, v233, v215\n\t" \
    "v_max3_u32 v215, v234, v235, v215\n\t" \
    "v_cmp_eq_u32 vcc, -1, v215\n\t" \
    "s_cbranch_vccz .Lv011_%=\n\t" \
    "v_max_u32 v215, v224, v225\n\t" \
    "v_max3_u32 v215, v226, v227, v215\n\t" \
    "v_cmp_eq_u32 vcc, -1, v215\n\t" \
    "s_cbranch_vccz .Lv012_%=\n\t" \
    "buffer_load_dwordx4 v[224:227], v253, s[60:63], s46 offen sc1\n\t" \
    ".Lv012_%=:\n\t" \
    "v_max_u32 v215, v228, v229\n\t" \
    "v_max3_u32 v215, v230, v231, v215\n\t" \
    "v_cmp_eq_u32 vcc, -1, v215\n\t" \
    "s_cbranch_vccz .Lv013_%=\n\t" \
    "buffer_load_dwordx4 v[228:231], v253, s[60:63], s47 offen sc1\n\t" \
    ".Lv013_%=:\n\t" \
    "v_max_u32 v215, v232, v233\n\t" \
    "v_max3_u32 v215, v234, v235, v215\n\t" \
    "v_cmp_eq_u32 vcc, -1, v215\n\t" \
    "s_cbranch_vccz .Lv014_%=\n\t" \
    "buffer_load_dwordx4 v[232:235], v253, s[60:63], s48 offen sc1\n\t" \
    ".Lv014_%=:\n\t" \
    "s_cmp_eq_u32 s41, 0\n\t" \
    "s_cbranch_scc1 .Lv011_%=\n\t" \
    "s_sub_u32 s41, s41, 1\n\t" \
    "s_waitcnt vmcnt(0)\n\t" \
    "s_branch .Lv010_%=\n\t" \
    ".Lv011_%=:\n\t" \
    LST2 \
    "ds_write_b128 v253, v[224:227] offset:40960\n\t" \
    "ds_write_b128 v253, v[228:231] offset:46080\n\t" \
    "ds_write_b128 v253, v[232:235] offset:41984\n\t" \
    "v_smfmac_f32_16x16x64_bf16 v[244:247], v[224:227], v[0:7], v212\n\t" \
    "v_smfmac_f32_16x16x64_bf16 v[248:251], v[224:227], v[8:15], v212\n\t" \
    "v_smfmac_f32_16x16x64_bf16 v[208:211], v[224:227], v[200:207], v212\n\t" \
    "s_waitcnt lgkmcnt(0)\n\t" \
    "s_barrier \n\t" \
    LST3 \
    "ds_read_b128 v[236:239], v253 offset:47104\n\t" \
    "ds_read_b128 v[240:243], v253 offset:43008\n\t" \
    "ds_read_b128 v[224:227], v253 offset:48128\n\t" \
    "ds_read_b128 v[200:203], v254 offset:34816\n\t" \
    "ds_read_b128 v[204:207], v254 offset:35840\n\t" \
    "v_smfmac_f32_16x16x64_bf16 v[208:211], v[228:231], v[16:23], v212\n\t" \
    "v_smfmac_f32_16x16x64_bf16 v[244:247], v[228:231], v[24:31], v212\n\t" \
    "v_smfmac_f32_16x16x64_bf16 v[248:251], v[228:231], v[32:39], v212\n\t" \
    "ds_read_b128 v[228:231], v253 offset:44032\n\t" \
    "v_smfmac_f32_16x16x64_bf16 v[208:211], v[232:235], v[216:223], v212\n\t" \
    "ds_read_b128 v[216:219], v254 offset:36864\n\t" \
    "ds_read_b128 v[220:223], v254 offset:37888\n\t" \
    "v_smfmac_f32_16x16x64_bf16 v[244:247], v[232:235], v[40:47], v212\n\t" \
    "v_smfmac_f32_16x16x64_bf16 v[248:251], v[232:235], v[48:55], v212\n\t" \
    "ds_read_b128 v[232:235], v253 offset:49152\n\t" \
    "s_waitcnt lgkmcnt(8)\n\t" \
    "v_smfmac_f32_16x16x64_bf16 v[208:211], v[236:239], v[56:63], v212\n\t" \
    "v_smfmac_f32_16x16x64_bf16 v[244:247], v[236:239], v[64:71], v212\n\t" \
    "v_smfmac_f32_16x16x64_bf16 v[248:251], v[236:239], v[72:79], v212\n\t" \
    "ds_read_b128 v[236:239], v253 offset:45056\n\t" \
    "s_waitcnt lgkmcnt(5)\n\t" \
    "v_smfmac_f32_16x16x64_bf16 v[208:211], v[240:243], v[200:207], v212\n\t" \
    "ds_read_b128 v[200:203], v254 offset:38912\n\t" \
    "ds_read_b128 v[204:207], v254 offset:39936\n\t" \
    "v_smfmac_f32_16x16x64_bf16 v[244:247], v[240:243], v[80:87], v212\n\t" \
    "v_smfmac_f32_16x16x64_bf16 v[248:251], v[240:243], v[88:95], v212\n\t" \
    "ds_read_b128 v[240:243], v253 offset:50176\n\t" \
    "v_smfmac_f32_16x16x64_bf16 v[208:211], v[224:227], v[96:103], v212\n\t" \
    "v_smfmac_f32_16x16x64_bf16 v[244:247], v[224:227], v[104:111], v212\n\t" \
    "v_smfmac_f32_16x16x64_bf16 v[248:251], v[224:227], v[112:119], v212\n\t" \
    "s_waitcnt lgkmcnt(7)\n\t" \
    "v_smfmac_f32_16x16x64_bf16 v[244:247], v[228:231], v[120:127], v212\n\t" \
    "s_waitcnt lgkmcnt(4)\n\t" \
    "v_smfmac_f32_16x16x64_bf16 v[244:247], v[232:235], v[144:151], v212\n\t" \
    "s_waitcnt lgkmcnt(3)\n\t" \
    "v_smfmac_f32_16x16x64_bf16 v[244:247], v[236:239], v[160:167], v212\n\t" \
    "s_waitcnt lgkmcnt(0)\n\t" \
    "v_smfmac_f32_16x16x64_bf16 v[244:247], v[240:243], v[184:191], v212\n\t" \
    "v_smfmac_f32_16x16x64_bf16 v[248:251], v[228:231], v[128:135], v212\n\t" \
    "v_smfmac_f32_16x16x64_bf16 v[248:251], v[232:235], v[152:159], v212\n\t" \
    "v_smfmac_f32_16x16x64_bf16 v[248:251], v[236:239], v[168:175], v212\n\t" \
    "v_add_f32 v244, v244, v246\n\t" \
    "v_add_f32 v245, v245, v247\n\t" \
    "v_cvt_pk_bf16_f32 v244, v244, v245\n\t" \
    "v_smfmac_f32_16x16x64_bf16 v[248:251], v[240:243], v[192:199], v212\n\t" \
    "ds_write_b16 v213, v244 offset:512\n\t" \
    "ds_write_b16_d16_hi v213, v244 offset:528\n\t" \
    "v_smfmac_f32_16x16x64_bf16 v[208:211], v[228:231], v[216:223], v212\n\t" \
    "v_smfmac_f32_16x16x64_bf16 v[208:211], v[232:235], v[136:143], v212\n\t" \
    "v_smfmac_f32_16x16x64_bf16 v[208:211], v[236:239], v[200:207], v212\n\t" \
    "v_add_f32 v248, v248, v250\n\t" \
    "v_add_f32 v249, v249, v251\n\t" \
    "v_cvt_pk_bf16_f32 v248, v248, v249\n\t" \
    "v_smfmac_f32_16x16x64_bf16 v[208:211], v[240:243], v[176:183], v212\n\t" \
    "ds_write_b16 v213, v248 offset:1024\n\t" \
    "ds_write_b16_d16_hi v213, v248 offset:1040\n\t" \
    "s_nop 5\n\t" \
    "v_add_f32 v208, v208, v210\n\t" \
    "v_add_f32 v209, v209, v211\n\t" \
    "v_cvt_pk_bf16_f32 v208, v208, v209\n\t" \
    "ds_write_b16 v213, v208 offset:0\n\t" \
    "ds_write_b16_d16_hi v213, v208 offset:16\n\t" \
    "s_setprio 0\n\t" \
    "s_waitcnt lgkmcnt(0)\n\t" \
    "s_barrier \n\t" \
    LST4 \
    "ds_read_b128 v[224:227], v253 offset:0\n\t" \
    "ds_read_b128 v[244:247], v254 offset:30720\n\t" \
    "ds_read_b128 v[228:231], v253 offset:1024\n\t" \
    "ds_read_b128 v[232:235], v253 offset:20480\n\t" \
    "ds_read_b128 v[236:239], v253 offset:21504\n\t" \
    "ds_read_b128 v[248:251], v254 offset:31744\n\t" \
    "s_add_u32 s40, s40, 1\n\t" \
    "s_cmp_lg_u32 s40, 64\n\t" \
    "s_cbranch_scc1 .Lv07_%=\n\t" \
    "s_branch .Lend%=\n\t" \
    ".Lend%=:\n\t" \
    "s_waitcnt vmcnt(0) lgkmcnt(0)\n\t" \
    "s_mov_b32 %[bud], s41\n\t"
#define ASM_RCRIT \
    "s_movk_i32 %[n], 0x400\n\t" \
    ".Lfa%=:\n\t" \
    "v_max_u32 v196, v180, v181\n\t" \
    "v_max3_u32 v196, v182, v183, v196\n\t" \
    "v_cmp_eq_u32 vcc, -1, v196\n\t" \
    "s_cbranch_vccz .Lha%=\n\t" \
    "buffer_load_dwordx4 v[180:183], %[ov], %[rs], %[sv] offen sc1\n\t" \
    "s_waitcnt vmcnt(12)\n\t" \
    "v_max_u32 v196, v180, v181\n\t" \
    "v_max3_u32 v196, v182, v183, v196\n\t" \
    "v_cmp_eq_u32 vcc, -1, v196\n\t" \
    "s_cbranch_vccz .Lha%=\n\t" \
    "s_nop 7\n\t" \
    "v_max_u32 v196, v180, v181\n\t" \
    "v_max3_u32 v196, v182, v183, v196\n\t" \
    "v_cmp_eq_u32 vcc, -1, v196\n\t" \
    "s_cbranch_vccz .Lha%=\n\t" \
    "s_nop 7\n\t" \
    "v_max_u32 v196, v180, v181\n\t" \
    "v_max3_u32 v196, v182, v183, v196\n\t" \
    "v_cmp_eq_u32 vcc, -1, v196\n\t" \
    "s_cbranch_vccz .Lha%=\n\t" \
    "s_nop 7\n\t" \
    "v_max_u32 v196, v180, v181\n\t" \
    "v_max3_u32 v196, v182, v183, v196\n\t" \
    "v_cmp_eq_u32 vcc, -1, v196\n\t" \
    "s_cbranch_vccz .Lha%=\n\t" \
    "s_nop 7\n\t" \
    "s_sub_u32 %[n], %[n], 1\n\t" \
    "s_cmp_lg_u32 %[n], 0\n\t" \
    "s_cbranch_scc1 .Lfa%=\n\t" \
    ".Lha%=:\n\t" \
    "s_cmp_eq_u32 %[n], 0\n\t" \
    "s_cbranch_scc1 .Lre%=\n\t" \
    ASM_ST3 \
    ".Lfb%=:\n\t" \
    "s_mov_b32 %[a], 0\n\t" \
    "v_max_u32 v196, v184, v185\n\t" \
    "v_max3_u32 v196, v186, v187, v196\n\t" \
    "v_cmp_eq_u32 vcc, -1, v196\n\t" \
    "s_cbranch_vccz .Lgb0_%=\n\t" \
    "buffer_load_dwordx4 v[184:187], %[o0], %[rs], %[sp] offen sc1\n\t" \
    "s_mov_b32 %[a], 1\n\t" \
    ".Lgb0_%=:\n\t" \
    "v_max_u32 v196, v188, v189\n\t" \
    "v_max3_u32 v196, v190, v191, v196\n\t" \
    "v_cmp_eq_u32 vcc, -1, v196\n\t" \
    "s_cbranch_vccz .Lgb1_%=\n\t" \
    "buffer_load_dwordx4 v[188:191], %[o1], %[rs], %[sp] offen sc1\n\t" \
    "s_mov_b32 %[a], 1\n\t" \
    ".Lgb1_%=:\n\t" \
    "v_max_u32 v196, v192, v193\n\t" \
    "v_max3_u32 v196, v194, v195, v196\n\t" \
    "v_cmp_eq_u32 vcc, -1, v196\n\t" \
    "s_cbranch_vccz .Lgb2_%=\n\t" \
    "buffer_load_dwordx4 v[192:195], %[o2], %[rs], %[sp] offen sc1\n\t" \
    "s_mov_b32 %[a], 1\n\t" \
    ".Lgb2_%=:\n\t" \
    "s_cmp_eq_u32 %[a], 0\n\t" \
    "s_cbranch_scc1 .Lhb%=\n\t" \
    "s_waitcnt vmcnt(12)\n\t" \
    "v_max_u32 v196, v184, v185\n\t" \
    "v_max3_u32 v196, v186, v187, v196\n\t" \
    "v_max3_u32 v196, v188, v189, v196\n\t" \
    "v_max3_u32 v196, v190, v191, v196\n\t" \
    "v_max3_u32 v196, v192, v193, v196\n\t" \
    "v_max3_u32 v196, v194, v195, v196\n\t" \
    "v_cmp_eq_u32 vcc, -1, v196\n\t" \
    "s_cbranch_vccz .Lhb%=\n\t" \
    "s_nop 7\n\t" \
    "v_max_u32 v196, v184, v185\n\t" \
    "v_max3_u32 v196, v186, v187, v196\n\t" \
    "v_max3_u32 v196, v188, v189, v196\n\t" \
    "v_max3_u32 v196, v190, v191, v196\n\t" \
    "v_max3_u32 v196, v192, v193, v196\n\t" \
    "v_max3_u32 v196, v194, v195, v196\n\t" \
    "v_cmp_eq_u32 vcc, -1, v196\n\t" \
    "s_cbranch_vccz .Lhb%=\n\t" \
    "s_nop 7\n\t" \
    "v_max_u32 v196, v184, v185\n\t" \
    "v_max3_u32 v196, v186, v187, v196\n\t" \
    "v_max3_u32 v196, v188, v189, v196\n\t" \
    "v_max3_u32 v196, v190, v191, v196\n\t" \
    "v_max3_u32 v196, v192, v193, v196\n\t" \
    "v_max3_u32 v196, v194, v195, v196\n\t" \
    "v_cmp_eq_u32 vcc, -1, v196\n\t" \
    "s_cbranch_vccz .Lhb%=\n\t" \
    "s_nop 7\n\t" \
    "v_max_u32 v196, v184, v185\n\t" \
    "v_max3_u32 v196, v186, v187, v196\n\t" \
    "v_max3_u32 v196, v188, v189, v196\n\t" \
    "v_max3_u32 v196, v190, v191, v196\n\t" \
    "v_max3_u32 v196, v192, v193, v196\n\t" \
    "v_max3_u32 v196, v194, v195, v196\n\t" \
    "v_cmp_eq_u32 vcc, -1, v196\n\t" \
    "s_cbranch_vccz .Lhb%=\n\t" \
    "s_nop 7\n\t" \
    "v_max_u32 v196, v184, v185\n\t" \
    "v_max3_u32 v196, v186, v187, v196\n\t" \
    "v_max3_u32 v196, v188, v189, v196\n\t" \
    "v_max3_u32 v196, v190, v191, v196\n\t" \
    "v_max3_u32 v196, v192, v193, v196\n\t" \
    "v_max3_u32 v196, v194, v195, v196\n\t" \
    "v_cmp_eq_u32 vcc, -1, v196\n\t" \
    "s_cbranch_vccz .Lhb%=\n\t" \
    "s_nop 7\n\t" \
    "v_max_u32 v196, v184, v185\n\t" \
    "v_max3_u32 v196, v186, v187, v196\n\t" \
    "v_max3_u32 v196, v188, v189, v196\n\t" \
    "v_max3_u32 v196, v190, v191, v196\n\t" \
    "v_max3_u32 v196, v192, v193, v196\n\t" \
    "v_max3_u32 v196, v194, v195, v196\n\t" \
    "v_cmp_eq_u32 vcc, -1, v196\n\t" \
    "s_cbranch_vccz .Lhb%=\n\t" \
    "s_nop 7\n\t" \
    "v_max_u32 v196, v184, v185\n\t" \
    "v_max3_u32 v196, v186, v187, v196\n\t" \
    "v_max3_u32 v196, v188, v189, v196\n\t" \
    "v_max3_u32 v196, v190, v191, v196\n\t" \
    "v_max3_u32 v196, v192, v193, v196\n\t" \
    "v_max3_u32 v196, v194, v195, v196\n\t" \
    "v_cmp_eq_u32 vcc, -1, v196\n\t" \
    "s_cbranch_vccz .Lhb%=\n\t" \
    "s_nop 7\n\t" \
    "v_max_u32 v196, v184, v185\n\t" \
    "v_max3_u32 v196, v186, v187, v196\n\t" \
    "v_max3_u32 v196, v188, v189, v196\n\t" \
    "v_max3_u32 v196, v190, v191, v196\n\t" \
    "v_max3_u32 v196, v192, v193, v196\n\t" \
    "v_max3_u32 v196, v194, v195, v196\n\t" \
    "v_cmp_eq_u32 vcc, -1, v196\n\t" \
    "s_cbranch_vccz .Lhb%=\n\t" \
    "s_nop 7\n\t" \
    "s_sub_u32 %[n], %[n], 1\n\t" \
    "s_cmp_lg_u32 %[n], 0\n\t" \
    "s_cbranch_scc1 .Lfb%=\n\t" \
    "s_branch .Lre%=\n\t" \
    ".Lhb%=:\n\t" \
    ASM_ST2 \
    "v_mfma_f32_16x16x32_bf16 v[176:179], v[192:195], v[180:183], 0\n\t" \
    "v_smfmac_f32_16x16x64_bf16 v[176:179], v[184:187], v[160:167], %[sidx]\n\t" \
    "v_smfmac_f32_16x16x64_bf16 v[176:179], v[188:191], v[168:175], %[sidx]\n\t" \
    "s_cmp_eq_u32 %[pub], 0\n\t" \
    "s_nop 7\n\t" \
    "v_add_f32 v176, v176, v178\n\t" \
    "v_add_f32 v177, v177, v179\n\t" \
    "ds_write2_b32 %[rw], v176, v177 offset1:80\n\t" \
    "s_waitcnt lgkmcnt(0)\n\t" \
    "s_barrier\n\t" ASM_ST \
    "s_cbranch_scc1 .Lre%=\n\t" \
    "ds_read_b128 v[200:203], %[rd] offset:0\n\t" \
    "ds_read_b128 v[204:207], %[rd] offset:16\n\t" \
    "ds_read_b128 v[208:211], %[rd] offset:32\n\t" \
    "ds_read_b128 v[212:215], %[rd] offset:48\n\t" \
    "s_waitcnt lgkmcnt(2)\n\t" \
    "v_pk_add_f32 v[200:201], v[200:201], v[202:203]\n\t" \
    "v_pk_add_f32 v[204:205], v[204:205], v[206:207]\n\t" \
    "s_waitcnt lgkmcnt(0)\n\t" \
    "v_pk_add_f32 v[208:209], v[208:209], v[210:211]\n\t" \
    "v_pk_add_f32 v[212:213], v[212:213], v[214:215]\n\t" \
    "v_pk_add_f32 v[200:201], v[200:201], v[204:205]\n\t" \
    "v_pk_add_f32 v[208:209], v[208:209], v[212:213]\n\t" \
    "v_pk_add_f32 v[200:201], v[200:201], v[208:209]\n\t" \
    "v_cvt_pk_bf16_f32 v200, v200, v201\n\t" \
    "v_min_u32 v200, -2, v200\n\t" \
    "buffer_store_dword v200, %[oo], %[rs], %[so] offen sc1\n\t" \
    ".Lre%=:\n\t" \
    ASM_STW
#define ASM_RX \
    "s_cmp_eq_u32 %[fin], 0\n\t" \
    "s_cbranch_scc1 .Lx0%=\n\t" \
    "buffer_load_dwordx2 v[250:251], %[xo], %[rd], %[sx] offen sc1\n\t" \
    ".Lx0%=:\n\t" \
    "buffer_load_dwordx4 v[200:203], %[l16], %[rs], %[s0] offen offset:0 sc1\n\t" \
    "buffer_load_dwordx4 v[204:207], %[l16], %[rs], %[s0] offen offset:1024 sc1\n\t" \
    "buffer_load_dwordx4 v[208:211], %[l16], %[rs], %[s0] offen offset:2048 sc1\n\t" \
    "buffer_load_dwordx4 v[212:215], %[l16], %[rs], %[s0] offen offset:3072 sc1\n\t" \
    "buffer_load_dwordx4 v[216:219], %[l16], %[rs], %[s1] offen offset:0 sc1\n\t" \
    "buffer_load_dwordx4 v[220:223], %[l16], %[rs], %[s1] offen offset:1024 sc1\n\t" \
    "buffer_load_dwordx4 v[224:227], %[l16], %[rs], %[s1] offen offset:2048 sc1\n\t" \
    "buffer_load_dwordx4 v[228:231], %[l16], %[rs], %[s1] offen offset:3072 sc1\n\t" \
    "buffer_load_dword v248, %[fo], %[rf], %[fs] offen sc1\n\t" \
    "s_mov_b32 %[a], 0x05040100\n\t" \
    "v_perm_b32 v232, v161, v160, %[a]\n\t" \
    "v_perm_b32 v233, v163, v162, %[a]\n\t" \
    "v_perm_b32 v234, v165, v164, %[a]\n\t" \
    "v_perm_b32 v235, v167, v166, %[a]\n\t" \
    "v_perm_b32 v236, v169, v168, %[a]\n\t" \
    "v_perm_b32 v237, v171, v170, %[a]\n\t" \
    "v_perm_b32 v238, v173, v172, %[a]\n\t" \
    "v_perm_b32 v239, v175, v174, %[a]\n\t" \
    "s_movk_i32 %[n], 0x200\n\t" \
    "s_nop 1\n\t" \
    "v_permlane16_swap_b32 v232, v234\n\t" \
    "v_permlane16_swap_b32 v233, v235\n\t" \
    "v_permlane16_swap_b32 v236, v238\n\t" \
    "v_permlane16_swap_b32 v237, v239\n\t" \
    "s_waitcnt vmcnt(8)\n\t" \
    "v_max_u32 v196, v200, v201\n\t" \
    "v_max3_u32 v196, v202, v203, v196\n\t" \
    "s_waitcnt vmcnt(7)\n\t" \
    "v_max3_u32 v196, v204, v205, v196\n\t" \
    "v_max3_u32 v196, v206, v207, v196\n\t" \
    "s_waitcnt vmcnt(6)\n\t" \
    "v_max3_u32 v196, v208, v209, v196\n\t" \
    "v_max3_u32 v196, v210, v211, v196\n\t" \
    "s_waitcnt vmcnt(5)\n\t" \
    "v_max3_u32 v196, v212, v213, v196\n\t" \
    "v_max3_u32 v196, v214, v215, v196\n\t" \
    "s_waitcnt vmcnt(4)\n\t" \
    "v_max3_u32 v196, v216, v217, v196\n\t" \
    "v_max3_u32 v196, v218, v219, v196\n\t" \
    "s_waitcnt vmcnt(3)\n\t" \
    "v_max3_u32 v196, v220, v221, v196\n\t" \
    "v_max3_u32 v196, v222, v223, v196\n\t" \
    "s_waitcnt vmcnt(2)\n\t" \
    "v_max3_u32 v196, v224, v225, v196\n\t" \
    "v_max3_u32 v196, v226, v227, v196\n\t" \
    "s_waitcnt vmcnt(1)\n\t" \
    "v_max3_u32 v196, v228, v229, v196\n\t" \
    "v_max3_u32 v196, v230, v231, v196\n\t" \
    "v_cmp_eq_u32 vcc, -1, v196\n\t" \
    "s_cbranch_vccz .Lxd%=\n\t" \
    ".Lxl%=:\n\t" \
    "s_mov_b32 %[a], 0\n\t" \
    "v_max_u32 v196, v200, v201\n\t" \
    "v_max3_u32 v196, v202, v203, v196\n\t" \
    "v_cmp_eq_u32 vcc, -1, v196\n\t" \
    "s_cbranch_vccz .Lxs0_%=\n\t" \
    "buffer_load_dwordx4 v[200:203], %[l16], %[rs], %[s0] offen offset:0 sc1\n\t" \
    "s_mov_b32 %[a], 1\n\t" \
    ".Lxs0_%=:\n\t" \
    "v_max_u32 v196, v204, v205\n\t" \
    "v_max3_u32 v196, v206, v207, v196\n\t" \
    "v_cmp_eq_u32 vcc, -1, v196\n\t" \
    "s_cbranch_vccz .Lxs1_%=\n\t" \
    "buffer_load_dwordx4 v[204:207], %[l16], %[rs], %[s0] offen offset:1024 sc1\n\t" \
    "s_mov_b32 %[a], 1\n\t" \
    ".Lxs1_%=:\n\t" \
    "v_max_u32 v196, v208, v209\n\t" \
    "v_max3_u32 v196, v210, v211, v196\n\t" \
    "v_cmp_eq_u32 vcc, -1, v196\n\t" \
    "s_cbranch_vccz .Lxs2_%=\n\t" \
    "buffer_load_dwordx4 v[208:211], %[l16], %[rs], %[s0] offen offset:2048 sc1\n\t" \
    "s_mov_b32 %[a], 1\n\t" \
    ".Lxs2_%=:\n\t" \
    "v_max_u32 v196, v212, v213\n\t" \
    "v_max3_u32 v196, v214, v215, v196\n\t" \
    "v_cmp_eq_u32 vcc, -1, v196\n\t" \
    "s_cbranch_vccz .Lxs3_%=\n\t" \
    "buffer_load_dwordx4 v[212:215], %[l16], %[rs], %[s0] offen offset:3072 sc1\n\t" \
    "s_mov_b32 %[a], 1\n\t" \
    ".Lxs3_%=:\n\t" \
    "v_max_u32 v196, v216, v217\n\t" \
    "v_max3_u32 v196, v218, v219, v196\n\t" \
    "v_cmp_eq_u32 vcc, -1, v196\n\t" \
    "s_cbranch_vccz .Lxs4_%=\n\t" \
    "buffer_load_dwordx4 v[216:219], %[l16], %[rs], %[s1] offen offset:0 sc1\n\t" \
    "s_mov_b32 %[a], 1\n\t" \
    ".Lxs4_%=:\n\t" \
    "v_max_u32 v196, v220, v221\n\t" \
    "v_max3_u32 v196, v222, v223, v196\n\t" \
    "v_cmp_eq_u32 vcc, -1, v196\n\t" \
    "s_cbranch_vccz .Lxs5_%=\n\t" \
    "buffer_load_dwordx4 v[220:223], %[l16], %[rs], %[s1] offen offset:1024 sc1\n\t" \
    "s_mov_b32 %[a], 1\n\t" \
    ".Lxs5_%=:\n\t" \
    "v_max_u32 v196, v224, v225\n\t" \
    "v_max3_u32 v196, v226, v227, v196\n\t" \
    "v_cmp_eq_u32 vcc, -1, v196\n\t" \
    "s_cbranch_vccz .Lxs6_%=\n\t" \
    "buffer_load_dwordx4 v[224:227], %[l16], %[rs], %[s1] offen offset:2048 sc1\n\t" \
    "s_mov_b32 %[a], 1\n\t" \
    ".Lxs6_%=:\n\t" \
    "v_max_u32 v196, v228, v229\n\t" \
    "v_max3_u32 v196, v230, v231, v196\n\t" \
    "v_cmp_eq_u32 vcc, -1, v196\n\t" \
    "s_cbranch_vccz .Lxs7_%=\n\t" \
    "buffer_load_dwordx4 v[228:231], %[l16], %[rs], %[s1] offen offset:3072 sc1\n\t" \
    "s_mov_b32 %[a], 1\n\t" \
    ".Lxs7_%=:\n\t" \
    "s_cmp_eq_u32 %[a], 0\n\t" \
    "s_cbranch_scc1 .Lxd%=\n\t" \
    "s_waitcnt vmcnt(0)\n\t" \
    "s_sub_u32 %[n], %[n], 1\n\t" \
    "s_cmp_lg_u32 %[n], 0\n\t" \
    "s_cbranch_scc1 .Lxl%=\n\t" \
    "s_branch .Lxe%=\n\t" \
    ".Lxd%=:\n\t" \
    ASM_ST2 \
    "v_mfma_f32_16x16x32_bf16 v[244:247], v[160:163], v[200:203], 0\n\t" \
    "v_mfma_f32_16x16x32_bf16 v[244:247], v[164:167], v[204:207], v[244:247]\n\t" \
    "v_mfma_f32_16x16x32_bf16 v[244:247], v[168:171], v[208:211], v[244:247]\n\t" \
    "v_mfma_f32_16x16x32_bf16 v[244:247], v[172:175], v[212:215], v[244:247]\n\t" \
    "v_mfma_f32_16x16x32_bf16 v[240:243], v[232:235], v[220:223], 0\n\t" \
    "v_mfma_f32_16x16x32_bf16 v[244:247], v[180:183], v[216:219], v[244:247]\n\t" \
    "v_mfma_f32_16x16x32_bf16 v[240:243], v[236:239], v[224:227], v[240:243]\n\t" \
    "v_mfma_f32_16x16x32_bf16 v[240:243], v[180:183], v[228:231], v[240:243]\n\t" \
    "s_waitcnt vmcnt(0)\n\t" \
    "v_cmp_ge_u32 vcc, %[tn], v248\n\t" \
    "s_mov_b32 %[a], 0\n\t" \
    "s_cbranch_vccnz .Lxv%=\n\t" \
    "buffer_load_dwordx4 v[160:163], %[l16], %[rd], %[sv] offen offset:0 sc1\n\t" \
    "buffer_load_dwordx4 v[164:167], %[l16], %[rd], %[sv] offen offset:1024 sc1\n\t" \
    "buffer_load_dwordx4 v[168:171], %[l16], %[rd], %[sv] offen offset:2048 sc1\n\t" \
    "buffer_load_dwordx4 v[172:175], %[l16], %[rd], %[sv] offen offset:3072 sc1\n\t" \
    "s_mov_b32 %[a], 1\n\t" \
    ".Lxv%=:\n\t" \
    "s_nop 7\n\t" \
    "ds_write_b128 %[pw], v[244:247] offset:8192\n\t" \
    "ds_write_b128 %[pw], v[240:243]\n\t" \
    "s_waitcnt lgkmcnt(0)\n\t" \
    "s_barrier\n\t" ASM_ST \
    "s_cmp_eq_u32 %[fin], 0\n\t" \
    "s_cbranch_scc1 .Lxe%=\n\t" \
    "ds_read_b128 v[200:203], %[pr] offset:0\n\t" \
    "ds_read_b128 v[204:207], %[pr] offset:1024\n\t" \
    "ds_read_b128 v[208:211], %[pr] offset:2048\n\t" \
    "ds_read_b128 v[212:215], %[pr] offset:3072\n\t" \
    "ds_read_b128 v[216:219], %[pr] offset:4096\n\t" \
    "ds_read_b128 v[220:223], %[pr] offset:5120\n\t" \
    "ds_read_b128 v[224:227], %[pr] offset:6144\n\t" \
    "ds_read_b128 v[228:231], %[pr] offset:7168\n\t" \
    "v_lshlrev_b32 v240, 16, v250\n\t" \
    "v_and_b32 v241, 0xffff0000, v250\n\t" \
    "v_lshlrev_b32 v242, 16, v251\n\t" \
    "v_and_b32 v243, 0xffff0000, v251\n\t" \
    "s_waitcnt lgkmcnt(6)\n\t" \
    "v_pk_add_f32 v[200:201], v[200:201], v[204:205]\n\t" \
    "v_pk_add_f32 v[202:203], v[202:203], v[206:207]\n\t" \
    "s_waitcnt lgkmcnt(4)\n\t" \
    "v_pk_add_f32 v[208:209], v[208:209], v[212:213]\n\t" \
    "v_pk_add_f32 v[210:211], v[210:211], v[214:215]\n\t" \
    "v_pk_add_f32 v[200:201], v[200:201], v[208:209]\n\t" \
    "v_pk_add_f32 v[202:203], v[202:203], v[210:211]\n\t" \
    "s_waitcnt lgkmcnt(2)\n\t" \
    "v_pk_add_f32 v[216:217], v[216:217], v[220:221]\n\t" \
    "v_pk_add_f32 v[218:219], v[218:219], v[222:223]\n\t" \
    "s_waitcnt lgkmcnt(0)\n\t" \
    "v_pk_add_f32 v[224:225], v[224:225], v[228:229]\n\t" \
    "v_pk_add_f32 v[226:227], v[226:227], v[230:231]\n\t" \
    "v_pk_add_f32 v[216:217], v[216:217], v[224:225]\n\t" \
    "v_pk_add_f32 v[218:219], v[218:219], v[226:227]\n\t" \
    "v_pk_add_f32 v[200:201], v[200:201], v[216:217]\n\t" \
    "v_pk_add_f32 v[202:203], v[202:203], v[218:219]\n\t" \
    "v_pk_add_f32 v[200:201], v[200:201], v[240:241]\n\t" \
    "v_pk_add_f32 v[202:203], v[202:203], v[242:243]\n\t" \
    "v_cvt_pk_bf16_f32 v200, v200, v201\n\t" \
    "v_cvt_pk_bf16_f32 v201, v202, v203\n\t" \
    "v_min_u32 v200, -2, v200\n\t" \
    "v_min_u32 v201, -2, v201\n\t" \
    "s_add_u32 %[sx], %[sx], 0x1000000\n\t" \
    "buffer_store_dwordx2 v[200:201], %[xo], %[rd], %[sx] offen sc1\n\t" \
    ".Lxe%=:\n\t" \
    ASM_STW
#define ASM_W4_M \
    "buffer_load_dwordx4 v[184:187], %[l16], %[rs], %[s1] offen offset:2048 sc1\n\t" \
    "buffer_load_dwordx4 v[180:183], %[l16], %[rs], %[s1] offen offset:1024 sc1\n\t" \
    "buffer_load_dwordx4 v[176:179], %[l16], %[rs], %[s1] offen offset:0 sc1\n\t" \
    "buffer_load_dwordx4 v[172:175], %[l16], %[rs], %[s0] offen offset:3072 sc1\n\t" \
    "buffer_load_dwordx4 v[168:171], %[l16], %[rs], %[s0] offen offset:2048 sc1\n\t" \
    "buffer_load_dwordx4 v[164:167], %[l16], %[rs], %[s0] offen offset:1024 sc1\n\t" \
    "buffer_load_dwordx4 v[160:163], %[l16], %[rs], %[s0] offen offset:0 sc1\n\t" \
    "v_mfma_f32_16x16x32_bf16 v[224:227], v[28:31], v[188:191], 0\n\t" \
    "v_mfma_f32_16x16x32_bf16 v[228:231], v[60:63], v[188:191], 0\n\t" \
    "v_mfma_f32_16x16x32_bf16 v[232:235], v[92:95], v[188:191], 0\n\t" \
    "v_mfma_f32_16x16x32_bf16 v[236:239], v[124:127], v[188:191], 0\n\t" \
    "v_mfma_f32_16x16x32_bf16 v[240:243], v[156:159], v[188:191], 0\n\t" \
    "s_waitcnt vmcnt(6)\n\t" \
    ".Lc6_%=:\n\t" \
    "v_max_u32 v244, v184, v185\n\t" \
    "v_max3_u32 v244, v186, v187, v244\n\t" \
    "v_cmp_eq_u32 vcc, -1, v244\n\t" \
    "s_cbranch_vccnz .Ls6_%=\n\t" \
    ".Lk6_%=:\n\t" \
    "v_mfma_f32_16x16x32_bf16 v[224:227], v[24:27], v[184:187], v[224:227]\n\t" \
    "v_mfma_f32_16x16x32_bf16 v[228:231], v[56:59], v[184:187], v[228:231]\n\t" \
    "v_mfma_f32_16x16x32_bf16 v[232:235], v[88:91], v[184:187], v[232:235]\n\t" \
    "v_mfma_f32_16x16x32_bf16 v[236:239], v[120:123], v[184:187], v[236:239]\n\t" \
    "v_mfma_f32_16x16x32_bf16 v[240:243], v[152:155], v[184:187], v[240:243]\n\t" \
    "s_waitcnt vmcnt(5)\n\t" \
    ".Lc5_%=:\n\t" \
    "v_max_u32 v244, v180, v181\n\t" \
    "v_max3_u32 v244, v182, v183, v244\n\t" \
    "v_cmp_eq_u32 vcc, -1, v244\n\t" \
    "s_cbranch_vccnz .Ls5_%=\n\t" \
    ".Lk5_%=:\n\t" \
    "v_mfma_f32_16x16x32_bf16 v[224:227], v[20:23], v[180:183], v[224:227]\n\t" \
    "v_mfma_f32_16x16x32_bf16 v[228:231], v[52:55], v[180:183], v[228:231]\n\t" \
    "v_mfma_f32_16x16x32_bf16 v[232:235], v[84:87], v[180:183], v[232:235]\n\t" \
    "v_mfma_f32_16x16x32_bf16 v[236:239], v[116:119], v[180:183], v[236:239]\n\t" \
    "v_mfma_f32_16x16x32_bf16 v[240:243], v[148:151], v[180:183], v[240:243]\n\t" \
    "s_waitcnt vmcnt(4)\n\t" \
    ".Lc4_%=:\n\t" \
    "v_max_u32 v244, v176, v177\n\t" \
    "v_max3_u32 v244, v178, v179, v244\n\t" \
    "v_cmp_eq_u32 vcc, -1, v244\n\t" \
    "s_cbranch_vccnz .Ls4_%=\n\t" \
    ".Lk4_%=:\n\t" \
    "v_mfma_f32_16x16x32_bf16 v[224:227], v[16:19], v[176:179], v[224:227]\n\t" \
    "v_mfma_f32_16x16x32_bf16 v[228:231], v[48:51], v[176:179], v[228:231]\n\t" \
    "v_mfma_f32_16x16x32_bf16 v[232:235], v[80:83], v[176:179], v[232:235]\n\t" \
    "v_mfma_f32_16x16x32_bf16 v[236:239], v[112:115], v[176:179], v[236:239]\n\t" \
    "v_mfma_f32_16x16x32_bf16 v[240:243], v[144:147], v[176:179], v[240:243]\n\t" \
    "s_waitcnt vmcnt(3)\n\t" \
    ".Lc3_%=:\n\t" \
    "v_max_u32 v244, v172, v173\n\t" \
    "v_max3_u32 v244, v174, v175, v244\n\t" \
    "v_cmp_eq_u32 vcc, -1, v244\n\t" \
    "s_cbranch_vccnz .Ls3_%=\n\t" \
    ".Lk3_%=:\n\t" \
    "v_mfma_f32_16x16x32_bf16 v[224:227], v[12:15], v[172:175], v[224:227]\n\t" \
    "v_mfma_f32_16x16x32_bf16 v[228:231], v[44:47], v[172:175], v[228:231]\n\t" \
    "v_mfma_f32_16x16x32_bf16 v[232:235], v[76:79], v[172:175], v[232:235]\n\t" \
    "v_mfma_f32_16x16x32_bf16 v[236:239], v[108:111], v[172:175], v[236:239]\n\t" \
    "v_mfma_f32_16x16x32_bf16 v[240:243], v[140:143], v[172:175], v[240:243]\n\t" \
    "s_waitcnt vmcnt(2)\n\t" \
    ".Lc2_%=:\n\t" \
    "v_max_u32 v244, v168, v169\n\t" \
    "v_max3_u32 v244, v170, v171, v244\n\t" \
    "v_cmp_eq_u32 vcc, -1, v244\n\t" \
    "s_cbranch_vccnz .Ls2_%=\n\t" \
    ".Lk2_%=:\n\t" \
    "v_mfma_f32_16x16x32_bf16 v[224:227], v[8:11], v[168:171], v[224:227]\n\t" \
    "v_mfma_f32_16x16x32_bf16 v[228:231], v[40:43], v[168:171], v[228:231]\n\t" \
    "v_mfma_f32_16x16x32_bf16 v[232:235], v[72:75], v[168:171], v[232:235]\n\t" \
    "v_mfma_f32_16x16x32_bf16 v[236:239], v[104:107], v[168:171], v[236:239]\n\t" \
    "v_mfma_f32_16x16x32_bf16 v[240:243], v[136:139], v[168:171], v[240:243]\n\t" \
    "s_waitcnt vmcnt(1)\n\t" \
    ".Lc1_%=:\n\t" \
    "v_max_u32 v244, v164, v165\n\t" \
    "v_max3_u32 v244, v166, v167, v244\n\t" \
    "v_cmp_eq_u32 vcc, -1, v244\n\t" \
    "s_cbranch_vccnz .Ls1_%=\n\t" \
    ".Lk1_%=:\n\t" \
    "v_mfma_f32_16x16x32_bf16 v[224:227], v[4:7], v[164:167], v[224:227]\n\t" \
    "v_mfma_f32_16x16x32_bf16 v[228:231], v[36:39], v[164:167], v[228:231]\n\t" \
    "v_mfma_f32_16x16x32_bf16 v[232:235], v[68:71], v[164:167], v[232:235]\n\t" \
    "v_mfma_f32_16x16x32_bf16 v[236:239], v[100:103], v[164:167], v[236:239]\n\t" \
    "v_mfma_f32_16x16x32_bf16 v[240:243], v[132:135], v[164:167], v[240:243]\n\t" \
    "s_waitcnt vmcnt(0)\n\t" \
    ".Lc0_%=:\n\t" \
    "v_max_u32 v244, v160, v161\n\t" \
    "v_max3_u32 v244, v162, v163, v244\n\t" \
    "v_cmp_eq_u32 vcc, -1, v244\n\t" \
    "s_cbranch_vccnz .Ls0_%=\n\t" \
    ".Lk0_%=:\n\t" \
    "v_mfma_f32_16x16x32_bf16 v[224:227], v[0:3], v[160:163], v[224:227]\n\t" \
    "v_mfma_f32_16x16x32_bf16 v[228:231], v[32:35], v[160:163], v[228:231]\n\t" \
    "v_mfma_f32_16x16x32_bf16 v[232:235], v[64:67], v[160:163], v[232:235]\n\t" \
    "v_mfma_f32_16x16x32_bf16 v[236:239], v[96:99], v[160:163], v[236:239]\n\t" \
    "v_mfma_f32_16x16x32_bf16 v[240:243], v[128:131], v[160:163], v[240:243]\n\t" \
    "ds_write_b128 %[ld], v[224:227] offset:0\n\t" \
    "ds_write_b128 %[ld], v[228:231] offset:6144\n\t" \
    "ds_write_b128 %[ld], v[232:235] offset:12288\n\t" \
    "s_nop 1\n\t" \
    "ds_write_b128 %[ld], v[236:239] offset:18432\n\t" \
    "s_nop 1\n\t" \
    "ds_write_b128 %[ld], v[240:243] offset:24576\n\t" \
    "s_branch .Lme%=\n\t" \
    ".Ls6_%=:\n\t" \
    "s_cmp_eq_u32 %[n], 0\n\t" \
    "s_cbranch_scc1 .Lk6_%=\n\t" \
    "s_sub_u32 %[n], %[n], 1\n\t" \
    "buffer_load_dwordx4 v[184:187], %[l16], %[rs], %[s1] offen offset:2048 sc1\n\t" \
    "s_waitcnt vmcnt(0)\n\t" \
    "s_branch .Lc6_%=\n\t" \
    ".Ls5_%=:\n\t" \
    "s_cmp_eq_u32 %[n], 0\n\t" \
    "s_cbranch_scc1 .Lk5_%=\n\t" \
    "s_sub_u32 %[n], %[n], 1\n\t" \
    "buffer_load_dwordx4 v[180:183], %[l16], %[rs], %[s1] offen offset:1024 sc1\n\t" \
    "s_waitcnt vmcnt(0)\n\t" \
    "s_branch .Lc5_%=\n\t" \
    ".Ls4_%=:\n\t" \
    "s_cmp_eq_u32 %[n], 0\n\t" \
    "s_cbranch_scc1 .Lk4_%=\n\t" \
    "s_sub_u32 %[n], %[n], 1\n\t" \
    "buffer_load_dwordx4 v[176:179], %[l16], %[rs], %[s1] offen offset:0 sc1\n\t" \
    "s_waitcnt vmcnt(0)\n\t" \
    "s_branch .Lc4_%=\n\t" \
    ".Ls3_%=:\n\t" \
    "s_cmp_eq_u32 %[n], 0\n\t" \
    "s_cbranch_scc1 .Lk3_%=\n\t" \
    "s_sub_u32 %[n], %[n], 1\n\t" \
    "buffer_load_dwordx4 v[172:175], %[l16], %[rs], %[s0] offen offset:3072 sc1\n\t" \
    "s_waitcnt vmcnt(0)\n\t" \
    "s_branch .Lc3_%=\n\t" \
    ".Ls2_%=:\n\t" \
    "s_cmp_eq_u32 %[n], 0\n\t" \
    "s_cbranch_scc1 .Lk2_%=\n\t" \
    "s_sub_u32 %[n], %[n], 1\n\t" \
    "buffer_load_dwordx4 v[168:171], %[l16], %[rs], %[s0] offen offset:2048 sc1\n\t" \
    "s_waitcnt vmcnt(0)\n\t" \
    "s_branch .Lc2_%=\n\t" \
    ".Ls1_%=:\n\t" \
    "s_cmp_eq_u32 %[n], 0\n\t" \
    "s_cbranch_scc1 .Lk1_%=\n\t" \
    "s_sub_u32 %[n], %[n], 1\n\t" \
    "buffer_load_dwordx4 v[164:167], %[l16], %[rs], %[s0] offen offset:1024 sc1\n\t" \
    "s_waitcnt vmcnt(0)\n\t" \
    "s_branch .Lc1_%=\n\t" \
    ".Ls0_%=:\n\t" \
    "s_cmp_eq_u32 %[n], 0\n\t" \
    "s_cbranch_scc1 .Lk0_%=\n\t" \
    "s_sub_u32 %[n], %[n], 1\n\t" \
    "buffer_load_dwordx4 v[160:163], %[l16], %[rs], %[s0] offen offset:0 sc1\n\t" \
    "s_waitcnt vmcnt(0)\n\t" \
    "s_branch .Lc0_%=\n\t" \
    ".Lme%=:\n\t"
#define ASM_W1_LOOP \
    "s_mov_b32 s40, 0\n\t" \
    "s_mov_b32 s41, 0x20000\n\t" \
    LSTINIT \
    "s_add_u32 m0, %[wm], 0x0\n\t" \
    "s_add_u32 s44, %[wo], 0x0\n\t" \
    "buffer_load_dwordx4 v176, %[wr], s44 offen nt lds\n\t" \
    "s_add_u32 m0, %[wm], 0x2000\n\t" \
    "s_add_u32 s44, %[wo], 0xa000\n\t" \
    "buffer_load_dwordx4 v176, %[wr], s44 offen nt lds\n\t" \
    "s_add_u32 m0, %[wm], 0x4000\n\t" \
    "s_add_u32 s44, %[wo], 0x14000\n\t" \
    "buffer_load_dwordx4 v176, %[wr], s44 offen nt lds\n\t" \
    "s_add_u32 m0, %[wm], 0x6000\n\t" \
    "s_add_u32 s44, %[wo], 0x1e000\n\t" \
    "buffer_load_dwordx4 v176, %[wr], s44 offen nt lds\n\t" \
    "s_add_u32 m0, %[wm], 0x8000\n\t" \
    "s_add_u32 s44, %[wo], 0x28000\n\t" \
    "buffer_load_dwordx4 v176, %[wr], s44 offen nt lds\n\t" \
    "s_add_u32 m0, %[wm], 0xa000\n\t" \
    "s_add_u32 s44, %[wo], 0x32000\n\t" \
    "buffer_load_dwordx4 v176, %[wr], s44 offen nt lds\n\t" \
    "s_add_u32 m0, %[wm], 0xc000\n\t" \
    "s_add_u32 s44, %[wo], 0x3c000\n\t" \
    "buffer_load_dwordx4 v176, %[wr], s44 offen nt lds\n\t" \
    "s_add_u32 m0, %[wm], 0xe000\n\t" \
    "s_add_u32 s44, %[wo], 0x46000\n\t" \
    "buffer_load_dwordx4 v176, %[wr], s44 offen nt lds\n\t" \
    "s_add_u32 m0, %[wm], 0x10000\n\t" \
    "s_add_u32 s44, %[wo], 0x50000\n\t" \
    "buffer_load_dwordx4 v176, %[wr], s44 offen nt lds\n\t" \
    "s_add_u32 m0, %[wm], 0x12000\n\t" \
    "s_add_u32 s44, %[wo], 0x5a000\n\t" \
    "buffer_load_dwordx4 v176, %[wr], s44 offen nt lds\n\t" \
    "s_add_u32 m0, %[wm], 0x14000\n\t" \
    "s_add_u32 s44, %[wo], 0x64000\n\t" \
    "buffer_load_dwordx4 v176, %[wr], s44 offen nt lds\n\t" \
    "s_add_u32 m0, %[wm], 0x16000\n\t" \
    "s_add_u32 s44, %[wo], 0x6e000\n\t" \
    "buffer_load_dwordx4 v176, %[wr], s44 offen nt lds\n\t" \
    "s_add_u32 m0, %[wm], 0x18000\n\t" \
    "s_add_u32 s44, %[wo], 0x78000\n\t" \
    "buffer_load_dwordx4 v176, %[wr], s44 offen nt lds\n\t" \
    "s_add_u32 m0, %[wm], 0x1a000\n\t" \
    "s_add_u32 s44, %[wo], 0x82000\n\t" \
    "buffer_load_dwordx4 v176, %[wr], s44 offen nt lds\n\t" \
    "s_add_u32 m0, %[wm], 0x1c000\n\t" \
    "s_add_u32 s44, %[wo], 0x8c000\n\t" \
    "buffer_load_dwordx4 v176, %[wr], s44 offen nt lds\n\t" \
    "s_add_u32 m0, %[wm], 0x1e000\n\t" \
    "s_add_u32 s44, %[wo], 0x96000\n\t" \
    "buffer_load_dwordx4 v176, %[wr], s44 offen nt lds\n\t" \
    "s_add_u32 m0, %[wm], 0x20000\n\t" \
    "s_add_u32 s44, %[wo], 0xa0000\n\t" \
    "buffer_load_dwordx4 v176, %[wr], s44 offen nt lds\n\t" \
    "s_add_u32 m0, %[wm], 0x22000\n\t" \
    "s_add_u32 s44, %[wo], 0xaa000\n\t" \
    "buffer_load_dwordx4 v176, %[wr], s44 offen nt lds\n\t" \
    "s_add_u32 m0, %[wm], 0x24000\n\t" \
    "s_add_u32 s44, %[wo], 0xb4000\n\t" \
    "buffer_load_dwordx4 v176, %[wr], s44 offen nt lds\n\t" \
    "s_add_u32 m0, %[wm], 0x26000\n\t" \
    "s_add_u32 s44, %[wo], 0xbe000\n\t" \
    "buffer_load_dwordx4 v176, %[wr], s44 offen nt lds\n\t" \
    "s_waitcnt vmcnt(0) lgkmcnt(0)\n\t" \
    "s_barrier \n\t" \
    "v_add_u32 v178, 0x0, v177\n\t" \
    "ds_read_b64_tr_b16 v[0:1], v178 offset:0\n\t" \
    "ds_read_b64_tr_b16 v[2:3], v178 offset:1024\n\t" \
    "ds_read_b64_tr_b16 v[4:5], v178 offset:8192\n\t" \
    "ds_read_b64_tr_b16 v[6:7], v178 offset:9216\n\t" \
    "ds_read_b64_tr_b16 v[8:9], v178 offset:16384\n\t" \
    "ds_read_b64_tr_b16 v[10:11], v178 offset:17408\n\t" \
    "ds_read_b64_tr_b16 v[12:13], v178 offset:24576\n\t" \
    "ds_read_b64_tr_b16 v[14:15], v178 offset:25600\n\t" \
    "v_add_u32 v178, 0x8000, v177\n\t" \
    "ds_read_b64_tr_b16 v[16:17], v178 offset:0\n\t" \
    "ds_read_b64_tr_b16 v[18:19], v178 offset:1024\n\t" \
    "ds_read_b64_tr_b16 v[20:21], v178 offset:8192\n\t" \
    "ds_read_b64_tr_b16 v[22:23], v178 offset:9216\n\t" \
    "ds_read_b64_tr_b16 v[24:25], v178 offset:16384\n\t" \
    "ds_read_b64_tr_b16 v[26:27], v178 offset:17408\n\t" \
    "ds_read_b64_tr_b16 v[28:29], v178 offset:24576\n\t" \
    "ds_read_b64_tr_b16 v[30:31], v178 offset:25600\n\t" \
    "v_add_u32 v178, 0x10000, v177\n\t" \
    "ds_read_b64_tr_b16 v[32:33], v178 offset:0\n\t" \
    "ds_read_b64_tr_b16 v[34:35], v178 offset:1024\n\t" \
    "ds_read_b64_tr_b16 v[36:37], v178 offset:8192\n\t" \
    "ds_read_b64_tr_b16 v[38:39], v178 offset:9216\n\t" \
    "ds_read_b64_tr_b16 v[40:41], v178 offset:16384\n\t" \
    "ds_read_b64_tr_b16 v[42:43], v178 offset:17408\n\t" \
    "ds_read_b64_tr_b16 v[44:45], v178 offset:24576\n\t" \
    "ds_read_b64_tr_b16 v[46:47], v178 offset:25600\n\t" \
    "v_add_u32 v178, 0x18000, v177\n\t" \
    "ds_read_b64_tr_b16 v[48:49], v178 offset:0\n\t" \
    "ds_read_b64_tr_b16 v[50:51], v178 offset:1024\n\t" \
    "ds_read_b64_tr_b16 v[52:53], v178 offset:8192\n\t" \
    "ds_read_b64_tr_b16 v[54:55], v178 offset:9216\n\t" \
    "ds_read_b64_tr_b16 v[56:57], v178 offset:16384\n\t" \
    "ds_read_b64_tr_b16 v[58:59], v178 offset:17408\n\t" \
    "ds_read_b64_tr_b16 v[60:61], v178 offset:24576\n\t" \
    "ds_read_b64_tr_b16 v[62:63], v178 offset:25600\n\t" \
    "v_add_u32 v178, 0x20000, v177\n\t" \
    "ds_read_b64_tr_b16 v[64:65], v178 offset:0\n\t" \
    "ds_read_b64_tr_b16 v[66:67], v178 offset:1024\n\t" \
    "ds_read_b64_tr_b16 v[68:69], v178 offset:8192\n\t" \
    "ds_read_b64_tr_b16 v[70:71], v178 offset:9216\n\t" \
    "ds_read_b64_tr_b16 v[72:73], v178 offset:16384\n\t" \
    "ds_read_b64_tr_b16 v[74:75], v178 offset:17408\n\t" \
    "ds_read_b64_tr_b16 v[76:77], v178 offset:24576\n\t" \
    "ds_read_b64_tr_b16 v[78:79], v178 offset:25600\n\t" \
    "v_mov_b32 v220, 0x3f803f80\n\t" \
    "v_mov_b32 v221, 0x3f803f80\n\t" \
    "v_mov_b32 v222, 0x3f803f80\n\t" \
    "v_mov_b32 v223, 0x3f803f80\n\t" \
    "v_mov_b32 v216, 0\n\t" \
    "v_mov_b32 v217, 0\n\t" \
    "v_mov_b32 v218, 0\n\t" \
    "v_mov_b32 v219, 0\n\t" \
    "s_waitcnt lgkmcnt(0)\n\t" \
    "s_barrier \n\t" \
    "ds_write_b32 v251, v220 offset:72\n\t" \
    "ds_write_b32 v251, v220 offset:2120\n\t" \
    "v_mfma_f32_16x16x32_bf16 v[160:163], v[0:3], v[220:223], 0\n\t" \
    "v_mfma_f32_16x16x32_bf16 v[160:163], v[4:7], v[220:223], v[160:163]\n\t" \
    "v_mfma_f32_16x16x32_bf16 v[160:163], v[8:11], v[220:223], v[160:163]\n\t" \
    "v_mfma_f32_16x16x32_bf16 v[160:163], v[12:15], v[220:223], v[160:163]\n\t" \
    "v_mfma_f32_16x16x32_bf16 v[160:163], v[16:19], v[220:223], v[160:163]\n\t" \
    "v_mfma_f32_16x16x32_bf16 v[160:163], v[20:23], v[220:223], v[160:163]\n\t" \
    "v_mfma_f32_16x16x32_bf16 v[160:163], v[24:27], v[220:223], v[160:163]\n\t" \
    "v_mfma_f32_16x16x32_bf16 v[160:163], v[28:31], v[220:223], v[160:163]\n\t" \
    "v_mfma_f32_16x16x32_bf16 v[160:163], v[32:35], v[220:223], v[160:163]\n\t" \
    "v_mfma_f32_16x16x32_bf16 v[160:163], v[36:39], v[220:223], v[160:163]\n\t" \
    "v_mfma_f32_16x16x32_bf16 v[160:163], v[40:43], v[220:223], v[160:163]\n\t" \
    "v_mfma_f32_16x16x32_bf16 v[160:163], v[44:47], v[220:223], v[160:163]\n\t" \
    "v_mfma_f32_16x16x32_bf16 v[160:163], v[48:51], v[220:223], v[160:163]\n\t" \
    "v_mfma_f32_16x16x32_bf16 v[160:163], v[52:55], v[220:223], v[160:163]\n\t" \
    "v_mfma_f32_16x16x32_bf16 v[160:163], v[56:59], v[220:223], v[160:163]\n\t" \
    "v_mfma_f32_16x16x32_bf16 v[160:163], v[60:63], v[220:223], v[160:163]\n\t" \
    "v_mfma_f32_16x16x32_bf16 v[160:163], v[64:67], v[220:223], v[160:163]\n\t" \
    "v_mfma_f32_16x16x32_bf16 v[160:163], v[68:71], v[220:223], v[160:163]\n\t" \
    "v_mfma_f32_16x16x32_bf16 v[160:163], v[72:75], v[220:223], v[160:163]\n\t" \
    "v_mfma_f32_16x16x32_bf16 v[160:163], v[76:79], v[220:223], v[160:163]\n\t" \
    "s_mov_b32 s52, 0x22222222\n\t" \
    "s_mov_b32 s53, 0x22222222\n\t" \
    "s_mov_b32 s54, 0x44444444\n\t" \
    "s_mov_b32 s55, 0x44444444\n\t" \
    "s_mov_b32 s56, 0x88888888\n\t" \
    "s_mov_b32 s57, 0x88888888\n\t" \
    ".Lw11_%=:\n\t" \
    "v_mov_b32 v248, 0\n\t" \
    "buffer_load_dword v248, v248, %[rs], %[go] offen sc1\n\t" \
    "s_waitcnt vmcnt(0)\n\t" \
    "v_cmp_ne_u32 vcc, 0, v248\n\t" \
    "s_cbranch_vccnz .Lw12_%=\n\t" \
    "s_cmp_eq_u32 s41, 0\n\t" \
    "s_cbranch_scc1 .Lw12_%=\n\t" \
    "s_sub_u32 s41, s41, 1\n\t" \
    "s_sleep 16\n\t" \
    "s_branch .Lw11_%=\n\t" \
    ".Lw12_%=:\n\t" \
    "s_sub_i32 s60, 0, %[ng]\n\t" \
    "s_sub_i32 s61, 1, %[ng]\n\t" \
    "s_add_i32 s62, s60, 6\n\t" \
    "s_add_i32 s63, s61, 6\n\t" \
    "s_mul_i32 s42, s61, 127\n\t" \
    "s_mul_i32 s44, %[ng], 0x733000\n\t" \
    "s_add_u32 s44, s44, 0x2400000\n\t" \
    "v_cmp_ne_u32 s[58:59], %[ng], v255\n\t" \
    "s_nop 1\n\t" \
    "s_mov_b64 exec, s[58:59]\n\t" \
    "buffer_store_dwordx2 v[216:217], v254, %[rs], s44 offen sc1\n\t" \
    "s_mov_b64 exec, -1\n\t" \
    "s_mov_b32 s43, 0\n\t" \
    "s_add_u32 s46, s43, %[pi]\n\t" \
    "s_add_u32 s47, s46, 0x8000\n\t" \
    "s_add_u32 s48, s46, 0x10000\n\t" \
    "buffer_load_dwordx4 v[208:211], v253, %[rs], s46 offen sc1\n\t" \
    "buffer_load_dwordx4 v[212:215], v253, %[rs], s47 offen sc1\n\t" \
    "s_cmp_gt_u32 %[wave], 3\n\t" \
    "s_cbranch_scc1 .Lw13_%=\n\t" \
    "buffer_load_dwordx4 v[216:219], v253, %[rs], s48 offen sc1\n\t" \
    ".Lw13_%=:\n\t" \
    "s_waitcnt vmcnt(0)\n\t" \
    "v_lshrrev_b32 v82, 1, v253\n\t" \
    ".Lw14_%=:\n\t" \
    "s_add_i32 s44, s40, s60\n\t" \
    "s_max_i32 s44, s44, 0\n\t" \
    "s_lshr_b32 s45, s44, 1\n\t" \
    "s_and_b32 s44, s44, 1\n\t" \
    "s_lshl_b32 s44, s44, 12\n\t" \
    "s_mul_i32 s49, s45, 0x14000\n\t" \
    "s_add_u32 s49, s49, s44\n\t" \
    "s_lshr_b32 s45, s45, 4\n\t" \
    "s_mul_i32 s45, s45, 0xc2000\n\t" \
    "s_add_u32 s49, s49, s45\n\t" \
    "s_add_i32 s44, s40, s61\n\t" \
    "s_max_i32 s44, s44, 0\n\t" \
    "s_lshr_b32 s45, s44, 1\n\t" \
    "s_and_b32 s44, s44, 1\n\t" \
    "s_lshl_b32 s44, s44, 12\n\t" \
    "s_mul_i32 s50, s45, 0x14000\n\t" \
    "s_add_u32 s50, s50, s44\n\t" \
    "s_lshr_b32 s45, s45, 4\n\t" \
    "s_mul_i32 s45, s45, 0xc2000\n\t" \
    "s_add_u32 s50, s50, s45\n\t" \
    "s_sub_u32 s50, s50, s49\n\t" \
    "s_add_u32 s49, s49, 0x2400000\n\t" \
    "v_mad_u32_u24 v80, v255, s50, v254\n\t" \
    "s_lshr_b32 s44, s40, 6\n\t" \
    "s_lshl_b32 s44, s44, 14\n\t" \
    "s_and_b32 s45, s40, 63\n\t" \
    "s_lshl_b32 s45, s45, 7\n\t" \
    "s_add_u32 s44, s44, s45\n\t" \
    "s_add_u32 s50, s44, 0x4d0000\n\t" \
    "s_add_i32 s44, s40, 1\n\t" \
    "s_max_i32 s44, s44, 0\n\t" \
    "s_lshr_b32 s45, s44, 1\n\t" \
    "s_and_b32 s44, s44, 1\n\t" \
    "s_lshl_b32 s44, s44, 12\n\t" \
    "s_mul_i32 s43, s45, 0x14000\n\t" \
    "s_add_u32 s43, s43, s44\n\t" \
    "s_lshr_b32 s45, s45, 4\n\t" \
    "s_mul_i32 s45, s45, 0xc2000\n\t" \
    "s_add_u32 s43, s43, s45\n\t" \
    LST0 \
    "v_max3_u32 v248, v208, v209, v210\n\t" \
    "v_max3_u32 v248, v211, v212, v248\n\t" \
    "v_max3_u32 v248, v213, v214, v248\n\t" \
    "v_max3_u32 v248, v215, v216, v248\n\t" \
    "v_max3_u32 v248, v217, v218, v248\n\t" \
    "v_max_u32 v248, v219, v248\n\t" \
    "v_cmp_eq_u32 vcc, -1, v248\n\t" \
    "s_cbranch_vccz .Lw16_%=\n\t" \
    ".Lw15_%=:\n\t" \
    "v_max_u32 v248, v208, v209\n\t" \
    "v_max3_u32 v248, v210, v211, v248\n\t" \
    "v_cmp_eq_u32 vcc, -1, v248\n\t" \
    "s_cbranch_vccz .Lw17_%=\n\t" \
    "buffer_load_dwordx4 v[208:211], v253, %[rs], s46 offen sc1\n\t" \
    ".Lw17_%=:\n\t" \
    "v_max_u32 v248, v212, v213\n\t" \
    "v_max3_u32 v248, v214, v215, v248\n\t" \
    "v_cmp_eq_u32 vcc, -1, v248\n\t" \
    "s_cbranch_vccz .Lw18_%=\n\t" \
    "buffer_load_dwordx4 v[212:215], v253, %[rs], s47 offen sc1\n\t" \
    ".Lw18_%=:\n\t" \
    "v_max_u32 v248, v216, v217\n\t" \
    "v_max3_u32 v248, v218, v219, v248\n\t" \
    "v_cmp_eq_u32 vcc, -1, v248\n\t" \
    "s_cbranch_vccz .Lw19_%=\n\t" \
    "buffer_load_dwordx4 v[216:219], v253, %[rs], s48 offen sc1\n\t" \
    ".Lw19_%=:\n\t" \
    "s_waitcnt vmcnt(20)\n\t" \
    "v_max3_u32 v248, v208, v209, v210\n\t" \
    "v_max3_u32 v248, v211, v212, v248\n\t" \
    "v_max3_u32 v248, v213, v214, v248\n\t" \
    "v_max3_u32 v248, v215, v216, v248\n\t" \
    "v_max3_u32 v248, v217, v218, v248\n\t" \
    "v_max_u32 v248, v219, v248\n\t" \
    "v_cmp_eq_u32 vcc, -1, v248\n\t" \
    "s_cbranch_vccz .Lw16_%=\n\t" \
    "s_sleep 1\n\t" \
    "v_max3_u32 v248, v208, v209, v210\n\t" \
    "v_max3_u32 v248, v211, v212, v248\n\t" \
    "v_max3_u32 v248, v213, v214, v248\n\t" \
    "v_max3_u32 v248, v215, v216, v248\n\t" \
    "v_max3_u32 v248, v217, v218, v248\n\t" \
    "v_max_u32 v248, v219, v248\n\t" \
    "v_cmp_eq_u32 vcc, -1, v248\n\t" \
    "s_cbranch_vccz .Lw16_%=\n\t" \
    "s_sleep 1\n\t" \
    "v_max3_u32 v248, v208, v209, v210\n\t" \
    "v_max3_u32 v248, v211, v212, v248\n\t" \
    "v_max3_u32 v248, v213, v214, v248\n\t" \
    "v_max3_u32 v248, v215, v216, v248\n\t" \
    "v_max3_u32 v248, v217, v218, v248\n\t" \
    "v_max_u32 v248, v219, v248\n\t" \
    "v_cmp_eq_u32 vcc, -1, v248\n\t" \
    "s_cbranch_vccz .Lw16_%=\n\t" \
    "s_sleep 1\n\t" \
    "v_max3_u32 v248, v208, v209, v210\n\t" \
    "v_max3_u32 v248, v211, v212, v248\n\t" \
    "v_max3_u32 v248, v213, v214, v248\n\t" \
    "v_max3_u32 v248, v215, v216, v248\n\t" \
    "v_max3_u32 v248, v217, v218, v248\n\t" \
    "v_max_u32 v248, v219, v248\n\t" \
    "v_cmp_eq_u32 vcc, -1, v248\n\t" \
    "s_cbranch_vccz .Lw16_%=\n\t" \
    "s_sleep 1\n\t" \
    "v_max3_u32 v248, v208, v209, v210\n\t" \
    "v_max3_u32 v248, v211, v212, v248\n\t" \
    "v_max3_u32 v248, v213, v214, v248\n\t" \
    "v_max3_u32 v248, v215, v216, v248\n\t" \
    "v_max3_u32 v248, v217, v218, v248\n\t" \
    "v_max_u32 v248, v219, v248\n\t" \
    "v_cmp_eq_u32 vcc, -1, v248\n\t" \
    "s_cbranch_vccz .Lw16_%=\n\t" \
    "s_sleep 1\n\t" \
    "v_max3_u32 v248, v208, v209, v210\n\t" \
    "v_max3_u32 v248, v211, v212, v248\n\t" \
    "v_max3_u32 v248, v213, v214, v248\n\t" \
    "v_max3_u32 v248, v215, v216, v248\n\t" \
    "v_max3_u32 v248, v217, v218, v248\n\t" \
    "v_max_u32 v248, v219, v248\n\t" \
    "v_cmp_eq_u32 vcc, -1, v248\n\t" \
    "s_cbranch_vccz .Lw16_%=\n\t" \
    "s_sleep 1\n\t" \
    "s_cmp_eq_u32 s41, 0\n\t" \
    "s_cbranch_scc1 .Lw16_%=\n\t" \
    "s_sub_u32 s41, s41, 1\n\t" \
    "s_branch .Lw15_%=\n\t" \
    ".Lw16_%=:\n\t" \
    LST1 \
    "ds_write_b128 v250, v[208:211]\n\t" \
    "ds_write_b128 v250, v[212:215] offset:8192\n\t" \
    "s_cmp_gt_u32 %[wave], 3\n\t" \
    "s_cbranch_scc1 .Lw110_%=\n\t" \
    "ds_write_b128 v250, v[216:219] offset:16384\n\t" \
    ".Lw110_%=:\n\t" \
    "v_mfma_f32_16x16x32_bf16 v[228:231], v[208:211], v[208:211], 0\n\t" \
    "v_mfma_f32_16x16x32_bf16 v[228:231], v[212:215], v[212:215], v[228:231]\n\t" \
    "v_mfma_f32_16x16x32_bf16 v[228:231], v[216:219], v[216:219], v[228:231]\n\t" \
    "v_mfma_f32_16x16x32_bf16 v[224:227], v[220:223], v[208:211], 0\n\t" \
    "v_mfma_f32_16x16x32_bf16 v[224:227], v[220:223], v[212:215], v[224:227]\n\t" \
    "v_mfma_f32_16x16x32_bf16 v[224:227], v[220:223], v[216:219], v[224:227]\n\t" \
    "v_cndmask_b32 v228, v228, v229, s[52:53]\n\t" \
    "v_cndmask_b32 v228, v228, v230, s[54:55]\n\t" \
    "v_cndmask_b32 v228, v228, v231, s[56:57]\n\t" \
    "s_add_u32 s46, s43, %[pi]\n\t" \
    "s_add_u32 s47, s46, 0x8000\n\t" \
    "s_add_u32 s48, s46, 0x10000\n\t" \
    "s_mov_b32 exec_lo, 0xf0000f\n\t" \
    "s_mov_b32 exec_hi, 0xf0000f00\n\t" \
    "s_nop 4\n\t" \
    "ds_write2_b32 v252, v224, v228 offset1:1\n\t" \
    "s_mov_b64 exec, -1\n\t" \
    "s_waitcnt lgkmcnt(0)\n\t" \
    "s_barrier \n\t" \
    LST2 \
    "s_cmp_lg_u32 %[wave], 4\n\t" \
    "s_cbranch_scc1 .Lw111_%=\n\t" \
    "s_setprio 1\n\t" \
    "ds_read_b128 v[232:235], v251 offset:0\n\t" \
    "ds_read_b128 v[236:239], v251 offset:16\n\t" \
    "ds_read_b128 v[240:243], v251 offset:32\n\t" \
    "ds_read_b128 v[244:247], v251 offset:48\n\t" \
    "s_waitcnt lgkmcnt(0)\n\t" \
    "v_add_f32 v232, v232, v240\n\t" \
    "v_add_f32 v233, v233, v241\n\t" \
    "v_add_f32 v234, v234, v242\n\t" \
    "v_add_f32 v235, v235, v243\n\t" \
    "v_add_f32 v236, v236, v244\n\t" \
    "v_add_f32 v237, v237, v245\n\t" \
    "v_add_f32 v238, v238, v246\n\t" \
    "v_add_f32 v239, v239, v247\n\t" \
    "v_add_f32 v232, v232, v236\n\t" \
    "v_add_f32 v233, v233, v237\n\t" \
    "v_add_f32 v234, v234, v238\n\t" \
    "v_add_f32 v235, v235, v239\n\t" \
    "v_add_f32 v232, v232, v234\n\t" \
    "v_add_f32 v233, v233, v235\n\t" \
    "v_mul_f32 v232, 0x3acccccd, v232\n\t" \
    "v_mul_f32 v233, 0x3acccccd, v233\n\t" \
    "v_fma_f32 v233, -v232, v232, v233\n\t" \
    "v_mov_b32 v234, v232\n\t" \
    "v_max_f32 v233, 0, v233\n\t" \
    "v_cvt_pk_bf16_f32 v234, v234, v234\n\t" \
    "v_and_b32 v234, 0xffff0000, v234\n\t" \
    "v_mov_b32 v236, v233\n\t" \
    "v_cvt_pk_bf16_f32 v236, v236, v236\n\t" \
    "v_and_b32 v236, 0xffff0000, v236\n\t" \
    "v_add_f32 v236, 0x3727c5ac, v236\n\t" \
    "v_cvt_pk_bf16_f32 v236, v236, v236\n\t" \
    "v_and_b32 v236, 0xffff0000, v236\n\t" \
    "v_sqrt_f32 v236, v236\n\t" \
    "s_nop 0\n\t" \
    "v_cvt_pk_bf16_f32 v236, v236, v236\n\t" \
    "v_and_b32 v236, 0xffff0000, v236\n\t" \
    "v_rcp_f32 v235, v236\n\t" \
    "s_nop 0\n\t" \
    "v_sub_f32 v232, v232, v234\n\t" \
    "v_mul_f32 v233, v233, v235\n\t" \
    "v_mul_f32 v232, v232, v235\n\t" \
    "v_mul_f32 v233, v233, v235\n\t" \
    "v_cvt_pk_bf16_f32 v232, v232, v232\n\t" \
    "v_and_b32 v232, 0xffff0000, v232\n\t" \
    "v_cvt_pk_bf16_f32 v233, v233, v233\n\t" \
    "v_and_b32 v233, 0xffff0000, v233\n\t" \
    "v_add_f32 v233, 0x3727c5ac, v233\n\t" \
    "v_cvt_pk_bf16_f32 v233, v233, v233\n\t" \
    "v_and_b32 v233, 0xffff0000, v233\n\t" \
    "v_sqrt_f32 v233, v233\n\t" \
    "s_nop 0\n\t" \
    "v_cvt_pk_bf16_f32 v233, v233, v233\n\t" \
    "v_and_b32 v233, 0xffff0000, v233\n\t" \
    "v_rcp_f32 v233, v233\n\t" \
    "v_fma_f32 v232, v232, v236, v234\n\t" \
    "v_mul_f32 v237, v235, v233\n\t" \
    "v_mul_f32 v238, -v232, v237\n\t" \
    "v_min_u32 v234, -2, v234\n\t" \
    "v_min_u32 v235, -2, v235\n\t" \
    "v_mov_b32 v239, s40\n\t" \
    "ds_write2_b32 v251, v237, v238 offset0:16 offset1:17\n\t" \
    "ds_write_b32 v251, v239 offset:72\n\t" \
    "s_setprio 0\n\t" \
    "s_cmp_eq_u32 %[ss], 0\n\t" \
    "s_cbranch_scc1 .Lw112_%=\n\t" \
    "s_mov_b64 exec, 0xffff\n\t" \
    "buffer_store_dwordx2 v[234:235], v82, %[rs], s50 offen sc1\n\t" \
    "s_mov_b64 exec, -1\n\t" \
    ".Lw112_%=:\n\t" \
    ".Lw111_%=:\n\t" \
    "ds_read_b128 v[176:179], v249 offset:0\n\t" \
    "ds_read_b128 v[180:183], v249 offset:1024\n\t" \
    "ds_read_b128 v[184:187], v249 offset:2048\n\t" \
    "ds_read_b128 v[188:191], v249 offset:3072\n\t" \
    "ds_read_b128 v[192:195], v249 offset:4096\n\t" \
    "ds_read_b128 v[196:199], v249 offset:5120\n\t" \
    "ds_read_b128 v[200:203], v249 offset:6144\n\t" \
    "buffer_load_dwordx4 v[208:211], v253, %[rs], s46 offen sc1\n\t" \
    "buffer_load_dwordx4 v[212:215], v253, %[rs], s47 offen sc1\n\t" \
    "s_cmp_gt_u32 %[wave], 3\n\t" \
    "s_cbranch_scc1 .Lw113_%=\n\t" \
    "buffer_load_dwordx4 v[216:219], v253, %[rs], s48 offen sc1\n\t" \
    ".Lw113_%=:\n\t" \
    "s_waitcnt lgkmcnt(6)\n\t" \
    "v_mfma_f32_16x16x32_bf16 v[168:171], v[0:3], v[176:179], 0\n\t" \
    "ds_read_b128 v[204:207], v249 offset:7168\n\t" \
    "s_waitcnt lgkmcnt(6)\n\t" \
    "v_mfma_f32_16x16x32_bf16 v[168:171], v[4:7], v[180:183], v[168:171]\n\t" \
    "ds_read_b128 v[176:179], v249 offset:8192\n\t" \
    "s_waitcnt lgkmcnt(6)\n\t" \
    "v_mfma_f32_16x16x32_bf16 v[168:171], v[8:11], v[184:187], v[168:171]\n\t" \
    "ds_read_b128 v[180:183], v249 offset:9216\n\t" \
    "s_waitcnt lgkmcnt(6)\n\t" \
    "v_mfma_f32_16x16x32_bf16 v[168:171], v[12:15], v[188:191], v[168:171]\n\t" \
    "ds_read_b128 v[184:187], v249 offset:10240\n\t" \
    "s_waitcnt lgkmcnt(6)\n\t" \
    "v_mfma_f32_16x16x32_bf16 v[168:171], v[16:19], v[192:195], v[168:171]\n\t" \
    "ds_read_b128 v[188:191], v249 offset:11264\n\t" \
    "s_waitcnt lgkmcnt(6)\n\t" \
    "v_mfma_f32_16x16x32_bf16 v[168:171], v[20:23], v[196:199], v[168:171]\n\t" \
    "ds_read_b128 v[192:195], v249 offset:12288\n\t" \
    "s_waitcnt lgkmcnt(6)\n\t" \
    "v_mfma_f32_16x16x32_bf16 v[168:171], v[24:27], v[200:203], v[168:171]\n\t" \
    "ds_read_b128 v[196:199], v249 offset:13312\n\t" \
    "s_waitcnt lgkmcnt(6)\n\t" \
    "v_mfma_f32_16x16x32_bf16 v[168:171], v[28:31], v[204:207], v[168:171]\n\t" \
    "ds_read_b128 v[200:203], v249 offset:14336\n\t" \
    "s_waitcnt lgkmcnt(6)\n\t" \
    "v_mfma_f32_16x16x32_bf16 v[168:171], v[32:35], v[176:179], v[168:171]\n\t" \
    "ds_read_b128 v[204:207], v249 offset:15360\n\t" \
    "s_waitcnt lgkmcnt(6)\n\t" \
    "v_mfma_f32_16x16x32_bf16 v[168:171], v[36:39], v[180:183], v[168:171]\n\t" \
    "ds_read_b128 v[176:179], v249 offset:16384\n\t" \
    "s_waitcnt lgkmcnt(6)\n\t" \
    "v_mfma_f32_16x16x32_bf16 v[168:171], v[40:43], v[184:187], v[168:171]\n\t" \
    "ds_read_b128 v[180:183], v249 offset:17408\n\t" \
    "s_waitcnt lgkmcnt(6)\n\t" \
    "v_mfma_f32_16x16x32_bf16 v[168:171], v[44:47], v[188:191], v[168:171]\n\t" \
    "ds_read_b128 v[184:187], v249 offset:18432\n\t" \
    "s_waitcnt lgkmcnt(6)\n\t" \
    "v_mfma_f32_16x16x32_bf16 v[168:171], v[48:51], v[192:195], v[168:171]\n\t" \
    "ds_read_b128 v[188:191], v249 offset:19456\n\t" \
    "s_waitcnt lgkmcnt(6)\n\t" \
    "v_mfma_f32_16x16x32_bf16 v[168:171], v[52:55], v[196:199], v[168:171]\n\t" \
    "ds_read_b32 v236, v251 offset:72\n\t" \
    "ds_read_b64 v[238:239], v251 offset:64\n\t" \
    "s_waitcnt lgkmcnt(7)\n\t" \
    "v_mfma_f32_16x16x32_bf16 v[168:171], v[56:59], v[200:203], v[168:171]\n\t" \
    "s_waitcnt lgkmcnt(6)\n\t" \
    "v_mfma_f32_16x16x32_bf16 v[168:171], v[60:63], v[204:207], v[168:171]\n\t" \
    "s_waitcnt lgkmcnt(5)\n\t" \
    "v_mfma_f32_16x16x32_bf16 v[168:171], v[64:67], v[176:179], v[168:171]\n\t" \
    "s_waitcnt lgkmcnt(4)\n\t" \
    "v_mfma_f32_16x16x32_bf16 v[168:171], v[68:71], v[180:183], v[168:171]\n\t" \
    ".Lw114_%=:\n\t" \
    "s_waitcnt lgkmcnt(0)\n\t" \
    "v_cmp_ne_u32 vcc, s40, v236\n\t" \
    "s_cbranch_vccz .Lw115_%=\n\t" \
    "ds_read_b32 v236, v251 offset:72\n\t" \
    "ds_read_b64 v[238:239], v251 offset:64\n\t" \
    "s_branch .Lw114_%=\n\t" \
    ".Lw115_%=:\n\t" \
    "s_waitcnt lgkmcnt(0)\n\t" \
    "v_mul_f32 v240, v160, v239\n\t" \
    "v_mul_f32 v241, v161, v239\n\t" \
    "v_mfma_f32_16x16x32_bf16 v[168:171], v[72:75], v[184:187], v[168:171]\n\t" \
    "v_mul_f32 v242, v162, v239\n\t" \
    "v_mul_f32 v243, v163, v239\n\t" \
    "v_mfma_f32_16x16x32_bf16 v[168:171], v[76:79], v[188:191], v[168:171]\n\t" \
    "v_mov_b32 v237, 0xc0135761\n\t" \
    LST3 \
    "s_nop 6\n\t" \
    "v_fma_f32 v168, v168, v238, v240\n\t" \
    "v_fma_f32 v169, v169, v238, v241\n\t" \
    "v_fma_f32 v170, v170, v238, v242\n\t" \
    "v_fma_f32 v171, v171, v238, v243\n\t" \
    "v_mul_f32 v176, v168, v168\n\t" \
    "v_mul_f32 v177, v169, v169\n\t" \
    "v_mul_f32 v178, v170, v170\n\t" \
    "v_mul_f32 v179, v171, v171\n\t" \
    "v_fmamk_f32 v176, v176, 0xbdd2d3e7, v237\n\t" \
    "v_fmamk_f32 v177, v177, 0xbdd2d3e7, v237\n\t" \
    "v_fmamk_f32 v178, v178, 0xbdd2d3e7, v237\n\t" \
    "v_fmamk_f32 v179, v179, 0xbdd2d3e7, v237\n\t" \
    "v_mul_f32 v176, v168, v176\n\t" \
    "v_mul_f32 v177, v169, v177\n\t" \
    "v_mul_f32 v178, v170, v178\n\t" \
    "v_mul_f32 v179, v171, v179\n\t" \
    "v_exp_f32 v176, v176\n\t" \
    "v_exp_f32 v177, v177\n\t" \
    "v_exp_f32 v178, v178\n\t" \
    "v_exp_f32 v179, v179\n\t" \
    "v_add_f32 v176, 1.0, v176\n\t" \
    "v_add_f32 v177, 1.0, v177\n\t" \
    "v_add_f32 v178, 1.0, v178\n\t" \
    "v_add_f32 v179, 1.0, v179\n\t" \
    "v_rcp_f32 v176, v176\n\t" \
    "v_rcp_f32 v177, v177\n\t" \
    "v_rcp_f32 v178, v178\n\t" \
    "v_rcp_f32 v179, v179\n\t" \
    "v_mul_f32 v168, v168, v176\n\t" \
    "v_mul_f32 v169, v169, v177\n\t" \
    "v_mul_f32 v170, v170, v178\n\t" \
    "v_mul_f32 v171, v171, v179\n\t" \
    "s_waitcnt vmcnt(0)\n\t" \
    "v_cvt_pk_bf16_f32 v184, v168, v169\n\t" \
    "v_cvt_pk_bf16_f32 v185, v170, v171\n\t" \
    "v_min_u32 v184, -2, v184\n\t" \
    "v_min_u32 v185, -2, v185\n\t" \
    "s_cmp_eq_u32 s40, s42\n\t" \
    "s_cselect_b64 s[44:45], s[58:59], 0\n\t" \
    "s_andn2_b64 exec, -1, s[44:45]\n\t" \
    "buffer_store_dwordx2 v[184:185], v80, %[rs], s49 offen sc1\n\t" \
    "s_mov_b64 exec, -1\n\t" \
    LST4 \
    "s_cmp_ge_u32 s40, 122\n\t" \
    "s_cbranch_scc1 .Lw116_%=\n\t" \
    "s_add_i32 s44, s40, s62\n\t" \
    "s_max_i32 s44, s44, 0\n\t" \
    "s_lshr_b32 s45, s44, 1\n\t" \
    "s_and_b32 s44, s44, 1\n\t" \
    "s_lshl_b32 s44, s44, 12\n\t" \
    "s_mul_i32 s51, s45, 0x14000\n\t" \
    "s_add_u32 s51, s51, s44\n\t" \
    "s_lshr_b32 s45, s45, 4\n\t" \
    "s_mul_i32 s45, s45, 0xc2000\n\t" \
    "s_add_u32 s51, s51, s45\n\t" \
    "s_add_i32 s44, s40, s63\n\t" \
    "s_max_i32 s44, s44, 0\n\t" \
    "s_lshr_b32 s45, s44, 1\n\t" \
    "s_and_b32 s44, s44, 1\n\t" \
    "s_lshl_b32 s44, s44, 12\n\t" \
    "s_mul_i32 s50, s45, 0x14000\n\t" \
    "s_add_u32 s50, s50, s44\n\t" \
    "s_lshr_b32 s45, s45, 4\n\t" \
    "s_mul_i32 s45, s45, 0xc2000\n\t" \
    "s_add_u32 s50, s50, s45\n\t" \
    "s_sub_u32 s50, s50, s51\n\t" \
    "s_add_u32 s51, s51, 0x2400000\n\t" \
    "v_mad_u32_u24 v81, v255, s50, v254\n\t" \
    "v_mov_b64 v[188:189], -1\n\t" \
    "s_add_u32 s44, s40, 6\n\t" \
    "s_cmp_eq_u32 s44, s42\n\t" \
    "s_cselect_b64 s[44:45], s[58:59], 0\n\t" \
    "s_andn2_b64 exec, -1, s[44:45]\n\t" \
    "buffer_store_dwordx2 v[188:189], v81, %[rs], s51 offen sc1\n\t" \
    "s_mov_b64 exec, -1\n\t" \
    ".Lw116_%=:\n\t" \
    "v_xor_b32 v249, 0x8000, v249\n\t" \
    "v_xor_b32 v250, 0x8000, v250\n\t" \
    "v_xor_b32 v251, 0x800, v251\n\t" \
    "v_xor_b32 v252, 0x800, v252\n\t" \
    LFLUSHW \
    "s_add_u32 s40, s40, 1\n\t" \
    "s_cmp_lg_u32 s40, 128\n\t" \
    "s_cbranch_scc1 .Lw14_%=\n\t" \
    "s_branch .Lend%=\n\t" \
    ".Lend%=:\n\t" \
    "s_waitcnt vmcnt(0) lgkmcnt(0)\n\t" \
    "s_mov_b32 %[bud], s41\n\t"
#define ASM_W2_LOOP \
    "s_mov_b32 s40, 0\n\t" \
    "s_mov_b32 s41, 0x20000\n\t" \
    LSTINIT \
    "s_add_u32 m0, %[wm], 0x0\n\t" \
    "s_add_u32 s44, %[wo], 0x0\n\t" \
    "buffer_load_dwordx4 v176, %[wr], s44 offen nt lds\n\t" \
    "s_add_u32 m0, %[wm], 0x2000\n\t" \
    "s_add_u32 s44, %[wo], 0xa000\n\t" \
    "buffer_load_dwordx4 v176, %[wr], s44 offen nt lds\n\t" \
    "s_add_u32 m0, %[wm], 0x4000\n\t" \
    "s_add_u32 s44, %[wo], 0x14000\n\t" \
    "buffer_load_dwordx4 v176, %[wr], s44 offen nt lds\n\t" \
    "s_add_u32 m0, %[wm], 0x6000\n\t" \
    "s_add_u32 s44, %[wo], 0x1e000\n\t" \
    "buffer_load_dwordx4 v176, %[wr], s44 offen nt lds\n\t" \
    "s_add_u32 m0, %[wm], 0x8000\n\t" \
    "s_add_u32 s44, %[wo], 0x28000\n\t" \
    "buffer_load_dwordx4 v176, %[wr], s44 offen nt lds\n\t" \
    "s_add_u32 m0, %[wm], 0xa000\n\t" \
    "s_add_u32 s44, %[wo], 0x32000\n\t" \
    "buffer_load_dwordx4 v176, %[wr], s44 offen nt lds\n\t" \
    "s_add_u32 m0, %[wm], 0xc000\n\t" \
    "s_add_u32 s44, %[wo], 0x3c000\n\t" \
    "buffer_load_dwordx4 v176, %[wr], s44 offen nt lds\n\t" \
    "s_add_u32 m0, %[wm], 0xe000\n\t" \
    "s_add_u32 s44, %[wo], 0x46000\n\t" \
    "buffer_load_dwordx4 v176, %[wr], s44 offen nt lds\n\t" \
    "s_add_u32 m0, %[wm], 0x10000\n\t" \
    "s_add_u32 s44, %[wo], 0x50000\n\t" \
    "buffer_load_dwordx4 v176, %[wr], s44 offen nt lds\n\t" \
    "s_add_u32 m0, %[wm], 0x12000\n\t" \
    "s_add_u32 s44, %[wo], 0x5a000\n\t" \
    "buffer_load_dwordx4 v176, %[wr], s44 offen nt lds\n\t" \
    "s_add_u32 m0, %[wm], 0x14000\n\t" \
    "s_add_u32 s44, %[wo], 0x64000\n\t" \
    "buffer_load_dwordx4 v176, %[wr], s44 offen nt lds\n\t" \
    "s_add_u32 m0, %[wm], 0x16000\n\t" \
    "s_add_u32 s44, %[wo], 0x6e000\n\t" \
    "buffer_load_dwordx4 v176, %[wr], s44 offen nt lds\n\t" \
    "s_add_u32 m0, %[wm], 0x18000\n\t" \
    "s_add_u32 s44, %[wo], 0x78000\n\t" \
    "buffer_load_dwordx4 v176, %[wr], s44 offen nt lds\n\t" \
    "s_add_u32 m0, %[wm], 0x1a000\n\t" \
    "s_add_u32 s44, %[wo], 0x82000\n\t" \
    "buffer_load_dwordx4 v176, %[wr], s44 offen nt lds\n\t" \
    "s_add_u32 m0, %[wm], 0x1c000\n\t" \
    "s_add_u32 s44, %[wo], 0x8c000\n\t" \
    "buffer_load_dwordx4 v176, %[wr], s44 offen nt lds\n\t" \
    "s_add_u32 m0, %[wm], 0x1e000\n\t" \
    "s_add_u32 s44, %[wo], 0x96000\n\t" \
    "buffer_load_dwordx4 v176, %[wr], s44 offen nt lds\n\t" \
    "s_add_u32 m0, %[wm], 0x20000\n\t" \
    "s_add_u32 s44, %[wo], 0xa0000\n\t" \
    "buffer_load_dwordx4 v176, %[wr], s44 offen nt lds\n\t" \
    "s_add_u32 m0, %[wm], 0x22000\n\t" \
    "s_add_u32 s44, %[wo], 0xaa000\n\t" \
    "buffer_load_dwordx4 v176, %[wr], s44 offen nt lds\n\t" \
    "s_add_u32 m0, %[wm], 0x24000\n\t" \
    "s_add_u32 s44, %[wo], 0xb4000\n\t" \
    "buffer_load_dwordx4 v176, %[wr], s44 offen nt lds\n\t" \
    "s_add_u32 m0, %[wm], 0x26000\n\t" \
    "s_add_u32 s44, %[wo], 0xbe000\n\t" \
    "buffer_load_dwordx4 v176, %[wr], s44 offen nt lds\n\t" \
    "s_waitcnt vmcnt(0) lgkmcnt(0)\n\t" \
    "s_barrier \n\t" \
    "v_add_u32 v178, 0x0, v177\n\t" \
    "ds_read_b64_tr_b16 v[0:1], v178 offset:0\n\t" \
    "ds_read_b64_tr_b16 v[2:3], v178 offset:1024\n\t" \
    "ds_read_b64_tr_b16 v[4:5], v178 offset:8192\n\t" \
    "ds_read_b64_tr_b16 v[6:7], v178 offset:9216\n\t" \
    "ds_read_b64_tr_b16 v[8:9], v178 offset:16384\n\t" \
    "ds_read_b64_tr_b16 v[10:11], v178 offset:17408\n\t" \
    "ds_read_b64_tr_b16 v[12:13], v178 offset:24576\n\t" \
    "ds_read_b64_tr_b16 v[14:15], v178 offset:25600\n\t" \
    "v_add_u32 v178, 0x8000, v177\n\t" \
    "ds_read_b64_tr_b16 v[16:17], v178 offset:0\n\t" \
    "ds_read_b64_tr_b16 v[18:19], v178 offset:1024\n\t" \
    "ds_read_b64_tr_b16 v[20:21], v178 offset:8192\n\t" \
    "ds_read_b64_tr_b16 v[22:23], v178 offset:9216\n\t" \
    "ds_read_b64_tr_b16 v[24:25], v178 offset:16384\n\t" \
    "ds_read_b64_tr_b16 v[26:27], v178 offset:17408\n\t" \
    "ds_read_b64_tr_b16 v[28:29], v178 offset:24576\n\t" \
    "ds_read_b64_tr_b16 v[30:31], v178 offset:25600\n\t" \
    "v_add_u32 v178, 0x10000, v177\n\t" \
    "ds_read_b64_tr_b16 v[32:33], v178 offset:0\n\t" \
    "ds_read_b64_tr_b16 v[34:35], v178 offset:1024\n\t" \
    "ds_read_b64_tr_b16 v[36:37], v178 offset:8192\n\t" \
    "ds_read_b64_tr_b16 v[38:39], v178 offset:9216\n\t" \
    "ds_read_b64_tr_b16 v[40:41], v178 offset:16384\n\t" \
    "ds_read_b64_tr_b16 v[42:43], v178 offset:17408\n\t" \
    "ds_read_b64_tr_b16 v[44:45], v178 offset:24576\n\t" \
    "ds_read_b64_tr_b16 v[46:47], v178 offset:25600\n\t" \
    "v_add_u32 v178, 0x18000, v177\n\t" \
    "ds_read_b64_tr_b16 v[48:49], v178 offset:0\n\t" \
    "ds_read_b64_tr_b16 v[50:51], v178 offset:1024\n\t" \
    "ds_read_b64_tr_b16 v[52:53], v178 offset:8192\n\t" \
    "ds_read_b64_tr_b16 v[54:55], v178 offset:9216\n\t" \
    "ds_read_b64_tr_b16 v[56:57], v178 offset:16384\n\t" \
    "ds_read_b64_tr_b16 v[58:59], v178 offset:17408\n\t" \
    "ds_read_b64_tr_b16 v[60:61], v178 offset:24576\n\t" \
    "ds_read_b64_tr_b16 v[62:63], v178 offset:25600\n\t" \
    "v_add_u32 v178, 0x20000, v177\n\t" \
    "ds_read_b64_tr_b16 v[64:65], v178 offset:0\n\t" \
    "ds_read_b64_tr_b16 v[66:67], v178 offset:1024\n\t" \
    "ds_read_b64_tr_b16 v[68:69], v178 offset:8192\n\t" \
    "ds_read_b64_tr_b16 v[70:71], v178 offset:9216\n\t" \
    "ds_read_b64_tr_b16 v[72:73], v178 offset:16384\n\t" \
    "ds_read_b64_tr_b16 v[74:75], v178 offset:17408\n\t" \
    "ds_read_b64_tr_b16 v[76:77], v178 offset:24576\n\t" \
    "ds_read_b64_tr_b16 v[78:79], v178 offset:25600\n\t" \
    "v_mov_b32 v220, 0x3f803f80\n\t" \
    "v_mov_b32 v221, 0x3f803f80\n\t" \
    "v_mov_b32 v222, 0x3f803f80\n\t" \
    "v_mov_b32 v223, 0x3f803f80\n\t" \
    "v_mov_b32 v216, 0\n\t" \
    "v_mov_b32 v217, 0\n\t" \
    "v_mov_b32 v218, 0\n\t" \
    "v_mov_b32 v219, 0\n\t" \
    "s_waitcnt lgkmcnt(0)\n\t" \
    "s_barrier \n\t" \
    ".Lw21_%=:\n\t" \
    "v_mov_b32 v248, 0\n\t" \
    "buffer_load_dword v248, v248, %[rs], %[go] offen sc1\n\t" \
    "s_waitcnt vmcnt(0)\n\t" \
    "v_cmp_ne_u32 vcc, 0, v248\n\t" \
    "s_cbranch_vccnz .Lw22_%=\n\t" \
    "s_cmp_eq_u32 s41, 0\n\t" \
    "s_cbranch_scc1 .Lw22_%=\n\t" \
    "s_sub_u32 s41, s41, 1\n\t" \
    "s_sleep 16\n\t" \
    "s_branch .Lw21_%=\n\t" \
    ".Lw22_%=:\n\t" \
    "s_mov_b32 s43, 0\n\t" \
    "s_add_u32 s46, s43, %[pi]\n\t" \
    "s_add_u32 s47, s46, 0x8000\n\t" \
    "s_add_u32 s48, s46, 0x10000\n\t" \
    "buffer_load_dwordx4 v[208:211], v253, %[rs], s46 offen sc1\n\t" \
    "buffer_load_dwordx4 v[212:215], v253, %[rs], s47 offen sc1\n\t" \
    "s_cmp_gt_u32 %[wave], 3\n\t" \
    "s_cbranch_scc1 .Lw23_%=\n\t" \
    "buffer_load_dwordx4 v[216:219], v253, %[rs], s48 offen sc1\n\t" \
    ".Lw23_%=:\n\t" \
    "s_waitcnt vmcnt(0)\n\t" \
    ".Lw24_%=:\n\t" \
    "s_add_u32 s49, s43, 0x2c00000\n\t" \
    "s_add_u32 s50, s43, 0x1c00000\n\t" \
    "s_lshr_b32 s44, s40, 6\n\t" \
    "s_lshl_b32 s44, s44, 14\n\t" \
    "s_and_b32 s45, s40, 63\n\t" \
    "s_lshl_b32 s45, s45, 7\n\t" \
    "s_add_u32 s44, s44, s45\n\t" \
    "s_add_u32 s51, s44, 0x4d0000\n\t" \
    "s_add_i32 s44, s40, 1\n\t" \
    "s_max_i32 s44, s44, 0\n\t" \
    "s_lshr_b32 s45, s44, 1\n\t" \
    "s_and_b32 s44, s44, 1\n\t" \
    "s_lshl_b32 s44, s44, 12\n\t" \
    "s_mul_i32 s43, s45, 0x14000\n\t" \
    "s_add_u32 s43, s43, s44\n\t" \
    "s_lshr_b32 s45, s45, 4\n\t" \
    "s_mul_i32 s45, s45, 0xc2000\n\t" \
    "s_add_u32 s43, s43, s45\n\t" \
    LST0 \
    "v_max3_u32 v248, v208, v209, v210\n\t" \
    "v_max3_u32 v248, v211, v212, v248\n\t" \
    "v_max3_u32 v248, v213, v214, v248\n\t" \
    "v_max3_u32 v248, v215, v216, v248\n\t" \
    "v_max3_u32 v248, v217, v218, v248\n\t" \
    "v_max_u32 v248, v219, v248\n\t" \
    "v_cmp_eq_u32 vcc, -1, v248\n\t" \
    "s_cbranch_vccz .Lw26_%=\n\t" \
    ".Lw25_%=:\n\t" \
    "v_max_u32 v248, v208, v209\n\t" \
    "v_max3_u32 v248, v210, v211, v248\n\t" \
    "v_cmp_eq_u32 vcc, -1, v248\n\t" \
    "s_cbranch_vccz .Lw27_%=\n\t" \
    "buffer_load_dwordx4 v[208:211], v253, %[rs], s46 offen sc1\n\t" \
    ".Lw27_%=:\n\t" \
    "v_max_u32 v248, v212, v213\n\t" \
    "v_max3_u32 v248, v214, v215, v248\n\t" \
    "v_cmp_eq_u32 vcc, -1, v248\n\t" \
    "s_cbranch_vccz .Lw28_%=\n\t" \
    "buffer_load_dwordx4 v[212:215], v253, %[rs], s47 offen sc1\n\t" \
    ".Lw28_%=:\n\t" \
    "v_max_u32 v248, v216, v217\n\t" \
    "v_max3_u32 v248, v218, v219, v248\n\t" \
    "v_cmp_eq_u32 vcc, -1, v248\n\t" \
    "s_cbranch_vccz .Lw29_%=\n\t" \
    "buffer_load_dwordx4 v[216:219], v253, %[rs], s48 offen sc1\n\t" \
    ".Lw29_%=:\n\t" \
    "s_waitcnt vmcnt(20)\n\t" \
    "v_max3_u32 v248, v208, v209, v210\n\t" \
    "v_max3_u32 v248, v211, v212, v248\n\t" \
    "v_max3_u32 v248, v213, v214, v248\n\t" \
    "v_max3_u32 v248, v215, v216, v248\n\t" \
    "v_max3_u32 v248, v217, v218, v248\n\t" \
    "v_max_u32 v248, v219, v248\n\t" \
    "v_cmp_eq_u32 vcc, -1, v248\n\t" \
    "s_cbranch_vccz .Lw26_%=\n\t" \
    "s_sleep 1\n\t" \
    "v_max3_u32 v248, v208, v209, v210\n\t" \
    "v_max3_u32 v248, v211, v212, v248\n\t" \
    "v_max3_u32 v248, v213, v214, v248\n\t" \
    "v_max3_u32 v248, v215, v216, v248\n\t" \
    "v_max3_u32 v248, v217, v218, v248\n\t" \
    "v_max_u32 v248, v219, v248\n\t" \
    "v_cmp_eq_u32 vcc, -1, v248\n\t" \
    "s_cbranch_vccz .Lw26_%=\n\t" \
    "s_sleep 1\n\t" \
    "v_max3_u32 v248, v208, v209, v210\n\t" \
    "v_max3_u32 v248, v211, v212, v248\n\t" \
    "v_max3_u32 v248, v213, v214, v248\n\t" \
    "v_max3_u32 v248, v215, v216, v248\n\t" \
    "v_max3_u32 v248, v217, v218, v248\n\t" \
    "v_max_u32 v248, v219, v248\n\t" \
    "v_cmp_eq_u32 vcc, -1, v248\n\t" \
    "s_cbranch_vccz .Lw26_%=\n\t" \
    "s_sleep 1\n\t" \
    "v_max3_u32 v248, v208, v209, v210\n\t" \
    "v_max3_u32 v248, v211, v212, v248\n\t" \
    "v_max3_u32 v248, v213, v214, v248\n\t" \
    "v_max3_u32 v248, v215, v216, v248\n\t" \
    "v_max3_u32 v248, v217, v218, v248\n\t" \
    "v_max_u32 v248, v219, v248\n\t" \
    "v_cmp_eq_u32 vcc, -1, v248\n\t" \
    "s_cbranch_vccz .Lw26_%=\n\t" \
    "s_sleep 1\n\t" \
    "v_max3_u32 v248, v208, v209, v210\n\t" \
    "v_max3_u32 v248, v211, v212, v248\n\t" \
    "v_max3_u32 v248, v213, v214, v248\n\t" \
    "v_max3_u32 v248, v215, v216, v248\n\t" \
    "v_max3_u32 v248, v217, v218, v248\n\t" \
    "v_max_u32 v248, v219, v248\n\t" \
    "v_cmp_eq_u32 vcc, -1, v248\n\t" \
    "s_cbranch_vccz .Lw26_%=\n\t" \
    "s_sleep 1\n\t" \
    "v_max3_u32 v248, v208, v209, v210\n\t" \
    "v_max3_u32 v248, v211, v212, v248\n\t" \
    "v_max3_u32 v248, v213, v214, v248\n\t" \
    "v_max3_u32 v248, v215, v216, v248\n\t" \
    "v_max3_u32 v248, v217, v218, v248\n\t" \
    "v_max_u32 v248, v219, v248\n\t" \
    "v_cmp_eq_u32 vcc, -1, v248\n\t" \
    "s_cbranch_vccz .Lw26_%=\n\t" \
    "s_sleep 1\n\t" \
    "s_cmp_eq_u32 s41, 0\n\t" \
    "s_cbranch_scc1 .Lw26_%=\n\t" \
    "s_sub_u32 s41, s41, 1\n\t" \
    "s_branch .Lw25_%=\n\t" \
    ".Lw26_%=:\n\t" \
    LST1 \
    "ds_write_b128 v250, v[208:211]\n\t" \
    "ds_write_b128 v250, v[212:215] offset:8192\n\t" \
    "s_cmp_gt_u32 %[wave], 3\n\t" \
    "s_cbranch_scc1 .Lw210_%=\n\t" \
    "ds_write_b128 v250, v[216:219] offset:16384\n\t" \
    ".Lw210_%=:\n\t" \
    "s_add_u32 s46, s43, %[pi]\n\t" \
    "s_add_u32 s47, s46, 0x8000\n\t" \
    "s_add_u32 s48, s46, 0x10000\n\t" \
    "s_waitcnt lgkmcnt(0)\n\t" \
    "s_barrier \n\t" \
    LST2 \
    "ds_read_b128 v[176:179], v249 offset:0\n\t" \
    "buffer_load_dwordx2 v[232:233], v254, %[rs], s50 offen sc1\n\t" \
    "buffer_load_dwordx2 v[234:235], v251, %[rs], s51 offen sc1\n\t" \
    "ds_read_b128 v[180:183], v249 offset:1024\n\t" \
    "ds_read_b128 v[184:187], v249 offset:2048\n\t" \
    "ds_read_b128 v[188:191], v249 offset:3072\n\t" \
    "ds_read_b128 v[192:195], v249 offset:4096\n\t" \
    "ds_read_b128 v[196:199], v249 offset:5120\n\t" \
    "ds_read_b128 v[200:203], v249 offset:6144\n\t" \
    "buffer_load_dwordx4 v[208:211], v253, %[rs], s46 offen sc1\n\t" \
    "buffer_load_dwordx4 v[212:215], v253, %[rs], s47 offen sc1\n\t" \
    "s_cmp_gt_u32 %[wave], 3\n\t" \
    "s_cbranch_scc1 .Lw211_%=\n\t" \
    "buffer_load_dwordx4 v[216:219], v253, %[rs], s48 offen sc1\n\t" \
    ".Lw211_%=:\n\t" \
    "s_waitcnt lgkmcnt(6)\n\t" \
    "v_mfma_f32_16x16x32_bf16 v[168:171], v[0:3], v[176:179], 0\n\t" \
    "ds_read_b128 v[204:207], v249 offset:7168\n\t" \
    "s_waitcnt lgkmcnt(6)\n\t" \
    "v_mfma_f32_16x16x32_bf16 v[168:171], v[4:7], v[180:183], v[168:171]\n\t" \
    "ds_read_b128 v[176:179], v249 offset:8192\n\t" \
    "s_waitcnt lgkmcnt(6)\n\t" \
    "v_mfma_f32_16x16x32_bf16 v[168:171], v[8:11], v[184:187], v[168:171]\n\t" \
    "ds_read_b128 v[180:183], v249 offset:9216\n\t" \
    "s_waitcnt lgkmcnt(6)\n\t" \
    "v_mfma_f32_16x16x32_bf16 v[168:171], v[12:15], v[188:191], v[168:171]\n\t" \
    "ds_read_b128 v[184:187], v249 offset:10240\n\t" \
    "s_waitcnt lgkmcnt(6)\n\t" \
    "v_mfma_f32_16x16x32_bf16 v[168:171], v[16:19], v[192:195], v[168:171]\n\t" \
    "ds_read_b128 v[188:191], v249 offset:11264\n\t" \
    "s_waitcnt lgkmcnt(6)\n\t" \
    "v_mfma_f32_16x16x32_bf16 v[168:171], v[20:23], v[196:199], v[168:171]\n\t" \
    "ds_read_b128 v[192:195], v249 offset:12288\n\t" \
    "s_waitcnt lgkmcnt(6)\n\t" \
    "v_mfma_f32_16x16x32_bf16 v[168:171], v[24:27], v[200:203], v[168:171]\n\t" \
    "ds_read_b128 v[196:199], v249 offset:13312\n\t" \
    "s_waitcnt lgkmcnt(6)\n\t" \
    "v_mfma_f32_16x16x32_bf16 v[168:171], v[28:31], v[204:207], v[168:171]\n\t" \
    "ds_read_b128 v[200:203], v249 offset:14336\n\t" \
    "s_waitcnt lgkmcnt(6)\n\t" \
    "v_mfma_f32_16x16x32_bf16 v[168:171], v[32:35], v[176:179], v[168:171]\n\t" \
    "ds_read_b128 v[204:207], v249 offset:15360\n\t" \
    "s_waitcnt lgkmcnt(6)\n\t" \
    "v_mfma_f32_16x16x32_bf16 v[168:171], v[36:39], v[180:183], v[168:171]\n\t" \
    "ds_read_b128 v[176:179], v249 offset:16384\n\t" \
    "s_waitcnt lgkmcnt(6)\n\t" \
    "v_mfma_f32_16x16x32_bf16 v[168:171], v[40:43], v[184:187], v[168:171]\n\t" \
    "ds_read_b128 v[180:183], v249 offset:17408\n\t" \
    "s_waitcnt lgkmcnt(6)\n\t" \
    "v_mfma_f32_16x16x32_bf16 v[168:171], v[44:47], v[188:191], v[168:171]\n\t" \
    "ds_read_b128 v[184:187], v249 offset:18432\n\t" \
    "s_waitcnt lgkmcnt(6)\n\t" \
    "v_mfma_f32_16x16x32_bf16 v[168:171], v[48:51], v[192:195], v[168:171]\n\t" \
    "ds_read_b128 v[188:191], v249 offset:19456\n\t" \
    "s_waitcnt lgkmcnt(6)\n\t" \
    "v_mfma_f32_16x16x32_bf16 v[168:171], v[52:55], v[196:199], v[168:171]\n\t" \
    "s_waitcnt lgkmcnt(5)\n\t" \
    "v_mfma_f32_16x16x32_bf16 v[168:171], v[56:59], v[200:203], v[168:171]\n\t" \
    "s_waitcnt lgkmcnt(4)\n\t" \
    "v_mfma_f32_16x16x32_bf16 v[168:171], v[60:63], v[204:207], v[168:171]\n\t" \
    "s_waitcnt lgkmcnt(3)\n\t" \
    "v_mfma_f32_16x16x32_bf16 v[168:171], v[64:67], v[176:179], v[168:171]\n\t" \
    "s_waitcnt lgkmcnt(2)\n\t" \
    "v_mfma_f32_16x16x32_bf16 v[168:171], v[68:71], v[180:183], v[168:171]\n\t" \
    "s_waitcnt lgkmcnt(1)\n\t" \
    "v_mfma_f32_16x16x32_bf16 v[168:171], v[72:75], v[184:187], v[168:171]\n\t" \
    "s_waitcnt lgkmcnt(0)\n\t" \
    "v_mfma_f32_16x16x32_bf16 v[168:171], v[76:79], v[188:191], v[168:171]\n\t" \
    LST3 \
    "s_waitcnt vmcnt(0)\n\t" \
    ".Lw212_%=:\n\t" \
    "v_max_u32 v248, v234, v235\n\t" \
    "v_cmp_eq_u32 vcc, -1, v248\n\t" \
    "s_cbranch_vccz .Lw213_%=\n\t" \
    "s_cmp_eq_u32 s41, 0\n\t" \
    "s_cbranch_scc1 .Lw213_%=\n\t" \
    "s_sub_u32 s41, s41, 1\n\t" \
    "buffer_load_dwordx2 v[234:235], v251, %[rs], s51 offen sc1\n\t" \
    "s_waitcnt vmcnt(0)\n\t" \
    "s_branch .Lw212_%=\n\t" \
    ".Lw213_%=:\n\t" \
    "v_lshlrev_b32 v176, 16, v232\n\t" \
    "v_and_b32 v177, 0xffff0000, v232\n\t" \
    "v_lshlrev_b32 v178, 16, v233\n\t" \
    "v_and_b32 v179, 0xffff0000, v233\n\t" \
    "v_sub_f32 v176, v176, v234\n\t" \
    "v_sub_f32 v177, v177, v234\n\t" \
    "v_sub_f32 v178, v178, v234\n\t" \
    "v_sub_f32 v179, v179, v234\n\t" \
    "v_mul_f32 v176, v176, v235\n\t" \
    "v_mul_f32 v177, v177, v235\n\t" \
    "v_mul_f32 v178, v178, v235\n\t" \
    "v_mul_f32 v179, v179, v235\n\t" \
    "v_cvt_pk_bf16_f32 v180, v176, v177\n\t" \
    "v_cvt_pk_bf16_f32 v181, v178, v179\n\t" \
    "v_lshlrev_b32 v176, 16, v180\n\t" \
    "v_and_b32 v177, 0xffff0000, v180\n\t" \
    "v_lshlrev_b32 v178, 16, v181\n\t" \
    "v_and_b32 v179, 0xffff0000, v181\n\t" \
    "v_add_f32 v168, v176, v168\n\t" \
    "v_add_f32 v169, v177, v169\n\t" \
    "v_add_f32 v170, v178, v170\n\t" \
    "v_add_f32 v171, v179, v171\n\t" \
    "v_cvt_pk_bf16_f32 v184, v168, v169\n\t" \
    "v_cvt_pk_bf16_f32 v185, v170, v171\n\t" \
    "v_min_u32 v184, -2, v184\n\t" \
    "v_min_u32 v185, -2, v185\n\t" \
    "buffer_store_dwordx2 v[184:185], v254, %[rs], s49 offen sc1\n\t" \
    LST4 \
    "s_cmp_ge_u32 s40, 122\n\t" \
    "s_cbranch_scc1 .Lw214_%=\n\t" \
    "s_add_i32 s44, s40, 6\n\t" \
    "s_max_i32 s44, s44, 0\n\t" \
    "s_lshr_b32 s45, s44, 1\n\t" \
    "s_and_b32 s44, s44, 1\n\t" \
    "s_lshl_b32 s44, s44, 12\n\t" \
    "s_mul_i32 s50, s45, 0x14000\n\t" \
    "s_add_u32 s50, s50, s44\n\t" \
    "s_lshr_b32 s45, s45, 4\n\t" \
    "s_mul_i32 s45, s45, 0xc2000\n\t" \
    "s_add_u32 s50, s50, s45\n\t" \
    "s_add_u32 s50, s50, 0x2c00000\n\t" \
    "v_mov_b64 v[188:189], -1\n\t" \
    "buffer_store_dwordx2 v[188:189], v254, %[rs], s50 offen sc1\n\t" \
    ".Lw214_%=:\n\t" \
    "v_xor_b32 v249, 0x8000, v249\n\t" \
    "v_xor_b32 v250, 0x8000, v250\n\t" \
    LFLUSHW \
    "s_add_u32 s40, s40, 1\n\t" \
    "s_cmp_lg_u32 s40, 128\n\t" \
    "s_cbranch_scc1 .Lw24_%=\n\t" \
    "s_branch .Lend%=\n\t" \
    ".Lend%=:\n\t" \
    "s_waitcnt vmcnt(0) lgkmcnt(0)\n\t" \
    "s_mov_b32 %[bud], s41\n\t"
#define ASM_W3_LOOP \
    "s_mov_b32 s40, 0\n\t" \
    "s_mov_b32 s41, 0x20000\n\t" \
    LSTINIT \
    "s_add_u32 m0, %[wm], 0x0\n\t" \
    "s_add_u32 s44, %[wo], 0x0\n\t" \
    "buffer_load_dwordx4 v176, %[wr], s44 offen nt lds\n\t" \
    "s_add_u32 m0, %[wm], 0x2000\n\t" \
    "s_add_u32 s44, %[wo], 0xc000\n\t" \
    "buffer_load_dwordx4 v176, %[wr], s44 offen nt lds\n\t" \
    "s_add_u32 m0, %[wm], 0x4000\n\t" \
    "s_add_u32 s44, %[wo], 0x18000\n\t" \
    "buffer_load_dwordx4 v176, %[wr], s44 offen nt lds\n\t" \
    "s_add_u32 m0, %[wm], 0x6000\n\t" \
    "s_add_u32 s44, %[wo], 0x24000\n\t" \
    "buffer_load_dwordx4 v176, %[wr], s44 offen nt lds\n\t" \
    "s_add_u32 m0, %[wm], 0x8000\n\t" \
    "s_add_u32 s44, %[wo], 0x30000\n\t" \
    "buffer_load_dwordx4 v176, %[wr], s44 offen nt lds\n\t" \
    "s_add_u32 m0, %[wm], 0xa000\n\t" \
    "s_add_u32 s44, %[wo], 0x3c000\n\t" \
    "buffer_load_dwordx4 v176, %[wr], s44 offen nt lds\n\t" \
    "s_add_u32 m0, %[wm], 0xc000\n\t" \
    "s_add_u32 s44, %[wo], 0x48000\n\t" \
    "buffer_load_dwordx4 v176, %[wr], s44 offen nt lds\n\t" \
    "s_add_u32 m0, %[wm], 0xe000\n\t" \
    "s_add_u32 s44, %[wo], 0x54000\n\t" \
    "buffer_load_dwordx4 v176, %[wr], s44 offen nt lds\n\t" \
    "s_add_u32 m0, %[wm], 0x10000\n\t" \
    "s_add_u32 s44, %[wo], 0x60000\n\t" \
    "buffer_load_dwordx4 v176, %[wr], s44 offen nt lds\n\t" \
    "s_add_u32 m0, %[wm], 0x12000\n\t" \
    "s_add_u32 s44, %[wo], 0x6c000\n\t" \
    "buffer_load_dwordx4 v176, %[wr], s44 offen nt lds\n\t" \
    "s_add_u32 m0, %[wm], 0x14000\n\t" \
    "s_add_u32 s44, %[wo], 0x78000\n\t" \
    "buffer_load_dwordx4 v176, %[wr], s44 offen nt lds\n\t" \
    "s_add_u32 m0, %[wm], 0x16000\n\t" \
    "s_add_u32 s44, %[wo], 0x84000\n\t" \
    "buffer_load_dwordx4 v176, %[wr], s44 offen nt lds\n\t" \
    "s_add_u32 m0, %[wm], 0x18000\n\t" \
    "s_add_u32 s44, %[wo], 0x90000\n\t" \
    "buffer_load_dwordx4 v176, %[wr], s44 offen nt lds\n\t" \
    "s_add_u32 m0, %[wm], 0x1a000\n\t" \
    "s_add_u32 s44, %[wo], 0x9c000\n\t" \
    "buffer_load_dwordx4 v176, %[wr], s44 offen nt lds\n\t" \
    "s_add_u32 m0, %[wm], 0x1c000\n\t" \
    "s_add_u32 s44, %[wo], 0xa8000\n\t" \
    "buffer_load_dwordx4 v176, %[wr], s44 offen nt lds\n\t" \
    "s_add_u32 m0, %[wm], 0x1e000\n\t" \
    "s_add_u32 s44, %[wo], 0xb4000\n\t" \
    "buffer_load_dwordx4 v176, %[wr], s44 offen nt lds\n\t" \
    "s_add_u32 m0, %[wm], 0x20000\n\t" \
    "s_add_u32 s44, %[wo], 0xc0000\n\t" \
    "buffer_load_dwordx4 v176, %[wr], s44 offen nt lds\n\t" \
    "s_add_u32 m0, %[wm], 0x22000\n\t" \
    "s_add_u32 s44, %[wo], 0xcc000\n\t" \
    "buffer_load_dwordx4 v176, %[wr], s44 offen nt lds\n\t" \
    "s_add_u32 m0, %[wm], 0x24000\n\t" \
    "s_add_u32 s44, %[wo], 0xd8000\n\t" \
    "buffer_load_dwordx4 v176, %[wr], s44 offen nt lds\n\t" \
    "s_add_u32 m0, %[wm], 0x26000\n\t" \
    "s_add_u32 s44, %[wo], 0xe4000\n\t" \
    "buffer_load_dwordx4 v176, %[wr], s44 offen nt lds\n\t" \
    "s_waitcnt vmcnt(0) lgkmcnt(0)\n\t" \
    "s_barrier \n\t" \
    "v_add_u32 v178, 0x0, v177\n\t" \
    "v_xor_b32 v179, 32, v178\n\t" \
    "ds_read_b64_tr_b16 v[0:1], v178 offset:0\n\t" \
    "ds_read_b64_tr_b16 v[2:3], v178 offset:2048\n\t" \
    "ds_read_b64_tr_b16 v[80:81], v179 offset:0\n\t" \
    "ds_read_b64_tr_b16 v[82:83], v179 offset:2048\n\t" \
    "ds_read_b64_tr_b16 v[4:5], v178 offset:16384\n\t" \
    "ds_read_b64_tr_b16 v[6:7], v178 offset:18432\n\t" \
    "ds_read_b64_tr_b16 v[84:85], v179 offset:16384\n\t" \
    "ds_read_b64_tr_b16 v[86:87], v179 offset:18432\n\t" \
    "v_add_u32 v178, 0x8000, v177\n\t" \
    "v_xor_b32 v179, 32, v178\n\t" \
    "ds_read_b64_tr_b16 v[8:9], v178 offset:0\n\t" \
    "ds_read_b64_tr_b16 v[10:11], v178 offset:2048\n\t" \
    "ds_read_b64_tr_b16 v[88:89], v179 offset:0\n\t" \
    "ds_read_b64_tr_b16 v[90:91], v179 offset:2048\n\t" \
    "ds_read_b64_tr_b16 v[12:13], v178 offset:16384\n\t" \
    "ds_read_b64_tr_b16 v[14:15], v178 offset:18432\n\t" \
    "ds_read_b64_tr_b16 v[92:93], v179 offset:16384\n\t" \
    "ds_read_b64_tr_b16 v[94:95], v179 offset:18432\n\t" \
    "v_add_u32 v178, 0x10000, v177\n\t" \
    "v_xor_b32 v179, 32, v178\n\t" \
    "ds_read_b64_tr_b16 v[16:17], v178 offset:0\n\t" \
    "ds_read_b64_tr_b16 v[18:19], v178 offset:2048\n\t" \
    "ds_read_b64_tr_b16 v[96:97], v179 offset:0\n\t" \
    "ds_read_b64_tr_b16 v[98:99], v179 offset:2048\n\t" \
    "ds_read_b64_tr_b16 v[20:21], v178 offset:16384\n\t" \
    "ds_read_b64_tr_b16 v[22:23], v178 offset:18432\n\t" \
    "ds_read_b64_tr_b16 v[100:101], v179 offset:16384\n\t" \
    "ds_read_b64_tr_b16 v[102:103], v179 offset:18432\n\t" \
    "v_add_u32 v178, 0x18000, v177\n\t" \
    "v_xor_b32 v179, 32, v178\n\t" \
    "ds_read_b64_tr_b16 v[24:25], v178 offset:0\n\t" \
    "ds_read_b64_tr_b16 v[26:27], v178 offset:2048\n\t" \
    "ds_read_b64_tr_b16 v[104:105], v179 offset:0\n\t" \
    "ds_read_b64_tr_b16 v[106:107], v179 offset:2048\n\t" \
    "ds_read_b64_tr_b16 v[28:29], v178 offset:16384\n\t" \
    "ds_read_b64_tr_b16 v[30:31], v178 offset:18432\n\t" \
    "ds_read_b64_tr_b16 v[108:109], v179 offset:16384\n\t" \
    "ds_read_b64_tr_b16 v[110:111], v179 offset:18432\n\t" \
    "v_add_u32 v178, 0x20000, v177\n\t" \
    "v_xor_b32 v179, 32, v178\n\t" \
    "ds_read_b64_tr_b16 v[32:33], v178 offset:0\n\t" \
    "ds_read_b64_tr_b16 v[34:35], v178 offset:2048\n\t" \
    "ds_read_b64_tr_b16 v[112:113], v179 offset:0\n\t" \
    "ds_read_b64_tr_b16 v[114:115], v179 offset:2048\n\t" \
    "ds_read_b64_tr_b16 v[36:37], v178 offset:16384\n\t" \
    "ds_read_b64_tr_b16 v[38:39], v178 offset:18432\n\t" \
    "ds_read_b64_tr_b16 v[116:117], v179 offset:16384\n\t" \
    "ds_read_b64_tr_b16 v[118:119], v179 offset:18432\n\t" \
    "s_waitcnt lgkmcnt(0)\n\t" \
    "s_barrier \n\t" \
    "s_add_u32 m0, %[wm], 0x0\n\t" \
    "s_add_u32 s44, %[wo], 0xf0000\n\t" \
    "buffer_load_dwordx4 v176, %[wr], s44 offen nt lds\n\t" \
    "s_add_u32 m0, %[wm], 0x2000\n\t" \
    "s_add_u32 s44, %[wo], 0xfc000\n\t" \
    "buffer_load_dwordx4 v176, %[wr], s44 offen nt lds\n\t" \
    "s_add_u32 m0, %[wm], 0x4000\n\t" \
    "s_add_u32 s44, %[wo], 0x108000\n\t" \
    "buffer_load_dwordx4 v176, %[wr], s44 offen nt lds\n\t" \
    "s_add_u32 m0, %[wm], 0x6000\n\t" \
    "s_add_u32 s44, %[wo], 0x114000\n\t" \
    "buffer_load_dwordx4 v176, %[wr], s44 offen nt lds\n\t" \
    "s_add_u32 m0, %[wm], 0x8000\n\t" \
    "s_add_u32 s44, %[wo], 0x120000\n\t" \
    "buffer_load_dwordx4 v176, %[wr], s44 offen nt lds\n\t" \
    "s_add_u32 m0, %[wm], 0xa000\n\t" \
    "s_add_u32 s44, %[wo], 0x12c000\n\t" \
    "buffer_load_dwordx4 v176, %[wr], s44 offen nt lds\n\t" \
    "s_add_u32 m0, %[wm], 0xc000\n\t" \
    "s_add_u32 s44, %[wo], 0x138000\n\t" \
    "buffer_load_dwordx4 v176, %[wr], s44 offen nt lds\n\t" \
    "s_add_u32 m0, %[wm], 0xe000\n\t" \
    "s_add_u32 s44, %[wo], 0x144000\n\t" \
    "buffer_load_dwordx4 v176, %[wr], s44 offen nt lds\n\t" \
    "s_add_u32 m0, %[wm], 0x10000\n\t" \
    "s_add_u32 s44, %[wo], 0x150000\n\t" \
    "buffer_load_dwordx4 v176, %[wr], s44 offen nt lds\n\t" \
    "s_add_u32 m0, %[wm], 0x12000\n\t" \
    "s_add_u32 s44, %[wo], 0x15c000\n\t" \
    "buffer_load_dwordx4 v176, %[wr], s44 offen nt lds\n\t" \
    "s_add_u32 m0, %[wm], 0x14000\n\t" \
    "s_add_u32 s44, %[wo], 0x168000\n\t" \
    "buffer_load_dwordx4 v176, %[wr], s44 offen nt lds\n\t" \
    "s_add_u32 m0, %[wm], 0x16000\n\t" \
    "s_add_u32 s44, %[wo], 0x174000\n\t" \
    "buffer_load_dwordx4 v176, %[wr], s44 offen nt lds\n\t" \
    "s_add_u32 m0, %[wm], 0x18000\n\t" \
    "s_add_u32 s44, %[wo], 0x180000\n\t" \
    "buffer_load_dwordx4 v176, %[wr], s44 offen nt lds\n\t" \
    "s_add_u32 m0, %[wm], 0x1a000\n\t" \
    "s_add_u32 s44, %[wo], 0x18c000\n\t" \
    "buffer_load_dwordx4 v176, %[wr], s44 offen nt lds\n\t" \
    "s_add_u32 m0, %[wm], 0x1c000\n\t" \
    "s_add_u32 s44, %[wo], 0x198000\n\t" \
    "buffer_load_dwordx4 v176, %[wr], s44 offen nt lds\n\t" \
    "s_add_u32 m0, %[wm], 0x1e000\n\t" \
    "s_add_u32 s44, %[wo], 0x1a4000\n\t" \
    "buffer_load_dwordx4 v176, %[wr], s44 offen nt lds\n\t" \
    "s_add_u32 m0, %[wm], 0x20000\n\t" \
    "s_add_u32 s44, %[wo], 0x1b0000\n\t" \
    "buffer_load_dwordx4 v176, %[wr], s44 offen nt lds\n\t" \
    "s_add_u32 m0, %[wm], 0x22000\n\t" \
    "s_add_u32 s44, %[wo], 0x1bc000\n\t" \
    "buffer_load_dwordx4 v176, %[wr], s44 offen nt lds\n\t" \
    "s_add_u32 m0, %[wm], 0x24000\n\t" \
    "s_add_u32 s44, %[wo], 0x1c8000\n\t" \
    "buffer_load_dwordx4 v176, %[wr], s44 offen nt lds\n\t" \
    "s_add_u32 m0, %[wm], 0x26000\n\t" \
    "s_add_u32 s44, %[wo], 0x1d4000\n\t" \
    "buffer_load_dwordx4 v176, %[wr], s44 offen nt lds\n\t" \
    "s_waitcnt vmcnt(0) lgkmcnt(0)\n\t" \
    "s_barrier \n\t" \
    "v_add_u32 v178, 0x0, v177\n\t" \
    "v_xor_b32 v179, 32, v178\n\t" \
    "ds_read_b64_tr_b16 v[40:41], v178 offset:0\n\t" \
    "ds_read_b64_tr_b16 v[42:43], v178 offset:2048\n\t" \
    "ds_read_b64_tr_b16 v[120:121], v179 offset:0\n\t" \
    "ds_read_b64_tr_b16 v[122:123], v179 offset:2048\n\t" \
    "ds_read_b64_tr_b16 v[44:45], v178 offset:16384\n\t" \
    "ds_read_b64_tr_b16 v[46:47], v178 offset:18432\n\t" \
    "ds_read_b64_tr_b16 v[124:125], v179 offset:16384\n\t" \
    "ds_read_b64_tr_b16 v[126:127], v179 offset:18432\n\t" \
    "v_add_u32 v178, 0x8000, v177\n\t" \
    "v_xor_b32 v179, 32, v178\n\t" \
    "ds_read_b64_tr_b16 v[48:49], v178 offset:0\n\t" \
    "ds_read_b64_tr_b16 v[50:51], v178 offset:2048\n\t" \
    "ds_read_b64_tr_b16 v[128:129], v179 offset:0\n\t" \
    "ds_read_b64_tr_b16 v[130:131], v179 offset:2048\n\t" \
    "ds_read_b64_tr_b16 v[52:53], v178 offset:16384\n\t" \
    "ds_read_b64_tr_b16 v[54:55], v178 offset:18432\n\t" \
    "ds_read_b64_tr_b16 v[132:133], v179 offset:16384\n\t" \
    "ds_read_b64_tr_b16 v[134:135], v179 offset:18432\n\t" \
    "v_add_u32 v178, 0x10000, v177\n\t" \
    "v_xor_b32 v179, 32, v178\n\t" \
    "ds_read_b64_tr_b16 v[56:57], v178 offset:0\n\t" \
    "ds_read_b64_tr_b16 v[58:59], v178 offset:2048\n\t" \
    "ds_read_b64_tr_b16 v[136:137], v179 offset:0\n\t" \
    "ds_read_b64_tr_b16 v[138:139], v179 offset:2048\n\t" \
    "ds_read_b64_tr_b16 v[60:61], v178 offset:16384\n\t" \
    "ds_read_b64_tr_b16 v[62:63], v178 offset:18432\n\t" \
    "ds_read_b64_tr_b16 v[140:141], v179 offset:16384\n\t" \
    "ds_read_b64_tr_b16 v[142:143], v179 offset:18432\n\t" \
    "v_add_u32 v178, 0x18000, v177\n\t" \
    "v_xor_b32 v179, 32, v178\n\t" \
    "ds_read_b64_tr_b16 v[64:65], v178 offset:0\n\t" \
    "ds_read_b64_tr_b16 v[66:67], v178 offset:2048\n\t" \
    "ds_read_b64_tr_b16 v[144:145], v179 offset:0\n\t" \
    "ds_read_b64_tr_b16 v[146:147], v179 offset:2048\n\t" \
    "ds_read_b64_tr_b16 v[68:69], v178 offset:16384\n\t" \
    "ds_read_b64_tr_b16 v[70:71], v178 offset:18432\n\t" \
    "ds_read_b64_tr_b16 v[148:149], v179 offset:16384\n\t" \
    "ds_read_b64_tr_b16 v[150:151], v179 offset:18432\n\t" \
    "v_add_u32 v178, 0x20000, v177\n\t" \
    "v_xor_b32 v179, 32, v178\n\t" \
    "ds_read_b64_tr_b16 v[72:73], v178 offset:0\n\t" \
    "ds_read_b64_tr_b16 v[74:75], v178 offset:2048\n\t" \
    "ds_read_b64_tr_b16 v[152:153], v179 offset:0\n\t" \
    "ds_read_b64_tr_b16 v[154:155], v179 offset:2048\n\t" \
    "ds_read_b64_tr_b16 v[76:77], v178 offset:16384\n\t" \
    "ds_read_b64_tr_b16 v[78:79], v178 offset:18432\n\t" \
    "ds_read_b64_tr_b16 v[156:157], v179 offset:16384\n\t" \
    "ds_read_b64_tr_b16 v[158:159], v179 offset:18432\n\t" \
    "v_mov_b32 v220, 0x3f803f80\n\t" \
    "v_mov_b32 v221, 0x3f803f80\n\t" \
    "v_mov_b32 v222, 0x3f803f80\n\t" \
    "v_mov_b32 v223, 0x3f803f80\n\t" \
    "v_mov_b32 v216, 0\n\t" \
    "v_mov_b32 v217, 0\n\t" \
    "v_mov_b32 v218, 0\n\t" \
    "v_mov_b32 v219, 0\n\t" \
    "s_waitcnt lgkmcnt(0)\n\t" \
    "s_barrier \n\t" \
    "ds_write_b32 v251, v220 offset:72\n\t" \
    "ds_write_b32 v251, v220 offset:2120\n\t" \
    "v_mfma_f32_16x16x32_bf16 v[160:163], v[0:3], v[220:223], 0\n\t" \
    "v_mfma_f32_16x16x32_bf16 v[164:167], v[80:83], v[220:223], 0\n\t" \
    "v_mfma_f32_16x16x32_bf16 v[160:163], v[4:7], v[220:223], v[160:163]\n\t" \
    "v_mfma_f32_16x16x32_bf16 v[164:167], v[84:87], v[220:223], v[164:167]\n\t" \
    "v_mfma_f32_16x16x32_bf16 v[160:163], v[8:11], v[220:223], v[160:163]\n\t" \
    "v_mfma_f32_16x16x32_bf16 v[164:167], v[88:91], v[220:223], v[164:167]\n\t" \
    "v_mfma_f32_16x16x32_bf16 v[160:163], v[12:15], v[220:223], v[160:163]\n\t" \
    "v_mfma_f32_16x16x32_bf16 v[164:167], v[92:95], v[220:223], v[164:167]\n\t" \
    "v_mfma_f32_16x16x32_bf16 v[160:163], v[16:19], v[220:223], v[160:163]\n\t" \
    "v_mfma_f32_16x16x32_bf16 v[164:167], v[96:99], v[220:223], v[164:167]\n\t" \
    "v_mfma_f32_16x16x32_bf16 v[160:163], v[20:23], v[220:223], v[160:163]\n\t" \
    "v_mfma_f32_16x16x32_bf16 v[164:167], v[100:103], v[220:223], v[164:167]\n\t" \
    "v_mfma_f32_16x16x32_bf16 v[160:163], v[24:27], v[220:223], v[160:163]\n\t" \
    "v_mfma_f32_16x16x32_bf16 v[164:167], v[104:107], v[220:223], v[164:167]\n\t" \
    "v_mfma_f32_16x16x32_bf16 v[160:163], v[28:31], v[220:223], v[160:163]\n\t" \
    "v_mfma_f32_16x16x32_bf16 v[164:167], v[108:111], v[220:223], v[164:167]\n\t" \
    "v_mfma_f32_16x16x32_bf16 v[160:163], v[32:35], v[220:223], v[160:163]\n\t" \
    "v_mfma_f32_16x16x32_bf16 v[164:167], v[112:115], v[220:223], v[164:167]\n\t" \
    "v_mfma_f32_16x16x32_bf16 v[160:163], v[36:39], v[220:223], v[160:163]\n\t" \
    "v_mfma_f32_16x16x32_bf16 v[164:167], v[116:119], v[220:223], v[164:167]\n\t" \
    "v_mfma_f32_16x16x32_bf16 v[160:163], v[40:43], v[220:223], v[160:163]\n\t" \
    "v_mfma_f32_16x16x32_bf16 v[164:167], v[120:123], v[220:223], v[164:167]\n\t" \
    "v_mfma_f32_16x16x32_bf16 v[160:163], v[44:47], v[220:223], v[160:163]\n\t" \
    "v_mfma_f32_16x16x32_bf16 v[164:167], v[124:127], v[220:223], v[164:167]\n\t" \
    "v_mfma_f32_16x16x32_bf16 v[160:163], v[48:51], v[220:223], v[160:163]\n\t" \
    "v_mfma_f32_16x16x32_bf16 v[164:167], v[128:131], v[220:223], v[164:167]\n\t" \
    "v_mfma_f32_16x16x32_bf16 v[160:163], v[52:55], v[220:223], v[160:163]\n\t" \
    "v_mfma_f32_16x16x32_bf16 v[164:167], v[132:135], v[220:223], v[164:167]\n\t" \
    "v_mfma_f32_16x16x32_bf16 v[160:163], v[56:59], v[220:223], v[160:163]\n\t" \
    "v_mfma_f32_16x16x32_bf16 v[164:167], v[136:139], v[220:223], v[164:167]\n\t" \
    "v_mfma_f32_16x16x32_bf16 v[160:163], v[60:63], v[220:223], v[160:163]\n\t" \
    "v_mfma_f32_16x16x32_bf16 v[164:167], v[140:143], v[220:223], v[164:167]\n\t" \
    "v_mfma_f32_16x16x32_bf16 v[160:163], v[64:67], v[220:223], v[160:163]\n\t" \
    "v_mfma_f32_16x16x32_bf16 v[164:167], v[144:147], v[220:223], v[164:167]\n\t" \
    "v_mfma_f32_16x16x32_bf16 v[160:163], v[68:71], v[220:223], v[160:163]\n\t" \
    "v_mfma_f32_16x16x32_bf16 v[164:167], v[148:151], v[220:223], v[164:167]\n\t" \
    "v_mfma_f32_16x16x32_bf16 v[160:163], v[72:75], v[220:223], v[160:163]\n\t" \
    "v_mfma_f32_16x16x32_bf16 v[164:167], v[152:155], v[220:223], v[164:167]\n\t" \
    "v_mfma_f32_16x16x32_bf16 v[160:163], v[76:79], v[220:223], v[160:163]\n\t" \
    "v_mfma_f32_16x16x32_bf16 v[164:167], v[156:159], v[220:223], v[164:167]\n\t" \
    "s_mov_b32 s52, 0x22222222\n\t" \
    "s_mov_b32 s53, 0x22222222\n\t" \
    "s_mov_b32 s54, 0x44444444\n\t" \
    "s_mov_b32 s55, 0x44444444\n\t" \
    "s_mov_b32 s56, 0x88888888\n\t" \
    "s_mov_b32 s57, 0x88888888\n\t" \
    ".Lw31_%=:\n\t" \
    "v_mov_b32 v248, 0\n\t" \
    "buffer_load_dword v248, v248, %[rs], %[go] offen sc1\n\t" \
    "s_waitcnt vmcnt(0)\n\t" \
    "v_cmp_ne_u32 vcc, 0, v248\n\t" \
    "s_cbranch_vccnz .Lw32_%=\n\t" \
    "s_cmp_eq_u32 s41, 0\n\t" \
    "s_cbranch_scc1 .Lw32_%=\n\t" \
    "s_sub_u32 s41, s41, 1\n\t" \
    "s_sleep 16\n\t" \
    "s_branch .Lw31_%=\n\t" \
    ".Lw32_%=:\n\t" \
    "s_mov_b32 s43, 0\n\t" \
    "s_add_u32 s46, s43, %[pi]\n\t" \
    "s_add_u32 s47, s46, 0x8000\n\t" \
    "s_add_u32 s48, s46, 0x10000\n\t" \
    "buffer_load_dwordx4 v[208:211], v253, %[rs], s46 offen sc1\n\t" \
    "buffer_load_dwordx4 v[212:215], v253, %[rs], s47 offen sc1\n\t" \
    "s_cmp_gt_u32 %[wave], 3\n\t" \
    "s_cbranch_scc1 .Lw33_%=\n\t" \
    "buffer_load_dwordx4 v[216:219], v253, %[rs], s48 offen sc1\n\t" \
    ".Lw33_%=:\n\t" \
    "s_waitcnt vmcnt(0)\n\t" \
    ".Lw34_%=:\n\t" \
    "s_add_i32 s44, s40, 0\n\t" \
    "s_max_i32 s44, s44, 0\n\t" \
    "s_lshr_b32 s45, s44, 1\n\t" \
    "s_and_b32 s44, s44, 1\n\t" \
    "s_lshl_b32 s44, s44, 12\n\t" \
    "s_mul_i32 s49, s45, 0x30000\n\t" \
    "s_add_u32 s49, s49, s44\n\t" \
    "s_lshr_b32 s45, s45, 3\n\t" \
    "s_mul_i32 s45, s45, 0x82000\n\t" \
    "s_add_u32 s49, s49, s45\n\t" \
    "s_add_u32 s49, s49, %[ng]\n\t" \
    "s_add_i32 s44, s40, 1\n\t" \
    "s_max_i32 s44, s44, 0\n\t" \
    "s_lshr_b32 s45, s44, 1\n\t" \
    "s_and_b32 s44, s44, 1\n\t" \
    "s_lshl_b32 s44, s44, 12\n\t" \
    "s_mul_i32 s43, s45, 0x14000\n\t" \
    "s_add_u32 s43, s43, s44\n\t" \
    "s_lshr_b32 s45, s45, 4\n\t" \
    "s_mul_i32 s45, s45, 0xc2000\n\t" \
    "s_add_u32 s43, s43, s45\n\t" \
    LST0 \
    "v_max3_u32 v248, v208, v209, v210\n\t" \
    "v_max3_u32 v248, v211, v212, v248\n\t" \
    "v_max3_u32 v248, v213, v214, v248\n\t" \
    "v_max3_u32 v248, v215, v216, v248\n\t" \
    "v_max3_u32 v248, v217, v218, v248\n\t" \
    "v_max_u32 v248, v219, v248\n\t" \
    "v_cmp_eq_u32 vcc, -1, v248\n\t" \
    "s_cbranch_vccz .Lw36_%=\n\t" \
    ".Lw35_%=:\n\t" \
    "v_max_u32 v248, v208, v209\n\t" \
    "v_max3_u32 v248, v210, v211, v248\n\t" \
    "v_cmp_eq_u32 vcc, -1, v248\n\t" \
    "s_cbranch_vccz .Lw37_%=\n\t" \
    "buffer_load_dwordx4 v[208:211], v253, %[rs], s46 offen sc1\n\t" \
    ".Lw37_%=:\n\t" \
    "v_max_u32 v248, v212, v213\n\t" \
    "v_max3_u32 v248, v214, v215, v248\n\t" \
    "v_cmp_eq_u32 vcc, -1, v248\n\t" \
    "s_cbranch_vccz .Lw38_%=\n\t" \
    "buffer_load_dwordx4 v[212:215], v253, %[rs], s47 offen sc1\n\t" \
    ".Lw38_%=:\n\t" \
    "v_max_u32 v248, v216, v217\n\t" \
    "v_max3_u32 v248, v218, v219, v248\n\t" \
    "v_cmp_eq_u32 vcc, -1, v248\n\t" \
    "s_cbranch_vccz .Lw39_%=\n\t" \
    "buffer_load_dwordx4 v[216:219], v253, %[rs], s48 offen sc1\n\t" \
    ".Lw39_%=:\n\t" \
    "s_waitcnt vmcnt(20)\n\t" \
    "v_max3_u32 v248, v208, v209, v210\n\t" \
    "v_max3_u32 v248, v211, v212, v248\n\t" \
    "v_max3_u32 v248, v213, v214, v248\n\t" \
    "v_max3_u32 v248, v215, v216, v248\n\t" \
    "v_max3_u32 v248, v217, v218, v248\n\t" \
    "v_max_u32 v248, v219, v248\n\t" \
    "v_cmp_eq_u32 vcc, -1, v248\n\t" \
    "s_cbranch_vccz .Lw36_%=\n\t" \
    "s_sleep 1\n\t" \
    "v_max3_u32 v248, v208, v209, v210\n\t" \
    "v_max3_u32 v248, v211, v212, v248\n\t" \
    "v_max3_u32 v248, v213, v214, v248\n\t" \
    "v_max3_u32 v248, v215, v216, v248\n\t" \
    "v_max3_u32 v248, v217, v218, v248\n\t" \
    "v_max_u32 v248, v219, v248\n\t" \
    "v_cmp_eq_u32 vcc, -1, v248\n\t" \
    "s_cbranch_vccz .Lw36_%=\n\t" \
    "s_sleep 1\n\t" \
    "v_max3_u32 v248, v208, v209, v210\n\t" \
    "v_max3_u32 v248, v211, v212, v248\n\t" \
    "v_max3_u32 v248, v213, v214, v248\n\t" \
    "v_max3_u32 v248, v215, v216, v248\n\t" \
    "v_max3_u32 v248, v217, v218, v248\n\t" \
    "v_max_u32 v248, v219, v248\n\t" \
    "v_cmp_eq_u32 vcc, -1, v248\n\t" \
    "s_cbranch_vccz .Lw36_%=\n\t" \
    "s_sleep 1\n\t" \
    "v_max3_u32 v248, v208, v209, v210\n\t" \
    "v_max3_u32 v248, v211, v212, v248\n\t" \
    "v_max3_u32 v248, v213, v214, v248\n\t" \
    "v_max3_u32 v248, v215, v216, v248\n\t" \
    "v_max3_u32 v248, v217, v218, v248\n\t" \
    "v_max_u32 v248, v219, v248\n\t" \
    "v_cmp_eq_u32 vcc, -1, v248\n\t" \
    "s_cbranch_vccz .Lw36_%=\n\t" \
    "s_sleep 1\n\t" \
    "v_max3_u32 v248, v208, v209, v210\n\t" \
    "v_max3_u32 v248, v211, v212, v248\n\t" \
    "v_max3_u32 v248, v213, v214, v248\n\t" \
    "v_max3_u32 v248, v215, v216, v248\n\t" \
    "v_max3_u32 v248, v217, v218, v248\n\t" \
    "v_max_u32 v248, v219, v248\n\t" \
    "v_cmp_eq_u32 vcc, -1, v248\n\t" \
    "s_cbranch_vccz .Lw36_%=\n\t" \
    "s_sleep 1\n\t" \
    "v_max3_u32 v248, v208, v209, v210\n\t" \
    "v_max3_u32 v248, v211, v212, v248\n\t" \
    "v_max3_u32 v248, v213, v214, v248\n\t" \
    "v_max3_u32 v248, v215, v216, v248\n\t" \
    "v_max3_u32 v248, v217, v218, v248\n\t" \
    "v_max_u32 v248, v219, v248\n\t" \
    "v_cmp_eq_u32 vcc, -1, v248\n\t" \
    "s_cbranch_vccz .Lw36_%=\n\t" \
    "s_sleep 1\n\t" \
    "s_cmp_eq_u32 s41, 0\n\t" \
    "s_cbranch_scc1 .Lw36_%=\n\t" \
    "s_sub_u32 s41, s41, 1\n\t" \
    "s_branch .Lw35_%=\n\t" \
    ".Lw36_%=:\n\t" \
    LST1 \
    "ds_write_b128 v250, v[208:211]\n\t" \
    "ds_write_b128 v250, v[212:215] offset:8192\n\t" \
    "s_cmp_gt_u32 %[wave], 3\n\t" \
    "s_cbranch_scc1 .Lw310_%=\n\t" \
    "ds_write_b128 v250, v[216:219] offset:16384\n\t" \
    ".Lw310_%=:\n\t" \
    "v_mfma_f32_16x16x32_bf16 v[228:231], v[208:211], v[208:211], 0\n\t" \
    "v_mfma_f32_16x16x32_bf16 v[228:231], v[212:215], v[212:215], v[228:231]\n\t" \
    "v_mfma_f32_16x16x32_bf16 v[228:231], v[216:219], v[216:219], v[228:231]\n\t" \
    "v_mfma_f32_16x16x32_bf16 v[224:227], v[220:223], v[208:211], 0\n\t" \
    "v_mfma_f32_16x16x32_bf16 v[224:227], v[220:223], v[212:215], v[224:227]\n\t" \
    "v_mfma_f32_16x16x32_bf16 v[224:227], v[220:223], v[216:219], v[224:227]\n\t" \
    "v_cndmask_b32 v228, v228, v229, s[52:53]\n\t" \
    "v_cndmask_b32 v228, v228, v230, s[54:55]\n\t" \
    "v_cndmask_b32 v228, v228, v231, s[56:57]\n\t" \
    "s_add_u32 s46, s43, %[pi]\n\t" \
    "s_add_u32 s47, s46, 0x8000\n\t" \
    "s_add_u32 s48, s46, 0x10000\n\t" \
    "s_mov_b32 exec_lo, 0xf0000f\n\t" \
    "s_mov_b32 exec_hi, 0xf0000f00\n\t" \
    "s_nop 4\n\t" \
    "ds_write2_b32 v252, v224, v228 offset1:1\n\t" \
    "s_mov_b64 exec, -1\n\t" \
    "s_waitcnt lgkmcnt(0)\n\t" \
    "s_barrier \n\t" \
    LST2 \
    "s_cmp_lg_u32 %[wave], 4\n\t" \
    "s_cbranch_scc1 .Lw311_%=\n\t" \
    "s_setprio 1\n\t" \
    "ds_read_b128 v[232:235], v251 offset:0\n\t" \
    "ds_read_b128 v[236:239], v251 offset:16\n\t" \
    "ds_read_b128 v[240:243], v251 offset:32\n\t" \
    "ds_read_b128 v[244:247], v251 offset:48\n\t" \
    "s_waitcnt lgkmcnt(0)\n\t" \
    "v_add_f32 v232, v232, v240\n\t" \
    "v_add_f32 v233, v233, v241\n\t" \
    "v_add_f32 v234, v234, v242\n\t" \
    "v_add_f32 v235, v235, v243\n\t" \
    "v_add_f32 v236, v236, v244\n\t" \
    "v_add_f32 v237, v237, v245\n\t" \
    "v_add_f32 v238, v238, v246\n\t" \
    "v_add_f32 v239, v239, v247\n\t" \
    "v_add_f32 v232, v232, v236\n\t" \
    "v_add_f32 v233, v233, v237\n\t" \
    "v_add_f32 v234, v234, v238\n\t" \
    "v_add_f32 v235, v235, v239\n\t" \
    "v_add_f32 v232, v232, v234\n\t" \
    "v_add_f32 v233, v233, v235\n\t" \
    "v_mul_f32 v232, 0x3acccccd, v232\n\t" \
    "v_mul_f32 v233, 0x3acccccd, v233\n\t" \
    "v_fma_f32 v233, -v232, v232, v233\n\t" \
    "v_mov_b32 v234, v232\n\t" \
    "v_max_f32 v233, 0, v233\n\t" \
    "v_cvt_pk_bf16_f32 v234, v234, v234\n\t" \
    "v_and_b32 v234, 0xffff0000, v234\n\t" \
    "v_mov_b32 v236, v233\n\t" \
    "v_cvt_pk_bf16_f32 v236, v236, v236\n\t" \
    "v_and_b32 v236, 0xffff0000, v236\n\t" \
    "v_add_f32 v236, 0x3727c5ac, v236\n\t" \
    "v_cvt_pk_bf16_f32 v236, v236, v236\n\t" \
    "v_and_b32 v236, 0xffff0000, v236\n\t" \
    "v_sqrt_f32 v236, v236\n\t" \
    "s_nop 0\n\t" \
    "v_cvt_pk_bf16_f32 v236, v236, v236\n\t" \
    "v_and_b32 v236, 0xffff0000, v236\n\t" \
    "v_rcp_f32 v235, v236\n\t" \
    "s_nop 0\n\t" \
    "v_mov_b32 v237, v235\n\t" \
    "v_mul_f32 v238, -v234, v235\n\t" \
    "v_mov_b32 v239, s40\n\t" \
    "ds_write2_b32 v251, v237, v238 offset0:16 offset1:17\n\t" \
    "ds_write_b32 v251, v239 offset:72\n\t" \
    "s_setprio 0\n\t" \
    ".Lw311_%=:\n\t" \
    "ds_read_b128 v[176:179], v249 offset:0\n\t" \
    "ds_read_b128 v[180:183], v249 offset:1024\n\t" \
    "ds_read_b128 v[184:187], v249 offset:2048\n\t" \
    "ds_read_b128 v[188:191], v249 offset:3072\n\t" \
    "ds_read_b128 v[192:195], v249 offset:4096\n\t" \
    "ds_read_b128 v[196:199], v249 offset:5120\n\t" \
    "ds_read_b128 v[200:203], v249 offset:6144\n\t" \
    "buffer_load_dwordx4 v[208:211], v253, %[rs], s46 offen sc1\n\t" \
    "buffer_load_dwordx4 v[212:215], v253, %[rs], s47 offen sc1\n\t" \
    "s_cmp_gt_u32 %[wave], 3\n\t" \
    "s_cbranch_scc1 .Lw313_%=\n\t" \
    "buffer_load_dwordx4 v[216:219], v253, %[rs], s48 offen sc1\n\t" \
    ".Lw313_%=:\n\t" \
    "s_waitcnt lgkmcnt(6)\n\t" \
    "v_mfma_f32_16x16x32_bf16 v[168:171], v[0:3], v[176:179], 0\n\t" \
    "ds_read_b128 v[204:207], v249 offset:7168\n\t" \
    "v_mfma_f32_16x16x32_bf16 v[172:175], v[80:83], v[176:179], 0\n\t" \
    "s_waitcnt lgkmcnt(6)\n\t" \
    "v_mfma_f32_16x16x32_bf16 v[168:171], v[4:7], v[180:183], v[168:171]\n\t" \
    "ds_read_b128 v[176:179], v249 offset:8192\n\t" \
    "v_mfma_f32_16x16x32_bf16 v[172:175], v[84:87], v[180:183], v[172:175]\n\t" \
    "s_waitcnt lgkmcnt(6)\n\t" \
    "v_mfma_f32_16x16x32_bf16 v[168:171], v[8:11], v[184:187], v[168:171]\n\t" \
    "ds_read_b128 v[180:183], v249 offset:9216\n\t" \
    "v_mfma_f32_16x16x32_bf16 v[172:175], v[88:91], v[184:187], v[172:175]\n\t" \
    "s_waitcnt lgkmcnt(6)\n\t" \
    "v_mfma_f32_16x16x32_bf16 v[168:171], v[12:15], v[188:191], v[168:171]\n\t" \
    "ds_read_b128 v[184:187], v249 offset:10240\n\t" \
    "v_mfma_f32_16x16x32_bf16 v[172:175], v[92:95], v[188:191], v[172:175]\n\t" \
    "s_waitcnt lgkmcnt(6)\n\t" \
    "v_mfma_f32_16x16x32_bf16 v[168:171], v[16:19], v[192:195], v[168:171]\n\t" \
    "ds_read_b128 v[188:191], v249 offset:11264\n\t" \
    "v_mfma_f32_16x16x32_bf16 v[172:175], v[96:99], v[192:195], v[172:175]\n\t" \
    "s_waitcnt lgkmcnt(6)\n\t" \
    "v_mfma_f32_16x16x32_bf16 v[168:171], v[20:23], v[196:199], v[168:171]\n\t" \
    "ds_read_b128 v[192:195], v249 offset:12288\n\t" \
    "v_mfma_f32_16x16x32_bf16 v[172:175], v[100:103], v[196:199], v[172:175]\n\t" \
    "s_waitcnt lgkmcnt(6)\n\t" \
    "v_mfma_f32_16x16x32_bf16 v[168:171], v[24:27], v[200:203], v[168:171]\n\t" \
    "ds_read_b128 v[196:199], v249 offset:13312\n\t" \
    "v_mfma_f32_16x16x32_bf16 v[172:175], v[104:107], v[200:203], v[172:175]\n\t" \
    "s_waitcnt lgkmcnt(6)\n\t" \
    "v_mfma_f32_16x16x32_bf16 v[168:171], v[28:31], v[204:207], v[168:171]\n\t" \
    "ds_read_b128 v[200:203], v249 offset:14336\n\t" \
    "v_mfma_f32_16x16x32_bf16 v[172:175], v[108:111], v[204:207], v[172:175]\n\t" \
    "s_waitcnt lgkmcnt(6)\n\t" \
    "v_mfma_f32_16x16x32_bf16 v[168:171], v[32:35], v[176:179], v[168:171]\n\t" \
    "ds_read_b128 v[204:207], v249 offset:15360\n\t" \
    "v_mfma_f32_16x16x32_bf16 v[172:175], v[112:115], v[176:179], v[172:175]\n\t" \
    "s_waitcnt lgkmcnt(6)\n\t" \
    "v_mfma_f32_16x16x32_bf16 v[168:171], v[36:39], v[180:183], v[168:171]\n\t" \
    "ds_read_b128 v[176:179], v249 offset:16384\n\t" \
    "v_mfma_f32_16x16x32_bf16 v[172:175], v[116:119], v[180:183], v[172:175]\n\t" \
    "s_waitcnt lgkmcnt(6)\n\t" \
    "v_mfma_f32_16x16x32_bf16 v[168:171], v[40:43], v[184:187], v[168:171]\n\t" \
    "ds_read_b128 v[180:183], v249 offset:17408\n\t" \
    "v_mfma_f32_16x16x32_bf16 v[172:175], v[120:123], v[184:187], v[172:175]\n\t" \
    "s_waitcnt lgkmcnt(6)\n\t" \
    "v_mfma_f32_16x16x32_bf16 v[168:171], v[44:47], v[188:191], v[168:171]\n\t" \
    "ds_read_b128 v[184:187], v249 offset:18432\n\t" \
    "v_mfma_f32_16x16x32_bf16 v[172:175], v[124:127], v[188:191], v[172:175]\n\t" \
    "s_waitcnt lgkmcnt(6)\n\t" \
    "v_mfma_f32_16x16x32_bf16 v[168:171], v[48:51], v[192:195], v[168:171]\n\t" \
    "ds_read_b128 v[188:191], v249 offset:19456\n\t" \
    "v_mfma_f32_16x16x32_bf16 v[172:175], v[128:131], v[192:195], v[172:175]\n\t" \
    "s_waitcnt lgkmcnt(6)\n\t" \
    "v_mfma_f32_16x16x32_bf16 v[168:171], v[52:55], v[196:199], v[168:171]\n\t" \
    "v_mfma_f32_16x16x32_bf16 v[172:175], v[132:135], v[196:199], v[172:175]\n\t" \
    "s_waitcnt lgkmcnt(5)\n\t" \
    "v_mfma_f32_16x16x32_bf16 v[168:171], v[56:59], v[200:203], v[168:171]\n\t" \
    "v_mfma_f32_16x16x32_bf16 v[172:175], v[136:139], v[200:203], v[172:175]\n\t" \
    "s_waitcnt lgkmcnt(4)\n\t" \
    "v_mfma_f32_16x16x32_bf16 v[168:171], v[60:63], v[204:207], v[168:171]\n\t" \
    "v_mfma_f32_16x16x32_bf16 v[172:175], v[140:143], v[204:207], v[172:175]\n\t" \
    "ds_read_b32 v236, v251 offset:72\n\t" \
    "ds_read_b64 v[238:239], v251 offset:64\n\t" \
    "s_waitcnt lgkmcnt(5)\n\t" \
    "v_mfma_f32_16x16x32_bf16 v[168:171], v[64:67], v[176:179], v[168:171]\n\t" \
    "v_mfma_f32_16x16x32_bf16 v[172:175], v[144:147], v[176:179], v[172:175]\n\t" \
    "s_waitcnt lgkmcnt(4)\n\t" \
    "v_mfma_f32_16x16x32_bf16 v[168:171], v[68:71], v[180:183], v[168:171]\n\t" \
    "v_mfma_f32_16x16x32_bf16 v[172:175], v[148:151], v[180:183], v[172:175]\n\t" \
    ".Lw314_%=:\n\t" \
    "s_waitcnt lgkmcnt(0)\n\t" \
    "v_cmp_ne_u32 vcc, s40, v236\n\t" \
    "s_cbranch_vccz .Lw315_%=\n\t" \
    "ds_read_b32 v236, v251 offset:72\n\t" \
    "ds_read_b64 v[238:239], v251 offset:64\n\t" \
    "s_branch .Lw314_%=\n\t" \
    ".Lw315_%=:\n\t" \
    "s_waitcnt lgkmcnt(0)\n\t" \
    "v_mul_f32 v240, v160, v239\n\t" \
    "v_mul_f32 v241, v161, v239\n\t" \
    "v_mfma_f32_16x16x32_bf16 v[168:171], v[72:75], v[184:187], v[168:171]\n\t" \
    "v_mul_f32 v242, v162, v239\n\t" \
    "v_mul_f32 v243, v163, v239\n\t" \
    "v_mfma_f32_16x16x32_bf16 v[172:175], v[152:155], v[184:187], v[172:175]\n\t" \
    "v_mul_f32 v244, v164, v239\n\t" \
    "v_mul_f32 v245, v165, v239\n\t" \
    "v_mfma_f32_16x16x32_bf16 v[168:171], v[76:79], v[188:191], v[168:171]\n\t" \
    "v_mul_f32 v246, v166, v239\n\t" \
    "v_mul_f32 v247, v167, v239\n\t" \
    "v_mfma_f32_16x16x32_bf16 v[172:175], v[156:159], v[188:191], v[172:175]\n\t" \
    "v_mov_b32 v237, 0xc0135761\n\t" \
    LST3 \
    "s_nop 6\n\t" \
    "v_fma_f32 v168, v168, v238, v240\n\t" \
    "v_fma_f32 v169, v169, v238, v241\n\t" \
    "v_fma_f32 v170, v170, v238, v242\n\t" \
    "v_fma_f32 v171, v171, v238, v243\n\t" \
    "v_fma_f32 v172, v172, v238, v244\n\t" \
    "v_fma_f32 v173, v173, v238, v245\n\t" \
    "v_fma_f32 v174, v174, v238, v246\n\t" \
    "v_fma_f32 v175, v175, v238, v247\n\t" \
    "v_mul_f32 v176, v168, v168\n\t" \
    "v_mul_f32 v177, v169, v169\n\t" \
    "v_mul_f32 v178, v170, v170\n\t" \
    "v_mul_f32 v179, v171, v171\n\t" \
    "v_mul_f32 v180, v172, v172\n\t" \
    "v_mul_f32 v181, v173, v173\n\t" \
    "v_mul_f32 v182, v174, v174\n\t" \
    "v_mul_f32 v183, v175, v175\n\t" \
    "v_fmamk_f32 v176, v176, 0xbdd2d3e7, v237\n\t" \
    "v_fmamk_f32 v177, v177, 0xbdd2d3e7, v237\n\t" \
    "v_fmamk_f32 v178, v178, 0xbdd2d3e7, v237\n\t" \
    "v_fmamk_f32 v179, v179, 0xbdd2d3e7, v237\n\t" \
    "v_fmamk_f32 v180, v180, 0xbdd2d3e7, v237\n\t" \
    "v_fmamk_f32 v181, v181, 0xbdd2d3e7, v237\n\t" \
    "v_fmamk_f32 v182, v182, 0xbdd2d3e7, v237\n\t" \
    "v_fmamk_f32 v183, v183, 0xbdd2d3e7, v237\n\t" \
    "v_mul_f32 v176, v168, v176\n\t" \
    "v_mul_f32 v177, v169, v177\n\t" \
    "v_mul_f32 v178, v170, v178\n\t" \
    "v_mul_f32 v179, v171, v179\n\t" \
    "v_mul_f32 v180, v172, v180\n\t" \
    "v_mul_f32 v181, v173, v181\n\t" \
    "v_mul_f32 v182, v174, v182\n\t" \
    "v_mul_f32 v183, v175, v183\n\t" \
    "v_exp_f32 v176, v176\n\t" \
    "v_exp_f32 v177, v177\n\t" \
    "v_exp_f32 v178, v178\n\t" \
    "v_exp_f32 v179, v179\n\t" \
    "v_exp_f32 v180, v180\n\t" \
    "v_exp_f32 v181, v181\n\t" \
    "v_exp_f32 v182, v182\n\t" \
    "v_exp_f32 v183, v183\n\t" \
    "v_add_f32 v176, 1.0, v176\n\t" \
    "v_add_f32 v177, 1.0, v177\n\t" \
    "v_add_f32 v178, 1.0, v178\n\t" \
    "v_add_f32 v179, 1.0, v179\n\t" \
    "v_add_f32 v180, 1.0, v180\n\t" \
    "v_add_f32 v181, 1.0, v181\n\t" \
    "v_add_f32 v182, 1.0, v182\n\t" \
    "v_add_f32 v183, 1.0, v183\n\t" \
    "v_rcp_f32 v176, v176\n\t" \
    "v_rcp_f32 v177, v177\n\t" \
    "v_rcp_f32 v178, v178\n\t" \
    "v_rcp_f32 v179, v179\n\t" \
    "v_rcp_f32 v180, v180\n\t" \
    "v_rcp_f32 v181, v181\n\t" \
    "v_rcp_f32 v182, v182\n\t" \
    "v_rcp_f32 v183, v183\n\t" \
    "v_mul_f32 v168, v168, v176\n\t" \
    "v_mul_f32 v169, v169, v177\n\t" \
    "v_mul_f32 v170, v170, v178\n\t" \
    "v_mul_f32 v171, v171, v179\n\t" \
    "v_mul_f32 v172, v172, v180\n\t" \
    "v_mul_f32 v173, v173, v181\n\t" \
    "v_mul_f32 v174, v174, v182\n\t" \
    "v_mul_f32 v175, v175, v183\n\t" \
    "s_waitcnt vmcnt(0)\n\t" \
    "v_cvt_pk_bf16_f32 v184, v168, v169\n\t" \
    "v_cvt_pk_bf16_f32 v185, v170, v171\n\t" \
    "v_cvt_pk_bf16_f32 v186, v172, v173\n\t" \
    "v_cvt_pk_bf16_f32 v187, v174, v175\n\t" \
    "v_min_u32 v184, -2, v184\n\t" \
    "v_min_u32 v185, -2, v185\n\t" \
    "v_min_u32 v186, -2, v186\n\t" \
    "v_min_u32 v187, -2, v187\n\t" \
    "s_nop 0\n\t" \
    "v_permlane16_swap_b32 v184, v186\n\t" \
    "v_permlane16_swap_b32 v185, v187\n\t" \
    "buffer_store_dwordx4 v[184:187], v254, %[rs], s49 offen sc1\n\t" \
    LST4 \
    "s_cmp_ge_u32 s40, 122\n\t" \
    "s_cbranch_scc1 .Lw316_%=\n\t" \
    "s_add_i32 s44, s40, 6\n\t" \
    "s_max_i32 s44, s44, 0\n\t" \
    "s_lshr_b32 s45, s44, 1\n\t" \
    "s_and_b32 s44, s44, 1\n\t" \
    "s_lshl_b32 s44, s44, 12\n\t" \
    "s_mul_i32 s50, s45, 0x30000\n\t" \
    "s_add_u32 s50, s50, s44\n\t" \
    "s_lshr_b32 s45, s45, 3\n\t" \
    "s_mul_i32 s45, s45, 0x82000\n\t" \
    "s_add_u32 s50, s50, s45\n\t" \
    "s_add_u32 s50, s50, %[ng]\n\t" \
    "v_mov_b32 v188, -1\n\t" \
    "v_mov_b32 v189, -1\n\t" \
    "v_mov_b32 v190, -1\n\t" \
    "v_mov_b32 v191, -1\n\t" \
    "buffer_store_dwordx4 v[188:191], v253, %[rs], s50 offen sc1\n\t" \
    ".Lw316_%=:\n\t" \
    "v_xor_b32 v249, 0x8000, v249\n\t" \
    "v_xor_b32 v250, 0x8000, v250\n\t" \
    "v_xor_b32 v251, 0x800, v251\n\t" \
    "v_xor_b32 v252, 0x800, v252\n\t" \
    LFLUSHW \
    "s_add_u32 s40, s40, 1\n\t" \
    "s_cmp_lg_u32 s40, 128\n\t" \
    "s_cbranch_scc1 .Lw34_%=\n\t" \
    "s_branch .Lend%=\n\t" \
    ".Lend%=:\n\t" \
    "s_waitcnt vmcnt(0) lgkmcnt(0)\n\t" \
    "s_mov_b32 %[bud], s41\n\t"
using u32x8 = __attribute__((ext_vector_type(8))) unsigned;
using u32x16 = __attribute__((ext_vector_type(16))) unsigned;
using u32x32 = __attribute__((ext_vector_type(32))) unsigned;
__device__ __forceinline__ u32x32 cat32(u32x8 a, u32x8 b, u32x8 c, u32x8 d) {
    const u32x16 lo = __builtin_shufflevector(a, b, 0, 1, 2, 3, 4, 5, 6, 7, 8, 9, 10, 11, 12, 13, 14, 15);
    const u32x16 hi = __builtin_shufflevector(c, d, 0, 1, 2, 3, 4, 5, 6, 7, 8, 9, 10, 11, 12, 13, 14, 15);
    return __builtin_shufflevector(lo, hi, 0, 1, 2, 3, 4, 5, 6, 7, 8, 9, 10, 11, 12, 13, 14, 15, 16, 17, 18, 19, 20, 21, 22, 23, 24, 25, 26, 27, 28, 29, 30, 31);
}
__device__ __forceinline__ unsigned c1_dst(int wave, int kind) {
    const int ln = lane_id(), c = ln & 15, g = ln >> 4, hf = (wave >> 1) & 1;
    if (kind == 2) {
        const unsigned b = L_KM + (c >> 2) * 256 + (wave & 1) * 128 + g * 32;
        return (b + hf * 8 + (c & 3) * 2) | (b + 1024 + hf * 1024 + (c & 3) * 4) << 16;
    }
    const unsigned b = L_Q + (c >> 3) * 256 + g * 64 + ((c >> 2) & 1) * 8;
    return (b + (wave & 1) * (kind ? 512 : 1536) + ((c >> 1) & 1) * 32 + hf * 4 + (c & 1) * 2) | (b + 1024 + (wave & 3) * 512 + (c & 1) * 32 + ((c >> 1) & 1) * 4) << 16;
}
__device__ __forceinline__ void dma1(rsrc_t rs, unsigned vo, unsigned so, unsigned dst) {
    asm volatile("s_mov_b32 m0, %3\n\ts_nop 0\n\tbuffer_load_dwordx4 %0, %1, %2 offen nt lds" :: "v"(vo), "s"(rs), "s"(so), "s"(dst) : "memory");
}
using s16x4 = __attribute__((ext_vector_type(4))) short;
__device__ __forceinline__ u32x4 tr_frag(unsigned a, unsigned step) {
    const u32x2 x = __builtin_bit_cast(u32x2, __builtin_amdgcn_ds_read_tr16_b64_v4i16((LAS s16x4*)(size_t)a)), y = __builtin_bit_cast(u32x2, __builtin_amdgcn_ds_read_tr16_b64_v4i16((LAS s16x4*)(size_t)(a + step)));
    return (u32x4){x[0], x[1], y[0], y[1]};
}
#define LDELAY
#define LOOP_CLOB "memory", "vcc", "scc", "v0", "v1", "v2", "v3", "v4", "v5", "v6", "v7", "v8", "v9", "v10", "v11", "v12", "v13", "v14", "v15", "v16", "v17", "v18", "v19", "v20", "v21", "v22", "v23", "v24", "v25", "v26", "v27", "v28", "v29", "v30", "v31", "v32", "v33", "v34", "v35", "v36", "v37", "v38", "v39", "v40", "v41", "v42", "v43", "v44", "v45", "v46", "v47", "v48", "v49", "v50", "v51", "v52", "v53", "v54", "v55", "v56", "v57", "v58", "v59", "v60", "v61", "v62", "v63", "v64", "v65", "v66", "v67", "v68", "v69", "v70", "v71", "v72", "v73", "v74", "v75", "v76", "v77", "v78", "v79", "v80", "v81", "v82", "v83", "v84", "v85", "v86", "v87", "v88", "v89", "v90", "v91", "v92", "v93", "v94", "v95", "v96", "v97", "v98", "v99", "v100", "v101", "v102", "v103", "v104", "v105", "v106", "v107", "v108", "v109", "v110", "v111", "v112", "v113", "v114", "v115", "v116", "v117", "v118", "v119", "v120", "v121", "v122", "v123", "v124", "v125", "v126", "v127", "v128", "v129", "v130", "v131", "v132", "v133", "v134", "v135", "v136", "v137", "v138", "v139", "v140", "v141", "v142", "v143", "v144", "v145", "v146", "v147", "v148", "v149", "v150", "v151", "v152", "v153", "v154", "v155", "v156", "v157", "v158", "v159", "v160", "v161", "v162", "v163", "v164", "v165", "v166", "v167", "v168", "v169", "v170", "v171", "v172", "v173", "v174", "v175", "v176", "v177", "v178", "v179", "v180", "v181", "v182", "v183", "v184", "v185", "v186", "v187", "v188", "v189", "v190", "v191", "v192", "v193", "v194", "v195", "v196", "v197", "v198", "v199", "v200", "v201", "v202", "v203", "v204", "v205", "v206", "v207", "v208", "v209", "v210", "v211", "v212", "v213", "v214", "v215", "v216", "v217", "v218", "v219", "v220", "v221", "v222", "v223", "v224", "v225", "v226", "v227", "v228", "v229", "v230", "v231", "v232", "v233", "v234", "v235", "v236", "v237", "v238", "v239", "v246", "v247", "v248", "v249", "v250", "v251", "v252", "s40", "s41", "s42", "s43", "s44", "s45", "s46", "s47", "s48", "s49", "s50", "s51", "s52", "s53", "s54", "s55", "s56", "s57", "s58", "s59", "s60", "s61", "s62", "s63", "s64", "s65", "s66", "s67", "s68", "s69", "s70", "s71", "s72", "s73", "s74", "s75", "s76", "s77", "s78", "s79"
__device__ __forceinline__ unsigned UNI(unsigned x) {
    unsigned r;
    asm volatile("s_nop 0\n\tv_readfirstlane_b32 %0, %1\n\ts_nop 4" : "=s"(r) : "v"(x));
    return r;
}
__device__ __forceinline__ void note_timeout(const Params& P, unsigned bud) {
    if (bud == 0) __builtin_amdgcn_raw_buffer_store_b32(1u, mkrsrc(P.ws + FL0, 512), 0x100, 0, 16);
}

#define LOOP_IO [bud] "=s"(bud), "+{v253}"(l16), "+{v240}"(a[0]), "+{v241}"(a[1]), "+{v242}"(a[2]), "+{v243}"(a[3]), "+{v244}"(a[4]), "+{v245}"(a[5])
#define LOOP_IN "{v254}"(b2), "{v255}"(dsto), [wave] "s"(wave), [ws] "s"(P.ws), [mo] "s"(FL_MB + blockIdx.x * 128 + wave * 4), [sz] "s"(RG_L), [lx] "s"(L << 13), [wr] "s"(wr), [wm] "s"(wm), [fl] "s"(FL_XN + cpy * 2048), \
                [cj] "s"(REGS - 75 * WIN), [cs] "s"(5 * WIN), [mb] "s"(RG_XM + cpy * XM_COPY)
__device__ __forceinline__ void qkv_addr(unsigned (&a)[6], int w4, int ln, unsigned base) {
#pragma unroll
    for (int k = 0; k < 3; ++k) { const unsigned u = (w4 * 3 + k) * 64 + ln; a[k] = (u / 24) * (HD * 2) + (u % 24) * 16 - k * 1024; }
    a[3] = base + ((ln >> 4) * 8 + ((ln >> 2) & 3)) * 384 + (ln & 3) * 8 + w4 * 96;
    a[4] = a[5] = 0;
}
__device__ __forceinline__ void s_role(const Params& P, int wave, int L, int h, int cpy) {
    const int ln = lane_id();
    unsigned l16 = ln * 16, a[6];
    qkv_addr(a, wave & 3, ln, (wave >> 2) * 12288);
    const rsrc_t wr = mkrsrc((wave < 4 ? P.wq : P.wk) + h * DH, (DM * HD - h * DH) * 2);
    const unsigned wm = UNI(wave * 3072);
    const int mt = (wave >> 1) & 1, hf = wave & 1;
    const unsigned xs = D_XN + mt * 4096 + hf * WIN, x1 = (2 - hf) * WIN + hf * 2048, m0 = L_X + mt * 20480 + hf * 4096, m1 = L_X + mt * 20480 + 8192 + hf * 2048;
    const float SCL = 0.07216878364870322f * 1.4426950408889634f;
    const unsigned px = RG_PX + (h >> 2) * WIN;
    const unsigned pb = wave == 0 ? RG_XP + (h / 12) * WIN + (h % 12) * 640 - L_Q : px + (wave == 2 ? (h & 3) * 1024 : 5120 + ((h >> 1) & 1) * 1024 + (h & 1) * 256);
    unsigned b2 = l16 + (wave < 3 ? L_Q : wave == 3 ? L_QX : L_W + wave * 10240), dsto = c1_dst(wave, 1 + (wave >> 2)), bud;
    asm volatile(ASM_S_LOOP : LOOP_IO
                 : LOOP_IN, [xs] "s"(xs), [x1] "s"(x1), [m0] "s"(m0), [m1] "s"(m1), [qs] "s"(UNI(L_KX + hf * 1024 + mt * 8)), [q2] "s"(UNI(L_KX + 2048 + (wave & 3) * 1024)), [pb] "s"(UNI(pb)),
                   [pm] "s"(UNI(px + (wave == 2 ? 4096 : 7168) + (h & 3) * 256)), [scl] "s"(__float_as_uint(SCL)) LST_IN
                 : LOOP_CLOB);
    note_timeout(P, bud);
}

__device__ __forceinline__ void v_role(const Params& P, int wave, int L, int h, int cpy) {
    const int ln = lane_id(), w4 = wave & 3;
    unsigned l16 = ln * 16, a[6];
    if (wave < 4) qkv_addr(a, w4, ln, 0);
    else {
#pragma unroll
        for (int k = 0; k < 5; ++k) { const unsigned u = k * 64 + ln; a[k] = (u / 20) * (DM * 2) + (u % 20) * 16 - (k & 3) * 1024; }
        a[5] = L_W + w4 * 20480 + (ln >> 2) * 320 + (ln & 3) * 8;
    }
    const unsigned wo = wave < 4 ? h * DH : h * DH * DM + w4 * 160;
    const rsrc_t wr = mkrsrc((wave < 4 ? P.wv : P.wo) + wo, (DM * HD - wo) * 2);
    const unsigned wm = UNI(wave < 4 ? wave * 3072 : L_W + w4 * 20480);
    unsigned b2 = l16 + (wave < 3 ? L_Q : wave == 3 ? L_QX : L_W + wave * 10240), dsto = wave < 4 ? c1_dst(wave, 0) : (l16 >> 8) * WIN + (l16 & 255), bud;
    if (wave >= 4) l16 += L_Q;
    asm volatile(ASM_V_LOOP : LOOP_IO
                 : LOOP_IN, [xs] "s"(D_XN + w4 * WIN), [vm] "s"(VXD - 1), [vx] "s"(vxb(w4, 0) + (h >> 3) * WIN + (h & 7) * 1024 - L_Q),
                   [fo] "s"(FL_VO + (w4 >> 1) * WIN + ((w4 & 1) * 32 + h) * 128 - L_Q), [vo] "s"(RG_VM + w4 * 10 * WIN + (h >> 2) * 1024 + (h & 3) * 256) LST_IN
                 : LOOP_CLOB);
    note_timeout(P, bud);
}

#define D_W0 4
#define D_A1 8
#define D_R1 9
#define D_W1 12
__device__ __forceinline__ void delay(int n) { for (int i = 0; i < n; ++i) __builtin_amdgcn_s_sleep(64); }
__device__ __forceinline__ unsigned handshake(rsrc_t rw, unsigned mo) {
    wait_vm<0>();
    unsigned v = SENT;
    for (int spins = 0; spins < SPIN_CAP && v == SENT; ++spins) {
        __builtin_amdgcn_s_sleep(4);
        v = __builtin_amdgcn_readfirstlane(__builtin_amdgcn_raw_buffer_load_b32(rw, 0, mo, 16));
    }
    __builtin_amdgcn_raw_buffer_store_b32(1u, rw, lane_id() ? OOB : 32768u, mo, 16);
    return (v >> 13) & 1;
}
template <class F>
__device__ __forceinline__ void x_tile(const Params& P, int tile, F target) {
    const int ln = lane_id(), l15 = ln & 15, l4 = ln >> 4;
    u32x4 v[20];
    float s = 0.f, q = 0.f;
#pragma unroll
    for (int c = 0; c < 20; ++c) v[c] = *(const u32x4*)(P.x + (size_t)(tile * 16 + l15) * DM + c * 32 + l4 * 8);
#pragma unroll
    for (int c = 0; c < 20; ++c)
#pragma unroll
        for (int e = 0; e < 4; ++e) s += lo2f(v[c][e]) + hi2f(v[c][e]);
    sum2_rows(s, q);
#pragma unroll
    for (int c = 0; c < 20; ++c) asm volatile("" : "+v"(v[c]));
    const float mean = s * (1.0f / DM);
#pragma unroll
    for (int c = 0; c < 20; ++c)
#pragma unroll
        for (int e = 0; e < 4; ++e) { const float a = lo2f(v[c][e]) - mean, b = hi2f(v[c][e]) - mean; q += a * a + b * b; }
    sum2_rows(s, q);
#pragma unroll
    for (int c = 0; c < 20; ++c) asm volatile("" : "+v"(v[c]));
    const float m1 = rbf(mean), r = 1.0f / ln_den(q * (1.0f / DM));
    const unsigned sb = segb(tile >> 1) + (tile & 1) * 4096;
    u32x4 n[20];
#pragma unroll
    for (int c = 0; c < 20; ++c) {
#pragma unroll
        for (int e = 0; e < 4; ++e) { const unsigned d = pack2(lo2f(v[c][e]) - m1, hi2f(v[c][e]) - m1); n[c][e] = pack2(lo2f(d) * r, hi2f(d) * r); }
        asm volatile("" : "+v"(n[c]));
    }
    const rsrc_t rr = target();
#pragma unroll
    for (int c = 0; c < 20; ++c) {
        st128(n[c], rr, ln * 16 + (c & 3) * 1024, D_XN + sb + (c >> 2) * WIN);
        st128(v[c], rr, ln * 16 + (c & 3) * 1024, D_XR + sb + (c >> 2) * WIN);
    }
    wait_vm<0>();
}
__device__ __forceinline__ void wait_go(const Params& P, rsrc_t rd, int k) {
    for (int spins = 0;; ++spins) {
        if (__builtin_amdgcn_readfirstlane(__builtin_amdgcn_raw_buffer_load_b32(rd, 0, FL_VO + k * 128, 16))) break;
        __builtin_amdgcn_s_sleep(16);
        if (spin_fail(spins * 4 + 3, P)) break;
    }
    asm volatile("" ::: "memory");
}

constexpr unsigned R_RED = 0;
constexpr unsigned R_PART = 8192;
__device__ __forceinline__ void r_role(const Params& P, int wave, int L, int j, int cpy) {
    const int lane = lane_id(), l15 = lane & 15, l4 = lane >> 4;
    const unsigned l16 = lane * 16;
    const rsrc_t rr = mkrsrc(lbase(P, L), RG_L), rd = rr;
    const int sidx = (lane & 2) ? 0xEEEE : 0x4444;
    unsigned offp[3];
    auto hq = [&](int q) { return RG_XP + cpy * XP_COPY + (q / 3) * WIN + (q % 3) * 2560; };
#define ROT 1
    const int hg = (wave + ROT * j) & 7;
    offp[0] = hq(hg) + (lane >> 5) * 640 + 128 + (lane & 31) * 16;
    offp[1] = offp[0] + 1280;
    offp[2] = (lane & 2) ? OOB : hq(hg) + l4 * 640 + ((l15 >> 2) * 2 + (l15 & 1)) * 16;
    const int j10 = j % 10;
    const unsigned arp = (wave < 3 && j10 < 8 && wave * 1024 + l16 < 2560) ? hq(j10) + (j / 10 - cpy) * XP_COPY + wave * 1024 + l16 : OOB;
    const unsigned arx = RG_PX + (j >> 2) * WIN + (j & 3) * 2048 + (wave & 1) * 1024 + l16;
    const unsigned offv = RG_VM + j * WIN + hg * 1024 + l16;
    const unsigned offo = (wave < (int)NCP) ? RG_XM + wave * XM_COPY + (j >> 5) * WIN + (j & 31) * 256 + lane * 4 : OOB;
    const int pubw = 1 - (wave >> 2);
    {
        const u32x4 s4 = sent4();
#pragma unroll
        for (int k = 0; k < (int)NSLOT; ++k) {
            st128(s4, rr, offv, k * VM_SLOT);
            if (k) __builtin_amdgcn_raw_buffer_store_b32(SENT, rr, offo, k * XM_SLOT, 16);
            if (wave < 3) st128(s4, rr, arp, k * XP_SLOT);
            if ((wave >> 1) == 2 && j < 32) st128(s4, rr, arx, k * PX_SLOT);
        }
        for (int u = j; u < 126; u += 40) st128(s4, rr, wave * 1024 + l16, u < 108 ? D_XA + (u < 60 ? u / 20 : 3) * 4 * REG + (u < 60 ? u % 20 : u - 60) * WIN : u < 124 ? RG_ST + (u - 108) * WIN : D_SS + (u - 124) * WIN);
        wait_vm<0>();
        bar();
        const int row = l4 * 2 + ((lane >> 2) & 1), col = 16 * j + 4 * (lane & 3) + 2 * ((lane >> 3) & 1);
        __builtin_amdgcn_raw_buffer_store_b32(nosent(*(const unsigned*)(P.mem0 + row * DM + col)), rr, offo, 0, 16);
    }
    auto red_addr = [](int fl) { return R_RED + fl * 80 + (fl >> 4) * 16; };
    const unsigned rw0 = red_addr(l4 * 16 + ((l15 >> 1) & 1) * 8 + (l15 >> 2)) + (l15 & 1) * 4 + (hg ^ (l15 >> 3)) * 8;
    const unsigned rrd = red_addr(lane);
    u32x16 vx;
    const rsrc_t rf = rr;
    const int jq = j / 10;
    const unsigned fo = (lane & 3) * 128, fs = FL_VO + (jq >> 1) * WIN + ((jq & 1) * 32 + hg * 4) * 128, vxo = (unsigned)((j - jq * 10) * 4 + (hg >> 1)) * WIN + (hg & 1) * 4096;
    const unsigned xo = (l4 >> 1) * 256 + l15 * 16 + (l4 & 1) * 8, pw = R_PART + hg * 1024 + l16, pr = R_PART + (wave & 1) * 8192 + l16;
    const int fin = (wave + 2) >> 3;
    asm volatile("" : "={v[160:175]}"(vx));
    auto vx_fetch = [&](int t) {
        for (int spins = 0;; ++spins) {
            const unsigned f = __builtin_amdgcn_raw_buffer_load_b32(rf, fo, fs, 16);
            if (__all(f > (unsigned)t)) break;
            __builtin_amdgcn_s_sleep(2);
            if (spin_fail(spins, P)) break;
        }
        asm volatile("buffer_load_dwordx4 v[160:163], %1, %2, %3 offen sc1\n\tbuffer_load_dwordx4 v[164:167], %1, %2, %3 offen offset:1024 sc1\n\t"
                     "buffer_load_dwordx4 v[168:171], %1, %2, %3 offen offset:2048 sc1\n\tbuffer_load_dwordx4 v[172:175], %1, %2, %3 offen offset:3072 sc1"
                     : "+{v[160:175]}"(vx) : "v"(l16), "s"(rd), "s"(vxb(jq, t) + vxo) : "memory");
    };
    vx_fetch(0);
    unsigned tb = 0;
    STDECL;
    for (int t = 0; t < NSEG; ++t) {
        const unsigned slot = t & (NSLOT - 1);
        ST(0);
        u32x4 vm = sent4(), pm = vm;
        u32x8 pv = __builtin_shufflevector(vm, vm, 0, 1, 2, 3, 0, 1, 2, 3);
#pragma unroll 1
        for (int spins = 1023;; spins += 1024) {
            unsigned n, a;
            ASM_ST_DECL;
            asm volatile(ASM_RCRIT : "+{v[180:183]}"(vm), "+{v[184:191]}"(pv), "+{v[192:195]}"(pm), [n] "=&s"(n), [a] "=&s"(a) ASM_ST_OUT ASM_ST2_OUT ASM_ST3_OUT
                         : [rs] "s"(rr), [ov] "v"(offv), [sv] "s"(slot * VM_SLOT), [o0] "v"(offp[0]), [o1] "v"(offp[1]), [o2] "v"(offp[2]), [sp] "s"(slot * XP_SLOT),
                           "{v[160:175]}"(vx), [sidx] "v"(sidx), [rw] "v"(rw0), [rd] "v"(rrd), [oo] "v"(offo),
                           [so] "s"(((t + 1) & (NSLOT - 1)) * XM_SLOT), [pub] "s"(pubw * (NSEG - 1 - t))
                         : "memory", "vcc", "scc", "v176", "v177", "v178", "v179", "v196", "v200", "v201", "v202", "v203", "v204", "v205", "v206", "v207", "v208", "v209", "v210", "v211", "v212", "v213", "v214", "v215");
            ASM_ST_SAVE(2); ASM_ST2_SAVE(1); ASM_ST3_SAVE(7);
            if (__builtin_amdgcn_readfirstlane(n)) break;
            if (spin_fail(spins, P)) { bar(); break; }
        }
        ST(3);
        if (wave >= 4) __builtin_amdgcn_s_sleep(RXDEL);
        if ((wave >> 1) == 2 && j < 32 && t >= 2) st128(sent4(), rr, arx, ((t - 2) & (NSLOT - 1)) * PX_SLOT);
        unsigned early;
#pragma unroll 1
        for (int spins = 1023;; spins += 1024) {
            unsigned n, sx = D_XR + segb(t) + (j >> 3) * WIN + (wave & 1) * 4096 + (j & 7) * 512;
            const unsigned pxb = RG_PX + slot * PX_SLOT + hg * WIN;
            ASM_ST_DECL;
            asm volatile(ASM_RX : "+{v[160:175]}"(vx), [n] "=&s"(n), [a] "=&s"(early), [sx] "+s"(sx) ASM_ST_OUT ASM_ST2_OUT
                         : "{v[180:183]}"(vm), "{v[184:191]}"(pv), "{v[192:195]}"(pm), [l16] "v"(l16), [rs] "s"(rr), [s0] "s"(pxb), [s1] "s"(pxb + 4096), [rd] "s"(rd), [xo] "v"(xo),
                           [fo] "v"(fo), [rf] "s"(rf), [fs] "s"(fs), [tn] "s"(t + 1), [sv] "s"(vxb(jq, t + 1) + vxo),
                           [pw] "v"(pw), [pr] "v"(pr), [fin] "s"(fin)
                         : "memory", "vcc", "scc", "v196", "v200", "v201", "v202", "v203", "v204", "v205", "v206", "v207", "v208", "v209", "v210", "v211", "v212", "v213", "v214", "v215", "v216", "v217", "v218", "v219", "v220", "v221", "v222", "v223", "v224", "v225", "v226", "v227", "v228", "v229", "v230", "v231", "v232", "v233", "v234", "v235", "v236", "v237", "v238", "v239", "v240", "v241", "v242", "v243", "v244", "v245", "v246", "v247", "v248", "v249", "v250", "v251");
            ASM_ST_SAVE(4); ASM_ST2_SAVE(5);
            if (__builtin_amdgcn_readfirstlane(n)) break;
            early = 0;
            if (spin_fail(spins, P)) { bar(); break; }
        }
        STFLUSH(t);
        st128(sent4(), rr, offv, slot * VM_SLOT);
        __builtin_amdgcn_raw_buffer_store_b32(SENT, rr, offo, slot * XM_SLOT, 16);
        if (wave < 3 && t >= 1) st128(sent4(), rr, arp, ((t - 1) & (NSLOT - 1)) * XP_SLOT);
        if (wave == 5 && t + AH < NSEG) st128(sent4(), rd, (lane >> 5) * 4096 + (lane & 31) * 16, D_XA + segb(t + AH) + (j >> 3) * WIN + (j & 7) * 512);
        if (t + 1 < NSEG && !__builtin_amdgcn_readfirstlane(early)) vx_fetch(t + 1);
    }
}

template <int WHICH>
__device__ __forceinline__ void w_role(const Params& P, int wave, int L, int cu) {
    constexpr int NT = (WHICH == 3) ? 2 : 1, N = (WHICH == 3) ? FF : DM, RB = NT * 256, RPI = 1024 / RB;
    const int ln = lane_id(), l15 = ln & 15, l4 = ln >> 4;
    const rsrc_t rd = mkrsrc(lbase(P, L), RG_L);
    const int nt0 = (cu * 8 + wave) * NT;
    const rsrc_t wr = mkrsrc((WHICH == 1 ? P.w1 : WHICH == 2 ? P.w2 : P.w3) + cu * (RB / 2), DM * N * 2 - cu * RB);
    auto swz = [](int row) { return ((row & 3) | ((row >> 1) & 4)) * 32; };
    const int dr = ln * 16 / RB, tr = l4 * 8 + ((ln >> 2) & 3);
    unsigned vo = dr * (N * 2) + ((ln * 16 % RB) ^ swz(wave * RPI + dr)), ta = tr * RB + ((wave * NT * 32 + (ln & 3) * 8) ^ swz(tr));
    const int grp = nt0 / 10, neg = grp & 1, sh = (WHICH == 1) ? (neg ? -1 : 1) * ((grp >> 1) + 1) : 0;
    unsigned ra = ln * 16, wa = ln * 16 + wave * 1024, sr = WHICH == 2 ? l15 * 8 : 24576 + l15 * 80, sw = sr + wave * 8, bud;
    const unsigned oo = WHICH == 3 ? (l4 & 1) * 512 + (l4 >> 1) * 256 + l15 * 16 : (nt0 >> 3) * WIN + (nt0 & 7) * 512 + (l4 >> 1) * 256 + ((l15 + sh) & 15) * 16 + (l4 & 1) * 8;
    const unsigned up = ((unsigned)(l15 + sh) > 15u) ^ neg;
#define W_ARGS : [bud] "=s"(bud), "+{v176}"(vo), "+{v177}"(ta), "+{v249}"(ra), "+{v250}"(wa), "+{v251}"(sr), "+{v252}"(sw) \
               : "{v253}"(ln * 16), "{v254}"(oo), "{v255}"(up), [wave] "s"(wave), [rs] "s"(rd), [wr] "s"(wr), [wm] "s"(UNI(wave * 1024)), [wo] "s"(UNI(wave * RPI * N * 2)), [go] "s"(FL_VO + (WHICH * 8 + cu) * 128), \
                 [pi] "s"(UNI((WHICH == 1 ? D_XA : WHICH == 2 ? D_HS : D_X1) + (wave >> 2) * WIN + (wave & 3) * 1024)), \
                 [ng] "s"(UNI(WHICH == 3 ? D_H2 + (nt0 >> 3) * WIN + ((nt0 >> 1) & 3) * 1024 : neg)), [ss] "s"(UNI(cu == 0)) LST_IN LST_INW \
               : "memory", "vcc", "scc", "v0", "v1", "v2", "v3", "v4", "v5", "v6", "v7", "v8", "v9", "v10", "v11", "v12", "v13", "v14", "v15", "v16", "v17", "v18", "v19", "v20", "v21", "v22", "v23", "v24", "v25", "v26", "v27", "v28", "v29", "v30", "v31", "v32", "v33", "v34", "v35", "v36", "v37", "v38", "v39", "v40", "v41", "v42", "v43", "v44", "v45", "v46", "v47", "v48", "v49", "v50", "v51", "v52", "v53", "v54", "v55", "v56", "v57", "v58", "v59", "v60", "v61", "v62", "v63", "v64", "v65", "v66", "v67", "v68", "v69", "v70", "v71", "v72", "v73", "v74", "v75", "v76", "v77", "v78", "v79", "v80", "v81", "v82", "v83", "v84", "v85", "v86", "v87", "v88", "v89", "v90", "v91", "v92", "v93", "v94", "v95", "v96", "v97", "v98", "v99", "v100", "v101", "v102", "v103", "v104", "v105", "v106", "v107", "v108", "v109", "v110", "v111", "v112", "v113", "v114", "v115", "v116", "v117", "v118", "v119", "v120", "v121", "v122", "v123", "v124", "v125", "v126", "v127", "v128", "v129", "v130", "v131", "v132", "v133", "v134", "v135", "v136", "v137", "v138", "v139", "v140", "v141", "v142", "v143", "v144", "v145", "v146", "v147", "v148", "v149", "v150", "v151", "v152", "v153", "v154", "v155", "v156", "v157", "v158", "v159", "v160", "v161", "v162", "v163", "v164", "v165", "v166", "v167", "v168", "v169", "v170", "v171", "v172", "v173", "v174", "v175", "v178", "v179", "v180", "v181", "v182", "v183", "v184", "v185", "v186", "v187", "v188", "v189", "v190", "v191", "v192", "v193", "v194", "v195", "v196", "v197", "v198", "v199", "v200", "v201", "v202", "v203", "v204", "v205", "v206", "v207", "v208", "v209", "v210", "v211", "v212", "v213", "v214", "v215", "v216", "v217", "v218", "v219", "v220", "v221", "v222", "v223", "v224", "v225", "v226", "v227", "v228", "v229", "v230", "v231", "v232", "v233", "v234", "v235", "v236", "v237", "v238", "v239", "v240", "v241", "v242", "v243", "v244", "v245", "v246", "v247", "v248", "s40", "s41", "s42", "s43", "s44", "s45", "s46", "s47", "s48", "s49", "s50", "s51", "s52", "s53", "s54", "s55", "s56", "s57", "s58", "s59", "s60", "s61", "s62", "s63", "s64", "s65", "s66", "s67", "s68", "s69", "s70", "s71", "s72", "s73", "s74", "s75", "s76", "s77", "s78", "s79"
    if (WHICH == 1) asm volatile(ASM_W1_LOOP W_ARGS);
    if (WHICH == 2) asm volatile(ASM_W2_LOOP W_ARGS);
    if (WHICH == 3) asm volatile(ASM_W3_LOOP W_ARGS);
    note_timeout(P, bud);
}

constexpr unsigned W4_FL = 122880;
__device__ __forceinline__ unsigned lds_vld(unsigned a) { asm volatile("" ::: "memory"); const unsigned v = *(volatile LAS unsigned*)(size_t)a; asm volatile("" ::: "memory"); return v; }
__device__ __forceinline__ void lds_vst(unsigned a, unsigned v) { asm volatile("" ::: "memory"); *(volatile LAS unsigned*)(size_t)a = v; asm volatile("" ::: "memory"); }
__device__ __forceinline__ void w4_role(const Params& P, int wave, int L, int cu) {
    const int ln = lane_id(), l15 = ln & 15, l4 = ln >> 4;
    const unsigned l16 = ln * 16;
    const rsrc_t rd = mkrsrc(lbase(P, L), RG_L);
    STDECL;
    if (wave >= 6) wait_go(P, rd, 32 + cu);
    if (wave < 6) {
        u32x32 w[5];
        {
            const rsrc_t wr = mkrsrc(P.w4 + wave * 256 * DM + cu * 80, (FF * DM - wave * 256 * DM - cu * 80) * 2);
            const unsigned ta = wave * 20480 + (l4 * 8 + ((ln >> 2) & 3)) * 160 + (ln & 3) * 8;
            u32x4 f[5][8];
#pragma unroll
            for (int r = 0; r < 2; ++r) {
#pragma unroll
                for (int i = 0; i < 5; ++i) {
                    const unsigned u = i * 64 + ln, vo = (u / 10) * (DM * 2) + (u % 10) * 16;
#pragma unroll
                    for (int k = 0; k < 4; ++k) dma1(wr, vo, (r * 128 + k * 32) * (DM * 2), wave * 20480 + k * 5120 + i * 1024);
                }
                wait_vm<0>();
#pragma unroll
                for (int k = 0; k < 4; ++k)
#pragma unroll
                    for (int n = 0; n < 5; ++n) f[n][r * 4 + k] = tr_frag(ta + k * 5120 + n * 32, 640);
                asm volatile("s_waitcnt lgkmcnt(0)" ::: "memory");
                SB();
            }
#pragma unroll
            for (int n = 0; n < 5; ++n) {
                u32x8 g[4];
#pragma unroll
                for (int i = 0; i < 4; ++i) g[i] = __builtin_shufflevector(f[n][2 * i], f[n][2 * i + 1], 0, 1, 2, 3, 4, 5, 6, 7);
                w[n] = cat32(g[0], g[1], g[2], g[3]);
            }
            asm volatile("" : "+{v[0:31]}"(w[0]), "+{v[32:63]}"(w[1]), "+{v[64:95]}"(w[2]), "+{v[96:127]}"(w[3]), "+{v[128:159]}"(w[4]));
        }
        wait_go(P, rd, 32 + cu);
        bar();
        unsigned budget = 1u << 17;
        for (int u = 0; u < 2 * NSEG; ++u) {
            ST(0);
            const unsigned gb = D_H2 + segh(u >> 1) + (u & 1) * 4096 + wave * 2 * WIN;
            while ((int)__builtin_amdgcn_readfirstlane(lds_vld(W4_FL + 24 + (u & 1) * 4)) < u - 3) __builtin_amdgcn_s_sleep(1);
            u32x4 can;
            for (int spins = 0;; ++spins) {
                can = LD128(rd, l16, gb + WIN + 3072);
                if (__all(umax4(0u, can) != SENT)) break;
                __builtin_amdgcn_s_sleep(1);
                if (spin_fail(spins, P)) { budget = 0; break; }
            }
            ST(1);
            asm volatile(ASM_W4_M : [n] "+s"(budget)
                         : "{v[188:191]}"(can), "{v[0:31]}"(w[0]), "{v[32:63]}"(w[1]), "{v[64:95]}"(w[2]), "{v[96:127]}"(w[3]), "{v[128:159]}"(w[4]),
                           [ld] "v"(l16 + wave * 1024 + (u & 3) * 30720), [l16] "v"(l16), [rs] "s"(rd), [s0] "s"(gb), [s1] "s"(gb + WIN)
                         : "memory", "vcc", "scc", "v160", "v161", "v162", "v163", "v164", "v165", "v166", "v167", "v168", "v169", "v170", "v171", "v172", "v173", "v174", "v175", "v176", "v177", "v178", "v179", "v180", "v181", "v182", "v183", "v184", "v185", "v186", "v187", "v192", "v193", "v194", "v195", "v196", "v197", "v198", "v199", "v200", "v201", "v202", "v203", "v204", "v205", "v206", "v207", "v208", "v209", "v210", "v211", "v212", "v213", "v214", "v215", "v216", "v217", "v218", "v219", "v220", "v221", "v222", "v223", "v224", "v225", "v226", "v227", "v228", "v229", "v230", "v231", "v232", "v233", "v234", "v235", "v236", "v237", "v238", "v239", "v240", "v241", "v242", "v243", "v244");
            lds_vst(W4_FL + wave * 4, u + 1);
            ST(2);
            if (u & 1) STFLUSH(u >> 1);
        }
    } else {
        const int mt = wave - 6;
        if (ln < 8) lds_vst(W4_FL + ln * 4, 0u);
        bar();
        const rsrc_t rn = mkrsrc(lbase(P, 1), RG_L), rr = rd, ro = mkrsrc(P.out, ACT);
        auto tl = [&](int n) { return (unsigned)((cu * 5 + n) >> 3) * WIN + ((cu * 5 + n) & 7) * 512 + mt * 4096; };
        const unsigned o8 = (l4 >> 1) * 256 + l15 * 16 + (l4 & 1) * 8, o16 = tl(l4 & 1) + (l4 >> 1) * 256 + l15 * 16, o16b = tl(2 + (l4 & 1)) + (l4 >> 1) * 256 + l15 * 16;
        const unsigned q8 = l15 * (DM * 2) + l4 * 8, q16 = l15 * (DM * 2) + (l4 & 1) * 32 + (l4 >> 1) * 16;
        for (int t = 0; t < NSEG; ++t) {
            const unsigned sb = segb(t);
            ST(0);
            f32x4 res[5];
            for (int spins = 0;; ++spins) {
                u32x2 r1[5], r2[5];
                unsigned m = 0;
#pragma unroll
                for (int n = 0; n < 5; ++n) { r1[n] = LD64(rd, o8, D_X1 + sb + tl(n)); r2[n] = LD64(rd, o8, D_XA + sb + tl(n)); }
#pragma unroll
                for (int n = 0; n < 5; ++n) {
                    m = umax4(m, (u32x4){r1[n][0], r1[n][1], r2[n][0], r2[n][1]});
                    res[n] = (f32x4){lo2f(r1[n][0]), hi2f(r1[n][0]), lo2f(r1[n][1]), hi2f(r1[n][1])} + (f32x4){lo2f(r2[n][0]), hi2f(r2[n][0]), lo2f(r2[n][1]), hi2f(r2[n][1])};
                }
                if (__all(m != SENT)) break;
                __builtin_amdgcn_s_sleep(8);
                if (spin_fail(spins * 4 + 3, P)) break;
            }
            asm volatile("" : "+v"(res[0]), "+v"(res[1]), "+v"(res[2]), "+v"(res[3]), "+v"(res[4]));
            ST(1);
            while (!__all(lds_vld(W4_FL + (ln < 6 ? ln : 0) * 4) > (unsigned)(2 * t + mt))) {}
            ST(2);
            {
                const unsigned la = ((t & 1) * 2 + mt) * 30720 + l16;
                f32x4 c[30];
#pragma unroll
                for (int i = 0; i < 30; ++i) c[i] = lds_ld<f32x4>(la + ((i % 5) * 6 + i / 5) * 1024);
#pragma unroll
                for (int i = 0; i < 30; ++i) { res[i % 5] += c[i]; asm volatile("" : "+v"(res[i % 5])); }
                lds_vst(W4_FL + 24 + mt * 4, 2 * t + mt + 1);
            }
            u32x2 o[5];
#pragma unroll
            for (int n = 0; n < 5; ++n) o[n] = (u32x2){pack2(res[n][0], res[n][1]), pack2(res[n][2], res[n][3])};
            if (L) {
                const unsigned ob = (unsigned)(t * 2 + mt) * 20480 + cu * 160;
                st128_<0>(merge16(o[0], o[1]), ro, q16, ob); st128_<0>(merge16(o[2], o[3]), ro, q16, ob + 64);
                __builtin_amdgcn_raw_buffer_store_b64(o[4], ro, q8, ob + 128, 0);
            } else {
                float s = 0.f, q = 0.f;
                const bf16x2 one2 = __builtin_bit_cast(bf16x2, 0x3F803F80u);
#pragma unroll
                for (int e = 0; e < 10; ++e) {
                    const unsigned u = o[e >> 1][e & 1];
                    const bf16x2 x = __builtin_bit_cast(bf16x2, u);
                    s = __builtin_amdgcn_fdot2_f32_bf16(x, one2, s, false);
                    q = __builtin_amdgcn_fdot2_f32_bf16(x, x, q, false);
                }
                sum2_rows(s, q);
                const unsigned st = RG_ST + (unsigned)(t >> 2) * WIN + (unsigned)((t & 3) * 2 + mt) * 1024;
                __builtin_amdgcn_raw_buffer_store_b64((u32x2){nosent(__float_as_uint(s)), nosent(__float_as_uint(q))}, rr, ln < 16 ? ln * 8 : OOB, st + cu * 128, 16);
                ST(3);
                st128(merge16(o[0], o[1]), rn, o16, D_XR + sb); st128(merge16(o[2], o[3]), rn, o16b, D_XR + sb);
                __builtin_amdgcn_raw_buffer_store_b64(o[4], rn, o8, D_XR + sb + tl(4), 16);
                __builtin_amdgcn_s_sleep(W4SL);
                for (int spins = 0;; ++spins) {
                    const u32x2 a = LD64(rr, ln * 8, st), b = LD64(rr, ln * 8 + 512, st);
                    s = __uint_as_float(a[0]) + __uint_as_float(b[0]); q = __uint_as_float(a[1]) + __uint_as_float(b[1]);
                    if (__all(umax4(0u, (u32x4){a[0], a[1], b[0], b[1]}) != SENT)) break;
                    asm volatile("" ::: "memory");
                    if (spin_fail(spins, P)) break;
                }
                ST(4);
                sum2_rows(s, q);
                const float mean = s * (1.0f / DM), var = __builtin_fmaxf(q * (1.0f / DM) - mean * mean, 0.f);
                const float r = rcp(ln_den(var)), mr = -rbf(mean) * r;
#pragma unroll
                for (int n = 0; n < 5; ++n)
                    o[n] = (u32x2){pack2(__builtin_fmaf(res[n][0], r, mr), __builtin_fmaf(res[n][1], r, mr)), pack2(__builtin_fmaf(res[n][2], r, mr), __builtin_fmaf(res[n][3], r, mr))};
                st128(merge16(o[0], o[1]), rn, o16, D_XN + sb); st128(merge16(o[2], o[3]), rn, o16b, D_XN + sb);
                __builtin_amdgcn_raw_buffer_store_b64(o[4], rn, o8, D_XN + sb + tl(4), 16);
                wait_vm<0>();
                st128(((u32x4){(unsigned)t + 1, (unsigned)t + 1, (unsigned)t + 1, (unsigned)t + 1}), rn, ln < 32 ? (ln >> 3) * 2048 + (ln & 7) * 16 : OOB, FL_XN + (cu * 2 + mt) * 128);
            }
            ST(5);
            STFLUSH(t);
        }
    }
}

constexpr int LEADER = 252;
__device__ __forceinline__ unsigned leader(const Params& P, rsrc_t rw, int wave) {
    const int ln = lane_id();
    if (wave == 0) {
        const unsigned o = ln ? OOB : 0u;
        __builtin_amdgcn_raw_buffer_store_b32(0u, rw, o, CAL0, 16);
        __builtin_amdgcn_raw_buffer_store_b32(0u, rw, o, CAL0 + 8192, 16);
        unsigned m[2] = {~0u, ~0u};
#pragma unroll
        for (int r = 0; r < CALN; ++r) {
            unsigned long long a, c;
            asm volatile("s_waitcnt vmcnt(0) lgkmcnt(0)\n\ts_memtime %0\n\ts_waitcnt lgkmcnt(0)\n\tbuffer_store_dword %2, %2, %3, %4 offen sc1\n\ts_waitcnt vmcnt(0)\n\ts_memtime %1\n\ts_waitcnt lgkmcnt(0)"
                         : "=&s"(a), "=&s"(c) : "v"(o), "s"(rw), "s"(CAL0 + (r & 1) * 8192) : "memory");
            const unsigned d = (unsigned)(c - a);
            m[r & 1] = d < m[r & 1] ? d : m[r & 1];
        }
        lds_vst(0, m[0] < m[1] ? 1u : 0u);
    }
    bar();
    const unsigned par = __builtin_amdgcn_readfirstlane(lds_vld(0));
    const u32x4 s4 = sent4(), z4 = s4 ^ s4;
    for (int k = 0; k < 6; ++k) st128(z4, rw, ln * 16, FL_VO + (k * 8 + wave) * 1024);
    for (int k = 0; k < 10; ++k) st128(s4, rw, ln * 16, RG_XM + (wave >> 1) * XM_COPY + (wave & 1) * 8192 + (k >> 3) * WIN + (k & 7) * 1024);
    const unsigned mo = (wave * 32 + (ln >> 1)) * 128 + (ln & 1) * 16;
    st128(z4, rw, mo, FL_MB + 32768);
    if (wave == 0) st128(z4, rw, ln ? OOB : 256u, FL0);
    wait_vm<0>();
    bar();
    bool open = wave * 32 + (ln >> 1) != LEADER;
    for (int spins = 0; spins < SPIN_CAP; ++spins) {
        st128(s4 & (par << 13), rw, open ? mo : OOB, FL_MB);
        __builtin_amdgcn_s_sleep(20);
        const u32x4 d = LD128(rw, mo, FL_MB + 32768);
        open = open && !(d[0] && d[1] && d[2] && d[3]);
        if (!__any(open)) break;
    }
    bar();
    return par;
}
__global__ __launch_bounds__(512, 2) void mega_kernel(Params P) {
    const int wg = blockIdx.x;
    const int wave = __builtin_amdgcn_readfirstlane(threadIdx.x >> 6);
    const int L = (wg >> 2) & 1, x = wg & 3, r = wg >> 3, cpy = x;
    const int nr = (x & 1) ? 10 + RK : 10 - RK, j = (x >> 1) * 20 + (x & 1) * (10 - RK) + r - 16, i0 = (x >> 1) * 12 + (x & 1) * (6 + RK) + r - 16 - nr;
    const int i = (i0 * 5) % 24;
    const rsrc_t rw = mkrsrc(P.ws, RG_L);
    const bool lead = wg == LEADER;
    if (!lead) __builtin_amdgcn_raw_buffer_store_b32(SENT, rw, lane_id() ? OOB : 0u, FL_MB + wg * 128 + wave * 4, 16);
    if (L && r < 16) delay(D_A1);
#define FIN() __builtin_amdgcn_endpgm()
    if (r < 8) { s_role(P, wave, L, x * 8 + r, cpy); FIN(); }
    if (r < 16) { v_role(P, wave, L, x * 8 + r - 8, cpy); FIN(); }
    if (lead) P.par = leader(P, rw, wave);
    else {
        const unsigned mo = FL_MB + wg * 128 + wave * 4;
        const bool ln = r - 16 < nr && j < 16, first = ln && !L && wave == 7;
        if (!first) { P.par = handshake(rw, mo); if (L || r - 16 >= nr) delay(!L ? D_W0 : r - 16 < nr ? D_R1 : D_W1); }
        if (first || (ln && L && wave)) x_tile(P, (L ? wave * 16 : 0) + j, [&]() { if (first) P.par = handshake(rw, mo); return mkrsrc(lbase(P, 0), RG_L); });
        if (ln && L) bar();
        if (first || (ln && L && !wave)) {
            const rsrc_t r0 = mkrsrc(lbase(P, 0), RG_L);
            const int lane = lane_id();
            const unsigned fo = lane < 32 ? (lane >> 3) * 2048 + (lane & 7) * 16 : OOB;
            if (L) for (int spins = 0; spins < SPIN_CAP && __any(lane < 32 && LD128(r0, fo, FL_XN + j * 128)[0] == 0u); ++spins) __builtin_amdgcn_s_sleep(8);
            st128(sent4() & (L ? 64u : 8u), r0, fo, FL_XN + j * 128);
        }
    }
    if (r - 16 < nr) { r_role(P, wave, L, j, cpy); FIN(); }
    if (i < 5) { w_role<1>(P, wave, L, i); FIN(); }
    if (i < 10) { w_role<2>(P, wave, L, i - 5); FIN(); }
    if (i < 16) { w_role<3>(P, wave, L, i - 10); FIN(); }
    w4_role(P, wave, L, i - 16);
    FIN();
}

extern "C" void kernel_launch(void* const* d_in, const int* in_sizes, int n_in, void* d_out, int out_size, void* d_ws,
                              size_t ws_size, hipStream_t stream) {
    char* ws = (char*)d_ws + (-(size_t)d_ws & (REG - 1));
    Params P;
    P.par = 0; P.pad = 0; P.ws = ws; P.out = (bf16_t*)d_out;
    P.x = (const bf16_t*)d_in[0]; P.mem0 = (const bf16_t*)d_in[1]; P.wq = (const bf16_t*)d_in[2]; P.wk = (const bf16_t*)d_in[3]; P.wv = (const bf16_t*)d_in[4]; P.wo = (const bf16_t*)d_in[5];
    P.w1 = (const bf16_t*)d_in[8]; P.w2 = (const bf16_t*)d_in[9]; P.w3 = (const bf16_t*)d_in[10]; P.w4 = (const bf16_t*)d_in[11];
    (void)hipFuncSetAttribute((const void*)mega_kernel, hipFuncAttributeMaxDynamicSharedMemorySize, MEGA_LDS);

    mega_kernel<<<256, 512, MEGA_LDS, stream>>>(P);
}
```

```cpp
#include <hip/hip_runtime.h>
#include <stdint.h>
#include <type_traits>

typedef unsigned short bf16_t;
using bf16x2 = __attribute__((ext_vector_type(2))) __bf16;
using bf16x8 = __attribute__((ext_vector_type(8))) __bf16;
using bf16x16 = __attribute__((ext_vector_type(16))) __bf16;
using f32x2 = __attribute__((ext_vector_type(2))) float;
using f32x4 = __attribute__((ext_vector_type(4))) float;
using u32x2 = __attribute__((ext_vector_type(2))) unsigned;
using u32x4 = __attribute__((ext_vector_type(4))) unsigned;
typedef __amdgpu_buffer_rsrc_t rsrc_t;

__device__ __forceinline__ unsigned pack2(float a, float b) {
    bf16x2 r = __builtin_convertvector((f32x2){a, b}, bf16x2);
    return __builtin_bit_cast(unsigned, r);
}
__device__ __forceinline__ float rbf(float f) { return __uint_as_float(pack2(f, 0.f) << 16); }
__device__ __forceinline__ float lo2f(unsigned u) { return __uint_as_float(u << 16); }
__device__ __forceinline__ float hi2f(unsigned u) { return __uint_as_float(u & 0xFFFF0000u); }
__device__ __forceinline__ rsrc_t mkrsrc(const void* p, unsigned bytes) {
    return __builtin_amdgcn_make_buffer_rsrc((void*)p, 0, bytes, 0x00020000);
}

constexpr int S_ = 2048, DM = 640, NH = 32, DH = 192, HD = NH * DH  , SEG = 32, NSEG = 64;
constexpr int FF = 1536;

#define NCPY 4
constexpr unsigned NSLOT = 4, NCP = NCPY;
constexpr unsigned WIN = 16384, REG = 2u << 20, REGS = REG + 8192;
constexpr unsigned XP_COPY = 3 * WIN, XP_SLOT = NCP * XP_COPY;
constexpr unsigned XM_COPY = 2 * WIN, XM_SLOT = NCP * XM_COPY;
constexpr unsigned VM_SLOT = REGS;
constexpr unsigned PX_SLOT = 8 * WIN;
constexpr unsigned RG_XM = 0, RG_XP = RG_XM + NSLOT * XM_SLOT, RG_PX = RG_XP + NSLOT * XP_SLOT, RG_ST = RG_PX + NSLOT * PX_SLOT, RG_VM = 2 * REG;
static_assert(RG_ST + 16 * WIN <= REG, "ring region");
constexpr unsigned FL0 = REG, FL_PAR = REG + 4096, FL_MB = REG + 65536;
constexpr unsigned FL_VO = RG_VM + 48 * WIN, FL_XN = FL_VO + 2 * WIN, D_SS = FL_VO + 4 * WIN, CAL0 = RG_VM + 64 * WIN;
constexpr unsigned ACT = S_ * DM * 2;
constexpr unsigned D_XR = 6 * REG, D_XN = 10 * REG, D_XA = 14 * REG, D_HS = 18 * REG, D_X1 = 22 * REG, D_H2 = 26 * REG, D_VX = 34 * REG;
__host__ __device__ constexpr unsigned segb(unsigned seg) { return seg * (5 * WIN) + (seg >> 4) * (REGS - 80 * WIN); }
__host__ __device__ constexpr unsigned segh(unsigned seg) { return seg * (12 * WIN) + (seg >> 3) * (REGS - 96 * WIN); }
#define CALN 4
#define RK 3
#define PXROT 5
#define W4SL 6
#define RXDEL 4
#define VXD 4
#define VXAUX 16
__host__ __device__ constexpr unsigned vxb(unsigned q, unsigned t) { return D_VX + q * (VXD / 2 * REG) + (t & (VXD - 1)) * (REG / 2) + (t & 2) * 4096; }
constexpr int AH = 3;
constexpr unsigned RG_L = D_VX + 2 * VXD * REG;

#define SB() __builtin_amdgcn_sched_barrier(0)
#define LAS __attribute__((address_space(3)))
template <class T> __device__ __forceinline__ T lds_ld(unsigned a) { return *(const LAS T*)(a); }
template <class T> __device__ __forceinline__ void lds_st(unsigned a, T v) { *(LAS T*)(a) = v; }
constexpr unsigned OOB = 0x7FFFFF00u;
template <int N> __device__ __forceinline__ void wait_vm() { asm volatile("s_waitcnt vmcnt(%0)" :: "i"(N) : "memory"); }
__device__ __forceinline__ void bar() {
    asm volatile("s_waitcnt lgkmcnt(0)" ::: "memory");
    __builtin_amdgcn_s_barrier();
    asm volatile("" ::: "memory");
}
__device__ __forceinline__ int lane_id() {
    int ln;
    asm volatile("v_mbcnt_lo_u32_b32 %0, -1, 0\n\tv_mbcnt_hi_u32_b32 %0, -1, %0" : "=v"(ln));
    return ln;
}
__device__ __forceinline__ void swap16(unsigned& a, unsigned& b) { asm volatile("s_nop 3\n\tv_permlane16_swap_b32 %0, %1\n\ts_nop 1" : "+v"(a), "+v"(b)); }
__device__ __forceinline__ void swap32(unsigned& a, unsigned& b) { asm volatile("s_nop 3\n\tv_permlane32_swap_b32 %0, %1\n\ts_nop 1" : "+v"(a), "+v"(b)); }
__device__ __forceinline__ float sum_x16(float x) { unsigned a = __float_as_uint(x), b = a; swap16(a, b); return __uint_as_float(a) + __uint_as_float(b); }
__device__ __forceinline__ float sum_x32(float x) { unsigned a = __float_as_uint(x), b = a; swap32(a, b); return __uint_as_float(a) + __uint_as_float(b); }
__device__ __forceinline__ float max_x16(float x) { unsigned a = __float_as_uint(x), b = a; swap16(a, b); return __builtin_fmaxf(__uint_as_float(a), __uint_as_float(b)); }
__device__ __forceinline__ float max_x32(float x) { unsigned a = __float_as_uint(x), b = a; swap32(a, b); return __builtin_fmaxf(__uint_as_float(a), __uint_as_float(b)); }
__device__ __forceinline__ void sum2_rows(float& s, float& q) {
    unsigned a = __float_as_uint(s), b = a, c = __float_as_uint(q), d = c;
    asm volatile("s_nop 2\n\tv_permlane16_swap_b32 %0, %1\n\tv_permlane16_swap_b32 %2, %3\n\tv_add_f32 %0, %0, %1\n\tv_add_f32 %2, %2, %3\n\tv_mov_b32 %1, %0\n\tv_mov_b32 %3, %2\n\t"
                 "s_nop 1\n\tv_permlane32_swap_b32 %0, %1\n\tv_permlane32_swap_b32 %2, %3\n\tv_add_f32 %0, %0, %1\n\tv_add_f32 %2, %2, %3" : "+v"(a), "+v"(b), "+v"(c), "+v"(d));
    s = __uint_as_float(a); q = __uint_as_float(c);
}
__device__ __forceinline__ u32x4 merge16(u32x2 a, u32x2 b) {
    unsigned a0 = a[0], a1 = a[1], b0 = b[0], b1 = b[1];
    asm volatile("s_nop 3\n\tv_permlane16_swap_b32 %0, %2\n\tv_permlane16_swap_b32 %1, %3\n\ts_nop 1" : "+v"(a0), "+v"(a1), "+v"(b0), "+v"(b1));
    return (u32x4){a0, a1, b0, b1};
}
template <int AUXV>
__device__ __forceinline__ void st128_(u32x4 v, rsrc_t r, unsigned voff, unsigned soff) {
    __builtin_amdgcn_raw_buffer_store_b128(v, r, voff, soff, AUXV);
    asm volatile("s_nop 1" :: "v"(v) : "memory");
}
#define st128(V, R, VO_, SO_) st128_<16>(V, R, VO_, SO_)
#define LD128(R, VO_, SO_) __builtin_amdgcn_raw_buffer_load_b128(R, VO_, SO_, 16)
#define LD64(R, VO_, SO_) __builtin_amdgcn_raw_buffer_load_b64(R, VO_, SO_, 16)
#define BF8(X) __builtin_bit_cast(bf16x8, X)
__device__ __forceinline__ float gelu_tanh(float x) {
    const float c0 = -2.0f * 0.7978845608028654f * 1.4426950408889634f, c1 = c0 * 0.044715f;
    const float e = __builtin_amdgcn_exp2f(x * __builtin_fmaf(x * x, c1, c0));
    return x * __builtin_amdgcn_rcpf(1.0f + e);
}
#define MFMA(A_, B_, C_) C_ = __builtin_amdgcn_mfma_f32_16x16x32_bf16(A_, B_, C_, 0, 0, 0)
#define SMF(A_, B_, C_) C_ = __builtin_amdgcn_smfmac_f32_16x16x64_bf16(A_, B_, C_, sidx, 0, 0)
__device__ __forceinline__ float ln_den(float var) { return rbf(__builtin_amdgcn_sqrtf(rbf(rbf(var) + 1e-5f))); }
__device__ __forceinline__ float rcp(float x) { return __builtin_amdgcn_rcpf(x); }

struct Params {
    char* ws;
    const bf16_t *x, *mem0, *wq, *wk, *wv, *wo, *w1, *w2, *w3, *w4;
    bf16_t* out;
    unsigned par, pad;
};

#define RLX_AGENT __ATOMIC_RELAXED, __HIP_MEMORY_SCOPE_AGENT
#define SENT 0xFFFFFFFFu
constexpr int SPIN_CAP = 400000;
__device__ __forceinline__ bool spin_fail(int spins, const Params& P) {
    if ((spins & 1023) == 1023) {
        const rsrc_t rt = mkrsrc(P.ws + FL0, 512);
        unsigned o;
        asm volatile("v_mov_b32 %0, 0x100" : "=v"(o));
        if (__builtin_amdgcn_readfirstlane(__builtin_amdgcn_raw_buffer_load_b32(rt, o, 0, 16)) != 0u) return true;
        if (spins > SPIN_CAP) { __builtin_amdgcn_raw_buffer_store_b32(o, rt, o, 0, 16); return true; }
    }
    return false;
}
__device__ __forceinline__ bool ok4(u32x4 v) { return v[0] != SENT && v[1] != SENT && v[2] != SENT && v[3] != SENT; }
__device__ __forceinline__ unsigned nosent(unsigned d) { return d < 0xFFFFFFFEu ? d : 0xFFFFFFFEu; }
__device__ __forceinline__ u32x4 nosent4(u32x4 v) { return (u32x4){nosent(v[0]), nosent(v[1]), nosent(v[2]), nosent(v[3])}; }
__device__ __forceinline__ unsigned umax3(unsigned a, unsigned b, unsigned c) { const unsigned m = a > b ? a : b; return m > c ? m : c; }
__device__ __forceinline__ unsigned umax4(unsigned m, u32x4 v) { return umax3(umax3(m, v[0], v[1]), v[2], v[3]); }
__device__ __forceinline__ u32x4 sent4() { unsigned sv; asm volatile("v_mov_b32 %0, -1" : "=v"(sv)); return (u32x4){sv, sv, sv, sv}; }
template <int N>
__device__ __forceinline__ void poll(rsrc_t rs, unsigned base, const unsigned (&off)[N], u32x4 (&v)[N], const Params& P) {
    for (int spins = 0;; ++spins) {
        unsigned m = 0;
#pragma unroll
        for (int k = 0; k < N; ++k) v[k] = LD128(rs, off[k], base);
#pragma unroll
        for (int k = 0; k < N; ++k) m = umax4(m, v[k]);
        if (__all(m != SENT)) return;
        asm volatile("" ::: "memory");
        if (spin_fail(spins, P)) return;
    }
}
__device__ __forceinline__ bf16x16 cat16(bf16x8 a, bf16x8 b) {
    return __builtin_shufflevector(a, b, 0, 1, 2, 3, 4, 5, 6, 7, 8, 9, 10, 11, 12, 13, 14, 15);
}
__device__ __forceinline__ bf16x8 half8(bf16x16 v, int h) {
    return h ? __builtin_shufflevector(v, v, 8, 9, 10, 11, 12, 13, 14, 15) : __builtin_shufflevector(v, v, 0, 1, 2, 3, 4, 5, 6, 7);
}
__device__ __forceinline__ bf16x16 lds_frag(unsigned a, unsigned step) { return cat16(lds_ld<bf16x8>(a), lds_ld<bf16x8>(a + step)); }
__device__ __forceinline__ unsigned toff(int row, int col) {
    return segb((unsigned)row >> 5) + (unsigned)(col >> 7) * WIN + (unsigned)(row & 16) * 256u + (unsigned)((col >> 3) & 15) * 256u + (unsigned)(row & 15) * 16u + (unsigned)(col & 7) * 2u;
}
__device__ __forceinline__ char* lbase(const Params& P, int L) { return P.ws + ((P.par ^ L) << 13); }

#define ASM_ST
#define ASM_ST_DECL
#define ASM_ST_OUT
#define ASM_ST_SAVE(k)
#define ASM_STW
#define ASM_ST3
#define ASM_ST3_OUT
#define ASM_ST3_SAVE(k)
#define ASM_ST2
#define ASM_ST2_OUT
#define ASM_ST2_SAVE(k)
#define LST0
#define LST1
#define LST2
#define LST3
#define LST4
#define LST5
#define LST6
#define LST7
#define LSTINIT
#define LFLUSH200
#define LFLUSH212
#define LFLUSHR
#define LFLUSHW
#define LST_INW
#define LST_IN
#define STDECL
#define ST(k)
#define STFLUSH(t)

constexpr unsigned L_X = 0;
constexpr unsigned L_MEM = 40960;
constexpr unsigned L_Q = 51200;
constexpr unsigned L_KM = 54272;
constexpr unsigned L_QX = 57344;
constexpr unsigned L_KX = 69632;
constexpr unsigned L_W = 81920;
constexpr int MEGA_LDS = 163840;
constexpr int wi(int i, int j) { return (i >> 1) * 5 + ((i & 1) ? 2 + j : j - 1); }

#define RPUB2
#define ASM_S_LOOP \
    "s_mov_b64 s[60:61], %[ws]\n\t" \
    "s_mov_b32 s62, %[sz]\n\t" \
    "s_mov_b32 s63, 0x20000\n\t" \
    "s_mov_b32 s40, 0\n\t" \
    "s_mov_b32 s41, 0x20000\n\t" \
    LSTINIT \
    LDELAY \
    "s_cmp_eq_u32 %[wave], 0\n\t" \
    "s_cbranch_scc1 .Ls0_%=\n\t" \
    "s_cmp_eq_u32 %[wave], 1\n\t" \
    "s_cbranch_scc1 .Ls1_%=\n\t" \
    "s_cmp_eq_u32 %[wave], 2\n\t" \
    "s_cbranch_scc1 .Ls2_%=\n\t" \
    "s_cmp_eq_u32 %[wave], 3\n\t" \
    "s_cbranch_scc1 .Ls3_%=\n\t" \
    ".Ls4_%=:\n\t" \
    "s_mov_b32 s42, %[xs]\n\t" \
    "s_add_u32 m0, %[wm], 0x18000\n\t" \
    "s_mov_b32 s44, 0x0\n\t" \
    "buffer_load_dwordx4 v240, %[wr], s44 offen offset:0 nt lds\n\t" \
    "buffer_load_dwordx4 v241, %[wr], s44 offen offset:1024 nt lds\n\t" \
    "buffer_load_dwordx4 v242, %[wr], s44 offen offset:2048 nt lds\n\t" \
    "s_add_u32 m0, %[wm], 0x1e000\n\t" \
    "s_mov_b32 s44, 0x60000\n\t" \
    "buffer_load_dwordx4 v240, %[wr], s44 offen offset:0 nt lds\n\t" \
    "buffer_load_dwordx4 v241, %[wr], s44 offen offset:1024 nt lds\n\t" \
    "buffer_load_dwordx4 v242, %[wr], s44 offen offset:2048 nt lds\n\t" \
    "s_add_u32 m0, %[wm], 0x0\n\t" \
    "s_mov_b32 s44, 0xc0000\n\t" \
    "buffer_load_dwordx4 v240, %[wr], s44 offen offset:0 nt lds\n\t" \
    "buffer_load_dwordx4 v241, %[wr], s44 offen offset:1024 nt lds\n\t" \
    "buffer_load_dwordx4 v242, %[wr], s44 offen offset:2048 nt lds\n\t" \
    "s_add_u32 m0, %[wm], 0x6000\n\t" \
    "s_mov_b32 s44, 0x120000\n\t" \
    "buffer_load_dwordx4 v240, %[wr], s44 offen offset:0 nt lds\n\t" \
    "buffer_load_dwordx4 v241, %[wr], s44 offen offset:1024 nt lds\n\t" \
    "buffer_load_dwordx4 v242, %[wr], s44 offen offset:2048 nt lds\n\t" \
    "s_add_u32 m0, %[wm], 0xc000\n\t" \
    "s_mov_b32 s44, 0x180000\n\t" \
    "buffer_load_dwordx4 v240, %[wr], s44 offen offset:0 nt lds\n\t" \
    "buffer_load_dwordx4 v241, %[wr], s44 offen offset:1024 nt lds\n\t" \
    "buffer_load_dwordx4 v242, %[wr], s44 offen offset:2048 nt lds\n\t" \
    "v_add_u32 v244, 0x18000, v243\n\t" \
    "s_waitcnt vmcnt(12) lgkmcnt(0)\n\t" \
    "s_barrier \n\t" \
    "s_add_u32 m0, %[wm], 0x12000\n\t" \
    "s_mov_b32 s44, 0x1e0000\n\t" \
    "buffer_load_dwordx4 v240, %[wr], s44 offen offset:0 nt lds\n\t" \
    "buffer_load_dwordx4 v241, %[wr], s44 offen offset:1024 nt lds\n\t" \
    "buffer_load_dwordx4 v242, %[wr], s44 offen offset:2048 nt lds\n\t" \
    "ds_read_b64_tr_b16 v[200:201], v244 offset:0\n\t" \
    "ds_read_b64_tr_b16 v[202:203], v244 offset:1536\n\t" \
    "ds_read_b64_tr_b16 v[0:1], v244 offset:32\n\t" \
    "ds_read_b64_tr_b16 v[2:3], v244 offset:1568\n\t" \
    "ds_read_b64_tr_b16 v[8:9], v244 offset:64\n\t" \
    "ds_read_b64_tr_b16 v[10:11], v244 offset:1600\n\t" \
    "v_add_u32 v244, 0x1e000, v243\n\t" \
    "s_waitcnt vmcnt(12) lgkmcnt(0)\n\t" \
    "s_barrier \n\t" \
    "s_add_u32 m0, %[wm], 0x18000\n\t" \
    "s_mov_b32 s44, 0x240000\n\t" \
    "buffer_load_dwordx4 v240, %[wr], s44 offen offset:0 nt lds\n\t" \
    "buffer_load_dwordx4 v241, %[wr], s44 offen offset:1024 nt lds\n\t" \
    "buffer_load_dwordx4 v242, %[wr], s44 offen offset:2048 nt lds\n\t" \
    "ds_read_b64_tr_b16 v[204:205], v244 offset:0\n\t" \
    "ds_read_b64_tr_b16 v[206:207], v244 offset:1536\n\t" \
    "ds_read_b64_tr_b16 v[4:5], v244 offset:32\n\t" \
    "ds_read_b64_tr_b16 v[6:7], v244 offset:1568\n\t" \
    "ds_read_b64_tr_b16 v[12:13], v244 offset:64\n\t" \
    "ds_read_b64_tr_b16 v[14:15], v244 offset:1600\n\t" \
    "v_add_u32 v244, 0x0, v243\n\t" \
    "s_waitcnt vmcnt(12) lgkmcnt(0)\n\t" \
    "s_barrier \n\t" \
    "s_add_u32 m0, %[wm], 0x1e000\n\t" \
    "s_mov_b32 s44, 0x2a0000\n\t" \
    "buffer_load_dwordx4 v240, %[wr], s44 offen offset:0 nt lds\n\t" \
    "buffer_load_dwordx4 v241, %[wr], s44 offen offset:1024 nt lds\n\t" \
    "buffer_load_dwordx4 v242, %[wr], s44 offen offset:2048 nt lds\n\t" \
    "ds_read_b64_tr_b16 v[16:17], v244 offset:0\n\t" \
    "ds_read_b64_tr_b16 v[18:19], v244 offset:1536\n\t" \
    "ds_read_b64_tr_b16 v[24:25], v244 offset:32\n\t" \
    "ds_read_b64_tr_b16 v[26:27], v244 offset:1568\n\t" \
    "ds_read_b64_tr_b16 v[32:33], v244 offset:64\n\t" \
    "ds_read_b64_tr_b16 v[34:35], v244 offset:1600\n\t" \
    "v_add_u32 v244, 0x6000, v243\n\t" \
    "s_waitcnt vmcnt(12) lgkmcnt(0)\n\t" \
    "s_barrier \n\t" \
    "s_add_u32 m0, %[wm], 0x0\n\t" \
    "s_mov_b32 s44, 0x300000\n\t" \
    "buffer_load_dwordx4 v240, %[wr], s44 offen offset:0 nt lds\n\t" \
    "buffer_load_dwordx4 v241, %[wr], s44 offen offset:1024 nt lds\n\t" \
    "buffer_load_dwordx4 v242, %[wr], s44 offen offset:2048 nt lds\n\t" \
    "ds_read_b64_tr_b16 v[20:21], v244 offset:0\n\t" \
    "ds_read_b64_tr_b16 v[22:23], v244 offset:1536\n\t" \
    "ds_read_b64_tr_b16 v[28:29], v244 offset:32\n\t" \
    "ds_read_b64_tr_b16 v[30:31], v244 offset:1568\n\t" \
    "ds_read_b64_tr_b16 v[36:37], v244 offset:64\n\t" \
    "ds_read_b64_tr_b16 v[38:39], v244 offset:1600\n\t" \
    "v_add_u32 v244, 0xc000, v243\n\t" \
    "s_waitcnt vmcnt(12) lgkmcnt(0)\n\t" \
    "s_barrier \n\t" \
    "s_add_u32 m0, %[wm], 0x6000\n\t" \
    "s_mov_b32 s44, 0x360000\n\t" \
    "buffer_load_dwordx4 v240, %[wr], s44 offen offset:0 nt lds\n\t" \
    "buffer_load_dwordx4 v241, %[wr], s44 offen offset:1024 nt lds\n\t" \
    "buffer_load_dwordx4 v242, %[wr], s44 offen offset:2048 nt lds\n\t" \
    "ds_read_b64_tr_b16 v[208:209], v244 offset:0\n\t" \
    "ds_read_b64_tr_b16 v[210:211], v244 offset:1536\n\t" \
    "ds_read_b64_tr_b16 v[40:41], v244 offset:32\n\t" \
    "ds_read_b64_tr_b16 v[42:43], v244 offset:1568\n\t" \
    "ds_read_b64_tr_b16 v[48:49], v244 offset:64\n\t" \
    "ds_read_b64_tr_b16 v[50:51], v244 offset:1600\n\t" \
    "v_add_u32 v244, 0x12000, v243\n\t" \
    "s_waitcnt vmcnt(12) lgkmcnt(0)\n\t" \
    "s_barrier \n\t" \
    "s_add_u32 m0, %[wm], 0xc000\n\t" \
    "s_mov_b32 s44, 0x3c0000\n\t" \
    "buffer_load_dwordx4 v240, %[wr], s44 offen offset:0 nt lds\n\t" \
    "buffer_load_dwordx4 v241, %[wr], s44 offen offset:1024 nt lds\n\t" \
    "buffer_load_dwordx4 v242, %[wr], s44 offen offset:2048 nt lds\n\t" \
    "ds_read_b64_tr_b16 v[212:213], v244 offset:0\n\t" \
    "ds_read_b64_tr_b16 v[214:215], v244 offset:1536\n\t" \
    "ds_read_b64_tr_b16 v[44:45], v244 offset:32\n\t" \
    "ds_read_b64_tr_b16 v[46:47], v244 offset:1568\n\t" \
    "ds_read_b64_tr_b16 v[52:53], v244 offset:64\n\t" \
    "ds_read_b64_tr_b16 v[54:55], v244 offset:1600\n\t" \
    "v_add_u32 v244, 0x18000, v243\n\t" \
    "s_waitcnt vmcnt(12) lgkmcnt(0)\n\t" \
    "s_barrier \n\t" \
    "v_mov_b32 v246, 0\n\t" \
    "buffer_load_dword v246, v246, s[60:63], %[mo] offen sc1\n\t" \
    "s_add_u32 m0, %[wm], 0x12000\n\t" \
    "s_mov_b32 s44, 0x420000\n\t" \
    "buffer_load_dwordx4 v240, %[wr], s44 offen offset:0 nt lds\n\t" \
    "buffer_load_dwordx4 v241, %[wr], s44 offen offset:1024 nt lds\n\t" \
    "buffer_load_dwordx4 v242, %[wr], s44 offen offset:2048 nt lds\n\t" \
    "ds_read_b64_tr_b16 v[56:57], v244 offset:0\n\t" \
    "ds_read_b64_tr_b16 v[58:59], v244 offset:1536\n\t" \
    "ds_read_b64_tr_b16 v[64:65], v244 offset:32\n\t" \
    "ds_read_b64_tr_b16 v[66:67], v244 offset:1568\n\t" \
    "ds_read_b64_tr_b16 v[72:73], v244 offset:64\n\t" \
    "ds_read_b64_tr_b16 v[74:75], v244 offset:1600\n\t" \
    "v_add_u32 v244, 0x1e000, v243\n\t" \
    "s_waitcnt vmcnt(12) lgkmcnt(0)\n\t" \
    "s_barrier \n\t" \
    "s_add_u32 m0, %[wm], 0x18000\n\t" \
    "s_mov_b32 s44, 0x480000\n\t" \
    "buffer_load_dwordx4 v240, %[wr], s44 offen offset:0 nt lds\n\t" \
    "buffer_load_dwordx4 v241, %[wr], s44 offen offset:1024 nt lds\n\t" \
    "buffer_load_dwordx4 v242, %[wr], s44 offen offset:2048 nt lds\n\t" \
    "ds_read_b64_tr_b16 v[60:61], v244 offset:0\n\t" \
    "ds_read_b64_tr_b16 v[62:63], v244 offset:1536\n\t" \
    "ds_read_b64_tr_b16 v[68:69], v244 offset:32\n\t" \
    "ds_read_b64_tr_b16 v[70:71], v244 offset:1568\n\t" \
    "ds_read_b64_tr_b16 v[76:77], v244 offset:64\n\t" \
    "ds_read_b64_tr_b16 v[78:79], v244 offset:1600\n\t" \
    "v_add_u32 v244, 0x0, v243\n\t" \
    "s_waitcnt vmcnt(12) lgkmcnt(0)\n\t" \
    "s_barrier \n\t" \
    "s_add_u32 m0, %[wm], 0x1e000\n\t" \
    "s_mov_b32 s44, 0x4e0000\n\t" \
    "buffer_load_dwordx4 v240, %[wr], s44 offen offset:0 nt lds\n\t" \
    "buffer_load_dwordx4 v241, %[wr], s44 offen offset:1024 nt lds\n\t" \
    "buffer_load_dwordx4 v242, %[wr], s44 offen offset:2048 nt lds\n\t" \
    "ds_read_b64_tr_b16 v[216:217], v244 offset:0\n\t" \
    "ds_read_b64_tr_b16 v[218:219], v244 offset:1536\n\t" \
    "ds_read_b64_tr_b16 v[80:81], v244 offset:32\n\t" \
    "ds_read_b64_tr_b16 v[82:83], v244 offset:1568\n\t" \
    "ds_read_b64_tr_b16 v[88:89], v244 offset:64\n\t" \
    "ds_read_b64_tr_b16 v[90:91], v244 offset:1600\n\t" \
    "v_add_u32 v244, 0x6000, v243\n\t" \
    "s_waitcnt vmcnt(12) lgkmcnt(0)\n\t" \
    "s_barrier \n\t" \
    "s_add_u32 m0, %[wm], 0x0\n\t" \
    "s_mov_b32 s44, 0x540000\n\t" \
    "buffer_load_dwordx4 v240, %[wr], s44 offen offset:0 nt lds\n\t" \
    "buffer_load_dwordx4 v241, %[wr], s44 offen offset:1024 nt lds\n\t" \
    "buffer_load_dwordx4 v242, %[wr], s44 offen offset:2048 nt lds\n\t" \
    "ds_read_b64_tr_b16 v[220:221], v244 offset:0\n\t" \
    "ds_read_b64_tr_b16 v[222:223], v244 offset:1536\n\t" \
    "ds_read_b64_tr_b16 v[84:85], v244 offset:32\n\t" \
    "ds_read_b64_tr_b16 v[86:87], v244 offset:1568\n\t" \
    "ds_read_b64_tr_b16 v[92:93], v244 offset:64\n\t" \
    "ds_read_b64_tr_b16 v[94:95], v244 offset:1600\n\t" \
    "v_add_u32 v244, 0xc000, v243\n\t" \
    "s_waitcnt vmcnt(12) lgkmcnt(0)\n\t" \
    "s_barrier \n\t" \
    "s_add_u32 m0, %[wm], 0x6000\n\t" \
    "s_mov_b32 s44, 0x5a0000\n\t" \
    "buffer_load_dwordx4 v240, %[wr], s44 offen offset:0 nt lds\n\t" \
    "buffer_load_dwordx4 v241, %[wr], s44 offen offset:1024 nt lds\n\t" \
    "buffer_load_dwordx4 v242, %[wr], s44 offen offset:2048 nt lds\n\t" \
    "ds_read_b64_tr_b16 v[96:97], v244 offset:0\n\t" \
    "ds_read_b64_tr_b16 v[98:99], v244 offset:1536\n\t" \
    "ds_read_b64_tr_b16 v[104:105], v244 offset:32\n\t" \
    "ds_read_b64_tr_b16 v[106:107], v244 offset:1568\n\t" \
    "ds_read_b64_tr_b16 v[112:113], v244 offset:64\n\t" \
    "ds_read_b64_tr_b16 v[114:115], v244 offset:1600\n\t" \
    "v_add_u32 v244, 0x12000, v243\n\t" \
    "s_waitcnt vmcnt(12) lgkmcnt(0)\n\t" \
    "s_barrier \n\t" \
    ".Ls41_%=:\n\t" \
    "v_readfirstlane_b32 s43, v246\n\t" \
    "s_nop 0\n\t" \
    "s_cmp_lg_u32 s43, -1\n\t" \
    "s_cbranch_scc1 .Ls42_%=\n\t" \
    "s_cmp_eq_u32 s41, 0\n\t" \
    "s_cbranch_scc1 .Ls42_%=\n\t" \
    "s_sub_u32 s41, s41, 1\n\t" \
    "s_sleep 4\n\t" \
    "v_mov_b32 v246, 0\n\t" \
    "buffer_load_dword v246, v246, s[60:63], %[mo] offen sc1\n\t" \
    "s_waitcnt vmcnt(0)\n\t" \
    "s_branch .Ls41_%=\n\t" \
    ".Ls42_%=:\n\t" \
    "v_mov_b32 v246, 0x8000\n\t" \
    "s_mov_b64 exec, 1\n\t" \
    "buffer_store_dword v246, v246, s[60:63], %[mo] offen sc1\n\t" \
    "s_mov_b64 exec, -1\n\t" \
    "s_and_b32 s43, s43, 0x2000\n\t" \
    "s_xor_b32 s43, s43, %[lx]\n\t" \
    "s_add_u32 s60, s60, s43\n\t" \
    "s_addc_u32 s61, s61, 0\n\t" \
    "v_and_b32 v252, 0xf0, v253\n\t" \
    "v_lshlrev_b32 v252, 3, v252\n\t" \
    "buffer_load_dword v252, v252, s[60:63], %[fl] offen sc1\n\t" \
    "s_add_u32 m0, %[wm], 0xc000\n\t" \
    "s_mov_b32 s44, 0x600000\n\t" \
    "buffer_load_dwordx4 v240, %[wr], s44 offen offset:0 nt lds\n\t" \
    "buffer_load_dwordx4 v241, %[wr], s44 offen offset:1024 nt lds\n\t" \
    "buffer_load_dwordx4 v242, %[wr], s44 offen offset:2048 nt lds\n\t" \
    "ds_read_b64_tr_b16 v[100:101], v244 offset:0\n\t" \
    "ds_read_b64_tr_b16 v[102:103], v244 offset:1536\n\t" \
    "ds_read_b64_tr_b16 v[108:109], v244 offset:32\n\t" \
    "ds_read_b64_tr_b16 v[110:111], v244 offset:1568\n\t" \
    "ds_read_b64_tr_b16 v[116:117], v244 offset:64\n\t" \
    "ds_read_b64_tr_b16 v[118:119], v244 offset:1600\n\t" \
    "v_add_u32 v244, 0x18000, v243\n\t" \
    "s_waitcnt vmcnt(12) lgkmcnt(0)\n\t" \
    "s_barrier \n\t" \
    "s_add_u32 m0, %[wm], 0x12000\n\t" \
    "s_mov_b32 s44, 0x660000\n\t" \
    "buffer_load_dwordx4 v240, %[wr], s44 offen offset:0 nt lds\n\t" \
    "buffer_load_dwordx4 v241, %[wr], s44 offen offset:1024 nt lds\n\t" \
    "buffer_load_dwordx4 v242, %[wr], s44 offen offset:2048 nt lds\n\t" \
    "ds_read_b64_tr_b16 v[224:225], v244 offset:0\n\t" \
    "ds_read_b64_tr_b16 v[226:227], v244 offset:1536\n\t" \
    "ds_read_b64_tr_b16 v[120:121], v244 offset:32\n\t" \
    "ds_read_b64_tr_b16 v[122:123], v244 offset:1568\n\t" \
    "ds_read_b64_tr_b16 v[128:129], v244 offset:64\n\t" \
    "ds_read_b64_tr_b16 v[130:131], v244 offset:1600\n\t" \
    "v_add_u32 v244, 0x1e000, v243\n\t" \
    "s_waitcnt vmcnt(12) lgkmcnt(0)\n\t" \
    "s_barrier \n\t" \
    "s_add_u32 m0, %[wm], 0x18000\n\t" \
    "s_mov_b32 s44, 0x6c0000\n\t" \
    "buffer_load_dwordx4 v240, %[wr], s44 offen offset:0 nt lds\n\t" \
    "buffer_load_dwordx4 v241, %[wr], s44 offen offset:1024 nt lds\n\t" \
    "buffer_load_dwordx4 v242, %[wr], s44 offen offset:2048 nt lds\n\t" \
    "ds_read_b64_tr_b16 v[228:229], v244 offset:0\n\t" \
    "ds_read_b64_tr_b16 v[230:231], v244 offset:1536\n\t" \
    "ds_read_b64_tr_b16 v[124:125], v244 offset:32\n\t" \
    "ds_read_b64_tr_b16 v[126:127], v244 offset:1568\n\t" \
    "ds_read_b64_tr_b16 v[132:133], v244 offset:64\n\t" \
    "ds_read_b64_tr_b16 v[134:135], v244 offset:1600\n\t" \
    "v_add_u32 v244, 0x0, v243\n\t" \
    "s_waitcnt vmcnt(12) lgkmcnt(0)\n\t" \
    "s_barrier \n\t" \
    "s_add_u32 m0, %[wm], 0x1e000\n\t" \
    "s_mov_b32 s44, 0x720000\n\t" \
    "buffer_load_dwordx4 v240, %[wr], s44 offen offset:0 nt lds\n\t" \
    "buffer_load_dwordx4 v241, %[wr], s44 offen offset:1024 nt lds\n\t" \
    "buffer_load_dwordx4 v242, %[wr], s44 offen offset:2048 nt lds\n\t" \
    "ds_read_b64_tr_b16 v[136:137], v244 offset:0\n\t" \
    "ds_read_b64_tr_b16 v[138:139], v244 offset:1536\n\t" \
    "ds_read_b64_tr_b16 v[144:145], v244 offset:32\n\t" \
    "ds_read_b64_tr_b16 v[146:147], v244 offset:1568\n\t" \
    "ds_read_b64_tr_b16 v[152:153], v244 offset:64\n\t" \
    "ds_read_b64_tr_b16 v[154:155], v244 offset:1600\n\t" \
    "v_add_u32 v244, 0x6000, v243\n\t" \
    "s_waitcnt vmcnt(12) lgkmcnt(0)\n\t" \
    "s_barrier \n\t" \
    "ds_read_b64_tr_b16 v[140:141], v244 offset:0\n\t" \
    "ds_read_b64_tr_b16 v[142:143], v244 offset:1536\n\t" \
    "ds_read_b64_tr_b16 v[148:149], v244 offset:32\n\t" \
    "ds_read_b64_tr_b16 v[150:151], v244 offset:1568\n\t" \
    "ds_read_b64_tr_b16 v[156:157], v244 offset:64\n\t" \
    "ds_read_b64_tr_b16 v[158:159], v244 offset:1600\n\t" \
    "v_add_u32 v244, 0xc000, v243\n\t" \
    "s_waitcnt vmcnt(9) lgkmcnt(0)\n\t" \
    "s_barrier \n\t" \
    "ds_read_b64_tr_b16 v[232:233], v244 offset:0\n\t" \
    "ds_read_b64_tr_b16 v[234:235], v244 offset:1536\n\t" \
    "ds_read_b64_tr_b16 v[160:161], v244 offset:32\n\t" \
    "ds_read_b64_tr_b16 v[162:163], v244 offset:1568\n\t" \
    "ds_read_b64_tr_b16 v[168:169], v244 offset:64\n\t" \
    "ds_read_b64_tr_b16 v[170:171], v244 offset:1600\n\t" \
    "s_mov_b32 s43, 0\n\t" \
    "v_cmp_eq_u32 vcc, 0, v252\n\t" \
    "s_cbranch_vccnz .Ls43_%=\n\t" \
    "s_mov_b32 m0, %[m0]\n\t" \
    "s_add_u32 s45, s42, %[x1]\n\t" \
    "buffer_load_dwordx4 v253, s[60:63], s42 offen offset:0 sc1 lds\n\t" \
    "buffer_load_dwordx4 v253, s[60:63], s42 offen offset:1024 sc1 lds\n\t" \
    "buffer_load_dwordx4 v253, s[60:63], s42 offen offset:2048 sc1 lds\n\t" \
    "buffer_load_dwordx4 v253, s[60:63], s42 offen offset:3072 sc1 lds\n\t" \
    "s_mov_b32 m0, %[m1]\n\t" \
    "s_nop 0\n\t" \
    "buffer_load_dwordx4 v253, s[60:63], s45 offen offset:0 sc1 lds\n\t" \
    "buffer_load_dwordx4 v253, s[60:63], s45 offen offset:1024 sc1 lds\n\t" \
    "s_mov_b32 s43, 1\n\t" \
    ".Ls43_%=:\n\t" \
    "v_add_u32 v244, 0x12000, v243\n\t" \
    "s_waitcnt vmcnt(6) lgkmcnt(0)\n\t" \
    "s_barrier \n\t" \
    "ds_read_b64_tr_b16 v[236:237], v244 offset:0\n\t" \
    "ds_read_b64_tr_b16 v[238:239], v244 offset:1536\n\t" \
    "ds_read_b64_tr_b16 v[164:165], v244 offset:32\n\t" \
    "ds_read_b64_tr_b16 v[166:167], v244 offset:1568\n\t" \
    "ds_read_b64_tr_b16 v[172:173], v244 offset:64\n\t" \
    "ds_read_b64_tr_b16 v[174:175], v244 offset:1600\n\t" \
    "v_add_u32 v244, 0x18000, v243\n\t" \
    "s_waitcnt vmcnt(3) lgkmcnt(0)\n\t" \
    "s_barrier \n\t" \
    "ds_read_b64_tr_b16 v[176:177], v244 offset:0\n\t" \
    "ds_read_b64_tr_b16 v[178:179], v244 offset:1536\n\t" \
    "ds_read_b64_tr_b16 v[184:185], v244 offset:32\n\t" \
    "ds_read_b64_tr_b16 v[186:187], v244 offset:1568\n\t" \
    "ds_read_b64_tr_b16 v[192:193], v244 offset:64\n\t" \
    "ds_read_b64_tr_b16 v[194:195], v244 offset:1600\n\t" \
    "v_add_u32 v244, 0x1e000, v243\n\t" \
    "s_waitcnt vmcnt(0) lgkmcnt(0)\n\t" \
    "s_barrier \n\t" \
    "ds_read_b64_tr_b16 v[180:181], v244 offset:0\n\t" \
    "ds_read_b64_tr_b16 v[182:183], v244 offset:1536\n\t" \
    "ds_read_b64_tr_b16 v[188:189], v244 offset:32\n\t" \
    "ds_read_b64_tr_b16 v[190:191], v244 offset:1568\n\t" \
    "ds_read_b64_tr_b16 v[196:197], v244 offset:64\n\t" \
    "ds_read_b64_tr_b16 v[198:199], v244 offset:1600\n\t" \
    "s_waitcnt lgkmcnt(0)\n\t" \
    "s_barrier \n\t" \
    LST0 \
    "ds_write_b128 v254, v[200:203] offset:0\n\t" \
    "ds_write_b128 v254, v[204:207] offset:1024\n\t" \
    "ds_write_b128 v254, v[208:211] offset:2048\n\t" \
    "ds_write_b128 v254, v[212:215] offset:3072\n\t" \
    "ds_write_b128 v254, v[216:219] offset:4096\n\t" \
    "ds_write_b128 v254, v[220:223] offset:5120\n\t" \
    "ds_write_b128 v254, v[224:227] offset:6144\n\t" \
    "ds_write_b128 v254, v[228:231] offset:7168\n\t" \
    "ds_write_b128 v254, v[232:235] offset:8192\n\t" \
    "ds_write_b128 v254, v[236:239] offset:9216\n\t" \
    "s_cmp_lg_u32 s43, 0\n\t" \
    "s_cbranch_scc1 .Ls44_%=\n\t" \
    "s_mov_b32 s44, 0\n\t" \
    ".Ls45_%=:\n\t" \
    "v_cmp_ge_u32 vcc, s44, v252\n\t" \
    "s_cbranch_vccz .Ls46_%=\n\t" \
    "s_cmp_eq_u32 s41, 0\n\t" \
    "s_cbranch_scc1 .Ls46_%=\n\t" \
    "s_sub_u32 s41, s41, 1\n\t" \
    "s_sleep 8\n\t" \
    "v_and_b32 v252, 0xf0, v253\n\t" \
    "v_lshlrev_b32 v252, 3, v252\n\t" \
    "buffer_load_dword v252, v252, s[60:63], %[fl] offen sc1\n\t" \
    "s_waitcnt vmcnt(0)\n\t" \
    "s_branch .Ls45_%=\n\t" \
    ".Ls46_%=:\n\t" \
    "s_mov_b32 m0, %[m0]\n\t" \
    "s_add_u32 s45, s42, %[x1]\n\t" \
    "buffer_load_dwordx4 v253, s[60:63], s42 offen offset:0 sc1 lds\n\t" \
    "buffer_load_dwordx4 v253, s[60:63], s42 offen offset:1024 sc1 lds\n\t" \
    "buffer_load_dwordx4 v253, s[60:63], s42 offen offset:2048 sc1 lds\n\t" \
    "buffer_load_dwordx4 v253, s[60:63], s42 offen offset:3072 sc1 lds\n\t" \
    "s_mov_b32 m0, %[m1]\n\t" \
    "s_nop 0\n\t" \
    "buffer_load_dwordx4 v253, s[60:63], s45 offen offset:0 sc1 lds\n\t" \
    "buffer_load_dwordx4 v253, s[60:63], s45 offen offset:1024 sc1 lds\n\t" \
    ".Ls44_%=:\n\t" \
    "s_waitcnt vmcnt(0) lgkmcnt(0)\n\t" \
    "s_barrier \n\t" \
    "ds_read_b128 v[224:227], v253 offset:0\n\t" \
    "ds_read_b128 v[244:247], v254 offset:0\n\t" \
    "ds_read_b128 v[228:231], v253 offset:1024\n\t" \
    "ds_read_b128 v[232:235], v253 offset:20480\n\t" \
    "ds_read_b128 v[236:239], v253 offset:21504\n\t" \
    "ds_read_b128 v[248:251], v254 offset:1024\n\t" \
    ".Ls47_%=:\n\t" \
    "s_cmp_lg_u32 %[wave], 4\n\t" \
    "s_cbranch_scc1 .Ls48_%=\n\t" \
    "s_sleep 9\n\t" \
    ".Ls48_%=:\n\t" \
    "s_waitcnt lgkmcnt(5)\n\t" \
    "v_mfma_f32_16x16x32_bf16 v[204:207], v[0:3], v[224:227], 0\n\t" \
    "s_add_u32 m0, %[m0], 0x3000\n\t" \
    "s_waitcnt lgkmcnt(3)\n\t" \
    "v_mfma_f32_16x16x32_bf16 v[204:207], v[4:7], v[228:231], v[204:207]\n\t" \
    "ds_read_b128 v[240:243], v253 offset:2048\n\t" \
    "s_add_u32 s45, s42, 0xc000\n\t" \
    "v_mfma_f32_16x16x32_bf16 v[208:211], v[8:11], v[224:227], 0\n\t" \
    "buffer_load_dwordx4 v253, s[60:63], s45 offen offset:0 sc1 lds\n\t" \
    "v_mfma_f32_16x16x32_bf16 v[208:211], v[12:15], v[228:231], v[208:211]\n\t" \
    "v_mfma_f32_16x16x32_bf16 v[200:203], v[244:247], v[224:227], 0\n\t" \
    "buffer_load_dwordx4 v253, s[60:63], s45 offen offset:1024 sc1 lds\n\t" \
    "s_waitcnt lgkmcnt(1)\n\t" \
    "v_mfma_f32_16x16x32_bf16 v[200:203], v[248:251], v[228:231], v[200:203]\n\t" \
    "v_mfma_f32_16x16x32_bf16 v[216:219], v[0:3], v[232:235], 0\n\t" \
    "ds_read_b128 v[224:227], v253 offset:3072\n\t" \
    "buffer_load_dwordx4 v253, s[60:63], s45 offen offset:2048 sc1 lds\n\t" \
    "v_mfma_f32_16x16x32_bf16 v[216:219], v[4:7], v[236:239], v[216:219]\n\t" \
    "ds_read_b128 v[228:231], v253 offset:22528\n\t" \
    "v_mfma_f32_16x16x32_bf16 v[220:223], v[8:11], v[232:235], 0\n\t" \
    "buffer_load_dwordx4 v253, s[60:63], s45 offen offset:3072 sc1 lds\n\t" \
    "v_mfma_f32_16x16x32_bf16 v[220:223], v[12:15], v[236:239], v[220:223]\n\t" \
    "v_mfma_f32_16x16x32_bf16 v[212:215], v[244:247], v[232:235], 0\n\t" \
    "ds_read_b128 v[244:247], v254 offset:2048\n\t" \
    "v_mfma_f32_16x16x32_bf16 v[212:215], v[248:251], v[236:239], v[212:215]\n\t" \
    "ds_read_b128 v[248:251], v254 offset:3072\n\t" \
    "s_waitcnt lgkmcnt(4)\n\t" \
    "v_mfma_f32_16x16x32_bf16 v[204:207], v[24:27], v[240:243], v[204:207]\n\t" \
    "ds_read_b128 v[232:235], v253 offset:23552\n\t" \
    "s_waitcnt lgkmcnt(4)\n\t" \
    "v_mfma_f32_16x16x32_bf16 v[204:207], v[28:31], v[224:227], v[204:207]\n\t" \
    "ds_read_b128 v[236:239], v253 offset:4096\n\t" \
    "v_mfma_f32_16x16x32_bf16 v[208:211], v[32:35], v[240:243], v[208:211]\n\t" \
    "v_mfma_f32_16x16x32_bf16 v[208:211], v[36:39], v[224:227], v[208:211]\n\t" \
    "v_mfma_f32_16x16x32_bf16 v[200:203], v[16:19], v[240:243], v[200:203]\n\t" \
    "v_mfma_f32_16x16x32_bf16 v[200:203], v[20:23], v[224:227], v[200:203]\n\t" \
    "s_waitcnt lgkmcnt(4)\n\t" \
    "v_mfma_f32_16x16x32_bf16 v[216:219], v[24:27], v[228:231], v[216:219]\n\t" \
    "ds_read_b128 v[240:243], v253 offset:5120\n\t" \
    "s_waitcnt lgkmcnt(2)\n\t" \
    "v_mfma_f32_16x16x32_bf16 v[216:219], v[28:31], v[232:235], v[216:219]\n\t" \
    "ds_read_b128 v[224:227], v253 offset:24576\n\t" \
    "v_mfma_f32_16x16x32_bf16 v[220:223], v[32:35], v[228:231], v[220:223]\n\t" \
    "v_mfma_f32_16x16x32_bf16 v[220:223], v[36:39], v[232:235], v[220:223]\n\t" \
    "v_mfma_f32_16x16x32_bf16 v[212:215], v[16:19], v[228:231], v[212:215]\n\t" \
    "v_mfma_f32_16x16x32_bf16 v[212:215], v[20:23], v[232:235], v[212:215]\n\t" \
    "s_waitcnt lgkmcnt(2)\n\t" \
    "v_mfma_f32_16x16x32_bf16 v[204:207], v[40:43], v[236:239], v[204:207]\n\t" \
    "ds_read_b128 v[228:231], v253 offset:25600\n\t" \
    "s_waitcnt lgkmcnt(2)\n\t" \
    "v_mfma_f32_16x16x32_bf16 v[204:207], v[44:47], v[240:243], v[204:207]\n\t" \
    "ds_read_b128 v[232:235], v253 offset:6144\n\t" \
    "v_mfma_f32_16x16x32_bf16 v[208:211], v[48:51], v[236:239], v[208:211]\n\t" \
    "v_mfma_f32_16x16x32_bf16 v[208:211], v[52:55], v[240:243], v[208:211]\n\t" \
    "v_mfma_f32_16x16x32_bf16 v[200:203], v[244:247], v[236:239], v[200:203]\n\t" \
    "v_mfma_f32_16x16x32_bf16 v[200:203], v[248:251], v[240:243], v[200:203]\n\t" \
    "s_waitcnt lgkmcnt(2)\n\t" \
    "v_mfma_f32_16x16x32_bf16 v[216:219], v[40:43], v[224:227], v[216:219]\n\t" \
    "ds_read_b128 v[236:239], v253 offset:7168\n\t" \
    "s_waitcnt lgkmcnt(2)\n\t" \
    "v_mfma_f32_16x16x32_bf16 v[216:219], v[44:47], v[228:231], v[216:219]\n\t" \
    "ds_read_b128 v[240:243], v253 offset:26624\n\t" \
    "v_mfma_f32_16x16x32_bf16 v[220:223], v[48:51], v[224:227], v[220:223]\n\t" \
    "v_mfma_f32_16x16x32_bf16 v[220:223], v[52:55], v[228:231], v[220:223]\n\t" \
    "v_mfma_f32_16x16x32_bf16 v[212:215], v[244:247], v[224:227], v[212:215]\n\t" \
    "ds_read_b128 v[244:247], v254 offset:4096\n\t" \
    "v_mfma_f32_16x16x32_bf16 v[212:215], v[248:251], v[228:231], v[212:215]\n\t" \
    "ds_read_b128 v[248:251], v254 offset:5120\n\t" \
    "s_waitcnt lgkmcnt(4)\n\t" \
    "v_mfma_f32_16x16x32_bf16 v[204:207], v[64:67], v[232:235], v[204:207]\n\t" \
    "ds_read_b128 v[224:227], v253 offset:27648\n\t" \
    "s_waitcnt lgkmcnt(4)\n\t" \
    "v_mfma_f32_16x16x32_bf16 v[204:207], v[68:71], v[236:239], v[204:207]\n\t" \
    "ds_read_b128 v[228:231], v253 offset:8192\n\t" \
    "v_mfma_f32_16x16x32_bf16 v[208:211], v[72:75], v[232:235], v[208:211]\n\t" \
    "v_mfma_f32_16x16x32_bf16 v[208:211], v[76:79], v[236:239], v[208:211]\n\t" \
    "v_mfma_f32_16x16x32_bf16 v[200:203], v[56:59], v[232:235], v[200:203]\n\t" \
    "v_mfma_f32_16x16x32_bf16 v[200:203], v[60:63], v[236:239], v[200:203]\n\t" \
    "v_and_b32 v252, 0xf0, v253\n\t" \
    "v_lshlrev_b32 v252, 3, v252\n\t" \
    "buffer_load_dword v252, v252, s[60:63], %[fl] offen sc1\n\t" \
    "s_waitcnt lgkmcnt(4)\n\t" \
    "v_mfma_f32_16x16x32_bf16 v[216:219], v[64:67], v[240:243], v[216:219]\n\t" \
    "ds_read_b128 v[232:235], v253 offset:9216\n\t" \
    "s_waitcnt lgkmcnt(2)\n\t" \
    "v_mfma_f32_16x16x32_bf16 v[216:219], v[68:71], v[224:227], v[216:219]\n\t" \
    "ds_read_b128 v[236:239], v253 offset:28672\n\t" \
    "v_mfma_f32_16x16x32_bf16 v[220:223], v[72:75], v[240:243], v[220:223]\n\t" \
    "v_mfma_f32_16x16x32_bf16 v[220:223], v[76:79], v[224:227], v[220:223]\n\t" \
    "v_mfma_f32_16x16x32_bf16 v[212:215], v[56:59], v[240:243], v[212:215]\n\t" \
    "v_mfma_f32_16x16x32_bf16 v[212:215], v[60:63], v[224:227], v[212:215]\n\t" \
    "s_waitcnt lgkmcnt(2)\n\t" \
    "v_mfma_f32_16x16x32_bf16 v[204:207], v[80:83], v[228:231], v[204:207]\n\t" \
    "ds_read_b128 v[240:243], v253 offset:29696\n\t" \
    "s_waitcnt lgkmcnt(2)\n\t" \
    "v_mfma_f32_16x16x32_bf16 v[204:207], v[84:87], v[232:235], v[204:207]\n\t" \
    "ds_read_b128 v[224:227], v253 offset:10240\n\t" \
    "v_mfma_f32_16x16x32_bf16 v[208:211], v[88:91], v[228:231], v[208:211]\n\t" \
    "v_mfma_f32_16x16x32_bf16 v[208:211], v[92:95], v[232:235], v[208:211]\n\t" \
    "v_mfma_f32_16x16x32_bf16 v[200:203], v[244:247], v[228:231], v[200:203]\n\t" \
    "v_mfma_f32_16x16x32_bf16 v[200:203], v[248:251], v[232:235], v[200:203]\n\t" \
    "s_waitcnt lgkmcnt(2)\n\t" \
    "v_mfma_f32_16x16x32_bf16 v[216:219], v[80:83], v[236:239], v[216:219]\n\t" \
    "ds_read_b128 v[228:231], v253 offset:11264\n\t" \
    "s_add_u32 s44, s40, 1\n\t" \
    "s_waitcnt lgkmcnt(2)\n\t" \
    "v_mfma_f32_16x16x32_bf16 v[216:219], v[84:87], v[240:243], v[216:219]\n\t" \
    "ds_read_b128 v[232:235], v253 offset:30720\n\t" \
    "s_and_b32 s45, s44, 15\n\t" \
    "v_mfma_f32_16x16x32_bf16 v[220:223], v[88:91], v[236:239], v[220:223]\n\t" \
    "s_cmp_eq_u32 s45, 0\n\t" \
    "v_mfma_f32_16x16x32_bf16 v[220:223], v[92:95], v[240:243], v[220:223]\n\t" \
    "s_cselect_b32 s45, %[cj], %[cs]\n\t" \
    "v_mfma_f32_16x16x32_bf16 v[212:215], v[244:247], v[236:239], v[212:215]\n\t" \
    "ds_read_b128 v[244:247], v254 offset:6144\n\t" \
    "s_cmp_eq_u32 s44, 64\n\t" \
    "v_mfma_f32_16x16x32_bf16 v[212:215], v[248:251], v[240:243], v[212:215]\n\t" \
    "ds_read_b128 v[248:251], v254 offset:7168\n\t" \
    "s_cselect_b32 s45, 0, s45\n\t" \
    "ds_read_b128 v[236:239], v253 offset:31744\n\t" \
    "s_setprio 1\n\t" \
    "s_waitcnt vmcnt(0) lgkmcnt(0)\n\t" \
    "s_barrier \n\t" \
    LST7 \
    "s_add_u32 s42, s42, s45\n\t" \
    "s_add_u32 s44, s40, 1\n\t" \
    "s_min_u32 s44, s44, 63\n\t" \
    ".Ls49_%=:\n\t" \
    "v_cmp_ge_u32 vcc, s44, v252\n\t" \
    "s_cbranch_vccz .Ls410_%=\n\t" \
    "s_cmp_eq_u32 s41, 0\n\t" \
    "s_cbranch_scc1 .Ls410_%=\n\t" \
    "s_sub_u32 s41, s41, 1\n\t" \
    "s_sleep 2\n\t" \
    "v_and_b32 v252, 0xf0, v253\n\t" \
    "v_lshlrev_b32 v252, 3, v252\n\t" \
    "buffer_load_dword v252, v252, s[60:63], %[fl] offen sc1\n\t" \
    "s_waitcnt vmcnt(0)\n\t" \
    "s_branch .Ls49_%=\n\t" \
    ".Ls410_%=:\n\t" \
    "s_mov_b32 m0, %[m0]\n\t" \
    "s_add_u32 s45, s42, %[x1]\n\t" \
    "buffer_load_dwordx4 v253, s[60:63], s42 offen offset:0 sc1 lds\n\t" \
    "buffer_load_dwordx4 v253, s[60:63], s42 offen offset:1024 sc1 lds\n\t" \
    "buffer_load_dwordx4 v253, s[60:63], s42 offen offset:2048 sc1 lds\n\t" \
    "buffer_load_dwordx4 v253, s[60:63], s42 offen offset:3072 sc1 lds\n\t" \
    "s_mov_b32 m0, %[m1]\n\t" \
    "s_nop 0\n\t" \
    "buffer_load_dwordx4 v253, s[60:63], s45 offen offset:0 sc1 lds\n\t" \
    "buffer_load_dwordx4 v253, s[60:63], s45 offen offset:1024 sc1 lds\n\t" \
    "v_mfma_f32_16x16x32_bf16 v[204:207], v[104:107], v[224:227], v[204:207]\n\t" \
    "v_mfma_f32_16x16x32_bf16 v[204:207], v[108:111], v[228:231], v[204:207]\n\t" \
    "ds_read_b128 v[240:243], v253 offset:12288\n\t" \
    "v_mfma_f32_16x16x32_bf16 v[208:211], v[112:115], v[224:227], v[208:211]\n\t" \
    "v_mfma_f32_16x16x32_bf16 v[208:211], v[116:119], v[228:231], v[208:211]\n\t" \
    "v_mfma_f32_16x16x32_bf16 v[200:203], v[96:99], v[224:227], v[200:203]\n\t" \
    "v_mfma_f32_16x16x32_bf16 v[200:203], v[100:103], v[228:231], v[200:203]\n\t" \
    "v_mfma_f32_16x16x32_bf16 v[216:219], v[104:107], v[232:235], v[216:219]\n\t" \
    "ds_read_b128 v[224:227], v253 offset:13312\n\t" \
    "v_mfma_f32_16x16x32_bf16 v[216:219], v[108:111], v[236:239], v[216:219]\n\t" \
    "ds_read_b128 v[228:231], v253 offset:32768\n\t" \
    "v_mfma_f32_16x16x32_bf16 v[220:223], v[112:115], v[232:235], v[220:223]\n\t" \
    "v_mfma_f32_16x16x32_bf16 v[220:223], v[116:119], v[236:239], v[220:223]\n\t" \
    "v_mfma_f32_16x16x32_bf16 v[212:215], v[96:99], v[232:235], v[212:215]\n\t" \
    "v_mfma_f32_16x16x32_bf16 v[212:215], v[100:103], v[236:239], v[212:215]\n\t" \
    "s_waitcnt lgkmcnt(2)\n\t" \
    "v_mfma_f32_16x16x32_bf16 v[204:207], v[120:123], v[240:243], v[204:207]\n\t" \
    "ds_read_b128 v[232:235], v253 offset:33792\n\t" \
    "s_waitcnt lgkmcnt(2)\n\t" \
    "v_mfma_f32_16x16x32_bf16 v[204:207], v[124:127], v[224:227], v[204:207]\n\t" \
    "ds_read_b128 v[236:239], v253 offset:16384\n\t" \
    "v_mfma_f32_16x16x32_bf16 v[208:211], v[128:131], v[240:243], v[208:211]\n\t" \
    "v_mfma_f32_16x16x32_bf16 v[208:211], v[132:135], v[224:227], v[208:211]\n\t" \
    "v_mfma_f32_16x16x32_bf16 v[200:203], v[244:247], v[240:243], v[200:203]\n\t" \
    "v_mfma_f32_16x16x32_bf16 v[200:203], v[248:251], v[224:227], v[200:203]\n\t" \
    "s_waitcnt lgkmcnt(2)\n\t" \
    "v_mfma_f32_16x16x32_bf16 v[216:219], v[120:123], v[228:231], v[216:219]\n\t" \
    "ds_read_b128 v[240:243], v253 offset:17408\n\t" \
    "s_waitcnt lgkmcnt(2)\n\t" \
    "v_mfma_f32_16x16x32_bf16 v[216:219], v[124:127], v[232:235], v[216:219]\n\t" \
    "ds_read_b128 v[224:227], v253 offset:36864\n\t" \
    "v_mfma_f32_16x16x32_bf16 v[220:223], v[128:131], v[228:231], v[220:223]\n\t" \
    "v_mfma_f32_16x16x32_bf16 v[220:223], v[132:135], v[232:235], v[220:223]\n\t" \
    "v_mfma_f32_16x16x32_bf16 v[212:215], v[244:247], v[228:231], v[212:215]\n\t" \
    "ds_read_b128 v[244:247], v254 offset:8192\n\t" \
    "v_mfma_f32_16x16x32_bf16 v[212:215], v[248:251], v[232:235], v[212:215]\n\t" \
    "ds_read_b128 v[248:251], v254 offset:9216\n\t" \
    "s_waitcnt lgkmcnt(4)\n\t" \
    "v_mfma_f32_16x16x32_bf16 v[204:207], v[160:163], v[236:239], v[204:207]\n\t" \
    "ds_read_b128 v[228:231], v253 offset:37888\n\t" \
    "s_waitcnt lgkmcnt(4)\n\t" \
    "v_mfma_f32_16x16x32_bf16 v[204:207], v[164:167], v[240:243], v[204:207]\n\t" \
    "ds_read_b128 v[232:235], v253 offset:14336\n\t" \
    "v_mfma_f32_16x16x32_bf16 v[208:211], v[168:171], v[236:239], v[208:211]\n\t" \
    "v_mfma_f32_16x16x32_bf16 v[208:211], v[172:175], v[240:243], v[208:211]\n\t" \
    "s_waitcnt lgkmcnt(3)\n\t" \
    "v_mfma_f32_16x16x32_bf16 v[200:203], v[244:247], v[236:239], v[200:203]\n\t" \
    "s_waitcnt lgkmcnt(2)\n\t" \
    "v_mfma_f32_16x16x32_bf16 v[200:203], v[248:251], v[240:243], v[200:203]\n\t" \
    "v_mfma_f32_16x16x32_bf16 v[216:219], v[160:163], v[224:227], v[216:219]\n\t" \
    "ds_read_b128 v[236:239], v253 offset:15360\n\t" \
    "v_add_u32 v252, %[q2], v253\n\t" \
    "s_waitcnt lgkmcnt(2)\n\t" \
    "v_mfma_f32_16x16x32_bf16 v[216:219], v[164:167], v[228:231], v[216:219]\n\t" \
    "ds_read_b128 v[240:243], v253 offset:18432\n\t" \
    "v_mfma_f32_16x16x32_bf16 v[220:223], v[168:171], v[224:227], v[220:223]\n\t" \
    "v_mfma_f32_16x16x32_bf16 v[220:223], v[172:175], v[228:231], v[220:223]\n\t" \
    "v_mfma_f32_16x16x32_bf16 v[212:215], v[244:247], v[224:227], v[212:215]\n\t" \
    "v_mfma_f32_16x16x32_bf16 v[212:215], v[248:251], v[228:231], v[212:215]\n\t" \
    "s_waitcnt lgkmcnt(2)\n\t" \
    "v_mfma_f32_16x16x32_bf16 v[204:207], v[144:147], v[232:235], v[204:207]\n\t" \
    "ds_read_b128 v[224:227], v253 offset:19456\n\t" \
    "v_mov_b64 v[244:245], 0\n\t" \
    "s_waitcnt lgkmcnt(2)\n\t" \
    "v_mfma_f32_16x16x32_bf16 v[204:207], v[148:151], v[236:239], v[204:207]\n\t" \
    "ds_read_b128 v[228:231], v253 offset:34816\n\t" \
    "v_mov_b64 v[246:247], 0\n\t" \
    "v_mfma_f32_16x16x32_bf16 v[208:211], v[152:155], v[232:235], v[208:211]\n\t" \
    "v_mov_b64 v[248:249], 0\n\t" \
    "v_mfma_f32_16x16x32_bf16 v[208:211], v[156:159], v[236:239], v[208:211]\n\t" \
    "v_mov_b64 v[250:251], 0\n\t" \
    "v_mfma_f32_16x16x32_bf16 v[200:203], v[136:139], v[232:235], v[200:203]\n\t" \
    "v_mfma_f32_16x16x32_bf16 v[200:203], v[140:143], v[236:239], v[200:203]\n\t" \
    "s_waitcnt lgkmcnt(2)\n\t" \
    "v_mfma_f32_16x16x32_bf16 v[204:207], v[184:187], v[240:243], v[204:207]\n\t" \
    "ds_read_b128 v[232:235], v253 offset:35840\n\t" \
    "s_waitcnt lgkmcnt(2)\n\t" \
    "v_mfma_f32_16x16x32_bf16 v[204:207], v[188:191], v[224:227], v[204:207]\n\t" \
    "ds_read_b128 v[236:239], v253 offset:38912\n\t" \
    "v_mfma_f32_16x16x32_bf16 v[208:211], v[192:195], v[240:243], v[208:211]\n\t" \
    "v_mfma_f32_16x16x32_bf16 v[208:211], v[196:199], v[224:227], v[208:211]\n\t" \
    "v_mfma_f32_16x16x32_bf16 v[200:203], v[176:179], v[240:243], v[200:203]\n\t" \
    "v_mfma_f32_16x16x32_bf16 v[200:203], v[180:183], v[224:227], v[200:203]\n\t" \
    "ds_read_b128 v[240:243], v253 offset:39936\n\t" \
    "s_waitcnt lgkmcnt(3)\n\t" \
    "v_mfma_f32_16x16x32_bf16 v[216:219], v[144:147], v[228:231], v[216:219]\n\t" \
    "s_waitcnt lgkmcnt(2)\n\t" \
    "v_mfma_f32_16x16x32_bf16 v[216:219], v[148:151], v[232:235], v[216:219]\n\t" \
    "v_cvt_pk_bf16_f32 v204, v204, v208\n\t" \
    "v_cvt_pk_bf16_f32 v205, v205, v209\n\t" \
    "s_waitcnt lgkmcnt(1)\n\t" \
    "v_mfma_f32_16x16x32_bf16 v[216:219], v[184:187], v[236:239], v[216:219]\n\t" \
    "v_cvt_pk_bf16_f32 v206, v206, v210\n\t" \
    "v_cvt_pk_bf16_f32 v207, v207, v211\n\t" \
    "s_waitcnt lgkmcnt(0)\n\t" \
    "v_mfma_f32_16x16x32_bf16 v[216:219], v[188:191], v[240:243], v[216:219]\n\t" \
    "ds_write_b128 v252, v[204:207] offset:0\n\t" \
    "v_mfma_f32_16x16x32_bf16 v[220:223], v[152:155], v[228:231], v[220:223]\n\t" \
    "v_mfma_f32_16x16x32_bf16 v[220:223], v[156:159], v[232:235], v[220:223]\n\t" \
    "v_add_u32 v252, %[qs], v253\n\t" \
    "v_cvt_pk_bf16_f32 v200, v200, v201\n\t" \
    "v_mfma_f32_16x16x32_bf16 v[220:223], v[192:195], v[236:239], v[220:223]\n\t" \
    "v_cvt_pk_bf16_f32 v201, v202, v203\n\t" \
    "ds_write_b64 v252, v[200:201] offset:0\n\t" \
    "v_mfma_f32_16x16x32_bf16 v[220:223], v[196:199], v[240:243], v[220:223]\n\t" \
    "v_mfma_f32_16x16x32_bf16 v[212:215], v[136:139], v[228:231], v[212:215]\n\t" \
    "v_mov_b64 v[208:209], 0\n\t" \
    "v_mfma_f32_16x16x32_bf16 v[212:215], v[140:143], v[232:235], v[212:215]\n\t" \
    "v_mov_b64 v[210:211], 0\n\t" \
    "v_mfma_f32_16x16x32_bf16 v[212:215], v[176:179], v[236:239], v[212:215]\n\t" \
    "v_add_u32 v252, %[q2], v253\n\t" \
    "v_cvt_pk_bf16_f32 v216, v216, v220\n\t" \
    "v_mfma_f32_16x16x32_bf16 v[212:215], v[180:183], v[240:243], v[212:215]\n\t" \
    "v_cvt_pk_bf16_f32 v217, v217, v221\n\t" \
    "v_cvt_pk_bf16_f32 v218, v218, v222\n\t" \
    "v_cvt_pk_bf16_f32 v219, v219, v223\n\t" \
    "ds_write_b128 v252, v[216:219] offset:6144\n\t" \
    "v_add_u32 v252, %[qs], v253\n\t" \
    "s_nop 2\n\t" \
    "v_cvt_pk_bf16_f32 v212, v212, v213\n\t" \
    "v_cvt_pk_bf16_f32 v213, v214, v215\n\t" \
    "ds_write_b64 v252, v[212:213] offset:6144\n\t" \
    LST6 \
    "s_setprio 2\n\t" \
    "s_waitcnt vmcnt(0) lgkmcnt(0)\n\t" \
    "s_barrier \n\t" \
    LST3 \
    "ds_read_b128 v[224:227], v253 offset:40960\n\t" \
    "ds_read_b128 v[228:231], v253 offset:41984\n\t" \
    "ds_read_b128 v[232:235], v253 offset:43008\n\t" \
    "ds_read_b128 v[236:239], v253 offset:44032\n\t" \
    "ds_read_b128 v[240:243], v253 offset:45056\n\t" \
    "ds_read_b128 v[200:203], v254 offset:0\n\t" \
    "ds_read_b128 v[204:207], v254 offset:1024\n\t" \
    "ds_read_b128 v[216:219], v254 offset:2048\n\t" \
    "ds_read_b128 v[220:223], v254 offset:3072\n\t" \
    LFLUSH212 \
    "v_bfe_i32 v212, v253, 5, 1\n\t" \
    "v_and_b32 v212, 0xaaaa, v212\n\t" \
    "v_or_b32 v212, 0x4444, v212\n\t" \
    "v_and_b32 v213, 0xffff, v255\n\t" \
    "v_lshrrev_b32 v214, 16, v255\n\t" \
    "s_waitcnt lgkmcnt(8)\n\t" \
    "v_smfmac_f32_16x16x64_bf16 v[244:247], v[224:227], v[0:7], v212\n\t" \
    "v_smfmac_f32_16x16x64_bf16 v[248:251], v[224:227], v[8:15], v212\n\t" \
    "s_waitcnt lgkmcnt(2)\n\t" \
    "v_smfmac_f32_16x16x64_bf16 v[208:211], v[224:227], v[200:207], v212\n\t" \
    "ds_read_b128 v[200:203], v254 offset:4096\n\t" \
    "ds_read_b128 v[204:207], v254 offset:5120\n\t" \
    "ds_read_b128 v[224:227], v253 offset:46080\n\t" \
    "v_smfmac_f32_16x16x64_bf16 v[208:211], v[228:231], v[16:23], v212\n\t" \
    "v_smfmac_f32_16x16x64_bf16 v[244:247], v[228:231], v[24:31], v212\n\t" \
    "v_smfmac_f32_16x16x64_bf16 v[248:251], v[228:231], v[32:39], v212\n\t" \
    "ds_read_b128 v[228:231], v253 offset:47104\n\t" \
    "s_waitcnt lgkmcnt(4)\n\t" \
    "v_smfmac_f32_16x16x64_bf16 v[208:211], v[232:235], v[216:223], v212\n\t" \
    "ds_read_b128 v[216:219], v254 offset:6144\n\t" \
    "ds_read_b128 v[220:223], v254 offset:7168\n\t" \
    "v_smfmac_f32_16x16x64_bf16 v[244:247], v[232:235], v[40:47], v212\n\t" \
    "v_smfmac_f32_16x16x64_bf16 v[248:251], v[232:235], v[48:55], v212\n\t" \
    "ds_read_b128 v[232:235], v253 offset:48128\n\t" \
    "v_smfmac_f32_16x16x64_bf16 v[208:211], v[236:239], v[56:63], v212\n\t" \
    "v_smfmac_f32_16x16x64_bf16 v[244:247], v[236:239], v[64:71], v212\n\t" \
    "v_smfmac_f32_16x16x64_bf16 v[248:251], v[236:239], v[72:79], v212\n\t" \
    "ds_read_b128 v[236:239], v253 offset:49152\n\t" \
    "s_waitcnt lgkmcnt(6)\n\t" \
    "v_smfmac_f32_16x16x64_bf16 v[208:211], v[240:243], v[200:207], v212\n\t" \
    "ds_read_b128 v[200:203], v254 offset:8192\n\t" \
    "ds_read_b128 v[204:207], v254 offset:9216\n\t" \
    "v_smfmac_f32_16x16x64_bf16 v[244:247], v[240:243], v[80:87], v212\n\t" \
    "v_smfmac_f32_16x16x64_bf16 v[248:251], v[240:243], v[88:95], v212\n\t" \
    "ds_read_b128 v[240:243], v253 offset:50176\n\t" \
    "s_waitcnt lgkmcnt(8)\n\t" \
    "v_smfmac_f32_16x16x64_bf16 v[208:211], v[224:227], v[96:103], v212\n\t" \
    "v_smfmac_f32_16x16x64_bf16 v[244:247], v[224:227], v[104:111], v212\n\t" \
    "v_smfmac_f32_16x16x64_bf16 v[248:251], v[224:227], v[112:119], v212\n\t" \
    "s_waitcnt lgkmcnt(5)\n\t" \
    "v_smfmac_f32_16x16x64_bf16 v[208:211], v[228:231], v[216:223], v212\n\t" \
    "s_waitcnt lgkmcnt(4)\n\t" \
    "v_smfmac_f32_16x16x64_bf16 v[208:211], v[232:235], v[136:143], v212\n\t" \
    "s_waitcnt lgkmcnt(1)\n\t" \
    "v_smfmac_f32_16x16x64_bf16 v[208:211], v[236:239], v[200:207], v212\n\t" \
    "s_waitcnt lgkmcnt(0)\n\t" \
    "v_smfmac_f32_16x16x64_bf16 v[208:211], v[240:243], v[176:183], v212\n\t" \
    "v_smfmac_f32_16x16x64_bf16 v[244:247], v[228:231], v[120:127], v212\n\t" \
    "v_smfmac_f32_16x16x64_bf16 v[244:247], v[232:235], v[144:151], v212\n\t" \
    "v_smfmac_f32_16x16x64_bf16 v[244:247], v[236:239], v[160:167], v212\n\t" \
    "v_add_f32 v208, v208, v210\n\t" \
    "v_add_f32 v209, v209, v211\n\t" \
    "v_cvt_pk_bf16_f32 v208, v208, v209\n\t" \
    "v_smfmac_f32_16x16x64_bf16 v[244:247], v[240:243], v[184:191], v212\n\t" \
    "ds_write_b16 v213, v208 offset:0\n\t" \
    "ds_write_b16_d16_hi v213, v208 offset:16\n\t" \
    "v_smfmac_f32_16x16x64_bf16 v[248:251], v[228:231], v[128:135], v212\n\t" \
    "v_smfmac_f32_16x16x64_bf16 v[248:251], v[232:235], v[152:159], v212\n\t" \
    "v_smfmac_f32_16x16x64_bf16 v[248:251], v[236:239], v[168:175], v212\n\t" \
    "v_add_f32 v244, v244, v246\n\t" \
    "v_add_f32 v245, v245, v247\n\t" \
    "v_smfmac_f32_16x16x64_bf16 v[248:251], v[240:243], v[192:199], v212\n\t" \
    "s_nop 7\n\t" \
    "v_add_f32 v248, v248, v250\n\t" \
    "v_add_f32 v249, v249, v251\n\t" \
    "v_cvt_pk_bf16_f32 v244, v244, v248\n\t" \
    "v_cvt_pk_bf16_f32 v245, v245, v249\n\t" \
    "ds_write2_b32 v214, v244, v245 offset1:4\n\t" \
    "s_setprio 0\n\t" \
    "s_waitcnt lgkmcnt(0)\n\t" \
    "s_barrier \n\t" \
    LST4 \
    "ds_read_b128 v[224:227], v253 offset:0\n\t" \
    "ds_read_b128 v[244:247], v254 offset:0\n\t" \
    "ds_read_b128 v[228:231], v253 offset:1024\n\t" \
    "ds_read_b128 v[232:235], v253 offset:20480\n\t" \
    "ds_read_b128 v[236:239], v253 offset:21504\n\t" \
    "ds_read_b128 v[248:251], v254 offset:1024\n\t" \
    "s_add_u32 s40, s40, 1\n\t" \
    "s_cmp_lg_u32 s40, 64\n\t" \
    "s_cbranch_scc1 .Ls47_%=\n\t" \
    "s_branch .Lend%=\n\t" \
    ".Ls3_%=:\n\t" \
    "s_add_u32 m0, %[wm], 0x18000\n\t" \
    "s_mov_b32 s44, 0x0\n\t" \
    "buffer_load_dwordx4 v240, %[wr], s44 offen offset:0 nt lds\n\t" \
    "buffer_load_dwordx4 v241, %[wr], s44 offen offset:1024 nt lds\n\t" \
    "buffer_load_dwordx4 v242, %[wr], s44 offen offset:2048 nt lds\n\t" \
    "s_add_u32 m0, %[wm], 0x1e000\n\t" \
    "s_mov_b32 s44, 0x60000\n\t" \
    "buffer_load_dwordx4 v240, %[wr], s44 offen offset:0 nt lds\n\t" \
    "buffer_load_dwordx4 v241, %[wr], s44 offen offset:1024 nt lds\n\t" \
    "buffer_load_dwordx4 v242, %[wr], s44 offen offset:2048 nt lds\n\t" \
    "s_add_u32 m0, %[wm], 0x0\n\t" \
    "s_mov_b32 s44, 0xc0000\n\t" \
    "buffer_load_dwordx4 v240, %[wr], s44 offen offset:0 nt lds\n\t" \
    "buffer_load_dwordx4 v241, %[wr], s44 offen offset:1024 nt lds\n\t" \
    "buffer_load_dwordx4 v242, %[wr], s44 offen offset:2048 nt lds\n\t" \
    "s_add_u32 m0, %[wm], 0x6000\n\t" \
    "s_mov_b32 s44, 0x120000\n\t" \
    "buffer_load_dwordx4 v240, %[wr], s44 offen offset:0 nt lds\n\t" \
    "buffer_load_dwordx4 v241, %[wr], s44 offen offset:1024 nt lds\n\t" \
    "buffer_load_dwordx4 v242, %[wr], s44 offen offset:2048 nt lds\n\t" \
    "s_add_u32 m0, %[wm], 0xc000\n\t" \
    "s_mov_b32 s44, 0x180000\n\t" \
    "buffer_load_dwordx4 v240, %[wr], s44 offen offset:0 nt lds\n\t" \
    "buffer_load_dwordx4 v241, %[wr], s44 offen offset:1024 nt lds\n\t" \
    "buffer_load_dwordx4 v242, %[wr], s44 offen offset:2048 nt lds\n\t" \
    "v_add_u32 v244, 0x18000, v243\n\t" \
    "s_waitcnt vmcnt(12) lgkmcnt(0)\n\t" \
    "s_barrier \n\t" \
    "s_add_u32 m0, %[wm], 0x12000\n\t" \
    "s_mov_b32 s44, 0x1e0000\n\t" \
    "buffer_load_dwordx4 v240, %[wr], s44 offen offset:0 nt lds\n\t" \
    "buffer_load_dwordx4 v241, %[wr], s44 offen offset:1024 nt lds\n\t" \
    "buffer_load_dwordx4 v242, %[wr], s44 offen offset:2048 nt lds\n\t" \
    "ds_read_b64_tr_b16 v[208:209], v244 offset:0\n\t" \
    "ds_read_b64_tr_b16 v[210:211], v244 offset:1536\n\t" \
    "ds_read_b64_tr_b16 v[40:41], v244 offset:32\n\t" \
    "ds_read_b64_tr_b16 v[42:43], v244 offset:1568\n\t" \
    "ds_read_b64_tr_b16 v[48:49], v244 offset:64\n\t" \
    "ds_read_b64_tr_b16 v[50:51], v244 offset:1600\n\t" \
    "v_add_u32 v244, 0x1e000, v243\n\t" \
    "s_waitcnt vmcnt(12) lgkmcnt(0)\n\t" \
    "s_barrier \n\t" \
    "s_add_u32 m0, %[wm], 0x18000\n\t" \
    "s_mov_b32 s44, 0x240000\n\t" \
    "buffer_load_dwordx4 v240, %[wr], s44 offen offset:0 nt lds\n\t" \
    "buffer_load_dwordx4 v241, %[wr], s44 offen offset:1024 nt lds\n\t" \
    "buffer_load_dwordx4 v242, %[wr], s44 offen offset:2048 nt lds\n\t" \
    "ds_read_b64_tr_b16 v[212:213], v244 offset:0\n\t" \
    "ds_read_b64_tr_b16 v[214:215], v244 offset:1536\n\t" \
    "ds_read_b64_tr_b16 v[44:45], v244 offset:32\n\t" \
    "ds_read_b64_tr_b16 v[46:47], v244 offset:1568\n\t" \
    "ds_read_b64_tr_b16 v[52:53], v244 offset:64\n\t" \
    "ds_read_b64_tr_b16 v[54:55], v244 offset:1600\n\t" \
    "v_add_u32 v244, 0x0, v243\n\t" \
    "s_waitcnt vmcnt(12) lgkmcnt(0)\n\t" \
    "s_barrier \n\t" \
    "s_add_u32 m0, %[wm], 0x1e000\n\t" \
    "s_mov_b32 s44, 0x2a0000\n\t" \
    "buffer_load_dwordx4 v240, %[wr], s44 offen offset:0 nt lds\n\t" \
    "buffer_load_dwordx4 v241, %[wr], s44 offen offset:1024 nt lds\n\t" \
    "buffer_load_dwordx4 v242, %[wr], s44 offen offset:2048 nt lds\n\t" \
    "ds_read_b64_tr_b16 v[56:57], v244 offset:0\n\t" \
    "ds_read_b64_tr_b16 v[58:59], v244 offset:1536\n\t" \
    "ds_read_b64_tr_b16 v[64:65], v244 offset:32\n\t" \
    "ds_read_b64_tr_b16 v[66:67], v244 offset:1568\n\t" \
    "ds_read_b64_tr_b16 v[72:73], v244 offset:64\n\t" \
    "ds_read_b64_tr_b16 v[74:75], v244 offset:1600\n\t" \
    "v_add_u32 v244, 0x6000, v243\n\t" \
    "s_waitcnt vmcnt(12) lgkmcnt(0)\n\t" \
    "s_barrier \n\t" \
    "s_add_u32 m0, %[wm], 0x0\n\t" \
    "s_mov_b32 s44, 0x300000\n\t" \
    "buffer_load_dwordx4 v240, %[wr], s44 offen offset:0 nt lds\n\t" \
    "buffer_load_dwordx4 v241, %[wr], s44 offen offset:1024 nt lds\n\t" \
    "buffer_load_dwordx4 v242, %[wr], s44 offen offset:2048 nt lds\n\t" \
    "ds_read_b64_tr_b16 v[60:61], v244 offset:0\n\t" \
    "ds_read_b64_tr_b16 v[62:63], v244 offset:1536\n\t" \
    "ds_read_b64_tr_b16 v[68:69], v244 offset:32\n\t" \
    "ds_read_b64_tr_b16 v[70:71], v244 offset:1568\n\t" \
    "ds_read_b64_tr_b16 v[76:77], v244 offset:64\n\t" \
    "ds_read_b64_tr_b16 v[78:79], v244 offset:1600\n\t" \
    "v_add_u32 v244, 0xc000, v243\n\t" \
    "s_waitcnt vmcnt(12) lgkmcnt(0)\n\t" \
    "s_barrier \n\t" \
    "s_add_u32 m0, %[wm], 0x6000\n\t" \
    "s_mov_b32 s44, 0x360000\n\t" \
    "buffer_load_dwordx4 v240, %[wr], s44 offen offset:0 nt lds\n\t" \
    "buffer_load_dwordx4 v241, %[wr], s44 offen offset:1024 nt lds\n\t" \
    "buffer_load_dwordx4 v242, %[wr], s44 offen offset:2048 nt lds\n\t" \
    "ds_read_b64_tr_b16 v[216:217], v244 offset:0\n\t" \
    "ds_read_b64_tr_b16 v[218:219], v244 offset:1536\n\t" \
    "ds_read_b64_tr_b16 v[80:81], v244 offset:32\n\t" \
    "ds_read_b64_tr_b16 v[82:83], v244 offset:1568\n\t" \
    "ds_read_b64_tr_b16 v[88:89], v244 offset:64\n\t" \
    "ds_read_b64_tr_b16 v[90:91], v244 offset:1600\n\t" \
    "v_add_u32 v244, 0x12000, v243\n\t" \
    "s_waitcnt vmcnt(12) lgkmcnt(0)\n\t" \
    "s_barrier \n\t" \
    "s_add_u32 m0, %[wm], 0xc000\n\t" \
    "s_mov_b32 s44, 0x3c0000\n\t" \
    "buffer_load_dwordx4 v240, %[wr], s44 offen offset:0 nt lds\n\t" \
    "buffer_load_dwordx4 v241, %[wr], s44 offen offset:1024 nt lds\n\t" \
    "buffer_load_dwordx4 v242, %[wr], s44 offen offset:2048 nt lds\n\t" \
    "ds_read_b64_tr_b16 v[220:221], v244 offset:0\n\t" \
    "ds_read_b64_tr_b16 v[222:223], v244 offset:1536\n\t" \
    "ds_read_b64_tr_b16 v[84:85], v244 offset:32\n\t" \
    "ds_read_b64_tr_b16 v[86:87], v244 offset:1568\n\t" \
    "ds_read_b64_tr_b16 v[92:93], v244 offset:64\n\t" \
    "ds_read_b64_tr_b16 v[94:95], v244 offset:1600\n\t" \
    "v_add_u32 v244, 0x18000, v243\n\t" \
    "s_waitcnt vmcnt(12) lgkmcnt(0)\n\t" \
    "s_barrier \n\t" \
    "v_mov_b32 v246, 0\n\t" \
    "buffer_load_dword v246, v246, s[60:63], %[mo] offen sc1\n\t" \
    "s_add_u32 m0, %[wm], 0x12000\n\t" \
    "s_mov_b32 s44, 0x420000\n\t" \
    "buffer_load_dwordx4 v240, %[wr], s44 offen offset:0 nt lds\n\t" \
    "buffer_load_dwordx4 v241, %[wr], s44 offen offset:1024 nt lds\n\t" \
    "buffer_load_dwordx4 v242, %[wr], s44 offen offset:2048 nt lds\n\t" \
    "ds_read_b64_tr_b16 v[96:97], v244 offset:0\n\t" \
    "ds_read_b64_tr_b16 v[98:99], v244 offset:1536\n\t" \
    "ds_read_b64_tr_b16 v[104:105], v244 offset:32\n\t" \
    "ds_read_b64_tr_b16 v[106:107], v244 offset:1568\n\t" \
    "ds_read_b64_tr_b16 v[112:113], v244 offset:64\n\t" \
    "ds_read_b64_tr_b16 v[114:115], v244 offset:1600\n\t" \
    "v_add_u32 v244, 0x1e000, v243\n\t" \
    "s_waitcnt vmcnt(12) lgkmcnt(0)\n\t" \
    "s_barrier \n\t" \
    "s_add_u32 m0, %[wm], 0x18000\n\t" \
    "s_mov_b32 s44, 0x480000\n\t" \
    "buffer_load_dwordx4 v240, %[wr], s44 offen offset:0 nt lds\n\t" \
    "buffer_load_dwordx4 v241, %[wr], s44 offen offset:1024 nt lds\n\t" \
    "buffer_load_dwordx4 v242, %[wr], s44 offen offset:2048 nt lds\n\t" \
    "ds_read_b64_tr_b16 v[100:101], v244 offset:0\n\t" \
    "ds_read_b64_tr_b16 v[102:103], v244 offset:1536\n\t" \
    "ds_read_b64_tr_b16 v[108:109], v244 offset:32\n\t" \
    "ds_read_b64_tr_b16 v[110:111], v244 offset:1568\n\t" \
    "ds_read_b64_tr_b16 v[116:117], v244 offset:64\n\t" \
    "ds_read_b64_tr_b16 v[118:119], v244 offset:1600\n\t" \
    "v_add_u32 v244, 0x0, v243\n\t" \
    "s_waitcnt vmcnt(12) lgkmcnt(0)\n\t" \
    "s_barrier \n\t" \
    "s_add_u32 m0, %[wm], 0x1e000\n\t" \
    "s_mov_b32 s44, 0x4e0000\n\t" \
    "buffer_load_dwordx4 v240, %[wr], s44 offen offset:0 nt lds\n\t" \
    "buffer_load_dwordx4 v241, %[wr], s44 offen offset:1024 nt lds\n\t" \
    "buffer_load_dwordx4 v242, %[wr], s44 offen offset:2048 nt lds\n\t" \
    "ds_read_b64_tr_b16 v[224:225], v244 offset:0\n\t" \
    "ds_read_b64_tr_b16 v[226:227], v244 offset:1536\n\t" \
    "ds_read_b64_tr_b16 v[120:121], v244 offset:32\n\t" \
    "ds_read_b64_tr_b16 v[122:123], v244 offset:1568\n\t" \
    "ds_read_b64_tr_b16 v[128:129], v244 offset:64\n\t" \
    "ds_read_b64_tr_b16 v[130:131], v244 offset:1600\n\t" \
    "v_add_u32 v244, 0x6000, v243\n\t" \
    "s_waitcnt vmcnt(12) lgkmcnt(0)\n\t" \
    "s_barrier \n\t" \
    "s_add_u32 m0, %[wm], 0x0\n\t" \
    "s_mov_b32 s44, 0x540000\n\t" \
    "buffer_load_dwordx4 v240, %[wr], s44 offen offset:0 nt lds\n\t" \
    "buffer_load_dwordx4 v241, %[wr], s44 offen offset:1024 nt lds\n\t" \
    "buffer_load_dwordx4 v242, %[wr], s44 offen offset:2048 nt lds\n\t" \
    "ds_read_b64_tr_b16 v[228:229], v244 offset:0\n\t" \
    "ds_read_b64_tr_b16 v[230:231], v244 offset:1536\n\t" \
    "ds_read_b64_tr_b16 v[124:125], v244 offset:32\n\t" \
    "ds_read_b64_tr_b16 v[126:127], v244 offset:1568\n\t" \
    "ds_read_b64_tr_b16 v[132:133], v244 offset:64\n\t" \
    "ds_read_b64_tr_b16 v[134:135], v244 offset:1600\n\t" \
    "v_add_u32 v244, 0xc000, v243\n\t" \
    "s_waitcnt vmcnt(12) lgkmcnt(0)\n\t" \
    "s_barrier \n\t" \
    "s_add_u32 m0, %[wm], 0x6000\n\t" \
    "s_mov_b32 s44, 0x5a0000\n\t" \
    "buffer_load_dwordx4 v240, %[wr], s44 offen offset:0 nt lds\n\t" \
    "buffer_load_dwordx4 v241, %[wr], s44 offen offset:1024 nt lds\n\t" \
    "buffer_load_dwordx4 v242, %[wr], s44 offen offset:2048 nt lds\n\t" \
    "ds_read_b64_tr_b16 v[136:137], v244 offset:0\n\t" \
    "ds_read_b64_tr_b16 v[138:139], v244 offset:1536\n\t" \
    "ds_read_b64_tr_b16 v[144:145], v244 offset:32\n\t" \
    "ds_read_b64_tr_b16 v[146:147], v244 offset:1568\n\t" \
    "ds_read_b64_tr_b16 v[152:153], v244 offset:64\n\t" \
    "ds_read_b64_tr_b16 v[154:155], v244 offset:1600\n\t" \
    "v_add_u32 v244, 0x12000, v243\n\t" \
    "s_waitcnt vmcnt(12) lgkmcnt(0)\n\t" \
    "s_barrier \n\t" \
    ".Ls31_%=:\n\t" \
    "v_readfirstlane_b32 s43, v246\n\t" \
    "s_nop 0\n\t" \
    "s_cmp_lg_u32 s43, -1\n\t" \
    "s_cbranch_scc1 .Ls32_%=\n\t" \
    "s_cmp_eq_u32 s41, 0\n\t" \
    "s_cbranch_scc1 .Ls32_%=\n\t" \
    "s_sub_u32 s41, s41, 1\n\t" \
    "s_sleep 4\n\t" \
    "v_mov_b32 v246, 0\n\t" \
    "buffer_load_dword v246, v246, s[60:63], %[mo] offen sc1\n\t" \
    "s_waitcnt vmcnt(0)\n\t" \
    "s_branch .Ls31_%=\n\t" \
    ".Ls32_%=:\n\t" \
    "v_mov_b32 v246, 0x8000\n\t" \
    "s_mov_b64 exec, 1\n\t" \
    "buffer_store_dword v246, v246, s[60:63], %[mo] offen sc1\n\t" \
    "s_mov_b64 exec, -1\n\t" \
    "s_and_b32 s43, s43, 0x2000\n\t" \
    "s_xor_b32 s43, s43, %[lx]\n\t" \
    "s_add_u32 s60, s60, s43\n\t" \
    "s_addc_u32 s61, s61, 0\n\t" \
    "s_add_u32 m0, %[wm], 0xc000\n\t" \
    "s_mov_b32 s44, 0x600000\n\t" \
    "buffer_load_dwordx4 v240, %[wr], s44 offen offset:0 nt lds\n\t" \
    "buffer_load_dwordx4 v241, %[wr], s44 offen offset:1024 nt lds\n\t" \
    "buffer_load_dwordx4 v242, %[wr], s44 offen offset:2048 nt lds\n\t" \
    "ds_read_b64_tr_b16 v[140:141], v244 offset:0\n\t" \
    "ds_read_b64_tr_b16 v[142:143], v244 offset:1536\n\t" \
    "ds_read_b64_tr_b16 v[148:149], v244 offset:32\n\t" \
    "ds_read_b64_tr_b16 v[150:151], v244 offset:1568\n\t" \
    "ds_read_b64_tr_b16 v[156:157], v244 offset:64\n\t" \
    "ds_read_b64_tr_b16 v[158:159], v244 offset:1600\n\t" \
    "v_add_u32 v244, 0x18000, v243\n\t" \
    "s_waitcnt vmcnt(12) lgkmcnt(0)\n\t" \
    "s_barrier \n\t" \
    "s_add_u32 m0, %[wm], 0x12000\n\t" \
    "s_mov_b32 s44, 0x660000\n\t" \
    "buffer_load_dwordx4 v240, %[wr], s44 offen offset:0 nt lds\n\t" \
    "buffer_load_dwordx4 v241, %[wr], s44 offen offset:1024 nt lds\n\t" \
    "buffer_load_dwordx4 v242, %[wr], s44 offen offset:2048 nt lds\n\t" \
    "ds_read_b64_tr_b16 v[232:233], v244 offset:0\n\t" \
    "ds_read_b64_tr_b16 v[234:235], v244 offset:1536\n\t" \
    "ds_read_b64_tr_b16 v[160:161], v244 offset:32\n\t" \
    "ds_read_b64_tr_b16 v[162:163], v244 offset:1568\n\t" \
    "ds_read_b64_tr_b16 v[168:169], v244 offset:64\n\t" \
    "ds_read_b64_tr_b16 v[170:171], v244 offset:1600\n\t" \
    "v_add_u32 v244, 0x1e000, v243\n\t" \
    "s_waitcnt vmcnt(12) lgkmcnt(0)\n\t" \
    "s_barrier \n\t" \
    "s_add_u32 m0, %[wm], 0x18000\n\t" \
    "s_mov_b32 s44, 0x6c0000\n\t" \
    "buffer_load_dwordx4 v240, %[wr], s44 offen offset:0 nt lds\n\t" \
    "buffer_load_dwordx4 v241, %[wr], s44 offen offset:1024 nt lds\n\t" \
    "buffer_load_dwordx4 v242, %[wr], s44 offen offset:2048 nt lds\n\t" \
    "ds_read_b64_tr_b16 v[236:237], v244 offset:0\n\t" \
    "ds_read_b64_tr_b16 v[238:239], v244 offset:1536\n\t" \
    "ds_read_b64_tr_b16 v[164:165], v244 offset:32\n\t" \
    "ds_read_b64_tr_b16 v[166:167], v244 offset:1568\n\t" \
    "ds_read_b64_tr_b16 v[172:173], v244 offset:64\n\t" \
    "ds_read_b64_tr_b16 v[174:175], v244 offset:1600\n\t" \
    "v_add_u32 v244, 0x0, v243\n\t" \
    "s_waitcnt vmcnt(12) lgkmcnt(0)\n\t" \
    "s_barrier \n\t" \
    "s_add_u32 m0, %[wm], 0x1e000\n\t" \
    "s_mov_b32 s44, 0x720000\n\t" \
    "buffer_load_dwordx4 v240, %[wr], s44 offen offset:0 nt lds\n\t" \
    "buffer_load_dwordx4 v241, %[wr], s44 offen offset:1024 nt lds\n\t" \
    "buffer_load_dwordx4 v242, %[wr], s44 offen offset:2048 nt lds\n\t" \
    "ds_read_b64_tr_b16 v[176:177], v244 offset:0\n\t" \
    "ds_read_b64_tr_b16 v[178:179], v244 offset:1536\n\t" \
    "ds_read_b64_tr_b16 v[184:185], v244 offset:32\n\t" \
    "ds_read_b64_tr_b16 v[186:187], v244 offset:1568\n\t" \
    "ds_read_b64_tr_b16 v[192:193], v244 offset:64\n\t" \
    "ds_read_b64_tr_b16 v[194:195], v244 offset:1600\n\t" \
    "v_add_u32 v244, 0x6000, v243\n\t" \
    "s_waitcnt vmcnt(12) lgkmcnt(0)\n\t" \
    "s_barrier \n\t" \
    "ds_read_b64_tr_b16 v[180:181], v244 offset:0\n\t" \
    "ds_read_b64_tr_b16 v[182:183], v244 offset:1536\n\t" \
    "ds_read_b64_tr_b16 v[188:189], v244 offset:32\n\t" \
    "ds_read_b64_tr_b16 v[190:191], v244 offset:1568\n\t" \
    "ds_read_b64_tr_b16 v[196:197], v244 offset:64\n\t" \
    "ds_read_b64_tr_b16 v[198:199], v244 offset:1600\n\t" \
    "v_add_u32 v244, 0xc000, v243\n\t" \
    "s_waitcnt vmcnt(9) lgkmcnt(0)\n\t" \
    "s_barrier \n\t" \
    "ds_read_b64_tr_b16 v[200:201], v244 offset:0\n\t" \
    "ds_read_b64_tr_b16 v[202:203], v244 offset:1536\n\t" \
    "ds_read_b64_tr_b16 v[0:1], v244 offset:32\n\t" \
    "ds_read_b64_tr_b16 v[2:3], v244 offset:1568\n\t" \
    "ds_read_b64_tr_b16 v[8:9], v244 offset:64\n\t" \
    "ds_read_b64_tr_b16 v[10:11], v244 offset:1600\n\t" \
    "v_add_u32 v244, 0x12000, v243\n\t" \
    "s_waitcnt vmcnt(6) lgkmcnt(0)\n\t" \
    "s_barrier \n\t" \
    "ds_read_b64_tr_b16 v[204:205], v244 offset:0\n\t" \
    "ds_read_b64_tr_b16 v[206:207], v244 offset:1536\n\t" \
    "ds_read_b64_tr_b16 v[4:5], v244 offset:32\n\t" \
    "ds_read_b64_tr_b16 v[6:7], v244 offset:1568\n\t" \
    "ds_read_b64_tr_b16 v[12:13], v244 offset:64\n\t" \
    "ds_read_b64_tr_b16 v[14:15], v244 offset:1600\n\t" \
    "v_add_u32 v244, 0x18000, v243\n\t" \
    "s_waitcnt vmcnt(3) lgkmcnt(0)\n\t" \
    "s_barrier \n\t" \
    "ds_read_b64_tr_b16 v[16:17], v244 offset:0\n\t" \
    "ds_read_b64_tr_b16 v[18:19], v244 offset:1536\n\t" \
    "ds_read_b64_tr_b16 v[24:25], v244 offset:32\n\t" \
    "ds_read_b64_tr_b16 v[26:27], v244 offset:1568\n\t" \
    "ds_read_b64_tr_b16 v[32:33], v244 offset:64\n\t" \
    "ds_read_b64_tr_b16 v[34:35], v244 offset:1600\n\t" \
    "v_add_u32 v244, 0x1e000, v243\n\t" \
    "s_waitcnt vmcnt(0) lgkmcnt(0)\n\t" \
    "s_barrier \n\t" \
    "ds_read_b64_tr_b16 v[20:21], v244 offset:0\n\t" \
    "ds_read_b64_tr_b16 v[22:23], v244 offset:1536\n\t" \
    "ds_read_b64_tr_b16 v[28:29], v244 offset:32\n\t" \
    "ds_read_b64_tr_b16 v[30:31], v244 offset:1568\n\t" \
    "ds_read_b64_tr_b16 v[36:37], v244 offset:64\n\t" \
    "ds_read_b64_tr_b16 v[38:39], v244 offset:1600\n\t" \
    "s_waitcnt lgkmcnt(0)\n\t" \
    "s_barrier \n\t" \
    LST0 \
    "ds_write_b128 v254, v[200:203] offset:55296\n\t" \
    "ds_write_b128 v254, v[204:207] offset:56320\n\t" \
    "ds_write_b128 v254, v[208:211] offset:57344\n\t" \
    "ds_write_b128 v254, v[212:215] offset:58368\n\t" \
    "ds_write_b128 v254, v[216:219] offset:59392\n\t" \
    "ds_write_b128 v254, v[220:223] offset:60416\n\t" \
    "ds_write_b128 v254, v[224:227] offset:61440\n\t" \
    "ds_write_b128 v254, v[228:231] offset:62464\n\t" \
    "ds_write_b128 v254, v[232:235] offset:63488\n\t" \
    "ds_write_b128 v254, v[236:239] offset:64512\n\t" \
    "s_waitcnt vmcnt(0) lgkmcnt(0)\n\t" \
    "s_barrier \n\t" \
    "ds_read_b128 v[208:211], v254 offset:24576\n\t" \
    "ds_read_b128 v[224:227], v253 offset:0\n\t" \
    "ds_read_b128 v[228:231], v253 offset:20480\n\t" \
    "ds_read_b128 v[212:215], v254 offset:25600\n\t" \
    "ds_read_b128 v[232:235], v253 offset:1024\n\t" \
    "ds_read_b128 v[236:239], v253 offset:21504\n\t" \
    "ds_read_b128 v[216:219], v254 offset:26624\n\t" \
    "ds_read_b128 v[240:243], v253 offset:2048\n\t" \
    "ds_read_b128 v[244:247], v253 offset:22528\n\t" \
    "ds_read_b128 v[220:223], v254 offset:27648\n\t" \
    "ds_read_b128 v[248:251], v253 offset:3072\n\t" \
    "s_waitcnt lgkmcnt(9)\n\t" \
    "v_mfma_f32_16x16x32_bf16 v[200:203], v[208:211], v[224:227], 0\n\t" \
    "ds_read_b128 v[224:227], v253 offset:23552\n\t" \
    "s_waitcnt lgkmcnt(9)\n\t" \
    "v_mfma_f32_16x16x32_bf16 v[204:207], v[208:211], v[228:231], 0\n\t" \
    "ds_read_b128 v[228:231], v253 offset:4096\n\t" \
    "ds_read_b128 v[208:211], v254 offset:28672\n\t" \
    "s_waitcnt lgkmcnt(9)\n\t" \
    "v_mfma_f32_16x16x32_bf16 v[200:203], v[212:215], v[232:235], v[200:203]\n\t" \
    "ds_read_b128 v[232:235], v253 offset:24576\n\t" \
    "s_waitcnt lgkmcnt(9)\n\t" \
    "v_mfma_f32_16x16x32_bf16 v[204:207], v[212:215], v[236:239], v[204:207]\n\t" \
    "ds_read_b128 v[236:239], v253 offset:5120\n\t" \
    "ds_read_b128 v[212:215], v254 offset:29696\n\t" \
    "s_waitcnt lgkmcnt(9)\n\t" \
    "v_mfma_f32_16x16x32_bf16 v[200:203], v[216:219], v[240:243], v[200:203]\n\t" \
    "ds_read_b128 v[240:243], v253 offset:25600\n\t" \
    ".Ls33_%=:\n\t" \
    "s_waitcnt lgkmcnt(9)\n\t" \
    "v_mfma_f32_16x16x32_bf16 v[204:207], v[216:219], v[244:247], v[204:207]\n\t" \
    "ds_read_b128 v[244:247], v253 offset:6144\n\t" \
    "ds_read_b128 v[216:219], v254 offset:30720\n\t" \
    "s_waitcnt lgkmcnt(9)\n\t" \
    "v_mfma_f32_16x16x32_bf16 v[200:203], v[220:223], v[248:251], v[200:203]\n\t" \
    "ds_read_b128 v[248:251], v253 offset:26624\n\t" \
    "s_waitcnt lgkmcnt(9)\n\t" \
    "v_mfma_f32_16x16x32_bf16 v[204:207], v[220:223], v[224:227], v[204:207]\n\t" \
    "ds_read_b128 v[224:227], v253 offset:7168\n\t" \
    "ds_read_b128 v[220:223], v254 offset:31744\n\t" \
    "s_waitcnt lgkmcnt(9)\n\t" \
    "v_mfma_f32_16x16x32_bf16 v[200:203], v[208:211], v[228:231], v[200:203]\n\t" \
    "ds_read_b128 v[228:231], v253 offset:27648\n\t" \
    "s_waitcnt lgkmcnt(9)\n\t" \
    "v_mfma_f32_16x16x32_bf16 v[204:207], v[208:211], v[232:235], v[204:207]\n\t" \
    "ds_read_b128 v[232:235], v253 offset:8192\n\t" \
    "ds_read_b128 v[208:211], v254 offset:32768\n\t" \
    "s_waitcnt lgkmcnt(9)\n\t" \
    "v_mfma_f32_16x16x32_bf16 v[200:203], v[212:215], v[236:239], v[200:203]\n\t" \
    "ds_read_b128 v[236:239], v253 offset:28672\n\t" \
    "s_waitcnt lgkmcnt(9)\n\t" \
    "v_mfma_f32_16x16x32_bf16 v[204:207], v[212:215], v[240:243], v[204:207]\n\t" \
    "ds_read_b128 v[240:243], v253 offset:9216\n\t" \
    "ds_read_b128 v[212:215], v254 offset:33792\n\t" \
    "s_waitcnt lgkmcnt(9)\n\t" \
    "v_mfma_f32_16x16x32_bf16 v[200:203], v[216:219], v[244:247], v[200:203]\n\t" \
    "ds_read_b128 v[244:247], v253 offset:29696\n\t" \
    "s_waitcnt lgkmcnt(9)\n\t" \
    "v_mfma_f32_16x16x32_bf16 v[204:207], v[216:219], v[248:251], v[204:207]\n\t" \
    "s_waitcnt lgkmcnt(7)\n\t" \
    "v_mfma_f32_16x16x32_bf16 v[200:203], v[220:223], v[224:227], v[200:203]\n\t" \
    "s_waitcnt lgkmcnt(6)\n\t" \
    "v_mfma_f32_16x16x32_bf16 v[204:207], v[220:223], v[228:231], v[204:207]\n\t" \
    "s_waitcnt lgkmcnt(4)\n\t" \
    "v_mfma_f32_16x16x32_bf16 v[200:203], v[208:211], v[232:235], v[200:203]\n\t" \
    "s_waitcnt lgkmcnt(3)\n\t" \
    "v_mfma_f32_16x16x32_bf16 v[204:207], v[208:211], v[236:239], v[204:207]\n\t" \
    "s_waitcnt lgkmcnt(1)\n\t" \
    "v_mfma_f32_16x16x32_bf16 v[200:203], v[212:215], v[240:243], v[200:203]\n\t" \
    "s_waitcnt lgkmcnt(0)\n\t" \
    "v_mfma_f32_16x16x32_bf16 v[204:207], v[212:215], v[244:247], v[204:207]\n\t" \
    "ds_read_b128 v[224:227], v253 offset:0\n\t" \
    "ds_read_b128 v[244:247], v254 offset:57344\n\t" \
    "ds_read_b128 v[228:231], v253 offset:1024\n\t" \
    "ds_read_b128 v[232:235], v253 offset:20480\n\t" \
    "ds_read_b128 v[236:239], v253 offset:21504\n\t" \
    "ds_read_b128 v[248:251], v254 offset:58368\n\t" \
    "s_nop 1\n\t" \
    "ds_write_b128 v253, v[200:203] offset:40960\n\t" \
    "ds_write_b128 v253, v[204:207] offset:41984\n\t" \
    "s_nop 1\n\t" \
    "s_waitcnt lgkmcnt(7)\n\t" \
    "v_mfma_f32_16x16x32_bf16 v[204:207], v[40:43], v[224:227], 0\n\t" \
    "s_waitcnt lgkmcnt(5)\n\t" \
    "v_mfma_f32_16x16x32_bf16 v[204:207], v[44:47], v[228:231], v[204:207]\n\t" \
    "ds_read_b128 v[240:243], v253 offset:2048\n\t" \
    "v_mfma_f32_16x16x32_bf16 v[208:211], v[48:51], v[224:227], 0\n\t" \
    "v_mfma_f32_16x16x32_bf16 v[208:211], v[52:55], v[228:231], v[208:211]\n\t" \
    "v_mfma_f32_16x16x32_bf16 v[200:203], v[244:247], v[224:227], 0\n\t" \
    "s_waitcnt lgkmcnt(3)\n\t" \
    "v_mfma_f32_16x16x32_bf16 v[200:203], v[248:251], v[228:231], v[200:203]\n\t" \
    "v_mfma_f32_16x16x32_bf16 v[216:219], v[40:43], v[232:235], 0\n\t" \
    "ds_read_b128 v[224:227], v253 offset:3072\n\t" \
    "v_mfma_f32_16x16x32_bf16 v[216:219], v[44:47], v[236:239], v[216:219]\n\t" \
    "ds_read_b128 v[228:231], v253 offset:22528\n\t" \
    "v_mfma_f32_16x16x32_bf16 v[220:223], v[48:51], v[232:235], 0\n\t" \
    "v_mfma_f32_16x16x32_bf16 v[220:223], v[52:55], v[236:239], v[220:223]\n\t" \
    "v_mfma_f32_16x16x32_bf16 v[212:215], v[244:247], v[232:235], 0\n\t" \
    "ds_read_b128 v[244:247], v254 offset:59392\n\t" \
    "v_mfma_f32_16x16x32_bf16 v[212:215], v[248:251], v[236:239], v[212:215]\n\t" \
    "ds_read_b128 v[248:251], v254 offset:60416\n\t" \
    "s_waitcnt lgkmcnt(4)\n\t" \
    "v_mfma_f32_16x16x32_bf16 v[204:207], v[64:67], v[240:243], v[204:207]\n\t" \
    "ds_read_b128 v[232:235], v253 offset:23552\n\t" \
    "s_waitcnt lgkmcnt(4)\n\t" \
    "v_mfma_f32_16x16x32_bf16 v[204:207], v[68:71], v[224:227], v[204:207]\n\t" \
    "ds_read_b128 v[236:239], v253 offset:4096\n\t" \
    "v_mfma_f32_16x16x32_bf16 v[208:211], v[72:75], v[240:243], v[208:211]\n\t" \
    "v_mfma_f32_16x16x32_bf16 v[208:211], v[76:79], v[224:227], v[208:211]\n\t" \
    "v_mfma_f32_16x16x32_bf16 v[200:203], v[56:59], v[240:243], v[200:203]\n\t" \
    "v_mfma_f32_16x16x32_bf16 v[200:203], v[60:63], v[224:227], v[200:203]\n\t" \
    "s_waitcnt lgkmcnt(4)\n\t" \
    "v_mfma_f32_16x16x32_bf16 v[216:219], v[64:67], v[228:231], v[216:219]\n\t" \
    "ds_read_b128 v[240:243], v253 offset:5120\n\t" \
    "s_waitcnt lgkmcnt(2)\n\t" \
    "v_mfma_f32_16x16x32_bf16 v[216:219], v[68:71], v[232:235], v[216:219]\n\t" \
    "ds_read_b128 v[224:227], v253 offset:24576\n\t" \
    "v_mfma_f32_16x16x32_bf16 v[220:223], v[72:75], v[228:231], v[220:223]\n\t" \
    "v_mfma_f32_16x16x32_bf16 v[220:223], v[76:79], v[232:235], v[220:223]\n\t" \
    "v_mfma_f32_16x16x32_bf16 v[212:215], v[56:59], v[228:231], v[212:215]\n\t" \
    "v_mfma_f32_16x16x32_bf16 v[212:215], v[60:63], v[232:235], v[212:215]\n\t" \
    "s_waitcnt lgkmcnt(2)\n\t" \
    "v_mfma_f32_16x16x32_bf16 v[204:207], v[80:83], v[236:239], v[204:207]\n\t" \
    "ds_read_b128 v[228:231], v253 offset:25600\n\t" \
    "s_waitcnt lgkmcnt(2)\n\t" \
    "v_mfma_f32_16x16x32_bf16 v[204:207], v[84:87], v[240:243], v[204:207]\n\t" \
    "ds_read_b128 v[232:235], v253 offset:6144\n\t" \
    "v_mfma_f32_16x16x32_bf16 v[208:211], v[88:91], v[236:239], v[208:211]\n\t" \
    "v_mfma_f32_16x16x32_bf16 v[208:211], v[92:95], v[240:243], v[208:211]\n\t" \
    "v_mfma_f32_16x16x32_bf16 v[200:203], v[244:247], v[236:239], v[200:203]\n\t" \
    "v_mfma_f32_16x16x32_bf16 v[200:203], v[248:251], v[240:243], v[200:203]\n\t" \
    "s_waitcnt lgkmcnt(2)\n\t" \
    "v_mfma_f32_16x16x32_bf16 v[216:219], v[80:83], v[224:227], v[216:219]\n\t" \
    "ds_read_b128 v[236:239], v253 offset:7168\n\t" \
    "s_waitcnt lgkmcnt(2)\n\t" \
    "v_mfma_f32_16x16x32_bf16 v[216:219], v[84:87], v[228:231], v[216:219]\n\t" \
    "ds_read_b128 v[240:243], v253 offset:26624\n\t" \
    "v_mfma_f32_16x16x32_bf16 v[220:223], v[88:91], v[224:227], v[220:223]\n\t" \
    "v_mfma_f32_16x16x32_bf16 v[220:223], v[92:95], v[228:231], v[220:223]\n\t" \
    "v_mfma_f32_16x16x32_bf16 v[212:215], v[244:247], v[224:227], v[212:215]\n\t" \
    "ds_read_b128 v[244:247], v254 offset:61440\n\t" \
    "v_mfma_f32_16x16x32_bf16 v[212:215], v[248:251], v[228:231], v[212:215]\n\t" \
    "ds_read_b128 v[248:251], v254 offset:62464\n\t" \
    "s_waitcnt lgkmcnt(4)\n\t" \
    "v_mfma_f32_16x16x32_bf16 v[204:207], v[104:107], v[232:235], v[204:207]\n\t" \
    "ds_read_b128 v[224:227], v253 offset:27648\n\t" \
    "s_waitcnt lgkmcnt(4)\n\t" \
    "v_mfma_f32_16x16x32_bf16 v[204:207], v[108:111], v[236:239], v[204:207]\n\t" \
    "ds_read_b128 v[228:231], v253 offset:8192\n\t" \
    "v_mfma_f32_16x16x32_bf16 v[208:211], v[112:115], v[232:235], v[208:211]\n\t" \
    "v_mfma_f32_16x16x32_bf16 v[208:211], v[116:119], v[236:239], v[208:211]\n\t" \
    "v_mfma_f32_16x16x32_bf16 v[200:203], v[96:99], v[232:235], v[200:203]\n\t" \
    "v_mfma_f32_16x16x32_bf16 v[200:203], v[100:103], v[236:239], v[200:203]\n\t" \
    "s_waitcnt lgkmcnt(4)\n\t" \
    "v_mfma_f32_16x16x32_bf16 v[216:219], v[104:107], v[240:243], v[216:219]\n\t" \
    "ds_read_b128 v[232:235], v253 offset:9216\n\t" \
    "s_waitcnt lgkmcnt(2)\n\t" \
    "v_mfma_f32_16x16x32_bf16 v[216:219], v[108:111], v[224:227], v[216:219]\n\t" \
    "ds_read_b128 v[236:239], v253 offset:28672\n\t" \
    "v_mfma_f32_16x16x32_bf16 v[220:223], v[112:115], v[240:243], v[220:223]\n\t" \
    "v_mfma_f32_16x16x32_bf16 v[220:223], v[116:119], v[224:227], v[220:223]\n\t" \
    "v_mfma_f32_16x16x32_bf16 v[212:215], v[96:99], v[240:243], v[212:215]\n\t" \
    "v_mfma_f32_16x16x32_bf16 v[212:215], v[100:103], v[224:227], v[212:215]\n\t" \
    "s_waitcnt lgkmcnt(2)\n\t" \
    "v_mfma_f32_16x16x32_bf16 v[204:207], v[120:123], v[228:231], v[204:207]\n\t" \
    "ds_read_b128 v[240:243], v253 offset:29696\n\t" \
    "s_waitcnt lgkmcnt(2)\n\t" \
    "v_mfma_f32_16x16x32_bf16 v[204:207], v[124:127], v[232:235], v[204:207]\n\t" \
    "ds_read_b128 v[224:227], v253 offset:10240\n\t" \
    "v_mfma_f32_16x16x32_bf16 v[208:211], v[128:131], v[228:231], v[208:211]\n\t" \
    "v_mfma_f32_16x16x32_bf16 v[208:211], v[132:135], v[232:235], v[208:211]\n\t" \
    "v_mfma_f32_16x16x32_bf16 v[200:203], v[244:247], v[228:231], v[200:203]\n\t" \
    "v_mfma_f32_16x16x32_bf16 v[200:203], v[248:251], v[232:235], v[200:203]\n\t" \
    "s_waitcnt lgkmcnt(2)\n\t" \
    "v_mfma_f32_16x16x32_bf16 v[216:219], v[120:123], v[236:239], v[216:219]\n\t" \
    "ds_read_b128 v[228:231], v253 offset:11264\n\t" \
    "s_waitcnt lgkmcnt(2)\n\t" \
    "v_mfma_f32_16x16x32_bf16 v[216:219], v[124:127], v[240:243], v[216:219]\n\t" \
    "ds_read_b128 v[232:235], v253 offset:30720\n\t" \
    "v_mfma_f32_16x16x32_bf16 v[220:223], v[128:131], v[236:239], v[220:223]\n\t" \
    "v_mfma_f32_16x16x32_bf16 v[220:223], v[132:135], v[240:243], v[220:223]\n\t" \
    "v_mfma_f32_16x16x32_bf16 v[212:215], v[244:247], v[236:239], v[212:215]\n\t" \
    "ds_read_b128 v[244:247], v254 offset:63488\n\t" \
    "v_mfma_f32_16x16x32_bf16 v[212:215], v[248:251], v[240:243], v[212:215]\n\t" \
    "ds_read_b128 v[248:251], v254 offset:64512\n\t" \
    "ds_read_b128 v[236:239], v253 offset:31744\n\t" \
    "s_setprio 2\n\t" \
    "s_waitcnt lgkmcnt(0)\n\t" \
    "s_barrier \n\t" \
    LST7 \
    "v_mfma_f32_16x16x32_bf16 v[204:207], v[144:147], v[224:227], v[204:207]\n\t" \
    "v_mfma_f32_16x16x32_bf16 v[204:207], v[148:151], v[228:231], v[204:207]\n\t" \
    "ds_read_b128 v[240:243], v253 offset:12288\n\t" \
    "v_mfma_f32_16x16x32_bf16 v[208:211], v[152:155], v[224:227], v[208:211]\n\t" \
    "v_mfma_f32_16x16x32_bf16 v[208:211], v[156:159], v[228:231], v[208:211]\n\t" \
    "v_mfma_f32_16x16x32_bf16 v[200:203], v[136:139], v[224:227], v[200:203]\n\t" \
    "v_mfma_f32_16x16x32_bf16 v[200:203], v[140:143], v[228:231], v[200:203]\n\t" \
    "v_mfma_f32_16x16x32_bf16 v[216:219], v[144:147], v[232:235], v[216:219]\n\t" \
    "ds_read_b128 v[224:227], v253 offset:13312\n\t" \
    "s_and_b32 s44, s40, 3\n\t" \
    "v_mfma_f32_16x16x32_bf16 v[216:219], v[148:151], v[236:239], v[216:219]\n\t" \
    "ds_read_b128 v[228:231], v253 offset:32768\n\t" \
    "s_lshl_b32 s44, s44, 17\n\t" \
    "v_mfma_f32_16x16x32_bf16 v[220:223], v[152:155], v[232:235], v[220:223]\n\t" \
    "s_add_u32 s44, s44, %[mb]\n\t" \
    "v_mfma_f32_16x16x32_bf16 v[220:223], v[156:159], v[236:239], v[220:223]\n\t" \
    "s_add_u32 s46, s44, 0x4000\n\t" \
    "v_mfma_f32_16x16x32_bf16 v[212:215], v[136:139], v[232:235], v[212:215]\n\t" \
    "s_add_u32 s47, s44, 0x4400\n\t" \
    "v_mfma_f32_16x16x32_bf16 v[212:215], v[140:143], v[236:239], v[212:215]\n\t" \
    "s_waitcnt lgkmcnt(2)\n\t" \
    "v_mfma_f32_16x16x32_bf16 v[204:207], v[160:163], v[240:243], v[204:207]\n\t" \
    "ds_read_b128 v[232:235], v253 offset:33792\n\t" \
    "s_waitcnt lgkmcnt(2)\n\t" \
    "v_mfma_f32_16x16x32_bf16 v[204:207], v[164:167], v[224:227], v[204:207]\n\t" \
    "ds_read_b128 v[236:239], v253 offset:16384\n\t" \
    "v_mfma_f32_16x16x32_bf16 v[208:211], v[168:171], v[240:243], v[208:211]\n\t" \
    "v_mfma_f32_16x16x32_bf16 v[208:211], v[172:175], v[224:227], v[208:211]\n\t" \
    "v_mfma_f32_16x16x32_bf16 v[200:203], v[244:247], v[240:243], v[200:203]\n\t" \
    "v_mfma_f32_16x16x32_bf16 v[200:203], v[248:251], v[224:227], v[200:203]\n\t" \
    "s_waitcnt lgkmcnt(2)\n\t" \
    "v_mfma_f32_16x16x32_bf16 v[216:219], v[160:163], v[228:231], v[216:219]\n\t" \
    "ds_read_b128 v[240:243], v253 offset:17408\n\t" \
    "s_waitcnt lgkmcnt(2)\n\t" \
    "v_mfma_f32_16x16x32_bf16 v[216:219], v[164:167], v[232:235], v[216:219]\n\t" \
    "ds_read_b128 v[224:227], v253 offset:36864\n\t" \
    "v_mfma_f32_16x16x32_bf16 v[220:223], v[168:171], v[228:231], v[220:223]\n\t" \
    "v_mfma_f32_16x16x32_bf16 v[220:223], v[172:175], v[232:235], v[220:223]\n\t" \
    "v_mfma_f32_16x16x32_bf16 v[212:215], v[244:247], v[228:231], v[212:215]\n\t" \
    "ds_read_b128 v[244:247], v254 offset:55296\n\t" \
    "v_mfma_f32_16x16x32_bf16 v[212:215], v[248:251], v[232:235], v[212:215]\n\t" \
    "ds_read_b128 v[248:251], v254 offset:56320\n\t" \
    "s_waitcnt lgkmcnt(4)\n\t" \
    "v_mfma_f32_16x16x32_bf16 v[204:207], v[0:3], v[236:239], v[204:207]\n\t" \
    "ds_read_b128 v[228:231], v253 offset:37888\n\t" \
    "s_waitcnt lgkmcnt(4)\n\t" \
    "v_mfma_f32_16x16x32_bf16 v[204:207], v[4:7], v[240:243], v[204:207]\n\t" \
    "ds_read_b128 v[232:235], v253 offset:14336\n\t" \
    "v_mfma_f32_16x16x32_bf16 v[208:211], v[8:11], v[236:239], v[208:211]\n\t" \
    "v_mfma_f32_16x16x32_bf16 v[208:211], v[12:15], v[240:243], v[208:211]\n\t" \
    "s_waitcnt lgkmcnt(3)\n\t" \
    "v_mfma_f32_16x16x32_bf16 v[200:203], v[244:247], v[236:239], v[200:203]\n\t" \
    "s_waitcnt lgkmcnt(2)\n\t" \
    "v_mfma_f32_16x16x32_bf16 v[200:203], v[248:251], v[240:243], v[200:203]\n\t" \
    "v_mfma_f32_16x16x32_bf16 v[216:219], v[0:3], v[224:227], v[216:219]\n\t" \
    "ds_read_b128 v[236:239], v253 offset:15360\n\t" \
    "s_waitcnt lgkmcnt(2)\n\t" \
    "v_mfma_f32_16x16x32_bf16 v[216:219], v[4:7], v[228:231], v[216:219]\n\t" \
    "ds_read_b128 v[240:243], v253 offset:18432\n\t" \
    "v_mfma_f32_16x16x32_bf16 v[220:223], v[8:11], v[224:227], v[220:223]\n\t" \
    "v_mfma_f32_16x16x32_bf16 v[220:223], v[12:15], v[228:231], v[220:223]\n\t" \
    "v_mfma_f32_16x16x32_bf16 v[212:215], v[244:247], v[224:227], v[212:215]\n\t" \
    "v_mfma_f32_16x16x32_bf16 v[212:215], v[248:251], v[228:231], v[212:215]\n\t" \
    "s_waitcnt lgkmcnt(2)\n\t" \
    "v_mfma_f32_16x16x32_bf16 v[204:207], v[184:187], v[232:235], v[204:207]\n\t" \
    "ds_read_b128 v[224:227], v253 offset:19456\n\t" \
    "v_mov_b64 v[244:245], 0\n\t" \
    "s_waitcnt lgkmcnt(2)\n\t" \
    "v_mfma_f32_16x16x32_bf16 v[204:207], v[188:191], v[236:239], v[204:207]\n\t" \
    "ds_read_b128 v[228:231], v253 offset:34816\n\t" \
    "v_mov_b64 v[246:247], 0\n\t" \
    "v_mfma_f32_16x16x32_bf16 v[208:211], v[192:195], v[232:235], v[208:211]\n\t" \
    "v_mov_b64 v[248:249], 0\n\t" \
    "v_mfma_f32_16x16x32_bf16 v[208:211], v[196:199], v[236:239], v[208:211]\n\t" \
    "v_mov_b64 v[250:251], 0\n\t" \
    "v_mfma_f32_16x16x32_bf16 v[200:203], v[176:179], v[232:235], v[200:203]\n\t" \
    "v_mfma_f32_16x16x32_bf16 v[200:203], v[180:183], v[236:239], v[200:203]\n\t" \
    "s_waitcnt lgkmcnt(2)\n\t" \
    "v_mfma_f32_16x16x32_bf16 v[204:207], v[24:27], v[240:243], v[204:207]\n\t" \
    "ds_read_b128 v[232:235], v253 offset:35840\n\t" \
    "s_waitcnt lgkmcnt(2)\n\t" \
    "v_mfma_f32_16x16x32_bf16 v[204:207], v[28:31], v[224:227], v[204:207]\n\t" \
    "ds_read_b128 v[236:239], v253 offset:38912\n\t" \
    "v_mfma_f32_16x16x32_bf16 v[208:211], v[32:35], v[240:243], v[208:211]\n\t" \
    "v_mfma_f32_16x16x32_bf16 v[208:211], v[36:39], v[224:227], v[208:211]\n\t" \
    "v_mfma_f32_16x16x32_bf16 v[200:203], v[16:19], v[240:243], v[200:203]\n\t" \
    "v_mfma_f32_16x16x32_bf16 v[200:203], v[20:23], v[224:227], v[200:203]\n\t" \
    "ds_read_b128 v[240:243], v253 offset:39936\n\t" \
    "s_waitcnt lgkmcnt(3)\n\t" \
    "v_mfma_f32_16x16x32_bf16 v[216:219], v[184:187], v[228:231], v[216:219]\n\t" \
    "s_waitcnt lgkmcnt(2)\n\t" \
    "v_mfma_f32_16x16x32_bf16 v[216:219], v[188:191], v[232:235], v[216:219]\n\t" \
    "v_cvt_pk_bf16_f32 v204, v204, v208\n\t" \
    "v_cvt_pk_bf16_f32 v205, v205, v209\n\t" \
    "s_waitcnt lgkmcnt(1)\n\t" \
    "v_mfma_f32_16x16x32_bf16 v[216:219], v[24:27], v[236:239], v[216:219]\n\t" \
    "v_cvt_pk_bf16_f32 v206, v206, v210\n\t" \
    "v_cvt_pk_bf16_f32 v207, v207, v211\n\t" \
    "s_waitcnt lgkmcnt(0)\n\t" \
    "v_mfma_f32_16x16x32_bf16 v[216:219], v[28:31], v[240:243], v[216:219]\n\t" \
    "ds_write_b128 v254, v[204:207] offset:5120\n\t" \
    "v_mfma_f32_16x16x32_bf16 v[220:223], v[192:195], v[228:231], v[220:223]\n\t" \
    "v_mfma_f32_16x16x32_bf16 v[220:223], v[196:199], v[232:235], v[220:223]\n\t" \
    "v_cvt_pk_bf16_f32 v200, v200, v201\n\t" \
    "v_cvt_pk_bf16_f32 v201, v202, v203\n\t" \
    "v_mfma_f32_16x16x32_bf16 v[220:223], v[32:35], v[236:239], v[220:223]\n\t" \
    "ds_write_b64 v254, v[200:201] offset:1032\n\t" \
    "v_mfma_f32_16x16x32_bf16 v[220:223], v[36:39], v[240:243], v[220:223]\n\t" \
    "v_mfma_f32_16x16x32_bf16 v[212:215], v[176:179], v[228:231], v[212:215]\n\t" \
    "ds_read_b128 v[200:203], v254 offset:55296\n\t" \
    "ds_read_b128 v[204:207], v254 offset:56320\n\t" \
    "v_mfma_f32_16x16x32_bf16 v[212:215], v[180:183], v[232:235], v[212:215]\n\t" \
    "v_mov_b64 v[208:209], 0\n\t" \
    "v_mov_b64 v[210:211], 0\n\t" \
    "v_mfma_f32_16x16x32_bf16 v[212:215], v[16:19], v[236:239], v[212:215]\n\t" \
    "v_cvt_pk_bf16_f32 v216, v216, v220\n\t" \
    "v_cvt_pk_bf16_f32 v217, v217, v221\n\t" \
    "v_mfma_f32_16x16x32_bf16 v[212:215], v[20:23], v[240:243], v[212:215]\n\t" \
    "v_cvt_pk_bf16_f32 v218, v218, v222\n\t" \
    "v_cvt_pk_bf16_f32 v219, v219, v223\n\t" \
    "s_setprio 0\n\t" \
    "ds_write_b128 v254, v[216:219] offset:11264\n\t" \
    "ds_read_b128 v[216:219], v254 offset:57344\n\t" \
    "ds_read_b128 v[220:223], v254 offset:58368\n\t" \
    "s_nop 2\n\t" \
    "v_cvt_pk_bf16_f32 v212, v212, v213\n\t" \
    "v_cvt_pk_bf16_f32 v213, v214, v215\n\t" \
    "ds_write_b64 v254, v[212:213] offset:7176\n\t" \
    LST6 \
    "s_sleep 3\n\t" \
    "s_mov_b32 m0, 0xc000\n\t" \
    "s_nop 0\n\t" \
    "buffer_load_dwordx4 v253, s[60:63], s46 offen sc1 lds\n\t" \
    "s_mov_b32 m0, 0xc400\n\t" \
    "s_nop 0\n\t" \
    "buffer_load_dwordx4 v253, s[60:63], s47 offen sc1 lds\n\t" \
    "s_waitcnt lgkmcnt(0)\n\t" \
    LFLUSH212 \
    "v_bfe_i32 v212, v253, 5, 1\n\t" \
    "v_and_b32 v212, 0xaaaa, v212\n\t" \
    "v_or_b32 v212, 0x4444, v212\n\t" \
    "v_and_b32 v213, 0xffff, v255\n\t" \
    "v_lshrrev_b32 v214, 16, v255\n\t" \
    ".Ls34_%=:\n\t" \
    "s_waitcnt vmcnt(0)\n\t" \
    "ds_read_b128 v[224:227], v253 offset:49152\n\t" \
    "ds_read_b128 v[228:231], v253 offset:50176\n\t" \
    "s_waitcnt lgkmcnt(1)\n\t" \
    "v_max_u32 v215, v224, v225\n\t" \
    "v_max3_u32 v215, v226, v227, v215\n\t" \
    "s_waitcnt lgkmcnt(0)\n\t" \
    "v_max3_u32 v215, v228, v229, v215\n\t" \
    "v_max3_u32 v215, v230, v231, v215\n\t" \
    "v_cmp_eq_u32 vcc, -1, v215\n\t" \
    "s_cbranch_vccz .Ls35_%=\n\t" \
    "v_max_u32 v215, v224, v225\n\t" \
    "v_max3_u32 v215, v226, v227, v215\n\t" \
    "v_cmp_eq_u32 vcc, -1, v215\n\t" \
    "s_cbranch_vccz .Ls36_%=\n\t" \
    "s_mov_b32 m0, 0xc000\n\t" \
    "s_nop 0\n\t" \
    "buffer_load_dwordx4 v253, s[60:63], s46 offen sc1 lds\n\t" \
    ".Ls36_%=:\n\t" \
    "v_max_u32 v215, v228, v229\n\t" \
    "v_max3_u32 v215, v230, v231, v215\n\t" \
    "v_cmp_eq_u32 vcc, -1, v215\n\t" \
    "s_cbranch_vccz .Ls37_%=\n\t" \
    "s_mov_b32 m0, 0xc400\n\t" \
    "s_nop 0\n\t" \
    "buffer_load_dwordx4 v253, s[60:63], s47 offen sc1 lds\n\t" \
    ".Ls37_%=:\n\t" \
    "s_cmp_eq_u32 s41, 0\n\t" \
    "s_cbranch_scc1 .Ls35_%=\n\t" \
    "s_sub_u32 s41, s41, 1\n\t" \
    "s_branch .Ls34_%=\n\t" \
    ".Ls35_%=:\n\t" \
    LST2 \
    "s_setprio 3\n\t" \
    "s_waitcnt lgkmcnt(0)\n\t" \
    "s_barrier \n\t" \
    LST3 \
    "ds_read_b128 v[232:235], v253 offset:40960\n\t" \
    "ds_read_b128 v[236:239], v253 offset:41984\n\t" \
    "ds_read_b128 v[240:243], v253 offset:43008\n\t" \
    "v_smfmac_f32_16x16x64_bf16 v[244:247], v[224:227], v[0:7], v212\n\t" \
    "v_smfmac_f32_16x16x64_bf16 v[248:251], v[224:227], v[8:15], v212\n\t" \
    "v_smfmac_f32_16x16x64_bf16 v[208:211], v[224:227], v[200:207], v212\n\t" \
    "ds_read_b128 v[200:203], v254 offset:59392\n\t" \
    "ds_read_b128 v[204:207], v254 offset:60416\n\t" \
    "ds_read_b128 v[224:227], v253 offset:44032\n\t" \
    "v_smfmac_f32_16x16x64_bf16 v[208:211], v[228:231], v[16:23], v212\n\t" \
    "v_smfmac_f32_16x16x64_bf16 v[244:247], v[228:231], v[24:31], v212\n\t" \
    "v_smfmac_f32_16x16x64_bf16 v[248:251], v[228:231], v[32:39], v212\n\t" \
    "ds_read_b128 v[228:231], v253 offset:45056\n\t" \
    "s_waitcnt lgkmcnt(6)\n\t" \
    "v_smfmac_f32_16x16x64_bf16 v[208:211], v[232:235], v[216:223], v212\n\t" \
    "ds_read_b128 v[216:219], v254 offset:61440\n\t" \
    "ds_read_b128 v[220:223], v254 offset:62464\n\t" \
    "v_smfmac_f32_16x16x64_bf16 v[244:247], v[232:235], v[40:47], v212\n\t" \
    "v_smfmac_f32_16x16x64_bf16 v[248:251], v[232:235], v[48:55], v212\n\t" \
    "ds_read_b128 v[232:235], v253 offset:46080\n\t" \
    "s_waitcnt lgkmcnt(8)\n\t" \
    "v_smfmac_f32_16x16x64_bf16 v[208:211], v[236:239], v[56:63], v212\n\t" \
    "v_smfmac_f32_16x16x64_bf16 v[244:247], v[236:239], v[64:71], v212\n\t" \
    "v_smfmac_f32_16x16x64_bf16 v[248:251], v[236:239], v[72:79], v212\n\t" \
    "ds_read_b128 v[236:239], v253 offset:47104\n\t" \
    "s_waitcnt lgkmcnt(6)\n\t" \
    "v_smfmac_f32_16x16x64_bf16 v[208:211], v[240:243], v[200:207], v212\n\t" \
    "ds_read_b128 v[200:203], v254 offset:63488\n\t" \
    "ds_read_b128 v[204:207], v254 offset:64512\n\t" \
    "v_smfmac_f32_16x16x64_bf16 v[244:247], v[240:243], v[80:87], v212\n\t" \
    "v_smfmac_f32_16x16x64_bf16 v[248:251], v[240:243], v[88:95], v212\n\t" \
    "ds_read_b128 v[240:243], v253 offset:48128\n\t" \
    "s_waitcnt lgkmcnt(8)\n\t" \
    "v_smfmac_f32_16x16x64_bf16 v[208:211], v[224:227], v[96:103], v212\n\t" \
    "v_smfmac_f32_16x16x64_bf16 v[244:247], v[224:227], v[104:111], v212\n\t" \
    "v_smfmac_f32_16x16x64_bf16 v[248:251], v[224:227], v[112:119], v212\n\t" \
    "s_waitcnt lgkmcnt(5)\n\t" \
    "v_smfmac_f32_16x16x64_bf16 v[208:211], v[228:231], v[216:223], v212\n\t" \
    "s_waitcnt lgkmcnt(4)\n\t" \
    "v_smfmac_f32_16x16x64_bf16 v[208:211], v[232:235], v[136:143], v212\n\t" \
    "s_waitcnt lgkmcnt(1)\n\t" \
    "v_smfmac_f32_16x16x64_bf16 v[208:211], v[236:239], v[200:207], v212\n\t" \
    "s_waitcnt lgkmcnt(0)\n\t" \
    "v_smfmac_f32_16x16x64_bf16 v[208:211], v[240:243], v[176:183], v212\n\t" \
    "v_smfmac_f32_16x16x64_bf16 v[244:247], v[228:231], v[120:127], v212\n\t" \
    "v_smfmac_f32_16x16x64_bf16 v[244:247], v[232:235], v[144:151], v212\n\t" \
    "v_smfmac_f32_16x16x64_bf16 v[244:247], v[236:239], v[160:167], v212\n\t" \
    "v_add_f32 v208, v208, v210\n\t" \
    "v_add_f32 v209, v209, v211\n\t" \
    "v_cvt_pk_bf16_f32 v208, v208, v209\n\t" \
    "v_smfmac_f32_16x16x64_bf16 v[244:247], v[240:243], v[184:191], v212\n\t" \
    "ds_write_b16 v213, v208 offset:0\n\t" \
    "ds_write_b16_d16_hi v213, v208 offset:16\n\t" \
    "v_smfmac_f32_16x16x64_bf16 v[248:251], v[228:231], v[128:135], v212\n\t" \
    "v_smfmac_f32_16x16x64_bf16 v[248:251], v[232:235], v[152:159], v212\n\t" \
    "v_smfmac_f32_16x16x64_bf16 v[248:251], v[236:239], v[168:175], v212\n\t" \
    "v_add_f32 v244, v244, v246\n\t" \
    "v_add_f32 v245, v245, v247\n\t" \
    "v_smfmac_f32_16x16x64_bf16 v[248:251], v[240:243], v[192:199], v212\n\t" \
    "s_nop 7\n\t" \
    "v_add_f32 v248, v248, v250\n\t" \
    "v_add_f32 v249, v249, v251\n\t" \
    "v_cvt_pk_bf16_f32 v244, v244, v248\n\t" \
    "v_cvt_pk_bf16_f32 v245, v245, v249\n\t" \
    "ds_write2_b32 v214, v244, v245 offset1:4\n\t" \
    "s_setprio 0\n\t" \
    "s_sleep 1\n\t" \
    "ds_read_b128 v[208:211], v254 offset:24576\n\t" \
    "ds_read_b128 v[224:227], v253 offset:0\n\t" \
    "ds_read_b128 v[228:231], v253 offset:20480\n\t" \
    "ds_read_b128 v[212:215], v254 offset:25600\n\t" \
    "ds_read_b128 v[232:235], v253 offset:1024\n\t" \
    "ds_read_b128 v[236:239], v253 offset:21504\n\t" \
    "ds_read_b128 v[216:219], v254 offset:26624\n\t" \
    "ds_read_b128 v[240:243], v253 offset:2048\n\t" \
    "ds_read_b128 v[244:247], v253 offset:22528\n\t" \
    "ds_read_b128 v[220:223], v254 offset:27648\n\t" \
    "ds_read_b128 v[248:251], v253 offset:3072\n\t" \
    "s_waitcnt lgkmcnt(9)\n\t" \
    "v_mfma_f32_16x16x32_bf16 v[200:203], v[208:211], v[224:227], 0\n\t" \
    "ds_read_b128 v[224:227], v253 offset:23552\n\t" \
    "s_waitcnt lgkmcnt(9)\n\t" \
    "v_mfma_f32_16x16x32_bf16 v[204:207], v[208:211], v[228:231], 0\n\t" \
    "ds_read_b128 v[228:231], v253 offset:4096\n\t" \
    "ds_read_b128 v[208:211], v254 offset:28672\n\t" \
    "s_waitcnt lgkmcnt(9)\n\t" \
    "v_mfma_f32_16x16x32_bf16 v[200:203], v[212:215], v[232:235], v[200:203]\n\t" \
    "ds_read_b128 v[232:235], v253 offset:24576\n\t" \
    "s_waitcnt lgkmcnt(9)\n\t" \
    "v_mfma_f32_16x16x32_bf16 v[204:207], v[212:215], v[236:239], v[204:207]\n\t" \
    "ds_read_b128 v[236:239], v253 offset:5120\n\t" \
    "ds_read_b128 v[212:215], v254 offset:29696\n\t" \
    "s_waitcnt lgkmcnt(9)\n\t" \
    "v_mfma_f32_16x16x32_bf16 v[200:203], v[216:219], v[240:243], v[200:203]\n\t" \
    "ds_read_b128 v[240:243], v253 offset:25600\n\t" \
    "s_waitcnt lgkmcnt(0)\n\t" \
    "s_barrier \n\t" \
    LST4 \
    "s_add_u32 s40, s40, 1\n\t" \
    "s_cmp_lg_u32 s40, 64\n\t" \
    "s_cbranch_scc1 .Ls33_%=\n\t" \
    "s_branch .Lend%=\n\t" \
    ".Ls2_%=:\n\t" \
    "s_add_u32 m0, %[wm], 0x18000\n\t" \
    "s_mov_b32 s44, 0x0\n\t" \
    "buffer_load_dwordx4 v240, %[wr], s44 offen offset:0 nt lds\n\t" \
    "buffer_load_dwordx4 v241, %[wr], s44 offen offset:1024 nt lds\n\t" \
    "buffer_load_dwordx4 v242, %[wr], s44 offen offset:2048 nt lds\n\t" \
    "s_add_u32 m0, %[wm], 0x1e000\n\t" \
    "s_mov_b32 s44, 0x60000\n\t" \
    "buffer_load_dwordx4 v240, %[wr], s44 offen offset:0 nt lds\n\t" \
    "buffer_load_dwordx4 v241, %[wr], s44 offen offset:1024 nt lds\n\t" \
    "buffer_load_dwordx4 v242, %[wr], s44 offen offset:2048 nt lds\n\t" \
    "s_add_u32 m0, %[wm], 0x0\n\t" \
    "s_mov_b32 s44, 0xc0000\n\t" \
    "buffer_load_dwordx4 v240, %[wr], s44 offen offset:0 nt lds\n\t" \
    "buffer_load_dwordx4 v241, %[wr], s44 offen offset:1024 nt lds\n\t" \
    "buffer_load_dwordx4 v242, %[wr], s44 offen offset:2048 nt lds\n\t" \
    "s_add_u32 m0, %[wm], 0x6000\n\t" \
    "s_mov_b32 s44, 0x120000\n\t" \
    "buffer_load_dwordx4 v240, %[wr], s44 offen offset:0 nt lds\n\t" \
    "buffer_load_dwordx4 v241, %[wr], s44 offen offset:1024 nt lds\n\t" \
    "buffer_load_dwordx4 v242, %[wr], s44 offen offset:2048 nt lds\n\t" \
    "s_add_u32 m0, %[wm], 0xc000\n\t" \
    "s_mov_b32 s44, 0x180000\n\t" \
    "buffer_load_dwordx4 v240, %[wr], s44 offen offset:0 nt lds\n\t" \
    "buffer_load_dwordx4 v241, %[wr], s44 offen offset:1024 nt lds\n\t" \
    "buffer_load_dwordx4 v242, %[wr], s44 offen offset:2048 nt lds\n\t" \
    "v_add_u32 v244, 0x18000, v243\n\t" \
    "s_waitcnt vmcnt(12) lgkmcnt(0)\n\t" \
    "s_barrier \n\t" \
    "s_add_u32 m0, %[wm], 0x12000\n\t" \
    "s_mov_b32 s44, 0x1e0000\n\t" \
    "buffer_load_dwordx4 v240, %[wr], s44 offen offset:0 nt lds\n\t" \
    "buffer_load_dwordx4 v241, %[wr], s44 offen offset:1024 nt lds\n\t" \
    "buffer_load_dwordx4 v242, %[wr], s44 offen offset:2048 nt lds\n\t" \
    "ds_read_b64_tr_b16 v[208:209], v244 offset:0\n\t" \
    "ds_read_b64_tr_b16 v[210:211], v244 offset:1536\n\t" \
    "ds_read_b64_tr_b16 v[40:41], v244 offset:32\n\t" \
    "ds_read_b64_tr_b16 v[42:43], v244 offset:1568\n\t" \
    "ds_read_b64_tr_b16 v[48:49], v244 offset:64\n\t" \
    "ds_read_b64_tr_b16 v[50:51], v244 offset:1600\n\t" \
    "v_add_u32 v244, 0x1e000, v243\n\t" \
    "s_waitcnt vmcnt(12) lgkmcnt(0)\n\t" \
    "s_barrier \n\t" \
    "s_add_u32 m0, %[wm], 0x18000\n\t" \
    "s_mov_b32 s44, 0x240000\n\t" \
    "buffer_load_dwordx4 v240, %[wr], s44 offen offset:0 nt lds\n\t" \
    "buffer_load_dwordx4 v241, %[wr], s44 offen offset:1024 nt lds\n\t" \
    "buffer_load_dwordx4 v242, %[wr], s44 offen offset:2048 nt lds\n\t" \
    "ds_read_b64_tr_b16 v[212:213], v244 offset:0\n\t" \
    "ds_read_b64_tr_b16 v[214:215], v244 offset:1536\n\t" \
    "ds_read_b64_tr_b16 v[44:45], v244 offset:32\n\t" \
    "ds_read_b64_tr_b16 v[46:47], v244 offset:1568\n\t" \
    "ds_read_b64_tr_b16 v[52:53], v244 offset:64\n\t" \
    "ds_read_b64_tr_b16 v[54:55], v244 offset:1600\n\t" \
    "v_add_u32 v244, 0x0, v243\n\t" \
    "s_waitcnt vmcnt(12) lgkmcnt(0)\n\t" \
    "s_barrier \n\t" \
    "s_add_u32 m0, %[wm], 0x1e000\n\t" \
    "s_mov_b32 s44, 0x2a0000\n\t" \
    "buffer_load_dwordx4 v240, %[wr], s44 offen offset:0 nt lds\n\t" \
    "buffer_load_dwordx4 v241, %[wr], s44 offen offset:1024 nt lds\n\t" \
    "buffer_load_dwordx4 v242, %[wr], s44 offen offset:2048 nt lds\n\t" \
    "ds_read_b64_tr_b16 v[56:57], v244 offset:0\n\t" \
    "ds_read_b64_tr_b16 v[58:59], v244 offset:1536\n\t" \
    "ds_read_b64_tr_b16 v[64:65], v244 offset:32\n\t" \
    "ds_read_b64_tr_b16 v[66:67], v244 offset:1568\n\t" \
    "ds_read_b64_tr_b16 v[72:73], v244 offset:64\n\t" \
    "ds_read_b64_tr_b16 v[74:75], v244 offset:1600\n\t" \
    "v_add_u32 v244, 0x6000, v243\n\t" \
    "s_waitcnt vmcnt(12) lgkmcnt(0)\n\t" \
    "s_barrier \n\t" \
    "s_add_u32 m0, %[wm], 0x0\n\t" \
    "s_mov_b32 s44, 0x300000\n\t" \
    "buffer_load_dwordx4 v240, %[wr], s44 offen offset:0 nt lds\n\t" \
    "buffer_load_dwordx4 v241, %[wr], s44 offen offset:1024 nt lds\n\t" \
    "buffer_load_dwordx4 v242, %[wr], s44 offen offset:2048 nt lds\n\t" \
    "ds_read_b64_tr_b16 v[60:61], v244 offset:0\n\t" \
    "ds_read_b64_tr_b16 v[62:63], v244 offset:1536\n\t" \
    "ds_read_b64_tr_b16 v[68:69], v244 offset:32\n\t" \
    "ds_read_b64_tr_b16 v[70:71], v244 offset:1568\n\t" \
    "ds_read_b64_tr_b16 v[76:77], v244 offset:64\n\t" \
    "ds_read_b64_tr_b16 v[78:79], v244 offset:1600\n\t" \
    "v_add_u32 v244, 0xc000, v243\n\t" \
    "s_waitcnt vmcnt(12) lgkmcnt(0)\n\t" \
    "s_barrier \n\t" \
    "s_add_u32 m0, %[wm], 0x6000\n\t" \
    "s_mov_b32 s44, 0x360000\n\t" \
    "buffer_load_dwordx4 v240, %[wr], s44 offen offset:0 nt lds\n\t" \
    "buffer_load_dwordx4 v241, %[wr], s44 offen offset:1024 nt lds\n\t" \
    "buffer_load_dwordx4 v242, %[wr], s44 offen offset:2048 nt lds\n\t" \
    "ds_read_b64_tr_b16 v[216:217], v244 offset:0\n\t" \
    "ds_read_b64_tr_b16 v[218:219], v244 offset:1536\n\t" \
    "ds_read_b64_tr_b16 v[80:81], v244 offset:32\n\t" \
    "ds_read_b64_tr_b16 v[82:83], v244 offset:1568\n\t" \
    "ds_read_b64_tr_b16 v[88:89], v244 offset:64\n\t" \
    "ds_read_b64_tr_b16 v[90:91], v244 offset:1600\n\t" \
    "v_add_u32 v244, 0x12000, v243\n\t" \
    "s_waitcnt vmcnt(12) lgkmcnt(0)\n\t" \
    "s_barrier \n\t" \
    "s_add_u32 m0, %[wm], 0xc000\n\t" \
    "s_mov_b32 s44, 0x3c0000\n\t" \
    "buffer_load_dwordx4 v240, %[wr], s44 offen offset:0 nt lds\n\t" \
    "buffer_load_dwordx4 v241, %[wr], s44 offen offset:1024 nt lds\n\t" \
    "buffer_load_dwordx4 v242, %[wr], s44 offen offset:2048 nt lds\n\t" \
    "ds_read_b64_tr_b16 v[220:221], v244 offset:0\n\t" \
    "ds_read_b64_tr_b16 v[222:223], v244 offset:1536\n\t" \
    "ds_read_b64_tr_b16 v[84:85], v244 offset:32\n\t" \
    "ds_read_b64_tr_b16 v[86:87], v244 offset:1568\n\t" \
    "ds_read_b64_tr_b16 v[92:93], v244 offset:64\n\t" \
    "ds_read_b64_tr_b16 v[94:95], v244 offset:1600\n\t" \
    "v_add_u32 v244, 0x18000, v243\n\t" \
    "s_waitcnt vmcnt(12) lgkmcnt(0)\n\t" \
    "s_barrier \n\t" \
    "v_mov_b32 v246, 0\n\t" \
    "buffer_load_dword v246, v246, s[60:63], %[mo] offen sc1\n\t" \
    "s_add_u32 m0, %[wm], 0x12000\n\t" \
    "s_mov_b32 s44, 0x420000\n\t" \
    "buffer_load_dwordx4 v240, %[wr], s44 offen offset:0 nt lds\n\t" \
    "buffer_load_dwordx4 v241, %[wr], s44 offen offset:1024 nt lds\n\t" \
    "buffer_load_dwordx4 v242, %[wr], s44 offen offset:2048 nt lds\n\t" \
    "ds_read_b64_tr_b16 v[96:97], v244 offset:0\n\t" \
    "ds_read_b64_tr_b16 v[98:99], v244 offset:1536\n\t" \
    "ds_read_b64_tr_b16 v[104:105], v244 offset:32\n\t" \
    "ds_read_b64_tr_b16 v[106:107], v244 offset:1568\n\t" \
    "ds_read_b64_tr_b16 v[112:113], v244 offset:64\n\t" \
    "ds_read_b64_tr_b16 v[114:115], v244 offset:1600\n\t" \
    "v_add_u32 v244, 0x1e000, v243\n\t" \
    "s_waitcnt vmcnt(12) lgkmcnt(0)\n\t" \
    "s_barrier \n\t" \
    "s_add_u32 m0, %[wm], 0x18000\n\t" \
    "s_mov_b32 s44, 0x480000\n\t" \
    "buffer_load_dwordx4 v240, %[wr], s44 offen offset:0 nt lds\n\t" \
    "buffer_load_dwordx4 v241, %[wr], s44 offen offset:1024 nt lds\n\t" \
    "buffer_load_dwordx4 v242, %[wr], s44 offen offset:2048 nt lds\n\t" \
    "ds_read_b64_tr_b16 v[100:101], v244 offset:0\n\t" \
    "ds_read_b64_tr_b16 v[102:103], v244 offset:1536\n\t" \
    "ds_read_b64_tr_b16 v[108:109], v244 offset:32\n\t" \
    "ds_read_b64_tr_b16 v[110:111], v244 offset:1568\n\t" \
    "ds_read_b64_tr_b16 v[116:117], v244 offset:64\n\t" \
    "ds_read_b64_tr_b16 v[118:119], v244 offset:1600\n\t" \
    "v_add_u32 v244, 0x0, v243\n\t" \
    "s_waitcnt vmcnt(12) lgkmcnt(0)\n\t" \
    "s_barrier \n\t" \
    "s_add_u32 m0, %[wm], 0x1e000\n\t" \
    "s_mov_b32 s44, 0x4e0000\n\t" \
    "buffer_load_dwordx4 v240, %[wr], s44 offen offset:0 nt lds\n\t" \
    "buffer_load_dwordx4 v241, %[wr], s44 offen offset:1024 nt lds\n\t" \
    "buffer_load_dwordx4 v242, %[wr], s44 offen offset:2048 nt lds\n\t" \
    "ds_read_b64_tr_b16 v[200:201], v244 offset:0\n\t" \
    "ds_read_b64_tr_b16 v[202:203], v244 offset:1536\n\t" \
    "ds_read_b64_tr_b16 v[0:1], v244 offset:32\n\t" \
    "ds_read_b64_tr_b16 v[2:3], v244 offset:1568\n\t" \
    "ds_read_b64_tr_b16 v[8:9], v244 offset:64\n\t" \
    "ds_read_b64_tr_b16 v[10:11], v244 offset:1600\n\t" \
    "v_add_u32 v244, 0x6000, v243\n\t" \
    "s_waitcnt vmcnt(12) lgkmcnt(0)\n\t" \
    "s_barrier \n\t" \
    "s_add_u32 m0, %[wm], 0x0\n\t" \
    "s_mov_b32 s44, 0x540000\n\t" \
    "buffer_load_dwordx4 v240, %[wr], s44 offen offset:0 nt lds\n\t" \
    "buffer_load_dwordx4 v241, %[wr], s44 offen offset:1024 nt lds\n\t" \
    "buffer_load_dwordx4 v242, %[wr], s44 offen offset:2048 nt lds\n\t" \
    "ds_read_b64_tr_b16 v[204:205], v244 offset:0\n\t" \
    "ds_read_b64_tr_b16 v[206:207], v244 offset:1536\n\t" \
    "ds_read_b64_tr_b16 v[4:5], v244 offset:32\n\t" \
    "ds_read_b64_tr_b16 v[6:7], v244 offset:1568\n\t" \
    "ds_read_b64_tr_b16 v[12:13], v244 offset:64\n\t" \
    "ds_read_b64_tr_b16 v[14:15], v244 offset:1600\n\t" \
    "v_add_u32 v244, 0xc000, v243\n\t" \
    "s_waitcnt vmcnt(12) lgkmcnt(0)\n\t" \
    "s_barrier \n\t" \
    "s_add_u32 m0, %[wm], 0x6000\n\t" \
    "s_mov_b32 s44, 0x5a0000\n\t" \
    "buffer_load_dwordx4 v240, %[wr], s44 offen offset:0 nt lds\n\t" \
    "buffer_load_dwordx4 v241, %[wr], s44 offen offset:1024 nt lds\n\t" \
    "buffer_load_dwordx4 v242, %[wr], s44 offen offset:2048 nt lds\n\t" \
    "ds_read_b64_tr_b16 v[224:225], v244 offset:0\n\t" \
    "ds_read_b64_tr_b16 v[226:227], v244 offset:1536\n\t" \
    "ds_read_b64_tr_b16 v[120:121], v244 offset:32\n\t" \
    "ds_read_b64_tr_b16 v[122:123], v244 offset:1568\n\t" \
    "ds_read_b64_tr_b16 v[128:129], v244 offset:64\n\t" \
    "ds_read_b64_tr_b16 v[130:131], v244 offset:1600\n\t" \
    "v_add_u32 v244, 0x12000, v243\n\t" \
    "s_waitcnt vmcnt(12) lgkmcnt(0)\n\t" \
    "s_barrier \n\t" \
    ".Ls21_%=:\n\t" \
    "v_readfirstlane_b32 s43, v246\n\t" \
    "s_nop 0\n\t" \
    "s_cmp_lg_u32 s43, -1\n\t" \
    "s_cbranch_scc1 .Ls22_%=\n\t" \
    "s_cmp_eq_u32 s41, 0\n\t" \
    "s_cbranch_scc1 .Ls22_%=\n\t" \
    "s_sub_u32 s41, s41, 1\n\t" \
    "s_sleep 4\n\t" \
    "v_mov_b32 v246, 0\n\t" \
    "buffer_load_dword v246, v246, s[60:63], %[mo] offen sc1\n\t" \
    "s_waitcnt vmcnt(0)\n\t" \
    "s_branch .Ls21_%=\n\t" \
    ".Ls22_%=:\n\t" \
    "v_mov_b32 v246, 0x8000\n\t" \
    "s_mov_b64 exec, 1\n\t" \
    "buffer_store_dword v246, v246, s[60:63], %[mo] offen sc1\n\t" \
    "s_mov_b64 exec, -1\n\t" \
    "s_and_b32 s43, s43, 0x2000\n\t" \
    "s_xor_b32 s43, s43, %[lx]\n\t" \
    "s_add_u32 s60, s60, s43\n\t" \
    "s_addc_u32 s61, s61, 0\n\t" \
    "s_add_u32 m0, %[wm], 0xc000\n\t" \
    "s_mov_b32 s44, 0x600000\n\t" \
    "buffer_load_dwordx4 v240, %[wr], s44 offen offset:0 nt lds\n\t" \
    "buffer_load_dwordx4 v241, %[wr], s44 offen offset:1024 nt lds\n\t" \
    "buffer_load_dwordx4 v242, %[wr], s44 offen offset:2048 nt lds\n\t" \
    "ds_read_b64_tr_b16 v[228:229], v244 offset:0\n\t" \
    "ds_read_b64_tr_b16 v[230:231], v244 offset:1536\n\t" \
    "ds_read_b64_tr_b16 v[124:125], v244 offset:32\n\t" \
    "ds_read_b64_tr_b16 v[126:127], v244 offset:1568\n\t" \
    "ds_read_b64_tr_b16 v[132:133], v244 offset:64\n\t" \
    "ds_read_b64_tr_b16 v[134:135], v244 offset:1600\n\t" \
    "v_add_u32 v244, 0x18000, v243\n\t" \
    "s_waitcnt vmcnt(12) lgkmcnt(0)\n\t" \
    "s_barrier \n\t" \
    "s_add_u32 m0, %[wm], 0x12000\n\t" \
    "s_mov_b32 s44, 0x660000\n\t" \
    "buffer_load_dwordx4 v240, %[wr], s44 offen offset:0 nt lds\n\t" \
    "buffer_load_dwordx4 v241, %[wr], s44 offen offset:1024 nt lds\n\t" \
    "buffer_load_dwordx4 v242, %[wr], s44 offen offset:2048 nt lds\n\t" \
    "ds_read_b64_tr_b16 v[136:137], v244 offset:0\n\t" \
    "ds_read_b64_tr_b16 v[138:139], v244 offset:1536\n\t" \
    "ds_read_b64_tr_b16 v[144:145], v244 offset:32\n\t" \
    "ds_read_b64_tr_b16 v[146:147], v244 offset:1568\n\t" \
    "ds_read_b64_tr_b16 v[152:153], v244 offset:64\n\t" \
    "ds_read_b64_tr_b16 v[154:155], v244 offset:1600\n\t" \
    "v_add_u32 v244, 0x1e000, v243\n\t" \
    "s_waitcnt vmcnt(12) lgkmcnt(0)\n\t" \
    "s_barrier \n\t" \
    "s_add_u32 m0, %[wm], 0x18000\n\t" \
    "s_mov_b32 s44, 0x6c0000\n\t" \
    "buffer_load_dwordx4 v240, %[wr], s44 offen offset:0 nt lds\n\t" \
    "buffer_load_dwordx4 v241, %[wr], s44 offen offset:1024 nt lds\n\t" \
    "buffer_load_dwordx4 v242, %[wr], s44 offen offset:2048 nt lds\n\t" \
    "ds_read_b64_tr_b16 v[140:141], v244 offset:0\n\t" \
    "ds_read_b64_tr_b16 v[142:143], v244 offset:1536\n\t" \
    "ds_read_b64_tr_b16 v[148:149], v244 offset:32\n\t" \
    "ds_read_b64_tr_b16 v[150:151], v244 offset:1568\n\t" \
    "ds_read_b64_tr_b16 v[156:157], v244 offset:64\n\t" \
    "ds_read_b64_tr_b16 v[158:159], v244 offset:1600\n\t" \
    "v_add_u32 v244, 0x0, v243\n\t" \
    "s_waitcnt vmcnt(12) lgkmcnt(0)\n\t" \
    "s_barrier \n\t" \
    "s_add_u32 m0, %[wm], 0x1e000\n\t" \
    "s_mov_b32 s44, 0x720000\n\t" \
    "buffer_load_dwordx4 v240, %[wr], s44 offen offset:0 nt lds\n\t" \
    "buffer_load_dwordx4 v241, %[wr], s44 offen offset:1024 nt lds\n\t" \
    "buffer_load_dwordx4 v242, %[wr], s44 offen offset:2048 nt lds\n\t" \
    "ds_read_b64_tr_b16 v[16:17], v244 offset:0\n\t" \
    "ds_read_b64_tr_b16 v[18:19], v244 offset:1536\n\t" \
    "ds_read_b64_tr_b16 v[24:25], v244 offset:32\n\t" \
    "ds_read_b64_tr_b16 v[26:27], v244 offset:1568\n\t" \
    "ds_read_b64_tr_b16 v[32:33], v244 offset:64\n\t" \
    "ds_read_b64_tr_b16 v[34:35], v244 offset:1600\n\t" \
    "v_add_u32 v244, 0x6000, v243\n\t" \
    "s_waitcnt vmcnt(12) lgkmcnt(0)\n\t" \
    "s_barrier \n\t" \
    "ds_read_b64_tr_b16 v[20:21], v244 offset:0\n\t" \
    "ds_read_b64_tr_b16 v[22:23], v244 offset:1536\n\t" \
    "ds_read_b64_tr_b16 v[28:29], v244 offset:32\n\t" \
    "ds_read_b64_tr_b16 v[30:31], v244 offset:1568\n\t" \
    "ds_read_b64_tr_b16 v[36:37], v244 offset:64\n\t" \
    "ds_read_b64_tr_b16 v[38:39], v244 offset:1600\n\t" \
    "v_add_u32 v244, 0xc000, v243\n\t" \
    "s_waitcnt vmcnt(9) lgkmcnt(0)\n\t" \
    "s_barrier \n\t" \
    "ds_read_b64_tr_b16 v[232:233], v244 offset:0\n\t" \
    "ds_read_b64_tr_b16 v[234:235], v244 offset:1536\n\t" \
    "ds_read_b64_tr_b16 v[160:161], v244 offset:32\n\t" \
    "ds_read_b64_tr_b16 v[162:163], v244 offset:1568\n\t" \
    "ds_read_b64_tr_b16 v[168:169], v244 offset:64\n\t" \
    "ds_read_b64_tr_b16 v[170:171], v244 offset:1600\n\t" \
    "v_add_u32 v244, 0x12000, v243\n\t" \
    "s_waitcnt vmcnt(6) lgkmcnt(0)\n\t" \
    "s_barrier \n\t" \
    "ds_read_b64_tr_b16 v[236:237], v244 offset:0\n\t" \
    "ds_read_b64_tr_b16 v[238:239], v244 offset:1536\n\t" \
    "ds_read_b64_tr_b16 v[164:165], v244 offset:32\n\t" \
    "ds_read_b64_tr_b16 v[166:167], v244 offset:1568\n\t" \
    "ds_read_b64_tr_b16 v[172:173], v244 offset:64\n\t" \
    "ds_read_b64_tr_b16 v[174:175], v244 offset:1600\n\t" \
    "v_add_u32 v244, 0x18000, v243\n\t" \
    "s_waitcnt vmcnt(3) lgkmcnt(0)\n\t" \
    "s_barrier \n\t" \
    "ds_read_b64_tr_b16 v[176:177], v244 offset:0\n\t" \
    "ds_read_b64_tr_b16 v[178:179], v244 offset:1536\n\t" \
    "ds_read_b64_tr_b16 v[184:185], v244 offset:32\n\t" \
    "ds_read_b64_tr_b16 v[186:187], v244 offset:1568\n\t" \
    "ds_read_b64_tr_b16 v[192:193], v244 offset:64\n\t" \
    "ds_read_b64_tr_b16 v[194:195], v244 offset:1600\n\t" \
    "v_add_u32 v244, 0x1e000, v243\n\t" \
    "s_waitcnt vmcnt(0) lgkmcnt(0)\n\t" \
    "s_barrier \n\t" \
    "ds_read_b64_tr_b16 v[180:181], v244 offset:0\n\t" \
    "ds_read_b64_tr_b16 v[182:183], v244 offset:1536\n\t" \
    "ds_read_b64_tr_b16 v[188:189], v244 offset:32\n\t" \
    "ds_read_b64_tr_b16 v[190:191], v244 offset:1568\n\t" \
    "ds_read_b64_tr_b16 v[196:197], v244 offset:64\n\t" \
    "ds_read_b64_tr_b16 v[198:199], v244 offset:1600\n\t" \
    "s_waitcnt lgkmcnt(0)\n\t" \
    "s_barrier \n\t" \
    LST0 \
    "ds_write_b128 v254, v[200:203] offset:51200\n\t" \
    "ds_write_b128 v254, v[204:207] offset:52224\n\t" \
    "ds_write_b128 v254, v[208:211] offset:53248\n\t" \
    "ds_write_b128 v254, v[212:215] offset:54272\n\t" \
    "ds_write_b128 v254, v[216:219] offset:55296\n\t" \
    "ds_write_b128 v254, v[220:223] offset:56320\n\t" \
    "ds_write_b128 v254, v[224:227] offset:57344\n\t" \
    "ds_write_b128 v254, v[228:231] offset:58368\n\t" \
    "ds_write_b128 v254, v[232:235] offset:59392\n\t" \
    "ds_write_b128 v254, v[236:239] offset:60416\n\t" \
    "s_waitcnt vmcnt(0) lgkmcnt(0)\n\t" \
    "s_barrier \n\t" \
    "ds_read_b128 v[224:227], v253 offset:0\n\t" \
    "ds_read_b128 v[244:247], v254 offset:53248\n\t" \
    "ds_read_b128 v[228:231], v253 offset:1024\n\t" \
    "ds_read_b128 v[232:235], v253 offset:20480\n\t" \
    "ds_read_b128 v[236:239], v253 offset:21504\n\t" \
    "ds_read_b128 v[248:251], v254 offset:54272\n\t" \
    ".Ls23_%=:\n\t" \
    "s_waitcnt lgkmcnt(5)\n\t" \
    "v_mfma_f32_16x16x32_bf16 v[204:207], v[40:43], v[224:227], 0\n\t" \
    "s_waitcnt lgkmcnt(3)\n\t" \
    "v_mfma_f32_16x16x32_bf16 v[204:207], v[44:47], v[228:231], v[204:207]\n\t" \
    "ds_read_b128 v[240:243], v253 offset:2048\n\t" \
    "v_mfma_f32_16x16x32_bf16 v[208:211], v[48:51], v[224:227], 0\n\t" \
    "v_mfma_f32_16x16x32_bf16 v[208:211], v[52:55], v[228:231], v[208:211]\n\t" \
    "v_mfma_f32_16x16x32_bf16 v[200:203], v[244:247], v[224:227], 0\n\t" \
    "s_waitcnt lgkmcnt(1)\n\t" \
    "v_mfma_f32_16x16x32_bf16 v[200:203], v[248:251], v[228:231], v[200:203]\n\t" \
    "v_mfma_f32_16x16x32_bf16 v[216:219], v[40:43], v[232:235], 0\n\t" \
    "ds_read_b128 v[224:227], v253 offset:3072\n\t" \
    "v_mfma_f32_16x16x32_bf16 v[216:219], v[44:47], v[236:239], v[216:219]\n\t" \
    "ds_read_b128 v[228:231], v253 offset:22528\n\t" \
    "v_mfma_f32_16x16x32_bf16 v[220:223], v[48:51], v[232:235], 0\n\t" \
    "v_mfma_f32_16x16x32_bf16 v[220:223], v[52:55], v[236:239], v[220:223]\n\t" \
    "v_mfma_f32_16x16x32_bf16 v[212:215], v[244:247], v[232:235], 0\n\t" \
    "ds_read_b128 v[244:247], v254 offset:55296\n\t" \
    "v_mfma_f32_16x16x32_bf16 v[212:215], v[248:251], v[236:239], v[212:215]\n\t" \
    "ds_read_b128 v[248:251], v254 offset:56320\n\t" \
    "s_waitcnt lgkmcnt(4)\n\t" \
    "v_mfma_f32_16x16x32_bf16 v[204:207], v[64:67], v[240:243], v[204:207]\n\t" \
    "ds_read_b128 v[232:235], v253 offset:23552\n\t" \
    "s_waitcnt lgkmcnt(4)\n\t" \
    "v_mfma_f32_16x16x32_bf16 v[204:207], v[68:71], v[224:227], v[204:207]\n\t" \
    "ds_read_b128 v[236:239], v253 offset:4096\n\t" \
    "v_mfma_f32_16x16x32_bf16 v[208:211], v[72:75], v[240:243], v[208:211]\n\t" \
    "v_mfma_f32_16x16x32_bf16 v[208:211], v[76:79], v[224:227], v[208:211]\n\t" \
    "v_mfma_f32_16x16x32_bf16 v[200:203], v[56:59], v[240:243], v[200:203]\n\t" \
    "v_mfma_f32_16x16x32_bf16 v[200:203], v[60:63], v[224:227], v[200:203]\n\t" \
    "s_waitcnt lgkmcnt(4)\n\t" \
    "v_mfma_f32_16x16x32_bf16 v[216:219], v[64:67], v[228:231], v[216:219]\n\t" \
    "ds_read_b128 v[240:243], v253 offset:5120\n\t" \
    "s_waitcnt lgkmcnt(2)\n\t" \
    "v_mfma_f32_16x16x32_bf16 v[216:219], v[68:71], v[232:235], v[216:219]\n\t" \
    "ds_read_b128 v[224:227], v253 offset:24576\n\t" \
    "v_mfma_f32_16x16x32_bf16 v[220:223], v[72:75], v[228:231], v[220:223]\n\t" \
    "v_mfma_f32_16x16x32_bf16 v[220:223], v[76:79], v[232:235], v[220:223]\n\t" \
    "v_mfma_f32_16x16x32_bf16 v[212:215], v[56:59], v[228:231], v[212:215]\n\t" \
    "v_mfma_f32_16x16x32_bf16 v[212:215], v[60:63], v[232:235], v[212:215]\n\t" \
    "s_waitcnt lgkmcnt(2)\n\t" \
    "v_mfma_f32_16x16x32_bf16 v[204:207], v[80:83], v[236:239], v[204:207]\n\t" \
    "ds_read_b128 v[228:231], v253 offset:25600\n\t" \
    "s_waitcnt lgkmcnt(2)\n\t" \
    "v_mfma_f32_16x16x32_bf16 v[204:207], v[84:87], v[240:243], v[204:207]\n\t" \
    "ds_read_b128 v[232:235], v253 offset:6144\n\t" \
    "v_mfma_f32_16x16x32_bf16 v[208:211], v[88:91], v[236:239], v[208:211]\n\t" \
    "v_mfma_f32_16x16x32_bf16 v[208:211], v[92:95], v[240:243], v[208:211]\n\t" \
    "v_mfma_f32_16x16x32_bf16 v[200:203], v[244:247], v[236:239], v[200:203]\n\t" \
    "v_mfma_f32_16x16x32_bf16 v[200:203], v[248:251], v[240:243], v[200:203]\n\t" \
    "s_waitcnt lgkmcnt(2)\n\t" \
    "v_mfma_f32_16x16x32_bf16 v[216:219], v[80:83], v[224:227], v[216:219]\n\t" \
    "ds_read_b128 v[236:239], v253 offset:7168\n\t" \
    "s_waitcnt lgkmcnt(2)\n\t" \
    "v_mfma_f32_16x16x32_bf16 v[216:219], v[84:87], v[228:231], v[216:219]\n\t" \
    "ds_read_b128 v[240:243], v253 offset:26624\n\t" \
    "v_mfma_f32_16x16x32_bf16 v[220:223], v[88:91], v[224:227], v[220:223]\n\t" \
    "v_mfma_f32_16x16x32_bf16 v[220:223], v[92:95], v[228:231], v[220:223]\n\t" \
    "v_mfma_f32_16x16x32_bf16 v[212:215], v[244:247], v[224:227], v[212:215]\n\t" \
    "ds_read_b128 v[244:247], v254 offset:51200\n\t" \
    "v_mfma_f32_16x16x32_bf16 v[212:215], v[248:251], v[228:231], v[212:215]\n\t" \
    "ds_read_b128 v[248:251], v254 offset:52224\n\t" \
    "s_waitcnt lgkmcnt(4)\n\t" \
    "v_mfma_f32_16x16x32_bf16 v[204:207], v[104:107], v[232:235], v[204:207]\n\t" \
    "ds_read_b128 v[224:227], v253 offset:27648\n\t" \
    "s_waitcnt lgkmcnt(4)\n\t" \
    "v_mfma_f32_16x16x32_bf16 v[204:207], v[108:111], v[236:239], v[204:207]\n\t" \
    "ds_read_b128 v[228:231], v253 offset:8192\n\t" \
    "v_mfma_f32_16x16x32_bf16 v[208:211], v[112:115], v[232:235], v[208:211]\n\t" \
    "v_mfma_f32_16x16x32_bf16 v[208:211], v[116:119], v[236:239], v[208:211]\n\t" \
    "v_mfma_f32_16x16x32_bf16 v[200:203], v[96:99], v[232:235], v[200:203]\n\t" \
    "v_mfma_f32_16x16x32_bf16 v[200:203], v[100:103], v[236:239], v[200:203]\n\t" \
    "s_waitcnt lgkmcnt(4)\n\t" \
    "v_mfma_f32_16x16x32_bf16 v[216:219], v[104:107], v[240:243], v[216:219]\n\t" \
    "ds_read_b128 v[232:235], v253 offset:9216\n\t" \
    "s_waitcnt lgkmcnt(2)\n\t" \
    "v_mfma_f32_16x16x32_bf16 v[216:219], v[108:111], v[224:227], v[216:219]\n\t" \
    "ds_read_b128 v[236:239], v253 offset:28672\n\t" \
    "v_mfma_f32_16x16x32_bf16 v[220:223], v[112:115], v[240:243], v[220:223]\n\t" \
    "v_mfma_f32_16x16x32_bf16 v[220:223], v[116:119], v[224:227], v[220:223]\n\t" \
    "v_mfma_f32_16x16x32_bf16 v[212:215], v[96:99], v[240:243], v[212:215]\n\t" \
    "v_mfma_f32_16x16x32_bf16 v[212:215], v[100:103], v[224:227], v[212:215]\n\t" \
    "s_waitcnt lgkmcnt(2)\n\t" \
    "v_mfma_f32_16x16x32_bf16 v[204:207], v[0:3], v[228:231], v[204:207]\n\t" \
    "ds_read_b128 v[240:243], v253 offset:29696\n\t" \
    "s_waitcnt lgkmcnt(2)\n\t" \
    "v_mfma_f32_16x16x32_bf16 v[204:207], v[4:7], v[232:235], v[204:207]\n\t" \
    "ds_read_b128 v[224:227], v253 offset:10240\n\t" \
    "v_mfma_f32_16x16x32_bf16 v[208:211], v[8:11], v[228:231], v[208:211]\n\t" \
    "v_mfma_f32_16x16x32_bf16 v[208:211], v[12:15], v[232:235], v[208:211]\n\t" \
    "v_mfma_f32_16x16x32_bf16 v[200:203], v[244:247], v[228:231], v[200:203]\n\t" \
    "v_mfma_f32_16x16x32_bf16 v[200:203], v[248:251], v[232:235], v[200:203]\n\t" \
    "s_waitcnt lgkmcnt(2)\n\t" \
    "v_mfma_f32_16x16x32_bf16 v[216:219], v[0:3], v[236:239], v[216:219]\n\t" \
    "ds_read_b128 v[228:231], v253 offset:11264\n\t" \
    "s_waitcnt lgkmcnt(2)\n\t" \
    "v_mfma_f32_16x16x32_bf16 v[216:219], v[4:7], v[240:243], v[216:219]\n\t" \
    "ds_read_b128 v[232:235], v253 offset:30720\n\t" \
    "v_mfma_f32_16x16x32_bf16 v[220:223], v[8:11], v[236:239], v[220:223]\n\t" \
    "v_mfma_f32_16x16x32_bf16 v[220:223], v[12:15], v[240:243], v[220:223]\n\t" \
    "v_mfma_f32_16x16x32_bf16 v[212:215], v[244:247], v[236:239], v[212:215]\n\t" \
    "ds_read_b128 v[244:247], v254 offset:57344\n\t" \
    "v_mfma_f32_16x16x32_bf16 v[212:215], v[248:251], v[240:243], v[212:215]\n\t" \
    "ds_read_b128 v[248:251], v254 offset:58368\n\t" \
    "ds_read_b128 v[236:239], v253 offset:31744\n\t" \
    "s_setprio 2\n\t" \
    "s_waitcnt lgkmcnt(0)\n\t" \
    "s_barrier \n\t" \
    LST7 \
    "v_mfma_f32_16x16x32_bf16 v[204:207], v[120:123], v[224:227], v[204:207]\n\t" \
    "v_mfma_f32_16x16x32_bf16 v[204:207], v[124:127], v[228:231], v[204:207]\n\t" \
    "ds_read_b128 v[240:243], v253 offset:12288\n\t" \
    "v_mfma_f32_16x16x32_bf16 v[208:211], v[128:131], v[224:227], v[208:211]\n\t" \
    "v_mfma_f32_16x16x32_bf16 v[208:211], v[132:135], v[228:231], v[208:211]\n\t" \
    "v_mfma_f32_16x16x32_bf16 v[200:203], v[244:247], v[224:227], v[200:203]\n\t" \
    "v_mfma_f32_16x16x32_bf16 v[200:203], v[248:251], v[228:231], v[200:203]\n\t" \
    "v_mfma_f32_16x16x32_bf16 v[216:219], v[120:123], v[232:235], v[216:219]\n\t" \
    "ds_read_b128 v[224:227], v253 offset:13312\n\t" \
    "s_and_b32 s44, s40, 3\n\t" \
    "v_mfma_f32_16x16x32_bf16 v[216:219], v[124:127], v[236:239], v[216:219]\n\t" \
    "ds_read_b128 v[228:231], v253 offset:32768\n\t" \
    "s_lshl_b32 s44, s44, 17\n\t" \
    "v_mfma_f32_16x16x32_bf16 v[220:223], v[128:131], v[232:235], v[220:223]\n\t" \
    "s_add_u32 s44, s44, %[mb]\n\t" \
    "v_mfma_f32_16x16x32_bf16 v[220:223], v[132:135], v[236:239], v[220:223]\n\t" \
    "s_add_u32 s46, s44, 0x1000\n\t" \
    "v_mfma_f32_16x16x32_bf16 v[212:215], v[244:247], v[232:235], v[212:215]\n\t" \
    "ds_read_b128 v[244:247], v254 offset:59392\n\t" \
    "s_add_u32 s47, s44, 0x1c00\n\t" \
    "v_mfma_f32_16x16x32_bf16 v[212:215], v[248:251], v[236:239], v[212:215]\n\t" \
    "ds_read_b128 v[248:251], v254 offset:60416\n\t" \
    "s_waitcnt lgkmcnt(4)\n\t" \
    "v_mfma_f32_16x16x32_bf16 v[204:207], v[144:147], v[240:243], v[204:207]\n\t" \
    "ds_read_b128 v[232:235], v253 offset:33792\n\t" \
    "s_waitcnt lgkmcnt(4)\n\t" \
    "v_mfma_f32_16x16x32_bf16 v[204:207], v[148:151], v[224:227], v[204:207]\n\t" \
    "ds_read_b128 v[236:239], v253 offset:16384\n\t" \
    "v_mfma_f32_16x16x32_bf16 v[208:211], v[152:155], v[240:243], v[208:211]\n\t" \
    "v_mfma_f32_16x16x32_bf16 v[208:211], v[156:159], v[224:227], v[208:211]\n\t" \
    "v_mfma_f32_16x16x32_bf16 v[200:203], v[136:139], v[240:243], v[200:203]\n\t" \
    "v_mfma_f32_16x16x32_bf16 v[200:203], v[140:143], v[224:227], v[200:203]\n\t" \
    "s_waitcnt lgkmcnt(4)\n\t" \
    "v_mfma_f32_16x16x32_bf16 v[216:219], v[144:147], v[228:231], v[216:219]\n\t" \
    "ds_read_b128 v[240:243], v253 offset:17408\n\t" \
    "s_waitcnt lgkmcnt(2)\n\t" \
    "v_mfma_f32_16x16x32_bf16 v[216:219], v[148:151], v[232:235], v[216:219]\n\t" \
    "ds_read_b128 v[224:227], v253 offset:36864\n\t" \
    "v_mfma_f32_16x16x32_bf16 v[220:223], v[152:155], v[228:231], v[220:223]\n\t" \
    "v_mfma_f32_16x16x32_bf16 v[220:223], v[156:159], v[232:235], v[220:223]\n\t" \
    "v_mfma_f32_16x16x32_bf16 v[212:215], v[136:139], v[228:231], v[212:215]\n\t" \
    "v_mfma_f32_16x16x32_bf16 v[212:215], v[140:143], v[232:235], v[212:215]\n\t" \
    "s_waitcnt lgkmcnt(2)\n\t" \
    "v_mfma_f32_16x16x32_bf16 v[204:207], v[160:163], v[236:239], v[204:207]\n\t" \
    "ds_read_b128 v[228:231], v253 offset:37888\n\t" \
    "s_waitcnt lgkmcnt(2)\n\t" \
    "v_mfma_f32_16x16x32_bf16 v[204:207], v[164:167], v[240:243], v[204:207]\n\t" \
    "ds_read_b128 v[232:235], v253 offset:14336\n\t" \
    "v_mfma_f32_16x16x32_bf16 v[208:211], v[168:171], v[236:239], v[208:211]\n\t" \
    "v_mfma_f32_16x16x32_bf16 v[208:211], v[172:175], v[240:243], v[208:211]\n\t" \
    "v_mfma_f32_16x16x32_bf16 v[200:203], v[244:247], v[236:239], v[200:203]\n\t" \
    "v_mfma_f32_16x16x32_bf16 v[200:203], v[248:251], v[240:243], v[200:203]\n\t" \
    "s_waitcnt lgkmcnt(2)\n\t" \
    "v_mfma_f32_16x16x32_bf16 v[216:219], v[160:163], v[224:227], v[216:219]\n\t" \
    "ds_read_b128 v[236:239], v253 offset:15360\n\t" \
    "s_waitcnt lgkmcnt(2)\n\t" \
    "v_mfma_f32_16x16x32_bf16 v[216:219], v[164:167], v[228:231], v[216:219]\n\t" \
    "ds_read_b128 v[240:243], v253 offset:18432\n\t" \
    "v_mfma_f32_16x16x32_bf16 v[220:223], v[168:171], v[224:227], v[220:223]\n\t" \
    "v_mfma_f32_16x16x32_bf16 v[220:223], v[172:175], v[228:231], v[220:223]\n\t" \
    "v_mfma_f32_16x16x32_bf16 v[212:215], v[244:247], v[224:227], v[212:215]\n\t" \
    "v_mfma_f32_16x16x32_bf16 v[212:215], v[248:251], v[228:231], v[212:215]\n\t" \
    "s_waitcnt lgkmcnt(2)\n\t" \
    "v_mfma_f32_16x16x32_bf16 v[204:207], v[24:27], v[232:235], v[204:207]\n\t" \
    "ds_read_b128 v[224:227], v253 offset:19456\n\t" \
    "v_mov_b64 v[244:245], 0\n\t" \
    "s_waitcnt lgkmcnt(2)\n\t" \
    "v_mfma_f32_16x16x32_bf16 v[204:207], v[28:31], v[236:239], v[204:207]\n\t" \
    "ds_read_b128 v[228:231], v253 offset:34816\n\t" \
    "v_mov_b64 v[246:247], 0\n\t" \
    "v_mfma_f32_16x16x32_bf16 v[208:211], v[32:35], v[232:235], v[208:211]\n\t" \
    "v_mov_b64 v[248:249], 0\n\t" \
    "v_mfma_f32_16x16x32_bf16 v[208:211], v[36:39], v[236:239], v[208:211]\n\t" \
    "v_mov_b64 v[250:251], 0\n\t" \
    "v_mfma_f32_16x16x32_bf16 v[200:203], v[16:19], v[232:235], v[200:203]\n\t" \
    "v_mfma_f32_16x16x32_bf16 v[200:203], v[20:23], v[236:239], v[200:203]\n\t" \
    "s_waitcnt lgkmcnt(2)\n\t" \
    "v_mfma_f32_16x16x32_bf16 v[204:207], v[184:187], v[240:243], v[204:207]\n\t" \
    "ds_read_b128 v[232:235], v253 offset:35840\n\t" \
    "s_waitcnt lgkmcnt(2)\n\t" \
    "v_mfma_f32_16x16x32_bf16 v[204:207], v[188:191], v[224:227], v[204:207]\n\t" \
    "ds_read_b128 v[236:239], v253 offset:38912\n\t" \
    "v_mfma_f32_16x16x32_bf16 v[208:211], v[192:195], v[240:243], v[208:211]\n\t" \
    "v_mfma_f32_16x16x32_bf16 v[208:211], v[196:199], v[224:227], v[208:211]\n\t" \
    "v_mfma_f32_16x16x32_bf16 v[200:203], v[176:179], v[240:243], v[200:203]\n\t" \
    "v_mfma_f32_16x16x32_bf16 v[200:203], v[180:183], v[224:227], v[200:203]\n\t" \
    "ds_read_b128 v[240:243], v253 offset:39936\n\t" \
    "s_waitcnt lgkmcnt(3)\n\t" \
    "v_mfma_f32_16x16x32_bf16 v[216:219], v[24:27], v[228:231], v[216:219]\n\t" \
    "s_waitcnt lgkmcnt(2)\n\t" \
    "v_mfma_f32_16x16x32_bf16 v[216:219], v[28:31], v[232:235], v[216:219]\n\t" \
    "v_cvt_pk_bf16_f32 v204, v204, v208\n\t" \
    "v_cvt_pk_bf16_f32 v205, v205, v209\n\t" \
    "s_waitcnt lgkmcnt(1)\n\t" \
    "v_mfma_f32_16x16x32_bf16 v[216:219], v[184:187], v[236:239], v[216:219]\n\t" \
    "v_cvt_pk_bf16_f32 v206, v206, v210\n\t" \
    "v_cvt_pk_bf16_f32 v207, v207, v211\n\t" \
    "s_waitcnt lgkmcnt(0)\n\t" \
    "v_mfma_f32_16x16x32_bf16 v[216:219], v[188:191], v[240:243], v[216:219]\n\t" \
    "ds_write_b128 v254, v[204:207] offset:10240\n\t" \
    "v_mfma_f32_16x16x32_bf16 v[220:223], v[32:35], v[228:231], v[220:223]\n\t" \
    "v_mfma_f32_16x16x32_bf16 v[220:223], v[36:39], v[232:235], v[220:223]\n\t" \
    "v_cvt_pk_bf16_f32 v200, v200, v201\n\t" \
    "v_cvt_pk_bf16_f32 v201, v202, v203\n\t" \
    "v_mfma_f32_16x16x32_bf16 v[220:223], v[192:195], v[236:239], v[220:223]\n\t" \
    "ds_write_b64 v254, v[200:201] offset:6152\n\t" \
    "v_mfma_f32_16x16x32_bf16 v[220:223], v[196:199], v[240:243], v[220:223]\n\t" \
    "v_mfma_f32_16x16x32_bf16 v[212:215], v[16:19], v[228:231], v[212:215]\n\t" \
    "ds_read_b128 v[200:203], v254 offset:51200\n\t" \
    "ds_read_b128 v[204:207], v254 offset:52224\n\t" \
    "v_mfma_f32_16x16x32_bf16 v[212:215], v[20:23], v[232:235], v[212:215]\n\t" \
    "v_mov_b64 v[208:209], 0\n\t" \
    "v_mov_b64 v[210:211], 0\n\t" \
    "v_mfma_f32_16x16x32_bf16 v[212:215], v[176:179], v[236:239], v[212:215]\n\t" \
    "v_cvt_pk_bf16_f32 v216, v216, v220\n\t" \
    "v_cvt_pk_bf16_f32 v217, v217, v221\n\t" \
    "v_mfma_f32_16x16x32_bf16 v[212:215], v[180:183], v[240:243], v[212:215]\n\t" \
    "v_cvt_pk_bf16_f32 v218, v218, v222\n\t" \
    "v_cvt_pk_bf16_f32 v219, v219, v223\n\t" \
    "s_setprio 0\n\t" \
    "ds_write_b128 v254, v[216:219] offset:16384\n\t" \
    "ds_read_b128 v[216:219], v254 offset:53248\n\t" \
    "ds_read_b128 v[220:223], v254 offset:54272\n\t" \
    "s_nop 2\n\t" \
    "v_cvt_pk_bf16_f32 v212, v212, v213\n\t" \
    "v_cvt_pk_bf16_f32 v213, v214, v215\n\t" \
    "ds_write_b64 v254, v[212:213] offset:12296\n\t" \
    LST6 \
    "s_sleep 3\n\t" \
    "s_mov_b32 m0, 0xb000\n\t" \
    "s_nop 0\n\t" \
    "buffer_load_dwordx4 v253, s[60:63], s46 offen sc1 lds\n\t" \
    "s_mov_b32 m0, 0xbc00\n\t" \
    "s_nop 0\n\t" \
    "buffer_load_dwordx4 v253, s[60:63], s47 offen sc1 lds\n\t" \
    "s_waitcnt lgkmcnt(0)\n\t" \
    LFLUSH212 \
    "v_bfe_i32 v212, v253, 5, 1\n\t" \
    "v_and_b32 v212, 0xaaaa, v212\n\t" \
    "v_or_b32 v212, 0x4444, v212\n\t" \
    "v_and_b32 v213, 0xffff, v255\n\t" \
    "v_lshrrev_b32 v214, 16, v255\n\t" \
    ".Ls24_%=:\n\t" \
    "s_waitcnt vmcnt(0)\n\t" \
    "ds_read_b128 v[224:227], v253 offset:45056\n\t" \
    "ds_read_b128 v[228:231], v253 offset:48128\n\t" \
    "s_waitcnt lgkmcnt(1)\n\t" \
    "v_max_u32 v215, v224, v225\n\t" \
    "v_max3_u32 v215, v226, v227, v215\n\t" \
    "s_waitcnt lgkmcnt(0)\n\t" \
    "v_max3_u32 v215, v228, v229, v215\n\t" \
    "v_max3_u32 v215, v230, v231, v215\n\t" \
    "v_cmp_eq_u32 vcc, -1, v215\n\t" \
    "s_cbranch_vccz .Ls25_%=\n\t" \
    "v_max_u32 v215, v224, v225\n\t" \
    "v_max3_u32 v215, v226, v227, v215\n\t" \
    "v_cmp_eq_u32 vcc, -1, v215\n\t" \
    "s_cbranch_vccz .Ls26_%=\n\t" \
    "s_mov_b32 m0, 0xb000\n\t" \
    "s_nop 0\n\t" \
    "buffer_load_dwordx4 v253, s[60:63], s46 offen sc1 lds\n\t" \
    ".Ls26_%=:\n\t" \
    "v_max_u32 v215, v228, v229\n\t" \
    "v_max3_u32 v215, v230, v231, v215\n\t" \
    "v_cmp_eq_u32 vcc, -1, v215\n\t" \
    "s_cbranch_vccz .Ls27_%=\n\t" \
    "s_mov_b32 m0, 0xbc00\n\t" \
    "s_nop 0\n\t" \
    "buffer_load_dwordx4 v253, s[60:63], s47 offen sc1 lds\n\t" \
    ".Ls27_%=:\n\t" \
    "s_cmp_eq_u32 s41, 0\n\t" \
    "s_cbranch_scc1 .Ls25_%=\n\t" \
    "s_sub_u32 s41, s41, 1\n\t" \
    "s_branch .Ls24_%=\n\t" \
    ".Ls25_%=:\n\t" \
    LST2 \
    "s_setprio 3\n\t" \
    "s_waitcnt lgkmcnt(0)\n\t" \
    "s_barrier \n\t" \
    LST3 \
    "ds_read_b128 v[232:235], v253 offset:40960\n\t" \
    "ds_read_b128 v[236:239], v253 offset:41984\n\t" \
    "ds_read_b128 v[240:243], v253 offset:43008\n\t" \
    "v_smfmac_f32_16x16x64_bf16 v[244:247], v[224:227], v[0:7], v212\n\t" \
    "v_smfmac_f32_16x16x64_bf16 v[248:251], v[224:227], v[8:15], v212\n\t" \
    "v_smfmac_f32_16x16x64_bf16 v[208:211], v[224:227], v[200:207], v212\n\t" \
    "ds_read_b128 v[200:203], v254 offset:55296\n\t" \
    "ds_read_b128 v[204:207], v254 offset:56320\n\t" \
    "ds_read_b128 v[224:227], v253 offset:44032\n\t" \
    "v_smfmac_f32_16x16x64_bf16 v[208:211], v[228:231], v[16:23], v212\n\t" \
    "v_smfmac_f32_16x16x64_bf16 v[244:247], v[228:231], v[24:31], v212\n\t" \
    "v_smfmac_f32_16x16x64_bf16 v[248:251], v[228:231], v[32:39], v212\n\t" \
    "ds_read_b128 v[228:231], v253 offset:46080\n\t" \
    "s_waitcnt lgkmcnt(6)\n\t" \
    "v_smfmac_f32_16x16x64_bf16 v[208:211], v[232:235], v[216:223], v212\n\t" \
    "ds_read_b128 v[216:219], v254 offset:57344\n\t" \
    "ds_read_b128 v[220:223], v254 offset:58368\n\t" \
    "v_smfmac_f32_16x16x64_bf16 v[244:247], v[232:235], v[40:47], v212\n\t" \
    "v_smfmac_f32_16x16x64_bf16 v[248:251], v[232:235], v[48:55], v212\n\t" \
    "ds_read_b128 v[232:235], v253 offset:47104\n\t" \
    "s_waitcnt lgkmcnt(8)\n\t" \
    "v_smfmac_f32_16x16x64_bf16 v[208:211], v[236:239], v[56:63], v212\n\t" \
    "v_smfmac_f32_16x16x64_bf16 v[244:247], v[236:239], v[64:71], v212\n\t" \
    "v_smfmac_f32_16x16x64_bf16 v[248:251], v[236:239], v[72:79], v212\n\t" \
    "ds_read_b128 v[236:239], v253 offset:49152\n\t" \
    "s_waitcnt lgkmcnt(6)\n\t" \
    "v_smfmac_f32_16x16x64_bf16 v[208:211], v[240:243], v[200:207], v212\n\t" \
    "ds_read_b128 v[200:203], v254 offset:59392\n\t" \
    "ds_read_b128 v[204:207], v254 offset:60416\n\t" \
    "v_smfmac_f32_16x16x64_bf16 v[244:247], v[240:243], v[80:87], v212\n\t" \
    "v_smfmac_f32_16x16x64_bf16 v[248:251], v[240:243], v[88:95], v212\n\t" \
    "ds_read_b128 v[240:243], v253 offset:50176\n\t" \
    "s_waitcnt lgkmcnt(8)\n\t" \
    "v_smfmac_f32_16x16x64_bf16 v[208:211], v[224:227], v[96:103], v212\n\t" \
    "v_smfmac_f32_16x16x64_bf16 v[244:247], v[224:227], v[104:111], v212\n\t" \
    "v_smfmac_f32_16x16x64_bf16 v[248:251], v[224:227], v[112:119], v212\n\t" \
    "s_waitcnt lgkmcnt(5)\n\t" \
    "v_smfmac_f32_16x16x64_bf16 v[208:211], v[228:231], v[216:223], v212\n\t" \
    "s_waitcnt lgkmcnt(4)\n\t" \
    "v_smfmac_f32_16x16x64_bf16 v[208:211], v[232:235], v[136:143], v212\n\t" \
    "s_waitcnt lgkmcnt(1)\n\t" \
    "v_smfmac_f32_16x16x64_bf16 v[208:211], v[236:239], v[200:207], v212\n\t" \
    "s_waitcnt lgkmcnt(0)\n\t" \
    "v_smfmac_f32_16x16x64_bf16 v[208:211], v[240:243], v[176:183], v212\n\t" \
    "v_smfmac_f32_16x16x64_bf16 v[244:247], v[228:231], v[120:127], v212\n\t" \
    "v_smfmac_f32_16x16x64_bf16 v[244:247], v[232:235], v[144:151], v212\n\t" \
    "v_smfmac_f32_16x16x64_bf16 v[244:247], v[236:239], v[160:167], v212\n\t" \
    "v_add_f32 v208, v208, v210\n\t" \
    "v_add_f32 v209, v209, v211\n\t" \
    "v_cvt_pk_bf16_f32 v208, v208, v209\n\t" \
    "v_smfmac_f32_16x16x64_bf16 v[244:247], v[240:243], v[184:191], v212\n\t" \
    "ds_write_b16 v213, v208 offset:0\n\t" \
    "ds_write_b16_d16_hi v213, v208 offset:16\n\t" \
    "v_smfmac_f32_16x16x64_bf16 v[248:251], v[228:231], v[128:135], v212\n\t" \
    "v_smfmac_f32_16x16x64_bf16 v[248:251], v[232:235], v[152:159], v212\n\t" \
    "v_smfmac_f32_16x16x64_bf16 v[248:251], v[236:239], v[168:175], v212\n\t" \
    "v_add_f32 v244, v244, v246\n\t" \
    "v_add_f32 v245, v245, v247\n\t" \
    "v_smfmac_f32_16x16x64_bf16 v[248:251], v[240:243], v[192:199], v212\n\t" \
    "s_nop 7\n\t" \
    "v_add_f32 v248, v248, v250\n\t" \
    "v_add_f32 v249, v249, v251\n\t" \
    "v_cvt_pk_bf16_f32 v244, v244, v248\n\t" \
    "v_cvt_pk_bf16_f32 v245, v245, v249\n\t" \
    "ds_write2_b32 v214, v244, v245 offset1:4\n\t" \
    "s_setprio 0\n\t" \
    "s_sleep 1\n\t" \
    "ds_read_b128 v[200:203], v254 offset:12288\n\t" \
    "ds_read_b128 v[224:227], v254 offset:24576\n\t" \
    "ds_read_b128 v[204:207], v254 offset:13312\n\t" \
    "ds_read_b128 v[228:231], v254 offset:25600\n\t" \
    "ds_read_b128 v[208:211], v254 offset:14336\n\t" \
    "ds_read_b128 v[232:235], v254 offset:26624\n\t" \
    "ds_read_b128 v[212:215], v254 offset:15360\n\t" \
    "ds_read_b128 v[236:239], v254 offset:27648\n\t" \
    "ds_read_b128 v[216:219], v254 offset:16384\n\t" \
    "ds_read_b128 v[220:223], v254 offset:17408\n\t" \
    "s_and_b32 s44, s40, 3\n\t" \
    "s_lshl_b32 s44, s44, 17\n\t" \
    "s_add_u32 s50, s44, %[pb]\n\t" \
    "s_add_u32 s51, s44, %[pm]\n\t" \
    "s_mov_b32 s52, 0xf0000\n\t" \
    "s_mov_b32 s53, 0xfff00ff\n\t" \
    "s_mov_b32 s54, 0x1f0001\n\t" \
    "s_mov_b32 s55, 0x1fff01ff\n\t" \
    "s_mov_b32 s56, 0x3f0003\n\t" \
    "s_mov_b32 s57, 0x3fff03ff\n\t" \
    "s_mov_b32 s58, 0x7f0007\n\t" \
    "s_mov_b32 s59, 0x7fff07ff\n\t" \
    "s_waitcnt lgkmcnt(8)\n\t" \
    "v_mfma_f32_16x16x32_bf16 v[240:243], v[224:227], v[200:203], 0\n\t" \
    "ds_read_b128 v[224:227], v254 offset:28672\n\t" \
    "s_waitcnt lgkmcnt(7)\n\t" \
    "v_mfma_f32_16x16x32_bf16 v[240:243], v[228:231], v[204:207], v[240:243]\n\t" \
    "ds_read_b128 v[228:231], v254 offset:29696\n\t" \
    "s_waitcnt lgkmcnt(6)\n\t" \
    "v_mfma_f32_16x16x32_bf16 v[240:243], v[232:235], v[208:211], v[240:243]\n\t" \
    "ds_read_b128 v[232:235], v254 offset:18432\n\t" \
    "s_waitcnt lgkmcnt(5)\n\t" \
    "v_mfma_f32_16x16x32_bf16 v[240:243], v[236:239], v[212:215], v[240:243]\n\t" \
    "ds_read_b128 v[236:239], v254 offset:19456\n\t" \
    "v_and_b32 v252, 0xffffff7f, v254\n\t" \
    "s_waitcnt lgkmcnt(0)\n\t" \
    "s_barrier \n\t" \
    LST4 \
    "v_mfma_f32_16x16x32_bf16 v[240:243], v[224:227], v[216:219], v[240:243]\n\t" \
    "ds_read_b128 v[224:227], v254 offset:20480\n\t" \
    "v_mfma_f32_16x16x32_bf16 v[240:243], v[228:231], v[220:223], v[240:243]\n\t" \
    "ds_read_b128 v[228:231], v254 offset:21504\n\t" \
    "v_mfma_f32_16x16x32_bf16 v[244:247], v[232:235], v[200:203], 0\n\t" \
    "ds_read_b128 v[232:235], v254 offset:22528\n\t" \
    "v_mfma_f32_16x16x32_bf16 v[244:247], v[236:239], v[204:207], v[244:247]\n\t" \
    "ds_read_b128 v[236:239], v254 offset:23552\n\t" \
    "s_waitcnt lgkmcnt(3)\n\t" \
    "v_mfma_f32_16x16x32_bf16 v[244:247], v[224:227], v[208:211], v[244:247]\n\t" \
    "ds_read_b128 v[224:227], v252 offset:3072\n\t" \
    "s_waitcnt lgkmcnt(3)\n\t" \
    "v_mfma_f32_16x16x32_bf16 v[244:247], v[228:231], v[212:215], v[244:247]\n\t" \
    "ds_read_b128 v[228:231], v252 offset:3200\n\t" \
    "s_waitcnt lgkmcnt(3)\n\t" \
    "v_mfma_f32_16x16x32_bf16 v[244:247], v[232:235], v[216:219], v[244:247]\n\t" \
    "ds_read_b128 v[232:235], v252 offset:4096\n\t" \
    "s_waitcnt lgkmcnt(3)\n\t" \
    "v_mfma_f32_16x16x32_bf16 v[244:247], v[236:239], v[220:223], v[244:247]\n\t" \
    "ds_read_b128 v[236:239], v252 offset:4224\n\t" \
    "s_waitcnt lgkmcnt(3)\n\t" \
    "v_mfma_f32_16x16x32_bf16 v[248:251], v[224:227], v[200:203], 0\n\t" \
    "ds_read_b128 v[224:227], v252 offset:5120\n\t" \
    "s_waitcnt lgkmcnt(3)\n\t" \
    "v_mfma_f32_16x16x32_bf16 v[248:251], v[228:231], v[204:207], v[248:251]\n\t" \
    "ds_read_b128 v[228:231], v252 offset:5248\n\t" \
    "s_waitcnt lgkmcnt(3)\n\t" \
    "v_mfma_f32_16x16x32_bf16 v[248:251], v[232:235], v[208:211], v[248:251]\n\t" \
    "s_waitcnt lgkmcnt(2)\n\t" \
    "v_mfma_f32_16x16x32_bf16 v[248:251], v[236:239], v[212:215], v[248:251]\n\t" \
    "s_waitcnt lgkmcnt(1)\n\t" \
    "v_mfma_f32_16x16x32_bf16 v[248:251], v[224:227], v[216:219], v[248:251]\n\t" \
    "s_waitcnt lgkmcnt(0)\n\t" \
    "v_mfma_f32_16x16x32_bf16 v[248:251], v[228:231], v[220:223], v[248:251]\n\t" \
    "v_mov_b32 v226, 0xff7fffff\n\t" \
    "v_cndmask_b32 v240, v240, v226, s[52:53]\n\t" \
    "v_cndmask_b32 v241, v241, v226, s[54:55]\n\t" \
    "v_cndmask_b32 v242, v242, v226, s[56:57]\n\t" \
    "v_cndmask_b32 v243, v243, v226, s[58:59]\n\t" \
    "v_max3_f32 v224, v240, v241, v242\n\t" \
    "v_max3_f32 v225, v244, v245, v246\n\t" \
    "v_max3_f32 v224, v224, v243, v247\n\t" \
    "v_lshrrev_b32 v200, 5, v253\n\t" \
    "v_and_b32 v201, 0xf0, v253\n\t" \
    "v_and_or_b32 v200, v200, 8, v201\n\t" \
    "v_max3_f32 v224, v224, v248, v249\n\t" \
    "v_max3_f32 v225, v225, v250, v251\n\t" \
    "v_max_f32 v224, v224, v225\n\t" \
    "v_mov_b32 v225, v224\n\t" \
    "s_nop 1\n\t" \
    "v_permlane16_swap_b32 v224, v225\n\t" \
    "v_max_f32 v224, v224, v225\n\t" \
    "v_mov_b32 v225, v224\n\t" \
    "s_nop 1\n\t" \
    "v_permlane32_swap_b32 v224, v225\n\t" \
    "v_max_f32 v224, v224, v225\n\t" \
    "v_mul_f32 v224, %[scl], v224\n\t" \
    "v_fma_f32 v240, v240, %[scl], -v224\n\t" \
    "v_fma_f32 v241, v241, %[scl], -v224\n\t" \
    "v_exp_f32 v240, v240\n\t" \
    "v_fma_f32 v242, v242, %[scl], -v224\n\t" \
    "v_exp_f32 v241, v241\n\t" \
    "v_fma_f32 v243, v243, %[scl], -v224\n\t" \
    "v_exp_f32 v242, v242\n\t" \
    "v_fma_f32 v244, v244, %[scl], -v224\n\t" \
    "v_exp_f32 v243, v243\n\t" \
    "v_fma_f32 v245, v245, %[scl], -v224\n\t" \
    "v_exp_f32 v244, v244\n\t" \
    "v_fma_f32 v246, v246, %[scl], -v224\n\t" \
    "v_exp_f32 v245, v245\n\t" \
    "v_fma_f32 v247, v247, %[scl], -v224\n\t" \
    "v_exp_f32 v246, v246\n\t" \
    "v_fma_f32 v248, v248, %[scl], -v224\n\t" \
    "v_exp_f32 v247, v247\n\t" \
    "v_fma_f32 v249, v249, %[scl], -v224\n\t" \
    "v_exp_f32 v248, v248\n\t" \
    "v_fma_f32 v250, v250, %[scl], -v224\n\t" \
    "v_exp_f32 v249, v249\n\t" \
    "v_fma_f32 v251, v251, %[scl], -v224\n\t" \
    "v_exp_f32 v250, v250\n\t" \
    "v_exp_f32 v251, v251\n\t" \
    "v_add_f32 v224, v240, v241\n\t" \
    "v_add_f32 v225, v242, v243\n\t" \
    "v_add_f32 v226, v244, v245\n\t" \
    "v_add_f32 v227, v246, v247\n\t" \
    "v_add_f32 v232, v248, v249\n\t" \
    "v_add_f32 v233, v250, v251\n\t" \
    "v_add_f32 v224, v224, v225\n\t" \
    "v_add_f32 v226, v226, v227\n\t" \
    "v_add_f32 v232, v232, v233\n\t" \
    "v_add_f32 v224, v224, v226\n\t" \
    "v_fma_f32 v224, v232, 0.5, v224\n\t" \
    "v_mov_b32 v225, v224\n\t" \
    "s_nop 1\n\t" \
    "v_permlane16_swap_b32 v224, v225\n\t" \
    "v_add_f32 v224, v224, v225\n\t" \
    "v_mov_b32 v225, v224\n\t" \
    "s_nop 1\n\t" \
    "v_permlane32_swap_b32 v224, v225\n\t" \
    "v_add_f32 v224, v224, v225\n\t" \
    "v_rcp_f32 v224, v224\n\t" \
    "s_nop 0\n\t" \
    "v_mul_f32 v240, v240, v224\n\t" \
    "v_mul_f32 v241, v241, v224\n\t" \
    "v_mul_f32 v242, v242, v224\n\t" \
    "v_mul_f32 v243, v243, v224\n\t" \
    "v_mul_f32 v244, v244, v224\n\t" \
    "v_mul_f32 v245, v245, v224\n\t" \
    "v_mul_f32 v246, v246, v224\n\t" \
    "v_mul_f32 v247, v247, v224\n\t" \
    "v_mul_f32 v248, v248, v224\n\t" \
    "v_mul_f32 v249, v249, v224\n\t" \
    "v_mul_f32 v250, v250, v224\n\t" \
    "v_mul_f32 v251, v251, v224\n\t" \
    "v_cvt_pk_bf16_f32 v204, |v244|, |v240|\n\t" \
    "v_cvt_pk_bf16_f32 v205, |v245|, |v241|\n\t" \
    "v_cvt_pk_bf16_f32 v206, |v246|, |v242|\n\t" \
    "v_cvt_pk_bf16_f32 v207, |v247|, |v243|\n\t" \
    "v_cvt_pk_bf16_f32 v208, |v248|, |v249|\n\t" \
    "v_cvt_pk_bf16_f32 v209, |v250|, |v251|\n\t" \
    "buffer_store_dwordx4 v[204:207], v253, s[60:63], s50 offen sc1\n\t" \
    "s_mov_b32 exec_lo, -1\n\t" \
    "s_mov_b32 exec_hi, 0\n\t" \
    "buffer_store_dwordx2 v[208:209], v200, s[60:63], s51 offen sc1\n\t" \
    "s_mov_b32 exec_hi, -1\n\t" \
    LST5 \
    "ds_read_b128 v[224:227], v253 offset:0\n\t" \
    "ds_read_b128 v[244:247], v254 offset:53248\n\t" \
    "ds_read_b128 v[228:231], v253 offset:1024\n\t" \
    "ds_read_b128 v[232:235], v253 offset:20480\n\t" \
    "ds_read_b128 v[236:239], v253 offset:21504\n\t" \
    "ds_read_b128 v[248:251], v254 offset:54272\n\t" \
    "s_add_u32 s40, s40, 1\n\t" \
    "s_cmp_lg_u32 s40, 64\n\t" \
    "s_cbranch_scc1 .Ls23_%=\n\t" \
    "s_branch .Lend%=\n\t" \
    ".Ls1_%=:\n\t" \
    "s_add_u32 m0, %[wm], 0x18000\n\t" \
    "s_mov_b32 s44, 0x0\n\t" \
    "buffer_load_dwordx4 v240, %[wr], s44 offen offset:0 nt lds\n\t" \
    "buffer_load_dwordx4 v241, %[wr], s44 offen offset:1024 nt lds\n\t" \
    "buffer_load_dwordx4 v242, %[wr], s44 offen offset:2048 nt lds\n\t" \
    "s_add_u32 m0, %[wm], 0x1e000\n\t" \
    "s_mov_b32 s44, 0x60000\n\t" \
    "buffer_load_dwordx4 v240, %[wr], s44 offen offset:0 nt lds\n\t" \
    "buffer_load_dwordx4 v241, %[wr], s44 offen offset:1024 nt lds\n\t" \
    "buffer_load_dwordx4 v242, %[wr], s44 offen offset:2048 nt lds\n\t" \
    "s_add_u32 m0, %[wm], 0x0\n\t" \
    "s_mov_b32 s44, 0xc0000\n\t" \
    "buffer_load_dwordx4 v240, %[wr], s44 offen offset:0 nt lds\n\t" \
    "buffer_load_dwordx4 v241, %[wr], s44 offen offset:1024 nt lds\n\t" \
    "buffer_load_dwordx4 v242, %[wr], s44 offen offset:2048 nt lds\n\t" \
    "s_add_u32 m0, %[wm], 0x6000\n\t" \
    "s_mov_b32 s44, 0x120000\n\t" \
    "buffer_load_dwordx4 v240, %[wr], s44 offen offset:0 nt lds\n\t" \
    "buffer_load_dwordx4 v241, %[wr], s44 offen offset:1024 nt lds\n\t" \
    "buffer_load_dwordx4 v242, %[wr], s44 offen offset:2048 nt lds\n\t" \
    "s_add_u32 m0, %[wm], 0xc000\n\t" \
    "s_mov_b32 s44, 0x180000\n\t" \
    "buffer_load_dwordx4 v240, %[wr], s44 offen offset:0 nt lds\n\t" \
    "buffer_load_dwordx4 v241, %[wr], s44 offen offset:1024 nt lds\n\t" \
    "buffer_load_dwordx4 v242, %[wr], s44 offen offset:2048 nt lds\n\t" \
    "v_add_u32 v244, 0x18000, v243\n\t" \
    "s_waitcnt vmcnt(12) lgkmcnt(0)\n\t" \
    "s_barrier \n\t" \
    "s_add_u32 m0, %[wm], 0x12000\n\t" \
    "s_mov_b32 s44, 0x1e0000\n\t" \
    "buffer_load_dwordx4 v240, %[wr], s44 offen offset:0 nt lds\n\t" \
    "buffer_load_dwordx4 v241, %[wr], s44 offen offset:1024 nt lds\n\t" \
    "buffer_load_dwordx4 v242, %[wr], s44 offen offset:2048 nt lds\n\t" \
    "ds_read_b64_tr_b16 v[56:57], v244 offset:0\n\t" \
    "ds_read_b64_tr_b16 v[58:59], v244 offset:1536\n\t" \
    "ds_read_b64_tr_b16 v[64:65], v244 offset:32\n\t" \
    "ds_read_b64_tr_b16 v[66:67], v244 offset:1568\n\t" \
    "ds_read_b64_tr_b16 v[72:73], v244 offset:64\n\t" \
    "ds_read_b64_tr_b16 v[74:75], v244 offset:1600\n\t" \
    "v_add_u32 v244, 0x1e000, v243\n\t" \
    "s_waitcnt vmcnt(12) lgkmcnt(0)\n\t" \
    "s_barrier \n\t" \
    "s_add_u32 m0, %[wm], 0x18000\n\t" \
    "s_mov_b32 s44, 0x240000\n\t" \
    "buffer_load_dwordx4 v240, %[wr], s44 offen offset:0 nt lds\n\t" \
    "buffer_load_dwordx4 v241, %[wr], s44 offen offset:1024 nt lds\n\t" \
    "buffer_load_dwordx4 v242, %[wr], s44 offen offset:2048 nt lds\n\t" \
    "ds_read_b64_tr_b16 v[60:61], v244 offset:0\n\t" \
    "ds_read_b64_tr_b16 v[62:63], v244 offset:1536\n\t" \
    "ds_read_b64_tr_b16 v[68:69], v244 offset:32\n\t" \
    "ds_read_b64_tr_b16 v[70:71], v244 offset:1568\n\t" \
    "ds_read_b64_tr_b16 v[76:77], v244 offset:64\n\t" \
    "ds_read_b64_tr_b16 v[78:79], v244 offset:1600\n\t" \
    "v_add_u32 v244, 0x0, v243\n\t" \
    "s_waitcnt vmcnt(12) lgkmcnt(0)\n\t" \
    "s_barrier \n\t" \
    "s_add_u32 m0, %[wm], 0x1e000\n\t" \
    "s_mov_b32 s44, 0x2a0000\n\t" \
    "buffer_load_dwordx4 v240, %[wr], s44 offen offset:0 nt lds\n\t" \
    "buffer_load_dwordx4 v241, %[wr], s44 offen offset:1024 nt lds\n\t" \
    "buffer_load_dwordx4 v242, %[wr], s44 offen offset:2048 nt lds\n\t" \
    "ds_read_b64_tr_b16 v[96:97], v244 offset:0\n\t" \
    "ds_read_b64_tr_b16 v[98:99], v244 offset:1536\n\t" \
    "ds_read_b64_tr_b16 v[104:105], v244 offset:32\n\t" \
    "ds_read_b64_tr_b16 v[106:107], v244 offset:1568\n\t" \
    "ds_read_b64_tr_b16 v[112:113], v244 offset:64\n\t" \
    "ds_read_b64_tr_b16 v[114:115], v244 offset:1600\n\t" \
    "v_add_u32 v244, 0x6000, v243\n\t" \
    "s_waitcnt vmcnt(12) lgkmcnt(0)\n\t" \
    "s_barrier \n\t" \
    "s_add_u32 m0, %[wm], 0x0\n\t" \
    "s_mov_b32 s44, 0x300000\n\t" \
    "buffer_load_dwordx4 v240, %[wr], s44 offen offset:0 nt lds\n\t" \
    "buffer_load_dwordx4 v241, %[wr], s44 offen offset:1024 nt lds\n\t" \
    "buffer_load_dwordx4 v242, %[wr], s44 offen offset:2048 nt lds\n\t" \
    "ds_read_b64_tr_b16 v[100:101], v244 offset:0\n\t" \
    "ds_read_b64_tr_b16 v[102:103], v244 offset:1536\n\t" \
    "ds_read_b64_tr_b16 v[108:109], v244 offset:32\n\t" \
    "ds_read_b64_tr_b16 v[110:111], v244 offset:1568\n\t" \
    "ds_read_b64_tr_b16 v[116:117], v244 offset:64\n\t" \
    "ds_read_b64_tr_b16 v[118:119], v244 offset:1600\n\t" \
    "v_add_u32 v244, 0xc000, v243\n\t" \
    "s_waitcnt vmcnt(12) lgkmcnt(0)\n\t" \
    "s_barrier \n\t" \
    "s_add_u32 m0, %[wm], 0x6000\n\t" \
    "s_mov_b32 s44, 0x360000\n\t" \
    "buffer_load_dwordx4 v240, %[wr], s44 offen offset:0 nt lds\n\t" \
    "buffer_load_dwordx4 v241, %[wr], s44 offen offset:1024 nt lds\n\t" \
    "buffer_load_dwordx4 v242, %[wr], s44 offen offset:2048 nt lds\n\t" \
    "ds_read_b64_tr_b16 v[200:201], v244 offset:0\n\t" \
    "ds_read_b64_tr_b16 v[202:203], v244 offset:1536\n\t" \
    "ds_read_b64_tr_b16 v[0:1], v244 offset:32\n\t" \
    "ds_read_b64_tr_b16 v[2:3], v244 offset:1568\n\t" \
    "ds_read_b64_tr_b16 v[8:9], v244 offset:64\n\t" \
    "ds_read_b64_tr_b16 v[10:11], v244 offset:1600\n\t" \
    "v_add_u32 v244, 0x12000, v243\n\t" \
    "s_waitcnt vmcnt(12) lgkmcnt(0)\n\t" \
    "s_barrier \n\t" \
    "s_add_u32 m0, %[wm], 0xc000\n\t" \
    "s_mov_b32 s44, 0x3c0000\n\t" \
    "buffer_load_dwordx4 v240, %[wr], s44 offen offset:0 nt lds\n\t" \
    "buffer_load_dwordx4 v241, %[wr], s44 offen offset:1024 nt lds\n\t" \
    "buffer_load_dwordx4 v242, %[wr], s44 offen offset:2048 nt lds\n\t" \
    "ds_read_b64_tr_b16 v[204:205], v244 offset:0\n\t" \
    "ds_read_b64_tr_b16 v[206:207], v244 offset:1536\n\t" \
    "ds_read_b64_tr_b16 v[4:5], v244 offset:32\n\t" \
    "ds_read_b64_tr_b16 v[6:7], v244 offset:1568\n\t" \
    "ds_read_b64_tr_b16 v[12:13], v244 offset:64\n\t" \
    "ds_read_b64_tr_b16 v[14:15], v244 offset:1600\n\t" \
    "v_add_u32 v244, 0x18000, v243\n\t" \
    "s_waitcnt vmcnt(12) lgkmcnt(0)\n\t" \
    "s_barrier \n\t" \
    "v_mov_b32 v246, 0\n\t" \
    "buffer_load_dword v246, v246, s[60:63], %[mo] offen sc1\n\t" \
    "s_add_u32 m0, %[wm], 0x12000\n\t" \
    "s_mov_b32 s44, 0x420000\n\t" \
    "buffer_load_dwordx4 v240, %[wr], s44 offen offset:0 nt lds\n\t" \
    "buffer_load_dwordx4 v241, %[wr], s44 offen offset:1024 nt lds\n\t" \
    "buffer_load_dwordx4 v242, %[wr], s44 offen offset:2048 nt lds\n\t" \
    "ds_read_b64_tr_b16 v[208:209], v244 offset:0\n\t" \
    "ds_read_b64_tr_b16 v[210:211], v244 offset:1536\n\t" \
    "ds_read_b64_tr_b16 v[40:41], v244 offset:32\n\t" \
    "ds_read_b64_tr_b16 v[42:43], v244 offset:1568\n\t" \
    "ds_read_b64_tr_b16 v[48:49], v244 offset:64\n\t" \
    "ds_read_b64_tr_b16 v[50:51], v244 offset:1600\n\t" \
    "v_add_u32 v244, 0x1e000, v243\n\t" \
    "s_waitcnt vmcnt(12) lgkmcnt(0)\n\t" \
    "s_barrier \n\t" \
    "s_add_u32 m0, %[wm], 0x18000\n\t" \
    "s_mov_b32 s44, 0x480000\n\t" \
    "buffer_load_dwordx4 v240, %[wr], s44 offen offset:0 nt lds\n\t" \
    "buffer_load_dwordx4 v241, %[wr], s44 offen offset:1024 nt lds\n\t" \
    "buffer_load_dwordx4 v242, %[wr], s44 offen offset:2048 nt lds\n\t" \
    "ds_read_b64_tr_b16 v[212:213], v244 offset:0\n\t" \
    "ds_read_b64_tr_b16 v[214:215], v244 offset:1536\n\t" \
    "ds_read_b64_tr_b16 v[44:45], v244 offset:32\n\t" \
    "ds_read_b64_tr_b16 v[46:47], v244 offset:1568\n\t" \
    "ds_read_b64_tr_b16 v[52:53], v244 offset:64\n\t" \
    "ds_read_b64_tr_b16 v[54:55], v244 offset:1600\n\t" \
    "v_add_u32 v244, 0x0, v243\n\t" \
    "s_waitcnt vmcnt(12) lgkmcnt(0)\n\t" \
    "s_barrier \n\t" \
    "s_add_u32 m0, %[wm], 0x1e000\n\t" \
    "s_mov_b32 s44, 0x4e0000\n\t" \
    "buffer_load_dwordx4 v240, %[wr], s44 offen offset:0 nt lds\n\t" \
    "buffer_load_dwordx4 v241, %[wr], s44 offen offset:1024 nt lds\n\t" \
    "buffer_load_dwordx4 v242, %[wr], s44 offen offset:2048 nt lds\n\t" \
    "ds_read_b64_tr_b16 v[136:137], v244 offset:0\n\t" \
    "ds_read_b64_tr_b16 v[138:139], v244 offset:1536\n\t" \
    "ds_read_b64_tr_b16 v[144:145], v244 offset:32\n\t" \
    "ds_read_b64_tr_b16 v[146:147], v244 offset:1568\n\t" \
    "ds_read_b64_tr_b16 v[152:153], v244 offset:64\n\t" \
    "ds_read_b64_tr_b16 v[154:155], v244 offset:1600\n\t" \
    "v_add_u32 v244, 0x6000, v243\n\t" \
    "s_waitcnt vmcnt(12) lgkmcnt(0)\n\t" \
    "s_barrier \n\t" \
    "s_add_u32 m0, %[wm], 0x0\n\t" \
    "s_mov_b32 s44, 0x540000\n\t" \
    "buffer_load_dwordx4 v240, %[wr], s44 offen offset:0 nt lds\n\t" \
    "buffer_load_dwordx4 v241, %[wr], s44 offen offset:1024 nt lds\n\t" \
    "buffer_load_dwordx4 v242, %[wr], s44 offen offset:2048 nt lds\n\t" \
    "ds_read_b64_tr_b16 v[140:141], v244 offset:0\n\t" \
    "ds_read_b64_tr_b16 v[142:143], v244 offset:1536\n\t" \
    "ds_read_b64_tr_b16 v[148:149], v244 offset:32\n\t" \
    "ds_read_b64_tr_b16 v[150:151], v244 offset:1568\n\t" \
    "ds_read_b64_tr_b16 v[156:157], v244 offset:64\n\t" \
    "ds_read_b64_tr_b16 v[158:159], v244 offset:1600\n\t" \
    "v_add_u32 v244, 0xc000, v243\n\t" \
    "s_waitcnt vmcnt(12) lgkmcnt(0)\n\t" \
    "s_barrier \n\t" \
    "s_add_u32 m0, %[wm], 0x6000\n\t" \
    "s_mov_b32 s44, 0x5a0000\n\t" \
    "buffer_load_dwordx4 v240, %[wr], s44 offen offset:0 nt lds\n\t" \
    "buffer_load_dwordx4 v241, %[wr], s44 offen offset:1024 nt lds\n\t" \
    "buffer_load_dwordx4 v242, %[wr], s44 offen offset:2048 nt lds\n\t" \
    "ds_read_b64_tr_b16 v[216:217], v244 offset:0\n\t" \
    "ds_read_b64_tr_b16 v[218:219], v244 offset:1536\n\t" \
    "ds_read_b64_tr_b16 v[80:81], v244 offset:32\n\t" \
    "ds_read_b64_tr_b16 v[82:83], v244 offset:1568\n\t" \
    "ds_read_b64_tr_b16 v[88:89], v244 offset:64\n\t" \
    "ds_read_b64_tr_b16 v[90:91], v244 offset:1600\n\t" \
    "v_add_u32 v244, 0x12000, v243\n\t" \
    "s_waitcnt vmcnt(12) lgkmcnt(0)\n\t" \
    "s_barrier \n\t" \
    ".Ls11_%=:\n\t" \
    "v_readfirstlane_b32 s43, v246\n\t" \
    "s_nop 0\n\t" \
    "s_cmp_lg_u32 s43, -1\n\t" \
    "s_cbranch_scc1 .Ls12_%=\n\t" \
    "s_cmp_eq_u32 s41, 0\n\t" \
    "s_cbranch_scc1 .Ls12_%=\n\t" \
    "s_sub_u32 s41, s41, 1\n\t" \
    "s_sleep 4\n\t" \
    "v_mov_b32 v246, 0\n\t" \
    "buffer_load_dword v246, v246, s[60:63], %[mo] offen sc1\n\t" \
    "s_waitcnt vmcnt(0)\n\t" \
    "s_branch .Ls11_%=\n\t" \
    ".Ls12_%=:\n\t" \
    "v_mov_b32 v246, 0x8000\n\t" \
    "s_mov_b64 exec, 1\n\t" \
    "buffer_store_dword v246, v246, s[60:63], %[mo] offen sc1\n\t" \
    "s_mov_b64 exec, -1\n\t" \
    "s_and_b32 s43, s43, 0x2000\n\t" \
    "s_xor_b32 s43, s43, %[lx]\n\t" \
    "s_add_u32 s60, s60, s43\n\t" \
    "s_addc_u32 s61, s61, 0\n\t" \
    "s_add_u32 m0, %[wm], 0xc000\n\t" \
    "s_mov_b32 s44, 0x600000\n\t" \
    "buffer_load_dwordx4 v240, %[wr], s44 offen offset:0 nt lds\n\t" \
    "buffer_load_dwordx4 v241, %[wr], s44 offen offset:1024 nt lds\n\t" \
    "buffer_load_dwordx4 v242, %[wr], s44 offen offset:2048 nt lds\n\t" \
    "ds_read_b64_tr_b16 v[220:221], v244 offset:0\n\t" \
    "ds_read_b64_tr_b16 v[222:223], v244 offset:1536\n\t" \
    "ds_read_b64_tr_b16 v[84:85], v244 offset:32\n\t" \
    "ds_read_b64_tr_b16 v[86:87], v244 offset:1568\n\t" \
    "ds_read_b64_tr_b16 v[92:93], v244 offset:64\n\t" \
    "ds_read_b64_tr_b16 v[94:95], v244 offset:1600\n\t" \
    "v_add_u32 v244, 0x18000, v243\n\t" \
    "s_waitcnt vmcnt(12) lgkmcnt(0)\n\t" \
    "s_barrier \n\t" \
    "s_add_u32 m0, %[wm], 0x12000\n\t" \
    "s_mov_b32 s44, 0x660000\n\t" \
    "buffer_load_dwordx4 v240, %[wr], s44 offen offset:0 nt lds\n\t" \
    "buffer_load_dwordx4 v241, %[wr], s44 offen offset:1024 nt lds\n\t" \
    "buffer_load_dwordx4 v242, %[wr], s44 offen offset:2048 nt lds\n\t" \
    "ds_read_b64_tr_b16 v[16:17], v244 offset:0\n\t" \
    "ds_read_b64_tr_b16 v[18:19], v244 offset:1536\n\t" \
    "ds_read_b64_tr_b16 v[24:25], v244 offset:32\n\t" \
    "ds_read_b64_tr_b16 v[26:27], v244 offset:1568\n\t" \
    "ds_read_b64_tr_b16 v[32:33], v244 offset:64\n\t" \
    "ds_read_b64_tr_b16 v[34:35], v244 offset:1600\n\t" \
    "v_add_u32 v244, 0x1e000, v243\n\t" \
    "s_waitcnt vmcnt(12) lgkmcnt(0)\n\t" \
    "s_barrier \n\t" \
    "s_add_u32 m0, %[wm], 0x18000\n\t" \
    "s_mov_b32 s44, 0x6c0000\n\t" \
    "buffer_load_dwordx4 v240, %[wr], s44 offen offset:0 nt lds\n\t" \
    "buffer_load_dwordx4 v241, %[wr], s44 offen offset:1024 nt lds\n\t" \
    "buffer_load_dwordx4 v242, %[wr], s44 offen offset:2048 nt lds\n\t" \
    "ds_read_b64_tr_b16 v[20:21], v244 offset:0\n\t" \
    "ds_read_b64_tr_b16 v[22:23], v244 offset:1536\n\t" \
    "ds_read_b64_tr_b16 v[28:29], v244 offset:32\n\t" \
    "ds_read_b64_tr_b16 v[30:31], v244 offset:1568\n\t" \
    "ds_read_b64_tr_b16 v[36:37], v244 offset:64\n\t" \
    "ds_read_b64_tr_b16 v[38:39], v244 offset:1600\n\t" \
    "v_add_u32 v244, 0x0, v243\n\t" \
    "s_waitcnt vmcnt(12) lgkmcnt(0)\n\t" \
    "s_barrier \n\t" \
    "s_add_u32 m0, %[wm], 0x1e000\n\t" \
    "s_mov_b32 s44, 0x720000\n\t" \
    "buffer_load_dwordx4 v240, %[wr], s44 offen offset:0 nt lds\n\t" \
    "buffer_load_dwordx4 v241, %[wr], s44 offen offset:1024 nt lds\n\t" \
    "buffer_load_dwordx4 v242, %[wr], s44 offen offset:2048 nt lds\n\t" \
    "ds_read_b64_tr_b16 v[224:225], v244 offset:0\n\t" \
    "ds_read_b64_tr_b16 v[226:227], v244 offset:1536\n\t" \
    "ds_read_b64_tr_b16 v[120:121], v244 offset:32\n\t" \
    "ds_read_b64_tr_b16 v[122:123], v244 offset:1568\n\t" \
    "ds_read_b64_tr_b16 v[128:129], v244 offset:64\n\t" \
    "ds_read_b64_tr_b16 v[130:131], v244 offset:1600\n\t" \
    "v_add_u32 v244, 0x6000, v243\n\t" \
    "s_waitcnt vmcnt(12) lgkmcnt(0)\n\t" \
    "s_barrier \n\t" \
    "ds_read_b64_tr_b16 v[228:229], v244 offset:0\n\t" \
    "ds_read_b64_tr_b16 v[230:231], v244 offset:1536\n\t" \
    "ds_read_b64_tr_b16 v[124:125], v244 offset:32\n\t" \
    "ds_read_b64_tr_b16 v[126:127], v244 offset:1568\n\t" \
    "ds_read_b64_tr_b16 v[132:133], v244 offset:64\n\t" \
    "ds_read_b64_tr_b16 v[134:135], v244 offset:1600\n\t" \
    "v_add_u32 v244, 0xc000, v243\n\t" \
    "s_waitcnt vmcnt(9) lgkmcnt(0)\n\t" \
    "s_barrier \n\t" \
    "ds_read_b64_tr_b16 v[232:233], v244 offset:0\n\t" \
    "ds_read_b64_tr_b16 v[234:235], v244 offset:1536\n\t" \
    "ds_read_b64_tr_b16 v[160:161], v244 offset:32\n\t" \
    "ds_read_b64_tr_b16 v[162:163], v244 offset:1568\n\t" \
    "ds_read_b64_tr_b16 v[168:169], v244 offset:64\n\t" \
    "ds_read_b64_tr_b16 v[170:171], v244 offset:1600\n\t" \
    "v_add_u32 v244, 0x12000, v243\n\t" \
    "s_waitcnt vmcnt(6) lgkmcnt(0)\n\t" \
    "s_barrier \n\t" \
    "ds_read_b64_tr_b16 v[236:237], v244 offset:0\n\t" \
    "ds_read_b64_tr_b16 v[238:239], v244 offset:1536\n\t" \
    "ds_read_b64_tr_b16 v[164:165], v244 offset:32\n\t" \
    "ds_read_b64_tr_b16 v[166:167], v244 offset:1568\n\t" \
    "ds_read_b64_tr_b16 v[172:173], v244 offset:64\n\t" \
    "ds_read_b64_tr_b16 v[174:175], v244 offset:1600\n\t" \
    "v_add_u32 v244, 0x18000, v243\n\t" \
    "s_waitcnt vmcnt(3) lgkmcnt(0)\n\t" \
    "s_barrier \n\t" \
    "ds_read_b64_tr_b16 v[176:177], v244 offset:0\n\t" \
    "ds_read_b64_tr_b16 v[178:179], v244 offset:1536\n\t" \
    "ds_read_b64_tr_b16 v[184:185], v244 offset:32\n\t" \
    "ds_read_b64_tr_b16 v[186:187], v244 offset:1568\n\t" \
    "ds_read_b64_tr_b16 v[192:193], v244 offset:64\n\t" \
    "ds_read_b64_tr_b16 v[194:195], v244 offset:1600\n\t" \
    "v_add_u32 v244, 0x1e000, v243\n\t" \
    "s_waitcnt vmcnt(0) lgkmcnt(0)\n\t" \
    "s_barrier \n\t" \
    "ds_read_b64_tr_b16 v[180:181], v244 offset:0\n\t" \
    "ds_read_b64_tr_b16 v[182:183], v244 offset:1536\n\t" \
    "ds_read_b64_tr_b16 v[188:189], v244 offset:32\n\t" \
    "ds_read_b64_tr_b16 v[190:191], v244 offset:1568\n\t" \
    "ds_read_b64_tr_b16 v[196:197], v244 offset:64\n\t" \
    "ds_read_b64_tr_b16 v[198:199], v244 offset:1600\n\t" \
    "s_waitcnt lgkmcnt(0)\n\t" \
    "s_barrier \n\t" \
    LST0 \
    "ds_write_b128 v254, v[200:203] offset:40960\n\t" \
    "ds_write_b128 v254, v[204:207] offset:41984\n\t" \
    "ds_write_b128 v254, v[208:211] offset:43008\n\t" \
    "ds_write_b128 v254, v[212:215] offset:44032\n\t" \
    "ds_write_b128 v254, v[216:219] offset:45056\n\t" \
    "ds_write_b128 v254, v[220:223] offset:46080\n\t" \
    "ds_write_b128 v254, v[224:227] offset:47104\n\t" \
    "ds_write_b128 v254, v[228:231] offset:48128\n\t" \
    "ds_write_b128 v254, v[232:235] offset:49152\n\t" \
    "ds_write_b128 v254, v[236:239] offset:50176\n\t" \
    "s_waitcnt vmcnt(0) lgkmcnt(0)\n\t" \
    "s_barrier \n\t" \
    "ds_read_b128 v[224:227], v253 offset:0\n\t" \
    "ds_read_b128 v[244:247], v254 offset:40960\n\t" \
    "ds_read_b128 v[228:231], v253 offset:1024\n\t" \
    "ds_read_b128 v[232:235], v253 offset:20480\n\t" \
    "ds_read_b128 v[236:239], v253 offset:21504\n\t" \
    "ds_read_b128 v[248:251], v254 offset:41984\n\t" \
    ".Ls13_%=:\n\t" \
    "s_waitcnt lgkmcnt(5)\n\t" \
    "v_mfma_f32_16x16x32_bf16 v[204:207], v[64:67], v[224:227], 0\n\t" \
    "s_waitcnt lgkmcnt(3)\n\t" \
    "v_mfma_f32_16x16x32_bf16 v[204:207], v[68:71], v[228:231], v[204:207]\n\t" \
    "ds_read_b128 v[240:243], v253 offset:2048\n\t" \
    "v_mfma_f32_16x16x32_bf16 v[208:211], v[72:75], v[224:227], 0\n\t" \
    "v_mfma_f32_16x16x32_bf16 v[208:211], v[76:79], v[228:231], v[208:211]\n\t" \
    "v_mfma_f32_16x16x32_bf16 v[200:203], v[56:59], v[224:227], 0\n\t" \
    "v_mfma_f32_16x16x32_bf16 v[200:203], v[60:63], v[228:231], v[200:203]\n\t" \
    "s_waitcnt lgkmcnt(3)\n\t" \
    "v_mfma_f32_16x16x32_bf16 v[216:219], v[64:67], v[232:235], 0\n\t" \
    "ds_read_b128 v[224:227], v253 offset:3072\n\t" \
    "s_waitcnt lgkmcnt(3)\n\t" \
    "v_mfma_f32_16x16x32_bf16 v[216:219], v[68:71], v[236:239], v[216:219]\n\t" \
    "ds_read_b128 v[228:231], v253 offset:22528\n\t" \
    "v_mfma_f32_16x16x32_bf16 v[220:223], v[72:75], v[232:235], 0\n\t" \
    "v_mfma_f32_16x16x32_bf16 v[220:223], v[76:79], v[236:239], v[220:223]\n\t" \
    "v_mfma_f32_16x16x32_bf16 v[212:215], v[56:59], v[232:235], 0\n\t" \
    "v_mfma_f32_16x16x32_bf16 v[212:215], v[60:63], v[236:239], v[212:215]\n\t" \
    "s_waitcnt lgkmcnt(2)\n\t" \
    "v_mfma_f32_16x16x32_bf16 v[204:207], v[104:107], v[240:243], v[204:207]\n\t" \
    "ds_read_b128 v[232:235], v253 offset:23552\n\t" \
    "s_waitcnt lgkmcnt(2)\n\t" \
    "v_mfma_f32_16x16x32_bf16 v[204:207], v[108:111], v[224:227], v[204:207]\n\t" \
    "ds_read_b128 v[236:239], v253 offset:4096\n\t" \
    "v_mfma_f32_16x16x32_bf16 v[208:211], v[112:115], v[240:243], v[208:211]\n\t" \
    "v_mfma_f32_16x16x32_bf16 v[208:211], v[116:119], v[224:227], v[208:211]\n\t" \
    "v_mfma_f32_16x16x32_bf16 v[200:203], v[96:99], v[240:243], v[200:203]\n\t" \
    "v_mfma_f32_16x16x32_bf16 v[200:203], v[100:103], v[224:227], v[200:203]\n\t" \
    "s_waitcnt lgkmcnt(2)\n\t" \
    "v_mfma_f32_16x16x32_bf16 v[216:219], v[104:107], v[228:231], v[216:219]\n\t" \
    "ds_read_b128 v[240:243], v253 offset:5120\n\t" \
    "s_waitcnt lgkmcnt(2)\n\t" \
    "v_mfma_f32_16x16x32_bf16 v[216:219], v[108:111], v[232:235], v[216:219]\n\t" \
    "ds_read_b128 v[224:227], v253 offset:24576\n\t" \
    "v_mfma_f32_16x16x32_bf16 v[220:223], v[112:115], v[228:231], v[220:223]\n\t" \
    "v_mfma_f32_16x16x32_bf16 v[220:223], v[116:119], v[232:235], v[220:223]\n\t" \
    "v_mfma_f32_16x16x32_bf16 v[212:215], v[96:99], v[228:231], v[212:215]\n\t" \
    "v_mfma_f32_16x16x32_bf16 v[212:215], v[100:103], v[232:235], v[212:215]\n\t" \
    "s_waitcnt lgkmcnt(2)\n\t" \
    "v_mfma_f32_16x16x32_bf16 v[204:207], v[0:3], v[236:239], v[204:207]\n\t" \
    "ds_read_b128 v[228:231], v253 offset:25600\n\t" \
    "s_waitcnt lgkmcnt(2)\n\t" \
    "v_mfma_f32_16x16x32_bf16 v[204:207], v[4:7], v[240:243], v[204:207]\n\t" \
    "ds_read_b128 v[232:235], v253 offset:6144\n\t" \
    "v_mfma_f32_16x16x32_bf16 v[208:211], v[8:11], v[236:239], v[208:211]\n\t" \
    "v_mfma_f32_16x16x32_bf16 v[208:211], v[12:15], v[240:243], v[208:211]\n\t" \
    "v_mfma_f32_16x16x32_bf16 v[200:203], v[244:247], v[236:239], v[200:203]\n\t" \
    "v_mfma_f32_16x16x32_bf16 v[200:203], v[248:251], v[240:243], v[200:203]\n\t" \
    "s_waitcnt lgkmcnt(2)\n\t" \
    "v_mfma_f32_16x16x32_bf16 v[216:219], v[0:3], v[224:227], v[216:219]\n\t" \
    "ds_read_b128 v[236:239], v253 offset:7168\n\t" \
    "s_waitcnt lgkmcnt(2)\n\t" \
    "v_mfma_f32_16x16x32_bf16 v[216:219], v[4:7], v[228:231], v[216:219]\n\t" \
    "ds_read_b128 v[240:243], v253 offset:26624\n\t" \
    "v_mfma_f32_16x16x32_bf16 v[220:223], v[8:11], v[224:227], v[220:223]\n\t" \
    "v_mfma_f32_16x16x32_bf16 v[220:223], v[12:15], v[228:231], v[220:223]\n\t" \
    "v_mfma_f32_16x16x32_bf16 v[212:215], v[244:247], v[224:227], v[212:215]\n\t" \
    "ds_read_b128 v[244:247], v254 offset:43008\n\t" \
    "v_mfma_f32_16x16x32_bf16 v[212:215], v[248:251], v[228:231], v[212:215]\n\t" \
    "ds_read_b128 v[248:251], v254 offset:44032\n\t" \
    "s_waitcnt lgkmcnt(4)\n\t" \
    "v_mfma_f32_16x16x32_bf16 v[204:207], v[40:43], v[232:235], v[204:207]\n\t" \
    "ds_read_b128 v[224:227], v253 offset:27648\n\t" \
    "s_waitcnt lgkmcnt(4)\n\t" \
    "v_mfma_f32_16x16x32_bf16 v[204:207], v[44:47], v[236:239], v[204:207]\n\t" \
    "ds_read_b128 v[228:231], v253 offset:8192\n\t" \
    "v_mfma_f32_16x16x32_bf16 v[208:211], v[48:51], v[232:235], v[208:211]\n\t" \
    "v_mfma_f32_16x16x32_bf16 v[208:211], v[52:55], v[236:239], v[208:211]\n\t" \
    "s_waitcnt lgkmcnt(3)\n\t" \
    "v_mfma_f32_16x16x32_bf16 v[200:203], v[244:247], v[232:235], v[200:203]\n\t" \
    "s_waitcnt lgkmcnt(2)\n\t" \
    "v_mfma_f32_16x16x32_bf16 v[200:203], v[248:251], v[236:239], v[200:203]\n\t" \
    "v_mfma_f32_16x16x32_bf16 v[216:219], v[40:43], v[240:243], v[216:219]\n\t" \
    "ds_read_b128 v[232:235], v253 offset:9216\n\t" \
    "s_waitcnt lgkmcnt(2)\n\t" \
    "v_mfma_f32_16x16x32_bf16 v[216:219], v[44:47], v[224:227], v[216:219]\n\t" \
    "ds_read_b128 v[236:239], v253 offset:28672\n\t" \
    "v_mfma_f32_16x16x32_bf16 v[220:223], v[48:51], v[240:243], v[220:223]\n\t" \
    "v_mfma_f32_16x16x32_bf16 v[220:223], v[52:55], v[224:227], v[220:223]\n\t" \
    "v_mfma_f32_16x16x32_bf16 v[212:215], v[244:247], v[240:243], v[212:215]\n\t" \
    "ds_read_b128 v[244:247], v254 offset:45056\n\t" \
    "v_mfma_f32_16x16x32_bf16 v[212:215], v[248:251], v[224:227], v[212:215]\n\t" \
    "ds_read_b128 v[248:251], v254 offset:46080\n\t" \
    "s_waitcnt lgkmcnt(4)\n\t" \
    "v_mfma_f32_16x16x32_bf16 v[204:207], v[144:147], v[228:231], v[204:207]\n\t" \
    "ds_read_b128 v[240:243], v253 offset:29696\n\t" \
    "s_waitcnt lgkmcnt(4)\n\t" \
    "v_mfma_f32_16x16x32_bf16 v[204:207], v[148:151], v[232:235], v[204:207]\n\t" \
    "ds_read_b128 v[224:227], v253 offset:10240\n\t" \
    "v_mfma_f32_16x16x32_bf16 v[208:211], v[152:155], v[228:231], v[208:211]\n\t" \
    "v_mfma_f32_16x16x32_bf16 v[208:211], v[156:159], v[232:235], v[208:211]\n\t" \
    "v_mfma_f32_16x16x32_bf16 v[200:203], v[136:139], v[228:231], v[200:203]\n\t" \
    "v_mfma_f32_16x16x32_bf16 v[200:203], v[140:143], v[232:235], v[200:203]\n\t" \
    "s_waitcnt lgkmcnt(4)\n\t" \
    "v_mfma_f32_16x16x32_bf16 v[216:219], v[144:147], v[236:239], v[216:219]\n\t" \
    "ds_read_b128 v[228:231], v253 offset:11264\n\t" \
    "s_waitcnt lgkmcnt(2)\n\t" \
    "v_mfma_f32_16x16x32_bf16 v[216:219], v[148:151], v[240:243], v[216:219]\n\t" \
    "ds_read_b128 v[232:235], v253 offset:30720\n\t" \
    "v_mfma_f32_16x16x32_bf16 v[220:223], v[152:155], v[236:239], v[220:223]\n\t" \
    "v_mfma_f32_16x16x32_bf16 v[220:223], v[156:159], v[240:243], v[220:223]\n\t" \
    "v_mfma_f32_16x16x32_bf16 v[212:215], v[136:139], v[236:239], v[212:215]\n\t" \
    "v_mfma_f32_16x16x32_bf16 v[212:215], v[140:143], v[240:243], v[212:215]\n\t" \
    "ds_read_b128 v[236:239], v253 offset:31744\n\t" \
    "s_setprio 2\n\t" \
    "s_waitcnt lgkmcnt(0)\n\t" \
    "s_barrier \n\t" \
    LST7 \
    "v_mfma_f32_16x16x32_bf16 v[204:207], v[80:83], v[224:227], v[204:207]\n\t" \
    "v_mfma_f32_16x16x32_bf16 v[204:207], v[84:87], v[228:231], v[204:207]\n\t" \
    "ds_read_b128 v[240:243], v253 offset:14336\n\t" \
    "v_mfma_f32_16x16x32_bf16 v[208:211], v[88:91], v[224:227], v[208:211]\n\t" \
    "v_mfma_f32_16x16x32_bf16 v[208:211], v[92:95], v[228:231], v[208:211]\n\t" \
    "v_mfma_f32_16x16x32_bf16 v[200:203], v[244:247], v[224:227], v[200:203]\n\t" \
    "v_mfma_f32_16x16x32_bf16 v[200:203], v[248:251], v[228:231], v[200:203]\n\t" \
    "v_mfma_f32_16x16x32_bf16 v[216:219], v[80:83], v[232:235], v[216:219]\n\t" \
    "ds_read_b128 v[224:227], v253 offset:15360\n\t" \
    "s_and_b32 s44, s40, 3\n\t" \
    "v_mfma_f32_16x16x32_bf16 v[216:219], v[84:87], v[236:239], v[216:219]\n\t" \
    "ds_read_b128 v[228:231], v253 offset:34816\n\t" \
    "s_lshl_b32 s44, s44, 17\n\t" \
    "v_mfma_f32_16x16x32_bf16 v[220:223], v[88:91], v[232:235], v[220:223]\n\t" \
    "s_add_u32 s44, s44, %[mb]\n\t" \
    "v_mfma_f32_16x16x32_bf16 v[220:223], v[92:95], v[236:239], v[220:223]\n\t" \
    "s_add_u32 s46, s44, 0x800\n\t" \
    "v_mfma_f32_16x16x32_bf16 v[212:215], v[244:247], v[232:235], v[212:215]\n\t" \
    "ds_read_b128 v[244:247], v254 offset:47104\n\t" \
    "s_add_u32 s47, s44, 0x1800\n\t" \
    "v_mfma_f32_16x16x32_bf16 v[212:215], v[248:251], v[236:239], v[212:215]\n\t" \
    "ds_read_b128 v[248:251], v254 offset:48128\n\t" \
    "s_add_u32 s48, s44, 0xc00\n\t" \
    "s_waitcnt lgkmcnt(4)\n\t" \
    "v_mfma_f32_16x16x32_bf16 v[204:207], v[120:123], v[240:243], v[204:207]\n\t" \
    "ds_read_b128 v[232:235], v253 offset:35840\n\t" \
    "s_waitcnt lgkmcnt(4)\n\t" \
    "v_mfma_f32_16x16x32_bf16 v[204:207], v[124:127], v[224:227], v[204:207]\n\t" \
    "ds_read_b128 v[236:239], v253 offset:16384\n\t" \
    "v_mfma_f32_16x16x32_bf16 v[208:211], v[128:131], v[240:243], v[208:211]\n\t" \
    "v_mfma_f32_16x16x32_bf16 v[208:211], v[132:135], v[224:227], v[208:211]\n\t" \
    "s_waitcnt lgkmcnt(3)\n\t" \
    "v_mfma_f32_16x16x32_bf16 v[200:203], v[244:247], v[240:243], v[200:203]\n\t" \
    "s_waitcnt lgkmcnt(2)\n\t" \
    "v_mfma_f32_16x16x32_bf16 v[200:203], v[248:251], v[224:227], v[200:203]\n\t" \
    "v_mfma_f32_16x16x32_bf16 v[216:219], v[120:123], v[228:231], v[216:219]\n\t" \
    "ds_read_b128 v[240:243], v253 offset:17408\n\t" \
    "s_waitcnt lgkmcnt(2)\n\t" \
    "v_mfma_f32_16x16x32_bf16 v[216:219], v[124:127], v[232:235], v[216:219]\n\t" \
    "ds_read_b128 v[224:227], v253 offset:36864\n\t" \
    "v_mfma_f32_16x16x32_bf16 v[220:223], v[128:131], v[228:231], v[220:223]\n\t" \
    "v_mfma_f32_16x16x32_bf16 v[220:223], v[132:135], v[232:235], v[220:223]\n\t" \
    "v_mfma_f32_16x16x32_bf16 v[212:215], v[244:247], v[228:231], v[212:215]\n\t" \
    "ds_read_b128 v[244:247], v254 offset:49152\n\t" \
    "v_mfma_f32_16x16x32_bf16 v[212:215], v[248:251], v[232:235], v[212:215]\n\t" \
    "ds_read_b128 v[248:251], v254 offset:50176\n\t" \
    "s_waitcnt lgkmcnt(4)\n\t" \
    "v_mfma_f32_16x16x32_bf16 v[204:207], v[160:163], v[236:239], v[204:207]\n\t" \
    "ds_read_b128 v[228:231], v253 offset:37888\n\t" \
    "s_waitcnt lgkmcnt(4)\n\t" \
    "v_mfma_f32_16x16x32_bf16 v[204:207], v[164:167], v[240:243], v[204:207]\n\t" \
    "ds_read_b128 v[232:235], v253 offset:12288\n\t" \
    "v_mfma_f32_16x16x32_bf16 v[208:211], v[168:171], v[236:239], v[208:211]\n\t" \
    "v_mfma_f32_16x16x32_bf16 v[208:211], v[172:175], v[240:243], v[208:211]\n\t" \
    "s_waitcnt lgkmcnt(3)\n\t" \
    "v_mfma_f32_16x16x32_bf16 v[200:203], v[244:247], v[236:239], v[200:203]\n\t" \
    "s_waitcnt lgkmcnt(2)\n\t" \
    "v_mfma_f32_16x16x32_bf16 v[200:203], v[248:251], v[240:243], v[200:203]\n\t" \
    "v_mfma_f32_16x16x32_bf16 v[216:219], v[160:163], v[224:227], v[216:219]\n\t" \
    "ds_read_b128 v[236:239], v253 offset:13312\n\t" \
    "s_waitcnt lgkmcnt(2)\n\t" \
    "v_mfma_f32_16x16x32_bf16 v[216:219], v[164:167], v[228:231], v[216:219]\n\t" \
    "ds_read_b128 v[240:243], v253 offset:18432\n\t" \
    "v_mfma_f32_16x16x32_bf16 v[220:223], v[168:171], v[224:227], v[220:223]\n\t" \
    "v_mfma_f32_16x16x32_bf16 v[220:223], v[172:175], v[228:231], v[220:223]\n\t" \
    "v_mfma_f32_16x16x32_bf16 v[212:215], v[244:247], v[224:227], v[212:215]\n\t" \
    "v_mfma_f32_16x16x32_bf16 v[212:215], v[248:251], v[228:231], v[212:215]\n\t" \
    "s_waitcnt lgkmcnt(2)\n\t" \
    "v_mfma_f32_16x16x32_bf16 v[204:207], v[24:27], v[232:235], v[204:207]\n\t" \
    "ds_read_b128 v[224:227], v253 offset:19456\n\t" \
    "v_mov_b64 v[244:245], 0\n\t" \
    "s_waitcnt lgkmcnt(2)\n\t" \
    "v_mfma_f32_16x16x32_bf16 v[204:207], v[28:31], v[236:239], v[204:207]\n\t" \
    "ds_read_b128 v[228:231], v253 offset:32768\n\t" \
    "v_mov_b64 v[246:247], 0\n\t" \
    "v_mfma_f32_16x16x32_bf16 v[208:211], v[32:35], v[232:235], v[208:211]\n\t" \
    "v_mov_b64 v[248:249], 0\n\t" \
    "v_mfma_f32_16x16x32_bf16 v[208:211], v[36:39], v[236:239], v[208:211]\n\t" \
    "v_mov_b64 v[250:251], 0\n\t" \
    "v_mfma_f32_16x16x32_bf16 v[200:203], v[16:19], v[232:235], v[200:203]\n\t" \
    "v_mfma_f32_16x16x32_bf16 v[200:203], v[20:23], v[236:239], v[200:203]\n\t" \
    "s_waitcnt lgkmcnt(2)\n\t" \
    "v_mfma_f32_16x16x32_bf16 v[204:207], v[184:187], v[240:243], v[204:207]\n\t" \
    "ds_read_b128 v[232:235], v253 offset:33792\n\t" \
    "s_waitcnt lgkmcnt(2)\n\t" \
    "v_mfma_f32_16x16x32_bf16 v[204:207], v[188:191], v[224:227], v[204:207]\n\t" \
    "ds_read_b128 v[236:239], v253 offset:38912\n\t" \
    "v_mfma_f32_16x16x32_bf16 v[208:211], v[192:195], v[240:243], v[208:211]\n\t" \
    "v_mfma_f32_16x16x32_bf16 v[208:211], v[196:199], v[224:227], v[208:211]\n\t" \
    "v_mfma_f32_16x16x32_bf16 v[200:203], v[176:179], v[240:243], v[200:203]\n\t" \
    "v_mfma_f32_16x16x32_bf16 v[200:203], v[180:183], v[224:227], v[200:203]\n\t" \
    "ds_read_b128 v[240:243], v253 offset:39936\n\t" \
    "s_waitcnt lgkmcnt(3)\n\t" \
    "v_mfma_f32_16x16x32_bf16 v[216:219], v[24:27], v[228:231], v[216:219]\n\t" \
    "s_waitcnt lgkmcnt(2)\n\t" \
    "v_mfma_f32_16x16x32_bf16 v[216:219], v[28:31], v[232:235], v[216:219]\n\t" \
    "v_cvt_pk_bf16_f32 v204, v204, v208\n\t" \
    "v_cvt_pk_bf16_f32 v205, v205, v209\n\t" \
    "s_waitcnt lgkmcnt(1)\n\t" \
    "v_mfma_f32_16x16x32_bf16 v[216:219], v[184:187], v[236:239], v[216:219]\n\t" \
    "v_cvt_pk_bf16_f32 v206, v206, v210\n\t" \
    "v_cvt_pk_bf16_f32 v207, v207, v211\n\t" \
    "s_waitcnt lgkmcnt(0)\n\t" \
    "v_mfma_f32_16x16x32_bf16 v[216:219], v[188:191], v[240:243], v[216:219]\n\t" \
    "ds_write_b128 v254, v[204:207] offset:9216\n\t" \
    "v_mfma_f32_16x16x32_bf16 v[220:223], v[32:35], v[228:231], v[220:223]\n\t" \
    "v_mfma_f32_16x16x32_bf16 v[220:223], v[36:39], v[232:235], v[220:223]\n\t" \
    "v_cvt_pk_bf16_f32 v200, v200, v201\n\t" \
    "v_cvt_pk_bf16_f32 v201, v202, v203\n\t" \
    "v_mfma_f32_16x16x32_bf16 v[220:223], v[192:195], v[236:239], v[220:223]\n\t" \
    "ds_write_b64 v254, v[200:201] offset:7168\n\t" \
    "v_mfma_f32_16x16x32_bf16 v[220:223], v[196:199], v[240:243], v[220:223]\n\t" \
    "v_mfma_f32_16x16x32_bf16 v[212:215], v[16:19], v[228:231], v[212:215]\n\t" \
    "ds_read_b128 v[200:203], v254 offset:40960\n\t" \
    "ds_read_b128 v[204:207], v254 offset:41984\n\t" \
    "v_mfma_f32_16x16x32_bf16 v[212:215], v[20:23], v[232:235], v[212:215]\n\t" \
    "v_mov_b64 v[208:209], 0\n\t" \
    "v_mov_b64 v[210:211], 0\n\t" \
    "v_mfma_f32_16x16x32_bf16 v[212:215], v[176:179], v[236:239], v[212:215]\n\t" \
    "v_cvt_pk_bf16_f32 v216, v216, v220\n\t" \
    "v_cvt_pk_bf16_f32 v217, v217, v221\n\t" \
    "v_mfma_f32_16x16x32_bf16 v[212:215], v[180:183], v[240:243], v[212:215]\n\t" \
    "v_cvt_pk_bf16_f32 v218, v218, v222\n\t" \
    "v_cvt_pk_bf16_f32 v219, v219, v223\n\t" \
    "s_setprio 0\n\t" \
    "ds_write_b128 v254, v[216:219] offset:15360\n\t" \
    "ds_read_b128 v[216:219], v254 offset:43008\n\t" \
    "ds_read_b128 v[220:223], v254 offset:44032\n\t" \
    "s_nop 2\n\t" \
    "v_cvt_pk_bf16_f32 v212, v212, v213\n\t" \
    "v_cvt_pk_bf16_f32 v213, v214, v215\n\t" \
    "ds_write_b64 v254, v[212:213] offset:13312\n\t" \
    LST6 \
    "s_sleep 3\n\t" \
    "s_mov_b32 m0, 0xa800\n\t" \
    "s_nop 0\n\t" \
    "buffer_load_dwordx4 v253, s[60:63], s46 offen sc1 lds\n\t" \
    "s_mov_b32 m0, 0xb800\n\t" \
    "s_nop 0\n\t" \
    "buffer_load_dwordx4 v253, s[60:63], s47 offen sc1 lds\n\t" \
    "s_mov_b32 m0, 0xac00\n\t" \
    "s_nop 0\n\t" \
    "buffer_load_dwordx4 v253, s[60:63], s48 offen sc1 lds\n\t" \
    "s_waitcnt lgkmcnt(0)\n\t" \
    LFLUSH212 \
    "v_bfe_i32 v212, v253, 5, 1\n\t" \
    "v_and_b32 v212, 0xaaaa, v212\n\t" \
    "v_or_b32 v212, 0x4444, v212\n\t" \
    "v_and_b32 v213, 0xffff, v255\n\t" \
    "v_lshrrev_b32 v214, 16, v255\n\t" \
    ".Ls14_%=:\n\t" \
    "s_waitcnt vmcnt(0)\n\t" \
    "ds_read_b128 v[224:227], v253 offset:43008\n\t" \
    "ds_read_b128 v[228:231], v253 offset:47104\n\t" \
    "ds_read_b128 v[232:235], v253 offset:44032\n\t" \
    "s_waitcnt lgkmcnt(2)\n\t" \
    "v_max_u32 v215, v224, v225\n\t" \
    "v_max3_u32 v215, v226, v227, v215\n\t" \
    "s_waitcnt lgkmcnt(1)\n\t" \
    "v_max3_u32 v215, v228, v229, v215\n\t" \
    "v_max3_u32 v215, v230, v231, v215\n\t" \
    "s_waitcnt lgkmcnt(0)\n\t" \
    "v_max3_u32 v215, v232, v233, v215\n\t" \
    "v_max3_u32 v215, v234, v235, v215\n\t" \
    "v_cmp_eq_u32 vcc, -1, v215\n\t" \
    "s_cbranch_vccz .Ls15_%=\n\t" \
    "v_max_u32 v215, v224, v225\n\t" \
    "v_max3_u32 v215, v226, v227, v215\n\t" \
    "v_cmp_eq_u32 vcc, -1, v215\n\t" \
    "s_cbranch_vccz .Ls16_%=\n\t" \
    "s_mov_b32 m0, 0xa800\n\t" \
    "s_nop 0\n\t" \
    "buffer_load_dwordx4 v253, s[60:63], s46 offen sc1 lds\n\t" \
    ".Ls16_%=:\n\t" \
    "v_max_u32 v215, v228, v229\n\t" \
    "v_max3_u32 v215, v230, v231, v215\n\t" \
    "v_cmp_eq_u32 vcc, -1, v215\n\t" \
    "s_cbranch_vccz .Ls17_%=\n\t" \
    "s_mov_b32 m0, 0xb800\n\t" \
    "s_nop 0\n\t" \
    "buffer_load_dwordx4 v253, s[60:63], s47 offen sc1 lds\n\t" \
    ".Ls17_%=:\n\t" \
    "v_max_u32 v215, v232, v233\n\t" \
    "v_max3_u32 v215, v234, v235, v215\n\t" \
    "v_cmp_eq_u32 vcc, -1, v215\n\t" \
    "s_cbranch_vccz .Ls18_%=\n\t" \
    "s_mov_b32 m0, 0xac00\n\t" \
    "s_nop 0\n\t" \
    "buffer_load_dwordx4 v253, s[60:63], s48 offen sc1 lds\n\t" \
    ".Ls18_%=:\n\t" \
    "s_cmp_eq_u32 s41, 0\n\t" \
    "s_cbranch_scc1 .Ls15_%=\n\t" \
    "s_sub_u32 s41, s41, 1\n\t" \
    "s_branch .Ls14_%=\n\t" \
    ".Ls15_%=:\n\t" \
    LST2 \
    "s_setprio 3\n\t" \
    "s_waitcnt lgkmcnt(0)\n\t" \
    "s_barrier \n\t" \
    LST3 \
    "ds_read_b128 v[236:239], v253 offset:40960\n\t" \
    "ds_read_b128 v[240:243], v253 offset:46080\n\t" \
    "v_smfmac_f32_16x16x64_bf16 v[244:247], v[224:227], v[0:7], v212\n\t" \
    "v_smfmac_f32_16x16x64_bf16 v[248:251], v[224:227], v[8:15], v212\n\t" \
    "v_smfmac_f32_16x16x64_bf16 v[208:211], v[224:227], v[200:207], v212\n\t" \
    "ds_read_b128 v[200:203], v254 offset:45056\n\t" \
    "ds_read_b128 v[204:207], v254 offset:46080\n\t" \
    "ds_read_b128 v[224:227], v253 offset:41984\n\t" \
    "v_smfmac_f32_16x16x64_bf16 v[208:211], v[228:231], v[16:23], v212\n\t" \
    "v_smfmac_f32_16x16x64_bf16 v[244:247], v[228:231], v[24:31], v212\n\t" \
    "v_smfmac_f32_16x16x64_bf16 v[248:251], v[228:231], v[32:39], v212\n\t" \
    "ds_read_b128 v[228:231], v253 offset:48128\n\t" \
    "v_smfmac_f32_16x16x64_bf16 v[208:211], v[232:235], v[216:223], v212\n\t" \
    "ds_read_b128 v[216:219], v254 offset:47104\n\t" \
    "ds_read_b128 v[220:223], v254 offset:48128\n\t" \
    "v_smfmac_f32_16x16x64_bf16 v[244:247], v[232:235], v[40:47], v212\n\t" \
    "v_smfmac_f32_16x16x64_bf16 v[248:251], v[232:235], v[48:55], v212\n\t" \
    "ds_read_b128 v[232:235], v253 offset:45056\n\t" \
    "s_waitcnt lgkmcnt(8)\n\t" \
    "v_smfmac_f32_16x16x64_bf16 v[208:211], v[236:239], v[56:63], v212\n\t" \
    "v_smfmac_f32_16x16x64_bf16 v[244:247], v[236:239], v[64:71], v212\n\t" \
    "v_smfmac_f32_16x16x64_bf16 v[248:251], v[236:239], v[72:79], v212\n\t" \
    "ds_read_b128 v[236:239], v253 offset:49152\n\t" \
    "s_waitcnt lgkmcnt(6)\n\t" \
    "v_smfmac_f32_16x16x64_bf16 v[208:211], v[240:243], v[200:207], v212\n\t" \
    "ds_read_b128 v[200:203], v254 offset:49152\n\t" \
    "ds_read_b128 v[204:207], v254 offset:50176\n\t" \
    "v_smfmac_f32_16x16x64_bf16 v[244:247], v[240:243], v[80:87], v212\n\t" \
    "v_smfmac_f32_16x16x64_bf16 v[248:251], v[240:243], v[88:95], v212\n\t" \
    "ds_read_b128 v[240:243], v253 offset:50176\n\t" \
    "s_waitcnt lgkmcnt(8)\n\t" \
    "v_smfmac_f32_16x16x64_bf16 v[208:211], v[224:227], v[96:103], v212\n\t" \
    "v_smfmac_f32_16x16x64_bf16 v[244:247], v[224:227], v[104:111], v212\n\t" \
    "v_smfmac_f32_16x16x64_bf16 v[248:251], v[224:227], v[112:119], v212\n\t" \
    "s_waitcnt lgkmcnt(5)\n\t" \
    "v_smfmac_f32_16x16x64_bf16 v[208:211], v[228:231], v[216:223], v212\n\t" \
    "s_waitcnt lgkmcnt(4)\n\t" \
    "v_smfmac_f32_16x16x64_bf16 v[208:211], v[232:235], v[136:143], v212\n\t" \
    "s_waitcnt lgkmcnt(1)\n\t" \
    "v_smfmac_f32_16x16x64_bf16 v[208:211], v[236:239], v[200:207], v212\n\t" \
    "s_waitcnt lgkmcnt(0)\n\t" \
    "v_smfmac_f32_16x16x64_bf16 v[208:211], v[240:243], v[176:183], v212\n\t" \
    "v_smfmac_f32_16x16x64_bf16 v[244:247], v[228:231], v[120:127], v212\n\t" \
    "v_smfmac_f32_16x16x64_bf16 v[244:247], v[232:235], v[144:151], v212\n\t" \
    "v_smfmac_f32_16x16x64_bf16 v[244:247], v[236:239], v[160:167], v212\n\t" \
    "v_add_f32 v208, v208, v210\n\t" \
    "v_add_f32 v209, v209, v211\n\t" \
    "v_cvt_pk_bf16_f32 v208, v208, v209\n\t" \
    "v_smfmac_f32_16x16x64_bf16 v[244:247], v[240:243], v[184:191], v212\n\t" \
    "ds_write_b16 v213, v208 offset:0\n\t" \
    "ds_write_b16_d16_hi v213, v208 offset:16\n\t" \
    "v_smfmac_f32_16x16x64_bf16 v[248:251], v[228:231], v[128:135], v212\n\t" \
    "v_smfmac_f32_16x16x64_bf16 v[248:251], v[232:235], v[152:159], v212\n\t" \
    "v_smfmac_f32_16x16x64_bf16 v[248:251], v[236:239], v[168:175], v212\n\t" \
    "v_add_f32 v244, v244, v246\n\t" \
    "v_add_f32 v245, v245, v247\n\t" \
    "v_smfmac_f32_16x16x64_bf16 v[248:251], v[240:243], v[192:199], v212\n\t" \
    "s_nop 7\n\t" \
    "v_add_f32 v248, v248, v250\n\t" \
    "v_add_f32 v249, v249, v251\n\t" \
    "v_cvt_pk_bf16_f32 v244, v244, v248\n\t" \
    "v_cvt_pk_bf16_f32 v245, v245, v249\n\t" \
    "ds_write2_b32 v214, v244, v245 offset1:4\n\t" \
    "s_setprio 0\n\t" \
    "s_sleep 1\n\t" \
    "ds_read_b128 v[200:203], v254 offset:6144\n\t" \
    "ds_read_b128 v[224:227], v254 offset:18432\n\t" \
    "ds_read_b128 v[204:207], v254 offset:7168\n\t" \
    "ds_read_b128 v[228:231], v254 offset:19456\n\t" \
    "ds_read_b128 v[208:211], v254 offset:8192\n\t" \
    "ds_read_b128 v[232:235], v254 offset:20480\n\t" \
    "ds_read_b128 v[212:215], v254 offset:9216\n\t" \
    "ds_read_b128 v[236:239], v254 offset:21504\n\t" \
    "ds_read_b128 v[216:219], v254 offset:10240\n\t" \
    "ds_read_b128 v[220:223], v254 offset:11264\n\t" \
    "s_and_b32 s44, s40, 3\n\t" \
    "s_lshl_b32 s44, s44, 17\n\t" \
    "s_add_u32 s50, s44, %[pb]\n\t" \
    "s_add_u32 s51, s44, %[pm]\n\t" \
    "s_mov_b32 s52, 0xf0000\n\t" \
    "s_mov_b32 s53, 0xfff00ff\n\t" \
    "s_mov_b32 s54, 0x1f0001\n\t" \
    "s_mov_b32 s55, 0x1fff01ff\n\t" \
    "s_mov_b32 s56, 0x3f0003\n\t" \
    "s_mov_b32 s57, 0x3fff03ff\n\t" \
    "s_mov_b32 s58, 0x7f0007\n\t" \
    "s_mov_b32 s59, 0x7fff07ff\n\t" \
    "s_waitcnt lgkmcnt(8)\n\t" \
    "v_mfma_f32_16x16x32_bf16 v[240:243], v[224:227], v[200:203], 0\n\t" \
    "ds_read_b128 v[224:227], v254 offset:22528\n\t" \
    "s_waitcnt lgkmcnt(7)\n\t" \
    "v_mfma_f32_16x16x32_bf16 v[240:243], v[228:231], v[204:207], v[240:243]\n\t" \
    "ds_read_b128 v[228:231], v254 offset:23552\n\t" \
    "s_waitcnt lgkmcnt(6)\n\t" \
    "v_mfma_f32_16x16x32_bf16 v[240:243], v[232:235], v[208:211], v[240:243]\n\t" \
    "s_waitcnt lgkmcnt(4)\n\t" \
    "v_mfma_f32_16x16x32_bf16 v[240:243], v[236:239], v[212:215], v[240:243]\n\t" \
    "v_and_b32 v252, 0xffffff7f, v254\n\t" \
    "s_waitcnt lgkmcnt(0)\n\t" \
    "s_barrier \n\t" \
    LST4 \
    "ds_read_b128 v[232:235], v252 offset:3072\n\t" \
    "ds_read_b128 v[236:239], v252 offset:3200\n\t" \
    "v_mfma_f32_16x16x32_bf16 v[240:243], v[224:227], v[216:219], v[240:243]\n\t" \
    "ds_read_b128 v[224:227], v252 offset:4096\n\t" \
    "v_mfma_f32_16x16x32_bf16 v[240:243], v[228:231], v[220:223], v[240:243]\n\t" \
    "ds_read_b128 v[228:231], v252 offset:4224\n\t" \
    "s_waitcnt lgkmcnt(3)\n\t" \
    "v_mfma_f32_16x16x32_bf16 v[248:251], v[232:235], v[200:203], 0\n\t" \
    "ds_read_b128 v[232:235], v252 offset:5120\n\t" \
    "s_waitcnt lgkmcnt(3)\n\t" \
    "v_mfma_f32_16x16x32_bf16 v[248:251], v[236:239], v[204:207], v[248:251]\n\t" \
    "ds_read_b128 v[236:239], v252 offset:5248\n\t" \
    "s_waitcnt lgkmcnt(3)\n\t" \
    "v_mfma_f32_16x16x32_bf16 v[248:251], v[224:227], v[208:211], v[248:251]\n\t" \
    "s_waitcnt lgkmcnt(2)\n\t" \
    "v_mfma_f32_16x16x32_bf16 v[248:251], v[228:231], v[212:215], v[248:251]\n\t" \
    "s_waitcnt lgkmcnt(1)\n\t" \
    "v_mfma_f32_16x16x32_bf16 v[248:251], v[232:235], v[216:219], v[248:251]\n\t" \
    "s_waitcnt lgkmcnt(0)\n\t" \
    "v_mfma_f32_16x16x32_bf16 v[248:251], v[236:239], v[220:223], v[248:251]\n\t" \
    "v_mov_b32 v226, 0xff7fffff\n\t" \
    "v_cndmask_b32 v240, v240, v226, s[52:53]\n\t" \
    "v_cndmask_b32 v241, v241, v226, s[54:55]\n\t" \
    "v_cndmask_b32 v242, v242, v226, s[56:57]\n\t" \
    "v_cndmask_b32 v243, v243, v226, s[58:59]\n\t" \
    "v_max3_f32 v224, v240, v241, v242\n\t" \
    "v_mov_b32 v225, v243\n\t" \
    "v_lshrrev_b32 v200, 5, v253\n\t" \
    "v_and_b32 v201, 0xf0, v253\n\t" \
    "v_and_or_b32 v200, v200, 8, v201\n\t" \
    "v_max3_f32 v224, v224, v248, v249\n\t" \
    "v_max3_f32 v225, v225, v250, v251\n\t" \
    "v_max_f32 v224, v224, v225\n\t" \
    "v_mov_b32 v225, v224\n\t" \
    "s_nop 1\n\t" \
    "v_permlane16_swap_b32 v224, v225\n\t" \
    "v_max_f32 v224, v224, v225\n\t" \
    "v_mov_b32 v225, v224\n\t" \
    "s_nop 1\n\t" \
    "v_permlane32_swap_b32 v224, v225\n\t" \
    "v_max_f32 v224, v224, v225\n\t" \
    "v_mul_f32 v224, %[scl], v224\n\t" \
    "v_fma_f32 v240, v240, %[scl], -v224\n\t" \
    "v_fma_f32 v241, v241, %[scl], -v224\n\t" \
    "v_exp_f32 v240, v240\n\t" \
    "v_fma_f32 v242, v242, %[scl], -v224\n\t" \
    "v_exp_f32 v241, v241\n\t" \
    "v_fma_f32 v243, v243, %[scl], -v224\n\t" \
    "v_exp_f32 v242, v242\n\t" \
    "v_fma_f32 v248, v248, %[scl], -v224\n\t" \
    "v_exp_f32 v243, v243\n\t" \
    "v_fma_f32 v249, v249, %[scl], -v224\n\t" \
    "v_exp_f32 v248, v248\n\t" \
    "v_fma_f32 v250, v250, %[scl], -v224\n\t" \
    "v_exp_f32 v249, v249\n\t" \
    "v_fma_f32 v251, v251, %[scl], -v224\n\t" \
    "v_exp_f32 v250, v250\n\t" \
    "v_exp_f32 v251, v251\n\t" \
    "v_add_f32 v224, v240, v241\n\t" \
    "v_add_f32 v225, v242, v243\n\t" \
    "v_add_f32 v232, v248, v249\n\t" \
    "v_add_f32 v233, v250, v251\n\t" \
    "v_add_f32 v224, v224, v225\n\t" \
    "v_add_f32 v232, v232, v233\n\t" \
    "v_fma_f32 v224, v232, 0.5, v224\n\t" \
    "v_mov_b32 v225, v224\n\t" \
    "s_nop 1\n\t" \
    "v_permlane16_swap_b32 v224, v225\n\t" \
    "v_add_f32 v224, v224, v225\n\t" \
    "v_mov_b32 v225, v224\n\t" \
    "s_nop 1\n\t" \
    "v_permlane32_swap_b32 v224, v225\n\t" \
    "v_add_f32 v224, v224, v225\n\t" \
    "v_rcp_f32 v224, v224\n\t" \
    "s_nop 0\n\t" \
    "v_mul_f32 v240, v240, v224\n\t" \
    "v_mul_f32 v241, v241, v224\n\t" \
    "v_mul_f32 v242, v242, v224\n\t" \
    "v_mul_f32 v243, v243, v224\n\t" \
    "v_mul_f32 v248, v248, v224\n\t" \
    "v_mul_f32 v249, v249, v224\n\t" \
    "v_mul_f32 v250, v250, v224\n\t" \
    "v_mul_f32 v251, v251, v224\n\t" \
    "v_cvt_pk_bf16_f32 v204, |v240|, |v241|\n\t" \
    "v_cvt_pk_bf16_f32 v205, |v242|, |v243|\n\t" \
    "v_cvt_pk_bf16_f32 v208, |v248|, |v249|\n\t" \
    "v_cvt_pk_bf16_f32 v209, |v250|, |v251|\n\t" \
    "v_permlane16_swap_b32 v204, v206\n\t" \
    "v_permlane16_swap_b32 v205, v207\n\t" \
    "s_mov_b32 exec_lo, 0xffff\n\t" \
    "s_mov_b32 exec_hi, 0xffff\n\t" \
    "buffer_store_dwordx4 v[204:207], v253, s[60:63], s50 offen sc1\n\t" \
    "s_mov_b32 exec_lo, -1\n\t" \
    "s_mov_b32 exec_hi, 0\n\t" \
    "buffer_store_dwordx2 v[208:209], v200, s[60:63], s51 offen sc1\n\t" \
    "s_mov_b32 exec_hi, -1\n\t" \
    LST5 \
    "ds_read_b128 v[224:227], v253 offset:0\n\t" \
    "ds_read_b128 v[244:247], v254 offset:40960\n\t" \
    "ds_read_b128 v[228:231], v253 offset:1024\n\t" \
    "ds_read_b128 v[232:235], v253 offset:20480\n\t" \
    "ds_read_b128 v[236:239], v253 offset:21504\n\t" \
    "ds_read_b128 v[248:251], v254 offset:41984\n\t" \
    "s_add_u32 s40, s40, 1\n\t" \
    "s_cmp_lg_u32 s40, 64\n\t" \
    "s_cbranch_scc1 .Ls13_%=\n\t" \
    "s_branch .Lend%=\n\t" \
    ".Ls0_%=:\n\t" \
    "s_add_u32 m0, %[wm], 0x18000\n\t" \
    "s_mov_b32 s44, 0x0\n\t" \
    "buffer_load_dwordx4 v240, %[wr], s44 offen offset:0 nt lds\n\t" \
    "buffer_load_dwordx4 v241, %[wr], s44 offen offset:1024 nt lds\n\t" \
    "buffer_load_dwordx4 v242, %[wr], s44 offen offset:2048 nt lds\n\t" \
    "s_add_u32 m0, %[wm], 0x1e000\n\t" \
    "s_mov_b32 s44, 0x60000\n\t" \
    "buffer_load_dwordx4 v240, %[wr], s44 offen offset:0 nt lds\n\t" \
    "buffer_load_dwordx4 v241, %[wr], s44 offen offset:1024 nt lds\n\t" \
    "buffer_load_dwordx4 v242, %[wr], s44 offen offset:2048 nt lds\n\t" \
    "s_add_u32 m0, %[wm], 0x0\n\t" \
    "s_mov_b32 s44, 0xc0000\n\t" \
    "buffer_load_dwordx4 v240, %[wr], s44 offen offset:0 nt lds\n\t" \
    "buffer_load_dwordx4 v241, %[wr], s44 offen offset:1024 nt lds\n\t" \
    "buffer_load_dwordx4 v242, %[wr], s44 offen offset:2048 nt lds\n\t" \
    "s_add_u32 m0, %[wm], 0x6000\n\t" \
    "s_mov_b32 s44, 0x120000\n\t" \
    "buffer_load_dwordx4 v240, %[wr], s44 offen offset:0 nt lds\n\t" \
    "buffer_load_dwordx4 v241, %[wr], s44 offen offset:1024 nt lds\n\t" \
    "buffer_load_dwordx4 v242, %[wr], s44 offen offset:2048 nt lds\n\t" \
    "s_add_u32 m0, %[wm], 0xc000\n\t" \
    "s_mov_b32 s44, 0x180000\n\t" \
    "buffer_load_dwordx4 v240, %[wr], s44 offen offset:0 nt lds\n\t" \
    "buffer_load_dwordx4 v241, %[wr], s44 offen offset:1024 nt lds\n\t" \
    "buffer_load_dwordx4 v242, %[wr], s44 offen offset:2048 nt lds\n\t" \
    "v_add_u32 v244, 0x18000, v243\n\t" \
    "s_waitcnt vmcnt(12) lgkmcnt(0)\n\t" \
    "s_barrier \n\t" \
    "s_add_u32 m0, %[wm], 0x12000\n\t" \
    "s_mov_b32 s44, 0x1e0000\n\t" \
    "buffer_load_dwordx4 v240, %[wr], s44 offen offset:0 nt lds\n\t" \
    "buffer_load_dwordx4 v241, %[wr], s44 offen offset:1024 nt lds\n\t" \
    "buffer_load_dwordx4 v242, %[wr], s44 offen offset:2048 nt lds\n\t" \
    "ds_read_b64_tr_b16 v[200:201], v244 offset:0\n\t" \
    "ds_read_b64_tr_b16 v[202:203], v244 offset:1536\n\t" \
    "ds_read_b64_tr_b16 v[0:1], v244 offset:32\n\t" \
    "ds_read_b64_tr_b16 v[2:3], v244 offset:1568\n\t" \
    "ds_read_b64_tr_b16 v[8:9], v244 offset:64\n\t" \
    "ds_read_b64_tr_b16 v[10:11], v244 offset:1600\n\t" \
    "v_add_u32 v244, 0x1e000, v243\n\t" \
    "s_waitcnt vmcnt(12) lgkmcnt(0)\n\t" \
    "s_barrier \n\t" \
    "s_add_u32 m0, %[wm], 0x18000\n\t" \
    "s_mov_b32 s44, 0x240000\n\t" \
    "buffer_load_dwordx4 v240, %[wr], s44 offen offset:0 nt lds\n\t" \
    "buffer_load_dwordx4 v241, %[wr], s44 offen offset:1024 nt lds\n\t" \
    "buffer_load_dwordx4 v242, %[wr], s44 offen offset:2048 nt lds\n\t" \
    "ds_read_b64_tr_b16 v[204:205], v244 offset:0\n\t" \
    "ds_read_b64_tr_b16 v[206:207], v244 offset:1536\n\t" \
    "ds_read_b64_tr_b16 v[4:5], v244 offset:32\n\t" \
    "ds_read_b64_tr_b16 v[6:7], v244 offset:1568\n\t" \
    "ds_read_b64_tr_b16 v[12:13], v244 offset:64\n\t" \
    "ds_read_b64_tr_b16 v[14:15], v244 offset:1600\n\t" \
    "v_add_u32 v244, 0x0, v243\n\t" \
    "s_waitcnt vmcnt(12) lgkmcnt(0)\n\t" \
    "s_barrier \n\t" \
    "s_add_u32 m0, %[wm], 0x1e000\n\t" \
    "s_mov_b32 s44, 0x2a0000\n\t" \
    "buffer_load_dwordx4 v240, %[wr], s44 offen offset:0 nt lds\n\t" \
    "buffer_load_dwordx4 v241, %[wr], s44 offen offset:1024 nt lds\n\t" \
    "buffer_load_dwordx4 v242, %[wr], s44 offen offset:2048 nt lds\n\t" \
    "ds_read_b64_tr_b16 v[208:209], v244 offset:0\n\t" \
    "ds_read_b64_tr_b16 v[210:211], v244 offset:1536\n\t" \
    "ds_read_b64_tr_b16 v[40:41], v244 offset:32\n\t" \
    "ds_read_b64_tr_b16 v[42:43], v244 offset:1568\n\t" \
    "ds_read_b64_tr_b16 v[48:49], v244 offset:64\n\t" \
    "ds_read_b64_tr_b16 v[50:51], v244 offset:1600\n\t" \
    "v_add_u32 v244, 0x6000, v243\n\t" \
    "s_waitcnt vmcnt(12) lgkmcnt(0)\n\t" \
    "s_barrier \n\t" \
    "s_add_u32 m0, %[wm], 0x0\n\t" \
    "s_mov_b32 s44, 0x300000\n\t" \
    "buffer_load_dwordx4 v240, %[wr], s44 offen offset:0 nt lds\n\t" \
    "buffer_load_dwordx4 v241, %[wr], s44 offen offset:1024 nt lds\n\t" \
    "buffer_load_dwordx4 v242, %[wr], s44 offen offset:2048 nt lds\n\t" \
    "ds_read_b64_tr_b16 v[212:213], v244 offset:0\n\t" \
    "ds_read_b64_tr_b16 v[214:215], v244 offset:1536\n\t" \
    "ds_read_b64_tr_b16 v[44:45], v244 offset:32\n\t" \
    "ds_read_b64_tr_b16 v[46:47], v244 offset:1568\n\t" \
    "ds_read_b64_tr_b16 v[52:53], v244 offset:64\n\t" \
    "ds_read_b64_tr_b16 v[54:55], v244 offset:1600\n\t" \
    "v_add_u32 v244, 0xc000, v243\n\t" \
    "s_waitcnt vmcnt(12) lgkmcnt(0)\n\t" \
    "s_barrier \n\t" \
    "s_add_u32 m0, %[wm], 0x6000\n\t" \
    "s_mov_b32 s44, 0x360000\n\t" \
    "buffer_load_dwordx4 v240, %[wr], s44 offen offset:0 nt lds\n\t" \
    "buffer_load_dwordx4 v241, %[wr], s44 offen offset:1024 nt lds\n\t" \
    "buffer_load_dwordx4 v242, %[wr], s44 offen offset:2048 nt lds\n\t" \
    "ds_read_b64_tr_b16 v[216:217], v244 offset:0\n\t" \
    "ds_read_b64_tr_b16 v[218:219], v244 offset:1536\n\t" \
    "ds_read_b64_tr_b16 v[80:81], v244 offset:32\n\t" \
    "ds_read_b64_tr_b16 v[82:83], v244 offset:1568\n\t" \
    "ds_read_b64_tr_b16 v[88:89], v244 offset:64\n\t" \
    "ds_read_b64_tr_b16 v[90:91], v244 offset:1600\n\t" \
    "v_add_u32 v244, 0x12000, v243\n\t" \
    "s_waitcnt vmcnt(12) lgkmcnt(0)\n\t" \
    "s_barrier \n\t" \
    "s_add_u32 m0, %[wm], 0xc000\n\t" \
    "s_mov_b32 s44, 0x3c0000\n\t" \
    "buffer_load_dwordx4 v240, %[wr], s44 offen offset:0 nt lds\n\t" \
    "buffer_load_dwordx4 v241, %[wr], s44 offen offset:1024 nt lds\n\t" \
    "buffer_load_dwordx4 v242, %[wr], s44 offen offset:2048 nt lds\n\t" \
    "ds_read_b64_tr_b16 v[220:221], v244 offset:0\n\t" \
    "ds_read_b64_tr_b16 v[222:223], v244 offset:1536\n\t" \
    "ds_read_b64_tr_b16 v[84:85], v244 offset:32\n\t" \
    "ds_read_b64_tr_b16 v[86:87], v244 offset:1568\n\t" \
    "ds_read_b64_tr_b16 v[92:93], v244 offset:64\n\t" \
    "ds_read_b64_tr_b16 v[94:95], v244 offset:1600\n\t" \
    "v_add_u32 v244, 0x18000, v243\n\t" \
    "s_waitcnt vmcnt(12) lgkmcnt(0)\n\t" \
    "s_barrier \n\t" \
    "v_mov_b32 v246, 0\n\t" \
    "buffer_load_dword v246, v246, s[60:63], %[mo] offen sc1\n\t" \
    "s_add_u32 m0, %[wm], 0x12000\n\t" \
    "s_mov_b32 s44, 0x420000\n\t" \
    "buffer_load_dwordx4 v240, %[wr], s44 offen offset:0 nt lds\n\t" \
    "buffer_load_dwordx4 v241, %[wr], s44 offen offset:1024 nt lds\n\t" \
    "buffer_load_dwordx4 v242, %[wr], s44 offen offset:2048 nt lds\n\t" \
    "ds_read_b64_tr_b16 v[224:225], v244 offset:0\n\t" \
    "ds_read_b64_tr_b16 v[226:227], v244 offset:1536\n\t" \
    "ds_read_b64_tr_b16 v[120:121], v244 offset:32\n\t" \
    "ds_read_b64_tr_b16 v[122:123], v244 offset:1568\n\t" \
    "ds_read_b64_tr_b16 v[128:129], v244 offset:64\n\t" \
    "ds_read_b64_tr_b16 v[130:131], v244 offset:1600\n\t" \
    "v_add_u32 v244, 0x1e000, v243\n\t" \
    "s_waitcnt vmcnt(12) lgkmcnt(0)\n\t" \
    "s_barrier \n\t" \
    "s_add_u32 m0, %[wm], 0x18000\n\t" \
    "s_mov_b32 s44, 0x480000\n\t" \
    "buffer_load_dwordx4 v240, %[wr], s44 offen offset:0 nt lds\n\t" \
    "buffer_load_dwordx4 v241, %[wr], s44 offen offset:1024 nt lds\n\t" \
    "buffer_load_dwordx4 v242, %[wr], s44 offen offset:2048 nt lds\n\t" \
    "ds_read_b64_tr_b16 v[228:229], v244 offset:0\n\t" \
    "ds_read_b64_tr_b16 v[230:231], v244 offset:1536\n\t" \
    "ds_read_b64_tr_b16 v[124:125], v244 offset:32\n\t" \
    "ds_read_b64_tr_b16 v[126:127], v244 offset:1568\n\t" \
    "ds_read_b64_tr_b16 v[132:133], v244 offset:64\n\t" \
    "ds_read_b64_tr_b16 v[134:135], v244 offset:1600\n\t" \
    "v_add_u32 v244, 0x0, v243\n\t" \
    "s_waitcnt vmcnt(12) lgkmcnt(0)\n\t" \
    "s_barrier \n\t" \
    "s_add_u32 m0, %[wm], 0x1e000\n\t" \
    "s_mov_b32 s44, 0x4e0000\n\t" \
    "buffer_load_dwordx4 v240, %[wr], s44 offen offset:0 nt lds\n\t" \
    "buffer_load_dwordx4 v241, %[wr], s44 offen offset:1024 nt lds\n\t" \
    "buffer_load_dwordx4 v242, %[wr], s44 offen offset:2048 nt lds\n\t" \
    "ds_read_b64_tr_b16 v[232:233], v244 offset:0\n\t" \
    "ds_read_b64_tr_b16 v[234:235], v244 offset:1536\n\t" \
    "ds_read_b64_tr_b16 v[160:161], v244 offset:32\n\t" \
    "ds_read_b64_tr_b16 v[162:163], v244 offset:1568\n\t" \
    "ds_read_b64_tr_b16 v[168:169], v244 offset:64\n\t" \
    "ds_read_b64_tr_b16 v[170:171], v244 offset:1600\n\t" \
    "v_add_u32 v244, 0x6000, v243\n\t" \
    "s_waitcnt vmcnt(12) lgkmcnt(0)\n\t" \
    "s_barrier \n\t" \
    "s_add_u32 m0, %[wm], 0x0\n\t" \
    "s_mov_b32 s44, 0x540000\n\t" \
    "buffer_load_dwordx4 v240, %[wr], s44 offen offset:0 nt lds\n\t" \
    "buffer_load_dwordx4 v241, %[wr], s44 offen offset:1024 nt lds\n\t" \
    "buffer_load_dwordx4 v242, %[wr], s44 offen offset:2048 nt lds\n\t" \
    "ds_read_b64_tr_b16 v[236:237], v244 offset:0\n\t" \
    "ds_read_b64_tr_b16 v[238:239], v244 offset:1536\n\t" \
    "ds_read_b64_tr_b16 v[164:165], v244 offset:32\n\t" \
    "ds_read_b64_tr_b16 v[166:167], v244 offset:1568\n\t" \
    "ds_read_b64_tr_b16 v[172:173], v244 offset:64\n\t" \
    "ds_read_b64_tr_b16 v[174:175], v244 offset:1600\n\t" \
    "v_add_u32 v244, 0xc000, v243\n\t" \
    "s_waitcnt vmcnt(12) lgkmcnt(0)\n\t" \
    "s_barrier \n\t" \
    "s_add_u32 m0, %[wm], 0x6000\n\t" \
    "s_mov_b32 s44, 0x5a0000\n\t" \
    "buffer_load_dwordx4 v240, %[wr], s44 offen offset:0 nt lds\n\t" \
    "buffer_load_dwordx4 v241, %[wr], s44 offen offset:1024 nt lds\n\t" \
    "buffer_load_dwordx4 v242, %[wr], s44 offen offset:2048 nt lds\n\t" \
    "ds_read_b64_tr_b16 v[16:17], v244 offset:0\n\t" \
    "ds_read_b64_tr_b16 v[18:19], v244 offset:1536\n\t" \
    "ds_read_b64_tr_b16 v[24:25], v244 offset:32\n\t" \
    "ds_read_b64_tr_b16 v[26:27], v244 offset:1568\n\t" \
    "ds_read_b64_tr_b16 v[32:33], v244 offset:64\n\t" \
    "ds_read_b64_tr_b16 v[34:35], v244 offset:1600\n\t" \
    "v_add_u32 v244, 0x12000, v243\n\t" \
    "s_waitcnt vmcnt(12) lgkmcnt(0)\n\t" \
    "s_barrier \n\t" \
    ".Ls01_%=:\n\t" \
    "v_readfirstlane_b32 s43, v246\n\t" \
    "s_nop 0\n\t" \
    "s_cmp_lg_u32 s43, -1\n\t" \
    "s_cbranch_scc1 .Ls02_%=\n\t" \
    "s_cmp_eq_u32 s41, 0\n\t" \
    "s_cbranch_scc1 .Ls02_%=\n\t" \
    "s_sub_u32 s41, s41, 1\n\t" \
    "s_sleep 4\n\t" \
    "v_mov_b32 v246, 0\n\t" \
    "buffer_load_dword v246, v246, s[60:63], %[mo] offen sc1\n\t" \
    "s_waitcnt vmcnt(0)\n\t" \
    "s_branch .Ls01_%=\n\t" \
    ".Ls02_%=:\n\t" \
    "v_mov_b32 v246, 0x8000\n\t" \
    "s_mov_b64 exec, 1\n\t" \
    "buffer_store_dword v246, v246, s[60:63], %[mo] offen sc1\n\t" \
    "s_mov_b64 exec, -1\n\t" \
    "s_and_b32 s43, s43, 0x2000\n\t" \
    "s_xor_b32 s43, s43, %[lx]\n\t" \
    "s_add_u32 s60, s60, s43\n\t" \
    "s_addc_u32 s61, s61, 0\n\t" \
    "s_add_u32 m0, %[wm], 0xc000\n\t" \
    "s_mov_b32 s44, 0x600000\n\t" \
    "buffer_load_dwordx4 v240, %[wr], s44 offen offset:0 nt lds\n\t" \
    "buffer_load_dwordx4 v241, %[wr], s44 offen offset:1024 nt lds\n\t" \
    "buffer_load_dwordx4 v242, %[wr], s44 offen offset:2048 nt lds\n\t" \
    "ds_read_b64_tr_b16 v[20:21], v244 offset:0\n\t" \
    "ds_read_b64_tr_b16 v[22:23], v244 offset:1536\n\t" \
    "ds_read_b64_tr_b16 v[28:29], v244 offset:32\n\t" \
    "ds_read_b64_tr_b16 v[30:31], v244 offset:1568\n\t" \
    "ds_read_b64_tr_b16 v[36:37], v244 offset:64\n\t" \
    "ds_read_b64_tr_b16 v[38:39], v244 offset:1600\n\t" \
    "v_add_u32 v244, 0x18000, v243\n\t" \
    "s_waitcnt vmcnt(12) lgkmcnt(0)\n\t" \
    "s_barrier \n\t" \
    "s_add_u32 m0, %[wm], 0x12000\n\t" \
    "s_mov_b32 s44, 0x660000\n\t" \
    "buffer_load_dwordx4 v240, %[wr], s44 offen offset:0 nt lds\n\t" \
    "buffer_load_dwordx4 v241, %[wr], s44 offen offset:1024 nt lds\n\t" \
    "buffer_load_dwordx4 v242, %[wr], s44 offen offset:2048 nt lds\n\t" \
    "ds_read_b64_tr_b16 v[56:57], v244 offset:0\n\t" \
    "ds_read_b64_tr_b16 v[58:59], v244 offset:1536\n\t" \
    "ds_read_b64_tr_b16 v[64:65], v244 offset:32\n\t" \
    "ds_read_b64_tr_b16 v[66:67], v244 offset:1568\n\t" \
    "ds_read_b64_tr_b16 v[72:73], v244 offset:64\n\t" \
    "ds_read_b64_tr_b16 v[74:75], v244 offset:1600\n\t" \
    "v_add_u32 v244, 0x1e000, v243\n\t" \
    "s_waitcnt vmcnt(12) lgkmcnt(0)\n\t" \
    "s_barrier \n\t" \
    "s_add_u32 m0, %[wm], 0x18000\n\t" \
    "s_mov_b32 s44, 0x6c0000\n\t" \
    "buffer_load_dwordx4 v240, %[wr], s44 offen offset:0 nt lds\n\t" \
    "buffer_load_dwordx4 v241, %[wr], s44 offen offset:1024 nt lds\n\t" \
    "buffer_load_dwordx4 v242, %[wr], s44 offen offset:2048 nt lds\n\t" \
    "ds_read_b64_tr_b16 v[60:61], v244 offset:0\n\t" \
    "ds_read_b64_tr_b16 v[62:63], v244 offset:1536\n\t" \
    "ds_read_b64_tr_b16 v[68:69], v244 offset:32\n\t" \
    "ds_read_b64_tr_b16 v[70:71], v244 offset:1568\n\t" \
    "ds_read_b64_tr_b16 v[76:77], v244 offset:64\n\t" \
    "ds_read_b64_tr_b16 v[78:79], v244 offset:1600\n\t" \
    "v_add_u32 v244, 0x0, v243\n\t" \
    "s_waitcnt vmcnt(12) lgkmcnt(0)\n\t" \
    "s_barrier \n\t" \
    "s_add_u32 m0, %[wm], 0x1e000\n\t" \
    "s_mov_b32 s44, 0x720000\n\t" \
    "buffer_load_dwordx4 v240, %[wr], s44 offen offset:0 nt lds\n\t" \
    "buffer_load_dwordx4 v241, %[wr], s44 offen offset:1024 nt lds\n\t" \
    "buffer_load_dwordx4 v242, %[wr], s44 offen offset:2048 nt lds\n\t" \
    "ds_read_b64_tr_b16 v[96:97], v244 offset:0\n\t" \
    "ds_read_b64_tr_b16 v[98:99], v244 offset:1536\n\t" \
    "ds_read_b64_tr_b16 v[104:105], v244 offset:32\n\t" \
    "ds_read_b64_tr_b16 v[106:107], v244 offset:1568\n\t" \
    "ds_read_b64_tr_b16 v[112:113], v244 offset:64\n\t" \
    "ds_read_b64_tr_b16 v[114:115], v244 offset:1600\n\t" \
    "v_add_u32 v244, 0x6000, v243\n\t" \
    "s_waitcnt vmcnt(12) lgkmcnt(0)\n\t" \
    "s_barrier \n\t" \
    "ds_read_b64_tr_b16 v[100:101], v244 offset:0\n\t" \
    "ds_read_b64_tr_b16 v[102:103], v244 offset:1536\n\t" \
    "ds_read_b64_tr_b16 v[108:109], v244 offset:32\n\t" \
    "ds_read_b64_tr_b16 v[110:111], v244 offset:1568\n\t" \
    "ds_read_b64_tr_b16 v[116:117], v244 offset:64\n\t" \
    "ds_read_b64_tr_b16 v[118:119], v244 offset:1600\n\t" \
    "v_add_u32 v244, 0xc000, v243\n\t" \
    "s_waitcnt vmcnt(9) lgkmcnt(0)\n\t" \
    "s_barrier \n\t" \
    "ds_read_b64_tr_b16 v[136:137], v244 offset:0\n\t" \
    "ds_read_b64_tr_b16 v[138:139], v244 offset:1536\n\t" \
    "ds_read_b64_tr_b16 v[144:145], v244 offset:32\n\t" \
    "ds_read_b64_tr_b16 v[146:147], v244 offset:1568\n\t" \
    "ds_read_b64_tr_b16 v[152:153], v244 offset:64\n\t" \
    "ds_read_b64_tr_b16 v[154:155], v244 offset:1600\n\t" \
    "v_add_u32 v244, 0x12000, v243\n\t" \
    "s_waitcnt vmcnt(6) lgkmcnt(0)\n\t" \
    "s_barrier \n\t" \
    "ds_read_b64_tr_b16 v[140:141], v244 offset:0\n\t" \
    "ds_read_b64_tr_b16 v[142:143], v244 offset:1536\n\t" \
    "ds_read_b64_tr_b16 v[148:149], v244 offset:32\n\t" \
    "ds_read_b64_tr_b16 v[150:151], v244 offset:1568\n\t" \
    "ds_read_b64_tr_b16 v[156:157], v244 offset:64\n\t" \
    "ds_read_b64_tr_b16 v[158:159], v244 offset:1600\n\t" \
    "v_add_u32 v244, 0x18000, v243\n\t" \
    "s_waitcnt vmcnt(3) lgkmcnt(0)\n\t" \
    "s_barrier \n\t" \
    "ds_read_b64_tr_b16 v[176:177], v244 offset:0\n\t" \
    "ds_read_b64_tr_b16 v[178:179], v244 offset:1536\n\t" \
    "ds_read_b64_tr_b16 v[184:185], v244 offset:32\n\t" \
    "ds_read_b64_tr_b16 v[186:187], v244 offset:1568\n\t" \
    "ds_read_b64_tr_b16 v[192:193], v244 offset:64\n\t" \
    "ds_read_b64_tr_b16 v[194:195], v244 offset:1600\n\t" \
    "v_add_u32 v244, 0x1e000, v243\n\t" \
    "s_waitcnt vmcnt(0) lgkmcnt(0)\n\t" \
    "s_barrier \n\t" \
    "ds_read_b64_tr_b16 v[180:181], v244 offset:0\n\t" \
    "ds_read_b64_tr_b16 v[182:183], v244 offset:1536\n\t" \
    "ds_read_b64_tr_b16 v[188:189], v244 offset:32\n\t" \
    "ds_read_b64_tr_b16 v[190:191], v244 offset:1568\n\t" \
    "ds_read_b64_tr_b16 v[196:197], v244 offset:64\n\t" \
    "ds_read_b64_tr_b16 v[198:199], v244 offset:1600\n\t" \
    "s_waitcnt lgkmcnt(0)\n\t" \
    "s_barrier \n\t" \
    LST0 \
    "ds_write_b128 v254, v[200:203] offset:30720\n\t" \
    "ds_write_b128 v254, v[204:207] offset:31744\n\t" \
    "ds_write_b128 v254, v[208:211] offset:32768\n\t" \
    "ds_write_b128 v254, v[212:215] offset:33792\n\t" \
    "ds_write_b128 v254, v[216:219] offset:34816\n\t" \
    "ds_write_b128 v254, v[220:223] offset:35840\n\t" \
    "ds_write_b128 v254, v[224:227] offset:36864\n\t" \
    "ds_write_b128 v254, v[228:231] offset:37888\n\t" \
    "ds_write_b128 v254, v[232:235] offset:38912\n\t" \
    "ds_write_b128 v254, v[236:239] offset:39936\n\t" \
    "s_waitcnt vmcnt(0) lgkmcnt(0)\n\t" \
    "s_barrier \n\t" \
    "ds_read_b128 v[224:227], v253 offset:0\n\t" \
    "ds_read_b128 v[228:231], v253 offset:1024\n\t" \
    "ds_read_b128 v[232:235], v253 offset:20480\n\t" \
    "ds_read_b128 v[236:239], v253 offset:21504\n\t" \
    ".Ls03_%=:\n\t" \
    "s_waitcnt lgkmcnt(3)\n\t" \
    "v_mfma_f32_16x16x32_bf16 v[204:207], v[0:3], v[224:227], 0\n\t" \
    "s_waitcnt lgkmcnt(2)\n\t" \
    "v_mfma_f32_16x16x32_bf16 v[204:207], v[4:7], v[228:231], v[204:207]\n\t" \
    "ds_read_b128 v[240:243], v253 offset:2048\n\t" \
    "v_mfma_f32_16x16x32_bf16 v[208:211], v[8:11], v[224:227], 0\n\t" \
    "v_mfma_f32_16x16x32_bf16 v[208:211], v[12:15], v[228:231], v[208:211]\n\t" \
    "s_waitcnt lgkmcnt(2)\n\t" \
    "v_mfma_f32_16x16x32_bf16 v[216:219], v[0:3], v[232:235], 0\n\t" \
    "ds_read_b128 v[224:227], v253 offset:3072\n\t" \
    "s_waitcnt lgkmcnt(2)\n\t" \
    "v_mfma_f32_16x16x32_bf16 v[216:219], v[4:7], v[236:239], v[216:219]\n\t" \
    "ds_read_b128 v[228:231], v253 offset:22528\n\t" \
    "v_mfma_f32_16x16x32_bf16 v[220:223], v[8:11], v[232:235], 0\n\t" \
    "v_mfma_f32_16x16x32_bf16 v[220:223], v[12:15], v[236:239], v[220:223]\n\t" \
    "s_waitcnt lgkmcnt(2)\n\t" \
    "v_mfma_f32_16x16x32_bf16 v[204:207], v[40:43], v[240:243], v[204:207]\n\t" \
    "ds_read_b128 v[232:235], v253 offset:23552\n\t" \
    "s_waitcnt lgkmcnt(2)\n\t" \
    "v_mfma_f32_16x16x32_bf16 v[204:207], v[44:47], v[224:227], v[204:207]\n\t" \
    "ds_read_b128 v[236:239], v253 offset:4096\n\t" \
    "v_mfma_f32_16x16x32_bf16 v[208:211], v[48:51], v[240:243], v[208:211]\n\t" \
    "v_mfma_f32_16x16x32_bf16 v[208:211], v[52:55], v[224:227], v[208:211]\n\t" \
    "s_waitcnt lgkmcnt(2)\n\t" \
    "v_mfma_f32_16x16x32_bf16 v[216:219], v[40:43], v[228:231], v[216:219]\n\t" \
    "ds_read_b128 v[240:243], v253 offset:5120\n\t" \
    "s_waitcnt lgkmcnt(2)\n\t" \
    "v_mfma_f32_16x16x32_bf16 v[216:219], v[44:47], v[232:235], v[216:219]\n\t" \
    "ds_read_b128 v[224:227], v253 offset:24576\n\t" \
    "v_mfma_f32_16x16x32_bf16 v[220:223], v[48:51], v[228:231], v[220:223]\n\t" \
    "v_mfma_f32_16x16x32_bf16 v[220:223], v[52:55], v[232:235], v[220:223]\n\t" \
    "s_waitcnt lgkmcnt(2)\n\t" \
    "v_mfma_f32_16x16x32_bf16 v[204:207], v[80:83], v[236:239], v[204:207]\n\t" \
    "ds_read_b128 v[228:231], v253 offset:25600\n\t" \
    "s_waitcnt lgkmcnt(2)\n\t" \
    "v_mfma_f32_16x16x32_bf16 v[204:207], v[84:87], v[240:243], v[204:207]\n\t" \
    "ds_read_b128 v[232:235], v253 offset:6144\n\t" \
    "v_mfma_f32_16x16x32_bf16 v[208:211], v[88:91], v[236:239], v[208:211]\n\t" \
    "v_mfma_f32_16x16x32_bf16 v[208:211], v[92:95], v[240:243], v[208:211]\n\t" \
    "s_waitcnt lgkmcnt(2)\n\t" \
    "v_mfma_f32_16x16x32_bf16 v[216:219], v[80:83], v[224:227], v[216:219]\n\t" \
    "ds_read_b128 v[236:239], v253 offset:7168\n\t" \
    "s_waitcnt lgkmcnt(2)\n\t" \
    "v_mfma_f32_16x16x32_bf16 v[216:219], v[84:87], v[228:231], v[216:219]\n\t" \
    "ds_read_b128 v[240:243], v253 offset:26624\n\t" \
    "v_mfma_f32_16x16x32_bf16 v[220:223], v[88:91], v[224:227], v[220:223]\n\t" \
    "v_mfma_f32_16x16x32_bf16 v[220:223], v[92:95], v[228:231], v[220:223]\n\t" \
    "s_waitcnt lgkmcnt(2)\n\t" \
    "v_mfma_f32_16x16x32_bf16 v[204:207], v[120:123], v[232:235], v[204:207]\n\t" \
    "ds_read_b128 v[224:227], v253 offset:27648\n\t" \
    "s_waitcnt lgkmcnt(2)\n\t" \
    "v_mfma_f32_16x16x32_bf16 v[204:207], v[124:127], v[236:239], v[204:207]\n\t" \
    "ds_read_b128 v[228:231], v253 offset:8192\n\t" \
    "v_mfma_f32_16x16x32_bf16 v[208:211], v[128:131], v[232:235], v[208:211]\n\t" \
    "v_mfma_f32_16x16x32_bf16 v[208:211], v[132:135], v[236:239], v[208:211]\n\t" \
    "s_waitcnt lgkmcnt(2)\n\t" \
    "v_mfma_f32_16x16x32_bf16 v[216:219], v[120:123], v[240:243], v[216:219]\n\t" \
    "ds_read_b128 v[232:235], v253 offset:9216\n\t" \
    "s_waitcnt lgkmcnt(2)\n\t" \
    "v_mfma_f32_16x16x32_bf16 v[216:219], v[124:127], v[224:227], v[216:219]\n\t" \
    "ds_read_b128 v[236:239], v253 offset:28672\n\t" \
    "v_mfma_f32_16x16x32_bf16 v[220:223], v[128:131], v[240:243], v[220:223]\n\t" \
    "v_mfma_f32_16x16x32_bf16 v[220:223], v[132:135], v[224:227], v[220:223]\n\t" \
    "s_waitcnt lgkmcnt(2)\n\t" \
    "v_mfma_f32_16x16x32_bf16 v[204:207], v[160:163], v[228:231], v[204:207]\n\t" \
    "ds_read_b128 v[240:243], v253 offset:29696\n\t" \
    "s_waitcnt lgkmcnt(2)\n\t" \
    "v_mfma_f32_16x16x32_bf16 v[204:207], v[164:167], v[232:235], v[204:207]\n\t" \
    "ds_read_b128 v[224:227], v253 offset:10240\n\t" \
    "v_mfma_f32_16x16x32_bf16 v[208:211], v[168:171], v[228:231], v[208:211]\n\t" \
    "v_mfma_f32_16x16x32_bf16 v[208:211], v[172:175], v[232:235], v[208:211]\n\t" \
    "s_waitcnt lgkmcnt(2)\n\t" \
    "v_mfma_f32_16x16x32_bf16 v[216:219], v[160:163], v[236:239], v[216:219]\n\t" \
    "ds_read_b128 v[228:231], v253 offset:11264\n\t" \
    "s_waitcnt lgkmcnt(2)\n\t" \
    "v_mfma_f32_16x16x32_bf16 v[216:219], v[164:167], v[240:243], v[216:219]\n\t" \
    "ds_read_b128 v[232:235], v253 offset:30720\n\t" \
    "v_mfma_f32_16x16x32_bf16 v[220:223], v[168:171], v[236:239], v[220:223]\n\t" \
    "v_mfma_f32_16x16x32_bf16 v[220:223], v[172:175], v[240:243], v[220:223]\n\t" \
    "ds_read_b128 v[236:239], v253 offset:31744\n\t" \
    "s_setprio 2\n\t" \
    "s_waitcnt lgkmcnt(0)\n\t" \
    "s_barrier \n\t" \
    LST7 \
    "ds_read_b128 v[200:203], v253 offset:40960\n\t" \
    "ds_read_b128 v[212:215], v253 offset:41984\n\t" \
    "v_mfma_f32_16x16x32_bf16 v[204:207], v[24:27], v[224:227], v[204:207]\n\t" \
    "v_mfma_f32_16x16x32_bf16 v[204:207], v[28:31], v[228:231], v[204:207]\n\t" \
    "ds_read_b128 v[240:243], v253 offset:12288\n\t" \
    "v_mfma_f32_16x16x32_bf16 v[208:211], v[32:35], v[224:227], v[208:211]\n\t" \
    "v_mfma_f32_16x16x32_bf16 v[208:211], v[36:39], v[228:231], v[208:211]\n\t" \
    "s_waitcnt lgkmcnt(2)\n\t" \
    "v_mfma_f32_16x16x32_bf16 v[200:203], v[16:19], v[224:227], v[200:203]\n\t" \
    "v_mfma_f32_16x16x32_bf16 v[200:203], v[20:23], v[228:231], v[200:203]\n\t" \
    "v_mfma_f32_16x16x32_bf16 v[216:219], v[24:27], v[232:235], v[216:219]\n\t" \
    "ds_read_b128 v[224:227], v253 offset:13312\n\t" \
    "s_and_b32 s44, s40, 3\n\t" \
    "v_mfma_f32_16x16x32_bf16 v[216:219], v[28:31], v[236:239], v[216:219]\n\t" \
    "ds_read_b128 v[228:231], v253 offset:32768\n\t" \
    "s_lshl_b32 s44, s44, 17\n\t" \
    "v_mfma_f32_16x16x32_bf16 v[220:223], v[32:35], v[232:235], v[220:223]\n\t" \
    "s_add_u32 s44, s44, %[mb]\n\t" \
    "v_mfma_f32_16x16x32_bf16 v[220:223], v[36:39], v[236:239], v[220:223]\n\t" \
    "s_add_u32 s46, s44, 0x0\n\t" \
    "s_waitcnt lgkmcnt(3)\n\t" \
    "v_mfma_f32_16x16x32_bf16 v[212:215], v[16:19], v[232:235], v[212:215]\n\t" \
    "s_add_u32 s47, s44, 0x1400\n\t" \
    "v_mfma_f32_16x16x32_bf16 v[212:215], v[20:23], v[236:239], v[212:215]\n\t" \
    "s_add_u32 s48, s44, 0x400\n\t" \
    "s_waitcnt lgkmcnt(2)\n\t" \
    "v_mfma_f32_16x16x32_bf16 v[204:207], v[64:67], v[240:243], v[204:207]\n\t" \
    "ds_read_b128 v[232:235], v253 offset:33792\n\t" \
    "s_waitcnt lgkmcnt(2)\n\t" \
    "v_mfma_f32_16x16x32_bf16 v[204:207], v[68:71], v[224:227], v[204:207]\n\t" \
    "ds_read_b128 v[236:239], v253 offset:14336\n\t" \
    "v_mfma_f32_16x16x32_bf16 v[208:211], v[72:75], v[240:243], v[208:211]\n\t" \
    "v_mfma_f32_16x16x32_bf16 v[208:211], v[76:79], v[224:227], v[208:211]\n\t" \
    "v_mfma_f32_16x16x32_bf16 v[200:203], v[56:59], v[240:243], v[200:203]\n\t" \
    "v_mfma_f32_16x16x32_bf16 v[200:203], v[60:63], v[224:227], v[200:203]\n\t" \
    "s_waitcnt lgkmcnt(2)\n\t" \
    "v_mfma_f32_16x16x32_bf16 v[216:219], v[64:67], v[228:231], v[216:219]\n\t" \
    "ds_read_b128 v[240:243], v253 offset:15360\n\t" \
    "s_waitcnt lgkmcnt(2)\n\t" \
    "v_mfma_f32_16x16x32_bf16 v[216:219], v[68:71], v[232:235], v[216:219]\n\t" \
    "ds_read_b128 v[224:227], v253 offset:34816\n\t" \
    "v_mfma_f32_16x16x32_bf16 v[220:223], v[72:75], v[228:231], v[220:223]\n\t" \
    "v_mfma_f32_16x16x32_bf16 v[220:223], v[76:79], v[232:235], v[220:223]\n\t" \
    "v_mfma_f32_16x16x32_bf16 v[212:215], v[56:59], v[228:231], v[212:215]\n\t" \
    "v_mfma_f32_16x16x32_bf16 v[212:215], v[60:63], v[232:235], v[212:215]\n\t" \
    "s_waitcnt lgkmcnt(2)\n\t" \
    "v_mfma_f32_16x16x32_bf16 v[204:207], v[104:107], v[236:239], v[204:207]\n\t" \
    "ds_read_b128 v[228:231], v253 offset:35840\n\t" \
    "s_waitcnt lgkmcnt(2)\n\t" \
    "v_mfma_f32_16x16x32_bf16 v[204:207], v[108:111], v[240:243], v[204:207]\n\t" \
    "ds_read_b128 v[232:235], v253 offset:16384\n\t" \
    "v_mfma_f32_16x16x32_bf16 v[208:211], v[112:115], v[236:239], v[208:211]\n\t" \
    "v_mfma_f32_16x16x32_bf16 v[208:211], v[116:119], v[240:243], v[208:211]\n\t" \
    "v_mfma_f32_16x16x32_bf16 v[200:203], v[96:99], v[236:239], v[200:203]\n\t" \
    "v_mfma_f32_16x16x32_bf16 v[200:203], v[100:103], v[240:243], v[200:203]\n\t" \
    "s_waitcnt lgkmcnt(2)\n\t" \
    "v_mfma_f32_16x16x32_bf16 v[216:219], v[104:107], v[224:227], v[216:219]\n\t" \
    "ds_read_b128 v[236:239], v253 offset:17408\n\t" \
    "s_waitcnt lgkmcnt(2)\n\t" \
    "v_mfma_f32_16x16x32_bf16 v[216:219], v[108:111], v[228:231], v[216:219]\n\t" \
    "ds_read_b128 v[240:243], v253 offset:18432\n\t" \
    "v_mfma_f32_16x16x32_bf16 v[220:223], v[112:115], v[224:227], v[220:223]\n\t" \
    "v_mfma_f32_16x16x32_bf16 v[220:223], v[116:119], v[228:231], v[220:223]\n\t" \
    "v_mfma_f32_16x16x32_bf16 v[212:215], v[96:99], v[224:227], v[212:215]\n\t" \
    "v_mfma_f32_16x16x32_bf16 v[212:215], v[100:103], v[228:231], v[212:215]\n\t" \
    "s_waitcnt lgkmcnt(2)\n\t" \
    "v_mfma_f32_16x16x32_bf16 v[204:207], v[144:147], v[232:235], v[204:207]\n\t" \
    "ds_read_b128 v[224:227], v253 offset:19456\n\t" \
    "v_mov_b64 v[244:245], 0\n\t" \
    "s_waitcnt lgkmcnt(2)\n\t" \
    "v_mfma_f32_16x16x32_bf16 v[204:207], v[148:151], v[236:239], v[204:207]\n\t" \
    "ds_read_b128 v[228:231], v253 offset:36864\n\t" \
    "v_mov_b64 v[246:247], 0\n\t" \
    "v_mfma_f32_16x16x32_bf16 v[208:211], v[152:155], v[232:235], v[208:211]\n\t" \
    "v_mov_b64 v[248:249], 0\n\t" \
    "v_mfma_f32_16x16x32_bf16 v[208:211], v[156:159], v[236:239], v[208:211]\n\t" \
    "v_mov_b64 v[250:251], 0\n\t" \
    "v_mfma_f32_16x16x32_bf16 v[200:203], v[136:139], v[232:235], v[200:203]\n\t" \
    "v_mfma_f32_16x16x32_bf16 v[200:203], v[140:143], v[236:239], v[200:203]\n\t" \
    "s_waitcnt lgkmcnt(2)\n\t" \
    "v_mfma_f32_16x16x32_bf16 v[204:207], v[184:187], v[240:243], v[204:207]\n\t" \
    "ds_read_b128 v[232:235], v253 offset:37888\n\t" \
    "s_waitcnt lgkmcnt(2)\n\t" \
    "v_mfma_f32_16x16x32_bf16 v[204:207], v[188:191], v[224:227], v[204:207]\n\t" \
    "ds_read_b128 v[236:239], v253 offset:38912\n\t" \
    "v_mfma_f32_16x16x32_bf16 v[208:211], v[192:195], v[240:243], v[208:211]\n\t" \
    "v_mfma_f32_16x16x32_bf16 v[208:211], v[196:199], v[224:227], v[208:211]\n\t" \
    "v_mfma_f32_16x16x32_bf16 v[200:203], v[176:179], v[240:243], v[200:203]\n\t" \
    "v_mfma_f32_16x16x32_bf16 v[200:203], v[180:183], v[224:227], v[200:203]\n\t" \
    "ds_read_b128 v[240:243], v253 offset:39936\n\t" \
    "s_waitcnt lgkmcnt(3)\n\t" \
    "v_mfma_f32_16x16x32_bf16 v[216:219], v[144:147], v[228:231], v[216:219]\n\t" \
    "s_waitcnt lgkmcnt(2)\n\t" \
    "v_mfma_f32_16x16x32_bf16 v[216:219], v[148:151], v[232:235], v[216:219]\n\t" \
    "v_cvt_pk_bf16_f32 v204, v204, v208\n\t" \
    "v_cvt_pk_bf16_f32 v205, v205, v209\n\t" \
    "s_waitcnt lgkmcnt(1)\n\t" \
    "v_mfma_f32_16x16x32_bf16 v[216:219], v[184:187], v[236:239], v[216:219]\n\t" \
    "v_cvt_pk_bf16_f32 v206, v206, v210\n\t" \
    "v_cvt_pk_bf16_f32 v207, v207, v211\n\t" \
    "s_waitcnt lgkmcnt(0)\n\t" \
    "v_mfma_f32_16x16x32_bf16 v[216:219], v[188:191], v[240:243], v[216:219]\n\t" \
    "ds_write_b128 v254, v[204:207] offset:8192\n\t" \
    "v_mfma_f32_16x16x32_bf16 v[220:223], v[152:155], v[228:231], v[220:223]\n\t" \
    "v_mfma_f32_16x16x32_bf16 v[220:223], v[156:159], v[232:235], v[220:223]\n\t" \
    "v_cvt_pk_bf16_f32 v200, v200, v201\n\t" \
    "v_cvt_pk_bf16_f32 v201, v202, v203\n\t" \
    "v_mfma_f32_16x16x32_bf16 v[220:223], v[192:195], v[236:239], v[220:223]\n\t" \
    "ds_write_b64 v254, v[200:201] offset:6144\n\t" \
    "v_mfma_f32_16x16x32_bf16 v[220:223], v[196:199], v[240:243], v[220:223]\n\t" \
    "v_mfma_f32_16x16x32_bf16 v[212:215], v[136:139], v[228:231], v[212:215]\n\t" \
    "ds_read_b128 v[200:203], v254 offset:30720\n\t" \
    "ds_read_b128 v[204:207], v254 offset:31744\n\t" \
    "v_mfma_f32_16x16x32_bf16 v[212:215], v[140:143], v[232:235], v[212:215]\n\t" \
    "v_mov_b64 v[208:209], 0\n\t" \
    "v_mov_b64 v[210:211], 0\n\t" \
    "v_mfma_f32_16x16x32_bf16 v[212:215], v[176:179], v[236:239], v[212:215]\n\t" \
    "v_cvt_pk_bf16_f32 v216, v216, v220\n\t" \
    "v_cvt_pk_bf16_f32 v217, v217, v221\n\t" \
    "v_mfma_f32_16x16x32_bf16 v[212:215], v[180:183], v[240:243], v[212:215]\n\t" \
    "v_cvt_pk_bf16_f32 v218, v218, v222\n\t" \
    "v_cvt_pk_bf16_f32 v219, v219, v223\n\t" \
    "s_setprio 0\n\t" \
    "ds_write_b128 v254, v[216:219] offset:14336\n\t" \
    "ds_read_b128 v[216:219], v254 offset:32768\n\t" \
    "ds_read_b128 v[220:223], v254 offset:33792\n\t" \
    "s_nop 2\n\t" \
    "v_cvt_pk_bf16_f32 v212, v212, v213\n\t" \
    "v_cvt_pk_bf16_f32 v213, v214, v215\n\t" \
    "ds_write_b64 v254, v[212:213] offset:12288\n\t" \
    LST6 \
    "s_sleep 3\n\t" \
    "s_mov_b32 m0, 0xa000\n\t" \
    "s_nop 0\n\t" \
    "buffer_load_dwordx4 v253, s[60:63], s46 offen sc1 lds\n\t" \
    "s_mov_b32 m0, 0xb400\n\t" \
    "s_nop 0\n\t" \
    "buffer_load_dwordx4 v253, s[60:63], s47 offen sc1 lds\n\t" \
    "s_mov_b32 m0, 0xa400\n\t" \
    "s_nop 0\n\t" \
    "buffer_load_dwordx4 v253, s[60:63], s48 offen sc1 lds\n\t" \
    "s_waitcnt lgkmcnt(0)\n\t" \
    LFLUSH212 \
    "v_bfe_i32 v212, v253, 5, 1\n\t" \
    "v_and_b32 v212, 0xaaaa, v212\n\t" \
    "v_or_b32 v212, 0x4444, v212\n\t" \
    "v_and_b32 v213, 0xffff, v255\n\t" \
    "v_lshrrev_b32 v214, 16, v255\n\t" \
    ".Ls04_%=:\n\t" \
    "s_waitcnt vmcnt(0)\n\t" \
    "ds_read_b128 v[224:227], v253 offset:40960\n\t" \
    "ds_read_b128 v[228:231], v253 offset:46080\n\t" \
    "ds_read_b128 v[232:235], v253 offset:41984\n\t" \
    "s_waitcnt lgkmcnt(2)\n\t" \
    "v_max_u32 v215, v224, v225\n\t" \
    "v_max3_u32 v215, v226, v227, v215\n\t" \
    "s_waitcnt lgkmcnt(1)\n\t" \
    "v_max3_u32 v215, v228, v229, v215\n\t" \
    "v_max3_u32 v215, v230, v231, v215\n\t" \
    "s_waitcnt lgkmcnt(0)\n\t" \
    "v_max3_u32 v215, v232, v233, v215\n\t" \
    "v_max3_u32 v215, v234, v235, v215\n\t" \
    "v_cmp_eq_u32 vcc, -1, v215\n\t" \
    "s_cbranch_vccz .Ls05_%=\n\t" \
    "v_max_u32 v215, v224, v225\n\t" \
    "v_max3_u32 v215, v226, v227, v215\n\t" \
    "v_cmp_eq_u32 vcc, -1, v215\n\t" \
    "s_cbranch_vccz .Ls06_%=\n\t" \
    "s_mov_b32 m0, 0xa000\n\t" \
    "s_nop 0\n\t" \
    "buffer_load_dwordx4 v253, s[60:63], s46 offen sc1 lds\n\t" \
    ".Ls06_%=:\n\t" \
    "v_max_u32 v215, v228, v229\n\t" \
    "v_max3_u32 v215, v230, v231, v215\n\t" \
    "v_cmp_eq_u32 vcc, -1, v215\n\t" \
    "s_cbranch_vccz .Ls07_%=\n\t" \
    "s_mov_b32 m0, 0xb400\n\t" \
    "s_nop 0\n\t" \
    "buffer_load_dwordx4 v253, s[60:63], s47 offen sc1 lds\n\t" \
    ".Ls07_%=:\n\t" \
    "v_max_u32 v215, v232, v233\n\t" \
    "v_max3_u32 v215, v234, v235, v215\n\t" \
    "v_cmp_eq_u32 vcc, -1, v215\n\t" \
    "s_cbranch_vccz .Ls08_%=\n\t" \
    "s_mov_b32 m0, 0xa400\n\t" \
    "s_nop 0\n\t" \
    "buffer_load_dwordx4 v253, s[60:63], s48 offen sc1 lds\n\t" \
    ".Ls08_%=:\n\t" \
    "s_cmp_eq_u32 s41, 0\n\t" \
    "s_cbranch_scc1 .Ls05_%=\n\t" \
    "s_sub_u32 s41, s41, 1\n\t" \
    "s_branch .Ls04_%=\n\t" \
    ".Ls05_%=:\n\t" \
    LST2 \
    "s_setprio 3\n\t" \
    "s_waitcnt lgkmcnt(0)\n\t" \
    "s_barrier \n\t" \
    LST3 \
    "ds_read_b128 v[236:239], v253 offset:47104\n\t" \
    "ds_read_b128 v[240:243], v253 offset:43008\n\t" \
    "v_smfmac_f32_16x16x64_bf16 v[244:247], v[224:227], v[0:7], v212\n\t" \
    "v_smfmac_f32_16x16x64_bf16 v[248:251], v[224:227], v[8:15], v212\n\t" \
    "v_smfmac_f32_16x16x64_bf16 v[208:211], v[224:227], v[200:207], v212\n\t" \
    "ds_read_b128 v[200:203], v254 offset:34816\n\t" \
    "ds_read_b128 v[204:207], v254 offset:35840\n\t" \
    "ds_read_b128 v[224:227], v253 offset:48128\n\t" \
    "v_smfmac_f32_16x16x64_bf16 v[208:211], v[228:231], v[16:23], v212\n\t" \
    "v_smfmac_f32_16x16x64_bf16 v[244:247], v[228:231], v[24:31], v212\n\t" \
    "v_smfmac_f32_16x16x64_bf16 v[248:251], v[228:231], v[32:39], v212\n\t" \
    "ds_read_b128 v[228:231], v253 offset:44032\n\t" \
    "v_smfmac_f32_16x16x64_bf16 v[208:211], v[232:235], v[216:223], v212\n\t" \
    "ds_read_b128 v[216:219], v254 offset:36864\n\t" \
    "ds_read_b128 v[220:223], v254 offset:37888\n\t" \
    "v_smfmac_f32_16x16x64_bf16 v[244:247], v[232:235], v[40:47], v212\n\t" \
    "v_smfmac_f32_16x16x64_bf16 v[248:251], v[232:235], v[48:55], v212\n\t" \
    "ds_read_b128 v[232:235], v253 offset:49152\n\t" \
    "s_waitcnt lgkmcnt(8)\n\t" \
    "v_smfmac_f32_16x16x64_bf16 v[208:211], v[236:239], v[56:63], v212\n\t" \
    "v_smfmac_f32_16x16x64_bf16 v[244:247], v[236:239], v[64:71], v212\n\t" \
    "v_smfmac_f32_16x16x64_bf16 v[248:251], v[236:239], v[72:79], v212\n\t" \
    "ds_read_b128 v[236:239], v253 offset:45056\n\t" \
    "s_waitcnt lgkmcnt(6)\n\t" \
    "v_smfmac_f32_16x16x64_bf16 v[208:211], v[240:243], v[200:207], v212\n\t" \
    "ds_read_b128 v[200:203], v254 offset:38912\n\t" \
    "ds_read_b128 v[204:207], v254 offset:39936\n\t" \
    "v_smfmac_f32_16x16x64_bf16 v[244:247], v[240:243], v[80:87], v212\n\t" \
    "v_smfmac_f32_16x16x64_bf16 v[248:251], v[240:243], v[88:95], v212\n\t" \
    "ds_read_b128 v[240:243], v253 offset:50176\n\t" \
    "s_waitcnt lgkmcnt(8)\n\t" \
    "v_smfmac_f32_16x16x64_bf16 v[208:211], v[224:227], v[96:103], v212\n\t" \
    "v_smfmac_f32_16x16x64_bf16 v[244:247], v[224:227], v[104:111], v212\n\t" \
    "v_smfmac_f32_16x16x64_bf16 v[248:251], v[224:227], v[112:119], v212\n\t" \
    "s_waitcnt lgkmcnt(5)\n\t" \
    "v_smfmac_f32_16x16x64_bf16 v[208:211], v[228:231], v[216:223], v212\n\t" \
    "s_waitcnt lgkmcnt(4)\n\t" \
    "v_smfmac_f32_16x16x64_bf16 v[208:211], v[232:235], v[136:143], v212\n\t" \
    "s_waitcnt lgkmcnt(1)\n\t" \
    "v_smfmac_f32_16x16x64_bf16 v[208:211], v[236:239], v[200:207], v212\n\t" \
    "s_waitcnt lgkmcnt(0)\n\t" \
    "v_smfmac_f32_16x16x64_bf16 v[208:211], v[240:243], v[176:183], v212\n\t" \
    "v_smfmac_f32_16x16x64_bf16 v[244:247], v[228:231], v[120:127], v212\n\t" \
    "v_smfmac_f32_16x16x64_bf16 v[244:247], v[232:235], v[144:151], v212\n\t" \
    "v_smfmac_f32_16x16x64_bf16 v[244:247], v[236:239], v[160:167], v212\n\t" \
    "v_add_f32 v208, v208, v210\n\t" \
    "v_add_f32 v209, v209, v211\n\t" \
    "v_cvt_pk_bf16_f32 v208, v208, v209\n\t" \
    "v_smfmac_f32_16x16x64_bf16 v[244:247], v[240:243], v[184:191], v212\n\t" \
    "ds_write_b16 v213, v208 offset:0\n\t" \
    "ds_write_b16_d16_hi v213, v208 offset:16\n\t" \
    "v_smfmac_f32_16x16x64_bf16 v[248:251], v[228:231], v[128:135], v212\n\t" \
    "v_smfmac_f32_16x16x64_bf16 v[248:251], v[232:235], v[152:159], v212\n\t" \
    "v_smfmac_f32_16x16x64_bf16 v[248:251], v[236:239], v[168:175], v212\n\t" \
    "v_add_f32 v244, v244, v246\n\t" \
    "v_add_f32 v245, v245, v247\n\t" \
    "v_smfmac_f32_16x16x64_bf16 v[248:251], v[240:243], v[192:199], v212\n\t" \
    "s_nop 7\n\t" \
    "v_add_f32 v248, v248, v250\n\t" \
    "v_add_f32 v249, v249, v251\n\t" \
    "v_cvt_pk_bf16_f32 v244, v244, v248\n\t" \
    "v_cvt_pk_bf16_f32 v245, v245, v249\n\t" \
    "ds_write2_b32 v214, v244, v245 offset1:4\n\t" \
    "s_setprio 0\n\t" \
    "s_sleep 2\n\t" \
    "ds_read_b128 v[200:203], v254 offset:18432\n\t" \
    "ds_read_b128 v[204:207], v254 offset:19456\n\t" \
    "ds_read_b128 v[214:217], v254 offset:24576\n\t" \
    "ds_read_b128 v[218:221], v254 offset:25600\n\t" \
    "ds_read_b128 v[222:225], v254 offset:20480\n\t" \
    "ds_read_b128 v[226:229], v254 offset:21504\n\t" \
    "ds_read_b128 v[230:233], v254 offset:26624\n\t" \
    "ds_read_b128 v[234:237], v254 offset:27648\n\t" \
    "v_and_b32 v213, 0xffffff7f, v254\n\t" \
    "v_mov_b64 v[242:243], 0\n\t" \
    "v_mov_b64 v[244:245], 0\n\t" \
    "v_mov_b64 v[246:247], 0\n\t" \
    "v_mov_b64 v[248:249], 0\n\t" \
    "v_mov_b64 v[250:251], 0\n\t" \
    "v_mov_b64 v[252:253], 0\n\t" \
    "s_and_b32 s44, s40, 3\n\t" \
    "s_mul_i32 s44, s44, 0x30000\n\t" \
    "s_add_u32 s50, s44, %[pb]\n\t" \
    "s_add_u32 s51, s50, 0xc000\n\t" \
    "s_add_u32 s52, s50, 0x18000\n\t" \
    "s_add_u32 s53, s50, 0x24000\n\t" \
    "s_setprio 3\n\t" \
    "s_waitcnt lgkmcnt(0)\n\t" \
    "s_barrier \n\t" \
    LST4 \
    "ds_read_b128 v[208:211], v254 offset:0\n\t" \
    "ds_read_b128 v[238:241], v254 offset:1024\n\t" \
    "s_waitcnt lgkmcnt(1)\n\t" \
    "v_smfmac_f32_16x16x64_bf16 v[242:245], v[208:211], v[200:207], v212\n\t" \
    "ds_read_b128 v[200:203], v213 offset:3072\n\t" \
    "ds_read_b128 v[204:207], v213 offset:3200\n\t" \
    "v_smfmac_f32_16x16x64_bf16 v[246:249], v[208:211], v[214:221], v212\n\t" \
    "ds_read_b128 v[214:217], v213 offset:4096\n\t" \
    "ds_read_b128 v[218:221], v213 offset:4224\n\t" \
    "s_waitcnt lgkmcnt(4)\n\t" \
    "v_smfmac_f32_16x16x64_bf16 v[242:245], v[238:241], v[222:229], v212\n\t" \
    "ds_read_b128 v[222:225], v254 offset:22528\n\t" \
    "ds_read_b128 v[226:229], v254 offset:23552\n\t" \
    "v_smfmac_f32_16x16x64_bf16 v[246:249], v[238:241], v[230:237], v212\n\t" \
    "ds_read_b128 v[230:233], v254 offset:28672\n\t" \
    "ds_read_b128 v[234:237], v254 offset:29696\n\t" \
    "s_waitcnt lgkmcnt(6)\n\t" \
    "v_smfmac_f32_16x16x64_bf16 v[250:253], v[208:211], v[200:207], v212\n\t" \
    "ds_read_b128 v[208:211], v254 offset:2048\n\t" \
    "ds_read_b128 v[200:203], v213 offset:5120\n\t" \
    "ds_read_b128 v[204:207], v213 offset:5248\n\t" \
    "s_waitcnt lgkmcnt(7)\n\t" \
    "v_smfmac_f32_16x16x64_bf16 v[250:253], v[238:241], v[214:221], v212\n\t" \
    "v_lshrrev_b32 v238, 3, v254\n\t" \
    "v_and_b32 v238, 0x6e, v238\n\t" \
    "v_lshrrev_b32 v239, 16, v255\n\t" \
    "s_waitcnt lgkmcnt(2)\n\t" \
    "v_smfmac_f32_16x16x64_bf16 v[242:245], v[208:211], v[222:229], v212\n\t" \
    "v_smfmac_f32_16x16x64_bf16 v[246:249], v[208:211], v[230:237], v212\n\t" \
    "s_waitcnt lgkmcnt(0)\n\t" \
    "v_smfmac_f32_16x16x64_bf16 v[250:253], v[208:211], v[200:207], v212\n\t" \
    "s_nop 1\n\t" \
    "v_add_f32 v242, v242, v244\n\t" \
    "v_add_f32 v243, v243, v245\n\t" \
    "s_nop 0\n\t" \
    "v_add_f32 v246, v246, v248\n\t" \
    "v_add_f32 v247, v247, v249\n\t" \
    "s_nop 0\n\t" \
    "v_add_f32 v250, v250, v252\n\t" \
    "v_add_f32 v251, v251, v253\n\t" \
    "v_max3_f32 v244, v242, v246, v250\n\t" \
    "v_max3_f32 v245, v243, v247, v251\n\t" \
    "s_nop 0\n\t" \
    "v_max_f32_dpp v244, v244, v244 quad_perm:[1,0,3,2] row_mask:0xf bank_mask:0xf\n\t" \
    "v_max_f32_dpp v245, v245, v245 quad_perm:[1,0,3,2] row_mask:0xf bank_mask:0xf\n\t" \
    "s_nop 0\n\t" \
    "v_max_f32_dpp v244, v244, v244 quad_perm:[2,3,0,1] row_mask:0xf bank_mask:0xf\n\t" \
    "v_max_f32_dpp v245, v245, v245 quad_perm:[2,3,0,1] row_mask:0xf bank_mask:0xf\n\t" \
    "s_nop 0\n\t" \
    "v_max_f32_dpp v244, v244, v244 row_half_mirror row_mask:0xf bank_mask:0xf\n\t" \
    "v_max_f32_dpp v245, v245, v245 row_half_mirror row_mask:0xf bank_mask:0xf\n\t" \
    "s_nop 0\n\t" \
    "v_max_f32_dpp v244, v244, v244 row_mirror row_mask:0xf bank_mask:0xf\n\t" \
    "v_max_f32_dpp v245, v245, v245 row_mirror row_mask:0xf bank_mask:0xf\n\t" \
    "v_mul_f32 v244, %[scl], v244\n\t" \
    "v_mul_f32 v245, %[scl], v245\n\t" \
    "v_fma_f32 v242, v242, %[scl], -v244\n\t" \
    "v_fma_f32 v243, v243, %[scl], -v245\n\t" \
    "v_fma_f32 v246, v246, %[scl], -v244\n\t" \
    "v_fma_f32 v247, v247, %[scl], -v245\n\t" \
    "v_exp_f32 v242, v242\n\t" \
    "v_exp_f32 v243, v243\n\t" \
    "v_fma_f32 v250, v250, %[scl], -v244\n\t" \
    "v_fma_f32 v251, v251, %[scl], -v245\n\t" \
    "v_exp_f32 v246, v246\n\t" \
    "v_exp_f32 v247, v247\n\t" \
    "v_exp_f32 v250, v250\n\t" \
    "v_exp_f32 v251, v251\n\t" \
    "v_add_f32 v244, v242, v246\n\t" \
    "v_add_f32 v245, v243, v247\n\t" \
    "v_fma_f32 v244, v250, 0.5, v244\n\t" \
    "v_fma_f32 v245, v251, 0.5, v245\n\t" \
    "s_nop 0\n\t" \
    "v_add_f32_dpp v244, v244, v244 quad_perm:[1,0,3,2] row_mask:0xf bank_mask:0xf\n\t" \
    "v_add_f32_dpp v245, v245, v245 quad_perm:[1,0,3,2] row_mask:0xf bank_mask:0xf\n\t" \
    "s_nop 0\n\t" \
    "v_add_f32_dpp v244, v244, v244 quad_perm:[2,3,0,1] row_mask:0xf bank_mask:0xf\n\t" \
    "v_add_f32_dpp v245, v245, v245 quad_perm:[2,3,0,1] row_mask:0xf bank_mask:0xf\n\t" \
    "s_nop 0\n\t" \
    "v_add_f32_dpp v244, v244, v244 row_half_mirror row_mask:0xf bank_mask:0xf\n\t" \
    "v_add_f32_dpp v245, v245, v245 row_half_mirror row_mask:0xf bank_mask:0xf\n\t" \
    "s_nop 0\n\t" \
    "v_add_f32_dpp v244, v244, v244 row_mirror row_mask:0xf bank_mask:0xf\n\t" \
    "v_add_f32_dpp v245, v245, v245 row_mirror row_mask:0xf bank_mask:0xf\n\t" \
    "v_rcp_f32 v244, v244\n\t" \
    "v_rcp_f32 v245, v245\n\t" \
    "s_wakeup\n\t" \
    "v_mul_f32 v250, v250, v244\n\t" \
    "v_mul_f32 v251, v251, v245\n\t" \
    "v_mul_f32 v242, v242, v244\n\t" \
    "v_mul_f32 v243, v243, v245\n\t" \
    "v_mul_f32 v246, v246, v244\n\t" \
    "v_mul_f32 v247, v247, v245\n\t" \
    "v_cvt_pk_bf16_f32 v250, |v250|, |v251|\n\t" \
    "ds_write_b16 v238, v250 offset:52096\n\t" \
    "ds_write_b16_d16_hi v238, v250 offset:52112\n\t" \
    "v_cvt_pk_bf16_f32 v242, |v242|, |v246|\n\t" \
    "v_cvt_pk_bf16_f32 v243, |v243|, |v247|\n\t" \
    "ds_write2_b32 v239, v242, v243 offset1:4\n\t" \
    "ds_read_b128 v[200:203], v254 offset:896\n\t" \
    "v_and_b32 v253, 0x3ff, v254\n\t" \
    "s_mov_b32 exec_hi, 0xff\n\t" \
    "s_waitcnt lgkmcnt(0)\n\t" \
    "buffer_store_dwordx4 v[200:203], v254, s[60:63], s50 offen sc1\n\t" \
    "buffer_store_dwordx4 v[200:203], v254, s[60:63], s51 offen sc1\n\t" \
    "buffer_store_dwordx4 v[200:203], v254, s[60:63], s52 offen sc1\n\t" \
    "buffer_store_dwordx4 v[200:203], v254, s[60:63], s53 offen sc1\n\t" \
    "s_mov_b32 exec_hi, -1\n\t" \
    "s_setprio 0\n\t" \
    LST5 \
    "ds_read_b128 v[224:227], v253 offset:0\n\t" \
    "ds_read_b128 v[228:231], v253 offset:1024\n\t" \
    "ds_read_b128 v[232:235], v253 offset:20480\n\t" \
    "ds_read_b128 v[236:239], v253 offset:21504\n\t" \
    "s_add_u32 s40, s40, 1\n\t" \
    "s_cmp_lg_u32 s40, 64\n\t" \
    "s_cbranch_scc1 .Ls03_%=\n\t" \
    "s_branch .Lend%=\n\t" \
    ".Lend%=:\n\t" \
    "s_waitcnt vmcnt(0) lgkmcnt(0)\n\t" \
    "s_mov_b32 %[bud], s41\n\t"
#define ASM_V_LOOP \
    "s_mov_b64 s[60:61], %[ws]\n\t" \
    "s_mov_b32 s62, %[sz]\n\t" \
    "s_mov_b32 s63, 0x20000\n\t" \
    "s_mov_b32 s40, 0\n\t" \
    "s_mov_b32 s41, 0x20000\n\t" \
    LSTINIT \
    LDELAY \
    "s_cmp_eq_u32 %[wave], 0\n\t" \
    "s_cbranch_scc1 .Lv0_%=\n\t" \
    "s_cmp_eq_u32 %[wave], 1\n\t" \
    "s_cbranch_scc1 .Lv1_%=\n\t" \
    "s_cmp_eq_u32 %[wave], 2\n\t" \
    "s_cbranch_scc1 .Lv2_%=\n\t" \
    "s_cmp_eq_u32 %[wave], 3\n\t" \
    "s_cbranch_scc1 .Lv3_%=\n\t" \
    ".Lv4_%=:\n\t" \
    "s_add_u32 m0, %[wm], 0x0\n\t" \
    "s_mov_b32 s44, 0x0\n\t" \
    "buffer_load_dwordx4 v240, %[wr], s44 offen offset:0 nt lds\n\t" \
    "buffer_load_dwordx4 v241, %[wr], s44 offen offset:1024 nt lds\n\t" \
    "buffer_load_dwordx4 v242, %[wr], s44 offen offset:2048 nt lds\n\t" \
    "buffer_load_dwordx4 v243, %[wr], s44 offen offset:3072 nt lds\n\t" \
    "s_add_u32 m0, %[wm], 0x1000\n\t" \
    "s_nop 0\n\t" \
    "buffer_load_dwordx4 v244, %[wr], s44 offen nt lds\n\t" \
    "s_add_u32 m0, %[wm], 0x1400\n\t" \
    "s_mov_b32 s44, 0x5000\n\t" \
    "buffer_load_dwordx4 v240, %[wr], s44 offen offset:0 nt lds\n\t" \
    "buffer_load_dwordx4 v241, %[wr], s44 offen offset:1024 nt lds\n\t" \
    "buffer_load_dwordx4 v242, %[wr], s44 offen offset:2048 nt lds\n\t" \
    "buffer_load_dwordx4 v243, %[wr], s44 offen offset:3072 nt lds\n\t" \
    "s_add_u32 m0, %[wm], 0x2400\n\t" \
    "s_nop 0\n\t" \
    "buffer_load_dwordx4 v244, %[wr], s44 offen nt lds\n\t" \
    "s_add_u32 m0, %[wm], 0x2800\n\t" \
    "s_mov_b32 s44, 0xa000\n\t" \
    "buffer_load_dwordx4 v240, %[wr], s44 offen offset:0 nt lds\n\t" \
    "buffer_load_dwordx4 v241, %[wr], s44 offen offset:1024 nt lds\n\t" \
    "buffer_load_dwordx4 v242, %[wr], s44 offen offset:2048 nt lds\n\t" \
    "buffer_load_dwordx4 v243, %[wr], s44 offen offset:3072 nt lds\n\t" \
    "s_add_u32 m0, %[wm], 0x3800\n\t" \
    "s_nop 0\n\t" \
    "buffer_load_dwordx4 v244, %[wr], s44 offen nt lds\n\t" \
    "s_add_u32 m0, %[wm], 0x3c00\n\t" \
    "s_mov_b32 s44, 0xf000\n\t" \
    "buffer_load_dwordx4 v240, %[wr], s44 offen offset:0 nt lds\n\t" \
    "buffer_load_dwordx4 v241, %[wr], s44 offen offset:1024 nt lds\n\t" \
    "buffer_load_dwordx4 v242, %[wr], s44 offen offset:2048 nt lds\n\t" \
    "buffer_load_dwordx4 v243, %[wr], s44 offen offset:3072 nt lds\n\t" \
    "s_add_u32 m0, %[wm], 0x4c00\n\t" \
    "s_nop 0\n\t" \
    "buffer_load_dwordx4 v244, %[wr], s44 offen nt lds\n\t" \
    "s_barrier \n\t" \
    "s_waitcnt vmcnt(15)\n\t" \
    "ds_read_b64_tr_b16 v[200:201], v245 offset:0\n\t" \
    "ds_read_b64_tr_b16 v[16:17], v245 offset:32\n\t" \
    "ds_read_b64_tr_b16 v[208:209], v245 offset:64\n\t" \
    "ds_read_b64_tr_b16 v[56:57], v245 offset:96\n\t" \
    "ds_read_b64_tr_b16 v[216:217], v245 offset:128\n\t" \
    "ds_read_b64_tr_b16 v[96:97], v245 offset:160\n\t" \
    "ds_read_b64_tr_b16 v[224:225], v245 offset:192\n\t" \
    "ds_read_b64_tr_b16 v[136:137], v245 offset:224\n\t" \
    "ds_read_b64_tr_b16 v[232:233], v245 offset:256\n\t" \
    "ds_read_b64_tr_b16 v[176:177], v245 offset:288\n\t" \
    "s_waitcnt lgkmcnt(0)\n\t" \
    "s_add_u32 m0, %[wm], 0x0\n\t" \
    "s_mov_b32 s44, 0x14000\n\t" \
    "buffer_load_dwordx4 v240, %[wr], s44 offen offset:0 nt lds\n\t" \
    "buffer_load_dwordx4 v241, %[wr], s44 offen offset:1024 nt lds\n\t" \
    "buffer_load_dwordx4 v242, %[wr], s44 offen offset:2048 nt lds\n\t" \
    "buffer_load_dwordx4 v243, %[wr], s44 offen offset:3072 nt lds\n\t" \
    "s_add_u32 m0, %[wm], 0x1000\n\t" \
    "s_nop 0\n\t" \
    "buffer_load_dwordx4 v244, %[wr], s44 offen nt lds\n\t" \
    "s_barrier \n\t" \
    "s_barrier \n\t" \
    "s_waitcnt vmcnt(15)\n\t" \
    "ds_read_b64_tr_b16 v[204:205], v245 offset:5120\n\t" \
    "ds_read_b64_tr_b16 v[20:21], v245 offset:5152\n\t" \
    "ds_read_b64_tr_b16 v[212:213], v245 offset:5184\n\t" \
    "ds_read_b64_tr_b16 v[60:61], v245 offset:5216\n\t" \
    "ds_read_b64_tr_b16 v[220:221], v245 offset:5248\n\t" \
    "ds_read_b64_tr_b16 v[100:101], v245 offset:5280\n\t" \
    "ds_read_b64_tr_b16 v[228:229], v245 offset:5312\n\t" \
    "ds_read_b64_tr_b16 v[140:141], v245 offset:5344\n\t" \
    "ds_read_b64_tr_b16 v[236:237], v245 offset:5376\n\t" \
    "ds_read_b64_tr_b16 v[180:181], v245 offset:5408\n\t" \
    "s_waitcnt lgkmcnt(0)\n\t" \
    "s_add_u32 m0, %[wm], 0x1400\n\t" \
    "s_mov_b32 s44, 0x19000\n\t" \
    "buffer_load_dwordx4 v240, %[wr], s44 offen offset:0 nt lds\n\t" \
    "buffer_load_dwordx4 v241, %[wr], s44 offen offset:1024 nt lds\n\t" \
    "buffer_load_dwordx4 v242, %[wr], s44 offen offset:2048 nt lds\n\t" \
    "buffer_load_dwordx4 v243, %[wr], s44 offen offset:3072 nt lds\n\t" \
    "s_add_u32 m0, %[wm], 0x2400\n\t" \
    "s_nop 0\n\t" \
    "buffer_load_dwordx4 v244, %[wr], s44 offen nt lds\n\t" \
    "s_barrier \n\t" \
    "s_barrier \n\t" \
    "s_waitcnt vmcnt(15)\n\t" \
    "ds_read_b64_tr_b16 v[0:1], v245 offset:10240\n\t" \
    "ds_read_b64_tr_b16 v[24:25], v245 offset:10272\n\t" \
    "ds_read_b64_tr_b16 v[40:41], v245 offset:10304\n\t" \
    "ds_read_b64_tr_b16 v[64:65], v245 offset:10336\n\t" \
    "ds_read_b64_tr_b16 v[80:81], v245 offset:10368\n\t" \
    "ds_read_b64_tr_b16 v[104:105], v245 offset:10400\n\t" \
    "ds_read_b64_tr_b16 v[120:121], v245 offset:10432\n\t" \
    "ds_read_b64_tr_b16 v[144:145], v245 offset:10464\n\t" \
    "ds_read_b64_tr_b16 v[160:161], v245 offset:10496\n\t" \
    "ds_read_b64_tr_b16 v[184:185], v245 offset:10528\n\t" \
    "s_waitcnt lgkmcnt(0)\n\t" \
    "s_add_u32 m0, %[wm], 0x2800\n\t" \
    "s_mov_b32 s44, 0x1e000\n\t" \
    "buffer_load_dwordx4 v240, %[wr], s44 offen offset:0 nt lds\n\t" \
    "buffer_load_dwordx4 v241, %[wr], s44 offen offset:1024 nt lds\n\t" \
    "buffer_load_dwordx4 v242, %[wr], s44 offen offset:2048 nt lds\n\t" \
    "buffer_load_dwordx4 v243, %[wr], s44 offen offset:3072 nt lds\n\t" \
    "s_add_u32 m0, %[wm], 0x3800\n\t" \
    "s_nop 0\n\t" \
    "buffer_load_dwordx4 v244, %[wr], s44 offen nt lds\n\t" \
    "s_barrier \n\t" \
    "s_waitcnt vmcnt(15)\n\t" \
    "ds_read_b64_tr_b16 v[4:5], v245 offset:15360\n\t" \
    "ds_read_b64_tr_b16 v[28:29], v245 offset:15392\n\t" \
    "ds_read_b64_tr_b16 v[44:45], v245 offset:15424\n\t" \
    "ds_read_b64_tr_b16 v[68:69], v245 offset:15456\n\t" \
    "ds_read_b64_tr_b16 v[84:85], v245 offset:15488\n\t" \
    "ds_read_b64_tr_b16 v[108:109], v245 offset:15520\n\t" \
    "ds_read_b64_tr_b16 v[124:125], v245 offset:15552\n\t" \
    "ds_read_b64_tr_b16 v[148:149], v245 offset:15584\n\t" \
    "ds_read_b64_tr_b16 v[164:165], v245 offset:15616\n\t" \
    "ds_read_b64_tr_b16 v[188:189], v245 offset:15648\n\t" \
    "s_waitcnt lgkmcnt(0)\n\t" \
    "s_add_u32 m0, %[wm], 0x3c00\n\t" \
    "s_mov_b32 s44, 0x23000\n\t" \
    "buffer_load_dwordx4 v240, %[wr], s44 offen offset:0 nt lds\n\t" \
    "buffer_load_dwordx4 v241, %[wr], s44 offen offset:1024 nt lds\n\t" \
    "buffer_load_dwordx4 v242, %[wr], s44 offen offset:2048 nt lds\n\t" \
    "buffer_load_dwordx4 v243, %[wr], s44 offen offset:3072 nt lds\n\t" \
    "s_add_u32 m0, %[wm], 0x4c00\n\t" \
    "s_nop 0\n\t" \
    "buffer_load_dwordx4 v244, %[wr], s44 offen nt lds\n\t" \
    "s_barrier \n\t" \
    "s_barrier \n\t" \
    "s_waitcnt vmcnt(15)\n\t" \
    "ds_read_b64_tr_b16 v[8:9], v245 offset:0\n\t" \
    "ds_read_b64_tr_b16 v[32:33], v245 offset:32\n\t" \
    "ds_read_b64_tr_b16 v[48:49], v245 offset:64\n\t" \
    "ds_read_b64_tr_b16 v[72:73], v245 offset:96\n\t" \
    "ds_read_b64_tr_b16 v[88:89], v245 offset:128\n\t" \
    "ds_read_b64_tr_b16 v[112:113], v245 offset:160\n\t" \
    "ds_read_b64_tr_b16 v[128:129], v245 offset:192\n\t" \
    "ds_read_b64_tr_b16 v[152:153], v245 offset:224\n\t" \
    "ds_read_b64_tr_b16 v[168:169], v245 offset:256\n\t" \
    "ds_read_b64_tr_b16 v[192:193], v245 offset:288\n\t" \
    "v_mov_b32 v246, 0\n\t" \
    "buffer_load_dword v246, v246, s[60:63], %[mo] offen sc1\n\t" \
    "s_waitcnt lgkmcnt(0)\n\t" \
    "s_add_u32 m0, %[wm], 0x0\n\t" \
    "s_mov_b32 s44, 0x28000\n\t" \
    "buffer_load_dwordx4 v240, %[wr], s44 offen offset:0 nt lds\n\t" \
    "buffer_load_dwordx4 v241, %[wr], s44 offen offset:1024 nt lds\n\t" \
    "buffer_load_dwordx4 v242, %[wr], s44 offen offset:2048 nt lds\n\t" \
    "buffer_load_dwordx4 v243, %[wr], s44 offen offset:3072 nt lds\n\t" \
    "s_add_u32 m0, %[wm], 0x1000\n\t" \
    "s_nop 0\n\t" \
    "buffer_load_dwordx4 v244, %[wr], s44 offen nt lds\n\t" \
    "s_barrier \n\t" \
    "s_barrier \n\t" \
    "s_waitcnt vmcnt(15)\n\t" \
    "ds_read_b64_tr_b16 v[12:13], v245 offset:5120\n\t" \
    "ds_read_b64_tr_b16 v[36:37], v245 offset:5152\n\t" \
    "ds_read_b64_tr_b16 v[52:53], v245 offset:5184\n\t" \
    "ds_read_b64_tr_b16 v[76:77], v245 offset:5216\n\t" \
    "ds_read_b64_tr_b16 v[92:93], v245 offset:5248\n\t" \
    "ds_read_b64_tr_b16 v[116:117], v245 offset:5280\n\t" \
    "ds_read_b64_tr_b16 v[132:133], v245 offset:5312\n\t" \
    "ds_read_b64_tr_b16 v[156:157], v245 offset:5344\n\t" \
    "ds_read_b64_tr_b16 v[172:173], v245 offset:5376\n\t" \
    "ds_read_b64_tr_b16 v[196:197], v245 offset:5408\n\t" \
    "s_waitcnt lgkmcnt(0)\n\t" \
    "s_add_u32 m0, %[wm], 0x1400\n\t" \
    "s_mov_b32 s44, 0x2d000\n\t" \
    "buffer_load_dwordx4 v240, %[wr], s44 offen offset:0 nt lds\n\t" \
    "buffer_load_dwordx4 v241, %[wr], s44 offen offset:1024 nt lds\n\t" \
    "buffer_load_dwordx4 v242, %[wr], s44 offen offset:2048 nt lds\n\t" \
    "buffer_load_dwordx4 v243, %[wr], s44 offen offset:3072 nt lds\n\t" \
    "s_add_u32 m0, %[wm], 0x2400\n\t" \
    "s_nop 0\n\t" \
    "buffer_load_dwordx4 v244, %[wr], s44 offen nt lds\n\t" \
    "s_barrier \n\t" \
    "s_waitcnt vmcnt(15)\n\t" \
    "ds_read_b64_tr_b16 v[202:203], v245 offset:10240\n\t" \
    "ds_read_b64_tr_b16 v[18:19], v245 offset:10272\n\t" \
    "ds_read_b64_tr_b16 v[210:211], v245 offset:10304\n\t" \
    "ds_read_b64_tr_b16 v[58:59], v245 offset:10336\n\t" \
    "ds_read_b64_tr_b16 v[218:219], v245 offset:10368\n\t" \
    "ds_read_b64_tr_b16 v[98:99], v245 offset:10400\n\t" \
    "ds_read_b64_tr_b16 v[226:227], v245 offset:10432\n\t" \
    "ds_read_b64_tr_b16 v[138:139], v245 offset:10464\n\t" \
    "ds_read_b64_tr_b16 v[234:235], v245 offset:10496\n\t" \
    "ds_read_b64_tr_b16 v[178:179], v245 offset:10528\n\t" \
    "s_waitcnt lgkmcnt(0)\n\t" \
    "s_add_u32 m0, %[wm], 0x2800\n\t" \
    "s_mov_b32 s44, 0x32000\n\t" \
    "buffer_load_dwordx4 v240, %[wr], s44 offen offset:0 nt lds\n\t" \
    "buffer_load_dwordx4 v241, %[wr], s44 offen offset:1024 nt lds\n\t" \
    "buffer_load_dwordx4 v242, %[wr], s44 offen offset:2048 nt lds\n\t" \
    "buffer_load_dwordx4 v243, %[wr], s44 offen offset:3072 nt lds\n\t" \
    "s_add_u32 m0, %[wm], 0x3800\n\t" \
    "s_nop 0\n\t" \
    "buffer_load_dwordx4 v244, %[wr], s44 offen nt lds\n\t" \
    "s_barrier \n\t" \
    "s_barrier \n\t" \
    "s_waitcnt vmcnt(15)\n\t" \
    "ds_read_b64_tr_b16 v[206:207], v245 offset:15360\n\t" \
    "ds_read_b64_tr_b16 v[22:23], v245 offset:15392\n\t" \
    "ds_read_b64_tr_b16 v[214:215], v245 offset:15424\n\t" \
    "ds_read_b64_tr_b16 v[62:63], v245 offset:15456\n\t" \
    "ds_read_b64_tr_b16 v[222:223], v245 offset:15488\n\t" \
    "ds_read_b64_tr_b16 v[102:103], v245 offset:15520\n\t" \
    "ds_read_b64_tr_b16 v[230:231], v245 offset:15552\n\t" \
    "ds_read_b64_tr_b16 v[142:143], v245 offset:15584\n\t" \
    "ds_read_b64_tr_b16 v[238:239], v245 offset:15616\n\t" \
    "ds_read_b64_tr_b16 v[182:183], v245 offset:15648\n\t" \
    "s_waitcnt lgkmcnt(0)\n\t" \
    "s_add_u32 m0, %[wm], 0x3c00\n\t" \
    "s_mov_b32 s44, 0x37000\n\t" \
    "buffer_load_dwordx4 v240, %[wr], s44 offen offset:0 nt lds\n\t" \
    "buffer_load_dwordx4 v241, %[wr], s44 offen offset:1024 nt lds\n\t" \
    "buffer_load_dwordx4 v242, %[wr], s44 offen offset:2048 nt lds\n\t" \
    "buffer_load_dwordx4 v243, %[wr], s44 offen offset:3072 nt lds\n\t" \
    "s_add_u32 m0, %[wm], 0x4c00\n\t" \
    "s_nop 0\n\t" \
    "buffer_load_dwordx4 v244, %[wr], s44 offen nt lds\n\t" \
    "s_barrier \n\t" \
    "s_barrier \n\t" \
    "s_waitcnt vmcnt(15)\n\t" \
    "ds_read_b64_tr_b16 v[2:3], v245 offset:0\n\t" \
    "ds_read_b64_tr_b16 v[26:27], v245 offset:32\n\t" \
    "ds_read_b64_tr_b16 v[42:43], v245 offset:64\n\t" \
    "ds_read_b64_tr_b16 v[66:67], v245 offset:96\n\t" \
    "ds_read_b64_tr_b16 v[82:83], v245 offset:128\n\t" \
    "ds_read_b64_tr_b16 v[106:107], v245 offset:160\n\t" \
    "ds_read_b64_tr_b16 v[122:123], v245 offset:192\n\t" \
    "ds_read_b64_tr_b16 v[146:147], v245 offset:224\n\t" \
    "ds_read_b64_tr_b16 v[162:163], v245 offset:256\n\t" \
    "ds_read_b64_tr_b16 v[186:187], v245 offset:288\n\t" \
    ".Lv41_%=:\n\t" \
    "v_readfirstlane_b32 s43, v246\n\t" \
    "s_nop 0\n\t" \
    "s_cmp_lg_u32 s43, -1\n\t" \
    "s_cbranch_scc1 .Lv42_%=\n\t" \
    "s_cmp_eq_u32 s41, 0\n\t" \
    "s_cbranch_scc1 .Lv42_%=\n\t" \
    "s_sub_u32 s41, s41, 1\n\t" \
    "s_sleep 4\n\t" \
    "v_mov_b32 v246, 0\n\t" \
    "buffer_load_dword v246, v246, s[60:63], %[mo] offen sc1\n\t" \
    "s_waitcnt vmcnt(0)\n\t" \
    "s_branch .Lv41_%=\n\t" \
    ".Lv42_%=:\n\t" \
    "v_mov_b32 v246, 0x8000\n\t" \
    "s_mov_b64 exec, 1\n\t" \
    "buffer_store_dword v246, v246, s[60:63], %[mo] offen sc1\n\t" \
    "s_mov_b64 exec, -1\n\t" \
    "s_and_b32 s43, s43, 0x2000\n\t" \
    "s_xor_b32 s43, s43, %[lx]\n\t" \
    "s_add_u32 s60, s60, s43\n\t" \
    "s_addc_u32 s61, s61, 0\n\t" \
    "s_barrier \n\t" \
    "s_waitcnt vmcnt(10)\n\t" \
    "ds_read_b64_tr_b16 v[6:7], v245 offset:5120\n\t" \
    "ds_read_b64_tr_b16 v[30:31], v245 offset:5152\n\t" \
    "ds_read_b64_tr_b16 v[46:47], v245 offset:5184\n\t" \
    "ds_read_b64_tr_b16 v[70:71], v245 offset:5216\n\t" \
    "ds_read_b64_tr_b16 v[86:87], v245 offset:5248\n\t" \
    "ds_read_b64_tr_b16 v[110:111], v245 offset:5280\n\t" \
    "ds_read_b64_tr_b16 v[126:127], v245 offset:5312\n\t" \
    "ds_read_b64_tr_b16 v[150:151], v245 offset:5344\n\t" \
    "ds_read_b64_tr_b16 v[166:167], v245 offset:5376\n\t" \
    "ds_read_b64_tr_b16 v[190:191], v245 offset:5408\n\t" \
    "s_barrier \n\t" \
    "s_barrier \n\t" \
    "s_waitcnt vmcnt(5)\n\t" \
    "ds_read_b64_tr_b16 v[10:11], v245 offset:10240\n\t" \
    "ds_read_b64_tr_b16 v[34:35], v245 offset:10272\n\t" \
    "ds_read_b64_tr_b16 v[50:51], v245 offset:10304\n\t" \
    "ds_read_b64_tr_b16 v[74:75], v245 offset:10336\n\t" \
    "ds_read_b64_tr_b16 v[90:91], v245 offset:10368\n\t" \
    "ds_read_b64_tr_b16 v[114:115], v245 offset:10400\n\t" \
    "ds_read_b64_tr_b16 v[130:131], v245 offset:10432\n\t" \
    "ds_read_b64_tr_b16 v[154:155], v245 offset:10464\n\t" \
    "ds_read_b64_tr_b16 v[170:171], v245 offset:10496\n\t" \
    "ds_read_b64_tr_b16 v[194:195], v245 offset:10528\n\t" \
    "s_barrier \n\t" \
    "s_barrier \n\t" \
    "s_waitcnt vmcnt(0)\n\t" \
    "ds_read_b64_tr_b16 v[14:15], v245 offset:15360\n\t" \
    "ds_read_b64_tr_b16 v[38:39], v245 offset:15392\n\t" \
    "ds_read_b64_tr_b16 v[54:55], v245 offset:15424\n\t" \
    "ds_read_b64_tr_b16 v[78:79], v245 offset:15456\n\t" \
    "ds_read_b64_tr_b16 v[94:95], v245 offset:15488\n\t" \
    "ds_read_b64_tr_b16 v[118:119], v245 offset:15520\n\t" \
    "ds_read_b64_tr_b16 v[134:135], v245 offset:15552\n\t" \
    "ds_read_b64_tr_b16 v[158:159], v245 offset:15584\n\t" \
    "ds_read_b64_tr_b16 v[174:175], v245 offset:15616\n\t" \
    "ds_read_b64_tr_b16 v[198:199], v245 offset:15648\n\t" \
    "s_waitcnt lgkmcnt(0)\n\t" \
    "s_barrier \n\t" \
    LST0 \
    "ds_write_b128 v254, v[200:203] offset:0\n\t" \
    "ds_write_b128 v254, v[204:207] offset:1024\n\t" \
    "ds_write_b128 v254, v[208:211] offset:2048\n\t" \
    "ds_write_b128 v254, v[212:215] offset:3072\n\t" \
    "ds_write_b128 v254, v[216:219] offset:4096\n\t" \
    "ds_write_b128 v254, v[220:223] offset:5120\n\t" \
    "ds_write_b128 v254, v[224:227] offset:6144\n\t" \
    "ds_write_b128 v254, v[228:231] offset:7168\n\t" \
    "ds_write_b128 v254, v[232:235] offset:8192\n\t" \
    "ds_write_b128 v254, v[236:239] offset:9216\n\t" \
    "s_waitcnt vmcnt(0) lgkmcnt(0)\n\t" \
    "s_barrier \n\t" \
    "v_mov_b64 v[200:201], 0\n\t" \
    "v_mov_b64 v[202:203], 0\n\t" \
    "ds_write_b128 v253, v[200:203] offset:3072\n\t" \
    "ds_write_b128 v253, v[200:203] offset:4096\n\t" \
    "v_bfe_i32 v252, v253, 5, 1\n\t" \
    "v_and_b32 v252, 0xaaaa, v252\n\t" \
    "v_or_b32 v252, 0x4444, v252\n\t" \
    ".Lv43_%=:\n\t" \
    "ds_read_b128 v[248:251], v254 offset:0\n\t" \
    "s_and_b32 s44, s40, %[vm]\n\t" \
    "s_lshl_b32 s44, s44, 20\n\t" \
    "s_and_b32 s45, s40, 2\n\t" \
    "s_lshl_b32 s45, s45, 12\n\t" \
    "s_add_u32 s44, s44, s45\n\t" \
    "s_add_u32 s42, s44, %[vx]\n\t" \
    "s_waitcnt lgkmcnt(0)\n\t" \
    "s_barrier \n\t" \
    "ds_read_b128 v[224:227], v253 offset:6144\n\t" \
    "ds_read_b128 v[232:235], v253 offset:8192\n\t" \
    "ds_read_b128 v[236:239], v253 offset:9216\n\t" \
    "ds_read_b128 v[240:243], v253 offset:10240\n\t" \
    "ds_read_b128 v[244:247], v253 offset:11264\n\t" \
    "ds_read_b128 v[228:231], v253 offset:7168\n\t" \
    "s_waitcnt lgkmcnt(5)\n\t" \
    "v_mfma_f32_16x16x32_bf16 v[200:203], v[224:227], v[16:19], 0\n\t" \
    "s_waitcnt lgkmcnt(4)\n\t" \
    "v_mfma_f32_16x16x32_bf16 v[200:203], v[232:235], v[24:27], v[200:203]\n\t" \
    "s_waitcnt lgkmcnt(3)\n\t" \
    "v_mfma_f32_16x16x32_bf16 v[200:203], v[236:239], v[28:31], v[200:203]\n\t" \
    "s_waitcnt lgkmcnt(2)\n\t" \
    "v_mfma_f32_16x16x32_bf16 v[200:203], v[240:243], v[32:35], v[200:203]\n\t" \
    "s_waitcnt lgkmcnt(1)\n\t" \
    "v_mfma_f32_16x16x32_bf16 v[200:203], v[244:247], v[36:39], v[200:203]\n\t" \
    "s_waitcnt lgkmcnt(0)\n\t" \
    "v_mfma_f32_16x16x32_bf16 v[200:203], v[228:231], v[20:23], v[200:203]\n\t" \
    "v_mfma_f32_16x16x32_bf16 v[204:207], v[224:227], v[248:251], 0\n\t" \
    "ds_read_b128 v[248:251], v254 offset:1024\n\t" \
    "v_mfma_f32_16x16x32_bf16 v[204:207], v[232:235], v[0:3], v[204:207]\n\t" \
    "v_mfma_f32_16x16x32_bf16 v[204:207], v[236:239], v[4:7], v[204:207]\n\t" \
    "v_mfma_f32_16x16x32_bf16 v[204:207], v[240:243], v[8:11], v[204:207]\n\t" \
    "v_mfma_f32_16x16x32_bf16 v[204:207], v[244:247], v[12:15], v[204:207]\n\t" \
    "s_waitcnt lgkmcnt(0)\n\t" \
    "v_mfma_f32_16x16x32_bf16 v[204:207], v[228:231], v[248:251], v[204:207]\n\t" \
    "ds_read_b128 v[248:251], v254 offset:2048\n\t" \
    "s_barrier \n\t" \
    LST7 \
    "v_mfma_f32_16x16x32_bf16 v[208:211], v[224:227], v[56:59], 0\n\t" \
    "v_mfma_f32_16x16x32_bf16 v[208:211], v[232:235], v[64:67], v[208:211]\n\t" \
    "v_mfma_f32_16x16x32_bf16 v[208:211], v[236:239], v[68:71], v[208:211]\n\t" \
    "v_mfma_f32_16x16x32_bf16 v[208:211], v[240:243], v[72:75], v[208:211]\n\t" \
    "v_mfma_f32_16x16x32_bf16 v[208:211], v[244:247], v[76:79], v[208:211]\n\t" \
    "v_mfma_f32_16x16x32_bf16 v[208:211], v[228:231], v[60:63], v[208:211]\n\t" \
    "s_waitcnt lgkmcnt(0)\n\t" \
    "v_mfma_f32_16x16x32_bf16 v[212:215], v[224:227], v[248:251], 0\n\t" \
    "ds_read_b128 v[248:251], v254 offset:3072\n\t" \
    "ds_read_b128 v[224:227], v253 offset:12288\n\t" \
    "v_mfma_f32_16x16x32_bf16 v[212:215], v[232:235], v[40:43], v[212:215]\n\t" \
    "ds_read_b128 v[232:235], v253 offset:14336\n\t" \
    "v_mfma_f32_16x16x32_bf16 v[212:215], v[236:239], v[44:47], v[212:215]\n\t" \
    "ds_read_b128 v[236:239], v253 offset:15360\n\t" \
    "v_mfma_f32_16x16x32_bf16 v[212:215], v[240:243], v[48:51], v[212:215]\n\t" \
    "ds_read_b128 v[240:243], v253 offset:16384\n\t" \
    "v_mfma_f32_16x16x32_bf16 v[212:215], v[244:247], v[52:55], v[212:215]\n\t" \
    "ds_read_b128 v[244:247], v253 offset:17408\n\t" \
    "s_waitcnt lgkmcnt(5)\n\t" \
    "v_mfma_f32_16x16x32_bf16 v[212:215], v[228:231], v[248:251], v[212:215]\n\t" \
    "ds_read_b128 v[248:251], v254 offset:4096\n\t" \
    "ds_read_b128 v[228:231], v253 offset:13312\n\t" \
    "s_waitcnt lgkmcnt(6)\n\t" \
    "v_mfma_f32_16x16x32_bf16 v[216:219], v[224:227], v[16:19], 0\n\t" \
    "s_waitcnt lgkmcnt(5)\n\t" \
    "v_mfma_f32_16x16x32_bf16 v[216:219], v[232:235], v[24:27], v[216:219]\n\t" \
    "s_waitcnt lgkmcnt(4)\n\t" \
    "v_mfma_f32_16x16x32_bf16 v[216:219], v[236:239], v[28:31], v[216:219]\n\t" \
    "s_waitcnt lgkmcnt(3)\n\t" \
    "v_mfma_f32_16x16x32_bf16 v[216:219], v[240:243], v[32:35], v[216:219]\n\t" \
    "s_waitcnt lgkmcnt(2)\n\t" \
    "v_mfma_f32_16x16x32_bf16 v[216:219], v[244:247], v[36:39], v[216:219]\n\t" \
    "s_waitcnt lgkmcnt(0)\n\t" \
    "v_mfma_f32_16x16x32_bf16 v[216:219], v[228:231], v[20:23], v[216:219]\n\t" \
    "v_mfma_f32_16x16x32_bf16 v[220:223], v[224:227], v[248:251], 0\n\t" \
    "ds_read_b128 v[248:251], v254 offset:5120\n\t" \
    "v_mfma_f32_16x16x32_bf16 v[220:223], v[232:235], v[80:83], v[220:223]\n\t" \
    "v_mfma_f32_16x16x32_bf16 v[220:223], v[236:239], v[84:87], v[220:223]\n\t" \
    "v_cvt_pk_bf16_f32 v200, v200, v216\n\t" \
    "v_cvt_pk_bf16_f32 v201, v201, v217\n\t" \
    "v_mfma_f32_16x16x32_bf16 v[220:223], v[240:243], v[88:91], v[220:223]\n\t" \
    "v_cvt_pk_bf16_f32 v202, v202, v218\n\t" \
    "v_cvt_pk_bf16_f32 v203, v203, v219\n\t" \
    "v_mfma_f32_16x16x32_bf16 v[220:223], v[244:247], v[92:95], v[220:223]\n\t" \
    "s_add_u32 s44, s42, 0x10000\n\t" \
    "s_waitcnt lgkmcnt(0)\n\t" \
    "v_mfma_f32_16x16x32_bf16 v[220:223], v[228:231], v[248:251], v[220:223]\n\t" \
    "ds_read_b128 v[248:251], v254 offset:0\n\t" \
    "buffer_store_dwordx4 v[200:203], v253, s[60:63], s44 offen sc1\n\t" \
    "v_mfma_f32_16x16x32_bf16 v[216:219], v[224:227], v[56:59], 0\n\t" \
    "v_mfma_f32_16x16x32_bf16 v[216:219], v[232:235], v[64:67], v[216:219]\n\t" \
    "v_mfma_f32_16x16x32_bf16 v[216:219], v[236:239], v[68:71], v[216:219]\n\t" \
    "v_mfma_f32_16x16x32_bf16 v[216:219], v[240:243], v[72:75], v[216:219]\n\t" \
    "v_mfma_f32_16x16x32_bf16 v[216:219], v[244:247], v[76:79], v[216:219]\n\t" \
    "v_mfma_f32_16x16x32_bf16 v[216:219], v[228:231], v[60:63], v[216:219]\n\t" \
    "s_waitcnt lgkmcnt(0)\n\t" \
    "v_mfma_f32_16x16x32_bf16 v[200:203], v[224:227], v[248:251], 0\n\t" \
    "ds_read_b128 v[248:251], v254 offset:1024\n\t" \
    "v_mfma_f32_16x16x32_bf16 v[200:203], v[232:235], v[0:3], v[200:203]\n\t" \
    "v_mfma_f32_16x16x32_bf16 v[200:203], v[236:239], v[4:7], v[200:203]\n\t" \
    "v_cvt_pk_bf16_f32 v208, v208, v216\n\t" \
    "v_cvt_pk_bf16_f32 v209, v209, v217\n\t" \
    "v_mfma_f32_16x16x32_bf16 v[200:203], v[240:243], v[8:11], v[200:203]\n\t" \
    "v_cvt_pk_bf16_f32 v210, v210, v218\n\t" \
    "v_cvt_pk_bf16_f32 v211, v211, v219\n\t" \
    "v_mfma_f32_16x16x32_bf16 v[200:203], v[244:247], v[12:15], v[200:203]\n\t" \
    "s_add_u32 s44, s42, 0x30000\n\t" \
    "s_waitcnt lgkmcnt(0)\n\t" \
    "v_mfma_f32_16x16x32_bf16 v[200:203], v[228:231], v[248:251], v[200:203]\n\t" \
    "ds_read_b128 v[248:251], v254 offset:2048\n\t" \
    "buffer_store_dwordx4 v[208:211], v253, s[60:63], s44 offen sc1\n\t" \
    "v_mfma_f32_16x16x32_bf16 v[216:219], v[224:227], v[96:99], 0\n\t" \
    "v_mfma_f32_16x16x32_bf16 v[216:219], v[232:235], v[104:107], v[216:219]\n\t" \
    "v_mfma_f32_16x16x32_bf16 v[216:219], v[236:239], v[108:111], v[216:219]\n\t" \
    "v_cvt_pk_bf16_f32 v204, v204, v200\n\t" \
    "v_mfma_f32_16x16x32_bf16 v[216:219], v[240:243], v[112:115], v[216:219]\n\t" \
    "v_cvt_pk_bf16_f32 v205, v205, v201\n\t" \
    "v_cvt_pk_bf16_f32 v206, v206, v202\n\t" \
    "v_mfma_f32_16x16x32_bf16 v[216:219], v[244:247], v[116:119], v[216:219]\n\t" \
    "v_cvt_pk_bf16_f32 v207, v207, v203\n\t" \
    "v_mfma_f32_16x16x32_bf16 v[216:219], v[228:231], v[100:103], v[216:219]\n\t" \
    "s_add_u32 s44, s42, 0x0\n\t" \
    "buffer_store_dwordx4 v[204:207], v253, s[60:63], s44 offen sc1\n\t" \
    "s_waitcnt lgkmcnt(0)\n\t" \
    "v_mfma_f32_16x16x32_bf16 v[208:211], v[224:227], v[248:251], 0\n\t" \
    "ds_read_b128 v[248:251], v254 offset:3072\n\t" \
    "v_mfma_f32_16x16x32_bf16 v[208:211], v[232:235], v[40:43], v[208:211]\n\t" \
    "v_mfma_f32_16x16x32_bf16 v[208:211], v[236:239], v[44:47], v[208:211]\n\t" \
    "v_mfma_f32_16x16x32_bf16 v[208:211], v[240:243], v[48:51], v[208:211]\n\t" \
    "v_mfma_f32_16x16x32_bf16 v[208:211], v[244:247], v[52:55], v[208:211]\n\t" \
    "s_waitcnt lgkmcnt(0)\n\t" \
    "v_mfma_f32_16x16x32_bf16 v[208:211], v[228:231], v[248:251], v[208:211]\n\t" \
    "ds_read_b128 v[248:251], v254 offset:6144\n\t" \
    "v_mfma_f32_16x16x32_bf16 v[200:203], v[224:227], v[136:139], 0\n\t" \
    "v_mfma_f32_16x16x32_bf16 v[200:203], v[232:235], v[144:147], v[200:203]\n\t" \
    "v_mfma_f32_16x16x32_bf16 v[200:203], v[236:239], v[148:151], v[200:203]\n\t" \
    "v_cvt_pk_bf16_f32 v212, v212, v208\n\t" \
    "v_cvt_pk_bf16_f32 v213, v213, v209\n\t" \
    "v_mfma_f32_16x16x32_bf16 v[200:203], v[240:243], v[152:155], v[200:203]\n\t" \
    "v_cvt_pk_bf16_f32 v214, v214, v210\n\t" \
    "v_cvt_pk_bf16_f32 v215, v215, v211\n\t" \
    "v_mfma_f32_16x16x32_bf16 v[200:203], v[244:247], v[156:159], v[200:203]\n\t" \
    "s_add_u32 s44, s42, 0x20000\n\t" \
    "v_mfma_f32_16x16x32_bf16 v[200:203], v[228:231], v[140:143], v[200:203]\n\t" \
    "buffer_store_dwordx4 v[212:215], v253, s[60:63], s44 offen sc1\n\t" \
    "s_waitcnt lgkmcnt(0)\n\t" \
    "v_mfma_f32_16x16x32_bf16 v[204:207], v[224:227], v[248:251], 0\n\t" \
    "ds_read_b128 v[248:251], v254 offset:7168\n\t" \
    "ds_read_b128 v[224:227], v253 offset:6144\n\t" \
    "v_mfma_f32_16x16x32_bf16 v[204:207], v[232:235], v[120:123], v[204:207]\n\t" \
    "ds_read_b128 v[232:235], v253 offset:8192\n\t" \
    "v_mfma_f32_16x16x32_bf16 v[204:207], v[236:239], v[124:127], v[204:207]\n\t" \
    "ds_read_b128 v[236:239], v253 offset:9216\n\t" \
    "v_mfma_f32_16x16x32_bf16 v[204:207], v[240:243], v[128:131], v[204:207]\n\t" \
    "ds_read_b128 v[240:243], v253 offset:10240\n\t" \
    "v_mfma_f32_16x16x32_bf16 v[204:207], v[244:247], v[132:135], v[204:207]\n\t" \
    "ds_read_b128 v[244:247], v253 offset:11264\n\t" \
    "s_waitcnt lgkmcnt(5)\n\t" \
    "v_mfma_f32_16x16x32_bf16 v[204:207], v[228:231], v[248:251], v[204:207]\n\t" \
    "ds_read_b128 v[248:251], v254 offset:4096\n\t" \
    "ds_read_b128 v[228:231], v253 offset:7168\n\t" \
    "s_waitcnt lgkmcnt(6)\n\t" \
    "v_mfma_f32_16x16x32_bf16 v[208:211], v[224:227], v[96:99], 0\n\t" \
    "s_waitcnt lgkmcnt(5)\n\t" \
    "v_mfma_f32_16x16x32_bf16 v[208:211], v[232:235], v[104:107], v[208:211]\n\t" \
    "s_waitcnt lgkmcnt(4)\n\t" \
    "v_mfma_f32_16x16x32_bf16 v[208:211], v[236:239], v[108:111], v[208:211]\n\t" \
    "s_waitcnt lgkmcnt(3)\n\t" \
    "v_mfma_f32_16x16x32_bf16 v[208:211], v[240:243], v[112:115], v[208:211]\n\t" \
    "s_waitcnt lgkmcnt(2)\n\t" \
    "v_mfma_f32_16x16x32_bf16 v[208:211], v[244:247], v[116:119], v[208:211]\n\t" \
    "s_waitcnt lgkmcnt(0)\n\t" \
    "v_mfma_f32_16x16x32_bf16 v[208:211], v[228:231], v[100:103], v[208:211]\n\t" \
    "v_mfma_f32_16x16x32_bf16 v[212:215], v[224:227], v[248:251], 0\n\t" \
    "ds_read_b128 v[248:251], v254 offset:5120\n\t" \
    "v_mfma_f32_16x16x32_bf16 v[212:215], v[232:235], v[80:83], v[212:215]\n\t" \
    "v_mfma_f32_16x16x32_bf16 v[212:215], v[236:239], v[84:87], v[212:215]\n\t" \
    "v_cvt_pk_bf16_f32 v216, v208, v216\n\t" \
    "v_cvt_pk_bf16_f32 v217, v209, v217\n\t" \
    "v_mfma_f32_16x16x32_bf16 v[212:215], v[240:243], v[88:91], v[212:215]\n\t" \
    "v_cvt_pk_bf16_f32 v218, v210, v218\n\t" \
    "v_cvt_pk_bf16_f32 v219, v211, v219\n\t" \
    "v_mfma_f32_16x16x32_bf16 v[212:215], v[244:247], v[92:95], v[212:215]\n\t" \
    "s_add_u32 s44, s42, 0x50000\n\t" \
    "s_waitcnt lgkmcnt(0)\n\t" \
    "v_mfma_f32_16x16x32_bf16 v[212:215], v[228:231], v[248:251], v[212:215]\n\t" \
    "ds_read_b128 v[248:251], v254 offset:6144\n\t" \
    "buffer_store_dwordx4 v[216:219], v253, s[60:63], s44 offen sc1\n\t" \
    "v_mfma_f32_16x16x32_bf16 v[208:211], v[224:227], v[176:179], 0\n\t" \
    "v_mfma_f32_16x16x32_bf16 v[208:211], v[232:235], v[184:187], v[208:211]\n\t" \
    "v_mfma_f32_16x16x32_bf16 v[208:211], v[236:239], v[188:191], v[208:211]\n\t" \
    "v_cvt_pk_bf16_f32 v220, v212, v220\n\t" \
    "v_mfma_f32_16x16x32_bf16 v[208:211], v[240:243], v[192:195], v[208:211]\n\t" \
    "v_cvt_pk_bf16_f32 v221, v213, v221\n\t" \
    "v_cvt_pk_bf16_f32 v222, v214, v222\n\t" \
    "v_mfma_f32_16x16x32_bf16 v[208:211], v[244:247], v[196:199], v[208:211]\n\t" \
    "v_cvt_pk_bf16_f32 v223, v215, v223\n\t" \
    "v_mfma_f32_16x16x32_bf16 v[208:211], v[228:231], v[180:183], v[208:211]\n\t" \
    "s_add_u32 s44, s42, 0x40000\n\t" \
    "buffer_store_dwordx4 v[220:223], v253, s[60:63], s44 offen sc1\n\t" \
    "s_waitcnt lgkmcnt(0)\n\t" \
    "v_mfma_f32_16x16x32_bf16 v[216:219], v[224:227], v[248:251], 0\n\t" \
    "ds_read_b128 v[248:251], v254 offset:7168\n\t" \
    "v_mfma_f32_16x16x32_bf16 v[216:219], v[232:235], v[120:123], v[216:219]\n\t" \
    "v_mfma_f32_16x16x32_bf16 v[216:219], v[236:239], v[124:127], v[216:219]\n\t" \
    "v_mfma_f32_16x16x32_bf16 v[216:219], v[240:243], v[128:131], v[216:219]\n\t" \
    "v_mfma_f32_16x16x32_bf16 v[216:219], v[244:247], v[132:135], v[216:219]\n\t" \
    "s_waitcnt lgkmcnt(0)\n\t" \
    "v_mfma_f32_16x16x32_bf16 v[216:219], v[228:231], v[248:251], v[216:219]\n\t" \
    "ds_read_b128 v[248:251], v254 offset:8192\n\t" \
    "v_mfma_f32_16x16x32_bf16 v[212:215], v[224:227], v[136:139], 0\n\t" \
    "v_mfma_f32_16x16x32_bf16 v[212:215], v[232:235], v[144:147], v[212:215]\n\t" \
    "v_mfma_f32_16x16x32_bf16 v[212:215], v[236:239], v[148:151], v[212:215]\n\t" \
    "v_cvt_pk_bf16_f32 v204, v216, v204\n\t" \
    "v_cvt_pk_bf16_f32 v205, v217, v205\n\t" \
    "v_mfma_f32_16x16x32_bf16 v[212:215], v[240:243], v[152:155], v[212:215]\n\t" \
    "v_cvt_pk_bf16_f32 v206, v218, v206\n\t" \
    "v_cvt_pk_bf16_f32 v207, v219, v207\n\t" \
    "v_mfma_f32_16x16x32_bf16 v[212:215], v[244:247], v[156:159], v[212:215]\n\t" \
    "s_add_u32 s44, s42, 0x60000\n\t" \
    "v_mfma_f32_16x16x32_bf16 v[212:215], v[228:231], v[140:143], v[212:215]\n\t" \
    "buffer_store_dwordx4 v[204:207], v253, s[60:63], s44 offen sc1\n\t" \
    "s_waitcnt lgkmcnt(0)\n\t" \
    "v_mfma_f32_16x16x32_bf16 v[220:223], v[224:227], v[248:251], 0\n\t" \
    "ds_read_b128 v[248:251], v254 offset:9216\n\t" \
    "ds_read_b128 v[224:227], v253 offset:12288\n\t" \
    "v_mfma_f32_16x16x32_bf16 v[220:223], v[232:235], v[160:163], v[220:223]\n\t" \
    "ds_read_b128 v[232:235], v253 offset:14336\n\t" \
    "v_mfma_f32_16x16x32_bf16 v[220:223], v[236:239], v[164:167], v[220:223]\n\t" \
    "ds_read_b128 v[236:239], v253 offset:15360\n\t" \
    "v_cvt_pk_bf16_f32 v200, v212, v200\n\t" \
    "v_mfma_f32_16x16x32_bf16 v[220:223], v[240:243], v[168:171], v[220:223]\n\t" \
    "ds_read_b128 v[240:243], v253 offset:16384\n\t" \
    "v_cvt_pk_bf16_f32 v201, v213, v201\n\t" \
    "v_cvt_pk_bf16_f32 v202, v214, v202\n\t" \
    "v_mfma_f32_16x16x32_bf16 v[220:223], v[244:247], v[172:175], v[220:223]\n\t" \
    "ds_read_b128 v[244:247], v253 offset:17408\n\t" \
    "v_cvt_pk_bf16_f32 v203, v215, v203\n\t" \
    "s_waitcnt lgkmcnt(5)\n\t" \
    "v_mfma_f32_16x16x32_bf16 v[220:223], v[228:231], v[248:251], v[220:223]\n\t" \
    "ds_read_b128 v[248:251], v254 offset:8192\n\t" \
    "ds_read_b128 v[228:231], v253 offset:13312\n\t" \
    "s_add_u32 s44, s42, 0x70000\n\t" \
    "buffer_store_dwordx4 v[200:203], v253, s[60:63], s44 offen sc1\n\t" \
    "s_waitcnt lgkmcnt(6)\n\t" \
    "v_mfma_f32_16x16x32_bf16 v[216:219], v[224:227], v[176:179], 0\n\t" \
    "s_waitcnt lgkmcnt(5)\n\t" \
    "v_mfma_f32_16x16x32_bf16 v[216:219], v[232:235], v[184:187], v[216:219]\n\t" \
    "s_waitcnt lgkmcnt(4)\n\t" \
    "v_mfma_f32_16x16x32_bf16 v[216:219], v[236:239], v[188:191], v[216:219]\n\t" \
    "s_waitcnt lgkmcnt(3)\n\t" \
    "v_mfma_f32_16x16x32_bf16 v[216:219], v[240:243], v[192:195], v[216:219]\n\t" \
    "s_waitcnt lgkmcnt(2)\n\t" \
    "v_mfma_f32_16x16x32_bf16 v[216:219], v[244:247], v[196:199], v[216:219]\n\t" \
    "s_waitcnt lgkmcnt(0)\n\t" \
    "v_mfma_f32_16x16x32_bf16 v[216:219], v[228:231], v[180:183], v[216:219]\n\t" \
    "v_mfma_f32_16x16x32_bf16 v[204:207], v[224:227], v[248:251], 0\n\t" \
    "ds_read_b128 v[248:251], v254 offset:9216\n\t" \
    "v_mfma_f32_16x16x32_bf16 v[204:207], v[232:235], v[160:163], v[204:207]\n\t" \
    "v_mfma_f32_16x16x32_bf16 v[204:207], v[236:239], v[164:167], v[204:207]\n\t" \
    "v_cvt_pk_bf16_f32 v208, v208, v216\n\t" \
    "v_cvt_pk_bf16_f32 v209, v209, v217\n\t" \
    "v_mfma_f32_16x16x32_bf16 v[204:207], v[240:243], v[168:171], v[204:207]\n\t" \
    "v_cvt_pk_bf16_f32 v210, v210, v218\n\t" \
    "v_cvt_pk_bf16_f32 v211, v211, v219\n\t" \
    "v_mfma_f32_16x16x32_bf16 v[204:207], v[244:247], v[172:175], v[204:207]\n\t" \
    "s_add_u32 s44, s42, 0x90000\n\t" \
    "s_waitcnt lgkmcnt(0)\n\t" \
    "v_mfma_f32_16x16x32_bf16 v[204:207], v[228:231], v[248:251], v[204:207]\n\t" \
    "buffer_store_dwordx4 v[208:211], v253, s[60:63], s44 offen sc1\n\t" \
    "s_nop 6\n\t" \
    "v_cvt_pk_bf16_f32 v220, v220, v204\n\t" \
    "v_cvt_pk_bf16_f32 v221, v221, v205\n\t" \
    "v_cvt_pk_bf16_f32 v222, v222, v206\n\t" \
    "v_cvt_pk_bf16_f32 v223, v223, v207\n\t" \
    "s_add_u32 s44, s42, 0x80000\n\t" \
    "buffer_store_dwordx4 v[220:223], v253, s[60:63], s44 offen sc1\n\t" \
    LST0 \
    "s_add_u32 s44, s40, 1\n\t" \
    "s_mov_b32 s45, s44\n\t" \
    "v_mov_b64 v[200:201], s[44:45]\n\t" \
    "v_mov_b64 v[202:203], s[44:45]\n\t" \
    "s_waitcnt lgkmcnt(0)\n\t" \
    "s_barrier \n\t" \
    LST3 \
    "ds_read_b128 v[220:223], v253 offset:3072\n\t" \
    "ds_read_b128 v[224:227], v253 offset:3072\n\t" \
    "ds_read_b128 v[228:231], v253 offset:3072\n\t" \
    "ds_read_b128 v[232:235], v253 offset:3072\n\t" \
    "ds_read_b128 v[236:239], v253 offset:3072\n\t" \
    "ds_read_b128 v[240:243], v253 offset:3072\n\t" \
    "ds_read_b128 v[244:247], v253 offset:3072\n\t" \
    "ds_read_b128 v[248:251], v253 offset:3072\n\t" \
    "ds_read_b128 v[212:215], v254 offset:0\n\t" \
    "ds_read_b128 v[216:219], v254 offset:1024\n\t" \
    "s_and_b32 s44, s40, 3\n\t" \
    "s_mul_i32 s44, s44, 0x202000\n\t" \
    "s_add_u32 s42, s44, %[vo]\n\t" \
    "s_mov_b64 exec, 0xff\n\t" \
    "s_waitcnt vmcnt(0)\n\t" \
    "buffer_store_dwordx4 v[200:203], v253, s[60:63], %[fo] offen sc1\n\t" \
    "s_mov_b64 exec, -1\n\t" \
    LST1 \
    "s_setprio 3\n\t" \
    "s_waitcnt lgkmcnt(0)\n\t" \
    "s_barrier \n\t" \
    LST4 \
    "ds_read_b128 v[204:207], v253 offset:1024\n\t" \
    "ds_read_b128 v[208:211], v253 offset:2048\n\t" \
    "ds_read_b128 v[200:203], v253 offset:0\n\t" \
    "s_waitcnt lgkmcnt(2)\n\t" \
    "v_smfmac_f32_16x16x64_bf16 v[224:227], v[204:207], v[24:31], v252\n\t" \
    "v_smfmac_f32_16x16x64_bf16 v[220:223], v[204:207], v[0:7], v252\n\t" \
    "s_waitcnt lgkmcnt(1)\n\t" \
    "v_smfmac_f32_16x16x64_bf16 v[224:227], v[208:211], v[32:39], v252\n\t" \
    "v_smfmac_f32_16x16x64_bf16 v[220:223], v[208:211], v[8:15], v252\n\t" \
    "s_waitcnt lgkmcnt(0)\n\t" \
    "v_smfmac_f32_16x16x64_bf16 v[224:227], v[200:203], v[16:23], v252\n\t" \
    "v_smfmac_f32_16x16x64_bf16 v[220:223], v[200:203], v[212:219], v252\n\t" \
    "ds_read_b128 v[212:215], v254 offset:2048\n\t" \
    "ds_read_b128 v[216:219], v254 offset:3072\n\t" \
    "v_smfmac_f32_16x16x64_bf16 v[232:235], v[204:207], v[64:71], v252\n\t" \
    "v_smfmac_f32_16x16x64_bf16 v[228:231], v[204:207], v[40:47], v252\n\t" \
    "v_smfmac_f32_16x16x64_bf16 v[232:235], v[208:211], v[72:79], v252\n\t" \
    "v_add_f32 v220, v220, v222\n\t" \
    "v_add_f32 v221, v221, v223\n\t" \
    "v_cvt_pk_bf16_f32 v220, v220, v221\n\t" \
    "v_smfmac_f32_16x16x64_bf16 v[228:231], v[208:211], v[48:55], v252\n\t" \
    "v_min_u32 v220, -2, v220\n\t" \
    "v_add_f32 v224, v224, v226\n\t" \
    "v_add_f32 v225, v225, v227\n\t" \
    "v_smfmac_f32_16x16x64_bf16 v[232:235], v[200:203], v[56:63], v252\n\t" \
    "v_cvt_pk_bf16_f32 v221, v224, v225\n\t" \
    "v_min_u32 v221, -2, v221\n\t" \
    "s_waitcnt lgkmcnt(0)\n\t" \
    "v_smfmac_f32_16x16x64_bf16 v[228:231], v[200:203], v[212:219], v252\n\t" \
    "ds_read_b128 v[212:215], v254 offset:4096\n\t" \
    "ds_read_b128 v[216:219], v254 offset:5120\n\t" \
    "v_smfmac_f32_16x16x64_bf16 v[240:243], v[204:207], v[104:111], v252\n\t" \
    "v_smfmac_f32_16x16x64_bf16 v[236:239], v[204:207], v[80:87], v252\n\t" \
    "v_smfmac_f32_16x16x64_bf16 v[240:243], v[208:211], v[112:119], v252\n\t" \
    "v_add_f32 v228, v228, v230\n\t" \
    "v_add_f32 v229, v229, v231\n\t" \
    "v_cvt_pk_bf16_f32 v222, v228, v229\n\t" \
    "v_smfmac_f32_16x16x64_bf16 v[236:239], v[208:211], v[88:95], v252\n\t" \
    "v_min_u32 v222, -2, v222\n\t" \
    "v_add_f32 v232, v232, v234\n\t" \
    "v_add_f32 v233, v233, v235\n\t" \
    "v_smfmac_f32_16x16x64_bf16 v[240:243], v[200:203], v[96:103], v252\n\t" \
    "v_cvt_pk_bf16_f32 v223, v232, v233\n\t" \
    "v_min_u32 v223, -2, v223\n\t" \
    "v_permlane32_swap_b32 v220, v222\n\t" \
    "s_waitcnt lgkmcnt(0)\n\t" \
    "v_smfmac_f32_16x16x64_bf16 v[236:239], v[200:203], v[212:219], v252\n\t" \
    "ds_read_b128 v[212:215], v254 offset:6144\n\t" \
    "ds_read_b128 v[216:219], v254 offset:7168\n\t" \
    "s_nop 0\n\t" \
    "v_permlane32_swap_b32 v221, v223\n\t" \
    "s_nop 1\n\t" \
    "v_permlane16_swap_b32 v220, v221\n\t" \
    "v_permlane16_swap_b32 v222, v223\n\t" \
    "buffer_store_dwordx4 v[220:223], v255, s[60:63], s42 offen sc1\n\t" \
    "s_add_u32 s42, s42, 0x10000\n\t" \
    "ds_read_b128 v[224:227], v253 offset:3072\n\t" \
    "ds_read_b128 v[228:231], v253 offset:3072\n\t" \
    "v_smfmac_f32_16x16x64_bf16 v[248:251], v[204:207], v[144:151], v252\n\t" \
    "v_smfmac_f32_16x16x64_bf16 v[244:247], v[204:207], v[120:127], v252\n\t" \
    "v_smfmac_f32_16x16x64_bf16 v[248:251], v[208:211], v[152:159], v252\n\t" \
    "v_add_f32 v236, v236, v238\n\t" \
    "v_add_f32 v237, v237, v239\n\t" \
    "v_cvt_pk_bf16_f32 v236, v236, v237\n\t" \
    "v_smfmac_f32_16x16x64_bf16 v[244:247], v[208:211], v[128:135], v252\n\t" \
    "v_min_u32 v236, -2, v236\n\t" \
    "v_add_f32 v240, v240, v242\n\t" \
    "v_add_f32 v241, v241, v243\n\t" \
    "v_smfmac_f32_16x16x64_bf16 v[248:251], v[200:203], v[136:143], v252\n\t" \
    "v_cvt_pk_bf16_f32 v237, v240, v241\n\t" \
    "v_min_u32 v237, -2, v237\n\t" \
    "s_waitcnt lgkmcnt(2)\n\t" \
    "v_smfmac_f32_16x16x64_bf16 v[244:247], v[200:203], v[212:219], v252\n\t" \
    "ds_read_b128 v[212:215], v254 offset:8192\n\t" \
    "ds_read_b128 v[216:219], v254 offset:9216\n\t" \
    "s_waitcnt lgkmcnt(2)\n\t" \
    "v_smfmac_f32_16x16x64_bf16 v[228:231], v[204:207], v[184:191], v252\n\t" \
    "v_smfmac_f32_16x16x64_bf16 v[224:227], v[204:207], v[160:167], v252\n\t" \
    "v_smfmac_f32_16x16x64_bf16 v[228:231], v[208:211], v[192:199], v252\n\t" \
    "v_and_b32 v232, 0x7fff, v255\n\t" \
    "v_lshrrev_b32 v233, 15, v255\n\t" \
    "v_lshl_add_u32 v232, v233, 3, v232\n\t" \
    "v_smfmac_f32_16x16x64_bf16 v[224:227], v[208:211], v[168:175], v252\n\t" \
    "v_add_f32 v244, v244, v246\n\t" \
    "v_add_f32 v245, v245, v247\n\t" \
    "v_cvt_pk_bf16_f32 v238, v244, v245\n\t" \
    "v_smfmac_f32_16x16x64_bf16 v[228:231], v[200:203], v[176:183], v252\n\t" \
    "v_min_u32 v238, -2, v238\n\t" \
    "v_add_f32 v248, v248, v250\n\t" \
    "v_add_f32 v249, v249, v251\n\t" \
    "s_waitcnt lgkmcnt(0)\n\t" \
    "v_smfmac_f32_16x16x64_bf16 v[224:227], v[200:203], v[212:219], v252\n\t" \
    "v_cvt_pk_bf16_f32 v239, v248, v249\n\t" \
    "v_min_u32 v239, -2, v239\n\t" \
    "v_permlane32_swap_b32 v236, v238\n\t" \
    "s_nop 0\n\t" \
    "v_permlane32_swap_b32 v237, v239\n\t" \
    "s_nop 1\n\t" \
    "v_permlane16_swap_b32 v236, v237\n\t" \
    "v_permlane16_swap_b32 v238, v239\n\t" \
    "buffer_store_dwordx4 v[236:239], v255, s[60:63], s42 offen sc1\n\t" \
    "s_add_u32 s42, s42, 0x10000\n\t" \
    "v_add_f32 v228, v228, v230\n\t" \
    "v_add_f32 v229, v229, v231\n\t" \
    "v_add_f32 v224, v224, v226\n\t" \
    "v_add_f32 v225, v225, v227\n\t" \
    "v_cvt_pk_bf16_f32 v229, v228, v229\n\t" \
    "v_cvt_pk_bf16_f32 v228, v224, v225\n\t" \
    "v_min_u32 v229, -2, v229\n\t" \
    "v_min_u32 v228, -2, v228\n\t" \
    "s_nop 1\n\t" \
    "v_permlane16_swap_b32 v228, v229\n\t" \
    "buffer_store_dwordx2 v[228:229], v232, s[60:63], s42 offen sc1\n\t" \
    "s_setprio 0\n\t" \
    LST5 \
    LFLUSH200 \
    "s_add_u32 s40, s40, 1\n\t" \
    "s_cmp_lg_u32 s40, 64\n\t" \
    "s_cbranch_scc1 .Lv43_%=\n\t" \
    "s_branch .Lend%=\n\t" \
    ".Lv3_%=:\n\t" \
    "s_mov_b32 s42, %[xs]\n\t" \
    "s_add_u32 m0, %[wm], 0xc000\n\t" \
    "s_mov_b32 s44, 0x0\n\t" \
    "buffer_load_dwordx4 v240, %[wr], s44 offen offset:0 nt lds\n\t" \
    "buffer_load_dwordx4 v241, %[wr], s44 offen offset:1024 nt lds\n\t" \
    "buffer_load_dwordx4 v242, %[wr], s44 offen offset:2048 nt lds\n\t" \
    "s_add_u32 m0, %[wm], 0xf000\n\t" \
    "s_mov_b32 s44, 0x60000\n\t" \
    "buffer_load_dwordx4 v240, %[wr], s44 offen offset:0 nt lds\n\t" \
    "buffer_load_dwordx4 v241, %[wr], s44 offen offset:1024 nt lds\n\t" \
    "buffer_load_dwordx4 v242, %[wr], s44 offen offset:2048 nt lds\n\t" \
    "s_add_u32 m0, %[wm], 0x0\n\t" \
    "s_mov_b32 s44, 0xc0000\n\t" \
    "buffer_load_dwordx4 v240, %[wr], s44 offen offset:0 nt lds\n\t" \
    "buffer_load_dwordx4 v241, %[wr], s44 offen offset:1024 nt lds\n\t" \
    "buffer_load_dwordx4 v242, %[wr], s44 offen offset:2048 nt lds\n\t" \
    "s_add_u32 m0, %[wm], 0x3000\n\t" \
    "s_mov_b32 s44, 0x120000\n\t" \
    "buffer_load_dwordx4 v240, %[wr], s44 offen offset:0 nt lds\n\t" \
    "buffer_load_dwordx4 v241, %[wr], s44 offen offset:1024 nt lds\n\t" \
    "buffer_load_dwordx4 v242, %[wr], s44 offen offset:2048 nt lds\n\t" \
    "s_add_u32 m0, %[wm], 0x6000\n\t" \
    "s_mov_b32 s44, 0x180000\n\t" \
    "buffer_load_dwordx4 v240, %[wr], s44 offen offset:0 nt lds\n\t" \
    "buffer_load_dwordx4 v241, %[wr], s44 offen offset:1024 nt lds\n\t" \
    "buffer_load_dwordx4 v242, %[wr], s44 offen offset:2048 nt lds\n\t" \
    "v_add_u32 v244, 0xc000, v243\n\t" \
    "s_waitcnt vmcnt(12) lgkmcnt(0)\n\t" \
    "s_barrier \n\t" \
    "s_add_u32 m0, %[wm], 0x9000\n\t" \
    "s_mov_b32 s44, 0x1e0000\n\t" \
    "buffer_load_dwordx4 v240, %[wr], s44 offen offset:0 nt lds\n\t" \
    "buffer_load_dwordx4 v241, %[wr], s44 offen offset:1024 nt lds\n\t" \
    "buffer_load_dwordx4 v242, %[wr], s44 offen offset:2048 nt lds\n\t" \
    "ds_read_b64_tr_b16 v[208:209], v244 offset:0\n\t" \
    "ds_read_b64_tr_b16 v[210:211], v244 offset:1536\n\t" \
    "ds_read_b64_tr_b16 v[40:41], v244 offset:32\n\t" \
    "ds_read_b64_tr_b16 v[42:43], v244 offset:1568\n\t" \
    "ds_read_b64_tr_b16 v[48:49], v244 offset:64\n\t" \
    "ds_read_b64_tr_b16 v[50:51], v244 offset:1600\n\t" \
    "v_add_u32 v244, 0xf000, v243\n\t" \
    "s_waitcnt vmcnt(12) lgkmcnt(0)\n\t" \
    "s_barrier \n\t" \
    "s_add_u32 m0, %[wm], 0xc000\n\t" \
    "s_mov_b32 s44, 0x240000\n\t" \
    "buffer_load_dwordx4 v240, %[wr], s44 offen offset:0 nt lds\n\t" \
    "buffer_load_dwordx4 v241, %[wr], s44 offen offset:1024 nt lds\n\t" \
    "buffer_load_dwordx4 v242, %[wr], s44 offen offset:2048 nt lds\n\t" \
    "ds_read_b64_tr_b16 v[212:213], v244 offset:0\n\t" \
    "ds_read_b64_tr_b16 v[214:215], v244 offset:1536\n\t" \
    "ds_read_b64_tr_b16 v[44:45], v244 offset:32\n\t" \
    "ds_read_b64_tr_b16 v[46:47], v244 offset:1568\n\t" \
    "ds_read_b64_tr_b16 v[52:53], v244 offset:64\n\t" \
    "ds_read_b64_tr_b16 v[54:55], v244 offset:1600\n\t" \
    "v_add_u32 v244, 0x0, v243\n\t" \
    "s_waitcnt vmcnt(12) lgkmcnt(0)\n\t" \
    "s_barrier \n\t" \
    "s_add_u32 m0, %[wm], 0xf000\n\t" \
    "s_mov_b32 s44, 0x2a0000\n\t" \
    "buffer_load_dwordx4 v240, %[wr], s44 offen offset:0 nt lds\n\t" \
    "buffer_load_dwordx4 v241, %[wr], s44 offen offset:1024 nt lds\n\t" \
    "buffer_load_dwordx4 v242, %[wr], s44 offen offset:2048 nt lds\n\t" \
    "ds_read_b64_tr_b16 v[56:57], v244 offset:0\n\t" \
    "ds_read_b64_tr_b16 v[58:59], v244 offset:1536\n\t" \
    "ds_read_b64_tr_b16 v[64:65], v244 offset:32\n\t" \
    "ds_read_b64_tr_b16 v[66:67], v244 offset:1568\n\t" \
    "ds_read_b64_tr_b16 v[72:73], v244 offset:64\n\t" \
    "ds_read_b64_tr_b16 v[74:75], v244 offset:1600\n\t" \
    "v_add_u32 v244, 0x3000, v243\n\t" \
    "s_waitcnt vmcnt(12) lgkmcnt(0)\n\t" \
    "s_barrier \n\t" \
    "s_add_u32 m0, %[wm], 0x0\n\t" \
    "s_mov_b32 s44, 0x300000\n\t" \
    "buffer_load_dwordx4 v240, %[wr], s44 offen offset:0 nt lds\n\t" \
    "buffer_load_dwordx4 v241, %[wr], s44 offen offset:1024 nt lds\n\t" \
    "buffer_load_dwordx4 v242, %[wr], s44 offen offset:2048 nt lds\n\t" \
    "ds_read_b64_tr_b16 v[60:61], v244 offset:0\n\t" \
    "ds_read_b64_tr_b16 v[62:63], v244 offset:1536\n\t" \
    "ds_read_b64_tr_b16 v[68:69], v244 offset:32\n\t" \
    "ds_read_b64_tr_b16 v[70:71], v244 offset:1568\n\t" \
    "ds_read_b64_tr_b16 v[76:77], v244 offset:64\n\t" \
    "ds_read_b64_tr_b16 v[78:79], v244 offset:1600\n\t" \
    "v_add_u32 v244, 0x6000, v243\n\t" \
    "s_waitcnt vmcnt(12) lgkmcnt(0)\n\t" \
    "s_barrier \n\t" \
    "s_add_u32 m0, %[wm], 0x3000\n\t" \
    "s_mov_b32 s44, 0x360000\n\t" \
    "buffer_load_dwordx4 v240, %[wr], s44 offen offset:0 nt lds\n\t" \
    "buffer_load_dwordx4 v241, %[wr], s44 offen offset:1024 nt lds\n\t" \
    "buffer_load_dwordx4 v242, %[wr], s44 offen offset:2048 nt lds\n\t" \
    "ds_read_b64_tr_b16 v[216:217], v244 offset:0\n\t" \
    "ds_read_b64_tr_b16 v[218:219], v244 offset:1536\n\t" \
    "ds_read_b64_tr_b16 v[80:81], v244 offset:32\n\t" \
    "ds_read_b64_tr_b16 v[82:83], v244 offset:1568\n\t" \
    "ds_read_b64_tr_b16 v[88:89], v244 offset:64\n\t" \
    "ds_read_b64_tr_b16 v[90:91], v244 offset:1600\n\t" \
    "v_add_u32 v244, 0x9000, v243\n\t" \
    "s_waitcnt vmcnt(12) lgkmcnt(0)\n\t" \
    "s_barrier \n\t" \
    "s_add_u32 m0, %[wm], 0x6000\n\t" \
    "s_mov_b32 s44, 0x3c0000\n\t" \
    "buffer_load_dwordx4 v240, %[wr], s44 offen offset:0 nt lds\n\t" \
    "buffer_load_dwordx4 v241, %[wr], s44 offen offset:1024 nt lds\n\t" \
    "buffer_load_dwordx4 v242, %[wr], s44 offen offset:2048 nt lds\n\t" \
    "ds_read_b64_tr_b16 v[220:221], v244 offset:0\n\t" \
    "ds_read_b64_tr_b16 v[222:223], v244 offset:1536\n\t" \
    "ds_read_b64_tr_b16 v[84:85], v244 offset:32\n\t" \
    "ds_read_b64_tr_b16 v[86:87], v244 offset:1568\n\t" \
    "ds_read_b64_tr_b16 v[92:93], v244 offset:64\n\t" \
    "ds_read_b64_tr_b16 v[94:95], v244 offset:1600\n\t" \
    "v_add_u32 v244, 0xc000, v243\n\t" \
    "s_waitcnt vmcnt(12) lgkmcnt(0)\n\t" \
    "s_barrier \n\t" \
    "v_mov_b32 v246, 0\n\t" \
    "buffer_load_dword v246, v246, s[60:63], %[mo] offen sc1\n\t" \
    "s_add_u32 m0, %[wm], 0x9000\n\t" \
    "s_mov_b32 s44, 0x420000\n\t" \
    "buffer_load_dwordx4 v240, %[wr], s44 offen offset:0 nt lds\n\t" \
    "buffer_load_dwordx4 v241, %[wr], s44 offen offset:1024 nt lds\n\t" \
    "buffer_load_dwordx4 v242, %[wr], s44 offen offset:2048 nt lds\n\t" \
    "ds_read_b64_tr_b16 v[96:97], v244 offset:0\n\t" \
    "ds_read_b64_tr_b16 v[98:99], v244 offset:1536\n\t" \
    "ds_read_b64_tr_b16 v[104:105], v244 offset:32\n\t" \
    "ds_read_b64_tr_b16 v[106:107], v244 offset:1568\n\t" \
    "ds_read_b64_tr_b16 v[112:113], v244 offset:64\n\t" \
    "ds_read_b64_tr_b16 v[114:115], v244 offset:1600\n\t" \
    "v_add_u32 v244, 0xf000, v243\n\t" \
    "s_waitcnt vmcnt(12) lgkmcnt(0)\n\t" \
    "s_barrier \n\t" \
    "s_add_u32 m0, %[wm], 0xc000\n\t" \
    "s_mov_b32 s44, 0x480000\n\t" \
    "buffer_load_dwordx4 v240, %[wr], s44 offen offset:0 nt lds\n\t" \
    "buffer_load_dwordx4 v241, %[wr], s44 offen offset:1024 nt lds\n\t" \
    "buffer_load_dwordx4 v242, %[wr], s44 offen offset:2048 nt lds\n\t" \
    "ds_read_b64_tr_b16 v[100:101], v244 offset:0\n\t" \
    "ds_read_b64_tr_b16 v[102:103], v244 offset:1536\n\t" \
    "ds_read_b64_tr_b16 v[108:109], v244 offset:32\n\t" \
    "ds_read_b64_tr_b16 v[110:111], v244 offset:1568\n\t" \
    "ds_read_b64_tr_b16 v[116:117], v244 offset:64\n\t" \
    "ds_read_b64_tr_b16 v[118:119], v244 offset:1600\n\t" \
    "v_add_u32 v244, 0x0, v243\n\t" \
    "s_waitcnt vmcnt(12) lgkmcnt(0)\n\t" \
    "s_barrier \n\t" \
    "s_add_u32 m0, %[wm], 0xf000\n\t" \
    "s_mov_b32 s44, 0x4e0000\n\t" \
    "buffer_load_dwordx4 v240, %[wr], s44 offen offset:0 nt lds\n\t" \
    "buffer_load_dwordx4 v241, %[wr], s44 offen offset:1024 nt lds\n\t" \
    "buffer_load_dwordx4 v242, %[wr], s44 offen offset:2048 nt lds\n\t" \
    "ds_read_b64_tr_b16 v[224:225], v244 offset:0\n\t" \
    "ds_read_b64_tr_b16 v[226:227], v244 offset:1536\n\t" \
    "ds_read_b64_tr_b16 v[120:121], v244 offset:32\n\t" \
    "ds_read_b64_tr_b16 v[122:123], v244 offset:1568\n\t" \
    "ds_read_b64_tr_b16 v[128:129], v244 offset:64\n\t" \
    "ds_read_b64_tr_b16 v[130:131], v244 offset:1600\n\t" \
    "v_add_u32 v244, 0x3000, v243\n\t" \
    "s_waitcnt vmcnt(12) lgkmcnt(0)\n\t" \
    "s_barrier \n\t" \
    "s_add_u32 m0, %[wm], 0x0\n\t" \
    "s_mov_b32 s44, 0x540000\n\t" \
    "buffer_load_dwordx4 v240, %[wr], s44 offen offset:0 nt lds\n\t" \
    "buffer_load_dwordx4 v241, %[wr], s44 offen offset:1024 nt lds\n\t" \
    "buffer_load_dwordx4 v242, %[wr], s44 offen offset:2048 nt lds\n\t" \
    "ds_read_b64_tr_b16 v[228:229], v244 offset:0\n\t" \
    "ds_read_b64_tr_b16 v[230:231], v244 offset:1536\n\t" \
    "ds_read_b64_tr_b16 v[124:125], v244 offset:32\n\t" \
    "ds_read_b64_tr_b16 v[126:127], v244 offset:1568\n\t" \
    "ds_read_b64_tr_b16 v[132:133], v244 offset:64\n\t" \
    "ds_read_b64_tr_b16 v[134:135], v244 offset:1600\n\t" \
    "v_add_u32 v244, 0x6000, v243\n\t" \
    "s_waitcnt vmcnt(12) lgkmcnt(0)\n\t" \
    "s_barrier \n\t" \
    "s_add_u32 m0, %[wm], 0x3000\n\t" \
    "s_mov_b32 s44, 0x5a0000\n\t" \
    "buffer_load_dwordx4 v240, %[wr], s44 offen offset:0 nt lds\n\t" \
    "buffer_load_dwordx4 v241, %[wr], s44 offen offset:1024 nt lds\n\t" \
    "buffer_load_dwordx4 v242, %[wr], s44 offen offset:2048 nt lds\n\t" \
    "ds_read_b64_tr_b16 v[136:137], v244 offset:0\n\t" \
    "ds_read_b64_tr_b16 v[138:139], v244 offset:1536\n\t" \
    "ds_read_b64_tr_b16 v[144:145], v244 offset:32\n\t" \
    "ds_read_b64_tr_b16 v[146:147], v244 offset:1568\n\t" \
    "ds_read_b64_tr_b16 v[152:153], v244 offset:64\n\t" \
    "ds_read_b64_tr_b16 v[154:155], v244 offset:1600\n\t" \
    "v_add_u32 v244, 0x9000, v243\n\t" \
    "s_waitcnt vmcnt(12) lgkmcnt(0)\n\t" \
    "s_barrier \n\t" \
    ".Lv31_%=:\n\t" \
    "v_readfirstlane_b32 s43, v246\n\t" \
    "s_nop 0\n\t" \
    "s_cmp_lg_u32 s43, -1\n\t" \
    "s_cbranch_scc1 .Lv32_%=\n\t" \
    "s_cmp_eq_u32 s41, 0\n\t" \
    "s_cbranch_scc1 .Lv32_%=\n\t" \
    "s_sub_u32 s41, s41, 1\n\t" \
    "s_sleep 4\n\t" \
    "v_mov_b32 v246, 0\n\t" \
    "buffer_load_dword v246, v246, s[60:63], %[mo] offen sc1\n\t" \
    "s_waitcnt vmcnt(0)\n\t" \
    "s_branch .Lv31_%=\n\t" \
    ".Lv32_%=:\n\t" \
    "v_mov_b32 v246, 0x8000\n\t" \
    "s_mov_b64 exec, 1\n\t" \
    "buffer_store_dword v246, v246, s[60:63], %[mo] offen sc1\n\t" \
    "s_mov_b64 exec, -1\n\t" \
    "s_and_b32 s43, s43, 0x2000\n\t" \
    "s_xor_b32 s43, s43, %[lx]\n\t" \
    "s_add_u32 s60, s60, s43\n\t" \
    "s_addc_u32 s61, s61, 0\n\t" \
    "v_and_b32 v252, 0xf0, v253\n\t" \
    "v_lshlrev_b32 v252, 3, v252\n\t" \
    "buffer_load_dword v252, v252, s[60:63], %[fl] offen sc1\n\t" \
    "s_add_u32 m0, %[wm], 0x6000\n\t" \
    "s_mov_b32 s44, 0x600000\n\t" \
    "buffer_load_dwordx4 v240, %[wr], s44 offen offset:0 nt lds\n\t" \
    "buffer_load_dwordx4 v241, %[wr], s44 offen offset:1024 nt lds\n\t" \
    "buffer_load_dwordx4 v242, %[wr], s44 offen offset:2048 nt lds\n\t" \
    "ds_read_b64_tr_b16 v[140:141], v244 offset:0\n\t" \
    "ds_read_b64_tr_b16 v[142:143], v244 offset:1536\n\t" \
    "ds_read_b64_tr_b16 v[148:149], v244 offset:32\n\t" \
    "ds_read_b64_tr_b16 v[150:151], v244 offset:1568\n\t" \
    "ds_read_b64_tr_b16 v[156:157], v244 offset:64\n\t" \
    "ds_read_b64_tr_b16 v[158:159], v244 offset:1600\n\t" \
    "v_add_u32 v244, 0xc000, v243\n\t" \
    "s_waitcnt vmcnt(12) lgkmcnt(0)\n\t" \
    "s_barrier \n\t" \
    "s_add_u32 m0, %[wm], 0x9000\n\t" \
    "s_mov_b32 s44, 0x660000\n\t" \
    "buffer_load_dwordx4 v240, %[wr], s44 offen offset:0 nt lds\n\t" \
    "buffer_load_dwordx4 v241, %[wr], s44 offen offset:1024 nt lds\n\t" \
    "buffer_load_dwordx4 v242, %[wr], s44 offen offset:2048 nt lds\n\t" \
    "ds_read_b64_tr_b16 v[232:233], v244 offset:0\n\t" \
    "ds_read_b64_tr_b16 v[234:235], v244 offset:1536\n\t" \
    "ds_read_b64_tr_b16 v[160:161], v244 offset:32\n\t" \
    "ds_read_b64_tr_b16 v[162:163], v244 offset:1568\n\t" \
    "ds_read_b64_tr_b16 v[168:169], v244 offset:64\n\t" \
    "ds_read_b64_tr_b16 v[170:171], v244 offset:1600\n\t" \
    "v_add_u32 v244, 0xf000, v243\n\t" \
    "s_waitcnt vmcnt(12) lgkmcnt(0)\n\t" \
    "s_barrier \n\t" \
    "s_add_u32 m0, %[wm], 0xc000\n\t" \
    "s_mov_b32 s44, 0x6c0000\n\t" \
    "buffer_load_dwordx4 v240, %[wr], s44 offen offset:0 nt lds\n\t" \
    "buffer_load_dwordx4 v241, %[wr], s44 offen offset:1024 nt lds\n\t" \
    "buffer_load_dwordx4 v242, %[wr], s44 offen offset:2048 nt lds\n\t" \
    "ds_read_b64_tr_b16 v[236:237], v244 offset:0\n\t" \
    "ds_read_b64_tr_b16 v[238:239], v244 offset:1536\n\t" \
    "ds_read_b64_tr_b16 v[164:165], v244 offset:32\n\t" \
    "ds_read_b64_tr_b16 v[166:167], v244 offset:1568\n\t" \
    "ds_read_b64_tr_b16 v[172:173], v244 offset:64\n\t" \
    "ds_read_b64_tr_b16 v[174:175], v244 offset:1600\n\t" \
    "v_add_u32 v244, 0x0, v243\n\t" \
    "s_waitcnt vmcnt(12) lgkmcnt(0)\n\t" \
    "s_barrier \n\t" \
    "s_add_u32 m0, %[wm], 0xf000\n\t" \
    "s_mov_b32 s44, 0x720000\n\t" \
    "buffer_load_dwordx4 v240, %[wr], s44 offen offset:0 nt lds\n\t" \
    "buffer_load_dwordx4 v241, %[wr], s44 offen offset:1024 nt lds\n\t" \
    "buffer_load_dwordx4 v242, %[wr], s44 offen offset:2048 nt lds\n\t" \
    "ds_read_b64_tr_b16 v[176:177], v244 offset:0\n\t" \
    "ds_read_b64_tr_b16 v[178:179], v244 offset:1536\n\t" \
    "ds_read_b64_tr_b16 v[184:185], v244 offset:32\n\t" \
    "ds_read_b64_tr_b16 v[186:187], v244 offset:1568\n\t" \
    "ds_read_b64_tr_b16 v[192:193], v244 offset:64\n\t" \
    "ds_read_b64_tr_b16 v[194:195], v244 offset:1600\n\t" \
    "v_add_u32 v244, 0x3000, v243\n\t" \
    "s_waitcnt vmcnt(12) lgkmcnt(0)\n\t" \
    "s_barrier \n\t" \
    "ds_read_b64_tr_b16 v[180:181], v244 offset:0\n\t" \
    "ds_read_b64_tr_b16 v[182:183], v244 offset:1536\n\t" \
    "ds_read_b64_tr_b16 v[188:189], v244 offset:32\n\t" \
    "ds_read_b64_tr_b16 v[190:191], v244 offset:1568\n\t" \
    "ds_read_b64_tr_b16 v[196:197], v244 offset:64\n\t" \
    "ds_read_b64_tr_b16 v[198:199], v244 offset:1600\n\t" \
    "v_add_u32 v244, 0x6000, v243\n\t" \
    "s_waitcnt vmcnt(9) lgkmcnt(0)\n\t" \
    "s_barrier \n\t" \
    "ds_read_b64_tr_b16 v[200:201], v244 offset:0\n\t" \
    "ds_read_b64_tr_b16 v[202:203], v244 offset:1536\n\t" \
    "ds_read_b64_tr_b16 v[0:1], v244 offset:32\n\t" \
    "ds_read_b64_tr_b16 v[2:3], v244 offset:1568\n\t" \
    "ds_read_b64_tr_b16 v[8:9], v244 offset:64\n\t" \
    "ds_read_b64_tr_b16 v[10:11], v244 offset:1600\n\t" \
    "v_add_u32 v244, 0x9000, v243\n\t" \
    "s_waitcnt vmcnt(6) lgkmcnt(0)\n\t" \
    "s_barrier \n\t" \
    "ds_read_b64_tr_b16 v[204:205], v244 offset:0\n\t" \
    "ds_read_b64_tr_b16 v[206:207], v244 offset:1536\n\t" \
    "ds_read_b64_tr_b16 v[4:5], v244 offset:32\n\t" \
    "ds_read_b64_tr_b16 v[6:7], v244 offset:1568\n\t" \
    "ds_read_b64_tr_b16 v[12:13], v244 offset:64\n\t" \
    "ds_read_b64_tr_b16 v[14:15], v244 offset:1600\n\t" \
    "v_add_u32 v244, 0xc000, v243\n\t" \
    "s_waitcnt vmcnt(3) lgkmcnt(0)\n\t" \
    "s_barrier \n\t" \
    "ds_read_b64_tr_b16 v[16:17], v244 offset:0\n\t" \
    "ds_read_b64_tr_b16 v[18:19], v244 offset:1536\n\t" \
    "ds_read_b64_tr_b16 v[24:25], v244 offset:32\n\t" \
    "ds_read_b64_tr_b16 v[26:27], v244 offset:1568\n\t" \
    "ds_read_b64_tr_b16 v[32:33], v244 offset:64\n\t" \
    "ds_read_b64_tr_b16 v[34:35], v244 offset:1600\n\t" \
    "s_mov_b32 s43, 0\n\t" \
    "v_cmp_eq_u32 vcc, 0, v252\n\t" \
    "s_cbranch_vccnz .Lv33_%=\n\t" \
    "s_mov_b32 m0, 0x3000\n\t" \
    "s_add_u32 s49, s42, 0x0\n\t" \
    "buffer_load_dwordx4 v253, s[60:63], s49 offen offset:0 sc1 lds\n\t" \
    "buffer_load_dwordx4 v253, s[60:63], s49 offen offset:1024 sc1 lds\n\t" \
    "buffer_load_dwordx4 v253, s[60:63], s49 offen offset:2048 sc1 lds\n\t" \
    "buffer_load_dwordx4 v253, s[60:63], s49 offen offset:3072 sc1 lds\n\t" \
    "s_mov_b32 m0, 0x4c00\n\t" \
    "s_add_u32 s49, s42, 0x4c00\n\t" \
    "buffer_load_dwordx4 v253, s[60:63], s49 offen offset:0 sc1 lds\n\t" \
    "s_mov_b32 m0, 0x8000\n\t" \
    "s_add_u32 s49, s42, 0x1000\n\t" \
    "buffer_load_dwordx4 v253, s[60:63], s49 offen offset:0 sc1 lds\n\t" \
    "buffer_load_dwordx4 v253, s[60:63], s49 offen offset:1024 sc1 lds\n\t" \
    "buffer_load_dwordx4 v253, s[60:63], s49 offen offset:2048 sc1 lds\n\t" \
    "buffer_load_dwordx4 v253, s[60:63], s49 offen offset:3072 sc1 lds\n\t" \
    "s_mov_b32 m0, 0x9c00\n\t" \
    "s_add_u32 s49, s42, 0x5c00\n\t" \
    "buffer_load_dwordx4 v253, s[60:63], s49 offen offset:0 sc1 lds\n\t" \
    "s_mov_b32 s43, 1\n\t" \
    ".Lv33_%=:\n\t" \
    "v_add_u32 v244, 0xf000, v243\n\t" \
    "s_waitcnt vmcnt(0) lgkmcnt(0)\n\t" \
    "s_barrier \n\t" \
    "ds_read_b64_tr_b16 v[20:21], v244 offset:0\n\t" \
    "ds_read_b64_tr_b16 v[22:23], v244 offset:1536\n\t" \
    "ds_read_b64_tr_b16 v[28:29], v244 offset:32\n\t" \
    "ds_read_b64_tr_b16 v[30:31], v244 offset:1568\n\t" \
    "ds_read_b64_tr_b16 v[36:37], v244 offset:64\n\t" \
    "ds_read_b64_tr_b16 v[38:39], v244 offset:1600\n\t" \
    "s_waitcnt lgkmcnt(0)\n\t" \
    "s_barrier \n\t" \
    LST0 \
    "ds_write_b128 v254, v[200:203] offset:55296\n\t" \
    "ds_write_b128 v254, v[204:207] offset:56320\n\t" \
    "ds_write_b128 v254, v[208:211] offset:57344\n\t" \
    "ds_write_b128 v254, v[212:215] offset:58368\n\t" \
    "ds_write_b128 v254, v[216:219] offset:59392\n\t" \
    "ds_write_b128 v254, v[220:223] offset:60416\n\t" \
    "ds_write_b128 v254, v[224:227] offset:61440\n\t" \
    "ds_write_b128 v254, v[228:231] offset:62464\n\t" \
    "ds_write_b128 v254, v[232:235] offset:63488\n\t" \
    "ds_write_b128 v254, v[236:239] offset:64512\n\t" \
    "s_cmp_lg_u32 s43, 0\n\t" \
    "s_cbranch_scc1 .Lv34_%=\n\t" \
    "s_mov_b32 s44, 0\n\t" \
    ".Lv35_%=:\n\t" \
    "v_cmp_ge_u32 vcc, s44, v252\n\t" \
    "s_cbranch_vccz .Lv36_%=\n\t" \
    "s_cmp_eq_u32 s41, 0\n\t" \
    "s_cbranch_scc1 .Lv36_%=\n\t" \
    "s_sub_u32 s41, s41, 1\n\t" \
    "s_sleep 8\n\t" \
    "v_and_b32 v252, 0xf0, v253\n\t" \
    "v_lshlrev_b32 v252, 3, v252\n\t" \
    "buffer_load_dword v252, v252, s[60:63], %[fl] offen sc1\n\t" \
    "s_waitcnt vmcnt(0)\n\t" \
    "s_branch .Lv35_%=\n\t" \
    ".Lv36_%=:\n\t" \
    "s_mov_b32 m0, 0x3000\n\t" \
    "s_add_u32 s49, s42, 0x0\n\t" \
    "buffer_load_dwordx4 v253, s[60:63], s49 offen offset:0 sc1 lds\n\t" \
    "buffer_load_dwordx4 v253, s[60:63], s49 offen offset:1024 sc1 lds\n\t" \
    "buffer_load_dwordx4 v253, s[60:63], s49 offen offset:2048 sc1 lds\n\t" \
    "buffer_load_dwordx4 v253, s[60:63], s49 offen offset:3072 sc1 lds\n\t" \
    "s_mov_b32 m0, 0x4c00\n\t" \
    "s_add_u32 s49, s42, 0x4c00\n\t" \
    "buffer_load_dwordx4 v253, s[60:63], s49 offen offset:0 sc1 lds\n\t" \
    "s_mov_b32 m0, 0x8000\n\t" \
    "s_add_u32 s49, s42, 0x1000\n\t" \
    "buffer_load_dwordx4 v253, s[60:63], s49 offen offset:0 sc1 lds\n\t" \
    "buffer_load_dwordx4 v253, s[60:63], s49 offen offset:1024 sc1 lds\n\t" \
    "buffer_load_dwordx4 v253, s[60:63], s49 offen offset:2048 sc1 lds\n\t" \
    "buffer_load_dwordx4 v253, s[60:63], s49 offen offset:3072 sc1 lds\n\t" \
    "s_mov_b32 m0, 0x9c00\n\t" \
    "s_add_u32 s49, s42, 0x5c00\n\t" \
    "buffer_load_dwordx4 v253, s[60:63], s49 offen offset:0 sc1 lds\n\t" \
    ".Lv34_%=:\n\t" \
    "s_waitcnt vmcnt(0) lgkmcnt(0)\n\t" \
    "s_barrier \n\t" \
    "ds_read_b128 v[224:227], v253 offset:16384\n\t" \
    "ds_read_b128 v[244:247], v254 offset:55296\n\t" \
    "ds_read_b128 v[228:231], v253 offset:17408\n\t" \
    "ds_read_b128 v[232:235], v253 offset:36864\n\t" \
    "ds_read_b128 v[236:239], v253 offset:37888\n\t" \
    "ds_read_b128 v[248:251], v254 offset:56320\n\t" \
    ".Lv37_%=:\n\t" \
    LST7 \
    "s_waitcnt lgkmcnt(5)\n\t" \
    "v_mfma_f32_16x16x32_bf16 v[204:207], v[0:3], v[224:227], 0\n\t" \
    "s_waitcnt lgkmcnt(3)\n\t" \
    "v_mfma_f32_16x16x32_bf16 v[204:207], v[4:7], v[228:231], v[204:207]\n\t" \
    "ds_read_b128 v[240:243], v253 offset:18432\n\t" \
    "v_mfma_f32_16x16x32_bf16 v[208:211], v[8:11], v[224:227], 0\n\t" \
    "v_mfma_f32_16x16x32_bf16 v[208:211], v[12:15], v[228:231], v[208:211]\n\t" \
    "v_mfma_f32_16x16x32_bf16 v[200:203], v[244:247], v[224:227], 0\n\t" \
    "s_waitcnt lgkmcnt(1)\n\t" \
    "v_mfma_f32_16x16x32_bf16 v[200:203], v[248:251], v[228:231], v[200:203]\n\t" \
    "v_mfma_f32_16x16x32_bf16 v[216:219], v[0:3], v[232:235], 0\n\t" \
    "ds_read_b128 v[224:227], v253 offset:19456\n\t" \
    "v_mfma_f32_16x16x32_bf16 v[216:219], v[4:7], v[236:239], v[216:219]\n\t" \
    "ds_read_b128 v[228:231], v253 offset:0\n\t" \
    "v_mfma_f32_16x16x32_bf16 v[220:223], v[8:11], v[232:235], 0\n\t" \
    "v_mfma_f32_16x16x32_bf16 v[220:223], v[12:15], v[236:239], v[220:223]\n\t" \
    "v_mfma_f32_16x16x32_bf16 v[212:215], v[244:247], v[232:235], 0\n\t" \
    "ds_read_b128 v[244:247], v254 offset:57344\n\t" \
    "v_mfma_f32_16x16x32_bf16 v[212:215], v[248:251], v[236:239], v[212:215]\n\t" \
    "ds_read_b128 v[248:251], v254 offset:58368\n\t" \
    "s_waitcnt lgkmcnt(4)\n\t" \
    "v_mfma_f32_16x16x32_bf16 v[204:207], v[24:27], v[240:243], v[204:207]\n\t" \
    "ds_read_b128 v[232:235], v253 offset:1024\n\t" \
    "s_waitcnt lgkmcnt(4)\n\t" \
    "v_mfma_f32_16x16x32_bf16 v[204:207], v[28:31], v[224:227], v[204:207]\n\t" \
    "ds_read_b128 v[236:239], v253 offset:20480\n\t" \
    "v_mfma_f32_16x16x32_bf16 v[208:211], v[32:35], v[240:243], v[208:211]\n\t" \
    "v_mfma_f32_16x16x32_bf16 v[208:211], v[36:39], v[224:227], v[208:211]\n\t" \
    "v_mfma_f32_16x16x32_bf16 v[200:203], v[16:19], v[240:243], v[200:203]\n\t" \
    "v_mfma_f32_16x16x32_bf16 v[200:203], v[20:23], v[224:227], v[200:203]\n\t" \
    "s_waitcnt lgkmcnt(4)\n\t" \
    "v_mfma_f32_16x16x32_bf16 v[204:207], v[40:43], v[228:231], v[204:207]\n\t" \
    "ds_read_b128 v[240:243], v253 offset:21504\n\t" \
    "s_waitcnt lgkmcnt(2)\n\t" \
    "v_mfma_f32_16x16x32_bf16 v[204:207], v[44:47], v[232:235], v[204:207]\n\t" \
    "ds_read_b128 v[224:227], v253 offset:2048\n\t" \
    "v_mfma_f32_16x16x32_bf16 v[208:211], v[48:51], v[228:231], v[208:211]\n\t" \
    "v_mfma_f32_16x16x32_bf16 v[208:211], v[52:55], v[232:235], v[208:211]\n\t" \
    "v_mfma_f32_16x16x32_bf16 v[200:203], v[244:247], v[228:231], v[200:203]\n\t" \
    "v_mfma_f32_16x16x32_bf16 v[200:203], v[248:251], v[232:235], v[200:203]\n\t" \
    "s_waitcnt lgkmcnt(2)\n\t" \
    "v_mfma_f32_16x16x32_bf16 v[216:219], v[40:43], v[236:239], v[216:219]\n\t" \
    "ds_read_b128 v[228:231], v253 offset:3072\n\t" \
    "s_add_u32 s44, s40, 1\n\t" \
    "s_waitcnt lgkmcnt(2)\n\t" \
    "v_mfma_f32_16x16x32_bf16 v[216:219], v[44:47], v[240:243], v[216:219]\n\t" \
    "ds_read_b128 v[232:235], v253 offset:4096\n\t" \
    "s_and_b32 s45, s44, 15\n\t" \
    "v_mfma_f32_16x16x32_bf16 v[220:223], v[48:51], v[236:239], v[220:223]\n\t" \
    "s_cmp_eq_u32 s45, 0\n\t" \
    "v_mfma_f32_16x16x32_bf16 v[220:223], v[52:55], v[240:243], v[220:223]\n\t" \
    "s_cselect_b32 s45, %[cj], %[cs]\n\t" \
    "v_mfma_f32_16x16x32_bf16 v[212:215], v[244:247], v[236:239], v[212:215]\n\t" \
    "ds_read_b128 v[244:247], v254 offset:59392\n\t" \
    "s_cmp_eq_u32 s44, 64\n\t" \
    "v_mfma_f32_16x16x32_bf16 v[212:215], v[248:251], v[240:243], v[212:215]\n\t" \
    "ds_read_b128 v[248:251], v254 offset:60416\n\t" \
    "s_cselect_b32 s45, 0, s45\n\t" \
    "s_waitcnt lgkmcnt(4)\n\t" \
    "v_mfma_f32_16x16x32_bf16 v[204:207], v[64:67], v[224:227], v[204:207]\n\t" \
    "ds_read_b128 v[236:239], v253 offset:5120\n\t" \
    "s_add_u32 s42, s42, s45\n\t" \
    "s_waitcnt lgkmcnt(4)\n\t" \
    "v_mfma_f32_16x16x32_bf16 v[204:207], v[68:71], v[228:231], v[204:207]\n\t" \
    "ds_read_b128 v[240:243], v253 offset:24576\n\t" \
    "v_mfma_f32_16x16x32_bf16 v[208:211], v[72:75], v[224:227], v[208:211]\n\t" \
    "v_mfma_f32_16x16x32_bf16 v[208:211], v[76:79], v[228:231], v[208:211]\n\t" \
    "v_mfma_f32_16x16x32_bf16 v[200:203], v[56:59], v[224:227], v[200:203]\n\t" \
    "v_mfma_f32_16x16x32_bf16 v[200:203], v[60:63], v[228:231], v[200:203]\n\t" \
    "s_waitcnt lgkmcnt(4)\n\t" \
    "v_mfma_f32_16x16x32_bf16 v[204:207], v[80:83], v[232:235], v[204:207]\n\t" \
    "ds_read_b128 v[224:227], v253 offset:25600\n\t" \
    "s_waitcnt lgkmcnt(2)\n\t" \
    "v_mfma_f32_16x16x32_bf16 v[204:207], v[84:87], v[236:239], v[204:207]\n\t" \
    "ds_read_b128 v[228:231], v253 offset:6144\n\t" \
    "v_mfma_f32_16x16x32_bf16 v[208:211], v[88:91], v[232:235], v[208:211]\n\t" \
    "v_mfma_f32_16x16x32_bf16 v[208:211], v[92:95], v[236:239], v[208:211]\n\t" \
    "v_mfma_f32_16x16x32_bf16 v[200:203], v[244:247], v[232:235], v[200:203]\n\t" \
    "v_mfma_f32_16x16x32_bf16 v[200:203], v[248:251], v[236:239], v[200:203]\n\t" \
    "s_waitcnt lgkmcnt(2)\n\t" \
    "v_mfma_f32_16x16x32_bf16 v[216:219], v[80:83], v[240:243], v[216:219]\n\t" \
    "ds_read_b128 v[232:235], v253 offset:7168\n\t" \
    "s_and_b32 s44, s40, 3\n\t" \
    "s_waitcnt lgkmcnt(2)\n\t" \
    "v_mfma_f32_16x16x32_bf16 v[216:219], v[84:87], v[224:227], v[216:219]\n\t" \
    "ds_read_b128 v[236:239], v253 offset:8192\n\t" \
    "s_lshl_b32 s44, s44, 17\n\t" \
    "v_mfma_f32_16x16x32_bf16 v[220:223], v[88:91], v[240:243], v[220:223]\n\t" \
    "s_add_u32 s44, s44, %[mb]\n\t" \
    "v_mfma_f32_16x16x32_bf16 v[220:223], v[92:95], v[224:227], v[220:223]\n\t" \
    "s_add_u32 s46, s44, 0x4000\n\t" \
    "v_mfma_f32_16x16x32_bf16 v[212:215], v[244:247], v[240:243], v[212:215]\n\t" \
    "ds_read_b128 v[244:247], v254 offset:61440\n\t" \
    "s_add_u32 s47, s44, 0x4400\n\t" \
    "v_mfma_f32_16x16x32_bf16 v[212:215], v[248:251], v[224:227], v[212:215]\n\t" \
    "ds_read_b128 v[248:251], v254 offset:62464\n\t" \
    "s_waitcnt lgkmcnt(4)\n\t" \
    "v_mfma_f32_16x16x32_bf16 v[204:207], v[104:107], v[228:231], v[204:207]\n\t" \
    "ds_read_b128 v[240:243], v253 offset:9216\n\t" \
    "s_waitcnt lgkmcnt(4)\n\t" \
    "v_mfma_f32_16x16x32_bf16 v[204:207], v[108:111], v[232:235], v[204:207]\n\t" \
    "ds_read_b128 v[224:227], v253 offset:28672\n\t" \
    "v_mfma_f32_16x16x32_bf16 v[208:211], v[112:115], v[228:231], v[208:211]\n\t" \
    "v_mfma_f32_16x16x32_bf16 v[208:211], v[116:119], v[232:235], v[208:211]\n\t" \
    "v_mfma_f32_16x16x32_bf16 v[200:203], v[96:99], v[228:231], v[200:203]\n\t" \
    "v_mfma_f32_16x16x32_bf16 v[200:203], v[100:103], v[232:235], v[200:203]\n\t" \
    "s_waitcnt lgkmcnt(4)\n\t" \
    "v_mfma_f32_16x16x32_bf16 v[204:207], v[120:123], v[236:239], v[204:207]\n\t" \
    "ds_read_b128 v[228:231], v253 offset:29696\n\t" \
    "s_waitcnt lgkmcnt(2)\n\t" \
    "v_mfma_f32_16x16x32_bf16 v[204:207], v[124:127], v[240:243], v[204:207]\n\t" \
    "ds_read_b128 v[232:235], v253 offset:10240\n\t" \
    "v_mfma_f32_16x16x32_bf16 v[208:211], v[128:131], v[236:239], v[208:211]\n\t" \
    "v_mfma_f32_16x16x32_bf16 v[208:211], v[132:135], v[240:243], v[208:211]\n\t" \
    "v_mfma_f32_16x16x32_bf16 v[200:203], v[244:247], v[236:239], v[200:203]\n\t" \
    "v_mfma_f32_16x16x32_bf16 v[200:203], v[248:251], v[240:243], v[200:203]\n\t" \
    "s_waitcnt lgkmcnt(2)\n\t" \
    "v_mfma_f32_16x16x32_bf16 v[216:219], v[120:123], v[224:227], v[216:219]\n\t" \
    "ds_read_b128 v[236:239], v253 offset:11264\n\t" \
    "s_waitcnt lgkmcnt(2)\n\t" \
    "v_mfma_f32_16x16x32_bf16 v[216:219], v[124:127], v[228:231], v[216:219]\n\t" \
    "ds_read_b128 v[240:243], v253 offset:12288\n\t" \
    "v_mfma_f32_16x16x32_bf16 v[220:223], v[128:131], v[224:227], v[220:223]\n\t" \
    "v_mfma_f32_16x16x32_bf16 v[220:223], v[132:135], v[228:231], v[220:223]\n\t" \
    "v_mfma_f32_16x16x32_bf16 v[212:215], v[244:247], v[224:227], v[212:215]\n\t" \
    "ds_read_b128 v[244:247], v254 offset:63488\n\t" \
    "v_mfma_f32_16x16x32_bf16 v[212:215], v[248:251], v[228:231], v[212:215]\n\t" \
    "ds_read_b128 v[248:251], v254 offset:64512\n\t" \
    "v_and_b32 v252, 0xf0, v253\n\t" \
    "v_lshlrev_b32 v252, 3, v252\n\t" \
    "buffer_load_dword v252, v252, s[60:63], %[fl] offen sc1\n\t" \
    "s_waitcnt lgkmcnt(4)\n\t" \
    "v_mfma_f32_16x16x32_bf16 v[204:207], v[144:147], v[232:235], v[204:207]\n\t" \
    "ds_read_b128 v[224:227], v253 offset:13312\n\t" \
    "s_waitcnt lgkmcnt(4)\n\t" \
    "v_mfma_f32_16x16x32_bf16 v[204:207], v[148:151], v[236:239], v[204:207]\n\t" \
    "ds_read_b128 v[228:231], v253 offset:32768\n\t" \
    "v_mfma_f32_16x16x32_bf16 v[208:211], v[152:155], v[232:235], v[208:211]\n\t" \
    "v_mfma_f32_16x16x32_bf16 v[208:211], v[156:159], v[236:239], v[208:211]\n\t" \
    "v_mfma_f32_16x16x32_bf16 v[200:203], v[136:139], v[232:235], v[200:203]\n\t" \
    "v_mfma_f32_16x16x32_bf16 v[200:203], v[140:143], v[236:239], v[200:203]\n\t" \
    "s_waitcnt lgkmcnt(4)\n\t" \
    "v_mfma_f32_16x16x32_bf16 v[204:207], v[160:163], v[240:243], v[204:207]\n\t" \
    "ds_read_b128 v[232:235], v253 offset:33792\n\t" \
    "s_waitcnt lgkmcnt(2)\n\t" \
    "v_mfma_f32_16x16x32_bf16 v[204:207], v[164:167], v[224:227], v[204:207]\n\t" \
    "ds_read_b128 v[236:239], v253 offset:14336\n\t" \
    "v_mfma_f32_16x16x32_bf16 v[208:211], v[168:171], v[240:243], v[208:211]\n\t" \
    "v_mfma_f32_16x16x32_bf16 v[208:211], v[172:175], v[224:227], v[208:211]\n\t" \
    "v_mfma_f32_16x16x32_bf16 v[200:203], v[244:247], v[240:243], v[200:203]\n\t" \
    "v_mfma_f32_16x16x32_bf16 v[200:203], v[248:251], v[224:227], v[200:203]\n\t" \
    "s_waitcnt lgkmcnt(2)\n\t" \
    "v_mfma_f32_16x16x32_bf16 v[216:219], v[160:163], v[228:231], v[216:219]\n\t" \
    "ds_read_b128 v[240:243], v253 offset:15360\n\t" \
    "s_waitcnt lgkmcnt(2)\n\t" \
    "v_mfma_f32_16x16x32_bf16 v[216:219], v[164:167], v[232:235], v[216:219]\n\t" \
    "ds_read_b128 v[224:227], v253 offset:38912\n\t" \
    "v_mfma_f32_16x16x32_bf16 v[220:223], v[168:171], v[228:231], v[220:223]\n\t" \
    "v_mfma_f32_16x16x32_bf16 v[220:223], v[172:175], v[232:235], v[220:223]\n\t" \
    "v_mfma_f32_16x16x32_bf16 v[212:215], v[244:247], v[228:231], v[212:215]\n\t" \
    "v_mfma_f32_16x16x32_bf16 v[212:215], v[248:251], v[232:235], v[212:215]\n\t" \
    "s_waitcnt lgkmcnt(2)\n\t" \
    "v_mfma_f32_16x16x32_bf16 v[204:207], v[184:187], v[236:239], v[204:207]\n\t" \
    "ds_read_b128 v[228:231], v253 offset:39936\n\t" \
    "s_waitcnt lgkmcnt(2)\n\t" \
    "v_mfma_f32_16x16x32_bf16 v[204:207], v[188:191], v[240:243], v[204:207]\n\t" \
    "ds_read_b128 v[232:235], v253 offset:22528\n\t" \
    "v_mfma_f32_16x16x32_bf16 v[208:211], v[192:195], v[236:239], v[208:211]\n\t" \
    "v_mfma_f32_16x16x32_bf16 v[208:211], v[196:199], v[240:243], v[208:211]\n\t" \
    "v_mfma_f32_16x16x32_bf16 v[200:203], v[176:179], v[236:239], v[200:203]\n\t" \
    "v_mfma_f32_16x16x32_bf16 v[200:203], v[180:183], v[240:243], v[200:203]\n\t" \
    "s_waitcnt lgkmcnt(2)\n\t" \
    "v_mfma_f32_16x16x32_bf16 v[216:219], v[24:27], v[224:227], v[216:219]\n\t" \
    "ds_read_b128 v[236:239], v253 offset:23552\n\t" \
    "s_waitcnt lgkmcnt(2)\n\t" \
    "v_mfma_f32_16x16x32_bf16 v[216:219], v[28:31], v[228:231], v[216:219]\n\t" \
    "ds_read_b128 v[240:243], v253 offset:26624\n\t" \
    "v_mfma_f32_16x16x32_bf16 v[220:223], v[32:35], v[224:227], v[220:223]\n\t" \
    "v_cvt_pk_bf16_f32 v204, v204, v205\n\t" \
    "v_mfma_f32_16x16x32_bf16 v[220:223], v[36:39], v[228:231], v[220:223]\n\t" \
    "v_cvt_pk_bf16_f32 v205, v206, v207\n\t" \
    "v_mfma_f32_16x16x32_bf16 v[212:215], v[16:19], v[224:227], v[212:215]\n\t" \
    "ds_write_b64 v254, v[204:205] offset:4104\n\t" \
    "v_mfma_f32_16x16x32_bf16 v[212:215], v[20:23], v[228:231], v[212:215]\n\t" \
    "v_cvt_pk_bf16_f32 v208, v208, v209\n\t" \
    "s_waitcnt lgkmcnt(3)\n\t" \
    "v_mfma_f32_16x16x32_bf16 v[216:219], v[64:67], v[232:235], v[216:219]\n\t" \
    "ds_read_b128 v[224:227], v253 offset:27648\n\t" \
    "v_cvt_pk_bf16_f32 v209, v210, v211\n\t" \
    "s_waitcnt lgkmcnt(3)\n\t" \
    "v_mfma_f32_16x16x32_bf16 v[216:219], v[68:71], v[236:239], v[216:219]\n\t" \
    "ds_read_b128 v[228:231], v253 offset:30720\n\t" \
    "ds_write_b64 v254, v[208:209] offset:5128\n\t" \
    "v_mfma_f32_16x16x32_bf16 v[220:223], v[72:75], v[232:235], v[220:223]\n\t" \
    "v_cvt_pk_bf16_f32 v200, v200, v201\n\t" \
    "v_mfma_f32_16x16x32_bf16 v[220:223], v[76:79], v[236:239], v[220:223]\n\t" \
    "v_cvt_pk_bf16_f32 v201, v202, v203\n\t" \
    "v_mfma_f32_16x16x32_bf16 v[212:215], v[56:59], v[232:235], v[212:215]\n\t" \
    "ds_write_b64 v254, v[200:201] offset:3080\n\t" \
    "v_mfma_f32_16x16x32_bf16 v[212:215], v[60:63], v[236:239], v[212:215]\n\t" \
    "s_waitcnt lgkmcnt(5)\n\t" \
    "v_mfma_f32_16x16x32_bf16 v[216:219], v[104:107], v[240:243], v[216:219]\n\t" \
    "ds_read_b128 v[232:235], v253 offset:31744\n\t" \
    "s_waitcnt lgkmcnt(4)\n\t" \
    "v_mfma_f32_16x16x32_bf16 v[216:219], v[108:111], v[224:227], v[216:219]\n\t" \
    "ds_read_b128 v[236:239], v253 offset:34816\n\t" \
    "v_mfma_f32_16x16x32_bf16 v[220:223], v[112:115], v[240:243], v[220:223]\n\t" \
    "v_mfma_f32_16x16x32_bf16 v[220:223], v[116:119], v[224:227], v[220:223]\n\t" \
    "v_mfma_f32_16x16x32_bf16 v[212:215], v[96:99], v[240:243], v[212:215]\n\t" \
    "v_mfma_f32_16x16x32_bf16 v[212:215], v[100:103], v[224:227], v[212:215]\n\t" \
    "ds_read_b128 v[240:243], v253 offset:35840\n\t" \
    "s_waitcnt lgkmcnt(3)\n\t" \
    "s_barrier \n\t" \
    "v_mfma_f32_16x16x32_bf16 v[216:219], v[144:147], v[228:231], v[216:219]\n\t" \
    "s_waitcnt lgkmcnt(2)\n\t" \
    "v_mfma_f32_16x16x32_bf16 v[216:219], v[148:151], v[232:235], v[216:219]\n\t" \
    "s_waitcnt lgkmcnt(1)\n\t" \
    "v_mfma_f32_16x16x32_bf16 v[216:219], v[184:187], v[236:239], v[216:219]\n\t" \
    "s_waitcnt lgkmcnt(0)\n\t" \
    "v_mfma_f32_16x16x32_bf16 v[216:219], v[188:191], v[240:243], v[216:219]\n\t" \
    "v_mfma_f32_16x16x32_bf16 v[220:223], v[152:155], v[228:231], v[220:223]\n\t" \
    "v_mfma_f32_16x16x32_bf16 v[220:223], v[156:159], v[232:235], v[220:223]\n\t" \
    "v_mfma_f32_16x16x32_bf16 v[220:223], v[192:195], v[236:239], v[220:223]\n\t" \
    "v_cvt_pk_bf16_f32 v216, v216, v217\n\t" \
    "v_cvt_pk_bf16_f32 v217, v218, v219\n\t" \
    "v_mfma_f32_16x16x32_bf16 v[220:223], v[196:199], v[240:243], v[220:223]\n\t" \
    "ds_write_b64 v254, v[216:217] offset:10248\n\t" \
    "v_mfma_f32_16x16x32_bf16 v[212:215], v[136:139], v[228:231], v[212:215]\n\t" \
    "v_mfma_f32_16x16x32_bf16 v[212:215], v[140:143], v[232:235], v[212:215]\n\t" \
    "v_mfma_f32_16x16x32_bf16 v[212:215], v[176:179], v[236:239], v[212:215]\n\t" \
    "v_cvt_pk_bf16_f32 v220, v220, v221\n\t" \
    "v_cvt_pk_bf16_f32 v221, v222, v223\n\t" \
    "v_mfma_f32_16x16x32_bf16 v[212:215], v[180:183], v[240:243], v[212:215]\n\t" \
    "ds_write_b64 v254, v[220:221] offset:11272\n\t" \
    "s_nop 6\n\t" \
    "v_cvt_pk_bf16_f32 v212, v212, v213\n\t" \
    "v_cvt_pk_bf16_f32 v213, v214, v215\n\t" \
    "ds_write_b64 v254, v[212:213] offset:9224\n\t" \
    LST6 \
    "s_waitcnt vmcnt(0) lgkmcnt(0)\n\t" \
    "s_barrier \n\t" \
    "s_setprio 2\n\t" \
    "s_add_u32 s44, s40, 1\n\t" \
    "s_min_u32 s44, s44, 63\n\t" \
    ".Lv38_%=:\n\t" \
    "v_cmp_ge_u32 vcc, s44, v252\n\t" \
    "s_cbranch_vccz .Lv39_%=\n\t" \
    "s_cmp_eq_u32 s41, 0\n\t" \
    "s_cbranch_scc1 .Lv39_%=\n\t" \
    "s_sub_u32 s41, s41, 1\n\t" \
    "s_sleep 2\n\t" \
    "v_and_b32 v252, 0xf0, v253\n\t" \
    "v_lshlrev_b32 v252, 3, v252\n\t" \
    "buffer_load_dword v252, v252, s[60:63], %[fl] offen sc1\n\t" \
    "s_waitcnt vmcnt(0)\n\t" \
    "s_branch .Lv38_%=\n\t" \
    ".Lv39_%=:\n\t" \
    "s_mov_b32 m0, 0x3000\n\t" \
    "s_add_u32 s49, s42, 0x0\n\t" \
    "buffer_load_dwordx4 v253, s[60:63], s49 offen offset:0 sc1 lds\n\t" \
    "s_sleep 1\n\t" \
    "buffer_load_dwordx4 v253, s[60:63], s49 offen offset:1024 sc1 lds\n\t" \
    "s_sleep 1\n\t" \
    "buffer_load_dwordx4 v253, s[60:63], s49 offen offset:2048 sc1 lds\n\t" \
    "s_sleep 1\n\t" \
    "buffer_load_dwordx4 v253, s[60:63], s49 offen offset:3072 sc1 lds\n\t" \
    "s_sleep 1\n\t" \
    "s_mov_b32 m0, 0x4c00\n\t" \
    "s_add_u32 s49, s42, 0x4c00\n\t" \
    "buffer_load_dwordx4 v253, s[60:63], s49 offen offset:0 sc1 lds\n\t" \
    "s_sleep 1\n\t" \
    "s_mov_b32 m0, 0x8000\n\t" \
    "s_add_u32 s49, s42, 0x1000\n\t" \
    "buffer_load_dwordx4 v253, s[60:63], s49 offen offset:0 sc1 lds\n\t" \
    "s_sleep 1\n\t" \
    "buffer_load_dwordx4 v253, s[60:63], s49 offen offset:1024 sc1 lds\n\t" \
    "s_sleep 1\n\t" \
    "buffer_load_dwordx4 v253, s[60:63], s49 offen offset:2048 sc1 lds\n\t" \
    "s_sleep 1\n\t" \
    "buffer_load_dwordx4 v253, s[60:63], s49 offen offset:3072 sc1 lds\n\t" \
    "s_sleep 1\n\t" \
    "s_mov_b32 m0, 0x9c00\n\t" \
    "s_add_u32 s49, s42, 0x5c00\n\t" \
    "buffer_load_dwordx4 v253, s[60:63], s49 offen offset:0 sc1 lds\n\t" \
    "s_sleep 1\n\t" \
    "ds_read_b128 v[200:203], v254 offset:55296\n\t" \
    "ds_read_b128 v[204:207], v254 offset:56320\n\t" \
    "ds_read_b128 v[216:219], v254 offset:57344\n\t" \
    "ds_read_b128 v[220:223], v254 offset:58368\n\t" \
    "v_mov_b64 v[208:209], 0\n\t" \
    "v_mov_b64 v[210:211], 0\n\t" \
    "v_mov_b64 v[244:245], 0\n\t" \
    "v_mov_b64 v[246:247], 0\n\t" \
    "v_mov_b64 v[248:249], 0\n\t" \
    "v_mov_b64 v[250:251], 0\n\t" \
    LST1 \
    "s_sleep 5\n\t" \
    "s_mov_b32 m0, 0xc000\n\t" \
    "s_nop 0\n\t" \
    "buffer_load_dwordx4 v253, s[60:63], s46 offen sc1 lds\n\t" \
    "s_mov_b32 m0, 0xc400\n\t" \
    "s_nop 0\n\t" \
    "buffer_load_dwordx4 v253, s[60:63], s47 offen sc1 lds\n\t" \
    "s_waitcnt lgkmcnt(0)\n\t" \
    LFLUSH212 \
    "v_bfe_i32 v212, v253, 5, 1\n\t" \
    "v_and_b32 v212, 0xaaaa, v212\n\t" \
    "v_or_b32 v212, 0x4444, v212\n\t" \
    "v_and_b32 v213, 0xffff, v255\n\t" \
    "v_lshrrev_b32 v214, 16, v255\n\t" \
    ".Lv310_%=:\n\t" \
    "s_waitcnt vmcnt(0)\n\t" \
    "ds_read_b128 v[224:227], v253 offset:49152\n\t" \
    "ds_read_b128 v[228:231], v253 offset:50176\n\t" \
    "s_waitcnt lgkmcnt(1)\n\t" \
    "v_max_u32 v215, v224, v225\n\t" \
    "v_max3_u32 v215, v226, v227, v215\n\t" \
    "s_waitcnt lgkmcnt(0)\n\t" \
    "v_max3_u32 v215, v228, v229, v215\n\t" \
    "v_max3_u32 v215, v230, v231, v215\n\t" \
    "v_cmp_eq_u32 vcc, -1, v215\n\t" \
    "s_cbranch_vccz .Lv311_%=\n\t" \
    "v_max_u32 v215, v224, v225\n\t" \
    "v_max3_u32 v215, v226, v227, v215\n\t" \
    "v_cmp_eq_u32 vcc, -1, v215\n\t" \
    "s_cbranch_vccz .Lv312_%=\n\t" \
    "s_mov_b32 m0, 0xc000\n\t" \
    "s_nop 0\n\t" \
    "buffer_load_dwordx4 v253, s[60:63], s46 offen sc1 lds\n\t" \
    ".Lv312_%=:\n\t" \
    "v_max_u32 v215, v228, v229\n\t" \
    "v_max3_u32 v215, v230, v231, v215\n\t" \
    "v_cmp_eq_u32 vcc, -1, v215\n\t" \
    "s_cbranch_vccz .Lv313_%=\n\t" \
    "s_mov_b32 m0, 0xc400\n\t" \
    "s_nop 0\n\t" \
    "buffer_load_dwordx4 v253, s[60:63], s47 offen sc1 lds\n\t" \
    ".Lv313_%=:\n\t" \
    "s_cmp_eq_u32 s41, 0\n\t" \
    "s_cbranch_scc1 .Lv311_%=\n\t" \
    "s_sub_u32 s41, s41, 1\n\t" \
    "s_branch .Lv310_%=\n\t" \
    ".Lv311_%=:\n\t" \
    LST2 \
    "s_waitcnt lgkmcnt(0)\n\t" \
    "s_barrier \n\t" \
    LST3 \
    "ds_read_b128 v[232:235], v253 offset:40960\n\t" \
    "ds_read_b128 v[236:239], v253 offset:41984\n\t" \
    "ds_read_b128 v[240:243], v253 offset:43008\n\t" \
    "v_smfmac_f32_16x16x64_bf16 v[244:247], v[224:227], v[0:7], v212\n\t" \
    "v_smfmac_f32_16x16x64_bf16 v[248:251], v[224:227], v[8:15], v212\n\t" \
    "v_smfmac_f32_16x16x64_bf16 v[208:211], v[224:227], v[200:207], v212\n\t" \
    "ds_read_b128 v[200:203], v254 offset:59392\n\t" \
    "ds_read_b128 v[204:207], v254 offset:60416\n\t" \
    "ds_read_b128 v[224:227], v253 offset:44032\n\t" \
    "v_smfmac_f32_16x16x64_bf16 v[208:211], v[228:231], v[16:23], v212\n\t" \
    "v_smfmac_f32_16x16x64_bf16 v[244:247], v[228:231], v[24:31], v212\n\t" \
    "v_smfmac_f32_16x16x64_bf16 v[248:251], v[228:231], v[32:39], v212\n\t" \
    "ds_read_b128 v[228:231], v253 offset:45056\n\t" \
    "s_waitcnt lgkmcnt(6)\n\t" \
    "v_smfmac_f32_16x16x64_bf16 v[208:211], v[232:235], v[216:223], v212\n\t" \
    "ds_read_b128 v[216:219], v254 offset:61440\n\t" \
    "ds_read_b128 v[220:223], v254 offset:62464\n\t" \
    "v_smfmac_f32_16x16x64_bf16 v[244:247], v[232:235], v[40:47], v212\n\t" \
    "v_smfmac_f32_16x16x64_bf16 v[248:251], v[232:235], v[48:55], v212\n\t" \
    "ds_read_b128 v[232:235], v253 offset:46080\n\t" \
    "s_waitcnt lgkmcnt(8)\n\t" \
    "v_smfmac_f32_16x16x64_bf16 v[208:211], v[236:239], v[56:63], v212\n\t" \
    "v_smfmac_f32_16x16x64_bf16 v[244:247], v[236:239], v[64:71], v212\n\t" \
    "v_smfmac_f32_16x16x64_bf16 v[248:251], v[236:239], v[72:79], v212\n\t" \
    "ds_read_b128 v[236:239], v253 offset:47104\n\t" \
    "s_waitcnt lgkmcnt(6)\n\t" \
    "v_smfmac_f32_16x16x64_bf16 v[208:211], v[240:243], v[200:207], v212\n\t" \
    "ds_read_b128 v[200:203], v254 offset:63488\n\t" \
    "ds_read_b128 v[204:207], v254 offset:64512\n\t" \
    "v_smfmac_f32_16x16x64_bf16 v[244:247], v[240:243], v[80:87], v212\n\t" \
    "v_smfmac_f32_16x16x64_bf16 v[248:251], v[240:243], v[88:95], v212\n\t" \
    "ds_read_b128 v[240:243], v253 offset:48128\n\t" \
    "s_waitcnt lgkmcnt(8)\n\t" \
    "v_smfmac_f32_16x16x64_bf16 v[208:211], v[224:227], v[96:103], v212\n\t" \
    "v_smfmac_f32_16x16x64_bf16 v[244:247], v[224:227], v[104:111], v212\n\t" \
    "v_smfmac_f32_16x16x64_bf16 v[248:251], v[224:227], v[112:119], v212\n\t" \
    "s_waitcnt lgkmcnt(7)\n\t" \
    "v_smfmac_f32_16x16x64_bf16 v[244:247], v[228:231], v[120:127], v212\n\t" \
    "s_waitcnt lgkmcnt(4)\n\t" \
    "v_smfmac_f32_16x16x64_bf16 v[244:247], v[232:235], v[144:151], v212\n\t" \
    "s_waitcnt lgkmcnt(3)\n\t" \
    "v_smfmac_f32_16x16x64_bf16 v[244:247], v[236:239], v[160:167], v212\n\t" \
    "s_waitcnt lgkmcnt(0)\n\t" \
    "v_smfmac_f32_16x16x64_bf16 v[244:247], v[240:243], v[184:191], v212\n\t" \
    "v_smfmac_f32_16x16x64_bf16 v[248:251], v[228:231], v[128:135], v212\n\t" \
    "v_smfmac_f32_16x16x64_bf16 v[248:251], v[232:235], v[152:159], v212\n\t" \
    "v_smfmac_f32_16x16x64_bf16 v[248:251], v[236:239], v[168:175], v212\n\t" \
    "v_add_f32 v244, v244, v246\n\t" \
    "v_add_f32 v245, v245, v247\n\t" \
    "v_cvt_pk_bf16_f32 v244, v244, v245\n\t" \
    "v_smfmac_f32_16x16x64_bf16 v[248:251], v[240:243], v[192:199], v212\n\t" \
    "ds_write_b16 v213, v244 offset:512\n\t" \
    "ds_write_b16_d16_hi v213, v244 offset:528\n\t" \
    "v_smfmac_f32_16x16x64_bf16 v[208:211], v[228:231], v[216:223], v212\n\t" \
    "v_smfmac_f32_16x16x64_bf16 v[208:211], v[232:235], v[136:143], v212\n\t" \
    "v_smfmac_f32_16x16x64_bf16 v[208:211], v[236:239], v[200:207], v212\n\t" \
    "v_add_f32 v248, v248, v250\n\t" \
    "v_add_f32 v249, v249, v251\n\t" \
    "v_cvt_pk_bf16_f32 v248, v248, v249\n\t" \
    "v_smfmac_f32_16x16x64_bf16 v[208:211], v[240:243], v[176:183], v212\n\t" \
    "ds_write_b16 v213, v248 offset:1024\n\t" \
    "ds_write_b16_d16_hi v213, v248 offset:1040\n\t" \
    "s_nop 5\n\t" \
    "v_add_f32 v208, v208, v210\n\t" \
    "v_add_f32 v209, v209, v211\n\t" \
    "v_cvt_pk_bf16_f32 v208, v208, v209\n\t" \
    "ds_write_b16 v213, v208 offset:0\n\t" \
    "ds_write_b16_d16_hi v213, v208 offset:16\n\t" \
    "s_setprio 0\n\t" \
    "s_waitcnt lgkmcnt(0)\n\t" \
    "s_barrier \n\t" \
    LST4 \
    "ds_read_b128 v[224:227], v253 offset:16384\n\t" \
    "ds_read_b128 v[244:247], v254 offset:55296\n\t" \
    "ds_read_b128 v[228:231], v253 offset:17408\n\t" \
    "ds_read_b128 v[232:235], v253 offset:36864\n\t" \
    "ds_read_b128 v[236:239], v253 offset:37888\n\t" \
    "ds_read_b128 v[248:251], v254 offset:56320\n\t" \
    "s_add_u32 s40, s40, 1\n\t" \
    "s_cmp_lg_u32 s40, 64\n\t" \
    "s_cbranch_scc1 .Lv37_%=\n\t" \
    "s_branch .Lend%=\n\t" \
    ".Lv2_%=:\n\t" \
    "s_mov_b32 s42, %[xs]\n\t" \
    "s_add_u32 m0, %[wm], 0xc000\n\t" \
    "s_mov_b32 s44, 0x0\n\t" \
    "buffer_load_dwordx4 v240, %[wr], s44 offen offset:0 nt lds\n\t" \
    "buffer_load_dwordx4 v241, %[wr], s44 offen offset:1024 nt lds\n\t" \
    "buffer_load_dwordx4 v242, %[wr], s44 offen offset:2048 nt lds\n\t" \
    "s_add_u32 m0, %[wm], 0xf000\n\t" \
    "s_mov_b32 s44, 0x60000\n\t" \
    "buffer_load_dwordx4 v240, %[wr], s44 offen offset:0 nt lds\n\t" \
    "buffer_load_dwordx4 v241, %[wr], s44 offen offset:1024 nt lds\n\t" \
    "buffer_load_dwordx4 v242, %[wr], s44 offen offset:2048 nt lds\n\t" \
    "s_add_u32 m0, %[wm], 0x0\n\t" \
    "s_mov_b32 s44, 0xc0000\n\t" \
    "buffer_load_dwordx4 v240, %[wr], s44 offen offset:0 nt lds\n\t" \
    "buffer_load_dwordx4 v241, %[wr], s44 offen offset:1024 nt lds\n\t" \
    "buffer_load_dwordx4 v242, %[wr], s44 offen offset:2048 nt lds\n\t" \
    "s_add_u32 m0, %[wm], 0x3000\n\t" \
    "s_mov_b32 s44, 0x120000\n\t" \
    "buffer_load_dwordx4 v240, %[wr], s44 offen offset:0 nt lds\n\t" \
    "buffer_load_dwordx4 v241, %[wr], s44 offen offset:1024 nt lds\n\t" \
    "buffer_load_dwordx4 v242, %[wr], s44 offen offset:2048 nt lds\n\t" \
    "s_add_u32 m0, %[wm], 0x6000\n\t" \
    "s_mov_b32 s44, 0x180000\n\t" \
    "buffer_load_dwordx4 v240, %[wr], s44 offen offset:0 nt lds\n\t" \
    "buffer_load_dwordx4 v241, %[wr], s44 offen offset:1024 nt lds\n\t" \
    "buffer_load_dwordx4 v242, %[wr], s44 offen offset:2048 nt lds\n\t" \
    "v_add_u32 v244, 0xc000, v243\n\t" \
    "s_waitcnt vmcnt(12) lgkmcnt(0)\n\t" \
    "s_barrier \n\t" \
    "s_add_u32 m0, %[wm], 0x9000\n\t" \
    "s_mov_b32 s44, 0x1e0000\n\t" \
    "buffer_load_dwordx4 v240, %[wr], s44 offen offset:0 nt lds\n\t" \
    "buffer_load_dwordx4 v241, %[wr], s44 offen offset:1024 nt lds\n\t" \
    "buffer_load_dwordx4 v242, %[wr], s44 offen offset:2048 nt lds\n\t" \
    "ds_read_b64_tr_b16 v[208:209], v244 offset:0\n\t" \
    "ds_read_b64_tr_b16 v[210:211], v244 offset:1536\n\t" \
    "ds_read_b64_tr_b16 v[40:41], v244 offset:32\n\t" \
    "ds_read_b64_tr_b16 v[42:43], v244 offset:1568\n\t" \
    "ds_read_b64_tr_b16 v[48:49], v244 offset:64\n\t" \
    "ds_read_b64_tr_b16 v[50:51], v244 offset:1600\n\t" \
    "v_add_u32 v244, 0xf000, v243\n\t" \
    "s_waitcnt vmcnt(12) lgkmcnt(0)\n\t" \
    "s_barrier \n\t" \
    "s_add_u32 m0, %[wm], 0xc000\n\t" \
    "s_mov_b32 s44, 0x240000\n\t" \
    "buffer_load_dwordx4 v240, %[wr], s44 offen offset:0 nt lds\n\t" \
    "buffer_load_dwordx4 v241, %[wr], s44 offen offset:1024 nt lds\n\t" \
    "buffer_load_dwordx4 v242, %[wr], s44 offen offset:2048 nt lds\n\t" \
    "ds_read_b64_tr_b16 v[212:213], v244 offset:0\n\t" \
    "ds_read_b64_tr_b16 v[214:215], v244 offset:1536\n\t" \
    "ds_read_b64_tr_b16 v[44:45], v244 offset:32\n\t" \
    "ds_read_b64_tr_b16 v[46:47], v244 offset:1568\n\t" \
    "ds_read_b64_tr_b16 v[52:53], v244 offset:64\n\t" \
    "ds_read_b64_tr_b16 v[54:55], v244 offset:1600\n\t" \
    "v_add_u32 v244, 0x0, v243\n\t" \
    "s_waitcnt vmcnt(12) lgkmcnt(0)\n\t" \
    "s_barrier \n\t" \
    "s_add_u32 m0, %[wm], 0xf000\n\t" \
    "s_mov_b32 s44, 0x2a0000\n\t" \
    "buffer_load_dwordx4 v240, %[wr], s44 offen offset:0 nt lds\n\t" \
    "buffer_load_dwordx4 v241, %[wr], s44 offen offset:1024 nt lds\n\t" \
    "buffer_load_dwordx4 v242, %[wr], s44 offen offset:2048 nt lds\n\t" \
    "ds_read_b64_tr_b16 v[56:57], v244 offset:0\n\t" \
    "ds_read_b64_tr_b16 v[58:59], v244 offset:1536\n\t" \
    "ds_read_b64_tr_b16 v[64:65], v244 offset:32\n\t" \
    "ds_read_b64_tr_b16 v[66:67], v244 offset:1568\n\t" \
    "ds_read_b64_tr_b16 v[72:73], v244 offset:64\n\t" \
    "ds_read_b64_tr_b16 v[74:75], v244 offset:1600\n\t" \
    "v_add_u32 v244, 0x3000, v243\n\t" \
    "s_waitcnt vmcnt(12) lgkmcnt(0)\n\t" \
    "s_barrier \n\t" \
    "s_add_u32 m0, %[wm], 0x0\n\t" \
    "s_mov_b32 s44, 0x300000\n\t" \
    "buffer_load_dwordx4 v240, %[wr], s44 offen offset:0 nt lds\n\t" \
    "buffer_load_dwordx4 v241, %[wr], s44 offen offset:1024 nt lds\n\t" \
    "buffer_load_dwordx4 v242, %[wr], s44 offen offset:2048 nt lds\n\t" \
    "ds_read_b64_tr_b16 v[60:61], v244 offset:0\n\t" \
    "ds_read_b64_tr_b16 v[62:63], v244 offset:1536\n\t" \
    "ds_read_b64_tr_b16 v[68:69], v244 offset:32\n\t" \
    "ds_read_b64_tr_b16 v[70:71], v244 offset:1568\n\t" \
    "ds_read_b64_tr_b16 v[76:77], v244 offset:64\n\t" \
    "ds_read_b64_tr_b16 v[78:79], v244 offset:1600\n\t" \
    "v_add_u32 v244, 0x6000, v243\n\t" \
    "s_waitcnt vmcnt(12) lgkmcnt(0)\n\t" \
    "s_barrier \n\t" \
    "s_add_u32 m0, %[wm], 0x3000\n\t" \
    "s_mov_b32 s44, 0x360000\n\t" \
    "buffer_load_dwordx4 v240, %[wr], s44 offen offset:0 nt lds\n\t" \
    "buffer_load_dwordx4 v241, %[wr], s44 offen offset:1024 nt lds\n\t" \
    "buffer_load_dwordx4 v242, %[wr], s44 offen offset:2048 nt lds\n\t" \
    "ds_read_b64_tr_b16 v[216:217], v244 offset:0\n\t" \
    "ds_read_b64_tr_b16 v[218:219], v244 offset:1536\n\t" \
    "ds_read_b64_tr_b16 v[80:81], v244 offset:32\n\t" \
    "ds_read_b64_tr_b16 v[82:83], v244 offset:1568\n\t" \
    "ds_read_b64_tr_b16 v[88:89], v244 offset:64\n\t" \
    "ds_read_b64_tr_b16 v[90:91], v244 offset:1600\n\t" \
    "v_add_u32 v244, 0x9000, v243\n\t" \
    "s_waitcnt vmcnt(12) lgkmcnt(0)\n\t" \
    "s_barrier \n\t" \
    "s_add_u32 m0, %[wm], 0x6000\n\t" \
    "s_mov_b32 s44, 0x3c0000\n\t" \
    "buffer_load_dwordx4 v240, %[wr], s44 offen offset:0 nt lds\n\t" \
    "buffer_load_dwordx4 v241, %[wr], s44 offen offset:1024 nt lds\n\t" \
    "buffer_load_dwordx4 v242, %[wr], s44 offen offset:2048 nt lds\n\t" \
    "ds_read_b64_tr_b16 v[220:221], v244 offset:0\n\t" \
    "ds_read_b64_tr_b16 v[222:223], v244 offset:1536\n\t" \
    "ds_read_b64_tr_b16 v[84:85], v244 offset:32\n\t" \
    "ds_read_b64_tr_b16 v[86:87], v244 offset:1568\n\t" \
    "ds_read_b64_tr_b16 v[92:93], v244 offset:64\n\t" \
    "ds_read_b64_tr_b16 v[94:95], v244 offset:1600\n\t" \
    "v_add_u32 v244, 0xc000, v243\n\t" \
    "s_waitcnt vmcnt(12) lgkmcnt(0)\n\t" \
    "s_barrier \n\t" \
    "v_mov_b32 v246, 0\n\t" \
    "buffer_load_dword v246, v246, s[60:63], %[mo] offen sc1\n\t" \
    "s_add_u32 m0, %[wm], 0x9000\n\t" \
    "s_mov_b32 s44, 0x420000\n\t" \
    "buffer_load_dwordx4 v240, %[wr], s44 offen offset:0 nt lds\n\t" \
    "buffer_load_dwordx4 v241, %[wr], s44 offen offset:1024 nt lds\n\t" \
    "buffer_load_dwordx4 v242, %[wr], s44 offen offset:2048 nt lds\n\t" \
    "ds_read_b64_tr_b16 v[96:97], v244 offset:0\n\t" \
    "ds_read_b64_tr_b16 v[98:99], v244 offset:1536\n\t" \
    "ds_read_b64_tr_b16 v[104:105], v244 offset:32\n\t" \
    "ds_read_b64_tr_b16 v[106:107], v244 offset:1568\n\t" \
    "ds_read_b64_tr_b16 v[112:113], v244 offset:64\n\t" \
    "ds_read_b64_tr_b16 v[114:115], v244 offset:1600\n\t" \
    "v_add_u32 v244, 0xf000, v243\n\t" \
    "s_waitcnt vmcnt(12) lgkmcnt(0)\n\t" \
    "s_barrier \n\t" \
    "s_add_u32 m0, %[wm], 0xc000\n\t" \
    "s_mov_b32 s44, 0x480000\n\t" \
    "buffer_load_dwordx4 v240, %[wr], s44 offen offset:0 nt lds\n\t" \
    "buffer_load_dwordx4 v241, %[wr], s44 offen offset:1024 nt lds\n\t" \
    "buffer_load_dwordx4 v242, %[wr], s44 offen offset:2048 nt lds\n\t" \
    "ds_read_b64_tr_b16 v[100:101], v244 offset:0\n\t" \
    "ds_read_b64_tr_b16 v[102:103], v244 offset:1536\n\t" \
    "ds_read_b64_tr_b16 v[108:109], v244 offset:32\n\t" \
    "ds_read_b64_tr_b16 v[110:111], v244 offset:1568\n\t" \
    "ds_read_b64_tr_b16 v[116:117], v244 offset:64\n\t" \
    "ds_read_b64_tr_b16 v[118:119], v244 offset:1600\n\t" \
    "v_add_u32 v244, 0x0, v243\n\t" \
    "s_waitcnt vmcnt(12) lgkmcnt(0)\n\t" \
    "s_barrier \n\t" \
    "s_add_u32 m0, %[wm], 0xf000\n\t" \
    "s_mov_b32 s44, 0x4e0000\n\t" \
    "buffer_load_dwordx4 v240, %[wr], s44 offen offset:0 nt lds\n\t" \
    "buffer_load_dwordx4 v241, %[wr], s44 offen offset:1024 nt lds\n\t" \
    "buffer_load_dwordx4 v242, %[wr], s44 offen offset:2048 nt lds\n\t" \
    "ds_read_b64_tr_b16 v[200:201], v244 offset:0\n\t" \
    "ds_read_b64_tr_b16 v[202:203], v244 offset:1536\n\t" \
    "ds_read_b64_tr_b16 v[0:1], v244 offset:32\n\t" \
    "ds_read_b64_tr_b16 v[2:3], v244 offset:1568\n\t" \
    "ds_read_b64_tr_b16 v[8:9], v244 offset:64\n\t" \
    "ds_read_b64_tr_b16 v[10:11], v244 offset:1600\n\t" \
    "v_add_u32 v244, 0x3000, v243\n\t" \
    "s_waitcnt vmcnt(12) lgkmcnt(0)\n\t" \
    "s_barrier \n\t" \
    "s_add_u32 m0, %[wm], 0x0\n\t" \
    "s_mov_b32 s44, 0x540000\n\t" \
    "buffer_load_dwordx4 v240, %[wr], s44 offen offset:0 nt lds\n\t" \
    "buffer_load_dwordx4 v241, %[wr], s44 offen offset:1024 nt lds\n\t" \
    "buffer_load_dwordx4 v242, %[wr], s44 offen offset:2048 nt lds\n\t" \
    "ds_read_b64_tr_b16 v[204:205], v244 offset:0\n\t" \
    "ds_read_b64_tr_b16 v[206:207], v244 offset:1536\n\t" \
    "ds_read_b64_tr_b16 v[4:5], v244 offset:32\n\t" \
    "ds_read_b64_tr_b16 v[6:7], v244 offset:1568\n\t" \
    "ds_read_b64_tr_b16 v[12:13], v244 offset:64\n\t" \
    "ds_read_b64_tr_b16 v[14:15], v244 offset:1600\n\t" \
    "v_add_u32 v244, 0x6000, v243\n\t" \
    "s_waitcnt vmcnt(12) lgkmcnt(0)\n\t" \
    "s_barrier \n\t" \
    "s_add_u32 m0, %[wm], 0x3000\n\t" \
    "s_mov_b32 s44, 0x5a0000\n\t" \
    "buffer_load_dwordx4 v240, %[wr], s44 offen offset:0 nt lds\n\t" \
    "buffer_load_dwordx4 v241, %[wr], s44 offen offset:1024 nt lds\n\t" \
    "buffer_load_dwordx4 v242, %[wr], s44 offen offset:2048 nt lds\n\t" \
    "ds_read_b64_tr_b16 v[224:225], v244 offset:0\n\t" \
    "ds_read_b64_tr_b16 v[226:227], v244 offset:1536\n\t" \
    "ds_read_b64_tr_b16 v[120:121], v244 offset:32\n\t" \
    "ds_read_b64_tr_b16 v[122:123], v244 offset:1568\n\t" \
    "ds_read_b64_tr_b16 v[128:129], v244 offset:64\n\t" \
    "ds_read_b64_tr_b16 v[130:131], v244 offset:1600\n\t" \
    "v_add_u32 v244, 0x9000, v243\n\t" \
    "s_waitcnt vmcnt(12) lgkmcnt(0)\n\t" \
    "s_barrier \n\t" \
    ".Lv21_%=:\n\t" \
    "v_readfirstlane_b32 s43, v246\n\t" \
    "s_nop 0\n\t" \
    "s_cmp_lg_u32 s43, -1\n\t" \
    "s_cbranch_scc1 .Lv22_%=\n\t" \
    "s_cmp_eq_u32 s41, 0\n\t" \
    "s_cbranch_scc1 .Lv22_%=\n\t" \
    "s_sub_u32 s41, s41, 1\n\t" \
    "s_sleep 4\n\t" \
    "v_mov_b32 v246, 0\n\t" \
    "buffer_load_dword v246, v246, s[60:63], %[mo] offen sc1\n\t" \
    "s_waitcnt vmcnt(0)\n\t" \
    "s_branch .Lv21_%=\n\t" \
    ".Lv22_%=:\n\t" \
    "v_mov_b32 v246, 0x8000\n\t" \
    "s_mov_b64 exec, 1\n\t" \
    "buffer_store_dword v246, v246, s[60:63], %[mo] offen sc1\n\t" \
    "s_mov_b64 exec, -1\n\t" \
    "s_and_b32 s43, s43, 0x2000\n\t" \
    "s_xor_b32 s43, s43, %[lx]\n\t" \
    "s_add_u32 s60, s60, s43\n\t" \
    "s_addc_u32 s61, s61, 0\n\t" \
    "v_and_b32 v252, 0xf0, v253\n\t" \
    "v_lshlrev_b32 v252, 3, v252\n\t" \
    "buffer_load_dword v252, v252, s[60:63], %[fl] offen sc1\n\t" \
    "s_add_u32 m0, %[wm], 0x6000\n\t" \
    "s_mov_b32 s44, 0x600000\n\t" \
    "buffer_load_dwordx4 v240, %[wr], s44 offen offset:0 nt lds\n\t" \
    "buffer_load_dwordx4 v241, %[wr], s44 offen offset:1024 nt lds\n\t" \
    "buffer_load_dwordx4 v242, %[wr], s44 offen offset:2048 nt lds\n\t" \
    "ds_read_b64_tr_b16 v[228:229], v244 offset:0\n\t" \
    "ds_read_b64_tr_b16 v[230:231], v244 offset:1536\n\t" \
    "ds_read_b64_tr_b16 v[124:125], v244 offset:32\n\t" \
    "ds_read_b64_tr_b16 v[126:127], v244 offset:1568\n\t" \
    "ds_read_b64_tr_b16 v[132:133], v244 offset:64\n\t" \
    "ds_read_b64_tr_b16 v[134:135], v244 offset:1600\n\t" \
    "v_add_u32 v244, 0xc000, v243\n\t" \
    "s_waitcnt vmcnt(12) lgkmcnt(0)\n\t" \
    "s_barrier \n\t" \
    "s_add_u32 m0, %[wm], 0x9000\n\t" \
    "s_mov_b32 s44, 0x660000\n\t" \
    "buffer_load_dwordx4 v240, %[wr], s44 offen offset:0 nt lds\n\t" \
    "buffer_load_dwordx4 v241, %[wr], s44 offen offset:1024 nt lds\n\t" \
    "buffer_load_dwordx4 v242, %[wr], s44 offen offset:2048 nt lds\n\t" \
    "ds_read_b64_tr_b16 v[136:137], v244 offset:0\n\t" \
    "ds_read_b64_tr_b16 v[138:139], v244 offset:1536\n\t" \
    "ds_read_b64_tr_b16 v[144:145], v244 offset:32\n\t" \
    "ds_read_b64_tr_b16 v[146:147], v244 offset:1568\n\t" \
    "ds_read_b64_tr_b16 v[152:153], v244 offset:64\n\t" \
    "ds_read_b64_tr_b16 v[154:155], v244 offset:1600\n\t" \
    "v_add_u32 v244, 0xf000, v243\n\t" \
    "s_waitcnt vmcnt(12) lgkmcnt(0)\n\t" \
    "s_barrier \n\t" \
    "s_add_u32 m0, %[wm], 0xc000\n\t" \
    "s_mov_b32 s44, 0x6c0000\n\t" \
    "buffer_load_dwordx4 v240, %[wr], s44 offen offset:0 nt lds\n\t" \
    "buffer_load_dwordx4 v241, %[wr], s44 offen offset:1024 nt lds\n\t" \
    "buffer_load_dwordx4 v242, %[wr], s44 offen offset:2048 nt lds\n\t" \
    "ds_read_b64_tr_b16 v[140:141], v244 offset:0\n\t" \
    "ds_read_b64_tr_b16 v[142:143], v244 offset:1536\n\t" \
    "ds_read_b64_tr_b16 v[148:149], v244 offset:32\n\t" \
    "ds_read_b64_tr_b16 v[150:151], v244 offset:1568\n\t" \
    "ds_read_b64_tr_b16 v[156:157], v244 offset:64\n\t" \
    "ds_read_b64_tr_b16 v[158:159], v244 offset:1600\n\t" \
    "v_add_u32 v244, 0x0, v243\n\t" \
    "s_waitcnt vmcnt(12) lgkmcnt(0)\n\t" \
    "s_barrier \n\t" \
    "s_add_u32 m0, %[wm], 0xf000\n\t" \
    "s_mov_b32 s44, 0x720000\n\t" \
    "buffer_load_dwordx4 v240, %[wr], s44 offen offset:0 nt lds\n\t" \
    "buffer_load_dwordx4 v241, %[wr], s44 offen offset:1024 nt lds\n\t" \
    "buffer_load_dwordx4 v242, %[wr], s44 offen offset:2048 nt lds\n\t" \
    "ds_read_b64_tr_b16 v[16:17], v244 offset:0\n\t" \
    "ds_read_b64_tr_b16 v[18:19], v244 offset:1536\n\t" \
    "ds_read_b64_tr_b16 v[24:25], v244 offset:32\n\t" \
    "ds_read_b64_tr_b16 v[26:27], v244 offset:1568\n\t" \
    "ds_read_b64_tr_b16 v[32:33], v244 offset:64\n\t" \
    "ds_read_b64_tr_b16 v[34:35], v244 offset:1600\n\t" \
    "v_add_u32 v244, 0x3000, v243\n\t" \
    "s_waitcnt vmcnt(12) lgkmcnt(0)\n\t" \
    "s_barrier \n\t" \
    "ds_read_b64_tr_b16 v[20:21], v244 offset:0\n\t" \
    "ds_read_b64_tr_b16 v[22:23], v244 offset:1536\n\t" \
    "ds_read_b64_tr_b16 v[28:29], v244 offset:32\n\t" \
    "ds_read_b64_tr_b16 v[30:31], v244 offset:1568\n\t" \
    "ds_read_b64_tr_b16 v[36:37], v244 offset:64\n\t" \
    "ds_read_b64_tr_b16 v[38:39], v244 offset:1600\n\t" \
    "v_add_u32 v244, 0x6000, v243\n\t" \
    "s_waitcnt vmcnt(9) lgkmcnt(0)\n\t" \
    "s_barrier \n\t" \
    "ds_read_b64_tr_b16 v[232:233], v244 offset:0\n\t" \
    "ds_read_b64_tr_b16 v[234:235], v244 offset:1536\n\t" \
    "ds_read_b64_tr_b16 v[160:161], v244 offset:32\n\t" \
    "ds_read_b64_tr_b16 v[162:163], v244 offset:1568\n\t" \
    "ds_read_b64_tr_b16 v[168:169], v244 offset:64\n\t" \
    "ds_read_b64_tr_b16 v[170:171], v244 offset:1600\n\t" \
    "v_add_u32 v244, 0x9000, v243\n\t" \
    "s_waitcnt vmcnt(6) lgkmcnt(0)\n\t" \
    "s_barrier \n\t" \
    "ds_read_b64_tr_b16 v[236:237], v244 offset:0\n\t" \
    "ds_read_b64_tr_b16 v[238:239], v244 offset:1536\n\t" \
    "ds_read_b64_tr_b16 v[164:165], v244 offset:32\n\t" \
    "ds_read_b64_tr_b16 v[166:167], v244 offset:1568\n\t" \
    "ds_read_b64_tr_b16 v[172:173], v244 offset:64\n\t" \
    "ds_read_b64_tr_b16 v[174:175], v244 offset:1600\n\t" \
    "v_add_u32 v244, 0xc000, v243\n\t" \
    "s_waitcnt vmcnt(3) lgkmcnt(0)\n\t" \
    "s_barrier \n\t" \
    "ds_read_b64_tr_b16 v[176:177], v244 offset:0\n\t" \
    "ds_read_b64_tr_b16 v[178:179], v244 offset:1536\n\t" \
    "ds_read_b64_tr_b16 v[184:185], v244 offset:32\n\t" \
    "ds_read_b64_tr_b16 v[186:187], v244 offset:1568\n\t" \
    "ds_read_b64_tr_b16 v[192:193], v244 offset:64\n\t" \
    "ds_read_b64_tr_b16 v[194:195], v244 offset:1600\n\t" \
    "s_mov_b32 s43, 0\n\t" \
    "v_cmp_eq_u32 vcc, 0, v252\n\t" \
    "s_cbranch_vccnz .Lv23_%=\n\t" \
    "s_mov_b32 m0, 0x2000\n\t" \
    "s_add_u32 s49, s42, 0x0\n\t" \
    "buffer_load_dwordx4 v253, s[60:63], s49 offen offset:0 sc1 lds\n\t" \
    "buffer_load_dwordx4 v253, s[60:63], s49 offen offset:1024 sc1 lds\n\t" \
    "buffer_load_dwordx4 v253, s[60:63], s49 offen offset:2048 sc1 lds\n\t" \
    "buffer_load_dwordx4 v253, s[60:63], s49 offen offset:3072 sc1 lds\n\t" \
    "s_mov_b32 m0, 0x4800\n\t" \
    "s_add_u32 s49, s42, 0x8800\n\t" \
    "buffer_load_dwordx4 v253, s[60:63], s49 offen offset:0 sc1 lds\n\t" \
    "s_mov_b32 m0, 0x7000\n\t" \
    "s_add_u32 s49, s42, 0x1000\n\t" \
    "buffer_load_dwordx4 v253, s[60:63], s49 offen offset:0 sc1 lds\n\t" \
    "buffer_load_dwordx4 v253, s[60:63], s49 offen offset:1024 sc1 lds\n\t" \
    "buffer_load_dwordx4 v253, s[60:63], s49 offen offset:2048 sc1 lds\n\t" \
    "buffer_load_dwordx4 v253, s[60:63], s49 offen offset:3072 sc1 lds\n\t" \
    "s_mov_b32 m0, 0x9800\n\t" \
    "s_add_u32 s49, s42, 0x9800\n\t" \
    "buffer_load_dwordx4 v253, s[60:63], s49 offen offset:0 sc1 lds\n\t" \
    "s_mov_b32 s43, 1\n\t" \
    ".Lv23_%=:\n\t" \
    "v_add_u32 v244, 0xf000, v243\n\t" \
    "s_waitcnt vmcnt(0) lgkmcnt(0)\n\t" \
    "s_barrier \n\t" \
    "ds_read_b64_tr_b16 v[180:181], v244 offset:0\n\t" \
    "ds_read_b64_tr_b16 v[182:183], v244 offset:1536\n\t" \
    "ds_read_b64_tr_b16 v[188:189], v244 offset:32\n\t" \
    "ds_read_b64_tr_b16 v[190:191], v244 offset:1568\n\t" \
    "ds_read_b64_tr_b16 v[196:197], v244 offset:64\n\t" \
    "ds_read_b64_tr_b16 v[198:199], v244 offset:1600\n\t" \
    "s_waitcnt lgkmcnt(0)\n\t" \
    "s_barrier \n\t" \
    LST0 \
    "ds_write_b128 v254, v[200:203] offset:51200\n\t" \
    "ds_write_b128 v254, v[204:207] offset:52224\n\t" \
    "ds_write_b128 v254, v[208:211] offset:53248\n\t" \
    "ds_write_b128 v254, v[212:215] offset:54272\n\t" \
    "ds_write_b128 v254, v[216:219] offset:55296\n\t" \
    "ds_write_b128 v254, v[220:223] offset:56320\n\t" \
    "ds_write_b128 v254, v[224:227] offset:57344\n\t" \
    "ds_write_b128 v254, v[228:231] offset:58368\n\t" \
    "ds_write_b128 v254, v[232:235] offset:59392\n\t" \
    "ds_write_b128 v254, v[236:239] offset:60416\n\t" \
    "s_cmp_lg_u32 s43, 0\n\t" \
    "s_cbranch_scc1 .Lv24_%=\n\t" \
    "s_mov_b32 s44, 0\n\t" \
    ".Lv25_%=:\n\t" \
    "v_cmp_ge_u32 vcc, s44, v252\n\t" \
    "s_cbranch_vccz .Lv26_%=\n\t" \
    "s_cmp_eq_u32 s41, 0\n\t" \
    "s_cbranch_scc1 .Lv26_%=\n\t" \
    "s_sub_u32 s41, s41, 1\n\t" \
    "s_sleep 8\n\t" \
    "v_and_b32 v252, 0xf0, v253\n\t" \
    "v_lshlrev_b32 v252, 3, v252\n\t" \
    "buffer_load_dword v252, v252, s[60:63], %[fl] offen sc1\n\t" \
    "s_waitcnt vmcnt(0)\n\t" \
    "s_branch .Lv25_%=\n\t" \
    ".Lv26_%=:\n\t" \
    "s_mov_b32 m0, 0x2000\n\t" \
    "s_add_u32 s49, s42, 0x0\n\t" \
    "buffer_load_dwordx4 v253, s[60:63], s49 offen offset:0 sc1 lds\n\t" \
    "buffer_load_dwordx4 v253, s[60:63], s49 offen offset:1024 sc1 lds\n\t" \
    "buffer_load_dwordx4 v253, s[60:63], s49 offen offset:2048 sc1 lds\n\t" \
    "buffer_load_dwordx4 v253, s[60:63], s49 offen offset:3072 sc1 lds\n\t" \
    "s_mov_b32 m0, 0x4800\n\t" \
    "s_add_u32 s49, s42, 0x8800\n\t" \
    "buffer_load_dwordx4 v253, s[60:63], s49 offen offset:0 sc1 lds\n\t" \
    "s_mov_b32 m0, 0x7000\n\t" \
    "s_add_u32 s49, s42, 0x1000\n\t" \
    "buffer_load_dwordx4 v253, s[60:63], s49 offen offset:0 sc1 lds\n\t" \
    "buffer_load_dwordx4 v253, s[60:63], s49 offen offset:1024 sc1 lds\n\t" \
    "buffer_load_dwordx4 v253, s[60:63], s49 offen offset:2048 sc1 lds\n\t" \
    "buffer_load_dwordx4 v253, s[60:63], s49 offen offset:3072 sc1 lds\n\t" \
    "s_mov_b32 m0, 0x9800\n\t" \
    "s_add_u32 s49, s42, 0x9800\n\t" \
    "buffer_load_dwordx4 v253, s[60:63], s49 offen offset:0 sc1 lds\n\t" \
    ".Lv24_%=:\n\t" \
    "s_waitcnt vmcnt(0) lgkmcnt(0)\n\t" \
    "s_barrier \n\t" \
    "ds_read_b128 v[224:227], v253 offset:8192\n\t" \
    "ds_read_b128 v[244:247], v254 offset:51200\n\t" \
    "ds_read_b128 v[228:231], v253 offset:9216\n\t" \
    "ds_read_b128 v[232:235], v253 offset:28672\n\t" \
    "ds_read_b128 v[236:239], v253 offset:29696\n\t" \
    "ds_read_b128 v[248:251], v254 offset:52224\n\t" \
    ".Lv27_%=:\n\t" \
    LST7 \
    "s_waitcnt lgkmcnt(5)\n\t" \
    "v_mfma_f32_16x16x32_bf16 v[204:207], v[0:3], v[224:227], 0\n\t" \
    "s_waitcnt lgkmcnt(3)\n\t" \
    "v_mfma_f32_16x16x32_bf16 v[204:207], v[4:7], v[228:231], v[204:207]\n\t" \
    "ds_read_b128 v[240:243], v253 offset:14336\n\t" \
    "v_mfma_f32_16x16x32_bf16 v[208:211], v[8:11], v[224:227], 0\n\t" \
    "v_mfma_f32_16x16x32_bf16 v[208:211], v[12:15], v[228:231], v[208:211]\n\t" \
    "v_mfma_f32_16x16x32_bf16 v[200:203], v[244:247], v[224:227], 0\n\t" \
    "s_waitcnt lgkmcnt(1)\n\t" \
    "v_mfma_f32_16x16x32_bf16 v[200:203], v[248:251], v[228:231], v[200:203]\n\t" \
    "v_mfma_f32_16x16x32_bf16 v[216:219], v[0:3], v[232:235], 0\n\t" \
    "ds_read_b128 v[224:227], v253 offset:15360\n\t" \
    "v_mfma_f32_16x16x32_bf16 v[216:219], v[4:7], v[236:239], v[216:219]\n\t" \
    "ds_read_b128 v[228:231], v253 offset:0\n\t" \
    "v_mfma_f32_16x16x32_bf16 v[220:223], v[8:11], v[232:235], 0\n\t" \
    "v_mfma_f32_16x16x32_bf16 v[220:223], v[12:15], v[236:239], v[220:223]\n\t" \
    "v_mfma_f32_16x16x32_bf16 v[212:215], v[244:247], v[232:235], 0\n\t" \
    "ds_read_b128 v[244:247], v254 offset:53248\n\t" \
    "v_mfma_f32_16x16x32_bf16 v[212:215], v[248:251], v[236:239], v[212:215]\n\t" \
    "ds_read_b128 v[248:251], v254 offset:54272\n\t" \
    "s_waitcnt lgkmcnt(4)\n\t" \
    "v_mfma_f32_16x16x32_bf16 v[204:207], v[24:27], v[240:243], v[204:207]\n\t" \
    "ds_read_b128 v[232:235], v253 offset:1024\n\t" \
    "s_waitcnt lgkmcnt(4)\n\t" \
    "v_mfma_f32_16x16x32_bf16 v[204:207], v[28:31], v[224:227], v[204:207]\n\t" \
    "ds_read_b128 v[236:239], v253 offset:20480\n\t" \
    "v_mfma_f32_16x16x32_bf16 v[208:211], v[32:35], v[240:243], v[208:211]\n\t" \
    "v_mfma_f32_16x16x32_bf16 v[208:211], v[36:39], v[224:227], v[208:211]\n\t" \
    "v_mfma_f32_16x16x32_bf16 v[200:203], v[16:19], v[240:243], v[200:203]\n\t" \
    "v_mfma_f32_16x16x32_bf16 v[200:203], v[20:23], v[224:227], v[200:203]\n\t" \
    "s_waitcnt lgkmcnt(4)\n\t" \
    "v_mfma_f32_16x16x32_bf16 v[204:207], v[40:43], v[228:231], v[204:207]\n\t" \
    "ds_read_b128 v[240:243], v253 offset:21504\n\t" \
    "s_waitcnt lgkmcnt(2)\n\t" \
    "v_mfma_f32_16x16x32_bf16 v[204:207], v[44:47], v[232:235], v[204:207]\n\t" \
    "ds_read_b128 v[224:227], v253 offset:2048\n\t" \
    "v_mfma_f32_16x16x32_bf16 v[208:211], v[48:51], v[228:231], v[208:211]\n\t" \
    "v_mfma_f32_16x16x32_bf16 v[208:211], v[52:55], v[232:235], v[208:211]\n\t" \
    "v_mfma_f32_16x16x32_bf16 v[200:203], v[244:247], v[228:231], v[200:203]\n\t" \
    "v_mfma_f32_16x16x32_bf16 v[200:203], v[248:251], v[232:235], v[200:203]\n\t" \
    "s_waitcnt lgkmcnt(2)\n\t" \
    "v_mfma_f32_16x16x32_bf16 v[216:219], v[40:43], v[236:239], v[216:219]\n\t" \
    "ds_read_b128 v[228:231], v253 offset:3072\n\t" \
    "s_add_u32 s44, s40, 1\n\t" \
    "s_waitcnt lgkmcnt(2)\n\t" \
    "v_mfma_f32_16x16x32_bf16 v[216:219], v[44:47], v[240:243], v[216:219]\n\t" \
    "ds_read_b128 v[232:235], v253 offset:4096\n\t" \
    "s_and_b32 s45, s44, 15\n\t" \
    "v_mfma_f32_16x16x32_bf16 v[220:223], v[48:51], v[236:239], v[220:223]\n\t" \
    "s_cmp_eq_u32 s45, 0\n\t" \
    "v_mfma_f32_16x16x32_bf16 v[220:223], v[52:55], v[240:243], v[220:223]\n\t" \
    "s_cselect_b32 s45, %[cj], %[cs]\n\t" \
    "v_mfma_f32_16x16x32_bf16 v[212:215], v[244:247], v[236:239], v[212:215]\n\t" \
    "ds_read_b128 v[244:247], v254 offset:55296\n\t" \
    "s_cmp_eq_u32 s44, 64\n\t" \
    "v_mfma_f32_16x16x32_bf16 v[212:215], v[248:251], v[240:243], v[212:215]\n\t" \
    "ds_read_b128 v[248:251], v254 offset:56320\n\t" \
    "s_cselect_b32 s45, 0, s45\n\t" \
    "s_waitcnt lgkmcnt(4)\n\t" \
    "v_mfma_f32_16x16x32_bf16 v[204:207], v[64:67], v[224:227], v[204:207]\n\t" \
    "ds_read_b128 v[236:239], v253 offset:5120\n\t" \
    "s_add_u32 s42, s42, s45\n\t" \
    "s_waitcnt lgkmcnt(4)\n\t" \
    "v_mfma_f32_16x16x32_bf16 v[204:207], v[68:71], v[228:231], v[204:207]\n\t" \
    "ds_read_b128 v[240:243], v253 offset:24576\n\t" \
    "v_mfma_f32_16x16x32_bf16 v[208:211], v[72:75], v[224:227], v[208:211]\n\t" \
    "v_mfma_f32_16x16x32_bf16 v[208:211], v[76:79], v[228:231], v[208:211]\n\t" \
    "v_mfma_f32_16x16x32_bf16 v[200:203], v[56:59], v[224:227], v[200:203]\n\t" \
    "v_mfma_f32_16x16x32_bf16 v[200:203], v[60:63], v[228:231], v[200:203]\n\t" \
    "s_waitcnt lgkmcnt(4)\n\t" \
    "v_mfma_f32_16x16x32_bf16 v[204:207], v[80:83], v[232:235], v[204:207]\n\t" \
    "ds_read_b128 v[224:227], v253 offset:25600\n\t" \
    "s_waitcnt lgkmcnt(2)\n\t" \
    "v_mfma_f32_16x16x32_bf16 v[204:207], v[84:87], v[236:239], v[204:207]\n\t" \
    "ds_read_b128 v[228:231], v253 offset:6144\n\t" \
    "v_mfma_f32_16x16x32_bf16 v[208:211], v[88:91], v[232:235], v[208:211]\n\t" \
    "v_mfma_f32_16x16x32_bf16 v[208:211], v[92:95], v[236:239], v[208:211]\n\t" \
    "v_mfma_f32_16x16x32_bf16 v[200:203], v[244:247], v[232:235], v[200:203]\n\t" \
    "v_mfma_f32_16x16x32_bf16 v[200:203], v[248:251], v[236:239], v[200:203]\n\t" \
    "s_waitcnt lgkmcnt(2)\n\t" \
    "v_mfma_f32_16x16x32_bf16 v[216:219], v[80:83], v[240:243], v[216:219]\n\t" \
    "ds_read_b128 v[232:235], v253 offset:7168\n\t" \
    "s_and_b32 s44, s40, 3\n\t" \
    "s_waitcnt lgkmcnt(2)\n\t" \
    "v_mfma_f32_16x16x32_bf16 v[216:219], v[84:87], v[224:227], v[216:219]\n\t" \
    "ds_read_b128 v[236:239], v253 offset:10240\n\t" \
    "s_lshl_b32 s44, s44, 17\n\t" \
    "v_mfma_f32_16x16x32_bf16 v[220:223], v[88:91], v[240:243], v[220:223]\n\t" \
    "s_add_u32 s44, s44, %[mb]\n\t" \
    "v_mfma_f32_16x16x32_bf16 v[220:223], v[92:95], v[224:227], v[220:223]\n\t" \
    "s_add_u32 s46, s44, 0x1000\n\t" \
    "v_mfma_f32_16x16x32_bf16 v[212:215], v[244:247], v[240:243], v[212:215]\n\t" \
    "ds_read_b128 v[244:247], v254 offset:57344\n\t" \
    "s_add_u32 s47, s44, 0x1c00\n\t" \
    "v_mfma_f32_16x16x32_bf16 v[212:215], v[248:251], v[224:227], v[212:215]\n\t" \
    "ds_read_b128 v[248:251], v254 offset:58368\n\t" \
    "s_waitcnt lgkmcnt(4)\n\t" \
    "v_mfma_f32_16x16x32_bf16 v[204:207], v[104:107], v[228:231], v[204:207]\n\t" \
    "ds_read_b128 v[240:243], v253 offset:11264\n\t" \
    "s_waitcnt lgkmcnt(4)\n\t" \
    "v_mfma_f32_16x16x32_bf16 v[204:207], v[108:111], v[232:235], v[204:207]\n\t" \
    "ds_read_b128 v[224:227], v253 offset:30720\n\t" \
    "v_mfma_f32_16x16x32_bf16 v[208:211], v[112:115], v[228:231], v[208:211]\n\t" \
    "v_mfma_f32_16x16x32_bf16 v[208:211], v[116:119], v[232:235], v[208:211]\n\t" \
    "v_mfma_f32_16x16x32_bf16 v[200:203], v[96:99], v[228:231], v[200:203]\n\t" \
    "v_mfma_f32_16x16x32_bf16 v[200:203], v[100:103], v[232:235], v[200:203]\n\t" \
    "s_waitcnt lgkmcnt(4)\n\t" \
    "v_mfma_f32_16x16x32_bf16 v[204:207], v[120:123], v[236:239], v[204:207]\n\t" \
    "ds_read_b128 v[228:231], v253 offset:31744\n\t" \
    "s_waitcnt lgkmcnt(2)\n\t" \
    "v_mfma_f32_16x16x32_bf16 v[204:207], v[124:127], v[240:243], v[204:207]\n\t" \
    "ds_read_b128 v[232:235], v253 offset:12288\n\t" \
    "v_mfma_f32_16x16x32_bf16 v[208:211], v[128:131], v[236:239], v[208:211]\n\t" \
    "v_mfma_f32_16x16x32_bf16 v[208:211], v[132:135], v[240:243], v[208:211]\n\t" \
    "v_mfma_f32_16x16x32_bf16 v[200:203], v[244:247], v[236:239], v[200:203]\n\t" \
    "v_mfma_f32_16x16x32_bf16 v[200:203], v[248:251], v[240:243], v[200:203]\n\t" \
    "s_waitcnt lgkmcnt(2)\n\t" \
    "v_mfma_f32_16x16x32_bf16 v[216:219], v[120:123], v[224:227], v[216:219]\n\t" \
    "ds_read_b128 v[236:239], v253 offset:13312\n\t" \
    "s_waitcnt lgkmcnt(2)\n\t" \
    "v_mfma_f32_16x16x32_bf16 v[216:219], v[124:127], v[228:231], v[216:219]\n\t" \
    "ds_read_b128 v[240:243], v253 offset:16384\n\t" \
    "v_mfma_f32_16x16x32_bf16 v[220:223], v[128:131], v[224:227], v[220:223]\n\t" \
    "v_mfma_f32_16x16x32_bf16 v[220:223], v[132:135], v[228:231], v[220:223]\n\t" \
    "v_mfma_f32_16x16x32_bf16 v[212:215], v[244:247], v[224:227], v[212:215]\n\t" \
    "ds_read_b128 v[244:247], v254 offset:59392\n\t" \
    "v_mfma_f32_16x16x32_bf16 v[212:215], v[248:251], v[228:231], v[212:215]\n\t" \
    "ds_read_b128 v[248:251], v254 offset:60416\n\t" \
    "v_and_b32 v252, 0xf0, v253\n\t" \
    "v_lshlrev_b32 v252, 3, v252\n\t" \
    "buffer_load_dword v252, v252, s[60:63], %[fl] offen sc1\n\t" \
    "s_waitcnt lgkmcnt(4)\n\t" \
    "v_mfma_f32_16x16x32_bf16 v[204:207], v[144:147], v[232:235], v[204:207]\n\t" \
    "ds_read_b128 v[224:227], v253 offset:17408\n\t" \
    "s_waitcnt lgkmcnt(4)\n\t" \
    "v_mfma_f32_16x16x32_bf16 v[204:207], v[148:151], v[236:239], v[204:207]\n\t" \
    "ds_read_b128 v[228:231], v253 offset:36864\n\t" \
    "v_mfma_f32_16x16x32_bf16 v[208:211], v[152:155], v[232:235], v[208:211]\n\t" \
    "v_mfma_f32_16x16x32_bf16 v[208:211], v[156:159], v[236:239], v[208:211]\n\t" \
    "v_mfma_f32_16x16x32_bf16 v[200:203], v[136:139], v[232:235], v[200:203]\n\t" \
    "v_mfma_f32_16x16x32_bf16 v[200:203], v[140:143], v[236:239], v[200:203]\n\t" \
    "s_waitcnt lgkmcnt(4)\n\t" \
    "v_mfma_f32_16x16x32_bf16 v[204:207], v[160:163], v[240:243], v[204:207]\n\t" \
    "ds_read_b128 v[232:235], v253 offset:37888\n\t" \
    "s_waitcnt lgkmcnt(2)\n\t" \
    "v_mfma_f32_16x16x32_bf16 v[204:207], v[164:167], v[224:227], v[204:207]\n\t" \
    "ds_read_b128 v[236:239], v253 offset:18432\n\t" \
    "v_mfma_f32_16x16x32_bf16 v[208:211], v[168:171], v[240:243], v[208:211]\n\t" \
    "v_mfma_f32_16x16x32_bf16 v[208:211], v[172:175], v[224:227], v[208:211]\n\t" \
    "v_mfma_f32_16x16x32_bf16 v[200:203], v[244:247], v[240:243], v[200:203]\n\t" \
    "v_mfma_f32_16x16x32_bf16 v[200:203], v[248:251], v[224:227], v[200:203]\n\t" \
    "s_waitcnt lgkmcnt(2)\n\t" \
    "v_mfma_f32_16x16x32_bf16 v[216:219], v[160:163], v[228:231], v[216:219]\n\t" \
    "ds_read_b128 v[240:243], v253 offset:19456\n\t" \
    "s_waitcnt lgkmcnt(2)\n\t" \
    "v_mfma_f32_16x16x32_bf16 v[216:219], v[164:167], v[232:235], v[216:219]\n\t" \
    "ds_read_b128 v[224:227], v253 offset:34816\n\t" \
    "v_mfma_f32_16x16x32_bf16 v[220:223], v[168:171], v[228:231], v[220:223]\n\t" \
    "v_mfma_f32_16x16x32_bf16 v[220:223], v[172:175], v[232:235], v[220:223]\n\t" \
    "v_mfma_f32_16x16x32_bf16 v[212:215], v[244:247], v[228:231], v[212:215]\n\t" \
    "v_mfma_f32_16x16x32_bf16 v[212:215], v[248:251], v[232:235], v[212:215]\n\t" \
    "s_waitcnt lgkmcnt(2)\n\t" \
    "v_mfma_f32_16x16x32_bf16 v[204:207], v[184:187], v[236:239], v[204:207]\n\t" \
    "ds_read_b128 v[228:231], v253 offset:35840\n\t" \
    "s_waitcnt lgkmcnt(2)\n\t" \
    "v_mfma_f32_16x16x32_bf16 v[204:207], v[188:191], v[240:243], v[204:207]\n\t" \
    "ds_read_b128 v[232:235], v253 offset:22528\n\t" \
    "v_mfma_f32_16x16x32_bf16 v[208:211], v[192:195], v[236:239], v[208:211]\n\t" \
    "v_mfma_f32_16x16x32_bf16 v[208:211], v[196:199], v[240:243], v[208:211]\n\t" \
    "v_mfma_f32_16x16x32_bf16 v[200:203], v[176:179], v[236:239], v[200:203]\n\t" \
    "v_mfma_f32_16x16x32_bf16 v[200:203], v[180:183], v[240:243], v[200:203]\n\t" \
    "s_waitcnt lgkmcnt(2)\n\t" \
    "v_mfma_f32_16x16x32_bf16 v[216:219], v[24:27], v[224:227], v[216:219]\n\t" \
    "ds_read_b128 v[236:239], v253 offset:23552\n\t" \
    "s_waitcnt lgkmcnt(2)\n\t" \
    "v_mfma_f32_16x16x32_bf16 v[216:219], v[28:31], v[228:231], v[216:219]\n\t" \
    "ds_read_b128 v[240:243], v253 offset:26624\n\t" \
    "v_mfma_f32_16x16x32_bf16 v[220:223], v[32:35], v[224:227], v[220:223]\n\t" \
    "v_cvt_pk_bf16_f32 v204, v204, v205\n\t" \
    "v_mfma_f32_16x16x32_bf16 v[220:223], v[36:39], v[228:231], v[220:223]\n\t" \
    "v_cvt_pk_bf16_f32 v205, v206, v207\n\t" \
    "v_mfma_f32_16x16x32_bf16 v[212:215], v[16:19], v[224:227], v[212:215]\n\t" \
    "ds_write_b64 v254, v[204:205] offset:7176\n\t" \
    "v_mfma_f32_16x16x32_bf16 v[212:215], v[20:23], v[228:231], v[212:215]\n\t" \
    "v_cvt_pk_bf16_f32 v208, v208, v209\n\t" \
    "s_waitcnt lgkmcnt(3)\n\t" \
    "v_mfma_f32_16x16x32_bf16 v[216:219], v[64:67], v[232:235], v[216:219]\n\t" \
    "ds_read_b128 v[224:227], v253 offset:27648\n\t" \
    "v_cvt_pk_bf16_f32 v209, v210, v211\n\t" \
    "s_waitcnt lgkmcnt(3)\n\t" \
    "v_mfma_f32_16x16x32_bf16 v[216:219], v[68:71], v[236:239], v[216:219]\n\t" \
    "ds_read_b128 v[228:231], v253 offset:32768\n\t" \
    "ds_write_b64 v254, v[208:209] offset:8200\n\t" \
    "v_mfma_f32_16x16x32_bf16 v[220:223], v[72:75], v[232:235], v[220:223]\n\t" \
    "v_cvt_pk_bf16_f32 v200, v200, v201\n\t" \
    "v_mfma_f32_16x16x32_bf16 v[220:223], v[76:79], v[236:239], v[220:223]\n\t" \
    "v_cvt_pk_bf16_f32 v201, v202, v203\n\t" \
    "v_mfma_f32_16x16x32_bf16 v[212:215], v[56:59], v[232:235], v[212:215]\n\t" \
    "ds_write_b64 v254, v[200:201] offset:6152\n\t" \
    "v_mfma_f32_16x16x32_bf16 v[212:215], v[60:63], v[236:239], v[212:215]\n\t" \
    "s_waitcnt lgkmcnt(5)\n\t" \
    "v_mfma_f32_16x16x32_bf16 v[216:219], v[104:107], v[240:243], v[216:219]\n\t" \
    "ds_read_b128 v[232:235], v253 offset:33792\n\t" \
    "s_waitcnt lgkmcnt(4)\n\t" \
    "v_mfma_f32_16x16x32_bf16 v[216:219], v[108:111], v[224:227], v[216:219]\n\t" \
    "ds_read_b128 v[236:239], v253 offset:38912\n\t" \
    "v_mfma_f32_16x16x32_bf16 v[220:223], v[112:115], v[240:243], v[220:223]\n\t" \
    "v_mfma_f32_16x16x32_bf16 v[220:223], v[116:119], v[224:227], v[220:223]\n\t" \
    "v_mfma_f32_16x16x32_bf16 v[212:215], v[96:99], v[240:243], v[212:215]\n\t" \
    "v_mfma_f32_16x16x32_bf16 v[212:215], v[100:103], v[224:227], v[212:215]\n\t" \
    "ds_read_b128 v[240:243], v253 offset:39936\n\t" \
    "s_waitcnt lgkmcnt(3)\n\t" \
    "s_barrier \n\t" \
    "v_mfma_f32_16x16x32_bf16 v[216:219], v[144:147], v[228:231], v[216:219]\n\t" \
    "s_waitcnt lgkmcnt(2)\n\t" \
    "v_mfma_f32_16x16x32_bf16 v[216:219], v[148:151], v[232:235], v[216:219]\n\t" \
    "s_waitcnt lgkmcnt(1)\n\t" \
    "v_mfma_f32_16x16x32_bf16 v[216:219], v[184:187], v[236:239], v[216:219]\n\t" \
    "s_waitcnt lgkmcnt(0)\n\t" \
    "v_mfma_f32_16x16x32_bf16 v[216:219], v[188:191], v[240:243], v[216:219]\n\t" \
    "v_mfma_f32_16x16x32_bf16 v[220:223], v[152:155], v[228:231], v[220:223]\n\t" \
    "v_mfma_f32_16x16x32_bf16 v[220:223], v[156:159], v[232:235], v[220:223]\n\t" \
    "v_mfma_f32_16x16x32_bf16 v[220:223], v[192:195], v[236:239], v[220:223]\n\t" \
    "v_cvt_pk_bf16_f32 v216, v216, v217\n\t" \
    "v_cvt_pk_bf16_f32 v217, v218, v219\n\t" \
    "v_mfma_f32_16x16x32_bf16 v[220:223], v[196:199], v[240:243], v[220:223]\n\t" \
    "ds_write_b64 v254, v[216:217] offset:13320\n\t" \
    "v_mfma_f32_16x16x32_bf16 v[212:215], v[136:139], v[228:231], v[212:215]\n\t" \
    "v_mfma_f32_16x16x32_bf16 v[212:215], v[140:143], v[232:235], v[212:215]\n\t" \
    "v_mfma_f32_16x16x32_bf16 v[212:215], v[176:179], v[236:239], v[212:215]\n\t" \
    "v_cvt_pk_bf16_f32 v220, v220, v221\n\t" \
    "v_cvt_pk_bf16_f32 v221, v222, v223\n\t" \
    "v_mfma_f32_16x16x32_bf16 v[212:215], v[180:183], v[240:243], v[212:215]\n\t" \
    "ds_write_b64 v254, v[220:221] offset:14344\n\t" \
    "s_nop 6\n\t" \
    "v_cvt_pk_bf16_f32 v212, v212, v213\n\t" \
    "v_cvt_pk_bf16_f32 v213, v214, v215\n\t" \
    "ds_write_b64 v254, v[212:213] offset:12296\n\t" \
    LST6 \
    "s_waitcnt vmcnt(0) lgkmcnt(0)\n\t" \
    "s_barrier \n\t" \
    "s_setprio 2\n\t" \
    "s_add_u32 s44, s40, 1\n\t" \
    "s_min_u32 s44, s44, 63\n\t" \
    ".Lv28_%=:\n\t" \
    "v_cmp_ge_u32 vcc, s44, v252\n\t" \
    "s_cbranch_vccz .Lv29_%=\n\t" \
    "s_cmp_eq_u32 s41, 0\n\t" \
    "s_cbranch_scc1 .Lv29_%=\n\t" \
    "s_sub_u32 s41, s41, 1\n\t" \
    "s_sleep 2\n\t" \
    "v_and_b32 v252, 0xf0, v253\n\t" \
    "v_lshlrev_b32 v252, 3, v252\n\t" \
    "buffer_load_dword v252, v252, s[60:63], %[fl] offen sc1\n\t" \
    "s_waitcnt vmcnt(0)\n\t" \
    "s_branch .Lv28_%=\n\t" \
    ".Lv29_%=:\n\t" \
    "s_mov_b32 m0, 0x2000\n\t" \
    "s_add_u32 s49, s42, 0x0\n\t" \
    "buffer_load_dwordx4 v253, s[60:63], s49 offen offset:0 sc1 lds\n\t" \
    "s_sleep 1\n\t" \
    "buffer_load_dwordx4 v253, s[60:63], s49 offen offset:1024 sc1 lds\n\t" \
    "s_sleep 1\n\t" \
    "buffer_load_dwordx4 v253, s[60:63], s49 offen offset:2048 sc1 lds\n\t" \
    "s_sleep 1\n\t" \
    "buffer_load_dwordx4 v253, s[60:63], s49 offen offset:3072 sc1 lds\n\t" \
    "s_sleep 1\n\t" \
    "s_mov_b32 m0, 0x4800\n\t" \
    "s_add_u32 s49, s42, 0x8800\n\t" \
    "buffer_load_dwordx4 v253, s[60:63], s49 offen offset:0 sc1 lds\n\t" \
    "s_sleep 1\n\t" \
    "s_mov_b32 m0, 0x7000\n\t" \
    "s_add_u32 s49, s42, 0x1000\n\t" \
    "buffer_load_dwordx4 v253, s[60:63], s49 offen offset:0 sc1 lds\n\t" \
    "s_sleep 1\n\t" \
    "buffer_load_dwordx4 v253, s[60:63], s49 offen offset:1024 sc1 lds\n\t" \
    "s_sleep 1\n\t" \
    "buffer_load_dwordx4 v253, s[60:63], s49 offen offset:2048 sc1 lds\n\t" \
    "s_sleep 1\n\t" \
    "buffer_load_dwordx4 v253, s[60:63], s49 offen offset:3072 sc1 lds\n\t" \
    "s_sleep 1\n\t" \
    "s_mov_b32 m0, 0x9800\n\t" \
    "s_add_u32 s49, s42, 0x9800\n\t" \
    "buffer_load_dwordx4 v253, s[60:63], s49 offen offset:0 sc1 lds\n\t" \
    "s_sleep 1\n\t" \
    "ds_read_b128 v[200:203], v254 offset:51200\n\t" \
    "ds_read_b128 v[204:207], v254 offset:52224\n\t" \
    "ds_read_b128 v[216:219], v254 offset:53248\n\t" \
    "ds_read_b128 v[220:223], v254 offset:54272\n\t" \
    "v_mov_b64 v[208:209], 0\n\t" \
    "v_mov_b64 v[210:211], 0\n\t" \
    "v_mov_b64 v[244:245], 0\n\t" \
    "v_mov_b64 v[246:247], 0\n\t" \
    "v_mov_b64 v[248:249], 0\n\t" \
    "v_mov_b64 v[250:251], 0\n\t" \
    LST1 \
    "s_sleep 5\n\t" \
    "s_mov_b32 m0, 0xb000\n\t" \
    "s_nop 0\n\t" \
    "buffer_load_dwordx4 v253, s[60:63], s46 offen sc1 lds\n\t" \
    "s_mov_b32 m0, 0xbc00\n\t" \
    "s_nop 0\n\t" \
    "buffer_load_dwordx4 v253, s[60:63], s47 offen sc1 lds\n\t" \
    "s_waitcnt lgkmcnt(0)\n\t" \
    LFLUSH212 \
    "v_bfe_i32 v212, v253, 5, 1\n\t" \
    "v_and_b32 v212, 0xaaaa, v212\n\t" \
    "v_or_b32 v212, 0x4444, v212\n\t" \
    "v_and_b32 v213, 0xffff, v255\n\t" \
    "v_lshrrev_b32 v214, 16, v255\n\t" \
    ".Lv210_%=:\n\t" \
    "s_waitcnt vmcnt(0)\n\t" \
    "ds_read_b128 v[224:227], v253 offset:45056\n\t" \
    "ds_read_b128 v[228:231], v253 offset:48128\n\t" \
    "s_waitcnt lgkmcnt(1)\n\t" \
    "v_max_u32 v215, v224, v225\n\t" \
    "v_max3_u32 v215, v226, v227, v215\n\t" \
    "s_waitcnt lgkmcnt(0)\n\t" \
    "v_max3_u32 v215, v228, v229, v215\n\t" \
    "v_max3_u32 v215, v230, v231, v215\n\t" \
    "v_cmp_eq_u32 vcc, -1, v215\n\t" \
    "s_cbranch_vccz .Lv211_%=\n\t" \
    "v_max_u32 v215, v224, v225\n\t" \
    "v_max3_u32 v215, v226, v227, v215\n\t" \
    "v_cmp_eq_u32 vcc, -1, v215\n\t" \
    "s_cbranch_vccz .Lv212_%=\n\t" \
    "s_mov_b32 m0, 0xb000\n\t" \
    "s_nop 0\n\t" \
    "buffer_load_dwordx4 v253, s[60:63], s46 offen sc1 lds\n\t" \
    ".Lv212_%=:\n\t" \
    "v_max_u32 v215, v228, v229\n\t" \
    "v_max3_u32 v215, v230, v231, v215\n\t" \
    "v_cmp_eq_u32 vcc, -1, v215\n\t" \
    "s_cbranch_vccz .Lv213_%=\n\t" \
    "s_mov_b32 m0, 0xbc00\n\t" \
    "s_nop 0\n\t" \
    "buffer_load_dwordx4 v253, s[60:63], s47 offen sc1 lds\n\t" \
    ".Lv213_%=:\n\t" \
    "s_cmp_eq_u32 s41, 0\n\t" \
    "s_cbranch_scc1 .Lv211_%=\n\t" \
    "s_sub_u32 s41, s41, 1\n\t" \
    "s_branch .Lv210_%=\n\t" \
    ".Lv211_%=:\n\t" \
    LST2 \
    "s_waitcnt lgkmcnt(0)\n\t" \
    "s_barrier \n\t" \
    LST3 \
    "ds_read_b128 v[232:235], v253 offset:40960\n\t" \
    "ds_read_b128 v[236:239], v253 offset:41984\n\t" \
    "ds_read_b128 v[240:243], v253 offset:43008\n\t" \
    "v_smfmac_f32_16x16x64_bf16 v[244:247], v[224:227], v[0:7], v212\n\t" \
    "v_smfmac_f32_16x16x64_bf16 v[248:251], v[224:227], v[8:15], v212\n\t" \
    "v_smfmac_f32_16x16x64_bf16 v[208:211], v[224:227], v[200:207], v212\n\t" \
    "ds_read_b128 v[200:203], v254 offset:55296\n\t" \
    "ds_read_b128 v[204:207], v254 offset:56320\n\t" \
    "ds_read_b128 v[224:227], v253 offset:44032\n\t" \
    "v_smfmac_f32_16x16x64_bf16 v[208:211], v[228:231], v[16:23], v212\n\t" \
    "v_smfmac_f32_16x16x64_bf16 v[244:247], v[228:231], v[24:31], v212\n\t" \
    "v_smfmac_f32_16x16x64_bf16 v[248:251], v[228:231], v[32:39], v212\n\t" \
    "ds_read_b128 v[228:231], v253 offset:46080\n\t" \
    "s_waitcnt lgkmcnt(6)\n\t" \
    "v_smfmac_f32_16x16x64_bf16 v[208:211], v[232:235], v[216:223], v212\n\t" \
    "ds_read_b128 v[216:219], v254 offset:57344\n\t" \
    "ds_read_b128 v[220:223], v254 offset:58368\n\t" \
    "v_smfmac_f32_16x16x64_bf16 v[244:247], v[232:235], v[40:47], v212\n\t" \
    "v_smfmac_f32_16x16x64_bf16 v[248:251], v[232:235], v[48:55], v212\n\t" \
    "ds_read_b128 v[232:235], v253 offset:47104\n\t" \
    "s_waitcnt lgkmcnt(8)\n\t" \
    "v_smfmac_f32_16x16x64_bf16 v[208:211], v[236:239], v[56:63], v212\n\t" \
    "v_smfmac_f32_16x16x64_bf16 v[244:247], v[236:239], v[64:71], v212\n\t" \
    "v_smfmac_f32_16x16x64_bf16 v[248:251], v[236:239], v[72:79], v212\n\t" \
    "ds_read_b128 v[236:239], v253 offset:49152\n\t" \
    "s_waitcnt lgkmcnt(6)\n\t" \
    "v_smfmac_f32_16x16x64_bf16 v[208:211], v[240:243], v[200:207], v212\n\t" \
    "ds_read_b128 v[200:203], v254 offset:59392\n\t" \
    "ds_read_b128 v[204:207], v254 offset:60416\n\t" \
    "v_smfmac_f32_16x16x64_bf16 v[244:247], v[240:243], v[80:87], v212\n\t" \
    "v_smfmac_f32_16x16x64_bf16 v[248:251], v[240:243], v[88:95], v212\n\t" \
    "ds_read_b128 v[240:243], v253 offset:50176\n\t" \
    "s_waitcnt lgkmcnt(8)\n\t" \
    "v_smfmac_f32_16x16x64_bf16 v[208:211], v[224:227], v[96:103], v212\n\t" \
    "v_smfmac_f32_16x16x64_bf16 v[244:247], v[224:227], v[104:111], v212\n\t" \
    "v_smfmac_f32_16x16x64_bf16 v[248:251], v[224:227], v[112:119], v212\n\t" \
    "s_waitcnt lgkmcnt(7)\n\t" \
    "v_smfmac_f32_16x16x64_bf16 v[244:247], v[228:231], v[120:127], v212\n\t" \
    "s_waitcnt lgkmcnt(4)\n\t" \
    "v_smfmac_f32_16x16x64_bf16 v[244:247], v[232:235], v[144:151], v212\n\t" \
    "s_waitcnt lgkmcnt(3)\n\t" \
    "v_smfmac_f32_16x16x64_bf16 v[244:247], v[236:239], v[160:167], v212\n\t" \
    "s_waitcnt lgkmcnt(0)\n\t" \
    "v_smfmac_f32_16x16x64_bf16 v[244:247], v[240:243], v[184:191], v212\n\t" \
    "v_smfmac_f32_16x16x64_bf16 v[248:251], v[228:231], v[128:135], v212\n\t" \
    "v_smfmac_f32_16x16x64_bf16 v[248:251], v[232:235], v[152:159], v212\n\t" \
    "v_smfmac_f32_16x16x64_bf16 v[248:251], v[236:239], v[168:175], v212\n\t" \
    "v_add_f32 v244, v244, v246\n\t" \
    "v_add_f32 v245, v245, v247\n\t" \
    "v_cvt_pk_bf16_f32 v244, v244, v245\n\t" \
    "v_smfmac_f32_16x16x64_bf16 v[248:251], v[240:243], v[192:199], v212\n\t" \
    "ds_write_b16 v213, v244 offset:512\n\t" \
    "ds_write_b16_d16_hi v213, v244 offset:528\n\t" \
    "v_smfmac_f32_16x16x64_bf16 v[208:211], v[228:231], v[216:223], v212\n\t" \
    "v_smfmac_f32_16x16x64_bf16 v[208:211], v[232:235], v[136:143], v212\n\t" \
    "v_smfmac_f32_16x16x64_bf16 v[208:211], v[236:239], v[200:207], v212\n\t" \
    "v_add_f32 v248, v248, v250\n\t" \
    "v_add_f32 v249, v249, v251\n\t" \
    "v_cvt_pk_bf16_f32 v248, v248, v249\n\t" \
    "v_smfmac_f32_16x16x64_bf16 v[208:211], v[240:243], v[176:183], v212\n\t" \
    "ds_write_b16 v213, v248 offset:1024\n\t" \
    "ds_write_b16_d16_hi v213, v248 offset:1040\n\t" \
    "s_nop 5\n\t" \
    "v_add_f32 v208, v208, v210\n\t" \
    "v_add_f32 v209, v209, v211\n\t" \
    "v_cvt_pk_bf16_f32 v208, v208, v209\n\t" \
    "ds_write_b16 v213, v208 offset:0\n\t" \
    "ds_write_b16_d16_hi v213, v208 offset:16\n\t" \
    "s_setprio 0\n\t" \
    "s_waitcnt lgkmcnt(0)\n\t" \
    "s_barrier \n\t" \
    LST4 \
    "ds_read_b128 v[224:227], v253 offset:8192\n\t" \
    "ds_read_b128 v[244:247], v254 offset:51200\n\t" \
    "ds_read_b128 v[228:231], v253 offset:9216\n\t" \
    "ds_read_b128 v[232:235], v253 offset:28672\n\t" \
    "ds_read_b128 v[236:239], v253 offset:29696\n\t" \
    "ds_read_b128 v[248:251], v254 offset:52224\n\t" \
    "s_add_u32 s40, s40, 1\n\t" \
    "s_cmp_lg_u32 s40, 64\n\t" \
    "s_cbranch_scc1 .Lv27_%=\n\t" \
    "s_branch .Lend%=\n\t" \
    ".Lv1_%=:\n\t" \
    "s_mov_b32 s42, %[xs]\n\t" \
    "s_add_u32 m0, %[wm], 0xc000\n\t" \
    "s_mov_b32 s44, 0x0\n\t" \
    "buffer_load_dwordx4 v240, %[wr], s44 offen offset:0 nt lds\n\t" \
    "buffer_load_dwordx4 v241, %[wr], s44 offen offset:1024 nt lds\n\t" \
    "buffer_load_dwordx4 v242, %[wr], s44 offen offset:2048 nt lds\n\t" \
    "s_add_u32 m0, %[wm], 0xf000\n\t" \
    "s_mov_b32 s44, 0x60000\n\t" \
    "buffer_load_dwordx4 v240, %[wr], s44 offen offset:0 nt lds\n\t" \
    "buffer_load_dwordx4 v241, %[wr], s44 offen offset:1024 nt lds\n\t" \
    "buffer_load_dwordx4 v242, %[wr], s44 offen offset:2048 nt lds\n\t" \
    "s_add_u32 m0, %[wm], 0x0\n\t" \
    "s_mov_b32 s44, 0xc0000\n\t" \
    "buffer_load_dwordx4 v240, %[wr], s44 offen offset:0 nt lds\n\t" \
    "buffer_load_dwordx4 v241, %[wr], s44 offen offset:1024 nt lds\n\t" \
    "buffer_load_dwordx4 v242, %[wr], s44 offen offset:2048 nt lds\n\t" \
    "s_add_u32 m0, %[wm], 0x3000\n\t" \
    "s_mov_b32 s44, 0x120000\n\t" \
    "buffer_load_dwordx4 v240, %[wr], s44 offen offset:0 nt lds\n\t" \
    "buffer_load_dwordx4 v241, %[wr], s44 offen offset:1024 nt lds\n\t" \
    "buffer_load_dwordx4 v242, %[wr], s44 offen offset:2048 nt lds\n\t" \
    "s_add_u32 m0, %[wm], 0x6000\n\t" \
    "s_mov_b32 s44, 0x180000\n\t" \
    "buffer_load_dwordx4 v240, %[wr], s44 offen offset:0 nt lds\n\t" \
    "buffer_load_dwordx4 v241, %[wr], s44 offen offset:1024 nt lds\n\t" \
    "buffer_load_dwordx4 v242, %[wr], s44 offen offset:2048 nt lds\n\t" \
    "v_add_u32 v244, 0xc000, v243\n\t" \
    "s_waitcnt vmcnt(12) lgkmcnt(0)\n\t" \
    "s_barrier \n\t" \
    "s_add_u32 m0, %[wm], 0x9000\n\t" \
    "s_mov_b32 s44, 0x1e0000\n\t" \
    "buffer_load_dwordx4 v240, %[wr], s44 offen offset:0 nt lds\n\t" \
    "buffer_load_dwordx4 v241, %[wr], s44 offen offset:1024 nt lds\n\t" \
    "buffer_load_dwordx4 v242, %[wr], s44 offen offset:2048 nt lds\n\t" \
    "ds_read_b64_tr_b16 v[56:57], v244 offset:0\n\t" \
    "ds_read_b64_tr_b16 v[58:59], v244 offset:1536\n\t" \
    "ds_read_b64_tr_b16 v[64:65], v244 offset:32\n\t" \
    "ds_read_b64_tr_b16 v[66:67], v244 offset:1568\n\t" \
    "ds_read_b64_tr_b16 v[72:73], v244 offset:64\n\t" \
    "ds_read_b64_tr_b16 v[74:75], v244 offset:1600\n\t" \
    "v_add_u32 v244, 0xf000, v243\n\t" \
    "s_waitcnt vmcnt(12) lgkmcnt(0)\n\t" \
    "s_barrier \n\t" \
    "s_add_u32 m0, %[wm], 0xc000\n\t" \
    "s_mov_b32 s44, 0x240000\n\t" \
    "buffer_load_dwordx4 v240, %[wr], s44 offen offset:0 nt lds\n\t" \
    "buffer_load_dwordx4 v241, %[wr], s44 offen offset:1024 nt lds\n\t" \
    "buffer_load_dwordx4 v242, %[wr], s44 offen offset:2048 nt lds\n\t" \
    "ds_read_b64_tr_b16 v[60:61], v244 offset:0\n\t" \
    "ds_read_b64_tr_b16 v[62:63], v244 offset:1536\n\t" \
    "ds_read_b64_tr_b16 v[68:69], v244 offset:32\n\t" \
    "ds_read_b64_tr_b16 v[70:71], v244 offset:1568\n\t" \
    "ds_read_b64_tr_b16 v[76:77], v244 offset:64\n\t" \
    "ds_read_b64_tr_b16 v[78:79], v244 offset:1600\n\t" \
    "v_add_u32 v244, 0x0, v243\n\t" \
    "s_waitcnt vmcnt(12) lgkmcnt(0)\n\t" \
    "s_barrier \n\t" \
    "s_add_u32 m0, %[wm], 0xf000\n\t" \
    "s_mov_b32 s44, 0x2a0000\n\t" \
    "buffer_load_dwordx4 v240, %[wr], s44 offen offset:0 nt lds\n\t" \
    "buffer_load_dwordx4 v241, %[wr], s44 offen offset:1024 nt lds\n\t" \
    "buffer_load_dwordx4 v242, %[wr], s44 offen offset:2048 nt lds\n\t" \
    "ds_read_b64_tr_b16 v[96:97], v244 offset:0\n\t" \
    "ds_read_b64_tr_b16 v[98:99], v244 offset:1536\n\t" \
    "ds_read_b64_tr_b16 v[104:105], v244 offset:32\n\t" \
    "ds_read_b64_tr_b16 v[106:107], v244 offset:1568\n\t" \
    "ds_read_b64_tr_b16 v[112:113], v244 offset:64\n\t" \
    "ds_read_b64_tr_b16 v[114:115], v244 offset:1600\n\t" \
    "v_add_u32 v244, 0x3000, v243\n\t" \
    "s_waitcnt vmcnt(12) lgkmcnt(0)\n\t" \
    "s_barrier \n\t" \
    "s_add_u32 m0, %[wm], 0x0\n\t" \
    "s_mov_b32 s44, 0x300000\n\t" \
    "buffer_load_dwordx4 v240, %[wr], s44 offen offset:0 nt lds\n\t" \
    "buffer_load_dwordx4 v241, %[wr], s44 offen offset:1024 nt lds\n\t" \
    "buffer_load_dwordx4 v242, %[wr], s44 offen offset:2048 nt lds\n\t" \
    "ds_read_b64_tr_b16 v[100:101], v244 offset:0\n\t" \
    "ds_read_b64_tr_b16 v[102:103], v244 offset:1536\n\t" \
    "ds_read_b64_tr_b16 v[108:109], v244 offset:32\n\t" \
    "ds_read_b64_tr_b16 v[110:111], v244 offset:1568\n\t" \
    "ds_read_b64_tr_b16 v[116:117], v244 offset:64\n\t" \
    "ds_read_b64_tr_b16 v[118:119], v244 offset:1600\n\t" \
    "v_add_u32 v244, 0x6000, v243\n\t" \
    "s_waitcnt vmcnt(12) lgkmcnt(0)\n\t" \
    "s_barrier \n\t" \
    "s_add_u32 m0, %[wm], 0x3000\n\t" \
    "s_mov_b32 s44, 0x360000\n\t" \
    "buffer_load_dwordx4 v240, %[wr], s44 offen offset:0 nt lds\n\t" \
    "buffer_load_dwordx4 v241, %[wr], s44 offen offset:1024 nt lds\n\t" \
    "buffer_load_dwordx4 v242, %[wr], s44 offen offset:2048 nt lds\n\t" \
    "ds_read_b64_tr_b16 v[200:201], v244 offset:0\n\t" \
    "ds_read_b64_tr_b16 v[202:203], v244 offset:1536\n\t" \
    "ds_read_b64_tr_b16 v[0:1], v244 offset:32\n\t" \
    "ds_read_b64_tr_b16 v[2:3], v244 offset:1568\n\t" \
    "ds_read_b64_tr_b16 v[8:9], v244 offset:64\n\t" \
    "ds_read_b64_tr_b16 v[10:11], v244 offset:1600\n\t" \
    "v_add_u32 v244, 0x9000, v243\n\t" \
    "s_waitcnt vmcnt(12) lgkmcnt(0)\n\t" \
    "s_barrier \n\t" \
    "s_add_u32 m0, %[wm], 0x6000\n\t" \
    "s_mov_b32 s44, 0x3c0000\n\t" \
    "buffer_load_dwordx4 v240, %[wr], s44 offen offset:0 nt lds\n\t" \
    "buffer_load_dwordx4 v241, %[wr], s44 offen offset:1024 nt lds\n\t" \
    "buffer_load_dwordx4 v242, %[wr], s44 offen offset:2048 nt lds\n\t" \
    "ds_read_b64_tr_b16 v[204:205], v244 offset:0\n\t" \
    "ds_read_b64_tr_b16 v[206:207], v244 offset:1536\n\t" \
    "ds_read_b64_tr_b16 v[4:5], v244 offset:32\n\t" \
    "ds_read_b64_tr_b16 v[6:7], v244 offset:1568\n\t" \
    "ds_read_b64_tr_b16 v[12:13], v244 offset:64\n\t" \
    "ds_read_b64_tr_b16 v[14:15], v244 offset:1600\n\t" \
    "v_add_u32 v244, 0xc000, v243\n\t" \
    "s_waitcnt vmcnt(12) lgkmcnt(0)\n\t" \
    "s_barrier \n\t" \
    "v_mov_b32 v246, 0\n\t" \
    "buffer_load_dword v246, v246, s[60:63], %[mo] offen sc1\n\t" \
    "s_add_u32 m0, %[wm], 0x9000\n\t" \
    "s_mov_b32 s44, 0x420000\n\t" \
    "buffer_load_dwordx4 v240, %[wr], s44 offen offset:0 nt lds\n\t" \
    "buffer_load_dwordx4 v241, %[wr], s44 offen offset:1024 nt lds\n\t" \
    "buffer_load_dwordx4 v242, %[wr], s44 offen offset:2048 nt lds\n\t" \
    "ds_read_b64_tr_b16 v[208:209], v244 offset:0\n\t" \
    "ds_read_b64_tr_b16 v[210:211], v244 offset:1536\n\t" \
    "ds_read_b64_tr_b16 v[40:41], v244 offset:32\n\t" \
    "ds_read_b64_tr_b16 v[42:43], v244 offset:1568\n\t" \
    "ds_read_b64_tr_b16 v[48:49], v244 offset:64\n\t" \
    "ds_read_b64_tr_b16 v[50:51], v244 offset:1600\n\t" \
    "v_add_u32 v244, 0xf000, v243\n\t" \
    "s_waitcnt vmcnt(12) lgkmcnt(0)\n\t" \
    "s_barrier \n\t" \
    "s_add_u32 m0, %[wm], 0xc000\n\t" \
    "s_mov_b32 s44, 0x480000\n\t" \
    "buffer_load_dwordx4 v240, %[wr], s44 offen offset:0 nt lds\n\t" \
    "buffer_load_dwordx4 v241, %[wr], s44 offen offset:1024 nt lds\n\t" \
    "buffer_load_dwordx4 v242, %[wr], s44 offen offset:2048 nt lds\n\t" \
    "ds_read_b64_tr_b16 v[212:213], v244 offset:0\n\t" \
    "ds_read_b64_tr_b16 v[214:215], v244 offset:1536\n\t" \
    "ds_read_b64_tr_b16 v[44:45], v244 offset:32\n\t" \
    "ds_read_b64_tr_b16 v[46:47], v244 offset:1568\n\t" \
    "ds_read_b64_tr_b16 v[52:53], v244 offset:64\n\t" \
    "ds_read_b64_tr_b16 v[54:55], v244 offset:1600\n\t" \
    "v_add_u32 v244, 0x0, v243\n\t" \
    "s_waitcnt vmcnt(12) lgkmcnt(0)\n\t" \
    "s_barrier \n\t" \
    "s_add_u32 m0, %[wm], 0xf000\n\t" \
    "s_mov_b32 s44, 0x4e0000\n\t" \
    "buffer_load_dwordx4 v240, %[wr], s44 offen offset:0 nt lds\n\t" \
    "buffer_load_dwordx4 v241, %[wr], s44 offen offset:1024 nt lds\n\t" \
    "buffer_load_dwordx4 v242, %[wr], s44 offen offset:2048 nt lds\n\t" \
    "ds_read_b64_tr_b16 v[136:137], v244 offset:0\n\t" \
    "ds_read_b64_tr_b16 v[138:139], v244 offset:1536\n\t" \
    "ds_read_b64_tr_b16 v[144:145], v244 offset:32\n\t" \
    "ds_read_b64_tr_b16 v[146:147], v244 offset:1568\n\t" \
    "ds_read_b64_tr_b16 v[152:153], v244 offset:64\n\t" \
    "ds_read_b64_tr_b16 v[154:155], v244 offset:1600\n\t" \
    "v_add_u32 v244, 0x3000, v243\n\t" \
    "s_waitcnt vmcnt(12) lgkmcnt(0)\n\t" \
    "s_barrier \n\t" \
    "s_add_u32 m0, %[wm], 0x0\n\t" \
    "s_mov_b32 s44, 0x540000\n\t" \
    "buffer_load_dwordx4 v240, %[wr], s44 offen offset:0 nt lds\n\t" \
    "buffer_load_dwordx4 v241, %[wr], s44 offen offset:1024 nt lds\n\t" \
    "buffer_load_dwordx4 v242, %[wr], s44 offen offset:2048 nt lds\n\t" \
    "ds_read_b64_tr_b16 v[140:141], v244 offset:0\n\t" \
    "ds_read_b64_tr_b16 v[142:143], v244 offset:1536\n\t" \
    "ds_read_b64_tr_b16 v[148:149], v244 offset:32\n\t" \
    "ds_read_b64_tr_b16 v[150:151], v244 offset:1568\n\t" \
    "ds_read_b64_tr_b16 v[156:157], v244 offset:64\n\t" \
    "ds_read_b64_tr_b16 v[158:159], v244 offset:1600\n\t" \
    "v_add_u32 v244, 0x6000, v243\n\t" \
    "s_waitcnt vmcnt(12) lgkmcnt(0)\n\t" \
    "s_barrier \n\t" \
    "s_add_u32 m0, %[wm], 0x3000\n\t" \
    "s_mov_b32 s44, 0x5a0000\n\t" \
    "buffer_load_dwordx4 v240, %[wr], s44 offen offset:0 nt lds\n\t" \
    "buffer_load_dwordx4 v241, %[wr], s44 offen offset:1024 nt lds\n\t" \
    "buffer_load_dwordx4 v242, %[wr], s44 offen offset:2048 nt lds\n\t" \
    "ds_read_b64_tr_b16 v[216:217], v244 offset:0\n\t" \
    "ds_read_b64_tr_b16 v[218:219], v244 offset:1536\n\t" \
    "ds_read_b64_tr_b16 v[80:81], v244 offset:32\n\t" \
    "ds_read_b64_tr_b16 v[82:83], v244 offset:1568\n\t" \
    "ds_read_b64_tr_b16 v[88:89], v244 offset:64\n\t" \
    "ds_read_b64_tr_b16 v[90:91], v244 offset:1600\n\t" \
    "v_add_u32 v244, 0x9000, v243\n\t" \
    "s_waitcnt vmcnt(12) lgkmcnt(0)\n\t" \
    "s_barrier \n\t" \
    ".Lv11_%=:\n\t" \
    "v_readfirstlane_b32 s43, v246\n\t" \
    "s_nop 0\n\t" \
    "s_cmp_lg_u32 s43, -1\n\t" \
    "s_cbranch_scc1 .Lv12_%=\n\t" \
    "s_cmp_eq_u32 s41, 0\n\t" \
    "s_cbranch_scc1 .Lv12_%=\n\t" \
    "s_sub_u32 s41, s41, 1\n\t" \
    "s_sleep 4\n\t" \
    "v_mov_b32 v246, 0\n\t" \
    "buffer_load_dword v246, v246, s[60:63], %[mo] offen sc1\n\t" \
    "s_waitcnt vmcnt(0)\n\t" \
    "s_branch .Lv11_%=\n\t" \
    ".Lv12_%=:\n\t" \
    "v_mov_b32 v246, 0x8000\n\t" \
    "s_mov_b64 exec, 1\n\t" \
    "buffer_store_dword v246, v246, s[60:63], %[mo] offen sc1\n\t" \
    "s_mov_b64 exec, -1\n\t" \
    "s_and_b32 s43, s43, 0x2000\n\t" \
    "s_xor_b32 s43, s43, %[lx]\n\t" \
    "s_add_u32 s60, s60, s43\n\t" \
    "s_addc_u32 s61, s61, 0\n\t" \
    "v_and_b32 v252, 0xf0, v253\n\t" \
    "v_lshlrev_b32 v252, 3, v252\n\t" \
    "buffer_load_dword v252, v252, s[60:63], %[fl] offen sc1\n\t" \
    "s_add_u32 m0, %[wm], 0x6000\n\t" \
    "s_mov_b32 s44, 0x600000\n\t" \
    "buffer_load_dwordx4 v240, %[wr], s44 offen offset:0 nt lds\n\t" \
    "buffer_load_dwordx4 v241, %[wr], s44 offen offset:1024 nt lds\n\t" \
    "buffer_load_dwordx4 v242, %[wr], s44 offen offset:2048 nt lds\n\t" \
    "ds_read_b64_tr_b16 v[220:221], v244 offset:0\n\t" \
    "ds_read_b64_tr_b16 v[222:223], v244 offset:1536\n\t" \
    "ds_read_b64_tr_b16 v[84:85], v244 offset:32\n\t" \
    "ds_read_b64_tr_b16 v[86:87], v244 offset:1568\n\t" \
    "ds_read_b64_tr_b16 v[92:93], v244 offset:64\n\t" \
    "ds_read_b64_tr_b16 v[94:95], v244 offset:1600\n\t" \
    "v_add_u32 v244, 0xc000, v243\n\t" \
    "s_waitcnt vmcnt(12) lgkmcnt(0)\n\t" \
    "s_barrier \n\t" \
    "s_add_u32 m0, %[wm], 0x9000\n\t" \
    "s_mov_b32 s44, 0x660000\n\t" \
    "buffer_load_dwordx4 v240, %[wr], s44 offen offset:0 nt lds\n\t" \
    "buffer_load_dwordx4 v241, %[wr], s44 offen offset:1024 nt lds\n\t" \
    "buffer_load_dwordx4 v242, %[wr], s44 offen offset:2048 nt lds\n\t" \
    "ds_read_b64_tr_b16 v[16:17], v244 offset:0\n\t" \
    "ds_read_b64_tr_b16 v[18:19], v244 offset:1536\n\t" \
    "ds_read_b64_tr_b16 v[24:25], v244 offset:32\n\t" \
    "ds_read_b64_tr_b16 v[26:27], v244 offset:1568\n\t" \
    "ds_read_b64_tr_b16 v[32:33], v244 offset:64\n\t" \
    "ds_read_b64_tr_b16 v[34:35], v244 offset:1600\n\t" \
    "v_add_u32 v244, 0xf000, v243\n\t" \
    "s_waitcnt vmcnt(12) lgkmcnt(0)\n\t" \
    "s_barrier \n\t" \
    "s_add_u32 m0, %[wm], 0xc000\n\t" \
    "s_mov_b32 s44, 0x6c0000\n\t" \
    "buffer_load_dwordx4 v240, %[wr], s44 offen offset:0 nt lds\n\t" \
    "buffer_load_dwordx4 v241, %[wr], s44 offen offset:1024 nt lds\n\t" \
    "buffer_load_dwordx4 v242, %[wr], s44 offen offset:2048 nt lds\n\t" \
    "ds_read_b64_tr_b16 v[20:21], v244 offset:0\n\t" \
    "ds_read_b64_tr_b16 v[22:23], v244 offset:1536\n\t" \
    "ds_read_b64_tr_b16 v[28:29], v244 offset:32\n\t" \
    "ds_read_b64_tr_b16 v[30:31], v244 offset:1568\n\t" \
    "ds_read_b64_tr_b16 v[36:37], v244 offset:64\n\t" \
    "ds_read_b64_tr_b16 v[38:39], v244 offset:1600\n\t" \
    "v_add_u32 v244, 0x0, v243\n\t" \
    "s_waitcnt vmcnt(12) lgkmcnt(0)\n\t" \
    "s_barrier \n\t" \
    "s_add_u32 m0, %[wm], 0xf000\n\t" \
    "s_mov_b32 s44, 0x720000\n\t" \
    "buffer_load_dwordx4 v240, %[wr], s44 offen offset:0 nt lds\n\t" \
    "buffer_load_dwordx4 v241, %[wr], s44 offen offset:1024 nt lds\n\t" \
    "buffer_load_dwordx4 v242, %[wr], s44 offen offset:2048 nt lds\n\t" \
    "ds_read_b64_tr_b16 v[224:225], v244 offset:0\n\t" \
    "ds_read_b64_tr_b16 v[226:227], v244 offset:1536\n\t" \
    "ds_read_b64_tr_b16 v[120:121], v244 offset:32\n\t" \
    "ds_read_b64_tr_b16 v[122:123], v244 offset:1568\n\t" \
    "ds_read_b64_tr_b16 v[128:129], v244 offset:64\n\t" \
    "ds_read_b64_tr_b16 v[130:131], v244 offset:1600\n\t" \
    "v_add_u32 v244, 0x3000, v243\n\t" \
    "s_waitcnt vmcnt(12) lgkmcnt(0)\n\t" \
    "s_barrier \n\t" \
    "ds_read_b64_tr_b16 v[228:229], v244 offset:0\n\t" \
    "ds_read_b64_tr_b16 v[230:231], v244 offset:1536\n\t" \
    "ds_read_b64_tr_b16 v[124:125], v244 offset:32\n\t" \
    "ds_read_b64_tr_b16 v[126:127], v244 offset:1568\n\t" \
    "ds_read_b64_tr_b16 v[132:133], v244 offset:64\n\t" \
    "ds_read_b64_tr_b16 v[134:135], v244 offset:1600\n\t" \
    "v_add_u32 v244, 0x6000, v243\n\t" \
    "s_waitcnt vmcnt(9) lgkmcnt(0)\n\t" \
    "s_barrier \n\t" \
    "ds_read_b64_tr_b16 v[232:233], v244 offset:0\n\t" \
    "ds_read_b64_tr_b16 v[234:235], v244 offset:1536\n\t" \
    "ds_read_b64_tr_b16 v[160:161], v244 offset:32\n\t" \
    "ds_read_b64_tr_b16 v[162:163], v244 offset:1568\n\t" \
    "ds_read_b64_tr_b16 v[168:169], v244 offset:64\n\t" \
    "ds_read_b64_tr_b16 v[170:171], v244 offset:1600\n\t" \
    "v_add_u32 v244, 0x9000, v243\n\t" \
    "s_waitcnt vmcnt(6) lgkmcnt(0)\n\t" \
    "s_barrier \n\t" \
    "ds_read_b64_tr_b16 v[236:237], v244 offset:0\n\t" \
    "ds_read_b64_tr_b16 v[238:239], v244 offset:1536\n\t" \
    "ds_read_b64_tr_b16 v[164:165], v244 offset:32\n\t" \
    "ds_read_b64_tr_b16 v[166:167], v244 offset:1568\n\t" \
    "ds_read_b64_tr_b16 v[172:173], v244 offset:64\n\t" \
    "ds_read_b64_tr_b16 v[174:175], v244 offset:1600\n\t" \
    "v_add_u32 v244, 0xc000, v243\n\t" \
    "s_waitcnt vmcnt(3) lgkmcnt(0)\n\t" \
    "s_barrier \n\t" \
    "ds_read_b64_tr_b16 v[176:177], v244 offset:0\n\t" \
    "ds_read_b64_tr_b16 v[178:179], v244 offset:1536\n\t" \
    "ds_read_b64_tr_b16 v[184:185], v244 offset:32\n\t" \
    "ds_read_b64_tr_b16 v[186:187], v244 offset:1568\n\t" \
    "ds_read_b64_tr_b16 v[192:193], v244 offset:64\n\t" \
    "ds_read_b64_tr_b16 v[194:195], v244 offset:1600\n\t" \
    "s_mov_b32 s43, 0\n\t" \
    "v_cmp_eq_u32 vcc, 0, v252\n\t" \
    "s_cbranch_vccnz .Lv13_%=\n\t" \
    "s_mov_b32 m0, 0x1000\n\t" \
    "s_add_u32 s49, s42, 0x0\n\t" \
    "buffer_load_dwordx4 v253, s[60:63], s49 offen offset:0 sc1 lds\n\t" \
    "buffer_load_dwordx4 v253, s[60:63], s49 offen offset:1024 sc1 lds\n\t" \
    "buffer_load_dwordx4 v253, s[60:63], s49 offen offset:2048 sc1 lds\n\t" \
    "buffer_load_dwordx4 v253, s[60:63], s49 offen offset:3072 sc1 lds\n\t" \
    "s_mov_b32 m0, 0x4400\n\t" \
    "s_add_u32 s49, s42, 0xc400\n\t" \
    "buffer_load_dwordx4 v253, s[60:63], s49 offen offset:0 sc1 lds\n\t" \
    "s_mov_b32 m0, 0x6000\n\t" \
    "s_add_u32 s49, s42, 0x1000\n\t" \
    "buffer_load_dwordx4 v253, s[60:63], s49 offen offset:0 sc1 lds\n\t" \
    "buffer_load_dwordx4 v253, s[60:63], s49 offen offset:1024 sc1 lds\n\t" \
    "buffer_load_dwordx4 v253, s[60:63], s49 offen offset:2048 sc1 lds\n\t" \
    "buffer_load_dwordx4 v253, s[60:63], s49 offen offset:3072 sc1 lds\n\t" \
    "s_mov_b32 m0, 0x9400\n\t" \
    "s_add_u32 s49, s42, 0xd400\n\t" \
    "buffer_load_dwordx4 v253, s[60:63], s49 offen offset:0 sc1 lds\n\t" \
    "s_mov_b32 s43, 1\n\t" \
    ".Lv13_%=:\n\t" \
    "v_add_u32 v244, 0xf000, v243\n\t" \
    "s_waitcnt vmcnt(0) lgkmcnt(0)\n\t" \
    "s_barrier \n\t" \
    "ds_read_b64_tr_b16 v[180:181], v244 offset:0\n\t" \
    "ds_read_b64_tr_b16 v[182:183], v244 offset:1536\n\t" \
    "ds_read_b64_tr_b16 v[188:189], v244 offset:32\n\t" \
    "ds_read_b64_tr_b16 v[190:191], v244 offset:1568\n\t" \
    "ds_read_b64_tr_b16 v[196:197], v244 offset:64\n\t" \
    "ds_read_b64_tr_b16 v[198:199], v244 offset:1600\n\t" \
    "s_waitcnt lgkmcnt(0)\n\t" \
    "s_barrier \n\t" \
    LST0 \
    "ds_write_b128 v254, v[200:203] offset:40960\n\t" \
    "ds_write_b128 v254, v[204:207] offset:41984\n\t" \
    "ds_write_b128 v254, v[208:211] offset:43008\n\t" \
    "ds_write_b128 v254, v[212:215] offset:44032\n\t" \
    "ds_write_b128 v254, v[216:219] offset:45056\n\t" \
    "ds_write_b128 v254, v[220:223] offset:46080\n\t" \
    "ds_write_b128 v254, v[224:227] offset:47104\n\t" \
    "ds_write_b128 v254, v[228:231] offset:48128\n\t" \
    "ds_write_b128 v254, v[232:235] offset:49152\n\t" \
    "ds_write_b128 v254, v[236:239] offset:50176\n\t" \
    "s_cmp_lg_u32 s43, 0\n\t" \
    "s_cbranch_scc1 .Lv14_%=\n\t" \
    "s_mov_b32 s44, 0\n\t" \
    ".Lv15_%=:\n\t" \
    "v_cmp_ge_u32 vcc, s44, v252\n\t" \
    "s_cbranch_vccz .Lv16_%=\n\t" \
    "s_cmp_eq_u32 s41, 0\n\t" \
    "s_cbranch_scc1 .Lv16_%=\n\t" \
    "s_sub_u32 s41, s41, 1\n\t" \
    "s_sleep 8\n\t" \
    "v_and_b32 v252, 0xf0, v253\n\t" \
    "v_lshlrev_b32 v252, 3, v252\n\t" \
    "buffer_load_dword v252, v252, s[60:63], %[fl] offen sc1\n\t" \
    "s_waitcnt vmcnt(0)\n\t" \
    "s_branch .Lv15_%=\n\t" \
    ".Lv16_%=:\n\t" \
    "s_mov_b32 m0, 0x1000\n\t" \
    "s_add_u32 s49, s42, 0x0\n\t" \
    "buffer_load_dwordx4 v253, s[60:63], s49 offen offset:0 sc1 lds\n\t" \
    "buffer_load_dwordx4 v253, s[60:63], s49 offen offset:1024 sc1 lds\n\t" \
    "buffer_load_dwordx4 v253, s[60:63], s49 offen offset:2048 sc1 lds\n\t" \
    "buffer_load_dwordx4 v253, s[60:63], s49 offen offset:3072 sc1 lds\n\t" \
    "s_mov_b32 m0, 0x4400\n\t" \
    "s_add_u32 s49, s42, 0xc400\n\t" \
    "buffer_load_dwordx4 v253, s[60:63], s49 offen offset:0 sc1 lds\n\t" \
    "s_mov_b32 m0, 0x6000\n\t" \
    "s_add_u32 s49, s42, 0x1000\n\t" \
    "buffer_load_dwordx4 v253, s[60:63], s49 offen offset:0 sc1 lds\n\t" \
    "buffer_load_dwordx4 v253, s[60:63], s49 offen offset:1024 sc1 lds\n\t" \
    "buffer_load_dwordx4 v253, s[60:63], s49 offen offset:2048 sc1 lds\n\t" \
    "buffer_load_dwordx4 v253, s[60:63], s49 offen offset:3072 sc1 lds\n\t" \
    "s_mov_b32 m0, 0x9400\n\t" \
    "s_add_u32 s49, s42, 0xd400\n\t" \
    "buffer_load_dwordx4 v253, s[60:63], s49 offen offset:0 sc1 lds\n\t" \
    ".Lv14_%=:\n\t" \
    "s_waitcnt vmcnt(0) lgkmcnt(0)\n\t" \
    "s_barrier \n\t" \
    "ds_read_b128 v[224:227], v253 offset:4096\n\t" \
    "ds_read_b128 v[244:247], v254 offset:40960\n\t" \
    "ds_read_b128 v[228:231], v253 offset:5120\n\t" \
    "ds_read_b128 v[232:235], v253 offset:24576\n\t" \
    "ds_read_b128 v[236:239], v253 offset:25600\n\t" \
    "ds_read_b128 v[248:251], v254 offset:41984\n\t" \
    ".Lv17_%=:\n\t" \
    LST7 \
    "s_waitcnt lgkmcnt(5)\n\t" \
    "v_mfma_f32_16x16x32_bf16 v[204:207], v[0:3], v[224:227], 0\n\t" \
    "s_waitcnt lgkmcnt(3)\n\t" \
    "v_mfma_f32_16x16x32_bf16 v[204:207], v[4:7], v[228:231], v[204:207]\n\t" \
    "ds_read_b128 v[240:243], v253 offset:12288\n\t" \
    "v_mfma_f32_16x16x32_bf16 v[208:211], v[8:11], v[224:227], 0\n\t" \
    "v_mfma_f32_16x16x32_bf16 v[208:211], v[12:15], v[228:231], v[208:211]\n\t" \
    "v_mfma_f32_16x16x32_bf16 v[200:203], v[244:247], v[224:227], 0\n\t" \
    "s_waitcnt lgkmcnt(1)\n\t" \
    "v_mfma_f32_16x16x32_bf16 v[200:203], v[248:251], v[228:231], v[200:203]\n\t" \
    "v_mfma_f32_16x16x32_bf16 v[216:219], v[0:3], v[232:235], 0\n\t" \
    "ds_read_b128 v[224:227], v253 offset:13312\n\t" \
    "v_mfma_f32_16x16x32_bf16 v[216:219], v[4:7], v[236:239], v[216:219]\n\t" \
    "ds_read_b128 v[228:231], v253 offset:6144\n\t" \
    "v_mfma_f32_16x16x32_bf16 v[220:223], v[8:11], v[232:235], 0\n\t" \
    "v_mfma_f32_16x16x32_bf16 v[220:223], v[12:15], v[236:239], v[220:223]\n\t" \
    "v_mfma_f32_16x16x32_bf16 v[212:215], v[244:247], v[232:235], 0\n\t" \
    "ds_read_b128 v[244:247], v254 offset:43008\n\t" \
    "v_mfma_f32_16x16x32_bf16 v[212:215], v[248:251], v[236:239], v[212:215]\n\t" \
    "ds_read_b128 v[248:251], v254 offset:44032\n\t" \
    "s_waitcnt lgkmcnt(4)\n\t" \
    "v_mfma_f32_16x16x32_bf16 v[204:207], v[24:27], v[240:243], v[204:207]\n\t" \
    "ds_read_b128 v[232:235], v253 offset:7168\n\t" \
    "s_waitcnt lgkmcnt(4)\n\t" \
    "v_mfma_f32_16x16x32_bf16 v[204:207], v[28:31], v[224:227], v[204:207]\n\t" \
    "ds_read_b128 v[236:239], v253 offset:26624\n\t" \
    "v_mfma_f32_16x16x32_bf16 v[208:211], v[32:35], v[240:243], v[208:211]\n\t" \
    "v_mfma_f32_16x16x32_bf16 v[208:211], v[36:39], v[224:227], v[208:211]\n\t" \
    "v_mfma_f32_16x16x32_bf16 v[200:203], v[16:19], v[240:243], v[200:203]\n\t" \
    "v_mfma_f32_16x16x32_bf16 v[200:203], v[20:23], v[224:227], v[200:203]\n\t" \
    "s_waitcnt lgkmcnt(4)\n\t" \
    "v_mfma_f32_16x16x32_bf16 v[204:207], v[40:43], v[228:231], v[204:207]\n\t" \
    "ds_read_b128 v[240:243], v253 offset:27648\n\t" \
    "s_waitcnt lgkmcnt(2)\n\t" \
    "v_mfma_f32_16x16x32_bf16 v[204:207], v[44:47], v[232:235], v[204:207]\n\t" \
    "ds_read_b128 v[224:227], v253 offset:0\n\t" \
    "v_mfma_f32_16x16x32_bf16 v[208:211], v[48:51], v[228:231], v[208:211]\n\t" \
    "v_mfma_f32_16x16x32_bf16 v[208:211], v[52:55], v[232:235], v[208:211]\n\t" \
    "v_mfma_f32_16x16x32_bf16 v[200:203], v[244:247], v[228:231], v[200:203]\n\t" \
    "v_mfma_f32_16x16x32_bf16 v[200:203], v[248:251], v[232:235], v[200:203]\n\t" \
    "s_waitcnt lgkmcnt(2)\n\t" \
    "v_mfma_f32_16x16x32_bf16 v[216:219], v[40:43], v[236:239], v[216:219]\n\t" \
    "ds_read_b128 v[228:231], v253 offset:1024\n\t" \
    "s_add_u32 s44, s40, 1\n\t" \
    "s_waitcnt lgkmcnt(2)\n\t" \
    "v_mfma_f32_16x16x32_bf16 v[216:219], v[44:47], v[240:243], v[216:219]\n\t" \
    "ds_read_b128 v[232:235], v253 offset:10240\n\t" \
    "s_and_b32 s45, s44, 15\n\t" \
    "v_mfma_f32_16x16x32_bf16 v[220:223], v[48:51], v[236:239], v[220:223]\n\t" \
    "s_cmp_eq_u32 s45, 0\n\t" \
    "v_mfma_f32_16x16x32_bf16 v[220:223], v[52:55], v[240:243], v[220:223]\n\t" \
    "s_cselect_b32 s45, %[cj], %[cs]\n\t" \
    "v_mfma_f32_16x16x32_bf16 v[212:215], v[244:247], v[236:239], v[212:215]\n\t" \
    "ds_read_b128 v[244:247], v254 offset:45056\n\t" \
    "s_cmp_eq_u32 s44, 64\n\t" \
    "v_mfma_f32_16x16x32_bf16 v[212:215], v[248:251], v[240:243], v[212:215]\n\t" \
    "ds_read_b128 v[248:251], v254 offset:46080\n\t" \
    "s_cselect_b32 s45, 0, s45\n\t" \
    "s_waitcnt lgkmcnt(4)\n\t" \
    "v_mfma_f32_16x16x32_bf16 v[204:207], v[64:67], v[224:227], v[204:207]\n\t" \
    "ds_read_b128 v[236:239], v253 offset:11264\n\t" \
    "s_add_u32 s42, s42, s45\n\t" \
    "s_waitcnt lgkmcnt(4)\n\t" \
    "v_mfma_f32_16x16x32_bf16 v[204:207], v[68:71], v[228:231], v[204:207]\n\t" \
    "ds_read_b128 v[240:243], v253 offset:30720\n\t" \
    "v_mfma_f32_16x16x32_bf16 v[208:211], v[72:75], v[224:227], v[208:211]\n\t" \
    "v_mfma_f32_16x16x32_bf16 v[208:211], v[76:79], v[228:231], v[208:211]\n\t" \
    "v_mfma_f32_16x16x32_bf16 v[200:203], v[56:59], v[224:227], v[200:203]\n\t" \
    "v_mfma_f32_16x16x32_bf16 v[200:203], v[60:63], v[228:231], v[200:203]\n\t" \
    "s_waitcnt lgkmcnt(4)\n\t" \
    "v_mfma_f32_16x16x32_bf16 v[204:207], v[80:83], v[232:235], v[204:207]\n\t" \
    "ds_read_b128 v[224:227], v253 offset:31744\n\t" \
    "s_waitcnt lgkmcnt(2)\n\t" \
    "v_mfma_f32_16x16x32_bf16 v[204:207], v[84:87], v[236:239], v[204:207]\n\t" \
    "ds_read_b128 v[228:231], v253 offset:2048\n\t" \
    "v_mfma_f32_16x16x32_bf16 v[208:211], v[88:91], v[232:235], v[208:211]\n\t" \
    "v_mfma_f32_16x16x32_bf16 v[208:211], v[92:95], v[236:239], v[208:211]\n\t" \
    "v_mfma_f32_16x16x32_bf16 v[200:203], v[244:247], v[232:235], v[200:203]\n\t" \
    "v_mfma_f32_16x16x32_bf16 v[200:203], v[248:251], v[236:239], v[200:203]\n\t" \
    "s_waitcnt lgkmcnt(2)\n\t" \
    "v_mfma_f32_16x16x32_bf16 v[216:219], v[80:83], v[240:243], v[216:219]\n\t" \
    "ds_read_b128 v[232:235], v253 offset:3072\n\t" \
    "s_and_b32 s44, s40, 3\n\t" \
    "s_waitcnt lgkmcnt(2)\n\t" \
    "v_mfma_f32_16x16x32_bf16 v[216:219], v[84:87], v[224:227], v[216:219]\n\t" \
    "ds_read_b128 v[236:239], v253 offset:14336\n\t" \
    "s_lshl_b32 s44, s44, 17\n\t" \
    "v_mfma_f32_16x16x32_bf16 v[220:223], v[88:91], v[240:243], v[220:223]\n\t" \
    "s_add_u32 s44, s44, %[mb]\n\t" \
    "v_mfma_f32_16x16x32_bf16 v[220:223], v[92:95], v[224:227], v[220:223]\n\t" \
    "s_add_u32 s46, s44, 0x800\n\t" \
    "v_mfma_f32_16x16x32_bf16 v[212:215], v[244:247], v[240:243], v[212:215]\n\t" \
    "ds_read_b128 v[244:247], v254 offset:47104\n\t" \
    "s_add_u32 s47, s44, 0x1800\n\t" \
    "v_mfma_f32_16x16x32_bf16 v[212:215], v[248:251], v[224:227], v[212:215]\n\t" \
    "ds_read_b128 v[248:251], v254 offset:48128\n\t" \
    "s_add_u32 s48, s44, 0xc00\n\t" \
    "s_waitcnt lgkmcnt(4)\n\t" \
    "v_mfma_f32_16x16x32_bf16 v[204:207], v[104:107], v[228:231], v[204:207]\n\t" \
    "ds_read_b128 v[240:243], v253 offset:15360\n\t" \
    "s_waitcnt lgkmcnt(4)\n\t" \
    "v_mfma_f32_16x16x32_bf16 v[204:207], v[108:111], v[232:235], v[204:207]\n\t" \
    "ds_read_b128 v[224:227], v253 offset:34816\n\t" \
    "v_mfma_f32_16x16x32_bf16 v[208:211], v[112:115], v[228:231], v[208:211]\n\t" \
    "v_mfma_f32_16x16x32_bf16 v[208:211], v[116:119], v[232:235], v[208:211]\n\t" \
    "v_mfma_f32_16x16x32_bf16 v[200:203], v[96:99], v[228:231], v[200:203]\n\t" \
    "v_mfma_f32_16x16x32_bf16 v[200:203], v[100:103], v[232:235], v[200:203]\n\t" \
    "s_waitcnt lgkmcnt(4)\n\t" \
    "v_mfma_f32_16x16x32_bf16 v[204:207], v[120:123], v[236:239], v[204:207]\n\t" \
    "ds_read_b128 v[228:231], v253 offset:35840\n\t" \
    "s_waitcnt lgkmcnt(2)\n\t" \
    "v_mfma_f32_16x16x32_bf16 v[204:207], v[124:127], v[240:243], v[204:207]\n\t" \
    "ds_read_b128 v[232:235], v253 offset:8192\n\t" \
    "v_mfma_f32_16x16x32_bf16 v[208:211], v[128:131], v[236:239], v[208:211]\n\t" \
    "v_mfma_f32_16x16x32_bf16 v[208:211], v[132:135], v[240:243], v[208:211]\n\t" \
    "v_mfma_f32_16x16x32_bf16 v[200:203], v[244:247], v[236:239], v[200:203]\n\t" \
    "v_mfma_f32_16x16x32_bf16 v[200:203], v[248:251], v[240:243], v[200:203]\n\t" \
    "s_waitcnt lgkmcnt(2)\n\t" \
    "v_mfma_f32_16x16x32_bf16 v[216:219], v[120:123], v[224:227], v[216:219]\n\t" \
    "ds_read_b128 v[236:239], v253 offset:9216\n\t" \
    "s_waitcnt lgkmcnt(2)\n\t" \
    "v_mfma_f32_16x16x32_bf16 v[216:219], v[124:127], v[228:231], v[216:219]\n\t" \
    "ds_read_b128 v[240:243], v253 offset:16384\n\t" \
    "v_mfma_f32_16x16x32_bf16 v[220:223], v[128:131], v[224:227], v[220:223]\n\t" \
    "v_mfma_f32_16x16x32_bf16 v[220:223], v[132:135], v[228:231], v[220:223]\n\t" \
    "v_mfma_f32_16x16x32_bf16 v[212:215], v[244:247], v[224:227], v[212:215]\n\t" \
    "ds_read_b128 v[244:247], v254 offset:49152\n\t" \
    "v_mfma_f32_16x16x32_bf16 v[212:215], v[248:251], v[228:231], v[212:215]\n\t" \
    "ds_read_b128 v[248:251], v254 offset:50176\n\t" \
    "v_and_b32 v252, 0xf0, v253\n\t" \
    "v_lshlrev_b32 v252, 3, v252\n\t" \
    "buffer_load_dword v252, v252, s[60:63], %[fl] offen sc1\n\t" \
    "s_waitcnt lgkmcnt(4)\n\t" \
    "v_mfma_f32_16x16x32_bf16 v[204:207], v[144:147], v[232:235], v[204:207]\n\t" \
    "ds_read_b128 v[224:227], v253 offset:17408\n\t" \
    "s_waitcnt lgkmcnt(4)\n\t" \
    "v_mfma_f32_16x16x32_bf16 v[204:207], v[148:151], v[236:239], v[204:207]\n\t" \
    "ds_read_b128 v[228:231], v253 offset:36864\n\t" \
    "v_mfma_f32_16x16x32_bf16 v[208:211], v[152:155], v[232:235], v[208:211]\n\t" \
    "v_mfma_f32_16x16x32_bf16 v[208:211], v[156:159], v[236:239], v[208:211]\n\t" \
    "v_mfma_f32_16x16x32_bf16 v[200:203], v[136:139], v[232:235], v[200:203]\n\t" \
    "v_mfma_f32_16x16x32_bf16 v[200:203], v[140:143], v[236:239], v[200:203]\n\t" \
    "s_waitcnt lgkmcnt(4)\n\t" \
    "v_mfma_f32_16x16x32_bf16 v[204:207], v[160:163], v[240:243], v[204:207]\n\t" \
    "ds_read_b128 v[232:235], v253 offset:37888\n\t" \
    "s_waitcnt lgkmcnt(2)\n\t" \
    "v_mfma_f32_16x16x32_bf16 v[204:207], v[164:167], v[224:227], v[204:207]\n\t" \
    "ds_read_b128 v[236:239], v253 offset:18432\n\t" \
    "v_mfma_f32_16x16x32_bf16 v[208:211], v[168:171], v[240:243], v[208:211]\n\t" \
    "v_mfma_f32_16x16x32_bf16 v[208:211], v[172:175], v[224:227], v[208:211]\n\t" \
    "v_mfma_f32_16x16x32_bf16 v[200:203], v[244:247], v[240:243], v[200:203]\n\t" \
    "v_mfma_f32_16x16x32_bf16 v[200:203], v[248:251], v[224:227], v[200:203]\n\t" \
    "s_waitcnt lgkmcnt(2)\n\t" \
    "v_mfma_f32_16x16x32_bf16 v[216:219], v[160:163], v[228:231], v[216:219]\n\t" \
    "ds_read_b128 v[240:243], v253 offset:19456\n\t" \
    "s_waitcnt lgkmcnt(2)\n\t" \
    "v_mfma_f32_16x16x32_bf16 v[216:219], v[164:167], v[232:235], v[216:219]\n\t" \
    "ds_read_b128 v[224:227], v253 offset:32768\n\t" \
    "v_mfma_f32_16x16x32_bf16 v[220:223], v[168:171], v[228:231], v[220:223]\n\t" \
    "v_mfma_f32_16x16x32_bf16 v[220:223], v[172:175], v[232:235], v[220:223]\n\t" \
    "v_mfma_f32_16x16x32_bf16 v[212:215], v[244:247], v[228:231], v[212:215]\n\t" \
    "v_mfma_f32_16x16x32_bf16 v[212:215], v[248:251], v[232:235], v[212:215]\n\t" \
    "s_waitcnt lgkmcnt(2)\n\t" \
    "v_mfma_f32_16x16x32_bf16 v[204:207], v[184:187], v[236:239], v[204:207]\n\t" \
    "ds_read_b128 v[228:231], v253 offset:33792\n\t" \
    "s_waitcnt lgkmcnt(2)\n\t" \
    "v_mfma_f32_16x16x32_bf16 v[204:207], v[188:191], v[240:243], v[204:207]\n\t" \
    "ds_read_b128 v[232:235], v253 offset:20480\n\t" \
    "v_mfma_f32_16x16x32_bf16 v[208:211], v[192:195], v[236:239], v[208:211]\n\t" \
    "v_mfma_f32_16x16x32_bf16 v[208:211], v[196:199], v[240:243], v[208:211]\n\t" \
    "v_mfma_f32_16x16x32_bf16 v[200:203], v[176:179], v[236:239], v[200:203]\n\t" \
    "v_mfma_f32_16x16x32_bf16 v[200:203], v[180:183], v[240:243], v[200:203]\n\t" \
    "s_waitcnt lgkmcnt(2)\n\t" \
    "v_mfma_f32_16x16x32_bf16 v[216:219], v[24:27], v[224:227], v[216:219]\n\t" \
    "ds_read_b128 v[236:239], v253 offset:21504\n\t" \
    "s_waitcnt lgkmcnt(2)\n\t" \
    "v_mfma_f32_16x16x32_bf16 v[216:219], v[28:31], v[228:231], v[216:219]\n\t" \
    "ds_read_b128 v[240:243], v253 offset:22528\n\t" \
    "v_mfma_f32_16x16x32_bf16 v[220:223], v[32:35], v[224:227], v[220:223]\n\t" \
    "v_cvt_pk_bf16_f32 v204, v204, v205\n\t" \
    "v_mfma_f32_16x16x32_bf16 v[220:223], v[36:39], v[228:231], v[220:223]\n\t" \
    "v_cvt_pk_bf16_f32 v205, v206, v207\n\t" \
    "v_mfma_f32_16x16x32_bf16 v[212:215], v[16:19], v[224:227], v[212:215]\n\t" \
    "ds_write_b64 v254, v[204:205] offset:10240\n\t" \
    "v_mfma_f32_16x16x32_bf16 v[212:215], v[20:23], v[228:231], v[212:215]\n\t" \
    "v_cvt_pk_bf16_f32 v208, v208, v209\n\t" \
    "s_waitcnt lgkmcnt(3)\n\t" \
    "v_mfma_f32_16x16x32_bf16 v[216:219], v[64:67], v[232:235], v[216:219]\n\t" \
    "ds_read_b128 v[224:227], v253 offset:23552\n\t" \
    "v_cvt_pk_bf16_f32 v209, v210, v211\n\t" \
    "s_waitcnt lgkmcnt(3)\n\t" \
    "v_mfma_f32_16x16x32_bf16 v[216:219], v[68:71], v[236:239], v[216:219]\n\t" \
    "ds_read_b128 v[228:231], v253 offset:28672\n\t" \
    "ds_write_b64 v254, v[208:209] offset:11264\n\t" \
    "v_mfma_f32_16x16x32_bf16 v[220:223], v[72:75], v[232:235], v[220:223]\n\t" \
    "v_cvt_pk_bf16_f32 v200, v200, v201\n\t" \
    "v_mfma_f32_16x16x32_bf16 v[220:223], v[76:79], v[236:239], v[220:223]\n\t" \
    "v_cvt_pk_bf16_f32 v201, v202, v203\n\t" \
    "v_mfma_f32_16x16x32_bf16 v[212:215], v[56:59], v[232:235], v[212:215]\n\t" \
    "ds_write_b64 v254, v[200:201] offset:9216\n\t" \
    "v_mfma_f32_16x16x32_bf16 v[212:215], v[60:63], v[236:239], v[212:215]\n\t" \
    "s_waitcnt lgkmcnt(5)\n\t" \
    "v_mfma_f32_16x16x32_bf16 v[216:219], v[104:107], v[240:243], v[216:219]\n\t" \
    "ds_read_b128 v[232:235], v253 offset:29696\n\t" \
    "s_waitcnt lgkmcnt(4)\n\t" \
    "v_mfma_f32_16x16x32_bf16 v[216:219], v[108:111], v[224:227], v[216:219]\n\t" \
    "ds_read_b128 v[236:239], v253 offset:38912\n\t" \
    "v_mfma_f32_16x16x32_bf16 v[220:223], v[112:115], v[240:243], v[220:223]\n\t" \
    "v_mfma_f32_16x16x32_bf16 v[220:223], v[116:119], v[224:227], v[220:223]\n\t" \
    "v_mfma_f32_16x16x32_bf16 v[212:215], v[96:99], v[240:243], v[212:215]\n\t" \
    "v_mfma_f32_16x16x32_bf16 v[212:215], v[100:103], v[224:227], v[212:215]\n\t" \
    "ds_read_b128 v[240:243], v253 offset:39936\n\t" \
    "s_waitcnt lgkmcnt(3)\n\t" \
    "s_barrier \n\t" \
    "v_mfma_f32_16x16x32_bf16 v[216:219], v[144:147], v[228:231], v[216:219]\n\t" \
    "s_waitcnt lgkmcnt(2)\n\t" \
    "v_mfma_f32_16x16x32_bf16 v[216:219], v[148:151], v[232:235], v[216:219]\n\t" \
    "s_waitcnt lgkmcnt(1)\n\t" \
    "v_mfma_f32_16x16x32_bf16 v[216:219], v[184:187], v[236:239], v[216:219]\n\t" \
    "s_waitcnt lgkmcnt(0)\n\t" \
    "v_mfma_f32_16x16x32_bf16 v[216:219], v[188:191], v[240:243], v[216:219]\n\t" \
    "v_mfma_f32_16x16x32_bf16 v[220:223], v[152:155], v[228:231], v[220:223]\n\t" \
    "v_mfma_f32_16x16x32_bf16 v[220:223], v[156:159], v[232:235], v[220:223]\n\t" \
    "v_mfma_f32_16x16x32_bf16 v[220:223], v[192:195], v[236:239], v[220:223]\n\t" \
    "v_cvt_pk_bf16_f32 v216, v216, v217\n\t" \
    "v_cvt_pk_bf16_f32 v217, v218, v219\n\t" \
    "v_mfma_f32_16x16x32_bf16 v[220:223], v[196:199], v[240:243], v[220:223]\n\t" \
    "ds_write_b64 v254, v[216:217] offset:16384\n\t" \
    "v_mfma_f32_16x16x32_bf16 v[212:215], v[136:139], v[228:231], v[212:215]\n\t" \
    "v_mfma_f32_16x16x32_bf16 v[212:215], v[140:143], v[232:235], v[212:215]\n\t" \
    "v_mfma_f32_16x16x32_bf16 v[212:215], v[176:179], v[236:239], v[212:215]\n\t" \
    "v_cvt_pk_bf16_f32 v220, v220, v221\n\t" \
    "v_cvt_pk_bf16_f32 v221, v222, v223\n\t" \
    "v_mfma_f32_16x16x32_bf16 v[212:215], v[180:183], v[240:243], v[212:215]\n\t" \
    "ds_write_b64 v254, v[220:221] offset:17408\n\t" \
    "s_nop 6\n\t" \
    "v_cvt_pk_bf16_f32 v212, v212, v213\n\t" \
    "v_cvt_pk_bf16_f32 v213, v214, v215\n\t" \
    "ds_write_b64 v254, v[212:213] offset:15360\n\t" \
    LST6 \
    "s_waitcnt vmcnt(0) lgkmcnt(0)\n\t" \
    "s_barrier \n\t" \
    "s_setprio 2\n\t" \
    "s_add_u32 s44, s40, 1\n\t" \
    "s_min_u32 s44, s44, 63\n\t" \
    ".Lv18_%=:\n\t" \
    "v_cmp_ge_u32 vcc, s44, v252\n\t" \
    "s_cbranch_vccz .Lv19_%=\n\t" \
    "s_cmp_eq_u32 s41, 0\n\t" \
    "s_cbranch_scc1 .Lv19_%=\n\t" \
    "s_sub_u32 s41, s41, 1\n\t" \
    "s_sleep 2\n\t" \
    "v_and_b32 v252, 0xf0, v253\n\t" \
    "v_lshlrev_b32 v252, 3, v252\n\t" \
    "buffer_load_dword v252, v252, s[60:63], %[fl] offen sc1\n\t" \
    "s_waitcnt vmcnt(0)\n\t" \
    "s_branch .Lv18_%=\n\t" \
    ".Lv19_%=:\n\t" \
    "s_mov_b32 m0, 0x1000\n\t" \
    "s_add_u32 s49, s42, 0x0\n\t" \
    "buffer_load_dwordx4 v253, s[60:63], s49 offen offset:0 sc1 lds\n\t" \
    "s_sleep 1\n\t" \
    "buffer_load_dwordx4 v253, s[60:63], s49 offen offset:1024 sc1 lds\n\t" \
    "s_sleep 1\n\t" \
    "buffer_load_dwordx4 v253, s[60:63], s49 offen offset:2048 sc1 lds\n\t" \
    "s_sleep 1\n\t" \
    "buffer_load_dwordx4 v253, s[60:63], s49 offen offset:3072 sc1 lds\n\t" \
    "s_sleep 1\n\t" \
    "s_mov_b32 m0, 0x4400\n\t" \
    "s_add_u32 s49, s42, 0xc400\n\t" \
    "buffer_load_dwordx4 v253, s[60:63], s49 offen offset:0 sc1 lds\n\t" \
    "s_sleep 1\n\t" \
    "s_mov_b32 m0, 0x6000\n\t" \
    "s_add_u32 s49, s42, 0x1000\n\t" \
    "buffer_load_dwordx4 v253, s[60:63], s49 offen offset:0 sc1 lds\n\t" \
    "s_sleep 1\n\t" \
    "buffer_load_dwordx4 v253, s[60:63], s49 offen offset:1024 sc1 lds\n\t" \
    "s_sleep 1\n\t" \
    "buffer_load_dwordx4 v253, s[60:63], s49 offen offset:2048 sc1 lds\n\t" \
    "s_sleep 1\n\t" \
    "buffer_load_dwordx4 v253, s[60:63], s49 offen offset:3072 sc1 lds\n\t" \
    "s_sleep 1\n\t" \
    "s_mov_b32 m0, 0x9400\n\t" \
    "s_add_u32 s49, s42, 0xd400\n\t" \
    "buffer_load_dwordx4 v253, s[60:63], s49 offen offset:0 sc1 lds\n\t" \
    "s_sleep 1\n\t" \
    "ds_read_b128 v[200:203], v254 offset:40960\n\t" \
    "ds_read_b128 v[204:207], v254 offset:41984\n\t" \
    "ds_read_b128 v[216:219], v254 offset:43008\n\t" \
    "ds_read_b128 v[220:223], v254 offset:44032\n\t" \
    "v_mov_b64 v[208:209], 0\n\t" \
    "v_mov_b64 v[210:211], 0\n\t" \
    "v_mov_b64 v[244:245], 0\n\t" \
    "v_mov_b64 v[246:247], 0\n\t" \
    "v_mov_b64 v[248:249], 0\n\t" \
    "v_mov_b64 v[250:251], 0\n\t" \
    LST1 \
    "s_sleep 5\n\t" \
    "s_mov_b32 m0, 0xa800\n\t" \
    "s_nop 0\n\t" \
    "buffer_load_dwordx4 v253, s[60:63], s46 offen sc1 lds\n\t" \
    "s_mov_b32 m0, 0xb800\n\t" \
    "s_nop 0\n\t" \
    "buffer_load_dwordx4 v253, s[60:63], s47 offen sc1 lds\n\t" \
    "s_mov_b32 m0, 0xac00\n\t" \
    "s_nop 0\n\t" \
    "buffer_load_dwordx4 v253, s[60:63], s48 offen sc1 lds\n\t" \
    "s_waitcnt lgkmcnt(0)\n\t" \
    LFLUSH212 \
    "v_bfe_i32 v212, v253, 5, 1\n\t" \
    "v_and_b32 v212, 0xaaaa, v212\n\t" \
    "v_or_b32 v212, 0x4444, v212\n\t" \
    "v_and_b32 v213, 0xffff, v255\n\t" \
    "v_lshrrev_b32 v214, 16, v255\n\t" \
    ".Lv110_%=:\n\t" \
    "s_waitcnt vmcnt(0)\n\t" \
    "ds_read_b128 v[224:227], v253 offset:43008\n\t" \
    "ds_read_b128 v[228:231], v253 offset:47104\n\t" \
    "ds_read_b128 v[232:235], v253 offset:44032\n\t" \
    "s_waitcnt lgkmcnt(2)\n\t" \
    "v_max_u32 v215, v224, v225\n\t" \
    "v_max3_u32 v215, v226, v227, v215\n\t" \
    "s_waitcnt lgkmcnt(1)\n\t" \
    "v_max3_u32 v215, v228, v229, v215\n\t" \
    "v_max3_u32 v215, v230, v231, v215\n\t" \
    "s_waitcnt lgkmcnt(0)\n\t" \
    "v_max3_u32 v215, v232, v233, v215\n\t" \
    "v_max3_u32 v215, v234, v235, v215\n\t" \
    "v_cmp_eq_u32 vcc, -1, v215\n\t" \
    "s_cbranch_vccz .Lv111_%=\n\t" \
    "v_max_u32 v215, v224, v225\n\t" \
    "v_max3_u32 v215, v226, v227, v215\n\t" \
    "v_cmp_eq_u32 vcc, -1, v215\n\t" \
    "s_cbranch_vccz .Lv112_%=\n\t" \
    "s_mov_b32 m0, 0xa800\n\t" \
    "s_nop 0\n\t" \
    "buffer_load_dwordx4 v253, s[60:63], s46 offen sc1 lds\n\t" \
    ".Lv112_%=:\n\t" \
    "v_max_u32 v215, v228, v229\n\t" \
    "v_max3_u32 v215, v230, v231, v215\n\t" \
    "v_cmp_eq_u32 vcc, -1, v215\n\t" \
    "s_cbranch_vccz .Lv113_%=\n\t" \
    "s_mov_b32 m0, 0xb800\n\t" \
    "s_nop 0\n\t" \
    "buffer_load_dwordx4 v253, s[60:63], s47 offen sc1 lds\n\t" \
    ".Lv113_%=:\n\t" \
    "v_max_u32 v215, v232, v233\n\t" \
    "v_max3_u32 v215, v234, v235, v215\n\t" \
    "v_cmp_eq_u32 vcc, -1, v215\n\t" \
    "s_cbranch_vccz .Lv114_%=\n\t" \
    "s_mov_b32 m0, 0xac00\n\t" \
    "s_nop 0\n\t" \
    "buffer_load_dwordx4 v253, s[60:63], s48 offen sc1 lds\n\t" \
    ".Lv114_%=:\n\t" \
    "s_cmp_eq_u32 s41, 0\n\t" \
    "s_cbranch_scc1 .Lv111_%=\n\t" \
    "s_sub_u32 s41, s41, 1\n\t" \
    "s_branch .Lv110_%=\n\t" \
    ".Lv111_%=:\n\t" \
    LST2 \
    "s_waitcnt lgkmcnt(0)\n\t" \
    "s_barrier \n\t" \
    LST3 \
    "ds_read_b128 v[236:239], v253 offset:40960\n\t" \
    "ds_read_b128 v[240:243], v253 offset:46080\n\t" \
    "v_smfmac_f32_16x16x64_bf16 v[244:247], v[224:227], v[0:7], v212\n\t" \
    "v_smfmac_f32_16x16x64_bf16 v[248:251], v[224:227], v[8:15], v212\n\t" \
    "v_smfmac_f32_16x16x64_bf16 v[208:211], v[224:227], v[200:207], v212\n\t" \
    "ds_read_b128 v[200:203], v254 offset:45056\n\t" \
    "ds_read_b128 v[204:207], v254 offset:46080\n\t" \
    "ds_read_b128 v[224:227], v253 offset:41984\n\t" \
    "v_smfmac_f32_16x16x64_bf16 v[208:211], v[228:231], v[16:23], v212\n\t" \
    "v_smfmac_f32_16x16x64_bf16 v[244:247], v[228:231], v[24:31], v212\n\t" \
    "v_smfmac_f32_16x16x64_bf16 v[248:251], v[228:231], v[32:39], v212\n\t" \
    "ds_read_b128 v[228:231], v253 offset:48128\n\t" \
    "v_smfmac_f32_16x16x64_bf16 v[208:211], v[232:235], v[216:223], v212\n\t" \
    "ds_read_b128 v[216:219], v254 offset:47104\n\t" \
    "ds_read_b128 v[220:223], v254 offset:48128\n\t" \
    "v_smfmac_f32_16x16x64_bf16 v[244:247], v[232:235], v[40:47], v212\n\t" \
    "v_smfmac_f32_16x16x64_bf16 v[248:251], v[232:235], v[48:55], v212\n\t" \
    "ds_read_b128 v[232:235], v253 offset:45056\n\t" \
    "s_waitcnt lgkmcnt(8)\n\t" \
    "v_smfmac_f32_16x16x64_bf16 v[208:211], v[236:239], v[56:63], v212\n\t" \
    "v_smfmac_f32_16x16x64_bf16 v[244:247], v[236:239], v[64:71], v212\n\t" \
    "v_smfmac_f32_16x16x64_bf16 v[248:251], v[236:239], v[72:79], v212\n\t" \
    "ds_read_b128 v[236:239], v253 offset:49152\n\t" \
    "s_waitcnt lgkmcnt(6)\n\t" \
    "v_smfmac_f32_16x16x64_bf16 v[208:211], v[240:243], v[200:207], v212\n\t" \
    "ds_read_b128 v[200:203], v254 offset:49152\n\t" \
    "ds_read_b128 v[204:207], v254 offset:50176\n\t" \
    "v_smfmac_f32_16x16x64_bf16 v[244:247], v[240:243], v[80:87], v212\n\t" \
    "v_smfmac_f32_16x16x64_bf16 v[248:251], v[240:243], v[88:95], v212\n\t" \
    "ds_read_b128 v[240:243], v253 offset:50176\n\t" \
    "s_waitcnt lgkmcnt(8)\n\t" \
    "v_smfmac_f32_16x16x64_bf16 v[208:211], v[224:227], v[96:103], v212\n\t" \
    "v_smfmac_f32_16x16x64_bf16 v[244:247], v[224:227], v[104:111], v212\n\t" \
    "v_smfmac_f32_16x16x64_bf16 v[248:251], v[224:227], v[112:119], v212\n\t" \
    "s_waitcnt lgkmcnt(7)\n\t" \
    "v_smfmac_f32_16x16x64_bf16 v[244:247], v[228:231], v[120:127], v212\n\t" \
    "s_waitcnt lgkmcnt(4)\n\t" \
    "v_smfmac_f32_16x16x64_bf16 v[244:247], v[232:235], v[144:151], v212\n\t" \
    "s_waitcnt lgkmcnt(3)\n\t" \
    "v_smfmac_f32_16x16x64_bf16 v[244:247], v[236:239], v[160:167], v212\n\t" \
    "s_waitcnt lgkmcnt(0)\n\t" \
    "v_smfmac_f32_16x16x64_bf16 v[244:247], v[240:243], v[184:191], v212\n\t" \
    "v_smfmac_f32_16x16x64_bf16 v[248:251], v[228:231], v[128:135], v212\n\t" \
    "v_smfmac_f32_16x16x64_bf16 v[248:251], v[232:235], v[152:159], v212\n\t" \
    "v_smfmac_f32_16x16x64_bf16 v[248:251], v[236:239], v[168:175], v212\n\t" \
    "v_add_f32 v244, v244, v246\n\t" \
    "v_add_f32 v245, v245, v247\n\t" \
    "v_cvt_pk_bf16_f32 v244, v244, v245\n\t" \
    "v_smfmac_f32_16x16x64_bf16 v[248:251], v[240:243], v[192:199], v212\n\t" \
    "ds_write_b16 v213, v244 offset:512\n\t" \
    "ds_write_b16_d16_hi v213, v244 offset:528\n\t" \
    "v_smfmac_f32_16x16x64_bf16 v[208:211], v[228:231], v[216:223], v212\n\t" \
    "v_smfmac_f32_16x16x64_bf16 v[208:211], v[232:235], v[136:143], v212\n\t" \
    "v_smfmac_f32_16x16x64_bf16 v[208:211], v[236:239], v[200:207], v212\n\t" \
    "v_add_f32 v248, v248, v250\n\t" \
    "v_add_f32 v249, v249, v251\n\t" \
    "v_cvt_pk_bf16_f32 v248, v248, v249\n\t" \
    "v_smfmac_f32_16x16x64_bf16 v[208:211], v[240:243], v[176:183], v212\n\t" \
    "ds_write_b16 v213, v248 offset:1024\n\t" \
    "ds_write_b16_d16_hi v213, v248 offset:1040\n\t" \
    "s_nop 5\n\t" \
    "v_add_f32 v208, v208, v210\n\t" \
    "v_add_f32 v209, v209, v211\n\t" \
    "v_cvt_pk_bf16_f32 v208, v208, v209\n\t" \
    "ds_write_b16 v213, v208 offset:0\n\t" \
    "ds_write_b16_d16_hi v213, v208 offset:16\n\t" \
    "s_setprio 0\n\t" \
    "s_waitcnt lgkmcnt(0)\n\t" \
    "s_barrier \n\t" \
    LST4 \
    "ds_read_b128 v[224:227], v253 offset:4096\n\t" \
    "ds_read_b128 v[244:247], v254 offset:40960\n\t" \
    "ds_read_b128 v[228:231], v253 offset:5120\n\t" \
    "ds_read_b128 v[232:235], v253 offset:24576\n\t" \
    "ds_read_b128 v[236:239], v253 offset:25600\n\t" \
    "ds_read_b128 v[248:251], v254 offset:41984\n\t" \
    "s_add_u32 s40, s40, 1\n\t" \
    "s_cmp_lg_u32 s40, 64\n\t" \
    "s_cbranch_scc1 .Lv17_%=\n\t" \
    "s_branch .Lend%=\n\t" \
    ".Lv0_%=:\n\t" \
    "s_mov_b32 s42, %[xs]\n\t" \
    "s_add_u32 m0, %[wm], 0xc000\n\t" \
    "s_mov_b32 s44, 0x0\n\t" \
    "buffer_load_dwordx4 v240, %[wr], s44 offen offset:0 nt lds\n\t" \
    "buffer_load_dwordx4 v241, %[wr], s44 offen offset:1024 nt lds\n\t" \
    "buffer_load_dwordx4 v242, %[wr], s44 offen offset:2048 nt lds\n\t" \
    "s_add_u32 m0, %[wm], 0xf000\n\t" \
    "s_mov_b32 s44, 0x60000\n\t" \
    "buffer_load_dwordx4 v240, %[wr], s44 offen offset:0 nt lds\n\t" \
    "buffer_load_dwordx4 v241, %[wr], s44 offen offset:1024 nt lds\n\t" \
    "buffer_load_dwordx4 v242, %[wr], s44 offen offset:2048 nt lds\n\t" \
    "s_add_u32 m0, %[wm], 0x0\n\t" \
    "s_mov_b32 s44, 0xc0000\n\t" \
    "buffer_load_dwordx4 v240, %[wr], s44 offen offset:0 nt lds\n\t" \
    "buffer_load_dwordx4 v241, %[wr], s44 offen offset:1024 nt lds\n\t" \
    "buffer_load_dwordx4 v242, %[wr], s44 offen offset:2048 nt lds\n\t" \
    "s_add_u32 m0, %[wm], 0x3000\n\t" \
    "s_mov_b32 s44, 0x120000\n\t" \
    "buffer_load_dwordx4 v240, %[wr], s44 offen offset:0 nt lds\n\t" \
    "buffer_load_dwordx4 v241, %[wr], s44 offen offset:1024 nt lds\n\t" \
    "buffer_load_dwordx4 v242, %[wr], s44 offen offset:2048 nt lds\n\t" \
    "s_add_u32 m0, %[wm], 0x6000\n\t" \
    "s_mov_b32 s44, 0x180000\n\t" \
    "buffer_load_dwordx4 v240, %[wr], s44 offen offset:0 nt lds\n\t" \
    "buffer_load_dwordx4 v241, %[wr], s44 offen offset:1024 nt lds\n\t" \
    "buffer_load_dwordx4 v242, %[wr], s44 offen offset:2048 nt lds\n\t" \
    "v_add_u32 v244, 0xc000, v243\n\t" \
    "s_waitcnt vmcnt(12) lgkmcnt(0)\n\t" \
    "s_barrier \n\t" \
    "s_add_u32 m0, %[wm], 0x9000\n\t" \
    "s_mov_b32 s44, 0x1e0000\n\t" \
    "buffer_load_dwordx4 v240, %[wr], s44 offen offset:0 nt lds\n\t" \
    "buffer_load_dwordx4 v241, %[wr], s44 offen offset:1024 nt lds\n\t" \
    "buffer_load_dwordx4 v242, %[wr], s44 offen offset:2048 nt lds\n\t" \
    "ds_read_b64_tr_b16 v[200:201], v244 offset:0\n\t" \
    "ds_read_b64_tr_b16 v[202:203], v244 offset:1536\n\t" \
    "ds_read_b64_tr_b16 v[0:1], v244 offset:32\n\t" \
    "ds_read_b64_tr_b16 v[2:3], v244 offset:1568\n\t" \
    "ds_read_b64_tr_b16 v[8:9], v244 offset:64\n\t" \
    "ds_read_b64_tr_b16 v[10:11], v244 offset:1600\n\t" \
    "v_add_u32 v244, 0xf000, v243\n\t" \
    "s_waitcnt vmcnt(12) lgkmcnt(0)\n\t" \
    "s_barrier \n\t" \
    "s_add_u32 m0, %[wm], 0xc000\n\t" \
    "s_mov_b32 s44, 0x240000\n\t" \
    "buffer_load_dwordx4 v240, %[wr], s44 offen offset:0 nt lds\n\t" \
    "buffer_load_dwordx4 v241, %[wr], s44 offen offset:1024 nt lds\n\t" \
    "buffer_load_dwordx4 v242, %[wr], s44 offen offset:2048 nt lds\n\t" \
    "ds_read_b64_tr_b16 v[204:205], v244 offset:0\n\t" \
    "ds_read_b64_tr_b16 v[206:207], v244 offset:1536\n\t" \
    "ds_read_b64_tr_b16 v[4:5], v244 offset:32\n\t" \
    "ds_read_b64_tr_b16 v[6:7], v244 offset:1568\n\t" \
    "ds_read_b64_tr_b16 v[12:13], v244 offset:64\n\t" \
    "ds_read_b64_tr_b16 v[14:15], v244 offset:1600\n\t" \
    "v_add_u32 v244, 0x0, v243\n\t" \
    "s_waitcnt vmcnt(12) lgkmcnt(0)\n\t" \
    "s_barrier \n\t" \
    "s_add_u32 m0, %[wm], 0xf000\n\t" \
    "s_mov_b32 s44, 0x2a0000\n\t" \
    "buffer_load_dwordx4 v240, %[wr], s44 offen offset:0 nt lds\n\t" \
    "buffer_load_dwordx4 v241, %[wr], s44 offen offset:1024 nt lds\n\t" \
    "buffer_load_dwordx4 v242, %[wr], s44 offen offset:2048 nt lds\n\t" \
    "ds_read_b64_tr_b16 v[208:209], v244 offset:0\n\t" \
    "ds_read_b64_tr_b16 v[210:211], v244 offset:1536\n\t" \
    "ds_read_b64_tr_b16 v[40:41], v244 offset:32\n\t" \
    "ds_read_b64_tr_b16 v[42:43], v244 offset:1568\n\t" \
    "ds_read_b64_tr_b16 v[48:49], v244 offset:64\n\t" \
    "ds_read_b64_tr_b16 v[50:51], v244 offset:1600\n\t" \
    "v_add_u32 v244, 0x3000, v243\n\t" \
    "s_waitcnt vmcnt(12) lgkmcnt(0)\n\t" \
    "s_barrier \n\t" \
    "s_add_u32 m0, %[wm], 0x0\n\t" \
    "s_mov_b32 s44, 0x300000\n\t" \
    "buffer_load_dwordx4 v240, %[wr], s44 offen offset:0 nt lds\n\t" \
    "buffer_load_dwordx4 v241, %[wr], s44 offen offset:1024 nt lds\n\t" \
    "buffer_load_dwordx4 v242, %[wr], s44 offen offset:2048 nt lds\n\t" \
    "ds_read_b64_tr_b16 v[212:213], v244 offset:0\n\t" \
    "ds_read_b64_tr_b16 v[214:215], v244 offset:1536\n\t" \
    "ds_read_b64_tr_b16 v[44:45], v244 offset:32\n\t" \
    "ds_read_b64_tr_b16 v[46:47], v244 offset:1568\n\t" \
    "ds_read_b64_tr_b16 v[52:53], v244 offset:64\n\t" \
    "ds_read_b64_tr_b16 v[54:55], v244 offset:1600\n\t" \
    "v_add_u32 v244, 0x6000, v243\n\t" \
    "s_waitcnt vmcnt(12) lgkmcnt(0)\n\t" \
    "s_barrier \n\t" \
    "s_add_u32 m0, %[wm], 0x3000\n\t" \
    "s_mov_b32 s44, 0x360000\n\t" \
    "buffer_load_dwordx4 v240, %[wr], s44 offen offset:0 nt lds\n\t" \
    "buffer_load_dwordx4 v241, %[wr], s44 offen offset:1024 nt lds\n\t" \
    "buffer_load_dwordx4 v242, %[wr], s44 offen offset:2048 nt lds\n\t" \
    "ds_read_b64_tr_b16 v[216:217], v244 offset:0\n\t" \
    "ds_read_b64_tr_b16 v[218:219], v244 offset:1536\n\t" \
    "ds_read_b64_tr_b16 v[80:81], v244 offset:32\n\t" \
    "ds_read_b64_tr_b16 v[82:83], v244 offset:1568\n\t" \
    "ds_read_b64_tr_b16 v[88:89], v244 offset:64\n\t" \
    "ds_read_b64_tr_b16 v[90:91], v244 offset:1600\n\t" \
    "v_add_u32 v244, 0x9000, v243\n\t" \
    "s_waitcnt vmcnt(12) lgkmcnt(0)\n\t" \
    "s_barrier \n\t" \
    "s_add_u32 m0, %[wm], 0x6000\n\t" \
    "s_mov_b32 s44, 0x3c0000\n\t" \
    "buffer_load_dwordx4 v240, %[wr], s44 offen offset:0 nt lds\n\t" \
    "buffer_load_dwordx4 v241, %[wr], s44 offen offset:1024 nt lds\n\t" \
    "buffer_load_dwordx4 v242, %[wr], s44 offen offset:2048 nt lds\n\t" \
    "ds_read_b64_tr_b16 v[220:221], v244 offset:0\n\t" \
    "ds_read_b64_tr_b16 v[222:223], v244 offset:1536\n\t" \
    "ds_read_b64_tr_b16 v[84:85], v244 offset:32\n\t" \
    "ds_read_b64_tr_b16 v[86:87], v244 offset:1568\n\t" \
    "ds_read_b64_tr_b16 v[92:93], v244 offset:64\n\t" \
    "ds_read_b64_tr_b16 v[94:95], v244 offset:1600\n\t" \
    "v_add_u32 v244, 0xc000, v243\n\t" \
    "s_waitcnt vmcnt(12) lgkmcnt(0)\n\t" \
    "s_barrier \n\t" \
    "v_mov_b32 v246, 0\n\t" \
    "buffer_load_dword v246, v246, s[60:63], %[mo] offen sc1\n\t" \
    "s_add_u32 m0, %[wm], 0x9000\n\t" \
    "s_mov_b32 s44, 0x420000\n\t" \
    "buffer_load_dwordx4 v240, %[wr], s44 offen offset:0 nt lds\n\t" \
    "buffer_load_dwordx4 v241, %[wr], s44 offen offset:1024 nt lds\n\t" \
    "buffer_load_dwordx4 v242, %[wr], s44 offen offset:2048 nt lds\n\t" \
    "ds_read_b64_tr_b16 v[224:225], v244 offset:0\n\t" \
    "ds_read_b64_tr_b16 v[226:227], v244 offset:1536\n\t" \
    "ds_read_b64_tr_b16 v[120:121], v244 offset:32\n\t" \
    "ds_read_b64_tr_b16 v[122:123], v244 offset:1568\n\t" \
    "ds_read_b64_tr_b16 v[128:129], v244 offset:64\n\t" \
    "ds_read_b64_tr_b16 v[130:131], v244 offset:1600\n\t" \
    "v_add_u32 v244, 0xf000, v243\n\t" \
    "s_waitcnt vmcnt(12) lgkmcnt(0)\n\t" \
    "s_barrier \n\t" \
    "s_add_u32 m0, %[wm], 0xc000\n\t" \
    "s_mov_b32 s44, 0x480000\n\t" \
    "buffer_load_dwordx4 v240, %[wr], s44 offen offset:0 nt lds\n\t" \
    "buffer_load_dwordx4 v241, %[wr], s44 offen offset:1024 nt lds\n\t" \
    "buffer_load_dwordx4 v242, %[wr], s44 offen offset:2048 nt lds\n\t" \
    "ds_read_b64_tr_b16 v[228:229], v244 offset:0\n\t" \
    "ds_read_b64_tr_b16 v[230:231], v244 offset:1536\n\t" \
    "ds_read_b64_tr_b16 v[124:125], v244 offset:32\n\t" \
    "ds_read_b64_tr_b16 v[126:127], v244 offset:1568\n\t" \
    "ds_read_b64_tr_b16 v[132:133], v244 offset:64\n\t" \
    "ds_read_b64_tr_b16 v[134:135], v244 offset:1600\n\t" \
    "v_add_u32 v244, 0x0, v243\n\t" \
    "s_waitcnt vmcnt(12) lgkmcnt(0)\n\t" \
    "s_barrier \n\t" \
    "s_add_u32 m0, %[wm], 0xf000\n\t" \
    "s_mov_b32 s44, 0x4e0000\n\t" \
    "buffer_load_dwordx4 v240, %[wr], s44 offen offset:0 nt lds\n\t" \
    "buffer_load_dwordx4 v241, %[wr], s44 offen offset:1024 nt lds\n\t" \
    "buffer_load_dwordx4 v242, %[wr], s44 offen offset:2048 nt lds\n\t" \
    "ds_read_b64_tr_b16 v[232:233], v244 offset:0\n\t" \
    "ds_read_b64_tr_b16 v[234:235], v244 offset:1536\n\t" \
    "ds_read_b64_tr_b16 v[160:161], v244 offset:32\n\t" \
    "ds_read_b64_tr_b16 v[162:163], v244 offset:1568\n\t" \
    "ds_read_b64_tr_b16 v[168:169], v244 offset:64\n\t" \
    "ds_read_b64_tr_b16 v[170:171], v244 offset:1600\n\t" \
    "v_add_u32 v244, 0x3000, v243\n\t" \
    "s_waitcnt vmcnt(12) lgkmcnt(0)\n\t" \
    "s_barrier \n\t" \
    "s_add_u32 m0, %[wm], 0x0\n\t" \
    "s_mov_b32 s44, 0x540000\n\t" \
    "buffer_load_dwordx4 v240, %[wr], s44 offen offset:0 nt lds\n\t" \
    "buffer_load_dwordx4 v241, %[wr], s44 offen offset:1024 nt lds\n\t" \
    "buffer_load_dwordx4 v242, %[wr], s44 offen offset:2048 nt lds\n\t" \
    "ds_read_b64_tr_b16 v[236:237], v244 offset:0\n\t" \
    "ds_read_b64_tr_b16 v[238:239], v244 offset:1536\n\t" \
    "ds_read_b64_tr_b16 v[164:165], v244 offset:32\n\t" \
    "ds_read_b64_tr_b16 v[166:167], v244 offset:1568\n\t" \
    "ds_read_b64_tr_b16 v[172:173], v244 offset:64\n\t" \
    "ds_read_b64_tr_b16 v[174:175], v244 offset:1600\n\t" \
    "v_add_u32 v244, 0x6000, v243\n\t" \
    "s_waitcnt vmcnt(12) lgkmcnt(0)\n\t" \
    "s_barrier \n\t" \
    "s_add_u32 m0, %[wm], 0x3000\n\t" \
    "s_mov_b32 s44, 0x5a0000\n\t" \
    "buffer_load_dwordx4 v240, %[wr], s44 offen offset:0 nt lds\n\t" \
    "buffer_load_dwordx4 v241, %[wr], s44 offen offset:1024 nt lds\n\t" \
    "buffer_load_dwordx4 v242, %[wr], s44 offen offset:2048 nt lds\n\t" \
    "ds_read_b64_tr_b16 v[16:17], v244 offset:0\n\t" \
    "ds_read_b64_tr_b16 v[18:19], v244 offset:1536\n\t" \
    "ds_read_b64_tr_b16 v[24:25], v244 offset:32\n\t" \
    "ds_read_b64_tr_b16 v[26:27], v244 offset:1568\n\t" \
    "ds_read_b64_tr_b16 v[32:33], v244 offset:64\n\t" \
    "ds_read_b64_tr_b16 v[34:35], v244 offset:1600\n\t" \
    "v_add_u32 v244, 0x9000, v243\n\t" \
    "s_waitcnt vmcnt(12) lgkmcnt(0)\n\t" \
    "s_barrier \n\t" \
    ".Lv01_%=:\n\t" \
    "v_readfirstlane_b32 s43, v246\n\t" \
    "s_nop 0\n\t" \
    "s_cmp_lg_u32 s43, -1\n\t" \
    "s_cbranch_scc1 .Lv02_%=\n\t" \
    "s_cmp_eq_u32 s41, 0\n\t" \
    "s_cbranch_scc1 .Lv02_%=\n\t" \
    "s_sub_u32 s41, s41, 1\n\t" \
    "s_sleep 4\n\t" \
    "v_mov_b32 v246, 0\n\t" \
    "buffer_load_dword v246, v246, s[60:63], %[mo] offen sc1\n\t" \
    "s_waitcnt vmcnt(0)\n\t" \
    "s_branch .Lv01_%=\n\t" \
    ".Lv02_%=:\n\t" \
    "v_mov_b32 v246, 0x8000\n\t" \
    "s_mov_b64 exec, 1\n\t" \
    "buffer_store_dword v246, v246, s[60:63], %[mo] offen sc1\n\t" \
    "s_mov_b64 exec, -1\n\t" \
    "s_and_b32 s43, s43, 0x2000\n\t" \
    "s_xor_b32 s43, s43, %[lx]\n\t" \
    "s_add_u32 s60, s60, s43\n\t" \
    "s_addc_u32 s61, s61, 0\n\t" \
    "v_and_b32 v252, 0xf0, v253\n\t" \
    "v_lshlrev_b32 v252, 3, v252\n\t" \
    "buffer_load_dword v252, v252, s[60:63], %[fl] offen sc1\n\t" \
    "s_add_u32 m0, %[wm], 0x6000\n\t" \
    "s_mov_b32 s44, 0x600000\n\t" \
    "buffer_load_dwordx4 v240, %[wr], s44 offen offset:0 nt lds\n\t" \
    "buffer_load_dwordx4 v241, %[wr], s44 offen offset:1024 nt lds\n\t" \
    "buffer_load_dwordx4 v242, %[wr], s44 offen offset:2048 nt lds\n\t" \
    "ds_read_b64_tr_b16 v[20:21], v244 offset:0\n\t" \
    "ds_read_b64_tr_b16 v[22:23], v244 offset:1536\n\t" \
    "ds_read_b64_tr_b16 v[28:29], v244 offset:32\n\t" \
    "ds_read_b64_tr_b16 v[30:31], v244 offset:1568\n\t" \
    "ds_read_b64_tr_b16 v[36:37], v244 offset:64\n\t" \
    "ds_read_b64_tr_b16 v[38:39], v244 offset:1600\n\t" \
    "v_add_u32 v244, 0xc000, v243\n\t" \
    "s_waitcnt vmcnt(12) lgkmcnt(0)\n\t" \
    "s_barrier \n\t" \
    "s_add_u32 m0, %[wm], 0x9000\n\t" \
    "s_mov_b32 s44, 0x660000\n\t" \
    "buffer_load_dwordx4 v240, %[wr], s44 offen offset:0 nt lds\n\t" \
    "buffer_load_dwordx4 v241, %[wr], s44 offen offset:1024 nt lds\n\t" \
    "buffer_load_dwordx4 v242, %[wr], s44 offen offset:2048 nt lds\n\t" \
    "ds_read_b64_tr_b16 v[56:57], v244 offset:0\n\t" \
    "ds_read_b64_tr_b16 v[58:59], v244 offset:1536\n\t" \
    "ds_read_b64_tr_b16 v[64:65], v244 offset:32\n\t" \
    "ds_read_b64_tr_b16 v[66:67], v244 offset:1568\n\t" \
    "ds_read_b64_tr_b16 v[72:73], v244 offset:64\n\t" \
    "ds_read_b64_tr_b16 v[74:75], v244 offset:1600\n\t" \
    "v_add_u32 v244, 0xf000, v243\n\t" \
    "s_waitcnt vmcnt(12) lgkmcnt(0)\n\t" \
    "s_barrier \n\t" \
    "s_add_u32 m0, %[wm], 0xc000\n\t" \
    "s_mov_b32 s44, 0x6c0000\n\t" \
    "buffer_load_dwordx4 v240, %[wr], s44 offen offset:0 nt lds\n\t" \
    "buffer_load_dwordx4 v241, %[wr], s44 offen offset:1024 nt lds\n\t" \
    "buffer_load_dwordx4 v242, %[wr], s44 offen offset:2048 nt lds\n\t" \
    "ds_read_b64_tr_b16 v[60:61], v244 offset:0\n\t" \
    "ds_read_b64_tr_b16 v[62:63], v244 offset:1536\n\t" \
    "ds_read_b64_tr_b16 v[68:69], v244 offset:32\n\t" \
    "ds_read_b64_tr_b16 v[70:71], v244 offset:1568\n\t" \
    "ds_read_b64_tr_b16 v[76:77], v244 offset:64\n\t" \
    "ds_read_b64_tr_b16 v[78:79], v244 offset:1600\n\t" \
    "v_add_u32 v244, 0x0, v243\n\t" \
    "s_waitcnt vmcnt(12) lgkmcnt(0)\n\t" \
    "s_barrier \n\t" \
    "s_add_u32 m0, %[wm], 0xf000\n\t" \
    "s_mov_b32 s44, 0x720000\n\t" \
    "buffer_load_dwordx4 v240, %[wr], s44 offen offset:0 nt lds\n\t" \
    "buffer_load_dwordx4 v241, %[wr], s44 offen offset:1024 nt lds\n\t" \
    "buffer_load_dwordx4 v242, %[wr], s44 offen offset:2048 nt lds\n\t" \
    "ds_read_b64_tr_b16 v[96:97], v244 offset:0\n\t" \
    "ds_read_b64_tr_b16 v[98:99], v244 offset:1536\n\t" \
    "ds_read_b64_tr_b16 v[104:105], v244 offset:32\n\t" \
    "ds_read_b64_tr_b16 v[106:107], v244 offset:1568\n\t" \
    "ds_read_b64_tr_b16 v[112:113], v244 offset:64\n\t" \
    "ds_read_b64_tr_b16 v[114:115], v244 offset:1600\n\t" \
    "v_add_u32 v244, 0x3000, v243\n\t" \
    "s_waitcnt vmcnt(12) lgkmcnt(0)\n\t" \
    "s_barrier \n\t" \
    "ds_read_b64_tr_b16 v[100:101], v244 offset:0\n\t" \
    "ds_read_b64_tr_b16 v[102:103], v244 offset:1536\n\t" \
    "ds_read_b64_tr_b16 v[108:109], v244 offset:32\n\t" \
    "ds_read_b64_tr_b16 v[110:111], v244 offset:1568\n\t" \
    "ds_read_b64_tr_b16 v[116:117], v244 offset:64\n\t" \
    "ds_read_b64_tr_b16 v[118:119], v244 offset:1600\n\t" \
    "v_add_u32 v244, 0x6000, v243\n\t" \
    "s_waitcnt vmcnt(9) lgkmcnt(0)\n\t" \
    "s_barrier \n\t" \
    "ds_read_b64_tr_b16 v[136:137], v244 offset:0\n\t" \
    "ds_read_b64_tr_b16 v[138:139], v244 offset:1536\n\t" \
    "ds_read_b64_tr_b16 v[144:145], v244 offset:32\n\t" \
    "ds_read_b64_tr_b16 v[146:147], v244 offset:1568\n\t" \
    "ds_read_b64_tr_b16 v[152:153], v244 offset:64\n\t" \
    "ds_read_b64_tr_b16 v[154:155], v244 offset:1600\n\t" \
    "v_add_u32 v244, 0x9000, v243\n\t" \
    "s_waitcnt vmcnt(6) lgkmcnt(0)\n\t" \
    "s_barrier \n\t" \
    "ds_read_b64_tr_b16 v[140:141], v244 offset:0\n\t" \
    "ds_read_b64_tr_b16 v[142:143], v244 offset:1536\n\t" \
    "ds_read_b64_tr_b16 v[148:149], v244 offset:32\n\t" \
    "ds_read_b64_tr_b16 v[150:151], v244 offset:1568\n\t" \
    "ds_read_b64_tr_b16 v[156:157], v244 offset:64\n\t" \
    "ds_read_b64_tr_b16 v[158:159], v244 offset:1600\n\t" \
    "v_add_u32 v244, 0xc000, v243\n\t" \
    "s_waitcnt vmcnt(3) lgkmcnt(0)\n\t" \
    "s_barrier \n\t" \
    "ds_read_b64_tr_b16 v[176:177], v244 offset:0\n\t" \
    "ds_read_b64_tr_b16 v[178:179], v244 offset:1536\n\t" \
    "ds_read_b64_tr_b16 v[184:185], v244 offset:32\n\t" \
    "ds_read_b64_tr_b16 v[186:187], v244 offset:1568\n\t" \
    "ds_read_b64_tr_b16 v[192:193], v244 offset:64\n\t" \
    "ds_read_b64_tr_b16 v[194:195], v244 offset:1600\n\t" \
    "s_mov_b32 s43, 0\n\t" \
    "v_cmp_eq_u32 vcc, 0, v252\n\t" \
    "s_cbranch_vccnz .Lv03_%=\n\t" \
    "s_mov_b32 m0, 0x0\n\t" \
    "s_add_u32 s49, s42, 0x0\n\t" \
    "buffer_load_dwordx4 v253, s[60:63], s49 offen offset:0 sc1 lds\n\t" \
    "buffer_load_dwordx4 v253, s[60:63], s49 offen offset:1024 sc1 lds\n\t" \
    "buffer_load_dwordx4 v253, s[60:63], s49 offen offset:2048 sc1 lds\n\t" \
    "buffer_load_dwordx4 v253, s[60:63], s49 offen offset:3072 sc1 lds\n\t" \
    "s_mov_b32 m0, 0x4000\n\t" \
    "s_add_u32 s49, s42, 0x10000\n\t" \
    "buffer_load_dwordx4 v253, s[60:63], s49 offen offset:0 sc1 lds\n\t" \
    "s_mov_b32 m0, 0x5000\n\t" \
    "s_add_u32 s49, s42, 0x1000\n\t" \
    "buffer_load_dwordx4 v253, s[60:63], s49 offen offset:0 sc1 lds\n\t" \
    "buffer_load_dwordx4 v253, s[60:63], s49 offen offset:1024 sc1 lds\n\t" \
    "buffer_load_dwordx4 v253, s[60:63], s49 offen offset:2048 sc1 lds\n\t" \
    "buffer_load_dwordx4 v253, s[60:63], s49 offen offset:3072 sc1 lds\n\t" \
    "s_mov_b32 m0, 0x9000\n\t" \
    "s_add_u32 s49, s42, 0x11000\n\t" \
    "buffer_load_dwordx4 v253, s[60:63], s49 offen offset:0 sc1 lds\n\t" \
    "s_mov_b32 s43, 1\n\t" \
    ".Lv03_%=:\n\t" \
    "v_add_u32 v244, 0xf000, v243\n\t" \
    "s_waitcnt vmcnt(0) lgkmcnt(0)\n\t" \
    "s_barrier \n\t" \
    "ds_read_b64_tr_b16 v[180:181], v244 offset:0\n\t" \
    "ds_read_b64_tr_b16 v[182:183], v244 offset:1536\n\t" \
    "ds_read_b64_tr_b16 v[188:189], v244 offset:32\n\t" \
    "ds_read_b64_tr_b16 v[190:191], v244 offset:1568\n\t" \
    "ds_read_b64_tr_b16 v[196:197], v244 offset:64\n\t" \
    "ds_read_b64_tr_b16 v[198:199], v244 offset:1600\n\t" \
    "s_waitcnt lgkmcnt(0)\n\t" \
    "s_barrier \n\t" \
    LST0 \
    "ds_write_b128 v254, v[200:203] offset:30720\n\t" \
    "ds_write_b128 v254, v[204:207] offset:31744\n\t" \
    "ds_write_b128 v254, v[208:211] offset:32768\n\t" \
    "ds_write_b128 v254, v[212:215] offset:33792\n\t" \
    "ds_write_b128 v254, v[216:219] offset:34816\n\t" \
    "ds_write_b128 v254, v[220:223] offset:35840\n\t" \
    "ds_write_b128 v254, v[224:227] offset:36864\n\t" \
    "ds_write_b128 v254, v[228:231] offset:37888\n\t" \
    "ds_write_b128 v254, v[232:235] offset:38912\n\t" \
    "ds_write_b128 v254, v[236:239] offset:39936\n\t" \
    "s_cmp_lg_u32 s43, 0\n\t" \
    "s_cbranch_scc1 .Lv04_%=\n\t" \
    "s_mov_b32 s44, 0\n\t" \
    ".Lv05_%=:\n\t" \
    "v_cmp_ge_u32 vcc, s44, v252\n\t" \
    "s_cbranch_vccz .Lv06_%=\n\t" \
    "s_cmp_eq_u32 s41, 0\n\t" \
    "s_cbranch_scc1 .Lv06_%=\n\t" \
    "s_sub_u32 s41, s41, 1\n\t" \
    "s_sleep 8\n\t" \
    "v_and_b32 v252, 0xf0, v253\n\t" \
    "v_lshlrev_b32 v252, 3, v252\n\t" \
    "buffer_load_dword v252, v252, s[60:63], %[fl] offen sc1\n\t" \
    "s_waitcnt vmcnt(0)\n\t" \
    "s_branch .Lv05_%=\n\t" \
    ".Lv06_%=:\n\t" \
    "s_mov_b32 m0, 0x0\n\t" \
    "s_add_u32 s49, s42, 0x0\n\t" \
    "buffer_load_dwordx4 v253, s[60:63], s49 offen offset:0 sc1 lds\n\t" \
    "buffer_load_dwordx4 v253, s[60:63], s49 offen offset:1024 sc1 lds\n\t" \
    "buffer_load_dwordx4 v253, s[60:63], s49 offen offset:2048 sc1 lds\n\t" \
    "buffer_load_dwordx4 v253, s[60:63], s49 offen offset:3072 sc1 lds\n\t" \
    "s_mov_b32 m0, 0x4000\n\t" \
    "s_add_u32 s49, s42, 0x10000\n\t" \
    "buffer_load_dwordx4 v253, s[60:63], s49 offen offset:0 sc1 lds\n\t" \
    "s_mov_b32 m0, 0x5000\n\t" \
    "s_add_u32 s49, s42, 0x1000\n\t" \
    "buffer_load_dwordx4 v253, s[60:63], s49 offen offset:0 sc1 lds\n\t" \
    "buffer_load_dwordx4 v253, s[60:63], s49 offen offset:1024 sc1 lds\n\t" \
    "buffer_load_dwordx4 v253, s[60:63], s49 offen offset:2048 sc1 lds\n\t" \
    "buffer_load_dwordx4 v253, s[60:63], s49 offen offset:3072 sc1 lds\n\t" \
    "s_mov_b32 m0, 0x9000\n\t" \
    "s_add_u32 s49, s42, 0x11000\n\t" \
    "buffer_load_dwordx4 v253, s[60:63], s49 offen offset:0 sc1 lds\n\t" \
    ".Lv04_%=:\n\t" \
    "s_waitcnt vmcnt(0) lgkmcnt(0)\n\t" \
    "s_barrier \n\t" \
    "ds_read_b128 v[224:227], v253 offset:0\n\t" \
    "ds_read_b128 v[244:247], v254 offset:30720\n\t" \
    "ds_read_b128 v[228:231], v253 offset:1024\n\t" \
    "ds_read_b128 v[232:235], v253 offset:20480\n\t" \
    "ds_read_b128 v[236:239], v253 offset:21504\n\t" \
    "ds_read_b128 v[248:251], v254 offset:31744\n\t" \
    ".Lv07_%=:\n\t" \
    LST7 \
    "s_waitcnt lgkmcnt(5)\n\t" \
    "v_mfma_f32_16x16x32_bf16 v[204:207], v[0:3], v[224:227], 0\n\t" \
    "s_waitcnt lgkmcnt(3)\n\t" \
    "v_mfma_f32_16x16x32_bf16 v[204:207], v[4:7], v[228:231], v[204:207]\n\t" \
    "ds_read_b128 v[240:243], v253 offset:10240\n\t" \
    "v_mfma_f32_16x16x32_bf16 v[208:211], v[8:11], v[224:227], 0\n\t" \
    "v_mfma_f32_16x16x32_bf16 v[208:211], v[12:15], v[228:231], v[208:211]\n\t" \
    "v_mfma_f32_16x16x32_bf16 v[200:203], v[244:247], v[224:227], 0\n\t" \
    "s_waitcnt lgkmcnt(1)\n\t" \
    "v_mfma_f32_16x16x32_bf16 v[200:203], v[248:251], v[228:231], v[200:203]\n\t" \
    "v_mfma_f32_16x16x32_bf16 v[216:219], v[0:3], v[232:235], 0\n\t" \
    "ds_read_b128 v[224:227], v253 offset:11264\n\t" \
    "v_mfma_f32_16x16x32_bf16 v[216:219], v[4:7], v[236:239], v[216:219]\n\t" \
    "ds_read_b128 v[228:231], v253 offset:2048\n\t" \
    "v_mfma_f32_16x16x32_bf16 v[220:223], v[8:11], v[232:235], 0\n\t" \
    "v_mfma_f32_16x16x32_bf16 v[220:223], v[12:15], v[236:239], v[220:223]\n\t" \
    "v_mfma_f32_16x16x32_bf16 v[212:215], v[244:247], v[232:235], 0\n\t" \
    "ds_read_b128 v[244:247], v254 offset:32768\n\t" \
    "v_mfma_f32_16x16x32_bf16 v[212:215], v[248:251], v[236:239], v[212:215]\n\t" \
    "ds_read_b128 v[248:251], v254 offset:33792\n\t" \
    "s_waitcnt lgkmcnt(4)\n\t" \
    "v_mfma_f32_16x16x32_bf16 v[204:207], v[24:27], v[240:243], v[204:207]\n\t" \
    "ds_read_b128 v[232:235], v253 offset:3072\n\t" \
    "s_waitcnt lgkmcnt(4)\n\t" \
    "v_mfma_f32_16x16x32_bf16 v[204:207], v[28:31], v[224:227], v[204:207]\n\t" \
    "ds_read_b128 v[236:239], v253 offset:22528\n\t" \
    "v_mfma_f32_16x16x32_bf16 v[208:211], v[32:35], v[240:243], v[208:211]\n\t" \
    "v_mfma_f32_16x16x32_bf16 v[208:211], v[36:39], v[224:227], v[208:211]\n\t" \
    "v_mfma_f32_16x16x32_bf16 v[200:203], v[16:19], v[240:243], v[200:203]\n\t" \
    "v_mfma_f32_16x16x32_bf16 v[200:203], v[20:23], v[224:227], v[200:203]\n\t" \
    "s_waitcnt lgkmcnt(4)\n\t" \
    "v_mfma_f32_16x16x32_bf16 v[204:207], v[40:43], v[228:231], v[204:207]\n\t" \
    "ds_read_b128 v[240:243], v253 offset:23552\n\t" \
    "s_waitcnt lgkmcnt(2)\n\t" \
    "v_mfma_f32_16x16x32_bf16 v[204:207], v[44:47], v[232:235], v[204:207]\n\t" \
    "ds_read_b128 v[224:227], v253 offset:12288\n\t" \
    "v_mfma_f32_16x16x32_bf16 v[208:211], v[48:51], v[228:231], v[208:211]\n\t" \
    "v_mfma_f32_16x16x32_bf16 v[208:211], v[52:55], v[232:235], v[208:211]\n\t" \
    "v_mfma_f32_16x16x32_bf16 v[200:203], v[244:247], v[228:231], v[200:203]\n\t" \
    "v_mfma_f32_16x16x32_bf16 v[200:203], v[248:251], v[232:235], v[200:203]\n\t" \
    "s_waitcnt lgkmcnt(2)\n\t" \
    "v_mfma_f32_16x16x32_bf16 v[216:219], v[40:43], v[236:239], v[216:219]\n\t" \
    "ds_read_b128 v[228:231], v253 offset:13312\n\t" \
    "s_add_u32 s44, s40, 1\n\t" \
    "s_waitcnt lgkmcnt(2)\n\t" \
    "v_mfma_f32_16x16x32_bf16 v[216:219], v[44:47], v[240:243], v[216:219]\n\t" \
    "ds_read_b128 v[232:235], v253 offset:4096\n\t" \
    "s_and_b32 s45, s44, 15\n\t" \
    "v_mfma_f32_16x16x32_bf16 v[220:223], v[48:51], v[236:239], v[220:223]\n\t" \
    "s_cmp_eq_u32 s45, 0\n\t" \
    "v_mfma_f32_16x16x32_bf16 v[220:223], v[52:55], v[240:243], v[220:223]\n\t" \
    "s_cselect_b32 s45, %[cj], %[cs]\n\t" \
    "v_mfma_f32_16x16x32_bf16 v[212:215], v[244:247], v[236:239], v[212:215]\n\t" \
    "ds_read_b128 v[244:247], v254 offset:34816\n\t" \
    "s_cmp_eq_u32 s44, 64\n\t" \
    "v_mfma_f32_16x16x32_bf16 v[212:215], v[248:251], v[240:243], v[212:215]\n\t" \
    "ds_read_b128 v[248:251], v254 offset:35840\n\t" \
    "s_cselect_b32 s45, 0, s45\n\t" \
    "s_waitcnt lgkmcnt(4)\n\t" \
    "v_mfma_f32_16x16x32_bf16 v[204:207], v[64:67], v[224:227], v[204:207]\n\t" \
    "ds_read_b128 v[236:239], v253 offset:5120\n\t" \
    "s_add_u32 s42, s42, s45\n\t" \
    "s_waitcnt lgkmcnt(4)\n\t" \
    "v_mfma_f32_16x16x32_bf16 v[204:207], v[68:71], v[228:231], v[204:207]\n\t" \
    "ds_read_b128 v[240:243], v253 offset:24576\n\t" \
    "v_mfma_f32_16x16x32_bf16 v[208:211], v[72:75], v[224:227], v[208:211]\n\t" \
    "v_mfma_f32_16x16x32_bf16 v[208:211], v[76:79], v[228:231], v[208:211]\n\t" \
    "v_mfma_f32_16x16x32_bf16 v[200:203], v[56:59], v[224:227], v[200:203]\n\t" \
    "v_mfma_f32_16x16x32_bf16 v[200:203], v[60:63], v[228:231], v[200:203]\n\t" \
    "s_waitcnt lgkmcnt(4)\n\t" \
    "v_mfma_f32_16x16x32_bf16 v[204:207], v[80:83], v[232:235], v[204:207]\n\t" \
    "ds_read_b128 v[224:227], v253 offset:25600\n\t" \
    "s_waitcnt lgkmcnt(2)\n\t" \
    "v_mfma_f32_16x16x32_bf16 v[204:207], v[84:87], v[236:239], v[204:207]\n\t" \
    "ds_read_b128 v[228:231], v253 offset:14336\n\t" \
    "v_mfma_f32_16x16x32_bf16 v[208:211], v[88:91], v[232:235], v[208:211]\n\t" \
    "v_mfma_f32_16x16x32_bf16 v[208:211], v[92:95], v[236:239], v[208:211]\n\t" \
    "v_mfma_f32_16x16x32_bf16 v[200:203], v[244:247], v[232:235], v[200:203]\n\t" \
    "v_mfma_f32_16x16x32_bf16 v[200:203], v[248:251], v[236:239], v[200:203]\n\t" \
    "s_waitcnt lgkmcnt(2)\n\t" \
    "v_mfma_f32_16x16x32_bf16 v[216:219], v[80:83], v[240:243], v[216:219]\n\t" \
    "ds_read_b128 v[232:235], v253 offset:15360\n\t" \
    "s_and_b32 s44, s40, 3\n\t" \
    "s_waitcnt lgkmcnt(2)\n\t" \
    "v_mfma_f32_16x16x32_bf16 v[216:219], v[84:87], v[224:227], v[216:219]\n\t" \
    "ds_read_b128 v[236:239], v253 offset:6144\n\t" \
    "s_lshl_b32 s44, s44, 17\n\t" \
    "v_mfma_f32_16x16x32_bf16 v[220:223], v[88:91], v[240:243], v[220:223]\n\t" \
    "s_add_u32 s44, s44, %[mb]\n\t" \
    "v_mfma_f32_16x16x32_bf16 v[220:223], v[92:95], v[224:227], v[220:223]\n\t" \
    "s_add_u32 s46, s44, 0x0\n\t" \
    "v_mfma_f32_16x16x32_bf16 v[212:215], v[244:247], v[240:243], v[212:215]\n\t" \
    "ds_read_b128 v[244:247], v254 offset:36864\n\t" \
    "s_add_u32 s47, s44, 0x1400\n\t" \
    "v_mfma_f32_16x16x32_bf16 v[212:215], v[248:251], v[224:227], v[212:215]\n\t" \
    "ds_read_b128 v[248:251], v254 offset:37888\n\t" \
    "s_add_u32 s48, s44, 0x400\n\t" \
    "s_waitcnt lgkmcnt(4)\n\t" \
    "v_mfma_f32_16x16x32_bf16 v[204:207], v[104:107], v[228:231], v[204:207]\n\t" \
    "ds_read_b128 v[240:243], v253 offset:7168\n\t" \
    "s_waitcnt lgkmcnt(4)\n\t" \
    "v_mfma_f32_16x16x32_bf16 v[204:207], v[108:111], v[232:235], v[204:207]\n\t" \
    "ds_read_b128 v[224:227], v253 offset:26624\n\t" \
    "v_mfma_f32_16x16x32_bf16 v[208:211], v[112:115], v[228:231], v[208:211]\n\t" \
    "v_mfma_f32_16x16x32_bf16 v[208:211], v[116:119], v[232:235], v[208:211]\n\t" \
    "v_mfma_f32_16x16x32_bf16 v[200:203], v[96:99], v[228:231], v[200:203]\n\t" \
    "v_mfma_f32_16x16x32_bf16 v[200:203], v[100:103], v[232:235], v[200:203]\n\t" \
    "s_waitcnt lgkmcnt(4)\n\t" \
    "v_mfma_f32_16x16x32_bf16 v[204:207], v[120:123], v[236:239], v[204:207]\n\t" \
    "ds_read_b128 v[228:231], v253 offset:27648\n\t" \
    "s_waitcnt lgkmcnt(2)\n\t" \
    "v_mfma_f32_16x16x32_bf16 v[204:207], v[124:127], v[240:243], v[204:207]\n\t" \
    "ds_read_b128 v[232:235], v253 offset:16384\n\t" \
    "v_mfma_f32_16x16x32_bf16 v[208:211], v[128:131], v[236:239], v[208:211]\n\t" \
    "v_mfma_f32_16x16x32_bf16 v[208:211], v[132:135], v[240:243], v[208:211]\n\t" \
    "v_mfma_f32_16x16x32_bf16 v[200:203], v[244:247], v[236:239], v[200:203]\n\t" \
    "v_mfma_f32_16x16x32_bf16 v[200:203], v[248:251], v[240:243], v[200:203]\n\t" \
    "s_waitcnt lgkmcnt(2)\n\t" \
    "v_mfma_f32_16x16x32_bf16 v[216:219], v[120:123], v[224:227], v[216:219]\n\t" \
    "ds_read_b128 v[236:239], v253 offset:17408\n\t" \
    "s_waitcnt lgkmcnt(2)\n\t" \
    "v_mfma_f32_16x16x32_bf16 v[216:219], v[124:127], v[228:231], v[216:219]\n\t" \
    "ds_read_b128 v[240:243], v253 offset:8192\n\t" \
    "v_mfma_f32_16x16x32_bf16 v[220:223], v[128:131], v[224:227], v[220:223]\n\t" \
    "v_mfma_f32_16x16x32_bf16 v[220:223], v[132:135], v[228:231], v[220:223]\n\t" \
    "v_mfma_f32_16x16x32_bf16 v[212:215], v[244:247], v[224:227], v[212:215]\n\t" \
    "ds_read_b128 v[244:247], v254 offset:38912\n\t" \
    "v_mfma_f32_16x16x32_bf16 v[212:215], v[248:251], v[228:231], v[212:215]\n\t" \
    "ds_read_b128 v[248:251], v254 offset:39936\n\t" \
    "v_and_b32 v252, 0xf0, v253\n\t" \
    "v_lshlrev_b32 v252, 3, v252\n\t" \
    "buffer_load_dword v252, v252, s[60:63], %[fl] offen sc1\n\t" \
    "s_waitcnt lgkmcnt(4)\n\t" \
    "v_mfma_f32_16x16x32_bf16 v[204:207], v[144:147], v[232:235], v[204:207]\n\t" \
    "ds_read_b128 v[224:227], v253 offset:9216\n\t" \
    "s_waitcnt lgkmcnt(4)\n\t" \
    "v_mfma_f32_16x16x32_bf16 v[204:207], v[148:151], v[236:239], v[204:207]\n\t" \
    "ds_read_b128 v[228:231], v253 offset:28672\n\t" \
    "v_mfma_f32_16x16x32_bf16 v[208:211], v[152:155], v[232:235], v[208:211]\n\t" \
    "v_mfma_f32_16x16x32_bf16 v[208:211], v[156:159], v[236:239], v[208:211]\n\t" \
    "v_mfma_f32_16x16x32_bf16 v[200:203], v[136:139], v[232:235], v[200:203]\n\t" \
    "v_mfma_f32_16x16x32_bf16 v[200:203], v[140:143], v[236:239], v[200:203]\n\t" \
    "s_waitcnt lgkmcnt(4)\n\t" \
    "v_mfma_f32_16x16x32_bf16 v[204:207], v[160:163], v[240:243], v[204:207]\n\t" \
    "ds_read_b128 v[232:235], v253 offset:29696\n\t" \
    "s_waitcnt lgkmcnt(2)\n\t" \
    "v_mfma_f32_16x16x32_bf16 v[204:207], v[164:167], v[224:227], v[204:207]\n\t" \
    "ds_read_b128 v[236:239], v253 offset:18432\n\t" \
    "v_mfma_f32_16x16x32_bf16 v[208:211], v[168:171], v[240:243], v[208:211]\n\t" \
    "v_mfma_f32_16x16x32_bf16 v[208:211], v[172:175], v[224:227], v[208:211]\n\t" \
    "v_mfma_f32_16x16x32_bf16 v[200:203], v[244:247], v[240:243], v[200:203]\n\t" \
    "v_mfma_f32_16x16x32_bf16 v[200:203], v[248:251], v[224:227], v[200:203]\n\t" \
    "s_waitcnt lgkmcnt(2)\n\t" \
    "v_mfma_f32_16x16x32_bf16 v[216:219], v[160:163], v[228:231], v[216:219]\n\t" \
    "ds_read_b128 v[240:243], v253 offset:19456\n\t" \
    "s_waitcnt lgkmcnt(2)\n\t" \
    "v_mfma_f32_16x16x32_bf16 v[216:219], v[164:167], v[232:235], v[216:219]\n\t" \
    "ds_read_b128 v[224:227], v253 offset:30720\n\t" \
    "v_mfma_f32_16x16x32_bf16 v[220:223], v[168:171], v[228:231], v[220:223]\n\t" \
    "v_mfma_f32_16x16x32_bf16 v[220:223], v[172:175], v[232:235], v[220:223]\n\t" \
    "v_mfma_f32_16x16x32_bf16 v[212:215], v[244:247], v[228:231], v[212:215]\n\t" \
    "v_mfma_f32_16x16x32_bf16 v[212:215], v[248:251], v[232:235], v[212:215]\n\t" \
    "s_waitcnt lgkmcnt(2)\n\t" \
    "v_mfma_f32_16x16x32_bf16 v[204:207], v[184:187], v[236:239], v[204:207]\n\t" \
    "ds_read_b128 v[228:231], v253 offset:31744\n\t" \
    "s_waitcnt lgkmcnt(2)\n\t" \
    "v_mfma_f32_16x16x32_bf16 v[204:207], v[188:191], v[240:243], v[204:207]\n\t" \
    "ds_read_b128 v[232:235], v253 offset:32768\n\t" \
    "v_mfma_f32_16x16x32_bf16 v[208:211], v[192:195], v[236:239], v[208:211]\n\t" \
    "v_mfma_f32_16x16x32_bf16 v[208:211], v[196:199], v[240:243], v[208:211]\n\t" \
    "v_mfma_f32_16x16x32_bf16 v[200:203], v[176:179], v[236:239], v[200:203]\n\t" \
    "v_mfma_f32_16x16x32_bf16 v[200:203], v[180:183], v[240:243], v[200:203]\n\t" \
    "s_waitcnt lgkmcnt(2)\n\t" \
    "v_mfma_f32_16x16x32_bf16 v[216:219], v[24:27], v[224:227], v[216:219]\n\t" \
    "ds_read_b128 v[236:239], v253 offset:33792\n\t" \
    "s_waitcnt lgkmcnt(2)\n\t" \
    "v_mfma_f32_16x16x32_bf16 v[216:219], v[28:31], v[228:231], v[216:219]\n\t" \
    "ds_read_b128 v[240:243], v253 offset:34816\n\t" \
    "v_mfma_f32_16x16x32_bf16 v[220:223], v[32:35], v[224:227], v[220:223]\n\t" \
    "v_cvt_pk_bf16_f32 v204, v204, v205\n\t" \
    "v_mfma_f32_16x16x32_bf16 v[220:223], v[36:39], v[228:231], v[220:223]\n\t" \
    "v_cvt_pk_bf16_f32 v205, v206, v207\n\t" \
    "v_mfma_f32_16x16x32_bf16 v[212:215], v[16:19], v[224:227], v[212:215]\n\t" \
    "ds_write_b64 v254, v[204:205] offset:7168\n\t" \
    "v_mfma_f32_16x16x32_bf16 v[212:215], v[20:23], v[228:231], v[212:215]\n\t" \
    "v_cvt_pk_bf16_f32 v208, v208, v209\n\t" \
    "s_waitcnt lgkmcnt(3)\n\t" \
    "v_mfma_f32_16x16x32_bf16 v[216:219], v[64:67], v[232:235], v[216:219]\n\t" \
    "ds_read_b128 v[224:227], v253 offset:35840\n\t" \
    "v_cvt_pk_bf16_f32 v209, v210, v211\n\t" \
    "s_waitcnt lgkmcnt(3)\n\t" \
    "v_mfma_f32_16x16x32_bf16 v[216:219], v[68:71], v[236:239], v[216:219]\n\t" \
    "ds_read_b128 v[228:231], v253 offset:36864\n\t" \
    "ds_write_b64 v254, v[208:209] offset:8192\n\t" \
    "v_mfma_f32_16x16x32_bf16 v[220:223], v[72:75], v[232:235], v[220:223]\n\t" \
    "v_cvt_pk_bf16_f32 v200, v200, v201\n\t" \
    "v_mfma_f32_16x16x32_bf16 v[220:223], v[76:79], v[236:239], v[220:223]\n\t" \
    "v_cvt_pk_bf16_f32 v201, v202, v203\n\t" \
    "v_mfma_f32_16x16x32_bf16 v[212:215], v[56:59], v[232:235], v[212:215]\n\t" \
    "ds_write_b64 v254, v[200:201] offset:6144\n\t" \
    "v_mfma_f32_16x16x32_bf16 v[212:215], v[60:63], v[236:239], v[212:215]\n\t" \
    "s_waitcnt lgkmcnt(5)\n\t" \
    "v_mfma_f32_16x16x32_bf16 v[216:219], v[104:107], v[240:243], v[216:219]\n\t" \
    "ds_read_b128 v[232:235], v253 offset:37888\n\t" \
    "s_waitcnt lgkmcnt(4)\n\t" \
    "v_mfma_f32_16x16x32_bf16 v[216:219], v[108:111], v[224:227], v[216:219]\n\t" \
    "ds_read_b128 v[236:239], v253 offset:38912\n\t" \
    "v_mfma_f32_16x16x32_bf16 v[220:223], v[112:115], v[240:243], v[220:223]\n\t" \
    "v_mfma_f32_16x16x32_bf16 v[220:223], v[116:119], v[224:227], v[220:223]\n\t" \
    "v_mfma_f32_16x16x32_bf16 v[212:215], v[96:99], v[240:243], v[212:215]\n\t" \
    "v_mfma_f32_16x16x32_bf16 v[212:215], v[100:103], v[224:227], v[212:215]\n\t" \
    "ds_read_b128 v[240:243], v253 offset:39936\n\t" \
    "s_waitcnt lgkmcnt(3)\n\t" \
    "s_barrier \n\t" \
    "v_mfma_f32_16x16x32_bf16 v[216:219], v[144:147], v[228:231], v[216:219]\n\t" \
    "s_waitcnt lgkmcnt(2)\n\t" \
    "v_mfma_f32_16x16x32_bf16 v[216:219], v[148:151], v[232:235], v[216:219]\n\t" \
    "s_waitcnt lgkmcnt(1)\n\t" \
    "v_mfma_f32_16x16x32_bf16 v[216:219], v[184:187], v[236:239], v[216:219]\n\t" \
    "s_waitcnt lgkmcnt(0)\n\t" \
    "v_mfma_f32_16x16x32_bf16 v[216:219], v[188:191], v[240:243], v[216:219]\n\t" \
    "v_mfma_f32_16x16x32_bf16 v[220:223], v[152:155], v[228:231], v[220:223]\n\t" \
    "v_mfma_f32_16x16x32_bf16 v[220:223], v[156:159], v[232:235], v[220:223]\n\t" \
    "v_mfma_f32_16x16x32_bf16 v[220:223], v[192:195], v[236:239], v[220:223]\n\t" \
    "v_cvt_pk_bf16_f32 v216, v216, v217\n\t" \
    "v_cvt_pk_bf16_f32 v217, v218, v219\n\t" \
    "v_mfma_f32_16x16x32_bf16 v[220:223], v[196:199], v[240:243], v[220:223]\n\t" \
    "ds_write_b64 v254, v[216:217] offset:13312\n\t" \
    "v_mfma_f32_16x16x32_bf16 v[212:215], v[136:139], v[228:231], v[212:215]\n\t" \
    "v_mfma_f32_16x16x32_bf16 v[212:215], v[140:143], v[232:235], v[212:215]\n\t" \
    "v_mfma_f32_16x16x32_bf16 v[212:215], v[176:179], v[236:239], v[212:215]\n\t" \
    "v_cvt_pk_bf16_f32 v220, v220, v221\n\t" \
    "v_cvt_pk_bf16_f32 v221, v222, v223\n\t" \
    "v_mfma_f32_16x16x32_bf16 v[212:215], v[180:183], v[240:243], v[212:215]\n\t" \
    "ds_write_b64 v254, v[220:221] offset:14336\n\t" \
    "s_nop 6\n\t" \
    "v_cvt_pk_bf16_f32 v212, v212, v213\n\t" \
    "v_cvt_pk_bf16_f32 v213, v214, v215\n\t" \
    "ds_write_b64 v254, v[212:213] offset:12288\n\t" \
    LST6 \
    "s_waitcnt vmcnt(0) lgkmcnt(0)\n\t" \
    "s_barrier \n\t" \
    "s_setprio 2\n\t" \
    "s_add_u32 s44, s40, 1\n\t" \
    "s_min_u32 s44, s44, 63\n\t" \
    ".Lv08_%=:\n\t" \
    "v_cmp_ge_u32 vcc, s44, v252\n\t" \
    "s_cbranch_vccz .Lv09_%=\n\t" \
    "s_cmp_eq_u32 s41, 0\n\t" \
    "s_cbranch_scc1 .Lv09_%=\n\t" \
    "s_sub_u32 s41, s41, 1\n\t" \
    "s_sleep 2\n\t" \
    "v_and_b32 v252, 0xf0, v253\n\t" \
    "v_lshlrev_b32 v252, 3, v252\n\t" \
    "buffer_load_dword v252, v252, s[60:63], %[fl] offen sc1\n\t" \
    "s_waitcnt vmcnt(0)\n\t" \
    "s_branch .Lv08_%=\n\t" \
    ".Lv09_%=:\n\t" \
    "s_mov_b32 m0, 0x0\n\t" \
    "s_add_u32 s49, s42, 0x0\n\t" \
    "buffer_load_dwordx4 v253, s[60:63], s49 offen offset:0 sc1 lds\n\t" \
    "s_sleep 1\n\t" \
    "buffer_load_dwordx4 v253, s[60:63], s49 offen offset:1024 sc1 lds\n\t" \
    "s_sleep 1\n\t" \
    "buffer_load_dwordx4 v253, s[60:63], s49 offen offset:2048 sc1 lds\n\t" \
    "s_sleep 1\n\t" \
    "buffer_load_dwordx4 v253, s[60:63], s49 offen offset:3072 sc1 lds\n\t" \
    "s_sleep 1\n\t" \
    "s_mov_b32 m0, 0x4000\n\t" \
    "s_add_u32 s49, s42, 0x10000\n\t" \
    "buffer_load_dwordx4 v253, s[60:63], s49 offen offset:0 sc1 lds\n\t" \
    "s_sleep 1\n\t" \
    "s_mov_b32 m0, 0x5000\n\t" \
    "s_add_u32 s49, s42, 0x1000\n\t" \
    "buffer_load_dwordx4 v253, s[60:63], s49 offen offset:0 sc1 lds\n\t" \
    "s_sleep 1\n\t" \
    "buffer_load_dwordx4 v253, s[60:63], s49 offen offset:1024 sc1 lds\n\t" \
    "s_sleep 1\n\t" \
    "buffer_load_dwordx4 v253, s[60:63], s49 offen offset:2048 sc1 lds\n\t" \
    "s_sleep 1\n\t" \
    "buffer_load_dwordx4 v253, s[60:63], s49 offen offset:3072 sc1 lds\n\t" \
    "s_sleep 1\n\t" \
    "s_mov_b32 m0, 0x9000\n\t" \
    "s_add_u32 s49, s42, 0x11000\n\t" \
    "buffer_load_dwordx4 v253, s[60:63], s49 offen offset:0 sc1 lds\n\t" \
    "s_sleep 1\n\t" \
    "ds_read_b128 v[200:203], v254 offset:30720\n\t" \
    "ds_read_b128 v[204:207], v254 offset:31744\n\t" \
    "ds_read_b128 v[216:219], v254 offset:32768\n\t" \
    "ds_read_b128 v[220:223], v254 offset:33792\n\t" \
    "v_mov_b64 v[208:209], 0\n\t" \
    "v_mov_b64 v[210:211], 0\n\t" \
    "v_mov_b64 v[244:245], 0\n\t" \
    "v_mov_b64 v[246:247], 0\n\t" \
    "v_mov_b64 v[248:249], 0\n\t" \
    "v_mov_b64 v[250:251], 0\n\t" \
    LST1 \
    "s_sleep 5\n\t" \
    "s_mov_b32 m0, 0xa000\n\t" \
    "s_nop 0\n\t" \
    "buffer_load_dwordx4 v253, s[60:63], s46 offen sc1 lds\n\t" \
    "s_mov_b32 m0, 0xb400\n\t" \
    "s_nop 0\n\t" \
    "buffer_load_dwordx4 v253, s[60:63], s47 offen sc1 lds\n\t" \
    "s_mov_b32 m0, 0xa400\n\t" \
    "s_nop 0\n\t" \
    "buffer_load_dwordx4 v253, s[60:63], s48 offen sc1 lds\n\t" \
    "s_waitcnt lgkmcnt(0)\n\t" \
    LFLUSH212 \
    "v_bfe_i32 v212, v253, 5, 1\n\t" \
    "v_and_b32 v212, 0xaaaa, v212\n\t" \
    "v_or_b32 v212, 0x4444, v212\n\t" \
    "v_and_b32 v213, 0xffff, v255\n\t" \
    "v_lshrrev_b32 v214, 16, v255\n\t" \
    ".Lv010_%=:\n\t" \
    "s_waitcnt vmcnt(0)\n\t" \
    "ds_read_b128 v[224:227], v253 offset:40960\n\t" \
    "ds_read_b128 v[228:231], v253 offset:46080\n\t" \
    "ds_read_b128 v[232:235], v253 offset:41984\n\t" \
    "s_waitcnt lgkmcnt(2)\n\t" \
    "v_max_u32 v215, v224, v225\n\t" \
    "v_max3_u32 v215, v226, v227, v215\n\t" \
    "s_waitcnt lgkmcnt(1)\n\t" \
    "v_max3_u32 v215, v228, v229, v215\n\t" \
    "v_max3_u32 v215, v230, v231, v215\n\t" \
    "s_waitcnt lgkmcnt(0)\n\t" \
    "v_max3_u32 v215, v232, v233, v215\n\t" \
    "v_max3_u32 v215, v234, v235, v215\n\t" \
    "v_cmp_eq_u32 vcc, -1, v215\n\t" \
    "s_cbranch_vccz .Lv011_%=\n\t" \
    "v_max_u32 v215, v224, v225\n\t" \
    "v_max3_u32 v215, v226, v227, v215\n\t" \
    "v_cmp_eq_u32 vcc, -1, v215\n\t" \
    "s_cbranch_vccz .Lv012_%=\n\t" \
    "s_mov_b32 m0, 0xa000\n\t" \
    "s_nop 0\n\t" \
    "buffer_load_dwordx4 v253, s[60:63], s46 offen sc1 lds\n\t" \
    ".Lv012_%=:\n\t" \
    "v_max_u32 v215, v228, v229\n\t" \
    "v_max3_u32 v215, v230, v231, v215\n\t" \
    "v_cmp_eq_u32 vcc, -1, v215\n\t" \
    "s_cbranch_vccz .Lv013_%=\n\t" \
    "s_mov_b32 m0, 0xb400\n\t" \
    "s_nop 0\n\t" \
    "buffer_load_dwordx4 v253, s[60:63], s47 offen sc1 lds\n\t" \
    ".Lv013_%=:\n\t" \
    "v_max_u32 v215, v232, v233\n\t" \
    "v_max3_u32 v215, v234, v235, v215\n\t" \
    "v_cmp_eq_u32 vcc, -1, v215\n\t" \
    "s_cbranch_vccz .Lv014_%=\n\t" \
    "s_mov_b32 m0, 0xa400\n\t" \
    "s_nop 0\n\t" \
    "buffer_load_dwordx4 v253, s[60:63], s48 offen sc1 lds\n\t" \
    ".Lv014_%=:\n\t" \
    "s_cmp_eq_u32 s41, 0\n\t" \
    "s_cbranch_scc1 .Lv011_%=\n\t" \
    "s_sub_u32 s41, s41, 1\n\t" \
    "s_branch .Lv010_%=\n\t" \
    ".Lv011_%=:\n\t" \
    LST2 \
    "s_waitcnt lgkmcnt(0)\n\t" \
    "s_barrier \n\t" \
    LST3 \
    "ds_read_b128 v[236:239], v253 offset:47104\n\t" \
    "ds_read_b128 v[240:243], v253 offset:43008\n\t" \
    "v_smfmac_f32_16x16x64_bf16 v[244:247], v[224:227], v[0:7], v212\n\t" \
    "v_smfmac_f32_16x16x64_bf16 v[248:251], v[224:227], v[8:15], v212\n\t" \
    "v_smfmac_f32_16x16x64_bf16 v[208:211], v[224:227], v[200:207], v212\n\t" \
    "ds_read_b128 v[200:203], v254 offset:34816\n\t" \
    "ds_read_b128 v[204:207], v254 offset:35840\n\t" \
    "ds_read_b128 v[224:227], v253 offset:48128\n\t" \
    "v_smfmac_f32_16x16x64_bf16 v[208:211], v[228:231], v[16:23], v212\n\t" \
    "v_smfmac_f32_16x16x64_bf16 v[244:247], v[228:231], v[24:31], v212\n\t" \
    "v_smfmac_f32_16x16x64_bf16 v[248:251], v[228:231], v[32:39], v212\n\t" \
    "ds_read_b128 v[228:231], v253 offset:44032\n\t" \
    "v_smfmac_f32_16x16x64_bf16 v[208:211], v[232:235], v[216:223], v212\n\t" \
    "ds_read_b128 v[216:219], v254 offset:36864\n\t" \
    "ds_read_b128 v[220:223], v254 offset:37888\n\t" \
    "v_smfmac_f32_16x16x64_bf16 v[244:247], v[232:235], v[40:47], v212\n\t" \
    "v_smfmac_f32_16x16x64_bf16 v[248:251], v[232:235], v[48:55], v212\n\t" \
    "ds_read_b128 v[232:235], v253 offset:49152\n\t" \
    "s_waitcnt lgkmcnt(8)\n\t" \
    "v_smfmac_f32_16x16x64_bf16 v[208:211], v[236:239], v[56:63], v212\n\t" \
    "v_smfmac_f32_16x16x64_bf16 v[244:247], v[236:239], v[64:71], v212\n\t" \
    "v_smfmac_f32_16x16x64_bf16 v[248:251], v[236:239], v[72:79], v212\n\t" \
    "ds_read_b128 v[236:239], v253 offset:45056\n\t" \
    "s_waitcnt lgkmcnt(6)\n\t" \
    "v_smfmac_f32_16x16x64_bf16 v[208:211], v[240:243], v[200:207], v212\n\t" \
    "ds_read_b128 v[200:203], v254 offset:38912\n\t" \
    "ds_read_b128 v[204:207], v254 offset:39936\n\t" \
    "v_smfmac_f32_16x16x64_bf16 v[244:247], v[240:243], v[80:87], v212\n\t" \
    "v_smfmac_f32_16x16x64_bf16 v[248:251], v[240:243], v[88:95], v212\n\t" \
    "ds_read_b128 v[240:243], v253 offset:50176\n\t" \
    "s_waitcnt lgkmcnt(8)\n\t" \
    "v_smfmac_f32_16x16x64_bf16 v[208:211], v[224:227], v[96:103], v212\n\t" \
    "v_smfmac_f32_16x16x64_bf16 v[244:247], v[224:227], v[104:111], v212\n\t" \
    "v_smfmac_f32_16x16x64_bf16 v[248:251], v[224:227], v[112:119], v212\n\t" \
    "s_waitcnt lgkmcnt(7)\n\t" \
    "v_smfmac_f32_16x16x64_bf16 v[244:247], v[228:231], v[120:127], v212\n\t" \
    "s_waitcnt lgkmcnt(4)\n\t" \
    "v_smfmac_f32_16x16x64_bf16 v[244:247], v[232:235], v[144:151], v212\n\t" \
    "s_waitcnt lgkmcnt(3)\n\t" \
    "v_smfmac_f32_16x16x64_bf16 v[244:247], v[236:239], v[160:167], v212\n\t" \
    "s_waitcnt lgkmcnt(0)\n\t" \
    "v_smfmac_f32_16x16x64_bf16 v[244:247], v[240:243], v[184:191], v212\n\t" \
    "v_smfmac_f32_16x16x64_bf16 v[248:251], v[228:231], v[128:135], v212\n\t" \
    "v_smfmac_f32_16x16x64_bf16 v[248:251], v[232:235], v[152:159], v212\n\t" \
    "v_smfmac_f32_16x16x64_bf16 v[248:251], v[236:239], v[168:175], v212\n\t" \
    "v_add_f32 v244, v244, v246\n\t" \
    "v_add_f32 v245, v245, v247\n\t" \
    "v_cvt_pk_bf16_f32 v244, v244, v245\n\t" \
    "v_smfmac_f32_16x16x64_bf16 v[248:251], v[240:243], v[192:199], v212\n\t" \
    "ds_write_b16 v213, v244 offset:512\n\t" \
    "ds_write_b16_d16_hi v213, v244 offset:528\n\t" \
    "v_smfmac_f32_16x16x64_bf16 v[208:211], v[228:231], v[216:223], v212\n\t" \
    "v_smfmac_f32_16x16x64_bf16 v[208:211], v[232:235], v[136:143], v212\n\t" \
    "v_smfmac_f32_16x16x64_bf16 v[208:211], v[236:239], v[200:207], v212\n\t" \
    "v_add_f32 v248, v248, v250\n\t" \
    "v_add_f32 v249, v249, v251\n\t" \
    "v_cvt_pk_bf16_f32 v248, v248, v249\n\t" \
    "v_smfmac_f32_16x16x64_bf16 v[208:211], v[240:243], v[176:183], v212\n\t" \
    "ds_write_b16 v213, v248 offset:1024\n\t" \
    "ds_write_b16_d16_hi v213, v248 offset:1040\n\t" \
    "s_nop 5\n\t" \
    "v_add_f32 v208, v208, v210\n\t" \
    "v_add_f32 v209, v209, v211\n\t" \
    "v_cvt_pk_bf16_f32 v208, v208, v209\n\t" \
    "ds_write_b16 v213, v208 offset:0\n\t" \
    "ds_write_b16_d16_hi v213, v208 offset:16\n\t" \
    "s_setprio 0\n\t" \
    "s_waitcnt lgkmcnt(0)\n\t" \
    "s_barrier \n\t" \
    LST4 \
    "ds_read_b128 v[224:227], v253 offset:0\n\t" \
    "ds_read_b128 v[244:247], v254 offset:30720\n\t" \
    "ds_read_b128 v[228:231], v253 offset:1024\n\t" \
    "ds_read_b128 v[232:235], v253 offset:20480\n\t" \
    "ds_read_b128 v[236:239], v253 offset:21504\n\t" \
    "ds_read_b128 v[248:251], v254 offset:31744\n\t" \
    "s_add_u32 s40, s40, 1\n\t" \
    "s_cmp_lg_u32 s40, 64\n\t" \
    "s_cbranch_scc1 .Lv07_%=\n\t" \
    "s_branch .Lend%=\n\t" \
    ".Lend%=:\n\t" \
    "s_waitcnt vmcnt(0) lgkmcnt(0)\n\t" \
    "s_mov_b32 %[bud], s41\n\t"
#define ASM_R_LOOP \
    "s_mov_b32 s40, 0\n\t" \
    "s_mov_b32 s41, 0x40000\n\t" \
    LSTINIT \
    "v_mov_b64 v[252:253], -1\n\t" \
    "v_mov_b64 v[254:255], -1\n\t" \
    "v_mov_b32 v248, 0\n\t" \
    "s_mov_b32 s53, 0x05040100\n\t" \
    ".Lr1_%=:\n\t" \
    "s_mov_b32 s43, s40\n\t" \
    "s_and_b32 s44, s40, 3\n\t" \
    "s_lshl_b32 s42, s44, 20\n\t" \
    "s_and_b32 s45, s40, 2\n\t" \
    "s_lshl_b32 s45, s45, 12\n\t" \
    "s_add_u32 s42, s42, s45\n\t" \
    "s_add_u32 s42, s42, %[vx]\n\t" \
    "s_mul_i32 s46, s44, 0x202000\n\t" \
    "s_mul_i32 s47, s44, 0x30000\n\t" \
    "s_lshl_b32 s49, s44, 17\n\t" \
    "s_add_u32 s49, s49, %[px]\n\t" \
    "s_add_u32 s50, s49, 0x1000\n\t" \
    "s_add_u32 s44, s40, 1\n\t" \
    "s_and_b32 s44, s44, 3\n\t" \
    "s_lshl_b32 s48, s44, 17\n\t" \
    "s_mul_i32 s52, s40, 0x14000\n\t" \
    "s_lshr_b32 s44, s40, 4\n\t" \
    "s_mul_i32 s44, s44, 0xc2000\n\t" \
    "s_add_u32 s52, s52, s44\n\t" \
    "s_add_u32 s52, s52, %[xr]\n\t" \
    "s_add_i32 s44, s40, %[ad]\n\t" \
    "s_and_b32 s45, s44, 3\n\t" \
    "s_mul_i32 s51, s45, %[as]\n\t" \
    "s_cmp_lt_i32 s44, 0\n\t" \
    "s_cselect_b32 s55, 0x40000000, 0\n\t" \
    "s_add_u32 s44, s40, 3\n\t" \
    "s_mul_i32 s54, s44, 0x14000\n\t" \
    "s_lshr_b32 s45, s44, 4\n\t" \
    "s_mul_i32 s45, s45, 0xc2000\n\t" \
    "s_add_u32 s54, s54, s45\n\t" \
    "s_cmp_eq_u32 %[wave], 7\n\t" \
    "s_cselect_b32 s51, s54, s51\n\t" \
    "s_cselect_b32 s55, 0, s55\n\t" \
    "s_cselect_b32 s44, 60, 62\n\t" \
    "s_cmp_gt_u32 s40, s44\n\t" \
    "s_cselect_b32 s55, 0x40000000, s55\n\t" \
    "v_or_b32 v197, s55, %[ae]\n\t" \
    "v_mov_b64 v[180:181], -1\n\t" \
    "v_mov_b64 v[182:183], -1\n\t" \
    "v_mov_b64 v[184:185], -1\n\t" \
    "v_mov_b64 v[186:187], -1\n\t" \
    "v_mov_b64 v[188:189], -1\n\t" \
    "v_mov_b64 v[190:191], -1\n\t" \
    "v_mov_b64 v[192:193], -1\n\t" \
    "v_mov_b64 v[194:195], -1\n\t" \
    "s_waitcnt vmcnt(0)\n\t" \
    ".Lr2_%=:\n\t" \
    "buffer_load_dword v248, %[fo], %[rs], %[fs] offen sc1\n\t" \
    "s_waitcnt vmcnt(0)\n\t" \
    "v_cmp_ge_u32 vcc, s43, v248\n\t" \
    "s_cbranch_vccz .Lr3_%=\n\t" \
    "s_sleep 2\n\t" \
    "s_cmp_eq_u32 s41, 0\n\t" \
    "s_cbranch_scc1 .Lr3_%=\n\t" \
    "s_sub_u32 s41, s41, 1\n\t" \
    "s_branch .Lr2_%=\n\t" \
    ".Lr3_%=:\n\t" \
    "buffer_load_dwordx4 v[160:163], %[l16], %[rs], s42 offen offset:0 sc1\n\t" \
    "buffer_load_dwordx4 v[164:167], %[l16], %[rs], s42 offen offset:1024 sc1\n\t" \
    "buffer_load_dwordx4 v[168:171], %[l16], %[rs], s42 offen offset:2048 sc1\n\t" \
    "buffer_load_dwordx4 v[172:175], %[l16], %[rs], s42 offen offset:3072 sc1\n\t" \
    LST0 \
    "s_sleep 8\n\t" \
    "s_cmp_lg_u32 s40, 63\n\t" \
    "s_cbranch_scc1 .Lr5_%=\n\t" \
    "buffer_load_dwordx4 v[180:183], %[ov], %[rs], s46 offen sc1\n\t" \
    ".Lr6_%=:\n\t" \
    "s_waitcnt vmcnt(12)\n\t" \
    "v_max_u32 v196, v180, v181\n\t" \
    "v_max3_u32 v196, v182, v183, v196\n\t" \
    "v_cmp_eq_u32 vcc, -1, v196\n\t" \
    "s_cbranch_vccz .Lr7_%=\n\t" \
    "s_nop 7\n\t" \
    "v_max_u32 v196, v180, v181\n\t" \
    "v_max3_u32 v196, v182, v183, v196\n\t" \
    "v_cmp_eq_u32 vcc, -1, v196\n\t" \
    "s_cbranch_vccz .Lr7_%=\n\t" \
    "s_nop 7\n\t" \
    "v_max_u32 v196, v180, v181\n\t" \
    "v_max3_u32 v196, v182, v183, v196\n\t" \
    "v_cmp_eq_u32 vcc, -1, v196\n\t" \
    "s_cbranch_vccz .Lr7_%=\n\t" \
    "s_nop 7\n\t" \
    "v_max_u32 v196, v180, v181\n\t" \
    "v_max3_u32 v196, v182, v183, v196\n\t" \
    "v_cmp_eq_u32 vcc, -1, v196\n\t" \
    "s_cbranch_vccz .Lr7_%=\n\t" \
    "s_nop 7\n\t" \
    "s_cmp_eq_u32 s41, 0\n\t" \
    "s_cbranch_scc1 .Lr7_%=\n\t" \
    "s_sub_u32 s41, s41, 1\n\t" \
    "v_max_u32 v196, v180, v181\n\t" \
    "v_max3_u32 v196, v182, v183, v196\n\t" \
    "v_cmp_eq_u32 vcc, -1, v196\n\t" \
    "s_cbranch_vccz .Lr8_%=\n\t" \
    "buffer_load_dwordx4 v[180:183], %[ov], %[rs], s46 offen sc1\n\t" \
    ".Lr8_%=:\n\t" \
    "s_branch .Lr6_%=\n\t" \
    ".Lr7_%=:\n\t" \
    "s_branch .Lr4_%=\n\t" \
    ".Lr5_%=:\n\t" \
    LST7 \
    "buffer_load_dwordx4 v[184:187], %[o0], %[rs], s47 offen sc1\n\t" \
    "buffer_load_dwordx4 v[188:191], %[o1], %[rs], s47 offen sc1\n\t" \
    "buffer_load_dwordx4 v[192:195], %[o2], %[rs], s47 offen sc1\n\t" \
    "buffer_load_dwordx4 v[180:183], %[ov], %[rs], s46 offen sc1\n\t" \
    ".Lr9_%=:\n\t" \
    "s_waitcnt vmcnt(12)\n\t" \
    "v_max_u32 v196, v184, v185\n\t" \
    "v_max3_u32 v196, v186, v187, v196\n\t" \
    "v_max3_u32 v196, v188, v189, v196\n\t" \
    "v_max3_u32 v196, v190, v191, v196\n\t" \
    "v_max3_u32 v196, v192, v193, v196\n\t" \
    "v_max3_u32 v196, v194, v195, v196\n\t" \
    "v_max3_u32 v196, v180, v181, v196\n\t" \
    "v_max3_u32 v196, v182, v183, v196\n\t" \
    "v_cmp_eq_u32 vcc, -1, v196\n\t" \
    "s_cbranch_vccz .Lr10_%=\n\t" \
    "s_nop 7\n\t" \
    "v_max_u32 v196, v184, v185\n\t" \
    "v_max3_u32 v196, v186, v187, v196\n\t" \
    "v_max3_u32 v196, v188, v189, v196\n\t" \
    "v_max3_u32 v196, v190, v191, v196\n\t" \
    "v_max3_u32 v196, v192, v193, v196\n\t" \
    "v_max3_u32 v196, v194, v195, v196\n\t" \
    "v_max3_u32 v196, v180, v181, v196\n\t" \
    "v_max3_u32 v196, v182, v183, v196\n\t" \
    "v_cmp_eq_u32 vcc, -1, v196\n\t" \
    "s_cbranch_vccz .Lr10_%=\n\t" \
    "s_nop 7\n\t" \
    "v_max_u32 v196, v184, v185\n\t" \
    "v_max3_u32 v196, v186, v187, v196\n\t" \
    "v_max3_u32 v196, v188, v189, v196\n\t" \
    "v_max3_u32 v196, v190, v191, v196\n\t" \
    "v_max3_u32 v196, v192, v193, v196\n\t" \
    "v_max3_u32 v196, v194, v195, v196\n\t" \
    "v_max3_u32 v196, v180, v181, v196\n\t" \
    "v_max3_u32 v196, v182, v183, v196\n\t" \
    "v_cmp_eq_u32 vcc, -1, v196\n\t" \
    "s_cbranch_vccz .Lr10_%=\n\t" \
    "s_nop 7\n\t" \
    "v_max_u32 v196, v184, v185\n\t" \
    "v_max3_u32 v196, v186, v187, v196\n\t" \
    "v_max3_u32 v196, v188, v189, v196\n\t" \
    "v_max3_u32 v196, v190, v191, v196\n\t" \
    "v_max3_u32 v196, v192, v193, v196\n\t" \
    "v_max3_u32 v196, v194, v195, v196\n\t" \
    "v_max3_u32 v196, v180, v181, v196\n\t" \
    "v_max3_u32 v196, v182, v183, v196\n\t" \
    "v_cmp_eq_u32 vcc, -1, v196\n\t" \
    "s_cbranch_vccz .Lr10_%=\n\t" \
    "s_nop 7\n\t" \
    "v_max_u32 v196, v184, v185\n\t" \
    "v_max3_u32 v196, v186, v187, v196\n\t" \
    "v_max3_u32 v196, v188, v189, v196\n\t" \
    "v_max3_u32 v196, v190, v191, v196\n\t" \
    "v_max3_u32 v196, v192, v193, v196\n\t" \
    "v_max3_u32 v196, v194, v195, v196\n\t" \
    "v_max3_u32 v196, v180, v181, v196\n\t" \
    "v_max3_u32 v196, v182, v183, v196\n\t" \
    "v_cmp_eq_u32 vcc, -1, v196\n\t" \
    "s_cbranch_vccz .Lr10_%=\n\t" \
    "s_nop 7\n\t" \
    "v_max_u32 v196, v184, v185\n\t" \
    "v_max3_u32 v196, v186, v187, v196\n\t" \
    "v_max3_u32 v196, v188, v189, v196\n\t" \
    "v_max3_u32 v196, v190, v191, v196\n\t" \
    "v_max3_u32 v196, v192, v193, v196\n\t" \
    "v_max3_u32 v196, v194, v195, v196\n\t" \
    "v_max3_u32 v196, v180, v181, v196\n\t" \
    "v_max3_u32 v196, v182, v183, v196\n\t" \
    "v_cmp_eq_u32 vcc, -1, v196\n\t" \
    "s_cbranch_vccz .Lr10_%=\n\t" \
    "s_nop 7\n\t" \
    "v_max_u32 v196, v184, v185\n\t" \
    "v_max3_u32 v196, v186, v187, v196\n\t" \
    "v_max3_u32 v196, v188, v189, v196\n\t" \
    "v_max3_u32 v196, v190, v191, v196\n\t" \
    "v_max3_u32 v196, v192, v193, v196\n\t" \
    "v_max3_u32 v196, v194, v195, v196\n\t" \
    "v_max3_u32 v196, v180, v181, v196\n\t" \
    "v_max3_u32 v196, v182, v183, v196\n\t" \
    "v_cmp_eq_u32 vcc, -1, v196\n\t" \
    "s_cbranch_vccz .Lr10_%=\n\t" \
    "s_nop 7\n\t" \
    "v_max_u32 v196, v184, v185\n\t" \
    "v_max3_u32 v196, v186, v187, v196\n\t" \
    "v_max3_u32 v196, v188, v189, v196\n\t" \
    "v_max3_u32 v196, v190, v191, v196\n\t" \
    "v_max3_u32 v196, v192, v193, v196\n\t" \
    "v_max3_u32 v196, v194, v195, v196\n\t" \
    "v_max3_u32 v196, v180, v181, v196\n\t" \
    "v_max3_u32 v196, v182, v183, v196\n\t" \
    "v_cmp_eq_u32 vcc, -1, v196\n\t" \
    "s_cbranch_vccz .Lr10_%=\n\t" \
    "s_nop 7\n\t" \
    "s_cmp_eq_u32 s41, 0\n\t" \
    "s_cbranch_scc1 .Lr10_%=\n\t" \
    "s_sub_u32 s41, s41, 1\n\t" \
    "v_max_u32 v196, v184, v185\n\t" \
    "v_max3_u32 v196, v186, v187, v196\n\t" \
    "v_cmp_eq_u32 vcc, -1, v196\n\t" \
    "s_cbranch_vccz .Lr11_%=\n\t" \
    "buffer_load_dwordx4 v[184:187], %[o0], %[rs], s47 offen sc1\n\t" \
    ".Lr11_%=:\n\t" \
    "v_max_u32 v196, v188, v189\n\t" \
    "v_max3_u32 v196, v190, v191, v196\n\t" \
    "v_cmp_eq_u32 vcc, -1, v196\n\t" \
    "s_cbranch_vccz .Lr12_%=\n\t" \
    "buffer_load_dwordx4 v[188:191], %[o1], %[rs], s47 offen sc1\n\t" \
    ".Lr12_%=:\n\t" \
    "v_max_u32 v196, v192, v193\n\t" \
    "v_max3_u32 v196, v194, v195, v196\n\t" \
    "v_cmp_eq_u32 vcc, -1, v196\n\t" \
    "s_cbranch_vccz .Lr13_%=\n\t" \
    "buffer_load_dwordx4 v[192:195], %[o2], %[rs], s47 offen sc1\n\t" \
    ".Lr13_%=:\n\t" \
    "v_max_u32 v196, v180, v181\n\t" \
    "v_max3_u32 v196, v182, v183, v196\n\t" \
    "v_cmp_eq_u32 vcc, -1, v196\n\t" \
    "s_cbranch_vccz .Lr14_%=\n\t" \
    "buffer_load_dwordx4 v[180:183], %[ov], %[rs], s46 offen sc1\n\t" \
    ".Lr14_%=:\n\t" \
    "s_branch .Lr9_%=\n\t" \
    ".Lr10_%=:\n\t" \
    LST1 \
    "v_mfma_f32_16x16x32_bf16 v[176:179], v[192:195], v[180:183], 0\n\t" \
    "v_smfmac_f32_16x16x64_bf16 v[176:179], v[184:187], v[160:167], %[sidx]\n\t" \
    "v_smfmac_f32_16x16x64_bf16 v[176:179], v[188:191], v[168:175], %[sidx]\n\t" \
    "s_cmp_gt_u32 %[wave], 3\n\t" \
    "s_nop 7\n\t" \
    "v_add_f32 v176, v176, v178\n\t" \
    "v_add_f32 v177, v177, v179\n\t" \
    "ds_write2_b32 %[rw], v176, v177 offset1:80\n\t" \
    "s_waitcnt lgkmcnt(0)\n\t" \
    "s_barrier \n\t" \
    LST2 \
    "s_cbranch_scc1 .Lr4_%=\n\t" \
    "ds_read_b128 v[128:131], %[rd] offset:0\n\t" \
    "ds_read_b128 v[132:135], %[rd] offset:16\n\t" \
    "ds_read_b128 v[136:139], %[rd] offset:32\n\t" \
    "ds_read_b128 v[140:143], %[rd] offset:48\n\t" \
    "s_waitcnt lgkmcnt(2)\n\t" \
    "v_pk_add_f32 v[128:129], v[128:129], v[130:131]\n\t" \
    "v_pk_add_f32 v[132:133], v[132:133], v[134:135]\n\t" \
    "s_waitcnt lgkmcnt(0)\n\t" \
    "v_pk_add_f32 v[136:137], v[136:137], v[138:139]\n\t" \
    "v_pk_add_f32 v[140:141], v[140:141], v[142:143]\n\t" \
    "v_pk_add_f32 v[128:129], v[128:129], v[132:133]\n\t" \
    "v_pk_add_f32 v[136:137], v[136:137], v[140:141]\n\t" \
    "v_pk_add_f32 v[128:129], v[128:129], v[136:137]\n\t" \
    "v_cvt_pk_bf16_f32 v128, v128, v129\n\t" \
    "v_min_u32 v128, -2, v128\n\t" \
    "buffer_store_dword v128, %[oo], %[rs], s48 offen sc1\n\t" \
    ".Lr4_%=:\n\t" \
    LST3 \
    "s_cmp_lt_u32 %[wave], 4\n\t" \
    "s_cbranch_scc1 .Lr16_%=\n\t" \
    "s_sleep 4\n\t" \
    ".Lr16_%=:\n\t" \
    "s_sleep 4\n\t" \
    "s_cmp_lt_u32 %[wave], 6\n\t" \
    "s_cbranch_scc1 .Lr15_%=\n\t" \
    "buffer_load_dwordx2 v[250:251], %[xo], %[rs], s52 offen sc1\n\t" \
    ".Lr15_%=:\n\t" \
    "buffer_load_dwordx4 v[220:223], %[l16], %[rs], s50 offen offset:1024 sc1\n\t" \
    "buffer_load_dwordx4 v[224:227], %[l16], %[rs], s50 offen offset:2048 sc1\n\t" \
    "buffer_load_dwordx4 v[228:231], %[l16], %[rs], s50 offen offset:3072 sc1\n\t" \
    "v_perm_b32 v232, v161, v160, s53\n\t" \
    "v_perm_b32 v233, v163, v162, s53\n\t" \
    "v_perm_b32 v234, v165, v164, s53\n\t" \
    "v_perm_b32 v235, v167, v166, s53\n\t" \
    "v_perm_b32 v236, v169, v168, s53\n\t" \
    "v_perm_b32 v237, v171, v170, s53\n\t" \
    "v_perm_b32 v238, v173, v172, s53\n\t" \
    "v_perm_b32 v239, v175, v174, s53\n\t" \
    "s_nop 1\n\t" \
    "v_permlane16_swap_b32 v232, v234\n\t" \
    "v_permlane16_swap_b32 v233, v235\n\t" \
    "v_permlane16_swap_b32 v236, v238\n\t" \
    "v_permlane16_swap_b32 v237, v239\n\t" \
    "s_waitcnt vmcnt(0)\n\t" \
    ".Lr17_%=:\n\t" \
    "v_max_u32 v196, v220, v221\n\t" \
    "v_max3_u32 v196, v222, v223, v196\n\t" \
    "v_max3_u32 v196, v224, v225, v196\n\t" \
    "v_max3_u32 v196, v226, v227, v196\n\t" \
    "v_max3_u32 v196, v228, v229, v196\n\t" \
    "v_max3_u32 v196, v230, v231, v196\n\t" \
    "v_cmp_eq_u32 vcc, -1, v196\n\t" \
    "s_cbranch_vccz .Lr18_%=\n\t" \
    "s_cmp_eq_u32 s41, 0\n\t" \
    "s_cbranch_scc1 .Lr18_%=\n\t" \
    "s_sub_u32 s41, s41, 1\n\t" \
    "v_max_u32 v196, v220, v221\n\t" \
    "v_max3_u32 v196, v222, v223, v196\n\t" \
    "v_cmp_eq_u32 vcc, -1, v196\n\t" \
    "s_cbranch_vccz .Lr19_%=\n\t" \
    "buffer_load_dwordx4 v[220:223], %[l16], %[rs], s50 offen offset:1024 sc1\n\t" \
    ".Lr19_%=:\n\t" \
    "v_max_u32 v196, v224, v225\n\t" \
    "v_max3_u32 v196, v226, v227, v196\n\t" \
    "v_cmp_eq_u32 vcc, -1, v196\n\t" \
    "s_cbranch_vccz .Lr20_%=\n\t" \
    "buffer_load_dwordx4 v[224:227], %[l16], %[rs], s50 offen offset:2048 sc1\n\t" \
    ".Lr20_%=:\n\t" \
    "v_max_u32 v196, v228, v229\n\t" \
    "v_max3_u32 v196, v230, v231, v196\n\t" \
    "v_cmp_eq_u32 vcc, -1, v196\n\t" \
    "s_cbranch_vccz .Lr21_%=\n\t" \
    "buffer_load_dwordx4 v[228:231], %[l16], %[rs], s50 offen offset:3072 sc1\n\t" \
    ".Lr21_%=:\n\t" \
    "s_waitcnt vmcnt(0)\n\t" \
    "s_branch .Lr17_%=\n\t" \
    ".Lr18_%=:\n\t" \
    "v_mfma_f32_16x16x32_bf16 v[240:243], v[232:235], v[220:223], 0\n\t" \
    "v_mfma_f32_16x16x32_bf16 v[240:243], v[236:239], v[224:227], v[240:243]\n\t" \
    "v_mfma_f32_16x16x32_bf16 v[240:243], v[180:183], v[228:231], v[240:243]\n\t" \
    "s_nop 7\n\t" \
    "ds_write_b128 %[pw], v[240:243]\n\t" \
    "s_waitcnt lgkmcnt(0)\n\t" \
    "s_barrier \n\t" \
    LST5 \
    "buffer_load_dwordx4 v[200:203], %[l16], %[rs], s49 offen offset:0 sc1\n\t" \
    "buffer_load_dwordx4 v[204:207], %[l16], %[rs], s49 offen offset:1024 sc1\n\t" \
    "buffer_load_dwordx4 v[208:211], %[l16], %[rs], s49 offen offset:2048 sc1\n\t" \
    "buffer_load_dwordx4 v[212:215], %[l16], %[rs], s49 offen offset:3072 sc1\n\t" \
    "buffer_load_dwordx4 v[216:219], %[l16], %[rs], s50 offen offset:0 sc1\n\t" \
    "s_cmp_lg_u32 %[wave], 6\n\t" \
    "s_cbranch_scc1 .Lr22_%=\n\t" \
    "ds_read_b128 v[128:131], %[pr] offset:0\n\t" \
    "ds_read_b128 v[132:135], %[pr] offset:1024\n\t" \
    "ds_read_b128 v[136:139], %[pr] offset:2048\n\t" \
    "ds_read_b128 v[140:143], %[pr] offset:3072\n\t" \
    "ds_read_b128 v[144:147], %[pr] offset:4096\n\t" \
    "ds_read_b128 v[148:151], %[pr] offset:5120\n\t" \
    "ds_read_b128 v[152:155], %[pr] offset:6144\n\t" \
    "ds_read_b128 v[156:159], %[pr] offset:7168\n\t" \
    "v_lshlrev_b32 v176, 16, v250\n\t" \
    "v_and_b32 v177, 0xffff0000, v250\n\t" \
    "v_lshlrev_b32 v178, 16, v251\n\t" \
    "v_and_b32 v179, 0xffff0000, v251\n\t" \
    "s_waitcnt lgkmcnt(6)\n\t" \
    "v_pk_add_f32 v[128:129], v[128:129], v[132:133]\n\t" \
    "v_pk_add_f32 v[130:131], v[130:131], v[134:135]\n\t" \
    "s_waitcnt lgkmcnt(4)\n\t" \
    "v_pk_add_f32 v[136:137], v[136:137], v[140:141]\n\t" \
    "v_pk_add_f32 v[138:139], v[138:139], v[142:143]\n\t" \
    "v_pk_add_f32 v[128:129], v[128:129], v[136:137]\n\t" \
    "v_pk_add_f32 v[130:131], v[130:131], v[138:139]\n\t" \
    "s_waitcnt lgkmcnt(2)\n\t" \
    "v_pk_add_f32 v[144:145], v[144:145], v[148:149]\n\t" \
    "v_pk_add_f32 v[146:147], v[146:147], v[150:151]\n\t" \
    "s_waitcnt lgkmcnt(0)\n\t" \
    "v_pk_add_f32 v[152:153], v[152:153], v[156:157]\n\t" \
    "v_pk_add_f32 v[154:155], v[154:155], v[158:159]\n\t" \
    "v_pk_add_f32 v[144:145], v[144:145], v[152:153]\n\t" \
    "v_pk_add_f32 v[146:147], v[146:147], v[154:155]\n\t" \
    "v_pk_add_f32 v[128:129], v[128:129], v[144:145]\n\t" \
    "v_pk_add_f32 v[130:131], v[130:131], v[146:147]\n\t" \
    "v_pk_add_f32 v[128:129], v[128:129], v[176:177]\n\t" \
    "v_pk_add_f32 v[130:131], v[130:131], v[178:179]\n\t" \
    "v_cvt_pk_bf16_f32 v128, v128, v129\n\t" \
    "v_cvt_pk_bf16_f32 v129, v130, v131\n\t" \
    "v_min_u32 v128, -2, v128\n\t" \
    "v_min_u32 v129, -2, v129\n\t" \
    "s_add_u32 s44, s52, 0x1000000\n\t" \
    "buffer_store_dwordx2 v[128:129], %[xo], %[rs], s44 offen sc1\n\t" \
    "s_waitcnt vmcnt(1)\n\t" \
    "s_branch .Lr23_%=\n\t" \
    ".Lr22_%=:\n\t" \
    "s_waitcnt vmcnt(0)\n\t" \
    ".Lr23_%=:\n\t" \
    ".Lr24_%=:\n\t" \
    "v_max_u32 v196, v200, v201\n\t" \
    "v_max3_u32 v196, v202, v203, v196\n\t" \
    "v_max3_u32 v196, v204, v205, v196\n\t" \
    "v_max3_u32 v196, v206, v207, v196\n\t" \
    "v_max3_u32 v196, v208, v209, v196\n\t" \
    "v_max3_u32 v196, v210, v211, v196\n\t" \
    "v_max3_u32 v196, v212, v213, v196\n\t" \
    "v_max3_u32 v196, v214, v215, v196\n\t" \
    "v_max3_u32 v196, v216, v217, v196\n\t" \
    "v_max3_u32 v196, v218, v219, v196\n\t" \
    "v_cmp_eq_u32 vcc, -1, v196\n\t" \
    "s_cbranch_vccz .Lr25_%=\n\t" \
    "s_cmp_eq_u32 s41, 0\n\t" \
    "s_cbranch_scc1 .Lr25_%=\n\t" \
    "s_sub_u32 s41, s41, 1\n\t" \
    "v_max_u32 v196, v200, v201\n\t" \
    "v_max3_u32 v196, v202, v203, v196\n\t" \
    "v_cmp_eq_u32 vcc, -1, v196\n\t" \
    "s_cbranch_vccz .Lr26_%=\n\t" \
    "buffer_load_dwordx4 v[200:203], %[l16], %[rs], s49 offen offset:0 sc1\n\t" \
    ".Lr26_%=:\n\t" \
    "v_max_u32 v196, v204, v205\n\t" \
    "v_max3_u32 v196, v206, v207, v196\n\t" \
    "v_cmp_eq_u32 vcc, -1, v196\n\t" \
    "s_cbranch_vccz .Lr27_%=\n\t" \
    "buffer_load_dwordx4 v[204:207], %[l16], %[rs], s49 offen offset:1024 sc1\n\t" \
    ".Lr27_%=:\n\t" \
    "v_max_u32 v196, v208, v209\n\t" \
    "v_max3_u32 v196, v210, v211, v196\n\t" \
    "v_cmp_eq_u32 vcc, -1, v196\n\t" \
    "s_cbranch_vccz .Lr28_%=\n\t" \
    "buffer_load_dwordx4 v[208:211], %[l16], %[rs], s49 offen offset:2048 sc1\n\t" \
    ".Lr28_%=:\n\t" \
    "v_max_u32 v196, v212, v213\n\t" \
    "v_max3_u32 v196, v214, v215, v196\n\t" \
    "v_cmp_eq_u32 vcc, -1, v196\n\t" \
    "s_cbranch_vccz .Lr29_%=\n\t" \
    "buffer_load_dwordx4 v[212:215], %[l16], %[rs], s49 offen offset:3072 sc1\n\t" \
    ".Lr29_%=:\n\t" \
    "v_max_u32 v196, v216, v217\n\t" \
    "v_max3_u32 v196, v218, v219, v196\n\t" \
    "v_cmp_eq_u32 vcc, -1, v196\n\t" \
    "s_cbranch_vccz .Lr30_%=\n\t" \
    "buffer_load_dwordx4 v[216:219], %[l16], %[rs], s50 offen offset:0 sc1\n\t" \
    ".Lr30_%=:\n\t" \
    "s_waitcnt vmcnt(0)\n\t" \
    "s_branch .Lr24_%=\n\t" \
    ".Lr25_%=:\n\t" \
    "v_mfma_f32_16x16x32_bf16 v[244:247], v[160:163], v[200:203], 0\n\t" \
    "v_mfma_f32_16x16x32_bf16 v[244:247], v[164:167], v[204:207], v[244:247]\n\t" \
    "v_mfma_f32_16x16x32_bf16 v[244:247], v[168:171], v[208:211], v[244:247]\n\t" \
    "v_mfma_f32_16x16x32_bf16 v[244:247], v[172:175], v[212:215], v[244:247]\n\t" \
    "v_mfma_f32_16x16x32_bf16 v[244:247], v[180:183], v[216:219], v[244:247]\n\t" \
    "s_nop 7\n\t" \
    "ds_write_b128 %[pw], v[244:247] offset:8192\n\t" \
    "s_waitcnt lgkmcnt(0)\n\t" \
    "s_barrier \n\t" \
    LST4 \
    "s_cmp_lg_u32 %[wave], 7\n\t" \
    "s_cbranch_scc1 .Lr31_%=\n\t" \
    "ds_read_b128 v[128:131], %[pr] offset:0\n\t" \
    "ds_read_b128 v[132:135], %[pr] offset:1024\n\t" \
    "ds_read_b128 v[136:139], %[pr] offset:2048\n\t" \
    "ds_read_b128 v[140:143], %[pr] offset:3072\n\t" \
    "ds_read_b128 v[144:147], %[pr] offset:4096\n\t" \
    "ds_read_b128 v[148:151], %[pr] offset:5120\n\t" \
    "ds_read_b128 v[152:155], %[pr] offset:6144\n\t" \
    "ds_read_b128 v[156:159], %[pr] offset:7168\n\t" \
    "v_lshlrev_b32 v176, 16, v250\n\t" \
    "v_and_b32 v177, 0xffff0000, v250\n\t" \
    "v_lshlrev_b32 v178, 16, v251\n\t" \
    "v_and_b32 v179, 0xffff0000, v251\n\t" \
    "s_waitcnt lgkmcnt(6)\n\t" \
    "v_pk_add_f32 v[128:129], v[128:129], v[132:133]\n\t" \
    "v_pk_add_f32 v[130:131], v[130:131], v[134:135]\n\t" \
    "s_waitcnt lgkmcnt(4)\n\t" \
    "v_pk_add_f32 v[136:137], v[136:137], v[140:141]\n\t" \
    "v_pk_add_f32 v[138:139], v[138:139], v[142:143]\n\t" \
    "v_pk_add_f32 v[128:129], v[128:129], v[136:137]\n\t" \
    "v_pk_add_f32 v[130:131], v[130:131], v[138:139]\n\t" \
    "s_waitcnt lgkmcnt(2)\n\t" \
    "v_pk_add_f32 v[144:145], v[144:145], v[148:149]\n\t" \
    "v_pk_add_f32 v[146:147], v[146:147], v[150:151]\n\t" \
    "s_waitcnt lgkmcnt(0)\n\t" \
    "v_pk_add_f32 v[152:153], v[152:153], v[156:157]\n\t" \
    "v_pk_add_f32 v[154:155], v[154:155], v[158:159]\n\t" \
    "v_pk_add_f32 v[144:145], v[144:145], v[152:153]\n\t" \
    "v_pk_add_f32 v[146:147], v[146:147], v[154:155]\n\t" \
    "v_pk_add_f32 v[128:129], v[128:129], v[144:145]\n\t" \
    "v_pk_add_f32 v[130:131], v[130:131], v[146:147]\n\t" \
    "v_pk_add_f32 v[128:129], v[128:129], v[176:177]\n\t" \
    "v_pk_add_f32 v[130:131], v[130:131], v[178:179]\n\t" \
    "v_cvt_pk_bf16_f32 v128, v128, v129\n\t" \
    "v_cvt_pk_bf16_f32 v129, v130, v131\n\t" \
    "v_min_u32 v128, -2, v128\n\t" \
    "v_min_u32 v129, -2, v129\n\t" \
    "s_add_u32 s44, s52, 0x1000000\n\t" \
    "buffer_store_dwordx2 v[128:129], %[xo], %[rs], s44 offen sc1\n\t" \
    ".Lr31_%=:\n\t" \
    "buffer_store_dwordx4 v[252:255], %[ov], %[rs], s46 offen sc1\n\t" \
    "buffer_store_dwordx4 v[252:255], v197, %[rs], s51 offen sc1\n\t" \
    LFLUSHR \
    "s_add_u32 s40, s40, 1\n\t" \
    "s_cmp_lg_u32 s40, 64\n\t" \
    "s_cbranch_scc1 .Lr1_%=\n\t" \
    "s_branch .Lend%=\n\t" \
    ".Lend%=:\n\t" \
    "s_waitcnt vmcnt(0) lgkmcnt(0)\n\t" \
    "s_mov_b32 %[bud], s41\n\t"
#define ASM_W4_M \
    "buffer_load_dwordx4 v[184:187], %[l16], %[rs], %[s1] offen offset:2048 sc1\n\t" \
    "buffer_load_dwordx4 v[180:183], %[l16], %[rs], %[s1] offen offset:1024 sc1\n\t" \
    "buffer_load_dwordx4 v[176:179], %[l16], %[rs], %[s1] offen offset:0 sc1\n\t" \
    "buffer_load_dwordx4 v[172:175], %[l16], %[rs], %[s0] offen offset:3072 sc1\n\t" \
    "buffer_load_dwordx4 v[168:171], %[l16], %[rs], %[s0] offen offset:2048 sc1\n\t" \
    "buffer_load_dwordx4 v[164:167], %[l16], %[rs], %[s0] offen offset:1024 sc1\n\t" \
    "buffer_load_dwordx4 v[160:163], %[l16], %[rs], %[s0] offen offset:0 sc1\n\t" \
    "v_mfma_f32_16x16x32_bf16 v[224:227], v[28:31], v[188:191], 0\n\t" \
    "v_mfma_f32_16x16x32_bf16 v[228:231], v[60:63], v[188:191], 0\n\t" \
    "v_mfma_f32_16x16x32_bf16 v[232:235], v[92:95], v[188:191], 0\n\t" \
    "v_mfma_f32_16x16x32_bf16 v[236:239], v[124:127], v[188:191], 0\n\t" \
    "v_mfma_f32_16x16x32_bf16 v[240:243], v[156:159], v[188:191], 0\n\t" \
    "s_waitcnt vmcnt(6)\n\t" \
    ".Lc6_%=:\n\t" \
    "v_max_u32 v244, v184, v185\n\t" \
    "v_max3_u32 v244, v186, v187, v244\n\t" \
    "v_cmp_eq_u32 vcc, -1, v244\n\t" \
    "s_cbranch_vccnz .Ls6_%=\n\t" \
    ".Lk6_%=:\n\t" \
    "v_mfma_f32_16x16x32_bf16 v[224:227], v[24:27], v[184:187], v[224:227]\n\t" \
    "v_mfma_f32_16x16x32_bf16 v[228:231], v[56:59], v[184:187], v[228:231]\n\t" \
    "v_mfma_f32_16x16x32_bf16 v[232:235], v[88:91], v[184:187], v[232:235]\n\t" \
    "v_mfma_f32_16x16x32_bf16 v[236:239], v[120:123], v[184:187], v[236:239]\n\t" \
    "v_mfma_f32_16x16x32_bf16 v[240:243], v[152:155], v[184:187], v[240:243]\n\t" \
    "s_waitcnt vmcnt(5)\n\t" \
    ".Lc5_%=:\n\t" \
    "v_max_u32 v244, v180, v181\n\t" \
    "v_max3_u32 v244, v182, v183, v244\n\t" \
    "v_cmp_eq_u32 vcc, -1, v244\n\t" \
    "s_cbranch_vccnz .Ls5_%=\n\t" \
    ".Lk5_%=:\n\t" \
    "v_mfma_f32_16x16x32_bf16 v[224:227], v[20:23], v[180:183], v[224:227]\n\t" \
    "v_mfma_f32_16x16x32_bf16 v[228:231], v[52:55], v[180:183], v[228:231]\n\t" \
    "v_mfma_f32_16x16x32_bf16 v[232:235], v[84:87], v[180:183], v[232:235]\n\t" \
    "v_mfma_f32_16x16x32_bf16 v[236:239], v[116:119], v[180:183], v[236:239]\n\t" \
    "v_mfma_f32_16x16x32_bf16 v[240:243], v[148:151], v[180:183], v[240:243]\n\t" \
    "s_waitcnt vmcnt(4)\n\t" \
    ".Lc4_%=:\n\t" \
    "v_max_u32 v244, v176, v177\n\t" \
    "v_max3_u32 v244, v178, v179, v244\n\t" \
    "v_cmp_eq_u32 vcc, -1, v244\n\t" \
    "s_cbranch_vccnz .Ls4_%=\n\t" \
    ".Lk4_%=:\n\t" \
    "v_mfma_f32_16x16x32_bf16 v[224:227], v[16:19], v[176:179], v[224:227]\n\t" \
    "v_mfma_f32_16x16x32_bf16 v[228:231], v[48:51], v[176:179], v[228:231]\n\t" \
    "v_mfma_f32_16x16x32_bf16 v[232:235], v[80:83], v[176:179], v[232:235]\n\t" \
    "v_mfma_f32_16x16x32_bf16 v[236:239], v[112:115], v[176:179], v[236:239]\n\t" \
    "v_mfma_f32_16x16x32_bf16 v[240:243], v[144:147], v[176:179], v[240:243]\n\t" \
    "s_waitcnt vmcnt(3)\n\t" \
    ".Lc3_%=:\n\t" \
    "v_max_u32 v244, v172, v173\n\t" \
    "v_max3_u32 v244, v174, v175, v244\n\t" \
    "v_cmp_eq_u32 vcc, -1, v244\n\t" \
    "s_cbranch_vccnz .Ls3_%=\n\t" \
    ".Lk3_%=:\n\t" \
    "v_mfma_f32_16x16x32_bf16 v[224:227], v[12:15], v[172:175], v[224:227]\n\t" \
    "v_mfma_f32_16x16x32_bf16 v[228:231], v[44:47], v[172:175], v[228:231]\n\t" \
    "v_mfma_f32_16x16x32_bf16 v[232:235], v[76:79], v[172:175], v[232:235]\n\t" \
    "v_mfma_f32_16x16x32_bf16 v[236:239], v[108:111], v[172:175], v[236:239]\n\t" \
    "v_mfma_f32_16x16x32_bf16 v[240:243], v[140:143], v[172:175], v[240:243]\n\t" \
    "s_waitcnt vmcnt(2)\n\t" \
    ".Lc2_%=:\n\t" \
    "v_max_u32 v244, v168, v169\n\t" \
    "v_max3_u32 v244, v170, v171, v244\n\t" \
    "v_cmp_eq_u32 vcc, -1, v244\n\t" \
    "s_cbranch_vccnz .Ls2_%=\n\t" \
    ".Lk2_%=:\n\t" \
    "v_mfma_f32_16x16x32_bf16 v[224:227], v[8:11], v[168:171], v[224:227]\n\t" \
    "v_mfma_f32_16x16x32_bf16 v[228:231], v[40:43], v[168:171], v[228:231]\n\t" \
    "v_mfma_f32_16x16x32_bf16 v[232:235], v[72:75], v[168:171], v[232:235]\n\t" \
    "v_mfma_f32_16x16x32_bf16 v[236:239], v[104:107], v[168:171], v[236:239]\n\t" \
    "v_mfma_f32_16x16x32_bf16 v[240:243], v[136:139], v[168:171], v[240:243]\n\t" \
    "s_waitcnt vmcnt(1)\n\t" \
    ".Lc1_%=:\n\t" \
    "v_max_u32 v244, v164, v165\n\t" \
    "v_max3_u32 v244, v166, v167, v244\n\t" \
    "v_cmp_eq_u32 vcc, -1, v244\n\t" \
    "s_cbranch_vccnz .Ls1_%=\n\t" \
    ".Lk1_%=:\n\t" \
    "v_mfma_f32_16x16x32_bf16 v[224:227], v[4:7], v[164:167], v[224:227]\n\t" \
    "v_mfma_f32_16x16x32_bf16 v[228:231], v[36:39], v[164:167], v[228:231]\n\t" \
    "v_mfma_f32_16x16x32_bf16 v[232:235], v[68:71], v[164:167], v[232:235]\n\t" \
    "v_mfma_f32_16x16x32_bf16 v[236:239], v[100:103], v[164:167], v[236:239]\n\t" \
    "v_mfma_f32_16x16x32_bf16 v[240:243], v[132:135], v[164:167], v[240:243]\n\t" \
    "s_waitcnt vmcnt(0)\n\t" \
    ".Lc0_%=:\n\t" \
    "v_max_u32 v244, v160, v161\n\t" \
    "v_max3_u32 v244, v162, v163, v244\n\t" \
    "v_cmp_eq_u32 vcc, -1, v244\n\t" \
    "s_cbranch_vccnz .Ls0_%=\n\t" \
    ".Lk0_%=:\n\t" \
    "v_mfma_f32_16x16x32_bf16 v[224:227], v[0:3], v[160:163], v[224:227]\n\t" \
    "v_mfma_f32_16x16x32_bf16 v[228:231], v[32:35], v[160:163], v[228:231]\n\t" \
    "v_mfma_f32_16x16x32_bf16 v[232:235], v[64:67], v[160:163], v[232:235]\n\t" \
    "v_mfma_f32_16x16x32_bf16 v[236:239], v[96:99], v[160:163], v[236:239]\n\t" \
    "v_mfma_f32_16x16x32_bf16 v[240:243], v[128:131], v[160:163], v[240:243]\n\t" \
    "ds_write_b128 %[ld], v[224:227] offset:0\n\t" \
    "ds_write_b128 %[ld], v[228:231] offset:6144\n\t" \
    "ds_write_b128 %[ld], v[232:235] offset:12288\n\t" \
    "s_nop 1\n\t" \
    "ds_write_b128 %[ld], v[236:239] offset:18432\n\t" \
    "s_nop 1\n\t" \
    "ds_write_b128 %[ld], v[240:243] offset:24576\n\t" \
    "s_branch .Lme%=\n\t" \
    ".Ls6_%=:\n\t" \
    "s_cmp_eq_u32 %[n], 0\n\t" \
    "s_cbranch_scc1 .Lk6_%=\n\t" \
    "s_sub_u32 %[n], %[n], 1\n\t" \
    "buffer_load_dwordx4 v[184:187], %[l16], %[rs], %[s1] offen offset:2048 sc1\n\t" \
    "s_waitcnt vmcnt(0)\n\t" \
    "s_branch .Lc6_%=\n\t" \
    ".Ls5_%=:\n\t" \
    "s_cmp_eq_u32 %[n], 0\n\t" \
    "s_cbranch_scc1 .Lk5_%=\n\t" \
    "s_sub_u32 %[n], %[n], 1\n\t" \
    "buffer_load_dwordx4 v[180:183], %[l16], %[rs], %[s1] offen offset:1024 sc1\n\t" \
    "s_waitcnt vmcnt(0)\n\t" \
    "s_branch .Lc5_%=\n\t" \
    ".Ls4_%=:\n\t" \
    "s_cmp_eq_u32 %[n], 0\n\t" \
    "s_cbranch_scc1 .Lk4_%=\n\t" \
    "s_sub_u32 %[n], %[n], 1\n\t" \
    "buffer_load_dwordx4 v[176:179], %[l16], %[rs], %[s1] offen offset:0 sc1\n\t" \
    "s_waitcnt vmcnt(0)\n\t" \
    "s_branch .Lc4_%=\n\t" \
    ".Ls3_%=:\n\t" \
    "s_cmp_eq_u32 %[n], 0\n\t" \
    "s_cbranch_scc1 .Lk3_%=\n\t" \
    "s_sub_u32 %[n], %[n], 1\n\t" \
    "buffer_load_dwordx4 v[172:175], %[l16], %[rs], %[s0] offen offset:3072 sc1\n\t" \
    "s_waitcnt vmcnt(0)\n\t" \
    "s_branch .Lc3_%=\n\t" \
    ".Ls2_%=:\n\t" \
    "s_cmp_eq_u32 %[n], 0\n\t" \
    "s_cbranch_scc1 .Lk2_%=\n\t" \
    "s_sub_u32 %[n], %[n], 1\n\t" \
    "buffer_load_dwordx4 v[168:171], %[l16], %[rs], %[s0] offen offset:2048 sc1\n\t" \
    "s_waitcnt vmcnt(0)\n\t" \
    "s_branch .Lc2_%=\n\t" \
    ".Ls1_%=:\n\t" \
    "s_cmp_eq_u32 %[n], 0\n\t" \
    "s_cbranch_scc1 .Lk1_%=\n\t" \
    "s_sub_u32 %[n], %[n], 1\n\t" \
    "buffer_load_dwordx4 v[164:167], %[l16], %[rs], %[s0] offen offset:1024 sc1\n\t" \
    "s_waitcnt vmcnt(0)\n\t" \
    "s_branch .Lc1_%=\n\t" \
    ".Ls0_%=:\n\t" \
    "s_cmp_eq_u32 %[n], 0\n\t" \
    "s_cbranch_scc1 .Lk0_%=\n\t" \
    "s_sub_u32 %[n], %[n], 1\n\t" \
    "buffer_load_dwordx4 v[160:163], %[l16], %[rs], %[s0] offen offset:0 sc1\n\t" \
    "s_waitcnt vmcnt(0)\n\t" \
    "s_branch .Lc0_%=\n\t" \
    ".Lme%=:\n\t"
#define ASM_W1_LOOP \
    "s_mov_b32 s40, 0\n\t" \
    "s_mov_b32 s41, 0x20000\n\t" \
    LSTINIT \
    "s_add_u32 m0, %[wm], 0x0\n\t" \
    "s_add_u32 s44, %[wo], 0x0\n\t" \
    "buffer_load_dwordx4 v176, %[wr], s44 offen nt lds\n\t" \
    "s_add_u32 m0, %[wm], 0x2000\n\t" \
    "s_add_u32 s44, %[wo], 0xa000\n\t" \
    "buffer_load_dwordx4 v176, %[wr], s44 offen nt lds\n\t" \
    "s_add_u32 m0, %[wm], 0x4000\n\t" \
    "s_add_u32 s44, %[wo], 0x14000\n\t" \
    "buffer_load_dwordx4 v176, %[wr], s44 offen nt lds\n\t" \
    "s_add_u32 m0, %[wm], 0x6000\n\t" \
    "s_add_u32 s44, %[wo], 0x1e000\n\t" \
    "buffer_load_dwordx4 v176, %[wr], s44 offen nt lds\n\t" \
    "s_add_u32 m0, %[wm], 0x8000\n\t" \
    "s_add_u32 s44, %[wo], 0x28000\n\t" \
    "buffer_load_dwordx4 v176, %[wr], s44 offen nt lds\n\t" \
    "s_add_u32 m0, %[wm], 0xa000\n\t" \
    "s_add_u32 s44, %[wo], 0x32000\n\t" \
    "buffer_load_dwordx4 v176, %[wr], s44 offen nt lds\n\t" \
    "s_add_u32 m0, %[wm], 0xc000\n\t" \
    "s_add_u32 s44, %[wo], 0x3c000\n\t" \
    "buffer_load_dwordx4 v176, %[wr], s44 offen nt lds\n\t" \
    "s_add_u32 m0, %[wm], 0xe000\n\t" \
    "s_add_u32 s44, %[wo], 0x46000\n\t" \
    "buffer_load_dwordx4 v176, %[wr], s44 offen nt lds\n\t" \
    "s_add_u32 m0, %[wm], 0x10000\n\t" \
    "s_add_u32 s44, %[wo], 0x50000\n\t" \
    "buffer_load_dwordx4 v176, %[wr], s44 offen nt lds\n\t" \
    "s_add_u32 m0, %[wm], 0x12000\n\t" \
    "s_add_u32 s44, %[wo], 0x5a000\n\t" \
    "buffer_load_dwordx4 v176, %[wr], s44 offen nt lds\n\t" \
    "s_add_u32 m0, %[wm], 0x14000\n\t" \
    "s_add_u32 s44, %[wo], 0x64000\n\t" \
    "buffer_load_dwordx4 v176, %[wr], s44 offen nt lds\n\t" \
    "s_add_u32 m0, %[wm], 0x16000\n\t" \
    "s_add_u32 s44, %[wo], 0x6e000\n\t" \
    "buffer_load_dwordx4 v176, %[wr], s44 offen nt lds\n\t" \
    "s_add_u32 m0, %[wm], 0x18000\n\t" \
    "s_add_u32 s44, %[wo], 0x78000\n\t" \
    "buffer_load_dwordx4 v176, %[wr], s44 offen nt lds\n\t" \
    "s_add_u32 m0, %[wm], 0x1a000\n\t" \
    "s_add_u32 s44, %[wo], 0x82000\n\t" \
    "buffer_load_dwordx4 v176, %[wr], s44 offen nt lds\n\t" \
    "s_add_u32 m0, %[wm], 0x1c000\n\t" \
    "s_add_u32 s44, %[wo], 0x8c000\n\t" \
    "buffer_load_dwordx4 v176, %[wr], s44 offen nt lds\n\t" \
    "s_add_u32 m0, %[wm], 0x1e000\n\t" \
    "s_add_u32 s44, %[wo], 0x96000\n\t" \
    "buffer_load_dwordx4 v176, %[wr], s44 offen nt lds\n\t" \
    "s_add_u32 m0, %[wm], 0x20000\n\t" \
    "s_add_u32 s44, %[wo], 0xa0000\n\t" \
    "buffer_load_dwordx4 v176, %[wr], s44 offen nt lds\n\t" \
    "s_add_u32 m0, %[wm], 0x22000\n\t" \
    "s_add_u32 s44, %[wo], 0xaa000\n\t" \
    "buffer_load_dwordx4 v176, %[wr], s44 offen nt lds\n\t" \
    "s_add_u32 m0, %[wm], 0x24000\n\t" \
    "s_add_u32 s44, %[wo], 0xb4000\n\t" \
    "buffer_load_dwordx4 v176, %[wr], s44 offen nt lds\n\t" \
    "s_add_u32 m0, %[wm], 0x26000\n\t" \
    "s_add_u32 s44, %[wo], 0xbe000\n\t" \
    "buffer_load_dwordx4 v176, %[wr], s44 offen nt lds\n\t" \
    "s_waitcnt vmcnt(0) lgkmcnt(0)\n\t" \
    "s_barrier \n\t" \
    "v_add_u32 v178, 0x0, v177\n\t" \
    "ds_read_b64_tr_b16 v[0:1], v178 offset:0\n\t" \
    "ds_read_b64_tr_b16 v[2:3], v178 offset:1024\n\t" \
    "ds_read_b64_tr_b16 v[4:5], v178 offset:8192\n\t" \
    "ds_read_b64_tr_b16 v[6:7], v178 offset:9216\n\t" \
    "ds_read_b64_tr_b16 v[8:9], v178 offset:16384\n\t" \
    "ds_read_b64_tr_b16 v[10:11], v178 offset:17408\n\t" \
    "ds_read_b64_tr_b16 v[12:13], v178 offset:24576\n\t" \
    "ds_read_b64_tr_b16 v[14:15], v178 offset:25600\n\t" \
    "v_add_u32 v178, 0x8000, v177\n\t" \
    "ds_read_b64_tr_b16 v[16:17], v178 offset:0\n\t" \
    "ds_read_b64_tr_b16 v[18:19], v178 offset:1024\n\t" \
    "ds_read_b64_tr_b16 v[20:21], v178 offset:8192\n\t" \
    "ds_read_b64_tr_b16 v[22:23], v178 offset:9216\n\t" \
    "ds_read_b64_tr_b16 v[24:25], v178 offset:16384\n\t" \
    "ds_read_b64_tr_b16 v[26:27], v178 offset:17408\n\t" \
    "ds_read_b64_tr_b16 v[28:29], v178 offset:24576\n\t" \
    "ds_read_b64_tr_b16 v[30:31], v178 offset:25600\n\t" \
    "v_add_u32 v178, 0x10000, v177\n\t" \
    "ds_read_b64_tr_b16 v[32:33], v178 offset:0\n\t" \
    "ds_read_b64_tr_b16 v[34:35], v178 offset:1024\n\t" \
    "ds_read_b64_tr_b16 v[36:37], v178 offset:8192\n\t" \
    "ds_read_b64_tr_b16 v[38:39], v178 offset:9216\n\t" \
    "ds_read_b64_tr_b16 v[40:41], v178 offset:16384\n\t" \
    "ds_read_b64_tr_b16 v[42:43], v178 offset:17408\n\t" \
    "ds_read_b64_tr_b16 v[44:45], v178 offset:24576\n\t" \
    "ds_read_b64_tr_b16 v[46:47], v178 offset:25600\n\t" \
    "v_add_u32 v178, 0x18000, v177\n\t" \
    "ds_read_b64_tr_b16 v[48:49], v178 offset:0\n\t" \
    "ds_read_b64_tr_b16 v[50:51], v178 offset:1024\n\t" \
    "ds_read_b64_tr_b16 v[52:53], v178 offset:8192\n\t" \
    "ds_read_b64_tr_b16 v[54:55], v178 offset:9216\n\t" \
    "ds_read_b64_tr_b16 v[56:57], v178 offset:16384\n\t" \
    "ds_read_b64_tr_b16 v[58:59], v178 offset:17408\n\t" \
    "ds_read_b64_tr_b16 v[60:61], v178 offset:24576\n\t" \
    "ds_read_b64_tr_b16 v[62:63], v178 offset:25600\n\t" \
    "v_add_u32 v178, 0x20000, v177\n\t" \
    "ds_read_b64_tr_b16 v[64:65], v178 offset:0\n\t" \
    "ds_read_b64_tr_b16 v[66:67], v178 offset:1024\n\t" \
    "ds_read_b64_tr_b16 v[68:69], v178 offset:8192\n\t" \
    "ds_read_b64_tr_b16 v[70:71], v178 offset:9216\n\t" \
    "ds_read_b64_tr_b16 v[72:73], v178 offset:16384\n\t" \
    "ds_read_b64_tr_b16 v[74:75], v178 offset:17408\n\t" \
    "ds_read_b64_tr_b16 v[76:77], v178 offset:24576\n\t" \
    "ds_read_b64_tr_b16 v[78:79], v178 offset:25600\n\t" \
    "v_mov_b32 v220, 0x3f803f80\n\t" \
    "v_mov_b32 v221, 0x3f803f80\n\t" \
    "v_mov_b32 v222, 0x3f803f80\n\t" \
    "v_mov_b32 v223, 0x3f803f80\n\t" \
    "v_mov_b32 v216, 0\n\t" \
    "v_mov_b32 v217, 0\n\t" \
    "v_mov_b32 v218, 0\n\t" \
    "v_mov_b32 v219, 0\n\t" \
    "s_waitcnt lgkmcnt(0)\n\t" \
    "s_barrier \n\t" \
    "ds_write_b32 v251, v220 offset:72\n\t" \
    "ds_write_b32 v251, v220 offset:2120\n\t" \
    "v_mfma_f32_16x16x32_bf16 v[160:163], v[0:3], v[220:223], 0\n\t" \
    "v_mfma_f32_16x16x32_bf16 v[160:163], v[4:7], v[220:223], v[160:163]\n\t" \
    "v_mfma_f32_16x16x32_bf16 v[160:163], v[8:11], v[220:223], v[160:163]\n\t" \
    "v_mfma_f32_16x16x32_bf16 v[160:163], v[12:15], v[220:223], v[160:163]\n\t" \
    "v_mfma_f32_16x16x32_bf16 v[160:163], v[16:19], v[220:223], v[160:163]\n\t" \
    "v_mfma_f32_16x16x32_bf16 v[160:163], v[20:23], v[220:223], v[160:163]\n\t" \
    "v_mfma_f32_16x16x32_bf16 v[160:163], v[24:27], v[220:223], v[160:163]\n\t" \
    "v_mfma_f32_16x16x32_bf16 v[160:163], v[28:31], v[220:223], v[160:163]\n\t" \
    "v_mfma_f32_16x16x32_bf16 v[160:163], v[32:35], v[220:223], v[160:163]\n\t" \
    "v_mfma_f32_16x16x32_bf16 v[160:163], v[36:39], v[220:223], v[160:163]\n\t" \
    "v_mfma_f32_16x16x32_bf16 v[160:163], v[40:43], v[220:223], v[160:163]\n\t" \
    "v_mfma_f32_16x16x32_bf16 v[160:163], v[44:47], v[220:223], v[160:163]\n\t" \
    "v_mfma_f32_16x16x32_bf16 v[160:163], v[48:51], v[220:223], v[160:163]\n\t" \
    "v_mfma_f32_16x16x32_bf16 v[160:163], v[52:55], v[220:223], v[160:163]\n\t" \
    "v_mfma_f32_16x16x32_bf16 v[160:163], v[56:59], v[220:223], v[160:163]\n\t" \
    "v_mfma_f32_16x16x32_bf16 v[160:163], v[60:63], v[220:223], v[160:163]\n\t" \
    "v_mfma_f32_16x16x32_bf16 v[160:163], v[64:67], v[220:223], v[160:163]\n\t" \
    "v_mfma_f32_16x16x32_bf16 v[160:163], v[68:71], v[220:223], v[160:163]\n\t" \
    "v_mfma_f32_16x16x32_bf16 v[160:163], v[72:75], v[220:223], v[160:163]\n\t" \
    "v_mfma_f32_16x16x32_bf16 v[160:163], v[76:79], v[220:223], v[160:163]\n\t" \
    "s_mov_b32 s52, 0x22222222\n\t" \
    "s_mov_b32 s53, 0x22222222\n\t" \
    "s_mov_b32 s54, 0x44444444\n\t" \
    "s_mov_b32 s55, 0x44444444\n\t" \
    "s_mov_b32 s56, 0x88888888\n\t" \
    "s_mov_b32 s57, 0x88888888\n\t" \
    ".Lw11_%=:\n\t" \
    "v_mov_b32 v248, 0\n\t" \
    "buffer_load_dword v248, v248, %[rs], %[go] offen sc1\n\t" \
    "s_waitcnt vmcnt(0)\n\t" \
    "v_cmp_ne_u32 vcc, 0, v248\n\t" \
    "s_cbranch_vccnz .Lw12_%=\n\t" \
    "s_cmp_eq_u32 s41, 0\n\t" \
    "s_cbranch_scc1 .Lw12_%=\n\t" \
    "s_sub_u32 s41, s41, 1\n\t" \
    "s_sleep 16\n\t" \
    "s_branch .Lw11_%=\n\t" \
    ".Lw12_%=:\n\t" \
    "s_sub_i32 s60, 0, %[ng]\n\t" \
    "s_sub_i32 s61, 1, %[ng]\n\t" \
    "s_add_i32 s62, s60, 6\n\t" \
    "s_add_i32 s63, s61, 6\n\t" \
    "s_mul_i32 s42, s61, 127\n\t" \
    "s_mul_i32 s44, %[ng], 0x733000\n\t" \
    "s_add_u32 s44, s44, 0x2400000\n\t" \
    "v_cmp_ne_u32 s[58:59], %[ng], v255\n\t" \
    "s_nop 1\n\t" \
    "s_mov_b64 exec, s[58:59]\n\t" \
    "buffer_store_dwordx2 v[216:217], v254, %[rs], s44 offen sc1\n\t" \
    "s_mov_b64 exec, -1\n\t" \
    "s_mov_b32 s43, 0\n\t" \
    "s_add_u32 s46, s43, %[pi]\n\t" \
    "s_add_u32 s47, s46, 0x8000\n\t" \
    "s_add_u32 s48, s46, 0x10000\n\t" \
    "buffer_load_dwordx4 v[208:211], v253, %[rs], s46 offen sc1\n\t" \
    "buffer_load_dwordx4 v[212:215], v253, %[rs], s47 offen sc1\n\t" \
    "s_cmp_gt_u32 %[wave], 3\n\t" \
    "s_cbranch_scc1 .Lw13_%=\n\t" \
    "buffer_load_dwordx4 v[216:219], v253, %[rs], s48 offen sc1\n\t" \
    ".Lw13_%=:\n\t" \
    "s_waitcnt vmcnt(0)\n\t" \
    "v_lshrrev_b32 v82, 1, v253\n\t" \
    ".Lw14_%=:\n\t" \
    "s_add_i32 s44, s40, s60\n\t" \
    "s_max_i32 s44, s44, 0\n\t" \
    "s_lshr_b32 s45, s44, 1\n\t" \
    "s_and_b32 s44, s44, 1\n\t" \
    "s_lshl_b32 s44, s44, 12\n\t" \
    "s_mul_i32 s49, s45, 0x14000\n\t" \
    "s_add_u32 s49, s49, s44\n\t" \
    "s_lshr_b32 s45, s45, 4\n\t" \
    "s_mul_i32 s45, s45, 0xc2000\n\t" \
    "s_add_u32 s49, s49, s45\n\t" \
    "s_add_i32 s44, s40, s61\n\t" \
    "s_max_i32 s44, s44, 0\n\t" \
    "s_lshr_b32 s45, s44, 1\n\t" \
    "s_and_b32 s44, s44, 1\n\t" \
    "s_lshl_b32 s44, s44, 12\n\t" \
    "s_mul_i32 s50, s45, 0x14000\n\t" \
    "s_add_u32 s50, s50, s44\n\t" \
    "s_lshr_b32 s45, s45, 4\n\t" \
    "s_mul_i32 s45, s45, 0xc2000\n\t" \
    "s_add_u32 s50, s50, s45\n\t" \
    "s_sub_u32 s50, s50, s49\n\t" \
    "s_add_u32 s49, s49, 0x2400000\n\t" \
    "v_mad_u32_u24 v80, v255, s50, v254\n\t" \
    "s_lshr_b32 s44, s40, 6\n\t" \
    "s_lshl_b32 s44, s44, 14\n\t" \
    "s_and_b32 s45, s40, 63\n\t" \
    "s_lshl_b32 s45, s45, 7\n\t" \
    "s_add_u32 s44, s44, s45\n\t" \
    "s_add_u32 s50, s44, 0x4d0000\n\t" \
    "s_add_i32 s44, s40, 1\n\t" \
    "s_max_i32 s44, s44, 0\n\t" \
    "s_lshr_b32 s45, s44, 1\n\t" \
    "s_and_b32 s44, s44, 1\n\t" \
    "s_lshl_b32 s44, s44, 12\n\t" \
    "s_mul_i32 s43, s45, 0x14000\n\t" \
    "s_add_u32 s43, s43, s44\n\t" \
    "s_lshr_b32 s45, s45, 4\n\t" \
    "s_mul_i32 s45, s45, 0xc2000\n\t" \
    "s_add_u32 s43, s43, s45\n\t" \
    LST0 \
    "v_max3_u32 v248, v208, v209, v210\n\t" \
    "v_max3_u32 v248, v211, v212, v248\n\t" \
    "v_max3_u32 v248, v213, v214, v248\n\t" \
    "v_max3_u32 v248, v215, v216, v248\n\t" \
    "v_max3_u32 v248, v217, v218, v248\n\t" \
    "v_max_u32 v248, v219, v248\n\t" \
    "v_cmp_eq_u32 vcc, -1, v248\n\t" \
    "s_cbranch_vccz .Lw16_%=\n\t" \
    ".Lw15_%=:\n\t" \
    "v_max_u32 v248, v208, v209\n\t" \
    "v_max3_u32 v248, v210, v211, v248\n\t" \
    "v_cmp_eq_u32 vcc, -1, v248\n\t" \
    "s_cbranch_vccz .Lw17_%=\n\t" \
    "buffer_load_dwordx4 v[208:211], v253, %[rs], s46 offen sc1\n\t" \
    ".Lw17_%=:\n\t" \
    "v_max_u32 v248, v212, v213\n\t" \
    "v_max3_u32 v248, v214, v215, v248\n\t" \
    "v_cmp_eq_u32 vcc, -1, v248\n\t" \
    "s_cbranch_vccz .Lw18_%=\n\t" \
    "buffer_load_dwordx4 v[212:215], v253, %[rs], s47 offen sc1\n\t" \
    ".Lw18_%=:\n\t" \
    "v_max_u32 v248, v216, v217\n\t" \
    "v_max3_u32 v248, v218, v219, v248\n\t" \
    "v_cmp_eq_u32 vcc, -1, v248\n\t" \
    "s_cbranch_vccz .Lw19_%=\n\t" \
    "buffer_load_dwordx4 v[216:219], v253, %[rs], s48 offen sc1\n\t" \
    ".Lw19_%=:\n\t" \
    "s_waitcnt vmcnt(20)\n\t" \
    "v_max3_u32 v248, v208, v209, v210\n\t" \
    "v_max3_u32 v248, v211, v212, v248\n\t" \
    "v_max3_u32 v248, v213, v214, v248\n\t" \
    "v_max3_u32 v248, v215, v216, v248\n\t" \
    "v_max3_u32 v248, v217, v218, v248\n\t" \
    "v_max_u32 v248, v219, v248\n\t" \
    "v_cmp_eq_u32 vcc, -1, v248\n\t" \
    "s_cbranch_vccz .Lw16_%=\n\t" \
    "s_sleep 1\n\t" \
    "v_max3_u32 v248, v208, v209, v210\n\t" \
    "v_max3_u32 v248, v211, v212, v248\n\t" \
    "v_max3_u32 v248, v213, v214, v248\n\t" \
    "v_max3_u32 v248, v215, v216, v248\n\t" \
    "v_max3_u32 v248, v217, v218, v248\n\t" \
    "v_max_u32 v248, v219, v248\n\t" \
    "v_cmp_eq_u32 vcc, -1, v248\n\t" \
    "s_cbranch_vccz .Lw16_%=\n\t" \
    "s_sleep 1\n\t" \
    "v_max3_u32 v248, v208, v209, v210\n\t" \
    "v_max3_u32 v248, v211, v212, v248\n\t" \
    "v_max3_u32 v248, v213, v214, v248\n\t" \
    "v_max3_u32 v248, v215, v216, v248\n\t" \
    "v_max3_u32 v248, v217, v218, v248\n\t" \
    "v_max_u32 v248, v219, v248\n\t" \
    "v_cmp_eq_u32 vcc, -1, v248\n\t" \
    "s_cbranch_vccz .Lw16_%=\n\t" \
    "s_sleep 1\n\t" \
    "v_max3_u32 v248, v208, v209, v210\n\t" \
    "v_max3_u32 v248, v211, v212, v248\n\t" \
    "v_max3_u32 v248, v213, v214, v248\n\t" \
    "v_max3_u32 v248, v215, v216, v248\n\t" \
    "v_max3_u32 v248, v217, v218, v248\n\t" \
    "v_max_u32 v248, v219, v248\n\t" \
    "v_cmp_eq_u32 vcc, -1, v248\n\t" \
    "s_cbranch_vccz .Lw16_%=\n\t" \
    "s_sleep 1\n\t" \
    "v_max3_u32 v248, v208, v209, v210\n\t" \
    "v_max3_u32 v248, v211, v212, v248\n\t" \
    "v_max3_u32 v248, v213, v214, v248\n\t" \
    "v_max3_u32 v248, v215, v216, v248\n\t" \
    "v_max3_u32 v248, v217, v218, v248\n\t" \
    "v_max_u32 v248, v219, v248\n\t" \
    "v_cmp_eq_u32 vcc, -1, v248\n\t" \
    "s_cbranch_vccz .Lw16_%=\n\t" \
    "s_sleep 1\n\t" \
    "v_max3_u32 v248, v208, v209, v210\n\t" \
    "v_max3_u32 v248, v211, v212, v248\n\t" \
    "v_max3_u32 v248, v213, v214, v248\n\t" \
    "v_max3_u32 v248, v215, v216, v248\n\t" \
    "v_max3_u32 v248, v217, v218, v248\n\t" \
    "v_max_u32 v248, v219, v248\n\t" \
    "v_cmp_eq_u32 vcc, -1, v248\n\t" \
    "s_cbranch_vccz .Lw16_%=\n\t" \
    "s_sleep 1\n\t" \
    "s_cmp_eq_u32 s41, 0\n\t" \
    "s_cbranch_scc1 .Lw16_%=\n\t" \
    "s_sub_u32 s41, s41, 1\n\t" \
    "s_branch .Lw15_%=\n\t" \
    ".Lw16_%=:\n\t" \
    LST1 \
    "ds_write_b128 v250, v[208:211]\n\t" \
    "ds_write_b128 v250, v[212:215] offset:8192\n\t" \
    "s_cmp_gt_u32 %[wave], 3\n\t" \
    "s_cbranch_scc1 .Lw110_%=\n\t" \
    "ds_write_b128 v250, v[216:219] offset:16384\n\t" \
    ".Lw110_%=:\n\t" \
    "v_mfma_f32_16x16x32_bf16 v[228:231], v[208:211], v[208:211], 0\n\t" \
    "v_mfma_f32_16x16x32_bf16 v[228:231], v[212:215], v[212:215], v[228:231]\n\t" \
    "v_mfma_f32_16x16x32_bf16 v[228:231], v[216:219], v[216:219], v[228:231]\n\t" \
    "v_mfma_f32_16x16x32_bf16 v[224:227], v[220:223], v[208:211], 0\n\t" \
    "v_mfma_f32_16x16x32_bf16 v[224:227], v[220:223], v[212:215], v[224:227]\n\t" \
    "v_mfma_f32_16x16x32_bf16 v[224:227], v[220:223], v[216:219], v[224:227]\n\t" \
    "v_cndmask_b32 v228, v228, v229, s[52:53]\n\t" \
    "v_cndmask_b32 v228, v228, v230, s[54:55]\n\t" \
    "v_cndmask_b32 v228, v228, v231, s[56:57]\n\t" \
    "s_add_u32 s46, s43, %[pi]\n\t" \
    "s_add_u32 s47, s46, 0x8000\n\t" \
    "s_add_u32 s48, s46, 0x10000\n\t" \
    "s_mov_b32 exec_lo, 0xf0000f\n\t" \
    "s_mov_b32 exec_hi, 0xf0000f00\n\t" \
    "s_nop 4\n\t" \
    "ds_write2_b32 v252, v224, v228 offset1:1\n\t" \
    "s_mov_b64 exec, -1\n\t" \
    "s_waitcnt lgkmcnt(0)\n\t" \
    "s_barrier \n\t" \
    LST2 \
    "s_cmp_lg_u32 %[wave], 4\n\t" \
    "s_cbranch_scc1 .Lw111_%=\n\t" \
    "s_setprio 1\n\t" \
    "ds_read_b128 v[232:235], v251 offset:0\n\t" \
    "ds_read_b128 v[236:239], v251 offset:16\n\t" \
    "ds_read_b128 v[240:243], v251 offset:32\n\t" \
    "ds_read_b128 v[244:247], v251 offset:48\n\t" \
    "s_waitcnt lgkmcnt(0)\n\t" \
    "v_add_f32 v232, v232, v240\n\t" \
    "v_add_f32 v233, v233, v241\n\t" \
    "v_add_f32 v234, v234, v242\n\t" \
    "v_add_f32 v235, v235, v243\n\t" \
    "v_add_f32 v236, v236, v244\n\t" \
    "v_add_f32 v237, v237, v245\n\t" \
    "v_add_f32 v238, v238, v246\n\t" \
    "v_add_f32 v239, v239, v247\n\t" \
    "v_add_f32 v232, v232, v236\n\t" \
    "v_add_f32 v233, v233, v237\n\t" \
    "v_add_f32 v234, v234, v238\n\t" \
    "v_add_f32 v235, v235, v239\n\t" \
    "v_add_f32 v232, v232, v234\n\t" \
    "v_add_f32 v233, v233, v235\n\t" \
    "v_mul_f32 v232, 0x3acccccd, v232\n\t" \
    "v_mul_f32 v233, 0x3acccccd, v233\n\t" \
    "v_fma_f32 v233, -v232, v232, v233\n\t" \
    "v_mov_b32 v234, v232\n\t" \
    "v_max_f32 v233, 0, v233\n\t" \
    "v_cvt_pk_bf16_f32 v234, v234, v234\n\t" \
    "v_and_b32 v234, 0xffff0000, v234\n\t" \
    "v_mov_b32 v236, v233\n\t" \
    "v_cvt_pk_bf16_f32 v236, v236, v236\n\t" \
    "v_and_b32 v236, 0xffff0000, v236\n\t" \
    "v_add_f32 v236, 0x3727c5ac, v236\n\t" \
    "v_cvt_pk_bf16_f32 v236, v236, v236\n\t" \
    "v_and_b32 v236, 0xffff0000, v236\n\t" \
    "v_sqrt_f32 v236, v236\n\t" \
    "s_nop 0\n\t" \
    "v_cvt_pk_bf16_f32 v236, v236, v236\n\t" \
    "v_and_b32 v236, 0xffff0000, v236\n\t" \
    "v_rcp_f32 v235, v236\n\t" \
    "s_nop 0\n\t" \
    "v_sub_f32 v232, v232, v234\n\t" \
    "v_mul_f32 v233, v233, v235\n\t" \
    "v_mul_f32 v232, v232, v235\n\t" \
    "v_mul_f32 v233, v233, v235\n\t" \
    "v_cvt_pk_bf16_f32 v232, v232, v232\n\t" \
    "v_and_b32 v232, 0xffff0000, v232\n\t" \
    "v_cvt_pk_bf16_f32 v233, v233, v233\n\t" \
    "v_and_b32 v233, 0xffff0000, v233\n\t" \
    "v_add_f32 v233, 0x3727c5ac, v233\n\t" \
    "v_cvt_pk_bf16_f32 v233, v233, v233\n\t" \
    "v_and_b32 v233, 0xffff0000, v233\n\t" \
    "v_sqrt_f32 v233, v233\n\t" \
    "s_nop 0\n\t" \
    "v_cvt_pk_bf16_f32 v233, v233, v233\n\t" \
    "v_and_b32 v233, 0xffff0000, v233\n\t" \
    "v_rcp_f32 v233, v233\n\t" \
    "v_fma_f32 v232, v232, v236, v234\n\t" \
    "v_mul_f32 v237, v235, v233\n\t" \
    "v_mul_f32 v238, -v232, v237\n\t" \
    "v_min_u32 v234, -2, v234\n\t" \
    "v_min_u32 v235, -2, v235\n\t" \
    "v_mov_b32 v239, s40\n\t" \
    "ds_write2_b32 v251, v237, v238 offset0:16 offset1:17\n\t" \
    "ds_write_b32 v251, v239 offset:72\n\t" \
    "s_setprio 0\n\t" \
    "s_cmp_eq_u32 %[ss], 0\n\t" \
    "s_cbranch_scc1 .Lw112_%=\n\t" \
    "s_mov_b64 exec, 0xffff\n\t" \
    "buffer_store_dwordx2 v[234:235], v82, %[rs], s50 offen sc1\n\t" \
    "s_mov_b64 exec, -1\n\t" \
    ".Lw112_%=:\n\t" \
    ".Lw111_%=:\n\t" \
    "ds_read_b128 v[176:179], v249 offset:0\n\t" \
    "ds_read_b128 v[180:183], v249 offset:1024\n\t" \
    "ds_read_b128 v[184:187], v249 offset:2048\n\t" \
    "ds_read_b128 v[188:191], v249 offset:3072\n\t" \
    "ds_read_b128 v[192:195], v249 offset:4096\n\t" \
    "ds_read_b128 v[196:199], v249 offset:5120\n\t" \
    "ds_read_b128 v[200:203], v249 offset:6144\n\t" \
    "buffer_load_dwordx4 v[208:211], v253, %[rs], s46 offen sc1\n\t" \
    "buffer_load_dwordx4 v[212:215], v253, %[rs], s47 offen sc1\n\t" \
    "s_cmp_gt_u32 %[wave], 3\n\t" \
    "s_cbranch_scc1 .Lw113_%=\n\t" \
    "buffer_load_dwordx4 v[216:219], v253, %[rs], s48 offen sc1\n\t" \
    ".Lw113_%=:\n\t" \
    "s_waitcnt lgkmcnt(6)\n\t" \
    "v_mfma_f32_16x16x32_bf16 v[168:171], v[0:3], v[176:179], 0\n\t" \
    "ds_read_b128 v[204:207], v249 offset:7168\n\t" \
    "s_waitcnt lgkmcnt(6)\n\t" \
    "v_mfma_f32_16x16x32_bf16 v[168:171], v[4:7], v[180:183], v[168:171]\n\t" \
    "ds_read_b128 v[176:179], v249 offset:8192\n\t" \
    "s_waitcnt lgkmcnt(6)\n\t" \
    "v_mfma_f32_16x16x32_bf16 v[168:171], v[8:11], v[184:187], v[168:171]\n\t" \
    "ds_read_b128 v[180:183], v249 offset:9216\n\t" \
    "s_waitcnt lgkmcnt(6)\n\t" \
    "v_mfma_f32_16x16x32_bf16 v[168:171], v[12:15], v[188:191], v[168:171]\n\t" \
    "ds_read_b128 v[184:187], v249 offset:10240\n\t" \
    "s_waitcnt lgkmcnt(6)\n\t" \
    "v_mfma_f32_16x16x32_bf16 v[168:171], v[16:19], v[192:195], v[168:171]\n\t" \
    "ds_read_b128 v[188:191], v249 offset:11264\n\t" \
    "s_waitcnt lgkmcnt(6)\n\t" \
    "v_mfma_f32_16x16x32_bf16 v[168:171], v[20:23], v[196:199], v[168:171]\n\t" \
    "ds_read_b128 v[192:195], v249 offset:12288\n\t" \
    "s_waitcnt lgkmcnt(6)\n\t" \
    "v_mfma_f32_16x16x32_bf16 v[168:171], v[24:27], v[200:203], v[168:171]\n\t" \
    "ds_read_b128 v[196:199], v249 offset:13312\n\t" \
    "s_waitcnt lgkmcnt(6)\n\t" \
    "v_mfma_f32_16x16x32_bf16 v[168:171], v[28:31], v[204:207], v[168:171]\n\t" \
    "ds_read_b128 v[200:203], v249 offset:14336\n\t" \
    "s_waitcnt lgkmcnt(6)\n\t" \
    "v_mfma_f32_16x16x32_bf16 v[168:171], v[32:35], v[176:179], v[168:171]\n\t" \
    "ds_read_b128 v[204:207], v249 offset:15360\n\t" \
    "s_waitcnt lgkmcnt(6)\n\t" \
    "v_mfma_f32_16x16x32_bf16 v[168:171], v[36:39], v[180:183], v[168:171]\n\t" \
    "ds_read_b128 v[176:179], v249 offset:16384\n\t" \
    "s_waitcnt lgkmcnt(6)\n\t" \
    "v_mfma_f32_16x16x32_bf16 v[168:171], v[40:43], v[184:187], v[168:171]\n\t" \
    "ds_read_b128 v[180:183], v249 offset:17408\n\t" \
    "s_waitcnt lgkmcnt(6)\n\t" \
    "v_mfma_f32_16x16x32_bf16 v[168:171], v[44:47], v[188:191], v[168:171]\n\t" \
    "ds_read_b128 v[184:187], v249 offset:18432\n\t" \
    "s_waitcnt lgkmcnt(6)\n\t" \
    "v_mfma_f32_16x16x32_bf16 v[168:171], v[48:51], v[192:195], v[168:171]\n\t" \
    "ds_read_b128 v[188:191], v249 offset:19456\n\t" \
    "s_waitcnt lgkmcnt(6)\n\t" \
    "v_mfma_f32_16x16x32_bf16 v[168:171], v[52:55], v[196:199], v[168:171]\n\t" \
    "ds_read_b32 v236, v251 offset:72\n\t" \
    "ds_read_b64 v[238:239], v251 offset:64\n\t" \
    "s_waitcnt lgkmcnt(7)\n\t" \
    "v_mfma_f32_16x16x32_bf16 v[168:171], v[56:59], v[200:203], v[168:171]\n\t" \
    "s_waitcnt lgkmcnt(6)\n\t" \
    "v_mfma_f32_16x16x32_bf16 v[168:171], v[60:63], v[204:207], v[168:171]\n\t" \
    "s_waitcnt lgkmcnt(5)\n\t" \
    "v_mfma_f32_16x16x32_bf16 v[168:171], v[64:67], v[176:179], v[168:171]\n\t" \
    "s_waitcnt lgkmcnt(4)\n\t" \
    "v_mfma_f32_16x16x32_bf16 v[168:171], v[68:71], v[180:183], v[168:171]\n\t" \
    ".Lw114_%=:\n\t" \
    "s_waitcnt lgkmcnt(0)\n\t" \
    "v_cmp_ne_u32 vcc, s40, v236\n\t" \
    "s_cbranch_vccz .Lw115_%=\n\t" \
    "ds_read_b32 v236, v251 offset:72\n\t" \
    "ds_read_b64 v[238:239], v251 offset:64\n\t" \
    "s_branch .Lw114_%=\n\t" \
    ".Lw115_%=:\n\t" \
    "s_waitcnt lgkmcnt(0)\n\t" \
    "v_mul_f32 v240, v160, v239\n\t" \
    "v_mul_f32 v241, v161, v239\n\t" \
    "v_mfma_f32_16x16x32_bf16 v[168:171], v[72:75], v[184:187], v[168:171]\n\t" \
    "v_mul_f32 v242, v162, v239\n\t" \
    "v_mul_f32 v243, v163, v239\n\t" \
    "v_mfma_f32_16x16x32_bf16 v[168:171], v[76:79], v[188:191], v[168:171]\n\t" \
    "v_mov_b32 v237, 0xc0135761\n\t" \
    LST3 \
    "s_nop 6\n\t" \
    "v_fma_f32 v168, v168, v238, v240\n\t" \
    "v_fma_f32 v169, v169, v238, v241\n\t" \
    "v_fma_f32 v170, v170, v238, v242\n\t" \
    "v_fma_f32 v171, v171, v238, v243\n\t" \
    "v_mul_f32 v176, v168, v168\n\t" \
    "v_mul_f32 v177, v169, v169\n\t" \
    "v_mul_f32 v178, v170, v170\n\t" \
    "v_mul_f32 v179, v171, v171\n\t" \
    "v_fmamk_f32 v176, v176, 0xbdd2d3e7, v237\n\t" \
    "v_fmamk_f32 v177, v177, 0xbdd2d3e7, v237\n\t" \
    "v_fmamk_f32 v178, v178, 0xbdd2d3e7, v237\n\t" \
    "v_fmamk_f32 v179, v179, 0xbdd2d3e7, v237\n\t" \
    "v_mul_f32 v176, v168, v176\n\t" \
    "v_mul_f32 v177, v169, v177\n\t" \
    "v_mul_f32 v178, v170, v178\n\t" \
    "v_mul_f32 v179, v171, v179\n\t" \
    "v_exp_f32 v176, v176\n\t" \
    "v_exp_f32 v177, v177\n\t" \
    "v_exp_f32 v178, v178\n\t" \
    "v_exp_f32 v179, v179\n\t" \
    "v_add_f32 v176, 1.0, v176\n\t" \
    "v_add_f32 v177, 1.0, v177\n\t" \
    "v_add_f32 v178, 1.0, v178\n\t" \
    "v_add_f32 v179, 1.0, v179\n\t" \
    "v_rcp_f32 v176, v176\n\t" \
    "v_rcp_f32 v177, v177\n\t" \
    "v_rcp_f32 v178, v178\n\t" \
    "v_rcp_f32 v179, v179\n\t" \
    "v_mul_f32 v168, v168, v176\n\t" \
    "v_mul_f32 v169, v169, v177\n\t" \
    "v_mul_f32 v170, v170, v178\n\t" \
    "v_mul_f32 v171, v171, v179\n\t" \
    "s_waitcnt vmcnt(0)\n\t" \
    "v_cvt_pk_bf16_f32 v184, v168, v169\n\t" \
    "v_cvt_pk_bf16_f32 v185, v170, v171\n\t" \
    "v_min_u32 v184, -2, v184\n\t" \
    "v_min_u32 v185, -2, v185\n\t" \
    "s_cmp_eq_u32 s40, s42\n\t" \
    "s_cselect_b64 s[44:45], s[58:59], 0\n\t" \
    "s_andn2_b64 exec, -1, s[44:45]\n\t" \
    "buffer_store_dwordx2 v[184:185], v80, %[rs], s49 offen sc1\n\t" \
    "s_mov_b64 exec, -1\n\t" \
    LST4 \
    "s_cmp_ge_u32 s40, 122\n\t" \
    "s_cbranch_scc1 .Lw116_%=\n\t" \
    "s_add_i32 s44, s40, s62\n\t" \
    "s_max_i32 s44, s44, 0\n\t" \
    "s_lshr_b32 s45, s44, 1\n\t" \
    "s_and_b32 s44, s44, 1\n\t" \
    "s_lshl_b32 s44, s44, 12\n\t" \
    "s_mul_i32 s51, s45, 0x14000\n\t" \
    "s_add_u32 s51, s51, s44\n\t" \
    "s_lshr_b32 s45, s45, 4\n\t" \
    "s_mul_i32 s45, s45, 0xc2000\n\t" \
    "s_add_u32 s51, s51, s45\n\t" \
    "s_add_i32 s44, s40, s63\n\t" \
    "s_max_i32 s44, s44, 0\n\t" \
    "s_lshr_b32 s45, s44, 1\n\t" \
    "s_and_b32 s44, s44, 1\n\t" \
    "s_lshl_b32 s44, s44, 12\n\t" \
    "s_mul_i32 s50, s45, 0x14000\n\t" \
    "s_add_u32 s50, s50, s44\n\t" \
    "s_lshr_b32 s45, s45, 4\n\t" \
    "s_mul_i32 s45, s45, 0xc2000\n\t" \
    "s_add_u32 s50, s50, s45\n\t" \
    "s_sub_u32 s50, s50, s51\n\t" \
    "s_add_u32 s51, s51, 0x2400000\n\t" \
    "v_mad_u32_u24 v81, v255, s50, v254\n\t" \
    "v_mov_b64 v[188:189], -1\n\t" \
    "s_add_u32 s44, s40, 6\n\t" \
    "s_cmp_eq_u32 s44, s42\n\t" \
    "s_cselect_b64 s[44:45], s[58:59], 0\n\t" \
    "s_andn2_b64 exec, -1, s[44:45]\n\t" \
    "buffer_store_dwordx2 v[188:189], v81, %[rs], s51 offen sc1\n\t" \
    "s_mov_b64 exec, -1\n\t" \
    ".Lw116_%=:\n\t" \
    "v_xor_b32 v249, 0x8000, v249\n\t" \
    "v_xor_b32 v250, 0x8000, v250\n\t" \
    "v_xor_b32 v251, 0x800, v251\n\t" \
    "v_xor_b32 v252, 0x800, v252\n\t" \
    LFLUSHW \
    "s_add_u32 s40, s40, 1\n\t" \
    "s_cmp_lg_u32 s40, 128\n\t" \
    "s_cbranch_scc1 .Lw14_%=\n\t" \
    "s_branch .Lend%=\n\t" \
    ".Lend%=:\n\t" \
    "s_waitcnt vmcnt(0) lgkmcnt(0)\n\t" \
    "s_mov_b32 %[bud], s41\n\t"
#define ASM_W2_LOOP \
    "s_mov_b32 s40, 0\n\t" \
    "s_mov_b32 s41, 0x20000\n\t" \
    LSTINIT \
    "s_add_u32 m0, %[wm], 0x0\n\t" \
    "s_add_u32 s44, %[wo], 0x0\n\t" \
    "buffer_load_dwordx4 v176, %[wr], s44 offen nt lds\n\t" \
    "s_add_u32 m0, %[wm], 0x2000\n\t" \
    "s_add_u32 s44, %[wo], 0xa000\n\t" \
    "buffer_load_dwordx4 v176, %[wr], s44 offen nt lds\n\t" \
    "s_add_u32 m0, %[wm], 0x4000\n\t" \
    "s_add_u32 s44, %[wo], 0x14000\n\t" \
    "buffer_load_dwordx4 v176, %[wr], s44 offen nt lds\n\t" \
    "s_add_u32 m0, %[wm], 0x6000\n\t" \
    "s_add_u32 s44, %[wo], 0x1e000\n\t" \
    "buffer_load_dwordx4 v176, %[wr], s44 offen nt lds\n\t" \
    "s_add_u32 m0, %[wm], 0x8000\n\t" \
    "s_add_u32 s44, %[wo], 0x28000\n\t" \
    "buffer_load_dwordx4 v176, %[wr], s44 offen nt lds\n\t" \
    "s_add_u32 m0, %[wm], 0xa000\n\t" \
    "s_add_u32 s44, %[wo], 0x32000\n\t" \
    "buffer_load_dwordx4 v176, %[wr], s44 offen nt lds\n\t" \
    "s_add_u32 m0, %[wm], 0xc000\n\t" \
    "s_add_u32 s44, %[wo], 0x3c000\n\t" \
    "buffer_load_dwordx4 v176, %[wr], s44 offen nt lds\n\t" \
    "s_add_u32 m0, %[wm], 0xe000\n\t" \
    "s_add_u32 s44, %[wo], 0x46000\n\t" \
    "buffer_load_dwordx4 v176, %[wr], s44 offen nt lds\n\t" \
    "s_add_u32 m0, %[wm], 0x10000\n\t" \
    "s_add_u32 s44, %[wo], 0x50000\n\t" \
    "buffer_load_dwordx4 v176, %[wr], s44 offen nt lds\n\t" \
    "s_add_u32 m0, %[wm], 0x12000\n\t" \
    "s_add_u32 s44, %[wo], 0x5a000\n\t" \
    "buffer_load_dwordx4 v176, %[wr], s44 offen nt lds\n\t" \
    "s_add_u32 m0, %[wm], 0x14000\n\t" \
    "s_add_u32 s44, %[wo], 0x64000\n\t" \
    "buffer_load_dwordx4 v176, %[wr], s44 offen nt lds\n\t" \
    "s_add_u32 m0, %[wm], 0x16000\n\t" \
    "s_add_u32 s44, %[wo], 0x6e000\n\t" \
    "buffer_load_dwordx4 v176, %[wr], s44 offen nt lds\n\t" \
    "s_add_u32 m0, %[wm], 0x18000\n\t" \
    "s_add_u32 s44, %[wo], 0x78000\n\t" \
    "buffer_load_dwordx4 v176, %[wr], s44 offen nt lds\n\t" \
    "s_add_u32 m0, %[wm], 0x1a000\n\t" \
    "s_add_u32 s44, %[wo], 0x82000\n\t" \
    "buffer_load_dwordx4 v176, %[wr], s44 offen nt lds\n\t" \
    "s_add_u32 m0, %[wm], 0x1c000\n\t" \
    "s_add_u32 s44, %[wo], 0x8c000\n\t" \
    "buffer_load_dwordx4 v176, %[wr], s44 offen nt lds\n\t" \
    "s_add_u32 m0, %[wm], 0x1e000\n\t" \
    "s_add_u32 s44, %[wo], 0x96000\n\t" \
    "buffer_load_dwordx4 v176, %[wr], s44 offen nt lds\n\t" \
    "s_add_u32 m0, %[wm], 0x20000\n\t" \
    "s_add_u32 s44, %[wo], 0xa0000\n\t" \
    "buffer_load_dwordx4 v176, %[wr], s44 offen nt lds\n\t" \
    "s_add_u32 m0, %[wm], 0x22000\n\t" \
    "s_add_u32 s44, %[wo], 0xaa000\n\t" \
    "buffer_load_dwordx4 v176, %[wr], s44 offen nt lds\n\t" \
    "s_add_u32 m0, %[wm], 0x24000\n\t" \
    "s_add_u32 s44, %[wo], 0xb4000\n\t" \
    "buffer_load_dwordx4 v176, %[wr], s44 offen nt lds\n\t" \
    "s_add_u32 m0, %[wm], 0x26000\n\t" \
    "s_add_u32 s44, %[wo], 0xbe000\n\t" \
    "buffer_load_dwordx4 v176, %[wr], s44 offen nt lds\n\t" \
    "s_waitcnt vmcnt(0) lgkmcnt(0)\n\t" \
    "s_barrier \n\t" \
    "v_add_u32 v178, 0x0, v177\n\t" \
    "ds_read_b64_tr_b16 v[0:1], v178 offset:0\n\t" \
    "ds_read_b64_tr_b16 v[2:3], v178 offset:1024\n\t" \
    "ds_read_b64_tr_b16 v[4:5], v178 offset:8192\n\t" \
    "ds_read_b64_tr_b16 v[6:7], v178 offset:9216\n\t" \
    "ds_read_b64_tr_b16 v[8:9], v178 offset:16384\n\t" \
    "ds_read_b64_tr_b16 v[10:11], v178 offset:17408\n\t" \
    "ds_read_b64_tr_b16 v[12:13], v178 offset:24576\n\t" \
    "ds_read_b64_tr_b16 v[14:15], v178 offset:25600\n\t" \
    "v_add_u32 v178, 0x8000, v177\n\t" \
    "ds_read_b64_tr_b16 v[16:17], v178 offset:0\n\t" \
    "ds_read_b64_tr_b16 v[18:19], v178 offset:1024\n\t" \
    "ds_read_b64_tr_b16 v[20:21], v178 offset:8192\n\t" \
    "ds_read_b64_tr_b16 v[22:23], v178 offset:9216\n\t" \
    "ds_read_b64_tr_b16 v[24:25], v178 offset:16384\n\t" \
    "ds_read_b64_tr_b16 v[26:27], v178 offset:17408\n\t" \
    "ds_read_b64_tr_b16 v[28:29], v178 offset:24576\n\t" \
    "ds_read_b64_tr_b16 v[30:31], v178 offset:25600\n\t" \
    "v_add_u32 v178, 0x10000, v177\n\t" \
    "ds_read_b64_tr_b16 v[32:33], v178 offset:0\n\t" \
    "ds_read_b64_tr_b16 v[34:35], v178 offset:1024\n\t" \
    "ds_read_b64_tr_b16 v[36:37], v178 offset:8192\n\t" \
    "ds_read_b64_tr_b16 v[38:39], v178 offset:9216\n\t" \
    "ds_read_b64_tr_b16 v[40:41], v178 offset:16384\n\t" \
    "ds_read_b64_tr_b16 v[42:43], v178 offset:17408\n\t" \
    "ds_read_b64_tr_b16 v[44:45], v178 offset:24576\n\t" \
    "ds_read_b64_tr_b16 v[46:47], v178 offset:25600\n\t" \
    "v_add_u32 v178, 0x18000, v177\n\t" \
    "ds_read_b64_tr_b16 v[48:49], v178 offset:0\n\t" \
    "ds_read_b64_tr_b16 v[50:51], v178 offset:1024\n\t" \
    "ds_read_b64_tr_b16 v[52:53], v178 offset:8192\n\t" \
    "ds_read_b64_tr_b16 v[54:55], v178 offset:9216\n\t" \
    "ds_read_b64_tr_b16 v[56:57], v178 offset:16384\n\t" \
    "ds_read_b64_tr_b16 v[58:59], v178 offset:17408\n\t" \
    "ds_read_b64_tr_b16 v[60:61], v178 offset:24576\n\t" \
    "ds_read_b64_tr_b16 v[62:63], v178 offset:25600\n\t" \
    "v_add_u32 v178, 0x20000, v177\n\t" \
    "ds_read_b64_tr_b16 v[64:65], v178 offset:0\n\t" \
    "ds_read_b64_tr_b16 v[66:67], v178 offset:1024\n\t" \
    "ds_read_b64_tr_b16 v[68:69], v178 offset:8192\n\t" \
    "ds_read_b64_tr_b16 v[70:71], v178 offset:9216\n\t" \
    "ds_read_b64_tr_b16 v[72:73], v178 offset:16384\n\t" \
    "ds_read_b64_tr_b16 v[74:75], v178 offset:17408\n\t" \
    "ds_read_b64_tr_b16 v[76:77], v178 offset:24576\n\t" \
    "ds_read_b64_tr_b16 v[78:79], v178 offset:25600\n\t" \
    "v_mov_b32 v220, 0x3f803f80\n\t" \
    "v_mov_b32 v221, 0x3f803f80\n\t" \
    "v_mov_b32 v222, 0x3f803f80\n\t" \
    "v_mov_b32 v223, 0x3f803f80\n\t" \
    "v_mov_b32 v216, 0\n\t" \
    "v_mov_b32 v217, 0\n\t" \
    "v_mov_b32 v218, 0\n\t" \
    "v_mov_b32 v219, 0\n\t" \
    "s_waitcnt lgkmcnt(0)\n\t" \
    "s_barrier \n\t" \
    ".Lw21_%=:\n\t" \
    "v_mov_b32 v248, 0\n\t" \
    "buffer_load_dword v248, v248, %[rs], %[go] offen sc1\n\t" \
    "s_waitcnt vmcnt(0)\n\t" \
    "v_cmp_ne_u32 vcc, 0, v248\n\t" \
    "s_cbranch_vccnz .Lw22_%=\n\t" \
    "s_cmp_eq_u32 s41, 0\n\t" \
    "s_cbranch_scc1 .Lw22_%=\n\t" \
    "s_sub_u32 s41, s41, 1\n\t" \
    "s_sleep 16\n\t" \
    "s_branch .Lw21_%=\n\t" \
    ".Lw22_%=:\n\t" \
    "s_mov_b32 s43, 0\n\t" \
    "s_add_u32 s46, s43, %[pi]\n\t" \
    "s_add_u32 s47, s46, 0x8000\n\t" \
    "s_add_u32 s48, s46, 0x10000\n\t" \
    "buffer_load_dwordx4 v[208:211], v253, %[rs], s46 offen sc1\n\t" \
    "buffer_load_dwordx4 v[212:215], v253, %[rs], s47 offen sc1\n\t" \
    "s_cmp_gt_u32 %[wave], 3\n\t" \
    "s_cbranch_scc1 .Lw23_%=\n\t" \
    "buffer_load_dwordx4 v[216:219], v253, %[rs], s48 offen sc1\n\t" \
    ".Lw23_%=:\n\t" \
    "s_waitcnt vmcnt(0)\n\t" \
    ".Lw24_%=:\n\t" \
    "s_add_u32 s49, s43, 0x2c00000\n\t" \
    "s_add_u32 s50, s43, 0x1c00000\n\t" \
    "s_lshr_b32 s44, s40, 6\n\t" \
    "s_lshl_b32 s44, s44, 14\n\t" \
    "s_and_b32 s45, s40, 63\n\t" \
    "s_lshl_b32 s45, s45, 7\n\t" \
    "s_add_u32 s44, s44, s45\n\t" \
    "s_add_u32 s51, s44, 0x4d0000\n\t" \
    "s_add_i32 s44, s40, 1\n\t" \
    "s_max_i32 s44, s44, 0\n\t" \
    "s_lshr_b32 s45, s44, 1\n\t" \
    "s_and_b32 s44, s44, 1\n\t" \
    "s_lshl_b32 s44, s44, 12\n\t" \
    "s_mul_i32 s43, s45, 0x14000\n\t" \
    "s_add_u32 s43, s43, s44\n\t" \
    "s_lshr_b32 s45, s45, 4\n\t" \
    "s_mul_i32 s45, s45, 0xc2000\n\t" \
    "s_add_u32 s43, s43, s45\n\t" \
    LST0 \
    "v_max3_u32 v248, v208, v209, v210\n\t" \
    "v_max3_u32 v248, v211, v212, v248\n\t" \
    "v_max3_u32 v248, v213, v214, v248\n\t" \
    "v_max3_u32 v248, v215, v216, v248\n\t" \
    "v_max3_u32 v248, v217, v218, v248\n\t" \
    "v_max_u32 v248, v219, v248\n\t" \
    "v_cmp_eq_u32 vcc, -1, v248\n\t" \
    "s_cbranch_vccz .Lw26_%=\n\t" \
    ".Lw25_%=:\n\t" \
    "v_max_u32 v248, v208, v209\n\t" \
    "v_max3_u32 v248, v210, v211, v248\n\t" \
    "v_cmp_eq_u32 vcc, -1, v248\n\t" \
    "s_cbranch_vccz .Lw27_%=\n\t" \
    "buffer_load_dwordx4 v[208:211], v253, %[rs], s46 offen sc1\n\t" \
    ".Lw27_%=:\n\t" \
    "v_max_u32 v248, v212, v213\n\t" \
    "v_max3_u32 v248, v214, v215, v248\n\t" \
    "v_cmp_eq_u32 vcc, -1, v248\n\t" \
    "s_cbranch_vccz .Lw28_%=\n\t" \
    "buffer_load_dwordx4 v[212:215], v253, %[rs], s47 offen sc1\n\t" \
    ".Lw28_%=:\n\t" \
    "v_max_u32 v248, v216, v217\n\t" \
    "v_max3_u32 v248, v218, v219, v248\n\t" \
    "v_cmp_eq_u32 vcc, -1, v248\n\t" \
    "s_cbranch_vccz .Lw29_%=\n\t" \
    "buffer_load_dwordx4 v[216:219], v253, %[rs], s48 offen sc1\n\t" \
    ".Lw29_%=:\n\t" \
    "s_waitcnt vmcnt(20)\n\t" \
    "v_max3_u32 v248, v208, v209, v210\n\t" \
    "v_max3_u32 v248, v211, v212, v248\n\t" \
    "v_max3_u32 v248, v213, v214, v248\n\t" \
    "v_max3_u32 v248, v215, v216, v248\n\t" \
    "v_max3_u32 v248, v217, v218, v248\n\t" \
    "v_max_u32 v248, v219, v248\n\t" \
    "v_cmp_eq_u32 vcc, -1, v248\n\t" \
    "s_cbranch_vccz .Lw26_%=\n\t" \
    "s_sleep 1\n\t" \
    "v_max3_u32 v248, v208, v209, v210\n\t" \
    "v_max3_u32 v248, v211, v212, v248\n\t" \
    "v_max3_u32 v248, v213, v214, v248\n\t" \
    "v_max3_u32 v248, v215, v216, v248\n\t" \
    "v_max3_u32 v248, v217, v218, v248\n\t" \
    "v_max_u32 v248, v219, v248\n\t" \
    "v_cmp_eq_u32 vcc, -1, v248\n\t" \
    "s_cbranch_vccz .Lw26_%=\n\t" \
    "s_sleep 1\n\t" \
    "v_max3_u32 v248, v208, v209, v210\n\t" \
    "v_max3_u32 v248, v211, v212, v248\n\t" \
    "v_max3_u32 v248, v213, v214, v248\n\t" \
    "v_max3_u32 v248, v215, v216, v248\n\t" \
    "v_max3_u32 v248, v217, v218, v248\n\t" \
    "v_max_u32 v248, v219, v248\n\t" \
    "v_cmp_eq_u32 vcc, -1, v248\n\t" \
    "s_cbranch_vccz .Lw26_%=\n\t" \
    "s_sleep 1\n\t" \
    "v_max3_u32 v248, v208, v209, v210\n\t" \
    "v_max3_u32 v248, v211, v212, v248\n\t" \
    "v_max3_u32 v248, v213, v214, v248\n\t" \
    "v_max3_u32 v248, v215, v216, v248\n\t" \
    "v_max3_u32 v248, v217, v218, v248\n\t" \
    "v_max_u32 v248, v219, v248\n\t" \
    "v_cmp_eq_u32 vcc, -1, v248\n\t" \
    "s_cbranch_vccz .Lw26_%=\n\t" \
    "s_sleep 1\n\t" \
    "v_max3_u32 v248, v208, v209, v210\n\t" \
    "v_max3_u32 v248, v211, v212, v248\n\t" \
    "v_max3_u32 v248, v213, v214, v248\n\t" \
    "v_max3_u32 v248, v215, v216, v248\n\t" \
    "v_max3_u32 v248, v217, v218, v248\n\t" \
    "v_max_u32 v248, v219, v248\n\t" \
    "v_cmp_eq_u32 vcc, -1, v248\n\t" \
    "s_cbranch_vccz .Lw26_%=\n\t" \
    "s_sleep 1\n\t" \
    "v_max3_u32 v248, v208, v209, v210\n\t" \
    "v_max3_u32 v248, v211, v212, v248\n\t" \
    "v_max3_u32 v248, v213, v214, v248\n\t" \
    "v_max3_u32 v248, v215, v216, v248\n\t" \
    "v_max3_u32 v248, v217, v218, v248\n\t" \
    "v_max_u32 v248, v219, v248\n\t" \
    "v_cmp_eq_u32 vcc, -1, v248\n\t" \
    "s_cbranch_vccz .Lw26_%=\n\t" \
    "s_sleep 1\n\t" \
    "s_cmp_eq_u32 s41, 0\n\t" \
    "s_cbranch_scc1 .Lw26_%=\n\t" \
    "s_sub_u32 s41, s41, 1\n\t" \
    "s_branch .Lw25_%=\n\t" \
    ".Lw26_%=:\n\t" \
    LST1 \
    "ds_write_b128 v250, v[208:211]\n\t" \
    "ds_write_b128 v250, v[212:215] offset:8192\n\t" \
    "s_cmp_gt_u32 %[wave], 3\n\t" \
    "s_cbranch_scc1 .Lw210_%=\n\t" \
    "ds_write_b128 v250, v[216:219] offset:16384\n\t" \
    ".Lw210_%=:\n\t" \
    "s_add_u32 s46, s43, %[pi]\n\t" \
    "s_add_u32 s47, s46, 0x8000\n\t" \
    "s_add_u32 s48, s46, 0x10000\n\t" \
    "s_waitcnt lgkmcnt(0)\n\t" \
    "s_barrier \n\t" \
    LST2 \
    "ds_read_b128 v[176:179], v249 offset:0\n\t" \
    "buffer_load_dwordx2 v[232:233], v254, %[rs], s50 offen sc1\n\t" \
    "buffer_load_dwordx2 v[234:235], v251, %[rs], s51 offen sc1\n\t" \
    "ds_read_b128 v[180:183], v249 offset:1024\n\t" \
    "ds_read_b128 v[184:187], v249 offset:2048\n\t" \
    "ds_read_b128 v[188:191], v249 offset:3072\n\t" \
    "ds_read_b128 v[192:195], v249 offset:4096\n\t" \
    "ds_read_b128 v[196:199], v249 offset:5120\n\t" \
    "ds_read_b128 v[200:203], v249 offset:6144\n\t" \
    "buffer_load_dwordx4 v[208:211], v253, %[rs], s46 offen sc1\n\t" \
    "buffer_load_dwordx4 v[212:215], v253, %[rs], s47 offen sc1\n\t" \
    "s_cmp_gt_u32 %[wave], 3\n\t" \
    "s_cbranch_scc1 .Lw211_%=\n\t" \
    "buffer_load_dwordx4 v[216:219], v253, %[rs], s48 offen sc1\n\t" \
    ".Lw211_%=:\n\t" \
    "s_waitcnt lgkmcnt(6)\n\t" \
    "v_mfma_f32_16x16x32_bf16 v[168:171], v[0:3], v[176:179], 0\n\t" \
    "ds_read_b128 v[204:207], v249 offset:7168\n\t" \
    "s_waitcnt lgkmcnt(6)\n\t" \
    "v_mfma_f32_16x16x32_bf16 v[168:171], v[4:7], v[180:183], v[168:171]\n\t" \
    "ds_read_b128 v[176:179], v249 offset:8192\n\t" \
    "s_waitcnt lgkmcnt(6)\n\t" \
    "v_mfma_f32_16x16x32_bf16 v[168:171], v[8:11], v[184:187], v[168:171]\n\t" \
    "ds_read_b128 v[180:183], v249 offset:9216\n\t" \
    "s_waitcnt lgkmcnt(6)\n\t" \
    "v_mfma_f32_16x16x32_bf16 v[168:171], v[12:15], v[188:191], v[168:171]\n\t" \
    "ds_read_b128 v[184:187], v249 offset:10240\n\t" \
    "s_waitcnt lgkmcnt(6)\n\t" \
    "v_mfma_f32_16x16x32_bf16 v[168:171], v[16:19], v[192:195], v[168:171]\n\t" \
    "ds_read_b128 v[188:191], v249 offset:11264\n\t" \
    "s_waitcnt lgkmcnt(6)\n\t" \
    "v_mfma_f32_16x16x32_bf16 v[168:171], v[20:23], v[196:199], v[168:171]\n\t" \
    "ds_read_b128 v[192:195], v249 offset:12288\n\t" \
    "s_waitcnt lgkmcnt(6)\n\t" \
    "v_mfma_f32_16x16x32_bf16 v[168:171], v[24:27], v[200:203], v[168:171]\n\t" \
    "ds_read_b128 v[196:199], v249 offset:13312\n\t" \
    "s_waitcnt lgkmcnt(6)\n\t" \
    "v_mfma_f32_16x16x32_bf16 v[168:171], v[28:31], v[204:207], v[168:171]\n\t" \
    "ds_read_b128 v[200:203], v249 offset:14336\n\t" \
    "s_waitcnt lgkmcnt(6)\n\t" \
    "v_mfma_f32_16x16x32_bf16 v[168:171], v[32:35], v[176:179], v[168:171]\n\t" \
    "ds_read_b128 v[204:207], v249 offset:15360\n\t" \
    "s_waitcnt lgkmcnt(6)\n\t" \
    "v_mfma_f32_16x16x32_bf16 v[168:171], v[36:39], v[180:183], v[168:171]\n\t" \
    "ds_read_b128 v[176:179], v249 offset:16384\n\t" \
    "s_waitcnt lgkmcnt(6)\n\t" \
    "v_mfma_f32_16x16x32_bf16 v[168:171], v[40:43], v[184:187], v[168:171]\n\t" \
    "ds_read_b128 v[180:183], v249 offset:17408\n\t" \
    "s_waitcnt lgkmcnt(6)\n\t" \
    "v_mfma_f32_16x16x32_bf16 v[168:171], v[44:47], v[188:191], v[168:171]\n\t" \
    "ds_read_b128 v[184:187], v249 offset:18432\n\t" \
    "s_waitcnt lgkmcnt(6)\n\t" \
    "v_mfma_f32_16x16x32_bf16 v[168:171], v[48:51], v[192:195], v[168:171]\n\t" \
    "ds_read_b128 v[188:191], v249 offset:19456\n\t" \
    "s_waitcnt lgkmcnt(6)\n\t" \
    "v_mfma_f32_16x16x32_bf16 v[168:171], v[52:55], v[196:199], v[168:171]\n\t" \
    "s_waitcnt lgkmcnt(5)\n\t" \
    "v_mfma_f32_16x16x32_bf16 v[168:171], v[56:59], v[200:203], v[168:171]\n\t" \
    "s_waitcnt lgkmcnt(4)\n\t" \
    "v_mfma_f32_16x16x32_bf16 v[168:171], v[60:63], v[204:207], v[168:171]\n\t" \
    "s_waitcnt lgkmcnt(3)\n\t" \
    "v_mfma_f32_16x16x32_bf16 v[168:171], v[64:67], v[176:179], v[168:171]\n\t" \
    "s_waitcnt lgkmcnt(2)\n\t" \
    "v_mfma_f32_16x16x32_bf16 v[168:171], v[68:71], v[180:183], v[168:171]\n\t" \
    "s_waitcnt lgkmcnt(1)\n\t" \
    "v_mfma_f32_16x16x32_bf16 v[168:171], v[72:75], v[184:187], v[168:171]\n\t" \
    "s_waitcnt lgkmcnt(0)\n\t" \
    "v_mfma_f32_16x16x32_bf16 v[168:171], v[76:79], v[188:191], v[168:171]\n\t" \
    LST3 \
    "s_waitcnt vmcnt(0)\n\t" \
    ".Lw212_%=:\n\t" \
    "v_max_u32 v248, v234, v235\n\t" \
    "v_cmp_eq_u32 vcc, -1, v248\n\t" \
    "s_cbranch_vccz .Lw213_%=\n\t" \
    "s_cmp_eq_u32 s41, 0\n\t" \
    "s_cbranch_scc1 .Lw213_%=\n\t" \
    "s_sub_u32 s41, s41, 1\n\t" \
    "buffer_load_dwordx2 v[234:235], v251, %[rs], s51 offen sc1\n\t" \
    "s_waitcnt vmcnt(0)\n\t" \
    "s_branch .Lw212_%=\n\t" \
    ".Lw213_%=:\n\t" \
    "v_lshlrev_b32 v176, 16, v232\n\t" \
    "v_and_b32 v177, 0xffff0000, v232\n\t" \
    "v_lshlrev_b32 v178, 16, v233\n\t" \
    "v_and_b32 v179, 0xffff0000, v233\n\t" \
    "v_sub_f32 v176, v176, v234\n\t" \
    "v_sub_f32 v177, v177, v234\n\t" \
    "v_sub_f32 v178, v178, v234\n\t" \
    "v_sub_f32 v179, v179, v234\n\t" \
    "v_mul_f32 v176, v176, v235\n\t" \
    "v_mul_f32 v177, v177, v235\n\t" \
    "v_mul_f32 v178, v178, v235\n\t" \
    "v_mul_f32 v179, v179, v235\n\t" \
    "v_cvt_pk_bf16_f32 v180, v176, v177\n\t" \
    "v_cvt_pk_bf16_f32 v181, v178, v179\n\t" \
    "v_lshlrev_b32 v176, 16, v180\n\t" \
    "v_and_b32 v177, 0xffff0000, v180\n\t" \
    "v_lshlrev_b32 v178, 16, v181\n\t" \
    "v_and_b32 v179, 0xffff0000, v181\n\t" \
    "v_add_f32 v168, v176, v168\n\t" \
    "v_add_f32 v169, v177, v169\n\t" \
    "v_add_f32 v170, v178, v170\n\t" \
    "v_add_f32 v171, v179, v171\n\t" \
    "v_cvt_pk_bf16_f32 v184, v168, v169\n\t" \
    "v_cvt_pk_bf16_f32 v185, v170, v171\n\t" \
    "v_min_u32 v184, -2, v184\n\t" \
    "v_min_u32 v185, -2, v185\n\t" \
    "buffer_store_dwordx2 v[184:185], v254, %[rs], s49 offen sc1\n\t" \
    LST4 \
    "s_cmp_ge_u32 s40, 122\n\t" \
    "s_cbranch_scc1 .Lw214_%=\n\t" \
    "s_add_i32 s44, s40, 6\n\t" \
    "s_max_i32 s44, s44, 0\n\t" \
    "s_lshr_b32 s45, s44, 1\n\t" \
    "s_and_b32 s44, s44, 1\n\t" \
    "s_lshl_b32 s44, s44, 12\n\t" \
    "s_mul_i32 s50, s45, 0x14000\n\t" \
    "s_add_u32 s50, s50, s44\n\t" \
    "s_lshr_b32 s45, s45, 4\n\t" \
    "s_mul_i32 s45, s45, 0xc2000\n\t" \
    "s_add_u32 s50, s50, s45\n\t" \
    "s_add_u32 s50, s50, 0x2c00000\n\t" \
    "v_mov_b64 v[188:189], -1\n\t" \
    "buffer_store_dwordx2 v[188:189], v254, %[rs], s50 offen sc1\n\t" \
    ".Lw214_%=:\n\t" \
    "v_xor_b32 v249, 0x8000, v249\n\t" \
    "v_xor_b32 v250, 0x8000, v250\n\t" \
    LFLUSHW \
    "s_add_u32 s40, s40, 1\n\t" \
    "s_cmp_lg_u32 s40, 128\n\t" \
    "s_cbranch_scc1 .Lw24_%=\n\t" \
    "s_branch .Lend%=\n\t" \
    ".Lend%=:\n\t" \
    "s_waitcnt vmcnt(0) lgkmcnt(0)\n\t" \
    "s_mov_b32 %[bud], s41\n\t"
#define ASM_W3_LOOP \
    "s_mov_b32 s40, 0\n\t" \
    "s_mov_b32 s41, 0x20000\n\t" \
    LSTINIT \
    "s_add_u32 m0, %[wm], 0x0\n\t" \
    "s_add_u32 s44, %[wo], 0x0\n\t" \
    "buffer_load_dwordx4 v176, %[wr], s44 offen nt lds\n\t" \
    "s_add_u32 m0, %[wm], 0x2000\n\t" \
    "s_add_u32 s44, %[wo], 0xc000\n\t" \
    "buffer_load_dwordx4 v176, %[wr], s44 offen nt lds\n\t" \
    "s_add_u32 m0, %[wm], 0x4000\n\t" \
    "s_add_u32 s44, %[wo], 0x18000\n\t" \
    "buffer_load_dwordx4 v176, %[wr], s44 offen nt lds\n\t" \
    "s_add_u32 m0, %[wm], 0x6000\n\t" \
    "s_add_u32 s44, %[wo], 0x24000\n\t" \
    "buffer_load_dwordx4 v176, %[wr], s44 offen nt lds\n\t" \
    "s_add_u32 m0, %[wm], 0x8000\n\t" \
    "s_add_u32 s44, %[wo], 0x30000\n\t" \
    "buffer_load_dwordx4 v176, %[wr], s44 offen nt lds\n\t" \
    "s_add_u32 m0, %[wm], 0xa000\n\t" \
    "s_add_u32 s44, %[wo], 0x3c000\n\t" \
    "buffer_load_dwordx4 v176, %[wr], s44 offen nt lds\n\t" \
    "s_add_u32 m0, %[wm], 0xc000\n\t" \
    "s_add_u32 s44, %[wo], 0x48000\n\t" \
    "buffer_load_dwordx4 v176, %[wr], s44 offen nt lds\n\t" \
    "s_add_u32 m0, %[wm], 0xe000\n\t" \
    "s_add_u32 s44, %[wo], 0x54000\n\t" \
    "buffer_load_dwordx4 v176, %[wr], s44 offen nt lds\n\t" \
    "s_add_u32 m0, %[wm], 0x10000\n\t" \
    "s_add_u32 s44, %[wo], 0x60000\n\t" \
    "buffer_load_dwordx4 v176, %[wr], s44 offen nt lds\n\t" \
    "s_add_u32 m0, %[wm], 0x12000\n\t" \
    "s_add_u32 s44, %[wo], 0x6c000\n\t" \
    "buffer_load_dwordx4 v176, %[wr], s44 offen nt lds\n\t" \
    "s_add_u32 m0, %[wm], 0x14000\n\t" \
    "s_add_u32 s44, %[wo], 0x78000\n\t" \
    "buffer_load_dwordx4 v176, %[wr], s44 offen nt lds\n\t" \
    "s_add_u32 m0, %[wm], 0x16000\n\t" \
    "s_add_u32 s44, %[wo], 0x84000\n\t" \
    "buffer_load_dwordx4 v176, %[wr], s44 offen nt lds\n\t" \
    "s_add_u32 m0, %[wm], 0x18000\n\t" \
    "s_add_u32 s44, %[wo], 0x90000\n\t" \
    "buffer_load_dwordx4 v176, %[wr], s44 offen nt lds\n\t" \
    "s_add_u32 m0, %[wm], 0x1a000\n\t" \
    "s_add_u32 s44, %[wo], 0x9c000\n\t" \
    "buffer_load_dwordx4 v176, %[wr], s44 offen nt lds\n\t" \
    "s_add_u32 m0, %[wm], 0x1c000\n\t" \
    "s_add_u32 s44, %[wo], 0xa8000\n\t" \
    "buffer_load_dwordx4 v176, %[wr], s44 offen nt lds\n\t" \
    "s_add_u32 m0, %[wm], 0x1e000\n\t" \
    "s_add_u32 s44, %[wo], 0xb4000\n\t" \
    "buffer_load_dwordx4 v176, %[wr], s44 offen nt lds\n\t" \
    "s_add_u32 m0, %[wm], 0x20000\n\t" \
    "s_add_u32 s44, %[wo], 0xc0000\n\t" \
    "buffer_load_dwordx4 v176, %[wr], s44 offen nt lds\n\t" \
    "s_add_u32 m0, %[wm], 0x22000\n\t" \
    "s_add_u32 s44, %[wo], 0xcc000\n\t" \
    "buffer_load_dwordx4 v176, %[wr], s44 offen nt lds\n\t" \
    "s_add_u32 m0, %[wm], 0x24000\n\t" \
    "s_add_u32 s44, %[wo], 0xd8000\n\t" \
    "buffer_load_dwordx4 v176, %[wr], s44 offen nt lds\n\t" \
    "s_add_u32 m0, %[wm], 0x26000\n\t" \
    "s_add_u32 s44, %[wo], 0xe4000\n\t" \
    "buffer_load_dwordx4 v176, %[wr], s44 offen nt lds\n\t" \
    "s_waitcnt vmcnt(0) lgkmcnt(0)\n\t" \
    "s_barrier \n\t" \
    "v_add_u32 v178, 0x0, v177\n\t" \
    "v_xor_b32 v179, 32, v178\n\t" \
    "ds_read_b64_tr_b16 v[0:1], v178 offset:0\n\t" \
    "ds_read_b64_tr_b16 v[2:3], v178 offset:2048\n\t" \
    "ds_read_b64_tr_b16 v[80:81], v179 offset:0\n\t" \
    "ds_read_b64_tr_b16 v[82:83], v179 offset:2048\n\t" \
    "ds_read_b64_tr_b16 v[4:5], v178 offset:16384\n\t" \
    "ds_read_b64_tr_b16 v[6:7], v178 offset:18432\n\t" \
    "ds_read_b64_tr_b16 v[84:85], v179 offset:16384\n\t" \
    "ds_read_b64_tr_b16 v[86:87], v179 offset:18432\n\t" \
    "v_add_u32 v178, 0x8000, v177\n\t" \
    "v_xor_b32 v179, 32, v178\n\t" \
    "ds_read_b64_tr_b16 v[8:9], v178 offset:0\n\t" \
    "ds_read_b64_tr_b16 v[10:11], v178 offset:2048\n\t" \
    "ds_read_b64_tr_b16 v[88:89], v179 offset:0\n\t" \
    "ds_read_b64_tr_b16 v[90:91], v179 offset:2048\n\t" \
    "ds_read_b64_tr_b16 v[12:13], v178 offset:16384\n\t" \
    "ds_read_b64_tr_b16 v[14:15], v178 offset:18432\n\t" \
    "ds_read_b64_tr_b16 v[92:93], v179 offset:16384\n\t" \
    "ds_read_b64_tr_b16 v[94:95], v179 offset:18432\n\t" \
    "v_add_u32 v178, 0x10000, v177\n\t" \
    "v_xor_b32 v179, 32, v178\n\t" \
    "ds_read_b64_tr_b16 v[16:17], v178 offset:0\n\t" \
    "ds_read_b64_tr_b16 v[18:19], v178 offset:2048\n\t" \
    "ds_read_b64_tr_b16 v[96:97], v179 offset:0\n\t" \
    "ds_read_b64_tr_b16 v[98:99], v179 offset:2048\n\t" \
    "ds_read_b64_tr_b16 v[20:21], v178 offset:16384\n\t" \
    "ds_read_b64_tr_b16 v[22:23], v178 offset:18432\n\t" \
    "ds_read_b64_tr_b16 v[100:101], v179 offset:16384\n\t" \
    "ds_read_b64_tr_b16 v[102:103], v179 offset:18432\n\t" \
    "v_add_u32 v178, 0x18000, v177\n\t" \
    "v_xor_b32 v179, 32, v178\n\t" \
    "ds_read_b64_tr_b16 v[24:25], v178 offset:0\n\t" \
    "ds_read_b64_tr_b16 v[26:27], v178 offset:2048\n\t" \
    "ds_read_b64_tr_b16 v[104:105], v179 offset:0\n\t" \
    "ds_read_b64_tr_b16 v[106:107], v179 offset:2048\n\t" \
    "ds_read_b64_tr_b16 v[28:29], v178 offset:16384\n\t" \
    "ds_read_b64_tr_b16 v[30:31], v178 offset:18432\n\t" \
    "ds_read_b64_tr_b16 v[108:109], v179 offset:16384\n\t" \
    "ds_read_b64_tr_b16 v[110:111], v179 offset:18432\n\t" \
    "v_add_u32 v178, 0x20000, v177\n\t" \
    "v_xor_b32 v179, 32, v178\n\t" \
    "ds_read_b64_tr_b16 v[32:33], v178 offset:0\n\t" \
    "ds_read_b64_tr_b16 v[34:35], v178 offset:2048\n\t" \
    "ds_read_b64_tr_b16 v[112:113], v179 offset:0\n\t" \
    "ds_read_b64_tr_b16 v[114:115], v179 offset:2048\n\t" \
    "ds_read_b64_tr_b16 v[36:37], v178 offset:16384\n\t" \
    "ds_read_b64_tr_b16 v[38:39], v178 offset:18432\n\t" \
    "ds_read_b64_tr_b16 v[116:117], v179 offset:16384\n\t" \
    "ds_read_b64_tr_b16 v[118:119], v179 offset:18432\n\t" \
    "s_waitcnt lgkmcnt(0)\n\t" \
    "s_barrier \n\t" \
    "s_add_u32 m0, %[wm], 0x0\n\t" \
    "s_add_u32 s44, %[wo], 0xf0000\n\t" \
    "buffer_load_dwordx4 v176, %[wr], s44 offen nt lds\n\t" \
    "s_add_u32 m0, %[wm], 0x2000\n\t" \
    "s_add_u32 s44, %[wo], 0xfc000\n\t" \
    "buffer_load_dwordx4 v176, %[wr], s44 offen nt lds\n\t" \
    "s_add_u32 m0, %[wm], 0x4000\n\t" \
    "s_add_u32 s44, %[wo], 0x108000\n\t" \
    "buffer_load_dwordx4 v176, %[wr], s44 offen nt lds\n\t" \
    "s_add_u32 m0, %[wm], 0x6000\n\t" \
    "s_add_u32 s44, %[wo], 0x114000\n\t" \
    "buffer_load_dwordx4 v176, %[wr], s44 offen nt lds\n\t" \
    "s_add_u32 m0, %[wm], 0x8000\n\t" \
    "s_add_u32 s44, %[wo], 0x120000\n\t" \
    "buffer_load_dwordx4 v176, %[wr], s44 offen nt lds\n\t" \
    "s_add_u32 m0, %[wm], 0xa000\n\t" \
    "s_add_u32 s44, %[wo], 0x12c000\n\t" \
    "buffer_load_dwordx4 v176, %[wr], s44 offen nt lds\n\t" \
    "s_add_u32 m0, %[wm], 0xc000\n\t" \
    "s_add_u32 s44, %[wo], 0x138000\n\t" \
    "buffer_load_dwordx4 v176, %[wr], s44 offen nt lds\n\t" \
    "s_add_u32 m0, %[wm], 0xe000\n\t" \
    "s_add_u32 s44, %[wo], 0x144000\n\t" \
    "buffer_load_dwordx4 v176, %[wr], s44 offen nt lds\n\t" \
    "s_add_u32 m0, %[wm], 0x10000\n\t" \
    "s_add_u32 s44, %[wo], 0x150000\n\t" \
    "buffer_load_dwordx4 v176, %[wr], s44 offen nt lds\n\t" \
    "s_add_u32 m0, %[wm], 0x12000\n\t" \
    "s_add_u32 s44, %[wo], 0x15c000\n\t" \
    "buffer_load_dwordx4 v176, %[wr], s44 offen nt lds\n\t" \
    "s_add_u32 m0, %[wm], 0x14000\n\t" \
    "s_add_u32 s44, %[wo], 0x168000\n\t" \
    "buffer_load_dwordx4 v176, %[wr], s44 offen nt lds\n\t" \
    "s_add_u32 m0, %[wm], 0x16000\n\t" \
    "s_add_u32 s44, %[wo], 0x174000\n\t" \
    "buffer_load_dwordx4 v176, %[wr], s44 offen nt lds\n\t" \
    "s_add_u32 m0, %[wm], 0x18000\n\t" \
    "s_add_u32 s44, %[wo], 0x180000\n\t" \
    "buffer_load_dwordx4 v176, %[wr], s44 offen nt lds\n\t" \
    "s_add_u32 m0, %[wm], 0x1a000\n\t" \
    "s_add_u32 s44, %[wo], 0x18c000\n\t" \
    "buffer_load_dwordx4 v176, %[wr], s44 offen nt lds\n\t" \
    "s_add_u32 m0, %[wm], 0x1c000\n\t" \
    "s_add_u32 s44, %[wo], 0x198000\n\t" \
    "buffer_load_dwordx4 v176, %[wr], s44 offen nt lds\n\t" \
    "s_add_u32 m0, %[wm], 0x1e000\n\t" \
    "s_add_u32 s44, %[wo], 0x1a4000\n\t" \
    "buffer_load_dwordx4 v176, %[wr], s44 offen nt lds\n\t" \
    "s_add_u32 m0, %[wm], 0x20000\n\t" \
    "s_add_u32 s44, %[wo], 0x1b0000\n\t" \
    "buffer_load_dwordx4 v176, %[wr], s44 offen nt lds\n\t" \
    "s_add_u32 m0, %[wm], 0x22000\n\t" \
    "s_add_u32 s44, %[wo], 0x1bc000\n\t" \
    "buffer_load_dwordx4 v176, %[wr], s44 offen nt lds\n\t" \
    "s_add_u32 m0, %[wm], 0x24000\n\t" \
    "s_add_u32 s44, %[wo], 0x1c8000\n\t" \
    "buffer_load_dwordx4 v176, %[wr], s44 offen nt lds\n\t" \
    "s_add_u32 m0, %[wm], 0x26000\n\t" \
    "s_add_u32 s44, %[wo], 0x1d4000\n\t" \
    "buffer_load_dwordx4 v176, %[wr], s44 offen nt lds\n\t" \
    "s_waitcnt vmcnt(0) lgkmcnt(0)\n\t" \
    "s_barrier \n\t" \
    "v_add_u32 v178, 0x0, v177\n\t" \
    "v_xor_b32 v179, 32, v178\n\t" \
    "ds_read_b64_tr_b16 v[40:41], v178 offset:0\n\t" \
    "ds_read_b64_tr_b16 v[42:43], v178 offset:2048\n\t" \
    "ds_read_b64_tr_b16 v[120:121], v179 offset:0\n\t" \
    "ds_read_b64_tr_b16 v[122:123], v179 offset:2048\n\t" \
    "ds_read_b64_tr_b16 v[44:45], v178 offset:16384\n\t" \
    "ds_read_b64_tr_b16 v[46:47], v178 offset:18432\n\t" \
    "ds_read_b64_tr_b16 v[124:125], v179 offset:16384\n\t" \
    "ds_read_b64_tr_b16 v[126:127], v179 offset:18432\n\t" \
    "v_add_u32 v178, 0x8000, v177\n\t" \
    "v_xor_b32 v179, 32, v178\n\t" \
    "ds_read_b64_tr_b16 v[48:49], v178 offset:0\n\t" \
    "ds_read_b64_tr_b16 v[50:51], v178 offset:2048\n\t" \
    "ds_read_b64_tr_b16 v[128:129], v179 offset:0\n\t" \
    "ds_read_b64_tr_b16 v[130:131], v179 offset:2048\n\t" \
    "ds_read_b64_tr_b16 v[52:53], v178 offset:16384\n\t" \
    "ds_read_b64_tr_b16 v[54:55], v178 offset:18432\n\t" \
    "ds_read_b64_tr_b16 v[132:133], v179 offset:16384\n\t" \
    "ds_read_b64_tr_b16 v[134:135], v179 offset:18432\n\t" \
    "v_add_u32 v178, 0x10000, v177\n\t" \
    "v_xor_b32 v179, 32, v178\n\t" \
    "ds_read_b64_tr_b16 v[56:57], v178 offset:0\n\t" \
    "ds_read_b64_tr_b16 v[58:59], v178 offset:2048\n\t" \
    "ds_read_b64_tr_b16 v[136:137], v179 offset:0\n\t" \
    "ds_read_b64_tr_b16 v[138:139], v179 offset:2048\n\t" \
    "ds_read_b64_tr_b16 v[60:61], v178 offset:16384\n\t" \
    "ds_read_b64_tr_b16 v[62:63], v178 offset:18432\n\t" \
    "ds_read_b64_tr_b16 v[140:141], v179 offset:16384\n\t" \
    "ds_read_b64_tr_b16 v[142:143], v179 offset:18432\n\t" \
    "v_add_u32 v178, 0x18000, v177\n\t" \
    "v_xor_b32 v179, 32, v178\n\t" \
    "ds_read_b64_tr_b16 v[64:65], v178 offset:0\n\t" \
    "ds_read_b64_tr_b16 v[66:67], v178 offset:2048\n\t" \
    "ds_read_b64_tr_b16 v[144:145], v179 offset:0\n\t" \
    "ds_read_b64_tr_b16 v[146:147], v179 offset:2048\n\t" \
    "ds_read_b64_tr_b16 v[68:69], v178 offset:16384\n\t" \
    "ds_read_b64_tr_b16 v[70:71], v178 offset:18432\n\t" \
    "ds_read_b64_tr_b16 v[148:149], v179 offset:16384\n\t" \
    "ds_read_b64_tr_b16 v[150:151], v179 offset:18432\n\t" \
    "v_add_u32 v178, 0x20000, v177\n\t" \
    "v_xor_b32 v179, 32, v178\n\t" \
    "ds_read_b64_tr_b16 v[72:73], v178 offset:0\n\t" \
    "ds_read_b64_tr_b16 v[74:75], v178 offset:2048\n\t" \
    "ds_read_b64_tr_b16 v[152:153], v179 offset:0\n\t" \
    "ds_read_b64_tr_b16 v[154:155], v179 offset:2048\n\t" \
    "ds_read_b64_tr_b16 v[76:77], v178 offset:16384\n\t" \
    "ds_read_b64_tr_b16 v[78:79], v178 offset:18432\n\t" \
    "ds_read_b64_tr_b16 v[156:157], v179 offset:16384\n\t" \
    "ds_read_b64_tr_b16 v[158:159], v179 offset:18432\n\t" \
    "v_mov_b32 v220, 0x3f803f80\n\t" \
    "v_mov_b32 v221, 0x3f803f80\n\t" \
    "v_mov_b32 v222, 0x3f803f80\n\t" \
    "v_mov_b32 v223, 0x3f803f80\n\t" \
    "v_mov_b32 v216, 0\n\t" \
    "v_mov_b32 v217, 0\n\t" \
    "v_mov_b32 v218, 0\n\t" \
    "v_mov_b32 v219, 0\n\t" \
    "s_waitcnt lgkmcnt(0)\n\t" \
    "s_barrier \n\t" \
    "ds_write_b32 v251, v220 offset:72\n\t" \
    "ds_write_b32 v251, v220 offset:2120\n\t" \
    "v_mfma_f32_16x16x32_bf16 v[160:163], v[0:3], v[220:223], 0\n\t" \
    "v_mfma_f32_16x16x32_bf16 v[164:167], v[80:83], v[220:223], 0\n\t" \
    "v_mfma_f32_16x16x32_bf16 v[160:163], v[4:7], v[220:223], v[160:163]\n\t" \
    "v_mfma_f32_16x16x32_bf16 v[164:167], v[84:87], v[220:223], v[164:167]\n\t" \
    "v_mfma_f32_16x16x32_bf16 v[160:163], v[8:11], v[220:223], v[160:163]\n\t" \
    "v_mfma_f32_16x16x32_bf16 v[164:167], v[88:91], v[220:223], v[164:167]\n\t" \
    "v_mfma_f32_16x16x32_bf16 v[160:163], v[12:15], v[220:223], v[160:163]\n\t" \
    "v_mfma_f32_16x16x32_bf16 v[164:167], v[92:95], v[220:223], v[164:167]\n\t" \
    "v_mfma_f32_16x16x32_bf16 v[160:163], v[16:19], v[220:223], v[160:163]\n\t" \
    "v_mfma_f32_16x16x32_bf16 v[164:167], v[96:99], v[220:223], v[164:167]\n\t" \
    "v_mfma_f32_16x16x32_bf16 v[160:163], v[20:23], v[220:223], v[160:163]\n\t" \
    "v_mfma_f32_16x16x32_bf16 v[164:167], v[100:103], v[220:223], v[164:167]\n\t" \
    "v_mfma_f32_16x16x32_bf16 v[160:163], v[24:27], v[220:223], v[160:163]\n\t" \
    "v_mfma_f32_16x16x32_bf16 v[164:167], v[104:107], v[220:223], v[164:167]\n\t" \
    "v_mfma_f32_16x16x32_bf16 v[160:163], v[28:31], v[220:223], v[160:163]\n\t" \
    "v_mfma_f32_16x16x32_bf16 v[164:167], v[108:111], v[220:223], v[164:167]\n\t" \
    "v_mfma_f32_16x16x32_bf16 v[160:163], v[32:35], v[220:223], v[160:163]\n\t" \
    "v_mfma_f32_16x16x32_bf16 v[164:167], v[112:115], v[220:223], v[164:167]\n\t" \
    "v_mfma_f32_16x16x32_bf16 v[160:163], v[36:39], v[220:223], v[160:163]\n\t" \
    "v_mfma_f32_16x16x32_bf16 v[164:167], v[116:119], v[220:223], v[164:167]\n\t" \
    "v_mfma_f32_16x16x32_bf16 v[160:163], v[40:43], v[220:223], v[160:163]\n\t" \
    "v_mfma_f32_16x16x32_bf16 v[164:167], v[120:123], v[220:223], v[164:167]\n\t" \
    "v_mfma_f32_16x16x32_bf16 v[160:163], v[44:47], v[220:223], v[160:163]\n\t" \
    "v_mfma_f32_16x16x32_bf16 v[164:167], v[124:127], v[220:223], v[164:167]\n\t" \
    "v_mfma_f32_16x16x32_bf16 v[160:163], v[48:51], v[220:223], v[160:163]\n\t" \
    "v_mfma_f32_16x16x32_bf16 v[164:167], v[128:131], v[220:223], v[164:167]\n\t" \
    "v_mfma_f32_16x16x32_bf16 v[160:163], v[52:55], v[220:223], v[160:163]\n\t" \
    "v_mfma_f32_16x16x32_bf16 v[164:167], v[132:135], v[220:223], v[164:167]\n\t" \
    "v_mfma_f32_16x16x32_bf16 v[160:163], v[56:59], v[220:223], v[160:163]\n\t" \
    "v_mfma_f32_16x16x32_bf16 v[164:167], v[136:139], v[220:223], v[164:167]\n\t" \
    "v_mfma_f32_16x16x32_bf16 v[160:163], v[60:63], v[220:223], v[160:163]\n\t" \
    "v_mfma_f32_16x16x32_bf16 v[164:167], v[140:143], v[220:223], v[164:167]\n\t" \
    "v_mfma_f32_16x16x32_bf16 v[160:163], v[64:67], v[220:223], v[160:163]\n\t" \
    "v_mfma_f32_16x16x32_bf16 v[164:167], v[144:147], v[220:223], v[164:167]\n\t" \
    "v_mfma_f32_16x16x32_bf16 v[160:163], v[68:71], v[220:223], v[160:163]\n\t" \
    "v_mfma_f32_16x16x32_bf16 v[164:167], v[148:151], v[220:223], v[164:167]\n\t" \
    "v_mfma_f32_16x16x32_bf16 v[160:163], v[72:75], v[220:223], v[160:163]\n\t" \
    "v_mfma_f32_16x16x32_bf16 v[164:167], v[152:155], v[220:223], v[164:167]\n\t" \
    "v_mfma_f32_16x16x32_bf16 v[160:163], v[76:79], v[220:223], v[160:163]\n\t" \
    "v_mfma_f32_16x16x32_bf16 v[164:167], v[156:159], v[220:223], v[164:167]\n\t" \
    "s_mov_b32 s52, 0x22222222\n\t" \
    "s_mov_b32 s53, 0x22222222\n\t" \
    "s_mov_b32 s54, 0x44444444\n\t" \
    "s_mov_b32 s55, 0x44444444\n\t" \
    "s_mov_b32 s56, 0x88888888\n\t" \
    "s_mov_b32 s57, 0x88888888\n\t" \
    ".Lw31_%=:\n\t" \
    "v_mov_b32 v248, 0\n\t" \
    "buffer_load_dword v248, v248, %[rs], %[go] offen sc1\n\t" \
    "s_waitcnt vmcnt(0)\n\t" \
    "v_cmp_ne_u32 vcc, 0, v248\n\t" \
    "s_cbranch_vccnz .Lw32_%=\n\t" \
    "s_cmp_eq_u32 s41, 0\n\t" \
    "s_cbranch_scc1 .Lw32_%=\n\t" \
    "s_sub_u32 s41, s41, 1\n\t" \
    "s_sleep 16\n\t" \
    "s_branch .Lw31_%=\n\t" \
    ".Lw32_%=:\n\t" \
    "s_mov_b32 s43, 0\n\t" \
    "s_add_u32 s46, s43, %[pi]\n\t" \
    "s_add_u32 s47, s46, 0x8000\n\t" \
    "s_add_u32 s48, s46, 0x10000\n\t" \
    "buffer_load_dwordx4 v[208:211], v253, %[rs], s46 offen sc1\n\t" \
    "buffer_load_dwordx4 v[212:215], v253, %[rs], s47 offen sc1\n\t" \
    "s_cmp_gt_u32 %[wave], 3\n\t" \
    "s_cbranch_scc1 .Lw33_%=\n\t" \
    "buffer_load_dwordx4 v[216:219], v253, %[rs], s48 offen sc1\n\t" \
    ".Lw33_%=:\n\t" \
    "s_waitcnt vmcnt(0)\n\t" \
    ".Lw34_%=:\n\t" \
    "s_add_i32 s44, s40, 0\n\t" \
    "s_max_i32 s44, s44, 0\n\t" \
    "s_lshr_b32 s45, s44, 1\n\t" \
    "s_and_b32 s44, s44, 1\n\t" \
    "s_lshl_b32 s44, s44, 12\n\t" \
    "s_mul_i32 s49, s45, 0x30000\n\t" \
    "s_add_u32 s49, s49, s44\n\t" \
    "s_lshr_b32 s45, s45, 3\n\t" \
    "s_mul_i32 s45, s45, 0x82000\n\t" \
    "s_add_u32 s49, s49, s45\n\t" \
    "s_add_u32 s49, s49, %[ng]\n\t" \
    "s_add_i32 s44, s40, 1\n\t" \
    "s_max_i32 s44, s44, 0\n\t" \
    "s_lshr_b32 s45, s44, 1\n\t" \
    "s_and_b32 s44, s44, 1\n\t" \
    "s_lshl_b32 s44, s44, 12\n\t" \
    "s_mul_i32 s43, s45, 0x14000\n\t" \
    "s_add_u32 s43, s43, s44\n\t" \
    "s_lshr_b32 s45, s45, 4\n\t" \
    "s_mul_i32 s45, s45, 0xc2000\n\t" \
    "s_add_u32 s43, s43, s45\n\t" \
    LST0 \
    "v_max3_u32 v248, v208, v209, v210\n\t" \
    "v_max3_u32 v248, v211, v212, v248\n\t" \
    "v_max3_u32 v248, v213, v214, v248\n\t" \
    "v_max3_u32 v248, v215, v216, v248\n\t" \
    "v_max3_u32 v248, v217, v218, v248\n\t" \
    "v_max_u32 v248, v219, v248\n\t" \
    "v_cmp_eq_u32 vcc, -1, v248\n\t" \
    "s_cbranch_vccz .Lw36_%=\n\t" \
    ".Lw35_%=:\n\t" \
    "v_max_u32 v248, v208, v209\n\t" \
    "v_max3_u32 v248, v210, v211, v248\n\t" \
    "v_cmp_eq_u32 vcc, -1, v248\n\t" \
    "s_cbranch_vccz .Lw37_%=\n\t" \
    "buffer_load_dwordx4 v[208:211], v253, %[rs], s46 offen sc1\n\t" \
    ".Lw37_%=:\n\t" \
    "v_max_u32 v248, v212, v213\n\t" \
    "v_max3_u32 v248, v214, v215, v248\n\t" \
    "v_cmp_eq_u32 vcc, -1, v248\n\t" \
    "s_cbranch_vccz .Lw38_%=\n\t" \
    "buffer_load_dwordx4 v[212:215], v253, %[rs], s47 offen sc1\n\t" \
    ".Lw38_%=:\n\t" \
    "v_max_u32 v248, v216, v217\n\t" \
    "v_max3_u32 v248, v218, v219, v248\n\t" \
    "v_cmp_eq_u32 vcc, -1, v248\n\t" \
    "s_cbranch_vccz .Lw39_%=\n\t" \
    "buffer_load_dwordx4 v[216:219], v253, %[rs], s48 offen sc1\n\t" \
    ".Lw39_%=:\n\t" \
    "s_waitcnt vmcnt(20)\n\t" \
    "v_max3_u32 v248, v208, v209, v210\n\t" \
    "v_max3_u32 v248, v211, v212, v248\n\t" \
    "v_max3_u32 v248, v213, v214, v248\n\t" \
    "v_max3_u32 v248, v215, v216, v248\n\t" \
    "v_max3_u32 v248, v217, v218, v248\n\t" \
    "v_max_u32 v248, v219, v248\n\t" \
    "v_cmp_eq_u32 vcc, -1, v248\n\t" \
    "s_cbranch_vccz .Lw36_%=\n\t" \
    "s_sleep 1\n\t" \
    "v_max3_u32 v248, v208, v209, v210\n\t" \
    "v_max3_u32 v248, v211, v212, v248\n\t" \
    "v_max3_u32 v248, v213, v214, v248\n\t" \
    "v_max3_u32 v248, v215, v216, v248\n\t" \
    "v_max3_u32 v248, v217, v218, v248\n\t" \
    "v_max_u32 v248, v219, v248\n\t" \
    "v_cmp_eq_u32 vcc, -1, v248\n\t" \
    "s_cbranch_vccz .Lw36_%=\n\t" \
    "s_sleep 1\n\t" \
    "v_max3_u32 v248, v208, v209, v210\n\t" \
    "v_max3_u32 v248, v211, v212, v248\n\t" \
    "v_max3_u32 v248, v213, v214, v248\n\t" \
    "v_max3_u32 v248, v215, v216, v248\n\t" \
    "v_max3_u32 v248, v217, v218, v248\n\t" \
    "v_max_u32 v248, v219, v248\n\t" \
    "v_cmp_eq_u32 vcc, -1, v248\n\t" \
    "s_cbranch_vccz .Lw36_%=\n\t" \
    "s_sleep 1\n\t" \
    "v_max3_u32 v248, v208, v209, v210\n\t" \
    "v_max3_u32 v248, v211, v212, v248\n\t" \
    "v_max3_u32 v248, v213, v214, v248\n\t" \
    "v_max3_u32 v248, v215, v216, v248\n\t" \
    "v_max3_u32 v248, v217, v218, v248\n\t" \
    "v_max_u32 v248, v219, v248\n\t" \
    "v_cmp_eq_u32 vcc, -1, v248\n\t" \
    "s_cbranch_vccz .Lw36_%=\n\t" \
    "s_sleep 1\n\t" \
    "v_max3_u32 v248, v208, v209, v210\n\t" \
    "v_max3_u32 v248, v211, v212, v248\n\t" \
    "v_max3_u32 v248, v213, v214, v248\n\t" \
    "v_max3_u32 v248, v215, v216, v248\n\t" \
    "v_max3_u32 v248, v217, v218, v248\n\t" \
    "v_max_u32 v248, v219, v248\n\t" \
    "v_cmp_eq_u32 vcc, -1, v248\n\t" \
    "s_cbranch_vccz .Lw36_%=\n\t" \
    "s_sleep 1\n\t" \
    "v_max3_u32 v248, v208, v209, v210\n\t" \
    "v_max3_u32 v248, v211, v212, v248\n\t" \
    "v_max3_u32 v248, v213, v214, v248\n\t" \
    "v_max3_u32 v248, v215, v216, v248\n\t" \
    "v_max3_u32 v248, v217, v218, v248\n\t" \
    "v_max_u32 v248, v219, v248\n\t" \
    "v_cmp_eq_u32 vcc, -1, v248\n\t" \
    "s_cbranch_vccz .Lw36_%=\n\t" \
    "s_sleep 1\n\t" \
    "s_cmp_eq_u32 s41, 0\n\t" \
    "s_cbranch_scc1 .Lw36_%=\n\t" \
    "s_sub_u32 s41, s41, 1\n\t" \
    "s_branch .Lw35_%=\n\t" \
    ".Lw36_%=:\n\t" \
    LST1 \
    "ds_write_b128 v250, v[208:211]\n\t" \
    "ds_write_b128 v250, v[212:215] offset:8192\n\t" \
    "s_cmp_gt_u32 %[wave], 3\n\t" \
    "s_cbranch_scc1 .Lw310_%=\n\t" \
    "ds_write_b128 v250, v[216:219] offset:16384\n\t" \
    ".Lw310_%=:\n\t" \
    "v_mfma_f32_16x16x32_bf16 v[228:231], v[208:211], v[208:211], 0\n\t" \
    "v_mfma_f32_16x16x32_bf16 v[228:231], v[212:215], v[212:215], v[228:231]\n\t" \
    "v_mfma_f32_16x16x32_bf16 v[228:231], v[216:219], v[216:219], v[228:231]\n\t" \
    "v_mfma_f32_16x16x32_bf16 v[224:227], v[220:223], v[208:211], 0\n\t" \
    "v_mfma_f32_16x16x32_bf16 v[224:227], v[220:223], v[212:215], v[224:227]\n\t" \
    "v_mfma_f32_16x16x32_bf16 v[224:227], v[220:223], v[216:219], v[224:227]\n\t" \
    "v_cndmask_b32 v228, v228, v229, s[52:53]\n\t" \
    "v_cndmask_b32 v228, v228, v230, s[54:55]\n\t" \
    "v_cndmask_b32 v228, v228, v231, s[56:57]\n\t" \
    "s_add_u32 s46, s43, %[pi]\n\t" \
    "s_add_u32 s47, s46, 0x8000\n\t" \
    "s_add_u32 s48, s46, 0x10000\n\t" \
    "s_mov_b32 exec_lo, 0xf0000f\n\t" \
    "s_mov_b32 exec_hi, 0xf0000f00\n\t" \
    "s_nop 4\n\t" \
    "ds_write2_b32 v252, v224, v228 offset1:1\n\t" \
    "s_mov_b64 exec, -1\n\t" \
    "s_waitcnt lgkmcnt(0)\n\t" \
    "s_barrier \n\t" \
    LST2 \
    "s_cmp_lg_u32 %[wave], 4\n\t" \
    "s_cbranch_scc1 .Lw311_%=\n\t" \
    "s_setprio 1\n\t" \
    "ds_read_b128 v[232:235], v251 offset:0\n\t" \
    "ds_read_b128 v[236:239], v251 offset:16\n\t" \
    "ds_read_b128 v[240:243], v251 offset:32\n\t" \
    "ds_read_b128 v[244:247], v251 offset:48\n\t" \
    "s_waitcnt lgkmcnt(0)\n\t" \
    "v_add_f32 v232, v232, v240\n\t" \
    "v_add_f32 v233, v233, v241\n\t" \
    "v_add_f32 v234, v234, v242\n\t" \
    "v_add_f32 v235, v235, v243\n\t" \
    "v_add_f32 v236, v236, v244\n\t" \
    "v_add_f32 v237, v237, v245\n\t" \
    "v_add_f32 v238, v238, v246\n\t" \
    "v_add_f32 v239, v239, v247\n\t" \
    "v_add_f32 v232, v232, v236\n\t" \
    "v_add_f32 v233, v233, v237\n\t" \
    "v_add_f32 v234, v234, v238\n\t" \
    "v_add_f32 v235, v235, v239\n\t" \
    "v_add_f32 v232, v232, v234\n\t" \
    "v_add_f32 v233, v233, v235\n\t" \
    "v_mul_f32 v232, 0x3acccccd, v232\n\t" \
    "v_mul_f32 v233, 0x3acccccd, v233\n\t" \
    "v_fma_f32 v233, -v232, v232, v233\n\t" \
    "v_mov_b32 v234, v232\n\t" \
    "v_max_f32 v233, 0, v233\n\t" \
    "v_cvt_pk_bf16_f32 v234, v234, v234\n\t" \
    "v_and_b32 v234, 0xffff0000, v234\n\t" \
    "v_mov_b32 v236, v233\n\t" \
    "v_cvt_pk_bf16_f32 v236, v236, v236\n\t" \
    "v_and_b32 v236, 0xffff0000, v236\n\t" \
    "v_add_f32 v236, 0x3727c5ac, v236\n\t" \
    "v_cvt_pk_bf16_f32 v236, v236, v236\n\t" \
    "v_and_b32 v236, 0xffff0000, v236\n\t" \
    "v_sqrt_f32 v236, v236\n\t" \
    "s_nop 0\n\t" \
    "v_cvt_pk_bf16_f32 v236, v236, v236\n\t" \
    "v_and_b32 v236, 0xffff0000, v236\n\t" \
    "v_rcp_f32 v235, v236\n\t" \
    "s_nop 0\n\t" \
    "v_mov_b32 v237, v235\n\t" \
    "v_mul_f32 v238, -v234, v235\n\t" \
    "v_mov_b32 v239, s40\n\t" \
    "ds_write2_b32 v251, v237, v238 offset0:16 offset1:17\n\t" \
    "ds_write_b32 v251, v239 offset:72\n\t" \
    "s_setprio 0\n\t" \
    ".Lw311_%=:\n\t" \
    "ds_read_b128 v[176:179], v249 offset:0\n\t" \
    "ds_read_b128 v[180:183], v249 offset:1024\n\t" \
    "ds_read_b128 v[184:187], v249 offset:2048\n\t" \
    "ds_read_b128 v[188:191], v249 offset:3072\n\t" \
    "ds_read_b128 v[192:195], v249 offset:4096\n\t" \
    "ds_read_b128 v[196:199], v249 offset:5120\n\t" \
    "ds_read_b128 v[200:203], v249 offset:6144\n\t" \
    "buffer_load_dwordx4 v[208:211], v253, %[rs], s46 offen sc1\n\t" \
    "buffer_load_dwordx4 v[212:215], v253, %[rs], s47 offen sc1\n\t" \
    "s_cmp_gt_u32 %[wave], 3\n\t" \
    "s_cbranch_scc1 .Lw313_%=\n\t" \
    "buffer_load_dwordx4 v[216:219], v253, %[rs], s48 offen sc1\n\t" \
    ".Lw313_%=:\n\t" \
    "s_waitcnt lgkmcnt(6)\n\t" \
    "v_mfma_f32_16x16x32_bf16 v[168:171], v[0:3], v[176:179], 0\n\t" \
    "ds_read_b128 v[204:207], v249 offset:7168\n\t" \
    "v_mfma_f32_16x16x32_bf16 v[172:175], v[80:83], v[176:179], 0\n\t" \
    "s_waitcnt lgkmcnt(6)\n\t" \
    "v_mfma_f32_16x16x32_bf16 v[168:171], v[4:7], v[180:183], v[168:171]\n\t" \
    "ds_read_b128 v[176:179], v249 offset:8192\n\t" \
    "v_mfma_f32_16x16x32_bf16 v[172:175], v[84:87], v[180:183], v[172:175]\n\t" \
    "s_waitcnt lgkmcnt(6)\n\t" \
    "v_mfma_f32_16x16x32_bf16 v[168:171], v[8:11], v[184:187], v[168:171]\n\t" \
    "ds_read_b128 v[180:183], v249 offset:9216\n\t" \
    "v_mfma_f32_16x16x32_bf16 v[172:175], v[88:91], v[184:187], v[172:175]\n\t" \
    "s_waitcnt lgkmcnt(6)\n\t" \
    "v_mfma_f32_16x16x32_bf16 v[168:171], v[12:15], v[188:191], v[168:171]\n\t" \
    "ds_read_b128 v[184:187], v249 offset:10240\n\t" \
    "v_mfma_f32_16x16x32_bf16 v[172:175], v[92:95], v[188:191], v[172:175]\n\t" \
    "s_waitcnt lgkmcnt(6)\n\t" \
    "v_mfma_f32_16x16x32_bf16 v[168:171], v[16:19], v[192:195], v[168:171]\n\t" \
    "ds_read_b128 v[188:191], v249 offset:11264\n\t" \
    "v_mfma_f32_16x16x32_bf16 v[172:175], v[96:99], v[192:195], v[172:175]\n\t" \
    "s_waitcnt lgkmcnt(6)\n\t" \
    "v_mfma_f32_16x16x32_bf16 v[168:171], v[20:23], v[196:199], v[168:171]\n\t" \
    "ds_read_b128 v[192:195], v249 offset:12288\n\t" \
    "v_mfma_f32_16x16x32_bf16 v[172:175], v[100:103], v[196:199], v[172:175]\n\t" \
    "s_waitcnt lgkmcnt(6)\n\t" \
    "v_mfma_f32_16x16x32_bf16 v[168:171], v[24:27], v[200:203], v[168:171]\n\t" \
    "ds_read_b128 v[196:199], v249 offset:13312\n\t" \
    "v_mfma_f32_16x16x32_bf16 v[172:175], v[104:107], v[200:203], v[172:175]\n\t" \
    "s_waitcnt lgkmcnt(6)\n\t" \
    "v_mfma_f32_16x16x32_bf16 v[168:171], v[28:31], v[204:207], v[168:171]\n\t" \
    "ds_read_b128 v[200:203], v249 offset:14336\n\t" \
    "v_mfma_f32_16x16x32_bf16 v[172:175], v[108:111], v[204:207], v[172:175]\n\t" \
    "s_waitcnt lgkmcnt(6)\n\t" \
    "v_mfma_f32_16x16x32_bf16 v[168:171], v[32:35], v[176:179], v[168:171]\n\t" \
    "ds_read_b128 v[204:207], v249 offset:15360\n\t" \
    "v_mfma_f32_16x16x32_bf16 v[172:175], v[112:115], v[176:179], v[172:175]\n\t" \
    "s_waitcnt lgkmcnt(6)\n\t" \
    "v_mfma_f32_16x16x32_bf16 v[168:171], v[36:39], v[180:183], v[168:171]\n\t" \
    "ds_read_b128 v[176:179], v249 offset:16384\n\t" \
    "v_mfma_f32_16x16x32_bf16 v[172:175], v[116:119], v[180:183], v[172:175]\n\t" \
    "s_waitcnt lgkmcnt(6)\n\t" \
    "v_mfma_f32_16x16x32_bf16 v[168:171], v[40:43], v[184:187], v[168:171]\n\t" \
    "ds_read_b128 v[180:183], v249 offset:17408\n\t" \
    "v_mfma_f32_16x16x32_bf16 v[172:175], v[120:123], v[184:187], v[172:175]\n\t" \
    "s_waitcnt lgkmcnt(6)\n\t" \
    "v_mfma_f32_16x16x32_bf16 v[168:171], v[44:47], v[188:191], v[168:171]\n\t" \
    "ds_read_b128 v[184:187], v249 offset:18432\n\t" \
    "v_mfma_f32_16x16x32_bf16 v[172:175], v[124:127], v[188:191], v[172:175]\n\t" \
    "s_waitcnt lgkmcnt(6)\n\t" \
    "v_mfma_f32_16x16x32_bf16 v[168:171], v[48:51], v[192:195], v[168:171]\n\t" \
    "ds_read_b128 v[188:191], v249 offset:19456\n\t" \
    "v_mfma_f32_16x16x32_bf16 v[172:175], v[128:131], v[192:195], v[172:175]\n\t" \
    "s_waitcnt lgkmcnt(6)\n\t" \
    "v_mfma_f32_16x16x32_bf16 v[168:171], v[52:55], v[196:199], v[168:171]\n\t" \
    "v_mfma_f32_16x16x32_bf16 v[172:175], v[132:135], v[196:199], v[172:175]\n\t" \
    "s_waitcnt lgkmcnt(5)\n\t" \
    "v_mfma_f32_16x16x32_bf16 v[168:171], v[56:59], v[200:203], v[168:171]\n\t" \
    "v_mfma_f32_16x16x32_bf16 v[172:175], v[136:139], v[200:203], v[172:175]\n\t" \
    "s_waitcnt lgkmcnt(4)\n\t" \
    "v_mfma_f32_16x16x32_bf16 v[168:171], v[60:63], v[204:207], v[168:171]\n\t" \
    "v_mfma_f32_16x16x32_bf16 v[172:175], v[140:143], v[204:207], v[172:175]\n\t" \
    "ds_read_b32 v236, v251 offset:72\n\t" \
    "ds_read_b64 v[238:239], v251 offset:64\n\t" \
    "s_waitcnt lgkmcnt(5)\n\t" \
    "v_mfma_f32_16x16x32_bf16 v[168:171], v[64:67], v[176:179], v[168:171]\n\t" \
    "v_mfma_f32_16x16x32_bf16 v[172:175], v[144:147], v[176:179], v[172:175]\n\t" \
    "s_waitcnt lgkmcnt(4)\n\t" \
    "v_mfma_f32_16x16x32_bf16 v[168:171], v[68:71], v[180:183], v[168:171]\n\t" \
    "v_mfma_f32_16x16x32_bf16 v[172:175], v[148:151], v[180:183], v[172:175]\n\t" \
    ".Lw314_%=:\n\t" \
    "s_waitcnt lgkmcnt(0)\n\t" \
    "v_cmp_ne_u32 vcc, s40, v236\n\t" \
    "s_cbranch_vccz .Lw315_%=\n\t" \
    "ds_read_b32 v236, v251 offset:72\n\t" \
    "ds_read_b64 v[238:239], v251 offset:64\n\t" \
    "s_branch .Lw314_%=\n\t" \
    ".Lw315_%=:\n\t" \
    "s_waitcnt lgkmcnt(0)\n\t" \
    "v_mul_f32 v240, v160, v239\n\t" \
    "v_mul_f32 v241, v161, v239\n\t" \
    "v_mfma_f32_16x16x32_bf16 v[168:171], v[72:75], v[184:187], v[168:171]\n\t" \
    "v_mul_f32 v242, v162, v239\n\t" \
    "v_mul_f32 v243, v163, v239\n\t" \
    "v_mfma_f32_16x16x32_bf16 v[172:175], v[152:155], v[184:187], v[172:175]\n\t" \
    "v_mul_f32 v244, v164, v239\n\t" \
    "v_mul_f32 v245, v165, v239\n\t" \
    "v_mfma_f32_16x16x32_bf16 v[168:171], v[76:79], v[188:191], v[168:171]\n\t" \
    "v_mul_f32 v246, v166, v239\n\t" \
    "v_mul_f32 v247, v167, v239\n\t" \
    "v_mfma_f32_16x16x32_bf16 v[172:175], v[156:159], v[188:191], v[172:175]\n\t" \
    "v_mov_b32 v237, 0xc0135761\n\t" \
    LST3 \
    "s_nop 6\n\t" \
    "v_fma_f32 v168, v168, v238, v240\n\t" \
    "v_fma_f32 v169, v169, v238, v241\n\t" \
    "v_fma_f32 v170, v170, v238, v242\n\t" \
    "v_fma_f32 v171, v171, v238, v243\n\t" \
    "v_fma_f32 v172, v172, v238, v244\n\t" \
    "v_fma_f32 v173, v173, v238, v245\n\t" \
    "v_fma_f32 v174, v174, v238, v246\n\t" \
    "v_fma_f32 v175, v175, v238, v247\n\t" \
    "v_mul_f32 v176, v168, v168\n\t" \
    "v_mul_f32 v177, v169, v169\n\t" \
    "v_mul_f32 v178, v170, v170\n\t" \
    "v_mul_f32 v179, v171, v171\n\t" \
    "v_mul_f32 v180, v172, v172\n\t" \
    "v_mul_f32 v181, v173, v173\n\t" \
    "v_mul_f32 v182, v174, v174\n\t" \
    "v_mul_f32 v183, v175, v175\n\t" \
    "v_fmamk_f32 v176, v176, 0xbdd2d3e7, v237\n\t" \
    "v_fmamk_f32 v177, v177, 0xbdd2d3e7, v237\n\t" \
    "v_fmamk_f32 v178, v178, 0xbdd2d3e7, v237\n\t" \
    "v_fmamk_f32 v179, v179, 0xbdd2d3e7, v237\n\t" \
    "v_fmamk_f32 v180, v180, 0xbdd2d3e7, v237\n\t" \
    "v_fmamk_f32 v181, v181, 0xbdd2d3e7, v237\n\t" \
    "v_fmamk_f32 v182, v182, 0xbdd2d3e7, v237\n\t" \
    "v_fmamk_f32 v183, v183, 0xbdd2d3e7, v237\n\t" \
    "v_mul_f32 v176, v168, v176\n\t" \
    "v_mul_f32 v177, v169, v177\n\t" \
    "v_mul_f32 v178, v170, v178\n\t" \
    "v_mul_f32 v179, v171, v179\n\t" \
    "v_mul_f32 v180, v172, v180\n\t" \
    "v_mul_f32 v181, v173, v181\n\t" \
    "v_mul_f32 v182, v174, v182\n\t" \
    "v_mul_f32 v183, v175, v183\n\t" \
    "v_exp_f32 v176, v176\n\t" \
    "v_exp_f32 v177, v177\n\t" \
    "v_exp_f32 v178, v178\n\t" \
    "v_exp_f32 v179, v179\n\t" \
    "v_exp_f32 v180, v180\n\t" \
    "v_exp_f32 v181, v181\n\t" \
    "v_exp_f32 v182, v182\n\t" \
    "v_exp_f32 v183, v183\n\t" \
    "v_add_f32 v176, 1.0, v176\n\t" \
    "v_add_f32 v177, 1.0, v177\n\t" \
    "v_add_f32 v178, 1.0, v178\n\t" \
    "v_add_f32 v179, 1.0, v179\n\t" \
    "v_add_f32 v180, 1.0, v180\n\t" \
    "v_add_f32 v181, 1.0, v181\n\t" \
    "v_add_f32 v182, 1.0, v182\n\t" \
    "v_add_f32 v183, 1.0, v183\n\t" \
    "v_rcp_f32 v176, v176\n\t" \
    "v_rcp_f32 v177, v177\n\t" \
    "v_rcp_f32 v178, v178\n\t" \
    "v_rcp_f32 v179, v179\n\t" \
    "v_rcp_f32 v180, v180\n\t" \
    "v_rcp_f32 v181, v181\n\t" \
    "v_rcp_f32 v182, v182\n\t" \
    "v_rcp_f32 v183, v183\n\t" \
    "v_mul_f32 v168, v168, v176\n\t" \
    "v_mul_f32 v169, v169, v177\n\t" \
    "v_mul_f32 v170, v170, v178\n\t" \
    "v_mul_f32 v171, v171, v179\n\t" \
    "v_mul_f32 v172, v172, v180\n\t" \
    "v_mul_f32 v173, v173, v181\n\t" \
    "v_mul_f32 v174, v174, v182\n\t" \
    "v_mul_f32 v175, v175, v183\n\t" \
    "s_waitcnt vmcnt(0)\n\t" \
    "v_cvt_pk_bf16_f32 v184, v168, v169\n\t" \
    "v_cvt_pk_bf16_f32 v185, v170, v171\n\t" \
    "v_cvt_pk_bf16_f32 v186, v172, v173\n\t" \
    "v_cvt_pk_bf16_f32 v187, v174, v175\n\t" \
    "v_min_u32 v184, -2, v184\n\t" \
    "v_min_u32 v185, -2, v185\n\t" \
    "v_min_u32 v186, -2, v186\n\t" \
    "v_min_u32 v187, -2, v187\n\t" \
    "s_nop 0\n\t" \
    "v_permlane16_swap_b32 v184, v186\n\t" \
    "v_permlane16_swap_b32 v185, v187\n\t" \
    "buffer_store_dwordx4 v[184:187], v254, %[rs], s49 offen sc1\n\t" \
    LST4 \
    "s_cmp_ge_u32 s40, 122\n\t" \
    "s_cbranch_scc1 .Lw316_%=\n\t" \
    "s_add_i32 s44, s40, 6\n\t" \
    "s_max_i32 s44, s44, 0\n\t" \
    "s_lshr_b32 s45, s44, 1\n\t" \
    "s_and_b32 s44, s44, 1\n\t" \
    "s_lshl_b32 s44, s44, 12\n\t" \
    "s_mul_i32 s50, s45, 0x30000\n\t" \
    "s_add_u32 s50, s50, s44\n\t" \
    "s_lshr_b32 s45, s45, 3\n\t" \
    "s_mul_i32 s45, s45, 0x82000\n\t" \
    "s_add_u32 s50, s50, s45\n\t" \
    "s_add_u32 s50, s50, %[ng]\n\t" \
    "v_mov_b32 v188, -1\n\t" \
    "v_mov_b32 v189, -1\n\t" \
    "v_mov_b32 v190, -1\n\t" \
    "v_mov_b32 v191, -1\n\t" \
    "buffer_store_dwordx4 v[188:191], v253, %[rs], s50 offen sc1\n\t" \
    ".Lw316_%=:\n\t" \
    "v_xor_b32 v249, 0x8000, v249\n\t" \
    "v_xor_b32 v250, 0x8000, v250\n\t" \
    "v_xor_b32 v251, 0x800, v251\n\t" \
    "v_xor_b32 v252, 0x800, v252\n\t" \
    LFLUSHW \
    "s_add_u32 s40, s40, 1\n\t" \
    "s_cmp_lg_u32 s40, 128\n\t" \
    "s_cbranch_scc1 .Lw34_%=\n\t" \
    "s_branch .Lend%=\n\t" \
    ".Lend%=:\n\t" \
    "s_waitcnt vmcnt(0) lgkmcnt(0)\n\t" \
    "s_mov_b32 %[bud], s41\n\t"
using u32x8 = __attribute__((ext_vector_type(8))) unsigned;
using u32x16 = __attribute__((ext_vector_type(16))) unsigned;
using u32x32 = __attribute__((ext_vector_type(32))) unsigned;
__device__ __forceinline__ u32x32 cat32(u32x8 a, u32x8 b, u32x8 c, u32x8 d) {
    const u32x16 lo = __builtin_shufflevector(a, b, 0, 1, 2, 3, 4, 5, 6, 7, 8, 9, 10, 11, 12, 13, 14, 15);
    const u32x16 hi = __builtin_shufflevector(c, d, 0, 1, 2, 3, 4, 5, 6, 7, 8, 9, 10, 11, 12, 13, 14, 15);
    return __builtin_shufflevector(lo, hi, 0, 1, 2, 3, 4, 5, 6, 7, 8, 9, 10, 11, 12, 13, 14, 15, 16, 17, 18, 19, 20, 21, 22, 23, 24, 25, 26, 27, 28, 29, 30, 31);
}
__device__ __forceinline__ unsigned c1_dst(int wave, int kind) {
    const int ln = lane_id(), c = ln & 15, g = ln >> 4, hf = (wave >> 1) & 1;
    if (kind == 2) {
        const unsigned b = L_KM + (c >> 2) * 256 + (wave & 1) * 128 + g * 32;
        return (b + hf * 8 + (c & 3) * 2) | (b + 1024 + hf * 1024 + (c & 3) * 4) << 16;
    }
    const unsigned b = L_Q + (c >> 3) * 256 + g * 64 + ((c >> 2) & 1) * 8;
    return (b + (wave & 1) * (kind ? 512 : 1536) + ((c >> 1) & 1) * 32 + hf * 4 + (c & 1) * 2) | (b + 1024 + (wave & 3) * 512 + (c & 1) * 32 + ((c >> 1) & 1) * 4) << 16;
}
__device__ __forceinline__ void dma1(rsrc_t rs, unsigned vo, unsigned so, unsigned dst) {
    asm volatile("s_mov_b32 m0, %3\n\ts_nop 0\n\tbuffer_load_dwordx4 %0, %1, %2 offen nt lds" :: "v"(vo), "s"(rs), "s"(so), "s"(dst) : "memory");
}
using s16x4 = __attribute__((ext_vector_type(4))) short;
__device__ __forceinline__ u32x4 tr_frag(unsigned a, unsigned step) {
    const u32x2 x = __builtin_bit_cast(u32x2, __builtin_amdgcn_ds_read_tr16_b64_v4i16((LAS s16x4*)(size_t)a)), y = __builtin_bit_cast(u32x2, __builtin_amdgcn_ds_read_tr16_b64_v4i16((LAS s16x4*)(size_t)(a + step)));
    return (u32x4){x[0], x[1], y[0], y[1]};
}
#define LDELAY
#define LOOP_CLOB "memory", "vcc", "scc", "v0", "v1", "v2", "v3", "v4", "v5", "v6", "v7", "v8", "v9", "v10", "v11", "v12", "v13", "v14", "v15", "v16", "v17", "v18", "v19", "v20", "v21", "v22", "v23", "v24", "v25", "v26", "v27", "v28", "v29", "v30", "v31", "v32", "v33", "v34", "v35", "v36", "v37", "v38", "v39", "v40", "v41", "v42", "v43", "v44", "v45", "v46", "v47", "v48", "v49", "v50", "v51", "v52", "v53", "v54", "v55", "v56", "v57", "v58", "v59", "v60", "v61", "v62", "v63", "v64", "v65", "v66", "v67", "v68", "v69", "v70", "v71", "v72", "v73", "v74", "v75", "v76", "v77", "v78", "v79", "v80", "v81", "v82", "v83", "v84", "v85", "v86", "v87", "v88", "v89", "v90", "v91", "v92", "v93", "v94", "v95", "v96", "v97", "v98", "v99", "v100", "v101", "v102", "v103", "v104", "v105", "v106", "v107", "v108", "v109", "v110", "v111", "v112", "v113", "v114", "v115", "v116", "v117", "v118", "v119", "v120", "v121", "v122", "v123", "v124", "v125", "v126", "v127", "v128", "v129", "v130", "v131", "v132", "v133", "v134", "v135", "v136", "v137", "v138", "v139", "v140", "v141", "v142", "v143", "v144", "v145", "v146", "v147", "v148", "v149", "v150", "v151", "v152", "v153", "v154", "v155", "v156", "v157", "v158", "v159", "v160", "v161", "v162", "v163", "v164", "v165", "v166", "v167", "v168", "v169", "v170", "v171", "v172", "v173", "v174", "v175", "v176", "v177", "v178", "v179", "v180", "v181", "v182", "v183", "v184", "v185", "v186", "v187", "v188", "v189", "v190", "v191", "v192", "v193", "v194", "v195", "v196", "v197", "v198", "v199", "v200", "v201", "v202", "v203", "v204", "v205", "v206", "v207", "v208", "v209", "v210", "v211", "v212", "v213", "v214", "v215", "v216", "v217", "v218", "v219", "v220", "v221", "v222", "v223", "v224", "v225", "v226", "v227", "v228", "v229", "v230", "v231", "v232", "v233", "v234", "v235", "v236", "v237", "v238", "v239", "v246", "v247", "v248", "v249", "v250", "v251", "v252", "s40", "s41", "s42", "s43", "s44", "s45", "s46", "s47", "s48", "s49", "s50", "s51", "s52", "s53", "s54", "s55", "s56", "s57", "s58", "s59", "s60", "s61", "s62", "s63", "s64", "s65", "s66", "s67", "s68", "s69", "s70", "s71", "s72", "s73", "s74", "s75", "s76", "s77", "s78", "s79"
__device__ __forceinline__ unsigned UNI(unsigned x) {
    unsigned r;
    asm volatile("s_nop 0\n\tv_readfirstlane_b32 %0, %1\n\ts_nop 4" : "=s"(r) : "v"(x));
    return r;
}
__device__ __forceinline__ void note_timeout(const Params& P, unsigned bud) {
    if (bud == 0) __builtin_amdgcn_raw_buffer_store_b32(1u, mkrsrc(P.ws + FL0, 512), 0x100, 0, 16);
}

#define LOOP_IO [bud] "=s"(bud), "+{v253}"(l16), "+{v240}"(a[0]), "+{v241}"(a[1]), "+{v242}"(a[2]), "+{v243}"(a[3]), "+{v244}"(a[4]), "+{v245}"(a[5])
#define LOOP_IN "{v254}"(b2), "{v255}"(dsto), [wave] "s"(wave), [ws] "s"(P.ws), [mo] "s"(FL_MB + blockIdx.x * 128 + wave * 4), [sz] "s"(RG_L), [lx] "s"(L << 13), [wr] "s"(wr), [wm] "s"(wm), [fl] "s"(FL_XN + cpy * 2048), \
                [cj] "s"(REGS - 75 * WIN), [cs] "s"(5 * WIN), [mb] "s"(RG_XM + cpy * XM_COPY)
__device__ __forceinline__ void qkv_addr(unsigned (&a)[6], int w4, int ln, unsigned base) {
#pragma unroll
    for (int k = 0; k < 3; ++k) { const unsigned u = (w4 * 3 + k) * 64 + ln; a[k] = (u / 24) * (HD * 2) + (u % 24) * 16 - k * 1024; }
    a[3] = base + ((ln >> 4) * 8 + ((ln >> 2) & 3)) * 384 + (ln & 3) * 8 + w4 * 96;
    a[4] = a[5] = 0;
}
__device__ __forceinline__ void s_role(const Params& P, int wave, int L, int h, int cpy) {
    const int ln = lane_id();
    unsigned l16 = ln * 16, a[6];
    qkv_addr(a, wave & 3, ln, (wave >> 2) * 12288);
    const rsrc_t wr = mkrsrc((wave < 4 ? P.wq : P.wk) + h * DH, (DM * HD - h * DH) * 2);
    const unsigned wm = UNI(wave * 3072);
    const int mt = (wave >> 1) & 1, hf = wave & 1;
    const unsigned xs = D_XN + mt * 4096 + hf * WIN, x1 = (2 - hf) * WIN + hf * 2048, m0 = L_X + mt * 20480 + hf * 4096, m1 = L_X + mt * 20480 + 8192 + hf * 2048;
    const float SCL = 0.07216878364870322f * 1.4426950408889634f;
    const unsigned px = RG_PX + (h >> 2) * WIN;
    const unsigned pb = wave == 0 ? RG_XP + (h / 12) * WIN + (h % 12) * 640 - L_Q : px + (wave == 2 ? (h & 3) * 1024 : 5120 + ((h >> 1) & 1) * 1024 + (h & 1) * 256);
    unsigned b2 = l16 + (wave < 3 ? L_Q : wave == 3 ? L_QX : L_W + wave * 10240), dsto = c1_dst(wave, 1 + (wave >> 2)), bud;
    asm volatile(ASM_S_LOOP : LOOP_IO
                 : LOOP_IN, [xs] "s"(xs), [x1] "s"(x1), [m0] "s"(m0), [m1] "s"(m1), [qs] "s"(UNI(L_KX + hf * 1024 + mt * 8)), [q2] "s"(UNI(L_KX + 2048 + (wave & 3) * 1024)), [pb] "s"(UNI(pb)),
                   [pm] "s"(UNI(px + (wave == 2 ? 4096 : 7168) + (h & 3) * 256)), [scl] "s"(__float_as_uint(SCL)) LST_IN
                 : LOOP_CLOB);
    note_timeout(P, bud);
}

__device__ __forceinline__ void v_role(const Params& P, int wave, int L, int h, int cpy) {
    const int ln = lane_id(), w4 = wave & 3;
    unsigned l16 = ln * 16, a[6];
    if (wave < 4) qkv_addr(a, w4, ln, 0);
    else {
#pragma unroll
        for (int k = 0; k < 5; ++k) { const unsigned u = k * 64 + ln; a[k] = (u / 20) * (DM * 2) + (u % 20) * 16 - (k & 3) * 1024; }
        a[5] = L_W + w4 * 20480 + (ln >> 2) * 320 + (ln & 3) * 8;
    }
    const unsigned wo = wave < 4 ? h * DH : h * DH * DM + w4 * 160;
    const rsrc_t wr = mkrsrc((wave < 4 ? P.wv : P.wo) + wo, (DM * HD - wo) * 2);
    const unsigned wm = UNI(wave < 4 ? wave * 3072 : L_W + w4 * 20480);
    unsigned b2 = l16 + (wave < 3 ? L_Q : wave == 3 ? L_QX : L_W + wave * 10240), dsto = wave < 4 ? c1_dst(wave, 0) : (l16 >> 8) * WIN + (l16 & 255), bud;
    if (wave >= 4) l16 += L_Q;
    asm volatile(ASM_V_LOOP : LOOP_IO
                 : LOOP_IN, [xs] "s"(D_XN + w4 * WIN), [vm] "s"(VXD - 1), [vx] "s"(vxb(w4, 0) + (h >> 3) * WIN + (h & 7) * 1024 - L_Q),
                   [fo] "s"(FL_VO + (w4 >> 1) * WIN + ((w4 & 1) * 32 + h) * 128 - L_Q), [vo] "s"(RG_VM + w4 * 10 * WIN + (h >> 2) * 1024 + (h & 3) * 256) LST_IN
                 : LOOP_CLOB);
    note_timeout(P, bud);
}

#define D_W0 4
#define D_A1 8
#define D_R1 9
#define D_W1 12
__device__ __forceinline__ void delay(int n) { for (int i = 0; i < n; ++i) __builtin_amdgcn_s_sleep(64); }
__device__ __forceinline__ unsigned handshake(rsrc_t rw, unsigned mo) {
    wait_vm<0>();
    unsigned v = SENT;
    for (int spins = 0; spins < SPIN_CAP && v == SENT; ++spins) {
        __builtin_amdgcn_s_sleep(4);
        v = __builtin_amdgcn_readfirstlane(__builtin_amdgcn_raw_buffer_load_b32(rw, 0, mo, 16));
    }
    __builtin_amdgcn_raw_buffer_store_b32(1u, rw, lane_id() ? OOB : 32768u, mo, 16);
    return (v >> 13) & 1;
}
template <class F>
__device__ __forceinline__ void x_tile(const Params& P, int tile, F target) {
    const int ln = lane_id(), l15 = ln & 15, l4 = ln >> 4;
    u32x4 v[20];
    float s = 0.f, q = 0.f;
#pragma unroll
    for (int c = 0; c < 20; ++c) v[c] = *(const u32x4*)(P.x + (size_t)(tile * 16 + l15) * DM + c * 32 + l4 * 8);
#pragma unroll
    for (int c = 0; c < 20; ++c)
#pragma unroll
        for (int e = 0; e < 4; ++e) s += lo2f(v[c][e]) + hi2f(v[c][e]);
    sum2_rows(s, q);
#pragma unroll
    for (int c = 0; c < 20; ++c) asm volatile("" : "+v"(v[c]));
    const float mean = s * (1.0f / DM);
#pragma unroll
    for (int c = 0; c < 20; ++c)
#pragma unroll
        for (int e = 0; e < 4; ++e) { const float a = lo2f(v[c][e]) - mean, b = hi2f(v[c][e]) - mean; q += a * a + b * b; }
    sum2_rows(s, q);
#pragma unroll
    for (int c = 0; c < 20; ++c) asm volatile("" : "+v"(v[c]));
    const float m1 = rbf(mean), r = 1.0f / ln_den(q * (1.0f / DM));
    const unsigned sb = segb(tile >> 1) + (tile & 1) * 4096;
    u32x4 n[20];
#pragma unroll
    for (int c = 0; c < 20; ++c) {
#pragma unroll
        for (int e = 0; e < 4; ++e) { const unsigned d = pack2(lo2f(v[c][e]) - m1, hi2f(v[c][e]) - m1); n[c][e] = pack2(lo2f(d) * r, hi2f(d) * r); }
        asm volatile("" : "+v"(n[c]));
    }
    const rsrc_t rr = target();
#pragma unroll
    for (int c = 0; c < 20; ++c) {
        st128(n[c], rr, ln * 16 + (c & 3) * 1024, D_XN + sb + (c >> 2) * WIN);
        st128(v[c], rr, ln * 16 + (c & 3) * 1024, D_XR + sb + (c >> 2) * WIN);
    }
    wait_vm<0>();
}
__device__ __forceinline__ void wait_go(const Params& P, rsrc_t rd, int k) {
    for (int spins = 0;; ++spins) {
        if (__builtin_amdgcn_readfirstlane(__builtin_amdgcn_raw_buffer_load_b32(rd, 0, FL_VO + k * 128, 16))) break;
        __builtin_amdgcn_s_sleep(16);
        if (spin_fail(spins * 4 + 3, P)) break;
    }
    asm volatile("" ::: "memory");
}

constexpr unsigned R_RED = 0;
constexpr unsigned R_PART = 8192;
__device__ __forceinline__ void r_role(const Params& P, int wave, int L, int j, int cpy) {
    const int lane = lane_id(), l15 = lane & 15, l4 = lane >> 4;
    const unsigned l16 = lane * 16;
    const rsrc_t rr = mkrsrc(lbase(P, L), RG_L), rd = rr;
    const int sidx = (lane & 2) ? 0xEEEE : 0x4444;
    unsigned offp[3];
    auto hq = [&](int q) { return RG_XP + cpy * XP_COPY + (q / 3) * WIN + (q % 3) * 2560; };
#define ROT 1
    const int hg = (wave + ROT * j) & 7;
    offp[0] = hq(hg) + (lane >> 5) * 640 + 128 + (lane & 31) * 16;
    offp[1] = offp[0] + 1280;
    offp[2] = (lane & 2) ? OOB : hq(hg) + l4 * 640 + ((l15 >> 2) * 2 + (l15 & 1)) * 16;
    const int jq = j / 10, j10 = j % 10;
    const unsigned ae = wave < 3 ? ((j10 < 8 && wave * 1024 + l16 < 2560) ? hq(j10) + (jq - cpy) * XP_COPY + wave * 1024 + l16 : OOB)
                      : wave == 3 ? RG_XM + jq * XM_COPY + (j10 >> 3) * WIN + (j10 & 7) * 1024 + l16
                      : wave < 6 ? (j < 32 ? RG_PX + (j >> 2) * WIN + (j & 3) * 2048 + (wave & 1) * 1024 + l16 : OOB)
                      : wave == 7 ? D_XA + (j >> 3) * WIN + (j & 7) * 512 + (lane >> 5) * 4096 + (lane & 31) * 16 : OOB;
    const unsigned as = wave < 3 ? XP_SLOT : PX_SLOT;
    static_assert(PX_SLOT == XM_SLOT && AH == 3 && VXD == 4, "generated loop");
    const unsigned offv = RG_VM + j * WIN + hg * 1024 + l16;
    const unsigned offo = (wave < (int)NCP) ? RG_XM + wave * XM_COPY + (j >> 5) * WIN + (j & 31) * 256 + lane * 4 : OOB;
    {
        const u32x4 s4 = sent4();
#pragma unroll
        for (int k = 0; k < (int)NSLOT; ++k) {
            st128(s4, rr, offv, k * VM_SLOT);
            if (wave < 6 && (k || wave != 3)) st128(s4, rr, ae, k * as);
        }
        for (int u = j; u < 126; u += 40) st128(s4, rr, wave * 1024 + l16, u < 108 ? D_XA + (u < 60 ? u / 20 : 3) * 4 * REG + (u < 60 ? u % 20 : u - 60) * WIN : u < 124 ? RG_ST + (u - 108) * WIN : D_SS + (u - 124) * WIN);
        wait_vm<0>();
        bar();
        const int row = l4 * 2 + ((lane >> 2) & 1), col = 16 * j + 4 * (lane & 3) + 2 * ((lane >> 3) & 1);
        __builtin_amdgcn_raw_buffer_store_b32(nosent(*(const unsigned*)(P.mem0 + row * DM + col)), rr, offo, 0, 16);
    }
    auto red_addr = [](int fl) { return R_RED + fl * 80 + (fl >> 4) * 16; };
    const unsigned rw0 = red_addr(l4 * 16 + ((l15 >> 1) & 1) * 8 + (l15 >> 2)) + (l15 & 1) * 4 + (hg ^ (l15 >> 3)) * 8;
    const unsigned rrd = red_addr(lane);
    unsigned bud;
    asm volatile(ASM_R_LOOP : [bud] "=s"(bud)
                 : [wave] "s"(wave), [rs] "s"(rr), [l16] "v"(l16), [ov] "v"(offv), [o0] "v"(offp[0]), [o1] "v"(offp[1]), [o2] "v"(offp[2]), [oo] "v"(offo), [sidx] "v"(sidx), [rw] "v"(rw0), [rd] "v"(rrd),
                   [xo] "v"((l4 >> 1) * 256 + l15 * 16 + (l4 & 1) * 8), [pw] "v"(R_PART + hg * 1024 + l16), [pr] "v"(R_PART + (wave & 1) * 8192 + l16), [fo] "v"((lane & 3) * 128), [ae] "v"(ae),
                   [fs] "s"(UNI(FL_VO + (jq >> 1) * WIN + ((jq & 1) * 32 + hg * 4) * 128)), [vx] "s"(UNI(vxb(jq, 0) + (j10 * 4 + (hg >> 1)) * WIN + (hg & 1) * 4096)), [px] "s"(UNI(RG_PX + hg * WIN)),
                   [xr] "s"(UNI(D_XR + (j >> 3) * WIN + (wave & 1) * 4096 + (j & 7) * 512)), [ad] "s"(UNI(wave < 3 ? -1 : wave == 3 ? 0 : -2)), [as] "s"(UNI(as)) LST_IN
                 : "memory", "vcc", "scc", "v128", "v129", "v130", "v131", "v132", "v133", "v134", "v135", "v136", "v137", "v138", "v139", "v140", "v141", "v142", "v143", "v144", "v145", "v146", "v147", "v148", "v149", "v150", "v151", "v152", "v153", "v154", "v155", "v156", "v157", "v158", "v159", "v160", "v161", "v162", "v163", "v164", "v165", "v166", "v167", "v168", "v169", "v170", "v171", "v172", "v173", "v174", "v175", "v176", "v177", "v178", "v179", "v180", "v181", "v182", "v183", "v184", "v185", "v186", "v187", "v188", "v189", "v190", "v191", "v192", "v193", "v194", "v195", "v196", "v197", "v198", "v199", "v200", "v201", "v202", "v203", "v204", "v205", "v206", "v207", "v208", "v209", "v210", "v211", "v212", "v213", "v214", "v215", "v216", "v217", "v218", "v219", "v220", "v221", "v222", "v223", "v224", "v225", "v226", "v227", "v228", "v229", "v230", "v231", "v232", "v233", "v234", "v235", "v236", "v237", "v238", "v239", "v240", "v241", "v242", "v243", "v244", "v245", "v246", "v247", "v248", "v249", "v250", "v251", "v252", "v253", "v254", "v255", "s40", "s41", "s42", "s43", "s44", "s45", "s46", "s47", "s48", "s49", "s50", "s51", "s52", "s53", "s54", "s55", "s64", "s65", "s66", "s67", "s68", "s69", "s70", "s71", "s72", "s73", "s74", "s75", "s76", "s77", "s78", "s79");
    note_timeout(P, bud);
}

template <int WHICH>
__device__ __forceinline__ void w_role(const Params& P, int wave, int L, int cu) {
    constexpr int NT = (WHICH == 3) ? 2 : 1, N = (WHICH == 3) ? FF : DM, RB = NT * 256, RPI = 1024 / RB;
    const int ln = lane_id(), l15 = ln & 15, l4 = ln >> 4;
    const rsrc_t rd = mkrsrc(lbase(P, L), RG_L);
    const int nt0 = (cu * 8 + wave) * NT;
    const rsrc_t wr = mkrsrc((WHICH == 1 ? P.w1 : WHICH == 2 ? P.w2 : P.w3) + cu * (RB / 2), DM * N * 2 - cu * RB);
    auto swz = [](int row) { return ((row & 3) | ((row >> 1) & 4)) * 32; };
    const int dr = ln * 16 / RB, tr = l4 * 8 + ((ln >> 2) & 3);
    unsigned vo = dr * (N * 2) + ((ln * 16 % RB) ^ swz(wave * RPI + dr)), ta = tr * RB + ((wave * NT * 32 + (ln & 3) * 8) ^ swz(tr));
    const int grp = nt0 / 10, neg = grp & 1, sh = (WHICH == 1) ? (neg ? -1 : 1) * ((grp >> 1) + 1) : 0;
    unsigned ra = ln * 16, wa = ln * 16 + wave * 1024, sr = WHICH == 2 ? l15 * 8 : 24576 + l15 * 80, sw = sr + wave * 8, bud;
    const unsigned oo = WHICH == 3 ? (l4 & 1) * 512 + (l4 >> 1) * 256 + l15 * 16 : (nt0 >> 3) * WIN + (nt0 & 7) * 512 + (l4 >> 1) * 256 + ((l15 + sh) & 15) * 16 + (l4 & 1) * 8;
    const unsigned up = ((unsigned)(l15 + sh) > 15u) ^ neg;
#define W_ARGS : [bud] "=s"(bud), "+{v176}"(vo), "+{v177}"(ta), "+{v249}"(ra), "+{v250}"(wa), "+{v251}"(sr), "+{v252}"(sw) \
               : "{v253}"(ln * 16), "{v254}"(oo), "{v255}"(up), [wave] "s"(wave), [rs] "s"(rd), [wr] "s"(wr), [wm] "s"(UNI(wave * 1024)), [wo] "s"(UNI(wave * RPI * N * 2)), [go] "s"(FL_VO + (WHICH * 8 + cu) * 128), \
                 [pi] "s"(UNI((WHICH == 1 ? D_XA : WHICH == 2 ? D_HS : D_X1) + (wave >> 2) * WIN + (wave & 3) * 1024)), \
                 [ng] "s"(UNI(WHICH == 3 ? D_H2 + (nt0 >> 3) * WIN + ((nt0 >> 1) & 3) * 1024 : neg)), [ss] "s"(UNI(cu == 0)) LST_IN LST_INW \
               : "memory", "vcc", "scc", "v0", "v1", "v2", "v3", "v4", "v5", "v6", "v7", "v8", "v9", "v10", "v11", "v12", "v13", "v14", "v15", "v16", "v17", "v18", "v19", "v20", "v21", "v22", "v23", "v24", "v25", "v26", "v27", "v28", "v29", "v30", "v31", "v32", "v33", "v34", "v35", "v36", "v37", "v38", "v39", "v40", "v41", "v42", "v43", "v44", "v45", "v46", "v47", "v48", "v49", "v50", "v51", "v52", "v53", "v54", "v55", "v56", "v57", "v58", "v59", "v60", "v61", "v62", "v63", "v64", "v65", "v66", "v67", "v68", "v69", "v70", "v71", "v72", "v73", "v74", "v75", "v76", "v77", "v78", "v79", "v80", "v81", "v82", "v83", "v84", "v85", "v86", "v87", "v88", "v89", "v90", "v91", "v92", "v93", "v94", "v95", "v96", "v97", "v98", "v99", "v100", "v101", "v102", "v103", "v104", "v105", "v106", "v107", "v108", "v109", "v110", "v111", "v112", "v113", "v114", "v115", "v116", "v117", "v118", "v119", "v120", "v121", "v122", "v123", "v124", "v125", "v126", "v127", "v128", "v129", "v130", "v131", "v132", "v133", "v134", "v135", "v136", "v137", "v138", "v139", "v140", "v141", "v142", "v143", "v144", "v145", "v146", "v147", "v148", "v149", "v150", "v151", "v152", "v153", "v154", "v155", "v156", "v157", "v158", "v159", "v160", "v161", "v162", "v163", "v164", "v165", "v166", "v167", "v168", "v169", "v170", "v171", "v172", "v173", "v174", "v175", "v178", "v179", "v180", "v181", "v182", "v183", "v184", "v185", "v186", "v187", "v188", "v189", "v190", "v191", "v192", "v193", "v194", "v195", "v196", "v197", "v198", "v199", "v200", "v201", "v202", "v203", "v204", "v205", "v206", "v207", "v208", "v209", "v210", "v211", "v212", "v213", "v214", "v215", "v216", "v217", "v218", "v219", "v220", "v221", "v222", "v223", "v224", "v225", "v226", "v227", "v228", "v229", "v230", "v231", "v232", "v233", "v234", "v235", "v236", "v237", "v238", "v239", "v240", "v241", "v242", "v243", "v244", "v245", "v246", "v247", "v248", "s40", "s41", "s42", "s43", "s44", "s45", "s46", "s47", "s48", "s49", "s50", "s51", "s52", "s53", "s54", "s55", "s56", "s57", "s58", "s59", "s60", "s61", "s62", "s63", "s64", "s65", "s66", "s67", "s68", "s69", "s70", "s71", "s72", "s73", "s74", "s75", "s76", "s77", "s78", "s79"
    if (WHICH == 1) asm volatile(ASM_W1_LOOP W_ARGS);
    if (WHICH == 2) asm volatile(ASM_W2_LOOP W_ARGS);
    if (WHICH == 3) asm volatile(ASM_W3_LOOP W_ARGS);
    note_timeout(P, bud);
}

constexpr unsigned W4_FL = 122880;
__device__ __forceinline__ unsigned lds_vld(unsigned a) { asm volatile("" ::: "memory"); const unsigned v = *(volatile LAS unsigned*)(size_t)a; asm volatile("" ::: "memory"); return v; }
__device__ __forceinline__ void lds_vst(unsigned a, unsigned v) { asm volatile("" ::: "memory"); *(volatile LAS unsigned*)(size_t)a = v; asm volatile("" ::: "memory"); }
__device__ __forceinline__ void w4_role(const Params& P, int wave, int L, int cu) {
    const int ln = lane_id(), l15 = ln & 15, l4 = ln >> 4;
    const unsigned l16 = ln * 16;
    const rsrc_t rd = mkrsrc(lbase(P, L), RG_L);
    STDECL;
    if (wave >= 6) wait_go(P, rd, 32 + cu);
    if (wave < 6) {
        u32x32 w[5];
        {
            const rsrc_t wr = mkrsrc(P.w4 + wave * 256 * DM + cu * 80, (FF * DM - wave * 256 * DM - cu * 80) * 2);
            const unsigned ta = wave * 20480 + (l4 * 8 + ((ln >> 2) & 3)) * 160 + (ln & 3) * 8;
            u32x4 f[5][8];
#pragma unroll
            for (int r = 0; r < 2; ++r) {
#pragma unroll
                for (int i = 0; i < 5; ++i) {
                    const unsigned u = i * 64 + ln, vo = (u / 10) * (DM * 2) + (u % 10) * 16;
#pragma unroll
                    for (int k = 0; k < 4; ++k) dma1(wr, vo, (r * 128 + k * 32) * (DM * 2), wave * 20480 + k * 5120 + i * 1024);
                }
                wait_vm<0>();
#pragma unroll
                for (int k = 0; k < 4; ++k)
#pragma unroll
                    for (int n = 0; n < 5; ++n) f[n][r * 4 + k] = tr_frag(ta + k * 5120 + n * 32, 640);
                asm volatile("s_waitcnt lgkmcnt(0)" ::: "memory");
                SB();
            }
#pragma unroll
            for (int n = 0; n < 5; ++n) {
                u32x8 g[4];
#pragma unroll
                for (int i = 0; i < 4; ++i) g[i] = __builtin_shufflevector(f[n][2 * i], f[n][2 * i + 1], 0, 1, 2, 3, 4, 5, 6, 7);
                w[n] = cat32(g[0], g[1], g[2], g[3]);
            }
            asm volatile("" : "+{v[0:31]}"(w[0]), "+{v[32:63]}"(w[1]), "+{v[64:95]}"(w[2]), "+{v[96:127]}"(w[3]), "+{v[128:159]}"(w[4]));
        }
        wait_go(P, rd, 32 + cu);
        bar();
        unsigned budget = 1u << 17;
        for (int u = 0; u < 2 * NSEG; ++u) {
            ST(0);
            const unsigned gb = D_H2 + segh(u >> 1) + (u & 1) * 4096 + wave * 2 * WIN;
            while ((int)__builtin_amdgcn_readfirstlane(lds_vld(W4_FL + 24 + (u & 1) * 4)) < u - 3) __builtin_amdgcn_s_sleep(1);
            u32x4 can;
            for (int spins = 0;; ++spins) {
                can = LD128(rd, l16, gb + WIN + 3072);
                if (__all(umax4(0u, can) != SENT)) break;
                __builtin_amdgcn_s_sleep(1);
                if (spin_fail(spins, P)) { budget = 0; break; }
            }
            ST(1);
            asm volatile(ASM_W4_M : [n] "+s"(budget)
                         : "{v[188:191]}"(can), "{v[0:31]}"(w[0]), "{v[32:63]}"(w[1]), "{v[64:95]}"(w[2]), "{v[96:127]}"(w[3]), "{v[128:159]}"(w[4]),
                           [ld] "v"(l16 + wave * 1024 + (u & 3) * 30720), [l16] "v"(l16), [rs] "s"(rd), [s0] "s"(gb), [s1] "s"(gb + WIN)
                         : "memory", "vcc", "scc", "v160", "v161", "v162", "v163", "v164", "v165", "v166", "v167", "v168", "v169", "v170", "v171", "v172", "v173", "v174", "v175", "v176", "v177", "v178", "v179", "v180", "v181", "v182", "v183", "v184", "v185", "v186", "v187", "v192", "v193", "v194", "v195", "v196", "v197", "v198", "v199", "v200", "v201", "v202", "v203", "v204", "v205", "v206", "v207", "v208", "v209", "v210", "v211", "v212", "v213", "v214", "v215", "v216", "v217", "v218", "v219", "v220", "v221", "v222", "v223", "v224", "v225", "v226", "v227", "v228", "v229", "v230", "v231", "v232", "v233", "v234", "v235", "v236", "v237", "v238", "v239", "v240", "v241", "v242", "v243", "v244");
            lds_vst(W4_FL + wave * 4, u + 1);
            ST(2);
            if (u & 1) STFLUSH(u >> 1);
        }
    } else {
        const int mt = wave - 6;
        if (ln < 8) lds_vst(W4_FL + ln * 4, 0u);
        bar();
        const rsrc_t rn = mkrsrc(lbase(P, 1), RG_L), rr = rd, ro = mkrsrc(P.out, ACT);
        auto tl = [&](int n) { return (unsigned)((cu * 5 + n) >> 3) * WIN + ((cu * 5 + n) & 7) * 512 + mt * 4096; };
        const unsigned o8 = (l4 >> 1) * 256 + l15 * 16 + (l4 & 1) * 8, o16 = tl(l4 & 1) + (l4 >> 1) * 256 + l15 * 16, o16b = tl(2 + (l4 & 1)) + (l4 >> 1) * 256 + l15 * 16;
        const unsigned q8 = l15 * (DM * 2) + l4 * 8, q16 = l15 * (DM * 2) + (l4 & 1) * 32 + (l4 >> 1) * 16;
        for (int t = 0; t < NSEG; ++t) {
            const unsigned sb = segb(t);
            ST(0);
            f32x4 res[5];
            for (int spins = 0;; ++spins) {
                u32x2 r1[5], r2[5];
                unsigned m = 0;
#pragma unroll
                for (int n = 0; n < 5; ++n) { r1[n] = LD64(rd, o8, D_X1 + sb + tl(n)); r2[n] = LD64(rd, o8, D_XA + sb + tl(n)); }
#pragma unroll
                for (int n = 0; n < 5; ++n) {
                    m = umax4(m, (u32x4){r1[n][0], r1[n][1], r2[n][0], r2[n][1]});
                    res[n] = (f32x4){lo2f(r1[n][0]), hi2f(r1[n][0]), lo2f(r1[n][1]), hi2f(r1[n][1])} + (f32x4){lo2f(r2[n][0]), hi2f(r2[n][0]), lo2f(r2[n][1]), hi2f(r2[n][1])};
                }
                if (__all(m != SENT)) break;
                __builtin_amdgcn_s_sleep(8);
                if (spin_fail(spins * 4 + 3, P)) break;
            }
            asm volatile("" : "+v"(res[0]), "+v"(res[1]), "+v"(res[2]), "+v"(res[3]), "+v"(res[4]));
            ST(1);
            while (!__all(lds_vld(W4_FL + (ln < 6 ? ln : 0) * 4) > (unsigned)(2 * t + mt))) {}
            ST(2);
            {
                const unsigned la = ((t & 1) * 2 + mt) * 30720 + l16;
                f32x4 c[30];
#pragma unroll
                for (int i = 0; i < 30; ++i) c[i] = lds_ld<f32x4>(la + ((i % 5) * 6 + i / 5) * 1024);
#pragma unroll
                for (int i = 0; i < 30; ++i) { res[i % 5] += c[i]; asm volatile("" : "+v"(res[i % 5])); }
                lds_vst(W4_FL + 24 + mt * 4, 2 * t + mt + 1);
            }
            u32x2 o[5];
#pragma unroll
            for (int n = 0; n < 5; ++n) o[n] = (u32x2){pack2(res[n][0], res[n][1]), pack2(res[n][2], res[n][3])};
            if (L) {
                const unsigned ob = (unsigned)(t * 2 + mt) * 20480 + cu * 160;
                st128_<0>(merge16(o[0], o[1]), ro, q16, ob); st128_<0>(merge16(o[2], o[3]), ro, q16, ob + 64);
                __builtin_amdgcn_raw_buffer_store_b64(o[4], ro, q8, ob + 128, 0);
            } else {
                float s = 0.f, q = 0.f;
                const bf16x2 one2 = __builtin_bit_cast(bf16x2, 0x3F803F80u);
#pragma unroll
                for (int e = 0; e < 10; ++e) {
                    const unsigned u = o[e >> 1][e & 1];
                    const bf16x2 x = __builtin_bit_cast(bf16x2, u);
                    s = __builtin_amdgcn_fdot2_f32_bf16(x, one2, s, false);
                    q = __builtin_amdgcn_fdot2_f32_bf16(x, x, q, false);
                }
                sum2_rows(s, q);
                const unsigned st = RG_ST + (unsigned)(t >> 2) * WIN + (unsigned)((t & 3) * 2 + mt) * 1024;
                __builtin_amdgcn_raw_buffer_store_b64((u32x2){nosent(__float_as_uint(s)), nosent(__float_as_uint(q))}, rr, ln < 16 ? ln * 8 : OOB, st + cu * 128, 16);
                ST(3);
                const u32x4 x0 = merge16(o[0], o[1]), x1 = merge16(o[2], o[3]);
                const u32x2 x2 = o[4];
                __builtin_amdgcn_s_sleep(W4SL);
                for (int spins = 0;; ++spins) {
                    const u32x2 a = LD64(rr, ln * 8, st), b = LD64(rr, ln * 8 + 512, st);
                    s = __uint_as_float(a[0]) + __uint_as_float(b[0]); q = __uint_as_float(a[1]) + __uint_as_float(b[1]);
                    if (__all(umax4(0u, (u32x4){a[0], a[1], b[0], b[1]}) != SENT)) break;
                    asm volatile("" ::: "memory");
                    if (spin_fail(spins, P)) break;
                }
                ST(4);
                sum2_rows(s, q);
                const float mean = s * (1.0f / DM), var = __builtin_fmaxf(q * (1.0f / DM) - mean * mean, 0.f);
                const float r = rcp(ln_den(var)), mr = -rbf(mean) * r;
#pragma unroll
                for (int n = 0; n < 5; ++n)
                    o[n] = (u32x2){pack2(__builtin_fmaf(res[n][0], r, mr), __builtin_fmaf(res[n][1], r, mr)), pack2(__builtin_fmaf(res[n][2], r, mr), __builtin_fmaf(res[n][3], r, mr))};
                st128(merge16(o[0], o[1]), rn, o16, D_XN + sb); st128(merge16(o[2], o[3]), rn, o16b, D_XN + sb);
                __builtin_amdgcn_raw_buffer_store_b64(o[4], rn, o8, D_XN + sb + tl(4), 16);
                st128(x0, rn, o16, D_XR + sb); st128(x1, rn, o16b, D_XR + sb);
                __builtin_amdgcn_raw_buffer_store_b64(x2, rn, o8, D_XR + sb + tl(4), 16);
                wait_vm<0>();
                st128(((u32x4){(unsigned)t + 1, (unsigned)t + 1, (unsigned)t + 1, (unsigned)t + 1}), rn, ln < 32 ? (ln >> 3) * 2048 + (ln & 7) * 16 : OOB, FL_XN + (cu * 2 + mt) * 128);
            }
            ST(5);
            STFLUSH(t);
        }
    }
}

constexpr int LEADER = 252;
__device__ __forceinline__ unsigned leader(const Params& P, rsrc_t rw, int wave) {
    const int ln = lane_id();
    if (wave == 0) {
        const unsigned o = ln ? OOB : 0u;
        __builtin_amdgcn_raw_buffer_store_b32(0u, rw, o, CAL0, 16);
        __builtin_amdgcn_raw_buffer_store_b32(0u, rw, o, CAL0 + 8192, 16);
        unsigned m[2] = {~0u, ~0u};
#pragma unroll
        for (int r = 0; r < CALN; ++r) {
            unsigned long long a, c;
            asm volatile("s_waitcnt vmcnt(0) lgkmcnt(0)\n\ts_memtime %0\n\ts_waitcnt lgkmcnt(0)\n\tbuffer_store_dword %2, %2, %3, %4 offen sc1\n\ts_waitcnt vmcnt(0)\n\ts_memtime %1\n\ts_waitcnt lgkmcnt(0)"
                         : "=&s"(a), "=&s"(c) : "v"(o), "s"(rw), "s"(CAL0 + (r & 1) * 8192) : "memory");
            const unsigned d = (unsigned)(c - a);
            m[r & 1] = d < m[r & 1] ? d : m[r & 1];
        }
        lds_vst(0, m[0] < m[1] ? 1u : 0u);
    }
    bar();
    const unsigned par = __builtin_amdgcn_readfirstlane(lds_vld(0));
    const u32x4 s4 = sent4(), z4 = s4 ^ s4;
    for (int k = 0; k < 6; ++k) st128(z4, rw, ln * 16, FL_VO + (k * 8 + wave) * 1024);
    for (int k = 0; k < 10; ++k) st128(s4, rw, ln * 16, RG_XM + (wave >> 1) * XM_COPY + (wave & 1) * 8192 + (k >> 3) * WIN + (k & 7) * 1024);
    const unsigned mo = (wave * 32 + (ln >> 1)) * 128 + (ln & 1) * 16;
    st128(z4, rw, mo, FL_MB + 32768);
    if (wave == 0) st128(z4, rw, ln ? OOB : 256u, FL0);
    wait_vm<0>();
    bar();
    bool open = wave * 32 + (ln >> 1) != LEADER;
    for (int spins = 0; spins < SPIN_CAP; ++spins) {
        st128(s4 & (par << 13), rw, open ? mo : OOB, FL_MB);
        __builtin_amdgcn_s_sleep(20);
        const u32x4 d = LD128(rw, mo, FL_MB + 32768);
        open = open && !(d[0] && d[1] && d[2] && d[3]);
        if (!__any(open)) break;
    }
    bar();
    return par;
}
__global__ __launch_bounds__(512, 2) void mega_kernel(Params P) {
    const int wg = blockIdx.x;
    const int wave = __builtin_amdgcn_readfirstlane(threadIdx.x >> 6);
    const int L = (wg >> 2) & 1, x = wg & 3, r = wg >> 3, cpy = x;
    const int nr = (x & 1) ? 10 + RK : 10 - RK, j = (x >> 1) * 20 + (x & 1) * (10 - RK) + r - 16, i0 = (x >> 1) * 12 + (x & 1) * (6 + RK) + r - 16 - nr;
    const int i = (i0 * 5) % 24;
    const rsrc_t rw = mkrsrc(P.ws, RG_L);
    const bool lead = wg == LEADER;
    if (!lead) __builtin_amdgcn_raw_buffer_store_b32(SENT, rw, lane_id() ? OOB : 0u, FL_MB + wg * 128 + wave * 4, 16);
    if (L && r < 16) delay(D_A1);
#define FIN() __builtin_amdgcn_endpgm()
    if (r < 8) { s_role(P, wave, L, x * 8 + r, cpy); FIN(); }
    if (r < 16) { v_role(P, wave, L, x * 8 + r - 8, cpy); FIN(); }
    if (lead) P.par = leader(P, rw, wave);
    else {
        const unsigned mo = FL_MB + wg * 128 + wave * 4;
        const bool ln = r - 16 < nr && j < 16, first = ln && !L && wave == 7;
        if (!first) { P.par = handshake(rw, mo); if (L || r - 16 >= nr) delay(!L ? D_W0 : r - 16 < nr ? D_R1 : D_W1); }
        if (first || (ln && L && wave)) x_tile(P, (L ? wave * 16 : 0) + j, [&]() { if (first) P.par = handshake(rw, mo); return mkrsrc(lbase(P, 0), RG_L); });
        if (ln && L) bar();
        if (first || (ln && L && !wave)) {
            const rsrc_t r0 = mkrsrc(lbase(P, 0), RG_L);
            const int lane = lane_id();
            const unsigned fo = lane < 32 ? (lane >> 3) * 2048 + (lane & 7) * 16 : OOB;
            if (L) for (int spins = 0; spins < SPIN_CAP && __any(lane < 32 && LD128(r0, fo, FL_XN + j * 128)[0] == 0u); ++spins) __builtin_amdgcn_s_sleep(8);
            st128(sent4() & (L ? 64u : 8u), r0, fo, FL_XN + j * 128);
        }
    }
    if (r - 16 < nr) { r_role(P, wave, L, j, cpy); FIN(); }
    if (i < 5) { w_role<1>(P, wave, L, i); FIN(); }
    if (i < 10) { w_role<2>(P, wave, L, i - 5); FIN(); }
    if (i < 16) { w_role<3>(P, wave, L, i - 10); FIN(); }
    w4_role(P, wave, L, i - 16);
    FIN();
}

extern "C" void kernel_launch(void* const* d_in, const int* in_sizes, int n_in, void* d_out, int out_size, void* d_ws,
                              size_t ws_size, hipStream_t stream) {
    char* ws = (char*)d_ws + (-(size_t)d_ws & (REG - 1));
    Params P;
    P.par = 0; P.pad = 0; P.ws = ws; P.out = (bf16_t*)d_out;
    P.x = (const bf16_t*)d_in[0]; P.mem0 = (const bf16_t*)d_in[1]; P.wq = (const bf16_t*)d_in[2]; P.wk = (const bf16_t*)d_in[3]; P.wv = (const bf16_t*)d_in[4]; P.wo = (const bf16_t*)d_in[5];
    P.w1 = (const bf16_t*)d_in[8]; P.w2 = (const bf16_t*)d_in[9]; P.w3 = (const bf16_t*)d_in[10]; P.w4 = (const bf16_t*)d_in[11];
    (void)hipFuncSetAttribute((const void*)mega_kernel, hipFuncAttributeMaxDynamicSharedMemorySize, MEGA_LDS);

    mega_kernel<<<256, 512, MEGA_LDS, stream>>>(P);
}
```

```cpp
#include <hip/hip_runtime.h>
#include <stdint.h>
#include <type_traits>

typedef unsigned short bf16_t;
using bf16x2 = __attribute__((ext_vector_type(2))) __bf16;
using bf16x8 = __attribute__((ext_vector_type(8))) __bf16;
using bf16x16 = __attribute__((ext_vector_type(16))) __bf16;
using f32x2 = __attribute__((ext_vector_type(2))) float;
using f32x4 = __attribute__((ext_vector_type(4))) float;
using u32x2 = __attribute__((ext_vector_type(2))) unsigned;
using u32x4 = __attribute__((ext_vector_type(4))) unsigned;
typedef __amdgpu_buffer_rsrc_t rsrc_t;

__device__ __forceinline__ unsigned pack2(float a, float b) {
    bf16x2 r = __builtin_convertvector((f32x2){a, b}, bf16x2);
    return __builtin_bit_cast(unsigned, r);
}
__device__ __forceinline__ float rbf(float f) { return __uint_as_float(pack2(f, 0.f) << 16); }
__device__ __forceinline__ float lo2f(unsigned u) { return __uint_as_float(u << 16); }
__device__ __forceinline__ float hi2f(unsigned u) { return __uint_as_float(u & 0xFFFF0000u); }
__device__ __forceinline__ rsrc_t mkrsrc(const void* p, unsigned bytes) {
    return __builtin_amdgcn_make_buffer_rsrc((void*)p, 0, bytes, 0x00020000);
}

constexpr int S_ = 2048, DM = 640, NH = 32, DH = 192, HD = NH * DH  , SEG = 32, NSEG = 64;
constexpr int FF = 1536;

#define NCPY 4
constexpr unsigned NSLOT = 4, NCP = NCPY;
constexpr unsigned WIN = 16384, REG = 2u << 20, REGS = REG + 8192;
constexpr unsigned XP_COPY = 3 * WIN, XP_SLOT = NCP * XP_COPY;
constexpr unsigned XM_COPY = 2 * WIN, XM_SLOT = NCP * XM_COPY;
constexpr unsigned VM_SLOT = REGS;
constexpr unsigned PX_SLOT = 8 * WIN;
constexpr unsigned RG_XM = 0, RG_XP = RG_XM + NSLOT * XM_SLOT, RG_PX = RG_XP + NSLOT * XP_SLOT, RG_ST = RG_PX + NSLOT * PX_SLOT, RG_VM = 2 * REG;
static_assert(RG_ST + 16 * WIN <= REG, "ring region");
constexpr unsigned FL0 = REG, FL_PAR = REG + 4096, FL_MB = REG + 65536;
constexpr unsigned FL_VO = RG_VM + 48 * WIN, FL_XN = FL_VO + 2 * WIN, D_SS = FL_VO + 4 * WIN, CAL0 = RG_VM + 64 * WIN;
constexpr unsigned ACT = S_ * DM * 2;
constexpr unsigned D_XR = 6 * REG, D_XN = 10 * REG, D_XA = 14 * REG, D_HS = 18 * REG, D_X1 = 22 * REG, D_H2 = 26 * REG, D_VX = 34 * REG;
__host__ __device__ constexpr unsigned segb(unsigned seg) { return seg * (5 * WIN) + (seg >> 4) * (REGS - 80 * WIN); }
__host__ __device__ constexpr unsigned segh(unsigned seg) { return seg * (12 * WIN) + (seg >> 3) * (REGS - 96 * WIN); }
#define CALN 4
#define RK 0
#define PXROT 5
#define W4SL 6
#define RXDEL 4
#define VXD 4
#define VXAUX 16
__host__ __device__ constexpr unsigned vxb(unsigned q, unsigned t) { return D_VX + q * (VXD / 2 * REG) + (t & (VXD - 1)) * (REG / 2) + (t & 2) * 4096; }
constexpr int AH = 3;
constexpr unsigned RG_L = D_VX + 2 * VXD * REG;

#define SB() __builtin_amdgcn_sched_barrier(0)
#define LAS __attribute__((address_space(3)))
template <class T> __device__ __forceinline__ T lds_ld(unsigned a) { return *(const LAS T*)(a); }
template <class T> __device__ __forceinline__ void lds_st(unsigned a, T v) { *(LAS T*)(a) = v; }
constexpr unsigned OOB = 0x7FFFFF00u;
template <int N> __device__ __forceinline__ void wait_vm() { asm volatile("s_waitcnt vmcnt(%0)" :: "i"(N) : "memory"); }
__device__ __forceinline__ void bar() {
    asm volatile("s_waitcnt lgkmcnt(0)" ::: "memory");
    __builtin_amdgcn_s_barrier();
    asm volatile("" ::: "memory");
}
__device__ __forceinline__ int lane_id() {
    int ln;
    asm volatile("v_mbcnt_lo_u32_b32 %0, -1, 0\n\tv_mbcnt_hi_u32_b32 %0, -1, %0" : "=v"(ln));
    return ln;
}
__device__ __forceinline__ void swap16(unsigned& a, unsigned& b) { asm volatile("s_nop 3\n\tv_permlane16_swap_b32 %0, %1\n\ts_nop 1" : "+v"(a), "+v"(b)); }
__device__ __forceinline__ void swap32(unsigned& a, unsigned& b) { asm volatile("s_nop 3\n\tv_permlane32_swap_b32 %0, %1\n\ts_nop 1" : "+v"(a), "+v"(b)); }
__device__ __forceinline__ float sum_x16(float x) { unsigned a = __float_as_uint(x), b = a; swap16(a, b); return __uint_as_float(a) + __uint_as_float(b); }
__device__ __forceinline__ float sum_x32(float x) { unsigned a = __float_as_uint(x), b = a; swap32(a, b); return __uint_as_float(a) + __uint_as_float(b); }
__device__ __forceinline__ float max_x16(float x) { unsigned a = __float_as_uint(x), b = a; swap16(a, b); return __builtin_fmaxf(__uint_as_float(a), __uint_as_float(b)); }
__device__ __forceinline__ float max_x32(float x) { unsigned a = __float_as_uint(x), b = a; swap32(a, b); return __builtin_fmaxf(__uint_as_float(a), __uint_as_float(b)); }
__device__ __forceinline__ void sum2_rows(float& s, float& q) {
    unsigned a = __float_as_uint(s), b = a, c = __float_as_uint(q), d = c;
    asm volatile("s_nop 2\n\tv_permlane16_swap_b32 %0, %1\n\tv_permlane16_swap_b32 %2, %3\n\tv_add_f32 %0, %0, %1\n\tv_add_f32 %2, %2, %3\n\tv_mov_b32 %1, %0\n\tv_mov_b32 %3, %2\n\t"
                 "s_nop 1\n\tv_permlane32_swap_b32 %0, %1\n\tv_permlane32_swap_b32 %2, %3\n\tv_add_f32 %0, %0, %1\n\tv_add_f32 %2, %2, %3" : "+v"(a), "+v"(b), "+v"(c), "+v"(d));
    s = __uint_as_float(a); q = __uint_as_float(c);
}
__device__ __forceinline__ u32x4 merge16(u32x2 a, u32x2 b) {
    unsigned a0 = a[0], a1 = a[1], b0 = b[0], b1 = b[1];
    asm volatile("s_nop 3\n\tv_permlane16_swap_b32 %0, %2\n\tv_permlane16_swap_b32 %1, %3\n\ts_nop 1" : "+v"(a0), "+v"(a1), "+v"(b0), "+v"(b1));
    return (u32x4){a0, a1, b0, b1};
}
template <int AUXV>
__device__ __forceinline__ void st128_(u32x4 v, rsrc_t r, unsigned voff, unsigned soff) {
    __builtin_amdgcn_raw_buffer_store_b128(v, r, voff, soff, AUXV);
    asm volatile("s_nop 1" :: "v"(v) : "memory");
}
#define st128(V, R, VO_, SO_) st128_<16>(V, R, VO_, SO_)
#define LD128(R, VO_, SO_) __builtin_amdgcn_raw_buffer_load_b128(R, VO_, SO_, 16)
#define LD64(R, VO_, SO_) __builtin_amdgcn_raw_buffer_load_b64(R, VO_, SO_, 16)
#define BF8(X) __builtin_bit_cast(bf16x8, X)
__device__ __forceinline__ float gelu_tanh(float x) {
    const float c0 = -2.0f * 0.7978845608028654f * 1.4426950408889634f, c1 = c0 * 0.044715f;
    const float e = __builtin_amdgcn_exp2f(x * __builtin_fmaf(x * x, c1, c0));
    return x * __builtin_amdgcn_rcpf(1.0f + e);
}
#define MFMA(A_, B_, C_) C_ = __builtin_amdgcn_mfma_f32_16x16x32_bf16(A_, B_, C_, 0, 0, 0)
#define SMF(A_, B_, C_) C_ = __builtin_amdgcn_smfmac_f32_16x16x64_bf16(A_, B_, C_, sidx, 0, 0)
__device__ __forceinline__ float ln_den(float var) { return rbf(__builtin_amdgcn_sqrtf(rbf(rbf(var) + 1e-5f))); }
__device__ __forceinline__ float rcp(float x) { return __builtin_amdgcn_rcpf(x); }

struct Params {
    char* ws;
    const bf16_t *x, *mem0, *wq, *wk, *wv, *wo, *w1, *w2, *w3, *w4;
    bf16_t* out;
    unsigned par, pad;
};

#define RLX_AGENT __ATOMIC_RELAXED, __HIP_MEMORY_SCOPE_AGENT
#define SENT 0xFFFFFFFFu
constexpr int SPIN_CAP = 400000;
__device__ __forceinline__ bool spin_fail(int spins, const Params& P) {
    if ((spins & 1023) == 1023) {
        const rsrc_t rt = mkrsrc(P.ws + FL0, 512);
        unsigned o;
        asm volatile("v_mov_b32 %0, 0x100" : "=v"(o));
        if (__builtin_amdgcn_readfirstlane(__builtin_amdgcn_raw_buffer_load_b32(rt, o, 0, 16)) != 0u) return true;
        if (spins > SPIN_CAP) { __builtin_amdgcn_raw_buffer_store_b32(o, rt, o, 0, 16); return true; }
    }
    return false;
}
__device__ __forceinline__ bool ok4(u32x4 v) { return v[0] != SENT && v[1] != SENT && v[2] != SENT && v[3] != SENT; }
__device__ __forceinline__ unsigned nosent(unsigned d) { return d < 0xFFFFFFFEu ? d : 0xFFFFFFFEu; }
__device__ __forceinline__ u32x4 nosent4(u32x4 v) { return (u32x4){nosent(v[0]), nosent(v[1]), nosent(v[2]), nosent(v[3])}; }
__device__ __forceinline__ unsigned umax3(unsigned a, unsigned b, unsigned c) { const unsigned m = a > b ? a : b; return m > c ? m : c; }
__device__ __forceinline__ unsigned umax4(unsigned m, u32x4 v) { return umax3(umax3(m, v[0], v[1]), v[2], v[3]); }
__device__ __forceinline__ u32x4 sent4() { unsigned sv; asm volatile("v_mov_b32 %0, -1" : "=v"(sv)); return (u32x4){sv, sv, sv, sv}; }
template <int N>
__device__ __forceinline__ void poll(rsrc_t rs, unsigned base, const unsigned (&off)[N], u32x4 (&v)[N], const Params& P) {
    for (int spins = 0;; ++spins) {
        unsigned m = 0;
#pragma unroll
        for (int k = 0; k < N; ++k) v[k] = LD128(rs, off[k], base);
#pragma unroll
        for (int k = 0; k < N; ++k) m = umax4(m, v[k]);
        if (__all(m != SENT)) return;
        asm volatile("" ::: "memory");
        if (spin_fail(spins, P)) return;
    }
}
__device__ __forceinline__ bf16x16 cat16(bf16x8 a, bf16x8 b) {
    return __builtin_shufflevector(a, b, 0, 1, 2, 3, 4, 5, 6, 7, 8, 9, 10, 11, 12, 13, 14, 15);
}
__device__ __forceinline__ bf16x8 half8(bf16x16 v, int h) {
    return h ? __builtin_shufflevector(v, v, 8, 9, 10, 11, 12, 13, 14, 15) : __builtin_shufflevector(v, v, 0, 1, 2, 3, 4, 5, 6, 7);
}
__device__ __forceinline__ bf16x16 lds_frag(unsigned a, unsigned step) { return cat16(lds_ld<bf16x8>(a), lds_ld<bf16x8>(a + step)); }
__device__ __forceinline__ unsigned toff(int row, int col) {
    return segb((unsigned)row >> 5) + (unsigned)(col >> 7) * WIN + (unsigned)(row & 16) * 256u + (unsigned)((col >> 3) & 15) * 256u + (unsigned)(row & 15) * 16u + (unsigned)(col & 7) * 2u;
}
__device__ __forceinline__ char* lbase(const Params& P, int L) { return P.ws + ((P.par ^ L) << 13); }

#define ASM_ST
#define ASM_ST_DECL
#define ASM_ST_OUT
#define ASM_ST_SAVE(k)
#define ASM_STW
#define ASM_ST3
#define ASM_ST3_OUT
#define ASM_ST3_SAVE(k)
#define ASM_ST2
#define ASM_ST2_OUT
#define ASM_ST2_SAVE(k)
#define LST0
#define LST1
#define LST2
#define LST3
#define LST4
#define LST5
#define LST6
#define LST7
#define LSTINIT
#define LFLUSH200
#define LFLUSH212
#define LFLUSHM
#define LFLUSHF
#define LFLUSHR
#define LFLUSHW
#define LST_INW
#define LST_IN
#define STDECL
#define ST(k)
#define STFLUSH(t)

#define LJIT
#define CJIT(m)
constexpr unsigned L_X = 0;
constexpr unsigned L_MEM = 40960;
constexpr unsigned L_Q = 51200;
constexpr unsigned L_KM = 54272;
constexpr unsigned L_QX = 57344;
constexpr unsigned L_KX = 69632;
constexpr unsigned L_W = 81920;
constexpr int MEGA_LDS = 163840;
constexpr int wi(int i, int j) { return (i >> 1) * 5 + ((i & 1) ? 2 + j : j - 1); }

#define RPUB2
#define ASM_S_LOOP \
    "s_mov_b64 s[60:61], %[ws]\n\t" \
    "s_mov_b32 s62, %[sz]\n\t" \
    "s_mov_b32 s63, 0x20000\n\t" \
    "s_mov_b32 s40, 0\n\t" \
    "s_mov_b32 s41, 0x20000\n\t" \
    LSTINIT \
    LDELAY \
    "s_cmp_eq_u32 %[wave], 0\n\t" \
    "s_cbranch_scc1 .Ls0_%=\n\t" \
    "s_cmp_eq_u32 %[wave], 1\n\t" \
    "s_cbranch_scc1 .Ls1_%=\n\t" \
    "s_cmp_eq_u32 %[wave], 2\n\t" \
    "s_cbranch_scc1 .Ls2_%=\n\t" \
    "s_cmp_eq_u32 %[wave], 3\n\t" \
    "s_cbranch_scc1 .Ls3_%=\n\t" \
    ".Ls4_%=:\n\t" \
    "s_mov_b32 s42, %[xs]\n\t" \
    "s_add_u32 m0, %[wm], 0x18000\n\t" \
    "s_mov_b32 s44, 0x0\n\t" \
    "buffer_load_dwordx4 v240, %[wr], s44 offen offset:0 nt lds\n\t" \
    "buffer_load_dwordx4 v241, %[wr], s44 offen offset:1024 nt lds\n\t" \
    "buffer_load_dwordx4 v242, %[wr], s44 offen offset:2048 nt lds\n\t" \
    "s_add_u32 m0, %[wm], 0x1e000\n\t" \
    "s_mov_b32 s44, 0x60000\n\t" \
    "buffer_load_dwordx4 v240, %[wr], s44 offen offset:0 nt lds\n\t" \
    "buffer_load_dwordx4 v241, %[wr], s44 offen offset:1024 nt lds\n\t" \
    "buffer_load_dwordx4 v242, %[wr], s44 offen offset:2048 nt lds\n\t" \
    "s_add_u32 m0, %[wm], 0x0\n\t" \
    "s_mov_b32 s44, 0xc0000\n\t" \
    "buffer_load_dwordx4 v240, %[wr], s44 offen offset:0 nt lds\n\t" \
    "buffer_load_dwordx4 v241, %[wr], s44 offen offset:1024 nt lds\n\t" \
    "buffer_load_dwordx4 v242, %[wr], s44 offen offset:2048 nt lds\n\t" \
    "s_add_u32 m0, %[wm], 0x6000\n\t" \
    "s_mov_b32 s44, 0x120000\n\t" \
    "buffer_load_dwordx4 v240, %[wr], s44 offen offset:0 nt lds\n\t" \
    "buffer_load_dwordx4 v241, %[wr], s44 offen offset:1024 nt lds\n\t" \
    "buffer_load_dwordx4 v242, %[wr], s44 offen offset:2048 nt lds\n\t" \
    "s_add_u32 m0, %[wm], 0xc000\n\t" \
    "s_mov_b32 s44, 0x180000\n\t" \
    "buffer_load_dwordx4 v240, %[wr], s44 offen offset:0 nt lds\n\t" \
    "buffer_load_dwordx4 v241, %[wr], s44 offen offset:1024 nt lds\n\t" \
    "buffer_load_dwordx4 v242, %[wr], s44 offen offset:2048 nt lds\n\t" \
    "v_add_u32 v244, 0x18000, v243\n\t" \
    "s_waitcnt vmcnt(12) lgkmcnt(0)\n\t" \
    "s_barrier \n\t" \
    "s_add_u32 m0, %[wm], 0x12000\n\t" \
    "s_mov_b32 s44, 0x1e0000\n\t" \
    "buffer_load_dwordx4 v240, %[wr], s44 offen offset:0 nt lds\n\t" \
    "buffer_load_dwordx4 v241, %[wr], s44 offen offset:1024 nt lds\n\t" \
    "buffer_load_dwordx4 v242, %[wr], s44 offen offset:2048 nt lds\n\t" \
    "ds_read_b64_tr_b16 v[200:201], v244 offset:0\n\t" \
    "ds_read_b64_tr_b16 v[202:203], v244 offset:1536\n\t" \
    "ds_read_b64_tr_b16 v[0:1], v244 offset:32\n\t" \
    "ds_read_b64_tr_b16 v[2:3], v244 offset:1568\n\t" \
    "ds_read_b64_tr_b16 v[8:9], v244 offset:64\n\t" \
    "ds_read_b64_tr_b16 v[10:11], v244 offset:1600\n\t" \
    "v_add_u32 v244, 0x1e000, v243\n\t" \
    "s_waitcnt vmcnt(12) lgkmcnt(0)\n\t" \
    "s_barrier \n\t" \
    "s_add_u32 m0, %[wm], 0x18000\n\t" \
    "s_mov_b32 s44, 0x240000\n\t" \
    "buffer_load_dwordx4 v240, %[wr], s44 offen offset:0 nt lds\n\t" \
    "buffer_load_dwordx4 v241, %[wr], s44 offen offset:1024 nt lds\n\t" \
    "buffer_load_dwordx4 v242, %[wr], s44 offen offset:2048 nt lds\n\t" \
    "ds_read_b64_tr_b16 v[204:205], v244 offset:0\n\t" \
    "ds_read_b64_tr_b16 v[206:207], v244 offset:1536\n\t" \
    "ds_read_b64_tr_b16 v[4:5], v244 offset:32\n\t" \
    "ds_read_b64_tr_b16 v[6:7], v244 offset:1568\n\t" \
    "ds_read_b64_tr_b16 v[12:13], v244 offset:64\n\t" \
    "ds_read_b64_tr_b16 v[14:15], v244 offset:1600\n\t" \
    "v_add_u32 v244, 0x0, v243\n\t" \
    "s_waitcnt vmcnt(12) lgkmcnt(0)\n\t" \
    "s_barrier \n\t" \
    "s_add_u32 m0, %[wm], 0x1e000\n\t" \
    "s_mov_b32 s44, 0x2a0000\n\t" \
    "buffer_load_dwordx4 v240, %[wr], s44 offen offset:0 nt lds\n\t" \
    "buffer_load_dwordx4 v241, %[wr], s44 offen offset:1024 nt lds\n\t" \
    "buffer_load_dwordx4 v242, %[wr], s44 offen offset:2048 nt lds\n\t" \
    "ds_read_b64_tr_b16 v[16:17], v244 offset:0\n\t" \
    "ds_read_b64_tr_b16 v[18:19], v244 offset:1536\n\t" \
    "ds_read_b64_tr_b16 v[24:25], v244 offset:32\n\t" \
    "ds_read_b64_tr_b16 v[26:27], v244 offset:1568\n\t" \
    "ds_read_b64_tr_b16 v[32:33], v244 offset:64\n\t" \
    "ds_read_b64_tr_b16 v[34:35], v244 offset:1600\n\t" \
    "v_add_u32 v244, 0x6000, v243\n\t" \
    "s_waitcnt vmcnt(12) lgkmcnt(0)\n\t" \
    "s_barrier \n\t" \
    "s_add_u32 m0, %[wm], 0x0\n\t" \
    "s_mov_b32 s44, 0x300000\n\t" \
    "buffer_load_dwordx4 v240, %[wr], s44 offen offset:0 nt lds\n\t" \
    "buffer_load_dwordx4 v241, %[wr], s44 offen offset:1024 nt lds\n\t" \
    "buffer_load_dwordx4 v242, %[wr], s44 offen offset:2048 nt lds\n\t" \
    "ds_read_b64_tr_b16 v[20:21], v244 offset:0\n\t" \
    "ds_read_b64_tr_b16 v[22:23], v244 offset:1536\n\t" \
    "ds_read_b64_tr_b16 v[28:29], v244 offset:32\n\t" \
    "ds_read_b64_tr_b16 v[30:31], v244 offset:1568\n\t" \
    "ds_read_b64_tr_b16 v[36:37], v244 offset:64\n\t" \
    "ds_read_b64_tr_b16 v[38:39], v244 offset:1600\n\t" \
    "v_add_u32 v244, 0xc000, v243\n\t" \
    "s_waitcnt vmcnt(12) lgkmcnt(0)\n\t" \
    "s_barrier \n\t" \
    "s_add_u32 m0, %[wm], 0x6000\n\t" \
    "s_mov_b32 s44, 0x360000\n\t" \
    "buffer_load_dwordx4 v240, %[wr], s44 offen offset:0 nt lds\n\t" \
    "buffer_load_dwordx4 v241, %[wr], s44 offen offset:1024 nt lds\n\t" \
    "buffer_load_dwordx4 v242, %[wr], s44 offen offset:2048 nt lds\n\t" \
    "ds_read_b64_tr_b16 v[208:209], v244 offset:0\n\t" \
    "ds_read_b64_tr_b16 v[210:211], v244 offset:1536\n\t" \
    "ds_read_b64_tr_b16 v[40:41], v244 offset:32\n\t" \
    "ds_read_b64_tr_b16 v[42:43], v244 offset:1568\n\t" \
    "ds_read_b64_tr_b16 v[48:49], v244 offset:64\n\t" \
    "ds_read_b64_tr_b16 v[50:51], v244 offset:1600\n\t" \
    "v_add_u32 v244, 0x12000, v243\n\t" \
    "s_waitcnt vmcnt(12) lgkmcnt(0)\n\t" \
    "s_barrier \n\t" \
    "s_add_u32 m0, %[wm], 0xc000\n\t" \
    "s_mov_b32 s44, 0x3c0000\n\t" \
    "buffer_load_dwordx4 v240, %[wr], s44 offen offset:0 nt lds\n\t" \
    "buffer_load_dwordx4 v241, %[wr], s44 offen offset:1024 nt lds\n\t" \
    "buffer_load_dwordx4 v242, %[wr], s44 offen offset:2048 nt lds\n\t" \
    "ds_read_b64_tr_b16 v[212:213], v244 offset:0\n\t" \
    "ds_read_b64_tr_b16 v[214:215], v244 offset:1536\n\t" \
    "ds_read_b64_tr_b16 v[44:45], v244 offset:32\n\t" \
    "ds_read_b64_tr_b16 v[46:47], v244 offset:1568\n\t" \
    "ds_read_b64_tr_b16 v[52:53], v244 offset:64\n\t" \
    "ds_read_b64_tr_b16 v[54:55], v244 offset:1600\n\t" \
    "v_add_u32 v244, 0x18000, v243\n\t" \
    "s_waitcnt vmcnt(12) lgkmcnt(0)\n\t" \
    "s_barrier \n\t" \
    "v_mov_b32 v246, 0\n\t" \
    "buffer_load_dword v246, v246, s[60:63], %[mo] offen sc1\n\t" \
    "s_add_u32 m0, %[wm], 0x12000\n\t" \
    "s_mov_b32 s44, 0x420000\n\t" \
    "buffer_load_dwordx4 v240, %[wr], s44 offen offset:0 nt lds\n\t" \
    "buffer_load_dwordx4 v241, %[wr], s44 offen offset:1024 nt lds\n\t" \
    "buffer_load_dwordx4 v242, %[wr], s44 offen offset:2048 nt lds\n\t" \
    "ds_read_b64_tr_b16 v[56:57], v244 offset:0\n\t" \
    "ds_read_b64_tr_b16 v[58:59], v244 offset:1536\n\t" \
    "ds_read_b64_tr_b16 v[64:65], v244 offset:32\n\t" \
    "ds_read_b64_tr_b16 v[66:67], v244 offset:1568\n\t" \
    "ds_read_b64_tr_b16 v[72:73], v244 offset:64\n\t" \
    "ds_read_b64_tr_b16 v[74:75], v244 offset:1600\n\t" \
    "v_add_u32 v244, 0x1e000, v243\n\t" \
    "s_waitcnt vmcnt(12) lgkmcnt(0)\n\t" \
    "s_barrier \n\t" \
    "s_add_u32 m0, %[wm], 0x18000\n\t" \
    "s_mov_b32 s44, 0x480000\n\t" \
    "buffer_load_dwordx4 v240, %[wr], s44 offen offset:0 nt lds\n\t" \
    "buffer_load_dwordx4 v241, %[wr], s44 offen offset:1024 nt lds\n\t" \
    "buffer_load_dwordx4 v242, %[wr], s44 offen offset:2048 nt lds\n\t" \
    "ds_read_b64_tr_b16 v[60:61], v244 offset:0\n\t" \
    "ds_read_b64_tr_b16 v[62:63], v244 offset:1536\n\t" \
    "ds_read_b64_tr_b16 v[68:69], v244 offset:32\n\t" \
    "ds_read_b64_tr_b16 v[70:71], v244 offset:1568\n\t" \
    "ds_read_b64_tr_b16 v[76:77], v244 offset:64\n\t" \
    "ds_read_b64_tr_b16 v[78:79], v244 offset:1600\n\t" \
    "v_add_u32 v244, 0x0, v243\n\t" \
    "s_waitcnt vmcnt(12) lgkmcnt(0)\n\t" \
    "s_barrier \n\t" \
    "s_add_u32 m0, %[wm], 0x1e000\n\t" \
    "s_mov_b32 s44, 0x4e0000\n\t" \
    "buffer_load_dwordx4 v240, %[wr], s44 offen offset:0 nt lds\n\t" \
    "buffer_load_dwordx4 v241, %[wr], s44 offen offset:1024 nt lds\n\t" \
    "buffer_load_dwordx4 v242, %[wr], s44 offen offset:2048 nt lds\n\t" \
    "ds_read_b64_tr_b16 v[216:217], v244 offset:0\n\t" \
    "ds_read_b64_tr_b16 v[218:219], v244 offset:1536\n\t" \
    "ds_read_b64_tr_b16 v[80:81], v244 offset:32\n\t" \
    "ds_read_b64_tr_b16 v[82:83], v244 offset:1568\n\t" \
    "ds_read_b64_tr_b16 v[88:89], v244 offset:64\n\t" \
    "ds_read_b64_tr_b16 v[90:91], v244 offset:1600\n\t" \
    "v_add_u32 v244, 0x6000, v243\n\t" \
    "s_waitcnt vmcnt(12) lgkmcnt(0)\n\t" \
    "s_barrier \n\t" \
    "s_add_u32 m0, %[wm], 0x0\n\t" \
    "s_mov_b32 s44, 0x540000\n\t" \
    "buffer_load_dwordx4 v240, %[wr], s44 offen offset:0 nt lds\n\t" \
    "buffer_load_dwordx4 v241, %[wr], s44 offen offset:1024 nt lds\n\t" \
    "buffer_load_dwordx4 v242, %[wr], s44 offen offset:2048 nt lds\n\t" \
    "ds_read_b64_tr_b16 v[220:221], v244 offset:0\n\t" \
    "ds_read_b64_tr_b16 v[222:223], v244 offset:1536\n\t" \
    "ds_read_b64_tr_b16 v[84:85], v244 offset:32\n\t" \
    "ds_read_b64_tr_b16 v[86:87], v244 offset:1568\n\t" \
    "ds_read_b64_tr_b16 v[92:93], v244 offset:64\n\t" \
    "ds_read_b64_tr_b16 v[94:95], v244 offset:1600\n\t" \
    "v_add_u32 v244, 0xc000, v243\n\t" \
    "s_waitcnt vmcnt(12) lgkmcnt(0)\n\t" \
    "s_barrier \n\t" \
    "s_add_u32 m0, %[wm], 0x6000\n\t" \
    "s_mov_b32 s44, 0x5a0000\n\t" \
    "buffer_load_dwordx4 v240, %[wr], s44 offen offset:0 nt lds\n\t" \
    "buffer_load_dwordx4 v241, %[wr], s44 offen offset:1024 nt lds\n\t" \
    "buffer_load_dwordx4 v242, %[wr], s44 offen offset:2048 nt lds\n\t" \
    "ds_read_b64_tr_b16 v[96:97], v244 offset:0\n\t" \
    "ds_read_b64_tr_b16 v[98:99], v244 offset:1536\n\t" \
    "ds_read_b64_tr_b16 v[104:105], v244 offset:32\n\t" \
    "ds_read_b64_tr_b16 v[106:107], v244 offset:1568\n\t" \
    "ds_read_b64_tr_b16 v[112:113], v244 offset:64\n\t" \
    "ds_read_b64_tr_b16 v[114:115], v244 offset:1600\n\t" \
    "v_add_u32 v244, 0x12000, v243\n\t" \
    "s_waitcnt vmcnt(12) lgkmcnt(0)\n\t" \
    "s_barrier \n\t" \
    ".Ls41_%=:\n\t" \
    "v_readfirstlane_b32 s43, v246\n\t" \
    "s_nop 0\n\t" \
    "s_cmp_lg_u32 s43, -1\n\t" \
    "s_cbranch_scc1 .Ls42_%=\n\t" \
    "s_cmp_eq_u32 s41, 0\n\t" \
    "s_cbranch_scc1 .Ls42_%=\n\t" \
    "s_sub_u32 s41, s41, 1\n\t" \
    "s_sleep 4\n\t" \
    "v_mov_b32 v246, 0\n\t" \
    "buffer_load_dword v246, v246, s[60:63], %[mo] offen sc1\n\t" \
    "s_waitcnt vmcnt(0)\n\t" \
    "s_branch .Ls41_%=\n\t" \
    ".Ls42_%=:\n\t" \
    "v_mov_b32 v246, 0x8000\n\t" \
    "s_mov_b64 exec, 1\n\t" \
    "buffer_store_dword v246, v246, s[60:63], %[mo] offen sc1\n\t" \
    "s_mov_b64 exec, -1\n\t" \
    "s_and_b32 s43, s43, 0x2000\n\t" \
    "s_xor_b32 s43, s43, %[lx]\n\t" \
    "s_add_u32 s60, s60, s43\n\t" \
    "s_addc_u32 s61, s61, 0\n\t" \
    "v_and_b32 v252, 0xf0, v253\n\t" \
    "v_lshlrev_b32 v252, 3, v252\n\t" \
    "buffer_load_dword v252, v252, s[60:63], %[fl] offen sc1\n\t" \
    "s_add_u32 m0, %[wm], 0xc000\n\t" \
    "s_mov_b32 s44, 0x600000\n\t" \
    "buffer_load_dwordx4 v240, %[wr], s44 offen offset:0 nt lds\n\t" \
    "buffer_load_dwordx4 v241, %[wr], s44 offen offset:1024 nt lds\n\t" \
    "buffer_load_dwordx4 v242, %[wr], s44 offen offset:2048 nt lds\n\t" \
    "ds_read_b64_tr_b16 v[100:101], v244 offset:0\n\t" \
    "ds_read_b64_tr_b16 v[102:103], v244 offset:1536\n\t" \
    "ds_read_b64_tr_b16 v[108:109], v244 offset:32\n\t" \
    "ds_read_b64_tr_b16 v[110:111], v244 offset:1568\n\t" \
    "ds_read_b64_tr_b16 v[116:117], v244 offset:64\n\t" \
    "ds_read_b64_tr_b16 v[118:119], v244 offset:1600\n\t" \
    "v_add_u32 v244, 0x18000, v243\n\t" \
    "s_waitcnt vmcnt(12) lgkmcnt(0)\n\t" \
    "s_barrier \n\t" \
    "s_add_u32 m0, %[wm], 0x12000\n\t" \
    "s_mov_b32 s44, 0x660000\n\t" \
    "buffer_load_dwordx4 v240, %[wr], s44 offen offset:0 nt lds\n\t" \
    "buffer_load_dwordx4 v241, %[wr], s44 offen offset:1024 nt lds\n\t" \
    "buffer_load_dwordx4 v242, %[wr], s44 offen offset:2048 nt lds\n\t" \
    "ds_read_b64_tr_b16 v[224:225], v244 offset:0\n\t" \
    "ds_read_b64_tr_b16 v[226:227], v244 offset:1536\n\t" \
    "ds_read_b64_tr_b16 v[120:121], v244 offset:32\n\t" \
    "ds_read_b64_tr_b16 v[122:123], v244 offset:1568\n\t" \
    "ds_read_b64_tr_b16 v[128:129], v244 offset:64\n\t" \
    "ds_read_b64_tr_b16 v[130:131], v244 offset:1600\n\t" \
    "v_add_u32 v244, 0x1e000, v243\n\t" \
    "s_waitcnt vmcnt(12) lgkmcnt(0)\n\t" \
    "s_barrier \n\t" \
    "s_add_u32 m0, %[wm], 0x18000\n\t" \
    "s_mov_b32 s44, 0x6c0000\n\t" \
    "buffer_load_dwordx4 v240, %[wr], s44 offen offset:0 nt lds\n\t" \
    "buffer_load_dwordx4 v241, %[wr], s44 offen offset:1024 nt lds\n\t" \
    "buffer_load_dwordx4 v242, %[wr], s44 offen offset:2048 nt lds\n\t" \
    "ds_read_b64_tr_b16 v[228:229], v244 offset:0\n\t" \
    "ds_read_b64_tr_b16 v[230:231], v244 offset:1536\n\t" \
    "ds_read_b64_tr_b16 v[124:125], v244 offset:32\n\t" \
    "ds_read_b64_tr_b16 v[126:127], v244 offset:1568\n\t" \
    "ds_read_b64_tr_b16 v[132:133], v244 offset:64\n\t" \
    "ds_read_b64_tr_b16 v[134:135], v244 offset:1600\n\t" \
    "v_add_u32 v244, 0x0, v243\n\t" \
    "s_waitcnt vmcnt(12) lgkmcnt(0)\n\t" \
    "s_barrier \n\t" \
    "s_add_u32 m0, %[wm], 0x1e000\n\t" \
    "s_mov_b32 s44, 0x720000\n\t" \
    "buffer_load_dwordx4 v240, %[wr], s44 offen offset:0 nt lds\n\t" \
    "buffer_load_dwordx4 v241, %[wr], s44 offen offset:1024 nt lds\n\t" \
    "buffer_load_dwordx4 v242, %[wr], s44 offen offset:2048 nt lds\n\t" \
    "ds_read_b64_tr_b16 v[136:137], v244 offset:0\n\t" \
    "ds_read_b64_tr_b16 v[138:139], v244 offset:1536\n\t" \
    "ds_read_b64_tr_b16 v[144:145], v244 offset:32\n\t" \
    "ds_read_b64_tr_b16 v[146:147], v244 offset:1568\n\t" \
    "ds_read_b64_tr_b16 v[152:153], v244 offset:64\n\t" \
    "ds_read_b64_tr_b16 v[154:155], v244 offset:1600\n\t" \
    "v_add_u32 v244, 0x6000, v243\n\t" \
    "s_waitcnt vmcnt(12) lgkmcnt(0)\n\t" \
    "s_barrier \n\t" \
    "ds_read_b64_tr_b16 v[140:141], v244 offset:0\n\t" \
    "ds_read_b64_tr_b16 v[142:143], v244 offset:1536\n\t" \
    "ds_read_b64_tr_b16 v[148:149], v244 offset:32\n\t" \
    "ds_read_b64_tr_b16 v[150:151], v244 offset:1568\n\t" \
    "ds_read_b64_tr_b16 v[156:157], v244 offset:64\n\t" \
    "ds_read_b64_tr_b16 v[158:159], v244 offset:1600\n\t" \
    "v_add_u32 v244, 0xc000, v243\n\t" \
    "s_waitcnt vmcnt(9) lgkmcnt(0)\n\t" \
    "s_barrier \n\t" \
    "ds_read_b64_tr_b16 v[232:233], v244 offset:0\n\t" \
    "ds_read_b64_tr_b16 v[234:235], v244 offset:1536\n\t" \
    "ds_read_b64_tr_b16 v[160:161], v244 offset:32\n\t" \
    "ds_read_b64_tr_b16 v[162:163], v244 offset:1568\n\t" \
    "ds_read_b64_tr_b16 v[168:169], v244 offset:64\n\t" \
    "ds_read_b64_tr_b16 v[170:171], v244 offset:1600\n\t" \
    "s_mov_b32 s43, 0\n\t" \
    "v_cmp_eq_u32 vcc, 0, v252\n\t" \
    "s_cbranch_vccnz .Ls43_%=\n\t" \
    "s_mov_b32 m0, %[m0]\n\t" \
    "s_add_u32 s45, s42, %[x1]\n\t" \
    "buffer_load_dwordx4 v253, s[60:63], s42 offen offset:0 sc1 lds\n\t" \
    "buffer_load_dwordx4 v253, s[60:63], s42 offen offset:1024 sc1 lds\n\t" \
    "buffer_load_dwordx4 v253, s[60:63], s42 offen offset:2048 sc1 lds\n\t" \
    "buffer_load_dwordx4 v253, s[60:63], s42 offen offset:3072 sc1 lds\n\t" \
    "s_mov_b32 m0, %[m1]\n\t" \
    "s_nop 0\n\t" \
    "buffer_load_dwordx4 v253, s[60:63], s45 offen offset:0 sc1 lds\n\t" \
    "buffer_load_dwordx4 v253, s[60:63], s45 offen offset:1024 sc1 lds\n\t" \
    "s_mov_b32 s43, 1\n\t" \
    ".Ls43_%=:\n\t" \
    "v_add_u32 v244, 0x12000, v243\n\t" \
    "s_waitcnt vmcnt(6) lgkmcnt(0)\n\t" \
    "s_barrier \n\t" \
    "ds_read_b64_tr_b16 v[236:237], v244 offset:0\n\t" \
    "ds_read_b64_tr_b16 v[238:239], v244 offset:1536\n\t" \
    "ds_read_b64_tr_b16 v[164:165], v244 offset:32\n\t" \
    "ds_read_b64_tr_b16 v[166:167], v244 offset:1568\n\t" \
    "ds_read_b64_tr_b16 v[172:173], v244 offset:64\n\t" \
    "ds_read_b64_tr_b16 v[174:175], v244 offset:1600\n\t" \
    "v_add_u32 v244, 0x18000, v243\n\t" \
    "s_waitcnt vmcnt(3) lgkmcnt(0)\n\t" \
    "s_barrier \n\t" \
    "ds_read_b64_tr_b16 v[176:177], v244 offset:0\n\t" \
    "ds_read_b64_tr_b16 v[178:179], v244 offset:1536\n\t" \
    "ds_read_b64_tr_b16 v[184:185], v244 offset:32\n\t" \
    "ds_read_b64_tr_b16 v[186:187], v244 offset:1568\n\t" \
    "ds_read_b64_tr_b16 v[192:193], v244 offset:64\n\t" \
    "ds_read_b64_tr_b16 v[194:195], v244 offset:1600\n\t" \
    "v_add_u32 v244, 0x1e000, v243\n\t" \
    "s_waitcnt vmcnt(0) lgkmcnt(0)\n\t" \
    "s_barrier \n\t" \
    "ds_read_b64_tr_b16 v[180:181], v244 offset:0\n\t" \
    "ds_read_b64_tr_b16 v[182:183], v244 offset:1536\n\t" \
    "ds_read_b64_tr_b16 v[188:189], v244 offset:32\n\t" \
    "ds_read_b64_tr_b16 v[190:191], v244 offset:1568\n\t" \
    "ds_read_b64_tr_b16 v[196:197], v244 offset:64\n\t" \
    "ds_read_b64_tr_b16 v[198:199], v244 offset:1600\n\t" \
    "s_waitcnt lgkmcnt(0)\n\t" \
    "s_barrier \n\t" \
    LST0 \
    LJIT \
    "ds_write_b128 v254, v[200:203] offset:0\n\t" \
    "ds_write_b128 v254, v[204:207] offset:1024\n\t" \
    "ds_write_b128 v254, v[208:211] offset:2048\n\t" \
    "ds_write_b128 v254, v[212:215] offset:3072\n\t" \
    "ds_write_b128 v254, v[216:219] offset:4096\n\t" \
    "ds_write_b128 v254, v[220:223] offset:5120\n\t" \
    "ds_write_b128 v254, v[224:227] offset:6144\n\t" \
    "ds_write_b128 v254, v[228:231] offset:7168\n\t" \
    "ds_write_b128 v254, v[232:235] offset:8192\n\t" \
    "ds_write_b128 v254, v[236:239] offset:9216\n\t" \
    "s_cmp_lg_u32 s43, 0\n\t" \
    "s_cbranch_scc1 .Ls44_%=\n\t" \
    "s_mov_b32 s44, 0\n\t" \
    ".Ls45_%=:\n\t" \
    "v_cmp_ge_u32 vcc, s44, v252\n\t" \
    "s_cbranch_vccz .Ls46_%=\n\t" \
    "s_cmp_eq_u32 s41, 0\n\t" \
    "s_cbranch_scc1 .Ls46_%=\n\t" \
    "s_sub_u32 s41, s41, 1\n\t" \
    "s_sleep 8\n\t" \
    "v_and_b32 v252, 0xf0, v253\n\t" \
    "v_lshlrev_b32 v252, 3, v252\n\t" \
    "buffer_load_dword v252, v252, s[60:63], %[fl] offen sc1\n\t" \
    "s_waitcnt vmcnt(0)\n\t" \
    "s_branch .Ls45_%=\n\t" \
    ".Ls46_%=:\n\t" \
    "s_mov_b32 m0, %[m0]\n\t" \
    "s_add_u32 s45, s42, %[x1]\n\t" \
    "buffer_load_dwordx4 v253, s[60:63], s42 offen offset:0 sc1 lds\n\t" \
    "buffer_load_dwordx4 v253, s[60:63], s42 offen offset:1024 sc1 lds\n\t" \
    "buffer_load_dwordx4 v253, s[60:63], s42 offen offset:2048 sc1 lds\n\t" \
    "buffer_load_dwordx4 v253, s[60:63], s42 offen offset:3072 sc1 lds\n\t" \
    "s_mov_b32 m0, %[m1]\n\t" \
    "s_nop 0\n\t" \
    "buffer_load_dwordx4 v253, s[60:63], s45 offen offset:0 sc1 lds\n\t" \
    "buffer_load_dwordx4 v253, s[60:63], s45 offen offset:1024 sc1 lds\n\t" \
    ".Ls44_%=:\n\t" \
    "s_waitcnt vmcnt(0) lgkmcnt(0)\n\t" \
    "s_barrier \n\t" \
    "ds_read_b128 v[224:227], v253 offset:0\n\t" \
    "ds_read_b128 v[244:247], v254 offset:0\n\t" \
    "ds_read_b128 v[228:231], v253 offset:1024\n\t" \
    "ds_read_b128 v[232:235], v253 offset:20480\n\t" \
    "ds_read_b128 v[236:239], v253 offset:21504\n\t" \
    "ds_read_b128 v[248:251], v254 offset:1024\n\t" \
    ".Ls47_%=:\n\t" \
    "s_cmp_lg_u32 %[wave], 4\n\t" \
    "s_cbranch_scc1 .Ls48_%=\n\t" \
    "s_sleep 9\n\t" \
    ".Ls48_%=:\n\t" \
    "s_waitcnt lgkmcnt(5)\n\t" \
    "v_mfma_f32_16x16x32_bf16 v[204:207], v[0:3], v[224:227], 0\n\t" \
    "s_add_u32 m0, %[m0], 0x3000\n\t" \
    "s_waitcnt lgkmcnt(3)\n\t" \
    "v_mfma_f32_16x16x32_bf16 v[204:207], v[4:7], v[228:231], v[204:207]\n\t" \
    "ds_read_b128 v[240:243], v253 offset:2048\n\t" \
    "s_add_u32 s45, s42, 0xc000\n\t" \
    "v_mfma_f32_16x16x32_bf16 v[208:211], v[8:11], v[224:227], 0\n\t" \
    "buffer_load_dwordx4 v253, s[60:63], s45 offen offset:0 sc1 lds\n\t" \
    "v_mfma_f32_16x16x32_bf16 v[208:211], v[12:15], v[228:231], v[208:211]\n\t" \
    "v_mfma_f32_16x16x32_bf16 v[200:203], v[244:247], v[224:227], 0\n\t" \
    "buffer_load_dwordx4 v253, s[60:63], s45 offen offset:1024 sc1 lds\n\t" \
    "s_waitcnt lgkmcnt(1)\n\t" \
    "v_mfma_f32_16x16x32_bf16 v[200:203], v[248:251], v[228:231], v[200:203]\n\t" \
    "v_mfma_f32_16x16x32_bf16 v[216:219], v[0:3], v[232:235], 0\n\t" \
    "ds_read_b128 v[224:227], v253 offset:3072\n\t" \
    "buffer_load_dwordx4 v253, s[60:63], s45 offen offset:2048 sc1 lds\n\t" \
    "v_mfma_f32_16x16x32_bf16 v[216:219], v[4:7], v[236:239], v[216:219]\n\t" \
    "ds_read_b128 v[228:231], v253 offset:22528\n\t" \
    "v_mfma_f32_16x16x32_bf16 v[220:223], v[8:11], v[232:235], 0\n\t" \
    "buffer_load_dwordx4 v253, s[60:63], s45 offen offset:3072 sc1 lds\n\t" \
    "v_mfma_f32_16x16x32_bf16 v[220:223], v[12:15], v[236:239], v[220:223]\n\t" \
    "v_mfma_f32_16x16x32_bf16 v[212:215], v[244:247], v[232:235], 0\n\t" \
    "ds_read_b128 v[244:247], v254 offset:2048\n\t" \
    "v_mfma_f32_16x16x32_bf16 v[212:215], v[248:251], v[236:239], v[212:215]\n\t" \
    "ds_read_b128 v[248:251], v254 offset:3072\n\t" \
    "s_waitcnt lgkmcnt(4)\n\t" \
    "v_mfma_f32_16x16x32_bf16 v[204:207], v[24:27], v[240:243], v[204:207]\n\t" \
    "ds_read_b128 v[232:235], v253 offset:23552\n\t" \
    "s_waitcnt lgkmcnt(4)\n\t" \
    "v_mfma_f32_16x16x32_bf16 v[204:207], v[28:31], v[224:227], v[204:207]\n\t" \
    "ds_read_b128 v[236:239], v253 offset:4096\n\t" \
    "v_mfma_f32_16x16x32_bf16 v[208:211], v[32:35], v[240:243], v[208:211]\n\t" \
    "v_mfma_f32_16x16x32_bf16 v[208:211], v[36:39], v[224:227], v[208:211]\n\t" \
    "v_mfma_f32_16x16x32_bf16 v[200:203], v[16:19], v[240:243], v[200:203]\n\t" \
    "v_mfma_f32_16x16x32_bf16 v[200:203], v[20:23], v[224:227], v[200:203]\n\t" \
    "s_waitcnt lgkmcnt(4)\n\t" \
    "v_mfma_f32_16x16x32_bf16 v[216:219], v[24:27], v[228:231], v[216:219]\n\t" \
    "ds_read_b128 v[240:243], v253 offset:5120\n\t" \
    "s_waitcnt lgkmcnt(2)\n\t" \
    "v_mfma_f32_16x16x32_bf16 v[216:219], v[28:31], v[232:235], v[216:219]\n\t" \
    "ds_read_b128 v[224:227], v253 offset:24576\n\t" \
    "v_mfma_f32_16x16x32_bf16 v[220:223], v[32:35], v[228:231], v[220:223]\n\t" \
    "v_mfma_f32_16x16x32_bf16 v[220:223], v[36:39], v[232:235], v[220:223]\n\t" \
    "v_mfma_f32_16x16x32_bf16 v[212:215], v[16:19], v[228:231], v[212:215]\n\t" \
    "v_mfma_f32_16x16x32_bf16 v[212:215], v[20:23], v[232:235], v[212:215]\n\t" \
    "s_waitcnt lgkmcnt(2)\n\t" \
    "v_mfma_f32_16x16x32_bf16 v[204:207], v[40:43], v[236:239], v[204:207]\n\t" \
    "ds_read_b128 v[228:231], v253 offset:25600\n\t" \
    "s_waitcnt lgkmcnt(2)\n\t" \
    "v_mfma_f32_16x16x32_bf16 v[204:207], v[44:47], v[240:243], v[204:207]\n\t" \
    "ds_read_b128 v[232:235], v253 offset:6144\n\t" \
    "v_mfma_f32_16x16x32_bf16 v[208:211], v[48:51], v[236:239], v[208:211]\n\t" \
    "v_mfma_f32_16x16x32_bf16 v[208:211], v[52:55], v[240:243], v[208:211]\n\t" \
    "v_mfma_f32_16x16x32_bf16 v[200:203], v[244:247], v[236:239], v[200:203]\n\t" \
    "v_mfma_f32_16x16x32_bf16 v[200:203], v[248:251], v[240:243], v[200:203]\n\t" \
    "s_waitcnt lgkmcnt(2)\n\t" \
    "v_mfma_f32_16x16x32_bf16 v[216:219], v[40:43], v[224:227], v[216:219]\n\t" \
    "ds_read_b128 v[236:239], v253 offset:7168\n\t" \
    "s_waitcnt lgkmcnt(2)\n\t" \
    "v_mfma_f32_16x16x32_bf16 v[216:219], v[44:47], v[228:231], v[216:219]\n\t" \
    "ds_read_b128 v[240:243], v253 offset:26624\n\t" \
    "v_mfma_f32_16x16x32_bf16 v[220:223], v[48:51], v[224:227], v[220:223]\n\t" \
    "v_mfma_f32_16x16x32_bf16 v[220:223], v[52:55], v[228:231], v[220:223]\n\t" \
    "v_mfma_f32_16x16x32_bf16 v[212:215], v[244:247], v[224:227], v[212:215]\n\t" \
    "ds_read_b128 v[244:247], v254 offset:4096\n\t" \
    "v_mfma_f32_16x16x32_bf16 v[212:215], v[248:251], v[228:231], v[212:215]\n\t" \
    "ds_read_b128 v[248:251], v254 offset:5120\n\t" \
    "s_waitcnt lgkmcnt(4)\n\t" \
    "v_mfma_f32_16x16x32_bf16 v[204:207], v[64:67], v[232:235], v[204:207]\n\t" \
    "ds_read_b128 v[224:227], v253 offset:27648\n\t" \
    "s_waitcnt lgkmcnt(4)\n\t" \
    "v_mfma_f32_16x16x32_bf16 v[204:207], v[68:71], v[236:239], v[204:207]\n\t" \
    "ds_read_b128 v[228:231], v253 offset:8192\n\t" \
    "v_mfma_f32_16x16x32_bf16 v[208:211], v[72:75], v[232:235], v[208:211]\n\t" \
    "v_mfma_f32_16x16x32_bf16 v[208:211], v[76:79], v[236:239], v[208:211]\n\t" \
    "v_mfma_f32_16x16x32_bf16 v[200:203], v[56:59], v[232:235], v[200:203]\n\t" \
    "v_mfma_f32_16x16x32_bf16 v[200:203], v[60:63], v[236:239], v[200:203]\n\t" \
    "v_and_b32 v252, 0xf0, v253\n\t" \
    "v_lshlrev_b32 v252, 3, v252\n\t" \
    "buffer_load_dword v252, v252, s[60:63], %[fl] offen sc1\n\t" \
    "s_waitcnt lgkmcnt(4)\n\t" \
    "v_mfma_f32_16x16x32_bf16 v[216:219], v[64:67], v[240:243], v[216:219]\n\t" \
    "ds_read_b128 v[232:235], v253 offset:9216\n\t" \
    "s_waitcnt lgkmcnt(2)\n\t" \
    "v_mfma_f32_16x16x32_bf16 v[216:219], v[68:71], v[224:227], v[216:219]\n\t" \
    "ds_read_b128 v[236:239], v253 offset:28672\n\t" \
    "v_mfma_f32_16x16x32_bf16 v[220:223], v[72:75], v[240:243], v[220:223]\n\t" \
    "v_mfma_f32_16x16x32_bf16 v[220:223], v[76:79], v[224:227], v[220:223]\n\t" \
    "v_mfma_f32_16x16x32_bf16 v[212:215], v[56:59], v[240:243], v[212:215]\n\t" \
    "v_mfma_f32_16x16x32_bf16 v[212:215], v[60:63], v[224:227], v[212:215]\n\t" \
    "s_waitcnt lgkmcnt(2)\n\t" \
    "v_mfma_f32_16x16x32_bf16 v[204:207], v[80:83], v[228:231], v[204:207]\n\t" \
    "ds_read_b128 v[240:243], v253 offset:29696\n\t" \
    "s_waitcnt lgkmcnt(2)\n\t" \
    "v_mfma_f32_16x16x32_bf16 v[204:207], v[84:87], v[232:235], v[204:207]\n\t" \
    "ds_read_b128 v[224:227], v253 offset:10240\n\t" \
    "v_mfma_f32_16x16x32_bf16 v[208:211], v[88:91], v[228:231], v[208:211]\n\t" \
    "v_mfma_f32_16x16x32_bf16 v[208:211], v[92:95], v[232:235], v[208:211]\n\t" \
    "v_mfma_f32_16x16x32_bf16 v[200:203], v[244:247], v[228:231], v[200:203]\n\t" \
    "v_mfma_f32_16x16x32_bf16 v[200:203], v[248:251], v[232:235], v[200:203]\n\t" \
    "s_waitcnt lgkmcnt(2)\n\t" \
    "v_mfma_f32_16x16x32_bf16 v[216:219], v[80:83], v[236:239], v[216:219]\n\t" \
    "ds_read_b128 v[228:231], v253 offset:11264\n\t" \
    "s_add_u32 s44, s40, 1\n\t" \
    "s_waitcnt lgkmcnt(2)\n\t" \
    "v_mfma_f32_16x16x32_bf16 v[216:219], v[84:87], v[240:243], v[216:219]\n\t" \
    "ds_read_b128 v[232:235], v253 offset:30720\n\t" \
    "s_and_b32 s45, s44, 15\n\t" \
    "v_mfma_f32_16x16x32_bf16 v[220:223], v[88:91], v[236:239], v[220:223]\n\t" \
    "s_cmp_eq_u32 s45, 0\n\t" \
    "v_mfma_f32_16x16x32_bf16 v[220:223], v[92:95], v[240:243], v[220:223]\n\t" \
    "s_cselect_b32 s45, %[cj], %[cs]\n\t" \
    "v_mfma_f32_16x16x32_bf16 v[212:215], v[244:247], v[236:239], v[212:215]\n\t" \
    "ds_read_b128 v[244:247], v254 offset:6144\n\t" \
    "s_cmp_eq_u32 s44, 64\n\t" \
    "v_mfma_f32_16x16x32_bf16 v[212:215], v[248:251], v[240:243], v[212:215]\n\t" \
    "ds_read_b128 v[248:251], v254 offset:7168\n\t" \
    "s_cselect_b32 s45, 0, s45\n\t" \
    "ds_read_b128 v[236:239], v253 offset:31744\n\t" \
    "s_setprio 1\n\t" \
    "s_waitcnt vmcnt(0) lgkmcnt(0)\n\t" \
    "s_barrier \n\t" \
    LST7 \
    LJIT \
    "s_add_u32 s42, s42, s45\n\t" \
    "s_add_u32 s44, s40, 1\n\t" \
    "s_min_u32 s44, s44, 63\n\t" \
    ".Ls49_%=:\n\t" \
    "v_cmp_ge_u32 vcc, s44, v252\n\t" \
    "s_cbranch_vccz .Ls410_%=\n\t" \
    "s_cmp_eq_u32 s41, 0\n\t" \
    "s_cbranch_scc1 .Ls410_%=\n\t" \
    "s_sub_u32 s41, s41, 1\n\t" \
    "s_sleep 2\n\t" \
    "v_and_b32 v252, 0xf0, v253\n\t" \
    "v_lshlrev_b32 v252, 3, v252\n\t" \
    "buffer_load_dword v252, v252, s[60:63], %[fl] offen sc1\n\t" \
    "s_waitcnt vmcnt(0)\n\t" \
    "s_branch .Ls49_%=\n\t" \
    ".Ls410_%=:\n\t" \
    "s_mov_b32 m0, %[m0]\n\t" \
    "s_add_u32 s45, s42, %[x1]\n\t" \
    "buffer_load_dwordx4 v253, s[60:63], s42 offen offset:0 sc1 lds\n\t" \
    "buffer_load_dwordx4 v253, s[60:63], s42 offen offset:1024 sc1 lds\n\t" \
    "buffer_load_dwordx4 v253, s[60:63], s42 offen offset:2048 sc1 lds\n\t" \
    "buffer_load_dwordx4 v253, s[60:63], s42 offen offset:3072 sc1 lds\n\t" \
    "s_mov_b32 m0, %[m1]\n\t" \
    "s_nop 0\n\t" \
    "buffer_load_dwordx4 v253, s[60:63], s45 offen offset:0 sc1 lds\n\t" \
    "buffer_load_dwordx4 v253, s[60:63], s45 offen offset:1024 sc1 lds\n\t" \
    "v_mfma_f32_16x16x32_bf16 v[204:207], v[104:107], v[224:227], v[204:207]\n\t" \
    "v_mfma_f32_16x16x32_bf16 v[204:207], v[108:111], v[228:231], v[204:207]\n\t" \
    "ds_read_b128 v[240:243], v253 offset:12288\n\t" \
    "v_mfma_f32_16x16x32_bf16 v[208:211], v[112:115], v[224:227], v[208:211]\n\t" \
    "v_mfma_f32_16x16x32_bf16 v[208:211], v[116:119], v[228:231], v[208:211]\n\t" \
    "v_mfma_f32_16x16x32_bf16 v[200:203], v[96:99], v[224:227], v[200:203]\n\t" \
    "v_mfma_f32_16x16x32_bf16 v[200:203], v[100:103], v[228:231], v[200:203]\n\t" \
    "v_mfma_f32_16x16x32_bf16 v[216:219], v[104:107], v[232:235], v[216:219]\n\t" \
    "ds_read_b128 v[224:227], v253 offset:13312\n\t" \
    "v_mfma_f32_16x16x32_bf16 v[216:219], v[108:111], v[236:239], v[216:219]\n\t" \
    "ds_read_b128 v[228:231], v253 offset:32768\n\t" \
    "v_mfma_f32_16x16x32_bf16 v[220:223], v[112:115], v[232:235], v[220:223]\n\t" \
    "v_mfma_f32_16x16x32_bf16 v[220:223], v[116:119], v[236:239], v[220:223]\n\t" \
    "v_mfma_f32_16x16x32_bf16 v[212:215], v[96:99], v[232:235], v[212:215]\n\t" \
    "v_mfma_f32_16x16x32_bf16 v[212:215], v[100:103], v[236:239], v[212:215]\n\t" \
    "s_waitcnt lgkmcnt(2)\n\t" \
    "v_mfma_f32_16x16x32_bf16 v[204:207], v[120:123], v[240:243], v[204:207]\n\t" \
    "ds_read_b128 v[232:235], v253 offset:33792\n\t" \
    "s_waitcnt lgkmcnt(2)\n\t" \
    "v_mfma_f32_16x16x32_bf16 v[204:207], v[124:127], v[224:227], v[204:207]\n\t" \
    "ds_read_b128 v[236:239], v253 offset:16384\n\t" \
    "v_mfma_f32_16x16x32_bf16 v[208:211], v[128:131], v[240:243], v[208:211]\n\t" \
    "v_mfma_f32_16x16x32_bf16 v[208:211], v[132:135], v[224:227], v[208:211]\n\t" \
    "v_mfma_f32_16x16x32_bf16 v[200:203], v[244:247], v[240:243], v[200:203]\n\t" \
    "v_mfma_f32_16x16x32_bf16 v[200:203], v[248:251], v[224:227], v[200:203]\n\t" \
    "s_waitcnt lgkmcnt(2)\n\t" \
    "v_mfma_f32_16x16x32_bf16 v[216:219], v[120:123], v[228:231], v[216:219]\n\t" \
    "ds_read_b128 v[240:243], v253 offset:17408\n\t" \
    "s_waitcnt lgkmcnt(2)\n\t" \
    "v_mfma_f32_16x16x32_bf16 v[216:219], v[124:127], v[232:235], v[216:219]\n\t" \
    "ds_read_b128 v[224:227], v253 offset:36864\n\t" \
    "v_mfma_f32_16x16x32_bf16 v[220:223], v[128:131], v[228:231], v[220:223]\n\t" \
    "v_mfma_f32_16x16x32_bf16 v[220:223], v[132:135], v[232:235], v[220:223]\n\t" \
    "v_mfma_f32_16x16x32_bf16 v[212:215], v[244:247], v[228:231], v[212:215]\n\t" \
    "ds_read_b128 v[244:247], v254 offset:8192\n\t" \
    "v_mfma_f32_16x16x32_bf16 v[212:215], v[248:251], v[232:235], v[212:215]\n\t" \
    "ds_read_b128 v[248:251], v254 offset:9216\n\t" \
    "s_waitcnt lgkmcnt(4)\n\t" \
    "v_mfma_f32_16x16x32_bf16 v[204:207], v[160:163], v[236:239], v[204:207]\n\t" \
    "ds_read_b128 v[228:231], v253 offset:37888\n\t" \
    "s_waitcnt lgkmcnt(4)\n\t" \
    "v_mfma_f32_16x16x32_bf16 v[204:207], v[164:167], v[240:243], v[204:207]\n\t" \
    "ds_read_b128 v[232:235], v253 offset:14336\n\t" \
    "v_mfma_f32_16x16x32_bf16 v[208:211], v[168:171], v[236:239], v[208:211]\n\t" \
    "v_mfma_f32_16x16x32_bf16 v[208:211], v[172:175], v[240:243], v[208:211]\n\t" \
    "s_waitcnt lgkmcnt(3)\n\t" \
    "v_mfma_f32_16x16x32_bf16 v[200:203], v[244:247], v[236:239], v[200:203]\n\t" \
    "s_waitcnt lgkmcnt(2)\n\t" \
    "v_mfma_f32_16x16x32_bf16 v[200:203], v[248:251], v[240:243], v[200:203]\n\t" \
    "v_mfma_f32_16x16x32_bf16 v[216:219], v[160:163], v[224:227], v[216:219]\n\t" \
    "ds_read_b128 v[236:239], v253 offset:15360\n\t" \
    "v_add_u32 v252, %[q2], v253\n\t" \
    "s_waitcnt lgkmcnt(2)\n\t" \
    "v_mfma_f32_16x16x32_bf16 v[216:219], v[164:167], v[228:231], v[216:219]\n\t" \
    "ds_read_b128 v[240:243], v253 offset:18432\n\t" \
    "v_mfma_f32_16x16x32_bf16 v[220:223], v[168:171], v[224:227], v[220:223]\n\t" \
    "v_mfma_f32_16x16x32_bf16 v[220:223], v[172:175], v[228:231], v[220:223]\n\t" \
    "v_mfma_f32_16x16x32_bf16 v[212:215], v[244:247], v[224:227], v[212:215]\n\t" \
    "v_mfma_f32_16x16x32_bf16 v[212:215], v[248:251], v[228:231], v[212:215]\n\t" \
    "s_waitcnt lgkmcnt(2)\n\t" \
    "v_mfma_f32_16x16x32_bf16 v[204:207], v[144:147], v[232:235], v[204:207]\n\t" \
    "ds_read_b128 v[224:227], v253 offset:19456\n\t" \
    "v_mov_b64 v[244:245], 0\n\t" \
    "s_waitcnt lgkmcnt(2)\n\t" \
    "v_mfma_f32_16x16x32_bf16 v[204:207], v[148:151], v[236:239], v[204:207]\n\t" \
    "ds_read_b128 v[228:231], v253 offset:34816\n\t" \
    "v_mov_b64 v[246:247], 0\n\t" \
    "v_mfma_f32_16x16x32_bf16 v[208:211], v[152:155], v[232:235], v[208:211]\n\t" \
    "v_mov_b64 v[248:249], 0\n\t" \
    "v_mfma_f32_16x16x32_bf16 v[208:211], v[156:159], v[236:239], v[208:211]\n\t" \
    "v_mov_b64 v[250:251], 0\n\t" \
    "v_mfma_f32_16x16x32_bf16 v[200:203], v[136:139], v[232:235], v[200:203]\n\t" \
    "v_mfma_f32_16x16x32_bf16 v[200:203], v[140:143], v[236:239], v[200:203]\n\t" \
    "s_waitcnt lgkmcnt(2)\n\t" \
    "v_mfma_f32_16x16x32_bf16 v[204:207], v[184:187], v[240:243], v[204:207]\n\t" \
    "ds_read_b128 v[232:235], v253 offset:35840\n\t" \
    "s_waitcnt lgkmcnt(2)\n\t" \
    "v_mfma_f32_16x16x32_bf16 v[204:207], v[188:191], v[224:227], v[204:207]\n\t" \
    "ds_read_b128 v[236:239], v253 offset:38912\n\t" \
    "v_mfma_f32_16x16x32_bf16 v[208:211], v[192:195], v[240:243], v[208:211]\n\t" \
    "v_mfma_f32_16x16x32_bf16 v[208:211], v[196:199], v[224:227], v[208:211]\n\t" \
    "v_mfma_f32_16x16x32_bf16 v[200:203], v[176:179], v[240:243], v[200:203]\n\t" \
    "v_mfma_f32_16x16x32_bf16 v[200:203], v[180:183], v[224:227], v[200:203]\n\t" \
    "ds_read_b128 v[240:243], v253 offset:39936\n\t" \
    "s_waitcnt lgkmcnt(3)\n\t" \
    "v_mfma_f32_16x16x32_bf16 v[216:219], v[144:147], v[228:231], v[216:219]\n\t" \
    "s_waitcnt lgkmcnt(2)\n\t" \
    "v_mfma_f32_16x16x32_bf16 v[216:219], v[148:151], v[232:235], v[216:219]\n\t" \
    "v_cvt_pk_bf16_f32 v204, v204, v208\n\t" \
    "v_cvt_pk_bf16_f32 v205, v205, v209\n\t" \
    "s_waitcnt lgkmcnt(1)\n\t" \
    "v_mfma_f32_16x16x32_bf16 v[216:219], v[184:187], v[236:239], v[216:219]\n\t" \
    "v_cvt_pk_bf16_f32 v206, v206, v210\n\t" \
    "v_cvt_pk_bf16_f32 v207, v207, v211\n\t" \
    "s_waitcnt lgkmcnt(0)\n\t" \
    "v_mfma_f32_16x16x32_bf16 v[216:219], v[188:191], v[240:243], v[216:219]\n\t" \
    "ds_write_b128 v252, v[204:207] offset:0\n\t" \
    "v_mfma_f32_16x16x32_bf16 v[220:223], v[152:155], v[228:231], v[220:223]\n\t" \
    "v_mfma_f32_16x16x32_bf16 v[220:223], v[156:159], v[232:235], v[220:223]\n\t" \
    "v_add_u32 v252, %[qs], v253\n\t" \
    "v_cvt_pk_bf16_f32 v200, v200, v201\n\t" \
    "v_mfma_f32_16x16x32_bf16 v[220:223], v[192:195], v[236:239], v[220:223]\n\t" \
    "v_cvt_pk_bf16_f32 v201, v202, v203\n\t" \
    "ds_write_b64 v252, v[200:201] offset:0\n\t" \
    "v_mfma_f32_16x16x32_bf16 v[220:223], v[196:199], v[240:243], v[220:223]\n\t" \
    "v_mfma_f32_16x16x32_bf16 v[212:215], v[136:139], v[228:231], v[212:215]\n\t" \
    "v_mov_b64 v[208:209], 0\n\t" \
    "v_mfma_f32_16x16x32_bf16 v[212:215], v[140:143], v[232:235], v[212:215]\n\t" \
    "v_mov_b64 v[210:211], 0\n\t" \
    "v_mfma_f32_16x16x32_bf16 v[212:215], v[176:179], v[236:239], v[212:215]\n\t" \
    "v_add_u32 v252, %[q2], v253\n\t" \
    "v_cvt_pk_bf16_f32 v216, v216, v220\n\t" \
    "v_mfma_f32_16x16x32_bf16 v[212:215], v[180:183], v[240:243], v[212:215]\n\t" \
    "v_cvt_pk_bf16_f32 v217, v217, v221\n\t" \
    "v_cvt_pk_bf16_f32 v218, v218, v222\n\t" \
    "v_cvt_pk_bf16_f32 v219, v219, v223\n\t" \
    "ds_write_b128 v252, v[216:219] offset:6144\n\t" \
    "v_add_u32 v252, %[qs], v253\n\t" \
    "s_nop 2\n\t" \
    "v_cvt_pk_bf16_f32 v212, v212, v213\n\t" \
    "v_cvt_pk_bf16_f32 v213, v214, v215\n\t" \
    "ds_write_b64 v252, v[212:213] offset:6144\n\t" \
    LST6 \
    LJIT \
    "s_setprio 2\n\t" \
    "s_waitcnt vmcnt(0) lgkmcnt(0)\n\t" \
    "s_barrier \n\t" \
    LST3 \
    LJIT \
    "ds_read_b128 v[224:227], v253 offset:40960\n\t" \
    "ds_read_b128 v[228:231], v253 offset:41984\n\t" \
    "ds_read_b128 v[232:235], v253 offset:43008\n\t" \
    "ds_read_b128 v[236:239], v253 offset:44032\n\t" \
    "ds_read_b128 v[240:243], v253 offset:45056\n\t" \
    "ds_read_b128 v[200:203], v254 offset:0\n\t" \
    "ds_read_b128 v[204:207], v254 offset:1024\n\t" \
    "ds_read_b128 v[216:219], v254 offset:2048\n\t" \
    "ds_read_b128 v[220:223], v254 offset:3072\n\t" \
    LFLUSH212 \
    "v_bfe_i32 v212, v253, 5, 1\n\t" \
    "v_and_b32 v212, 0xaaaa, v212\n\t" \
    "v_or_b32 v212, 0x4444, v212\n\t" \
    "v_and_b32 v213, 0xffff, v255\n\t" \
    "v_lshrrev_b32 v214, 16, v255\n\t" \
    "s_waitcnt lgkmcnt(8)\n\t" \
    "v_smfmac_f32_16x16x64_bf16 v[244:247], v[224:227], v[0:7], v212\n\t" \
    "v_smfmac_f32_16x16x64_bf16 v[248:251], v[224:227], v[8:15], v212\n\t" \
    "s_waitcnt lgkmcnt(2)\n\t" \
    "v_smfmac_f32_16x16x64_bf16 v[208:211], v[224:227], v[200:207], v212\n\t" \
    "ds_read_b128 v[200:203], v254 offset:4096\n\t" \
    "ds_read_b128 v[204:207], v254 offset:5120\n\t" \
    "ds_read_b128 v[224:227], v253 offset:46080\n\t" \
    "v_smfmac_f32_16x16x64_bf16 v[208:211], v[228:231], v[16:23], v212\n\t" \
    "v_smfmac_f32_16x16x64_bf16 v[244:247], v[228:231], v[24:31], v212\n\t" \
    "v_smfmac_f32_16x16x64_bf16 v[248:251], v[228:231], v[32:39], v212\n\t" \
    "ds_read_b128 v[228:231], v253 offset:47104\n\t" \
    "s_waitcnt lgkmcnt(4)\n\t" \
    "v_smfmac_f32_16x16x64_bf16 v[208:211], v[232:235], v[216:223], v212\n\t" \
    "ds_read_b128 v[216:219], v254 offset:6144\n\t" \
    "ds_read_b128 v[220:223], v254 offset:7168\n\t" \
    "v_smfmac_f32_16x16x64_bf16 v[244:247], v[232:235], v[40:47], v212\n\t" \
    "v_smfmac_f32_16x16x64_bf16 v[248:251], v[232:235], v[48:55], v212\n\t" \
    "ds_read_b128 v[232:235], v253 offset:48128\n\t" \
    "v_smfmac_f32_16x16x64_bf16 v[208:211], v[236:239], v[56:63], v212\n\t" \
    "v_smfmac_f32_16x16x64_bf16 v[244:247], v[236:239], v[64:71], v212\n\t" \
    "v_smfmac_f32_16x16x64_bf16 v[248:251], v[236:239], v[72:79], v212\n\t" \
    "ds_read_b128 v[236:239], v253 offset:49152\n\t" \
    "s_waitcnt lgkmcnt(6)\n\t" \
    "v_smfmac_f32_16x16x64_bf16 v[208:211], v[240:243], v[200:207], v212\n\t" \
    "ds_read_b128 v[200:203], v254 offset:8192\n\t" \
    "ds_read_b128 v[204:207], v254 offset:9216\n\t" \
    "v_smfmac_f32_16x16x64_bf16 v[244:247], v[240:243], v[80:87], v212\n\t" \
    "v_smfmac_f32_16x16x64_bf16 v[248:251], v[240:243], v[88:95], v212\n\t" \
    "ds_read_b128 v[240:243], v253 offset:50176\n\t" \
    "s_waitcnt lgkmcnt(8)\n\t" \
    "v_smfmac_f32_16x16x64_bf16 v[208:211], v[224:227], v[96:103], v212\n\t" \
    "v_smfmac_f32_16x16x64_bf16 v[244:247], v[224:227], v[104:111], v212\n\t" \
    "v_smfmac_f32_16x16x64_bf16 v[248:251], v[224:227], v[112:119], v212\n\t" \
    "s_waitcnt lgkmcnt(5)\n\t" \
    "v_smfmac_f32_16x16x64_bf16 v[208:211], v[228:231], v[216:223], v212\n\t" \
    "s_waitcnt lgkmcnt(4)\n\t" \
    "v_smfmac_f32_16x16x64_bf16 v[208:211], v[232:235], v[136:143], v212\n\t" \
    "s_waitcnt lgkmcnt(1)\n\t" \
    "v_smfmac_f32_16x16x64_bf16 v[208:211], v[236:239], v[200:207], v212\n\t" \
    "s_waitcnt lgkmcnt(0)\n\t" \
    "v_smfmac_f32_16x16x64_bf16 v[208:211], v[240:243], v[176:183], v212\n\t" \
    "v_smfmac_f32_16x16x64_bf16 v[244:247], v[228:231], v[120:127], v212\n\t" \
    "v_smfmac_f32_16x16x64_bf16 v[244:247], v[232:235], v[144:151], v212\n\t" \
    "v_smfmac_f32_16x16x64_bf16 v[244:247], v[236:239], v[160:167], v212\n\t" \
    "v_add_f32 v208, v208, v210\n\t" \
    "v_add_f32 v209, v209, v211\n\t" \
    "v_cvt_pk_bf16_f32 v208, v208, v209\n\t" \
    "v_smfmac_f32_16x16x64_bf16 v[244:247], v[240:243], v[184:191], v212\n\t" \
    "ds_write_b16 v213, v208 offset:0\n\t" \
    "ds_write_b16_d16_hi v213, v208 offset:16\n\t" \
    "v_smfmac_f32_16x16x64_bf16 v[248:251], v[228:231], v[128:135], v212\n\t" \
    "v_smfmac_f32_16x16x64_bf16 v[248:251], v[232:235], v[152:159], v212\n\t" \
    "v_smfmac_f32_16x16x64_bf16 v[248:251], v[236:239], v[168:175], v212\n\t" \
    "v_add_f32 v244, v244, v246\n\t" \
    "v_add_f32 v245, v245, v247\n\t" \
    "v_smfmac_f32_16x16x64_bf16 v[248:251], v[240:243], v[192:199], v212\n\t" \
    "s_nop 7\n\t" \
    "v_add_f32 v248, v248, v250\n\t" \
    "v_add_f32 v249, v249, v251\n\t" \
    "v_cvt_pk_bf16_f32 v244, v244, v248\n\t" \
    "v_cvt_pk_bf16_f32 v245, v245, v249\n\t" \
    "ds_write2_b32 v214, v244, v245 offset1:4\n\t" \
    "s_setprio 0\n\t" \
    "s_waitcnt lgkmcnt(0)\n\t" \
    "s_barrier \n\t" \
    LST4 \
    LJIT \
    "ds_read_b128 v[224:227], v253 offset:0\n\t" \
    "ds_read_b128 v[244:247], v254 offset:0\n\t" \
    "ds_read_b128 v[228:231], v253 offset:1024\n\t" \
    "ds_read_b128 v[232:235], v253 offset:20480\n\t" \
    "ds_read_b128 v[236:239], v253 offset:21504\n\t" \
    "ds_read_b128 v[248:251], v254 offset:1024\n\t" \
    "s_add_u32 s40, s40, 1\n\t" \
    "s_cmp_lg_u32 s40, 64\n\t" \
    "s_cbranch_scc1 .Ls47_%=\n\t" \
    "s_branch .Lend%=\n\t" \
    ".Ls3_%=:\n\t" \
    "s_add_u32 m0, %[wm], 0x18000\n\t" \
    "s_mov_b32 s44, 0x0\n\t" \
    "buffer_load_dwordx4 v240, %[wr], s44 offen offset:0 nt lds\n\t" \
    "buffer_load_dwordx4 v241, %[wr], s44 offen offset:1024 nt lds\n\t" \
    "buffer_load_dwordx4 v242, %[wr], s44 offen offset:2048 nt lds\n\t" \
    "s_add_u32 m0, %[wm], 0x1e000\n\t" \
    "s_mov_b32 s44, 0x60000\n\t" \
    "buffer_load_dwordx4 v240, %[wr], s44 offen offset:0 nt lds\n\t" \
    "buffer_load_dwordx4 v241, %[wr], s44 offen offset:1024 nt lds\n\t" \
    "buffer_load_dwordx4 v242, %[wr], s44 offen offset:2048 nt lds\n\t" \
    "s_add_u32 m0, %[wm], 0x0\n\t" \
    "s_mov_b32 s44, 0xc0000\n\t" \
    "buffer_load_dwordx4 v240, %[wr], s44 offen offset:0 nt lds\n\t" \
    "buffer_load_dwordx4 v241, %[wr], s44 offen offset:1024 nt lds\n\t" \
    "buffer_load_dwordx4 v242, %[wr], s44 offen offset:2048 nt lds\n\t" \
    "s_add_u32 m0, %[wm], 0x6000\n\t" \
    "s_mov_b32 s44, 0x120000\n\t" \
    "buffer_load_dwordx4 v240, %[wr], s44 offen offset:0 nt lds\n\t" \
    "buffer_load_dwordx4 v241, %[wr], s44 offen offset:1024 nt lds\n\t" \
    "buffer_load_dwordx4 v242, %[wr], s44 offen offset:2048 nt lds\n\t" \
    "s_add_u32 m0, %[wm], 0xc000\n\t" \
    "s_mov_b32 s44, 0x180000\n\t" \
    "buffer_load_dwordx4 v240, %[wr], s44 offen offset:0 nt lds\n\t" \
    "buffer_load_dwordx4 v241, %[wr], s44 offen offset:1024 nt lds\n\t" \
    "buffer_load_dwordx4 v242, %[wr], s44 offen offset:2048 nt lds\n\t" \
    "v_add_u32 v244, 0x18000, v243\n\t" \
    "s_waitcnt vmcnt(12) lgkmcnt(0)\n\t" \
    "s_barrier \n\t" \
    "s_add_u32 m0, %[wm], 0x12000\n\t" \
    "s_mov_b32 s44, 0x1e0000\n\t" \
    "buffer_load_dwordx4 v240, %[wr], s44 offen offset:0 nt lds\n\t" \
    "buffer_load_dwordx4 v241, %[wr], s44 offen offset:1024 nt lds\n\t" \
    "buffer_load_dwordx4 v242, %[wr], s44 offen offset:2048 nt lds\n\t" \
    "ds_read_b64_tr_b16 v[208:209], v244 offset:0\n\t" \
    "ds_read_b64_tr_b16 v[210:211], v244 offset:1536\n\t" \
    "ds_read_b64_tr_b16 v[40:41], v244 offset:32\n\t" \
    "ds_read_b64_tr_b16 v[42:43], v244 offset:1568\n\t" \
    "ds_read_b64_tr_b16 v[48:49], v244 offset:64\n\t" \
    "ds_read_b64_tr_b16 v[50:51], v244 offset:1600\n\t" \
    "v_add_u32 v244, 0x1e000, v243\n\t" \
    "s_waitcnt vmcnt(12) lgkmcnt(0)\n\t" \
    "s_barrier \n\t" \
    "s_add_u32 m0, %[wm], 0x18000\n\t" \
    "s_mov_b32 s44, 0x240000\n\t" \
    "buffer_load_dwordx4 v240, %[wr], s44 offen offset:0 nt lds\n\t" \
    "buffer_load_dwordx4 v241, %[wr], s44 offen offset:1024 nt lds\n\t" \
    "buffer_load_dwordx4 v242, %[wr], s44 offen offset:2048 nt lds\n\t" \
    "ds_read_b64_tr_b16 v[212:213], v244 offset:0\n\t" \
    "ds_read_b64_tr_b16 v[214:215], v244 offset:1536\n\t" \
    "ds_read_b64_tr_b16 v[44:45], v244 offset:32\n\t" \
    "ds_read_b64_tr_b16 v[46:47], v244 offset:1568\n\t" \
    "ds_read_b64_tr_b16 v[52:53], v244 offset:64\n\t" \
    "ds_read_b64_tr_b16 v[54:55], v244 offset:1600\n\t" \
    "v_add_u32 v244, 0x0, v243\n\t" \
    "s_waitcnt vmcnt(12) lgkmcnt(0)\n\t" \
    "s_barrier \n\t" \
    "s_add_u32 m0, %[wm], 0x1e000\n\t" \
    "s_mov_b32 s44, 0x2a0000\n\t" \
    "buffer_load_dwordx4 v240, %[wr], s44 offen offset:0 nt lds\n\t" \
    "buffer_load_dwordx4 v241, %[wr], s44 offen offset:1024 nt lds\n\t" \
    "buffer_load_dwordx4 v242, %[wr], s44 offen offset:2048 nt lds\n\t" \
    "ds_read_b64_tr_b16 v[56:57], v244 offset:0\n\t" \
    "ds_read_b64_tr_b16 v[58:59], v244 offset:1536\n\t" \
    "ds_read_b64_tr_b16 v[64:65], v244 offset:32\n\t" \
    "ds_read_b64_tr_b16 v[66:67], v244 offset:1568\n\t" \
    "ds_read_b64_tr_b16 v[72:73], v244 offset:64\n\t" \
    "ds_read_b64_tr_b16 v[74:75], v244 offset:1600\n\t" \
    "v_add_u32 v244, 0x6000, v243\n\t" \
    "s_waitcnt vmcnt(12) lgkmcnt(0)\n\t" \
    "s_barrier \n\t" \
    "s_add_u32 m0, %[wm], 0x0\n\t" \
    "s_mov_b32 s44, 0x300000\n\t" \
    "buffer_load_dwordx4 v240, %[wr], s44 offen offset:0 nt lds\n\t" \
    "buffer_load_dwordx4 v241, %[wr], s44 offen offset:1024 nt lds\n\t" \
    "buffer_load_dwordx4 v242, %[wr], s44 offen offset:2048 nt lds\n\t" \
    "ds_read_b64_tr_b16 v[60:61], v244 offset:0\n\t" \
    "ds_read_b64_tr_b16 v[62:63], v244 offset:1536\n\t" \
    "ds_read_b64_tr_b16 v[68:69], v244 offset:32\n\t" \
    "ds_read_b64_tr_b16 v[70:71], v244 offset:1568\n\t" \
    "ds_read_b64_tr_b16 v[76:77], v244 offset:64\n\t" \
    "ds_read_b64_tr_b16 v[78:79], v244 offset:1600\n\t" \
    "v_add_u32 v244, 0xc000, v243\n\t" \
    "s_waitcnt vmcnt(12) lgkmcnt(0)\n\t" \
    "s_barrier \n\t" \
    "s_add_u32 m0, %[wm], 0x6000\n\t" \
    "s_mov_b32 s44, 0x360000\n\t" \
    "buffer_load_dwordx4 v240, %[wr], s44 offen offset:0 nt lds\n\t" \
    "buffer_load_dwordx4 v241, %[wr], s44 offen offset:1024 nt lds\n\t" \
    "buffer_load_dwordx4 v242, %[wr], s44 offen offset:2048 nt lds\n\t" \
    "ds_read_b64_tr_b16 v[216:217], v244 offset:0\n\t" \
    "ds_read_b64_tr_b16 v[218:219], v244 offset:1536\n\t" \
    "ds_read_b64_tr_b16 v[80:81], v244 offset:32\n\t" \
    "ds_read_b64_tr_b16 v[82:83], v244 offset:1568\n\t" \
    "ds_read_b64_tr_b16 v[88:89], v244 offset:64\n\t" \
    "ds_read_b64_tr_b16 v[90:91], v244 offset:1600\n\t" \
    "v_add_u32 v244, 0x12000, v243\n\t" \
    "s_waitcnt vmcnt(12) lgkmcnt(0)\n\t" \
    "s_barrier \n\t" \
    "s_add_u32 m0, %[wm], 0xc000\n\t" \
    "s_mov_b32 s44, 0x3c0000\n\t" \
    "buffer_load_dwordx4 v240, %[wr], s44 offen offset:0 nt lds\n\t" \
    "buffer_load_dwordx4 v241, %[wr], s44 offen offset:1024 nt lds\n\t" \
    "buffer_load_dwordx4 v242, %[wr], s44 offen offset:2048 nt lds\n\t" \
    "ds_read_b64_tr_b16 v[220:221], v244 offset:0\n\t" \
    "ds_read_b64_tr_b16 v[222:223], v244 offset:1536\n\t" \
    "ds_read_b64_tr_b16 v[84:85], v244 offset:32\n\t" \
    "ds_read_b64_tr_b16 v[86:87], v244 offset:1568\n\t" \
    "ds_read_b64_tr_b16 v[92:93], v244 offset:64\n\t" \
    "ds_read_b64_tr_b16 v[94:95], v244 offset:1600\n\t" \
    "v_add_u32 v244, 0x18000, v243\n\t" \
    "s_waitcnt vmcnt(12) lgkmcnt(0)\n\t" \
    "s_barrier \n\t" \
    "v_mov_b32 v246, 0\n\t" \
    "buffer_load_dword v246, v246, s[60:63], %[mo] offen sc1\n\t" \
    "s_add_u32 m0, %[wm], 0x12000\n\t" \
    "s_mov_b32 s44, 0x420000\n\t" \
    "buffer_load_dwordx4 v240, %[wr], s44 offen offset:0 nt lds\n\t" \
    "buffer_load_dwordx4 v241, %[wr], s44 offen offset:1024 nt lds\n\t" \
    "buffer_load_dwordx4 v242, %[wr], s44 offen offset:2048 nt lds\n\t" \
    "ds_read_b64_tr_b16 v[96:97], v244 offset:0\n\t" \
    "ds_read_b64_tr_b16 v[98:99], v244 offset:1536\n\t" \
    "ds_read_b64_tr_b16 v[104:105], v244 offset:32\n\t" \
    "ds_read_b64_tr_b16 v[106:107], v244 offset:1568\n\t" \
    "ds_read_b64_tr_b16 v[112:113], v244 offset:64\n\t" \
    "ds_read_b64_tr_b16 v[114:115], v244 offset:1600\n\t" \
    "v_add_u32 v244, 0x1e000, v243\n\t" \
    "s_waitcnt vmcnt(12) lgkmcnt(0)\n\t" \
    "s_barrier \n\t" \
    "s_add_u32 m0, %[wm], 0x18000\n\t" \
    "s_mov_b32 s44, 0x480000\n\t" \
    "buffer_load_dwordx4 v240, %[wr], s44 offen offset:0 nt lds\n\t" \
    "buffer_load_dwordx4 v241, %[wr], s44 offen offset:1024 nt lds\n\t" \
    "buffer_load_dwordx4 v242, %[wr], s44 offen offset:2048 nt lds\n\t" \
    "ds_read_b64_tr_b16 v[100:101], v244 offset:0\n\t" \
    "ds_read_b64_tr_b16 v[102:103], v244 offset:1536\n\t" \
    "ds_read_b64_tr_b16 v[108:109], v244 offset:32\n\t" \
    "ds_read_b64_tr_b16 v[110:111], v244 offset:1568\n\t" \
    "ds_read_b64_tr_b16 v[116:117], v244 offset:64\n\t" \
    "ds_read_b64_tr_b16 v[118:119], v244 offset:1600\n\t" \
    "v_add_u32 v244, 0x0, v243\n\t" \
    "s_waitcnt vmcnt(12) lgkmcnt(0)\n\t" \
    "s_barrier \n\t" \
    "s_add_u32 m0, %[wm], 0x1e000\n\t" \
    "s_mov_b32 s44, 0x4e0000\n\t" \
    "buffer_load_dwordx4 v240, %[wr], s44 offen offset:0 nt lds\n\t" \
    "buffer_load_dwordx4 v241, %[wr], s44 offen offset:1024 nt lds\n\t" \
    "buffer_load_dwordx4 v242, %[wr], s44 offen offset:2048 nt lds\n\t" \
    "ds_read_b64_tr_b16 v[224:225], v244 offset:0\n\t" \
    "ds_read_b64_tr_b16 v[226:227], v244 offset:1536\n\t" \
    "ds_read_b64_tr_b16 v[120:121], v244 offset:32\n\t" \
    "ds_read_b64_tr_b16 v[122:123], v244 offset:1568\n\t" \
    "ds_read_b64_tr_b16 v[128:129], v244 offset:64\n\t" \
    "ds_read_b64_tr_b16 v[130:131], v244 offset:1600\n\t" \
    "v_add_u32 v244, 0x6000, v243\n\t" \
    "s_waitcnt vmcnt(12) lgkmcnt(0)\n\t" \
    "s_barrier \n\t" \
    "s_add_u32 m0, %[wm], 0x0\n\t" \
    "s_mov_b32 s44, 0x540000\n\t" \
    "buffer_load_dwordx4 v240, %[wr], s44 offen offset:0 nt lds\n\t" \
    "buffer_load_dwordx4 v241, %[wr], s44 offen offset:1024 nt lds\n\t" \
    "buffer_load_dwordx4 v242, %[wr], s44 offen offset:2048 nt lds\n\t" \
    "ds_read_b64_tr_b16 v[228:229], v244 offset:0\n\t" \
    "ds_read_b64_tr_b16 v[230:231], v244 offset:1536\n\t" \
    "ds_read_b64_tr_b16 v[124:125], v244 offset:32\n\t" \
    "ds_read_b64_tr_b16 v[126:127], v244 offset:1568\n\t" \
    "ds_read_b64_tr_b16 v[132:133], v244 offset:64\n\t" \
    "ds_read_b64_tr_b16 v[134:135], v244 offset:1600\n\t" \
    "v_add_u32 v244, 0xc000, v243\n\t" \
    "s_waitcnt vmcnt(12) lgkmcnt(0)\n\t" \
    "s_barrier \n\t" \
    "s_add_u32 m0, %[wm], 0x6000\n\t" \
    "s_mov_b32 s44, 0x5a0000\n\t" \
    "buffer_load_dwordx4 v240, %[wr], s44 offen offset:0 nt lds\n\t" \
    "buffer_load_dwordx4 v241, %[wr], s44 offen offset:1024 nt lds\n\t" \
    "buffer_load_dwordx4 v242, %[wr], s44 offen offset:2048 nt lds\n\t" \
    "ds_read_b64_tr_b16 v[136:137], v244 offset:0\n\t" \
    "ds_read_b64_tr_b16 v[138:139], v244 offset:1536\n\t" \
    "ds_read_b64_tr_b16 v[144:145], v244 offset:32\n\t" \
    "ds_read_b64_tr_b16 v[146:147], v244 offset:1568\n\t" \
    "ds_read_b64_tr_b16 v[152:153], v244 offset:64\n\t" \
    "ds_read_b64_tr_b16 v[154:155], v244 offset:1600\n\t" \
    "v_add_u32 v244, 0x12000, v243\n\t" \
    "s_waitcnt vmcnt(12) lgkmcnt(0)\n\t" \
    "s_barrier \n\t" \
    ".Ls31_%=:\n\t" \
    "v_readfirstlane_b32 s43, v246\n\t" \
    "s_nop 0\n\t" \
    "s_cmp_lg_u32 s43, -1\n\t" \
    "s_cbranch_scc1 .Ls32_%=\n\t" \
    "s_cmp_eq_u32 s41, 0\n\t" \
    "s_cbranch_scc1 .Ls32_%=\n\t" \
    "s_sub_u32 s41, s41, 1\n\t" \
    "s_sleep 4\n\t" \
    "v_mov_b32 v246, 0\n\t" \
    "buffer_load_dword v246, v246, s[60:63], %[mo] offen sc1\n\t" \
    "s_waitcnt vmcnt(0)\n\t" \
    "s_branch .Ls31_%=\n\t" \
    ".Ls32_%=:\n\t" \
    "v_mov_b32 v246, 0x8000\n\t" \
    "s_mov_b64 exec, 1\n\t" \
    "buffer_store_dword v246, v246, s[60:63], %[mo] offen sc1\n\t" \
    "s_mov_b64 exec, -1\n\t" \
    "s_and_b32 s43, s43, 0x2000\n\t" \
    "s_xor_b32 s43, s43, %[lx]\n\t" \
    "s_add_u32 s60, s60, s43\n\t" \
    "s_addc_u32 s61, s61, 0\n\t" \
    "s_add_u32 m0, %[wm], 0xc000\n\t" \
    "s_mov_b32 s44, 0x600000\n\t" \
    "buffer_load_dwordx4 v240, %[wr], s44 offen offset:0 nt lds\n\t" \
    "buffer_load_dwordx4 v241, %[wr], s44 offen offset:1024 nt lds\n\t" \
    "buffer_load_dwordx4 v242, %[wr], s44 offen offset:2048 nt lds\n\t" \
    "ds_read_b64_tr_b16 v[140:141], v244 offset:0\n\t" \
    "ds_read_b64_tr_b16 v[142:143], v244 offset:1536\n\t" \
    "ds_read_b64_tr_b16 v[148:149], v244 offset:32\n\t" \
    "ds_read_b64_tr_b16 v[150:151], v244 offset:1568\n\t" \
    "ds_read_b64_tr_b16 v[156:157], v244 offset:64\n\t" \
    "ds_read_b64_tr_b16 v[158:159], v244 offset:1600\n\t" \
    "v_add_u32 v244, 0x18000, v243\n\t" \
    "s_waitcnt vmcnt(12) lgkmcnt(0)\n\t" \
    "s_barrier \n\t" \
    "s_add_u32 m0, %[wm], 0x12000\n\t" \
    "s_mov_b32 s44, 0x660000\n\t" \
    "buffer_load_dwordx4 v240, %[wr], s44 offen offset:0 nt lds\n\t" \
    "buffer_load_dwordx4 v241, %[wr], s44 offen offset:1024 nt lds\n\t" \
    "buffer_load_dwordx4 v242, %[wr], s44 offen offset:2048 nt lds\n\t" \
    "ds_read_b64_tr_b16 v[232:233], v244 offset:0\n\t" \
    "ds_read_b64_tr_b16 v[234:235], v244 offset:1536\n\t" \
    "ds_read_b64_tr_b16 v[160:161], v244 offset:32\n\t" \
    "ds_read_b64_tr_b16 v[162:163], v244 offset:1568\n\t" \
    "ds_read_b64_tr_b16 v[168:169], v244 offset:64\n\t" \
    "ds_read_b64_tr_b16 v[170:171], v244 offset:1600\n\t" \
    "v_add_u32 v244, 0x1e000, v243\n\t" \
    "s_waitcnt vmcnt(12) lgkmcnt(0)\n\t" \
    "s_barrier \n\t" \
    "s_add_u32 m0, %[wm], 0x18000\n\t" \
    "s_mov_b32 s44, 0x6c0000\n\t" \
    "buffer_load_dwordx4 v240, %[wr], s44 offen offset:0 nt lds\n\t" \
    "buffer_load_dwordx4 v241, %[wr], s44 offen offset:1024 nt lds\n\t" \
    "buffer_load_dwordx4 v242, %[wr], s44 offen offset:2048 nt lds\n\t" \
    "ds_read_b64_tr_b16 v[236:237], v244 offset:0\n\t" \
    "ds_read_b64_tr_b16 v[238:239], v244 offset:1536\n\t" \
    "ds_read_b64_tr_b16 v[164:165], v244 offset:32\n\t" \
    "ds_read_b64_tr_b16 v[166:167], v244 offset:1568\n\t" \
    "ds_read_b64_tr_b16 v[172:173], v244 offset:64\n\t" \
    "ds_read_b64_tr_b16 v[174:175], v244 offset:1600\n\t" \
    "v_add_u32 v244, 0x0, v243\n\t" \
    "s_waitcnt vmcnt(12) lgkmcnt(0)\n\t" \
    "s_barrier \n\t" \
    "s_add_u32 m0, %[wm], 0x1e000\n\t" \
    "s_mov_b32 s44, 0x720000\n\t" \
    "buffer_load_dwordx4 v240, %[wr], s44 offen offset:0 nt lds\n\t" \
    "buffer_load_dwordx4 v241, %[wr], s44 offen offset:1024 nt lds\n\t" \
    "buffer_load_dwordx4 v242, %[wr], s44 offen offset:2048 nt lds\n\t" \
    "ds_read_b64_tr_b16 v[176:177], v244 offset:0\n\t" \
    "ds_read_b64_tr_b16 v[178:179], v244 offset:1536\n\t" \
    "ds_read_b64_tr_b16 v[184:185], v244 offset:32\n\t" \
    "ds_read_b64_tr_b16 v[186:187], v244 offset:1568\n\t" \
    "ds_read_b64_tr_b16 v[192:193], v244 offset:64\n\t" \
    "ds_read_b64_tr_b16 v[194:195], v244 offset:1600\n\t" \
    "v_add_u32 v244, 0x6000, v243\n\t" \
    "s_waitcnt vmcnt(12) lgkmcnt(0)\n\t" \
    "s_barrier \n\t" \
    "ds_read_b64_tr_b16 v[180:181], v244 offset:0\n\t" \
    "ds_read_b64_tr_b16 v[182:183], v244 offset:1536\n\t" \
    "ds_read_b64_tr_b16 v[188:189], v244 offset:32\n\t" \
    "ds_read_b64_tr_b16 v[190:191], v244 offset:1568\n\t" \
    "ds_read_b64_tr_b16 v[196:197], v244 offset:64\n\t" \
    "ds_read_b64_tr_b16 v[198:199], v244 offset:1600\n\t" \
    "v_add_u32 v244, 0xc000, v243\n\t" \
    "s_waitcnt vmcnt(9) lgkmcnt(0)\n\t" \
    "s_barrier \n\t" \
    "ds_read_b64_tr_b16 v[200:201], v244 offset:0\n\t" \
    "ds_read_b64_tr_b16 v[202:203], v244 offset:1536\n\t" \
    "ds_read_b64_tr_b16 v[0:1], v244 offset:32\n\t" \
    "ds_read_b64_tr_b16 v[2:3], v244 offset:1568\n\t" \
    "ds_read_b64_tr_b16 v[8:9], v244 offset:64\n\t" \
    "ds_read_b64_tr_b16 v[10:11], v244 offset:1600\n\t" \
    "v_add_u32 v244, 0x12000, v243\n\t" \
    "s_waitcnt vmcnt(6) lgkmcnt(0)\n\t" \
    "s_barrier \n\t" \
    "ds_read_b64_tr_b16 v[204:205], v244 offset:0\n\t" \
    "ds_read_b64_tr_b16 v[206:207], v244 offset:1536\n\t" \
    "ds_read_b64_tr_b16 v[4:5], v244 offset:32\n\t" \
    "ds_read_b64_tr_b16 v[6:7], v244 offset:1568\n\t" \
    "ds_read_b64_tr_b16 v[12:13], v244 offset:64\n\t" \
    "ds_read_b64_tr_b16 v[14:15], v244 offset:1600\n\t" \
    "v_add_u32 v244, 0x18000, v243\n\t" \
    "s_waitcnt vmcnt(3) lgkmcnt(0)\n\t" \
    "s_barrier \n\t" \
    "ds_read_b64_tr_b16 v[16:17], v244 offset:0\n\t" \
    "ds_read_b64_tr_b16 v[18:19], v244 offset:1536\n\t" \
    "ds_read_b64_tr_b16 v[24:25], v244 offset:32\n\t" \
    "ds_read_b64_tr_b16 v[26:27], v244 offset:1568\n\t" \
    "ds_read_b64_tr_b16 v[32:33], v244 offset:64\n\t" \
    "ds_read_b64_tr_b16 v[34:35], v244 offset:1600\n\t" \
    "v_add_u32 v244, 0x1e000, v243\n\t" \
    "s_waitcnt vmcnt(0) lgkmcnt(0)\n\t" \
    "s_barrier \n\t" \
    "ds_read_b64_tr_b16 v[20:21], v244 offset:0\n\t" \
    "ds_read_b64_tr_b16 v[22:23], v244 offset:1536\n\t" \
    "ds_read_b64_tr_b16 v[28:29], v244 offset:32\n\t" \
    "ds_read_b64_tr_b16 v[30:31], v244 offset:1568\n\t" \
    "ds_read_b64_tr_b16 v[36:37], v244 offset:64\n\t" \
    "ds_read_b64_tr_b16 v[38:39], v244 offset:1600\n\t" \
    "s_waitcnt lgkmcnt(0)\n\t" \
    "s_barrier \n\t" \
    LST0 \
    LJIT \
    "ds_write_b128 v254, v[200:203] offset:55296\n\t" \
    "ds_write_b128 v254, v[204:207] offset:56320\n\t" \
    "ds_write_b128 v254, v[208:211] offset:57344\n\t" \
    "ds_write_b128 v254, v[212:215] offset:58368\n\t" \
    "ds_write_b128 v254, v[216:219] offset:59392\n\t" \
    "ds_write_b128 v254, v[220:223] offset:60416\n\t" \
    "ds_write_b128 v254, v[224:227] offset:61440\n\t" \
    "ds_write_b128 v254, v[228:231] offset:62464\n\t" \
    "ds_write_b128 v254, v[232:235] offset:63488\n\t" \
    "ds_write_b128 v254, v[236:239] offset:64512\n\t" \
    "s_waitcnt vmcnt(0) lgkmcnt(0)\n\t" \
    "s_barrier \n\t" \
    "ds_read_b128 v[208:211], v254 offset:24576\n\t" \
    "ds_read_b128 v[224:227], v253 offset:0\n\t" \
    "ds_read_b128 v[228:231], v253 offset:20480\n\t" \
    "ds_read_b128 v[212:215], v254 offset:25600\n\t" \
    "ds_read_b128 v[232:235], v253 offset:1024\n\t" \
    "ds_read_b128 v[236:239], v253 offset:21504\n\t" \
    "ds_read_b128 v[216:219], v254 offset:26624\n\t" \
    "ds_read_b128 v[240:243], v253 offset:2048\n\t" \
    "ds_read_b128 v[244:247], v253 offset:22528\n\t" \
    "ds_read_b128 v[220:223], v254 offset:27648\n\t" \
    "ds_read_b128 v[248:251], v253 offset:3072\n\t" \
    "s_waitcnt lgkmcnt(9)\n\t" \
    "v_mfma_f32_16x16x32_bf16 v[200:203], v[208:211], v[224:227], 0\n\t" \
    "ds_read_b128 v[224:227], v253 offset:23552\n\t" \
    "s_waitcnt lgkmcnt(9)\n\t" \
    "v_mfma_f32_16x16x32_bf16 v[204:207], v[208:211], v[228:231], 0\n\t" \
    "ds_read_b128 v[228:231], v253 offset:4096\n\t" \
    "ds_read_b128 v[208:211], v254 offset:28672\n\t" \
    "s_waitcnt lgkmcnt(9)\n\t" \
    "v_mfma_f32_16x16x32_bf16 v[200:203], v[212:215], v[232:235], v[200:203]\n\t" \
    "ds_read_b128 v[232:235], v253 offset:24576\n\t" \
    "s_waitcnt lgkmcnt(9)\n\t" \
    "v_mfma_f32_16x16x32_bf16 v[204:207], v[212:215], v[236:239], v[204:207]\n\t" \
    "ds_read_b128 v[236:239], v253 offset:5120\n\t" \
    "ds_read_b128 v[212:215], v254 offset:29696\n\t" \
    "s_waitcnt lgkmcnt(9)\n\t" \
    "v_mfma_f32_16x16x32_bf16 v[200:203], v[216:219], v[240:243], v[200:203]\n\t" \
    "ds_read_b128 v[240:243], v253 offset:25600\n\t" \
    ".Ls33_%=:\n\t" \
    "s_waitcnt lgkmcnt(9)\n\t" \
    "v_mfma_f32_16x16x32_bf16 v[204:207], v[216:219], v[244:247], v[204:207]\n\t" \
    "ds_read_b128 v[244:247], v253 offset:6144\n\t" \
    "ds_read_b128 v[216:219], v254 offset:30720\n\t" \
    "s_waitcnt lgkmcnt(9)\n\t" \
    "v_mfma_f32_16x16x32_bf16 v[200:203], v[220:223], v[248:251], v[200:203]\n\t" \
    "ds_read_b128 v[248:251], v253 offset:26624\n\t" \
    "s_waitcnt lgkmcnt(9)\n\t" \
    "v_mfma_f32_16x16x32_bf16 v[204:207], v[220:223], v[224:227], v[204:207]\n\t" \
    "ds_read_b128 v[224:227], v253 offset:7168\n\t" \
    "ds_read_b128 v[220:223], v254 offset:31744\n\t" \
    "s_waitcnt lgkmcnt(9)\n\t" \
    "v_mfma_f32_16x16x32_bf16 v[200:203], v[208:211], v[228:231], v[200:203]\n\t" \
    "ds_read_b128 v[228:231], v253 offset:27648\n\t" \
    "s_waitcnt lgkmcnt(9)\n\t" \
    "v_mfma_f32_16x16x32_bf16 v[204:207], v[208:211], v[232:235], v[204:207]\n\t" \
    "ds_read_b128 v[232:235], v253 offset:8192\n\t" \
    "ds_read_b128 v[208:211], v254 offset:32768\n\t" \
    "s_waitcnt lgkmcnt(9)\n\t" \
    "v_mfma_f32_16x16x32_bf16 v[200:203], v[212:215], v[236:239], v[200:203]\n\t" \
    "ds_read_b128 v[236:239], v253 offset:28672\n\t" \
    "s_waitcnt lgkmcnt(9)\n\t" \
    "v_mfma_f32_16x16x32_bf16 v[204:207], v[212:215], v[240:243], v[204:207]\n\t" \
    "ds_read_b128 v[240:243], v253 offset:9216\n\t" \
    "ds_read_b128 v[212:215], v254 offset:33792\n\t" \
    "s_waitcnt lgkmcnt(9)\n\t" \
    "v_mfma_f32_16x16x32_bf16 v[200:203], v[216:219], v[244:247], v[200:203]\n\t" \
    "ds_read_b128 v[244:247], v253 offset:29696\n\t" \
    "s_waitcnt lgkmcnt(9)\n\t" \
    "v_mfma_f32_16x16x32_bf16 v[204:207], v[216:219], v[248:251], v[204:207]\n\t" \
    "s_waitcnt lgkmcnt(7)\n\t" \
    "v_mfma_f32_16x16x32_bf16 v[200:203], v[220:223], v[224:227], v[200:203]\n\t" \
    "s_waitcnt lgkmcnt(6)\n\t" \
    "v_mfma_f32_16x16x32_bf16 v[204:207], v[220:223], v[228:231], v[204:207]\n\t" \
    "s_waitcnt lgkmcnt(4)\n\t" \
    "v_mfma_f32_16x16x32_bf16 v[200:203], v[208:211], v[232:235], v[200:203]\n\t" \
    "s_waitcnt lgkmcnt(3)\n\t" \
    "v_mfma_f32_16x16x32_bf16 v[204:207], v[208:211], v[236:239], v[204:207]\n\t" \
    "s_waitcnt lgkmcnt(1)\n\t" \
    "v_mfma_f32_16x16x32_bf16 v[200:203], v[212:215], v[240:243], v[200:203]\n\t" \
    "s_waitcnt lgkmcnt(0)\n\t" \
    "v_mfma_f32_16x16x32_bf16 v[204:207], v[212:215], v[244:247], v[204:207]\n\t" \
    "ds_read_b128 v[224:227], v253 offset:0\n\t" \
    "ds_read_b128 v[244:247], v254 offset:57344\n\t" \
    "ds_read_b128 v[228:231], v253 offset:1024\n\t" \
    "ds_read_b128 v[232:235], v253 offset:20480\n\t" \
    "ds_read_b128 v[236:239], v253 offset:21504\n\t" \
    "ds_read_b128 v[248:251], v254 offset:58368\n\t" \
    "s_nop 1\n\t" \
    "ds_write_b128 v253, v[200:203] offset:40960\n\t" \
    "ds_write_b128 v253, v[204:207] offset:41984\n\t" \
    "s_nop 1\n\t" \
    "s_waitcnt lgkmcnt(7)\n\t" \
    "v_mfma_f32_16x16x32_bf16 v[204:207], v[40:43], v[224:227], 0\n\t" \
    "s_waitcnt lgkmcnt(5)\n\t" \
    "v_mfma_f32_16x16x32_bf16 v[204:207], v[44:47], v[228:231], v[204:207]\n\t" \
    "ds_read_b128 v[240:243], v253 offset:2048\n\t" \
    "v_mfma_f32_16x16x32_bf16 v[208:211], v[48:51], v[224:227], 0\n\t" \
    "v_mfma_f32_16x16x32_bf16 v[208:211], v[52:55], v[228:231], v[208:211]\n\t" \
    "v_mfma_f32_16x16x32_bf16 v[200:203], v[244:247], v[224:227], 0\n\t" \
    "s_waitcnt lgkmcnt(3)\n\t" \
    "v_mfma_f32_16x16x32_bf16 v[200:203], v[248:251], v[228:231], v[200:203]\n\t" \
    "v_mfma_f32_16x16x32_bf16 v[216:219], v[40:43], v[232:235], 0\n\t" \
    "ds_read_b128 v[224:227], v253 offset:3072\n\t" \
    "v_mfma_f32_16x16x32_bf16 v[216:219], v[44:47], v[236:239], v[216:219]\n\t" \
    "ds_read_b128 v[228:231], v253 offset:22528\n\t" \
    "v_mfma_f32_16x16x32_bf16 v[220:223], v[48:51], v[232:235], 0\n\t" \
    "v_mfma_f32_16x16x32_bf16 v[220:223], v[52:55], v[236:239], v[220:223]\n\t" \
    "v_mfma_f32_16x16x32_bf16 v[212:215], v[244:247], v[232:235], 0\n\t" \
    "ds_read_b128 v[244:247], v254 offset:59392\n\t" \
    "v_mfma_f32_16x16x32_bf16 v[212:215], v[248:251], v[236:239], v[212:215]\n\t" \
    "ds_read_b128 v[248:251], v254 offset:60416\n\t" \
    "s_waitcnt lgkmcnt(4)\n\t" \
    "v_mfma_f32_16x16x32_bf16 v[204:207], v[64:67], v[240:243], v[204:207]\n\t" \
    "ds_read_b128 v[232:235], v253 offset:23552\n\t" \
    "s_waitcnt lgkmcnt(4)\n\t" \
    "v_mfma_f32_16x16x32_bf16 v[204:207], v[68:71], v[224:227], v[204:207]\n\t" \
    "ds_read_b128 v[236:239], v253 offset:4096\n\t" \
    "v_mfma_f32_16x16x32_bf16 v[208:211], v[72:75], v[240:243], v[208:211]\n\t" \
    "v_mfma_f32_16x16x32_bf16 v[208:211], v[76:79], v[224:227], v[208:211]\n\t" \
    "v_mfma_f32_16x16x32_bf16 v[200:203], v[56:59], v[240:243], v[200:203]\n\t" \
    "v_mfma_f32_16x16x32_bf16 v[200:203], v[60:63], v[224:227], v[200:203]\n\t" \
    "s_waitcnt lgkmcnt(4)\n\t" \
    "v_mfma_f32_16x16x32_bf16 v[216:219], v[64:67], v[228:231], v[216:219]\n\t" \
    "ds_read_b128 v[240:243], v253 offset:5120\n\t" \
    "s_waitcnt lgkmcnt(2)\n\t" \
    "v_mfma_f32_16x16x32_bf16 v[216:219], v[68:71], v[232:235], v[216:219]\n\t" \
    "ds_read_b128 v[224:227], v253 offset:24576\n\t" \
    "v_mfma_f32_16x16x32_bf16 v[220:223], v[72:75], v[228:231], v[220:223]\n\t" \
    "v_mfma_f32_16x16x32_bf16 v[220:223], v[76:79], v[232:235], v[220:223]\n\t" \
    "v_mfma_f32_16x16x32_bf16 v[212:215], v[56:59], v[228:231], v[212:215]\n\t" \
    "v_mfma_f32_16x16x32_bf16 v[212:215], v[60:63], v[232:235], v[212:215]\n\t" \
    "s_waitcnt lgkmcnt(2)\n\t" \
    "v_mfma_f32_16x16x32_bf16 v[204:207], v[80:83], v[236:239], v[204:207]\n\t" \
    "ds_read_b128 v[228:231], v253 offset:25600\n\t" \
    "s_waitcnt lgkmcnt(2)\n\t" \
    "v_mfma_f32_16x16x32_bf16 v[204:207], v[84:87], v[240:243], v[204:207]\n\t" \
    "ds_read_b128 v[232:235], v253 offset:6144\n\t" \
    "v_mfma_f32_16x16x32_bf16 v[208:211], v[88:91], v[236:239], v[208:211]\n\t" \
    "v_mfma_f32_16x16x32_bf16 v[208:211], v[92:95], v[240:243], v[208:211]\n\t" \
    "v_mfma_f32_16x16x32_bf16 v[200:203], v[244:247], v[236:239], v[200:203]\n\t" \
    "v_mfma_f32_16x16x32_bf16 v[200:203], v[248:251], v[240:243], v[200:203]\n\t" \
    "s_waitcnt lgkmcnt(2)\n\t" \
    "v_mfma_f32_16x16x32_bf16 v[216:219], v[80:83], v[224:227], v[216:219]\n\t" \
    "ds_read_b128 v[236:239], v253 offset:7168\n\t" \
    "s_waitcnt lgkmcnt(2)\n\t" \
    "v_mfma_f32_16x16x32_bf16 v[216:219], v[84:87], v[228:231], v[216:219]\n\t" \
    "ds_read_b128 v[240:243], v253 offset:26624\n\t" \
    "v_mfma_f32_16x16x32_bf16 v[220:223], v[88:91], v[224:227], v[220:223]\n\t" \
    "v_mfma_f32_16x16x32_bf16 v[220:223], v[92:95], v[228:231], v[220:223]\n\t" \
    "v_mfma_f32_16x16x32_bf16 v[212:215], v[244:247], v[224:227], v[212:215]\n\t" \
    "ds_read_b128 v[244:247], v254 offset:61440\n\t" \
    "v_mfma_f32_16x16x32_bf16 v[212:215], v[248:251], v[228:231], v[212:215]\n\t" \
    "ds_read_b128 v[248:251], v254 offset:62464\n\t" \
    "s_waitcnt lgkmcnt(4)\n\t" \
    "v_mfma_f32_16x16x32_bf16 v[204:207], v[104:107], v[232:235], v[204:207]\n\t" \
    "ds_read_b128 v[224:227], v253 offset:27648\n\t" \
    "s_waitcnt lgkmcnt(4)\n\t" \
    "v_mfma_f32_16x16x32_bf16 v[204:207], v[108:111], v[236:239], v[204:207]\n\t" \
    "ds_read_b128 v[228:231], v253 offset:8192\n\t" \
    "v_mfma_f32_16x16x32_bf16 v[208:211], v[112:115], v[232:235], v[208:211]\n\t" \
    "v_mfma_f32_16x16x32_bf16 v[208:211], v[116:119], v[236:239], v[208:211]\n\t" \
    "v_mfma_f32_16x16x32_bf16 v[200:203], v[96:99], v[232:235], v[200:203]\n\t" \
    "v_mfma_f32_16x16x32_bf16 v[200:203], v[100:103], v[236:239], v[200:203]\n\t" \
    "s_waitcnt lgkmcnt(4)\n\t" \
    "v_mfma_f32_16x16x32_bf16 v[216:219], v[104:107], v[240:243], v[216:219]\n\t" \
    "ds_read_b128 v[232:235], v253 offset:9216\n\t" \
    "s_waitcnt lgkmcnt(2)\n\t" \
    "v_mfma_f32_16x16x32_bf16 v[216:219], v[108:111], v[224:227], v[216:219]\n\t" \
    "ds_read_b128 v[236:239], v253 offset:28672\n\t" \
    "v_mfma_f32_16x16x32_bf16 v[220:223], v[112:115], v[240:243], v[220:223]\n\t" \
    "v_mfma_f32_16x16x32_bf16 v[220:223], v[116:119], v[224:227], v[220:223]\n\t" \
    "v_mfma_f32_16x16x32_bf16 v[212:215], v[96:99], v[240:243], v[212:215]\n\t" \
    "v_mfma_f32_16x16x32_bf16 v[212:215], v[100:103], v[224:227], v[212:215]\n\t" \
    "s_waitcnt lgkmcnt(2)\n\t" \
    "v_mfma_f32_16x16x32_bf16 v[204:207], v[120:123], v[228:231], v[204:207]\n\t" \
    "ds_read_b128 v[240:243], v253 offset:29696\n\t" \
    "s_waitcnt lgkmcnt(2)\n\t" \
    "v_mfma_f32_16x16x32_bf16 v[204:207], v[124:127], v[232:235], v[204:207]\n\t" \
    "ds_read_b128 v[224:227], v253 offset:10240\n\t" \
    "v_mfma_f32_16x16x32_bf16 v[208:211], v[128:131], v[228:231], v[208:211]\n\t" \
    "v_mfma_f32_16x16x32_bf16 v[208:211], v[132:135], v[232:235], v[208:211]\n\t" \
    "v_mfma_f32_16x16x32_bf16 v[200:203], v[244:247], v[228:231], v[200:203]\n\t" \
    "v_mfma_f32_16x16x32_bf16 v[200:203], v[248:251], v[232:235], v[200:203]\n\t" \
    "s_waitcnt lgkmcnt(2)\n\t" \
    "v_mfma_f32_16x16x32_bf16 v[216:219], v[120:123], v[236:239], v[216:219]\n\t" \
    "ds_read_b128 v[228:231], v253 offset:11264\n\t" \
    "s_waitcnt lgkmcnt(2)\n\t" \
    "v_mfma_f32_16x16x32_bf16 v[216:219], v[124:127], v[240:243], v[216:219]\n\t" \
    "ds_read_b128 v[232:235], v253 offset:30720\n\t" \
    "v_mfma_f32_16x16x32_bf16 v[220:223], v[128:131], v[236:239], v[220:223]\n\t" \
    "v_mfma_f32_16x16x32_bf16 v[220:223], v[132:135], v[240:243], v[220:223]\n\t" \
    "v_mfma_f32_16x16x32_bf16 v[212:215], v[244:247], v[236:239], v[212:215]\n\t" \
    "ds_read_b128 v[244:247], v254 offset:63488\n\t" \
    "v_mfma_f32_16x16x32_bf16 v[212:215], v[248:251], v[240:243], v[212:215]\n\t" \
    "ds_read_b128 v[248:251], v254 offset:64512\n\t" \
    "ds_read_b128 v[236:239], v253 offset:31744\n\t" \
    "s_setprio 2\n\t" \
    "s_waitcnt lgkmcnt(0)\n\t" \
    "s_barrier \n\t" \
    LST7 \
    LJIT \
    "v_mfma_f32_16x16x32_bf16 v[204:207], v[144:147], v[224:227], v[204:207]\n\t" \
    "v_mfma_f32_16x16x32_bf16 v[204:207], v[148:151], v[228:231], v[204:207]\n\t" \
    "ds_read_b128 v[240:243], v253 offset:12288\n\t" \
    "v_mfma_f32_16x16x32_bf16 v[208:211], v[152:155], v[224:227], v[208:211]\n\t" \
    "v_mfma_f32_16x16x32_bf16 v[208:211], v[156:159], v[228:231], v[208:211]\n\t" \
    "v_mfma_f32_16x16x32_bf16 v[200:203], v[136:139], v[224:227], v[200:203]\n\t" \
    "v_mfma_f32_16x16x32_bf16 v[200:203], v[140:143], v[228:231], v[200:203]\n\t" \
    "v_mfma_f32_16x16x32_bf16 v[216:219], v[144:147], v[232:235], v[216:219]\n\t" \
    "ds_read_b128 v[224:227], v253 offset:13312\n\t" \
    "s_and_b32 s44, s40, 3\n\t" \
    "v_mfma_f32_16x16x32_bf16 v[216:219], v[148:151], v[236:239], v[216:219]\n\t" \
    "ds_read_b128 v[228:231], v253 offset:32768\n\t" \
    "s_lshl_b32 s44, s44, 17\n\t" \
    "v_mfma_f32_16x16x32_bf16 v[220:223], v[152:155], v[232:235], v[220:223]\n\t" \
    "s_add_u32 s44, s44, %[mb]\n\t" \
    "v_mfma_f32_16x16x32_bf16 v[220:223], v[156:159], v[236:239], v[220:223]\n\t" \
    "s_add_u32 s46, s44, 0x4000\n\t" \
    "v_mfma_f32_16x16x32_bf16 v[212:215], v[136:139], v[232:235], v[212:215]\n\t" \
    "s_add_u32 s47, s44, 0x4400\n\t" \
    "v_mfma_f32_16x16x32_bf16 v[212:215], v[140:143], v[236:239], v[212:215]\n\t" \
    "s_waitcnt lgkmcnt(2)\n\t" \
    "v_mfma_f32_16x16x32_bf16 v[204:207], v[160:163], v[240:243], v[204:207]\n\t" \
    "ds_read_b128 v[232:235], v253 offset:33792\n\t" \
    "s_waitcnt lgkmcnt(2)\n\t" \
    "v_mfma_f32_16x16x32_bf16 v[204:207], v[164:167], v[224:227], v[204:207]\n\t" \
    "ds_read_b128 v[236:239], v253 offset:16384\n\t" \
    "v_mfma_f32_16x16x32_bf16 v[208:211], v[168:171], v[240:243], v[208:211]\n\t" \
    "v_mfma_f32_16x16x32_bf16 v[208:211], v[172:175], v[224:227], v[208:211]\n\t" \
    "v_mfma_f32_16x16x32_bf16 v[200:203], v[244:247], v[240:243], v[200:203]\n\t" \
    "v_mfma_f32_16x16x32_bf16 v[200:203], v[248:251], v[224:227], v[200:203]\n\t" \
    "s_waitcnt lgkmcnt(2)\n\t" \
    "v_mfma_f32_16x16x32_bf16 v[216:219], v[160:163], v[228:231], v[216:219]\n\t" \
    "ds_read_b128 v[240:243], v253 offset:17408\n\t" \
    "s_waitcnt lgkmcnt(2)\n\t" \
    "v_mfma_f32_16x16x32_bf16 v[216:219], v[164:167], v[232:235], v[216:219]\n\t" \
    "ds_read_b128 v[224:227], v253 offset:36864\n\t" \
    "v_mfma_f32_16x16x32_bf16 v[220:223], v[168:171], v[228:231], v[220:223]\n\t" \
    "v_mfma_f32_16x16x32_bf16 v[220:223], v[172:175], v[232:235], v[220:223]\n\t" \
    "v_mfma_f32_16x16x32_bf16 v[212:215], v[244:247], v[228:231], v[212:215]\n\t" \
    "ds_read_b128 v[244:247], v254 offset:55296\n\t" \
    "v_mfma_f32_16x16x32_bf16 v[212:215], v[248:251], v[232:235], v[212:215]\n\t" \
    "ds_read_b128 v[248:251], v254 offset:56320\n\t" \
    "s_waitcnt lgkmcnt(4)\n\t" \
    "v_mfma_f32_16x16x32_bf16 v[204:207], v[0:3], v[236:239], v[204:207]\n\t" \
    "ds_read_b128 v[228:231], v253 offset:37888\n\t" \
    "s_waitcnt lgkmcnt(4)\n\t" \
    "v_mfma_f32_16x16x32_bf16 v[204:207], v[4:7], v[240:243], v[204:207]\n\t" \
    "ds_read_b128 v[232:235], v253 offset:14336\n\t" \
    "v_mfma_f32_16x16x32_bf16 v[208:211], v[8:11], v[236:239], v[208:211]\n\t" \
    "v_mfma_f32_16x16x32_bf16 v[208:211], v[12:15], v[240:243], v[208:211]\n\t" \
    "s_waitcnt lgkmcnt(3)\n\t" \
    "v_mfma_f32_16x16x32_bf16 v[200:203], v[244:247], v[236:239], v[200:203]\n\t" \
    "s_waitcnt lgkmcnt(2)\n\t" \
    "v_mfma_f32_16x16x32_bf16 v[200:203], v[248:251], v[240:243], v[200:203]\n\t" \
    "v_mfma_f32_16x16x32_bf16 v[216:219], v[0:3], v[224:227], v[216:219]\n\t" \
    "ds_read_b128 v[236:239], v253 offset:15360\n\t" \
    "s_waitcnt lgkmcnt(2)\n\t" \
    "v_mfma_f32_16x16x32_bf16 v[216:219], v[4:7], v[228:231], v[216:219]\n\t" \
    "ds_read_b128 v[240:243], v253 offset:18432\n\t" \
    "v_mfma_f32_16x16x32_bf16 v[220:223], v[8:11], v[224:227], v[220:223]\n\t" \
    "v_mfma_f32_16x16x32_bf16 v[220:223], v[12:15], v[228:231], v[220:223]\n\t" \
    "v_mfma_f32_16x16x32_bf16 v[212:215], v[244:247], v[224:227], v[212:215]\n\t" \
    "v_mfma_f32_16x16x32_bf16 v[212:215], v[248:251], v[228:231], v[212:215]\n\t" \
    "s_waitcnt lgkmcnt(2)\n\t" \
    "v_mfma_f32_16x16x32_bf16 v[204:207], v[184:187], v[232:235], v[204:207]\n\t" \
    "ds_read_b128 v[224:227], v253 offset:19456\n\t" \
    "v_mov_b64 v[244:245], 0\n\t" \
    "s_waitcnt lgkmcnt(2)\n\t" \
    "v_mfma_f32_16x16x32_bf16 v[204:207], v[188:191], v[236:239], v[204:207]\n\t" \
    "ds_read_b128 v[228:231], v253 offset:34816\n\t" \
    "v_mov_b64 v[246:247], 0\n\t" \
    "v_mfma_f32_16x16x32_bf16 v[208:211], v[192:195], v[232:235], v[208:211]\n\t" \
    "v_mov_b64 v[248:249], 0\n\t" \
    "v_mfma_f32_16x16x32_bf16 v[208:211], v[196:199], v[236:239], v[208:211]\n\t" \
    "v_mov_b64 v[250:251], 0\n\t" \
    "v_mfma_f32_16x16x32_bf16 v[200:203], v[176:179], v[232:235], v[200:203]\n\t" \
    "v_mfma_f32_16x16x32_bf16 v[200:203], v[180:183], v[236:239], v[200:203]\n\t" \
    "s_waitcnt lgkmcnt(2)\n\t" \
    "v_mfma_f32_16x16x32_bf16 v[204:207], v[24:27], v[240:243], v[204:207]\n\t" \
    "ds_read_b128 v[232:235], v253 offset:35840\n\t" \
    "s_waitcnt lgkmcnt(2)\n\t" \
    "v_mfma_f32_16x16x32_bf16 v[204:207], v[28:31], v[224:227], v[204:207]\n\t" \
    "ds_read_b128 v[236:239], v253 offset:38912\n\t" \
    "v_mfma_f32_16x16x32_bf16 v[208:211], v[32:35], v[240:243], v[208:211]\n\t" \
    "v_mfma_f32_16x16x32_bf16 v[208:211], v[36:39], v[224:227], v[208:211]\n\t" \
    "v_mfma_f32_16x16x32_bf16 v[200:203], v[16:19], v[240:243], v[200:203]\n\t" \
    "v_mfma_f32_16x16x32_bf16 v[200:203], v[20:23], v[224:227], v[200:203]\n\t" \
    "ds_read_b128 v[240:243], v253 offset:39936\n\t" \
    "s_waitcnt lgkmcnt(3)\n\t" \
    "v_mfma_f32_16x16x32_bf16 v[216:219], v[184:187], v[228:231], v[216:219]\n\t" \
    "s_waitcnt lgkmcnt(2)\n\t" \
    "v_mfma_f32_16x16x32_bf16 v[216:219], v[188:191], v[232:235], v[216:219]\n\t" \
    "v_cvt_pk_bf16_f32 v204, v204, v208\n\t" \
    "v_cvt_pk_bf16_f32 v205, v205, v209\n\t" \
    "s_waitcnt lgkmcnt(1)\n\t" \
    "v_mfma_f32_16x16x32_bf16 v[216:219], v[24:27], v[236:239], v[216:219]\n\t" \
    "v_cvt_pk_bf16_f32 v206, v206, v210\n\t" \
    "v_cvt_pk_bf16_f32 v207, v207, v211\n\t" \
    "s_waitcnt lgkmcnt(0)\n\t" \
    "v_mfma_f32_16x16x32_bf16 v[216:219], v[28:31], v[240:243], v[216:219]\n\t" \
    "ds_write_b128 v254, v[204:207] offset:5120\n\t" \
    "v_mfma_f32_16x16x32_bf16 v[220:223], v[192:195], v[228:231], v[220:223]\n\t" \
    "v_mfma_f32_16x16x32_bf16 v[220:223], v[196:199], v[232:235], v[220:223]\n\t" \
    "v_cvt_pk_bf16_f32 v200, v200, v201\n\t" \
    "v_cvt_pk_bf16_f32 v201, v202, v203\n\t" \
    "v_mfma_f32_16x16x32_bf16 v[220:223], v[32:35], v[236:239], v[220:223]\n\t" \
    "ds_write_b64 v254, v[200:201] offset:1032\n\t" \
    "v_mfma_f32_16x16x32_bf16 v[220:223], v[36:39], v[240:243], v[220:223]\n\t" \
    "v_mfma_f32_16x16x32_bf16 v[212:215], v[176:179], v[228:231], v[212:215]\n\t" \
    "ds_read_b128 v[200:203], v254 offset:55296\n\t" \
    "ds_read_b128 v[204:207], v254 offset:56320\n\t" \
    "v_mfma_f32_16x16x32_bf16 v[212:215], v[180:183], v[232:235], v[212:215]\n\t" \
    "v_mov_b64 v[208:209], 0\n\t" \
    "v_mov_b64 v[210:211], 0\n\t" \
    "v_mfma_f32_16x16x32_bf16 v[212:215], v[16:19], v[236:239], v[212:215]\n\t" \
    "v_cvt_pk_bf16_f32 v216, v216, v220\n\t" \
    "v_cvt_pk_bf16_f32 v217, v217, v221\n\t" \
    "v_mfma_f32_16x16x32_bf16 v[212:215], v[20:23], v[240:243], v[212:215]\n\t" \
    "v_cvt_pk_bf16_f32 v218, v218, v222\n\t" \
    "v_cvt_pk_bf16_f32 v219, v219, v223\n\t" \
    "s_setprio 0\n\t" \
    "ds_write_b128 v254, v[216:219] offset:11264\n\t" \
    "ds_read_b128 v[216:219], v254 offset:57344\n\t" \
    "ds_read_b128 v[220:223], v254 offset:58368\n\t" \
    "s_nop 2\n\t" \
    "v_cvt_pk_bf16_f32 v212, v212, v213\n\t" \
    "v_cvt_pk_bf16_f32 v213, v214, v215\n\t" \
    "ds_write_b64 v254, v[212:213] offset:7176\n\t" \
    LST6 \
    LJIT \
    "s_sleep 3\n\t" \
    "s_mov_b32 m0, 0xc000\n\t" \
    "s_nop 0\n\t" \
    "buffer_load_dwordx4 v253, s[60:63], s46 offen sc1 lds\n\t" \
    "s_mov_b32 m0, 0xc400\n\t" \
    "s_nop 0\n\t" \
    "buffer_load_dwordx4 v253, s[60:63], s47 offen sc1 lds\n\t" \
    "s_waitcnt lgkmcnt(0)\n\t" \
    LFLUSH212 \
    "v_bfe_i32 v212, v253, 5, 1\n\t" \
    "v_and_b32 v212, 0xaaaa, v212\n\t" \
    "v_or_b32 v212, 0x4444, v212\n\t" \
    "v_and_b32 v213, 0xffff, v255\n\t" \
    "v_lshrrev_b32 v214, 16, v255\n\t" \
    ".Ls34_%=:\n\t" \
    "s_waitcnt vmcnt(0)\n\t" \
    "ds_read_b128 v[224:227], v253 offset:49152\n\t" \
    "ds_read_b128 v[228:231], v253 offset:50176\n\t" \
    "s_waitcnt lgkmcnt(1)\n\t" \
    "v_max_u32 v215, v224, v225\n\t" \
    "v_max3_u32 v215, v226, v227, v215\n\t" \
    "s_waitcnt lgkmcnt(0)\n\t" \
    "v_max3_u32 v215, v228, v229, v215\n\t" \
    "v_max3_u32 v215, v230, v231, v215\n\t" \
    "v_cmp_eq_u32 vcc, -1, v215\n\t" \
    "s_cbranch_vccz .Ls35_%=\n\t" \
    "v_max_u32 v215, v224, v225\n\t" \
    "v_max3_u32 v215, v226, v227, v215\n\t" \
    "v_cmp_eq_u32 vcc, -1, v215\n\t" \
    "s_cbranch_vccz .Ls36_%=\n\t" \
    "s_mov_b32 m0, 0xc000\n\t" \
    "s_nop 0\n\t" \
    "buffer_load_dwordx4 v253, s[60:63], s46 offen sc1 lds\n\t" \
    ".Ls36_%=:\n\t" \
    "v_max_u32 v215, v228, v229\n\t" \
    "v_max3_u32 v215, v230, v231, v215\n\t" \
    "v_cmp_eq_u32 vcc, -1, v215\n\t" \
    "s_cbranch_vccz .Ls37_%=\n\t" \
    "s_mov_b32 m0, 0xc400\n\t" \
    "s_nop 0\n\t" \
    "buffer_load_dwordx4 v253, s[60:63], s47 offen sc1 lds\n\t" \
    ".Ls37_%=:\n\t" \
    "s_cmp_eq_u32 s41, 0\n\t" \
    "s_cbranch_scc1 .Ls35_%=\n\t" \
    "s_sub_u32 s41, s41, 1\n\t" \
    "s_branch .Ls34_%=\n\t" \
    ".Ls35_%=:\n\t" \
    LST2 \
    LJIT \
    "s_setprio 3\n\t" \
    "s_waitcnt lgkmcnt(0)\n\t" \
    "s_barrier \n\t" \
    LST3 \
    LJIT \
    "ds_read_b128 v[232:235], v253 offset:40960\n\t" \
    "ds_read_b128 v[236:239], v253 offset:41984\n\t" \
    "ds_read_b128 v[240:243], v253 offset:43008\n\t" \
    "v_smfmac_f32_16x16x64_bf16 v[244:247], v[224:227], v[0:7], v212\n\t" \
    "v_smfmac_f32_16x16x64_bf16 v[248:251], v[224:227], v[8:15], v212\n\t" \
    "v_smfmac_f32_16x16x64_bf16 v[208:211], v[224:227], v[200:207], v212\n\t" \
    "ds_read_b128 v[200:203], v254 offset:59392\n\t" \
    "ds_read_b128 v[204:207], v254 offset:60416\n\t" \
    "ds_read_b128 v[224:227], v253 offset:44032\n\t" \
    "v_smfmac_f32_16x16x64_bf16 v[208:211], v[228:231], v[16:23], v212\n\t" \
    "v_smfmac_f32_16x16x64_bf16 v[244:247], v[228:231], v[24:31], v212\n\t" \
    "v_smfmac_f32_16x16x64_bf16 v[248:251], v[228:231], v[32:39], v212\n\t" \
    "ds_read_b128 v[228:231], v253 offset:45056\n\t" \
    "s_waitcnt lgkmcnt(6)\n\t" \
    "v_smfmac_f32_16x16x64_bf16 v[208:211], v[232:235], v[216:223], v212\n\t" \
    "ds_read_b128 v[216:219], v254 offset:61440\n\t" \
    "ds_read_b128 v[220:223], v254 offset:62464\n\t" \
    "v_smfmac_f32_16x16x64_bf16 v[244:247], v[232:235], v[40:47], v212\n\t" \
    "v_smfmac_f32_16x16x64_bf16 v[248:251], v[232:235], v[48:55], v212\n\t" \
    "ds_read_b128 v[232:235], v253 offset:46080\n\t" \
    "s_waitcnt lgkmcnt(8)\n\t" \
    "v_smfmac_f32_16x16x64_bf16 v[208:211], v[236:239], v[56:63], v212\n\t" \
    "v_smfmac_f32_16x16x64_bf16 v[244:247], v[236:239], v[64:71], v212\n\t" \
    "v_smfmac_f32_16x16x64_bf16 v[248:251], v[236:239], v[72:79], v212\n\t" \
    "ds_read_b128 v[236:239], v253 offset:47104\n\t" \
    "s_waitcnt lgkmcnt(6)\n\t" \
    "v_smfmac_f32_16x16x64_bf16 v[208:211], v[240:243], v[200:207], v212\n\t" \
    "ds_read_b128 v[200:203], v254 offset:63488\n\t" \
    "ds_read_b128 v[204:207], v254 offset:64512\n\t" \
    "v_smfmac_f32_16x16x64_bf16 v[244:247], v[240:243], v[80:87], v212\n\t" \
    "v_smfmac_f32_16x16x64_bf16 v[248:251], v[240:243], v[88:95], v212\n\t" \
    "ds_read_b128 v[240:243], v253 offset:48128\n\t" \
    "s_waitcnt lgkmcnt(8)\n\t" \
    "v_smfmac_f32_16x16x64_bf16 v[208:211], v[224:227], v[96:103], v212\n\t" \
    "v_smfmac_f32_16x16x64_bf16 v[244:247], v[224:227], v[104:111], v212\n\t" \
    "v_smfmac_f32_16x16x64_bf16 v[248:251], v[224:227], v[112:119], v212\n\t" \
    "s_waitcnt lgkmcnt(5)\n\t" \
    "v_smfmac_f32_16x16x64_bf16 v[208:211], v[228:231], v[216:223], v212\n\t" \
    "s_waitcnt lgkmcnt(4)\n\t" \
    "v_smfmac_f32_16x16x64_bf16 v[208:211], v[232:235], v[136:143], v212\n\t" \
    "s_waitcnt lgkmcnt(1)\n\t" \
    "v_smfmac_f32_16x16x64_bf16 v[208:211], v[236:239], v[200:207], v212\n\t" \
    "s_waitcnt lgkmcnt(0)\n\t" \
    "v_smfmac_f32_16x16x64_bf16 v[208:211], v[240:243], v[176:183], v212\n\t" \
    "v_smfmac_f32_16x16x64_bf16 v[244:247], v[228:231], v[120:127], v212\n\t" \
    "v_smfmac_f32_16x16x64_bf16 v[244:247], v[232:235], v[144:151], v212\n\t" \
    "v_smfmac_f32_16x16x64_bf16 v[244:247], v[236:239], v[160:167], v212\n\t" \
    "v_add_f32 v208, v208, v210\n\t" \
    "v_add_f32 v209, v209, v211\n\t" \
    "v_cvt_pk_bf16_f32 v208, v208, v209\n\t" \
    "v_smfmac_f32_16x16x64_bf16 v[244:247], v[240:243], v[184:191], v212\n\t" \
    "ds_write_b16 v213, v208 offset:0\n\t" \
    "ds_write_b16_d16_hi v213, v208 offset:16\n\t" \
    "v_smfmac_f32_16x16x64_bf16 v[248:251], v[228:231], v[128:135], v212\n\t" \
    "v_smfmac_f32_16x16x64_bf16 v[248:251], v[232:235], v[152:159], v212\n\t" \
    "v_smfmac_f32_16x16x64_bf16 v[248:251], v[236:239], v[168:175], v212\n\t" \
    "v_add_f32 v244, v244, v246\n\t" \
    "v_add_f32 v245, v245, v247\n\t" \
    "v_smfmac_f32_16x16x64_bf16 v[248:251], v[240:243], v[192:199], v212\n\t" \
    "s_nop 7\n\t" \
    "v_add_f32 v248, v248, v250\n\t" \
    "v_add_f32 v249, v249, v251\n\t" \
    "v_cvt_pk_bf16_f32 v244, v244, v248\n\t" \
    "v_cvt_pk_bf16_f32 v245, v245, v249\n\t" \
    "ds_write2_b32 v214, v244, v245 offset1:4\n\t" \
    "s_setprio 0\n\t" \
    "s_sleep 1\n\t" \
    "ds_read_b128 v[208:211], v254 offset:24576\n\t" \
    "ds_read_b128 v[224:227], v253 offset:0\n\t" \
    "ds_read_b128 v[228:231], v253 offset:20480\n\t" \
    "ds_read_b128 v[212:215], v254 offset:25600\n\t" \
    "ds_read_b128 v[232:235], v253 offset:1024\n\t" \
    "ds_read_b128 v[236:239], v253 offset:21504\n\t" \
    "ds_read_b128 v[216:219], v254 offset:26624\n\t" \
    "ds_read_b128 v[240:243], v253 offset:2048\n\t" \
    "ds_read_b128 v[244:247], v253 offset:22528\n\t" \
    "ds_read_b128 v[220:223], v254 offset:27648\n\t" \
    "ds_read_b128 v[248:251], v253 offset:3072\n\t" \
    "s_waitcnt lgkmcnt(9)\n\t" \
    "v_mfma_f32_16x16x32_bf16 v[200:203], v[208:211], v[224:227], 0\n\t" \
    "ds_read_b128 v[224:227], v253 offset:23552\n\t" \
    "s_waitcnt lgkmcnt(9)\n\t" \
    "v_mfma_f32_16x16x32_bf16 v[204:207], v[208:211], v[228:231], 0\n\t" \
    "ds_read_b128 v[228:231], v253 offset:4096\n\t" \
    "ds_read_b128 v[208:211], v254 offset:28672\n\t" \
    "s_waitcnt lgkmcnt(9)\n\t" \
    "v_mfma_f32_16x16x32_bf16 v[200:203], v[212:215], v[232:235], v[200:203]\n\t" \
    "ds_read_b128 v[232:235], v253 offset:24576\n\t" \
    "s_waitcnt lgkmcnt(9)\n\t" \
    "v_mfma_f32_16x16x32_bf16 v[204:207], v[212:215], v[236:239], v[204:207]\n\t" \
    "ds_read_b128 v[236:239], v253 offset:5120\n\t" \
    "ds_read_b128 v[212:215], v254 offset:29696\n\t" \
    "s_waitcnt lgkmcnt(9)\n\t" \
    "v_mfma_f32_16x16x32_bf16 v[200:203], v[216:219], v[240:243], v[200:203]\n\t" \
    "ds_read_b128 v[240:243], v253 offset:25600\n\t" \
    "s_waitcnt lgkmcnt(0)\n\t" \
    "s_barrier \n\t" \
    LST4 \
    LJIT \
    "s_add_u32 s40, s40, 1\n\t" \
    "s_cmp_lg_u32 s40, 64\n\t" \
    "s_cbranch_scc1 .Ls33_%=\n\t" \
    "s_branch .Lend%=\n\t" \
    ".Ls2_%=:\n\t" \
    "s_add_u32 m0, %[wm], 0x18000\n\t" \
    "s_mov_b32 s44, 0x0\n\t" \
    "buffer_load_dwordx4 v240, %[wr], s44 offen offset:0 nt lds\n\t" \
    "buffer_load_dwordx4 v241, %[wr], s44 offen offset:1024 nt lds\n\t" \
    "buffer_load_dwordx4 v242, %[wr], s44 offen offset:2048 nt lds\n\t" \
    "s_add_u32 m0, %[wm], 0x1e000\n\t" \
    "s_mov_b32 s44, 0x60000\n\t" \
    "buffer_load_dwordx4 v240, %[wr], s44 offen offset:0 nt lds\n\t" \
    "buffer_load_dwordx4 v241, %[wr], s44 offen offset:1024 nt lds\n\t" \
    "buffer_load_dwordx4 v242, %[wr], s44 offen offset:2048 nt lds\n\t" \
    "s_add_u32 m0, %[wm], 0x0\n\t" \
    "s_mov_b32 s44, 0xc0000\n\t" \
    "buffer_load_dwordx4 v240, %[wr], s44 offen offset:0 nt lds\n\t" \
    "buffer_load_dwordx4 v241, %[wr], s44 offen offset:1024 nt lds\n\t" \
    "buffer_load_dwordx4 v242, %[wr], s44 offen offset:2048 nt lds\n\t" \
    "s_add_u32 m0, %[wm], 0x6000\n\t" \
    "s_mov_b32 s44, 0x120000\n\t" \
    "buffer_load_dwordx4 v240, %[wr], s44 offen offset:0 nt lds\n\t" \
    "buffer_load_dwordx4 v241, %[wr], s44 offen offset:1024 nt lds\n\t" \
    "buffer_load_dwordx4 v242, %[wr], s44 offen offset:2048 nt lds\n\t" \
    "s_add_u32 m0, %[wm], 0xc000\n\t" \
    "s_mov_b32 s44, 0x180000\n\t" \
    "buffer_load_dwordx4 v240, %[wr], s44 offen offset:0 nt lds\n\t" \
    "buffer_load_dwordx4 v241, %[wr], s44 offen offset:1024 nt lds\n\t" \
    "buffer_load_dwordx4 v242, %[wr], s44 offen offset:2048 nt lds\n\t" \
    "v_add_u32 v244, 0x18000, v243\n\t" \
    "s_waitcnt vmcnt(12) lgkmcnt(0)\n\t" \
    "s_barrier \n\t" \
    "s_add_u32 m0, %[wm], 0x12000\n\t" \
    "s_mov_b32 s44, 0x1e0000\n\t" \
    "buffer_load_dwordx4 v240, %[wr], s44 offen offset:0 nt lds\n\t" \
    "buffer_load_dwordx4 v241, %[wr], s44 offen offset:1024 nt lds\n\t" \
    "buffer_load_dwordx4 v242, %[wr], s44 offen offset:2048 nt lds\n\t" \
    "ds_read_b64_tr_b16 v[208:209], v244 offset:0\n\t" \
    "ds_read_b64_tr_b16 v[210:211], v244 offset:1536\n\t" \
    "ds_read_b64_tr_b16 v[40:41], v244 offset:32\n\t" \
    "ds_read_b64_tr_b16 v[42:43], v244 offset:1568\n\t" \
    "ds_read_b64_tr_b16 v[48:49], v244 offset:64\n\t" \
    "ds_read_b64_tr_b16 v[50:51], v244 offset:1600\n\t" \
    "v_add_u32 v244, 0x1e000, v243\n\t" \
    "s_waitcnt vmcnt(12) lgkmcnt(0)\n\t" \
    "s_barrier \n\t" \
    "s_add_u32 m0, %[wm], 0x18000\n\t" \
    "s_mov_b32 s44, 0x240000\n\t" \
    "buffer_load_dwordx4 v240, %[wr], s44 offen offset:0 nt lds\n\t" \
    "buffer_load_dwordx4 v241, %[wr], s44 offen offset:1024 nt lds\n\t" \
    "buffer_load_dwordx4 v242, %[wr], s44 offen offset:2048 nt lds\n\t" \
    "ds_read_b64_tr_b16 v[212:213], v244 offset:0\n\t" \
    "ds_read_b64_tr_b16 v[214:215], v244 offset:1536\n\t" \
    "ds_read_b64_tr_b16 v[44:45], v244 offset:32\n\t" \
    "ds_read_b64_tr_b16 v[46:47], v244 offset:1568\n\t" \
    "ds_read_b64_tr_b16 v[52:53], v244 offset:64\n\t" \
    "ds_read_b64_tr_b16 v[54:55], v244 offset:1600\n\t" \
    "v_add_u32 v244, 0x0, v243\n\t" \
    "s_waitcnt vmcnt(12) lgkmcnt(0)\n\t" \
    "s_barrier \n\t" \
    "s_add_u32 m0, %[wm], 0x1e000\n\t" \
    "s_mov_b32 s44, 0x2a0000\n\t" \
    "buffer_load_dwordx4 v240, %[wr], s44 offen offset:0 nt lds\n\t" \
    "buffer_load_dwordx4 v241, %[wr], s44 offen offset:1024 nt lds\n\t" \
    "buffer_load_dwordx4 v242, %[wr], s44 offen offset:2048 nt lds\n\t" \
    "ds_read_b64_tr_b16 v[56:57], v244 offset:0\n\t" \
    "ds_read_b64_tr_b16 v[58:59], v244 offset:1536\n\t" \
    "ds_read_b64_tr_b16 v[64:65], v244 offset:32\n\t" \
    "ds_read_b64_tr_b16 v[66:67], v244 offset:1568\n\t" \
    "ds_read_b64_tr_b16 v[72:73], v244 offset:64\n\t" \
    "ds_read_b64_tr_b16 v[74:75], v244 offset:1600\n\t" \
    "v_add_u32 v244, 0x6000, v243\n\t" \
    "s_waitcnt vmcnt(12) lgkmcnt(0)\n\t" \
    "s_barrier \n\t" \
    "s_add_u32 m0, %[wm], 0x0\n\t" \
    "s_mov_b32 s44, 0x300000\n\t" \
    "buffer_load_dwordx4 v240, %[wr], s44 offen offset:0 nt lds\n\t" \
    "buffer_load_dwordx4 v241, %[wr], s44 offen offset:1024 nt lds\n\t" \
    "buffer_load_dwordx4 v242, %[wr], s44 offen offset:2048 nt lds\n\t" \
    "ds_read_b64_tr_b16 v[60:61], v244 offset:0\n\t" \
    "ds_read_b64_tr_b16 v[62:63], v244 offset:1536\n\t" \
    "ds_read_b64_tr_b16 v[68:69], v244 offset:32\n\t" \
    "ds_read_b64_tr_b16 v[70:71], v244 offset:1568\n\t" \
    "ds_read_b64_tr_b16 v[76:77], v244 offset:64\n\t" \
    "ds_read_b64_tr_b16 v[78:79], v244 offset:1600\n\t" \
    "v_add_u32 v244, 0xc000, v243\n\t" \
    "s_waitcnt vmcnt(12) lgkmcnt(0)\n\t" \
    "s_barrier \n\t" \
    "s_add_u32 m0, %[wm], 0x6000\n\t" \
    "s_mov_b32 s44, 0x360000\n\t" \
    "buffer_load_dwordx4 v240, %[wr], s44 offen offset:0 nt lds\n\t" \
    "buffer_load_dwordx4 v241, %[wr], s44 offen offset:1024 nt lds\n\t" \
    "buffer_load_dwordx4 v242, %[wr], s44 offen offset:2048 nt lds\n\t" \
    "ds_read_b64_tr_b16 v[216:217], v244 offset:0\n\t" \
    "ds_read_b64_tr_b16 v[218:219], v244 offset:1536\n\t" \
    "ds_read_b64_tr_b16 v[80:81], v244 offset:32\n\t" \
    "ds_read_b64_tr_b16 v[82:83], v244 offset:1568\n\t" \
    "ds_read_b64_tr_b16 v[88:89], v244 offset:64\n\t" \
    "ds_read_b64_tr_b16 v[90:91], v244 offset:1600\n\t" \
    "v_add_u32 v244, 0x12000, v243\n\t" \
    "s_waitcnt vmcnt(12) lgkmcnt(0)\n\t" \
    "s_barrier \n\t" \
    "s_add_u32 m0, %[wm], 0xc000\n\t" \
    "s_mov_b32 s44, 0x3c0000\n\t" \
    "buffer_load_dwordx4 v240, %[wr], s44 offen offset:0 nt lds\n\t" \
    "buffer_load_dwordx4 v241, %[wr], s44 offen offset:1024 nt lds\n\t" \
    "buffer_load_dwordx4 v242, %[wr], s44 offen offset:2048 nt lds\n\t" \
    "ds_read_b64_tr_b16 v[220:221], v244 offset:0\n\t" \
    "ds_read_b64_tr_b16 v[222:223], v244 offset:1536\n\t" \
    "ds_read_b64_tr_b16 v[84:85], v244 offset:32\n\t" \
    "ds_read_b64_tr_b16 v[86:87], v244 offset:1568\n\t" \
    "ds_read_b64_tr_b16 v[92:93], v244 offset:64\n\t" \
    "ds_read_b64_tr_b16 v[94:95], v244 offset:1600\n\t" \
    "v_add_u32 v244, 0x18000, v243\n\t" \
    "s_waitcnt vmcnt(12) lgkmcnt(0)\n\t" \
    "s_barrier \n\t" \
    "v_mov_b32 v246, 0\n\t" \
    "buffer_load_dword v246, v246, s[60:63], %[mo] offen sc1\n\t" \
    "s_add_u32 m0, %[wm], 0x12000\n\t" \
    "s_mov_b32 s44, 0x420000\n\t" \
    "buffer_load_dwordx4 v240, %[wr], s44 offen offset:0 nt lds\n\t" \
    "buffer_load_dwordx4 v241, %[wr], s44 offen offset:1024 nt lds\n\t" \
    "buffer_load_dwordx4 v242, %[wr], s44 offen offset:2048 nt lds\n\t" \
    "ds_read_b64_tr_b16 v[96:97], v244 offset:0\n\t" \
    "ds_read_b64_tr_b16 v[98:99], v244 offset:1536\n\t" \
    "ds_read_b64_tr_b16 v[104:105], v244 offset:32\n\t" \
    "ds_read_b64_tr_b16 v[106:107], v244 offset:1568\n\t" \
    "ds_read_b64_tr_b16 v[112:113], v244 offset:64\n\t" \
    "ds_read_b64_tr_b16 v[114:115], v244 offset:1600\n\t" \
    "v_add_u32 v244, 0x1e000, v243\n\t" \
    "s_waitcnt vmcnt(12) lgkmcnt(0)\n\t" \
    "s_barrier \n\t" \
    "s_add_u32 m0, %[wm], 0x18000\n\t" \
    "s_mov_b32 s44, 0x480000\n\t" \
    "buffer_load_dwordx4 v240, %[wr], s44 offen offset:0 nt lds\n\t" \
    "buffer_load_dwordx4 v241, %[wr], s44 offen offset:1024 nt lds\n\t" \
    "buffer_load_dwordx4 v242, %[wr], s44 offen offset:2048 nt lds\n\t" \
    "ds_read_b64_tr_b16 v[100:101], v244 offset:0\n\t" \
    "ds_read_b64_tr_b16 v[102:103], v244 offset:1536\n\t" \
    "ds_read_b64_tr_b16 v[108:109], v244 offset:32\n\t" \
    "ds_read_b64_tr_b16 v[110:111], v244 offset:1568\n\t" \
    "ds_read_b64_tr_b16 v[116:117], v244 offset:64\n\t" \
    "ds_read_b64_tr_b16 v[118:119], v244 offset:1600\n\t" \
    "v_add_u32 v244, 0x0, v243\n\t" \
    "s_waitcnt vmcnt(12) lgkmcnt(0)\n\t" \
    "s_barrier \n\t" \
    "s_add_u32 m0, %[wm], 0x1e000\n\t" \
    "s_mov_b32 s44, 0x4e0000\n\t" \
    "buffer_load_dwordx4 v240, %[wr], s44 offen offset:0 nt lds\n\t" \
    "buffer_load_dwordx4 v241, %[wr], s44 offen offset:1024 nt lds\n\t" \
    "buffer_load_dwordx4 v242, %[wr], s44 offen offset:2048 nt lds\n\t" \
    "ds_read_b64_tr_b16 v[200:201], v244 offset:0\n\t" \
    "ds_read_b64_tr_b16 v[202:203], v244 offset:1536\n\t" \
    "ds_read_b64_tr_b16 v[0:1], v244 offset:32\n\t" \
    "ds_read_b64_tr_b16 v[2:3], v244 offset:1568\n\t" \
    "ds_read_b64_tr_b16 v[8:9], v244 offset:64\n\t" \
    "ds_read_b64_tr_b16 v[10:11], v244 offset:1600\n\t" \
    "v_add_u32 v244, 0x6000, v243\n\t" \
    "s_waitcnt vmcnt(12) lgkmcnt(0)\n\t" \
    "s_barrier \n\t" \
    "s_add_u32 m0, %[wm], 0x0\n\t" \
    "s_mov_b32 s44, 0x540000\n\t" \
    "buffer_load_dwordx4 v240, %[wr], s44 offen offset:0 nt lds\n\t" \
    "buffer_load_dwordx4 v241, %[wr], s44 offen offset:1024 nt lds\n\t" \
    "buffer_load_dwordx4 v242, %[wr], s44 offen offset:2048 nt lds\n\t" \
    "ds_read_b64_tr_b16 v[204:205], v244 offset:0\n\t" \
    "ds_read_b64_tr_b16 v[206:207], v244 offset:1536\n\t" \
    "ds_read_b64_tr_b16 v[4:5], v244 offset:32\n\t" \
    "ds_read_b64_tr_b16 v[6:7], v244 offset:1568\n\t" \
    "ds_read_b64_tr_b16 v[12:13], v244 offset:64\n\t" \
    "ds_read_b64_tr_b16 v[14:15], v244 offset:1600\n\t" \
    "v_add_u32 v244, 0xc000, v243\n\t" \
    "s_waitcnt vmcnt(12) lgkmcnt(0)\n\t" \
    "s_barrier \n\t" \
    "s_add_u32 m0, %[wm], 0x6000\n\t" \
    "s_mov_b32 s44, 0x5a0000\n\t" \
    "buffer_load_dwordx4 v240, %[wr], s44 offen offset:0 nt lds\n\t" \
    "buffer_load_dwordx4 v241, %[wr], s44 offen offset:1024 nt lds\n\t" \
    "buffer_load_dwordx4 v242, %[wr], s44 offen offset:2048 nt lds\n\t" \
    "ds_read_b64_tr_b16 v[224:225], v244 offset:0\n\t" \
    "ds_read_b64_tr_b16 v[226:227], v244 offset:1536\n\t" \
    "ds_read_b64_tr_b16 v[120:121], v244 offset:32\n\t" \
    "ds_read_b64_tr_b16 v[122:123], v244 offset:1568\n\t" \
    "ds_read_b64_tr_b16 v[128:129], v244 offset:64\n\t" \
    "ds_read_b64_tr_b16 v[130:131], v244 offset:1600\n\t" \
    "v_add_u32 v244, 0x12000, v243\n\t" \
    "s_waitcnt vmcnt(12) lgkmcnt(0)\n\t" \
    "s_barrier \n\t" \
    ".Ls21_%=:\n\t" \
    "v_readfirstlane_b32 s43, v246\n\t" \
    "s_nop 0\n\t" \
    "s_cmp_lg_u32 s43, -1\n\t" \
    "s_cbranch_scc1 .Ls22_%=\n\t" \
    "s_cmp_eq_u32 s41, 0\n\t" \
    "s_cbranch_scc1 .Ls22_%=\n\t" \
    "s_sub_u32 s41, s41, 1\n\t" \
    "s_sleep 4\n\t" \
    "v_mov_b32 v246, 0\n\t" \
    "buffer_load_dword v246, v246, s[60:63], %[mo] offen sc1\n\t" \
    "s_waitcnt vmcnt(0)\n\t" \
    "s_branch .Ls21_%=\n\t" \
    ".Ls22_%=:\n\t" \
    "v_mov_b32 v246, 0x8000\n\t" \
    "s_mov_b64 exec, 1\n\t" \
    "buffer_store_dword v246, v246, s[60:63], %[mo] offen sc1\n\t" \
    "s_mov_b64 exec, -1\n\t" \
    "s_and_b32 s43, s43, 0x2000\n\t" \
    "s_xor_b32 s43, s43, %[lx]\n\t" \
    "s_add_u32 s60, s60, s43\n\t" \
    "s_addc_u32 s61, s61, 0\n\t" \
    "s_add_u32 m0, %[wm], 0xc000\n\t" \
    "s_mov_b32 s44, 0x600000\n\t" \
    "buffer_load_dwordx4 v240, %[wr], s44 offen offset:0 nt lds\n\t" \
    "buffer_load_dwordx4 v241, %[wr], s44 offen offset:1024 nt lds\n\t" \
    "buffer_load_dwordx4 v242, %[wr], s44 offen offset:2048 nt lds\n\t" \
    "ds_read_b64_tr_b16 v[228:229], v244 offset:0\n\t" \
    "ds_read_b64_tr_b16 v[230:231], v244 offset:1536\n\t" \
    "ds_read_b64_tr_b16 v[124:125], v244 offset:32\n\t" \
    "ds_read_b64_tr_b16 v[126:127], v244 offset:1568\n\t" \
    "ds_read_b64_tr_b16 v[132:133], v244 offset:64\n\t" \
    "ds_read_b64_tr_b16 v[134:135], v244 offset:1600\n\t" \
    "v_add_u32 v244, 0x18000, v243\n\t" \
    "s_waitcnt vmcnt(12) lgkmcnt(0)\n\t" \
    "s_barrier \n\t" \
    "s_add_u32 m0, %[wm], 0x12000\n\t" \
    "s_mov_b32 s44, 0x660000\n\t" \
    "buffer_load_dwordx4 v240, %[wr], s44 offen offset:0 nt lds\n\t" \
    "buffer_load_dwordx4 v241, %[wr], s44 offen offset:1024 nt lds\n\t" \
    "buffer_load_dwordx4 v242, %[wr], s44 offen offset:2048 nt lds\n\t" \
    "ds_read_b64_tr_b16 v[136:137], v244 offset:0\n\t" \
    "ds_read_b64_tr_b16 v[138:139], v244 offset:1536\n\t" \
    "ds_read_b64_tr_b16 v[144:145], v244 offset:32\n\t" \
    "ds_read_b64_tr_b16 v[146:147], v244 offset:1568\n\t" \
    "ds_read_b64_tr_b16 v[152:153], v244 offset:64\n\t" \
    "ds_read_b64_tr_b16 v[154:155], v244 offset:1600\n\t" \
    "v_add_u32 v244, 0x1e000, v243\n\t" \
    "s_waitcnt vmcnt(12) lgkmcnt(0)\n\t" \
    "s_barrier \n\t" \
    "s_add_u32 m0, %[wm], 0x18000\n\t" \
    "s_mov_b32 s44, 0x6c0000\n\t" \
    "buffer_load_dwordx4 v240, %[wr], s44 offen offset:0 nt lds\n\t" \
    "buffer_load_dwordx4 v241, %[wr], s44 offen offset:1024 nt lds\n\t" \
    "buffer_load_dwordx4 v242, %[wr], s44 offen offset:2048 nt lds\n\t" \
    "ds_read_b64_tr_b16 v[140:141], v244 offset:0\n\t" \
    "ds_read_b64_tr_b16 v[142:143], v244 offset:1536\n\t" \
    "ds_read_b64_tr_b16 v[148:149], v244 offset:32\n\t" \
    "ds_read_b64_tr_b16 v[150:151], v244 offset:1568\n\t" \
    "ds_read_b64_tr_b16 v[156:157], v244 offset:64\n\t" \
    "ds_read_b64_tr_b16 v[158:159], v244 offset:1600\n\t" \
    "v_add_u32 v244, 0x0, v243\n\t" \
    "s_waitcnt vmcnt(12) lgkmcnt(0)\n\t" \
    "s_barrier \n\t" \
    "s_add_u32 m0, %[wm], 0x1e000\n\t" \
    "s_mov_b32 s44, 0x720000\n\t" \
    "buffer_load_dwordx4 v240, %[wr], s44 offen offset:0 nt lds\n\t" \
    "buffer_load_dwordx4 v241, %[wr], s44 offen offset:1024 nt lds\n\t" \
    "buffer_load_dwordx4 v242, %[wr], s44 offen offset:2048 nt lds\n\t" \
    "ds_read_b64_tr_b16 v[16:17], v244 offset:0\n\t" \
    "ds_read_b64_tr_b16 v[18:19], v244 offset:1536\n\t" \
    "ds_read_b64_tr_b16 v[24:25], v244 offset:32\n\t" \
    "ds_read_b64_tr_b16 v[26:27], v244 offset:1568\n\t" \
    "ds_read_b64_tr_b16 v[32:33], v244 offset:64\n\t" \
    "ds_read_b64_tr_b16 v[34:35], v244 offset:1600\n\t" \
    "v_add_u32 v244, 0x6000, v243\n\t" \
    "s_waitcnt vmcnt(12) lgkmcnt(0)\n\t" \
    "s_barrier \n\t" \
    "ds_read_b64_tr_b16 v[20:21], v244 offset:0\n\t" \
    "ds_read_b64_tr_b16 v[22:23], v244 offset:1536\n\t" \
    "ds_read_b64_tr_b16 v[28:29], v244 offset:32\n\t" \
    "ds_read_b64_tr_b16 v[30:31], v244 offset:1568\n\t" \
    "ds_read_b64_tr_b16 v[36:37], v244 offset:64\n\t" \
    "ds_read_b64_tr_b16 v[38:39], v244 offset:1600\n\t" \
    "v_add_u32 v244, 0xc000, v243\n\t" \
    "s_waitcnt vmcnt(9) lgkmcnt(0)\n\t" \
    "s_barrier \n\t" \
    "ds_read_b64_tr_b16 v[232:233], v244 offset:0\n\t" \
    "ds_read_b64_tr_b16 v[234:235], v244 offset:1536\n\t" \
    "ds_read_b64_tr_b16 v[160:161], v244 offset:32\n\t" \
    "ds_read_b64_tr_b16 v[162:163], v244 offset:1568\n\t" \
    "ds_read_b64_tr_b16 v[168:169], v244 offset:64\n\t" \
    "ds_read_b64_tr_b16 v[170:171], v244 offset:1600\n\t" \
    "v_add_u32 v244, 0x12000, v243\n\t" \
    "s_waitcnt vmcnt(6) lgkmcnt(0)\n\t" \
    "s_barrier \n\t" \
    "ds_read_b64_tr_b16 v[236:237], v244 offset:0\n\t" \
    "ds_read_b64_tr_b16 v[238:239], v244 offset:1536\n\t" \
    "ds_read_b64_tr_b16 v[164:165], v244 offset:32\n\t" \
    "ds_read_b64_tr_b16 v[166:167], v244 offset:1568\n\t" \
    "ds_read_b64_tr_b16 v[172:173], v244 offset:64\n\t" \
    "ds_read_b64_tr_b16 v[174:175], v244 offset:1600\n\t" \
    "v_add_u32 v244, 0x18000, v243\n\t" \
    "s_waitcnt vmcnt(3) lgkmcnt(0)\n\t" \
    "s_barrier \n\t" \
    "ds_read_b64_tr_b16 v[176:177], v244 offset:0\n\t" \
    "ds_read_b64_tr_b16 v[178:179], v244 offset:1536\n\t" \
    "ds_read_b64_tr_b16 v[184:185], v244 offset:32\n\t" \
    "ds_read_b64_tr_b16 v[186:187], v244 offset:1568\n\t" \
    "ds_read_b64_tr_b16 v[192:193], v244 offset:64\n\t" \
    "ds_read_b64_tr_b16 v[194:195], v244 offset:1600\n\t" \
    "v_add_u32 v244, 0x1e000, v243\n\t" \
    "s_waitcnt vmcnt(0) lgkmcnt(0)\n\t" \
    "s_barrier \n\t" \
    "ds_read_b64_tr_b16 v[180:181], v244 offset:0\n\t" \
    "ds_read_b64_tr_b16 v[182:183], v244 offset:1536\n\t" \
    "ds_read_b64_tr_b16 v[188:189], v244 offset:32\n\t" \
    "ds_read_b64_tr_b16 v[190:191], v244 offset:1568\n\t" \
    "ds_read_b64_tr_b16 v[196:197], v244 offset:64\n\t" \
    "ds_read_b64_tr_b16 v[198:199], v244 offset:1600\n\t" \
    "s_waitcnt lgkmcnt(0)\n\t" \
    "s_barrier \n\t" \
    LST0 \
    LJIT \
    "ds_write_b128 v254, v[200:203] offset:51200\n\t" \
    "ds_write_b128 v254, v[204:207] offset:52224\n\t" \
    "ds_write_b128 v254, v[208:211] offset:53248\n\t" \
    "ds_write_b128 v254, v[212:215] offset:54272\n\t" \
    "ds_write_b128 v254, v[216:219] offset:55296\n\t" \
    "ds_write_b128 v254, v[220:223] offset:56320\n\t" \
    "ds_write_b128 v254, v[224:227] offset:57344\n\t" \
    "ds_write_b128 v254, v[228:231] offset:58368\n\t" \
    "ds_write_b128 v254, v[232:235] offset:59392\n\t" \
    "ds_write_b128 v254, v[236:239] offset:60416\n\t" \
    "s_waitcnt vmcnt(0) lgkmcnt(0)\n\t" \
    "s_barrier \n\t" \
    "ds_read_b128 v[224:227], v253 offset:0\n\t" \
    "ds_read_b128 v[244:247], v254 offset:53248\n\t" \
    "ds_read_b128 v[228:231], v253 offset:1024\n\t" \
    "ds_read_b128 v[232:235], v253 offset:20480\n\t" \
    "ds_read_b128 v[236:239], v253 offset:21504\n\t" \
    "ds_read_b128 v[248:251], v254 offset:54272\n\t" \
    ".Ls23_%=:\n\t" \
    "s_waitcnt lgkmcnt(5)\n\t" \
    "v_mfma_f32_16x16x32_bf16 v[204:207], v[40:43], v[224:227], 0\n\t" \
    "s_waitcnt lgkmcnt(3)\n\t" \
    "v_mfma_f32_16x16x32_bf16 v[204:207], v[44:47], v[228:231], v[204:207]\n\t" \
    "ds_read_b128 v[240:243], v253 offset:2048\n\t" \
    "v_mfma_f32_16x16x32_bf16 v[208:211], v[48:51], v[224:227], 0\n\t" \
    "v_mfma_f32_16x16x32_bf16 v[208:211], v[52:55], v[228:231], v[208:211]\n\t" \
    "v_mfma_f32_16x16x32_bf16 v[200:203], v[244:247], v[224:227], 0\n\t" \
    "s_waitcnt lgkmcnt(1)\n\t" \
    "v_mfma_f32_16x16x32_bf16 v[200:203], v[248:251], v[228:231], v[200:203]\n\t" \
    "v_mfma_f32_16x16x32_bf16 v[216:219], v[40:43], v[232:235], 0\n\t" \
    "ds_read_b128 v[224:227], v253 offset:3072\n\t" \
    "v_mfma_f32_16x16x32_bf16 v[216:219], v[44:47], v[236:239], v[216:219]\n\t" \
    "ds_read_b128 v[228:231], v253 offset:22528\n\t" \
    "v_mfma_f32_16x16x32_bf16 v[220:223], v[48:51], v[232:235], 0\n\t" \
    "v_mfma_f32_16x16x32_bf16 v[220:223], v[52:55], v[236:239], v[220:223]\n\t" \
    "v_mfma_f32_16x16x32_bf16 v[212:215], v[244:247], v[232:235], 0\n\t" \
    "ds_read_b128 v[244:247], v254 offset:55296\n\t" \
    "v_mfma_f32_16x16x32_bf16 v[212:215], v[248:251], v[236:239], v[212:215]\n\t" \
    "ds_read_b128 v[248:251], v254 offset:56320\n\t" \
    "s_waitcnt lgkmcnt(4)\n\t" \
    "v_mfma_f32_16x16x32_bf16 v[204:207], v[64:67], v[240:243], v[204:207]\n\t" \
    "ds_read_b128 v[232:235], v253 offset:23552\n\t" \
    "s_waitcnt lgkmcnt(4)\n\t" \
    "v_mfma_f32_16x16x32_bf16 v[204:207], v[68:71], v[224:227], v[204:207]\n\t" \
    "ds_read_b128 v[236:239], v253 offset:4096\n\t" \
    "v_mfma_f32_16x16x32_bf16 v[208:211], v[72:75], v[240:243], v[208:211]\n\t" \
    "v_mfma_f32_16x16x32_bf16 v[208:211], v[76:79], v[224:227], v[208:211]\n\t" \
    "v_mfma_f32_16x16x32_bf16 v[200:203], v[56:59], v[240:243], v[200:203]\n\t" \
    "v_mfma_f32_16x16x32_bf16 v[200:203], v[60:63], v[224:227], v[200:203]\n\t" \
    "s_waitcnt lgkmcnt(4)\n\t" \
    "v_mfma_f32_16x16x32_bf16 v[216:219], v[64:67], v[228:231], v[216:219]\n\t" \
    "ds_read_b128 v[240:243], v253 offset:5120\n\t" \
    "s_waitcnt lgkmcnt(2)\n\t" \
    "v_mfma_f32_16x16x32_bf16 v[216:219], v[68:71], v[232:235], v[216:219]\n\t" \
    "ds_read_b128 v[224:227], v253 offset:24576\n\t" \
    "v_mfma_f32_16x16x32_bf16 v[220:223], v[72:75], v[228:231], v[220:223]\n\t" \
    "v_mfma_f32_16x16x32_bf16 v[220:223], v[76:79], v[232:235], v[220:223]\n\t" \
    "v_mfma_f32_16x16x32_bf16 v[212:215], v[56:59], v[228:231], v[212:215]\n\t" \
    "v_mfma_f32_16x16x32_bf16 v[212:215], v[60:63], v[232:235], v[212:215]\n\t" \
    "s_waitcnt lgkmcnt(2)\n\t" \
    "v_mfma_f32_16x16x32_bf16 v[204:207], v[80:83], v[236:239], v[204:207]\n\t" \
    "ds_read_b128 v[228:231], v253 offset:25600\n\t" \
    "s_waitcnt lgkmcnt(2)\n\t" \
    "v_mfma_f32_16x16x32_bf16 v[204:207], v[84:87], v[240:243], v[204:207]\n\t" \
    "ds_read_b128 v[232:235], v253 offset:6144\n\t" \
    "v_mfma_f32_16x16x32_bf16 v[208:211], v[88:91], v[236:239], v[208:211]\n\t" \
    "v_mfma_f32_16x16x32_bf16 v[208:211], v[92:95], v[240:243], v[208:211]\n\t" \
    "v_mfma_f32_16x16x32_bf16 v[200:203], v[244:247], v[236:239], v[200:203]\n\t" \
    "v_mfma_f32_16x16x32_bf16 v[200:203], v[248:251], v[240:243], v[200:203]\n\t" \
    "s_waitcnt lgkmcnt(2)\n\t" \
    "v_mfma_f32_16x16x32_bf16 v[216:219], v[80:83], v[224:227], v[216:219]\n\t" \
    "ds_read_b128 v[236:239], v253 offset:7168\n\t" \
    "s_waitcnt lgkmcnt(2)\n\t" \
    "v_mfma_f32_16x16x32_bf16 v[216:219], v[84:87], v[228:231], v[216:219]\n\t" \
    "ds_read_b128 v[240:243], v253 offset:26624\n\t" \
    "v_mfma_f32_16x16x32_bf16 v[220:223], v[88:91], v[224:227], v[220:223]\n\t" \
    "v_mfma_f32_16x16x32_bf16 v[220:223], v[92:95], v[228:231], v[220:223]\n\t" \
    "v_mfma_f32_16x16x32_bf16 v[212:215], v[244:247], v[224:227], v[212:215]\n\t" \
    "ds_read_b128 v[244:247], v254 offset:51200\n\t" \
    "v_mfma_f32_16x16x32_bf16 v[212:215], v[248:251], v[228:231], v[212:215]\n\t" \
    "ds_read_b128 v[248:251], v254 offset:52224\n\t" \
    "s_waitcnt lgkmcnt(4)\n\t" \
    "v_mfma_f32_16x16x32_bf16 v[204:207], v[104:107], v[232:235], v[204:207]\n\t" \
    "ds_read_b128 v[224:227], v253 offset:27648\n\t" \
    "s_waitcnt lgkmcnt(4)\n\t" \
    "v_mfma_f32_16x16x32_bf16 v[204:207], v[108:111], v[236:239], v[204:207]\n\t" \
    "ds_read_b128 v[228:231], v253 offset:8192\n\t" \
    "v_mfma_f32_16x16x32_bf16 v[208:211], v[112:115], v[232:235], v[208:211]\n\t" \
    "v_mfma_f32_16x16x32_bf16 v[208:211], v[116:119], v[236:239], v[208:211]\n\t" \
    "v_mfma_f32_16x16x32_bf16 v[200:203], v[96:99], v[232:235], v[200:203]\n\t" \
    "v_mfma_f32_16x16x32_bf16 v[200:203], v[100:103], v[236:239], v[200:203]\n\t" \
    "s_waitcnt lgkmcnt(4)\n\t" \
    "v_mfma_f32_16x16x32_bf16 v[216:219], v[104:107], v[240:243], v[216:219]\n\t" \
    "ds_read_b128 v[232:235], v253 offset:9216\n\t" \
    "s_waitcnt lgkmcnt(2)\n\t" \
    "v_mfma_f32_16x16x32_bf16 v[216:219], v[108:111], v[224:227], v[216:219]\n\t" \
    "ds_read_b128 v[236:239], v253 offset:28672\n\t" \
    "v_mfma_f32_16x16x32_bf16 v[220:223], v[112:115], v[240:243], v[220:223]\n\t" \
    "v_mfma_f32_16x16x32_bf16 v[220:223], v[116:119], v[224:227], v[220:223]\n\t" \
    "v_mfma_f32_16x16x32_bf16 v[212:215], v[96:99], v[240:243], v[212:215]\n\t" \
    "v_mfma_f32_16x16x32_bf16 v[212:215], v[100:103], v[224:227], v[212:215]\n\t" \
    "s_waitcnt lgkmcnt(2)\n\t" \
    "v_mfma_f32_16x16x32_bf16 v[204:207], v[0:3], v[228:231], v[204:207]\n\t" \
    "ds_read_b128 v[240:243], v253 offset:29696\n\t" \
    "s_waitcnt lgkmcnt(2)\n\t" \
    "v_mfma_f32_16x16x32_bf16 v[204:207], v[4:7], v[232:235], v[204:207]\n\t" \
    "ds_read_b128 v[224:227], v253 offset:10240\n\t" \
    "v_mfma_f32_16x16x32_bf16 v[208:211], v[8:11], v[228:231], v[208:211]\n\t" \
    "v_mfma_f32_16x16x32_bf16 v[208:211], v[12:15], v[232:235], v[208:211]\n\t" \
    "v_mfma_f32_16x16x32_bf16 v[200:203], v[244:247], v[228:231], v[200:203]\n\t" \
    "v_mfma_f32_16x16x32_bf16 v[200:203], v[248:251], v[232:235], v[200:203]\n\t" \
    "s_waitcnt lgkmcnt(2)\n\t" \
    "v_mfma_f32_16x16x32_bf16 v[216:219], v[0:3], v[236:239], v[216:219]\n\t" \
    "ds_read_b128 v[228:231], v253 offset:11264\n\t" \
    "s_waitcnt lgkmcnt(2)\n\t" \
    "v_mfma_f32_16x16x32_bf16 v[216:219], v[4:7], v[240:243], v[216:219]\n\t" \
    "ds_read_b128 v[232:235], v253 offset:30720\n\t" \
    "v_mfma_f32_16x16x32_bf16 v[220:223], v[8:11], v[236:239], v[220:223]\n\t" \
    "v_mfma_f32_16x16x32_bf16 v[220:223], v[12:15], v[240:243], v[220:223]\n\t" \
    "v_mfma_f32_16x16x32_bf16 v[212:215], v[244:247], v[236:239], v[212:215]\n\t" \
    "ds_read_b128 v[244:247], v254 offset:57344\n\t" \
    "v_mfma_f32_16x16x32_bf16 v[212:215], v[248:251], v[240:243], v[212:215]\n\t" \
    "ds_read_b128 v[248:251], v254 offset:58368\n\t" \
    "ds_read_b128 v[236:239], v253 offset:31744\n\t" \
    "s_setprio 2\n\t" \
    "s_waitcnt lgkmcnt(0)\n\t" \
    "s_barrier \n\t" \
    LST7 \
    LJIT \
    "v_mfma_f32_16x16x32_bf16 v[204:207], v[120:123], v[224:227], v[204:207]\n\t" \
    "v_mfma_f32_16x16x32_bf16 v[204:207], v[124:127], v[228:231], v[204:207]\n\t" \
    "ds_read_b128 v[240:243], v253 offset:12288\n\t" \
    "v_mfma_f32_16x16x32_bf16 v[208:211], v[128:131], v[224:227], v[208:211]\n\t" \
    "v_mfma_f32_16x16x32_bf16 v[208:211], v[132:135], v[228:231], v[208:211]\n\t" \
    "v_mfma_f32_16x16x32_bf16 v[200:203], v[244:247], v[224:227], v[200:203]\n\t" \
    "v_mfma_f32_16x16x32_bf16 v[200:203], v[248:251], v[228:231], v[200:203]\n\t" \
    "v_mfma_f32_16x16x32_bf16 v[216:219], v[120:123], v[232:235], v[216:219]\n\t" \
    "ds_read_b128 v[224:227], v253 offset:13312\n\t" \
    "s_and_b32 s44, s40, 3\n\t" \
    "v_mfma_f32_16x16x32_bf16 v[216:219], v[124:127], v[236:239], v[216:219]\n\t" \
    "ds_read_b128 v[228:231], v253 offset:32768\n\t" \
    "s_lshl_b32 s44, s44, 17\n\t" \
    "v_mfma_f32_16x16x32_bf16 v[220:223], v[128:131], v[232:235], v[220:223]\n\t" \
    "s_add_u32 s44, s44, %[mb]\n\t" \
    "v_mfma_f32_16x16x32_bf16 v[220:223], v[132:135], v[236:239], v[220:223]\n\t" \
    "s_add_u32 s46, s44, 0x1000\n\t" \
    "v_mfma_f32_16x16x32_bf16 v[212:215], v[244:247], v[232:235], v[212:215]\n\t" \
    "ds_read_b128 v[244:247], v254 offset:59392\n\t" \
    "s_add_u32 s47, s44, 0x1c00\n\t" \
    "v_mfma_f32_16x16x32_bf16 v[212:215], v[248:251], v[236:239], v[212:215]\n\t" \
    "ds_read_b128 v[248:251], v254 offset:60416\n\t" \
    "s_waitcnt lgkmcnt(4)\n\t" \
    "v_mfma_f32_16x16x32_bf16 v[204:207], v[144:147], v[240:243], v[204:207]\n\t" \
    "ds_read_b128 v[232:235], v253 offset:33792\n\t" \
    "s_waitcnt lgkmcnt(4)\n\t" \
    "v_mfma_f32_16x16x32_bf16 v[204:207], v[148:151], v[224:227], v[204:207]\n\t" \
    "ds_read_b128 v[236:239], v253 offset:16384\n\t" \
    "v_mfma_f32_16x16x32_bf16 v[208:211], v[152:155], v[240:243], v[208:211]\n\t" \
    "v_mfma_f32_16x16x32_bf16 v[208:211], v[156:159], v[224:227], v[208:211]\n\t" \
    "v_mfma_f32_16x16x32_bf16 v[200:203], v[136:139], v[240:243], v[200:203]\n\t" \
    "v_mfma_f32_16x16x32_bf16 v[200:203], v[140:143], v[224:227], v[200:203]\n\t" \
    "s_waitcnt lgkmcnt(4)\n\t" \
    "v_mfma_f32_16x16x32_bf16 v[216:219], v[144:147], v[228:231], v[216:219]\n\t" \
    "ds_read_b128 v[240:243], v253 offset:17408\n\t" \
    "s_waitcnt lgkmcnt(2)\n\t" \
    "v_mfma_f32_16x16x32_bf16 v[216:219], v[148:151], v[232:235], v[216:219]\n\t" \
    "ds_read_b128 v[224:227], v253 offset:36864\n\t" \
    "v_mfma_f32_16x16x32_bf16 v[220:223], v[152:155], v[228:231], v[220:223]\n\t" \
    "v_mfma_f32_16x16x32_bf16 v[220:223], v[156:159], v[232:235], v[220:223]\n\t" \
    "v_mfma_f32_16x16x32_bf16 v[212:215], v[136:139], v[228:231], v[212:215]\n\t" \
    "v_mfma_f32_16x16x32_bf16 v[212:215], v[140:143], v[232:235], v[212:215]\n\t" \
    "s_waitcnt lgkmcnt(2)\n\t" \
    "v_mfma_f32_16x16x32_bf16 v[204:207], v[160:163], v[236:239], v[204:207]\n\t" \
    "ds_read_b128 v[228:231], v253 offset:37888\n\t" \
    "s_waitcnt lgkmcnt(2)\n\t" \
    "v_mfma_f32_16x16x32_bf16 v[204:207], v[164:167], v[240:243], v[204:207]\n\t" \
    "ds_read_b128 v[232:235], v253 offset:14336\n\t" \
    "v_mfma_f32_16x16x32_bf16 v[208:211], v[168:171], v[236:239], v[208:211]\n\t" \
    "v_mfma_f32_16x16x32_bf16 v[208:211], v[172:175], v[240:243], v[208:211]\n\t" \
    "v_mfma_f32_16x16x32_bf16 v[200:203], v[244:247], v[236:239], v[200:203]\n\t" \
    "v_mfma_f32_16x16x32_bf16 v[200:203], v[248:251], v[240:243], v[200:203]\n\t" \
    "s_waitcnt lgkmcnt(2)\n\t" \
    "v_mfma_f32_16x16x32_bf16 v[216:219], v[160:163], v[224:227], v[216:219]\n\t" \
    "ds_read_b128 v[236:239], v253 offset:15360\n\t" \
    "s_waitcnt lgkmcnt(2)\n\t" \
    "v_mfma_f32_16x16x32_bf16 v[216:219], v[164:167], v[228:231], v[216:219]\n\t" \
    "ds_read_b128 v[240:243], v253 offset:18432\n\t" \
    "v_mfma_f32_16x16x32_bf16 v[220:223], v[168:171], v[224:227], v[220:223]\n\t" \
    "v_mfma_f32_16x16x32_bf16 v[220:223], v[172:175], v[228:231], v[220:223]\n\t" \
    "v_mfma_f32_16x16x32_bf16 v[212:215], v[244:247], v[224:227], v[212:215]\n\t" \
    "v_mfma_f32_16x16x32_bf16 v[212:215], v[248:251], v[228:231], v[212:215]\n\t" \
    "s_waitcnt lgkmcnt(2)\n\t" \
    "v_mfma_f32_16x16x32_bf16 v[204:207], v[24:27], v[232:235], v[204:207]\n\t" \
    "ds_read_b128 v[224:227], v253 offset:19456\n\t" \
    "v_mov_b64 v[244:245], 0\n\t" \
    "s_waitcnt lgkmcnt(2)\n\t" \
    "v_mfma_f32_16x16x32_bf16 v[204:207], v[28:31], v[236:239], v[204:207]\n\t" \
    "ds_read_b128 v[228:231], v253 offset:34816\n\t" \
    "v_mov_b64 v[246:247], 0\n\t" \
    "v_mfma_f32_16x16x32_bf16 v[208:211], v[32:35], v[232:235], v[208:211]\n\t" \
    "v_mov_b64 v[248:249], 0\n\t" \
    "v_mfma_f32_16x16x32_bf16 v[208:211], v[36:39], v[236:239], v[208:211]\n\t" \
    "v_mov_b64 v[250:251], 0\n\t" \
    "v_mfma_f32_16x16x32_bf16 v[200:203], v[16:19], v[232:235], v[200:203]\n\t" \
    "v_mfma_f32_16x16x32_bf16 v[200:203], v[20:23], v[236:239], v[200:203]\n\t" \
    "s_waitcnt lgkmcnt(2)\n\t" \
    "v_mfma_f32_16x16x32_bf16 v[204:207], v[184:187], v[240:243], v[204:207]\n\t" \
    "ds_read_b128 v[232:235], v253 offset:35840\n\t" \
    "s_waitcnt lgkmcnt(2)\n\t" \
    "v_mfma_f32_16x16x32_bf16 v[204:207], v[188:191], v[224:227], v[204:207]\n\t" \
    "ds_read_b128 v[236:239], v253 offset:38912\n\t" \
    "v_mfma_f32_16x16x32_bf16 v[208:211], v[192:195], v[240:243], v[208:211]\n\t" \
    "v_mfma_f32_16x16x32_bf16 v[208:211], v[196:199], v[224:227], v[208:211]\n\t" \
    "v_mfma_f32_16x16x32_bf16 v[200:203], v[176:179], v[240:243], v[200:203]\n\t" \
    "v_mfma_f32_16x16x32_bf16 v[200:203], v[180:183], v[224:227], v[200:203]\n\t" \
    "ds_read_b128 v[240:243], v253 offset:39936\n\t" \
    "s_waitcnt lgkmcnt(3)\n\t" \
    "v_mfma_f32_16x16x32_bf16 v[216:219], v[24:27], v[228:231], v[216:219]\n\t" \
    "s_waitcnt lgkmcnt(2)\n\t" \
    "v_mfma_f32_16x16x32_bf16 v[216:219], v[28:31], v[232:235], v[216:219]\n\t" \
    "v_cvt_pk_bf16_f32 v204, v204, v208\n\t" \
    "v_cvt_pk_bf16_f32 v205, v205, v209\n\t" \
    "s_waitcnt lgkmcnt(1)\n\t" \
    "v_mfma_f32_16x16x32_bf16 v[216:219], v[184:187], v[236:239], v[216:219]\n\t" \
    "v_cvt_pk_bf16_f32 v206, v206, v210\n\t" \
    "v_cvt_pk_bf16_f32 v207, v207, v211\n\t" \
    "s_waitcnt lgkmcnt(0)\n\t" \
    "v_mfma_f32_16x16x32_bf16 v[216:219], v[188:191], v[240:243], v[216:219]\n\t" \
    "ds_write_b128 v254, v[204:207] offset:10240\n\t" \
    "v_mfma_f32_16x16x32_bf16 v[220:223], v[32:35], v[228:231], v[220:223]\n\t" \
    "v_mfma_f32_16x16x32_bf16 v[220:223], v[36:39], v[232:235], v[220:223]\n\t" \
    "v_cvt_pk_bf16_f32 v200, v200, v201\n\t" \
    "v_cvt_pk_bf16_f32 v201, v202, v203\n\t" \
    "v_mfma_f32_16x16x32_bf16 v[220:223], v[192:195], v[236:239], v[220:223]\n\t" \
    "ds_write_b64 v254, v[200:201] offset:6152\n\t" \
    "v_mfma_f32_16x16x32_bf16 v[220:223], v[196:199], v[240:243], v[220:223]\n\t" \
    "v_mfma_f32_16x16x32_bf16 v[212:215], v[16:19], v[228:231], v[212:215]\n\t" \
    "ds_read_b128 v[200:203], v254 offset:51200\n\t" \
    "ds_read_b128 v[204:207], v254 offset:52224\n\t" \
    "v_mfma_f32_16x16x32_bf16 v[212:215], v[20:23], v[232:235], v[212:215]\n\t" \
    "v_mov_b64 v[208:209], 0\n\t" \
    "v_mov_b64 v[210:211], 0\n\t" \
    "v_mfma_f32_16x16x32_bf16 v[212:215], v[176:179], v[236:239], v[212:215]\n\t" \
    "v_cvt_pk_bf16_f32 v216, v216, v220\n\t" \
    "v_cvt_pk_bf16_f32 v217, v217, v221\n\t" \
    "v_mfma_f32_16x16x32_bf16 v[212:215], v[180:183], v[240:243], v[212:215]\n\t" \
    "v_cvt_pk_bf16_f32 v218, v218, v222\n\t" \
    "v_cvt_pk_bf16_f32 v219, v219, v223\n\t" \
    "s_setprio 0\n\t" \
    "ds_write_b128 v254, v[216:219] offset:16384\n\t" \
    "ds_read_b128 v[216:219], v254 offset:53248\n\t" \
    "ds_read_b128 v[220:223], v254 offset:54272\n\t" \
    "s_nop 2\n\t" \
    "v_cvt_pk_bf16_f32 v212, v212, v213\n\t" \
    "v_cvt_pk_bf16_f32 v213, v214, v215\n\t" \
    "ds_write_b64 v254, v[212:213] offset:12296\n\t" \
    LST6 \
    LJIT \
    "s_sleep 3\n\t" \
    "s_mov_b32 m0, 0xb000\n\t" \
    "s_nop 0\n\t" \
    "buffer_load_dwordx4 v253, s[60:63], s46 offen sc1 lds\n\t" \
    "s_mov_b32 m0, 0xbc00\n\t" \
    "s_nop 0\n\t" \
    "buffer_load_dwordx4 v253, s[60:63], s47 offen sc1 lds\n\t" \
    "s_waitcnt lgkmcnt(0)\n\t" \
    LFLUSH212 \
    "v_bfe_i32 v212, v253, 5, 1\n\t" \
    "v_and_b32 v212, 0xaaaa, v212\n\t" \
    "v_or_b32 v212, 0x4444, v212\n\t" \
    "v_and_b32 v213, 0xffff, v255\n\t" \
    "v_lshrrev_b32 v214, 16, v255\n\t" \
    ".Ls24_%=:\n\t" \
    "s_waitcnt vmcnt(0)\n\t" \
    "ds_read_b128 v[224:227], v253 offset:45056\n\t" \
    "ds_read_b128 v[228:231], v253 offset:48128\n\t" \
    "s_waitcnt lgkmcnt(1)\n\t" \
    "v_max_u32 v215, v224, v225\n\t" \
    "v_max3_u32 v215, v226, v227, v215\n\t" \
    "s_waitcnt lgkmcnt(0)\n\t" \
    "v_max3_u32 v215, v228, v229, v215\n\t" \
    "v_max3_u32 v215, v230, v231, v215\n\t" \
    "v_cmp_eq_u32 vcc, -1, v215\n\t" \
    "s_cbranch_vccz .Ls25_%=\n\t" \
    "v_max_u32 v215, v224, v225\n\t" \
    "v_max3_u32 v215, v226, v227, v215\n\t" \
    "v_cmp_eq_u32 vcc, -1, v215\n\t" \
    "s_cbranch_vccz .Ls26_%=\n\t" \
    "s_mov_b32 m0, 0xb000\n\t" \
    "s_nop 0\n\t" \
    "buffer_load_dwordx4 v253, s[60:63], s46 offen sc1 lds\n\t" \
    ".Ls26_%=:\n\t" \
    "v_max_u32 v215, v228, v229\n\t" \
    "v_max3_u32 v215, v230, v231, v215\n\t" \
    "v_cmp_eq_u32 vcc, -1, v215\n\t" \
    "s_cbranch_vccz .Ls27_%=\n\t" \
    "s_mov_b32 m0, 0xbc00\n\t" \
    "s_nop 0\n\t" \
    "buffer_load_dwordx4 v253, s[60:63], s47 offen sc1 lds\n\t" \
    ".Ls27_%=:\n\t" \
    "s_cmp_eq_u32 s41, 0\n\t" \
    "s_cbranch_scc1 .Ls25_%=\n\t" \
    "s_sub_u32 s41, s41, 1\n\t" \
    "s_branch .Ls24_%=\n\t" \
    ".Ls25_%=:\n\t" \
    LST2 \
    LJIT \
    "s_setprio 3\n\t" \
    "s_waitcnt lgkmcnt(0)\n\t" \
    "s_barrier \n\t" \
    LST3 \
    LJIT \
    "ds_read_b128 v[232:235], v253 offset:40960\n\t" \
    "ds_read_b128 v[236:239], v253 offset:41984\n\t" \
    "ds_read_b128 v[240:243], v253 offset:43008\n\t" \
    "v_smfmac_f32_16x16x64_bf16 v[244:247], v[224:227], v[0:7], v212\n\t" \
    "v_smfmac_f32_16x16x64_bf16 v[248:251], v[224:227], v[8:15], v212\n\t" \
    "v_smfmac_f32_16x16x64_bf16 v[208:211], v[224:227], v[200:207], v212\n\t" \
    "ds_read_b128 v[200:203], v254 offset:55296\n\t" \
    "ds_read_b128 v[204:207], v254 offset:56320\n\t" \
    "ds_read_b128 v[224:227], v253 offset:44032\n\t" \
    "v_smfmac_f32_16x16x64_bf16 v[208:211], v[228:231], v[16:23], v212\n\t" \
    "v_smfmac_f32_16x16x64_bf16 v[244:247], v[228:231], v[24:31], v212\n\t" \
    "v_smfmac_f32_16x16x64_bf16 v[248:251], v[228:231], v[32:39], v212\n\t" \
    "ds_read_b128 v[228:231], v253 offset:46080\n\t" \
    "s_waitcnt lgkmcnt(6)\n\t" \
    "v_smfmac_f32_16x16x64_bf16 v[208:211], v[232:235], v[216:223], v212\n\t" \
    "ds_read_b128 v[216:219], v254 offset:57344\n\t" \
    "ds_read_b128 v[220:223], v254 offset:58368\n\t" \
    "v_smfmac_f32_16x16x64_bf16 v[244:247], v[232:235], v[40:47], v212\n\t" \
    "v_smfmac_f32_16x16x64_bf16 v[248:251], v[232:235], v[48:55], v212\n\t" \
    "ds_read_b128 v[232:235], v253 offset:47104\n\t" \
    "s_waitcnt lgkmcnt(8)\n\t" \
    "v_smfmac_f32_16x16x64_bf16 v[208:211], v[236:239], v[56:63], v212\n\t" \
    "v_smfmac_f32_16x16x64_bf16 v[244:247], v[236:239], v[64:71], v212\n\t" \
    "v_smfmac_f32_16x16x64_bf16 v[248:251], v[236:239], v[72:79], v212\n\t" \
    "ds_read_b128 v[236:239], v253 offset:49152\n\t" \
    "s_waitcnt lgkmcnt(6)\n\t" \
    "v_smfmac_f32_16x16x64_bf16 v[208:211], v[240:243], v[200:207], v212\n\t" \
    "ds_read_b128 v[200:203], v254 offset:59392\n\t" \
    "ds_read_b128 v[204:207], v254 offset:60416\n\t" \
    "v_smfmac_f32_16x16x64_bf16 v[244:247], v[240:243], v[80:87], v212\n\t" \
    "v_smfmac_f32_16x16x64_bf16 v[248:251], v[240:243], v[88:95], v212\n\t" \
    "ds_read_b128 v[240:243], v253 offset:50176\n\t" \
    "s_waitcnt lgkmcnt(8)\n\t" \
    "v_smfmac_f32_16x16x64_bf16 v[208:211], v[224:227], v[96:103], v212\n\t" \
    "v_smfmac_f32_16x16x64_bf16 v[244:247], v[224:227], v[104:111], v212\n\t" \
    "v_smfmac_f32_16x16x64_bf16 v[248:251], v[224:227], v[112:119], v212\n\t" \
    "s_waitcnt lgkmcnt(5)\n\t" \
    "v_smfmac_f32_16x16x64_bf16 v[208:211], v[228:231], v[216:223], v212\n\t" \
    "s_waitcnt lgkmcnt(4)\n\t" \
    "v_smfmac_f32_16x16x64_bf16 v[208:211], v[232:235], v[136:143], v212\n\t" \
    "s_waitcnt lgkmcnt(1)\n\t" \
    "v_smfmac_f32_16x16x64_bf16 v[208:211], v[236:239], v[200:207], v212\n\t" \
    "s_waitcnt lgkmcnt(0)\n\t" \
    "v_smfmac_f32_16x16x64_bf16 v[208:211], v[240:243], v[176:183], v212\n\t" \
    "v_smfmac_f32_16x16x64_bf16 v[244:247], v[228:231], v[120:127], v212\n\t" \
    "v_smfmac_f32_16x16x64_bf16 v[244:247], v[232:235], v[144:151], v212\n\t" \
    "v_smfmac_f32_16x16x64_bf16 v[244:247], v[236:239], v[160:167], v212\n\t" \
    "v_add_f32 v208, v208, v210\n\t" \
    "v_add_f32 v209, v209, v211\n\t" \
    "v_cvt_pk_bf16_f32 v208, v208, v209\n\t" \
    "v_smfmac_f32_16x16x64_bf16 v[244:247], v[240:243], v[184:191], v212\n\t" \
    "ds_write_b16 v213, v208 offset:0\n\t" \
    "ds_write_b16_d16_hi v213, v208 offset:16\n\t" \
    "v_smfmac_f32_16x16x64_bf16 v[248:251], v[228:231], v[128:135], v212\n\t" \
    "v_smfmac_f32_16x16x64_bf16 v[248:251], v[232:235], v[152:159], v212\n\t" \
    "v_smfmac_f32_16x16x64_bf16 v[248:251], v[236:239], v[168:175], v212\n\t" \
    "v_add_f32 v244, v244, v246\n\t" \
    "v_add_f32 v245, v245, v247\n\t" \
    "v_smfmac_f32_16x16x64_bf16 v[248:251], v[240:243], v[192:199], v212\n\t" \
    "s_nop 7\n\t" \
    "v_add_f32 v248, v248, v250\n\t" \
    "v_add_f32 v249, v249, v251\n\t" \
    "v_cvt_pk_bf16_f32 v244, v244, v248\n\t" \
    "v_cvt_pk_bf16_f32 v245, v245, v249\n\t" \
    "ds_write2_b32 v214, v244, v245 offset1:4\n\t" \
    "s_setprio 0\n\t" \
    "s_sleep 1\n\t" \
    "ds_read_b128 v[200:203], v254 offset:12288\n\t" \
    "ds_read_b128 v[224:227], v254 offset:24576\n\t" \
    "ds_read_b128 v[204:207], v254 offset:13312\n\t" \
    "ds_read_b128 v[228:231], v254 offset:25600\n\t" \
    "ds_read_b128 v[208:211], v254 offset:14336\n\t" \
    "ds_read_b128 v[232:235], v254 offset:26624\n\t" \
    "ds_read_b128 v[212:215], v254 offset:15360\n\t" \
    "ds_read_b128 v[236:239], v254 offset:27648\n\t" \
    "ds_read_b128 v[216:219], v254 offset:16384\n\t" \
    "ds_read_b128 v[220:223], v254 offset:17408\n\t" \
    "s_and_b32 s44, s40, 3\n\t" \
    "s_lshl_b32 s44, s44, 17\n\t" \
    "s_add_u32 s50, s44, %[pb]\n\t" \
    "s_add_u32 s51, s44, %[pm]\n\t" \
    "s_mov_b32 s52, 0xf0000\n\t" \
    "s_mov_b32 s53, 0xfff00ff\n\t" \
    "s_mov_b32 s54, 0x1f0001\n\t" \
    "s_mov_b32 s55, 0x1fff01ff\n\t" \
    "s_mov_b32 s56, 0x3f0003\n\t" \
    "s_mov_b32 s57, 0x3fff03ff\n\t" \
    "s_mov_b32 s58, 0x7f0007\n\t" \
    "s_mov_b32 s59, 0x7fff07ff\n\t" \
    "s_waitcnt lgkmcnt(8)\n\t" \
    "v_mfma_f32_16x16x32_bf16 v[240:243], v[224:227], v[200:203], 0\n\t" \
    "ds_read_b128 v[224:227], v254 offset:28672\n\t" \
    "s_waitcnt lgkmcnt(7)\n\t" \
    "v_mfma_f32_16x16x32_bf16 v[240:243], v[228:231], v[204:207], v[240:243]\n\t" \
    "ds_read_b128 v[228:231], v254 offset:29696\n\t" \
    "s_waitcnt lgkmcnt(6)\n\t" \
    "v_mfma_f32_16x16x32_bf16 v[240:243], v[232:235], v[208:211], v[240:243]\n\t" \
    "ds_read_b128 v[232:235], v254 offset:18432\n\t" \
    "s_waitcnt lgkmcnt(5)\n\t" \
    "v_mfma_f32_16x16x32_bf16 v[240:243], v[236:239], v[212:215], v[240:243]\n\t" \
    "ds_read_b128 v[236:239], v254 offset:19456\n\t" \
    "v_and_b32 v252, 0xffffff7f, v254\n\t" \
    "s_waitcnt lgkmcnt(0)\n\t" \
    "s_barrier \n\t" \
    LST4 \
    LJIT \
    "v_mfma_f32_16x16x32_bf16 v[240:243], v[224:227], v[216:219], v[240:243]\n\t" \
    "ds_read_b128 v[224:227], v254 offset:20480\n\t" \
    "v_mfma_f32_16x16x32_bf16 v[240:243], v[228:231], v[220:223], v[240:243]\n\t" \
    "ds_read_b128 v[228:231], v254 offset:21504\n\t" \
    "v_mfma_f32_16x16x32_bf16 v[244:247], v[232:235], v[200:203], 0\n\t" \
    "ds_read_b128 v[232:235], v254 offset:22528\n\t" \
    "v_mfma_f32_16x16x32_bf16 v[244:247], v[236:239], v[204:207], v[244:247]\n\t" \
    "ds_read_b128 v[236:239], v254 offset:23552\n\t" \
    "s_waitcnt lgkmcnt(3)\n\t" \
    "v_mfma_f32_16x16x32_bf16 v[244:247], v[224:227], v[208:211], v[244:247]\n\t" \
    "ds_read_b128 v[224:227], v252 offset:3072\n\t" \
    "s_waitcnt lgkmcnt(3)\n\t" \
    "v_mfma_f32_16x16x32_bf16 v[244:247], v[228:231], v[212:215], v[244:247]\n\t" \
    "ds_read_b128 v[228:231], v252 offset:3200\n\t" \
    "s_waitcnt lgkmcnt(3)\n\t" \
    "v_mfma_f32_16x16x32_bf16 v[244:247], v[232:235], v[216:219], v[244:247]\n\t" \
    "ds_read_b128 v[232:235], v252 offset:4096\n\t" \
    "s_waitcnt lgkmcnt(3)\n\t" \
    "v_mfma_f32_16x16x32_bf16 v[244:247], v[236:239], v[220:223], v[244:247]\n\t" \
    "ds_read_b128 v[236:239], v252 offset:4224\n\t" \
    "s_waitcnt lgkmcnt(3)\n\t" \
    "v_mfma_f32_16x16x32_bf16 v[248:251], v[224:227], v[200:203], 0\n\t" \
    "ds_read_b128 v[224:227], v252 offset:5120\n\t" \
    "s_waitcnt lgkmcnt(3)\n\t" \
    "v_mfma_f32_16x16x32_bf16 v[248:251], v[228:231], v[204:207], v[248:251]\n\t" \
    "ds_read_b128 v[228:231], v252 offset:5248\n\t" \
    "s_waitcnt lgkmcnt(3)\n\t" \
    "v_mfma_f32_16x16x32_bf16 v[248:251], v[232:235], v[208:211], v[248:251]\n\t" \
    "s_waitcnt lgkmcnt(2)\n\t" \
    "v_mfma_f32_16x16x32_bf16 v[248:251], v[236:239], v[212:215], v[248:251]\n\t" \
    "s_waitcnt lgkmcnt(1)\n\t" \
    "v_mfma_f32_16x16x32_bf16 v[248:251], v[224:227], v[216:219], v[248:251]\n\t" \
    "s_waitcnt lgkmcnt(0)\n\t" \
    "v_mfma_f32_16x16x32_bf16 v[248:251], v[228:231], v[220:223], v[248:251]\n\t" \
    "v_mov_b32 v226, 0xff7fffff\n\t" \
    "v_cndmask_b32 v240, v240, v226, s[52:53]\n\t" \
    "v_cndmask_b32 v241, v241, v226, s[54:55]\n\t" \
    "v_cndmask_b32 v242, v242, v226, s[56:57]\n\t" \
    "v_cndmask_b32 v243, v243, v226, s[58:59]\n\t" \
    "v_max3_f32 v224, v240, v241, v242\n\t" \
    "v_max3_f32 v225, v244, v245, v246\n\t" \
    "v_max3_f32 v224, v224, v243, v247\n\t" \
    "v_lshrrev_b32 v200, 5, v253\n\t" \
    "v_and_b32 v201, 0xf0, v253\n\t" \
    "v_and_or_b32 v200, v200, 8, v201\n\t" \
    "v_max3_f32 v224, v224, v248, v249\n\t" \
    "v_max3_f32 v225, v225, v250, v251\n\t" \
    "v_max_f32 v224, v224, v225\n\t" \
    "v_mov_b32 v225, v224\n\t" \
    "s_nop 1\n\t" \
    "v_permlane16_swap_b32 v224, v225\n\t" \
    "v_max_f32 v224, v224, v225\n\t" \
    "v_mov_b32 v225, v224\n\t" \
    "s_nop 1\n\t" \
    "v_permlane32_swap_b32 v224, v225\n\t" \
    "v_max_f32 v224, v224, v225\n\t" \
    "v_mul_f32 v224, %[scl], v224\n\t" \
    "v_fma_f32 v240, v240, %[scl], -v224\n\t" \
    "v_fma_f32 v241, v241, %[scl], -v224\n\t" \
    "v_exp_f32 v240, v240\n\t" \
    "v_fma_f32 v242, v242, %[scl], -v224\n\t" \
    "v_exp_f32 v241, v241\n\t" \
    "v_fma_f32 v243, v243, %[scl], -v224\n\t" \
    "v_exp_f32 v242, v242\n\t" \
    "v_fma_f32 v244, v244, %[scl], -v224\n\t" \
    "v_exp_f32 v243, v243\n\t" \
    "v_fma_f32 v245, v245, %[scl], -v224\n\t" \
    "v_exp_f32 v244, v244\n\t" \
    "v_fma_f32 v246, v246, %[scl], -v224\n\t" \
    "v_exp_f32 v245, v245\n\t" \
    "v_fma_f32 v247, v247, %[scl], -v224\n\t" \
    "v_exp_f32 v246, v246\n\t" \
    "v_fma_f32 v248, v248, %[scl], -v224\n\t" \
    "v_exp_f32 v247, v247\n\t" \
    "v_fma_f32 v249, v249, %[scl], -v224\n\t" \
    "v_exp_f32 v248, v248\n\t" \
    "v_fma_f32 v250, v250, %[scl], -v224\n\t" \
    "v_exp_f32 v249, v249\n\t" \
    "v_fma_f32 v251, v251, %[scl], -v224\n\t" \
    "v_exp_f32 v250, v250\n\t" \
    "v_exp_f32 v251, v251\n\t" \
    "v_add_f32 v224, v240, v241\n\t" \
    "v_add_f32 v225, v242, v243\n\t" \
    "v_add_f32 v226, v244, v245\n\t" \
    "v_add_f32 v227, v246, v247\n\t" \
    "v_add_f32 v232, v248, v249\n\t" \
    "v_add_f32 v233, v250, v251\n\t" \
    "v_add_f32 v224, v224, v225\n\t" \
    "v_add_f32 v226, v226, v227\n\t" \
    "v_add_f32 v232, v232, v233\n\t" \
    "v_add_f32 v224, v224, v226\n\t" \
    "v_fma_f32 v224, v232, 0.5, v224\n\t" \
    "v_mov_b32 v225, v224\n\t" \
    "s_nop 1\n\t" \
    "v_permlane16_swap_b32 v224, v225\n\t" \
    "v_add_f32 v224, v224, v225\n\t" \
    "v_mov_b32 v225, v224\n\t" \
    "s_nop 1\n\t" \
    "v_permlane32_swap_b32 v224, v225\n\t" \
    "v_add_f32 v224, v224, v225\n\t" \
    "v_rcp_f32 v224, v224\n\t" \
    "s_nop 0\n\t" \
    "v_mul_f32 v240, v240, v224\n\t" \
    "v_mul_f32 v241, v241, v224\n\t" \
    "v_mul_f32 v242, v242, v224\n\t" \
    "v_mul_f32 v243, v243, v224\n\t" \
    "v_mul_f32 v244, v244, v224\n\t" \
    "v_mul_f32 v245, v245, v224\n\t" \
    "v_mul_f32 v246, v246, v224\n\t" \
    "v_mul_f32 v247, v247, v224\n\t" \
    "v_mul_f32 v248, v248, v224\n\t" \
    "v_mul_f32 v249, v249, v224\n\t" \
    "v_mul_f32 v250, v250, v224\n\t" \
    "v_mul_f32 v251, v251, v224\n\t" \
    "v_cvt_pk_bf16_f32 v204, |v244|, |v240|\n\t" \
    "v_cvt_pk_bf16_f32 v205, |v245|, |v241|\n\t" \
    "v_cvt_pk_bf16_f32 v206, |v246|, |v242|\n\t" \
    "v_cvt_pk_bf16_f32 v207, |v247|, |v243|\n\t" \
    "v_cvt_pk_bf16_f32 v208, |v248|, |v249|\n\t" \
    "v_cvt_pk_bf16_f32 v209, |v250|, |v251|\n\t" \
    "buffer_store_dwordx4 v[204:207], v253, s[60:63], s50 offen sc1\n\t" \
    "s_mov_b32 exec_lo, -1\n\t" \
    "s_mov_b32 exec_hi, 0\n\t" \
    "buffer_store_dwordx2 v[208:209], v200, s[60:63], s51 offen sc1\n\t" \
    "s_mov_b32 exec_hi, -1\n\t" \
    LST5 \
    LJIT \
    "ds_read_b128 v[224:227], v253 offset:0\n\t" \
    "ds_read_b128 v[244:247], v254 offset:53248\n\t" \
    "ds_read_b128 v[228:231], v253 offset:1024\n\t" \
    "ds_read_b128 v[232:235], v253 offset:20480\n\t" \
    "ds_read_b128 v[236:239], v253 offset:21504\n\t" \
    "ds_read_b128 v[248:251], v254 offset:54272\n\t" \
    "s_add_u32 s40, s40, 1\n\t" \
    "s_cmp_lg_u32 s40, 64\n\t" \
    "s_cbranch_scc1 .Ls23_%=\n\t" \
    "s_branch .Lend%=\n\t" \
    ".Ls1_%=:\n\t" \
    "s_add_u32 m0, %[wm], 0x18000\n\t" \
    "s_mov_b32 s44, 0x0\n\t" \
    "buffer_load_dwordx4 v240, %[wr], s44 offen offset:0 nt lds\n\t" \
    "buffer_load_dwordx4 v241, %[wr], s44 offen offset:1024 nt lds\n\t" \
    "buffer_load_dwordx4 v242, %[wr], s44 offen offset:2048 nt lds\n\t" \
    "s_add_u32 m0, %[wm], 0x1e000\n\t" \
    "s_mov_b32 s44, 0x60000\n\t" \
    "buffer_load_dwordx4 v240, %[wr], s44 offen offset:0 nt lds\n\t" \
    "buffer_load_dwordx4 v241, %[wr], s44 offen offset:1024 nt lds\n\t" \
    "buffer_load_dwordx4 v242, %[wr], s44 offen offset:2048 nt lds\n\t" \
    "s_add_u32 m0, %[wm], 0x0\n\t" \
    "s_mov_b32 s44, 0xc0000\n\t" \
    "buffer_load_dwordx4 v240, %[wr], s44 offen offset:0 nt lds\n\t" \
    "buffer_load_dwordx4 v241, %[wr], s44 offen offset:1024 nt lds\n\t" \
    "buffer_load_dwordx4 v242, %[wr], s44 offen offset:2048 nt lds\n\t" \
    "s_add_u32 m0, %[wm], 0x6000\n\t" \
    "s_mov_b32 s44, 0x120000\n\t" \
    "buffer_load_dwordx4 v240, %[wr], s44 offen offset:0 nt lds\n\t" \
    "buffer_load_dwordx4 v241, %[wr], s44 offen offset:1024 nt lds\n\t" \
    "buffer_load_dwordx4 v242, %[wr], s44 offen offset:2048 nt lds\n\t" \
    "s_add_u32 m0, %[wm], 0xc000\n\t" \
    "s_mov_b32 s44, 0x180000\n\t" \
    "buffer_load_dwordx4 v240, %[wr], s44 offen offset:0 nt lds\n\t" \
    "buffer_load_dwordx4 v241, %[wr], s44 offen offset:1024 nt lds\n\t" \
    "buffer_load_dwordx4 v242, %[wr], s44 offen offset:2048 nt lds\n\t" \
    "v_add_u32 v244, 0x18000, v243\n\t" \
    "s_waitcnt vmcnt(12) lgkmcnt(0)\n\t" \
    "s_barrier \n\t" \
    "s_add_u32 m0, %[wm], 0x12000\n\t" \
    "s_mov_b32 s44, 0x1e0000\n\t" \
    "buffer_load_dwordx4 v240, %[wr], s44 offen offset:0 nt lds\n\t" \
    "buffer_load_dwordx4 v241, %[wr], s44 offen offset:1024 nt lds\n\t" \
    "buffer_load_dwordx4 v242, %[wr], s44 offen offset:2048 nt lds\n\t" \
    "ds_read_b64_tr_b16 v[56:57], v244 offset:0\n\t" \
    "ds_read_b64_tr_b16 v[58:59], v244 offset:1536\n\t" \
    "ds_read_b64_tr_b16 v[64:65], v244 offset:32\n\t" \
    "ds_read_b64_tr_b16 v[66:67], v244 offset:1568\n\t" \
    "ds_read_b64_tr_b16 v[72:73], v244 offset:64\n\t" \
    "ds_read_b64_tr_b16 v[74:75], v244 offset:1600\n\t" \
    "v_add_u32 v244, 0x1e000, v243\n\t" \
    "s_waitcnt vmcnt(12) lgkmcnt(0)\n\t" \
    "s_barrier \n\t" \
    "s_add_u32 m0, %[wm], 0x18000\n\t" \
    "s_mov_b32 s44, 0x240000\n\t" \
    "buffer_load_dwordx4 v240, %[wr], s44 offen offset:0 nt lds\n\t" \
    "buffer_load_dwordx4 v241, %[wr], s44 offen offset:1024 nt lds\n\t" \
    "buffer_load_dwordx4 v242, %[wr], s44 offen offset:2048 nt lds\n\t" \
    "ds_read_b64_tr_b16 v[60:61], v244 offset:0\n\t" \
    "ds_read_b64_tr_b16 v[62:63], v244 offset:1536\n\t" \
    "ds_read_b64_tr_b16 v[68:69], v244 offset:32\n\t" \
    "ds_read_b64_tr_b16 v[70:71], v244 offset:1568\n\t" \
    "ds_read_b64_tr_b16 v[76:77], v244 offset:64\n\t" \
    "ds_read_b64_tr_b16 v[78:79], v244 offset:1600\n\t" \
    "v_add_u32 v244, 0x0, v243\n\t" \
    "s_waitcnt vmcnt(12) lgkmcnt(0)\n\t" \
    "s_barrier \n\t" \
    "s_add_u32 m0, %[wm], 0x1e000\n\t" \
    "s_mov_b32 s44, 0x2a0000\n\t" \
    "buffer_load_dwordx4 v240, %[wr], s44 offen offset:0 nt lds\n\t" \
    "buffer_load_dwordx4 v241, %[wr], s44 offen offset:1024 nt lds\n\t" \
    "buffer_load_dwordx4 v242, %[wr], s44 offen offset:2048 nt lds\n\t" \
    "ds_read_b64_tr_b16 v[96:97], v244 offset:0\n\t" \
    "ds_read_b64_tr_b16 v[98:99], v244 offset:1536\n\t" \
    "ds_read_b64_tr_b16 v[104:105], v244 offset:32\n\t" \
    "ds_read_b64_tr_b16 v[106:107], v244 offset:1568\n\t" \
    "ds_read_b64_tr_b16 v[112:113], v244 offset:64\n\t" \
    "ds_read_b64_tr_b16 v[114:115], v244 offset:1600\n\t" \
    "v_add_u32 v244, 0x6000, v243\n\t" \
    "s_waitcnt vmcnt(12) lgkmcnt(0)\n\t" \
    "s_barrier \n\t" \
    "s_add_u32 m0, %[wm], 0x0\n\t" \
    "s_mov_b32 s44, 0x300000\n\t" \
    "buffer_load_dwordx4 v240, %[wr], s44 offen offset:0 nt lds\n\t" \
    "buffer_load_dwordx4 v241, %[wr], s44 offen offset:1024 nt lds\n\t" \
    "buffer_load_dwordx4 v242, %[wr], s44 offen offset:2048 nt lds\n\t" \
    "ds_read_b64_tr_b16 v[100:101], v244 offset:0\n\t" \
    "ds_read_b64_tr_b16 v[102:103], v244 offset:1536\n\t" \
    "ds_read_b64_tr_b16 v[108:109], v244 offset:32\n\t" \
    "ds_read_b64_tr_b16 v[110:111], v244 offset:1568\n\t" \
    "ds_read_b64_tr_b16 v[116:117], v244 offset:64\n\t" \
    "ds_read_b64_tr_b16 v[118:119], v244 offset:1600\n\t" \
    "v_add_u32 v244, 0xc000, v243\n\t" \
    "s_waitcnt vmcnt(12) lgkmcnt(0)\n\t" \
    "s_barrier \n\t" \
    "s_add_u32 m0, %[wm], 0x6000\n\t" \
    "s_mov_b32 s44, 0x360000\n\t" \
    "buffer_load_dwordx4 v240, %[wr], s44 offen offset:0 nt lds\n\t" \
    "buffer_load_dwordx4 v241, %[wr], s44 offen offset:1024 nt lds\n\t" \
    "buffer_load_dwordx4 v242, %[wr], s44 offen offset:2048 nt lds\n\t" \
    "ds_read_b64_tr_b16 v[200:201], v244 offset:0\n\t" \
    "ds_read_b64_tr_b16 v[202:203], v244 offset:1536\n\t" \
    "ds_read_b64_tr_b16 v[0:1], v244 offset:32\n\t" \
    "ds_read_b64_tr_b16 v[2:3], v244 offset:1568\n\t" \
    "ds_read_b64_tr_b16 v[8:9], v244 offset:64\n\t" \
    "ds_read_b64_tr_b16 v[10:11], v244 offset:1600\n\t" \
    "v_add_u32 v244, 0x12000, v243\n\t" \
    "s_waitcnt vmcnt(12) lgkmcnt(0)\n\t" \
    "s_barrier \n\t" \
    "s_add_u32 m0, %[wm], 0xc000\n\t" \
    "s_mov_b32 s44, 0x3c0000\n\t" \
    "buffer_load_dwordx4 v240, %[wr], s44 offen offset:0 nt lds\n\t" \
    "buffer_load_dwordx4 v241, %[wr], s44 offen offset:1024 nt lds\n\t" \
    "buffer_load_dwordx4 v242, %[wr], s44 offen offset:2048 nt lds\n\t" \
    "ds_read_b64_tr_b16 v[204:205], v244 offset:0\n\t" \
    "ds_read_b64_tr_b16 v[206:207], v244 offset:1536\n\t" \
    "ds_read_b64_tr_b16 v[4:5], v244 offset:32\n\t" \
    "ds_read_b64_tr_b16 v[6:7], v244 offset:1568\n\t" \
    "ds_read_b64_tr_b16 v[12:13], v244 offset:64\n\t" \
    "ds_read_b64_tr_b16 v[14:15], v244 offset:1600\n\t" \
    "v_add_u32 v244, 0x18000, v243\n\t" \
    "s_waitcnt vmcnt(12) lgkmcnt(0)\n\t" \
    "s_barrier \n\t" \
    "v_mov_b32 v246, 0\n\t" \
    "buffer_load_dword v246, v246, s[60:63], %[mo] offen sc1\n\t" \
    "s_add_u32 m0, %[wm], 0x12000\n\t" \
    "s_mov_b32 s44, 0x420000\n\t" \
    "buffer_load_dwordx4 v240, %[wr], s44 offen offset:0 nt lds\n\t" \
    "buffer_load_dwordx4 v241, %[wr], s44 offen offset:1024 nt lds\n\t" \
    "buffer_load_dwordx4 v242, %[wr], s44 offen offset:2048 nt lds\n\t" \
    "ds_read_b64_tr_b16 v[208:209], v244 offset:0\n\t" \
    "ds_read_b64_tr_b16 v[210:211], v244 offset:1536\n\t" \
    "ds_read_b64_tr_b16 v[40:41], v244 offset:32\n\t" \
    "ds_read_b64_tr_b16 v[42:43], v244 offset:1568\n\t" \
    "ds_read_b64_tr_b16 v[48:49], v244 offset:64\n\t" \
    "ds_read_b64_tr_b16 v[50:51], v244 offset:1600\n\t" \
    "v_add_u32 v244, 0x1e000, v243\n\t" \
    "s_waitcnt vmcnt(12) lgkmcnt(0)\n\t" \
    "s_barrier \n\t" \
    "s_add_u32 m0, %[wm], 0x18000\n\t" \
    "s_mov_b32 s44, 0x480000\n\t" \
    "buffer_load_dwordx4 v240, %[wr], s44 offen offset:0 nt lds\n\t" \
    "buffer_load_dwordx4 v241, %[wr], s44 offen offset:1024 nt lds\n\t" \
    "buffer_load_dwordx4 v242, %[wr], s44 offen offset:2048 nt lds\n\t" \
    "ds_read_b64_tr_b16 v[212:213], v244 offset:0\n\t" \
    "ds_read_b64_tr_b16 v[214:215], v244 offset:1536\n\t" \
    "ds_read_b64_tr_b16 v[44:45], v244 offset:32\n\t" \
    "ds_read_b64_tr_b16 v[46:47], v244 offset:1568\n\t" \
    "ds_read_b64_tr_b16 v[52:53], v244 offset:64\n\t" \
    "ds_read_b64_tr_b16 v[54:55], v244 offset:1600\n\t" \
    "v_add_u32 v244, 0x0, v243\n\t" \
    "s_waitcnt vmcnt(12) lgkmcnt(0)\n\t" \
    "s_barrier \n\t" \
    "s_add_u32 m0, %[wm], 0x1e000\n\t" \
    "s_mov_b32 s44, 0x4e0000\n\t" \
    "buffer_load_dwordx4 v240, %[wr], s44 offen offset:0 nt lds\n\t" \
    "buffer_load_dwordx4 v241, %[wr], s44 offen offset:1024 nt lds\n\t" \
    "buffer_load_dwordx4 v242, %[wr], s44 offen offset:2048 nt lds\n\t" \
    "ds_read_b64_tr_b16 v[136:137], v244 offset:0\n\t" \
    "ds_read_b64_tr_b16 v[138:139], v244 offset:1536\n\t" \
    "ds_read_b64_tr_b16 v[144:145], v244 offset:32\n\t" \
    "ds_read_b64_tr_b16 v[146:147], v244 offset:1568\n\t" \
    "ds_read_b64_tr_b16 v[152:153], v244 offset:64\n\t" \
    "ds_read_b64_tr_b16 v[154:155], v244 offset:1600\n\t" \
    "v_add_u32 v244, 0x6000, v243\n\t" \
    "s_waitcnt vmcnt(12) lgkmcnt(0)\n\t" \
    "s_barrier \n\t" \
    "s_add_u32 m0, %[wm], 0x0\n\t" \
    "s_mov_b32 s44, 0x540000\n\t" \
    "buffer_load_dwordx4 v240, %[wr], s44 offen offset:0 nt lds\n\t" \
    "buffer_load_dwordx4 v241, %[wr], s44 offen offset:1024 nt lds\n\t" \
    "buffer_load_dwordx4 v242, %[wr], s44 offen offset:2048 nt lds\n\t" \
    "ds_read_b64_tr_b16 v[140:141], v244 offset:0\n\t" \
    "ds_read_b64_tr_b16 v[142:143], v244 offset:1536\n\t" \
    "ds_read_b64_tr_b16 v[148:149], v244 offset:32\n\t" \
    "ds_read_b64_tr_b16 v[150:151], v244 offset:1568\n\t" \
    "ds_read_b64_tr_b16 v[156:157], v244 offset:64\n\t" \
    "ds_read_b64_tr_b16 v[158:159], v244 offset:1600\n\t" \
    "v_add_u32 v244, 0xc000, v243\n\t" \
    "s_waitcnt vmcnt(12) lgkmcnt(0)\n\t" \
    "s_barrier \n\t" \
    "s_add_u32 m0, %[wm], 0x6000\n\t" \
    "s_mov_b32 s44, 0x5a0000\n\t" \
    "buffer_load_dwordx4 v240, %[wr], s44 offen offset:0 nt lds\n\t" \
    "buffer_load_dwordx4 v241, %[wr], s44 offen offset:1024 nt lds\n\t" \
    "buffer_load_dwordx4 v242, %[wr], s44 offen offset:2048 nt lds\n\t" \
    "ds_read_b64_tr_b16 v[216:217], v244 offset:0\n\t" \
    "ds_read_b64_tr_b16 v[218:219], v244 offset:1536\n\t" \
    "ds_read_b64_tr_b16 v[80:81], v244 offset:32\n\t" \
    "ds_read_b64_tr_b16 v[82:83], v244 offset:1568\n\t" \
    "ds_read_b64_tr_b16 v[88:89], v244 offset:64\n\t" \
    "ds_read_b64_tr_b16 v[90:91], v244 offset:1600\n\t" \
    "v_add_u32 v244, 0x12000, v243\n\t" \
    "s_waitcnt vmcnt(12) lgkmcnt(0)\n\t" \
    "s_barrier \n\t" \
    ".Ls11_%=:\n\t" \
    "v_readfirstlane_b32 s43, v246\n\t" \
    "s_nop 0\n\t" \
    "s_cmp_lg_u32 s43, -1\n\t" \
    "s_cbranch_scc1 .Ls12_%=\n\t" \
    "s_cmp_eq_u32 s41, 0\n\t" \
    "s_cbranch_scc1 .Ls12_%=\n\t" \
    "s_sub_u32 s41, s41, 1\n\t" \
    "s_sleep 4\n\t" \
    "v_mov_b32 v246, 0\n\t" \
    "buffer_load_dword v246, v246, s[60:63], %[mo] offen sc1\n\t" \
    "s_waitcnt vmcnt(0)\n\t" \
    "s_branch .Ls11_%=\n\t" \
    ".Ls12_%=:\n\t" \
    "v_mov_b32 v246, 0x8000\n\t" \
    "s_mov_b64 exec, 1\n\t" \
    "buffer_store_dword v246, v246, s[60:63], %[mo] offen sc1\n\t" \
    "s_mov_b64 exec, -1\n\t" \
    "s_and_b32 s43, s43, 0x2000\n\t" \
    "s_xor_b32 s43, s43, %[lx]\n\t" \
    "s_add_u32 s60, s60, s43\n\t" \
    "s_addc_u32 s61, s61, 0\n\t" \
    "s_add_u32 m0, %[wm], 0xc000\n\t" \
    "s_mov_b32 s44, 0x600000\n\t" \
    "buffer_load_dwordx4 v240, %[wr], s44 offen offset:0 nt lds\n\t" \
    "buffer_load_dwordx4 v241, %[wr], s44 offen offset:1024 nt lds\n\t" \
    "buffer_load_dwordx4 v242, %[wr], s44 offen offset:2048 nt lds\n\t" \
    "ds_read_b64_tr_b16 v[220:221], v244 offset:0\n\t" \
    "ds_read_b64_tr_b16 v[222:223], v244 offset:1536\n\t" \
    "ds_read_b64_tr_b16 v[84:85], v244 offset:32\n\t" \
    "ds_read_b64_tr_b16 v[86:87], v244 offset:1568\n\t" \
    "ds_read_b64_tr_b16 v[92:93], v244 offset:64\n\t" \
    "ds_read_b64_tr_b16 v[94:95], v244 offset:1600\n\t" \
    "v_add_u32 v244, 0x18000, v243\n\t" \
    "s_waitcnt vmcnt(12) lgkmcnt(0)\n\t" \
    "s_barrier \n\t" \
    "s_add_u32 m0, %[wm], 0x12000\n\t" \
    "s_mov_b32 s44, 0x660000\n\t" \
    "buffer_load_dwordx4 v240, %[wr], s44 offen offset:0 nt lds\n\t" \
    "buffer_load_dwordx4 v241, %[wr], s44 offen offset:1024 nt lds\n\t" \
    "buffer_load_dwordx4 v242, %[wr], s44 offen offset:2048 nt lds\n\t" \
    "ds_read_b64_tr_b16 v[16:17], v244 offset:0\n\t" \
    "ds_read_b64_tr_b16 v[18:19], v244 offset:1536\n\t" \
    "ds_read_b64_tr_b16 v[24:25], v244 offset:32\n\t" \
    "ds_read_b64_tr_b16 v[26:27], v244 offset:1568\n\t" \
    "ds_read_b64_tr_b16 v[32:33], v244 offset:64\n\t" \
    "ds_read_b64_tr_b16 v[34:35], v244 offset:1600\n\t" \
    "v_add_u32 v244, 0x1e000, v243\n\t" \
    "s_waitcnt vmcnt(12) lgkmcnt(0)\n\t" \
    "s_barrier \n\t" \
    "s_add_u32 m0, %[wm], 0x18000\n\t" \
    "s_mov_b32 s44, 0x6c0000\n\t" \
    "buffer_load_dwordx4 v240, %[wr], s44 offen offset:0 nt lds\n\t" \
    "buffer_load_dwordx4 v241, %[wr], s44 offen offset:1024 nt lds\n\t" \
    "buffer_load_dwordx4 v242, %[wr], s44 offen offset:2048 nt lds\n\t" \
    "ds_read_b64_tr_b16 v[20:21], v244 offset:0\n\t" \
    "ds_read_b64_tr_b16 v[22:23], v244 offset:1536\n\t" \
    "ds_read_b64_tr_b16 v[28:29], v244 offset:32\n\t" \
    "ds_read_b64_tr_b16 v[30:31], v244 offset:1568\n\t" \
    "ds_read_b64_tr_b16 v[36:37], v244 offset:64\n\t" \
    "ds_read_b64_tr_b16 v[38:39], v244 offset:1600\n\t" \
    "v_add_u32 v244, 0x0, v243\n\t" \
    "s_waitcnt vmcnt(12) lgkmcnt(0)\n\t" \
    "s_barrier \n\t" \
    "s_add_u32 m0, %[wm], 0x1e000\n\t" \
    "s_mov_b32 s44, 0x720000\n\t" \
    "buffer_load_dwordx4 v240, %[wr], s44 offen offset:0 nt lds\n\t" \
    "buffer_load_dwordx4 v241, %[wr], s44 offen offset:1024 nt lds\n\t" \
    "buffer_load_dwordx4 v242, %[wr], s44 offen offset:2048 nt lds\n\t" \
    "ds_read_b64_tr_b16 v[224:225], v244 offset:0\n\t" \
    "ds_read_b64_tr_b16 v[226:227], v244 offset:1536\n\t" \
    "ds_read_b64_tr_b16 v[120:121], v244 offset:32\n\t" \
    "ds_read_b64_tr_b16 v[122:123], v244 offset:1568\n\t" \
    "ds_read_b64_tr_b16 v[128:129], v244 offset:64\n\t" \
    "ds_read_b64_tr_b16 v[130:131], v244 offset:1600\n\t" \
    "v_add_u32 v244, 0x6000, v243\n\t" \
    "s_waitcnt vmcnt(12) lgkmcnt(0)\n\t" \
    "s_barrier \n\t" \
    "ds_read_b64_tr_b16 v[228:229], v244 offset:0\n\t" \
    "ds_read_b64_tr_b16 v[230:231], v244 offset:1536\n\t" \
    "ds_read_b64_tr_b16 v[124:125], v244 offset:32\n\t" \
    "ds_read_b64_tr_b16 v[126:127], v244 offset:1568\n\t" \
    "ds_read_b64_tr_b16 v[132:133], v244 offset:64\n\t" \
    "ds_read_b64_tr_b16 v[134:135], v244 offset:1600\n\t" \
    "v_add_u32 v244, 0xc000, v243\n\t" \
    "s_waitcnt vmcnt(9) lgkmcnt(0)\n\t" \
    "s_barrier \n\t" \
    "ds_read_b64_tr_b16 v[232:233], v244 offset:0\n\t" \
    "ds_read_b64_tr_b16 v[234:235], v244 offset:1536\n\t" \
    "ds_read_b64_tr_b16 v[160:161], v244 offset:32\n\t" \
    "ds_read_b64_tr_b16 v[162:163], v244 offset:1568\n\t" \
    "ds_read_b64_tr_b16 v[168:169], v244 offset:64\n\t" \
    "ds_read_b64_tr_b16 v[170:171], v244 offset:1600\n\t" \
    "v_add_u32 v244, 0x12000, v243\n\t" \
    "s_waitcnt vmcnt(6) lgkmcnt(0)\n\t" \
    "s_barrier \n\t" \
    "ds_read_b64_tr_b16 v[236:237], v244 offset:0\n\t" \
    "ds_read_b64_tr_b16 v[238:239], v244 offset:1536\n\t" \
    "ds_read_b64_tr_b16 v[164:165], v244 offset:32\n\t" \
    "ds_read_b64_tr_b16 v[166:167], v244 offset:1568\n\t" \
    "ds_read_b64_tr_b16 v[172:173], v244 offset:64\n\t" \
    "ds_read_b64_tr_b16 v[174:175], v244 offset:1600\n\t" \
    "v_add_u32 v244, 0x18000, v243\n\t" \
    "s_waitcnt vmcnt(3) lgkmcnt(0)\n\t" \
    "s_barrier \n\t" \
    "ds_read_b64_tr_b16 v[176:177], v244 offset:0\n\t" \
    "ds_read_b64_tr_b16 v[178:179], v244 offset:1536\n\t" \
    "ds_read_b64_tr_b16 v[184:185], v244 offset:32\n\t" \
    "ds_read_b64_tr_b16 v[186:187], v244 offset:1568\n\t" \
    "ds_read_b64_tr_b16 v[192:193], v244 offset:64\n\t" \
    "ds_read_b64_tr_b16 v[194:195], v244 offset:1600\n\t" \
    "v_add_u32 v244, 0x1e000, v243\n\t" \
    "s_waitcnt vmcnt(0) lgkmcnt(0)\n\t" \
    "s_barrier \n\t" \
    "ds_read_b64_tr_b16 v[180:181], v244 offset:0\n\t" \
    "ds_read_b64_tr_b16 v[182:183], v244 offset:1536\n\t" \
    "ds_read_b64_tr_b16 v[188:189], v244 offset:32\n\t" \
    "ds_read_b64_tr_b16 v[190:191], v244 offset:1568\n\t" \
    "ds_read_b64_tr_b16 v[196:197], v244 offset:64\n\t" \
    "ds_read_b64_tr_b16 v[198:199], v244 offset:1600\n\t" \
    "s_waitcnt lgkmcnt(0)\n\t" \
    "s_barrier \n\t" \
    LST0 \
    LJIT \
    "ds_write_b128 v254, v[200:203] offset:40960\n\t" \
    "ds_write_b128 v254, v[204:207] offset:41984\n\t" \
    "ds_write_b128 v254, v[208:211] offset:43008\n\t" \
    "ds_write_b128 v254, v[212:215] offset:44032\n\t" \
    "ds_write_b128 v254, v[216:219] offset:45056\n\t" \
    "ds_write_b128 v254, v[220:223] offset:46080\n\t" \
    "ds_write_b128 v254, v[224:227] offset:47104\n\t" \
    "ds_write_b128 v254, v[228:231] offset:48128\n\t" \
    "ds_write_b128 v254, v[232:235] offset:49152\n\t" \
    "ds_write_b128 v254, v[236:239] offset:50176\n\t" \
    "s_waitcnt vmcnt(0) lgkmcnt(0)\n\t" \
    "s_barrier \n\t" \
    "ds_read_b128 v[224:227], v253 offset:0\n\t" \
    "ds_read_b128 v[244:247], v254 offset:40960\n\t" \
    "ds_read_b128 v[228:231], v253 offset:1024\n\t" \
    "ds_read_b128 v[232:235], v253 offset:20480\n\t" \
    "ds_read_b128 v[236:239], v253 offset:21504\n\t" \
    "ds_read_b128 v[248:251], v254 offset:41984\n\t" \
    ".Ls13_%=:\n\t" \
    "s_waitcnt lgkmcnt(5)\n\t" \
    "v_mfma_f32_16x16x32_bf16 v[204:207], v[64:67], v[224:227], 0\n\t" \
    "s_waitcnt lgkmcnt(3)\n\t" \
    "v_mfma_f32_16x16x32_bf16 v[204:207], v[68:71], v[228:231], v[204:207]\n\t" \
    "ds_read_b128 v[240:243], v253 offset:2048\n\t" \
    "v_mfma_f32_16x16x32_bf16 v[208:211], v[72:75], v[224:227], 0\n\t" \
    "v_mfma_f32_16x16x32_bf16 v[208:211], v[76:79], v[228:231], v[208:211]\n\t" \
    "v_mfma_f32_16x16x32_bf16 v[200:203], v[56:59], v[224:227], 0\n\t" \
    "v_mfma_f32_16x16x32_bf16 v[200:203], v[60:63], v[228:231], v[200:203]\n\t" \
    "s_waitcnt lgkmcnt(3)\n\t" \
    "v_mfma_f32_16x16x32_bf16 v[216:219], v[64:67], v[232:235], 0\n\t" \
    "ds_read_b128 v[224:227], v253 offset:3072\n\t" \
    "s_waitcnt lgkmcnt(3)\n\t" \
    "v_mfma_f32_16x16x32_bf16 v[216:219], v[68:71], v[236:239], v[216:219]\n\t" \
    "ds_read_b128 v[228:231], v253 offset:22528\n\t" \
    "v_mfma_f32_16x16x32_bf16 v[220:223], v[72:75], v[232:235], 0\n\t" \
    "v_mfma_f32_16x16x32_bf16 v[220:223], v[76:79], v[236:239], v[220:223]\n\t" \
    "v_mfma_f32_16x16x32_bf16 v[212:215], v[56:59], v[232:235], 0\n\t" \
    "v_mfma_f32_16x16x32_bf16 v[212:215], v[60:63], v[236:239], v[212:215]\n\t" \
    "s_waitcnt lgkmcnt(2)\n\t" \
    "v_mfma_f32_16x16x32_bf16 v[204:207], v[104:107], v[240:243], v[204:207]\n\t" \
    "ds_read_b128 v[232:235], v253 offset:23552\n\t" \
    "s_waitcnt lgkmcnt(2)\n\t" \
    "v_mfma_f32_16x16x32_bf16 v[204:207], v[108:111], v[224:227], v[204:207]\n\t" \
    "ds_read_b128 v[236:239], v253 offset:4096\n\t" \
    "v_mfma_f32_16x16x32_bf16 v[208:211], v[112:115], v[240:243], v[208:211]\n\t" \
    "v_mfma_f32_16x16x32_bf16 v[208:211], v[116:119], v[224:227], v[208:211]\n\t" \
    "v_mfma_f32_16x16x32_bf16 v[200:203], v[96:99], v[240:243], v[200:203]\n\t" \
    "v_mfma_f32_16x16x32_bf16 v[200:203], v[100:103], v[224:227], v[200:203]\n\t" \
    "s_waitcnt lgkmcnt(2)\n\t" \
    "v_mfma_f32_16x16x32_bf16 v[216:219], v[104:107], v[228:231], v[216:219]\n\t" \
    "ds_read_b128 v[240:243], v253 offset:5120\n\t" \
    "s_waitcnt lgkmcnt(2)\n\t" \
    "v_mfma_f32_16x16x32_bf16 v[216:219], v[108:111], v[232:235], v[216:219]\n\t" \
    "ds_read_b128 v[224:227], v253 offset:24576\n\t" \
    "v_mfma_f32_16x16x32_bf16 v[220:223], v[112:115], v[228:231], v[220:223]\n\t" \
    "v_mfma_f32_16x16x32_bf16 v[220:223], v[116:119], v[232:235], v[220:223]\n\t" \
    "v_mfma_f32_16x16x32_bf16 v[212:215], v[96:99], v[228:231], v[212:215]\n\t" \
    "v_mfma_f32_16x16x32_bf16 v[212:215], v[100:103], v[232:235], v[212:215]\n\t" \
    "s_waitcnt lgkmcnt(2)\n\t" \
    "v_mfma_f32_16x16x32_bf16 v[204:207], v[0:3], v[236:239], v[204:207]\n\t" \
    "ds_read_b128 v[228:231], v253 offset:25600\n\t" \
    "s_waitcnt lgkmcnt(2)\n\t" \
    "v_mfma_f32_16x16x32_bf16 v[204:207], v[4:7], v[240:243], v[204:207]\n\t" \
    "ds_read_b128 v[232:235], v253 offset:6144\n\t" \
    "v_mfma_f32_16x16x32_bf16 v[208:211], v[8:11], v[236:239], v[208:211]\n\t" \
    "v_mfma_f32_16x16x32_bf16 v[208:211], v[12:15], v[240:243], v[208:211]\n\t" \
    "v_mfma_f32_16x16x32_bf16 v[200:203], v[244:247], v[236:239], v[200:203]\n\t" \
    "v_mfma_f32_16x16x32_bf16 v[200:203], v[248:251], v[240:243], v[200:203]\n\t" \
    "s_waitcnt lgkmcnt(2)\n\t" \
    "v_mfma_f32_16x16x32_bf16 v[216:219], v[0:3], v[224:227], v[216:219]\n\t" \
    "ds_read_b128 v[236:239], v253 offset:7168\n\t" \
    "s_waitcnt lgkmcnt(2)\n\t" \
    "v_mfma_f32_16x16x32_bf16 v[216:219], v[4:7], v[228:231], v[216:219]\n\t" \
    "ds_read_b128 v[240:243], v253 offset:26624\n\t" \
    "v_mfma_f32_16x16x32_bf16 v[220:223], v[8:11], v[224:227], v[220:223]\n\t" \
    "v_mfma_f32_16x16x32_bf16 v[220:223], v[12:15], v[228:231], v[220:223]\n\t" \
    "v_mfma_f32_16x16x32_bf16 v[212:215], v[244:247], v[224:227], v[212:215]\n\t" \
    "ds_read_b128 v[244:247], v254 offset:43008\n\t" \
    "v_mfma_f32_16x16x32_bf16 v[212:215], v[248:251], v[228:231], v[212:215]\n\t" \
    "ds_read_b128 v[248:251], v254 offset:44032\n\t" \
    "s_waitcnt lgkmcnt(4)\n\t" \
    "v_mfma_f32_16x16x32_bf16 v[204:207], v[40:43], v[232:235], v[204:207]\n\t" \
    "ds_read_b128 v[224:227], v253 offset:27648\n\t" \
    "s_waitcnt lgkmcnt(4)\n\t" \
    "v_mfma_f32_16x16x32_bf16 v[204:207], v[44:47], v[236:239], v[204:207]\n\t" \
    "ds_read_b128 v[228:231], v253 offset:8192\n\t" \
    "v_mfma_f32_16x16x32_bf16 v[208:211], v[48:51], v[232:235], v[208:211]\n\t" \
    "v_mfma_f32_16x16x32_bf16 v[208:211], v[52:55], v[236:239], v[208:211]\n\t" \
    "s_waitcnt lgkmcnt(3)\n\t" \
    "v_mfma_f32_16x16x32_bf16 v[200:203], v[244:247], v[232:235], v[200:203]\n\t" \
    "s_waitcnt lgkmcnt(2)\n\t" \
    "v_mfma_f32_16x16x32_bf16 v[200:203], v[248:251], v[236:239], v[200:203]\n\t" \
    "v_mfma_f32_16x16x32_bf16 v[216:219], v[40:43], v[240:243], v[216:219]\n\t" \
    "ds_read_b128 v[232:235], v253 offset:9216\n\t" \
    "s_waitcnt lgkmcnt(2)\n\t" \
    "v_mfma_f32_16x16x32_bf16 v[216:219], v[44:47], v[224:227], v[216:219]\n\t" \
    "ds_read_b128 v[236:239], v253 offset:28672\n\t" \
    "v_mfma_f32_16x16x32_bf16 v[220:223], v[48:51], v[240:243], v[220:223]\n\t" \
    "v_mfma_f32_16x16x32_bf16 v[220:223], v[52:55], v[224:227], v[220:223]\n\t" \
    "v_mfma_f32_16x16x32_bf16 v[212:215], v[244:247], v[240:243], v[212:215]\n\t" \
    "ds_read_b128 v[244:247], v254 offset:45056\n\t" \
    "v_mfma_f32_16x16x32_bf16 v[212:215], v[248:251], v[224:227], v[212:215]\n\t" \
    "ds_read_b128 v[248:251], v254 offset:46080\n\t" \
    "s_waitcnt lgkmcnt(4)\n\t" \
    "v_mfma_f32_16x16x32_bf16 v[204:207], v[144:147], v[228:231], v[204:207]\n\t" \
    "ds_read_b128 v[240:243], v253 offset:29696\n\t" \
    "s_waitcnt lgkmcnt(4)\n\t" \
    "v_mfma_f32_16x16x32_bf16 v[204:207], v[148:151], v[232:235], v[204:207]\n\t" \
    "ds_read_b128 v[224:227], v253 offset:10240\n\t" \
    "v_mfma_f32_16x16x32_bf16 v[208:211], v[152:155], v[228:231], v[208:211]\n\t" \
    "v_mfma_f32_16x16x32_bf16 v[208:211], v[156:159], v[232:235], v[208:211]\n\t" \
    "v_mfma_f32_16x16x32_bf16 v[200:203], v[136:139], v[228:231], v[200:203]\n\t" \
    "v_mfma_f32_16x16x32_bf16 v[200:203], v[140:143], v[232:235], v[200:203]\n\t" \
    "s_waitcnt lgkmcnt(4)\n\t" \
    "v_mfma_f32_16x16x32_bf16 v[216:219], v[144:147], v[236:239], v[216:219]\n\t" \
    "ds_read_b128 v[228:231], v253 offset:11264\n\t" \
    "s_waitcnt lgkmcnt(2)\n\t" \
    "v_mfma_f32_16x16x32_bf16 v[216:219], v[148:151], v[240:243], v[216:219]\n\t" \
    "ds_read_b128 v[232:235], v253 offset:30720\n\t" \
    "v_mfma_f32_16x16x32_bf16 v[220:223], v[152:155], v[236:239], v[220:223]\n\t" \
    "v_mfma_f32_16x16x32_bf16 v[220:223], v[156:159], v[240:243], v[220:223]\n\t" \
    "v_mfma_f32_16x16x32_bf16 v[212:215], v[136:139], v[236:239], v[212:215]\n\t" \
    "v_mfma_f32_16x16x32_bf16 v[212:215], v[140:143], v[240:243], v[212:215]\n\t" \
    "ds_read_b128 v[236:239], v253 offset:31744\n\t" \
    "s_setprio 2\n\t" \
    "s_waitcnt lgkmcnt(0)\n\t" \
    "s_barrier \n\t" \
    LST7 \
    LJIT \
    "v_mfma_f32_16x16x32_bf16 v[204:207], v[80:83], v[224:227], v[204:207]\n\t" \
    "v_mfma_f32_16x16x32_bf16 v[204:207], v[84:87], v[228:231], v[204:207]\n\t" \
    "ds_read_b128 v[240:243], v253 offset:14336\n\t" \
    "v_mfma_f32_16x16x32_bf16 v[208:211], v[88:91], v[224:227], v[208:211]\n\t" \
    "v_mfma_f32_16x16x32_bf16 v[208:211], v[92:95], v[228:231], v[208:211]\n\t" \
    "v_mfma_f32_16x16x32_bf16 v[200:203], v[244:247], v[224:227], v[200:203]\n\t" \
    "v_mfma_f32_16x16x32_bf16 v[200:203], v[248:251], v[228:231], v[200:203]\n\t" \
    "v_mfma_f32_16x16x32_bf16 v[216:219], v[80:83], v[232:235], v[216:219]\n\t" \
    "ds_read_b128 v[224:227], v253 offset:15360\n\t" \
    "s_and_b32 s44, s40, 3\n\t" \
    "v_mfma_f32_16x16x32_bf16 v[216:219], v[84:87], v[236:239], v[216:219]\n\t" \
    "ds_read_b128 v[228:231], v253 offset:34816\n\t" \
    "s_lshl_b32 s44, s44, 17\n\t" \
    "v_mfma_f32_16x16x32_bf16 v[220:223], v[88:91], v[232:235], v[220:223]\n\t" \
    "s_add_u32 s44, s44, %[mb]\n\t" \
    "v_mfma_f32_16x16x32_bf16 v[220:223], v[92:95], v[236:239], v[220:223]\n\t" \
    "s_add_u32 s46, s44, 0x800\n\t" \
    "v_mfma_f32_16x16x32_bf16 v[212:215], v[244:247], v[232:235], v[212:215]\n\t" \
    "ds_read_b128 v[244:247], v254 offset:47104\n\t" \
    "s_add_u32 s47, s44, 0x1800\n\t" \
    "v_mfma_f32_16x16x32_bf16 v[212:215], v[248:251], v[236:239], v[212:215]\n\t" \
    "ds_read_b128 v[248:251], v254 offset:48128\n\t" \
    "s_add_u32 s48, s44, 0xc00\n\t" \
    "s_waitcnt lgkmcnt(4)\n\t" \
    "v_mfma_f32_16x16x32_bf16 v[204:207], v[120:123], v[240:243], v[204:207]\n\t" \
    "ds_read_b128 v[232:235], v253 offset:35840\n\t" \
    "s_waitcnt lgkmcnt(4)\n\t" \
    "v_mfma_f32_16x16x32_bf16 v[204:207], v[124:127], v[224:227], v[204:207]\n\t" \
    "ds_read_b128 v[236:239], v253 offset:16384\n\t" \
    "v_mfma_f32_16x16x32_bf16 v[208:211], v[128:131], v[240:243], v[208:211]\n\t" \
    "v_mfma_f32_16x16x32_bf16 v[208:211], v[132:135], v[224:227], v[208:211]\n\t" \
    "s_waitcnt lgkmcnt(3)\n\t" \
    "v_mfma_f32_16x16x32_bf16 v[200:203], v[244:247], v[240:243], v[200:203]\n\t" \
    "s_waitcnt lgkmcnt(2)\n\t" \
    "v_mfma_f32_16x16x32_bf16 v[200:203], v[248:251], v[224:227], v[200:203]\n\t" \
    "v_mfma_f32_16x16x32_bf16 v[216:219], v[120:123], v[228:231], v[216:219]\n\t" \
    "ds_read_b128 v[240:243], v253 offset:17408\n\t" \
    "s_waitcnt lgkmcnt(2)\n\t" \
    "v_mfma_f32_16x16x32_bf16 v[216:219], v[124:127], v[232:235], v[216:219]\n\t" \
    "ds_read_b128 v[224:227], v253 offset:36864\n\t" \
    "v_mfma_f32_16x16x32_bf16 v[220:223], v[128:131], v[228:231], v[220:223]\n\t" \
    "v_mfma_f32_16x16x32_bf16 v[220:223], v[132:135], v[232:235], v[220:223]\n\t" \
    "v_mfma_f32_16x16x32_bf16 v[212:215], v[244:247], v[228:231], v[212:215]\n\t" \
    "ds_read_b128 v[244:247], v254 offset:49152\n\t" \
    "v_mfma_f32_16x16x32_bf16 v[212:215], v[248:251], v[232:235], v[212:215]\n\t" \
    "ds_read_b128 v[248:251], v254 offset:50176\n\t" \
    "s_waitcnt lgkmcnt(4)\n\t" \
    "v_mfma_f32_16x16x32_bf16 v[204:207], v[160:163], v[236:239], v[204:207]\n\t" \
    "ds_read_b128 v[228:231], v253 offset:37888\n\t" \
    "s_waitcnt lgkmcnt(4)\n\t" \
    "v_mfma_f32_16x16x32_bf16 v[204:207], v[164:167], v[240:243], v[204:207]\n\t" \
    "ds_read_b128 v[232:235], v253 offset:12288\n\t" \
    "v_mfma_f32_16x16x32_bf16 v[208:211], v[168:171], v[236:239], v[208:211]\n\t" \
    "v_mfma_f32_16x16x32_bf16 v[208:211], v[172:175], v[240:243], v[208:211]\n\t" \
    "s_waitcnt lgkmcnt(3)\n\t" \
    "v_mfma_f32_16x16x32_bf16 v[200:203], v[244:247], v[236:239], v[200:203]\n\t" \
    "s_waitcnt lgkmcnt(2)\n\t" \
    "v_mfma_f32_16x16x32_bf16 v[200:203], v[248:251], v[240:243], v[200:203]\n\t" \
    "v_mfma_f32_16x16x32_bf16 v[216:219], v[160:163], v[224:227], v[216:219]\n\t" \
    "ds_read_b128 v[236:239], v253 offset:13312\n\t" \
    "s_waitcnt lgkmcnt(2)\n\t" \
    "v_mfma_f32_16x16x32_bf16 v[216:219], v[164:167], v[228:231], v[216:219]\n\t" \
    "ds_read_b128 v[240:243], v253 offset:18432\n\t" \
    "v_mfma_f32_16x16x32_bf16 v[220:223], v[168:171], v[224:227], v[220:223]\n\t" \
    "v_mfma_f32_16x16x32_bf16 v[220:223], v[172:175], v[228:231], v[220:223]\n\t" \
    "v_mfma_f32_16x16x32_bf16 v[212:215], v[244:247], v[224:227], v[212:215]\n\t" \
    "v_mfma_f32_16x16x32_bf16 v[212:215], v[248:251], v[228:231], v[212:215]\n\t" \
    "s_waitcnt lgkmcnt(2)\n\t" \
    "v_mfma_f32_16x16x32_bf16 v[204:207], v[24:27], v[232:235], v[204:207]\n\t" \
    "ds_read_b128 v[224:227], v253 offset:19456\n\t" \
    "v_mov_b64 v[244:245], 0\n\t" \
    "s_waitcnt lgkmcnt(2)\n\t" \
    "v_mfma_f32_16x16x32_bf16 v[204:207], v[28:31], v[236:239], v[204:207]\n\t" \
    "ds_read_b128 v[228:231], v253 offset:32768\n\t" \
    "v_mov_b64 v[246:247], 0\n\t" \
    "v_mfma_f32_16x16x32_bf16 v[208:211], v[32:35], v[232:235], v[208:211]\n\t" \
    "v_mov_b64 v[248:249], 0\n\t" \
    "v_mfma_f32_16x16x32_bf16 v[208:211], v[36:39], v[236:239], v[208:211]\n\t" \
    "v_mov_b64 v[250:251], 0\n\t" \
    "v_mfma_f32_16x16x32_bf16 v[200:203], v[16:19], v[232:235], v[200:203]\n\t" \
    "v_mfma_f32_16x16x32_bf16 v[200:203], v[20:23], v[236:239], v[200:203]\n\t" \
    "s_waitcnt lgkmcnt(2)\n\t" \
    "v_mfma_f32_16x16x32_bf16 v[204:207], v[184:187], v[240:243], v[204:207]\n\t" \
    "ds_read_b128 v[232:235], v253 offset:33792\n\t" \
    "s_waitcnt lgkmcnt(2)\n\t" \
    "v_mfma_f32_16x16x32_bf16 v[204:207], v[188:191], v[224:227], v[204:207]\n\t" \
    "ds_read_b128 v[236:239], v253 offset:38912\n\t" \
    "v_mfma_f32_16x16x32_bf16 v[208:211], v[192:195], v[240:243], v[208:211]\n\t" \
    "v_mfma_f32_16x16x32_bf16 v[208:211], v[196:199], v[224:227], v[208:211]\n\t" \
    "v_mfma_f32_16x16x32_bf16 v[200:203], v[176:179], v[240:243], v[200:203]\n\t" \
    "v_mfma_f32_16x16x32_bf16 v[200:203], v[180:183], v[224:227], v[200:203]\n\t" \
    "ds_read_b128 v[240:243], v253 offset:39936\n\t" \
    "s_waitcnt lgkmcnt(3)\n\t" \
    "v_mfma_f32_16x16x32_bf16 v[216:219], v[24:27], v[228:231], v[216:219]\n\t" \
    "s_waitcnt lgkmcnt(2)\n\t" \
    "v_mfma_f32_16x16x32_bf16 v[216:219], v[28:31], v[232:235], v[216:219]\n\t" \
    "v_cvt_pk_bf16_f32 v204, v204, v208\n\t" \
    "v_cvt_pk_bf16_f32 v205, v205, v209\n\t" \
    "s_waitcnt lgkmcnt(1)\n\t" \
    "v_mfma_f32_16x16x32_bf16 v[216:219], v[184:187], v[236:239], v[216:219]\n\t" \
    "v_cvt_pk_bf16_f32 v206, v206, v210\n\t" \
    "v_cvt_pk_bf16_f32 v207, v207, v211\n\t" \
    "s_waitcnt lgkmcnt(0)\n\t" \
    "v_mfma_f32_16x16x32_bf16 v[216:219], v[188:191], v[240:243], v[216:219]\n\t" \
    "ds_write_b128 v254, v[204:207] offset:9216\n\t" \
    "v_mfma_f32_16x16x32_bf16 v[220:223], v[32:35], v[228:231], v[220:223]\n\t" \
    "v_mfma_f32_16x16x32_bf16 v[220:223], v[36:39], v[232:235], v[220:223]\n\t" \
    "v_cvt_pk_bf16_f32 v200, v200, v201\n\t" \
    "v_cvt_pk_bf16_f32 v201, v202, v203\n\t" \
    "v_mfma_f32_16x16x32_bf16 v[220:223], v[192:195], v[236:239], v[220:223]\n\t" \
    "ds_write_b64 v254, v[200:201] offset:7168\n\t" \
    "v_mfma_f32_16x16x32_bf16 v[220:223], v[196:199], v[240:243], v[220:223]\n\t" \
    "v_mfma_f32_16x16x32_bf16 v[212:215], v[16:19], v[228:231], v[212:215]\n\t" \
    "ds_read_b128 v[200:203], v254 offset:40960\n\t" \
    "ds_read_b128 v[204:207], v254 offset:41984\n\t" \
    "v_mfma_f32_16x16x32_bf16 v[212:215], v[20:23], v[232:235], v[212:215]\n\t" \
    "v_mov_b64 v[208:209], 0\n\t" \
    "v_mov_b64 v[210:211], 0\n\t" \
    "v_mfma_f32_16x16x32_bf16 v[212:215], v[176:179], v[236:239], v[212:215]\n\t" \
    "v_cvt_pk_bf16_f32 v216, v216, v220\n\t" \
    "v_cvt_pk_bf16_f32 v217, v217, v221\n\t" \
    "v_mfma_f32_16x16x32_bf16 v[212:215], v[180:183], v[240:243], v[212:215]\n\t" \
    "v_cvt_pk_bf16_f32 v218, v218, v222\n\t" \
    "v_cvt_pk_bf16_f32 v219, v219, v223\n\t" \
    "s_setprio 0\n\t" \
    "ds_write_b128 v254, v[216:219] offset:15360\n\t" \
    "ds_read_b128 v[216:219], v254 offset:43008\n\t" \
    "ds_read_b128 v[220:223], v254 offset:44032\n\t" \
    "s_nop 2\n\t" \
    "v_cvt_pk_bf16_f32 v212, v212, v213\n\t" \
    "v_cvt_pk_bf16_f32 v213, v214, v215\n\t" \
    "ds_write_b64 v254, v[212:213] offset:13312\n\t" \
    LST6 \
    LJIT \
    "s_sleep 3\n\t" \
    "s_mov_b32 m0, 0xa800\n\t" \
    "s_nop 0\n\t" \
    "buffer_load_dwordx4 v253, s[60:63], s46 offen sc1 lds\n\t" \
    "s_mov_b32 m0, 0xb800\n\t" \
    "s_nop 0\n\t" \
    "buffer_load_dwordx4 v253, s[60:63], s47 offen sc1 lds\n\t" \
    "s_mov_b32 m0, 0xac00\n\t" \
    "s_nop 0\n\t" \
    "buffer_load_dwordx4 v253, s[60:63], s48 offen sc1 lds\n\t" \
    "s_waitcnt lgkmcnt(0)\n\t" \
    LFLUSH212 \
    "v_bfe_i32 v212, v253, 5, 1\n\t" \
    "v_and_b32 v212, 0xaaaa, v212\n\t" \
    "v_or_b32 v212, 0x4444, v212\n\t" \
    "v_and_b32 v213, 0xffff, v255\n\t" \
    "v_lshrrev_b32 v214, 16, v255\n\t" \
    ".Ls14_%=:\n\t" \
    "s_waitcnt vmcnt(0)\n\t" \
    "ds_read_b128 v[224:227], v253 offset:43008\n\t" \
    "ds_read_b128 v[228:231], v253 offset:47104\n\t" \
    "ds_read_b128 v[232:235], v253 offset:44032\n\t" \
    "s_waitcnt lgkmcnt(2)\n\t" \
    "v_max_u32 v215, v224, v225\n\t" \
    "v_max3_u32 v215, v226, v227, v215\n\t" \
    "s_waitcnt lgkmcnt(1)\n\t" \
    "v_max3_u32 v215, v228, v229, v215\n\t" \
    "v_max3_u32 v215, v230, v231, v215\n\t" \
    "s_waitcnt lgkmcnt(0)\n\t" \
    "v_max3_u32 v215, v232, v233, v215\n\t" \
    "v_max3_u32 v215, v234, v235, v215\n\t" \
    "v_cmp_eq_u32 vcc, -1, v215\n\t" \
    "s_cbranch_vccz .Ls15_%=\n\t" \
    "v_max_u32 v215, v224, v225\n\t" \
    "v_max3_u32 v215, v226, v227, v215\n\t" \
    "v_cmp_eq_u32 vcc, -1, v215\n\t" \
    "s_cbranch_vccz .Ls16_%=\n\t" \
    "s_mov_b32 m0, 0xa800\n\t" \
    "s_nop 0\n\t" \
    "buffer_load_dwordx4 v253, s[60:63], s46 offen sc1 lds\n\t" \
    ".Ls16_%=:\n\t" \
    "v_max_u32 v215, v228, v229\n\t" \
    "v_max3_u32 v215, v230, v231, v215\n\t" \
    "v_cmp_eq_u32 vcc, -1, v215\n\t" \
    "s_cbranch_vccz .Ls17_%=\n\t" \
    "s_mov_b32 m0, 0xb800\n\t" \
    "s_nop 0\n\t" \
    "buffer_load_dwordx4 v253, s[60:63], s47 offen sc1 lds\n\t" \
    ".Ls17_%=:\n\t" \
    "v_max_u32 v215, v232, v233\n\t" \
    "v_max3_u32 v215, v234, v235, v215\n\t" \
    "v_cmp_eq_u32 vcc, -1, v215\n\t" \
    "s_cbranch_vccz .Ls18_%=\n\t" \
    "s_mov_b32 m0, 0xac00\n\t" \
    "s_nop 0\n\t" \
    "buffer_load_dwordx4 v253, s[60:63], s48 offen sc1 lds\n\t" \
    ".Ls18_%=:\n\t" \
    "s_cmp_eq_u32 s41, 0\n\t" \
    "s_cbranch_scc1 .Ls15_%=\n\t" \
    "s_sub_u32 s41, s41, 1\n\t" \
    "s_branch .Ls14_%=\n\t" \
    ".Ls15_%=:\n\t" \
    LST2 \
    LJIT \
    "s_setprio 3\n\t" \
    "s_waitcnt lgkmcnt(0)\n\t" \
    "s_barrier \n\t" \
    LST3 \
    LJIT \
    "ds_read_b128 v[236:239], v253 offset:40960\n\t" \
    "ds_read_b128 v[240:243], v253 offset:46080\n\t" \
    "v_smfmac_f32_16x16x64_bf16 v[244:247], v[224:227], v[0:7], v212\n\t" \
    "v_smfmac_f32_16x16x64_bf16 v[248:251], v[224:227], v[8:15], v212\n\t" \
    "v_smfmac_f32_16x16x64_bf16 v[208:211], v[224:227], v[200:207], v212\n\t" \
    "ds_read_b128 v[200:203], v254 offset:45056\n\t" \
    "ds_read_b128 v[204:207], v254 offset:46080\n\t" \
    "ds_read_b128 v[224:227], v253 offset:41984\n\t" \
    "v_smfmac_f32_16x16x64_bf16 v[208:211], v[228:231], v[16:23], v212\n\t" \
    "v_smfmac_f32_16x16x64_bf16 v[244:247], v[228:231], v[24:31], v212\n\t" \
    "v_smfmac_f32_16x16x64_bf16 v[248:251], v[228:231], v[32:39], v212\n\t" \
    "ds_read_b128 v[228:231], v253 offset:48128\n\t" \
    "v_smfmac_f32_16x16x64_bf16 v[208:211], v[232:235], v[216:223], v212\n\t" \
    "ds_read_b128 v[216:219], v254 offset:47104\n\t" \
    "ds_read_b128 v[220:223], v254 offset:48128\n\t" \
    "v_smfmac_f32_16x16x64_bf16 v[244:247], v[232:235], v[40:47], v212\n\t" \
    "v_smfmac_f32_16x16x64_bf16 v[248:251], v[232:235], v[48:55], v212\n\t" \
    "ds_read_b128 v[232:235], v253 offset:45056\n\t" \
    "s_waitcnt lgkmcnt(8)\n\t" \
    "v_smfmac_f32_16x16x64_bf16 v[208:211], v[236:239], v[56:63], v212\n\t" \
    "v_smfmac_f32_16x16x64_bf16 v[244:247], v[236:239], v[64:71], v212\n\t" \
    "v_smfmac_f32_16x16x64_bf16 v[248:251], v[236:239], v[72:79], v212\n\t" \
    "ds_read_b128 v[236:239], v253 offset:49152\n\t" \
    "s_waitcnt lgkmcnt(6)\n\t" \
    "v_smfmac_f32_16x16x64_bf16 v[208:211], v[240:243], v[200:207], v212\n\t" \
    "ds_read_b128 v[200:203], v254 offset:49152\n\t" \
    "ds_read_b128 v[204:207], v254 offset:50176\n\t" \
    "v_smfmac_f32_16x16x64_bf16 v[244:247], v[240:243], v[80:87], v212\n\t" \
    "v_smfmac_f32_16x16x64_bf16 v[248:251], v[240:243], v[88:95], v212\n\t" \
    "ds_read_b128 v[240:243], v253 offset:50176\n\t" \
    "s_waitcnt lgkmcnt(8)\n\t" \
    "v_smfmac_f32_16x16x64_bf16 v[208:211], v[224:227], v[96:103], v212\n\t" \
    "v_smfmac_f32_16x16x64_bf16 v[244:247], v[224:227], v[104:111], v212\n\t" \
    "v_smfmac_f32_16x16x64_bf16 v[248:251], v[224:227], v[112:119], v212\n\t" \
    "s_waitcnt lgkmcnt(5)\n\t" \
    "v_smfmac_f32_16x16x64_bf16 v[208:211], v[228:231], v[216:223], v212\n\t" \
    "s_waitcnt lgkmcnt(4)\n\t" \
    "v_smfmac_f32_16x16x64_bf16 v[208:211], v[232:235], v[136:143], v212\n\t" \
    "s_waitcnt lgkmcnt(1)\n\t" \
    "v_smfmac_f32_16x16x64_bf16 v[208:211], v[236:239], v[200:207], v212\n\t" \
    "s_waitcnt lgkmcnt(0)\n\t" \
    "v_smfmac_f32_16x16x64_bf16 v[208:211], v[240:243], v[176:183], v212\n\t" \
    "v_smfmac_f32_16x16x64_bf16 v[244:247], v[228:231], v[120:127], v212\n\t" \
    "v_smfmac_f32_16x16x64_bf16 v[244:247], v[232:235], v[144:151], v212\n\t" \
    "v_smfmac_f32_16x16x64_bf16 v[244:247], v[236:239], v[160:167], v212\n\t" \
    "v_add_f32 v208, v208, v210\n\t" \
    "v_add_f32 v209, v209, v211\n\t" \
    "v_cvt_pk_bf16_f32 v208, v208, v209\n\t" \
    "v_smfmac_f32_16x16x64_bf16 v[244:247], v[240:243], v[184:191], v212\n\t" \
    "ds_write_b16 v213, v208 offset:0\n\t" \
    "ds_write_b16_d16_hi v213, v208 offset:16\n\t" \
    "v_smfmac_f32_16x16x64_bf16 v[248:251], v[228:231], v[128:135], v212\n\t" \
    "v_smfmac_f32_16x16x64_bf16 v[248:251], v[232:235], v[152:159], v212\n\t" \
    "v_smfmac_f32_16x16x64_bf16 v[248:251], v[236:239], v[168:175], v212\n\t" \
    "v_add_f32 v244, v244, v246\n\t" \
    "v_add_f32 v245, v245, v247\n\t" \
    "v_smfmac_f32_16x16x64_bf16 v[248:251], v[240:243], v[192:199], v212\n\t" \
    "s_nop 7\n\t" \
    "v_add_f32 v248, v248, v250\n\t" \
    "v_add_f32 v249, v249, v251\n\t" \
    "v_cvt_pk_bf16_f32 v244, v244, v248\n\t" \
    "v_cvt_pk_bf16_f32 v245, v245, v249\n\t" \
    "ds_write2_b32 v214, v244, v245 offset1:4\n\t" \
    "s_setprio 0\n\t" \
    "s_sleep 1\n\t" \
    "ds_read_b128 v[200:203], v254 offset:6144\n\t" \
    "ds_read_b128 v[224:227], v254 offset:18432\n\t" \
    "ds_read_b128 v[204:207], v254 offset:7168\n\t" \
    "ds_read_b128 v[228:231], v254 offset:19456\n\t" \
    "ds_read_b128 v[208:211], v254 offset:8192\n\t" \
    "ds_read_b128 v[232:235], v254 offset:20480\n\t" \
    "ds_read_b128 v[212:215], v254 offset:9216\n\t" \
    "ds_read_b128 v[236:239], v254 offset:21504\n\t" \
    "ds_read_b128 v[216:219], v254 offset:10240\n\t" \
    "ds_read_b128 v[220:223], v254 offset:11264\n\t" \
    "s_and_b32 s44, s40, 3\n\t" \
    "s_lshl_b32 s44, s44, 17\n\t" \
    "s_add_u32 s50, s44, %[pb]\n\t" \
    "s_add_u32 s51, s44, %[pm]\n\t" \
    "s_mov_b32 s52, 0xf0000\n\t" \
    "s_mov_b32 s53, 0xfff00ff\n\t" \
    "s_mov_b32 s54, 0x1f0001\n\t" \
    "s_mov_b32 s55, 0x1fff01ff\n\t" \
    "s_mov_b32 s56, 0x3f0003\n\t" \
    "s_mov_b32 s57, 0x3fff03ff\n\t" \
    "s_mov_b32 s58, 0x7f0007\n\t" \
    "s_mov_b32 s59, 0x7fff07ff\n\t" \
    "s_waitcnt lgkmcnt(8)\n\t" \
    "v_mfma_f32_16x16x32_bf16 v[240:243], v[224:227], v[200:203], 0\n\t" \
    "ds_read_b128 v[224:227], v254 offset:22528\n\t" \
    "s_waitcnt lgkmcnt(7)\n\t" \
    "v_mfma_f32_16x16x32_bf16 v[240:243], v[228:231], v[204:207], v[240:243]\n\t" \
    "ds_read_b128 v[228:231], v254 offset:23552\n\t" \
    "s_waitcnt lgkmcnt(6)\n\t" \
    "v_mfma_f32_16x16x32_bf16 v[240:243], v[232:235], v[208:211], v[240:243]\n\t" \
    "s_waitcnt lgkmcnt(4)\n\t" \
    "v_mfma_f32_16x16x32_bf16 v[240:243], v[236:239], v[212:215], v[240:243]\n\t" \
    "v_and_b32 v252, 0xffffff7f, v254\n\t" \
    "s_waitcnt lgkmcnt(0)\n\t" \
    "s_barrier \n\t" \
    LST4 \
    LJIT \
    "ds_read_b128 v[232:235], v252 offset:3072\n\t" \
    "ds_read_b128 v[236:239], v252 offset:3200\n\t" \
    "v_mfma_f32_16x16x32_bf16 v[240:243], v[224:227], v[216:219], v[240:243]\n\t" \
    "ds_read_b128 v[224:227], v252 offset:4096\n\t" \
    "v_mfma_f32_16x16x32_bf16 v[240:243], v[228:231], v[220:223], v[240:243]\n\t" \
    "ds_read_b128 v[228:231], v252 offset:4224\n\t" \
    "s_waitcnt lgkmcnt(3)\n\t" \
    "v_mfma_f32_16x16x32_bf16 v[248:251], v[232:235], v[200:203], 0\n\t" \
    "ds_read_b128 v[232:235], v252 offset:5120\n\t" \
    "s_waitcnt lgkmcnt(3)\n\t" \
    "v_mfma_f32_16x16x32_bf16 v[248:251], v[236:239], v[204:207], v[248:251]\n\t" \
    "ds_read_b128 v[236:239], v252 offset:5248\n\t" \
    "s_waitcnt lgkmcnt(3)\n\t" \
    "v_mfma_f32_16x16x32_bf16 v[248:251], v[224:227], v[208:211], v[248:251]\n\t" \
    "s_waitcnt lgkmcnt(2)\n\t" \
    "v_mfma_f32_16x16x32_bf16 v[248:251], v[228:231], v[212:215], v[248:251]\n\t" \
    "s_waitcnt lgkmcnt(1)\n\t" \
    "v_mfma_f32_16x16x32_bf16 v[248:251], v[232:235], v[216:219], v[248:251]\n\t" \
    "s_waitcnt lgkmcnt(0)\n\t" \
    "v_mfma_f32_16x16x32_bf16 v[248:251], v[236:239], v[220:223], v[248:251]\n\t" \
    "v_mov_b32 v226, 0xff7fffff\n\t" \
    "v_cndmask_b32 v240, v240, v226, s[52:53]\n\t" \
    "v_cndmask_b32 v241, v241, v226, s[54:55]\n\t" \
    "v_cndmask_b32 v242, v242, v226, s[56:57]\n\t" \
    "v_cndmask_b32 v243, v243, v226, s[58:59]\n\t" \
    "v_max3_f32 v224, v240, v241, v242\n\t" \
    "v_mov_b32 v225, v243\n\t" \
    "v_lshrrev_b32 v200, 5, v253\n\t" \
    "v_and_b32 v201, 0xf0, v253\n\t" \
    "v_and_or_b32 v200, v200, 8, v201\n\t" \
    "v_max3_f32 v224, v224, v248, v249\n\t" \
    "v_max3_f32 v225, v225, v250, v251\n\t" \
    "v_max_f32 v224, v224, v225\n\t" \
    "v_mov_b32 v225, v224\n\t" \
    "s_nop 1\n\t" \
    "v_permlane16_swap_b32 v224, v225\n\t" \
    "v_max_f32 v224, v224, v225\n\t" \
    "v_mov_b32 v225, v224\n\t" \
    "s_nop 1\n\t" \
    "v_permlane32_swap_b32 v224, v225\n\t" \
    "v_max_f32 v224, v224, v225\n\t" \
    "v_mul_f32 v224, %[scl], v224\n\t" \
    "v_fma_f32 v240, v240, %[scl], -v224\n\t" \
    "v_fma_f32 v241, v241, %[scl], -v224\n\t" \
    "v_exp_f32 v240, v240\n\t" \
    "v_fma_f32 v242, v242, %[scl], -v224\n\t" \
    "v_exp_f32 v241, v241\n\t" \
    "v_fma_f32 v243, v243, %[scl], -v224\n\t" \
    "v_exp_f32 v242, v242\n\t" \
    "v_fma_f32 v248, v248, %[scl], -v224\n\t" \
    "v_exp_f32 v243, v243\n\t" \
    "v_fma_f32 v249, v249, %[scl], -v224\n\t" \
    "v_exp_f32 v248, v248\n\t" \
    "v_fma_f32 v250, v250, %[scl], -v224\n\t" \
    "v_exp_f32 v249, v249\n\t" \
    "v_fma_f32 v251, v251, %[scl], -v224\n\t" \
    "v_exp_f32 v250, v250\n\t" \
    "v_exp_f32 v251, v251\n\t" \
    "v_add_f32 v224, v240, v241\n\t" \
    "v_add_f32 v225, v242, v243\n\t" \
    "v_add_f32 v232, v248, v249\n\t" \
    "v_add_f32 v233, v250, v251\n\t" \
    "v_add_f32 v224, v224, v225\n\t" \
    "v_add_f32 v232, v232, v233\n\t" \
    "v_fma_f32 v224, v232, 0.5, v224\n\t" \
    "v_mov_b32 v225, v224\n\t" \
    "s_nop 1\n\t" \
    "v_permlane16_swap_b32 v224, v225\n\t" \
    "v_add_f32 v224, v224, v225\n\t" \
    "v_mov_b32 v225, v224\n\t" \
    "s_nop 1\n\t" \
    "v_permlane32_swap_b32 v224, v225\n\t" \
    "v_add_f32 v224, v224, v225\n\t" \
    "v_rcp_f32 v224, v224\n\t" \
    "s_nop 0\n\t" \
    "v_mul_f32 v240, v240, v224\n\t" \
    "v_mul_f32 v241, v241, v224\n\t" \
    "v_mul_f32 v242, v242, v224\n\t" \
    "v_mul_f32 v243, v243, v224\n\t" \
    "v_mul_f32 v248, v248, v224\n\t" \
    "v_mul_f32 v249, v249, v224\n\t" \
    "v_mul_f32 v250, v250, v224\n\t" \
    "v_mul_f32 v251, v251, v224\n\t" \
    "v_cvt_pk_bf16_f32 v204, |v240|, |v241|\n\t" \
    "v_cvt_pk_bf16_f32 v205, |v242|, |v243|\n\t" \
    "v_cvt_pk_bf16_f32 v208, |v248|, |v249|\n\t" \
    "v_cvt_pk_bf16_f32 v209, |v250|, |v251|\n\t" \
    "v_permlane16_swap_b32 v204, v206\n\t" \
    "v_permlane16_swap_b32 v205, v207\n\t" \
    "s_mov_b32 exec_lo, 0xffff\n\t" \
    "s_mov_b32 exec_hi, 0xffff\n\t" \
    "buffer_store_dwordx4 v[204:207], v253, s[60:63], s50 offen sc1\n\t" \
    "s_mov_b32 exec_lo, -1\n\t" \
    "s_mov_b32 exec_hi, 0\n\t" \
    "buffer_store_dwordx2 v[208:209], v200, s[60:63], s51 offen sc1\n\t" \
    "s_mov_b32 exec_hi, -1\n\t" \
    LST5 \
    LJIT \
    "ds_read_b128 v[224:227], v253 offset:0\n\t" \
    "ds_read_b128 v[244:247], v254 offset:40960\n\t" \
    "ds_read_b128 v[228:231], v253 offset:1024\n\t" \
    "ds_read_b128 v[232:235], v253 offset:20480\n\t" \
    "ds_read_b128 v[236:239], v253 offset:21504\n\t" \
    "ds_read_b128 v[248:251], v254 offset:41984\n\t" \
    "s_add_u32 s40, s40, 1\n\t" \
    "s_cmp_lg_u32 s40, 64\n\t" \
    "s_cbranch_scc1 .Ls13_%=\n\t" \
    "s_branch .Lend%=\n\t" \
    ".Ls0_%=:\n\t" \
    "s_add_u32 m0, %[wm], 0x18000\n\t" \
    "s_mov_b32 s44, 0x0\n\t" \
    "buffer_load_dwordx4 v240, %[wr], s44 offen offset:0 nt lds\n\t" \
    "buffer_load_dwordx4 v241, %[wr], s44 offen offset:1024 nt lds\n\t" \
    "buffer_load_dwordx4 v242, %[wr], s44 offen offset:2048 nt lds\n\t" \
    "s_add_u32 m0, %[wm], 0x1e000\n\t" \
    "s_mov_b32 s44, 0x60000\n\t" \
    "buffer_load_dwordx4 v240, %[wr], s44 offen offset:0 nt lds\n\t" \
    "buffer_load_dwordx4 v241, %[wr], s44 offen offset:1024 nt lds\n\t" \
    "buffer_load_dwordx4 v242, %[wr], s44 offen offset:2048 nt lds\n\t" \
    "s_add_u32 m0, %[wm], 0x0\n\t" \
    "s_mov_b32 s44, 0xc0000\n\t" \
    "buffer_load_dwordx4 v240, %[wr], s44 offen offset:0 nt lds\n\t" \
    "buffer_load_dwordx4 v241, %[wr], s44 offen offset:1024 nt lds\n\t" \
    "buffer_load_dwordx4 v242, %[wr], s44 offen offset:2048 nt lds\n\t" \
    "s_add_u32 m0, %[wm], 0x6000\n\t" \
    "s_mov_b32 s44, 0x120000\n\t" \
    "buffer_load_dwordx4 v240, %[wr], s44 offen offset:0 nt lds\n\t" \
    "buffer_load_dwordx4 v241, %[wr], s44 offen offset:1024 nt lds\n\t" \
    "buffer_load_dwordx4 v242, %[wr], s44 offen offset:2048 nt lds\n\t" \
    "s_add_u32 m0, %[wm], 0xc000\n\t" \
    "s_mov_b32 s44, 0x180000\n\t" \
    "buffer_load_dwordx4 v240, %[wr], s44 offen offset:0 nt lds\n\t" \
    "buffer_load_dwordx4 v241, %[wr], s44 offen offset:1024 nt lds\n\t" \
    "buffer_load_dwordx4 v242, %[wr], s44 offen offset:2048 nt lds\n\t" \
    "v_add_u32 v244, 0x18000, v243\n\t" \
    "s_waitcnt vmcnt(12) lgkmcnt(0)\n\t" \
    "s_barrier \n\t" \
    "s_add_u32 m0, %[wm], 0x12000\n\t" \
    "s_mov_b32 s44, 0x1e0000\n\t" \
    "buffer_load_dwordx4 v240, %[wr], s44 offen offset:0 nt lds\n\t" \
    "buffer_load_dwordx4 v241, %[wr], s44 offen offset:1024 nt lds\n\t" \
    "buffer_load_dwordx4 v242, %[wr], s44 offen offset:2048 nt lds\n\t" \
    "ds_read_b64_tr_b16 v[200:201], v244 offset:0\n\t" \
    "ds_read_b64_tr_b16 v[202:203], v244 offset:1536\n\t" \
    "ds_read_b64_tr_b16 v[0:1], v244 offset:32\n\t" \
    "ds_read_b64_tr_b16 v[2:3], v244 offset:1568\n\t" \
    "ds_read_b64_tr_b16 v[8:9], v244 offset:64\n\t" \
    "ds_read_b64_tr_b16 v[10:11], v244 offset:1600\n\t" \
    "v_add_u32 v244, 0x1e000, v243\n\t" \
    "s_waitcnt vmcnt(12) lgkmcnt(0)\n\t" \
    "s_barrier \n\t" \
    "s_add_u32 m0, %[wm], 0x18000\n\t" \
    "s_mov_b32 s44, 0x240000\n\t" \
    "buffer_load_dwordx4 v240, %[wr], s44 offen offset:0 nt lds\n\t" \
    "buffer_load_dwordx4 v241, %[wr], s44 offen offset:1024 nt lds\n\t" \
    "buffer_load_dwordx4 v242, %[wr], s44 offen offset:2048 nt lds\n\t" \
    "ds_read_b64_tr_b16 v[204:205], v244 offset:0\n\t" \
    "ds_read_b64_tr_b16 v[206:207], v244 offset:1536\n\t" \
    "ds_read_b64_tr_b16 v[4:5], v244 offset:32\n\t" \
    "ds_read_b64_tr_b16 v[6:7], v244 offset:1568\n\t" \
    "ds_read_b64_tr_b16 v[12:13], v244 offset:64\n\t" \
    "ds_read_b64_tr_b16 v[14:15], v244 offset:1600\n\t" \
    "v_add_u32 v244, 0x0, v243\n\t" \
    "s_waitcnt vmcnt(12) lgkmcnt(0)\n\t" \
    "s_barrier \n\t" \
    "s_add_u32 m0, %[wm], 0x1e000\n\t" \
    "s_mov_b32 s44, 0x2a0000\n\t" \
    "buffer_load_dwordx4 v240, %[wr], s44 offen offset:0 nt lds\n\t" \
    "buffer_load_dwordx4 v241, %[wr], s44 offen offset:1024 nt lds\n\t" \
    "buffer_load_dwordx4 v242, %[wr], s44 offen offset:2048 nt lds\n\t" \
    "ds_read_b64_tr_b16 v[208:209], v244 offset:0\n\t" \
    "ds_read_b64_tr_b16 v[210:211], v244 offset:1536\n\t" \
    "ds_read_b64_tr_b16 v[40:41], v244 offset:32\n\t" \
    "ds_read_b64_tr_b16 v[42:43], v244 offset:1568\n\t" \
    "ds_read_b64_tr_b16 v[48:49], v244 offset:64\n\t" \
    "ds_read_b64_tr_b16 v[50:51], v244 offset:1600\n\t" \
    "v_add_u32 v244, 0x6000, v243\n\t" \
    "s_waitcnt vmcnt(12) lgkmcnt(0)\n\t" \
    "s_barrier \n\t" \
    "s_add_u32 m0, %[wm], 0x0\n\t" \
    "s_mov_b32 s44, 0x300000\n\t" \
    "buffer_load_dwordx4 v240, %[wr], s44 offen offset:0 nt lds\n\t" \
    "buffer_load_dwordx4 v241, %[wr], s44 offen offset:1024 nt lds\n\t" \
    "buffer_load_dwordx4 v242, %[wr], s44 offen offset:2048 nt lds\n\t" \
    "ds_read_b64_tr_b16 v[212:213], v244 offset:0\n\t" \
    "ds_read_b64_tr_b16 v[214:215], v244 offset:1536\n\t" \
    "ds_read_b64_tr_b16 v[44:45], v244 offset:32\n\t" \
    "ds_read_b64_tr_b16 v[46:47], v244 offset:1568\n\t" \
    "ds_read_b64_tr_b16 v[52:53], v244 offset:64\n\t" \
    "ds_read_b64_tr_b16 v[54:55], v244 offset:1600\n\t" \
    "v_add_u32 v244, 0xc000, v243\n\t" \
    "s_waitcnt vmcnt(12) lgkmcnt(0)\n\t" \
    "s_barrier \n\t" \
    "s_add_u32 m0, %[wm], 0x6000\n\t" \
    "s_mov_b32 s44, 0x360000\n\t" \
    "buffer_load_dwordx4 v240, %[wr], s44 offen offset:0 nt lds\n\t" \
    "buffer_load_dwordx4 v241, %[wr], s44 offen offset:1024 nt lds\n\t" \
    "buffer_load_dwordx4 v242, %[wr], s44 offen offset:2048 nt lds\n\t" \
    "ds_read_b64_tr_b16 v[216:217], v244 offset:0\n\t" \
    "ds_read_b64_tr_b16 v[218:219], v244 offset:1536\n\t" \
    "ds_read_b64_tr_b16 v[80:81], v244 offset:32\n\t" \
    "ds_read_b64_tr_b16 v[82:83], v244 offset:1568\n\t" \
    "ds_read_b64_tr_b16 v[88:89], v244 offset:64\n\t" \
    "ds_read_b64_tr_b16 v[90:91], v244 offset:1600\n\t" \
    "v_add_u32 v244, 0x12000, v243\n\t" \
    "s_waitcnt vmcnt(12) lgkmcnt(0)\n\t" \
    "s_barrier \n\t" \
    "s_add_u32 m0, %[wm], 0xc000\n\t" \
    "s_mov_b32 s44, 0x3c0000\n\t" \
    "buffer_load_dwordx4 v240, %[wr], s44 offen offset:0 nt lds\n\t" \
    "buffer_load_dwordx4 v241, %[wr], s44 offen offset:1024 nt lds\n\t" \
    "buffer_load_dwordx4 v242, %[wr], s44 offen offset:2048 nt lds\n\t" \
    "ds_read_b64_tr_b16 v[220:221], v244 offset:0\n\t" \
    "ds_read_b64_tr_b16 v[222:223], v244 offset:1536\n\t" \
    "ds_read_b64_tr_b16 v[84:85], v244 offset:32\n\t" \
    "ds_read_b64_tr_b16 v[86:87], v244 offset:1568\n\t" \
    "ds_read_b64_tr_b16 v[92:93], v244 offset:64\n\t" \
    "ds_read_b64_tr_b16 v[94:95], v244 offset:1600\n\t" \
    "v_add_u32 v244, 0x18000, v243\n\t" \
    "s_waitcnt vmcnt(12) lgkmcnt(0)\n\t" \
    "s_barrier \n\t" \
    "v_mov_b32 v246, 0\n\t" \
    "buffer_load_dword v246, v246, s[60:63], %[mo] offen sc1\n\t" \
    "s_add_u32 m0, %[wm], 0x12000\n\t" \
    "s_mov_b32 s44, 0x420000\n\t" \
    "buffer_load_dwordx4 v240, %[wr], s44 offen offset:0 nt lds\n\t" \
    "buffer_load_dwordx4 v241, %[wr], s44 offen offset:1024 nt lds\n\t" \
    "buffer_load_dwordx4 v242, %[wr], s44 offen offset:2048 nt lds\n\t" \
    "ds_read_b64_tr_b16 v[224:225], v244 offset:0\n\t" \
    "ds_read_b64_tr_b16 v[226:227], v244 offset:1536\n\t" \
    "ds_read_b64_tr_b16 v[120:121], v244 offset:32\n\t" \
    "ds_read_b64_tr_b16 v[122:123], v244 offset:1568\n\t" \
    "ds_read_b64_tr_b16 v[128:129], v244 offset:64\n\t" \
    "ds_read_b64_tr_b16 v[130:131], v244 offset:1600\n\t" \
    "v_add_u32 v244, 0x1e000, v243\n\t" \
    "s_waitcnt vmcnt(12) lgkmcnt(0)\n\t" \
    "s_barrier \n\t" \
    "s_add_u32 m0, %[wm], 0x18000\n\t" \
    "s_mov_b32 s44, 0x480000\n\t" \
    "buffer_load_dwordx4 v240, %[wr], s44 offen offset:0 nt lds\n\t" \
    "buffer_load_dwordx4 v241, %[wr], s44 offen offset:1024 nt lds\n\t" \
    "buffer_load_dwordx4 v242, %[wr], s44 offen offset:2048 nt lds\n\t" \
    "ds_read_b64_tr_b16 v[228:229], v244 offset:0\n\t" \
    "ds_read_b64_tr_b16 v[230:231], v244 offset:1536\n\t" \
    "ds_read_b64_tr_b16 v[124:125], v244 offset:32\n\t" \
    "ds_read_b64_tr_b16 v[126:127], v244 offset:1568\n\t" \
    "ds_read_b64_tr_b16 v[132:133], v244 offset:64\n\t" \
    "ds_read_b64_tr_b16 v[134:135], v244 offset:1600\n\t" \
    "v_add_u32 v244, 0x0, v243\n\t" \
    "s_waitcnt vmcnt(12) lgkmcnt(0)\n\t" \
    "s_barrier \n\t" \
    "s_add_u32 m0, %[wm], 0x1e000\n\t" \
    "s_mov_b32 s44, 0x4e0000\n\t" \
    "buffer_load_dwordx4 v240, %[wr], s44 offen offset:0 nt lds\n\t" \
    "buffer_load_dwordx4 v241, %[wr], s44 offen offset:1024 nt lds\n\t" \
    "buffer_load_dwordx4 v242, %[wr], s44 offen offset:2048 nt lds\n\t" \
    "ds_read_b64_tr_b16 v[232:233], v244 offset:0\n\t" \
    "ds_read_b64_tr_b16 v[234:235], v244 offset:1536\n\t" \
    "ds_read_b64_tr_b16 v[160:161], v244 offset:32\n\t" \
    "ds_read_b64_tr_b16 v[162:163], v244 offset:1568\n\t" \
    "ds_read_b64_tr_b16 v[168:169], v244 offset:64\n\t" \
    "ds_read_b64_tr_b16 v[170:171], v244 offset:1600\n\t" \
    "v_add_u32 v244, 0x6000, v243\n\t" \
    "s_waitcnt vmcnt(12) lgkmcnt(0)\n\t" \
    "s_barrier \n\t" \
    "s_add_u32 m0, %[wm], 0x0\n\t" \
    "s_mov_b32 s44, 0x540000\n\t" \
    "buffer_load_dwordx4 v240, %[wr], s44 offen offset:0 nt lds\n\t" \
    "buffer_load_dwordx4 v241, %[wr], s44 offen offset:1024 nt lds\n\t" \
    "buffer_load_dwordx4 v242, %[wr], s44 offen offset:2048 nt lds\n\t" \
    "ds_read_b64_tr_b16 v[236:237], v244 offset:0\n\t" \
    "ds_read_b64_tr_b16 v[238:239], v244 offset:1536\n\t" \
    "ds_read_b64_tr_b16 v[164:165], v244 offset:32\n\t" \
    "ds_read_b64_tr_b16 v[166:167], v244 offset:1568\n\t" \
    "ds_read_b64_tr_b16 v[172:173], v244 offset:64\n\t" \
    "ds_read_b64_tr_b16 v[174:175], v244 offset:1600\n\t" \
    "v_add_u32 v244, 0xc000, v243\n\t" \
    "s_waitcnt vmcnt(12) lgkmcnt(0)\n\t" \
    "s_barrier \n\t" \
    "s_add_u32 m0, %[wm], 0x6000\n\t" \
    "s_mov_b32 s44, 0x5a0000\n\t" \
    "buffer_load_dwordx4 v240, %[wr], s44 offen offset:0 nt lds\n\t" \
    "buffer_load_dwordx4 v241, %[wr], s44 offen offset:1024 nt lds\n\t" \
    "buffer_load_dwordx4 v242, %[wr], s44 offen offset:2048 nt lds\n\t" \
    "ds_read_b64_tr_b16 v[16:17], v244 offset:0\n\t" \
    "ds_read_b64_tr_b16 v[18:19], v244 offset:1536\n\t" \
    "ds_read_b64_tr_b16 v[24:25], v244 offset:32\n\t" \
    "ds_read_b64_tr_b16 v[26:27], v244 offset:1568\n\t" \
    "ds_read_b64_tr_b16 v[32:33], v244 offset:64\n\t" \
    "ds_read_b64_tr_b16 v[34:35], v244 offset:1600\n\t" \
    "v_add_u32 v244, 0x12000, v243\n\t" \
    "s_waitcnt vmcnt(12) lgkmcnt(0)\n\t" \
    "s_barrier \n\t" \
    ".Ls01_%=:\n\t" \
    "v_readfirstlane_b32 s43, v246\n\t" \
    "s_nop 0\n\t" \
    "s_cmp_lg_u32 s43, -1\n\t" \
    "s_cbranch_scc1 .Ls02_%=\n\t" \
    "s_cmp_eq_u32 s41, 0\n\t" \
    "s_cbranch_scc1 .Ls02_%=\n\t" \
    "s_sub_u32 s41, s41, 1\n\t" \
    "s_sleep 4\n\t" \
    "v_mov_b32 v246, 0\n\t" \
    "buffer_load_dword v246, v246, s[60:63], %[mo] offen sc1\n\t" \
    "s_waitcnt vmcnt(0)\n\t" \
    "s_branch .Ls01_%=\n\t" \
    ".Ls02_%=:\n\t" \
    "v_mov_b32 v246, 0x8000\n\t" \
    "s_mov_b64 exec, 1\n\t" \
    "buffer_store_dword v246, v246, s[60:63], %[mo] offen sc1\n\t" \
    "s_mov_b64 exec, -1\n\t" \
    "s_and_b32 s43, s43, 0x2000\n\t" \
    "s_xor_b32 s43, s43, %[lx]\n\t" \
    "s_add_u32 s60, s60, s43\n\t" \
    "s_addc_u32 s61, s61, 0\n\t" \
    "s_add_u32 m0, %[wm], 0xc000\n\t" \
    "s_mov_b32 s44, 0x600000\n\t" \
    "buffer_load_dwordx4 v240, %[wr], s44 offen offset:0 nt lds\n\t" \
    "buffer_load_dwordx4 v241, %[wr], s44 offen offset:1024 nt lds\n\t" \
    "buffer_load_dwordx4 v242, %[wr], s44 offen offset:2048 nt lds\n\t" \
    "ds_read_b64_tr_b16 v[20:21], v244 offset:0\n\t" \
    "ds_read_b64_tr_b16 v[22:23], v244 offset:1536\n\t" \
    "ds_read_b64_tr_b16 v[28:29], v244 offset:32\n\t" \
    "ds_read_b64_tr_b16 v[30:31], v244 offset:1568\n\t" \
    "ds_read_b64_tr_b16 v[36:37], v244 offset:64\n\t" \
    "ds_read_b64_tr_b16 v[38:39], v244 offset:1600\n\t" \
    "v_add_u32 v244, 0x18000, v243\n\t" \
    "s_waitcnt vmcnt(12) lgkmcnt(0)\n\t" \
    "s_barrier \n\t" \
    "s_add_u32 m0, %[wm], 0x12000\n\t" \
    "s_mov_b32 s44, 0x660000\n\t" \
    "buffer_load_dwordx4 v240, %[wr], s44 offen offset:0 nt lds\n\t" \
    "buffer_load_dwordx4 v241, %[wr], s44 offen offset:1024 nt lds\n\t" \
    "buffer_load_dwordx4 v242, %[wr], s44 offen offset:2048 nt lds\n\t" \
    "ds_read_b64_tr_b16 v[56:57], v244 offset:0\n\t" \
    "ds_read_b64_tr_b16 v[58:59], v244 offset:1536\n\t" \
    "ds_read_b64_tr_b16 v[64:65], v244 offset:32\n\t" \
    "ds_read_b64_tr_b16 v[66:67], v244 offset:1568\n\t" \
    "ds_read_b64_tr_b16 v[72:73], v244 offset:64\n\t" \
    "ds_read_b64_tr_b16 v[74:75], v244 offset:1600\n\t" \
    "v_add_u32 v244, 0x1e000, v243\n\t" \
    "s_waitcnt vmcnt(12) lgkmcnt(0)\n\t" \
    "s_barrier \n\t" \
    "s_add_u32 m0, %[wm], 0x18000\n\t" \
    "s_mov_b32 s44, 0x6c0000\n\t" \
    "buffer_load_dwordx4 v240, %[wr], s44 offen offset:0 nt lds\n\t" \
    "buffer_load_dwordx4 v241, %[wr], s44 offen offset:1024 nt lds\n\t" \
    "buffer_load_dwordx4 v242, %[wr], s44 offen offset:2048 nt lds\n\t" \
    "ds_read_b64_tr_b16 v[60:61], v244 offset:0\n\t" \
    "ds_read_b64_tr_b16 v[62:63], v244 offset:1536\n\t" \
    "ds_read_b64_tr_b16 v[68:69], v244 offset:32\n\t" \
    "ds_read_b64_tr_b16 v[70:71], v244 offset:1568\n\t" \
    "ds_read_b64_tr_b16 v[76:77], v244 offset:64\n\t" \
    "ds_read_b64_tr_b16 v[78:79], v244 offset:1600\n\t" \
    "v_add_u32 v244, 0x0, v243\n\t" \
    "s_waitcnt vmcnt(12) lgkmcnt(0)\n\t" \
    "s_barrier \n\t" \
    "s_add_u32 m0, %[wm], 0x1e000\n\t" \
    "s_mov_b32 s44, 0x720000\n\t" \
    "buffer_load_dwordx4 v240, %[wr], s44 offen offset:0 nt lds\n\t" \
    "buffer_load_dwordx4 v241, %[wr], s44 offen offset:1024 nt lds\n\t" \
    "buffer_load_dwordx4 v242, %[wr], s44 offen offset:2048 nt lds\n\t" \
    "ds_read_b64_tr_b16 v[96:97], v244 offset:0\n\t" \
    "ds_read_b64_tr_b16 v[98:99], v244 offset:1536\n\t" \
    "ds_read_b64_tr_b16 v[104:105], v244 offset:32\n\t" \
    "ds_read_b64_tr_b16 v[106:107], v244 offset:1568\n\t" \
    "ds_read_b64_tr_b16 v[112:113], v244 offset:64\n\t" \
    "ds_read_b64_tr_b16 v[114:115], v244 offset:1600\n\t" \
    "v_add_u32 v244, 0x6000, v243\n\t" \
    "s_waitcnt vmcnt(12) lgkmcnt(0)\n\t" \
    "s_barrier \n\t" \
    "ds_read_b64_tr_b16 v[100:101], v244 offset:0\n\t" \
    "ds_read_b64_tr_b16 v[102:103], v244 offset:1536\n\t" \
    "ds_read_b64_tr_b16 v[108:109], v244 offset:32\n\t" \
    "ds_read_b64_tr_b16 v[110:111], v244 offset:1568\n\t" \
    "ds_read_b64_tr_b16 v[116:117], v244 offset:64\n\t" \
    "ds_read_b64_tr_b16 v[118:119], v244 offset:1600\n\t" \
    "v_add_u32 v244, 0xc000, v243\n\t" \
    "s_waitcnt vmcnt(9) lgkmcnt(0)\n\t" \
    "s_barrier \n\t" \
    "ds_read_b64_tr_b16 v[136:137], v244 offset:0\n\t" \
    "ds_read_b64_tr_b16 v[138:139], v244 offset:1536\n\t" \
    "ds_read_b64_tr_b16 v[144:145], v244 offset:32\n\t" \
    "ds_read_b64_tr_b16 v[146:147], v244 offset:1568\n\t" \
    "ds_read_b64_tr_b16 v[152:153], v244 offset:64\n\t" \
    "ds_read_b64_tr_b16 v[154:155], v244 offset:1600\n\t" \
    "v_add_u32 v244, 0x12000, v243\n\t" \
    "s_waitcnt vmcnt(6) lgkmcnt(0)\n\t" \
    "s_barrier \n\t" \
    "ds_read_b64_tr_b16 v[140:141], v244 offset:0\n\t" \
    "ds_read_b64_tr_b16 v[142:143], v244 offset:1536\n\t" \
    "ds_read_b64_tr_b16 v[148:149], v244 offset:32\n\t" \
    "ds_read_b64_tr_b16 v[150:151], v244 offset:1568\n\t" \
    "ds_read_b64_tr_b16 v[156:157], v244 offset:64\n\t" \
    "ds_read_b64_tr_b16 v[158:159], v244 offset:1600\n\t" \
    "v_add_u32 v244, 0x18000, v243\n\t" \
    "s_waitcnt vmcnt(3) lgkmcnt(0)\n\t" \
    "s_barrier \n\t" \
    "ds_read_b64_tr_b16 v[176:177], v244 offset:0\n\t" \
    "ds_read_b64_tr_b16 v[178:179], v244 offset:1536\n\t" \
    "ds_read_b64_tr_b16 v[184:185], v244 offset:32\n\t" \
    "ds_read_b64_tr_b16 v[186:187], v244 offset:1568\n\t" \
    "ds_read_b64_tr_b16 v[192:193], v244 offset:64\n\t" \
    "ds_read_b64_tr_b16 v[194:195], v244 offset:1600\n\t" \
    "v_add_u32 v244, 0x1e000, v243\n\t" \
    "s_waitcnt vmcnt(0) lgkmcnt(0)\n\t" \
    "s_barrier \n\t" \
    "ds_read_b64_tr_b16 v[180:181], v244 offset:0\n\t" \
    "ds_read_b64_tr_b16 v[182:183], v244 offset:1536\n\t" \
    "ds_read_b64_tr_b16 v[188:189], v244 offset:32\n\t" \
    "ds_read_b64_tr_b16 v[190:191], v244 offset:1568\n\t" \
    "ds_read_b64_tr_b16 v[196:197], v244 offset:64\n\t" \
    "ds_read_b64_tr_b16 v[198:199], v244 offset:1600\n\t" \
    "s_waitcnt lgkmcnt(0)\n\t" \
    "s_barrier \n\t" \
    LST0 \
    LJIT \
    "ds_write_b128 v254, v[200:203] offset:30720\n\t" \
    "ds_write_b128 v254, v[204:207] offset:31744\n\t" \
    "ds_write_b128 v254, v[208:211] offset:32768\n\t" \
    "ds_write_b128 v254, v[212:215] offset:33792\n\t" \
    "ds_write_b128 v254, v[216:219] offset:34816\n\t" \
    "ds_write_b128 v254, v[220:223] offset:35840\n\t" \
    "ds_write_b128 v254, v[224:227] offset:36864\n\t" \
    "ds_write_b128 v254, v[228:231] offset:37888\n\t" \
    "ds_write_b128 v254, v[232:235] offset:38912\n\t" \
    "ds_write_b128 v254, v[236:239] offset:39936\n\t" \
    "s_waitcnt vmcnt(0) lgkmcnt(0)\n\t" \
    "s_barrier \n\t" \
    "ds_read_b128 v[224:227], v253 offset:0\n\t" \
    "ds_read_b128 v[228:231], v253 offset:1024\n\t" \
    "ds_read_b128 v[232:235], v253 offset:20480\n\t" \
    "ds_read_b128 v[236:239], v253 offset:21504\n\t" \
    ".Ls03_%=:\n\t" \
    "s_waitcnt lgkmcnt(3)\n\t" \
    "v_mfma_f32_16x16x32_bf16 v[204:207], v[0:3], v[224:227], 0\n\t" \
    "s_waitcnt lgkmcnt(2)\n\t" \
    "v_mfma_f32_16x16x32_bf16 v[204:207], v[4:7], v[228:231], v[204:207]\n\t" \
    "ds_read_b128 v[240:243], v253 offset:2048\n\t" \
    "v_mfma_f32_16x16x32_bf16 v[208:211], v[8:11], v[224:227], 0\n\t" \
    "v_mfma_f32_16x16x32_bf16 v[208:211], v[12:15], v[228:231], v[208:211]\n\t" \
    "s_waitcnt lgkmcnt(2)\n\t" \
    "v_mfma_f32_16x16x32_bf16 v[216:219], v[0:3], v[232:235], 0\n\t" \
    "ds_read_b128 v[224:227], v253 offset:3072\n\t" \
    "s_waitcnt lgkmcnt(2)\n\t" \
    "v_mfma_f32_16x16x32_bf16 v[216:219], v[4:7], v[236:239], v[216:219]\n\t" \
    "ds_read_b128 v[228:231], v253 offset:22528\n\t" \
    "v_mfma_f32_16x16x32_bf16 v[220:223], v[8:11], v[232:235], 0\n\t" \
    "v_mfma_f32_16x16x32_bf16 v[220:223], v[12:15], v[236:239], v[220:223]\n\t" \
    "s_waitcnt lgkmcnt(2)\n\t" \
    "v_mfma_f32_16x16x32_bf16 v[204:207], v[40:43], v[240:243], v[204:207]\n\t" \
    "ds_read_b128 v[232:235], v253 offset:23552\n\t" \
    "s_waitcnt lgkmcnt(2)\n\t" \
    "v_mfma_f32_16x16x32_bf16 v[204:207], v[44:47], v[224:227], v[204:207]\n\t" \
    "ds_read_b128 v[236:239], v253 offset:4096\n\t" \
    "v_mfma_f32_16x16x32_bf16 v[208:211], v[48:51], v[240:243], v[208:211]\n\t" \
    "v_mfma_f32_16x16x32_bf16 v[208:211], v[52:55], v[224:227], v[208:211]\n\t" \
    "s_waitcnt lgkmcnt(2)\n\t" \
    "v_mfma_f32_16x16x32_bf16 v[216:219], v[40:43], v[228:231], v[216:219]\n\t" \
    "ds_read_b128 v[240:243], v253 offset:5120\n\t" \
    "s_waitcnt lgkmcnt(2)\n\t" \
    "v_mfma_f32_16x16x32_bf16 v[216:219], v[44:47], v[232:235], v[216:219]\n\t" \
    "ds_read_b128 v[224:227], v253 offset:24576\n\t" \
    "v_mfma_f32_16x16x32_bf16 v[220:223], v[48:51], v[228:231], v[220:223]\n\t" \
    "v_mfma_f32_16x16x32_bf16 v[220:223], v[52:55], v[232:235], v[220:223]\n\t" \
    "s_waitcnt lgkmcnt(2)\n\t" \
    "v_mfma_f32_16x16x32_bf16 v[204:207], v[80:83], v[236:239], v[204:207]\n\t" \
    "ds_read_b128 v[228:231], v253 offset:25600\n\t" \
    "s_waitcnt lgkmcnt(2)\n\t" \
    "v_mfma_f32_16x16x32_bf16 v[204:207], v[84:87], v[240:243], v[204:207]\n\t" \
    "ds_read_b128 v[232:235], v253 offset:6144\n\t" \
    "v_mfma_f32_16x16x32_bf16 v[208:211], v[88:91], v[236:239], v[208:211]\n\t" \
    "v_mfma_f32_16x16x32_bf16 v[208:211], v[92:95], v[240:243], v[208:211]\n\t" \
    "s_waitcnt lgkmcnt(2)\n\t" \
    "v_mfma_f32_16x16x32_bf16 v[216:219], v[80:83], v[224:227], v[216:219]\n\t" \
    "ds_read_b128 v[236:239], v253 offset:7168\n\t" \
    "s_waitcnt lgkmcnt(2)\n\t" \
    "v_mfma_f32_16x16x32_bf16 v[216:219], v[84:87], v[228:231], v[216:219]\n\t" \
    "ds_read_b128 v[240:243], v253 offset:26624\n\t" \
    "v_mfma_f32_16x16x32_bf16 v[220:223], v[88:91], v[224:227], v[220:223]\n\t" \
    "v_mfma_f32_16x16x32_bf16 v[220:223], v[92:95], v[228:231], v[220:223]\n\t" \
    "s_waitcnt lgkmcnt(2)\n\t" \
    "v_mfma_f32_16x16x32_bf16 v[204:207], v[120:123], v[232:235], v[204:207]\n\t" \
    "ds_read_b128 v[224:227], v253 offset:27648\n\t" \
    "s_waitcnt lgkmcnt(2)\n\t" \
    "v_mfma_f32_16x16x32_bf16 v[204:207], v[124:127], v[236:239], v[204:207]\n\t" \
    "ds_read_b128 v[228:231], v253 offset:8192\n\t" \
    "v_mfma_f32_16x16x32_bf16 v[208:211], v[128:131], v[232:235], v[208:211]\n\t" \
    "v_mfma_f32_16x16x32_bf16 v[208:211], v[132:135], v[236:239], v[208:211]\n\t" \
    "s_waitcnt lgkmcnt(2)\n\t" \
    "v_mfma_f32_16x16x32_bf16 v[216:219], v[120:123], v[240:243], v[216:219]\n\t" \
    "ds_read_b128 v[232:235], v253 offset:9216\n\t" \
    "s_waitcnt lgkmcnt(2)\n\t" \
    "v_mfma_f32_16x16x32_bf16 v[216:219], v[124:127], v[224:227], v[216:219]\n\t" \
    "ds_read_b128 v[236:239], v253 offset:28672\n\t" \
    "v_mfma_f32_16x16x32_bf16 v[220:223], v[128:131], v[240:243], v[220:223]\n\t" \
    "v_mfma_f32_16x16x32_bf16 v[220:223], v[132:135], v[224:227], v[220:223]\n\t" \
    "s_waitcnt lgkmcnt(2)\n\t" \
    "v_mfma_f32_16x16x32_bf16 v[204:207], v[160:163], v[228:231], v[204:207]\n\t" \
    "ds_read_b128 v[240:243], v253 offset:29696\n\t" \
    "s_waitcnt lgkmcnt(2)\n\t" \
    "v_mfma_f32_16x16x32_bf16 v[204:207], v[164:167], v[232:235], v[204:207]\n\t" \
    "ds_read_b128 v[224:227], v253 offset:10240\n\t" \
    "v_mfma_f32_16x16x32_bf16 v[208:211], v[168:171], v[228:231], v[208:211]\n\t" \
    "v_mfma_f32_16x16x32_bf16 v[208:211], v[172:175], v[232:235], v[208:211]\n\t" \
    "s_waitcnt lgkmcnt(2)\n\t" \
    "v_mfma_f32_16x16x32_bf16 v[216:219], v[160:163], v[236:239], v[216:219]\n\t" \
    "ds_read_b128 v[228:231], v253 offset:11264\n\t" \
    "s_waitcnt lgkmcnt(2)\n\t" \
    "v_mfma_f32_16x16x32_bf16 v[216:219], v[164:167], v[240:243], v[216:219]\n\t" \
    "ds_read_b128 v[232:235], v253 offset:30720\n\t" \
    "v_mfma_f32_16x16x32_bf16 v[220:223], v[168:171], v[236:239], v[220:223]\n\t" \
    "v_mfma_f32_16x16x32_bf16 v[220:223], v[172:175], v[240:243], v[220:223]\n\t" \
    "ds_read_b128 v[236:239], v253 offset:31744\n\t" \
    "s_setprio 2\n\t" \
    "s_waitcnt lgkmcnt(0)\n\t" \
    "s_barrier \n\t" \
    LST7 \
    LJIT \
    "ds_read_b128 v[200:203], v253 offset:40960\n\t" \
    "ds_read_b128 v[212:215], v253 offset:41984\n\t" \
    "v_mfma_f32_16x16x32_bf16 v[204:207], v[24:27], v[224:227], v[204:207]\n\t" \
    "v_mfma_f32_16x16x32_bf16 v[204:207], v[28:31], v[228:231], v[204:207]\n\t" \
    "ds_read_b128 v[240:243], v253 offset:12288\n\t" \
    "v_mfma_f32_16x16x32_bf16 v[208:211], v[32:35], v[224:227], v[208:211]\n\t" \
    "v_mfma_f32_16x16x32_bf16 v[208:211], v[36:39], v[228:231], v[208:211]\n\t" \
    "s_waitcnt lgkmcnt(2)\n\t" \
    "v_mfma_f32_16x16x32_bf16 v[200:203], v[16:19], v[224:227], v[200:203]\n\t" \
    "v_mfma_f32_16x16x32_bf16 v[200:203], v[20:23], v[228:231], v[200:203]\n\t" \
    "v_mfma_f32_16x16x32_bf16 v[216:219], v[24:27], v[232:235], v[216:219]\n\t" \
    "ds_read_b128 v[224:227], v253 offset:13312\n\t" \
    "s_and_b32 s44, s40, 3\n\t" \
    "v_mfma_f32_16x16x32_bf16 v[216:219], v[28:31], v[236:239], v[216:219]\n\t" \
    "ds_read_b128 v[228:231], v253 offset:32768\n\t" \
    "s_lshl_b32 s44, s44, 17\n\t" \
    "v_mfma_f32_16x16x32_bf16 v[220:223], v[32:35], v[232:235], v[220:223]\n\t" \
    "s_add_u32 s44, s44, %[mb]\n\t" \
    "v_mfma_f32_16x16x32_bf16 v[220:223], v[36:39], v[236:239], v[220:223]\n\t" \
    "s_add_u32 s46, s44, 0x0\n\t" \
    "s_waitcnt lgkmcnt(3)\n\t" \
    "v_mfma_f32_16x16x32_bf16 v[212:215], v[16:19], v[232:235], v[212:215]\n\t" \
    "s_add_u32 s47, s44, 0x1400\n\t" \
    "v_mfma_f32_16x16x32_bf16 v[212:215], v[20:23], v[236:239], v[212:215]\n\t" \
    "s_add_u32 s48, s44, 0x400\n\t" \
    "s_waitcnt lgkmcnt(2)\n\t" \
    "v_mfma_f32_16x16x32_bf16 v[204:207], v[64:67], v[240:243], v[204:207]\n\t" \
    "ds_read_b128 v[232:235], v253 offset:33792\n\t" \
    "s_waitcnt lgkmcnt(2)\n\t" \
    "v_mfma_f32_16x16x32_bf16 v[204:207], v[68:71], v[224:227], v[204:207]\n\t" \
    "ds_read_b128 v[236:239], v253 offset:14336\n\t" \
    "v_mfma_f32_16x16x32_bf16 v[208:211], v[72:75], v[240:243], v[208:211]\n\t" \
    "v_mfma_f32_16x16x32_bf16 v[208:211], v[76:79], v[224:227], v[208:211]\n\t" \
    "v_mfma_f32_16x16x32_bf16 v[200:203], v[56:59], v[240:243], v[200:203]\n\t" \
    "v_mfma_f32_16x16x32_bf16 v[200:203], v[60:63], v[224:227], v[200:203]\n\t" \
    "s_waitcnt lgkmcnt(2)\n\t" \
    "v_mfma_f32_16x16x32_bf16 v[216:219], v[64:67], v[228:231], v[216:219]\n\t" \
    "ds_read_b128 v[240:243], v253 offset:15360\n\t" \
    "s_waitcnt lgkmcnt(2)\n\t" \
    "v_mfma_f32_16x16x32_bf16 v[216:219], v[68:71], v[232:235], v[216:219]\n\t" \
    "ds_read_b128 v[224:227], v253 offset:34816\n\t" \
    "v_mfma_f32_16x16x32_bf16 v[220:223], v[72:75], v[228:231], v[220:223]\n\t" \
    "v_mfma_f32_16x16x32_bf16 v[220:223], v[76:79], v[232:235], v[220:223]\n\t" \
    "v_mfma_f32_16x16x32_bf16 v[212:215], v[56:59], v[228:231], v[212:215]\n\t" \
    "v_mfma_f32_16x16x32_bf16 v[212:215], v[60:63], v[232:235], v[212:215]\n\t" \
    "s_waitcnt lgkmcnt(2)\n\t" \
    "v_mfma_f32_16x16x32_bf16 v[204:207], v[104:107], v[236:239], v[204:207]\n\t" \
    "ds_read_b128 v[228:231], v253 offset:35840\n\t" \
    "s_waitcnt lgkmcnt(2)\n\t" \
    "v_mfma_f32_16x16x32_bf16 v[204:207], v[108:111], v[240:243], v[204:207]\n\t" \
    "ds_read_b128 v[232:235], v253 offset:16384\n\t" \
    "v_mfma_f32_16x16x32_bf16 v[208:211], v[112:115], v[236:239], v[208:211]\n\t" \
    "v_mfma_f32_16x16x32_bf16 v[208:211], v[116:119], v[240:243], v[208:211]\n\t" \
    "v_mfma_f32_16x16x32_bf16 v[200:203], v[96:99], v[236:239], v[200:203]\n\t" \
    "v_mfma_f32_16x16x32_bf16 v[200:203], v[100:103], v[240:243], v[200:203]\n\t" \
    "s_waitcnt lgkmcnt(2)\n\t" \
    "v_mfma_f32_16x16x32_bf16 v[216:219], v[104:107], v[224:227], v[216:219]\n\t" \
    "ds_read_b128 v[236:239], v253 offset:17408\n\t" \
    "s_waitcnt lgkmcnt(2)\n\t" \
    "v_mfma_f32_16x16x32_bf16 v[216:219], v[108:111], v[228:231], v[216:219]\n\t" \
    "ds_read_b128 v[240:243], v253 offset:18432\n\t" \
    "v_mfma_f32_16x16x32_bf16 v[220:223], v[112:115], v[224:227], v[220:223]\n\t" \
    "v_mfma_f32_16x16x32_bf16 v[220:223], v[116:119], v[228:231], v[220:223]\n\t" \
    "v_mfma_f32_16x16x32_bf16 v[212:215], v[96:99], v[224:227], v[212:215]\n\t" \
    "v_mfma_f32_16x16x32_bf16 v[212:215], v[100:103], v[228:231], v[212:215]\n\t" \
    "s_waitcnt lgkmcnt(2)\n\t" \
    "v_mfma_f32_16x16x32_bf16 v[204:207], v[144:147], v[232:235], v[204:207]\n\t" \
    "ds_read_b128 v[224:227], v253 offset:19456\n\t" \
    "v_mov_b64 v[244:245], 0\n\t" \
    "s_waitcnt lgkmcnt(2)\n\t" \
    "v_mfma_f32_16x16x32_bf16 v[204:207], v[148:151], v[236:239], v[204:207]\n\t" \
    "ds_read_b128 v[228:231], v253 offset:36864\n\t" \
    "v_mov_b64 v[246:247], 0\n\t" \
    "v_mfma_f32_16x16x32_bf16 v[208:211], v[152:155], v[232:235], v[208:211]\n\t" \
    "v_mov_b64 v[248:249], 0\n\t" \
    "v_mfma_f32_16x16x32_bf16 v[208:211], v[156:159], v[236:239], v[208:211]\n\t" \
    "v_mov_b64 v[250:251], 0\n\t" \
    "v_mfma_f32_16x16x32_bf16 v[200:203], v[136:139], v[232:235], v[200:203]\n\t" \
    "v_mfma_f32_16x16x32_bf16 v[200:203], v[140:143], v[236:239], v[200:203]\n\t" \
    "s_waitcnt lgkmcnt(2)\n\t" \
    "v_mfma_f32_16x16x32_bf16 v[204:207], v[184:187], v[240:243], v[204:207]\n\t" \
    "ds_read_b128 v[232:235], v253 offset:37888\n\t" \
    "s_waitcnt lgkmcnt(2)\n\t" \
    "v_mfma_f32_16x16x32_bf16 v[204:207], v[188:191], v[224:227], v[204:207]\n\t" \
    "ds_read_b128 v[236:239], v253 offset:38912\n\t" \
    "v_mfma_f32_16x16x32_bf16 v[208:211], v[192:195], v[240:243], v[208:211]\n\t" \
    "v_mfma_f32_16x16x32_bf16 v[208:211], v[196:199], v[224:227], v[208:211]\n\t" \
    "v_mfma_f32_16x16x32_bf16 v[200:203], v[176:179], v[240:243], v[200:203]\n\t" \
    "v_mfma_f32_16x16x32_bf16 v[200:203], v[180:183], v[224:227], v[200:203]\n\t" \
    "ds_read_b128 v[240:243], v253 offset:39936\n\t" \
    "s_waitcnt lgkmcnt(3)\n\t" \
    "v_mfma_f32_16x16x32_bf16 v[216:219], v[144:147], v[228:231], v[216:219]\n\t" \
    "s_waitcnt lgkmcnt(2)\n\t" \
    "v_mfma_f32_16x16x32_bf16 v[216:219], v[148:151], v[232:235], v[216:219]\n\t" \
    "v_cvt_pk_bf16_f32 v204, v204, v208\n\t" \
    "v_cvt_pk_bf16_f32 v205, v205, v209\n\t" \
    "s_waitcnt lgkmcnt(1)\n\t" \
    "v_mfma_f32_16x16x32_bf16 v[216:219], v[184:187], v[236:239], v[216:219]\n\t" \
    "v_cvt_pk_bf16_f32 v206, v206, v210\n\t" \
    "v_cvt_pk_bf16_f32 v207, v207, v211\n\t" \
    "s_waitcnt lgkmcnt(0)\n\t" \
    "v_mfma_f32_16x16x32_bf16 v[216:219], v[188:191], v[240:243], v[216:219]\n\t" \
    "ds_write_b128 v254, v[204:207] offset:8192\n\t" \
    "v_mfma_f32_16x16x32_bf16 v[220:223], v[152:155], v[228:231], v[220:223]\n\t" \
    "v_mfma_f32_16x16x32_bf16 v[220:223], v[156:159], v[232:235], v[220:223]\n\t" \
    "v_cvt_pk_bf16_f32 v200, v200, v201\n\t" \
    "v_cvt_pk_bf16_f32 v201, v202, v203\n\t" \
    "v_mfma_f32_16x16x32_bf16 v[220:223], v[192:195], v[236:239], v[220:223]\n\t" \
    "ds_write_b64 v254, v[200:201] offset:6144\n\t" \
    "v_mfma_f32_16x16x32_bf16 v[220:223], v[196:199], v[240:243], v[220:223]\n\t" \
    "v_mfma_f32_16x16x32_bf16 v[212:215], v[136:139], v[228:231], v[212:215]\n\t" \
    "ds_read_b128 v[200:203], v254 offset:30720\n\t" \
    "ds_read_b128 v[204:207], v254 offset:31744\n\t" \
    "v_mfma_f32_16x16x32_bf16 v[212:215], v[140:143], v[232:235], v[212:215]\n\t" \
    "v_mov_b64 v[208:209], 0\n\t" \
    "v_mov_b64 v[210:211], 0\n\t" \
    "v_mfma_f32_16x16x32_bf16 v[212:215], v[176:179], v[236:239], v[212:215]\n\t" \
    "v_cvt_pk_bf16_f32 v216, v216, v220\n\t" \
    "v_cvt_pk_bf16_f32 v217, v217, v221\n\t" \
    "v_mfma_f32_16x16x32_bf16 v[212:215], v[180:183], v[240:243], v[212:215]\n\t" \
    "v_cvt_pk_bf16_f32 v218, v218, v222\n\t" \
    "v_cvt_pk_bf16_f32 v219, v219, v223\n\t" \
    "s_setprio 0\n\t" \
    "ds_write_b128 v254, v[216:219] offset:14336\n\t" \
    "ds_read_b128 v[216:219], v254 offset:32768\n\t" \
    "ds_read_b128 v[220:223], v254 offset:33792\n\t" \
    "s_nop 2\n\t" \
    "v_cvt_pk_bf16_f32 v212, v212, v213\n\t" \
    "v_cvt_pk_bf16_f32 v213, v214, v215\n\t" \
    "ds_write_b64 v254, v[212:213] offset:12288\n\t" \
    LST6 \
    LJIT \
    "s_sleep 3\n\t" \
    "s_mov_b32 m0, 0xa000\n\t" \
    "s_nop 0\n\t" \
    "buffer_load_dwordx4 v253, s[60:63], s46 offen sc1 lds\n\t" \
    "s_mov_b32 m0, 0xb400\n\t" \
    "s_nop 0\n\t" \
    "buffer_load_dwordx4 v253, s[60:63], s47 offen sc1 lds\n\t" \
    "s_mov_b32 m0, 0xa400\n\t" \
    "s_nop 0\n\t" \
    "buffer_load_dwordx4 v253, s[60:63], s48 offen sc1 lds\n\t" \
    "s_waitcnt lgkmcnt(0)\n\t" \
    LFLUSH212 \
    "v_bfe_i32 v212, v253, 5, 1\n\t" \
    "v_and_b32 v212, 0xaaaa, v212\n\t" \
    "v_or_b32 v212, 0x4444, v212\n\t" \
    "v_and_b32 v213, 0xffff, v255\n\t" \
    "v_lshrrev_b32 v214, 16, v255\n\t" \
    ".Ls04_%=:\n\t" \
    "s_waitcnt vmcnt(0)\n\t" \
    "ds_read_b128 v[224:227], v253 offset:40960\n\t" \
    "ds_read_b128 v[228:231], v253 offset:46080\n\t" \
    "ds_read_b128 v[232:235], v253 offset:41984\n\t" \
    "s_waitcnt lgkmcnt(2)\n\t" \
    "v_max_u32 v215, v224, v225\n\t" \
    "v_max3_u32 v215, v226, v227, v215\n\t" \
    "s_waitcnt lgkmcnt(1)\n\t" \
    "v_max3_u32 v215, v228, v229, v215\n\t" \
    "v_max3_u32 v215, v230, v231, v215\n\t" \
    "s_waitcnt lgkmcnt(0)\n\t" \
    "v_max3_u32 v215, v232, v233, v215\n\t" \
    "v_max3_u32 v215, v234, v235, v215\n\t" \
    "v_cmp_eq_u32 vcc, -1, v215\n\t" \
    "s_cbranch_vccz .Ls05_%=\n\t" \
    "v_max_u32 v215, v224, v225\n\t" \
    "v_max3_u32 v215, v226, v227, v215\n\t" \
    "v_cmp_eq_u32 vcc, -1, v215\n\t" \
    "s_cbranch_vccz .Ls06_%=\n\t" \
    "s_mov_b32 m0, 0xa000\n\t" \
    "s_nop 0\n\t" \
    "buffer_load_dwordx4 v253, s[60:63], s46 offen sc1 lds\n\t" \
    ".Ls06_%=:\n\t" \
    "v_max_u32 v215, v228, v229\n\t" \
    "v_max3_u32 v215, v230, v231, v215\n\t" \
    "v_cmp_eq_u32 vcc, -1, v215\n\t" \
    "s_cbranch_vccz .Ls07_%=\n\t" \
    "s_mov_b32 m0, 0xb400\n\t" \
    "s_nop 0\n\t" \
    "buffer_load_dwordx4 v253, s[60:63], s47 offen sc1 lds\n\t" \
    ".Ls07_%=:\n\t" \
    "v_max_u32 v215, v232, v233\n\t" \
    "v_max3_u32 v215, v234, v235, v215\n\t" \
    "v_cmp_eq_u32 vcc, -1, v215\n\t" \
    "s_cbranch_vccz .Ls08_%=\n\t" \
    "s_mov_b32 m0, 0xa400\n\t" \
    "s_nop 0\n\t" \
    "buffer_load_dwordx4 v253, s[60:63], s48 offen sc1 lds\n\t" \
    ".Ls08_%=:\n\t" \
    "s_cmp_eq_u32 s41, 0\n\t" \
    "s_cbranch_scc1 .Ls05_%=\n\t" \
    "s_sub_u32 s41, s41, 1\n\t" \
    "s_branch .Ls04_%=\n\t" \
    ".Ls05_%=:\n\t" \
    LST2 \
    LJIT \
    "s_setprio 3\n\t" \
    "s_waitcnt lgkmcnt(0)\n\t" \
    "s_barrier \n\t" \
    LST3 \
    LJIT \
    "ds_read_b128 v[236:239], v253 offset:47104\n\t" \
    "ds_read_b128 v[240:243], v253 offset:43008\n\t" \
    "v_smfmac_f32_16x16x64_bf16 v[244:247], v[224:227], v[0:7], v212\n\t" \
    "v_smfmac_f32_16x16x64_bf16 v[248:251], v[224:227], v[8:15], v212\n\t" \
    "v_smfmac_f32_16x16x64_bf16 v[208:211], v[224:227], v[200:207], v212\n\t" \
    "ds_read_b128 v[200:203], v254 offset:34816\n\t" \
    "ds_read_b128 v[204:207], v254 offset:35840\n\t" \
    "ds_read_b128 v[224:227], v253 offset:48128\n\t" \
    "v_smfmac_f32_16x16x64_bf16 v[208:211], v[228:231], v[16:23], v212\n\t" \
    "v_smfmac_f32_16x16x64_bf16 v[244:247], v[228:231], v[24:31], v212\n\t" \
    "v_smfmac_f32_16x16x64_bf16 v[248:251], v[228:231], v[32:39], v212\n\t" \
    "ds_read_b128 v[228:231], v253 offset:44032\n\t" \
    "v_smfmac_f32_16x16x64_bf16 v[208:211], v[232:235], v[216:223], v212\n\t" \
    "ds_read_b128 v[216:219], v254 offset:36864\n\t" \
    "ds_read_b128 v[220:223], v254 offset:37888\n\t" \
    "v_smfmac_f32_16x16x64_bf16 v[244:247], v[232:235], v[40:47], v212\n\t" \
    "v_smfmac_f32_16x16x64_bf16 v[248:251], v[232:235], v[48:55], v212\n\t" \
    "ds_read_b128 v[232:235], v253 offset:49152\n\t" \
    "s_waitcnt lgkmcnt(8)\n\t" \
    "v_smfmac_f32_16x16x64_bf16 v[208:211], v[236:239], v[56:63], v212\n\t" \
    "v_smfmac_f32_16x16x64_bf16 v[244:247], v[236:239], v[64:71], v212\n\t" \
    "v_smfmac_f32_16x16x64_bf16 v[248:251], v[236:239], v[72:79], v212\n\t" \
    "ds_read_b128 v[236:239], v253 offset:45056\n\t" \
    "s_waitcnt lgkmcnt(6)\n\t" \
    "v_smfmac_f32_16x16x64_bf16 v[208:211], v[240:243], v[200:207], v212\n\t" \
    "ds_read_b128 v[200:203], v254 offset:38912\n\t" \
    "ds_read_b128 v[204:207], v254 offset:39936\n\t" \
    "v_smfmac_f32_16x16x64_bf16 v[244:247], v[240:243], v[80:87], v212\n\t" \
    "v_smfmac_f32_16x16x64_bf16 v[248:251], v[240:243], v[88:95], v212\n\t" \
    "ds_read_b128 v[240:243], v253 offset:50176\n\t" \
    "s_waitcnt lgkmcnt(8)\n\t" \
    "v_smfmac_f32_16x16x64_bf16 v[208:211], v[224:227], v[96:103], v212\n\t" \
    "v_smfmac_f32_16x16x64_bf16 v[244:247], v[224:227], v[104:111], v212\n\t" \
    "v_smfmac_f32_16x16x64_bf16 v[248:251], v[224:227], v[112:119], v212\n\t" \
    "s_waitcnt lgkmcnt(5)\n\t" \
    "v_smfmac_f32_16x16x64_bf16 v[208:211], v[228:231], v[216:223], v212\n\t" \
    "s_waitcnt lgkmcnt(4)\n\t" \
    "v_smfmac_f32_16x16x64_bf16 v[208:211], v[232:235], v[136:143], v212\n\t" \
    "s_waitcnt lgkmcnt(1)\n\t" \
    "v_smfmac_f32_16x16x64_bf16 v[208:211], v[236:239], v[200:207], v212\n\t" \
    "s_waitcnt lgkmcnt(0)\n\t" \
    "v_smfmac_f32_16x16x64_bf16 v[208:211], v[240:243], v[176:183], v212\n\t" \
    "v_smfmac_f32_16x16x64_bf16 v[244:247], v[228:231], v[120:127], v212\n\t" \
    "v_smfmac_f32_16x16x64_bf16 v[244:247], v[232:235], v[144:151], v212\n\t" \
    "v_smfmac_f32_16x16x64_bf16 v[244:247], v[236:239], v[160:167], v212\n\t" \
    "v_add_f32 v208, v208, v210\n\t" \
    "v_add_f32 v209, v209, v211\n\t" \
    "v_cvt_pk_bf16_f32 v208, v208, v209\n\t" \
    "v_smfmac_f32_16x16x64_bf16 v[244:247], v[240:243], v[184:191], v212\n\t" \
    "ds_write_b16 v213, v208 offset:0\n\t" \
    "ds_write_b16_d16_hi v213, v208 offset:16\n\t" \
    "v_smfmac_f32_16x16x64_bf16 v[248:251], v[228:231], v[128:135], v212\n\t" \
    "v_smfmac_f32_16x16x64_bf16 v[248:251], v[232:235], v[152:159], v212\n\t" \
    "v_smfmac_f32_16x16x64_bf16 v[248:251], v[236:239], v[168:175], v212\n\t" \
    "v_add_f32 v244, v244, v246\n\t" \
    "v_add_f32 v245, v245, v247\n\t" \
    "v_smfmac_f32_16x16x64_bf16 v[248:251], v[240:243], v[192:199], v212\n\t" \
    "s_nop 7\n\t" \
    "v_add_f32 v248, v248, v250\n\t" \
    "v_add_f32 v249, v249, v251\n\t" \
    "v_cvt_pk_bf16_f32 v244, v244, v248\n\t" \
    "v_cvt_pk_bf16_f32 v245, v245, v249\n\t" \
    "ds_write2_b32 v214, v244, v245 offset1:4\n\t" \
    "s_setprio 0\n\t" \
    "s_sleep 2\n\t" \
    "ds_read_b128 v[200:203], v254 offset:18432\n\t" \
    "ds_read_b128 v[204:207], v254 offset:19456\n\t" \
    "ds_read_b128 v[214:217], v254 offset:24576\n\t" \
    "ds_read_b128 v[218:221], v254 offset:25600\n\t" \
    "ds_read_b128 v[222:225], v254 offset:20480\n\t" \
    "ds_read_b128 v[226:229], v254 offset:21504\n\t" \
    "ds_read_b128 v[230:233], v254 offset:26624\n\t" \
    "ds_read_b128 v[234:237], v254 offset:27648\n\t" \
    "v_and_b32 v213, 0xffffff7f, v254\n\t" \
    "v_mov_b64 v[242:243], 0\n\t" \
    "v_mov_b64 v[244:245], 0\n\t" \
    "v_mov_b64 v[246:247], 0\n\t" \
    "v_mov_b64 v[248:249], 0\n\t" \
    "v_mov_b64 v[250:251], 0\n\t" \
    "v_mov_b64 v[252:253], 0\n\t" \
    "s_and_b32 s44, s40, 3\n\t" \
    "s_mul_i32 s44, s44, 0x30000\n\t" \
    "s_add_u32 s50, s44, %[pb]\n\t" \
    "s_add_u32 s51, s50, 0xc000\n\t" \
    "s_add_u32 s52, s50, 0x18000\n\t" \
    "s_add_u32 s53, s50, 0x24000\n\t" \
    "s_setprio 3\n\t" \
    "s_waitcnt lgkmcnt(0)\n\t" \
    "s_barrier \n\t" \
    LST4 \
    LJIT \
    "ds_read_b128 v[208:211], v254 offset:0\n\t" \
    "ds_read_b128 v[238:241], v254 offset:1024\n\t" \
    "s_waitcnt lgkmcnt(1)\n\t" \
    "v_smfmac_f32_16x16x64_bf16 v[242:245], v[208:211], v[200:207], v212\n\t" \
    "ds_read_b128 v[200:203], v213 offset:3072\n\t" \
    "ds_read_b128 v[204:207], v213 offset:3200\n\t" \
    "v_smfmac_f32_16x16x64_bf16 v[246:249], v[208:211], v[214:221], v212\n\t" \
    "ds_read_b128 v[214:217], v213 offset:4096\n\t" \
    "ds_read_b128 v[218:221], v213 offset:4224\n\t" \
    "s_waitcnt lgkmcnt(4)\n\t" \
    "v_smfmac_f32_16x16x64_bf16 v[242:245], v[238:241], v[222:229], v212\n\t" \
    "ds_read_b128 v[222:225], v254 offset:22528\n\t" \
    "ds_read_b128 v[226:229], v254 offset:23552\n\t" \
    "v_smfmac_f32_16x16x64_bf16 v[246:249], v[238:241], v[230:237], v212\n\t" \
    "ds_read_b128 v[230:233], v254 offset:28672\n\t" \
    "ds_read_b128 v[234:237], v254 offset:29696\n\t" \
    "s_waitcnt lgkmcnt(6)\n\t" \
    "v_smfmac_f32_16x16x64_bf16 v[250:253], v[208:211], v[200:207], v212\n\t" \
    "ds_read_b128 v[208:211], v254 offset:2048\n\t" \
    "ds_read_b128 v[200:203], v213 offset:5120\n\t" \
    "ds_read_b128 v[204:207], v213 offset:5248\n\t" \
    "s_waitcnt lgkmcnt(7)\n\t" \
    "v_smfmac_f32_16x16x64_bf16 v[250:253], v[238:241], v[214:221], v212\n\t" \
    "v_lshrrev_b32 v238, 3, v254\n\t" \
    "v_and_b32 v238, 0x6e, v238\n\t" \
    "v_lshrrev_b32 v239, 16, v255\n\t" \
    "s_waitcnt lgkmcnt(2)\n\t" \
    "v_smfmac_f32_16x16x64_bf16 v[242:245], v[208:211], v[222:229], v212\n\t" \
    "v_smfmac_f32_16x16x64_bf16 v[246:249], v[208:211], v[230:237], v212\n\t" \
    "s_waitcnt lgkmcnt(0)\n\t" \
    "v_smfmac_f32_16x16x64_bf16 v[250:253], v[208:211], v[200:207], v212\n\t" \
    "s_nop 1\n\t" \
    "v_add_f32 v242, v242, v244\n\t" \
    "v_add_f32 v243, v243, v245\n\t" \
    "s_nop 0\n\t" \
    "v_add_f32 v246, v246, v248\n\t" \
    "v_add_f32 v247, v247, v249\n\t" \
    "s_nop 0\n\t" \
    "v_add_f32 v250, v250, v252\n\t" \
    "v_add_f32 v251, v251, v253\n\t" \
    "v_max3_f32 v244, v242, v246, v250\n\t" \
    "v_max3_f32 v245, v243, v247, v251\n\t" \
    "s_nop 0\n\t" \
    "v_max_f32_dpp v244, v244, v244 quad_perm:[1,0,3,2] row_mask:0xf bank_mask:0xf\n\t" \
    "v_max_f32_dpp v245, v245, v245 quad_perm:[1,0,3,2] row_mask:0xf bank_mask:0xf\n\t" \
    "s_nop 0\n\t" \
    "v_max_f32_dpp v244, v244, v244 quad_perm:[2,3,0,1] row_mask:0xf bank_mask:0xf\n\t" \
    "v_max_f32_dpp v245, v245, v245 quad_perm:[2,3,0,1] row_mask:0xf bank_mask:0xf\n\t" \
    "s_nop 0\n\t" \
    "v_max_f32_dpp v244, v244, v244 row_half_mirror row_mask:0xf bank_mask:0xf\n\t" \
    "v_max_f32_dpp v245, v245, v245 row_half_mirror row_mask:0xf bank_mask:0xf\n\t" \
    "s_nop 0\n\t" \
    "v_max_f32_dpp v244, v244, v244 row_mirror row_mask:0xf bank_mask:0xf\n\t" \
    "v_max_f32_dpp v245, v245, v245 row_mirror row_mask:0xf bank_mask:0xf\n\t" \
    "v_mul_f32 v244, %[scl], v244\n\t" \
    "v_mul_f32 v245, %[scl], v245\n\t" \
    "v_fma_f32 v242, v242, %[scl], -v244\n\t" \
    "v_fma_f32 v243, v243, %[scl], -v245\n\t" \
    "v_fma_f32 v246, v246, %[scl], -v244\n\t" \
    "v_fma_f32 v247, v247, %[scl], -v245\n\t" \
    "v_exp_f32 v242, v242\n\t" \
    "v_exp_f32 v243, v243\n\t" \
    "v_fma_f32 v250, v250, %[scl], -v244\n\t" \
    "v_fma_f32 v251, v251, %[scl], -v245\n\t" \
    "v_exp_f32 v246, v246\n\t" \
    "v_exp_f32 v247, v247\n\t" \
    "v_exp_f32 v250, v250\n\t" \
    "v_exp_f32 v251, v251\n\t" \
    "v_add_f32 v244, v242, v246\n\t" \
    "v_add_f32 v245, v243, v247\n\t" \
    "v_fma_f32 v244, v250, 0.5, v244\n\t" \
    "v_fma_f32 v245, v251, 0.5, v245\n\t" \
    "s_nop 0\n\t" \
    "v_add_f32_dpp v244, v244, v244 quad_perm:[1,0,3,2] row_mask:0xf bank_mask:0xf\n\t" \
    "v_add_f32_dpp v245, v245, v245 quad_perm:[1,0,3,2] row_mask:0xf bank_mask:0xf\n\t" \
    "s_nop 0\n\t" \
    "v_add_f32_dpp v244, v244, v244 quad_perm:[2,3,0,1] row_mask:0xf bank_mask:0xf\n\t" \
    "v_add_f32_dpp v245, v245, v245 quad_perm:[2,3,0,1] row_mask:0xf bank_mask:0xf\n\t" \
    "s_nop 0\n\t" \
    "v_add_f32_dpp v244, v244, v244 row_half_mirror row_mask:0xf bank_mask:0xf\n\t" \
    "v_add_f32_dpp v245, v245, v245 row_half_mirror row_mask:0xf bank_mask:0xf\n\t" \
    "s_nop 0\n\t" \
    "v_add_f32_dpp v244, v244, v244 row_mirror row_mask:0xf bank_mask:0xf\n\t" \
    "v_add_f32_dpp v245, v245, v245 row_mirror row_mask:0xf bank_mask:0xf\n\t" \
    "v_rcp_f32 v244, v244\n\t" \
    "v_rcp_f32 v245, v245\n\t" \
    "s_wakeup\n\t" \
    "v_mul_f32 v250, v250, v244\n\t" \
    "v_mul_f32 v251, v251, v245\n\t" \
    "v_mul_f32 v242, v242, v244\n\t" \
    "v_mul_f32 v243, v243, v245\n\t" \
    "v_mul_f32 v246, v246, v244\n\t" \
    "v_mul_f32 v247, v247, v245\n\t" \
    "v_cvt_pk_bf16_f32 v250, |v250|, |v251|\n\t" \
    "ds_write_b16 v238, v250 offset:52096\n\t" \
    "ds_write_b16_d16_hi v238, v250 offset:52112\n\t" \
    "v_cvt_pk_bf16_f32 v242, |v242|, |v246|\n\t" \
    "v_cvt_pk_bf16_f32 v243, |v243|, |v247|\n\t" \
    "ds_write2_b32 v239, v242, v243 offset1:4\n\t" \
    "ds_read_b128 v[200:203], v254 offset:896\n\t" \
    "v_and_b32 v253, 0x3ff, v254\n\t" \
    "s_mov_b32 exec_hi, 0xff\n\t" \
    "s_waitcnt lgkmcnt(0)\n\t" \
    "buffer_store_dwordx4 v[200:203], v254, s[60:63], s50 offen sc1\n\t" \
    "buffer_store_dwordx4 v[200:203], v254, s[60:63], s51 offen sc1\n\t" \
    "buffer_store_dwordx4 v[200:203], v254, s[60:63], s52 offen sc1\n\t" \
    "buffer_store_dwordx4 v[200:203], v254, s[60:63], s53 offen sc1\n\t" \
    "s_mov_b32 exec_hi, -1\n\t" \
    "s_setprio 0\n\t" \
    LST5 \
    LJIT \
    "ds_read_b128 v[224:227], v253 offset:0\n\t" \
    "ds_read_b128 v[228:231], v253 offset:1024\n\t" \
    "ds_read_b128 v[232:235], v253 offset:20480\n\t" \
    "ds_read_b128 v[236:239], v253 offset:21504\n\t" \
    "s_add_u32 s40, s40, 1\n\t" \
    "s_cmp_lg_u32 s40, 64\n\t" \
    "s_cbranch_scc1 .Ls03_%=\n\t" \
    "s_branch .Lend%=\n\t" \
    ".Lend%=:\n\t" \
    "s_waitcnt vmcnt(0) lgkmcnt(0)\n\t" \
    "s_mov_b32 %[bud], s41\n\t"
#define ASM_V_LOOP \
    "s_mov_b64 s[60:61], %[ws]\n\t" \
    "s_mov_b32 s62, %[sz]\n\t" \
    "s_mov_b32 s63, 0x20000\n\t" \
    "s_mov_b32 s40, 0\n\t" \
    "s_mov_b32 s41, 0x20000\n\t" \
    LSTINIT \
    LDELAY \
    "s_cmp_eq_u32 %[wave], 0\n\t" \
    "s_cbranch_scc1 .Lv0_%=\n\t" \
    "s_cmp_eq_u32 %[wave], 1\n\t" \
    "s_cbranch_scc1 .Lv1_%=\n\t" \
    "s_cmp_eq_u32 %[wave], 2\n\t" \
    "s_cbranch_scc1 .Lv2_%=\n\t" \
    "s_cmp_eq_u32 %[wave], 3\n\t" \
    "s_cbranch_scc1 .Lv3_%=\n\t" \
    ".Lv4_%=:\n\t" \
    "s_add_u32 m0, %[wm], 0x0\n\t" \
    "s_mov_b32 s44, 0x0\n\t" \
    "buffer_load_dwordx4 v240, %[wr], s44 offen offset:0 nt lds\n\t" \
    "buffer_load_dwordx4 v241, %[wr], s44 offen offset:1024 nt lds\n\t" \
    "buffer_load_dwordx4 v242, %[wr], s44 offen offset:2048 nt lds\n\t" \
    "buffer_load_dwordx4 v243, %[wr], s44 offen offset:3072 nt lds\n\t" \
    "s_add_u32 m0, %[wm], 0x1000\n\t" \
    "s_nop 0\n\t" \
    "buffer_load_dwordx4 v244, %[wr], s44 offen nt lds\n\t" \
    "s_add_u32 m0, %[wm], 0x1400\n\t" \
    "s_mov_b32 s44, 0x5000\n\t" \
    "buffer_load_dwordx4 v240, %[wr], s44 offen offset:0 nt lds\n\t" \
    "buffer_load_dwordx4 v241, %[wr], s44 offen offset:1024 nt lds\n\t" \
    "buffer_load_dwordx4 v242, %[wr], s44 offen offset:2048 nt lds\n\t" \
    "buffer_load_dwordx4 v243, %[wr], s44 offen offset:3072 nt lds\n\t" \
    "s_add_u32 m0, %[wm], 0x2400\n\t" \
    "s_nop 0\n\t" \
    "buffer_load_dwordx4 v244, %[wr], s44 offen nt lds\n\t" \
    "s_add_u32 m0, %[wm], 0x2800\n\t" \
    "s_mov_b32 s44, 0xa000\n\t" \
    "buffer_load_dwordx4 v240, %[wr], s44 offen offset:0 nt lds\n\t" \
    "buffer_load_dwordx4 v241, %[wr], s44 offen offset:1024 nt lds\n\t" \
    "buffer_load_dwordx4 v242, %[wr], s44 offen offset:2048 nt lds\n\t" \
    "buffer_load_dwordx4 v243, %[wr], s44 offen offset:3072 nt lds\n\t" \
    "s_add_u32 m0, %[wm], 0x3800\n\t" \
    "s_nop 0\n\t" \
    "buffer_load_dwordx4 v244, %[wr], s44 offen nt lds\n\t" \
    "s_add_u32 m0, %[wm], 0x3c00\n\t" \
    "s_mov_b32 s44, 0xf000\n\t" \
    "buffer_load_dwordx4 v240, %[wr], s44 offen offset:0 nt lds\n\t" \
    "buffer_load_dwordx4 v241, %[wr], s44 offen offset:1024 nt lds\n\t" \
    "buffer_load_dwordx4 v242, %[wr], s44 offen offset:2048 nt lds\n\t" \
    "buffer_load_dwordx4 v243, %[wr], s44 offen offset:3072 nt lds\n\t" \
    "s_add_u32 m0, %[wm], 0x4c00\n\t" \
    "s_nop 0\n\t" \
    "buffer_load_dwordx4 v244, %[wr], s44 offen nt lds\n\t" \
    "s_barrier \n\t" \
    "s_waitcnt vmcnt(15)\n\t" \
    "ds_read_b64_tr_b16 v[200:201], v245 offset:0\n\t" \
    "ds_read_b64_tr_b16 v[16:17], v245 offset:32\n\t" \
    "ds_read_b64_tr_b16 v[208:209], v245 offset:64\n\t" \
    "ds_read_b64_tr_b16 v[56:57], v245 offset:96\n\t" \
    "ds_read_b64_tr_b16 v[216:217], v245 offset:128\n\t" \
    "ds_read_b64_tr_b16 v[96:97], v245 offset:160\n\t" \
    "ds_read_b64_tr_b16 v[224:225], v245 offset:192\n\t" \
    "ds_read_b64_tr_b16 v[136:137], v245 offset:224\n\t" \
    "ds_read_b64_tr_b16 v[232:233], v245 offset:256\n\t" \
    "ds_read_b64_tr_b16 v[176:177], v245 offset:288\n\t" \
    "s_waitcnt lgkmcnt(0)\n\t" \
    "s_add_u32 m0, %[wm], 0x0\n\t" \
    "s_mov_b32 s44, 0x14000\n\t" \
    "buffer_load_dwordx4 v240, %[wr], s44 offen offset:0 nt lds\n\t" \
    "buffer_load_dwordx4 v241, %[wr], s44 offen offset:1024 nt lds\n\t" \
    "buffer_load_dwordx4 v242, %[wr], s44 offen offset:2048 nt lds\n\t" \
    "buffer_load_dwordx4 v243, %[wr], s44 offen offset:3072 nt lds\n\t" \
    "s_add_u32 m0, %[wm], 0x1000\n\t" \
    "s_nop 0\n\t" \
    "buffer_load_dwordx4 v244, %[wr], s44 offen nt lds\n\t" \
    "s_barrier \n\t" \
    "s_barrier \n\t" \
    "s_waitcnt vmcnt(15)\n\t" \
    "ds_read_b64_tr_b16 v[204:205], v245 offset:5120\n\t" \
    "ds_read_b64_tr_b16 v[20:21], v245 offset:5152\n\t" \
    "ds_read_b64_tr_b16 v[212:213], v245 offset:5184\n\t" \
    "ds_read_b64_tr_b16 v[60:61], v245 offset:5216\n\t" \
    "ds_read_b64_tr_b16 v[220:221], v245 offset:5248\n\t" \
    "ds_read_b64_tr_b16 v[100:101], v245 offset:5280\n\t" \
    "ds_read_b64_tr_b16 v[228:229], v245 offset:5312\n\t" \
    "ds_read_b64_tr_b16 v[140:141], v245 offset:5344\n\t" \
    "ds_read_b64_tr_b16 v[236:237], v245 offset:5376\n\t" \
    "ds_read_b64_tr_b16 v[180:181], v245 offset:5408\n\t" \
    "s_waitcnt lgkmcnt(0)\n\t" \
    "s_add_u32 m0, %[wm], 0x1400\n\t" \
    "s_mov_b32 s44, 0x19000\n\t" \
    "buffer_load_dwordx4 v240, %[wr], s44 offen offset:0 nt lds\n\t" \
    "buffer_load_dwordx4 v241, %[wr], s44 offen offset:1024 nt lds\n\t" \
    "buffer_load_dwordx4 v242, %[wr], s44 offen offset:2048 nt lds\n\t" \
    "buffer_load_dwordx4 v243, %[wr], s44 offen offset:3072 nt lds\n\t" \
    "s_add_u32 m0, %[wm], 0x2400\n\t" \
    "s_nop 0\n\t" \
    "buffer_load_dwordx4 v244, %[wr], s44 offen nt lds\n\t" \
    "s_barrier \n\t" \
    "s_barrier \n\t" \
    "s_waitcnt vmcnt(15)\n\t" \
    "ds_read_b64_tr_b16 v[0:1], v245 offset:10240\n\t" \
    "ds_read_b64_tr_b16 v[24:25], v245 offset:10272\n\t" \
    "ds_read_b64_tr_b16 v[40:41], v245 offset:10304\n\t" \
    "ds_read_b64_tr_b16 v[64:65], v245 offset:10336\n\t" \
    "ds_read_b64_tr_b16 v[80:81], v245 offset:10368\n\t" \
    "ds_read_b64_tr_b16 v[104:105], v245 offset:10400\n\t" \
    "ds_read_b64_tr_b16 v[120:121], v245 offset:10432\n\t" \
    "ds_read_b64_tr_b16 v[144:145], v245 offset:10464\n\t" \
    "ds_read_b64_tr_b16 v[160:161], v245 offset:10496\n\t" \
    "ds_read_b64_tr_b16 v[184:185], v245 offset:10528\n\t" \
    "s_waitcnt lgkmcnt(0)\n\t" \
    "s_add_u32 m0, %[wm], 0x2800\n\t" \
    "s_mov_b32 s44, 0x1e000\n\t" \
    "buffer_load_dwordx4 v240, %[wr], s44 offen offset:0 nt lds\n\t" \
    "buffer_load_dwordx4 v241, %[wr], s44 offen offset:1024 nt lds\n\t" \
    "buffer_load_dwordx4 v242, %[wr], s44 offen offset:2048 nt lds\n\t" \
    "buffer_load_dwordx4 v243, %[wr], s44 offen offset:3072 nt lds\n\t" \
    "s_add_u32 m0, %[wm], 0x3800\n\t" \
    "s_nop 0\n\t" \
    "buffer_load_dwordx4 v244, %[wr], s44 offen nt lds\n\t" \
    "s_barrier \n\t" \
    "s_waitcnt vmcnt(15)\n\t" \
    "ds_read_b64_tr_b16 v[4:5], v245 offset:15360\n\t" \
    "ds_read_b64_tr_b16 v[28:29], v245 offset:15392\n\t" \
    "ds_read_b64_tr_b16 v[44:45], v245 offset:15424\n\t" \
    "ds_read_b64_tr_b16 v[68:69], v245 offset:15456\n\t" \
    "ds_read_b64_tr_b16 v[84:85], v245 offset:15488\n\t" \
    "ds_read_b64_tr_b16 v[108:109], v245 offset:15520\n\t" \
    "ds_read_b64_tr_b16 v[124:125], v245 offset:15552\n\t" \
    "ds_read_b64_tr_b16 v[148:149], v245 offset:15584\n\t" \
    "ds_read_b64_tr_b16 v[164:165], v245 offset:15616\n\t" \
    "ds_read_b64_tr_b16 v[188:189], v245 offset:15648\n\t" \
    "s_waitcnt lgkmcnt(0)\n\t" \
    "s_add_u32 m0, %[wm], 0x3c00\n\t" \
    "s_mov_b32 s44, 0x23000\n\t" \
    "buffer_load_dwordx4 v240, %[wr], s44 offen offset:0 nt lds\n\t" \
    "buffer_load_dwordx4 v241, %[wr], s44 offen offset:1024 nt lds\n\t" \
    "buffer_load_dwordx4 v242, %[wr], s44 offen offset:2048 nt lds\n\t" \
    "buffer_load_dwordx4 v243, %[wr], s44 offen offset:3072 nt lds\n\t" \
    "s_add_u32 m0, %[wm], 0x4c00\n\t" \
    "s_nop 0\n\t" \
    "buffer_load_dwordx4 v244, %[wr], s44 offen nt lds\n\t" \
    "s_barrier \n\t" \
    "s_barrier \n\t" \
    "s_waitcnt vmcnt(15)\n\t" \
    "ds_read_b64_tr_b16 v[8:9], v245 offset:0\n\t" \
    "ds_read_b64_tr_b16 v[32:33], v245 offset:32\n\t" \
    "ds_read_b64_tr_b16 v[48:49], v245 offset:64\n\t" \
    "ds_read_b64_tr_b16 v[72:73], v245 offset:96\n\t" \
    "ds_read_b64_tr_b16 v[88:89], v245 offset:128\n\t" \
    "ds_read_b64_tr_b16 v[112:113], v245 offset:160\n\t" \
    "ds_read_b64_tr_b16 v[128:129], v245 offset:192\n\t" \
    "ds_read_b64_tr_b16 v[152:153], v245 offset:224\n\t" \
    "ds_read_b64_tr_b16 v[168:169], v245 offset:256\n\t" \
    "ds_read_b64_tr_b16 v[192:193], v245 offset:288\n\t" \
    "v_mov_b32 v246, 0\n\t" \
    "buffer_load_dword v246, v246, s[60:63], %[mo] offen sc1\n\t" \
    "s_waitcnt lgkmcnt(0)\n\t" \
    "s_add_u32 m0, %[wm], 0x0\n\t" \
    "s_mov_b32 s44, 0x28000\n\t" \
    "buffer_load_dwordx4 v240, %[wr], s44 offen offset:0 nt lds\n\t" \
    "buffer_load_dwordx4 v241, %[wr], s44 offen offset:1024 nt lds\n\t" \
    "buffer_load_dwordx4 v242, %[wr], s44 offen offset:2048 nt lds\n\t" \
    "buffer_load_dwordx4 v243, %[wr], s44 offen offset:3072 nt lds\n\t" \
    "s_add_u32 m0, %[wm], 0x1000\n\t" \
    "s_nop 0\n\t" \
    "buffer_load_dwordx4 v244, %[wr], s44 offen nt lds\n\t" \
    "s_barrier \n\t" \
    "s_barrier \n\t" \
    "s_waitcnt vmcnt(15)\n\t" \
    "ds_read_b64_tr_b16 v[12:13], v245 offset:5120\n\t" \
    "ds_read_b64_tr_b16 v[36:37], v245 offset:5152\n\t" \
    "ds_read_b64_tr_b16 v[52:53], v245 offset:5184\n\t" \
    "ds_read_b64_tr_b16 v[76:77], v245 offset:5216\n\t" \
    "ds_read_b64_tr_b16 v[92:93], v245 offset:5248\n\t" \
    "ds_read_b64_tr_b16 v[116:117], v245 offset:5280\n\t" \
    "ds_read_b64_tr_b16 v[132:133], v245 offset:5312\n\t" \
    "ds_read_b64_tr_b16 v[156:157], v245 offset:5344\n\t" \
    "ds_read_b64_tr_b16 v[172:173], v245 offset:5376\n\t" \
    "ds_read_b64_tr_b16 v[196:197], v245 offset:5408\n\t" \
    "s_waitcnt lgkmcnt(0)\n\t" \
    "s_add_u32 m0, %[wm], 0x1400\n\t" \
    "s_mov_b32 s44, 0x2d000\n\t" \
    "buffer_load_dwordx4 v240, %[wr], s44 offen offset:0 nt lds\n\t" \
    "buffer_load_dwordx4 v241, %[wr], s44 offen offset:1024 nt lds\n\t" \
    "buffer_load_dwordx4 v242, %[wr], s44 offen offset:2048 nt lds\n\t" \
    "buffer_load_dwordx4 v243, %[wr], s44 offen offset:3072 nt lds\n\t" \
    "s_add_u32 m0, %[wm], 0x2400\n\t" \
    "s_nop 0\n\t" \
    "buffer_load_dwordx4 v244, %[wr], s44 offen nt lds\n\t" \
    "s_barrier \n\t" \
    "s_waitcnt vmcnt(15)\n\t" \
    "ds_read_b64_tr_b16 v[202:203], v245 offset:10240\n\t" \
    "ds_read_b64_tr_b16 v[18:19], v245 offset:10272\n\t" \
    "ds_read_b64_tr_b16 v[210:211], v245 offset:10304\n\t" \
    "ds_read_b64_tr_b16 v[58:59], v245 offset:10336\n\t" \
    "ds_read_b64_tr_b16 v[218:219], v245 offset:10368\n\t" \
    "ds_read_b64_tr_b16 v[98:99], v245 offset:10400\n\t" \
    "ds_read_b64_tr_b16 v[226:227], v245 offset:10432\n\t" \
    "ds_read_b64_tr_b16 v[138:139], v245 offset:10464\n\t" \
    "ds_read_b64_tr_b16 v[234:235], v245 offset:10496\n\t" \
    "ds_read_b64_tr_b16 v[178:179], v245 offset:10528\n\t" \
    "s_waitcnt lgkmcnt(0)\n\t" \
    "s_add_u32 m0, %[wm], 0x2800\n\t" \
    "s_mov_b32 s44, 0x32000\n\t" \
    "buffer_load_dwordx4 v240, %[wr], s44 offen offset:0 nt lds\n\t" \
    "buffer_load_dwordx4 v241, %[wr], s44 offen offset:1024 nt lds\n\t" \
    "buffer_load_dwordx4 v242, %[wr], s44 offen offset:2048 nt lds\n\t" \
    "buffer_load_dwordx4 v243, %[wr], s44 offen offset:3072 nt lds\n\t" \
    "s_add_u32 m0, %[wm], 0x3800\n\t" \
    "s_nop 0\n\t" \
    "buffer_load_dwordx4 v244, %[wr], s44 offen nt lds\n\t" \
    "s_barrier \n\t" \
    "s_barrier \n\t" \
    "s_waitcnt vmcnt(15)\n\t" \
    "ds_read_b64_tr_b16 v[206:207], v245 offset:15360\n\t" \
    "ds_read_b64_tr_b16 v[22:23], v245 offset:15392\n\t" \
    "ds_read_b64_tr_b16 v[214:215], v245 offset:15424\n\t" \
    "ds_read_b64_tr_b16 v[62:63], v245 offset:15456\n\t" \
    "ds_read_b64_tr_b16 v[222:223], v245 offset:15488\n\t" \
    "ds_read_b64_tr_b16 v[102:103], v245 offset:15520\n\t" \
    "ds_read_b64_tr_b16 v[230:231], v245 offset:15552\n\t" \
    "ds_read_b64_tr_b16 v[142:143], v245 offset:15584\n\t" \
    "ds_read_b64_tr_b16 v[238:239], v245 offset:15616\n\t" \
    "ds_read_b64_tr_b16 v[182:183], v245 offset:15648\n\t" \
    "s_waitcnt lgkmcnt(0)\n\t" \
    "s_add_u32 m0, %[wm], 0x3c00\n\t" \
    "s_mov_b32 s44, 0x37000\n\t" \
    "buffer_load_dwordx4 v240, %[wr], s44 offen offset:0 nt lds\n\t" \
    "buffer_load_dwordx4 v241, %[wr], s44 offen offset:1024 nt lds\n\t" \
    "buffer_load_dwordx4 v242, %[wr], s44 offen offset:2048 nt lds\n\t" \
    "buffer_load_dwordx4 v243, %[wr], s44 offen offset:3072 nt lds\n\t" \
    "s_add_u32 m0, %[wm], 0x4c00\n\t" \
    "s_nop 0\n\t" \
    "buffer_load_dwordx4 v244, %[wr], s44 offen nt lds\n\t" \
    "s_barrier \n\t" \
    "s_barrier \n\t" \
    "s_waitcnt vmcnt(15)\n\t" \
    "ds_read_b64_tr_b16 v[2:3], v245 offset:0\n\t" \
    "ds_read_b64_tr_b16 v[26:27], v245 offset:32\n\t" \
    "ds_read_b64_tr_b16 v[42:43], v245 offset:64\n\t" \
    "ds_read_b64_tr_b16 v[66:67], v245 offset:96\n\t" \
    "ds_read_b64_tr_b16 v[82:83], v245 offset:128\n\t" \
    "ds_read_b64_tr_b16 v[106:107], v245 offset:160\n\t" \
    "ds_read_b64_tr_b16 v[122:123], v245 offset:192\n\t" \
    "ds_read_b64_tr_b16 v[146:147], v245 offset:224\n\t" \
    "ds_read_b64_tr_b16 v[162:163], v245 offset:256\n\t" \
    "ds_read_b64_tr_b16 v[186:187], v245 offset:288\n\t" \
    ".Lv41_%=:\n\t" \
    "v_readfirstlane_b32 s43, v246\n\t" \
    "s_nop 0\n\t" \
    "s_cmp_lg_u32 s43, -1\n\t" \
    "s_cbranch_scc1 .Lv42_%=\n\t" \
    "s_cmp_eq_u32 s41, 0\n\t" \
    "s_cbranch_scc1 .Lv42_%=\n\t" \
    "s_sub_u32 s41, s41, 1\n\t" \
    "s_sleep 4\n\t" \
    "v_mov_b32 v246, 0\n\t" \
    "buffer_load_dword v246, v246, s[60:63], %[mo] offen sc1\n\t" \
    "s_waitcnt vmcnt(0)\n\t" \
    "s_branch .Lv41_%=\n\t" \
    ".Lv42_%=:\n\t" \
    "v_mov_b32 v246, 0x8000\n\t" \
    "s_mov_b64 exec, 1\n\t" \
    "buffer_store_dword v246, v246, s[60:63], %[mo] offen sc1\n\t" \
    "s_mov_b64 exec, -1\n\t" \
    "s_and_b32 s43, s43, 0x2000\n\t" \
    "s_xor_b32 s43, s43, %[lx]\n\t" \
    "s_add_u32 s60, s60, s43\n\t" \
    "s_addc_u32 s61, s61, 0\n\t" \
    "s_barrier \n\t" \
    "s_waitcnt vmcnt(10)\n\t" \
    "ds_read_b64_tr_b16 v[6:7], v245 offset:5120\n\t" \
    "ds_read_b64_tr_b16 v[30:31], v245 offset:5152\n\t" \
    "ds_read_b64_tr_b16 v[46:47], v245 offset:5184\n\t" \
    "ds_read_b64_tr_b16 v[70:71], v245 offset:5216\n\t" \
    "ds_read_b64_tr_b16 v[86:87], v245 offset:5248\n\t" \
    "ds_read_b64_tr_b16 v[110:111], v245 offset:5280\n\t" \
    "ds_read_b64_tr_b16 v[126:127], v245 offset:5312\n\t" \
    "ds_read_b64_tr_b16 v[150:151], v245 offset:5344\n\t" \
    "ds_read_b64_tr_b16 v[166:167], v245 offset:5376\n\t" \
    "ds_read_b64_tr_b16 v[190:191], v245 offset:5408\n\t" \
    "s_barrier \n\t" \
    "s_barrier \n\t" \
    "s_waitcnt vmcnt(5)\n\t" \
    "ds_read_b64_tr_b16 v[10:11], v245 offset:10240\n\t" \
    "ds_read_b64_tr_b16 v[34:35], v245 offset:10272\n\t" \
    "ds_read_b64_tr_b16 v[50:51], v245 offset:10304\n\t" \
    "ds_read_b64_tr_b16 v[74:75], v245 offset:10336\n\t" \
    "ds_read_b64_tr_b16 v[90:91], v245 offset:10368\n\t" \
    "ds_read_b64_tr_b16 v[114:115], v245 offset:10400\n\t" \
    "ds_read_b64_tr_b16 v[130:131], v245 offset:10432\n\t" \
    "ds_read_b64_tr_b16 v[154:155], v245 offset:10464\n\t" \
    "ds_read_b64_tr_b16 v[170:171], v245 offset:10496\n\t" \
    "ds_read_b64_tr_b16 v[194:195], v245 offset:10528\n\t" \
    "s_barrier \n\t" \
    "s_barrier \n\t" \
    "s_waitcnt vmcnt(0)\n\t" \
    "ds_read_b64_tr_b16 v[14:15], v245 offset:15360\n\t" \
    "ds_read_b64_tr_b16 v[38:39], v245 offset:15392\n\t" \
    "ds_read_b64_tr_b16 v[54:55], v245 offset:15424\n\t" \
    "ds_read_b64_tr_b16 v[78:79], v245 offset:15456\n\t" \
    "ds_read_b64_tr_b16 v[94:95], v245 offset:15488\n\t" \
    "ds_read_b64_tr_b16 v[118:119], v245 offset:15520\n\t" \
    "ds_read_b64_tr_b16 v[134:135], v245 offset:15552\n\t" \
    "ds_read_b64_tr_b16 v[158:159], v245 offset:15584\n\t" \
    "ds_read_b64_tr_b16 v[174:175], v245 offset:15616\n\t" \
    "ds_read_b64_tr_b16 v[198:199], v245 offset:15648\n\t" \
    "s_waitcnt lgkmcnt(0)\n\t" \
    "s_barrier \n\t" \
    LST0 \
    LJIT \
    "ds_write_b128 v254, v[200:203] offset:0\n\t" \
    "ds_write_b128 v254, v[204:207] offset:1024\n\t" \
    "ds_write_b128 v254, v[208:211] offset:2048\n\t" \
    "ds_write_b128 v254, v[212:215] offset:3072\n\t" \
    "ds_write_b128 v254, v[216:219] offset:4096\n\t" \
    "ds_write_b128 v254, v[220:223] offset:5120\n\t" \
    "ds_write_b128 v254, v[224:227] offset:6144\n\t" \
    "ds_write_b128 v254, v[228:231] offset:7168\n\t" \
    "ds_write_b128 v254, v[232:235] offset:8192\n\t" \
    "ds_write_b128 v254, v[236:239] offset:9216\n\t" \
    "s_waitcnt vmcnt(0) lgkmcnt(0)\n\t" \
    "s_barrier \n\t" \
    "v_mov_b64 v[200:201], 0\n\t" \
    "v_mov_b64 v[202:203], 0\n\t" \
    "ds_write_b128 v253, v[200:203] offset:3072\n\t" \
    "ds_write_b128 v253, v[200:203] offset:4096\n\t" \
    "v_bfe_i32 v252, v253, 5, 1\n\t" \
    "v_and_b32 v252, 0xaaaa, v252\n\t" \
    "v_or_b32 v252, 0x4444, v252\n\t" \
    ".Lv43_%=:\n\t" \
    "ds_read_b128 v[248:251], v254 offset:0\n\t" \
    "s_and_b32 s44, s40, %[vm]\n\t" \
    "s_lshl_b32 s44, s44, 20\n\t" \
    "s_and_b32 s45, s40, 2\n\t" \
    "s_lshl_b32 s45, s45, 12\n\t" \
    "s_add_u32 s44, s44, s45\n\t" \
    "s_add_u32 s42, s44, %[vx]\n\t" \
    "s_waitcnt lgkmcnt(0)\n\t" \
    "s_barrier \n\t" \
    "ds_read_b128 v[224:227], v253 offset:6144\n\t" \
    "ds_read_b128 v[232:235], v253 offset:8192\n\t" \
    "ds_read_b128 v[236:239], v253 offset:9216\n\t" \
    "ds_read_b128 v[240:243], v253 offset:10240\n\t" \
    "ds_read_b128 v[244:247], v253 offset:11264\n\t" \
    "ds_read_b128 v[228:231], v253 offset:7168\n\t" \
    "s_waitcnt lgkmcnt(5)\n\t" \
    "v_mfma_f32_16x16x32_bf16 v[200:203], v[224:227], v[16:19], 0\n\t" \
    "s_waitcnt lgkmcnt(4)\n\t" \
    "v_mfma_f32_16x16x32_bf16 v[200:203], v[232:235], v[24:27], v[200:203]\n\t" \
    "s_waitcnt lgkmcnt(3)\n\t" \
    "v_mfma_f32_16x16x32_bf16 v[200:203], v[236:239], v[28:31], v[200:203]\n\t" \
    "s_waitcnt lgkmcnt(2)\n\t" \
    "v_mfma_f32_16x16x32_bf16 v[200:203], v[240:243], v[32:35], v[200:203]\n\t" \
    "s_waitcnt lgkmcnt(1)\n\t" \
    "v_mfma_f32_16x16x32_bf16 v[200:203], v[244:247], v[36:39], v[200:203]\n\t" \
    "s_waitcnt lgkmcnt(0)\n\t" \
    "v_mfma_f32_16x16x32_bf16 v[200:203], v[228:231], v[20:23], v[200:203]\n\t" \
    "v_mfma_f32_16x16x32_bf16 v[204:207], v[224:227], v[248:251], 0\n\t" \
    "ds_read_b128 v[248:251], v254 offset:1024\n\t" \
    "v_mfma_f32_16x16x32_bf16 v[204:207], v[232:235], v[0:3], v[204:207]\n\t" \
    "v_mfma_f32_16x16x32_bf16 v[204:207], v[236:239], v[4:7], v[204:207]\n\t" \
    "v_mfma_f32_16x16x32_bf16 v[204:207], v[240:243], v[8:11], v[204:207]\n\t" \
    "v_mfma_f32_16x16x32_bf16 v[204:207], v[244:247], v[12:15], v[204:207]\n\t" \
    "s_waitcnt lgkmcnt(0)\n\t" \
    "v_mfma_f32_16x16x32_bf16 v[204:207], v[228:231], v[248:251], v[204:207]\n\t" \
    "ds_read_b128 v[248:251], v254 offset:2048\n\t" \
    "s_barrier \n\t" \
    LST7 \
    LJIT \
    "v_mfma_f32_16x16x32_bf16 v[208:211], v[224:227], v[56:59], 0\n\t" \
    "v_mfma_f32_16x16x32_bf16 v[208:211], v[232:235], v[64:67], v[208:211]\n\t" \
    "v_mfma_f32_16x16x32_bf16 v[208:211], v[236:239], v[68:71], v[208:211]\n\t" \
    "v_mfma_f32_16x16x32_bf16 v[208:211], v[240:243], v[72:75], v[208:211]\n\t" \
    "v_mfma_f32_16x16x32_bf16 v[208:211], v[244:247], v[76:79], v[208:211]\n\t" \
    "v_mfma_f32_16x16x32_bf16 v[208:211], v[228:231], v[60:63], v[208:211]\n\t" \
    "s_waitcnt lgkmcnt(0)\n\t" \
    "v_mfma_f32_16x16x32_bf16 v[212:215], v[224:227], v[248:251], 0\n\t" \
    "ds_read_b128 v[248:251], v254 offset:3072\n\t" \
    "ds_read_b128 v[224:227], v253 offset:12288\n\t" \
    "v_mfma_f32_16x16x32_bf16 v[212:215], v[232:235], v[40:43], v[212:215]\n\t" \
    "ds_read_b128 v[232:235], v253 offset:14336\n\t" \
    "v_mfma_f32_16x16x32_bf16 v[212:215], v[236:239], v[44:47], v[212:215]\n\t" \
    "ds_read_b128 v[236:239], v253 offset:15360\n\t" \
    "v_mfma_f32_16x16x32_bf16 v[212:215], v[240:243], v[48:51], v[212:215]\n\t" \
    "ds_read_b128 v[240:243], v253 offset:16384\n\t" \
    "v_mfma_f32_16x16x32_bf16 v[212:215], v[244:247], v[52:55], v[212:215]\n\t" \
    "ds_read_b128 v[244:247], v253 offset:17408\n\t" \
    "s_waitcnt lgkmcnt(5)\n\t" \
    "v_mfma_f32_16x16x32_bf16 v[212:215], v[228:231], v[248:251], v[212:215]\n\t" \
    "ds_read_b128 v[248:251], v254 offset:4096\n\t" \
    "ds_read_b128 v[228:231], v253 offset:13312\n\t" \
    "s_waitcnt lgkmcnt(6)\n\t" \
    "v_mfma_f32_16x16x32_bf16 v[216:219], v[224:227], v[16:19], 0\n\t" \
    "s_waitcnt lgkmcnt(5)\n\t" \
    "v_mfma_f32_16x16x32_bf16 v[216:219], v[232:235], v[24:27], v[216:219]\n\t" \
    "s_waitcnt lgkmcnt(4)\n\t" \
    "v_mfma_f32_16x16x32_bf16 v[216:219], v[236:239], v[28:31], v[216:219]\n\t" \
    "s_waitcnt lgkmcnt(3)\n\t" \
    "v_mfma_f32_16x16x32_bf16 v[216:219], v[240:243], v[32:35], v[216:219]\n\t" \
    "s_waitcnt lgkmcnt(2)\n\t" \
    "v_mfma_f32_16x16x32_bf16 v[216:219], v[244:247], v[36:39], v[216:219]\n\t" \
    "s_waitcnt lgkmcnt(0)\n\t" \
    "v_mfma_f32_16x16x32_bf16 v[216:219], v[228:231], v[20:23], v[216:219]\n\t" \
    "v_mfma_f32_16x16x32_bf16 v[220:223], v[224:227], v[248:251], 0\n\t" \
    "ds_read_b128 v[248:251], v254 offset:5120\n\t" \
    "v_mfma_f32_16x16x32_bf16 v[220:223], v[232:235], v[80:83], v[220:223]\n\t" \
    "v_mfma_f32_16x16x32_bf16 v[220:223], v[236:239], v[84:87], v[220:223]\n\t" \
    "v_cvt_pk_bf16_f32 v200, v200, v216\n\t" \
    "v_cvt_pk_bf16_f32 v201, v201, v217\n\t" \
    "v_mfma_f32_16x16x32_bf16 v[220:223], v[240:243], v[88:91], v[220:223]\n\t" \
    "v_cvt_pk_bf16_f32 v202, v202, v218\n\t" \
    "v_cvt_pk_bf16_f32 v203, v203, v219\n\t" \
    "v_mfma_f32_16x16x32_bf16 v[220:223], v[244:247], v[92:95], v[220:223]\n\t" \
    "s_add_u32 s44, s42, 0x10000\n\t" \
    "s_waitcnt lgkmcnt(0)\n\t" \
    "v_mfma_f32_16x16x32_bf16 v[220:223], v[228:231], v[248:251], v[220:223]\n\t" \
    "ds_read_b128 v[248:251], v254 offset:0\n\t" \
    "buffer_store_dwordx4 v[200:203], v253, s[60:63], s44 offen sc1\n\t" \
    "v_mfma_f32_16x16x32_bf16 v[216:219], v[224:227], v[56:59], 0\n\t" \
    "v_mfma_f32_16x16x32_bf16 v[216:219], v[232:235], v[64:67], v[216:219]\n\t" \
    "v_mfma_f32_16x16x32_bf16 v[216:219], v[236:239], v[68:71], v[216:219]\n\t" \
    "v_mfma_f32_16x16x32_bf16 v[216:219], v[240:243], v[72:75], v[216:219]\n\t" \
    "v_mfma_f32_16x16x32_bf16 v[216:219], v[244:247], v[76:79], v[216:219]\n\t" \
    "v_mfma_f32_16x16x32_bf16 v[216:219], v[228:231], v[60:63], v[216:219]\n\t" \
    "s_waitcnt lgkmcnt(0)\n\t" \
    "v_mfma_f32_16x16x32_bf16 v[200:203], v[224:227], v[248:251], 0\n\t" \
    "ds_read_b128 v[248:251], v254 offset:1024\n\t" \
    "v_mfma_f32_16x16x32_bf16 v[200:203], v[232:235], v[0:3], v[200:203]\n\t" \
    "v_mfma_f32_16x16x32_bf16 v[200:203], v[236:239], v[4:7], v[200:203]\n\t" \
    "v_cvt_pk_bf16_f32 v208, v208, v216\n\t" \
    "v_cvt_pk_bf16_f32 v209, v209, v217\n\t" \
    "v_mfma_f32_16x16x32_bf16 v[200:203], v[240:243], v[8:11], v[200:203]\n\t" \
    "v_cvt_pk_bf16_f32 v210, v210, v218\n\t" \
    "v_cvt_pk_bf16_f32 v211, v211, v219\n\t" \
    "v_mfma_f32_16x16x32_bf16 v[200:203], v[244:247], v[12:15], v[200:203]\n\t" \
    "s_add_u32 s44, s42, 0x30000\n\t" \
    "s_waitcnt lgkmcnt(0)\n\t" \
    "v_mfma_f32_16x16x32_bf16 v[200:203], v[228:231], v[248:251], v[200:203]\n\t" \
    "ds_read_b128 v[248:251], v254 offset:2048\n\t" \
    "buffer_store_dwordx4 v[208:211], v253, s[60:63], s44 offen sc1\n\t" \
    "v_mfma_f32_16x16x32_bf16 v[216:219], v[224:227], v[96:99], 0\n\t" \
    "v_mfma_f32_16x16x32_bf16 v[216:219], v[232:235], v[104:107], v[216:219]\n\t" \
    "v_mfma_f32_16x16x32_bf16 v[216:219], v[236:239], v[108:111], v[216:219]\n\t" \
    "v_cvt_pk_bf16_f32 v204, v204, v200\n\t" \
    "v_mfma_f32_16x16x32_bf16 v[216:219], v[240:243], v[112:115], v[216:219]\n\t" \
    "v_cvt_pk_bf16_f32 v205, v205, v201\n\t" \
    "v_cvt_pk_bf16_f32 v206, v206, v202\n\t" \
    "v_mfma_f32_16x16x32_bf16 v[216:219], v[244:247], v[116:119], v[216:219]\n\t" \
    "v_cvt_pk_bf16_f32 v207, v207, v203\n\t" \
    "v_mfma_f32_16x16x32_bf16 v[216:219], v[228:231], v[100:103], v[216:219]\n\t" \
    "s_add_u32 s44, s42, 0x0\n\t" \
    "buffer_store_dwordx4 v[204:207], v253, s[60:63], s44 offen sc1\n\t" \
    "s_waitcnt lgkmcnt(0)\n\t" \
    "v_mfma_f32_16x16x32_bf16 v[208:211], v[224:227], v[248:251], 0\n\t" \
    "ds_read_b128 v[248:251], v254 offset:3072\n\t" \
    "v_mfma_f32_16x16x32_bf16 v[208:211], v[232:235], v[40:43], v[208:211]\n\t" \
    "v_mfma_f32_16x16x32_bf16 v[208:211], v[236:239], v[44:47], v[208:211]\n\t" \
    "v_mfma_f32_16x16x32_bf16 v[208:211], v[240:243], v[48:51], v[208:211]\n\t" \
    "v_mfma_f32_16x16x32_bf16 v[208:211], v[244:247], v[52:55], v[208:211]\n\t" \
    "s_waitcnt lgkmcnt(0)\n\t" \
    "v_mfma_f32_16x16x32_bf16 v[208:211], v[228:231], v[248:251], v[208:211]\n\t" \
    "ds_read_b128 v[248:251], v254 offset:6144\n\t" \
    "v_mfma_f32_16x16x32_bf16 v[200:203], v[224:227], v[136:139], 0\n\t" \
    "v_mfma_f32_16x16x32_bf16 v[200:203], v[232:235], v[144:147], v[200:203]\n\t" \
    "v_mfma_f32_16x16x32_bf16 v[200:203], v[236:239], v[148:151], v[200:203]\n\t" \
    "v_cvt_pk_bf16_f32 v212, v212, v208\n\t" \
    "v_cvt_pk_bf16_f32 v213, v213, v209\n\t" \
    "v_mfma_f32_16x16x32_bf16 v[200:203], v[240:243], v[152:155], v[200:203]\n\t" \
    "v_cvt_pk_bf16_f32 v214, v214, v210\n\t" \
    "v_cvt_pk_bf16_f32 v215, v215, v211\n\t" \
    "v_mfma_f32_16x16x32_bf16 v[200:203], v[244:247], v[156:159], v[200:203]\n\t" \
    "s_add_u32 s44, s42, 0x20000\n\t" \
    "v_mfma_f32_16x16x32_bf16 v[200:203], v[228:231], v[140:143], v[200:203]\n\t" \
    "buffer_store_dwordx4 v[212:215], v253, s[60:63], s44 offen sc1\n\t" \
    "s_waitcnt lgkmcnt(0)\n\t" \
    "v_mfma_f32_16x16x32_bf16 v[204:207], v[224:227], v[248:251], 0\n\t" \
    "ds_read_b128 v[248:251], v254 offset:7168\n\t" \
    "ds_read_b128 v[224:227], v253 offset:6144\n\t" \
    "v_mfma_f32_16x16x32_bf16 v[204:207], v[232:235], v[120:123], v[204:207]\n\t" \
    "ds_read_b128 v[232:235], v253 offset:8192\n\t" \
    "v_mfma_f32_16x16x32_bf16 v[204:207], v[236:239], v[124:127], v[204:207]\n\t" \
    "ds_read_b128 v[236:239], v253 offset:9216\n\t" \
    "v_mfma_f32_16x16x32_bf16 v[204:207], v[240:243], v[128:131], v[204:207]\n\t" \
    "ds_read_b128 v[240:243], v253 offset:10240\n\t" \
    "v_mfma_f32_16x16x32_bf16 v[204:207], v[244:247], v[132:135], v[204:207]\n\t" \
    "ds_read_b128 v[244:247], v253 offset:11264\n\t" \
    "s_waitcnt lgkmcnt(5)\n\t" \
    "v_mfma_f32_16x16x32_bf16 v[204:207], v[228:231], v[248:251], v[204:207]\n\t" \
    "ds_read_b128 v[248:251], v254 offset:4096\n\t" \
    "ds_read_b128 v[228:231], v253 offset:7168\n\t" \
    "s_waitcnt lgkmcnt(6)\n\t" \
    "v_mfma_f32_16x16x32_bf16 v[208:211], v[224:227], v[96:99], 0\n\t" \
    "s_waitcnt lgkmcnt(5)\n\t" \
    "v_mfma_f32_16x16x32_bf16 v[208:211], v[232:235], v[104:107], v[208:211]\n\t" \
    "s_waitcnt lgkmcnt(4)\n\t" \
    "v_mfma_f32_16x16x32_bf16 v[208:211], v[236:239], v[108:111], v[208:211]\n\t" \
    "s_waitcnt lgkmcnt(3)\n\t" \
    "v_mfma_f32_16x16x32_bf16 v[208:211], v[240:243], v[112:115], v[208:211]\n\t" \
    "s_waitcnt lgkmcnt(2)\n\t" \
    "v_mfma_f32_16x16x32_bf16 v[208:211], v[244:247], v[116:119], v[208:211]\n\t" \
    "s_waitcnt lgkmcnt(0)\n\t" \
    "v_mfma_f32_16x16x32_bf16 v[208:211], v[228:231], v[100:103], v[208:211]\n\t" \
    "v_mfma_f32_16x16x32_bf16 v[212:215], v[224:227], v[248:251], 0\n\t" \
    "ds_read_b128 v[248:251], v254 offset:5120\n\t" \
    "v_mfma_f32_16x16x32_bf16 v[212:215], v[232:235], v[80:83], v[212:215]\n\t" \
    "v_mfma_f32_16x16x32_bf16 v[212:215], v[236:239], v[84:87], v[212:215]\n\t" \
    "v_cvt_pk_bf16_f32 v216, v208, v216\n\t" \
    "v_cvt_pk_bf16_f32 v217, v209, v217\n\t" \
    "v_mfma_f32_16x16x32_bf16 v[212:215], v[240:243], v[88:91], v[212:215]\n\t" \
    "v_cvt_pk_bf16_f32 v218, v210, v218\n\t" \
    "v_cvt_pk_bf16_f32 v219, v211, v219\n\t" \
    "v_mfma_f32_16x16x32_bf16 v[212:215], v[244:247], v[92:95], v[212:215]\n\t" \
    "s_add_u32 s44, s42, 0x50000\n\t" \
    "s_waitcnt lgkmcnt(0)\n\t" \
    "v_mfma_f32_16x16x32_bf16 v[212:215], v[228:231], v[248:251], v[212:215]\n\t" \
    "ds_read_b128 v[248:251], v254 offset:6144\n\t" \
    "buffer_store_dwordx4 v[216:219], v253, s[60:63], s44 offen sc1\n\t" \
    "v_mfma_f32_16x16x32_bf16 v[208:211], v[224:227], v[176:179], 0\n\t" \
    "v_mfma_f32_16x16x32_bf16 v[208:211], v[232:235], v[184:187], v[208:211]\n\t" \
    "v_mfma_f32_16x16x32_bf16 v[208:211], v[236:239], v[188:191], v[208:211]\n\t" \
    "v_cvt_pk_bf16_f32 v220, v212, v220\n\t" \
    "v_mfma_f32_16x16x32_bf16 v[208:211], v[240:243], v[192:195], v[208:211]\n\t" \
    "v_cvt_pk_bf16_f32 v221, v213, v221\n\t" \
    "v_cvt_pk_bf16_f32 v222, v214, v222\n\t" \
    "v_mfma_f32_16x16x32_bf16 v[208:211], v[244:247], v[196:199], v[208:211]\n\t" \
    "v_cvt_pk_bf16_f32 v223, v215, v223\n\t" \
    "v_mfma_f32_16x16x32_bf16 v[208:211], v[228:231], v[180:183], v[208:211]\n\t" \
    "s_add_u32 s44, s42, 0x40000\n\t" \
    "buffer_store_dwordx4 v[220:223], v253, s[60:63], s44 offen sc1\n\t" \
    "s_waitcnt lgkmcnt(0)\n\t" \
    "v_mfma_f32_16x16x32_bf16 v[216:219], v[224:227], v[248:251], 0\n\t" \
    "ds_read_b128 v[248:251], v254 offset:7168\n\t" \
    "v_mfma_f32_16x16x32_bf16 v[216:219], v[232:235], v[120:123], v[216:219]\n\t" \
    "v_mfma_f32_16x16x32_bf16 v[216:219], v[236:239], v[124:127], v[216:219]\n\t" \
    "v_mfma_f32_16x16x32_bf16 v[216:219], v[240:243], v[128:131], v[216:219]\n\t" \
    "v_mfma_f32_16x16x32_bf16 v[216:219], v[244:247], v[132:135], v[216:219]\n\t" \
    "s_waitcnt lgkmcnt(0)\n\t" \
    "v_mfma_f32_16x16x32_bf16 v[216:219], v[228:231], v[248:251], v[216:219]\n\t" \
    "ds_read_b128 v[248:251], v254 offset:8192\n\t" \
    "v_mfma_f32_16x16x32_bf16 v[212:215], v[224:227], v[136:139], 0\n\t" \
    "v_mfma_f32_16x16x32_bf16 v[212:215], v[232:235], v[144:147], v[212:215]\n\t" \
    "v_mfma_f32_16x16x32_bf16 v[212:215], v[236:239], v[148:151], v[212:215]\n\t" \
    "v_cvt_pk_bf16_f32 v204, v216, v204\n\t" \
    "v_cvt_pk_bf16_f32 v205, v217, v205\n\t" \
    "v_mfma_f32_16x16x32_bf16 v[212:215], v[240:243], v[152:155], v[212:215]\n\t" \
    "v_cvt_pk_bf16_f32 v206, v218, v206\n\t" \
    "v_cvt_pk_bf16_f32 v207, v219, v207\n\t" \
    "v_mfma_f32_16x16x32_bf16 v[212:215], v[244:247], v[156:159], v[212:215]\n\t" \
    "s_add_u32 s44, s42, 0x60000\n\t" \
    "v_mfma_f32_16x16x32_bf16 v[212:215], v[228:231], v[140:143], v[212:215]\n\t" \
    "buffer_store_dwordx4 v[204:207], v253, s[60:63], s44 offen sc1\n\t" \
    "s_waitcnt lgkmcnt(0)\n\t" \
    "v_mfma_f32_16x16x32_bf16 v[220:223], v[224:227], v[248:251], 0\n\t" \
    "ds_read_b128 v[248:251], v254 offset:9216\n\t" \
    "ds_read_b128 v[224:227], v253 offset:12288\n\t" \
    "v_mfma_f32_16x16x32_bf16 v[220:223], v[232:235], v[160:163], v[220:223]\n\t" \
    "ds_read_b128 v[232:235], v253 offset:14336\n\t" \
    "v_mfma_f32_16x16x32_bf16 v[220:223], v[236:239], v[164:167], v[220:223]\n\t" \
    "ds_read_b128 v[236:239], v253 offset:15360\n\t" \
    "v_cvt_pk_bf16_f32 v200, v212, v200\n\t" \
    "v_mfma_f32_16x16x32_bf16 v[220:223], v[240:243], v[168:171], v[220:223]\n\t" \
    "ds_read_b128 v[240:243], v253 offset:16384\n\t" \
    "v_cvt_pk_bf16_f32 v201, v213, v201\n\t" \
    "v_cvt_pk_bf16_f32 v202, v214, v202\n\t" \
    "v_mfma_f32_16x16x32_bf16 v[220:223], v[244:247], v[172:175], v[220:223]\n\t" \
    "ds_read_b128 v[244:247], v253 offset:17408\n\t" \
    "v_cvt_pk_bf16_f32 v203, v215, v203\n\t" \
    "s_waitcnt lgkmcnt(5)\n\t" \
    "v_mfma_f32_16x16x32_bf16 v[220:223], v[228:231], v[248:251], v[220:223]\n\t" \
    "ds_read_b128 v[248:251], v254 offset:8192\n\t" \
    "ds_read_b128 v[228:231], v253 offset:13312\n\t" \
    "s_add_u32 s44, s42, 0x70000\n\t" \
    "buffer_store_dwordx4 v[200:203], v253, s[60:63], s44 offen sc1\n\t" \
    "s_waitcnt lgkmcnt(6)\n\t" \
    "v_mfma_f32_16x16x32_bf16 v[216:219], v[224:227], v[176:179], 0\n\t" \
    "s_waitcnt lgkmcnt(5)\n\t" \
    "v_mfma_f32_16x16x32_bf16 v[216:219], v[232:235], v[184:187], v[216:219]\n\t" \
    "s_waitcnt lgkmcnt(4)\n\t" \
    "v_mfma_f32_16x16x32_bf16 v[216:219], v[236:239], v[188:191], v[216:219]\n\t" \
    "s_waitcnt lgkmcnt(3)\n\t" \
    "v_mfma_f32_16x16x32_bf16 v[216:219], v[240:243], v[192:195], v[216:219]\n\t" \
    "s_waitcnt lgkmcnt(2)\n\t" \
    "v_mfma_f32_16x16x32_bf16 v[216:219], v[244:247], v[196:199], v[216:219]\n\t" \
    "s_waitcnt lgkmcnt(0)\n\t" \
    "v_mfma_f32_16x16x32_bf16 v[216:219], v[228:231], v[180:183], v[216:219]\n\t" \
    "v_mfma_f32_16x16x32_bf16 v[204:207], v[224:227], v[248:251], 0\n\t" \
    "ds_read_b128 v[248:251], v254 offset:9216\n\t" \
    "v_mfma_f32_16x16x32_bf16 v[204:207], v[232:235], v[160:163], v[204:207]\n\t" \
    "v_mfma_f32_16x16x32_bf16 v[204:207], v[236:239], v[164:167], v[204:207]\n\t" \
    "v_cvt_pk_bf16_f32 v208, v208, v216\n\t" \
    "v_cvt_pk_bf16_f32 v209, v209, v217\n\t" \
    "v_mfma_f32_16x16x32_bf16 v[204:207], v[240:243], v[168:171], v[204:207]\n\t" \
    "v_cvt_pk_bf16_f32 v210, v210, v218\n\t" \
    "v_cvt_pk_bf16_f32 v211, v211, v219\n\t" \
    "v_mfma_f32_16x16x32_bf16 v[204:207], v[244:247], v[172:175], v[204:207]\n\t" \
    "s_add_u32 s44, s42, 0x90000\n\t" \
    "s_waitcnt lgkmcnt(0)\n\t" \
    "v_mfma_f32_16x16x32_bf16 v[204:207], v[228:231], v[248:251], v[204:207]\n\t" \
    "buffer_store_dwordx4 v[208:211], v253, s[60:63], s44 offen sc1\n\t" \
    "s_nop 6\n\t" \
    "v_cvt_pk_bf16_f32 v220, v220, v204\n\t" \
    "v_cvt_pk_bf16_f32 v221, v221, v205\n\t" \
    "v_cvt_pk_bf16_f32 v222, v222, v206\n\t" \
    "v_cvt_pk_bf16_f32 v223, v223, v207\n\t" \
    "s_add_u32 s44, s42, 0x80000\n\t" \
    "buffer_store_dwordx4 v[220:223], v253, s[60:63], s44 offen sc1\n\t" \
    LST0 \
    LJIT \
    "s_add_u32 s44, s40, 1\n\t" \
    "s_mov_b32 s45, s44\n\t" \
    "v_mov_b64 v[200:201], s[44:45]\n\t" \
    "v_mov_b64 v[202:203], s[44:45]\n\t" \
    "s_waitcnt lgkmcnt(0)\n\t" \
    "s_barrier \n\t" \
    LST3 \
    LJIT \
    "ds_read_b128 v[220:223], v253 offset:3072\n\t" \
    "ds_read_b128 v[224:227], v253 offset:3072\n\t" \
    "ds_read_b128 v[228:231], v253 offset:3072\n\t" \
    "ds_read_b128 v[232:235], v253 offset:3072\n\t" \
    "ds_read_b128 v[236:239], v253 offset:3072\n\t" \
    "ds_read_b128 v[240:243], v253 offset:3072\n\t" \
    "ds_read_b128 v[244:247], v253 offset:3072\n\t" \
    "ds_read_b128 v[248:251], v253 offset:3072\n\t" \
    "ds_read_b128 v[212:215], v254 offset:0\n\t" \
    "ds_read_b128 v[216:219], v254 offset:1024\n\t" \
    "s_and_b32 s44, s40, 3\n\t" \
    "s_mul_i32 s44, s44, 0x202000\n\t" \
    "s_add_u32 s42, s44, %[vo]\n\t" \
    "s_mov_b64 exec, 0xff\n\t" \
    "s_waitcnt vmcnt(0)\n\t" \
    "buffer_store_dwordx4 v[200:203], v253, s[60:63], %[fo] offen sc1\n\t" \
    "s_mov_b64 exec, -1\n\t" \
    LST1 \
    LJIT \
    "s_setprio 3\n\t" \
    "s_waitcnt lgkmcnt(0)\n\t" \
    "s_barrier \n\t" \
    LST4 \
    LJIT \
    "ds_read_b128 v[204:207], v253 offset:1024\n\t" \
    "ds_read_b128 v[208:211], v253 offset:2048\n\t" \
    "ds_read_b128 v[200:203], v253 offset:0\n\t" \
    "s_waitcnt lgkmcnt(2)\n\t" \
    "v_smfmac_f32_16x16x64_bf16 v[224:227], v[204:207], v[24:31], v252\n\t" \
    "v_smfmac_f32_16x16x64_bf16 v[220:223], v[204:207], v[0:7], v252\n\t" \
    "s_waitcnt lgkmcnt(1)\n\t" \
    "v_smfmac_f32_16x16x64_bf16 v[224:227], v[208:211], v[32:39], v252\n\t" \
    "v_smfmac_f32_16x16x64_bf16 v[220:223], v[208:211], v[8:15], v252\n\t" \
    "s_waitcnt lgkmcnt(0)\n\t" \
    "v_smfmac_f32_16x16x64_bf16 v[224:227], v[200:203], v[16:23], v252\n\t" \
    "v_smfmac_f32_16x16x64_bf16 v[220:223], v[200:203], v[212:219], v252\n\t" \
    "ds_read_b128 v[212:215], v254 offset:2048\n\t" \
    "ds_read_b128 v[216:219], v254 offset:3072\n\t" \
    "v_smfmac_f32_16x16x64_bf16 v[232:235], v[204:207], v[64:71], v252\n\t" \
    "v_smfmac_f32_16x16x64_bf16 v[228:231], v[204:207], v[40:47], v252\n\t" \
    "v_smfmac_f32_16x16x64_bf16 v[232:235], v[208:211], v[72:79], v252\n\t" \
    "v_add_f32 v220, v220, v222\n\t" \
    "v_add_f32 v221, v221, v223\n\t" \
    "v_cvt_pk_bf16_f32 v220, v220, v221\n\t" \
    "v_smfmac_f32_16x16x64_bf16 v[228:231], v[208:211], v[48:55], v252\n\t" \
    "v_min_u32 v220, -2, v220\n\t" \
    "v_add_f32 v224, v224, v226\n\t" \
    "v_add_f32 v225, v225, v227\n\t" \
    "v_smfmac_f32_16x16x64_bf16 v[232:235], v[200:203], v[56:63], v252\n\t" \
    "v_cvt_pk_bf16_f32 v221, v224, v225\n\t" \
    "v_min_u32 v221, -2, v221\n\t" \
    "s_waitcnt lgkmcnt(0)\n\t" \
    "v_smfmac_f32_16x16x64_bf16 v[228:231], v[200:203], v[212:219], v252\n\t" \
    "ds_read_b128 v[212:215], v254 offset:4096\n\t" \
    "ds_read_b128 v[216:219], v254 offset:5120\n\t" \
    "v_smfmac_f32_16x16x64_bf16 v[240:243], v[204:207], v[104:111], v252\n\t" \
    "v_smfmac_f32_16x16x64_bf16 v[236:239], v[204:207], v[80:87], v252\n\t" \
    "v_smfmac_f32_16x16x64_bf16 v[240:243], v[208:211], v[112:119], v252\n\t" \
    "v_add_f32 v228, v228, v230\n\t" \
    "v_add_f32 v229, v229, v231\n\t" \
    "v_cvt_pk_bf16_f32 v222, v228, v229\n\t" \
    "v_smfmac_f32_16x16x64_bf16 v[236:239], v[208:211], v[88:95], v252\n\t" \
    "v_min_u32 v222, -2, v222\n\t" \
    "v_add_f32 v232, v232, v234\n\t" \
    "v_add_f32 v233, v233, v235\n\t" \
    "v_smfmac_f32_16x16x64_bf16 v[240:243], v[200:203], v[96:103], v252\n\t" \
    "v_cvt_pk_bf16_f32 v223, v232, v233\n\t" \
    "v_min_u32 v223, -2, v223\n\t" \
    "v_permlane32_swap_b32 v220, v222\n\t" \
    "s_waitcnt lgkmcnt(0)\n\t" \
    "v_smfmac_f32_16x16x64_bf16 v[236:239], v[200:203], v[212:219], v252\n\t" \
    "ds_read_b128 v[212:215], v254 offset:6144\n\t" \
    "ds_read_b128 v[216:219], v254 offset:7168\n\t" \
    "s_nop 0\n\t" \
    "v_permlane32_swap_b32 v221, v223\n\t" \
    "s_nop 1\n\t" \
    "v_permlane16_swap_b32 v220, v221\n\t" \
    "v_permlane16_swap_b32 v222, v223\n\t" \
    "buffer_store_dwordx4 v[220:223], v255, s[60:63], s42 offen sc1\n\t" \
    "s_add_u32 s42, s42, 0x10000\n\t" \
    "ds_read_b128 v[224:227], v253 offset:3072\n\t" \
    "ds_read_b128 v[228:231], v253 offset:3072\n\t" \
    "v_smfmac_f32_16x16x64_bf16 v[248:251], v[204:207], v[144:151], v252\n\t" \
    "v_smfmac_f32_16x16x64_bf16 v[244:247], v[204:207], v[120:127], v252\n\t" \
    "v_smfmac_f32_16x16x64_bf16 v[248:251], v[208:211], v[152:159], v252\n\t" \
    "v_add_f32 v236, v236, v238\n\t" \
    "v_add_f32 v237, v237, v239\n\t" \
    "v_cvt_pk_bf16_f32 v236, v236, v237\n\t" \
    "v_smfmac_f32_16x16x64_bf16 v[244:247], v[208:211], v[128:135], v252\n\t" \
    "v_min_u32 v236, -2, v236\n\t" \
    "v_add_f32 v240, v240, v242\n\t" \
    "v_add_f32 v241, v241, v243\n\t" \
    "v_smfmac_f32_16x16x64_bf16 v[248:251], v[200:203], v[136:143], v252\n\t" \
    "v_cvt_pk_bf16_f32 v237, v240, v241\n\t" \
    "v_min_u32 v237, -2, v237\n\t" \
    "s_waitcnt lgkmcnt(2)\n\t" \
    "v_smfmac_f32_16x16x64_bf16 v[244:247], v[200:203], v[212:219], v252\n\t" \
    "ds_read_b128 v[212:215], v254 offset:8192\n\t" \
    "ds_read_b128 v[216:219], v254 offset:9216\n\t" \
    "s_waitcnt lgkmcnt(2)\n\t" \
    "v_smfmac_f32_16x16x64_bf16 v[228:231], v[204:207], v[184:191], v252\n\t" \
    "v_smfmac_f32_16x16x64_bf16 v[224:227], v[204:207], v[160:167], v252\n\t" \
    "v_smfmac_f32_16x16x64_bf16 v[228:231], v[208:211], v[192:199], v252\n\t" \
    "v_and_b32 v232, 0x7fff, v255\n\t" \
    "v_lshrrev_b32 v233, 15, v255\n\t" \
    "v_lshl_add_u32 v232, v233, 3, v232\n\t" \
    "v_smfmac_f32_16x16x64_bf16 v[224:227], v[208:211], v[168:175], v252\n\t" \
    "v_add_f32 v244, v244, v246\n\t" \
    "v_add_f32 v245, v245, v247\n\t" \
    "v_cvt_pk_bf16_f32 v238, v244, v245\n\t" \
    "v_smfmac_f32_16x16x64_bf16 v[228:231], v[200:203], v[176:183], v252\n\t" \
    "v_min_u32 v238, -2, v238\n\t" \
    "v_add_f32 v248, v248, v250\n\t" \
    "v_add_f32 v249, v249, v251\n\t" \
    "s_waitcnt lgkmcnt(0)\n\t" \
    "v_smfmac_f32_16x16x64_bf16 v[224:227], v[200:203], v[212:219], v252\n\t" \
    "v_cvt_pk_bf16_f32 v239, v248, v249\n\t" \
    "v_min_u32 v239, -2, v239\n\t" \
    "v_permlane32_swap_b32 v236, v238\n\t" \
    "s_nop 0\n\t" \
    "v_permlane32_swap_b32 v237, v239\n\t" \
    "s_nop 1\n\t" \
    "v_permlane16_swap_b32 v236, v237\n\t" \
    "v_permlane16_swap_b32 v238, v239\n\t" \
    "buffer_store_dwordx4 v[236:239], v255, s[60:63], s42 offen sc1\n\t" \
    "s_add_u32 s42, s42, 0x10000\n\t" \
    "v_add_f32 v228, v228, v230\n\t" \
    "v_add_f32 v229, v229, v231\n\t" \
    "v_add_f32 v224, v224, v226\n\t" \
    "v_add_f32 v225, v225, v227\n\t" \
    "v_cvt_pk_bf16_f32 v229, v228, v229\n\t" \
    "v_cvt_pk_bf16_f32 v228, v224, v225\n\t" \
    "v_min_u32 v229, -2, v229\n\t" \
    "v_min_u32 v228, -2, v228\n\t" \
    "s_nop 1\n\t" \
    "v_permlane16_swap_b32 v228, v229\n\t" \
    "buffer_store_dwordx2 v[228:229], v232, s[60:63], s42 offen sc1\n\t" \
    "s_setprio 0\n\t" \
    LST5 \
    LJIT \
    LFLUSH200 \
    "s_add_u32 s40, s40, 1\n\t" \
    "s_cmp_lg_u32 s40, 64\n\t" \
    "s_cbranch_scc1 .Lv43_%=\n\t" \
    "s_branch .Lend%=\n\t" \
    ".Lv3_%=:\n\t" \
    "s_mov_b32 s42, %[xs]\n\t" \
    "s_add_u32 m0, %[wm], 0xc000\n\t" \
    "s_mov_b32 s44, 0x0\n\t" \
    "buffer_load_dwordx4 v240, %[wr], s44 offen offset:0 nt lds\n\t" \
    "buffer_load_dwordx4 v241, %[wr], s44 offen offset:1024 nt lds\n\t" \
    "buffer_load_dwordx4 v242, %[wr], s44 offen offset:2048 nt lds\n\t" \
    "s_add_u32 m0, %[wm], 0xf000\n\t" \
    "s_mov_b32 s44, 0x60000\n\t" \
    "buffer_load_dwordx4 v240, %[wr], s44 offen offset:0 nt lds\n\t" \
    "buffer_load_dwordx4 v241, %[wr], s44 offen offset:1024 nt lds\n\t" \
    "buffer_load_dwordx4 v242, %[wr], s44 offen offset:2048 nt lds\n\t" \
    "s_add_u32 m0, %[wm], 0x0\n\t" \
    "s_mov_b32 s44, 0xc0000\n\t" \
    "buffer_load_dwordx4 v240, %[wr], s44 offen offset:0 nt lds\n\t" \
    "buffer_load_dwordx4 v241, %[wr], s44 offen offset:1024 nt lds\n\t" \
    "buffer_load_dwordx4 v242, %[wr], s44 offen offset:2048 nt lds\n\t" \
    "s_add_u32 m0, %[wm], 0x3000\n\t" \
    "s_mov_b32 s44, 0x120000\n\t" \
    "buffer_load_dwordx4 v240, %[wr], s44 offen offset:0 nt lds\n\t" \
    "buffer_load_dwordx4 v241, %[wr], s44 offen offset:1024 nt lds\n\t" \
    "buffer_load_dwordx4 v242, %[wr], s44 offen offset:2048 nt lds\n\t" \
    "s_add_u32 m0, %[wm], 0x6000\n\t" \
    "s_mov_b32 s44, 0x180000\n\t" \
    "buffer_load_dwordx4 v240, %[wr], s44 offen offset:0 nt lds\n\t" \
    "buffer_load_dwordx4 v241, %[wr], s44 offen offset:1024 nt lds\n\t" \
    "buffer_load_dwordx4 v242, %[wr], s44 offen offset:2048 nt lds\n\t" \
    "v_add_u32 v244, 0xc000, v243\n\t" \
    "s_waitcnt vmcnt(12) lgkmcnt(0)\n\t" \
    "s_barrier \n\t" \
    "s_add_u32 m0, %[wm], 0x9000\n\t" \
    "s_mov_b32 s44, 0x1e0000\n\t" \
    "buffer_load_dwordx4 v240, %[wr], s44 offen offset:0 nt lds\n\t" \
    "buffer_load_dwordx4 v241, %[wr], s44 offen offset:1024 nt lds\n\t" \
    "buffer_load_dwordx4 v242, %[wr], s44 offen offset:2048 nt lds\n\t" \
    "ds_read_b64_tr_b16 v[208:209], v244 offset:0\n\t" \
    "ds_read_b64_tr_b16 v[210:211], v244 offset:1536\n\t" \
    "ds_read_b64_tr_b16 v[40:41], v244 offset:32\n\t" \
    "ds_read_b64_tr_b16 v[42:43], v244 offset:1568\n\t" \
    "ds_read_b64_tr_b16 v[48:49], v244 offset:64\n\t" \
    "ds_read_b64_tr_b16 v[50:51], v244 offset:1600\n\t" \
    "v_add_u32 v244, 0xf000, v243\n\t" \
    "s_waitcnt vmcnt(12) lgkmcnt(0)\n\t" \
    "s_barrier \n\t" \
    "s_add_u32 m0, %[wm], 0xc000\n\t" \
    "s_mov_b32 s44, 0x240000\n\t" \
    "buffer_load_dwordx4 v240, %[wr], s44 offen offset:0 nt lds\n\t" \
    "buffer_load_dwordx4 v241, %[wr], s44 offen offset:1024 nt lds\n\t" \
    "buffer_load_dwordx4 v242, %[wr], s44 offen offset:2048 nt lds\n\t" \
    "ds_read_b64_tr_b16 v[212:213], v244 offset:0\n\t" \
    "ds_read_b64_tr_b16 v[214:215], v244 offset:1536\n\t" \
    "ds_read_b64_tr_b16 v[44:45], v244 offset:32\n\t" \
    "ds_read_b64_tr_b16 v[46:47], v244 offset:1568\n\t" \
    "ds_read_b64_tr_b16 v[52:53], v244 offset:64\n\t" \
    "ds_read_b64_tr_b16 v[54:55], v244 offset:1600\n\t" \
    "v_add_u32 v244, 0x0, v243\n\t" \
    "s_waitcnt vmcnt(12) lgkmcnt(0)\n\t" \
    "s_barrier \n\t" \
    "s_add_u32 m0, %[wm], 0xf000\n\t" \
    "s_mov_b32 s44, 0x2a0000\n\t" \
    "buffer_load_dwordx4 v240, %[wr], s44 offen offset:0 nt lds\n\t" \
    "buffer_load_dwordx4 v241, %[wr], s44 offen offset:1024 nt lds\n\t" \
    "buffer_load_dwordx4 v242, %[wr], s44 offen offset:2048 nt lds\n\t" \
    "ds_read_b64_tr_b16 v[56:57], v244 offset:0\n\t" \
    "ds_read_b64_tr_b16 v[58:59], v244 offset:1536\n\t" \
    "ds_read_b64_tr_b16 v[64:65], v244 offset:32\n\t" \
    "ds_read_b64_tr_b16 v[66:67], v244 offset:1568\n\t" \
    "ds_read_b64_tr_b16 v[72:73], v244 offset:64\n\t" \
    "ds_read_b64_tr_b16 v[74:75], v244 offset:1600\n\t" \
    "v_add_u32 v244, 0x3000, v243\n\t" \
    "s_waitcnt vmcnt(12) lgkmcnt(0)\n\t" \
    "s_barrier \n\t" \
    "s_add_u32 m0, %[wm], 0x0\n\t" \
    "s_mov_b32 s44, 0x300000\n\t" \
    "buffer_load_dwordx4 v240, %[wr], s44 offen offset:0 nt lds\n\t" \
    "buffer_load_dwordx4 v241, %[wr], s44 offen offset:1024 nt lds\n\t" \
    "buffer_load_dwordx4 v242, %[wr], s44 offen offset:2048 nt lds\n\t" \
    "ds_read_b64_tr_b16 v[60:61], v244 offset:0\n\t" \
    "ds_read_b64_tr_b16 v[62:63], v244 offset:1536\n\t" \
    "ds_read_b64_tr_b16 v[68:69], v244 offset:32\n\t" \
    "ds_read_b64_tr_b16 v[70:71], v244 offset:1568\n\t" \
    "ds_read_b64_tr_b16 v[76:77], v244 offset:64\n\t" \
    "ds_read_b64_tr_b16 v[78:79], v244 offset:1600\n\t" \
    "v_add_u32 v244, 0x6000, v243\n\t" \
    "s_waitcnt vmcnt(12) lgkmcnt(0)\n\t" \
    "s_barrier \n\t" \
    "s_add_u32 m0, %[wm], 0x3000\n\t" \
    "s_mov_b32 s44, 0x360000\n\t" \
    "buffer_load_dwordx4 v240, %[wr], s44 offen offset:0 nt lds\n\t" \
    "buffer_load_dwordx4 v241, %[wr], s44 offen offset:1024 nt lds\n\t" \
    "buffer_load_dwordx4 v242, %[wr], s44 offen offset:2048 nt lds\n\t" \
    "ds_read_b64_tr_b16 v[216:217], v244 offset:0\n\t" \
    "ds_read_b64_tr_b16 v[218:219], v244 offset:1536\n\t" \
    "ds_read_b64_tr_b16 v[80:81], v244 offset:32\n\t" \
    "ds_read_b64_tr_b16 v[82:83], v244 offset:1568\n\t" \
    "ds_read_b64_tr_b16 v[88:89], v244 offset:64\n\t" \
    "ds_read_b64_tr_b16 v[90:91], v244 offset:1600\n\t" \
    "v_add_u32 v244, 0x9000, v243\n\t" \
    "s_waitcnt vmcnt(12) lgkmcnt(0)\n\t" \
    "s_barrier \n\t" \
    "s_add_u32 m0, %[wm], 0x6000\n\t" \
    "s_mov_b32 s44, 0x3c0000\n\t" \
    "buffer_load_dwordx4 v240, %[wr], s44 offen offset:0 nt lds\n\t" \
    "buffer_load_dwordx4 v241, %[wr], s44 offen offset:1024 nt lds\n\t" \
    "buffer_load_dwordx4 v242, %[wr], s44 offen offset:2048 nt lds\n\t" \
    "ds_read_b64_tr_b16 v[220:221], v244 offset:0\n\t" \
    "ds_read_b64_tr_b16 v[222:223], v244 offset:1536\n\t" \
    "ds_read_b64_tr_b16 v[84:85], v244 offset:32\n\t" \
    "ds_read_b64_tr_b16 v[86:87], v244 offset:1568\n\t" \
    "ds_read_b64_tr_b16 v[92:93], v244 offset:64\n\t" \
    "ds_read_b64_tr_b16 v[94:95], v244 offset:1600\n\t" \
    "v_add_u32 v244, 0xc000, v243\n\t" \
    "s_waitcnt vmcnt(12) lgkmcnt(0)\n\t" \
    "s_barrier \n\t" \
    "v_mov_b32 v246, 0\n\t" \
    "buffer_load_dword v246, v246, s[60:63], %[mo] offen sc1\n\t" \
    "s_add_u32 m0, %[wm], 0x9000\n\t" \
    "s_mov_b32 s44, 0x420000\n\t" \
    "buffer_load_dwordx4 v240, %[wr], s44 offen offset:0 nt lds\n\t" \
    "buffer_load_dwordx4 v241, %[wr], s44 offen offset:1024 nt lds\n\t" \
    "buffer_load_dwordx4 v242, %[wr], s44 offen offset:2048 nt lds\n\t" \
    "ds_read_b64_tr_b16 v[96:97], v244 offset:0\n\t" \
    "ds_read_b64_tr_b16 v[98:99], v244 offset:1536\n\t" \
    "ds_read_b64_tr_b16 v[104:105], v244 offset:32\n\t" \
    "ds_read_b64_tr_b16 v[106:107], v244 offset:1568\n\t" \
    "ds_read_b64_tr_b16 v[112:113], v244 offset:64\n\t" \
    "ds_read_b64_tr_b16 v[114:115], v244 offset:1600\n\t" \
    "v_add_u32 v244, 0xf000, v243\n\t" \
    "s_waitcnt vmcnt(12) lgkmcnt(0)\n\t" \
    "s_barrier \n\t" \
    "s_add_u32 m0, %[wm], 0xc000\n\t" \
    "s_mov_b32 s44, 0x480000\n\t" \
    "buffer_load_dwordx4 v240, %[wr], s44 offen offset:0 nt lds\n\t" \
    "buffer_load_dwordx4 v241, %[wr], s44 offen offset:1024 nt lds\n\t" \
    "buffer_load_dwordx4 v242, %[wr], s44 offen offset:2048 nt lds\n\t" \
    "ds_read_b64_tr_b16 v[100:101], v244 offset:0\n\t" \
    "ds_read_b64_tr_b16 v[102:103], v244 offset:1536\n\t" \
    "ds_read_b64_tr_b16 v[108:109], v244 offset:32\n\t" \
    "ds_read_b64_tr_b16 v[110:111], v244 offset:1568\n\t" \
    "ds_read_b64_tr_b16 v[116:117], v244 offset:64\n\t" \
    "ds_read_b64_tr_b16 v[118:119], v244 offset:1600\n\t" \
    "v_add_u32 v244, 0x0, v243\n\t" \
    "s_waitcnt vmcnt(12) lgkmcnt(0)\n\t" \
    "s_barrier \n\t" \
    "s_add_u32 m0, %[wm], 0xf000\n\t" \
    "s_mov_b32 s44, 0x4e0000\n\t" \
    "buffer_load_dwordx4 v240, %[wr], s44 offen offset:0 nt lds\n\t" \
    "buffer_load_dwordx4 v241, %[wr], s44 offen offset:1024 nt lds\n\t" \
    "buffer_load_dwordx4 v242, %[wr], s44 offen offset:2048 nt lds\n\t" \
    "ds_read_b64_tr_b16 v[224:225], v244 offset:0\n\t" \
    "ds_read_b64_tr_b16 v[226:227], v244 offset:1536\n\t" \
    "ds_read_b64_tr_b16 v[120:121], v244 offset:32\n\t" \
    "ds_read_b64_tr_b16 v[122:123], v244 offset:1568\n\t" \
    "ds_read_b64_tr_b16 v[128:129], v244 offset:64\n\t" \
    "ds_read_b64_tr_b16 v[130:131], v244 offset:1600\n\t" \
    "v_add_u32 v244, 0x3000, v243\n\t" \
    "s_waitcnt vmcnt(12) lgkmcnt(0)\n\t" \
    "s_barrier \n\t" \
    "s_add_u32 m0, %[wm], 0x0\n\t" \
    "s_mov_b32 s44, 0x540000\n\t" \
    "buffer_load_dwordx4 v240, %[wr], s44 offen offset:0 nt lds\n\t" \
    "buffer_load_dwordx4 v241, %[wr], s44 offen offset:1024 nt lds\n\t" \
    "buffer_load_dwordx4 v242, %[wr], s44 offen offset:2048 nt lds\n\t" \
    "ds_read_b64_tr_b16 v[228:229], v244 offset:0\n\t" \
    "ds_read_b64_tr_b16 v[230:231], v244 offset:1536\n\t" \
    "ds_read_b64_tr_b16 v[124:125], v244 offset:32\n\t" \
    "ds_read_b64_tr_b16 v[126:127], v244 offset:1568\n\t" \
    "ds_read_b64_tr_b16 v[132:133], v244 offset:64\n\t" \
    "ds_read_b64_tr_b16 v[134:135], v244 offset:1600\n\t" \
    "v_add_u32 v244, 0x6000, v243\n\t" \
    "s_waitcnt vmcnt(12) lgkmcnt(0)\n\t" \
    "s_barrier \n\t" \
    "s_add_u32 m0, %[wm], 0x3000\n\t" \
    "s_mov_b32 s44, 0x5a0000\n\t" \
    "buffer_load_dwordx4 v240, %[wr], s44 offen offset:0 nt lds\n\t" \
    "buffer_load_dwordx4 v241, %[wr], s44 offen offset:1024 nt lds\n\t" \
    "buffer_load_dwordx4 v242, %[wr], s44 offen offset:2048 nt lds\n\t" \
    "ds_read_b64_tr_b16 v[136:137], v244 offset:0\n\t" \
    "ds_read_b64_tr_b16 v[138:139], v244 offset:1536\n\t" \
    "ds_read_b64_tr_b16 v[144:145], v244 offset:32\n\t" \
    "ds_read_b64_tr_b16 v[146:147], v244 offset:1568\n\t" \
    "ds_read_b64_tr_b16 v[152:153], v244 offset:64\n\t" \
    "ds_read_b64_tr_b16 v[154:155], v244 offset:1600\n\t" \
    "v_add_u32 v244, 0x9000, v243\n\t" \
    "s_waitcnt vmcnt(12) lgkmcnt(0)\n\t" \
    "s_barrier \n\t" \
    ".Lv31_%=:\n\t" \
    "v_readfirstlane_b32 s43, v246\n\t" \
    "s_nop 0\n\t" \
    "s_cmp_lg_u32 s43, -1\n\t" \
    "s_cbranch_scc1 .Lv32_%=\n\t" \
    "s_cmp_eq_u32 s41, 0\n\t" \
    "s_cbranch_scc1 .Lv32_%=\n\t" \
    "s_sub_u32 s41, s41, 1\n\t" \
    "s_sleep 4\n\t" \
    "v_mov_b32 v246, 0\n\t" \
    "buffer_load_dword v246, v246, s[60:63], %[mo] offen sc1\n\t" \
    "s_waitcnt vmcnt(0)\n\t" \
    "s_branch .Lv31_%=\n\t" \
    ".Lv32_%=:\n\t" \
    "v_mov_b32 v246, 0x8000\n\t" \
    "s_mov_b64 exec, 1\n\t" \
    "buffer_store_dword v246, v246, s[60:63], %[mo] offen sc1\n\t" \
    "s_mov_b64 exec, -1\n\t" \
    "s_and_b32 s43, s43, 0x2000\n\t" \
    "s_xor_b32 s43, s43, %[lx]\n\t" \
    "s_add_u32 s60, s60, s43\n\t" \
    "s_addc_u32 s61, s61, 0\n\t" \
    "v_and_b32 v252, 0xf0, v253\n\t" \
    "v_lshlrev_b32 v252, 3, v252\n\t" \
    "buffer_load_dword v252, v252, s[60:63], %[fl] offen sc1\n\t" \
    "s_add_u32 m0, %[wm], 0x6000\n\t" \
    "s_mov_b32 s44, 0x600000\n\t" \
    "buffer_load_dwordx4 v240, %[wr], s44 offen offset:0 nt lds\n\t" \
    "buffer_load_dwordx4 v241, %[wr], s44 offen offset:1024 nt lds\n\t" \
    "buffer_load_dwordx4 v242, %[wr], s44 offen offset:2048 nt lds\n\t" \
    "ds_read_b64_tr_b16 v[140:141], v244 offset:0\n\t" \
    "ds_read_b64_tr_b16 v[142:143], v244 offset:1536\n\t" \
    "ds_read_b64_tr_b16 v[148:149], v244 offset:32\n\t" \
    "ds_read_b64_tr_b16 v[150:151], v244 offset:1568\n\t" \
    "ds_read_b64_tr_b16 v[156:157], v244 offset:64\n\t" \
    "ds_read_b64_tr_b16 v[158:159], v244 offset:1600\n\t" \
    "v_add_u32 v244, 0xc000, v243\n\t" \
    "s_waitcnt vmcnt(12) lgkmcnt(0)\n\t" \
    "s_barrier \n\t" \
    "s_add_u32 m0, %[wm], 0x9000\n\t" \
    "s_mov_b32 s44, 0x660000\n\t" \
    "buffer_load_dwordx4 v240, %[wr], s44 offen offset:0 nt lds\n\t" \
    "buffer_load_dwordx4 v241, %[wr], s44 offen offset:1024 nt lds\n\t" \
    "buffer_load_dwordx4 v242, %[wr], s44 offen offset:2048 nt lds\n\t" \
    "ds_read_b64_tr_b16 v[232:233], v244 offset:0\n\t" \
    "ds_read_b64_tr_b16 v[234:235], v244 offset:1536\n\t" \
    "ds_read_b64_tr_b16 v[160:161], v244 offset:32\n\t" \
    "ds_read_b64_tr_b16 v[162:163], v244 offset:1568\n\t" \
    "ds_read_b64_tr_b16 v[168:169], v244 offset:64\n\t" \
    "ds_read_b64_tr_b16 v[170:171], v244 offset:1600\n\t" \
    "v_add_u32 v244, 0xf000, v243\n\t" \
    "s_waitcnt vmcnt(12) lgkmcnt(0)\n\t" \
    "s_barrier \n\t" \
    "s_add_u32 m0, %[wm], 0xc000\n\t" \
    "s_mov_b32 s44, 0x6c0000\n\t" \
    "buffer_load_dwordx4 v240, %[wr], s44 offen offset:0 nt lds\n\t" \
    "buffer_load_dwordx4 v241, %[wr], s44 offen offset:1024 nt lds\n\t" \
    "buffer_load_dwordx4 v242, %[wr], s44 offen offset:2048 nt lds\n\t" \
    "ds_read_b64_tr_b16 v[236:237], v244 offset:0\n\t" \
    "ds_read_b64_tr_b16 v[238:239], v244 offset:1536\n\t" \
    "ds_read_b64_tr_b16 v[164:165], v244 offset:32\n\t" \
    "ds_read_b64_tr_b16 v[166:167], v244 offset:1568\n\t" \
    "ds_read_b64_tr_b16 v[172:173], v244 offset:64\n\t" \
    "ds_read_b64_tr_b16 v[174:175], v244 offset:1600\n\t" \
    "v_add_u32 v244, 0x0, v243\n\t" \
    "s_waitcnt vmcnt(12) lgkmcnt(0)\n\t" \
    "s_barrier \n\t" \
    "s_add_u32 m0, %[wm], 0xf000\n\t" \
    "s_mov_b32 s44, 0x720000\n\t" \
    "buffer_load_dwordx4 v240, %[wr], s44 offen offset:0 nt lds\n\t" \
    "buffer_load_dwordx4 v241, %[wr], s44 offen offset:1024 nt lds\n\t" \
    "buffer_load_dwordx4 v242, %[wr], s44 offen offset:2048 nt lds\n\t" \
    "ds_read_b64_tr_b16 v[176:177], v244 offset:0\n\t" \
    "ds_read_b64_tr_b16 v[178:179], v244 offset:1536\n\t" \
    "ds_read_b64_tr_b16 v[184:185], v244 offset:32\n\t" \
    "ds_read_b64_tr_b16 v[186:187], v244 offset:1568\n\t" \
    "ds_read_b64_tr_b16 v[192:193], v244 offset:64\n\t" \
    "ds_read_b64_tr_b16 v[194:195], v244 offset:1600\n\t" \
    "v_add_u32 v244, 0x3000, v243\n\t" \
    "s_waitcnt vmcnt(12) lgkmcnt(0)\n\t" \
    "s_barrier \n\t" \
    "ds_read_b64_tr_b16 v[180:181], v244 offset:0\n\t" \
    "ds_read_b64_tr_b16 v[182:183], v244 offset:1536\n\t" \
    "ds_read_b64_tr_b16 v[188:189], v244 offset:32\n\t" \
    "ds_read_b64_tr_b16 v[190:191], v244 offset:1568\n\t" \
    "ds_read_b64_tr_b16 v[196:197], v244 offset:64\n\t" \
    "ds_read_b64_tr_b16 v[198:199], v244 offset:1600\n\t" \
    "v_add_u32 v244, 0x6000, v243\n\t" \
    "s_waitcnt vmcnt(9) lgkmcnt(0)\n\t" \
    "s_barrier \n\t" \
    "ds_read_b64_tr_b16 v[200:201], v244 offset:0\n\t" \
    "ds_read_b64_tr_b16 v[202:203], v244 offset:1536\n\t" \
    "ds_read_b64_tr_b16 v[0:1], v244 offset:32\n\t" \
    "ds_read_b64_tr_b16 v[2:3], v244 offset:1568\n\t" \
    "ds_read_b64_tr_b16 v[8:9], v244 offset:64\n\t" \
    "ds_read_b64_tr_b16 v[10:11], v244 offset:1600\n\t" \
    "v_add_u32 v244, 0x9000, v243\n\t" \
    "s_waitcnt vmcnt(6) lgkmcnt(0)\n\t" \
    "s_barrier \n\t" \
    "ds_read_b64_tr_b16 v[204:205], v244 offset:0\n\t" \
    "ds_read_b64_tr_b16 v[206:207], v244 offset:1536\n\t" \
    "ds_read_b64_tr_b16 v[4:5], v244 offset:32\n\t" \
    "ds_read_b64_tr_b16 v[6:7], v244 offset:1568\n\t" \
    "ds_read_b64_tr_b16 v[12:13], v244 offset:64\n\t" \
    "ds_read_b64_tr_b16 v[14:15], v244 offset:1600\n\t" \
    "v_add_u32 v244, 0xc000, v243\n\t" \
    "s_waitcnt vmcnt(3) lgkmcnt(0)\n\t" \
    "s_barrier \n\t" \
    "ds_read_b64_tr_b16 v[16:17], v244 offset:0\n\t" \
    "ds_read_b64_tr_b16 v[18:19], v244 offset:1536\n\t" \
    "ds_read_b64_tr_b16 v[24:25], v244 offset:32\n\t" \
    "ds_read_b64_tr_b16 v[26:27], v244 offset:1568\n\t" \
    "ds_read_b64_tr_b16 v[32:33], v244 offset:64\n\t" \
    "ds_read_b64_tr_b16 v[34:35], v244 offset:1600\n\t" \
    "s_mov_b32 s43, 0\n\t" \
    "v_cmp_eq_u32 vcc, 0, v252\n\t" \
    "s_cbranch_vccnz .Lv33_%=\n\t" \
    "s_mov_b32 m0, 0x3000\n\t" \
    "s_add_u32 s49, s42, 0x0\n\t" \
    "buffer_load_dwordx4 v253, s[60:63], s49 offen offset:0 sc1 lds\n\t" \
    "buffer_load_dwordx4 v253, s[60:63], s49 offen offset:1024 sc1 lds\n\t" \
    "buffer_load_dwordx4 v253, s[60:63], s49 offen offset:2048 sc1 lds\n\t" \
    "buffer_load_dwordx4 v253, s[60:63], s49 offen offset:3072 sc1 lds\n\t" \
    "s_mov_b32 m0, 0x4c00\n\t" \
    "s_add_u32 s49, s42, 0x4c00\n\t" \
    "buffer_load_dwordx4 v253, s[60:63], s49 offen offset:0 sc1 lds\n\t" \
    "s_mov_b32 m0, 0x8000\n\t" \
    "s_add_u32 s49, s42, 0x1000\n\t" \
    "buffer_load_dwordx4 v253, s[60:63], s49 offen offset:0 sc1 lds\n\t" \
    "buffer_load_dwordx4 v253, s[60:63], s49 offen offset:1024 sc1 lds\n\t" \
    "buffer_load_dwordx4 v253, s[60:63], s49 offen offset:2048 sc1 lds\n\t" \
    "buffer_load_dwordx4 v253, s[60:63], s49 offen offset:3072 sc1 lds\n\t" \
    "s_mov_b32 m0, 0x9c00\n\t" \
    "s_add_u32 s49, s42, 0x5c00\n\t" \
    "buffer_load_dwordx4 v253, s[60:63], s49 offen offset:0 sc1 lds\n\t" \
    "s_mov_b32 s43, 1\n\t" \
    ".Lv33_%=:\n\t" \
    "v_add_u32 v244, 0xf000, v243\n\t" \
    "s_waitcnt vmcnt(0) lgkmcnt(0)\n\t" \
    "s_barrier \n\t" \
    "ds_read_b64_tr_b16 v[20:21], v244 offset:0\n\t" \
    "ds_read_b64_tr_b16 v[22:23], v244 offset:1536\n\t" \
    "ds_read_b64_tr_b16 v[28:29], v244 offset:32\n\t" \
    "ds_read_b64_tr_b16 v[30:31], v244 offset:1568\n\t" \
    "ds_read_b64_tr_b16 v[36:37], v244 offset:64\n\t" \
    "ds_read_b64_tr_b16 v[38:39], v244 offset:1600\n\t" \
    "s_waitcnt lgkmcnt(0)\n\t" \
    "s_barrier \n\t" \
    LST0 \
    LJIT \
    "ds_write_b128 v254, v[200:203] offset:55296\n\t" \
    "ds_write_b128 v254, v[204:207] offset:56320\n\t" \
    "ds_write_b128 v254, v[208:211] offset:57344\n\t" \
    "ds_write_b128 v254, v[212:215] offset:58368\n\t" \
    "ds_write_b128 v254, v[216:219] offset:59392\n\t" \
    "ds_write_b128 v254, v[220:223] offset:60416\n\t" \
    "ds_write_b128 v254, v[224:227] offset:61440\n\t" \
    "ds_write_b128 v254, v[228:231] offset:62464\n\t" \
    "ds_write_b128 v254, v[232:235] offset:63488\n\t" \
    "ds_write_b128 v254, v[236:239] offset:64512\n\t" \
    "s_cmp_lg_u32 s43, 0\n\t" \
    "s_cbranch_scc1 .Lv34_%=\n\t" \
    "s_mov_b32 s44, 0\n\t" \
    ".Lv35_%=:\n\t" \
    "v_cmp_ge_u32 vcc, s44, v252\n\t" \
    "s_cbranch_vccz .Lv36_%=\n\t" \
    "s_cmp_eq_u32 s41, 0\n\t" \
    "s_cbranch_scc1 .Lv36_%=\n\t" \
    "s_sub_u32 s41, s41, 1\n\t" \
    "s_sleep 8\n\t" \
    "v_and_b32 v252, 0xf0, v253\n\t" \
    "v_lshlrev_b32 v252, 3, v252\n\t" \
    "buffer_load_dword v252, v252, s[60:63], %[fl] offen sc1\n\t" \
    "s_waitcnt vmcnt(0)\n\t" \
    "s_branch .Lv35_%=\n\t" \
    ".Lv36_%=:\n\t" \
    "s_mov_b32 m0, 0x3000\n\t" \
    "s_add_u32 s49, s42, 0x0\n\t" \
    "buffer_load_dwordx4 v253, s[60:63], s49 offen offset:0 sc1 lds\n\t" \
    "buffer_load_dwordx4 v253, s[60:63], s49 offen offset:1024 sc1 lds\n\t" \
    "buffer_load_dwordx4 v253, s[60:63], s49 offen offset:2048 sc1 lds\n\t" \
    "buffer_load_dwordx4 v253, s[60:63], s49 offen offset:3072 sc1 lds\n\t" \
    "s_mov_b32 m0, 0x4c00\n\t" \
    "s_add_u32 s49, s42, 0x4c00\n\t" \
    "buffer_load_dwordx4 v253, s[60:63], s49 offen offset:0 sc1 lds\n\t" \
    "s_mov_b32 m0, 0x8000\n\t" \
    "s_add_u32 s49, s42, 0x1000\n\t" \
    "buffer_load_dwordx4 v253, s[60:63], s49 offen offset:0 sc1 lds\n\t" \
    "buffer_load_dwordx4 v253, s[60:63], s49 offen offset:1024 sc1 lds\n\t" \
    "buffer_load_dwordx4 v253, s[60:63], s49 offen offset:2048 sc1 lds\n\t" \
    "buffer_load_dwordx4 v253, s[60:63], s49 offen offset:3072 sc1 lds\n\t" \
    "s_mov_b32 m0, 0x9c00\n\t" \
    "s_add_u32 s49, s42, 0x5c00\n\t" \
    "buffer_load_dwordx4 v253, s[60:63], s49 offen offset:0 sc1 lds\n\t" \
    ".Lv34_%=:\n\t" \
    "s_waitcnt vmcnt(0) lgkmcnt(0)\n\t" \
    "s_barrier \n\t" \
    "ds_read_b128 v[224:227], v253 offset:16384\n\t" \
    "ds_read_b128 v[244:247], v254 offset:55296\n\t" \
    "ds_read_b128 v[228:231], v253 offset:17408\n\t" \
    "ds_read_b128 v[232:235], v253 offset:36864\n\t" \
    "ds_read_b128 v[236:239], v253 offset:37888\n\t" \
    "ds_read_b128 v[248:251], v254 offset:56320\n\t" \
    ".Lv37_%=:\n\t" \
    LST7 \
    LJIT \
    "s_waitcnt lgkmcnt(5)\n\t" \
    "v_mfma_f32_16x16x32_bf16 v[204:207], v[0:3], v[224:227], 0\n\t" \
    "s_waitcnt lgkmcnt(3)\n\t" \
    "v_mfma_f32_16x16x32_bf16 v[204:207], v[4:7], v[228:231], v[204:207]\n\t" \
    "ds_read_b128 v[240:243], v253 offset:18432\n\t" \
    "v_mfma_f32_16x16x32_bf16 v[208:211], v[8:11], v[224:227], 0\n\t" \
    "v_mfma_f32_16x16x32_bf16 v[208:211], v[12:15], v[228:231], v[208:211]\n\t" \
    "v_mfma_f32_16x16x32_bf16 v[200:203], v[244:247], v[224:227], 0\n\t" \
    "s_waitcnt lgkmcnt(1)\n\t" \
    "v_mfma_f32_16x16x32_bf16 v[200:203], v[248:251], v[228:231], v[200:203]\n\t" \
    "v_mfma_f32_16x16x32_bf16 v[216:219], v[0:3], v[232:235], 0\n\t" \
    "ds_read_b128 v[224:227], v253 offset:19456\n\t" \
    "v_mfma_f32_16x16x32_bf16 v[216:219], v[4:7], v[236:239], v[216:219]\n\t" \
    "ds_read_b128 v[228:231], v253 offset:0\n\t" \
    "v_mfma_f32_16x16x32_bf16 v[220:223], v[8:11], v[232:235], 0\n\t" \
    "v_mfma_f32_16x16x32_bf16 v[220:223], v[12:15], v[236:239], v[220:223]\n\t" \
    "v_mfma_f32_16x16x32_bf16 v[212:215], v[244:247], v[232:235], 0\n\t" \
    "ds_read_b128 v[244:247], v254 offset:57344\n\t" \
    "v_mfma_f32_16x16x32_bf16 v[212:215], v[248:251], v[236:239], v[212:215]\n\t" \
    "ds_read_b128 v[248:251], v254 offset:58368\n\t" \
    "s_waitcnt lgkmcnt(4)\n\t" \
    "v_mfma_f32_16x16x32_bf16 v[204:207], v[24:27], v[240:243], v[204:207]\n\t" \
    "ds_read_b128 v[232:235], v253 offset:1024\n\t" \
    "s_waitcnt lgkmcnt(4)\n\t" \
    "v_mfma_f32_16x16x32_bf16 v[204:207], v[28:31], v[224:227], v[204:207]\n\t" \
    "ds_read_b128 v[236:239], v253 offset:20480\n\t" \
    "v_mfma_f32_16x16x32_bf16 v[208:211], v[32:35], v[240:243], v[208:211]\n\t" \
    "v_mfma_f32_16x16x32_bf16 v[208:211], v[36:39], v[224:227], v[208:211]\n\t" \
    "v_mfma_f32_16x16x32_bf16 v[200:203], v[16:19], v[240:243], v[200:203]\n\t" \
    "v_mfma_f32_16x16x32_bf16 v[200:203], v[20:23], v[224:227], v[200:203]\n\t" \
    "s_waitcnt lgkmcnt(4)\n\t" \
    "v_mfma_f32_16x16x32_bf16 v[204:207], v[40:43], v[228:231], v[204:207]\n\t" \
    "ds_read_b128 v[240:243], v253 offset:21504\n\t" \
    "s_waitcnt lgkmcnt(2)\n\t" \
    "v_mfma_f32_16x16x32_bf16 v[204:207], v[44:47], v[232:235], v[204:207]\n\t" \
    "ds_read_b128 v[224:227], v253 offset:2048\n\t" \
    "v_mfma_f32_16x16x32_bf16 v[208:211], v[48:51], v[228:231], v[208:211]\n\t" \
    "v_mfma_f32_16x16x32_bf16 v[208:211], v[52:55], v[232:235], v[208:211]\n\t" \
    "v_mfma_f32_16x16x32_bf16 v[200:203], v[244:247], v[228:231], v[200:203]\n\t" \
    "v_mfma_f32_16x16x32_bf16 v[200:203], v[248:251], v[232:235], v[200:203]\n\t" \
    "s_waitcnt lgkmcnt(2)\n\t" \
    "v_mfma_f32_16x16x32_bf16 v[216:219], v[40:43], v[236:239], v[216:219]\n\t" \
    "ds_read_b128 v[228:231], v253 offset:3072\n\t" \
    "s_add_u32 s44, s40, 1\n\t" \
    "s_waitcnt lgkmcnt(2)\n\t" \
    "v_mfma_f32_16x16x32_bf16 v[216:219], v[44:47], v[240:243], v[216:219]\n\t" \
    "ds_read_b128 v[232:235], v253 offset:4096\n\t" \
    "s_and_b32 s45, s44, 15\n\t" \
    "v_mfma_f32_16x16x32_bf16 v[220:223], v[48:51], v[236:239], v[220:223]\n\t" \
    "s_cmp_eq_u32 s45, 0\n\t" \
    "v_mfma_f32_16x16x32_bf16 v[220:223], v[52:55], v[240:243], v[220:223]\n\t" \
    "s_cselect_b32 s45, %[cj], %[cs]\n\t" \
    "v_mfma_f32_16x16x32_bf16 v[212:215], v[244:247], v[236:239], v[212:215]\n\t" \
    "ds_read_b128 v[244:247], v254 offset:59392\n\t" \
    "s_cmp_eq_u32 s44, 64\n\t" \
    "v_mfma_f32_16x16x32_bf16 v[212:215], v[248:251], v[240:243], v[212:215]\n\t" \
    "ds_read_b128 v[248:251], v254 offset:60416\n\t" \
    "s_cselect_b32 s45, 0, s45\n\t" \
    "s_waitcnt lgkmcnt(4)\n\t" \
    "v_mfma_f32_16x16x32_bf16 v[204:207], v[64:67], v[224:227], v[204:207]\n\t" \
    "ds_read_b128 v[236:239], v253 offset:5120\n\t" \
    "s_add_u32 s42, s42, s45\n\t" \
    "s_waitcnt lgkmcnt(4)\n\t" \
    "v_mfma_f32_16x16x32_bf16 v[204:207], v[68:71], v[228:231], v[204:207]\n\t" \
    "ds_read_b128 v[240:243], v253 offset:24576\n\t" \
    "v_mfma_f32_16x16x32_bf16 v[208:211], v[72:75], v[224:227], v[208:211]\n\t" \
    "v_mfma_f32_16x16x32_bf16 v[208:211], v[76:79], v[228:231], v[208:211]\n\t" \
    "v_mfma_f32_16x16x32_bf16 v[200:203], v[56:59], v[224:227], v[200:203]\n\t" \
    "v_mfma_f32_16x16x32_bf16 v[200:203], v[60:63], v[228:231], v[200:203]\n\t" \
    "s_waitcnt lgkmcnt(4)\n\t" \
    "v_mfma_f32_16x16x32_bf16 v[204:207], v[80:83], v[232:235], v[204:207]\n\t" \
    "ds_read_b128 v[224:227], v253 offset:25600\n\t" \
    "s_waitcnt lgkmcnt(2)\n\t" \
    "v_mfma_f32_16x16x32_bf16 v[204:207], v[84:87], v[236:239], v[204:207]\n\t" \
    "ds_read_b128 v[228:231], v253 offset:6144\n\t" \
    "v_mfma_f32_16x16x32_bf16 v[208:211], v[88:91], v[232:235], v[208:211]\n\t" \
    "v_mfma_f32_16x16x32_bf16 v[208:211], v[92:95], v[236:239], v[208:211]\n\t" \
    "v_mfma_f32_16x16x32_bf16 v[200:203], v[244:247], v[232:235], v[200:203]\n\t" \
    "v_mfma_f32_16x16x32_bf16 v[200:203], v[248:251], v[236:239], v[200:203]\n\t" \
    "s_waitcnt lgkmcnt(2)\n\t" \
    "v_mfma_f32_16x16x32_bf16 v[216:219], v[80:83], v[240:243], v[216:219]\n\t" \
    "ds_read_b128 v[232:235], v253 offset:7168\n\t" \
    "s_and_b32 s44, s40, 3\n\t" \
    "s_waitcnt lgkmcnt(2)\n\t" \
    "v_mfma_f32_16x16x32_bf16 v[216:219], v[84:87], v[224:227], v[216:219]\n\t" \
    "ds_read_b128 v[236:239], v253 offset:8192\n\t" \
    "s_lshl_b32 s44, s44, 17\n\t" \
    "v_mfma_f32_16x16x32_bf16 v[220:223], v[88:91], v[240:243], v[220:223]\n\t" \
    "s_add_u32 s44, s44, %[mb]\n\t" \
    "v_mfma_f32_16x16x32_bf16 v[220:223], v[92:95], v[224:227], v[220:223]\n\t" \
    "s_add_u32 s46, s44, 0x4000\n\t" \
    "v_mfma_f32_16x16x32_bf16 v[212:215], v[244:247], v[240:243], v[212:215]\n\t" \
    "ds_read_b128 v[244:247], v254 offset:61440\n\t" \
    "s_add_u32 s47, s44, 0x4400\n\t" \
    "v_mfma_f32_16x16x32_bf16 v[212:215], v[248:251], v[224:227], v[212:215]\n\t" \
    "ds_read_b128 v[248:251], v254 offset:62464\n\t" \
    "s_waitcnt lgkmcnt(4)\n\t" \
    "v_mfma_f32_16x16x32_bf16 v[204:207], v[104:107], v[228:231], v[204:207]\n\t" \
    "ds_read_b128 v[240:243], v253 offset:9216\n\t" \
    "s_waitcnt lgkmcnt(4)\n\t" \
    "v_mfma_f32_16x16x32_bf16 v[204:207], v[108:111], v[232:235], v[204:207]\n\t" \
    "ds_read_b128 v[224:227], v253 offset:28672\n\t" \
    "v_mfma_f32_16x16x32_bf16 v[208:211], v[112:115], v[228:231], v[208:211]\n\t" \
    "v_mfma_f32_16x16x32_bf16 v[208:211], v[116:119], v[232:235], v[208:211]\n\t" \
    "v_mfma_f32_16x16x32_bf16 v[200:203], v[96:99], v[228:231], v[200:203]\n\t" \
    "v_mfma_f32_16x16x32_bf16 v[200:203], v[100:103], v[232:235], v[200:203]\n\t" \
    "s_waitcnt lgkmcnt(4)\n\t" \
    "v_mfma_f32_16x16x32_bf16 v[204:207], v[120:123], v[236:239], v[204:207]\n\t" \
    "ds_read_b128 v[228:231], v253 offset:29696\n\t" \
    "s_waitcnt lgkmcnt(2)\n\t" \
    "v_mfma_f32_16x16x32_bf16 v[204:207], v[124:127], v[240:243], v[204:207]\n\t" \
    "ds_read_b128 v[232:235], v253 offset:10240\n\t" \
    "v_mfma_f32_16x16x32_bf16 v[208:211], v[128:131], v[236:239], v[208:211]\n\t" \
    "v_mfma_f32_16x16x32_bf16 v[208:211], v[132:135], v[240:243], v[208:211]\n\t" \
    "v_mfma_f32_16x16x32_bf16 v[200:203], v[244:247], v[236:239], v[200:203]\n\t" \
    "v_mfma_f32_16x16x32_bf16 v[200:203], v[248:251], v[240:243], v[200:203]\n\t" \
    "s_waitcnt lgkmcnt(2)\n\t" \
    "v_mfma_f32_16x16x32_bf16 v[216:219], v[120:123], v[224:227], v[216:219]\n\t" \
    "ds_read_b128 v[236:239], v253 offset:11264\n\t" \
    "s_waitcnt lgkmcnt(2)\n\t" \
    "v_mfma_f32_16x16x32_bf16 v[216:219], v[124:127], v[228:231], v[216:219]\n\t" \
    "ds_read_b128 v[240:243], v253 offset:12288\n\t" \
    "v_mfma_f32_16x16x32_bf16 v[220:223], v[128:131], v[224:227], v[220:223]\n\t" \
    "v_mfma_f32_16x16x32_bf16 v[220:223], v[132:135], v[228:231], v[220:223]\n\t" \
    "v_mfma_f32_16x16x32_bf16 v[212:215], v[244:247], v[224:227], v[212:215]\n\t" \
    "ds_read_b128 v[244:247], v254 offset:63488\n\t" \
    "v_mfma_f32_16x16x32_bf16 v[212:215], v[248:251], v[228:231], v[212:215]\n\t" \
    "ds_read_b128 v[248:251], v254 offset:64512\n\t" \
    "v_and_b32 v252, 0xf0, v253\n\t" \
    "v_lshlrev_b32 v252, 3, v252\n\t" \
    "buffer_load_dword v252, v252, s[60:63], %[fl] offen sc1\n\t" \
    "s_waitcnt lgkmcnt(4)\n\t" \
    "v_mfma_f32_16x16x32_bf16 v[204:207], v[144:147], v[232:235], v[204:207]\n\t" \
    "ds_read_b128 v[224:227], v253 offset:13312\n\t" \
    "s_waitcnt lgkmcnt(4)\n\t" \
    "v_mfma_f32_16x16x32_bf16 v[204:207], v[148:151], v[236:239], v[204:207]\n\t" \
    "ds_read_b128 v[228:231], v253 offset:32768\n\t" \
    "v_mfma_f32_16x16x32_bf16 v[208:211], v[152:155], v[232:235], v[208:211]\n\t" \
    "v_mfma_f32_16x16x32_bf16 v[208:211], v[156:159], v[236:239], v[208:211]\n\t" \
    "v_mfma_f32_16x16x32_bf16 v[200:203], v[136:139], v[232:235], v[200:203]\n\t" \
    "v_mfma_f32_16x16x32_bf16 v[200:203], v[140:143], v[236:239], v[200:203]\n\t" \
    "s_waitcnt lgkmcnt(4)\n\t" \
    "v_mfma_f32_16x16x32_bf16 v[204:207], v[160:163], v[240:243], v[204:207]\n\t" \
    "ds_read_b128 v[232:235], v253 offset:33792\n\t" \
    "s_waitcnt lgkmcnt(2)\n\t" \
    "v_mfma_f32_16x16x32_bf16 v[204:207], v[164:167], v[224:227], v[204:207]\n\t" \
    "ds_read_b128 v[236:239], v253 offset:14336\n\t" \
    "v_mfma_f32_16x16x32_bf16 v[208:211], v[168:171], v[240:243], v[208:211]\n\t" \
    "v_mfma_f32_16x16x32_bf16 v[208:211], v[172:175], v[224:227], v[208:211]\n\t" \
    "v_mfma_f32_16x16x32_bf16 v[200:203], v[244:247], v[240:243], v[200:203]\n\t" \
    "v_mfma_f32_16x16x32_bf16 v[200:203], v[248:251], v[224:227], v[200:203]\n\t" \
    "s_waitcnt lgkmcnt(2)\n\t" \
    "v_mfma_f32_16x16x32_bf16 v[216:219], v[160:163], v[228:231], v[216:219]\n\t" \
    "ds_read_b128 v[240:243], v253 offset:15360\n\t" \
    "s_waitcnt lgkmcnt(2)\n\t" \
    "v_mfma_f32_16x16x32_bf16 v[216:219], v[164:167], v[232:235], v[216:219]\n\t" \
    "ds_read_b128 v[224:227], v253 offset:38912\n\t" \
    "v_mfma_f32_16x16x32_bf16 v[220:223], v[168:171], v[228:231], v[220:223]\n\t" \
    "v_mfma_f32_16x16x32_bf16 v[220:223], v[172:175], v[232:235], v[220:223]\n\t" \
    "v_mfma_f32_16x16x32_bf16 v[212:215], v[244:247], v[228:231], v[212:215]\n\t" \
    "v_mfma_f32_16x16x32_bf16 v[212:215], v[248:251], v[232:235], v[212:215]\n\t" \
    "s_waitcnt lgkmcnt(2)\n\t" \
    "v_mfma_f32_16x16x32_bf16 v[204:207], v[184:187], v[236:239], v[204:207]\n\t" \
    "ds_read_b128 v[228:231], v253 offset:39936\n\t" \
    "s_waitcnt lgkmcnt(2)\n\t" \
    "v_mfma_f32_16x16x32_bf16 v[204:207], v[188:191], v[240:243], v[204:207]\n\t" \
    "ds_read_b128 v[232:235], v253 offset:22528\n\t" \
    "v_mfma_f32_16x16x32_bf16 v[208:211], v[192:195], v[236:239], v[208:211]\n\t" \
    "v_mfma_f32_16x16x32_bf16 v[208:211], v[196:199], v[240:243], v[208:211]\n\t" \
    "v_mfma_f32_16x16x32_bf16 v[200:203], v[176:179], v[236:239], v[200:203]\n\t" \
    "v_mfma_f32_16x16x32_bf16 v[200:203], v[180:183], v[240:243], v[200:203]\n\t" \
    "s_waitcnt lgkmcnt(2)\n\t" \
    "v_mfma_f32_16x16x32_bf16 v[216:219], v[24:27], v[224:227], v[216:219]\n\t" \
    "ds_read_b128 v[236:239], v253 offset:23552\n\t" \
    "s_waitcnt lgkmcnt(2)\n\t" \
    "v_mfma_f32_16x16x32_bf16 v[216:219], v[28:31], v[228:231], v[216:219]\n\t" \
    "ds_read_b128 v[240:243], v253 offset:26624\n\t" \
    "v_mfma_f32_16x16x32_bf16 v[220:223], v[32:35], v[224:227], v[220:223]\n\t" \
    "v_cvt_pk_bf16_f32 v204, v204, v205\n\t" \
    "v_mfma_f32_16x16x32_bf16 v[220:223], v[36:39], v[228:231], v[220:223]\n\t" \
    "v_cvt_pk_bf16_f32 v205, v206, v207\n\t" \
    "v_mfma_f32_16x16x32_bf16 v[212:215], v[16:19], v[224:227], v[212:215]\n\t" \
    "ds_write_b64 v254, v[204:205] offset:4104\n\t" \
    "v_mfma_f32_16x16x32_bf16 v[212:215], v[20:23], v[228:231], v[212:215]\n\t" \
    "v_cvt_pk_bf16_f32 v208, v208, v209\n\t" \
    "s_waitcnt lgkmcnt(3)\n\t" \
    "v_mfma_f32_16x16x32_bf16 v[216:219], v[64:67], v[232:235], v[216:219]\n\t" \
    "ds_read_b128 v[224:227], v253 offset:27648\n\t" \
    "v_cvt_pk_bf16_f32 v209, v210, v211\n\t" \
    "s_waitcnt lgkmcnt(3)\n\t" \
    "v_mfma_f32_16x16x32_bf16 v[216:219], v[68:71], v[236:239], v[216:219]\n\t" \
    "ds_read_b128 v[228:231], v253 offset:30720\n\t" \
    "ds_write_b64 v254, v[208:209] offset:5128\n\t" \
    "v_mfma_f32_16x16x32_bf16 v[220:223], v[72:75], v[232:235], v[220:223]\n\t" \
    "v_cvt_pk_bf16_f32 v200, v200, v201\n\t" \
    "v_mfma_f32_16x16x32_bf16 v[220:223], v[76:79], v[236:239], v[220:223]\n\t" \
    "v_cvt_pk_bf16_f32 v201, v202, v203\n\t" \
    "v_mfma_f32_16x16x32_bf16 v[212:215], v[56:59], v[232:235], v[212:215]\n\t" \
    "ds_write_b64 v254, v[200:201] offset:3080\n\t" \
    "v_mfma_f32_16x16x32_bf16 v[212:215], v[60:63], v[236:239], v[212:215]\n\t" \
    "s_waitcnt lgkmcnt(5)\n\t" \
    "v_mfma_f32_16x16x32_bf16 v[216:219], v[104:107], v[240:243], v[216:219]\n\t" \
    "ds_read_b128 v[232:235], v253 offset:31744\n\t" \
    "s_waitcnt lgkmcnt(4)\n\t" \
    "v_mfma_f32_16x16x32_bf16 v[216:219], v[108:111], v[224:227], v[216:219]\n\t" \
    "ds_read_b128 v[236:239], v253 offset:34816\n\t" \
    "v_mfma_f32_16x16x32_bf16 v[220:223], v[112:115], v[240:243], v[220:223]\n\t" \
    "v_mfma_f32_16x16x32_bf16 v[220:223], v[116:119], v[224:227], v[220:223]\n\t" \
    "v_mfma_f32_16x16x32_bf16 v[212:215], v[96:99], v[240:243], v[212:215]\n\t" \
    "v_mfma_f32_16x16x32_bf16 v[212:215], v[100:103], v[224:227], v[212:215]\n\t" \
    "ds_read_b128 v[240:243], v253 offset:35840\n\t" \
    "s_waitcnt lgkmcnt(3)\n\t" \
    "s_barrier \n\t" \
    "v_mfma_f32_16x16x32_bf16 v[216:219], v[144:147], v[228:231], v[216:219]\n\t" \
    "s_waitcnt lgkmcnt(2)\n\t" \
    "v_mfma_f32_16x16x32_bf16 v[216:219], v[148:151], v[232:235], v[216:219]\n\t" \
    "s_waitcnt lgkmcnt(1)\n\t" \
    "v_mfma_f32_16x16x32_bf16 v[216:219], v[184:187], v[236:239], v[216:219]\n\t" \
    "s_waitcnt lgkmcnt(0)\n\t" \
    "v_mfma_f32_16x16x32_bf16 v[216:219], v[188:191], v[240:243], v[216:219]\n\t" \
    "v_mfma_f32_16x16x32_bf16 v[220:223], v[152:155], v[228:231], v[220:223]\n\t" \
    "v_mfma_f32_16x16x32_bf16 v[220:223], v[156:159], v[232:235], v[220:223]\n\t" \
    "v_mfma_f32_16x16x32_bf16 v[220:223], v[192:195], v[236:239], v[220:223]\n\t" \
    "v_cvt_pk_bf16_f32 v216, v216, v217\n\t" \
    "v_cvt_pk_bf16_f32 v217, v218, v219\n\t" \
    "v_mfma_f32_16x16x32_bf16 v[220:223], v[196:199], v[240:243], v[220:223]\n\t" \
    "ds_write_b64 v254, v[216:217] offset:10248\n\t" \
    "v_mfma_f32_16x16x32_bf16 v[212:215], v[136:139], v[228:231], v[212:215]\n\t" \
    "v_mfma_f32_16x16x32_bf16 v[212:215], v[140:143], v[232:235], v[212:215]\n\t" \
    "v_mfma_f32_16x16x32_bf16 v[212:215], v[176:179], v[236:239], v[212:215]\n\t" \
    "v_cvt_pk_bf16_f32 v220, v220, v221\n\t" \
    "v_cvt_pk_bf16_f32 v221, v222, v223\n\t" \
    "v_mfma_f32_16x16x32_bf16 v[212:215], v[180:183], v[240:243], v[212:215]\n\t" \
    "ds_write_b64 v254, v[220:221] offset:11272\n\t" \
    "s_nop 6\n\t" \
    "v_cvt_pk_bf16_f32 v212, v212, v213\n\t" \
    "v_cvt_pk_bf16_f32 v213, v214, v215\n\t" \
    "ds_write_b64 v254, v[212:213] offset:9224\n\t" \
    LST6 \
    LJIT \
    "s_waitcnt vmcnt(0) lgkmcnt(0)\n\t" \
    "s_barrier \n\t" \
    "s_setprio 2\n\t" \
    "s_add_u32 s44, s40, 1\n\t" \
    "s_min_u32 s44, s44, 63\n\t" \
    ".Lv38_%=:\n\t" \
    "v_cmp_ge_u32 vcc, s44, v252\n\t" \
    "s_cbranch_vccz .Lv39_%=\n\t" \
    "s_cmp_eq_u32 s41, 0\n\t" \
    "s_cbranch_scc1 .Lv39_%=\n\t" \
    "s_sub_u32 s41, s41, 1\n\t" \
    "s_sleep 2\n\t" \
    "v_and_b32 v252, 0xf0, v253\n\t" \
    "v_lshlrev_b32 v252, 3, v252\n\t" \
    "buffer_load_dword v252, v252, s[60:63], %[fl] offen sc1\n\t" \
    "s_waitcnt vmcnt(0)\n\t" \
    "s_branch .Lv38_%=\n\t" \
    ".Lv39_%=:\n\t" \
    "s_mov_b32 m0, 0x3000\n\t" \
    "s_add_u32 s49, s42, 0x0\n\t" \
    "buffer_load_dwordx4 v253, s[60:63], s49 offen offset:0 sc1 lds\n\t" \
    "s_sleep 1\n\t" \
    "buffer_load_dwordx4 v253, s[60:63], s49 offen offset:1024 sc1 lds\n\t" \
    "s_sleep 1\n\t" \
    "buffer_load_dwordx4 v253, s[60:63], s49 offen offset:2048 sc1 lds\n\t" \
    "s_sleep 1\n\t" \
    "buffer_load_dwordx4 v253, s[60:63], s49 offen offset:3072 sc1 lds\n\t" \
    "s_sleep 1\n\t" \
    "s_mov_b32 m0, 0x4c00\n\t" \
    "s_add_u32 s49, s42, 0x4c00\n\t" \
    "buffer_load_dwordx4 v253, s[60:63], s49 offen offset:0 sc1 lds\n\t" \
    "s_sleep 1\n\t" \
    "s_mov_b32 m0, 0x8000\n\t" \
    "s_add_u32 s49, s42, 0x1000\n\t" \
    "buffer_load_dwordx4 v253, s[60:63], s49 offen offset:0 sc1 lds\n\t" \
    "s_sleep 1\n\t" \
    "buffer_load_dwordx4 v253, s[60:63], s49 offen offset:1024 sc1 lds\n\t" \
    "s_sleep 1\n\t" \
    "buffer_load_dwordx4 v253, s[60:63], s49 offen offset:2048 sc1 lds\n\t" \
    "s_sleep 1\n\t" \
    "buffer_load_dwordx4 v253, s[60:63], s49 offen offset:3072 sc1 lds\n\t" \
    "s_sleep 1\n\t" \
    "s_mov_b32 m0, 0x9c00\n\t" \
    "s_add_u32 s49, s42, 0x5c00\n\t" \
    "buffer_load_dwordx4 v253, s[60:63], s49 offen offset:0 sc1 lds\n\t" \
    "s_sleep 1\n\t" \
    "ds_read_b128 v[200:203], v254 offset:55296\n\t" \
    "ds_read_b128 v[204:207], v254 offset:56320\n\t" \
    "ds_read_b128 v[216:219], v254 offset:57344\n\t" \
    "ds_read_b128 v[220:223], v254 offset:58368\n\t" \
    "v_mov_b64 v[208:209], 0\n\t" \
    "v_mov_b64 v[210:211], 0\n\t" \
    "v_mov_b64 v[244:245], 0\n\t" \
    "v_mov_b64 v[246:247], 0\n\t" \
    "v_mov_b64 v[248:249], 0\n\t" \
    "v_mov_b64 v[250:251], 0\n\t" \
    LST1 \
    LJIT \
    "s_sleep 5\n\t" \
    "s_mov_b32 m0, 0xc000\n\t" \
    "s_nop 0\n\t" \
    "buffer_load_dwordx4 v253, s[60:63], s46 offen sc1 lds\n\t" \
    "s_mov_b32 m0, 0xc400\n\t" \
    "s_nop 0\n\t" \
    "buffer_load_dwordx4 v253, s[60:63], s47 offen sc1 lds\n\t" \
    "s_waitcnt lgkmcnt(0)\n\t" \
    LFLUSH212 \
    "v_bfe_i32 v212, v253, 5, 1\n\t" \
    "v_and_b32 v212, 0xaaaa, v212\n\t" \
    "v_or_b32 v212, 0x4444, v212\n\t" \
    "v_and_b32 v213, 0xffff, v255\n\t" \
    "v_lshrrev_b32 v214, 16, v255\n\t" \
    ".Lv310_%=:\n\t" \
    "s_waitcnt vmcnt(0)\n\t" \
    "ds_read_b128 v[224:227], v253 offset:49152\n\t" \
    "ds_read_b128 v[228:231], v253 offset:50176\n\t" \
    "s_waitcnt lgkmcnt(1)\n\t" \
    "v_max_u32 v215, v224, v225\n\t" \
    "v_max3_u32 v215, v226, v227, v215\n\t" \
    "s_waitcnt lgkmcnt(0)\n\t" \
    "v_max3_u32 v215, v228, v229, v215\n\t" \
    "v_max3_u32 v215, v230, v231, v215\n\t" \
    "v_cmp_eq_u32 vcc, -1, v215\n\t" \
    "s_cbranch_vccz .Lv311_%=\n\t" \
    "v_max_u32 v215, v224, v225\n\t" \
    "v_max3_u32 v215, v226, v227, v215\n\t" \
    "v_cmp_eq_u32 vcc, -1, v215\n\t" \
    "s_cbranch_vccz .Lv312_%=\n\t" \
    "s_mov_b32 m0, 0xc000\n\t" \
    "s_nop 0\n\t" \
    "buffer_load_dwordx4 v253, s[60:63], s46 offen sc1 lds\n\t" \
    ".Lv312_%=:\n\t" \
    "v_max_u32 v215, v228, v229\n\t" \
    "v_max3_u32 v215, v230, v231, v215\n\t" \
    "v_cmp_eq_u32 vcc, -1, v215\n\t" \
    "s_cbranch_vccz .Lv313_%=\n\t" \
    "s_mov_b32 m0, 0xc400\n\t" \
    "s_nop 0\n\t" \
    "buffer_load_dwordx4 v253, s[60:63], s47 offen sc1 lds\n\t" \
    ".Lv313_%=:\n\t" \
    "s_cmp_eq_u32 s41, 0\n\t" \
    "s_cbranch_scc1 .Lv311_%=\n\t" \
    "s_sub_u32 s41, s41, 1\n\t" \
    "s_branch .Lv310_%=\n\t" \
    ".Lv311_%=:\n\t" \
    LST2 \
    LJIT \
    "s_waitcnt lgkmcnt(0)\n\t" \
    "s_barrier \n\t" \
    LST3 \
    LJIT \
    "ds_read_b128 v[232:235], v253 offset:40960\n\t" \
    "ds_read_b128 v[236:239], v253 offset:41984\n\t" \
    "ds_read_b128 v[240:243], v253 offset:43008\n\t" \
    "v_smfmac_f32_16x16x64_bf16 v[244:247], v[224:227], v[0:7], v212\n\t" \
    "v_smfmac_f32_16x16x64_bf16 v[248:251], v[224:227], v[8:15], v212\n\t" \
    "v_smfmac_f32_16x16x64_bf16 v[208:211], v[224:227], v[200:207], v212\n\t" \
    "ds_read_b128 v[200:203], v254 offset:59392\n\t" \
    "ds_read_b128 v[204:207], v254 offset:60416\n\t" \
    "ds_read_b128 v[224:227], v253 offset:44032\n\t" \
    "v_smfmac_f32_16x16x64_bf16 v[208:211], v[228:231], v[16:23], v212\n\t" \
    "v_smfmac_f32_16x16x64_bf16 v[244:247], v[228:231], v[24:31], v212\n\t" \
    "v_smfmac_f32_16x16x64_bf16 v[248:251], v[228:231], v[32:39], v212\n\t" \
    "ds_read_b128 v[228:231], v253 offset:45056\n\t" \
    "s_waitcnt lgkmcnt(6)\n\t" \
    "v_smfmac_f32_16x16x64_bf16 v[208:211], v[232:235], v[216:223], v212\n\t" \
    "ds_read_b128 v[216:219], v254 offset:61440\n\t" \
    "ds_read_b128 v[220:223], v254 offset:62464\n\t" \
    "v_smfmac_f32_16x16x64_bf16 v[244:247], v[232:235], v[40:47], v212\n\t" \
    "v_smfmac_f32_16x16x64_bf16 v[248:251], v[232:235], v[48:55], v212\n\t" \
    "ds_read_b128 v[232:235], v253 offset:46080\n\t" \
    "s_waitcnt lgkmcnt(8)\n\t" \
    "v_smfmac_f32_16x16x64_bf16 v[208:211], v[236:239], v[56:63], v212\n\t" \
    "v_smfmac_f32_16x16x64_bf16 v[244:247], v[236:239], v[64:71], v212\n\t" \
    "v_smfmac_f32_16x16x64_bf16 v[248:251], v[236:239], v[72:79], v212\n\t" \
    "ds_read_b128 v[236:239], v253 offset:47104\n\t" \
    "s_waitcnt lgkmcnt(6)\n\t" \
    "v_smfmac_f32_16x16x64_bf16 v[208:211], v[240:243], v[200:207], v212\n\t" \
    "ds_read_b128 v[200:203], v254 offset:63488\n\t" \
    "ds_read_b128 v[204:207], v254 offset:64512\n\t" \
    "v_smfmac_f32_16x16x64_bf16 v[244:247], v[240:243], v[80:87], v212\n\t" \
    "v_smfmac_f32_16x16x64_bf16 v[248:251], v[240:243], v[88:95], v212\n\t" \
    "ds_read_b128 v[240:243], v253 offset:48128\n\t" \
    "s_waitcnt lgkmcnt(8)\n\t" \
    "v_smfmac_f32_16x16x64_bf16 v[208:211], v[224:227], v[96:103], v212\n\t" \
    "v_smfmac_f32_16x16x64_bf16 v[244:247], v[224:227], v[104:111], v212\n\t" \
    "v_smfmac_f32_16x16x64_bf16 v[248:251], v[224:227], v[112:119], v212\n\t" \
    "s_waitcnt lgkmcnt(7)\n\t" \
    "v_smfmac_f32_16x16x64_bf16 v[244:247], v[228:231], v[120:127], v212\n\t" \
    "s_waitcnt lgkmcnt(4)\n\t" \
    "v_smfmac_f32_16x16x64_bf16 v[244:247], v[232:235], v[144:151], v212\n\t" \
    "s_waitcnt lgkmcnt(3)\n\t" \
    "v_smfmac_f32_16x16x64_bf16 v[244:247], v[236:239], v[160:167], v212\n\t" \
    "s_waitcnt lgkmcnt(0)\n\t" \
    "v_smfmac_f32_16x16x64_bf16 v[244:247], v[240:243], v[184:191], v212\n\t" \
    "v_smfmac_f32_16x16x64_bf16 v[248:251], v[228:231], v[128:135], v212\n\t" \
    "v_smfmac_f32_16x16x64_bf16 v[248:251], v[232:235], v[152:159], v212\n\t" \
    "v_smfmac_f32_16x16x64_bf16 v[248:251], v[236:239], v[168:175], v212\n\t" \
    "v_add_f32 v244, v244, v246\n\t" \
    "v_add_f32 v245, v245, v247\n\t" \
    "v_cvt_pk_bf16_f32 v244, v244, v245\n\t" \
    "v_smfmac_f32_16x16x64_bf16 v[248:251], v[240:243], v[192:199], v212\n\t" \
    "ds_write_b16 v213, v244 offset:512\n\t" \
    "ds_write_b16_d16_hi v213, v244 offset:528\n\t" \
    "v_smfmac_f32_16x16x64_bf16 v[208:211], v[228:231], v[216:223], v212\n\t" \
    "v_smfmac_f32_16x16x64_bf16 v[208:211], v[232:235], v[136:143], v212\n\t" \
    "v_smfmac_f32_16x16x64_bf16 v[208:211], v[236:239], v[200:207], v212\n\t" \
    "v_add_f32 v248, v248, v250\n\t" \
    "v_add_f32 v249, v249, v251\n\t" \
    "v_cvt_pk_bf16_f32 v248, v248, v249\n\t" \
    "v_smfmac_f32_16x16x64_bf16 v[208:211], v[240:243], v[176:183], v212\n\t" \
    "ds_write_b16 v213, v248 offset:1024\n\t" \
    "ds_write_b16_d16_hi v213, v248 offset:1040\n\t" \
    "s_nop 5\n\t" \
    "v_add_f32 v208, v208, v210\n\t" \
    "v_add_f32 v209, v209, v211\n\t" \
    "v_cvt_pk_bf16_f32 v208, v208, v209\n\t" \
    "ds_write_b16 v213, v208 offset:0\n\t" \
    "ds_write_b16_d16_hi v213, v208 offset:16\n\t" \
    "s_setprio 0\n\t" \
    "s_waitcnt lgkmcnt(0)\n\t" \
    "s_barrier \n\t" \
    LST4 \
    LJIT \
    "ds_read_b128 v[224:227], v253 offset:16384\n\t" \
    "ds_read_b128 v[244:247], v254 offset:55296\n\t" \
    "ds_read_b128 v[228:231], v253 offset:17408\n\t" \
    "ds_read_b128 v[232:235], v253 offset:36864\n\t" \
    "ds_read_b128 v[236:239], v253 offset:37888\n\t" \
    "ds_read_b128 v[248:251], v254 offset:56320\n\t" \
    "s_add_u32 s40, s40, 1\n\t" \
    "s_cmp_lg_u32 s40, 64\n\t" \
    "s_cbranch_scc1 .Lv37_%=\n\t" \
    "s_branch .Lend%=\n\t" \
    ".Lv2_%=:\n\t" \
    "s_mov_b32 s42, %[xs]\n\t" \
    "s_add_u32 m0, %[wm], 0xc000\n\t" \
    "s_mov_b32 s44, 0x0\n\t" \
    "buffer_load_dwordx4 v240, %[wr], s44 offen offset:0 nt lds\n\t" \
    "buffer_load_dwordx4 v241, %[wr], s44 offen offset:1024 nt lds\n\t" \
    "buffer_load_dwordx4 v242, %[wr], s44 offen offset:2048 nt lds\n\t" \
    "s_add_u32 m0, %[wm], 0xf000\n\t" \
    "s_mov_b32 s44, 0x60000\n\t" \
    "buffer_load_dwordx4 v240, %[wr], s44 offen offset:0 nt lds\n\t" \
    "buffer_load_dwordx4 v241, %[wr], s44 offen offset:1024 nt lds\n\t" \
    "buffer_load_dwordx4 v242, %[wr], s44 offen offset:2048 nt lds\n\t" \
    "s_add_u32 m0, %[wm], 0x0\n\t" \
    "s_mov_b32 s44, 0xc0000\n\t" \
    "buffer_load_dwordx4 v240, %[wr], s44 offen offset:0 nt lds\n\t" \
    "buffer_load_dwordx4 v241, %[wr], s44 offen offset:1024 nt lds\n\t" \
    "buffer_load_dwordx4 v242, %[wr], s44 offen offset:2048 nt lds\n\t" \
    "s_add_u32 m0, %[wm], 0x3000\n\t" \
    "s_mov_b32 s44, 0x120000\n\t" \
    "buffer_load_dwordx4 v240, %[wr], s44 offen offset:0 nt lds\n\t" \
    "buffer_load_dwordx4 v241, %[wr], s44 offen offset:1024 nt lds\n\t" \
    "buffer_load_dwordx4 v242, %[wr], s44 offen offset:2048 nt lds\n\t" \
    "s_add_u32 m0, %[wm], 0x6000\n\t" \
    "s_mov_b32 s44, 0x180000\n\t" \
    "buffer_load_dwordx4 v240, %[wr], s44 offen offset:0 nt lds\n\t" \
    "buffer_load_dwordx4 v241, %[wr], s44 offen offset:1024 nt lds\n\t" \
    "buffer_load_dwordx4 v242, %[wr], s44 offen offset:2048 nt lds\n\t" \
    "v_add_u32 v244, 0xc000, v243\n\t" \
    "s_waitcnt vmcnt(12) lgkmcnt(0)\n\t" \
    "s_barrier \n\t" \
    "s_add_u32 m0, %[wm], 0x9000\n\t" \
    "s_mov_b32 s44, 0x1e0000\n\t" \
    "buffer_load_dwordx4 v240, %[wr], s44 offen offset:0 nt lds\n\t" \
    "buffer_load_dwordx4 v241, %[wr], s44 offen offset:1024 nt lds\n\t" \
    "buffer_load_dwordx4 v242, %[wr], s44 offen offset:2048 nt lds\n\t" \
    "ds_read_b64_tr_b16 v[208:209], v244 offset:0\n\t" \
    "ds_read_b64_tr_b16 v[210:211], v244 offset:1536\n\t" \
    "ds_read_b64_tr_b16 v[40:41], v244 offset:32\n\t" \
    "ds_read_b64_tr_b16 v[42:43], v244 offset:1568\n\t" \
    "ds_read_b64_tr_b16 v[48:49], v244 offset:64\n\t" \
    "ds_read_b64_tr_b16 v[50:51], v244 offset:1600\n\t" \
    "v_add_u32 v244, 0xf000, v243\n\t" \
    "s_waitcnt vmcnt(12) lgkmcnt(0)\n\t" \
    "s_barrier \n\t" \
    "s_add_u32 m0, %[wm], 0xc000\n\t" \
    "s_mov_b32 s44, 0x240000\n\t" \
    "buffer_load_dwordx4 v240, %[wr], s44 offen offset:0 nt lds\n\t" \
    "buffer_load_dwordx4 v241, %[wr], s44 offen offset:1024 nt lds\n\t" \
    "buffer_load_dwordx4 v242, %[wr], s44 offen offset:2048 nt lds\n\t" \
    "ds_read_b64_tr_b16 v[212:213], v244 offset:0\n\t" \
    "ds_read_b64_tr_b16 v[214:215], v244 offset:1536\n\t" \
    "ds_read_b64_tr_b16 v[44:45], v244 offset:32\n\t" \
    "ds_read_b64_tr_b16 v[46:47], v244 offset:1568\n\t" \
    "ds_read_b64_tr_b16 v[52:53], v244 offset:64\n\t" \
    "ds_read_b64_tr_b16 v[54:55], v244 offset:1600\n\t" \
    "v_add_u32 v244, 0x0, v243\n\t" \
    "s_waitcnt vmcnt(12) lgkmcnt(0)\n\t" \
    "s_barrier \n\t" \
    "s_add_u32 m0, %[wm], 0xf000\n\t" \
    "s_mov_b32 s44, 0x2a0000\n\t" \
    "buffer_load_dwordx4 v240, %[wr], s44 offen offset:0 nt lds\n\t" \
    "buffer_load_dwordx4 v241, %[wr], s44 offen offset:1024 nt lds\n\t" \
    "buffer_load_dwordx4 v242, %[wr], s44 offen offset:2048 nt lds\n\t" \
    "ds_read_b64_tr_b16 v[56:57], v244 offset:0\n\t" \
    "ds_read_b64_tr_b16 v[58:59], v244 offset:1536\n\t" \
    "ds_read_b64_tr_b16 v[64:65], v244 offset:32\n\t" \
    "ds_read_b64_tr_b16 v[66:67], v244 offset:1568\n\t" \
    "ds_read_b64_tr_b16 v[72:73], v244 offset:64\n\t" \
    "ds_read_b64_tr_b16 v[74:75], v244 offset:1600\n\t" \
    "v_add_u32 v244, 0x3000, v243\n\t" \
    "s_waitcnt vmcnt(12) lgkmcnt(0)\n\t" \
    "s_barrier \n\t" \
    "s_add_u32 m0, %[wm], 0x0\n\t" \
    "s_mov_b32 s44, 0x300000\n\t" \
    "buffer_load_dwordx4 v240, %[wr], s44 offen offset:0 nt lds\n\t" \
    "buffer_load_dwordx4 v241, %[wr], s44 offen offset:1024 nt lds\n\t" \
    "buffer_load_dwordx4 v242, %[wr], s44 offen offset:2048 nt lds\n\t" \
    "ds_read_b64_tr_b16 v[60:61], v244 offset:0\n\t" \
    "ds_read_b64_tr_b16 v[62:63], v244 offset:1536\n\t" \
    "ds_read_b64_tr_b16 v[68:69], v244 offset:32\n\t" \
    "ds_read_b64_tr_b16 v[70:71], v244 offset:1568\n\t" \
    "ds_read_b64_tr_b16 v[76:77], v244 offset:64\n\t" \
    "ds_read_b64_tr_b16 v[78:79], v244 offset:1600\n\t" \
    "v_add_u32 v244, 0x6000, v243\n\t" \
    "s_waitcnt vmcnt(12) lgkmcnt(0)\n\t" \
    "s_barrier \n\t" \
    "s_add_u32 m0, %[wm], 0x3000\n\t" \
    "s_mov_b32 s44, 0x360000\n\t" \
    "buffer_load_dwordx4 v240, %[wr], s44 offen offset:0 nt lds\n\t" \
    "buffer_load_dwordx4 v241, %[wr], s44 offen offset:1024 nt lds\n\t" \
    "buffer_load_dwordx4 v242, %[wr], s44 offen offset:2048 nt lds\n\t" \
    "ds_read_b64_tr_b16 v[216:217], v244 offset:0\n\t" \
    "ds_read_b64_tr_b16 v[218:219], v244 offset:1536\n\t" \
    "ds_read_b64_tr_b16 v[80:81], v244 offset:32\n\t" \
    "ds_read_b64_tr_b16 v[82:83], v244 offset:1568\n\t" \
    "ds_read_b64_tr_b16 v[88:89], v244 offset:64\n\t" \
    "ds_read_b64_tr_b16 v[90:91], v244 offset:1600\n\t" \
    "v_add_u32 v244, 0x9000, v243\n\t" \
    "s_waitcnt vmcnt(12) lgkmcnt(0)\n\t" \
    "s_barrier \n\t" \
    "s_add_u32 m0, %[wm], 0x6000\n\t" \
    "s_mov_b32 s44, 0x3c0000\n\t" \
    "buffer_load_dwordx4 v240, %[wr], s44 offen offset:0 nt lds\n\t" \
    "buffer_load_dwordx4 v241, %[wr], s44 offen offset:1024 nt lds\n\t" \
    "buffer_load_dwordx4 v242, %[wr], s44 offen offset:2048 nt lds\n\t" \
    "ds_read_b64_tr_b16 v[220:221], v244 offset:0\n\t" \
    "ds_read_b64_tr_b16 v[222:223], v244 offset:1536\n\t" \
    "ds_read_b64_tr_b16 v[84:85], v244 offset:32\n\t" \
    "ds_read_b64_tr_b16 v[86:87], v244 offset:1568\n\t" \
    "ds_read_b64_tr_b16 v[92:93], v244 offset:64\n\t" \
    "ds_read_b64_tr_b16 v[94:95], v244 offset:1600\n\t" \
    "v_add_u32 v244, 0xc000, v243\n\t" \
    "s_waitcnt vmcnt(12) lgkmcnt(0)\n\t" \
    "s_barrier \n\t" \
    "v_mov_b32 v246, 0\n\t" \
    "buffer_load_dword v246, v246, s[60:63], %[mo] offen sc1\n\t" \
    "s_add_u32 m0, %[wm], 0x9000\n\t" \
    "s_mov_b32 s44, 0x420000\n\t" \
    "buffer_load_dwordx4 v240, %[wr], s44 offen offset:0 nt lds\n\t" \
    "buffer_load_dwordx4 v241, %[wr], s44 offen offset:1024 nt lds\n\t" \
    "buffer_load_dwordx4 v242, %[wr], s44 offen offset:2048 nt lds\n\t" \
    "ds_read_b64_tr_b16 v[96:97], v244 offset:0\n\t" \
    "ds_read_b64_tr_b16 v[98:99], v244 offset:1536\n\t" \
    "ds_read_b64_tr_b16 v[104:105], v244 offset:32\n\t" \
    "ds_read_b64_tr_b16 v[106:107], v244 offset:1568\n\t" \
    "ds_read_b64_tr_b16 v[112:113], v244 offset:64\n\t" \
    "ds_read_b64_tr_b16 v[114:115], v244 offset:1600\n\t" \
    "v_add_u32 v244, 0xf000, v243\n\t" \
    "s_waitcnt vmcnt(12) lgkmcnt(0)\n\t" \
    "s_barrier \n\t" \
    "s_add_u32 m0, %[wm], 0xc000\n\t" \
    "s_mov_b32 s44, 0x480000\n\t" \
    "buffer_load_dwordx4 v240, %[wr], s44 offen offset:0 nt lds\n\t" \
    "buffer_load_dwordx4 v241, %[wr], s44 offen offset:1024 nt lds\n\t" \
    "buffer_load_dwordx4 v242, %[wr], s44 offen offset:2048 nt lds\n\t" \
    "ds_read_b64_tr_b16 v[100:101], v244 offset:0\n\t" \
    "ds_read_b64_tr_b16 v[102:103], v244 offset:1536\n\t" \
    "ds_read_b64_tr_b16 v[108:109], v244 offset:32\n\t" \
    "ds_read_b64_tr_b16 v[110:111], v244 offset:1568\n\t" \
    "ds_read_b64_tr_b16 v[116:117], v244 offset:64\n\t" \
    "ds_read_b64_tr_b16 v[118:119], v244 offset:1600\n\t" \
    "v_add_u32 v244, 0x0, v243\n\t" \
    "s_waitcnt vmcnt(12) lgkmcnt(0)\n\t" \
    "s_barrier \n\t" \
    "s_add_u32 m0, %[wm], 0xf000\n\t" \
    "s_mov_b32 s44, 0x4e0000\n\t" \
    "buffer_load_dwordx4 v240, %[wr], s44 offen offset:0 nt lds\n\t" \
    "buffer_load_dwordx4 v241, %[wr], s44 offen offset:1024 nt lds\n\t" \
    "buffer_load_dwordx4 v242, %[wr], s44 offen offset:2048 nt lds\n\t" \
    "ds_read_b64_tr_b16 v[200:201], v244 offset:0\n\t" \
    "ds_read_b64_tr_b16 v[202:203], v244 offset:1536\n\t" \
    "ds_read_b64_tr_b16 v[0:1], v244 offset:32\n\t" \
    "ds_read_b64_tr_b16 v[2:3], v244 offset:1568\n\t" \
    "ds_read_b64_tr_b16 v[8:9], v244 offset:64\n\t" \
    "ds_read_b64_tr_b16 v[10:11], v244 offset:1600\n\t" \
    "v_add_u32 v244, 0x3000, v243\n\t" \
    "s_waitcnt vmcnt(12) lgkmcnt(0)\n\t" \
    "s_barrier \n\t" \
    "s_add_u32 m0, %[wm], 0x0\n\t" \
    "s_mov_b32 s44, 0x540000\n\t" \
    "buffer_load_dwordx4 v240, %[wr], s44 offen offset:0 nt lds\n\t" \
    "buffer_load_dwordx4 v241, %[wr], s44 offen offset:1024 nt lds\n\t" \
    "buffer_load_dwordx4 v242, %[wr], s44 offen offset:2048 nt lds\n\t" \
    "ds_read_b64_tr_b16 v[204:205], v244 offset:0\n\t" \
    "ds_read_b64_tr_b16 v[206:207], v244 offset:1536\n\t" \
    "ds_read_b64_tr_b16 v[4:5], v244 offset:32\n\t" \
    "ds_read_b64_tr_b16 v[6:7], v244 offset:1568\n\t" \
    "ds_read_b64_tr_b16 v[12:13], v244 offset:64\n\t" \
    "ds_read_b64_tr_b16 v[14:15], v244 offset:1600\n\t" \
    "v_add_u32 v244, 0x6000, v243\n\t" \
    "s_waitcnt vmcnt(12) lgkmcnt(0)\n\t" \
    "s_barrier \n\t" \
    "s_add_u32 m0, %[wm], 0x3000\n\t" \
    "s_mov_b32 s44, 0x5a0000\n\t" \
    "buffer_load_dwordx4 v240, %[wr], s44 offen offset:0 nt lds\n\t" \
    "buffer_load_dwordx4 v241, %[wr], s44 offen offset:1024 nt lds\n\t" \
    "buffer_load_dwordx4 v242, %[wr], s44 offen offset:2048 nt lds\n\t" \
    "ds_read_b64_tr_b16 v[224:225], v244 offset:0\n\t" \
    "ds_read_b64_tr_b16 v[226:227], v244 offset:1536\n\t" \
    "ds_read_b64_tr_b16 v[120:121], v244 offset:32\n\t" \
    "ds_read_b64_tr_b16 v[122:123], v244 offset:1568\n\t" \
    "ds_read_b64_tr_b16 v[128:129], v244 offset:64\n\t" \
    "ds_read_b64_tr_b16 v[130:131], v244 offset:1600\n\t" \
    "v_add_u32 v244, 0x9000, v243\n\t" \
    "s_waitcnt vmcnt(12) lgkmcnt(0)\n\t" \
    "s_barrier \n\t" \
    ".Lv21_%=:\n\t" \
    "v_readfirstlane_b32 s43, v246\n\t" \
    "s_nop 0\n\t" \
    "s_cmp_lg_u32 s43, -1\n\t" \
    "s_cbranch_scc1 .Lv22_%=\n\t" \
    "s_cmp_eq_u32 s41, 0\n\t" \
    "s_cbranch_scc1 .Lv22_%=\n\t" \
    "s_sub_u32 s41, s41, 1\n\t" \
    "s_sleep 4\n\t" \
    "v_mov_b32 v246, 0\n\t" \
    "buffer_load_dword v246, v246, s[60:63], %[mo] offen sc1\n\t" \
    "s_waitcnt vmcnt(0)\n\t" \
    "s_branch .Lv21_%=\n\t" \
    ".Lv22_%=:\n\t" \
    "v_mov_b32 v246, 0x8000\n\t" \
    "s_mov_b64 exec, 1\n\t" \
    "buffer_store_dword v246, v246, s[60:63], %[mo] offen sc1\n\t" \
    "s_mov_b64 exec, -1\n\t" \
    "s_and_b32 s43, s43, 0x2000\n\t" \
    "s_xor_b32 s43, s43, %[lx]\n\t" \
    "s_add_u32 s60, s60, s43\n\t" \
    "s_addc_u32 s61, s61, 0\n\t" \
    "v_and_b32 v252, 0xf0, v253\n\t" \
    "v_lshlrev_b32 v252, 3, v252\n\t" \
    "buffer_load_dword v252, v252, s[60:63], %[fl] offen sc1\n\t" \
    "s_add_u32 m0, %[wm], 0x6000\n\t" \
    "s_mov_b32 s44, 0x600000\n\t" \
    "buffer_load_dwordx4 v240, %[wr], s44 offen offset:0 nt lds\n\t" \
    "buffer_load_dwordx4 v241, %[wr], s44 offen offset:1024 nt lds\n\t" \
    "buffer_load_dwordx4 v242, %[wr], s44 offen offset:2048 nt lds\n\t" \
    "ds_read_b64_tr_b16 v[228:229], v244 offset:0\n\t" \
    "ds_read_b64_tr_b16 v[230:231], v244 offset:1536\n\t" \
    "ds_read_b64_tr_b16 v[124:125], v244 offset:32\n\t" \
    "ds_read_b64_tr_b16 v[126:127], v244 offset:1568\n\t" \
    "ds_read_b64_tr_b16 v[132:133], v244 offset:64\n\t" \
    "ds_read_b64_tr_b16 v[134:135], v244 offset:1600\n\t" \
    "v_add_u32 v244, 0xc000, v243\n\t" \
    "s_waitcnt vmcnt(12) lgkmcnt(0)\n\t" \
    "s_barrier \n\t" \
    "s_add_u32 m0, %[wm], 0x9000\n\t" \
    "s_mov_b32 s44, 0x660000\n\t" \
    "buffer_load_dwordx4 v240, %[wr], s44 offen offset:0 nt lds\n\t" \
    "buffer_load_dwordx4 v241, %[wr], s44 offen offset:1024 nt lds\n\t" \
    "buffer_load_dwordx4 v242, %[wr], s44 offen offset:2048 nt lds\n\t" \
    "ds_read_b64_tr_b16 v[136:137], v244 offset:0\n\t" \
    "ds_read_b64_tr_b16 v[138:139], v244 offset:1536\n\t" \
    "ds_read_b64_tr_b16 v[144:145], v244 offset:32\n\t" \
    "ds_read_b64_tr_b16 v[146:147], v244 offset:1568\n\t" \
    "ds_read_b64_tr_b16 v[152:153], v244 offset:64\n\t" \
    "ds_read_b64_tr_b16 v[154:155], v244 offset:1600\n\t" \
    "v_add_u32 v244, 0xf000, v243\n\t" \
    "s_waitcnt vmcnt(12) lgkmcnt(0)\n\t" \
    "s_barrier \n\t" \
    "s_add_u32 m0, %[wm], 0xc000\n\t" \
    "s_mov_b32 s44, 0x6c0000\n\t" \
    "buffer_load_dwordx4 v240, %[wr], s44 offen offset:0 nt lds\n\t" \
    "buffer_load_dwordx4 v241, %[wr], s44 offen offset:1024 nt lds\n\t" \
    "buffer_load_dwordx4 v242, %[wr], s44 offen offset:2048 nt lds\n\t" \
    "ds_read_b64_tr_b16 v[140:141], v244 offset:0\n\t" \
    "ds_read_b64_tr_b16 v[142:143], v244 offset:1536\n\t" \
    "ds_read_b64_tr_b16 v[148:149], v244 offset:32\n\t" \
    "ds_read_b64_tr_b16 v[150:151], v244 offset:1568\n\t" \
    "ds_read_b64_tr_b16 v[156:157], v244 offset:64\n\t" \
    "ds_read_b64_tr_b16 v[158:159], v244 offset:1600\n\t" \
    "v_add_u32 v244, 0x0, v243\n\t" \
    "s_waitcnt vmcnt(12) lgkmcnt(0)\n\t" \
    "s_barrier \n\t" \
    "s_add_u32 m0, %[wm], 0xf000\n\t" \
    "s_mov_b32 s44, 0x720000\n\t" \
    "buffer_load_dwordx4 v240, %[wr], s44 offen offset:0 nt lds\n\t" \
    "buffer_load_dwordx4 v241, %[wr], s44 offen offset:1024 nt lds\n\t" \
    "buffer_load_dwordx4 v242, %[wr], s44 offen offset:2048 nt lds\n\t" \
    "ds_read_b64_tr_b16 v[16:17], v244 offset:0\n\t" \
    "ds_read_b64_tr_b16 v[18:19], v244 offset:1536\n\t" \
    "ds_read_b64_tr_b16 v[24:25], v244 offset:32\n\t" \
    "ds_read_b64_tr_b16 v[26:27], v244 offset:1568\n\t" \
    "ds_read_b64_tr_b16 v[32:33], v244 offset:64\n\t" \
    "ds_read_b64_tr_b16 v[34:35], v244 offset:1600\n\t" \
    "v_add_u32 v244, 0x3000, v243\n\t" \
    "s_waitcnt vmcnt(12) lgkmcnt(0)\n\t" \
    "s_barrier \n\t" \
    "ds_read_b64_tr_b16 v[20:21], v244 offset:0\n\t" \
    "ds_read_b64_tr_b16 v[22:23], v244 offset:1536\n\t" \
    "ds_read_b64_tr_b16 v[28:29], v244 offset:32\n\t" \
    "ds_read_b64_tr_b16 v[30:31], v244 offset:1568\n\t" \
    "ds_read_b64_tr_b16 v[36:37], v244 offset:64\n\t" \
    "ds_read_b64_tr_b16 v[38:39], v244 offset:1600\n\t" \
    "v_add_u32 v244, 0x6000, v243\n\t" \
    "s_waitcnt vmcnt(9) lgkmcnt(0)\n\t" \
    "s_barrier \n\t" \
    "ds_read_b64_tr_b16 v[232:233], v244 offset:0\n\t" \
    "ds_read_b64_tr_b16 v[234:235], v244 offset:1536\n\t" \
    "ds_read_b64_tr_b16 v[160:161], v244 offset:32\n\t" \
    "ds_read_b64_tr_b16 v[162:163], v244 offset:1568\n\t" \
    "ds_read_b64_tr_b16 v[168:169], v244 offset:64\n\t" \
    "ds_read_b64_tr_b16 v[170:171], v244 offset:1600\n\t" \
    "v_add_u32 v244, 0x9000, v243\n\t" \
    "s_waitcnt vmcnt(6) lgkmcnt(0)\n\t" \
    "s_barrier \n\t" \
    "ds_read_b64_tr_b16 v[236:237], v244 offset:0\n\t" \
    "ds_read_b64_tr_b16 v[238:239], v244 offset:1536\n\t" \
    "ds_read_b64_tr_b16 v[164:165], v244 offset:32\n\t" \
    "ds_read_b64_tr_b16 v[166:167], v244 offset:1568\n\t" \
    "ds_read_b64_tr_b16 v[172:173], v244 offset:64\n\t" \
    "ds_read_b64_tr_b16 v[174:175], v244 offset:1600\n\t" \
    "v_add_u32 v244, 0xc000, v243\n\t" \
    "s_waitcnt vmcnt(3) lgkmcnt(0)\n\t" \
    "s_barrier \n\t" \
    "ds_read_b64_tr_b16 v[176:177], v244 offset:0\n\t" \
    "ds_read_b64_tr_b16 v[178:179], v244 offset:1536\n\t" \
    "ds_read_b64_tr_b16 v[184:185], v244 offset:32\n\t" \
    "ds_read_b64_tr_b16 v[186:187], v244 offset:1568\n\t" \
    "ds_read_b64_tr_b16 v[192:193], v244 offset:64\n\t" \
    "ds_read_b64_tr_b16 v[194:195], v244 offset:1600\n\t" \
    "s_mov_b32 s43, 0\n\t" \
    "v_cmp_eq_u32 vcc, 0, v252\n\t" \
    "s_cbranch_vccnz .Lv23_%=\n\t" \
    "s_mov_b32 m0, 0x2000\n\t" \
    "s_add_u32 s49, s42, 0x0\n\t" \
    "buffer_load_dwordx4 v253, s[60:63], s49 offen offset:0 sc1 lds\n\t" \
    "buffer_load_dwordx4 v253, s[60:63], s49 offen offset:1024 sc1 lds\n\t" \
    "buffer_load_dwordx4 v253, s[60:63], s49 offen offset:2048 sc1 lds\n\t" \
    "buffer_load_dwordx4 v253, s[60:63], s49 offen offset:3072 sc1 lds\n\t" \
    "s_mov_b32 m0, 0x4800\n\t" \
    "s_add_u32 s49, s42, 0x8800\n\t" \
    "buffer_load_dwordx4 v253, s[60:63], s49 offen offset:0 sc1 lds\n\t" \
    "s_mov_b32 m0, 0x7000\n\t" \
    "s_add_u32 s49, s42, 0x1000\n\t" \
    "buffer_load_dwordx4 v253, s[60:63], s49 offen offset:0 sc1 lds\n\t" \
    "buffer_load_dwordx4 v253, s[60:63], s49 offen offset:1024 sc1 lds\n\t" \
    "buffer_load_dwordx4 v253, s[60:63], s49 offen offset:2048 sc1 lds\n\t" \
    "buffer_load_dwordx4 v253, s[60:63], s49 offen offset:3072 sc1 lds\n\t" \
    "s_mov_b32 m0, 0x9800\n\t" \
    "s_add_u32 s49, s42, 0x9800\n\t" \
    "buffer_load_dwordx4 v253, s[60:63], s49 offen offset:0 sc1 lds\n\t" \
    "s_mov_b32 s43, 1\n\t" \
    ".Lv23_%=:\n\t" \
    "v_add_u32 v244, 0xf000, v243\n\t" \
    "s_waitcnt vmcnt(0) lgkmcnt(0)\n\t" \
    "s_barrier \n\t" \
    "ds_read_b64_tr_b16 v[180:181], v244 offset:0\n\t" \
    "ds_read_b64_tr_b16 v[182:183], v244 offset:1536\n\t" \
    "ds_read_b64_tr_b16 v[188:189], v244 offset:32\n\t" \
    "ds_read_b64_tr_b16 v[190:191], v244 offset:1568\n\t" \
    "ds_read_b64_tr_b16 v[196:197], v244 offset:64\n\t" \
    "ds_read_b64_tr_b16 v[198:199], v244 offset:1600\n\t" \
    "s_waitcnt lgkmcnt(0)\n\t" \
    "s_barrier \n\t" \
    LST0 \
    LJIT \
    "ds_write_b128 v254, v[200:203] offset:51200\n\t" \
    "ds_write_b128 v254, v[204:207] offset:52224\n\t" \
    "ds_write_b128 v254, v[208:211] offset:53248\n\t" \
    "ds_write_b128 v254, v[212:215] offset:54272\n\t" \
    "ds_write_b128 v254, v[216:219] offset:55296\n\t" \
    "ds_write_b128 v254, v[220:223] offset:56320\n\t" \
    "ds_write_b128 v254, v[224:227] offset:57344\n\t" \
    "ds_write_b128 v254, v[228:231] offset:58368\n\t" \
    "ds_write_b128 v254, v[232:235] offset:59392\n\t" \
    "ds_write_b128 v254, v[236:239] offset:60416\n\t" \
    "s_cmp_lg_u32 s43, 0\n\t" \
    "s_cbranch_scc1 .Lv24_%=\n\t" \
    "s_mov_b32 s44, 0\n\t" \
    ".Lv25_%=:\n\t" \
    "v_cmp_ge_u32 vcc, s44, v252\n\t" \
    "s_cbranch_vccz .Lv26_%=\n\t" \
    "s_cmp_eq_u32 s41, 0\n\t" \
    "s_cbranch_scc1 .Lv26_%=\n\t" \
    "s_sub_u32 s41, s41, 1\n\t" \
    "s_sleep 8\n\t" \
    "v_and_b32 v252, 0xf0, v253\n\t" \
    "v_lshlrev_b32 v252, 3, v252\n\t" \
    "buffer_load_dword v252, v252, s[60:63], %[fl] offen sc1\n\t" \
    "s_waitcnt vmcnt(0)\n\t" \
    "s_branch .Lv25_%=\n\t" \
    ".Lv26_%=:\n\t" \
    "s_mov_b32 m0, 0x2000\n\t" \
    "s_add_u32 s49, s42, 0x0\n\t" \
    "buffer_load_dwordx4 v253, s[60:63], s49 offen offset:0 sc1 lds\n\t" \
    "buffer_load_dwordx4 v253, s[60:63], s49 offen offset:1024 sc1 lds\n\t" \
    "buffer_load_dwordx4 v253, s[60:63], s49 offen offset:2048 sc1 lds\n\t" \
    "buffer_load_dwordx4 v253, s[60:63], s49 offen offset:3072 sc1 lds\n\t" \
    "s_mov_b32 m0, 0x4800\n\t" \
    "s_add_u32 s49, s42, 0x8800\n\t" \
    "buffer_load_dwordx4 v253, s[60:63], s49 offen offset:0 sc1 lds\n\t" \
    "s_mov_b32 m0, 0x7000\n\t" \
    "s_add_u32 s49, s42, 0x1000\n\t" \
    "buffer_load_dwordx4 v253, s[60:63], s49 offen offset:0 sc1 lds\n\t" \
    "buffer_load_dwordx4 v253, s[60:63], s49 offen offset:1024 sc1 lds\n\t" \
    "buffer_load_dwordx4 v253, s[60:63], s49 offen offset:2048 sc1 lds\n\t" \
    "buffer_load_dwordx4 v253, s[60:63], s49 offen offset:3072 sc1 lds\n\t" \
    "s_mov_b32 m0, 0x9800\n\t" \
    "s_add_u32 s49, s42, 0x9800\n\t" \
    "buffer_load_dwordx4 v253, s[60:63], s49 offen offset:0 sc1 lds\n\t" \
    ".Lv24_%=:\n\t" \
    "s_waitcnt vmcnt(0) lgkmcnt(0)\n\t" \
    "s_barrier \n\t" \
    "ds_read_b128 v[224:227], v253 offset:8192\n\t" \
    "ds_read_b128 v[244:247], v254 offset:51200\n\t" \
    "ds_read_b128 v[228:231], v253 offset:9216\n\t" \
    "ds_read_b128 v[232:235], v253 offset:28672\n\t" \
    "ds_read_b128 v[236:239], v253 offset:29696\n\t" \
    "ds_read_b128 v[248:251], v254 offset:52224\n\t" \
    ".Lv27_%=:\n\t" \
    LST7 \
    LJIT \
    "s_waitcnt lgkmcnt(5)\n\t" \
    "v_mfma_f32_16x16x32_bf16 v[204:207], v[0:3], v[224:227], 0\n\t" \
    "s_waitcnt lgkmcnt(3)\n\t" \
    "v_mfma_f32_16x16x32_bf16 v[204:207], v[4:7], v[228:231], v[204:207]\n\t" \
    "ds_read_b128 v[240:243], v253 offset:14336\n\t" \
    "v_mfma_f32_16x16x32_bf16 v[208:211], v[8:11], v[224:227], 0\n\t" \
    "v_mfma_f32_16x16x32_bf16 v[208:211], v[12:15], v[228:231], v[208:211]\n\t" \
    "v_mfma_f32_16x16x32_bf16 v[200:203], v[244:247], v[224:227], 0\n\t" \
    "s_waitcnt lgkmcnt(1)\n\t" \
    "v_mfma_f32_16x16x32_bf16 v[200:203], v[248:251], v[228:231], v[200:203]\n\t" \
    "v_mfma_f32_16x16x32_bf16 v[216:219], v[0:3], v[232:235], 0\n\t" \
    "ds_read_b128 v[224:227], v253 offset:15360\n\t" \
    "v_mfma_f32_16x16x32_bf16 v[216:219], v[4:7], v[236:239], v[216:219]\n\t" \
    "ds_read_b128 v[228:231], v253 offset:0\n\t" \
    "v_mfma_f32_16x16x32_bf16 v[220:223], v[8:11], v[232:235], 0\n\t" \
    "v_mfma_f32_16x16x32_bf16 v[220:223], v[12:15], v[236:239], v[220:223]\n\t" \
    "v_mfma_f32_16x16x32_bf16 v[212:215], v[244:247], v[232:235], 0\n\t" \
    "ds_read_b128 v[244:247], v254 offset:53248\n\t" \
    "v_mfma_f32_16x16x32_bf16 v[212:215], v[248:251], v[236:239], v[212:215]\n\t" \
    "ds_read_b128 v[248:251], v254 offset:54272\n\t" \
    "s_waitcnt lgkmcnt(4)\n\t" \
    "v_mfma_f32_16x16x32_bf16 v[204:207], v[24:27], v[240:243], v[204:207]\n\t" \
    "ds_read_b128 v[232:235], v253 offset:1024\n\t" \
    "s_waitcnt lgkmcnt(4)\n\t" \
    "v_mfma_f32_16x16x32_bf16 v[204:207], v[28:31], v[224:227], v[204:207]\n\t" \
    "ds_read_b128 v[236:239], v253 offset:20480\n\t" \
    "v_mfma_f32_16x16x32_bf16 v[208:211], v[32:35], v[240:243], v[208:211]\n\t" \
    "v_mfma_f32_16x16x32_bf16 v[208:211], v[36:39], v[224:227], v[208:211]\n\t" \
    "v_mfma_f32_16x16x32_bf16 v[200:203], v[16:19], v[240:243], v[200:203]\n\t" \
    "v_mfma_f32_16x16x32_bf16 v[200:203], v[20:23], v[224:227], v[200:203]\n\t" \
    "s_waitcnt lgkmcnt(4)\n\t" \
    "v_mfma_f32_16x16x32_bf16 v[204:207], v[40:43], v[228:231], v[204:207]\n\t" \
    "ds_read_b128 v[240:243], v253 offset:21504\n\t" \
    "s_waitcnt lgkmcnt(2)\n\t" \
    "v_mfma_f32_16x16x32_bf16 v[204:207], v[44:47], v[232:235], v[204:207]\n\t" \
    "ds_read_b128 v[224:227], v253 offset:2048\n\t" \
    "v_mfma_f32_16x16x32_bf16 v[208:211], v[48:51], v[228:231], v[208:211]\n\t" \
    "v_mfma_f32_16x16x32_bf16 v[208:211], v[52:55], v[232:235], v[208:211]\n\t" \
    "v_mfma_f32_16x16x32_bf16 v[200:203], v[244:247], v[228:231], v[200:203]\n\t" \
    "v_mfma_f32_16x16x32_bf16 v[200:203], v[248:251], v[232:235], v[200:203]\n\t" \
    "s_waitcnt lgkmcnt(2)\n\t" \
    "v_mfma_f32_16x16x32_bf16 v[216:219], v[40:43], v[236:239], v[216:219]\n\t" \
    "ds_read_b128 v[228:231], v253 offset:3072\n\t" \
    "s_add_u32 s44, s40, 1\n\t" \
    "s_waitcnt lgkmcnt(2)\n\t" \
    "v_mfma_f32_16x16x32_bf16 v[216:219], v[44:47], v[240:243], v[216:219]\n\t" \
    "ds_read_b128 v[232:235], v253 offset:4096\n\t" \
    "s_and_b32 s45, s44, 15\n\t" \
    "v_mfma_f32_16x16x32_bf16 v[220:223], v[48:51], v[236:239], v[220:223]\n\t" \
    "s_cmp_eq_u32 s45, 0\n\t" \
    "v_mfma_f32_16x16x32_bf16 v[220:223], v[52:55], v[240:243], v[220:223]\n\t" \
    "s_cselect_b32 s45, %[cj], %[cs]\n\t" \
    "v_mfma_f32_16x16x32_bf16 v[212:215], v[244:247], v[236:239], v[212:215]\n\t" \
    "ds_read_b128 v[244:247], v254 offset:55296\n\t" \
    "s_cmp_eq_u32 s44, 64\n\t" \
    "v_mfma_f32_16x16x32_bf16 v[212:215], v[248:251], v[240:243], v[212:215]\n\t" \
    "ds_read_b128 v[248:251], v254 offset:56320\n\t" \
    "s_cselect_b32 s45, 0, s45\n\t" \
    "s_waitcnt lgkmcnt(4)\n\t" \
    "v_mfma_f32_16x16x32_bf16 v[204:207], v[64:67], v[224:227], v[204:207]\n\t" \
    "ds_read_b128 v[236:239], v253 offset:5120\n\t" \
    "s_add_u32 s42, s42, s45\n\t" \
    "s_waitcnt lgkmcnt(4)\n\t" \
    "v_mfma_f32_16x16x32_bf16 v[204:207], v[68:71], v[228:231], v[204:207]\n\t" \
    "ds_read_b128 v[240:243], v253 offset:24576\n\t" \
    "v_mfma_f32_16x16x32_bf16 v[208:211], v[72:75], v[224:227], v[208:211]\n\t" \
    "v_mfma_f32_16x16x32_bf16 v[208:211], v[76:79], v[228:231], v[208:211]\n\t" \
    "v_mfma_f32_16x16x32_bf16 v[200:203], v[56:59], v[224:227], v[200:203]\n\t" \
    "v_mfma_f32_16x16x32_bf16 v[200:203], v[60:63], v[228:231], v[200:203]\n\t" \
    "s_waitcnt lgkmcnt(4)\n\t" \
    "v_mfma_f32_16x16x32_bf16 v[204:207], v[80:83], v[232:235], v[204:207]\n\t" \
    "ds_read_b128 v[224:227], v253 offset:25600\n\t" \
    "s_waitcnt lgkmcnt(2)\n\t" \
    "v_mfma_f32_16x16x32_bf16 v[204:207], v[84:87], v[236:239], v[204:207]\n\t" \
    "ds_read_b128 v[228:231], v253 offset:6144\n\t" \
    "v_mfma_f32_16x16x32_bf16 v[208:211], v[88:91], v[232:235], v[208:211]\n\t" \
    "v_mfma_f32_16x16x32_bf16 v[208:211], v[92:95], v[236:239], v[208:211]\n\t" \
    "v_mfma_f32_16x16x32_bf16 v[200:203], v[244:247], v[232:235], v[200:203]\n\t" \
    "v_mfma_f32_16x16x32_bf16 v[200:203], v[248:251], v[236:239], v[200:203]\n\t" \
    "s_waitcnt lgkmcnt(2)\n\t" \
    "v_mfma_f32_16x16x32_bf16 v[216:219], v[80:83], v[240:243], v[216:219]\n\t" \
    "ds_read_b128 v[232:235], v253 offset:7168\n\t" \
    "s_and_b32 s44, s40, 3\n\t" \
    "s_waitcnt lgkmcnt(2)\n\t" \
    "v_mfma_f32_16x16x32_bf16 v[216:219], v[84:87], v[224:227], v[216:219]\n\t" \
    "ds_read_b128 v[236:239], v253 offset:10240\n\t" \
    "s_lshl_b32 s44, s44, 17\n\t" \
    "v_mfma_f32_16x16x32_bf16 v[220:223], v[88:91], v[240:243], v[220:223]\n\t" \
    "s_add_u32 s44, s44, %[mb]\n\t" \
    "v_mfma_f32_16x16x32_bf16 v[220:223], v[92:95], v[224:227], v[220:223]\n\t" \
    "s_add_u32 s46, s44, 0x1000\n\t" \
    "v_mfma_f32_16x16x32_bf16 v[212:215], v[244:247], v[240:243], v[212:215]\n\t" \
    "ds_read_b128 v[244:247], v254 offset:57344\n\t" \
    "s_add_u32 s47, s44, 0x1c00\n\t" \
    "v_mfma_f32_16x16x32_bf16 v[212:215], v[248:251], v[224:227], v[212:215]\n\t" \
    "ds_read_b128 v[248:251], v254 offset:58368\n\t" \
    "s_waitcnt lgkmcnt(4)\n\t" \
    "v_mfma_f32_16x16x32_bf16 v[204:207], v[104:107], v[228:231], v[204:207]\n\t" \
    "ds_read_b128 v[240:243], v253 offset:11264\n\t" \
    "s_waitcnt lgkmcnt(4)\n\t" \
    "v_mfma_f32_16x16x32_bf16 v[204:207], v[108:111], v[232:235], v[204:207]\n\t" \
    "ds_read_b128 v[224:227], v253 offset:30720\n\t" \
    "v_mfma_f32_16x16x32_bf16 v[208:211], v[112:115], v[228:231], v[208:211]\n\t" \
    "v_mfma_f32_16x16x32_bf16 v[208:211], v[116:119], v[232:235], v[208:211]\n\t" \
    "v_mfma_f32_16x16x32_bf16 v[200:203], v[96:99], v[228:231], v[200:203]\n\t" \
    "v_mfma_f32_16x16x32_bf16 v[200:203], v[100:103], v[232:235], v[200:203]\n\t" \
    "s_waitcnt lgkmcnt(4)\n\t" \
    "v_mfma_f32_16x16x32_bf16 v[204:207], v[120:123], v[236:239], v[204:207]\n\t" \
    "ds_read_b128 v[228:231], v253 offset:31744\n\t" \
    "s_waitcnt lgkmcnt(2)\n\t" \
    "v_mfma_f32_16x16x32_bf16 v[204:207], v[124:127], v[240:243], v[204:207]\n\t" \
    "ds_read_b128 v[232:235], v253 offset:12288\n\t" \
    "v_mfma_f32_16x16x32_bf16 v[208:211], v[128:131], v[236:239], v[208:211]\n\t" \
    "v_mfma_f32_16x16x32_bf16 v[208:211], v[132:135], v[240:243], v[208:211]\n\t" \
    "v_mfma_f32_16x16x32_bf16 v[200:203], v[244:247], v[236:239], v[200:203]\n\t" \
    "v_mfma_f32_16x16x32_bf16 v[200:203], v[248:251], v[240:243], v[200:203]\n\t" \
    "s_waitcnt lgkmcnt(2)\n\t" \
    "v_mfma_f32_16x16x32_bf16 v[216:219], v[120:123], v[224:227], v[216:219]\n\t" \
    "ds_read_b128 v[236:239], v253 offset:13312\n\t" \
    "s_waitcnt lgkmcnt(2)\n\t" \
    "v_mfma_f32_16x16x32_bf16 v[216:219], v[124:127], v[228:231], v[216:219]\n\t" \
    "ds_read_b128 v[240:243], v253 offset:16384\n\t" \
    "v_mfma_f32_16x16x32_bf16 v[220:223], v[128:131], v[224:227], v[220:223]\n\t" \
    "v_mfma_f32_16x16x32_bf16 v[220:223], v[132:135], v[228:231], v[220:223]\n\t" \
    "v_mfma_f32_16x16x32_bf16 v[212:215], v[244:247], v[224:227], v[212:215]\n\t" \
    "ds_read_b128 v[244:247], v254 offset:59392\n\t" \
    "v_mfma_f32_16x16x32_bf16 v[212:215], v[248:251], v[228:231], v[212:215]\n\t" \
    "ds_read_b128 v[248:251], v254 offset:60416\n\t" \
    "v_and_b32 v252, 0xf0, v253\n\t" \
    "v_lshlrev_b32 v252, 3, v252\n\t" \
    "buffer_load_dword v252, v252, s[60:63], %[fl] offen sc1\n\t" \
    "s_waitcnt lgkmcnt(4)\n\t" \
    "v_mfma_f32_16x16x32_bf16 v[204:207], v[144:147], v[232:235], v[204:207]\n\t" \
    "ds_read_b128 v[224:227], v253 offset:17408\n\t" \
    "s_waitcnt lgkmcnt(4)\n\t" \
    "v_mfma_f32_16x16x32_bf16 v[204:207], v[148:151], v[236:239], v[204:207]\n\t" \
    "ds_read_b128 v[228:231], v253 offset:36864\n\t" \
    "v_mfma_f32_16x16x32_bf16 v[208:211], v[152:155], v[232:235], v[208:211]\n\t" \
    "v_mfma_f32_16x16x32_bf16 v[208:211], v[156:159], v[236:239], v[208:211]\n\t" \
    "v_mfma_f32_16x16x32_bf16 v[200:203], v[136:139], v[232:235], v[200:203]\n\t" \
    "v_mfma_f32_16x16x32_bf16 v[200:203], v[140:143], v[236:239], v[200:203]\n\t" \
    "s_waitcnt lgkmcnt(4)\n\t" \
    "v_mfma_f32_16x16x32_bf16 v[204:207], v[160:163], v[240:243], v[204:207]\n\t" \
    "ds_read_b128 v[232:235], v253 offset:37888\n\t" \
    "s_waitcnt lgkmcnt(2)\n\t" \
    "v_mfma_f32_16x16x32_bf16 v[204:207], v[164:167], v[224:227], v[204:207]\n\t" \
    "ds_read_b128 v[236:239], v253 offset:18432\n\t" \
    "v_mfma_f32_16x16x32_bf16 v[208:211], v[168:171], v[240:243], v[208:211]\n\t" \
    "v_mfma_f32_16x16x32_bf16 v[208:211], v[172:175], v[224:227], v[208:211]\n\t" \
    "v_mfma_f32_16x16x32_bf16 v[200:203], v[244:247], v[240:243], v[200:203]\n\t" \
    "v_mfma_f32_16x16x32_bf16 v[200:203], v[248:251], v[224:227], v[200:203]\n\t" \
    "s_waitcnt lgkmcnt(2)\n\t" \
    "v_mfma_f32_16x16x32_bf16 v[216:219], v[160:163], v[228:231], v[216:219]\n\t" \
    "ds_read_b128 v[240:243], v253 offset:19456\n\t" \
    "s_waitcnt lgkmcnt(2)\n\t" \
    "v_mfma_f32_16x16x32_bf16 v[216:219], v[164:167], v[232:235], v[216:219]\n\t" \
    "ds_read_b128 v[224:227], v253 offset:34816\n\t" \
    "v_mfma_f32_16x16x32_bf16 v[220:223], v[168:171], v[228:231], v[220:223]\n\t" \
    "v_mfma_f32_16x16x32_bf16 v[220:223], v[172:175], v[232:235], v[220:223]\n\t" \
    "v_mfma_f32_16x16x32_bf16 v[212:215], v[244:247], v[228:231], v[212:215]\n\t" \
    "v_mfma_f32_16x16x32_bf16 v[212:215], v[248:251], v[232:235], v[212:215]\n\t" \
    "s_waitcnt lgkmcnt(2)\n\t" \
    "v_mfma_f32_16x16x32_bf16 v[204:207], v[184:187], v[236:239], v[204:207]\n\t" \
    "ds_read_b128 v[228:231], v253 offset:35840\n\t" \
    "s_waitcnt lgkmcnt(2)\n\t" \
    "v_mfma_f32_16x16x32_bf16 v[204:207], v[188:191], v[240:243], v[204:207]\n\t" \
    "ds_read_b128 v[232:235], v253 offset:22528\n\t" \
    "v_mfma_f32_16x16x32_bf16 v[208:211], v[192:195], v[236:239], v[208:211]\n\t" \
    "v_mfma_f32_16x16x32_bf16 v[208:211], v[196:199], v[240:243], v[208:211]\n\t" \
    "v_mfma_f32_16x16x32_bf16 v[200:203], v[176:179], v[236:239], v[200:203]\n\t" \
    "v_mfma_f32_16x16x32_bf16 v[200:203], v[180:183], v[240:243], v[200:203]\n\t" \
    "s_waitcnt lgkmcnt(2)\n\t" \
    "v_mfma_f32_16x16x32_bf16 v[216:219], v[24:27], v[224:227], v[216:219]\n\t" \
    "ds_read_b128 v[236:239], v253 offset:23552\n\t" \
    "s_waitcnt lgkmcnt(2)\n\t" \
    "v_mfma_f32_16x16x32_bf16 v[216:219], v[28:31], v[228:231], v[216:219]\n\t" \
    "ds_read_b128 v[240:243], v253 offset:26624\n\t" \
    "v_mfma_f32_16x16x32_bf16 v[220:223], v[32:35], v[224:227], v[220:223]\n\t" \
    "v_cvt_pk_bf16_f32 v204, v204, v205\n\t" \
    "v_mfma_f32_16x16x32_bf16 v[220:223], v[36:39], v[228:231], v[220:223]\n\t" \
    "v_cvt_pk_bf16_f32 v205, v206, v207\n\t" \
    "v_mfma_f32_16x16x32_bf16 v[212:215], v[16:19], v[224:227], v[212:215]\n\t" \
    "ds_write_b64 v254, v[204:205] offset:7176\n\t" \
    "v_mfma_f32_16x16x32_bf16 v[212:215], v[20:23], v[228:231], v[212:215]\n\t" \
    "v_cvt_pk_bf16_f32 v208, v208, v209\n\t" \
    "s_waitcnt lgkmcnt(3)\n\t" \
    "v_mfma_f32_16x16x32_bf16 v[216:219], v[64:67], v[232:235], v[216:219]\n\t" \
    "ds_read_b128 v[224:227], v253 offset:27648\n\t" \
    "v_cvt_pk_bf16_f32 v209, v210, v211\n\t" \
    "s_waitcnt lgkmcnt(3)\n\t" \
    "v_mfma_f32_16x16x32_bf16 v[216:219], v[68:71], v[236:239], v[216:219]\n\t" \
    "ds_read_b128 v[228:231], v253 offset:32768\n\t" \
    "ds_write_b64 v254, v[208:209] offset:8200\n\t" \
    "v_mfma_f32_16x16x32_bf16 v[220:223], v[72:75], v[232:235], v[220:223]\n\t" \
    "v_cvt_pk_bf16_f32 v200, v200, v201\n\t" \
    "v_mfma_f32_16x16x32_bf16 v[220:223], v[76:79], v[236:239], v[220:223]\n\t" \
    "v_cvt_pk_bf16_f32 v201, v202, v203\n\t" \
    "v_mfma_f32_16x16x32_bf16 v[212:215], v[56:59], v[232:235], v[212:215]\n\t" \
    "ds_write_b64 v254, v[200:201] offset:6152\n\t" \
    "v_mfma_f32_16x16x32_bf16 v[212:215], v[60:63], v[236:239], v[212:215]\n\t" \
    "s_waitcnt lgkmcnt(5)\n\t" \
    "v_mfma_f32_16x16x32_bf16 v[216:219], v[104:107], v[240:243], v[216:219]\n\t" \
    "ds_read_b128 v[232:235], v253 offset:33792\n\t" \
    "s_waitcnt lgkmcnt(4)\n\t" \
    "v_mfma_f32_16x16x32_bf16 v[216:219], v[108:111], v[224:227], v[216:219]\n\t" \
    "ds_read_b128 v[236:239], v253 offset:38912\n\t" \
    "v_mfma_f32_16x16x32_bf16 v[220:223], v[112:115], v[240:243], v[220:223]\n\t" \
    "v_mfma_f32_16x16x32_bf16 v[220:223], v[116:119], v[224:227], v[220:223]\n\t" \
    "v_mfma_f32_16x16x32_bf16 v[212:215], v[96:99], v[240:243], v[212:215]\n\t" \
    "v_mfma_f32_16x16x32_bf16 v[212:215], v[100:103], v[224:227], v[212:215]\n\t" \
    "ds_read_b128 v[240:243], v253 offset:39936\n\t" \
    "s_waitcnt lgkmcnt(3)\n\t" \
    "s_barrier \n\t" \
    "v_mfma_f32_16x16x32_bf16 v[216:219], v[144:147], v[228:231], v[216:219]\n\t" \
    "s_waitcnt lgkmcnt(2)\n\t" \
    "v_mfma_f32_16x16x32_bf16 v[216:219], v[148:151], v[232:235], v[216:219]\n\t" \
    "s_waitcnt lgkmcnt(1)\n\t" \
    "v_mfma_f32_16x16x32_bf16 v[216:219], v[184:187], v[236:239], v[216:219]\n\t" \
    "s_waitcnt lgkmcnt(0)\n\t" \
    "v_mfma_f32_16x16x32_bf16 v[216:219], v[188:191], v[240:243], v[216:219]\n\t" \
    "v_mfma_f32_16x16x32_bf16 v[220:223], v[152:155], v[228:231], v[220:223]\n\t" \
    "v_mfma_f32_16x16x32_bf16 v[220:223], v[156:159], v[232:235], v[220:223]\n\t" \
    "v_mfma_f32_16x16x32_bf16 v[220:223], v[192:195], v[236:239], v[220:223]\n\t" \
    "v_cvt_pk_bf16_f32 v216, v216, v217\n\t" \
    "v_cvt_pk_bf16_f32 v217, v218, v219\n\t" \
    "v_mfma_f32_16x16x32_bf16 v[220:223], v[196:199], v[240:243], v[220:223]\n\t" \
    "ds_write_b64 v254, v[216:217] offset:13320\n\t" \
    "v_mfma_f32_16x16x32_bf16 v[212:215], v[136:139], v[228:231], v[212:215]\n\t" \
    "v_mfma_f32_16x16x32_bf16 v[212:215], v[140:143], v[232:235], v[212:215]\n\t" \
    "v_mfma_f32_16x16x32_bf16 v[212:215], v[176:179], v[236:239], v[212:215]\n\t" \
    "v_cvt_pk_bf16_f32 v220, v220, v221\n\t" \
    "v_cvt_pk_bf16_f32 v221, v222, v223\n\t" \
    "v_mfma_f32_16x16x32_bf16 v[212:215], v[180:183], v[240:243], v[212:215]\n\t" \
    "ds_write_b64 v254, v[220:221] offset:14344\n\t" \
    "s_nop 6\n\t" \
    "v_cvt_pk_bf16_f32 v212, v212, v213\n\t" \
    "v_cvt_pk_bf16_f32 v213, v214, v215\n\t" \
    "ds_write_b64 v254, v[212:213] offset:12296\n\t" \
    LST6 \
    LJIT \
    "s_waitcnt vmcnt(0) lgkmcnt(0)\n\t" \
    "s_barrier \n\t" \
    "s_setprio 2\n\t" \
    "s_add_u32 s44, s40, 1\n\t" \
    "s_min_u32 s44, s44, 63\n\t" \
    ".Lv28_%=:\n\t" \
    "v_cmp_ge_u32 vcc, s44, v252\n\t" \
    "s_cbranch_vccz .Lv29_%=\n\t" \
    "s_cmp_eq_u32 s41, 0\n\t" \
    "s_cbranch_scc1 .Lv29_%=\n\t" \
    "s_sub_u32 s41, s41, 1\n\t" \
    "s_sleep 2\n\t" \
    "v_and_b32 v252, 0xf0, v253\n\t" \
    "v_lshlrev_b32 v252, 3, v252\n\t" \
    "buffer_load_dword v252, v252, s[60:63], %[fl] offen sc1\n\t" \
    "s_waitcnt vmcnt(0)\n\t" \
    "s_branch .Lv28_%=\n\t" \
    ".Lv29_%=:\n\t" \
    "s_mov_b32 m0, 0x2000\n\t" \
    "s_add_u32 s49, s42, 0x0\n\t" \
    "buffer_load_dwordx4 v253, s[60:63], s49 offen offset:0 sc1 lds\n\t" \
    "s_sleep 1\n\t" \
    "buffer_load_dwordx4 v253, s[60:63], s49 offen offset:1024 sc1 lds\n\t" \
    "s_sleep 1\n\t" \
    "buffer_load_dwordx4 v253, s[60:63], s49 offen offset:2048 sc1 lds\n\t" \
    "s_sleep 1\n\t" \
    "buffer_load_dwordx4 v253, s[60:63], s49 offen offset:3072 sc1 lds\n\t" \
    "s_sleep 1\n\t" \
    "s_mov_b32 m0, 0x4800\n\t" \
    "s_add_u32 s49, s42, 0x8800\n\t" \
    "buffer_load_dwordx4 v253, s[60:63], s49 offen offset:0 sc1 lds\n\t" \
    "s_sleep 1\n\t" \
    "s_mov_b32 m0, 0x7000\n\t" \
    "s_add_u32 s49, s42, 0x1000\n\t" \
    "buffer_load_dwordx4 v253, s[60:63], s49 offen offset:0 sc1 lds\n\t" \
    "s_sleep 1\n\t" \
    "buffer_load_dwordx4 v253, s[60:63], s49 offen offset:1024 sc1 lds\n\t" \
    "s_sleep 1\n\t" \
    "buffer_load_dwordx4 v253, s[60:63], s49 offen offset:2048 sc1 lds\n\t" \
    "s_sleep 1\n\t" \
    "buffer_load_dwordx4 v253, s[60:63], s49 offen offset:3072 sc1 lds\n\t" \
    "s_sleep 1\n\t" \
    "s_mov_b32 m0, 0x9800\n\t" \
    "s_add_u32 s49, s42, 0x9800\n\t" \
    "buffer_load_dwordx4 v253, s[60:63], s49 offen offset:0 sc1 lds\n\t" \
    "s_sleep 1\n\t" \
    "ds_read_b128 v[200:203], v254 offset:51200\n\t" \
    "ds_read_b128 v[204:207], v254 offset:52224\n\t" \
    "ds_read_b128 v[216:219], v254 offset:53248\n\t" \
    "ds_read_b128 v[220:223], v254 offset:54272\n\t" \
    "v_mov_b64 v[208:209], 0\n\t" \
    "v_mov_b64 v[210:211], 0\n\t" \
    "v_mov_b64 v[244:245], 0\n\t" \
    "v_mov_b64 v[246:247], 0\n\t" \
    "v_mov_b64 v[248:249], 0\n\t" \
    "v_mov_b64 v[250:251], 0\n\t" \
    LST1 \
    LJIT \
    "s_sleep 5\n\t" \
    "s_mov_b32 m0, 0xb000\n\t" \
    "s_nop 0\n\t" \
    "buffer_load_dwordx4 v253, s[60:63], s46 offen sc1 lds\n\t" \
    "s_mov_b32 m0, 0xbc00\n\t" \
    "s_nop 0\n\t" \
    "buffer_load_dwordx4 v253, s[60:63], s47 offen sc1 lds\n\t" \
    "s_waitcnt lgkmcnt(0)\n\t" \
    LFLUSH212 \
    "v_bfe_i32 v212, v253, 5, 1\n\t" \
    "v_and_b32 v212, 0xaaaa, v212\n\t" \
    "v_or_b32 v212, 0x4444, v212\n\t" \
    "v_and_b32 v213, 0xffff, v255\n\t" \
    "v_lshrrev_b32 v214, 16, v255\n\t" \
    ".Lv210_%=:\n\t" \
    "s_waitcnt vmcnt(0)\n\t" \
    "ds_read_b128 v[224:227], v253 offset:45056\n\t" \
    "ds_read_b128 v[228:231], v253 offset:48128\n\t" \
    "s_waitcnt lgkmcnt(1)\n\t" \
    "v_max_u32 v215, v224, v225\n\t" \
    "v_max3_u32 v215, v226, v227, v215\n\t" \
    "s_waitcnt lgkmcnt(0)\n\t" \
    "v_max3_u32 v215, v228, v229, v215\n\t" \
    "v_max3_u32 v215, v230, v231, v215\n\t" \
    "v_cmp_eq_u32 vcc, -1, v215\n\t" \
    "s_cbranch_vccz .Lv211_%=\n\t" \
    "v_max_u32 v215, v224, v225\n\t" \
    "v_max3_u32 v215, v226, v227, v215\n\t" \
    "v_cmp_eq_u32 vcc, -1, v215\n\t" \
    "s_cbranch_vccz .Lv212_%=\n\t" \
    "s_mov_b32 m0, 0xb000\n\t" \
    "s_nop 0\n\t" \
    "buffer_load_dwordx4 v253, s[60:63], s46 offen sc1 lds\n\t" \
    ".Lv212_%=:\n\t" \
    "v_max_u32 v215, v228, v229\n\t" \
    "v_max3_u32 v215, v230, v231, v215\n\t" \
    "v_cmp_eq_u32 vcc, -1, v215\n\t" \
    "s_cbranch_vccz .Lv213_%=\n\t" \
    "s_mov_b32 m0, 0xbc00\n\t" \
    "s_nop 0\n\t" \
    "buffer_load_dwordx4 v253, s[60:63], s47 offen sc1 lds\n\t" \
    ".Lv213_%=:\n\t" \
    "s_cmp_eq_u32 s41, 0\n\t" \
    "s_cbranch_scc1 .Lv211_%=\n\t" \
    "s_sub_u32 s41, s41, 1\n\t" \
    "s_branch .Lv210_%=\n\t" \
    ".Lv211_%=:\n\t" \
    LST2 \
    LJIT \
    "s_waitcnt lgkmcnt(0)\n\t" \
    "s_barrier \n\t" \
    LST3 \
    LJIT \
    "ds_read_b128 v[232:235], v253 offset:40960\n\t" \
    "ds_read_b128 v[236:239], v253 offset:41984\n\t" \
    "ds_read_b128 v[240:243], v253 offset:43008\n\t" \
    "v_smfmac_f32_16x16x64_bf16 v[244:247], v[224:227], v[0:7], v212\n\t" \
    "v_smfmac_f32_16x16x64_bf16 v[248:251], v[224:227], v[8:15], v212\n\t" \
    "v_smfmac_f32_16x16x64_bf16 v[208:211], v[224:227], v[200:207], v212\n\t" \
    "ds_read_b128 v[200:203], v254 offset:55296\n\t" \
    "ds_read_b128 v[204:207], v254 offset:56320\n\t" \
    "ds_read_b128 v[224:227], v253 offset:44032\n\t" \
    "v_smfmac_f32_16x16x64_bf16 v[208:211], v[228:231], v[16:23], v212\n\t" \
    "v_smfmac_f32_16x16x64_bf16 v[244:247], v[228:231], v[24:31], v212\n\t" \
    "v_smfmac_f32_16x16x64_bf16 v[248:251], v[228:231], v[32:39], v212\n\t" \
    "ds_read_b128 v[228:231], v253 offset:46080\n\t" \
    "s_waitcnt lgkmcnt(6)\n\t" \
    "v_smfmac_f32_16x16x64_bf16 v[208:211], v[232:235], v[216:223], v212\n\t" \
    "ds_read_b128 v[216:219], v254 offset:57344\n\t" \
    "ds_read_b128 v[220:223], v254 offset:58368\n\t" \
    "v_smfmac_f32_16x16x64_bf16 v[244:247], v[232:235], v[40:47], v212\n\t" \
    "v_smfmac_f32_16x16x64_bf16 v[248:251], v[232:235], v[48:55], v212\n\t" \
    "ds_read_b128 v[232:235], v253 offset:47104\n\t" \
    "s_waitcnt lgkmcnt(8)\n\t" \
    "v_smfmac_f32_16x16x64_bf16 v[208:211], v[236:239], v[56:63], v212\n\t" \
    "v_smfmac_f32_16x16x64_bf16 v[244:247], v[236:239], v[64:71], v212\n\t" \
    "v_smfmac_f32_16x16x64_bf16 v[248:251], v[236:239], v[72:79], v212\n\t" \
    "ds_read_b128 v[236:239], v253 offset:49152\n\t" \
    "s_waitcnt lgkmcnt(6)\n\t" \
    "v_smfmac_f32_16x16x64_bf16 v[208:211], v[240:243], v[200:207], v212\n\t" \
    "ds_read_b128 v[200:203], v254 offset:59392\n\t" \
    "ds_read_b128 v[204:207], v254 offset:60416\n\t" \
    "v_smfmac_f32_16x16x64_bf16 v[244:247], v[240:243], v[80:87], v212\n\t" \
    "v_smfmac_f32_16x16x64_bf16 v[248:251], v[240:243], v[88:95], v212\n\t" \
    "ds_read_b128 v[240:243], v253 offset:50176\n\t" \
    "s_waitcnt lgkmcnt(8)\n\t" \
    "v_smfmac_f32_16x16x64_bf16 v[208:211], v[224:227], v[96:103], v212\n\t" \
    "v_smfmac_f32_16x16x64_bf16 v[244:247], v[224:227], v[104:111], v212\n\t" \
    "v_smfmac_f32_16x16x64_bf16 v[248:251], v[224:227], v[112:119], v212\n\t" \
    "s_waitcnt lgkmcnt(7)\n\t" \
    "v_smfmac_f32_16x16x64_bf16 v[244:247], v[228:231], v[120:127], v212\n\t" \
    "s_waitcnt lgkmcnt(4)\n\t" \
    "v_smfmac_f32_16x16x64_bf16 v[244:247], v[232:235], v[144:151], v212\n\t" \
    "s_waitcnt lgkmcnt(3)\n\t" \
    "v_smfmac_f32_16x16x64_bf16 v[244:247], v[236:239], v[160:167], v212\n\t" \
    "s_waitcnt lgkmcnt(0)\n\t" \
    "v_smfmac_f32_16x16x64_bf16 v[244:247], v[240:243], v[184:191], v212\n\t" \
    "v_smfmac_f32_16x16x64_bf16 v[248:251], v[228:231], v[128:135], v212\n\t" \
    "v_smfmac_f32_16x16x64_bf16 v[248:251], v[232:235], v[152:159], v212\n\t" \
    "v_smfmac_f32_16x16x64_bf16 v[248:251], v[236:239], v[168:175], v212\n\t" \
    "v_add_f32 v244, v244, v246\n\t" \
    "v_add_f32 v245, v245, v247\n\t" \
    "v_cvt_pk_bf16_f32 v244, v244, v245\n\t" \
    "v_smfmac_f32_16x16x64_bf16 v[248:251], v[240:243], v[192:199], v212\n\t" \
    "ds_write_b16 v213, v244 offset:512\n\t" \
    "ds_write_b16_d16_hi v213, v244 offset:528\n\t" \
    "v_smfmac_f32_16x16x64_bf16 v[208:211], v[228:231], v[216:223], v212\n\t" \
    "v_smfmac_f32_16x16x64_bf16 v[208:211], v[232:235], v[136:143], v212\n\t" \
    "v_smfmac_f32_16x16x64_bf16 v[208:211], v[236:239], v[200:207], v212\n\t" \
    "v_add_f32 v248, v248, v250\n\t" \
    "v_add_f32 v249, v249, v251\n\t" \
    "v_cvt_pk_bf16_f32 v248, v248, v249\n\t" \
    "v_smfmac_f32_16x16x64_bf16 v[208:211], v[240:243], v[176:183], v212\n\t" \
    "ds_write_b16 v213, v248 offset:1024\n\t" \
    "ds_write_b16_d16_hi v213, v248 offset:1040\n\t" \
    "s_nop 5\n\t" \
    "v_add_f32 v208, v208, v210\n\t" \
    "v_add_f32 v209, v209, v211\n\t" \
    "v_cvt_pk_bf16_f32 v208, v208, v209\n\t" \
    "ds_write_b16 v213, v208 offset:0\n\t" \
    "ds_write_b16_d16_hi v213, v208 offset:16\n\t" \
    "s_setprio 0\n\t" \
    "s_waitcnt lgkmcnt(0)\n\t" \
    "s_barrier \n\t" \
    LST4 \
    LJIT \
    "ds_read_b128 v[224:227], v253 offset:8192\n\t" \
    "ds_read_b128 v[244:247], v254 offset:51200\n\t" \
    "ds_read_b128 v[228:231], v253 offset:9216\n\t" \
    "ds_read_b128 v[232:235], v253 offset:28672\n\t" \
    "ds_read_b128 v[236:239], v253 offset:29696\n\t" \
    "ds_read_b128 v[248:251], v254 offset:52224\n\t" \
    "s_add_u32 s40, s40, 1\n\t" \
    "s_cmp_lg_u32 s40, 64\n\t" \
    "s_cbranch_scc1 .Lv27_%=\n\t" \
    "s_branch .Lend%=\n\t" \
    ".Lv1_%=:\n\t" \
    "s_mov_b32 s42, %[xs]\n\t" \
    "s_add_u32 m0, %[wm], 0xc000\n\t" \
    "s_mov_b32 s44, 0x0\n\t" \
    "buffer_load_dwordx4 v240, %[wr], s44 offen offset:0 nt lds\n\t" \
    "buffer_load_dwordx4 v241, %[wr], s44 offen offset:1024 nt lds\n\t" \
    "buffer_load_dwordx4 v242, %[wr], s44 offen offset:2048 nt lds\n\t" \
    "s_add_u32 m0, %[wm], 0xf000\n\t" \
    "s_mov_b32 s44, 0x60000\n\t" \
    "buffer_load_dwordx4 v240, %[wr], s44 offen offset:0 nt lds\n\t" \
    "buffer_load_dwordx4 v241, %[wr], s44 offen offset:1024 nt lds\n\t" \
    "buffer_load_dwordx4 v242, %[wr], s44 offen offset:2048 nt lds\n\t" \
    "s_add_u32 m0, %[wm], 0x0\n\t" \
    "s_mov_b32 s44, 0xc0000\n\t" \
    "buffer_load_dwordx4 v240, %[wr], s44 offen offset:0 nt lds\n\t" \
    "buffer_load_dwordx4 v241, %[wr], s44 offen offset:1024 nt lds\n\t" \
    "buffer_load_dwordx4 v242, %[wr], s44 offen offset:2048 nt lds\n\t" \
    "s_add_u32 m0, %[wm], 0x3000\n\t" \
    "s_mov_b32 s44, 0x120000\n\t" \
    "buffer_load_dwordx4 v240, %[wr], s44 offen offset:0 nt lds\n\t" \
    "buffer_load_dwordx4 v241, %[wr], s44 offen offset:1024 nt lds\n\t" \
    "buffer_load_dwordx4 v242, %[wr], s44 offen offset:2048 nt lds\n\t" \
    "s_add_u32 m0, %[wm], 0x6000\n\t" \
    "s_mov_b32 s44, 0x180000\n\t" \
    "buffer_load_dwordx4 v240, %[wr], s44 offen offset:0 nt lds\n\t" \
    "buffer_load_dwordx4 v241, %[wr], s44 offen offset:1024 nt lds\n\t" \
    "buffer_load_dwordx4 v242, %[wr], s44 offen offset:2048 nt lds\n\t" \
    "v_add_u32 v244, 0xc000, v243\n\t" \
    "s_waitcnt vmcnt(12) lgkmcnt(0)\n\t" \
    "s_barrier \n\t" \
    "s_add_u32 m0, %[wm], 0x9000\n\t" \
    "s_mov_b32 s44, 0x1e0000\n\t" \
    "buffer_load_dwordx4 v240, %[wr], s44 offen offset:0 nt lds\n\t" \
    "buffer_load_dwordx4 v241, %[wr], s44 offen offset:1024 nt lds\n\t" \
    "buffer_load_dwordx4 v242, %[wr], s44 offen offset:2048 nt lds\n\t" \
    "ds_read_b64_tr_b16 v[56:57], v244 offset:0\n\t" \
    "ds_read_b64_tr_b16 v[58:59], v244 offset:1536\n\t" \
    "ds_read_b64_tr_b16 v[64:65], v244 offset:32\n\t" \
    "ds_read_b64_tr_b16 v[66:67], v244 offset:1568\n\t" \
    "ds_read_b64_tr_b16 v[72:73], v244 offset:64\n\t" \
    "ds_read_b64_tr_b16 v[74:75], v244 offset:1600\n\t" \
    "v_add_u32 v244, 0xf000, v243\n\t" \
    "s_waitcnt vmcnt(12) lgkmcnt(0)\n\t" \
    "s_barrier \n\t" \
    "s_add_u32 m0, %[wm], 0xc000\n\t" \
    "s_mov_b32 s44, 0x240000\n\t" \
    "buffer_load_dwordx4 v240, %[wr], s44 offen offset:0 nt lds\n\t" \
    "buffer_load_dwordx4 v241, %[wr], s44 offen offset:1024 nt lds\n\t" \
    "buffer_load_dwordx4 v242, %[wr], s44 offen offset:2048 nt lds\n\t" \
    "ds_read_b64_tr_b16 v[60:61], v244 offset:0\n\t" \
    "ds_read_b64_tr_b16 v[62:63], v244 offset:1536\n\t" \
    "ds_read_b64_tr_b16 v[68:69], v244 offset:32\n\t" \
    "ds_read_b64_tr_b16 v[70:71], v244 offset:1568\n\t" \
    "ds_read_b64_tr_b16 v[76:77], v244 offset:64\n\t" \
    "ds_read_b64_tr_b16 v[78:79], v244 offset:1600\n\t" \
    "v_add_u32 v244, 0x0, v243\n\t" \
    "s_waitcnt vmcnt(12) lgkmcnt(0)\n\t" \
    "s_barrier \n\t" \
    "s_add_u32 m0, %[wm], 0xf000\n\t" \
    "s_mov_b32 s44, 0x2a0000\n\t" \
    "buffer_load_dwordx4 v240, %[wr], s44 offen offset:0 nt lds\n\t" \
    "buffer_load_dwordx4 v241, %[wr], s44 offen offset:1024 nt lds\n\t" \
    "buffer_load_dwordx4 v242, %[wr], s44 offen offset:2048 nt lds\n\t" \
    "ds_read_b64_tr_b16 v[96:97], v244 offset:0\n\t" \
    "ds_read_b64_tr_b16 v[98:99], v244 offset:1536\n\t" \
    "ds_read_b64_tr_b16 v[104:105], v244 offset:32\n\t" \
    "ds_read_b64_tr_b16 v[106:107], v244 offset:1568\n\t" \
    "ds_read_b64_tr_b16 v[112:113], v244 offset:64\n\t" \
    "ds_read_b64_tr_b16 v[114:115], v244 offset:1600\n\t" \
    "v_add_u32 v244, 0x3000, v243\n\t" \
    "s_waitcnt vmcnt(12) lgkmcnt(0)\n\t" \
    "s_barrier \n\t" \
    "s_add_u32 m0, %[wm], 0x0\n\t" \
    "s_mov_b32 s44, 0x300000\n\t" \
    "buffer_load_dwordx4 v240, %[wr], s44 offen offset:0 nt lds\n\t" \
    "buffer_load_dwordx4 v241, %[wr], s44 offen offset:1024 nt lds\n\t" \
    "buffer_load_dwordx4 v242, %[wr], s44 offen offset:2048 nt lds\n\t" \
    "ds_read_b64_tr_b16 v[100:101], v244 offset:0\n\t" \
    "ds_read_b64_tr_b16 v[102:103], v244 offset:1536\n\t" \
    "ds_read_b64_tr_b16 v[108:109], v244 offset:32\n\t" \
    "ds_read_b64_tr_b16 v[110:111], v244 offset:1568\n\t" \
    "ds_read_b64_tr_b16 v[116:117], v244 offset:64\n\t" \
    "ds_read_b64_tr_b16 v[118:119], v244 offset:1600\n\t" \
    "v_add_u32 v244, 0x6000, v243\n\t" \
    "s_waitcnt vmcnt(12) lgkmcnt(0)\n\t" \
    "s_barrier \n\t" \
    "s_add_u32 m0, %[wm], 0x3000\n\t" \
    "s_mov_b32 s44, 0x360000\n\t" \
    "buffer_load_dwordx4 v240, %[wr], s44 offen offset:0 nt lds\n\t" \
    "buffer_load_dwordx4 v241, %[wr], s44 offen offset:1024 nt lds\n\t" \
    "buffer_load_dwordx4 v242, %[wr], s44 offen offset:2048 nt lds\n\t" \
    "ds_read_b64_tr_b16 v[200:201], v244 offset:0\n\t" \
    "ds_read_b64_tr_b16 v[202:203], v244 offset:1536\n\t" \
    "ds_read_b64_tr_b16 v[0:1], v244 offset:32\n\t" \
    "ds_read_b64_tr_b16 v[2:3], v244 offset:1568\n\t" \
    "ds_read_b64_tr_b16 v[8:9], v244 offset:64\n\t" \
    "ds_read_b64_tr_b16 v[10:11], v244 offset:1600\n\t" \
    "v_add_u32 v244, 0x9000, v243\n\t" \
    "s_waitcnt vmcnt(12) lgkmcnt(0)\n\t" \
    "s_barrier \n\t" \
    "s_add_u32 m0, %[wm], 0x6000\n\t" \
    "s_mov_b32 s44, 0x3c0000\n\t" \
    "buffer_load_dwordx4 v240, %[wr], s44 offen offset:0 nt lds\n\t" \
    "buffer_load_dwordx4 v241, %[wr], s44 offen offset:1024 nt lds\n\t" \
    "buffer_load_dwordx4 v242, %[wr], s44 offen offset:2048 nt lds\n\t" \
    "ds_read_b64_tr_b16 v[204:205], v244 offset:0\n\t" \
    "ds_read_b64_tr_b16 v[206:207], v244 offset:1536\n\t" \
    "ds_read_b64_tr_b16 v[4:5], v244 offset:32\n\t" \
    "ds_read_b64_tr_b16 v[6:7], v244 offset:1568\n\t" \
    "ds_read_b64_tr_b16 v[12:13], v244 offset:64\n\t" \
    "ds_read_b64_tr_b16 v[14:15], v244 offset:1600\n\t" \
    "v_add_u32 v244, 0xc000, v243\n\t" \
    "s_waitcnt vmcnt(12) lgkmcnt(0)\n\t" \
    "s_barrier \n\t" \
    "v_mov_b32 v246, 0\n\t" \
    "buffer_load_dword v246, v246, s[60:63], %[mo] offen sc1\n\t" \
    "s_add_u32 m0, %[wm], 0x9000\n\t" \
    "s_mov_b32 s44, 0x420000\n\t" \
    "buffer_load_dwordx4 v240, %[wr], s44 offen offset:0 nt lds\n\t" \
    "buffer_load_dwordx4 v241, %[wr], s44 offen offset:1024 nt lds\n\t" \
    "buffer_load_dwordx4 v242, %[wr], s44 offen offset:2048 nt lds\n\t" \
    "ds_read_b64_tr_b16 v[208:209], v244 offset:0\n\t" \
    "ds_read_b64_tr_b16 v[210:211], v244 offset:1536\n\t" \
    "ds_read_b64_tr_b16 v[40:41], v244 offset:32\n\t" \
    "ds_read_b64_tr_b16 v[42:43], v244 offset:1568\n\t" \
    "ds_read_b64_tr_b16 v[48:49], v244 offset:64\n\t" \
    "ds_read_b64_tr_b16 v[50:51], v244 offset:1600\n\t" \
    "v_add_u32 v244, 0xf000, v243\n\t" \
    "s_waitcnt vmcnt(12) lgkmcnt(0)\n\t" \
    "s_barrier \n\t" \
    "s_add_u32 m0, %[wm], 0xc000\n\t" \
    "s_mov_b32 s44, 0x480000\n\t" \
    "buffer_load_dwordx4 v240, %[wr], s44 offen offset:0 nt lds\n\t" \
    "buffer_load_dwordx4 v241, %[wr], s44 offen offset:1024 nt lds\n\t" \
    "buffer_load_dwordx4 v242, %[wr], s44 offen offset:2048 nt lds\n\t" \
    "ds_read_b64_tr_b16 v[212:213], v244 offset:0\n\t" \
    "ds_read_b64_tr_b16 v[214:215], v244 offset:1536\n\t" \
    "ds_read_b64_tr_b16 v[44:45], v244 offset:32\n\t" \
    "ds_read_b64_tr_b16 v[46:47], v244 offset:1568\n\t" \
    "ds_read_b64_tr_b16 v[52:53], v244 offset:64\n\t" \
    "ds_read_b64_tr_b16 v[54:55], v244 offset:1600\n\t" \
    "v_add_u32 v244, 0x0, v243\n\t" \
    "s_waitcnt vmcnt(12) lgkmcnt(0)\n\t" \
    "s_barrier \n\t" \
    "s_add_u32 m0, %[wm], 0xf000\n\t" \
    "s_mov_b32 s44, 0x4e0000\n\t" \
    "buffer_load_dwordx4 v240, %[wr], s44 offen offset:0 nt lds\n\t" \
    "buffer_load_dwordx4 v241, %[wr], s44 offen offset:1024 nt lds\n\t" \
    "buffer_load_dwordx4 v242, %[wr], s44 offen offset:2048 nt lds\n\t" \
    "ds_read_b64_tr_b16 v[136:137], v244 offset:0\n\t" \
    "ds_read_b64_tr_b16 v[138:139], v244 offset:1536\n\t" \
    "ds_read_b64_tr_b16 v[144:145], v244 offset:32\n\t" \
    "ds_read_b64_tr_b16 v[146:147], v244 offset:1568\n\t" \
    "ds_read_b64_tr_b16 v[152:153], v244 offset:64\n\t" \
    "ds_read_b64_tr_b16 v[154:155], v244 offset:1600\n\t" \
    "v_add_u32 v244, 0x3000, v243\n\t" \
    "s_waitcnt vmcnt(12) lgkmcnt(0)\n\t" \
    "s_barrier \n\t" \
    "s_add_u32 m0, %[wm], 0x0\n\t" \
    "s_mov_b32 s44, 0x540000\n\t" \
    "buffer_load_dwordx4 v240, %[wr], s44 offen offset:0 nt lds\n\t" \
    "buffer_load_dwordx4 v241, %[wr], s44 offen offset:1024 nt lds\n\t" \
    "buffer_load_dwordx4 v242, %[wr], s44 offen offset:2048 nt lds\n\t" \
    "ds_read_b64_tr_b16 v[140:141], v244 offset:0\n\t" \
    "ds_read_b64_tr_b16 v[142:143], v244 offset:1536\n\t" \
    "ds_read_b64_tr_b16 v[148:149], v244 offset:32\n\t" \
    "ds_read_b64_tr_b16 v[150:151], v244 offset:1568\n\t" \
    "ds_read_b64_tr_b16 v[156:157], v244 offset:64\n\t" \
    "ds_read_b64_tr_b16 v[158:159], v244 offset:1600\n\t" \
    "v_add_u32 v244, 0x6000, v243\n\t" \
    "s_waitcnt vmcnt(12) lgkmcnt(0)\n\t" \
    "s_barrier \n\t" \
    "s_add_u32 m0, %[wm], 0x3000\n\t" \
    "s_mov_b32 s44, 0x5a0000\n\t" \
    "buffer_load_dwordx4 v240, %[wr], s44 offen offset:0 nt lds\n\t" \
    "buffer_load_dwordx4 v241, %[wr], s44 offen offset:1024 nt lds\n\t" \
    "buffer_load_dwordx4 v242, %[wr], s44 offen offset:2048 nt lds\n\t" \
    "ds_read_b64_tr_b16 v[216:217], v244 offset:0\n\t" \
    "ds_read_b64_tr_b16 v[218:219], v244 offset:1536\n\t" \
    "ds_read_b64_tr_b16 v[80:81], v244 offset:32\n\t" \
    "ds_read_b64_tr_b16 v[82:83], v244 offset:1568\n\t" \
    "ds_read_b64_tr_b16 v[88:89], v244 offset:64\n\t" \
    "ds_read_b64_tr_b16 v[90:91], v244 offset:1600\n\t" \
    "v_add_u32 v244, 0x9000, v243\n\t" \
    "s_waitcnt vmcnt(12) lgkmcnt(0)\n\t" \
    "s_barrier \n\t" \
    ".Lv11_%=:\n\t" \
    "v_readfirstlane_b32 s43, v246\n\t" \
    "s_nop 0\n\t" \
    "s_cmp_lg_u32 s43, -1\n\t" \
    "s_cbranch_scc1 .Lv12_%=\n\t" \
    "s_cmp_eq_u32 s41, 0\n\t" \
    "s_cbranch_scc1 .Lv12_%=\n\t" \
    "s_sub_u32 s41, s41, 1\n\t" \
    "s_sleep 4\n\t" \
    "v_mov_b32 v246, 0\n\t" \
    "buffer_load_dword v246, v246, s[60:63], %[mo] offen sc1\n\t" \
    "s_waitcnt vmcnt(0)\n\t" \
    "s_branch .Lv11_%=\n\t" \
    ".Lv12_%=:\n\t" \
    "v_mov_b32 v246, 0x8000\n\t" \
    "s_mov_b64 exec, 1\n\t" \
    "buffer_store_dword v246, v246, s[60:63], %[mo] offen sc1\n\t" \
    "s_mov_b64 exec, -1\n\t" \
    "s_and_b32 s43, s43, 0x2000\n\t" \
    "s_xor_b32 s43, s43, %[lx]\n\t" \
    "s_add_u32 s60, s60, s43\n\t" \
    "s_addc_u32 s61, s61, 0\n\t" \
    "v_and_b32 v252, 0xf0, v253\n\t" \
    "v_lshlrev_b32 v252, 3, v252\n\t" \
    "buffer_load_dword v252, v252, s[60:63], %[fl] offen sc1\n\t" \
    "s_add_u32 m0, %[wm], 0x6000\n\t" \
    "s_mov_b32 s44, 0x600000\n\t" \
    "buffer_load_dwordx4 v240, %[wr], s44 offen offset:0 nt lds\n\t" \
    "buffer_load_dwordx4 v241, %[wr], s44 offen offset:1024 nt lds\n\t" \
    "buffer_load_dwordx4 v242, %[wr], s44 offen offset:2048 nt lds\n\t" \
    "ds_read_b64_tr_b16 v[220:221], v244 offset:0\n\t" \
    "ds_read_b64_tr_b16 v[222:223], v244 offset:1536\n\t" \
    "ds_read_b64_tr_b16 v[84:85], v244 offset:32\n\t" \
    "ds_read_b64_tr_b16 v[86:87], v244 offset:1568\n\t" \
    "ds_read_b64_tr_b16 v[92:93], v244 offset:64\n\t" \
    "ds_read_b64_tr_b16 v[94:95], v244 offset:1600\n\t" \
    "v_add_u32 v244, 0xc000, v243\n\t" \
    "s_waitcnt vmcnt(12) lgkmcnt(0)\n\t" \
    "s_barrier \n\t" \
    "s_add_u32 m0, %[wm], 0x9000\n\t" \
    "s_mov_b32 s44, 0x660000\n\t" \
    "buffer_load_dwordx4 v240, %[wr], s44 offen offset:0 nt lds\n\t" \
    "buffer_load_dwordx4 v241, %[wr], s44 offen offset:1024 nt lds\n\t" \
    "buffer_load_dwordx4 v242, %[wr], s44 offen offset:2048 nt lds\n\t" \
    "ds_read_b64_tr_b16 v[16:17], v244 offset:0\n\t" \
    "ds_read_b64_tr_b16 v[18:19], v244 offset:1536\n\t" \
    "ds_read_b64_tr_b16 v[24:25], v244 offset:32\n\t" \
    "ds_read_b64_tr_b16 v[26:27], v244 offset:1568\n\t" \
    "ds_read_b64_tr_b16 v[32:33], v244 offset:64\n\t" \
    "ds_read_b64_tr_b16 v[34:35], v244 offset:1600\n\t" \
    "v_add_u32 v244, 0xf000, v243\n\t" \
    "s_waitcnt vmcnt(12) lgkmcnt(0)\n\t" \
    "s_barrier \n\t" \
    "s_add_u32 m0, %[wm], 0xc000\n\t" \
    "s_mov_b32 s44, 0x6c0000\n\t" \
    "buffer_load_dwordx4 v240, %[wr], s44 offen offset:0 nt lds\n\t" \
    "buffer_load_dwordx4 v241, %[wr], s44 offen offset:1024 nt lds\n\t" \
    "buffer_load_dwordx4 v242, %[wr], s44 offen offset:2048 nt lds\n\t" \
    "ds_read_b64_tr_b16 v[20:21], v244 offset:0\n\t" \
    "ds_read_b64_tr_b16 v[22:23], v244 offset:1536\n\t" \
    "ds_read_b64_tr_b16 v[28:29], v244 offset:32\n\t" \
    "ds_read_b64_tr_b16 v[30:31], v244 offset:1568\n\t" \
    "ds_read_b64_tr_b16 v[36:37], v244 offset:64\n\t" \
    "ds_read_b64_tr_b16 v[38:39], v244 offset:1600\n\t" \
    "v_add_u32 v244, 0x0, v243\n\t" \
    "s_waitcnt vmcnt(12) lgkmcnt(0)\n\t" \
    "s_barrier \n\t" \
    "s_add_u32 m0, %[wm], 0xf000\n\t" \
    "s_mov_b32 s44, 0x720000\n\t" \
    "buffer_load_dwordx4 v240, %[wr], s44 offen offset:0 nt lds\n\t" \
    "buffer_load_dwordx4 v241, %[wr], s44 offen offset:1024 nt lds\n\t" \
    "buffer_load_dwordx4 v242, %[wr], s44 offen offset:2048 nt lds\n\t" \
    "ds_read_b64_tr_b16 v[224:225], v244 offset:0\n\t" \
    "ds_read_b64_tr_b16 v[226:227], v244 offset:1536\n\t" \
    "ds_read_b64_tr_b16 v[120:121], v244 offset:32\n\t" \
    "ds_read_b64_tr_b16 v[122:123], v244 offset:1568\n\t" \
    "ds_read_b64_tr_b16 v[128:129], v244 offset:64\n\t" \
    "ds_read_b64_tr_b16 v[130:131], v244 offset:1600\n\t" \
    "v_add_u32 v244, 0x3000, v243\n\t" \
    "s_waitcnt vmcnt(12) lgkmcnt(0)\n\t" \
    "s_barrier \n\t" \
    "ds_read_b64_tr_b16 v[228:229], v244 offset:0\n\t" \
    "ds_read_b64_tr_b16 v[230:231], v244 offset:1536\n\t" \
    "ds_read_b64_tr_b16 v[124:125], v244 offset:32\n\t" \
    "ds_read_b64_tr_b16 v[126:127], v244 offset:1568\n\t" \
    "ds_read_b64_tr_b16 v[132:133], v244 offset:64\n\t" \
    "ds_read_b64_tr_b16 v[134:135], v244 offset:1600\n\t" \
    "v_add_u32 v244, 0x6000, v243\n\t" \
    "s_waitcnt vmcnt(9) lgkmcnt(0)\n\t" \
    "s_barrier \n\t" \
    "ds_read_b64_tr_b16 v[232:233], v244 offset:0\n\t" \
    "ds_read_b64_tr_b16 v[234:235], v244 offset:1536\n\t" \
    "ds_read_b64_tr_b16 v[160:161], v244 offset:32\n\t" \
    "ds_read_b64_tr_b16 v[162:163], v244 offset:1568\n\t" \
    "ds_read_b64_tr_b16 v[168:169], v244 offset:64\n\t" \
    "ds_read_b64_tr_b16 v[170:171], v244 offset:1600\n\t" \
    "v_add_u32 v244, 0x9000, v243\n\t" \
    "s_waitcnt vmcnt(6) lgkmcnt(0)\n\t" \
    "s_barrier \n\t" \
    "ds_read_b64_tr_b16 v[236:237], v244 offset:0\n\t" \
    "ds_read_b64_tr_b16 v[238:239], v244 offset:1536\n\t" \
    "ds_read_b64_tr_b16 v[164:165], v244 offset:32\n\t" \
    "ds_read_b64_tr_b16 v[166:167], v244 offset:1568\n\t" \
    "ds_read_b64_tr_b16 v[172:173], v244 offset:64\n\t" \
    "ds_read_b64_tr_b16 v[174:175], v244 offset:1600\n\t" \
    "v_add_u32 v244, 0xc000, v243\n\t" \
    "s_waitcnt vmcnt(3) lgkmcnt(0)\n\t" \
    "s_barrier \n\t" \
    "ds_read_b64_tr_b16 v[176:177], v244 offset:0\n\t" \
    "ds_read_b64_tr_b16 v[178:179], v244 offset:1536\n\t" \
    "ds_read_b64_tr_b16 v[184:185], v244 offset:32\n\t" \
    "ds_read_b64_tr_b16 v[186:187], v244 offset:1568\n\t" \
    "ds_read_b64_tr_b16 v[192:193], v244 offset:64\n\t" \
    "ds_read_b64_tr_b16 v[194:195], v244 offset:1600\n\t" \
    "s_mov_b32 s43, 0\n\t" \
    "v_cmp_eq_u32 vcc, 0, v252\n\t" \
    "s_cbranch_vccnz .Lv13_%=\n\t" \
    "s_mov_b32 m0, 0x1000\n\t" \
    "s_add_u32 s49, s42, 0x0\n\t" \
    "buffer_load_dwordx4 v253, s[60:63], s49 offen offset:0 sc1 lds\n\t" \
    "buffer_load_dwordx4 v253, s[60:63], s49 offen offset:1024 sc1 lds\n\t" \
    "buffer_load_dwordx4 v253, s[60:63], s49 offen offset:2048 sc1 lds\n\t" \
    "buffer_load_dwordx4 v253, s[60:63], s49 offen offset:3072 sc1 lds\n\t" \
    "s_mov_b32 m0, 0x4400\n\t" \
    "s_add_u32 s49, s42, 0xc400\n\t" \
    "buffer_load_dwordx4 v253, s[60:63], s49 offen offset:0 sc1 lds\n\t" \
    "s_mov_b32 m0, 0x6000\n\t" \
    "s_add_u32 s49, s42, 0x1000\n\t" \
    "buffer_load_dwordx4 v253, s[60:63], s49 offen offset:0 sc1 lds\n\t" \
    "buffer_load_dwordx4 v253, s[60:63], s49 offen offset:1024 sc1 lds\n\t" \
    "buffer_load_dwordx4 v253, s[60:63], s49 offen offset:2048 sc1 lds\n\t" \
    "buffer_load_dwordx4 v253, s[60:63], s49 offen offset:3072 sc1 lds\n\t" \
    "s_mov_b32 m0, 0x9400\n\t" \
    "s_add_u32 s49, s42, 0xd400\n\t" \
    "buffer_load_dwordx4 v253, s[60:63], s49 offen offset:0 sc1 lds\n\t" \
    "s_mov_b32 s43, 1\n\t" \
    ".Lv13_%=:\n\t" \
    "v_add_u32 v244, 0xf000, v243\n\t" \
    "s_waitcnt vmcnt(0) lgkmcnt(0)\n\t" \
    "s_barrier \n\t" \
    "ds_read_b64_tr_b16 v[180:181], v244 offset:0\n\t" \
    "ds_read_b64_tr_b16 v[182:183], v244 offset:1536\n\t" \
    "ds_read_b64_tr_b16 v[188:189], v244 offset:32\n\t" \
    "ds_read_b64_tr_b16 v[190:191], v244 offset:1568\n\t" \
    "ds_read_b64_tr_b16 v[196:197], v244 offset:64\n\t" \
    "ds_read_b64_tr_b16 v[198:199], v244 offset:1600\n\t" \
    "s_waitcnt lgkmcnt(0)\n\t" \
    "s_barrier \n\t" \
    LST0 \
    LJIT \
    "ds_write_b128 v254, v[200:203] offset:40960\n\t" \
    "ds_write_b128 v254, v[204:207] offset:41984\n\t" \
    "ds_write_b128 v254, v[208:211] offset:43008\n\t" \
    "ds_write_b128 v254, v[212:215] offset:44032\n\t" \
    "ds_write_b128 v254, v[216:219] offset:45056\n\t" \
    "ds_write_b128 v254, v[220:223] offset:46080\n\t" \
    "ds_write_b128 v254, v[224:227] offset:47104\n\t" \
    "ds_write_b128 v254, v[228:231] offset:48128\n\t" \
    "ds_write_b128 v254, v[232:235] offset:49152\n\t" \
    "ds_write_b128 v254, v[236:239] offset:50176\n\t" \
    "s_cmp_lg_u32 s43, 0\n\t" \
    "s_cbranch_scc1 .Lv14_%=\n\t" \
    "s_mov_b32 s44, 0\n\t" \
    ".Lv15_%=:\n\t" \
    "v_cmp_ge_u32 vcc, s44, v252\n\t" \
    "s_cbranch_vccz .Lv16_%=\n\t" \
    "s_cmp_eq_u32 s41, 0\n\t" \
    "s_cbranch_scc1 .Lv16_%=\n\t" \
    "s_sub_u32 s41, s41, 1\n\t" \
    "s_sleep 8\n\t" \
    "v_and_b32 v252, 0xf0, v253\n\t" \
    "v_lshlrev_b32 v252, 3, v252\n\t" \
    "buffer_load_dword v252, v252, s[60:63], %[fl] offen sc1\n\t" \
    "s_waitcnt vmcnt(0)\n\t" \
    "s_branch .Lv15_%=\n\t" \
    ".Lv16_%=:\n\t" \
    "s_mov_b32 m0, 0x1000\n\t" \
    "s_add_u32 s49, s42, 0x0\n\t" \
    "buffer_load_dwordx4 v253, s[60:63], s49 offen offset:0 sc1 lds\n\t" \
    "buffer_load_dwordx4 v253, s[60:63], s49 offen offset:1024 sc1 lds\n\t" \
    "buffer_load_dwordx4 v253, s[60:63], s49 offen offset:2048 sc1 lds\n\t" \
    "buffer_load_dwordx4 v253, s[60:63], s49 offen offset:3072 sc1 lds\n\t" \
    "s_mov_b32 m0, 0x4400\n\t" \
    "s_add_u32 s49, s42, 0xc400\n\t" \
    "buffer_load_dwordx4 v253, s[60:63], s49 offen offset:0 sc1 lds\n\t" \
    "s_mov_b32 m0, 0x6000\n\t" \
    "s_add_u32 s49, s42, 0x1000\n\t" \
    "buffer_load_dwordx4 v253, s[60:63], s49 offen offset:0 sc1 lds\n\t" \
    "buffer_load_dwordx4 v253, s[60:63], s49 offen offset:1024 sc1 lds\n\t" \
    "buffer_load_dwordx4 v253, s[60:63], s49 offen offset:2048 sc1 lds\n\t" \
    "buffer_load_dwordx4 v253, s[60:63], s49 offen offset:3072 sc1 lds\n\t" \
    "s_mov_b32 m0, 0x9400\n\t" \
    "s_add_u32 s49, s42, 0xd400\n\t" \
    "buffer_load_dwordx4 v253, s[60:63], s49 offen offset:0 sc1 lds\n\t" \
    ".Lv14_%=:\n\t" \
    "s_waitcnt vmcnt(0) lgkmcnt(0)\n\t" \
    "s_barrier \n\t" \
    "ds_read_b128 v[224:227], v253 offset:4096\n\t" \
    "ds_read_b128 v[244:247], v254 offset:40960\n\t" \
    "ds_read_b128 v[228:231], v253 offset:5120\n\t" \
    "ds_read_b128 v[232:235], v253 offset:24576\n\t" \
    "ds_read_b128 v[236:239], v253 offset:25600\n\t" \
    "ds_read_b128 v[248:251], v254 offset:41984\n\t" \
    ".Lv17_%=:\n\t" \
    LST7 \
    LJIT \
    "s_waitcnt lgkmcnt(5)\n\t" \
    "v_mfma_f32_16x16x32_bf16 v[204:207], v[0:3], v[224:227], 0\n\t" \
    "s_waitcnt lgkmcnt(3)\n\t" \
    "v_mfma_f32_16x16x32_bf16 v[204:207], v[4:7], v[228:231], v[204:207]\n\t" \
    "ds_read_b128 v[240:243], v253 offset:12288\n\t" \
    "v_mfma_f32_16x16x32_bf16 v[208:211], v[8:11], v[224:227], 0\n\t" \
    "v_mfma_f32_16x16x32_bf16 v[208:211], v[12:15], v[228:231], v[208:211]\n\t" \
    "v_mfma_f32_16x16x32_bf16 v[200:203], v[244:247], v[224:227], 0\n\t" \
    "s_waitcnt lgkmcnt(1)\n\t" \
    "v_mfma_f32_16x16x32_bf16 v[200:203], v[248:251], v[228:231], v[200:203]\n\t" \
    "v_mfma_f32_16x16x32_bf16 v[216:219], v[0:3], v[232:235], 0\n\t" \
    "ds_read_b128 v[224:227], v253 offset:13312\n\t" \
    "v_mfma_f32_16x16x32_bf16 v[216:219], v[4:7], v[236:239], v[216:219]\n\t" \
    "ds_read_b128 v[228:231], v253 offset:6144\n\t" \
    "v_mfma_f32_16x16x32_bf16 v[220:223], v[8:11], v[232:235], 0\n\t" \
    "v_mfma_f32_16x16x32_bf16 v[220:223], v[12:15], v[236:239], v[220:223]\n\t" \
    "v_mfma_f32_16x16x32_bf16 v[212:215], v[244:247], v[232:235], 0\n\t" \
    "ds_read_b128 v[244:247], v254 offset:43008\n\t" \
    "v_mfma_f32_16x16x32_bf16 v[212:215], v[248:251], v[236:239], v[212:215]\n\t" \
    "ds_read_b128 v[248:251], v254 offset:44032\n\t" \
    "s_waitcnt lgkmcnt(4)\n\t" \
    "v_mfma_f32_16x16x32_bf16 v[204:207], v[24:27], v[240:243], v[204:207]\n\t" \
    "ds_read_b128 v[232:235], v253 offset:7168\n\t" \
    "s_waitcnt lgkmcnt(4)\n\t" \
    "v_mfma_f32_16x16x32_bf16 v[204:207], v[28:31], v[224:227], v[204:207]\n\t" \
    "ds_read_b128 v[236:239], v253 offset:26624\n\t" \
    "v_mfma_f32_16x16x32_bf16 v[208:211], v[32:35], v[240:243], v[208:211]\n\t" \
    "v_mfma_f32_16x16x32_bf16 v[208:211], v[36:39], v[224:227], v[208:211]\n\t" \
    "v_mfma_f32_16x16x32_bf16 v[200:203], v[16:19], v[240:243], v[200:203]\n\t" \
    "v_mfma_f32_16x16x32_bf16 v[200:203], v[20:23], v[224:227], v[200:203]\n\t" \
    "s_waitcnt lgkmcnt(4)\n\t" \
    "v_mfma_f32_16x16x32_bf16 v[204:207], v[40:43], v[228:231], v[204:207]\n\t" \
    "ds_read_b128 v[240:243], v253 offset:27648\n\t" \
    "s_waitcnt lgkmcnt(2)\n\t" \
    "v_mfma_f32_16x16x32_bf16 v[204:207], v[44:47], v[232:235], v[204:207]\n\t" \
    "ds_read_b128 v[224:227], v253 offset:0\n\t" \
    "v_mfma_f32_16x16x32_bf16 v[208:211], v[48:51], v[228:231], v[208:211]\n\t" \
    "v_mfma_f32_16x16x32_bf16 v[208:211], v[52:55], v[232:235], v[208:211]\n\t" \
    "v_mfma_f32_16x16x32_bf16 v[200:203], v[244:247], v[228:231], v[200:203]\n\t" \
    "v_mfma_f32_16x16x32_bf16 v[200:203], v[248:251], v[232:235], v[200:203]\n\t" \
    "s_waitcnt lgkmcnt(2)\n\t" \
    "v_mfma_f32_16x16x32_bf16 v[216:219], v[40:43], v[236:239], v[216:219]\n\t" \
    "ds_read_b128 v[228:231], v253 offset:1024\n\t" \
    "s_add_u32 s44, s40, 1\n\t" \
    "s_waitcnt lgkmcnt(2)\n\t" \
    "v_mfma_f32_16x16x32_bf16 v[216:219], v[44:47], v[240:243], v[216:219]\n\t" \
    "ds_read_b128 v[232:235], v253 offset:10240\n\t" \
    "s_and_b32 s45, s44, 15\n\t" \
    "v_mfma_f32_16x16x32_bf16 v[220:223], v[48:51], v[236:239], v[220:223]\n\t" \
    "s_cmp_eq_u32 s45, 0\n\t" \
    "v_mfma_f32_16x16x32_bf16 v[220:223], v[52:55], v[240:243], v[220:223]\n\t" \
    "s_cselect_b32 s45, %[cj], %[cs]\n\t" \
    "v_mfma_f32_16x16x32_bf16 v[212:215], v[244:247], v[236:239], v[212:215]\n\t" \
    "ds_read_b128 v[244:247], v254 offset:45056\n\t" \
    "s_cmp_eq_u32 s44, 64\n\t" \
    "v_mfma_f32_16x16x32_bf16 v[212:215], v[248:251], v[240:243], v[212:215]\n\t" \
    "ds_read_b128 v[248:251], v254 offset:46080\n\t" \
    "s_cselect_b32 s45, 0, s45\n\t" \
    "s_waitcnt lgkmcnt(4)\n\t" \
    "v_mfma_f32_16x16x32_bf16 v[204:207], v[64:67], v[224:227], v[204:207]\n\t" \
    "ds_read_b128 v[236:239], v253 offset:11264\n\t" \
    "s_add_u32 s42, s42, s45\n\t" \
    "s_waitcnt lgkmcnt(4)\n\t" \
    "v_mfma_f32_16x16x32_bf16 v[204:207], v[68:71], v[228:231], v[204:207]\n\t" \
    "ds_read_b128 v[240:243], v253 offset:30720\n\t" \
    "v_mfma_f32_16x16x32_bf16 v[208:211], v[72:75], v[224:227], v[208:211]\n\t" \
    "v_mfma_f32_16x16x32_bf16 v[208:211], v[76:79], v[228:231], v[208:211]\n\t" \
    "v_mfma_f32_16x16x32_bf16 v[200:203], v[56:59], v[224:227], v[200:203]\n\t" \
    "v_mfma_f32_16x16x32_bf16 v[200:203], v[60:63], v[228:231], v[200:203]\n\t" \
    "s_waitcnt lgkmcnt(4)\n\t" \
    "v_mfma_f32_16x16x32_bf16 v[204:207], v[80:83], v[232:235], v[204:207]\n\t" \
    "ds_read_b128 v[224:227], v253 offset:31744\n\t" \
    "s_waitcnt lgkmcnt(2)\n\t" \
    "v_mfma_f32_16x16x32_bf16 v[204:207], v[84:87], v[236:239], v[204:207]\n\t" \
    "ds_read_b128 v[228:231], v253 offset:2048\n\t" \
    "v_mfma_f32_16x16x32_bf16 v[208:211], v[88:91], v[232:235], v[208:211]\n\t" \
    "v_mfma_f32_16x16x32_bf16 v[208:211], v[92:95], v[236:239], v[208:211]\n\t" \
    "v_mfma_f32_16x16x32_bf16 v[200:203], v[244:247], v[232:235], v[200:203]\n\t" \
    "v_mfma_f32_16x16x32_bf16 v[200:203], v[248:251], v[236:239], v[200:203]\n\t" \
    "s_waitcnt lgkmcnt(2)\n\t" \
    "v_mfma_f32_16x16x32_bf16 v[216:219], v[80:83], v[240:243], v[216:219]\n\t" \
    "ds_read_b128 v[232:235], v253 offset:3072\n\t" \
    "s_and_b32 s44, s40, 3\n\t" \
    "s_waitcnt lgkmcnt(2)\n\t" \
    "v_mfma_f32_16x16x32_bf16 v[216:219], v[84:87], v[224:227], v[216:219]\n\t" \
    "ds_read_b128 v[236:239], v253 offset:14336\n\t" \
    "s_lshl_b32 s44, s44, 17\n\t" \
    "v_mfma_f32_16x16x32_bf16 v[220:223], v[88:91], v[240:243], v[220:223]\n\t" \
    "s_add_u32 s44, s44, %[mb]\n\t" \
    "v_mfma_f32_16x16x32_bf16 v[220:223], v[92:95], v[224:227], v[220:223]\n\t" \
    "s_add_u32 s46, s44, 0x800\n\t" \
    "v_mfma_f32_16x16x32_bf16 v[212:215], v[244:247], v[240:243], v[212:215]\n\t" \
    "ds_read_b128 v[244:247], v254 offset:47104\n\t" \
    "s_add_u32 s47, s44, 0x1800\n\t" \
    "v_mfma_f32_16x16x32_bf16 v[212:215], v[248:251], v[224:227], v[212:215]\n\t" \
    "ds_read_b128 v[248:251], v254 offset:48128\n\t" \
    "s_add_u32 s48, s44, 0xc00\n\t" \
    "s_waitcnt lgkmcnt(4)\n\t" \
    "v_mfma_f32_16x16x32_bf16 v[204:207], v[104:107], v[228:231], v[204:207]\n\t" \
    "ds_read_b128 v[240:243], v253 offset:15360\n\t" \
    "s_waitcnt lgkmcnt(4)\n\t" \
    "v_mfma_f32_16x16x32_bf16 v[204:207], v[108:111], v[232:235], v[204:207]\n\t" \
    "ds_read_b128 v[224:227], v253 offset:34816\n\t" \
    "v_mfma_f32_16x16x32_bf16 v[208:211], v[112:115], v[228:231], v[208:211]\n\t" \
    "v_mfma_f32_16x16x32_bf16 v[208:211], v[116:119], v[232:235], v[208:211]\n\t" \
    "v_mfma_f32_16x16x32_bf16 v[200:203], v[96:99], v[228:231], v[200:203]\n\t" \
    "v_mfma_f32_16x16x32_bf16 v[200:203], v[100:103], v[232:235], v[200:203]\n\t" \
    "s_waitcnt lgkmcnt(4)\n\t" \
    "v_mfma_f32_16x16x32_bf16 v[204:207], v[120:123], v[236:239], v[204:207]\n\t" \
    "ds_read_b128 v[228:231], v253 offset:35840\n\t" \
    "s_waitcnt lgkmcnt(2)\n\t" \
    "v_mfma_f32_16x16x32_bf16 v[204:207], v[124:127], v[240:243], v[204:207]\n\t" \
    "ds_read_b128 v[232:235], v253 offset:8192\n\t" \
    "v_mfma_f32_16x16x32_bf16 v[208:211], v[128:131], v[236:239], v[208:211]\n\t" \
    "v_mfma_f32_16x16x32_bf16 v[208:211], v[132:135], v[240:243], v[208:211]\n\t" \
    "v_mfma_f32_16x16x32_bf16 v[200:203], v[244:247], v[236:239], v[200:203]\n\t" \
    "v_mfma_f32_16x16x32_bf16 v[200:203], v[248:251], v[240:243], v[200:203]\n\t" \
    "s_waitcnt lgkmcnt(2)\n\t" \
    "v_mfma_f32_16x16x32_bf16 v[216:219], v[120:123], v[224:227], v[216:219]\n\t" \
    "ds_read_b128 v[236:239], v253 offset:9216\n\t" \
    "s_waitcnt lgkmcnt(2)\n\t" \
    "v_mfma_f32_16x16x32_bf16 v[216:219], v[124:127], v[228:231], v[216:219]\n\t" \
    "ds_read_b128 v[240:243], v253 offset:16384\n\t" \
    "v_mfma_f32_16x16x32_bf16 v[220:223], v[128:131], v[224:227], v[220:223]\n\t" \
    "v_mfma_f32_16x16x32_bf16 v[220:223], v[132:135], v[228:231], v[220:223]\n\t" \
    "v_mfma_f32_16x16x32_bf16 v[212:215], v[244:247], v[224:227], v[212:215]\n\t" \
    "ds_read_b128 v[244:247], v254 offset:49152\n\t" \
    "v_mfma_f32_16x16x32_bf16 v[212:215], v[248:251], v[228:231], v[212:215]\n\t" \
    "ds_read_b128 v[248:251], v254 offset:50176\n\t" \
    "v_and_b32 v252, 0xf0, v253\n\t" \
    "v_lshlrev_b32 v252, 3, v252\n\t" \
    "buffer_load_dword v252, v252, s[60:63], %[fl] offen sc1\n\t" \
    "s_waitcnt lgkmcnt(4)\n\t" \
    "v_mfma_f32_16x16x32_bf16 v[204:207], v[144:147], v[232:235], v[204:207]\n\t" \
    "ds_read_b128 v[224:227], v253 offset:17408\n\t" \
    "s_waitcnt lgkmcnt(4)\n\t" \
    "v_mfma_f32_16x16x32_bf16 v[204:207], v[148:151], v[236:239], v[204:207]\n\t" \
    "ds_read_b128 v[228:231], v253 offset:36864\n\t" \
    "v_mfma_f32_16x16x32_bf16 v[208:211], v[152:155], v[232:235], v[208:211]\n\t" \
    "v_mfma_f32_16x16x32_bf16 v[208:211], v[156:159], v[236:239], v[208:211]\n\t" \
    "v_mfma_f32_16x16x32_bf16 v[200:203], v[136:139], v[232:235], v[200:203]\n\t" \
    "v_mfma_f32_16x16x32_bf16 v[200:203], v[140:143], v[236:239], v[200:203]\n\t" \
    "s_waitcnt lgkmcnt(4)\n\t" \
    "v_mfma_f32_16x16x32_bf16 v[204:207], v[160:163], v[240:243], v[204:207]\n\t" \
    "ds_read_b128 v[232:235], v253 offset:37888\n\t" \
    "s_waitcnt lgkmcnt(2)\n\t" \
    "v_mfma_f32_16x16x32_bf16 v[204:207], v[164:167], v[224:227], v[204:207]\n\t" \
    "ds_read_b128 v[236:239], v253 offset:18432\n\t" \
    "v_mfma_f32_16x16x32_bf16 v[208:211], v[168:171], v[240:243], v[208:211]\n\t" \
    "v_mfma_f32_16x16x32_bf16 v[208:211], v[172:175], v[224:227], v[208:211]\n\t" \
    "v_mfma_f32_16x16x32_bf16 v[200:203], v[244:247], v[240:243], v[200:203]\n\t" \
    "v_mfma_f32_16x16x32_bf16 v[200:203], v[248:251], v[224:227], v[200:203]\n\t" \
    "s_waitcnt lgkmcnt(2)\n\t" \
    "v_mfma_f32_16x16x32_bf16 v[216:219], v[160:163], v[228:231], v[216:219]\n\t" \
    "ds_read_b128 v[240:243], v253 offset:19456\n\t" \
    "s_waitcnt lgkmcnt(2)\n\t" \
    "v_mfma_f32_16x16x32_bf16 v[216:219], v[164:167], v[232:235], v[216:219]\n\t" \
    "ds_read_b128 v[224:227], v253 offset:32768\n\t" \
    "v_mfma_f32_16x16x32_bf16 v[220:223], v[168:171], v[228:231], v[220:223]\n\t" \
    "v_mfma_f32_16x16x32_bf16 v[220:223], v[172:175], v[232:235], v[220:223]\n\t" \
    "v_mfma_f32_16x16x32_bf16 v[212:215], v[244:247], v[228:231], v[212:215]\n\t" \
    "v_mfma_f32_16x16x32_bf16 v[212:215], v[248:251], v[232:235], v[212:215]\n\t" \
    "s_waitcnt lgkmcnt(2)\n\t" \
    "v_mfma_f32_16x16x32_bf16 v[204:207], v[184:187], v[236:239], v[204:207]\n\t" \
    "ds_read_b128 v[228:231], v253 offset:33792\n\t" \
    "s_waitcnt lgkmcnt(2)\n\t" \
    "v_mfma_f32_16x16x32_bf16 v[204:207], v[188:191], v[240:243], v[204:207]\n\t" \
    "ds_read_b128 v[232:235], v253 offset:20480\n\t" \
    "v_mfma_f32_16x16x32_bf16 v[208:211], v[192:195], v[236:239], v[208:211]\n\t" \
    "v_mfma_f32_16x16x32_bf16 v[208:211], v[196:199], v[240:243], v[208:211]\n\t" \
    "v_mfma_f32_16x16x32_bf16 v[200:203], v[176:179], v[236:239], v[200:203]\n\t" \
    "v_mfma_f32_16x16x32_bf16 v[200:203], v[180:183], v[240:243], v[200:203]\n\t" \
    "s_waitcnt lgkmcnt(2)\n\t" \
    "v_mfma_f32_16x16x32_bf16 v[216:219], v[24:27], v[224:227], v[216:219]\n\t" \
    "ds_read_b128 v[236:239], v253 offset:21504\n\t" \
    "s_waitcnt lgkmcnt(2)\n\t" \
    "v_mfma_f32_16x16x32_bf16 v[216:219], v[28:31], v[228:231], v[216:219]\n\t" \
    "ds_read_b128 v[240:243], v253 offset:22528\n\t" \
    "v_mfma_f32_16x16x32_bf16 v[220:223], v[32:35], v[224:227], v[220:223]\n\t" \
    "v_cvt_pk_bf16_f32 v204, v204, v205\n\t" \
    "v_mfma_f32_16x16x32_bf16 v[220:223], v[36:39], v[228:231], v[220:223]\n\t" \
    "v_cvt_pk_bf16_f32 v205, v206, v207\n\t" \
    "v_mfma_f32_16x16x32_bf16 v[212:215], v[16:19], v[224:227], v[212:215]\n\t" \
    "ds_write_b64 v254, v[204:205] offset:10240\n\t" \
    "v_mfma_f32_16x16x32_bf16 v[212:215], v[20:23], v[228:231], v[212:215]\n\t" \
    "v_cvt_pk_bf16_f32 v208, v208, v209\n\t" \
    "s_waitcnt lgkmcnt(3)\n\t" \
    "v_mfma_f32_16x16x32_bf16 v[216:219], v[64:67], v[232:235], v[216:219]\n\t" \
    "ds_read_b128 v[224:227], v253 offset:23552\n\t" \
    "v_cvt_pk_bf16_f32 v209, v210, v211\n\t" \
    "s_waitcnt lgkmcnt(3)\n\t" \
    "v_mfma_f32_16x16x32_bf16 v[216:219], v[68:71], v[236:239], v[216:219]\n\t" \
    "ds_read_b128 v[228:231], v253 offset:28672\n\t" \
    "ds_write_b64 v254, v[208:209] offset:11264\n\t" \
    "v_mfma_f32_16x16x32_bf16 v[220:223], v[72:75], v[232:235], v[220:223]\n\t" \
    "v_cvt_pk_bf16_f32 v200, v200, v201\n\t" \
    "v_mfma_f32_16x16x32_bf16 v[220:223], v[76:79], v[236:239], v[220:223]\n\t" \
    "v_cvt_pk_bf16_f32 v201, v202, v203\n\t" \
    "v_mfma_f32_16x16x32_bf16 v[212:215], v[56:59], v[232:235], v[212:215]\n\t" \
    "ds_write_b64 v254, v[200:201] offset:9216\n\t" \
    "v_mfma_f32_16x16x32_bf16 v[212:215], v[60:63], v[236:239], v[212:215]\n\t" \
    "s_waitcnt lgkmcnt(5)\n\t" \
    "v_mfma_f32_16x16x32_bf16 v[216:219], v[104:107], v[240:243], v[216:219]\n\t" \
    "ds_read_b128 v[232:235], v253 offset:29696\n\t" \
    "s_waitcnt lgkmcnt(4)\n\t" \
    "v_mfma_f32_16x16x32_bf16 v[216:219], v[108:111], v[224:227], v[216:219]\n\t" \
    "ds_read_b128 v[236:239], v253 offset:38912\n\t" \
    "v_mfma_f32_16x16x32_bf16 v[220:223], v[112:115], v[240:243], v[220:223]\n\t" \
    "v_mfma_f32_16x16x32_bf16 v[220:223], v[116:119], v[224:227], v[220:223]\n\t" \
    "v_mfma_f32_16x16x32_bf16 v[212:215], v[96:99], v[240:243], v[212:215]\n\t" \
    "v_mfma_f32_16x16x32_bf16 v[212:215], v[100:103], v[224:227], v[212:215]\n\t" \
    "ds_read_b128 v[240:243], v253 offset:39936\n\t" \
    "s_waitcnt lgkmcnt(3)\n\t" \
    "s_barrier \n\t" \
    "v_mfma_f32_16x16x32_bf16 v[216:219], v[144:147], v[228:231], v[216:219]\n\t" \
    "s_waitcnt lgkmcnt(2)\n\t" \
    "v_mfma_f32_16x16x32_bf16 v[216:219], v[148:151], v[232:235], v[216:219]\n\t" \
    "s_waitcnt lgkmcnt(1)\n\t" \
    "v_mfma_f32_16x16x32_bf16 v[216:219], v[184:187], v[236:239], v[216:219]\n\t" \
    "s_waitcnt lgkmcnt(0)\n\t" \
    "v_mfma_f32_16x16x32_bf16 v[216:219], v[188:191], v[240:243], v[216:219]\n\t" \
    "v_mfma_f32_16x16x32_bf16 v[220:223], v[152:155], v[228:231], v[220:223]\n\t" \
    "v_mfma_f32_16x16x32_bf16 v[220:223], v[156:159], v[232:235], v[220:223]\n\t" \
    "v_mfma_f32_16x16x32_bf16 v[220:223], v[192:195], v[236:239], v[220:223]\n\t" \
    "v_cvt_pk_bf16_f32 v216, v216, v217\n\t" \
    "v_cvt_pk_bf16_f32 v217, v218, v219\n\t" \
    "v_mfma_f32_16x16x32_bf16 v[220:223], v[196:199], v[240:243], v[220:223]\n\t" \
    "ds_write_b64 v254, v[216:217] offset:16384\n\t" \
    "v_mfma_f32_16x16x32_bf16 v[212:215], v[136:139], v[228:231], v[212:215]\n\t" \
    "v_mfma_f32_16x16x32_bf16 v[212:215], v[140:143], v[232:235], v[212:215]\n\t" \
    "v_mfma_f32_16x16x32_bf16 v[212:215], v[176:179], v[236:239], v[212:215]\n\t" \
    "v_cvt_pk_bf16_f32 v220, v220, v221\n\t" \
    "v_cvt_pk_bf16_f32 v221, v222, v223\n\t" \
    "v_mfma_f32_16x16x32_bf16 v[212:215], v[180:183], v[240:243], v[212:215]\n\t" \
    "ds_write_b64 v254, v[220:221] offset:17408\n\t" \
    "s_nop 6\n\t" \
    "v_cvt_pk_bf16_f32 v212, v212, v213\n\t" \
    "v_cvt_pk_bf16_f32 v213, v214, v215\n\t" \
    "ds_write_b64 v254, v[212:213] offset:15360\n\t" \
    LST6 \
    LJIT \
    "s_waitcnt vmcnt(0) lgkmcnt(0)\n\t" \
    "s_barrier \n\t" \
    "s_setprio 2\n\t" \
    "s_add_u32 s44, s40, 1\n\t" \
    "s_min_u32 s44, s44, 63\n\t" \
    ".Lv18_%=:\n\t" \
    "v_cmp_ge_u32 vcc, s44, v252\n\t" \
    "s_cbranch_vccz .Lv19_%=\n\t" \
    "s_cmp_eq_u32 s41, 0\n\t" \
    "s_cbranch_scc1 .Lv19_%=\n\t" \
    "s_sub_u32 s41, s41, 1\n\t" \
    "s_sleep 2\n\t" \
    "v_and_b32 v252, 0xf0, v253\n\t" \
    "v_lshlrev_b32 v252, 3, v252\n\t" \
    "buffer_load_dword v252, v252, s[60:63], %[fl] offen sc1\n\t" \
    "s_waitcnt vmcnt(0)\n\t" \
    "s_branch .Lv18_%=\n\t" \
    ".Lv19_%=:\n\t" \
    "s_mov_b32 m0, 0x1000\n\t" \
    "s_add_u32 s49, s42, 0x0\n\t" \
    "buffer_load_dwordx4 v253, s[60:63], s49 offen offset:0 sc1 lds\n\t" \
    "s_sleep 1\n\t" \
    "buffer_load_dwordx4 v253, s[60:63], s49 offen offset:1024 sc1 lds\n\t" \
    "s_sleep 1\n\t" \
    "buffer_load_dwordx4 v253, s[60:63], s49 offen offset:2048 sc1 lds\n\t" \
    "s_sleep 1\n\t" \
    "buffer_load_dwordx4 v253, s[60:63], s49 offen offset:3072 sc1 lds\n\t" \
    "s_sleep 1\n\t" \
    "s_mov_b32 m0, 0x4400\n\t" \
    "s_add_u32 s49, s42, 0xc400\n\t" \
    "buffer_load_dwordx4 v253, s[60:63], s49 offen offset:0 sc1 lds\n\t" \
    "s_sleep 1\n\t" \
    "s_mov_b32 m0, 0x6000\n\t" \
    "s_add_u32 s49, s42, 0x1000\n\t" \
    "buffer_load_dwordx4 v253, s[60:63], s49 offen offset:0 sc1 lds\n\t" \
    "s_sleep 1\n\t" \
    "buffer_load_dwordx4 v253, s[60:63], s49 offen offset:1024 sc1 lds\n\t" \
    "s_sleep 1\n\t" \
    "buffer_load_dwordx4 v253, s[60:63], s49 offen offset:2048 sc1 lds\n\t" \
    "s_sleep 1\n\t" \
    "buffer_load_dwordx4 v253, s[60:63], s49 offen offset:3072 sc1 lds\n\t" \
    "s_sleep 1\n\t" \
    "s_mov_b32 m0, 0x9400\n\t" \
    "s_add_u32 s49, s42, 0xd400\n\t" \
    "buffer_load_dwordx4 v253, s[60:63], s49 offen offset:0 sc1 lds\n\t" \
    "s_sleep 1\n\t" \
    "ds_read_b128 v[200:203], v254 offset:40960\n\t" \
    "ds_read_b128 v[204:207], v254 offset:41984\n\t" \
    "ds_read_b128 v[216:219], v254 offset:43008\n\t" \
    "ds_read_b128 v[220:223], v254 offset:44032\n\t" \
    "v_mov_b64 v[208:209], 0\n\t" \
    "v_mov_b64 v[210:211], 0\n\t" \
    "v_mov_b64 v[244:245], 0\n\t" \
    "v_mov_b64 v[246:247], 0\n\t" \
    "v_mov_b64 v[248:249], 0\n\t" \
    "v_mov_b64 v[250:251], 0\n\t" \
    LST1 \
    LJIT \
    "s_sleep 5\n\t" \
    "s_mov_b32 m0, 0xa800\n\t" \
    "s_nop 0\n\t" \
    "buffer_load_dwordx4 v253, s[60:63], s46 offen sc1 lds\n\t" \
    "s_mov_b32 m0, 0xb800\n\t" \
    "s_nop 0\n\t" \
    "buffer_load_dwordx4 v253, s[60:63], s47 offen sc1 lds\n\t" \
    "s_mov_b32 m0, 0xac00\n\t" \
    "s_nop 0\n\t" \
    "buffer_load_dwordx4 v253, s[60:63], s48 offen sc1 lds\n\t" \
    "s_waitcnt lgkmcnt(0)\n\t" \
    LFLUSH212 \
    "v_bfe_i32 v212, v253, 5, 1\n\t" \
    "v_and_b32 v212, 0xaaaa, v212\n\t" \
    "v_or_b32 v212, 0x4444, v212\n\t" \
    "v_and_b32 v213, 0xffff, v255\n\t" \
    "v_lshrrev_b32 v214, 16, v255\n\t" \
    ".Lv110_%=:\n\t" \
    "s_waitcnt vmcnt(0)\n\t" \
    "ds_read_b128 v[224:227], v253 offset:43008\n\t" \
    "ds_read_b128 v[228:231], v253 offset:47104\n\t" \
    "ds_read_b128 v[232:235], v253 offset:44032\n\t" \
    "s_waitcnt lgkmcnt(2)\n\t" \
    "v_max_u32 v215, v224, v225\n\t" \
    "v_max3_u32 v215, v226, v227, v215\n\t" \
    "s_waitcnt lgkmcnt(1)\n\t" \
    "v_max3_u32 v215, v228, v229, v215\n\t" \
    "v_max3_u32 v215, v230, v231, v215\n\t" \
    "s_waitcnt lgkmcnt(0)\n\t" \
    "v_max3_u32 v215, v232, v233, v215\n\t" \
    "v_max3_u32 v215, v234, v235, v215\n\t" \
    "v_cmp_eq_u32 vcc, -1, v215\n\t" \
    "s_cbranch_vccz .Lv111_%=\n\t" \
    "v_max_u32 v215, v224, v225\n\t" \
    "v_max3_u32 v215, v226, v227, v215\n\t" \
    "v_cmp_eq_u32 vcc, -1, v215\n\t" \
    "s_cbranch_vccz .Lv112_%=\n\t" \
    "s_mov_b32 m0, 0xa800\n\t" \
    "s_nop 0\n\t" \
    "buffer_load_dwordx4 v253, s[60:63], s46 offen sc1 lds\n\t" \
    ".Lv112_%=:\n\t" \
    "v_max_u32 v215, v228, v229\n\t" \
    "v_max3_u32 v215, v230, v231, v215\n\t" \
    "v_cmp_eq_u32 vcc, -1, v215\n\t" \
    "s_cbranch_vccz .Lv113_%=\n\t" \
    "s_mov_b32 m0, 0xb800\n\t" \
    "s_nop 0\n\t" \
    "buffer_load_dwordx4 v253, s[60:63], s47 offen sc1 lds\n\t" \
    ".Lv113_%=:\n\t" \
    "v_max_u32 v215, v232, v233\n\t" \
    "v_max3_u32 v215, v234, v235, v215\n\t" \
    "v_cmp_eq_u32 vcc, -1, v215\n\t" \
    "s_cbranch_vccz .Lv114_%=\n\t" \
    "s_mov_b32 m0, 0xac00\n\t" \
    "s_nop 0\n\t" \
    "buffer_load_dwordx4 v253, s[60:63], s48 offen sc1 lds\n\t" \
    ".Lv114_%=:\n\t" \
    "s_cmp_eq_u32 s41, 0\n\t" \
    "s_cbranch_scc1 .Lv111_%=\n\t" \
    "s_sub_u32 s41, s41, 1\n\t" \
    "s_branch .Lv110_%=\n\t" \
    ".Lv111_%=:\n\t" \
    LST2 \
    LJIT \
    "s_waitcnt lgkmcnt(0)\n\t" \
    "s_barrier \n\t" \
    LST3 \
    LJIT \
    "ds_read_b128 v[236:239], v253 offset:40960\n\t" \
    "ds_read_b128 v[240:243], v253 offset:46080\n\t" \
    "v_smfmac_f32_16x16x64_bf16 v[244:247], v[224:227], v[0:7], v212\n\t" \
    "v_smfmac_f32_16x16x64_bf16 v[248:251], v[224:227], v[8:15], v212\n\t" \
    "v_smfmac_f32_16x16x64_bf16 v[208:211], v[224:227], v[200:207], v212\n\t" \
    "ds_read_b128 v[200:203], v254 offset:45056\n\t" \
    "ds_read_b128 v[204:207], v254 offset:46080\n\t" \
    "ds_read_b128 v[224:227], v253 offset:41984\n\t" \
    "v_smfmac_f32_16x16x64_bf16 v[208:211], v[228:231], v[16:23], v212\n\t" \
    "v_smfmac_f32_16x16x64_bf16 v[244:247], v[228:231], v[24:31], v212\n\t" \
    "v_smfmac_f32_16x16x64_bf16 v[248:251], v[228:231], v[32:39], v212\n\t" \
    "ds_read_b128 v[228:231], v253 offset:48128\n\t" \
    "v_smfmac_f32_16x16x64_bf16 v[208:211], v[232:235], v[216:223], v212\n\t" \
    "ds_read_b128 v[216:219], v254 offset:47104\n\t" \
    "ds_read_b128 v[220:223], v254 offset:48128\n\t" \
    "v_smfmac_f32_16x16x64_bf16 v[244:247], v[232:235], v[40:47], v212\n\t" \
    "v_smfmac_f32_16x16x64_bf16 v[248:251], v[232:235], v[48:55], v212\n\t" \
    "ds_read_b128 v[232:235], v253 offset:45056\n\t" \
    "s_waitcnt lgkmcnt(8)\n\t" \
    "v_smfmac_f32_16x16x64_bf16 v[208:211], v[236:239], v[56:63], v212\n\t" \
    "v_smfmac_f32_16x16x64_bf16 v[244:247], v[236:239], v[64:71], v212\n\t" \
    "v_smfmac_f32_16x16x64_bf16 v[248:251], v[236:239], v[72:79], v212\n\t" \
    "ds_read_b128 v[236:239], v253 offset:49152\n\t" \
    "s_waitcnt lgkmcnt(6)\n\t" \
    "v_smfmac_f32_16x16x64_bf16 v[208:211], v[240:243], v[200:207], v212\n\t" \
    "ds_read_b128 v[200:203], v254 offset:49152\n\t" \
    "ds_read_b128 v[204:207], v254 offset:50176\n\t" \
    "v_smfmac_f32_16x16x64_bf16 v[244:247], v[240:243], v[80:87], v212\n\t" \
    "v_smfmac_f32_16x16x64_bf16 v[248:251], v[240:243], v[88:95], v212\n\t" \
    "ds_read_b128 v[240:243], v253 offset:50176\n\t" \
    "s_waitcnt lgkmcnt(8)\n\t" \
    "v_smfmac_f32_16x16x64_bf16 v[208:211], v[224:227], v[96:103], v212\n\t" \
    "v_smfmac_f32_16x16x64_bf16 v[244:247], v[224:227], v[104:111], v212\n\t" \
    "v_smfmac_f32_16x16x64_bf16 v[248:251], v[224:227], v[112:119], v212\n\t" \
    "s_waitcnt lgkmcnt(7)\n\t" \
    "v_smfmac_f32_16x16x64_bf16 v[244:247], v[228:231], v[120:127], v212\n\t" \
    "s_waitcnt lgkmcnt(4)\n\t" \
    "v_smfmac_f32_16x16x64_bf16 v[244:247], v[232:235], v[144:151], v212\n\t" \
    "s_waitcnt lgkmcnt(3)\n\t" \
    "v_smfmac_f32_16x16x64_bf16 v[244:247], v[236:239], v[160:167], v212\n\t" \
    "s_waitcnt lgkmcnt(0)\n\t" \
    "v_smfmac_f32_16x16x64_bf16 v[244:247], v[240:243], v[184:191], v212\n\t" \
    "v_smfmac_f32_16x16x64_bf16 v[248:251], v[228:231], v[128:135], v212\n\t" \
    "v_smfmac_f32_16x16x64_bf16 v[248:251], v[232:235], v[152:159], v212\n\t" \
    "v_smfmac_f32_16x16x64_bf16 v[248:251], v[236:239], v[168:175], v212\n\t" \
    "v_add_f32 v244, v244, v246\n\t" \
    "v_add_f32 v245, v245, v247\n\t" \
    "v_cvt_pk_bf16_f32 v244, v244, v245\n\t" \
    "v_smfmac_f32_16x16x64_bf16 v[248:251], v[240:243], v[192:199], v212\n\t" \
    "ds_write_b16 v213, v244 offset:512\n\t" \
    "ds_write_b16_d16_hi v213, v244 offset:528\n\t" \
    "v_smfmac_f32_16x16x64_bf16 v[208:211], v[228:231], v[216:223], v212\n\t" \
    "v_smfmac_f32_16x16x64_bf16 v[208:211], v[232:235], v[136:143], v212\n\t" \
    "v_smfmac_f32_16x16x64_bf16 v[208:211], v[236:239], v[200:207], v212\n\t" \
    "v_add_f32 v248, v248, v250\n\t" \
    "v_add_f32 v249, v249, v251\n\t" \
    "v_cvt_pk_bf16_f32 v248, v248, v249\n\t" \
    "v_smfmac_f32_16x16x64_bf16 v[208:211], v[240:243], v[176:183], v212\n\t" \
    "ds_write_b16 v213, v248 offset:1024\n\t" \
    "ds_write_b16_d16_hi v213, v248 offset:1040\n\t" \
    "s_nop 5\n\t" \
    "v_add_f32 v208, v208, v210\n\t" \
    "v_add_f32 v209, v209, v211\n\t" \
    "v_cvt_pk_bf16_f32 v208, v208, v209\n\t" \
    "ds_write_b16 v213, v208 offset:0\n\t" \
    "ds_write_b16_d16_hi v213, v208 offset:16\n\t" \
    "s_setprio 0\n\t" \
    "s_waitcnt lgkmcnt(0)\n\t" \
    "s_barrier \n\t" \
    LST4 \
    LJIT \
    "ds_read_b128 v[224:227], v253 offset:4096\n\t" \
    "ds_read_b128 v[244:247], v254 offset:40960\n\t" \
    "ds_read_b128 v[228:231], v253 offset:5120\n\t" \
    "ds_read_b128 v[232:235], v253 offset:24576\n\t" \
    "ds_read_b128 v[236:239], v253 offset:25600\n\t" \
    "ds_read_b128 v[248:251], v254 offset:41984\n\t" \
    "s_add_u32 s40, s40, 1\n\t" \
    "s_cmp_lg_u32 s40, 64\n\t" \
    "s_cbranch_scc1 .Lv17_%=\n\t" \
    "s_branch .Lend%=\n\t" \
    ".Lv0_%=:\n\t" \
    "s_mov_b32 s42, %[xs]\n\t" \
    "s_add_u32 m0, %[wm], 0xc000\n\t" \
    "s_mov_b32 s44, 0x0\n\t" \
    "buffer_load_dwordx4 v240, %[wr], s44 offen offset:0 nt lds\n\t" \
    "buffer_load_dwordx4 v241, %[wr], s44 offen offset:1024 nt lds\n\t" \
    "buffer_load_dwordx4 v242, %[wr], s44 offen offset:2048 nt lds\n\t" \
    "s_add_u32 m0, %[wm], 0xf000\n\t" \
    "s_mov_b32 s44, 0x60000\n\t" \
    "buffer_load_dwordx4 v240, %[wr], s44 offen offset:0 nt lds\n\t" \
    "buffer_load_dwordx4 v241, %[wr], s44 offen offset:1024 nt lds\n\t" \
    "buffer_load_dwordx4 v242, %[wr], s44 offen offset:2048 nt lds\n\t" \
    "s_add_u32 m0, %[wm], 0x0\n\t" \
    "s_mov_b32 s44, 0xc0000\n\t" \
    "buffer_load_dwordx4 v240, %[wr], s44 offen offset:0 nt lds\n\t" \
    "buffer_load_dwordx4 v241, %[wr], s44 offen offset:1024 nt lds\n\t" \
    "buffer_load_dwordx4 v242, %[wr], s44 offen offset:2048 nt lds\n\t" \
    "s_add_u32 m0, %[wm], 0x3000\n\t" \
    "s_mov_b32 s44, 0x120000\n\t" \
    "buffer_load_dwordx4 v240, %[wr], s44 offen offset:0 nt lds\n\t" \
    "buffer_load_dwordx4 v241, %[wr], s44 offen offset:1024 nt lds\n\t" \
    "buffer_load_dwordx4 v242, %[wr], s44 offen offset:2048 nt lds\n\t" \
    "s_add_u32 m0, %[wm], 0x6000\n\t" \
    "s_mov_b32 s44, 0x180000\n\t" \
    "buffer_load_dwordx4 v240, %[wr], s44 offen offset:0 nt lds\n\t" \
    "buffer_load_dwordx4 v241, %[wr], s44 offen offset:1024 nt lds\n\t" \
    "buffer_load_dwordx4 v242, %[wr], s44 offen offset:2048 nt lds\n\t" \
    "v_add_u32 v244, 0xc000, v243\n\t" \
    "s_waitcnt vmcnt(12) lgkmcnt(0)\n\t" \
    "s_barrier \n\t" \
    "s_add_u32 m0, %[wm], 0x9000\n\t" \
    "s_mov_b32 s44, 0x1e0000\n\t" \
    "buffer_load_dwordx4 v240, %[wr], s44 offen offset:0 nt lds\n\t" \
    "buffer_load_dwordx4 v241, %[wr], s44 offen offset:1024 nt lds\n\t" \
    "buffer_load_dwordx4 v242, %[wr], s44 offen offset:2048 nt lds\n\t" \
    "ds_read_b64_tr_b16 v[200:201], v244 offset:0\n\t" \
    "ds_read_b64_tr_b16 v[202:203], v244 offset:1536\n\t" \
    "ds_read_b64_tr_b16 v[0:1], v244 offset:32\n\t" \
    "ds_read_b64_tr_b16 v[2:3], v244 offset:1568\n\t" \
    "ds_read_b64_tr_b16 v[8:9], v244 offset:64\n\t" \
    "ds_read_b64_tr_b16 v[10:11], v244 offset:1600\n\t" \
    "v_add_u32 v244, 0xf000, v243\n\t" \
    "s_waitcnt vmcnt(12) lgkmcnt(0)\n\t" \
    "s_barrier \n\t" \
    "s_add_u32 m0, %[wm], 0xc000\n\t" \
    "s_mov_b32 s44, 0x240000\n\t" \
    "buffer_load_dwordx4 v240, %[wr], s44 offen offset:0 nt lds\n\t" \
    "buffer_load_dwordx4 v241, %[wr], s44 offen offset:1024 nt lds\n\t" \
    "buffer_load_dwordx4 v242, %[wr], s44 offen offset:2048 nt lds\n\t" \
    "ds_read_b64_tr_b16 v[204:205], v244 offset:0\n\t" \
    "ds_read_b64_tr_b16 v[206:207], v244 offset:1536\n\t" \
    "ds_read_b64_tr_b16 v[4:5], v244 offset:32\n\t" \
    "ds_read_b64_tr_b16 v[6:7], v244 offset:1568\n\t" \
    "ds_read_b64_tr_b16 v[12:13], v244 offset:64\n\t" \
    "ds_read_b64_tr_b16 v[14:15], v244 offset:1600\n\t" \
    "v_add_u32 v244, 0x0, v243\n\t" \
    "s_waitcnt vmcnt(12) lgkmcnt(0)\n\t" \
    "s_barrier \n\t" \
    "s_add_u32 m0, %[wm], 0xf000\n\t" \
    "s_mov_b32 s44, 0x2a0000\n\t" \
    "buffer_load_dwordx4 v240, %[wr], s44 offen offset:0 nt lds\n\t" \
    "buffer_load_dwordx4 v241, %[wr], s44 offen offset:1024 nt lds\n\t" \
    "buffer_load_dwordx4 v242, %[wr], s44 offen offset:2048 nt lds\n\t" \
    "ds_read_b64_tr_b16 v[208:209], v244 offset:0\n\t" \
    "ds_read_b64_tr_b16 v[210:211], v244 offset:1536\n\t" \
    "ds_read_b64_tr_b16 v[40:41], v244 offset:32\n\t" \
    "ds_read_b64_tr_b16 v[42:43], v244 offset:1568\n\t" \
    "ds_read_b64_tr_b16 v[48:49], v244 offset:64\n\t" \
    "ds_read_b64_tr_b16 v[50:51], v244 offset:1600\n\t" \
    "v_add_u32 v244, 0x3000, v243\n\t" \
    "s_waitcnt vmcnt(12) lgkmcnt(0)\n\t" \
    "s_barrier \n\t" \
    "s_add_u32 m0, %[wm], 0x0\n\t" \
    "s_mov_b32 s44, 0x300000\n\t" \
    "buffer_load_dwordx4 v240, %[wr], s44 offen offset:0 nt lds\n\t" \
    "buffer_load_dwordx4 v241, %[wr], s44 offen offset:1024 nt lds\n\t" \
    "buffer_load_dwordx4 v242, %[wr], s44 offen offset:2048 nt lds\n\t" \
    "ds_read_b64_tr_b16 v[212:213], v244 offset:0\n\t" \
    "ds_read_b64_tr_b16 v[214:215], v244 offset:1536\n\t" \
    "ds_read_b64_tr_b16 v[44:45], v244 offset:32\n\t" \
    "ds_read_b64_tr_b16 v[46:47], v244 offset:1568\n\t" \
    "ds_read_b64_tr_b16 v[52:53], v244 offset:64\n\t" \
    "ds_read_b64_tr_b16 v[54:55], v244 offset:1600\n\t" \
    "v_add_u32 v244, 0x6000, v243\n\t" \
    "s_waitcnt vmcnt(12) lgkmcnt(0)\n\t" \
    "s_barrier \n\t" \
    "s_add_u32 m0, %[wm], 0x3000\n\t" \
    "s_mov_b32 s44, 0x360000\n\t" \
    "buffer_load_dwordx4 v240, %[wr], s44 offen offset:0 nt lds\n\t" \
    "buffer_load_dwordx4 v241, %[wr], s44 offen offset:1024 nt lds\n\t" \
    "buffer_load_dwordx4 v242, %[wr], s44 offen offset:2048 nt lds\n\t" \
    "ds_read_b64_tr_b16 v[216:217], v244 offset:0\n\t" \
    "ds_read_b64_tr_b16 v[218:219], v244 offset:1536\n\t" \
    "ds_read_b64_tr_b16 v[80:81], v244 offset:32\n\t" \
    "ds_read_b64_tr_b16 v[82:83], v244 offset:1568\n\t" \
    "ds_read_b64_tr_b16 v[88:89], v244 offset:64\n\t" \
    "ds_read_b64_tr_b16 v[90:91], v244 offset:1600\n\t" \
    "v_add_u32 v244, 0x9000, v243\n\t" \
    "s_waitcnt vmcnt(12) lgkmcnt(0)\n\t" \
    "s_barrier \n\t" \
    "s_add_u32 m0, %[wm], 0x6000\n\t" \
    "s_mov_b32 s44, 0x3c0000\n\t" \
    "buffer_load_dwordx4 v240, %[wr], s44 offen offset:0 nt lds\n\t" \
    "buffer_load_dwordx4 v241, %[wr], s44 offen offset:1024 nt lds\n\t" \
    "buffer_load_dwordx4 v242, %[wr], s44 offen offset:2048 nt lds\n\t" \
    "ds_read_b64_tr_b16 v[220:221], v244 offset:0\n\t" \
    "ds_read_b64_tr_b16 v[222:223], v244 offset:1536\n\t" \
    "ds_read_b64_tr_b16 v[84:85], v244 offset:32\n\t" \
    "ds_read_b64_tr_b16 v[86:87], v244 offset:1568\n\t" \
    "ds_read_b64_tr_b16 v[92:93], v244 offset:64\n\t" \
    "ds_read_b64_tr_b16 v[94:95], v244 offset:1600\n\t" \
    "v_add_u32 v244, 0xc000, v243\n\t" \
    "s_waitcnt vmcnt(12) lgkmcnt(0)\n\t" \
    "s_barrier \n\t" \
    "v_mov_b32 v246, 0\n\t" \
    "buffer_load_dword v246, v246, s[60:63], %[mo] offen sc1\n\t" \
    "s_add_u32 m0, %[wm], 0x9000\n\t" \
    "s_mov_b32 s44, 0x420000\n\t" \
    "buffer_load_dwordx4 v240, %[wr], s44 offen offset:0 nt lds\n\t" \
    "buffer_load_dwordx4 v241, %[wr], s44 offen offset:1024 nt lds\n\t" \
    "buffer_load_dwordx4 v242, %[wr], s44 offen offset:2048 nt lds\n\t" \
    "ds_read_b64_tr_b16 v[224:225], v244 offset:0\n\t" \
    "ds_read_b64_tr_b16 v[226:227], v244 offset:1536\n\t" \
    "ds_read_b64_tr_b16 v[120:121], v244 offset:32\n\t" \
    "ds_read_b64_tr_b16 v[122:123], v244 offset:1568\n\t" \
    "ds_read_b64_tr_b16 v[128:129], v244 offset:64\n\t" \
    "ds_read_b64_tr_b16 v[130:131], v244 offset:1600\n\t" \
    "v_add_u32 v244, 0xf000, v243\n\t" \
    "s_waitcnt vmcnt(12) lgkmcnt(0)\n\t" \
    "s_barrier \n\t" \
    "s_add_u32 m0, %[wm], 0xc000\n\t" \
    "s_mov_b32 s44, 0x480000\n\t" \
    "buffer_load_dwordx4 v240, %[wr], s44 offen offset:0 nt lds\n\t" \
    "buffer_load_dwordx4 v241, %[wr], s44 offen offset:1024 nt lds\n\t" \
    "buffer_load_dwordx4 v242, %[wr], s44 offen offset:2048 nt lds\n\t" \
    "ds_read_b64_tr_b16 v[228:229], v244 offset:0\n\t" \
    "ds_read_b64_tr_b16 v[230:231], v244 offset:1536\n\t" \
    "ds_read_b64_tr_b16 v[124:125], v244 offset:32\n\t" \
    "ds_read_b64_tr_b16 v[126:127], v244 offset:1568\n\t" \
    "ds_read_b64_tr_b16 v[132:133], v244 offset:64\n\t" \
    "ds_read_b64_tr_b16 v[134:135], v244 offset:1600\n\t" \
    "v_add_u32 v244, 0x0, v243\n\t" \
    "s_waitcnt vmcnt(12) lgkmcnt(0)\n\t" \
    "s_barrier \n\t" \
    "s_add_u32 m0, %[wm], 0xf000\n\t" \
    "s_mov_b32 s44, 0x4e0000\n\t" \
    "buffer_load_dwordx4 v240, %[wr], s44 offen offset:0 nt lds\n\t" \
    "buffer_load_dwordx4 v241, %[wr], s44 offen offset:1024 nt lds\n\t" \
    "buffer_load_dwordx4 v242, %[wr], s44 offen offset:2048 nt lds\n\t" \
    "ds_read_b64_tr_b16 v[232:233], v244 offset:0\n\t" \
    "ds_read_b64_tr_b16 v[234:235], v244 offset:1536\n\t" \
    "ds_read_b64_tr_b16 v[160:161], v244 offset:32\n\t" \
    "ds_read_b64_tr_b16 v[162:163], v244 offset:1568\n\t" \
    "ds_read_b64_tr_b16 v[168:169], v244 offset:64\n\t" \
    "ds_read_b64_tr_b16 v[170:171], v244 offset:1600\n\t" \
    "v_add_u32 v244, 0x3000, v243\n\t" \
    "s_waitcnt vmcnt(12) lgkmcnt(0)\n\t" \
    "s_barrier \n\t" \
    "s_add_u32 m0, %[wm], 0x0\n\t" \
    "s_mov_b32 s44, 0x540000\n\t" \
    "buffer_load_dwordx4 v240, %[wr], s44 offen offset:0 nt lds\n\t" \
    "buffer_load_dwordx4 v241, %[wr], s44 offen offset:1024 nt lds\n\t" \
    "buffer_load_dwordx4 v242, %[wr], s44 offen offset:2048 nt lds\n\t" \
    "ds_read_b64_tr_b16 v[236:237], v244 offset:0\n\t" \
    "ds_read_b64_tr_b16 v[238:239], v244 offset:1536\n\t" \
    "ds_read_b64_tr_b16 v[164:165], v244 offset:32\n\t" \
    "ds_read_b64_tr_b16 v[166:167], v244 offset:1568\n\t" \
    "ds_read_b64_tr_b16 v[172:173], v244 offset:64\n\t" \
    "ds_read_b64_tr_b16 v[174:175], v244 offset:1600\n\t" \
    "v_add_u32 v244, 0x6000, v243\n\t" \
    "s_waitcnt vmcnt(12) lgkmcnt(0)\n\t" \
    "s_barrier \n\t" \
    "s_add_u32 m0, %[wm], 0x3000\n\t" \
    "s_mov_b32 s44, 0x5a0000\n\t" \
    "buffer_load_dwordx4 v240, %[wr], s44 offen offset:0 nt lds\n\t" \
    "buffer_load_dwordx4 v241, %[wr], s44 offen offset:1024 nt lds\n\t" \
    "buffer_load_dwordx4 v242, %[wr], s44 offen offset:2048 nt lds\n\t" \
    "ds_read_b64_tr_b16 v[16:17], v244 offset:0\n\t" \
    "ds_read_b64_tr_b16 v[18:19], v244 offset:1536\n\t" \
    "ds_read_b64_tr_b16 v[24:25], v244 offset:32\n\t" \
    "ds_read_b64_tr_b16 v[26:27], v244 offset:1568\n\t" \
    "ds_read_b64_tr_b16 v[32:33], v244 offset:64\n\t" \
    "ds_read_b64_tr_b16 v[34:35], v244 offset:1600\n\t" \
    "v_add_u32 v244, 0x9000, v243\n\t" \
    "s_waitcnt vmcnt(12) lgkmcnt(0)\n\t" \
    "s_barrier \n\t" \
    ".Lv01_%=:\n\t" \
    "v_readfirstlane_b32 s43, v246\n\t" \
    "s_nop 0\n\t" \
    "s_cmp_lg_u32 s43, -1\n\t" \
    "s_cbranch_scc1 .Lv02_%=\n\t" \
    "s_cmp_eq_u32 s41, 0\n\t" \
    "s_cbranch_scc1 .Lv02_%=\n\t" \
    "s_sub_u32 s41, s41, 1\n\t" \
    "s_sleep 4\n\t" \
    "v_mov_b32 v246, 0\n\t" \
    "buffer_load_dword v246, v246, s[60:63], %[mo] offen sc1\n\t" \
    "s_waitcnt vmcnt(0)\n\t" \
    "s_branch .Lv01_%=\n\t" \
    ".Lv02_%=:\n\t" \
    "v_mov_b32 v246, 0x8000\n\t" \
    "s_mov_b64 exec, 1\n\t" \
    "buffer_store_dword v246, v246, s[60:63], %[mo] offen sc1\n\t" \
    "s_mov_b64 exec, -1\n\t" \
    "s_and_b32 s43, s43, 0x2000\n\t" \
    "s_xor_b32 s43, s43, %[lx]\n\t" \
    "s_add_u32 s60, s60, s43\n\t" \
    "s_addc_u32 s61, s61, 0\n\t" \
    "v_and_b32 v252, 0xf0, v253\n\t" \
    "v_lshlrev_b32 v252, 3, v252\n\t" \
    "buffer_load_dword v252, v252, s[60:63], %[fl] offen sc1\n\t" \
    "s_add_u32 m0, %[wm], 0x6000\n\t" \
    "s_mov_b32 s44, 0x600000\n\t" \
    "buffer_load_dwordx4 v240, %[wr], s44 offen offset:0 nt lds\n\t" \
    "buffer_load_dwordx4 v241, %[wr], s44 offen offset:1024 nt lds\n\t" \
    "buffer_load_dwordx4 v242, %[wr], s44 offen offset:2048 nt lds\n\t" \
    "ds_read_b64_tr_b16 v[20:21], v244 offset:0\n\t" \
    "ds_read_b64_tr_b16 v[22:23], v244 offset:1536\n\t" \
    "ds_read_b64_tr_b16 v[28:29], v244 offset:32\n\t" \
    "ds_read_b64_tr_b16 v[30:31], v244 offset:1568\n\t" \
    "ds_read_b64_tr_b16 v[36:37], v244 offset:64\n\t" \
    "ds_read_b64_tr_b16 v[38:39], v244 offset:1600\n\t" \
    "v_add_u32 v244, 0xc000, v243\n\t" \
    "s_waitcnt vmcnt(12) lgkmcnt(0)\n\t" \
    "s_barrier \n\t" \
    "s_add_u32 m0, %[wm], 0x9000\n\t" \
    "s_mov_b32 s44, 0x660000\n\t" \
    "buffer_load_dwordx4 v240, %[wr], s44 offen offset:0 nt lds\n\t" \
    "buffer_load_dwordx4 v241, %[wr], s44 offen offset:1024 nt lds\n\t" \
    "buffer_load_dwordx4 v242, %[wr], s44 offen offset:2048 nt lds\n\t" \
    "ds_read_b64_tr_b16 v[56:57], v244 offset:0\n\t" \
    "ds_read_b64_tr_b16 v[58:59], v244 offset:1536\n\t" \
    "ds_read_b64_tr_b16 v[64:65], v244 offset:32\n\t" \
    "ds_read_b64_tr_b16 v[66:67], v244 offset:1568\n\t" \
    "ds_read_b64_tr_b16 v[72:73], v244 offset:64\n\t" \
    "ds_read_b64_tr_b16 v[74:75], v244 offset:1600\n\t" \
    "v_add_u32 v244, 0xf000, v243\n\t" \
    "s_waitcnt vmcnt(12) lgkmcnt(0)\n\t" \
    "s_barrier \n\t" \
    "s_add_u32 m0, %[wm], 0xc000\n\t" \
    "s_mov_b32 s44, 0x6c0000\n\t" \
    "buffer_load_dwordx4 v240, %[wr], s44 offen offset:0 nt lds\n\t" \
    "buffer_load_dwordx4 v241, %[wr], s44 offen offset:1024 nt lds\n\t" \
    "buffer_load_dwordx4 v242, %[wr], s44 offen offset:2048 nt lds\n\t" \
    "ds_read_b64_tr_b16 v[60:61], v244 offset:0\n\t" \
    "ds_read_b64_tr_b16 v[62:63], v244 offset:1536\n\t" \
    "ds_read_b64_tr_b16 v[68:69], v244 offset:32\n\t" \
    "ds_read_b64_tr_b16 v[70:71], v244 offset:1568\n\t" \
    "ds_read_b64_tr_b16 v[76:77], v244 offset:64\n\t" \
    "ds_read_b64_tr_b16 v[78:79], v244 offset:1600\n\t" \
    "v_add_u32 v244, 0x0, v243\n\t" \
    "s_waitcnt vmcnt(12) lgkmcnt(0)\n\t" \
    "s_barrier \n\t" \
    "s_add_u32 m0, %[wm], 0xf000\n\t" \
    "s_mov_b32 s44, 0x720000\n\t" \
    "buffer_load_dwordx4 v240, %[wr], s44 offen offset:0 nt lds\n\t" \
    "buffer_load_dwordx4 v241, %[wr], s44 offen offset:1024 nt lds\n\t" \
    "buffer_load_dwordx4 v242, %[wr], s44 offen offset:2048 nt lds\n\t" \
    "ds_read_b64_tr_b16 v[96:97], v244 offset:0\n\t" \
    "ds_read_b64_tr_b16 v[98:99], v244 offset:1536\n\t" \
    "ds_read_b64_tr_b16 v[104:105], v244 offset:32\n\t" \
    "ds_read_b64_tr_b16 v[106:107], v244 offset:1568\n\t" \
    "ds_read_b64_tr_b16 v[112:113], v244 offset:64\n\t" \
    "ds_read_b64_tr_b16 v[114:115], v244 offset:1600\n\t" \
    "v_add_u32 v244, 0x3000, v243\n\t" \
    "s_waitcnt vmcnt(12) lgkmcnt(0)\n\t" \
    "s_barrier \n\t" \
    "ds_read_b64_tr_b16 v[100:101], v244 offset:0\n\t" \
    "ds_read_b64_tr_b16 v[102:103], v244 offset:1536\n\t" \
    "ds_read_b64_tr_b16 v[108:109], v244 offset:32\n\t" \
    "ds_read_b64_tr_b16 v[110:111], v244 offset:1568\n\t" \
    "ds_read_b64_tr_b16 v[116:117], v244 offset:64\n\t" \
    "ds_read_b64_tr_b16 v[118:119], v244 offset:1600\n\t" \
    "v_add_u32 v244, 0x6000, v243\n\t" \
    "s_waitcnt vmcnt(9) lgkmcnt(0)\n\t" \
    "s_barrier \n\t" \
    "ds_read_b64_tr_b16 v[136:137], v244 offset:0\n\t" \
    "ds_read_b64_tr_b16 v[138:139], v244 offset:1536\n\t" \
    "ds_read_b64_tr_b16 v[144:145], v244 offset:32\n\t" \
    "ds_read_b64_tr_b16 v[146:147], v244 offset:1568\n\t" \
    "ds_read_b64_tr_b16 v[152:153], v244 offset:64\n\t" \
    "ds_read_b64_tr_b16 v[154:155], v244 offset:1600\n\t" \
    "v_add_u32 v244, 0x9000, v243\n\t" \
    "s_waitcnt vmcnt(6) lgkmcnt(0)\n\t" \
    "s_barrier \n\t" \
    "ds_read_b64_tr_b16 v[140:141], v244 offset:0\n\t" \
    "ds_read_b64_tr_b16 v[142:143], v244 offset:1536\n\t" \
    "ds_read_b64_tr_b16 v[148:149], v244 offset:32\n\t" \
    "ds_read_b64_tr_b16 v[150:151], v244 offset:1568\n\t" \
    "ds_read_b64_tr_b16 v[156:157], v244 offset:64\n\t" \
    "ds_read_b64_tr_b16 v[158:159], v244 offset:1600\n\t" \
    "v_add_u32 v244, 0xc000, v243\n\t" \
    "s_waitcnt vmcnt(3) lgkmcnt(0)\n\t" \
    "s_barrier \n\t" \
    "ds_read_b64_tr_b16 v[176:177], v244 offset:0\n\t" \
    "ds_read_b64_tr_b16 v[178:179], v244 offset:1536\n\t" \
    "ds_read_b64_tr_b16 v[184:185], v244 offset:32\n\t" \
    "ds_read_b64_tr_b16 v[186:187], v244 offset:1568\n\t" \
    "ds_read_b64_tr_b16 v[192:193], v244 offset:64\n\t" \
    "ds_read_b64_tr_b16 v[194:195], v244 offset:1600\n\t" \
    "s_mov_b32 s43, 0\n\t" \
    "v_cmp_eq_u32 vcc, 0, v252\n\t" \
    "s_cbranch_vccnz .Lv03_%=\n\t" \
    "s_mov_b32 m0, 0x0\n\t" \
    "s_add_u32 s49, s42, 0x0\n\t" \
    "buffer_load_dwordx4 v253, s[60:63], s49 offen offset:0 sc1 lds\n\t" \
    "buffer_load_dwordx4 v253, s[60:63], s49 offen offset:1024 sc1 lds\n\t" \
    "buffer_load_dwordx4 v253, s[60:63], s49 offen offset:2048 sc1 lds\n\t" \
    "buffer_load_dwordx4 v253, s[60:63], s49 offen offset:3072 sc1 lds\n\t" \
    "s_mov_b32 m0, 0x4000\n\t" \
    "s_add_u32 s49, s42, 0x10000\n\t" \
    "buffer_load_dwordx4 v253, s[60:63], s49 offen offset:0 sc1 lds\n\t" \
    "s_mov_b32 m0, 0x5000\n\t" \
    "s_add_u32 s49, s42, 0x1000\n\t" \
    "buffer_load_dwordx4 v253, s[60:63], s49 offen offset:0 sc1 lds\n\t" \
    "buffer_load_dwordx4 v253, s[60:63], s49 offen offset:1024 sc1 lds\n\t" \
    "buffer_load_dwordx4 v253, s[60:63], s49 offen offset:2048 sc1 lds\n\t" \
    "buffer_load_dwordx4 v253, s[60:63], s49 offen offset:3072 sc1 lds\n\t" \
    "s_mov_b32 m0, 0x9000\n\t" \
    "s_add_u32 s49, s42, 0x11000\n\t" \
    "buffer_load_dwordx4 v253, s[60:63], s49 offen offset:0 sc1 lds\n\t" \
    "s_mov_b32 s43, 1\n\t" \
    ".Lv03_%=:\n\t" \
    "v_add_u32 v244, 0xf000, v243\n\t" \
    "s_waitcnt vmcnt(0) lgkmcnt(0)\n\t" \
    "s_barrier \n\t" \
    "ds_read_b64_tr_b16 v[180:181], v244 offset:0\n\t" \
    "ds_read_b64_tr_b16 v[182:183], v244 offset:1536\n\t" \
    "ds_read_b64_tr_b16 v[188:189], v244 offset:32\n\t" \
    "ds_read_b64_tr_b16 v[190:191], v244 offset:1568\n\t" \
    "ds_read_b64_tr_b16 v[196:197], v244 offset:64\n\t" \
    "ds_read_b64_tr_b16 v[198:199], v244 offset:1600\n\t" \
    "s_waitcnt lgkmcnt(0)\n\t" \
    "s_barrier \n\t" \
    LST0 \
    LJIT \
    "ds_write_b128 v254, v[200:203] offset:30720\n\t" \
    "ds_write_b128 v254, v[204:207] offset:31744\n\t" \
    "ds_write_b128 v254, v[208:211] offset:32768\n\t" \
    "ds_write_b128 v254, v[212:215] offset:33792\n\t" \
    "ds_write_b128 v254, v[216:219] offset:34816\n\t" \
    "ds_write_b128 v254, v[220:223] offset:35840\n\t" \
    "ds_write_b128 v254, v[224:227] offset:36864\n\t" \
    "ds_write_b128 v254, v[228:231] offset:37888\n\t" \
    "ds_write_b128 v254, v[232:235] offset:38912\n\t" \
    "ds_write_b128 v254, v[236:239] offset:39936\n\t" \
    "s_cmp_lg_u32 s43, 0\n\t" \
    "s_cbranch_scc1 .Lv04_%=\n\t" \
    "s_mov_b32 s44, 0\n\t" \
    ".Lv05_%=:\n\t" \
    "v_cmp_ge_u32 vcc, s44, v252\n\t" \
    "s_cbranch_vccz .Lv06_%=\n\t" \
    "s_cmp_eq_u32 s41, 0\n\t" \
    "s_cbranch_scc1 .Lv06_%=\n\t" \
    "s_sub_u32 s41, s41, 1\n\t" \
    "s_sleep 8\n\t" \
    "v_and_b32 v252, 0xf0, v253\n\t" \
    "v_lshlrev_b32 v252, 3, v252\n\t" \
    "buffer_load_dword v252, v252, s[60:63], %[fl] offen sc1\n\t" \
    "s_waitcnt vmcnt(0)\n\t" \
    "s_branch .Lv05_%=\n\t" \
    ".Lv06_%=:\n\t" \
    "s_mov_b32 m0, 0x0\n\t" \
    "s_add_u32 s49, s42, 0x0\n\t" \
    "buffer_load_dwordx4 v253, s[60:63], s49 offen offset:0 sc1 lds\n\t" \
    "buffer_load_dwordx4 v253, s[60:63], s49 offen offset:1024 sc1 lds\n\t" \
    "buffer_load_dwordx4 v253, s[60:63], s49 offen offset:2048 sc1 lds\n\t" \
    "buffer_load_dwordx4 v253, s[60:63], s49 offen offset:3072 sc1 lds\n\t" \
    "s_mov_b32 m0, 0x4000\n\t" \
    "s_add_u32 s49, s42, 0x10000\n\t" \
    "buffer_load_dwordx4 v253, s[60:63], s49 offen offset:0 sc1 lds\n\t" \
    "s_mov_b32 m0, 0x5000\n\t" \
    "s_add_u32 s49, s42, 0x1000\n\t" \
    "buffer_load_dwordx4 v253, s[60:63], s49 offen offset:0 sc1 lds\n\t" \
    "buffer_load_dwordx4 v253, s[60:63], s49 offen offset:1024 sc1 lds\n\t" \
    "buffer_load_dwordx4 v253, s[60:63], s49 offen offset:2048 sc1 lds\n\t" \
    "buffer_load_dwordx4 v253, s[60:63], s49 offen offset:3072 sc1 lds\n\t" \
    "s_mov_b32 m0, 0x9000\n\t" \
    "s_add_u32 s49, s42, 0x11000\n\t" \
    "buffer_load_dwordx4 v253, s[60:63], s49 offen offset:0 sc1 lds\n\t" \
    ".Lv04_%=:\n\t" \
    "s_waitcnt vmcnt(0) lgkmcnt(0)\n\t" \
    "s_barrier \n\t" \
    "ds_read_b128 v[224:227], v253 offset:0\n\t" \
    "ds_read_b128 v[244:247], v254 offset:30720\n\t" \
    "ds_read_b128 v[228:231], v253 offset:1024\n\t" \
    "ds_read_b128 v[232:235], v253 offset:20480\n\t" \
    "ds_read_b128 v[236:239], v253 offset:21504\n\t" \
    "ds_read_b128 v[248:251], v254 offset:31744\n\t" \
    ".Lv07_%=:\n\t" \
    LST7 \
    LJIT \
    "s_waitcnt lgkmcnt(5)\n\t" \
    "v_mfma_f32_16x16x32_bf16 v[204:207], v[0:3], v[224:227], 0\n\t" \
    "s_waitcnt lgkmcnt(3)\n\t" \
    "v_mfma_f32_16x16x32_bf16 v[204:207], v[4:7], v[228:231], v[204:207]\n\t" \
    "ds_read_b128 v[240:243], v253 offset:10240\n\t" \
    "v_mfma_f32_16x16x32_bf16 v[208:211], v[8:11], v[224:227], 0\n\t" \
    "v_mfma_f32_16x16x32_bf16 v[208:211], v[12:15], v[228:231], v[208:211]\n\t" \
    "v_mfma_f32_16x16x32_bf16 v[200:203], v[244:247], v[224:227], 0\n\t" \
    "s_waitcnt lgkmcnt(1)\n\t" \
    "v_mfma_f32_16x16x32_bf16 v[200:203], v[248:251], v[228:231], v[200:203]\n\t" \
    "v_mfma_f32_16x16x32_bf16 v[216:219], v[0:3], v[232:235], 0\n\t" \
    "ds_read_b128 v[224:227], v253 offset:11264\n\t" \
    "v_mfma_f32_16x16x32_bf16 v[216:219], v[4:7], v[236:239], v[216:219]\n\t" \
    "ds_read_b128 v[228:231], v253 offset:2048\n\t" \
    "v_mfma_f32_16x16x32_bf16 v[220:223], v[8:11], v[232:235], 0\n\t" \
    "v_mfma_f32_16x16x32_bf16 v[220:223], v[12:15], v[236:239], v[220:223]\n\t" \
    "v_mfma_f32_16x16x32_bf16 v[212:215], v[244:247], v[232:235], 0\n\t" \
    "ds_read_b128 v[244:247], v254 offset:32768\n\t" \
    "v_mfma_f32_16x16x32_bf16 v[212:215], v[248:251], v[236:239], v[212:215]\n\t" \
    "ds_read_b128 v[248:251], v254 offset:33792\n\t" \
    "s_waitcnt lgkmcnt(4)\n\t" \
    "v_mfma_f32_16x16x32_bf16 v[204:207], v[24:27], v[240:243], v[204:207]\n\t" \
    "ds_read_b128 v[232:235], v253 offset:3072\n\t" \
    "s_waitcnt lgkmcnt(4)\n\t" \
    "v_mfma_f32_16x16x32_bf16 v[204:207], v[28:31], v[224:227], v[204:207]\n\t" \
    "ds_read_b128 v[236:239], v253 offset:22528\n\t" \
    "v_mfma_f32_16x16x32_bf16 v[208:211], v[32:35], v[240:243], v[208:211]\n\t" \
    "v_mfma_f32_16x16x32_bf16 v[208:211], v[36:39], v[224:227], v[208:211]\n\t" \
    "v_mfma_f32_16x16x32_bf16 v[200:203], v[16:19], v[240:243], v[200:203]\n\t" \
    "v_mfma_f32_16x16x32_bf16 v[200:203], v[20:23], v[224:227], v[200:203]\n\t" \
    "s_waitcnt lgkmcnt(4)\n\t" \
    "v_mfma_f32_16x16x32_bf16 v[204:207], v[40:43], v[228:231], v[204:207]\n\t" \
    "ds_read_b128 v[240:243], v253 offset:23552\n\t" \
    "s_waitcnt lgkmcnt(2)\n\t" \
    "v_mfma_f32_16x16x32_bf16 v[204:207], v[44:47], v[232:235], v[204:207]\n\t" \
    "ds_read_b128 v[224:227], v253 offset:12288\n\t" \
    "v_mfma_f32_16x16x32_bf16 v[208:211], v[48:51], v[228:231], v[208:211]\n\t" \
    "v_mfma_f32_16x16x32_bf16 v[208:211], v[52:55], v[232:235], v[208:211]\n\t" \
    "v_mfma_f32_16x16x32_bf16 v[200:203], v[244:247], v[228:231], v[200:203]\n\t" \
    "v_mfma_f32_16x16x32_bf16 v[200:203], v[248:251], v[232:235], v[200:203]\n\t" \
    "s_waitcnt lgkmcnt(2)\n\t" \
    "v_mfma_f32_16x16x32_bf16 v[216:219], v[40:43], v[236:239], v[216:219]\n\t" \
    "ds_read_b128 v[228:231], v253 offset:13312\n\t" \
    "s_add_u32 s44, s40, 1\n\t" \
    "s_waitcnt lgkmcnt(2)\n\t" \
    "v_mfma_f32_16x16x32_bf16 v[216:219], v[44:47], v[240:243], v[216:219]\n\t" \
    "ds_read_b128 v[232:235], v253 offset:4096\n\t" \
    "s_and_b32 s45, s44, 15\n\t" \
    "v_mfma_f32_16x16x32_bf16 v[220:223], v[48:51], v[236:239], v[220:223]\n\t" \
    "s_cmp_eq_u32 s45, 0\n\t" \
    "v_mfma_f32_16x16x32_bf16 v[220:223], v[52:55], v[240:243], v[220:223]\n\t" \
    "s_cselect_b32 s45, %[cj], %[cs]\n\t" \
    "v_mfma_f32_16x16x32_bf16 v[212:215], v[244:247], v[236:239], v[212:215]\n\t" \
    "ds_read_b128 v[244:247], v254 offset:34816\n\t" \
    "s_cmp_eq_u32 s44, 64\n\t" \
    "v_mfma_f32_16x16x32_bf16 v[212:215], v[248:251], v[240:243], v[212:215]\n\t" \
    "ds_read_b128 v[248:251], v254 offset:35840\n\t" \
    "s_cselect_b32 s45, 0, s45\n\t" \
    "s_waitcnt lgkmcnt(4)\n\t" \
    "v_mfma_f32_16x16x32_bf16 v[204:207], v[64:67], v[224:227], v[204:207]\n\t" \
    "ds_read_b128 v[236:239], v253 offset:5120\n\t" \
    "s_add_u32 s42, s42, s45\n\t" \
    "s_waitcnt lgkmcnt(4)\n\t" \
    "v_mfma_f32_16x16x32_bf16 v[204:207], v[68:71], v[228:231], v[204:207]\n\t" \
    "ds_read_b128 v[240:243], v253 offset:24576\n\t" \
    "v_mfma_f32_16x16x32_bf16 v[208:211], v[72:75], v[224:227], v[208:211]\n\t" \
    "v_mfma_f32_16x16x32_bf16 v[208:211], v[76:79], v[228:231], v[208:211]\n\t" \
    "v_mfma_f32_16x16x32_bf16 v[200:203], v[56:59], v[224:227], v[200:203]\n\t" \
    "v_mfma_f32_16x16x32_bf16 v[200:203], v[60:63], v[228:231], v[200:203]\n\t" \
    "s_waitcnt lgkmcnt(4)\n\t" \
    "v_mfma_f32_16x16x32_bf16 v[204:207], v[80:83], v[232:235], v[204:207]\n\t" \
    "ds_read_b128 v[224:227], v253 offset:25600\n\t" \
    "s_waitcnt lgkmcnt(2)\n\t" \
    "v_mfma_f32_16x16x32_bf16 v[204:207], v[84:87], v[236:239], v[204:207]\n\t" \
    "ds_read_b128 v[228:231], v253 offset:14336\n\t" \
    "v_mfma_f32_16x16x32_bf16 v[208:211], v[88:91], v[232:235], v[208:211]\n\t" \
    "v_mfma_f32_16x16x32_bf16 v[208:211], v[92:95], v[236:239], v[208:211]\n\t" \
    "v_mfma_f32_16x16x32_bf16 v[200:203], v[244:247], v[232:235], v[200:203]\n\t" \
    "v_mfma_f32_16x16x32_bf16 v[200:203], v[248:251], v[236:239], v[200:203]\n\t" \
    "s_waitcnt lgkmcnt(2)\n\t" \
    "v_mfma_f32_16x16x32_bf16 v[216:219], v[80:83], v[240:243], v[216:219]\n\t" \
    "ds_read_b128 v[232:235], v253 offset:15360\n\t" \
    "s_and_b32 s44, s40, 3\n\t" \
    "s_waitcnt lgkmcnt(2)\n\t" \
    "v_mfma_f32_16x16x32_bf16 v[216:219], v[84:87], v[224:227], v[216:219]\n\t" \
    "ds_read_b128 v[236:239], v253 offset:6144\n\t" \
    "s_lshl_b32 s44, s44, 17\n\t" \
    "v_mfma_f32_16x16x32_bf16 v[220:223], v[88:91], v[240:243], v[220:223]\n\t" \
    "s_add_u32 s44, s44, %[mb]\n\t" \
    "v_mfma_f32_16x16x32_bf16 v[220:223], v[92:95], v[224:227], v[220:223]\n\t" \
    "s_add_u32 s46, s44, 0x0\n\t" \
    "v_mfma_f32_16x16x32_bf16 v[212:215], v[244:247], v[240:243], v[212:215]\n\t" \
    "ds_read_b128 v[244:247], v254 offset:36864\n\t" \
    "s_add_u32 s47, s44, 0x1400\n\t" \
    "v_mfma_f32_16x16x32_bf16 v[212:215], v[248:251], v[224:227], v[212:215]\n\t" \
    "ds_read_b128 v[248:251], v254 offset:37888\n\t" \
    "s_add_u32 s48, s44, 0x400\n\t" \
    "s_waitcnt lgkmcnt(4)\n\t" \
    "v_mfma_f32_16x16x32_bf16 v[204:207], v[104:107], v[228:231], v[204:207]\n\t" \
    "ds_read_b128 v[240:243], v253 offset:7168\n\t" \
    "s_waitcnt lgkmcnt(4)\n\t" \
    "v_mfma_f32_16x16x32_bf16 v[204:207], v[108:111], v[232:235], v[204:207]\n\t" \
    "ds_read_b128 v[224:227], v253 offset:26624\n\t" \
    "v_mfma_f32_16x16x32_bf16 v[208:211], v[112:115], v[228:231], v[208:211]\n\t" \
    "v_mfma_f32_16x16x32_bf16 v[208:211], v[116:119], v[232:235], v[208:211]\n\t" \
    "v_mfma_f32_16x16x32_bf16 v[200:203], v[96:99], v[228:231], v[200:203]\n\t" \
    "v_mfma_f32_16x16x32_bf16 v[200:203], v[100:103], v[232:235], v[200:203]\n\t" \
    "s_waitcnt lgkmcnt(4)\n\t" \
    "v_mfma_f32_16x16x32_bf16 v[204:207], v[120:123], v[236:239], v[204:207]\n\t" \
    "ds_read_b128 v[228:231], v253 offset:27648\n\t" \
    "s_waitcnt lgkmcnt(2)\n\t" \
    "v_mfma_f32_16x16x32_bf16 v[204:207], v[124:127], v[240:243], v[204:207]\n\t" \
    "ds_read_b128 v[232:235], v253 offset:16384\n\t" \
    "v_mfma_f32_16x16x32_bf16 v[208:211], v[128:131], v[236:239], v[208:211]\n\t" \
    "v_mfma_f32_16x16x32_bf16 v[208:211], v[132:135], v[240:243], v[208:211]\n\t" \
    "v_mfma_f32_16x16x32_bf16 v[200:203], v[244:247], v[236:239], v[200:203]\n\t" \
    "v_mfma_f32_16x16x32_bf16 v[200:203], v[248:251], v[240:243], v[200:203]\n\t" \
    "s_waitcnt lgkmcnt(2)\n\t" \
    "v_mfma_f32_16x16x32_bf16 v[216:219], v[120:123], v[224:227], v[216:219]\n\t" \
    "ds_read_b128 v[236:239], v253 offset:17408\n\t" \
    "s_waitcnt lgkmcnt(2)\n\t" \
    "v_mfma_f32_16x16x32_bf16 v[216:219], v[124:127], v[228:231], v[216:219]\n\t" \
    "ds_read_b128 v[240:243], v253 offset:8192\n\t" \
    "v_mfma_f32_16x16x32_bf16 v[220:223], v[128:131], v[224:227], v[220:223]\n\t" \
    "v_mfma_f32_16x16x32_bf16 v[220:223], v[132:135], v[228:231], v[220:223]\n\t" \
    "v_mfma_f32_16x16x32_bf16 v[212:215], v[244:247], v[224:227], v[212:215]\n\t" \
    "ds_read_b128 v[244:247], v254 offset:38912\n\t" \
    "v_mfma_f32_16x16x32_bf16 v[212:215], v[248:251], v[228:231], v[212:215]\n\t" \
    "ds_read_b128 v[248:251], v254 offset:39936\n\t" \
    "v_and_b32 v252, 0xf0, v253\n\t" \
    "v_lshlrev_b32 v252, 3, v252\n\t" \
    "buffer_load_dword v252, v252, s[60:63], %[fl] offen sc1\n\t" \
    "s_waitcnt lgkmcnt(4)\n\t" \
    "v_mfma_f32_16x16x32_bf16 v[204:207], v[144:147], v[232:235], v[204:207]\n\t" \
    "ds_read_b128 v[224:227], v253 offset:9216\n\t" \
    "s_waitcnt lgkmcnt(4)\n\t" \
    "v_mfma_f32_16x16x32_bf16 v[204:207], v[148:151], v[236:239], v[204:207]\n\t" \
    "ds_read_b128 v[228:231], v253 offset:28672\n\t" \
    "v_mfma_f32_16x16x32_bf16 v[208:211], v[152:155], v[232:235], v[208:211]\n\t" \
    "v_mfma_f32_16x16x32_bf16 v[208:211], v[156:159], v[236:239], v[208:211]\n\t" \
    "v_mfma_f32_16x16x32_bf16 v[200:203], v[136:139], v[232:235], v[200:203]\n\t" \
    "v_mfma_f32_16x16x32_bf16 v[200:203], v[140:143], v[236:239], v[200:203]\n\t" \
    "s_waitcnt lgkmcnt(4)\n\t" \
    "v_mfma_f32_16x16x32_bf16 v[204:207], v[160:163], v[240:243], v[204:207]\n\t" \
    "ds_read_b128 v[232:235], v253 offset:29696\n\t" \
    "s_waitcnt lgkmcnt(2)\n\t" \
    "v_mfma_f32_16x16x32_bf16 v[204:207], v[164:167], v[224:227], v[204:207]\n\t" \
    "ds_read_b128 v[236:239], v253 offset:18432\n\t" \
    "v_mfma_f32_16x16x32_bf16 v[208:211], v[168:171], v[240:243], v[208:211]\n\t" \
    "v_mfma_f32_16x16x32_bf16 v[208:211], v[172:175], v[224:227], v[208:211]\n\t" \
    "v_mfma_f32_16x16x32_bf16 v[200:203], v[244:247], v[240:243], v[200:203]\n\t" \
    "v_mfma_f32_16x16x32_bf16 v[200:203], v[248:251], v[224:227], v[200:203]\n\t" \
    "s_waitcnt lgkmcnt(2)\n\t" \
    "v_mfma_f32_16x16x32_bf16 v[216:219], v[160:163], v[228:231], v[216:219]\n\t" \
    "ds_read_b128 v[240:243], v253 offset:19456\n\t" \
    "s_waitcnt lgkmcnt(2)\n\t" \
    "v_mfma_f32_16x16x32_bf16 v[216:219], v[164:167], v[232:235], v[216:219]\n\t" \
    "ds_read_b128 v[224:227], v253 offset:30720\n\t" \
    "v_mfma_f32_16x16x32_bf16 v[220:223], v[168:171], v[228:231], v[220:223]\n\t" \
    "v_mfma_f32_16x16x32_bf16 v[220:223], v[172:175], v[232:235], v[220:223]\n\t" \
    "v_mfma_f32_16x16x32_bf16 v[212:215], v[244:247], v[228:231], v[212:215]\n\t" \
    "v_mfma_f32_16x16x32_bf16 v[212:215], v[248:251], v[232:235], v[212:215]\n\t" \
    "s_waitcnt lgkmcnt(2)\n\t" \
    "v_mfma_f32_16x16x32_bf16 v[204:207], v[184:187], v[236:239], v[204:207]\n\t" \
    "ds_read_b128 v[228:231], v253 offset:31744\n\t" \
    "s_waitcnt lgkmcnt(2)\n\t" \
    "v_mfma_f32_16x16x32_bf16 v[204:207], v[188:191], v[240:243], v[204:207]\n\t" \
    "ds_read_b128 v[232:235], v253 offset:32768\n\t" \
    "v_mfma_f32_16x16x32_bf16 v[208:211], v[192:195], v[236:239], v[208:211]\n\t" \
    "v_mfma_f32_16x16x32_bf16 v[208:211], v[196:199], v[240:243], v[208:211]\n\t" \
    "v_mfma_f32_16x16x32_bf16 v[200:203], v[176:179], v[236:239], v[200:203]\n\t" \
    "v_mfma_f32_16x16x32_bf16 v[200:203], v[180:183], v[240:243], v[200:203]\n\t" \
    "s_waitcnt lgkmcnt(2)\n\t" \
    "v_mfma_f32_16x16x32_bf16 v[216:219], v[24:27], v[224:227], v[216:219]\n\t" \
    "ds_read_b128 v[236:239], v253 offset:33792\n\t" \
    "s_waitcnt lgkmcnt(2)\n\t" \
    "v_mfma_f32_16x16x32_bf16 v[216:219], v[28:31], v[228:231], v[216:219]\n\t" \
    "ds_read_b128 v[240:243], v253 offset:34816\n\t" \
    "v_mfma_f32_16x16x32_bf16 v[220:223], v[32:35], v[224:227], v[220:223]\n\t" \
    "v_cvt_pk_bf16_f32 v204, v204, v205\n\t" \
    "v_mfma_f32_16x16x32_bf16 v[220:223], v[36:39], v[228:231], v[220:223]\n\t" \
    "v_cvt_pk_bf16_f32 v205, v206, v207\n\t" \
    "v_mfma_f32_16x16x32_bf16 v[212:215], v[16:19], v[224:227], v[212:215]\n\t" \
    "ds_write_b64 v254, v[204:205] offset:7168\n\t" \
    "v_mfma_f32_16x16x32_bf16 v[212:215], v[20:23], v[228:231], v[212:215]\n\t" \
    "v_cvt_pk_bf16_f32 v208, v208, v209\n\t" \
    "s_waitcnt lgkmcnt(3)\n\t" \
    "v_mfma_f32_16x16x32_bf16 v[216:219], v[64:67], v[232:235], v[216:219]\n\t" \
    "ds_read_b128 v[224:227], v253 offset:35840\n\t" \
    "v_cvt_pk_bf16_f32 v209, v210, v211\n\t" \
    "s_waitcnt lgkmcnt(3)\n\t" \
    "v_mfma_f32_16x16x32_bf16 v[216:219], v[68:71], v[236:239], v[216:219]\n\t" \
    "ds_read_b128 v[228:231], v253 offset:36864\n\t" \
    "ds_write_b64 v254, v[208:209] offset:8192\n\t" \
    "v_mfma_f32_16x16x32_bf16 v[220:223], v[72:75], v[232:235], v[220:223]\n\t" \
    "v_cvt_pk_bf16_f32 v200, v200, v201\n\t" \
    "v_mfma_f32_16x16x32_bf16 v[220:223], v[76:79], v[236:239], v[220:223]\n\t" \
    "v_cvt_pk_bf16_f32 v201, v202, v203\n\t" \
    "v_mfma_f32_16x16x32_bf16 v[212:215], v[56:59], v[232:235], v[212:215]\n\t" \
    "ds_write_b64 v254, v[200:201] offset:6144\n\t" \
    "v_mfma_f32_16x16x32_bf16 v[212:215], v[60:63], v[236:239], v[212:215]\n\t" \
    "s_waitcnt lgkmcnt(5)\n\t" \
    "v_mfma_f32_16x16x32_bf16 v[216:219], v[104:107], v[240:243], v[216:219]\n\t" \
    "ds_read_b128 v[232:235], v253 offset:37888\n\t" \
    "s_waitcnt lgkmcnt(4)\n\t" \
    "v_mfma_f32_16x16x32_bf16 v[216:219], v[108:111], v[224:227], v[216:219]\n\t" \
    "ds_read_b128 v[236:239], v253 offset:38912\n\t" \
    "v_mfma_f32_16x16x32_bf16 v[220:223], v[112:115], v[240:243], v[220:223]\n\t" \
    "v_mfma_f32_16x16x32_bf16 v[220:223], v[116:119], v[224:227], v[220:223]\n\t" \
    "v_mfma_f32_16x16x32_bf16 v[212:215], v[96:99], v[240:243], v[212:215]\n\t" \
    "v_mfma_f32_16x16x32_bf16 v[212:215], v[100:103], v[224:227], v[212:215]\n\t" \
    "ds_read_b128 v[240:243], v253 offset:39936\n\t" \
    "s_waitcnt lgkmcnt(3)\n\t" \
    "s_barrier \n\t" \
    "v_mfma_f32_16x16x32_bf16 v[216:219], v[144:147], v[228:231], v[216:219]\n\t" \
    "s_waitcnt lgkmcnt(2)\n\t" \
    "v_mfma_f32_16x16x32_bf16 v[216:219], v[148:151], v[232:235], v[216:219]\n\t" \
    "s_waitcnt lgkmcnt(1)\n\t" \
    "v_mfma_f32_16x16x32_bf16 v[216:219], v[184:187], v[236:239], v[216:219]\n\t" \
    "s_waitcnt lgkmcnt(0)\n\t" \
    "v_mfma_f32_16x16x32_bf16 v[216:219], v[188:191], v[240:243], v[216:219]\n\t" \
    "v_mfma_f32_16x16x32_bf16 v[220:223], v[152:155], v[228:231], v[220:223]\n\t" \
    "v_mfma_f32_16x16x32_bf16 v[220:223], v[156:159], v[232:235], v[220:223]\n\t" \
    "v_mfma_f32_16x16x32_bf16 v[220:223], v[192:195], v[236:239], v[220:223]\n\t" \
    "v_cvt_pk_bf16_f32 v216, v216, v217\n\t" \
    "v_cvt_pk_bf16_f32 v217, v218, v219\n\t" \
    "v_mfma_f32_16x16x32_bf16 v[220:223], v[196:199], v[240:243], v[220:223]\n\t" \
    "ds_write_b64 v254, v[216:217] offset:13312\n\t" \
    "v_mfma_f32_16x16x32_bf16 v[212:215], v[136:139], v[228:231], v[212:215]\n\t" \
    "v_mfma_f32_16x16x32_bf16 v[212:215], v[140:143], v[232:235], v[212:215]\n\t" \
    "v_mfma_f32_16x16x32_bf16 v[212:215], v[176:179], v[236:239], v[212:215]\n\t" \
    "v_cvt_pk_bf16_f32 v220, v220, v221\n\t" \
    "v_cvt_pk_bf16_f32 v221, v222, v223\n\t" \
    "v_mfma_f32_16x16x32_bf16 v[212:215], v[180:183], v[240:243], v[212:215]\n\t" \
    "ds_write_b64 v254, v[220:221] offset:14336\n\t" \
    "s_nop 6\n\t" \
    "v_cvt_pk_bf16_f32 v212, v212, v213\n\t" \
    "v_cvt_pk_bf16_f32 v213, v214, v215\n\t" \
    "ds_write_b64 v254, v[212:213] offset:12288\n\t" \
    LST6 \
    LJIT \
    "s_waitcnt vmcnt(0) lgkmcnt(0)\n\t" \
    "s_barrier \n\t" \
    "s_setprio 2\n\t" \
    "s_add_u32 s44, s40, 1\n\t" \
    "s_min_u32 s44, s44, 63\n\t" \
    ".Lv08_%=:\n\t" \
    "v_cmp_ge_u32 vcc, s44, v252\n\t" \
    "s_cbranch_vccz .Lv09_%=\n\t" \
    "s_cmp_eq_u32 s41, 0\n\t" \
    "s_cbranch_scc1 .Lv09_%=\n\t" \
    "s_sub_u32 s41, s41, 1\n\t" \
    "s_sleep 2\n\t" \
    "v_and_b32 v252, 0xf0, v253\n\t" \
    "v_lshlrev_b32 v252, 3, v252\n\t" \
    "buffer_load_dword v252, v252, s[60:63], %[fl] offen sc1\n\t" \
    "s_waitcnt vmcnt(0)\n\t" \
    "s_branch .Lv08_%=\n\t" \
    ".Lv09_%=:\n\t" \
    "s_mov_b32 m0, 0x0\n\t" \
    "s_add_u32 s49, s42, 0x0\n\t" \
    "buffer_load_dwordx4 v253, s[60:63], s49 offen offset:0 sc1 lds\n\t" \
    "s_sleep 1\n\t" \
    "buffer_load_dwordx4 v253, s[60:63], s49 offen offset:1024 sc1 lds\n\t" \
    "s_sleep 1\n\t" \
    "buffer_load_dwordx4 v253, s[60:63], s49 offen offset:2048 sc1 lds\n\t" \
    "s_sleep 1\n\t" \
    "buffer_load_dwordx4 v253, s[60:63], s49 offen offset:3072 sc1 lds\n\t" \
    "s_sleep 1\n\t" \
    "s_mov_b32 m0, 0x4000\n\t" \
    "s_add_u32 s49, s42, 0x10000\n\t" \
    "buffer_load_dwordx4 v253, s[60:63], s49 offen offset:0 sc1 lds\n\t" \
    "s_sleep 1\n\t" \
    "s_mov_b32 m0, 0x5000\n\t" \
    "s_add_u32 s49, s42, 0x1000\n\t" \
    "buffer_load_dwordx4 v253, s[60:63], s49 offen offset:0 sc1 lds\n\t" \
    "s_sleep 1\n\t" \
    "buffer_load_dwordx4 v253, s[60:63], s49 offen offset:1024 sc1 lds\n\t" \
    "s_sleep 1\n\t" \
    "buffer_load_dwordx4 v253, s[60:63], s49 offen offset:2048 sc1 lds\n\t" \
    "s_sleep 1\n\t" \
    "buffer_load_dwordx4 v253, s[60:63], s49 offen offset:3072 sc1 lds\n\t" \
    "s_sleep 1\n\t" \
    "s_mov_b32 m0, 0x9000\n\t" \
    "s_add_u32 s49, s42, 0x11000\n\t" \
    "buffer_load_dwordx4 v253, s[60:63], s49 offen offset:0 sc1 lds\n\t" \
    "s_sleep 1\n\t" \
    "ds_read_b128 v[200:203], v254 offset:30720\n\t" \
    "ds_read_b128 v[204:207], v254 offset:31744\n\t" \
    "ds_read_b128 v[216:219], v254 offset:32768\n\t" \
    "ds_read_b128 v[220:223], v254 offset:33792\n\t" \
    "v_mov_b64 v[208:209], 0\n\t" \
    "v_mov_b64 v[210:211], 0\n\t" \
    "v_mov_b64 v[244:245], 0\n\t" \
    "v_mov_b64 v[246:247], 0\n\t" \
    "v_mov_b64 v[248:249], 0\n\t" \
    "v_mov_b64 v[250:251], 0\n\t" \
    LST1 \
    LJIT \
    "s_sleep 5\n\t" \
    "s_mov_b32 m0, 0xa000\n\t" \
    "s_nop 0\n\t" \
    "buffer_load_dwordx4 v253, s[60:63], s46 offen sc1 lds\n\t" \
    "s_mov_b32 m0, 0xb400\n\t" \
    "s_nop 0\n\t" \
    "buffer_load_dwordx4 v253, s[60:63], s47 offen sc1 lds\n\t" \
    "s_mov_b32 m0, 0xa400\n\t" \
    "s_nop 0\n\t" \
    "buffer_load_dwordx4 v253, s[60:63], s48 offen sc1 lds\n\t" \
    "s_waitcnt lgkmcnt(0)\n\t" \
    LFLUSH212 \
    "v_bfe_i32 v212, v253, 5, 1\n\t" \
    "v_and_b32 v212, 0xaaaa, v212\n\t" \
    "v_or_b32 v212, 0x4444, v212\n\t" \
    "v_and_b32 v213, 0xffff, v255\n\t" \
    "v_lshrrev_b32 v214, 16, v255\n\t" \
    ".Lv010_%=:\n\t" \
    "s_waitcnt vmcnt(0)\n\t" \
    "ds_read_b128 v[224:227], v253 offset:40960\n\t" \
    "ds_read_b128 v[228:231], v253 offset:46080\n\t" \
    "ds_read_b128 v[232:235], v253 offset:41984\n\t" \
    "s_waitcnt lgkmcnt(2)\n\t" \
    "v_max_u32 v215, v224, v225\n\t" \
    "v_max3_u32 v215, v226, v227, v215\n\t" \
    "s_waitcnt lgkmcnt(1)\n\t" \
    "v_max3_u32 v215, v228, v229, v215\n\t" \
    "v_max3_u32 v215, v230, v231, v215\n\t" \
    "s_waitcnt lgkmcnt(0)\n\t" \
    "v_max3_u32 v215, v232, v233, v215\n\t" \
    "v_max3_u32 v215, v234, v235, v215\n\t" \
    "v_cmp_eq_u32 vcc, -1, v215\n\t" \
    "s_cbranch_vccz .Lv011_%=\n\t" \
    "v_max_u32 v215, v224, v225\n\t" \
    "v_max3_u32 v215, v226, v227, v215\n\t" \
    "v_cmp_eq_u32 vcc, -1, v215\n\t" \
    "s_cbranch_vccz .Lv012_%=\n\t" \
    "s_mov_b32 m0, 0xa000\n\t" \
    "s_nop 0\n\t" \
    "buffer_load_dwordx4 v253, s[60:63], s46 offen sc1 lds\n\t" \
    ".Lv012_%=:\n\t" \
    "v_max_u32 v215, v228, v229\n\t" \
    "v_max3_u32 v215, v230, v231, v215\n\t" \
    "v_cmp_eq_u32 vcc, -1, v215\n\t" \
    "s_cbranch_vccz .Lv013_%=\n\t" \
    "s_mov_b32 m0, 0xb400\n\t" \
    "s_nop 0\n\t" \
    "buffer_load_dwordx4 v253, s[60:63], s47 offen sc1 lds\n\t" \
    ".Lv013_%=:\n\t" \
    "v_max_u32 v215, v232, v233\n\t" \
    "v_max3_u32 v215, v234, v235, v215\n\t" \
    "v_cmp_eq_u32 vcc, -1, v215\n\t" \
    "s_cbranch_vccz .Lv014_%=\n\t" \
    "s_mov_b32 m0, 0xa400\n\t" \
    "s_nop 0\n\t" \
    "buffer_load_dwordx4 v253, s[60:63], s48 offen sc1 lds\n\t" \
    ".Lv014_%=:\n\t" \
    "s_cmp_eq_u32 s41, 0\n\t" \
    "s_cbranch_scc1 .Lv011_%=\n\t" \
    "s_sub_u32 s41, s41, 1\n\t" \
    "s_branch .Lv010_%=\n\t" \
    ".Lv011_%=:\n\t" \
    LST2 \
    LJIT \
    "s_waitcnt lgkmcnt(0)\n\t" \
    "s_barrier \n\t" \
    LST3 \
    LJIT \
    "ds_read_b128 v[236:239], v253 offset:47104\n\t" \
    "ds_read_b128 v[240:243], v253 offset:43008\n\t" \
    "v_smfmac_f32_16x16x64_bf16 v[244:247], v[224:227], v[0:7], v212\n\t" \
    "v_smfmac_f32_16x16x64_bf16 v[248:251], v[224:227], v[8:15], v212\n\t" \
    "v_smfmac_f32_16x16x64_bf16 v[208:211], v[224:227], v[200:207], v212\n\t" \
    "ds_read_b128 v[200:203], v254 offset:34816\n\t" \
    "ds_read_b128 v[204:207], v254 offset:35840\n\t" \
    "ds_read_b128 v[224:227], v253 offset:48128\n\t" \
    "v_smfmac_f32_16x16x64_bf16 v[208:211], v[228:231], v[16:23], v212\n\t" \
    "v_smfmac_f32_16x16x64_bf16 v[244:247], v[228:231], v[24:31], v212\n\t" \
    "v_smfmac_f32_16x16x64_bf16 v[248:251], v[228:231], v[32:39], v212\n\t" \
    "ds_read_b128 v[228:231], v253 offset:44032\n\t" \
    "v_smfmac_f32_16x16x64_bf16 v[208:211], v[232:235], v[216:223], v212\n\t" \
    "ds_read_b128 v[216:219], v254 offset:36864\n\t" \
    "ds_read_b128 v[220:223], v254 offset:37888\n\t" \
    "v_smfmac_f32_16x16x64_bf16 v[244:247], v[232:235], v[40:47], v212\n\t" \
    "v_smfmac_f32_16x16x64_bf16 v[248:251], v[232:235], v[48:55], v212\n\t" \
    "ds_read_b128 v[232:235], v253 offset:49152\n\t" \
    "s_waitcnt lgkmcnt(8)\n\t" \
    "v_smfmac_f32_16x16x64_bf16 v[208:211], v[236:239], v[56:63], v212\n\t" \
    "v_smfmac_f32_16x16x64_bf16 v[244:247], v[236:239], v[64:71], v212\n\t" \
    "v_smfmac_f32_16x16x64_bf16 v[248:251], v[236:239], v[72:79], v212\n\t" \
    "ds_read_b128 v[236:239], v253 offset:45056\n\t" \
    "s_waitcnt lgkmcnt(6)\n\t" \
    "v_smfmac_f32_16x16x64_bf16 v[208:211], v[240:243], v[200:207], v212\n\t" \
    "ds_read_b128 v[200:203], v254 offset:38912\n\t" \
    "ds_read_b128 v[204:207], v254 offset:39936\n\t" \
    "v_smfmac_f32_16x16x64_bf16 v[244:247], v[240:243], v[80:87], v212\n\t" \
    "v_smfmac_f32_16x16x64_bf16 v[248:251], v[240:243], v[88:95], v212\n\t" \
    "ds_read_b128 v[240:243], v253 offset:50176\n\t" \
    "s_waitcnt lgkmcnt(8)\n\t" \
    "v_smfmac_f32_16x16x64_bf16 v[208:211], v[224:227], v[96:103], v212\n\t" \
    "v_smfmac_f32_16x16x64_bf16 v[244:247], v[224:227], v[104:111], v212\n\t" \
    "v_smfmac_f32_16x16x64_bf16 v[248:251], v[224:227], v[112:119], v212\n\t" \
    "s_waitcnt lgkmcnt(7)\n\t" \
    "v_smfmac_f32_16x16x64_bf16 v[244:247], v[228:231], v[120:127], v212\n\t" \
    "s_waitcnt lgkmcnt(4)\n\t" \
    "v_smfmac_f32_16x16x64_bf16 v[244:247], v[232:235], v[144:151], v212\n\t" \
    "s_waitcnt lgkmcnt(3)\n\t" \
    "v_smfmac_f32_16x16x64_bf16 v[244:247], v[236:239], v[160:167], v212\n\t" \
    "s_waitcnt lgkmcnt(0)\n\t" \
    "v_smfmac_f32_16x16x64_bf16 v[244:247], v[240:243], v[184:191], v212\n\t" \
    "v_smfmac_f32_16x16x64_bf16 v[248:251], v[228:231], v[128:135], v212\n\t" \
    "v_smfmac_f32_16x16x64_bf16 v[248:251], v[232:235], v[152:159], v212\n\t" \
    "v_smfmac_f32_16x16x64_bf16 v[248:251], v[236:239], v[168:175], v212\n\t" \
    "v_add_f32 v244, v244, v246\n\t" \
    "v_add_f32 v245, v245, v247\n\t" \
    "v_cvt_pk_bf16_f32 v244, v244, v245\n\t" \
    "v_smfmac_f32_16x16x64_bf16 v[248:251], v[240:243], v[192:199], v212\n\t" \
    "ds_write_b16 v213, v244 offset:512\n\t" \
    "ds_write_b16_d16_hi v213, v244 offset:528\n\t" \
    "v_smfmac_f32_16x16x64_bf16 v[208:211], v[228:231], v[216:223], v212\n\t" \
    "v_smfmac_f32_16x16x64_bf16 v[208:211], v[232:235], v[136:143], v212\n\t" \
    "v_smfmac_f32_16x16x64_bf16 v[208:211], v[236:239], v[200:207], v212\n\t" \
    "v_add_f32 v248, v248, v250\n\t" \
    "v_add_f32 v249, v249, v251\n\t" \
    "v_cvt_pk_bf16_f32 v248, v248, v249\n\t" \
    "v_smfmac_f32_16x16x64_bf16 v[208:211], v[240:243], v[176:183], v212\n\t" \
    "ds_write_b16 v213, v248 offset:1024\n\t" \
    "ds_write_b16_d16_hi v213, v248 offset:1040\n\t" \
    "s_nop 5\n\t" \
    "v_add_f32 v208, v208, v210\n\t" \
    "v_add_f32 v209, v209, v211\n\t" \
    "v_cvt_pk_bf16_f32 v208, v208, v209\n\t" \
    "ds_write_b16 v213, v208 offset:0\n\t" \
    "ds_write_b16_d16_hi v213, v208 offset:16\n\t" \
    "s_setprio 0\n\t" \
    "s_waitcnt lgkmcnt(0)\n\t" \
    "s_barrier \n\t" \
    LST4 \
    LJIT \
    "ds_read_b128 v[224:227], v253 offset:0\n\t" \
    "ds_read_b128 v[244:247], v254 offset:30720\n\t" \
    "ds_read_b128 v[228:231], v253 offset:1024\n\t" \
    "ds_read_b128 v[232:235], v253 offset:20480\n\t" \
    "ds_read_b128 v[236:239], v253 offset:21504\n\t" \
    "ds_read_b128 v[248:251], v254 offset:31744\n\t" \
    "s_add_u32 s40, s40, 1\n\t" \
    "s_cmp_lg_u32 s40, 64\n\t" \
    "s_cbranch_scc1 .Lv07_%=\n\t" \
    "s_branch .Lend%=\n\t" \
    ".Lend%=:\n\t" \
    "s_waitcnt vmcnt(0) lgkmcnt(0)\n\t" \
    "s_mov_b32 %[bud], s41\n\t"
#define ASM_R_LOOP \
    "s_mov_b32 s40, 0\n\t" \
    "s_mov_b32 s41, 0x40000\n\t" \
    LSTINIT \
    "v_mov_b64 v[252:253], -1\n\t" \
    "v_mov_b64 v[254:255], -1\n\t" \
    "v_mov_b32 v248, 0\n\t" \
    "s_mov_b32 s53, 0x05040100\n\t" \
    ".Lr1_%=:\n\t" \
    "s_mov_b32 s43, s40\n\t" \
    "s_and_b32 s44, s40, 3\n\t" \
    "s_lshl_b32 s42, s44, 20\n\t" \
    "s_and_b32 s45, s40, 2\n\t" \
    "s_lshl_b32 s45, s45, 12\n\t" \
    "s_add_u32 s42, s42, s45\n\t" \
    "s_add_u32 s42, s42, %[vx]\n\t" \
    "s_mul_i32 s46, s44, 0x202000\n\t" \
    "s_mul_i32 s47, s44, 0x30000\n\t" \
    "s_lshl_b32 s49, s44, 17\n\t" \
    "s_add_u32 s49, s49, %[px]\n\t" \
    "s_add_u32 s50, s49, 0x1000\n\t" \
    "s_add_u32 s44, s40, 1\n\t" \
    "s_and_b32 s44, s44, 3\n\t" \
    "s_lshl_b32 s48, s44, 17\n\t" \
    "s_mul_i32 s52, s40, 0x14000\n\t" \
    "s_lshr_b32 s44, s40, 4\n\t" \
    "s_mul_i32 s44, s44, 0xc2000\n\t" \
    "s_add_u32 s52, s52, s44\n\t" \
    "s_add_u32 s52, s52, %[xr]\n\t" \
    "s_add_i32 s44, s40, %[ad]\n\t" \
    "s_and_b32 s45, s44, 3\n\t" \
    "s_mul_i32 s51, s45, %[as]\n\t" \
    "s_cmp_lt_i32 s44, 0\n\t" \
    "s_cselect_b32 s55, 0x40000000, 0\n\t" \
    "s_add_u32 s44, s40, 3\n\t" \
    "s_mul_i32 s54, s44, 0x14000\n\t" \
    "s_lshr_b32 s45, s44, 4\n\t" \
    "s_mul_i32 s45, s45, 0xc2000\n\t" \
    "s_add_u32 s54, s54, s45\n\t" \
    "s_cmp_eq_u32 %[wave], 7\n\t" \
    "s_cselect_b32 s51, s54, s51\n\t" \
    "s_cselect_b32 s55, 0, s55\n\t" \
    "s_cselect_b32 s44, 60, 62\n\t" \
    "s_cmp_gt_u32 s40, s44\n\t" \
    "s_cselect_b32 s55, 0x40000000, s55\n\t" \
    "v_or_b32 v197, s55, %[ae]\n\t" \
    "v_mov_b64 v[180:181], -1\n\t" \
    "v_mov_b64 v[182:183], -1\n\t" \
    "v_mov_b64 v[184:185], -1\n\t" \
    "v_mov_b64 v[186:187], -1\n\t" \
    "v_mov_b64 v[188:189], -1\n\t" \
    "v_mov_b64 v[190:191], -1\n\t" \
    "v_mov_b64 v[192:193], -1\n\t" \
    "v_mov_b64 v[194:195], -1\n\t" \
    "s_waitcnt vmcnt(0)\n\t" \
    ".Lr2_%=:\n\t" \
    "buffer_load_dword v248, %[fo], %[rs], %[fs] offen sc1\n\t" \
    "s_waitcnt vmcnt(0)\n\t" \
    "v_cmp_ge_u32 vcc, s43, v248\n\t" \
    "s_cbranch_vccz .Lr3_%=\n\t" \
    "s_sleep 2\n\t" \
    "s_cmp_eq_u32 s41, 0\n\t" \
    "s_cbranch_scc1 .Lr3_%=\n\t" \
    "s_sub_u32 s41, s41, 1\n\t" \
    "s_branch .Lr2_%=\n\t" \
    ".Lr3_%=:\n\t" \
    "buffer_load_dwordx4 v[160:163], %[l16], %[rs], s42 offen offset:0 sc1\n\t" \
    "buffer_load_dwordx4 v[164:167], %[l16], %[rs], s42 offen offset:1024 sc1\n\t" \
    "buffer_load_dwordx4 v[168:171], %[l16], %[rs], s42 offen offset:2048 sc1\n\t" \
    "buffer_load_dwordx4 v[172:175], %[l16], %[rs], s42 offen offset:3072 sc1\n\t" \
    LST0 \
    LJIT \
    "s_sleep 8\n\t" \
    "s_cmp_lg_u32 s40, 63\n\t" \
    "s_cbranch_scc1 .Lr5_%=\n\t" \
    "buffer_load_dwordx4 v[180:183], %[ov], %[rs], s46 offen sc1\n\t" \
    ".Lr6_%=:\n\t" \
    "s_waitcnt vmcnt(12)\n\t" \
    "v_max_u32 v196, v180, v181\n\t" \
    "v_max3_u32 v196, v182, v183, v196\n\t" \
    "v_cmp_eq_u32 vcc, -1, v196\n\t" \
    "s_cbranch_vccz .Lr7_%=\n\t" \
    "s_nop 7\n\t" \
    "v_max_u32 v196, v180, v181\n\t" \
    "v_max3_u32 v196, v182, v183, v196\n\t" \
    "v_cmp_eq_u32 vcc, -1, v196\n\t" \
    "s_cbranch_vccz .Lr7_%=\n\t" \
    "s_nop 7\n\t" \
    "v_max_u32 v196, v180, v181\n\t" \
    "v_max3_u32 v196, v182, v183, v196\n\t" \
    "v_cmp_eq_u32 vcc, -1, v196\n\t" \
    "s_cbranch_vccz .Lr7_%=\n\t" \
    "s_nop 7\n\t" \
    "v_max_u32 v196, v180, v181\n\t" \
    "v_max3_u32 v196, v182, v183, v196\n\t" \
    "v_cmp_eq_u32 vcc, -1, v196\n\t" \
    "s_cbranch_vccz .Lr7_%=\n\t" \
    "s_nop 7\n\t" \
    "s_cmp_eq_u32 s41, 0\n\t" \
    "s_cbranch_scc1 .Lr7_%=\n\t" \
    "s_sub_u32 s41, s41, 1\n\t" \
    "v_max_u32 v196, v180, v181\n\t" \
    "v_max3_u32 v196, v182, v183, v196\n\t" \
    "v_cmp_eq_u32 vcc, -1, v196\n\t" \
    "s_cbranch_vccz .Lr8_%=\n\t" \
    "buffer_load_dwordx4 v[180:183], %[ov], %[rs], s46 offen sc1\n\t" \
    ".Lr8_%=:\n\t" \
    "s_branch .Lr6_%=\n\t" \
    ".Lr7_%=:\n\t" \
    "s_branch .Lr4_%=\n\t" \
    ".Lr5_%=:\n\t" \
    LST7 \
    LJIT \
    "buffer_load_dwordx4 v[184:187], %[o0], %[rs], s47 offen sc1\n\t" \
    "buffer_load_dwordx4 v[188:191], %[o1], %[rs], s47 offen sc1\n\t" \
    "buffer_load_dwordx4 v[192:195], %[o2], %[rs], s47 offen sc1\n\t" \
    "buffer_load_dwordx4 v[180:183], %[ov], %[rs], s46 offen sc1\n\t" \
    ".Lr9_%=:\n\t" \
    "s_waitcnt vmcnt(12)\n\t" \
    "v_max_u32 v196, v184, v185\n\t" \
    "v_max3_u32 v196, v186, v187, v196\n\t" \
    "v_max3_u32 v196, v188, v189, v196\n\t" \
    "v_max3_u32 v196, v190, v191, v196\n\t" \
    "v_max3_u32 v196, v192, v193, v196\n\t" \
    "v_max3_u32 v196, v194, v195, v196\n\t" \
    "v_max3_u32 v196, v180, v181, v196\n\t" \
    "v_max3_u32 v196, v182, v183, v196\n\t" \
    "v_cmp_eq_u32 vcc, -1, v196\n\t" \
    "s_cbranch_vccz .Lr10_%=\n\t" \
    "s_nop 7\n\t" \
    "v_max_u32 v196, v184, v185\n\t" \
    "v_max3_u32 v196, v186, v187, v196\n\t" \
    "v_max3_u32 v196, v188, v189, v196\n\t" \
    "v_max3_u32 v196, v190, v191, v196\n\t" \
    "v_max3_u32 v196, v192, v193, v196\n\t" \
    "v_max3_u32 v196, v194, v195, v196\n\t" \
    "v_max3_u32 v196, v180, v181, v196\n\t" \
    "v_max3_u32 v196, v182, v183, v196\n\t" \
    "v_cmp_eq_u32 vcc, -1, v196\n\t" \
    "s_cbranch_vccz .Lr10_%=\n\t" \
    "s_nop 7\n\t" \
    "v_max_u32 v196, v184, v185\n\t" \
    "v_max3_u32 v196, v186, v187, v196\n\t" \
    "v_max3_u32 v196, v188, v189, v196\n\t" \
    "v_max3_u32 v196, v190, v191, v196\n\t" \
    "v_max3_u32 v196, v192, v193, v196\n\t" \
    "v_max3_u32 v196, v194, v195, v196\n\t" \
    "v_max3_u32 v196, v180, v181, v196\n\t" \
    "v_max3_u32 v196, v182, v183, v196\n\t" \
    "v_cmp_eq_u32 vcc, -1, v196\n\t" \
    "s_cbranch_vccz .Lr10_%=\n\t" \
    "s_nop 7\n\t" \
    "v_max_u32 v196, v184, v185\n\t" \
    "v_max3_u32 v196, v186, v187, v196\n\t" \
    "v_max3_u32 v196, v188, v189, v196\n\t" \
    "v_max3_u32 v196, v190, v191, v196\n\t" \
    "v_max3_u32 v196, v192, v193, v196\n\t" \
    "v_max3_u32 v196, v194, v195, v196\n\t" \
    "v_max3_u32 v196, v180, v181, v196\n\t" \
    "v_max3_u32 v196, v182, v183, v196\n\t" \
    "v_cmp_eq_u32 vcc, -1, v196\n\t" \
    "s_cbranch_vccz .Lr10_%=\n\t" \
    "s_nop 7\n\t" \
    "v_max_u32 v196, v184, v185\n\t" \
    "v_max3_u32 v196, v186, v187, v196\n\t" \
    "v_max3_u32 v196, v188, v189, v196\n\t" \
    "v_max3_u32 v196, v190, v191, v196\n\t" \
    "v_max3_u32 v196, v192, v193, v196\n\t" \
    "v_max3_u32 v196, v194, v195, v196\n\t" \
    "v_max3_u32 v196, v180, v181, v196\n\t" \
    "v_max3_u32 v196, v182, v183, v196\n\t" \
    "v_cmp_eq_u32 vcc, -1, v196\n\t" \
    "s_cbranch_vccz .Lr10_%=\n\t" \
    "s_nop 7\n\t" \
    "v_max_u32 v196, v184, v185\n\t" \
    "v_max3_u32 v196, v186, v187, v196\n\t" \
    "v_max3_u32 v196, v188, v189, v196\n\t" \
    "v_max3_u32 v196, v190, v191, v196\n\t" \
    "v_max3_u32 v196, v192, v193, v196\n\t" \
    "v_max3_u32 v196, v194, v195, v196\n\t" \
    "v_max3_u32 v196, v180, v181, v196\n\t" \
    "v_max3_u32 v196, v182, v183, v196\n\t" \
    "v_cmp_eq_u32 vcc, -1, v196\n\t" \
    "s_cbranch_vccz .Lr10_%=\n\t" \
    "s_nop 7\n\t" \
    "v_max_u32 v196, v184, v185\n\t" \
    "v_max3_u32 v196, v186, v187, v196\n\t" \
    "v_max3_u32 v196, v188, v189, v196\n\t" \
    "v_max3_u32 v196, v190, v191, v196\n\t" \
    "v_max3_u32 v196, v192, v193, v196\n\t" \
    "v_max3_u32 v196, v194, v195, v196\n\t" \
    "v_max3_u32 v196, v180, v181, v196\n\t" \
    "v_max3_u32 v196, v182, v183, v196\n\t" \
    "v_cmp_eq_u32 vcc, -1, v196\n\t" \
    "s_cbranch_vccz .Lr10_%=\n\t" \
    "s_nop 7\n\t" \
    "v_max_u32 v196, v184, v185\n\t" \
    "v_max3_u32 v196, v186, v187, v196\n\t" \
    "v_max3_u32 v196, v188, v189, v196\n\t" \
    "v_max3_u32 v196, v190, v191, v196\n\t" \
    "v_max3_u32 v196, v192, v193, v196\n\t" \
    "v_max3_u32 v196, v194, v195, v196\n\t" \
    "v_max3_u32 v196, v180, v181, v196\n\t" \
    "v_max3_u32 v196, v182, v183, v196\n\t" \
    "v_cmp_eq_u32 vcc, -1, v196\n\t" \
    "s_cbranch_vccz .Lr10_%=\n\t" \
    "s_nop 7\n\t" \
    "s_cmp_eq_u32 s41, 0\n\t" \
    "s_cbranch_scc1 .Lr10_%=\n\t" \
    "s_sub_u32 s41, s41, 1\n\t" \
    "v_max_u32 v196, v184, v185\n\t" \
    "v_max3_u32 v196, v186, v187, v196\n\t" \
    "v_cmp_eq_u32 vcc, -1, v196\n\t" \
    "s_cbranch_vccz .Lr11_%=\n\t" \
    "buffer_load_dwordx4 v[184:187], %[o0], %[rs], s47 offen sc1\n\t" \
    ".Lr11_%=:\n\t" \
    "v_max_u32 v196, v188, v189\n\t" \
    "v_max3_u32 v196, v190, v191, v196\n\t" \
    "v_cmp_eq_u32 vcc, -1, v196\n\t" \
    "s_cbranch_vccz .Lr12_%=\n\t" \
    "buffer_load_dwordx4 v[188:191], %[o1], %[rs], s47 offen sc1\n\t" \
    ".Lr12_%=:\n\t" \
    "v_max_u32 v196, v192, v193\n\t" \
    "v_max3_u32 v196, v194, v195, v196\n\t" \
    "v_cmp_eq_u32 vcc, -1, v196\n\t" \
    "s_cbranch_vccz .Lr13_%=\n\t" \
    "buffer_load_dwordx4 v[192:195], %[o2], %[rs], s47 offen sc1\n\t" \
    ".Lr13_%=:\n\t" \
    "v_max_u32 v196, v180, v181\n\t" \
    "v_max3_u32 v196, v182, v183, v196\n\t" \
    "v_cmp_eq_u32 vcc, -1, v196\n\t" \
    "s_cbranch_vccz .Lr14_%=\n\t" \
    "buffer_load_dwordx4 v[180:183], %[ov], %[rs], s46 offen sc1\n\t" \
    ".Lr14_%=:\n\t" \
    "s_branch .Lr9_%=\n\t" \
    ".Lr10_%=:\n\t" \
    LST1 \
    LJIT \
    "v_mfma_f32_16x16x32_bf16 v[176:179], v[192:195], v[180:183], 0\n\t" \
    "v_smfmac_f32_16x16x64_bf16 v[176:179], v[184:187], v[160:167], %[sidx]\n\t" \
    "v_smfmac_f32_16x16x64_bf16 v[176:179], v[188:191], v[168:175], %[sidx]\n\t" \
    "s_cmp_gt_u32 %[wave], 3\n\t" \
    "s_nop 7\n\t" \
    "v_add_f32 v176, v176, v178\n\t" \
    "v_add_f32 v177, v177, v179\n\t" \
    "ds_write2_b32 %[rw], v176, v177 offset1:80\n\t" \
    "s_waitcnt lgkmcnt(0)\n\t" \
    "s_barrier \n\t" \
    LST2 \
    LJIT \
    "s_cbranch_scc1 .Lr4_%=\n\t" \
    "ds_read_b128 v[128:131], %[rd] offset:0\n\t" \
    "ds_read_b128 v[132:135], %[rd] offset:16\n\t" \
    "ds_read_b128 v[136:139], %[rd] offset:32\n\t" \
    "ds_read_b128 v[140:143], %[rd] offset:48\n\t" \
    "s_waitcnt lgkmcnt(2)\n\t" \
    "v_pk_add_f32 v[128:129], v[128:129], v[130:131]\n\t" \
    "v_pk_add_f32 v[132:133], v[132:133], v[134:135]\n\t" \
    "s_waitcnt lgkmcnt(0)\n\t" \
    "v_pk_add_f32 v[136:137], v[136:137], v[138:139]\n\t" \
    "v_pk_add_f32 v[140:141], v[140:141], v[142:143]\n\t" \
    "v_pk_add_f32 v[128:129], v[128:129], v[132:133]\n\t" \
    "v_pk_add_f32 v[136:137], v[136:137], v[140:141]\n\t" \
    "v_pk_add_f32 v[128:129], v[128:129], v[136:137]\n\t" \
    "v_cvt_pk_bf16_f32 v128, v128, v129\n\t" \
    "v_min_u32 v128, -2, v128\n\t" \
    "buffer_store_dword v128, %[oo], %[rs], s48 offen sc1\n\t" \
    ".Lr4_%=:\n\t" \
    LST3 \
    LJIT \
    "s_cmp_lt_u32 %[wave], 4\n\t" \
    "s_cbranch_scc1 .Lr16_%=\n\t" \
    "s_sleep 4\n\t" \
    ".Lr16_%=:\n\t" \
    "s_sleep 2\n\t" \
    "s_cmp_lt_u32 %[wave], 6\n\t" \
    "s_cbranch_scc1 .Lr15_%=\n\t" \
    "buffer_load_dwordx2 v[250:251], %[xo], %[rs], s52 offen sc1\n\t" \
    ".Lr15_%=:\n\t" \
    "buffer_load_dwordx4 v[220:223], %[l16], %[rs], s50 offen offset:1024 sc1\n\t" \
    "buffer_load_dwordx4 v[224:227], %[l16], %[rs], s50 offen offset:2048 sc1\n\t" \
    "buffer_load_dwordx4 v[228:231], %[l16], %[rs], s50 offen offset:3072 sc1\n\t" \
    "v_perm_b32 v232, v161, v160, s53\n\t" \
    "v_perm_b32 v233, v163, v162, s53\n\t" \
    "v_perm_b32 v234, v165, v164, s53\n\t" \
    "v_perm_b32 v235, v167, v166, s53\n\t" \
    "v_perm_b32 v236, v169, v168, s53\n\t" \
    "v_perm_b32 v237, v171, v170, s53\n\t" \
    "v_perm_b32 v238, v173, v172, s53\n\t" \
    "v_perm_b32 v239, v175, v174, s53\n\t" \
    "s_nop 1\n\t" \
    "v_permlane16_swap_b32 v232, v234\n\t" \
    "v_permlane16_swap_b32 v233, v235\n\t" \
    "v_permlane16_swap_b32 v236, v238\n\t" \
    "v_permlane16_swap_b32 v237, v239\n\t" \
    "s_waitcnt vmcnt(0)\n\t" \
    ".Lr17_%=:\n\t" \
    "v_max_u32 v196, v220, v221\n\t" \
    "v_max3_u32 v196, v222, v223, v196\n\t" \
    "v_max3_u32 v196, v224, v225, v196\n\t" \
    "v_max3_u32 v196, v226, v227, v196\n\t" \
    "v_max3_u32 v196, v228, v229, v196\n\t" \
    "v_max3_u32 v196, v230, v231, v196\n\t" \
    "v_cmp_eq_u32 vcc, -1, v196\n\t" \
    "s_cbranch_vccz .Lr18_%=\n\t" \
    "s_cmp_eq_u32 s41, 0\n\t" \
    "s_cbranch_scc1 .Lr18_%=\n\t" \
    "s_sub_u32 s41, s41, 1\n\t" \
    "v_max_u32 v196, v220, v221\n\t" \
    "v_max3_u32 v196, v222, v223, v196\n\t" \
    "v_cmp_eq_u32 vcc, -1, v196\n\t" \
    "s_cbranch_vccz .Lr19_%=\n\t" \
    "buffer_load_dwordx4 v[220:223], %[l16], %[rs], s50 offen offset:1024 sc1\n\t" \
    ".Lr19_%=:\n\t" \
    "v_max_u32 v196, v224, v225\n\t" \
    "v_max3_u32 v196, v226, v227, v196\n\t" \
    "v_cmp_eq_u32 vcc, -1, v196\n\t" \
    "s_cbranch_vccz .Lr20_%=\n\t" \
    "buffer_load_dwordx4 v[224:227], %[l16], %[rs], s50 offen offset:2048 sc1\n\t" \
    ".Lr20_%=:\n\t" \
    "v_max_u32 v196, v228, v229\n\t" \
    "v_max3_u32 v196, v230, v231, v196\n\t" \
    "v_cmp_eq_u32 vcc, -1, v196\n\t" \
    "s_cbranch_vccz .Lr21_%=\n\t" \
    "buffer_load_dwordx4 v[228:231], %[l16], %[rs], s50 offen offset:3072 sc1\n\t" \
    ".Lr21_%=:\n\t" \
    "s_waitcnt vmcnt(0)\n\t" \
    "s_branch .Lr17_%=\n\t" \
    ".Lr18_%=:\n\t" \
    "v_mfma_f32_16x16x32_bf16 v[240:243], v[232:235], v[220:223], 0\n\t" \
    "v_mfma_f32_16x16x32_bf16 v[240:243], v[236:239], v[224:227], v[240:243]\n\t" \
    "v_mfma_f32_16x16x32_bf16 v[240:243], v[180:183], v[228:231], v[240:243]\n\t" \
    "s_nop 7\n\t" \
    "ds_write_b128 %[pw], v[240:243]\n\t" \
    "s_waitcnt lgkmcnt(0)\n\t" \
    "s_barrier \n\t" \
    LST5 \
    LJIT \
    "buffer_load_dwordx4 v[200:203], %[l16], %[rs], s49 offen offset:0 sc1\n\t" \
    "buffer_load_dwordx4 v[204:207], %[l16], %[rs], s49 offen offset:1024 sc1\n\t" \
    "buffer_load_dwordx4 v[208:211], %[l16], %[rs], s49 offen offset:2048 sc1\n\t" \
    "buffer_load_dwordx4 v[212:215], %[l16], %[rs], s49 offen offset:3072 sc1\n\t" \
    "buffer_load_dwordx4 v[216:219], %[l16], %[rs], s50 offen offset:0 sc1\n\t" \
    "s_cmp_lg_u32 %[wave], 6\n\t" \
    "s_cbranch_scc1 .Lr22_%=\n\t" \
    "ds_read_b128 v[128:131], %[pr] offset:0\n\t" \
    "ds_read_b128 v[132:135], %[pr] offset:1024\n\t" \
    "ds_read_b128 v[136:139], %[pr] offset:2048\n\t" \
    "ds_read_b128 v[140:143], %[pr] offset:3072\n\t" \
    "ds_read_b128 v[144:147], %[pr] offset:4096\n\t" \
    "ds_read_b128 v[148:151], %[pr] offset:5120\n\t" \
    "ds_read_b128 v[152:155], %[pr] offset:6144\n\t" \
    "ds_read_b128 v[156:159], %[pr] offset:7168\n\t" \
    "v_lshlrev_b32 v176, 16, v250\n\t" \
    "v_and_b32 v177, 0xffff0000, v250\n\t" \
    "v_lshlrev_b32 v178, 16, v251\n\t" \
    "v_and_b32 v179, 0xffff0000, v251\n\t" \
    "s_waitcnt lgkmcnt(6)\n\t" \
    "v_pk_add_f32 v[128:129], v[128:129], v[132:133]\n\t" \
    "v_pk_add_f32 v[130:131], v[130:131], v[134:135]\n\t" \
    "s_waitcnt lgkmcnt(4)\n\t" \
    "v_pk_add_f32 v[136:137], v[136:137], v[140:141]\n\t" \
    "v_pk_add_f32 v[138:139], v[138:139], v[142:143]\n\t" \
    "v_pk_add_f32 v[128:129], v[128:129], v[136:137]\n\t" \
    "v_pk_add_f32 v[130:131], v[130:131], v[138:139]\n\t" \
    "s_waitcnt lgkmcnt(2)\n\t" \
    "v_pk_add_f32 v[144:145], v[144:145], v[148:149]\n\t" \
    "v_pk_add_f32 v[146:147], v[146:147], v[150:151]\n\t" \
    "s_waitcnt lgkmcnt(0)\n\t" \
    "v_pk_add_f32 v[152:153], v[152:153], v[156:157]\n\t" \
    "v_pk_add_f32 v[154:155], v[154:155], v[158:159]\n\t" \
    "v_pk_add_f32 v[144:145], v[144:145], v[152:153]\n\t" \
    "v_pk_add_f32 v[146:147], v[146:147], v[154:155]\n\t" \
    "v_pk_add_f32 v[128:129], v[128:129], v[144:145]\n\t" \
    "v_pk_add_f32 v[130:131], v[130:131], v[146:147]\n\t" \
    "v_pk_add_f32 v[128:129], v[128:129], v[176:177]\n\t" \
    "v_pk_add_f32 v[130:131], v[130:131], v[178:179]\n\t" \
    "v_cvt_pk_bf16_f32 v128, v128, v129\n\t" \
    "v_cvt_pk_bf16_f32 v129, v130, v131\n\t" \
    "v_min_u32 v128, -2, v128\n\t" \
    "v_min_u32 v129, -2, v129\n\t" \
    "s_add_u32 s44, s52, 0x1000000\n\t" \
    "buffer_store_dwordx2 v[128:129], %[xo], %[rs], s44 offen sc1\n\t" \
    "s_waitcnt vmcnt(1)\n\t" \
    "s_branch .Lr23_%=\n\t" \
    ".Lr22_%=:\n\t" \
    "s_waitcnt vmcnt(0)\n\t" \
    ".Lr23_%=:\n\t" \
    ".Lr24_%=:\n\t" \
    "v_max_u32 v196, v200, v201\n\t" \
    "v_max3_u32 v196, v202, v203, v196\n\t" \
    "v_max3_u32 v196, v204, v205, v196\n\t" \
    "v_max3_u32 v196, v206, v207, v196\n\t" \
    "v_max3_u32 v196, v208, v209, v196\n\t" \
    "v_max3_u32 v196, v210, v211, v196\n\t" \
    "v_max3_u32 v196, v212, v213, v196\n\t" \
    "v_max3_u32 v196, v214, v215, v196\n\t" \
    "v_max3_u32 v196, v216, v217, v196\n\t" \
    "v_max3_u32 v196, v218, v219, v196\n\t" \
    "v_cmp_eq_u32 vcc, -1, v196\n\t" \
    "s_cbranch_vccz .Lr25_%=\n\t" \
    "s_cmp_eq_u32 s41, 0\n\t" \
    "s_cbranch_scc1 .Lr25_%=\n\t" \
    "s_sub_u32 s41, s41, 1\n\t" \
    "v_max_u32 v196, v200, v201\n\t" \
    "v_max3_u32 v196, v202, v203, v196\n\t" \
    "v_cmp_eq_u32 vcc, -1, v196\n\t" \
    "s_cbranch_vccz .Lr26_%=\n\t" \
    "buffer_load_dwordx4 v[200:203], %[l16], %[rs], s49 offen offset:0 sc1\n\t" \
    ".Lr26_%=:\n\t" \
    "v_max_u32 v196, v204, v205\n\t" \
    "v_max3_u32 v196, v206, v207, v196\n\t" \
    "v_cmp_eq_u32 vcc, -1, v196\n\t" \
    "s_cbranch_vccz .Lr27_%=\n\t" \
    "buffer_load_dwordx4 v[204:207], %[l16], %[rs], s49 offen offset:1024 sc1\n\t" \
    ".Lr27_%=:\n\t" \
    "v_max_u32 v196, v208, v209\n\t" \
    "v_max3_u32 v196, v210, v211, v196\n\t" \
    "v_cmp_eq_u32 vcc, -1, v196\n\t" \
    "s_cbranch_vccz .Lr28_%=\n\t" \
    "buffer_load_dwordx4 v[208:211], %[l16], %[rs], s49 offen offset:2048 sc1\n\t" \
    ".Lr28_%=:\n\t" \
    "v_max_u32 v196, v212, v213\n\t" \
    "v_max3_u32 v196, v214, v215, v196\n\t" \
    "v_cmp_eq_u32 vcc, -1, v196\n\t" \
    "s_cbranch_vccz .Lr29_%=\n\t" \
    "buffer_load_dwordx4 v[212:215], %[l16], %[rs], s49 offen offset:3072 sc1\n\t" \
    ".Lr29_%=:\n\t" \
    "v_max_u32 v196, v216, v217\n\t" \
    "v_max3_u32 v196, v218, v219, v196\n\t" \
    "v_cmp_eq_u32 vcc, -1, v196\n\t" \
    "s_cbranch_vccz .Lr30_%=\n\t" \
    "buffer_load_dwordx4 v[216:219], %[l16], %[rs], s50 offen offset:0 sc1\n\t" \
    ".Lr30_%=:\n\t" \
    "s_waitcnt vmcnt(0)\n\t" \
    "s_branch .Lr24_%=\n\t" \
    ".Lr25_%=:\n\t" \
    "v_mfma_f32_16x16x32_bf16 v[244:247], v[160:163], v[200:203], 0\n\t" \
    "v_mfma_f32_16x16x32_bf16 v[244:247], v[164:167], v[204:207], v[244:247]\n\t" \
    "v_mfma_f32_16x16x32_bf16 v[244:247], v[168:171], v[208:211], v[244:247]\n\t" \
    "v_mfma_f32_16x16x32_bf16 v[244:247], v[172:175], v[212:215], v[244:247]\n\t" \
    "v_mfma_f32_16x16x32_bf16 v[244:247], v[180:183], v[216:219], v[244:247]\n\t" \
    "s_nop 7\n\t" \
    "ds_write_b128 %[pw], v[244:247] offset:8192\n\t" \
    "s_waitcnt lgkmcnt(0)\n\t" \
    "s_barrier \n\t" \
    LST4 \
    LJIT \
    "s_cmp_lg_u32 %[wave], 7\n\t" \
    "s_cbranch_scc1 .Lr31_%=\n\t" \
    "ds_read_b128 v[128:131], %[pr] offset:0\n\t" \
    "ds_read_b128 v[132:135], %[pr] offset:1024\n\t" \
    "ds_read_b128 v[136:139], %[pr] offset:2048\n\t" \
    "ds_read_b128 v[140:143], %[pr] offset:3072\n\t" \
    "ds_read_b128 v[144:147], %[pr] offset:4096\n\t" \
    "ds_read_b128 v[148:151], %[pr] offset:5120\n\t" \
    "ds_read_b128 v[152:155], %[pr] offset:6144\n\t" \
    "ds_read_b128 v[156:159], %[pr] offset:7168\n\t" \
    "v_lshlrev_b32 v176, 16, v250\n\t" \
    "v_and_b32 v177, 0xffff0000, v250\n\t" \
    "v_lshlrev_b32 v178, 16, v251\n\t" \
    "v_and_b32 v179, 0xffff0000, v251\n\t" \
    "s_waitcnt lgkmcnt(6)\n\t" \
    "v_pk_add_f32 v[128:129], v[128:129], v[132:133]\n\t" \
    "v_pk_add_f32 v[130:131], v[130:131], v[134:135]\n\t" \
    "s_waitcnt lgkmcnt(4)\n\t" \
    "v_pk_add_f32 v[136:137], v[136:137], v[140:141]\n\t" \
    "v_pk_add_f32 v[138:139], v[138:139], v[142:143]\n\t" \
    "v_pk_add_f32 v[128:129], v[128:129], v[136:137]\n\t" \
    "v_pk_add_f32 v[130:131], v[130:131], v[138:139]\n\t" \
    "s_waitcnt lgkmcnt(2)\n\t" \
    "v_pk_add_f32 v[144:145], v[144:145], v[148:149]\n\t" \
    "v_pk_add_f32 v[146:147], v[146:147], v[150:151]\n\t" \
    "s_waitcnt lgkmcnt(0)\n\t" \
    "v_pk_add_f32 v[152:153], v[152:153], v[156:157]\n\t" \
    "v_pk_add_f32 v[154:155], v[154:155], v[158:159]\n\t" \
    "v_pk_add_f32 v[144:145], v[144:145], v[152:153]\n\t" \
    "v_pk_add_f32 v[146:147], v[146:147], v[154:155]\n\t" \
    "v_pk_add_f32 v[128:129], v[128:129], v[144:145]\n\t" \
    "v_pk_add_f32 v[130:131], v[130:131], v[146:147]\n\t" \
    "v_pk_add_f32 v[128:129], v[128:129], v[176:177]\n\t" \
    "v_pk_add_f32 v[130:131], v[130:131], v[178:179]\n\t" \
    "v_cvt_pk_bf16_f32 v128, v128, v129\n\t" \
    "v_cvt_pk_bf16_f32 v129, v130, v131\n\t" \
    "v_min_u32 v128, -2, v128\n\t" \
    "v_min_u32 v129, -2, v129\n\t" \
    "s_add_u32 s44, s52, 0x1000000\n\t" \
    "buffer_store_dwordx2 v[128:129], %[xo], %[rs], s44 offen sc1\n\t" \
    ".Lr31_%=:\n\t" \
    "buffer_store_dwordx4 v[252:255], %[ov], %[rs], s46 offen sc1\n\t" \
    "buffer_store_dwordx4 v[252:255], v197, %[rs], s51 offen sc1\n\t" \
    LFLUSHR \
    "s_add_u32 s40, s40, 1\n\t" \
    "s_cmp_lg_u32 s40, 64\n\t" \
    "s_cbranch_scc1 .Lr1_%=\n\t" \
    "s_branch .Lend%=\n\t" \
    ".Lend%=:\n\t" \
    "s_waitcnt vmcnt(0) lgkmcnt(0)\n\t" \
    "s_mov_b32 %[bud], s41\n\t"
#define ASM_W4_MUL \
    "s_mov_b32 s40, 0\n\t" \
    "s_mov_b32 s41, 0x40000\n\t" \
    LSTINIT \
    "s_mov_b32 s43, 0\n\t" \
    "s_mov_b32 s46, %[hb]\n\t" \
    "s_add_u32 s47, s46, 0x4000\n\t" \
    "v_mov_b64 v[188:189], -1\n\t" \
    "v_mov_b32 v246, 0x1e000\n\t" \
    ".Lm1_%=:\n\t" \
    LST0 \
    LJIT \
    ".Lm2_%=:\n\t" \
    "ds_read_b32 v245, v246 offset:24\n\t" \
    "s_sub_i32 s44, s40, 3\n\t" \
    "s_waitcnt lgkmcnt(0)\n\t" \
    "v_cmp_gt_i32 vcc, s44, v245\n\t" \
    "s_cbranch_vccnz .Lm2_%=\n\t" \
    "s_add_u32 s44, s40, 1\n\t" \
    "s_lshr_b32 s45, s44, 1\n\t" \
    "s_and_b32 s44, s44, 1\n\t" \
    "s_lshl_b32 s44, s44, 12\n\t" \
    "s_mul_i32 s48, s45, 0x30000\n\t" \
    "s_add_u32 s48, s48, s44\n\t" \
    "s_lshr_b32 s45, s45, 3\n\t" \
    "s_mul_i32 s45, s45, 0x82000\n\t" \
    "s_add_u32 s48, s48, s45\n\t" \
    "s_add_u32 s48, s48, %[hb]\n\t" \
    "s_add_u32 s49, s48, 0x4000\n\t" \
    "s_cmp_lg_u32 s43, 0\n\t" \
    "s_cbranch_scc1 .Lm5_%=\n\t" \
    ".Lm3_%=:\n\t" \
    "v_max_u32 v244, v188, v189\n\t" \
    "v_max3_u32 v244, v190, v191, v244\n\t" \
    "v_cmp_eq_u32 vcc, -1, v244\n\t" \
    "s_cbranch_vccz .Lm4_%=\n\t" \
    "s_cmp_eq_u32 s41, 0\n\t" \
    "s_cbranch_scc1 .Lm4_%=\n\t" \
    "s_sub_u32 s41, s41, 1\n\t" \
    "s_sleep 3\n\t" \
    "buffer_load_dwordx4 v[188:191], %[l16], %[rs], s47 offen offset:3072 sc1\n\t" \
    "s_waitcnt vmcnt(0)\n\t" \
    "s_branch .Lm3_%=\n\t" \
    ".Lm4_%=:\n\t" \
    "buffer_load_dwordx4 v[184:187], %[l16], %[rs], s47 offen offset:2048 sc1\n\t" \
    "buffer_load_dwordx4 v[180:183], %[l16], %[rs], s47 offen offset:1024 sc1\n\t" \
    "buffer_load_dwordx4 v[176:179], %[l16], %[rs], s47 offen offset:0 sc1\n\t" \
    "buffer_load_dwordx4 v[172:175], %[l16], %[rs], s46 offen offset:3072 sc1\n\t" \
    "buffer_load_dwordx4 v[168:171], %[l16], %[rs], s46 offen offset:2048 sc1\n\t" \
    "buffer_load_dwordx4 v[164:167], %[l16], %[rs], s46 offen offset:1024 sc1\n\t" \
    "buffer_load_dwordx4 v[160:163], %[l16], %[rs], s46 offen offset:0 sc1\n\t" \
    ".Lm5_%=:\n\t" \
    LST1 \
    LJIT \
    "v_mfma_f32_16x16x32_bf16 v[224:227], v[28:31], v[188:191], 0\n\t" \
    "v_mfma_f32_16x16x32_bf16 v[228:231], v[60:63], v[188:191], 0\n\t" \
    "v_mfma_f32_16x16x32_bf16 v[232:235], v[92:95], v[188:191], 0\n\t" \
    "v_mfma_f32_16x16x32_bf16 v[236:239], v[124:127], v[188:191], 0\n\t" \
    "v_mfma_f32_16x16x32_bf16 v[240:243], v[156:159], v[188:191], 0\n\t" \
    "s_waitcnt vmcnt(6)\n\t" \
    ".Lm6_%=:\n\t" \
    "v_max_u32 v244, v184, v185\n\t" \
    "v_max3_u32 v244, v186, v187, v244\n\t" \
    "v_cmp_eq_u32 vcc, -1, v244\n\t" \
    "s_cbranch_vccnz .Lm7_%=\n\t" \
    ".Lm8_%=:\n\t" \
    "v_mfma_f32_16x16x32_bf16 v[224:227], v[24:27], v[184:187], v[224:227]\n\t" \
    "v_mfma_f32_16x16x32_bf16 v[228:231], v[56:59], v[184:187], v[228:231]\n\t" \
    "v_mfma_f32_16x16x32_bf16 v[232:235], v[88:91], v[184:187], v[232:235]\n\t" \
    "v_mfma_f32_16x16x32_bf16 v[236:239], v[120:123], v[184:187], v[236:239]\n\t" \
    "v_mfma_f32_16x16x32_bf16 v[240:243], v[152:155], v[184:187], v[240:243]\n\t" \
    "s_waitcnt vmcnt(5)\n\t" \
    ".Lm9_%=:\n\t" \
    "v_max_u32 v244, v180, v181\n\t" \
    "v_max3_u32 v244, v182, v183, v244\n\t" \
    "v_cmp_eq_u32 vcc, -1, v244\n\t" \
    "s_cbranch_vccnz .Lm10_%=\n\t" \
    ".Lm11_%=:\n\t" \
    "v_mfma_f32_16x16x32_bf16 v[224:227], v[20:23], v[180:183], v[224:227]\n\t" \
    "v_mfma_f32_16x16x32_bf16 v[228:231], v[52:55], v[180:183], v[228:231]\n\t" \
    "v_mfma_f32_16x16x32_bf16 v[232:235], v[84:87], v[180:183], v[232:235]\n\t" \
    "v_mfma_f32_16x16x32_bf16 v[236:239], v[116:119], v[180:183], v[236:239]\n\t" \
    "v_mfma_f32_16x16x32_bf16 v[240:243], v[148:151], v[180:183], v[240:243]\n\t" \
    "s_waitcnt vmcnt(4)\n\t" \
    ".Lm12_%=:\n\t" \
    "v_max_u32 v244, v176, v177\n\t" \
    "v_max3_u32 v244, v178, v179, v244\n\t" \
    "v_cmp_eq_u32 vcc, -1, v244\n\t" \
    "s_cbranch_vccnz .Lm13_%=\n\t" \
    ".Lm14_%=:\n\t" \
    "v_mfma_f32_16x16x32_bf16 v[224:227], v[16:19], v[176:179], v[224:227]\n\t" \
    "v_mfma_f32_16x16x32_bf16 v[228:231], v[48:51], v[176:179], v[228:231]\n\t" \
    "v_mfma_f32_16x16x32_bf16 v[232:235], v[80:83], v[176:179], v[232:235]\n\t" \
    "v_mfma_f32_16x16x32_bf16 v[236:239], v[112:115], v[176:179], v[236:239]\n\t" \
    "v_mfma_f32_16x16x32_bf16 v[240:243], v[144:147], v[176:179], v[240:243]\n\t" \
    "s_waitcnt vmcnt(3)\n\t" \
    ".Lm15_%=:\n\t" \
    "v_max_u32 v244, v172, v173\n\t" \
    "v_max3_u32 v244, v174, v175, v244\n\t" \
    "v_cmp_eq_u32 vcc, -1, v244\n\t" \
    "s_cbranch_vccnz .Lm16_%=\n\t" \
    ".Lm17_%=:\n\t" \
    "v_mfma_f32_16x16x32_bf16 v[224:227], v[12:15], v[172:175], v[224:227]\n\t" \
    "v_mfma_f32_16x16x32_bf16 v[228:231], v[44:47], v[172:175], v[228:231]\n\t" \
    "v_mfma_f32_16x16x32_bf16 v[232:235], v[76:79], v[172:175], v[232:235]\n\t" \
    "v_mfma_f32_16x16x32_bf16 v[236:239], v[108:111], v[172:175], v[236:239]\n\t" \
    "v_mfma_f32_16x16x32_bf16 v[240:243], v[140:143], v[172:175], v[240:243]\n\t" \
    "s_waitcnt vmcnt(2)\n\t" \
    ".Lm18_%=:\n\t" \
    "v_max_u32 v244, v168, v169\n\t" \
    "v_max3_u32 v244, v170, v171, v244\n\t" \
    "v_cmp_eq_u32 vcc, -1, v244\n\t" \
    "s_cbranch_vccnz .Lm19_%=\n\t" \
    ".Lm20_%=:\n\t" \
    "buffer_load_dwordx4 v[220:223], %[l16], %[rs], s49 offen offset:3072 sc1\n\t" \
    "v_mfma_f32_16x16x32_bf16 v[224:227], v[8:11], v[168:171], v[224:227]\n\t" \
    "v_mfma_f32_16x16x32_bf16 v[228:231], v[40:43], v[168:171], v[228:231]\n\t" \
    "v_mfma_f32_16x16x32_bf16 v[232:235], v[72:75], v[168:171], v[232:235]\n\t" \
    "v_mfma_f32_16x16x32_bf16 v[236:239], v[104:107], v[168:171], v[236:239]\n\t" \
    "v_mfma_f32_16x16x32_bf16 v[240:243], v[136:139], v[168:171], v[240:243]\n\t" \
    "s_waitcnt vmcnt(2)\n\t" \
    ".Lm21_%=:\n\t" \
    "v_max_u32 v244, v164, v165\n\t" \
    "v_max3_u32 v244, v166, v167, v244\n\t" \
    "v_cmp_eq_u32 vcc, -1, v244\n\t" \
    "s_cbranch_vccnz .Lm22_%=\n\t" \
    ".Lm23_%=:\n\t" \
    "v_mfma_f32_16x16x32_bf16 v[224:227], v[4:7], v[164:167], v[224:227]\n\t" \
    "v_mfma_f32_16x16x32_bf16 v[228:231], v[36:39], v[164:167], v[228:231]\n\t" \
    "v_mfma_f32_16x16x32_bf16 v[232:235], v[68:71], v[164:167], v[232:235]\n\t" \
    "v_mfma_f32_16x16x32_bf16 v[236:239], v[100:103], v[164:167], v[236:239]\n\t" \
    "v_mfma_f32_16x16x32_bf16 v[240:243], v[132:135], v[164:167], v[240:243]\n\t" \
    "s_waitcnt vmcnt(1)\n\t" \
    ".Lm24_%=:\n\t" \
    "v_max_u32 v244, v160, v161\n\t" \
    "v_max3_u32 v244, v162, v163, v244\n\t" \
    "v_cmp_eq_u32 vcc, -1, v244\n\t" \
    "s_cbranch_vccnz .Lm25_%=\n\t" \
    ".Lm26_%=:\n\t" \
    "v_mfma_f32_16x16x32_bf16 v[224:227], v[0:3], v[160:163], v[224:227]\n\t" \
    "v_mfma_f32_16x16x32_bf16 v[228:231], v[32:35], v[160:163], v[228:231]\n\t" \
    "v_mfma_f32_16x16x32_bf16 v[232:235], v[64:67], v[160:163], v[232:235]\n\t" \
    "v_mfma_f32_16x16x32_bf16 v[236:239], v[96:99], v[160:163], v[236:239]\n\t" \
    "v_mfma_f32_16x16x32_bf16 v[240:243], v[128:131], v[160:163], v[240:243]\n\t" \
    "s_add_u32 s40, s40, 1\n\t" \
    "v_mov_b32 v245, s40\n\t" \
    "ds_write_b128 %[ld], v[224:227] offset:0\n\t" \
    "ds_write_b128 %[ld], v[228:231] offset:6144\n\t" \
    "ds_write_b128 %[ld], v[232:235] offset:12288\n\t" \
    "s_nop 0\n\t" \
    "ds_write_b128 %[ld], v[236:239] offset:18432\n\t" \
    "s_nop 1\n\t" \
    "ds_write_b128 %[ld], v[240:243] offset:24576\n\t" \
    "ds_write_b32 %[fa], v245\n\t" \
    LST2 \
    LJIT \
    "s_mov_b32 s43, 0\n\t" \
    "s_waitcnt vmcnt(0)\n\t" \
    "v_max_u32 v244, v220, v221\n\t" \
    "v_max3_u32 v244, v222, v223, v244\n\t" \
    "v_cmp_eq_u32 vcc, -1, v244\n\t" \
    "s_cbranch_vccnz .Lm27_%=\n\t" \
    "buffer_load_dwordx4 v[216:219], %[l16], %[rs], s49 offen offset:2048 sc1\n\t" \
    "buffer_load_dwordx4 v[212:215], %[l16], %[rs], s49 offen offset:1024 sc1\n\t" \
    "buffer_load_dwordx4 v[208:211], %[l16], %[rs], s49 offen offset:0 sc1\n\t" \
    "buffer_load_dwordx4 v[204:207], %[l16], %[rs], s48 offen offset:3072 sc1\n\t" \
    "buffer_load_dwordx4 v[200:203], %[l16], %[rs], s48 offen offset:2048 sc1\n\t" \
    "buffer_load_dwordx4 v[196:199], %[l16], %[rs], s48 offen offset:1024 sc1\n\t" \
    "buffer_load_dwordx4 v[192:195], %[l16], %[rs], s48 offen offset:0 sc1\n\t" \
    "s_mov_b32 s43, 1\n\t" \
    ".Lm27_%=:\n\t" \
    LST0 \
    LJIT \
    ".Lm28_%=:\n\t" \
    "ds_read_b32 v245, v246 offset:28\n\t" \
    "s_sub_i32 s44, s40, 3\n\t" \
    "s_waitcnt lgkmcnt(0)\n\t" \
    "v_cmp_gt_i32 vcc, s44, v245\n\t" \
    "s_cbranch_vccnz .Lm28_%=\n\t" \
    "s_add_u32 s44, s40, 1\n\t" \
    "s_lshr_b32 s45, s44, 1\n\t" \
    "s_and_b32 s44, s44, 1\n\t" \
    "s_lshl_b32 s44, s44, 12\n\t" \
    "s_mul_i32 s46, s45, 0x30000\n\t" \
    "s_add_u32 s46, s46, s44\n\t" \
    "s_lshr_b32 s45, s45, 3\n\t" \
    "s_mul_i32 s45, s45, 0x82000\n\t" \
    "s_add_u32 s46, s46, s45\n\t" \
    "s_add_u32 s46, s46, %[hb]\n\t" \
    "s_add_u32 s47, s46, 0x4000\n\t" \
    "s_cmp_lg_u32 s43, 0\n\t" \
    "s_cbranch_scc1 .Lm31_%=\n\t" \
    ".Lm29_%=:\n\t" \
    "v_max_u32 v244, v220, v221\n\t" \
    "v_max3_u32 v244, v222, v223, v244\n\t" \
    "v_cmp_eq_u32 vcc, -1, v244\n\t" \
    "s_cbranch_vccz .Lm30_%=\n\t" \
    "s_cmp_eq_u32 s41, 0\n\t" \
    "s_cbranch_scc1 .Lm30_%=\n\t" \
    "s_sub_u32 s41, s41, 1\n\t" \
    "s_sleep 3\n\t" \
    "buffer_load_dwordx4 v[220:223], %[l16], %[rs], s49 offen offset:3072 sc1\n\t" \
    "s_waitcnt vmcnt(0)\n\t" \
    "s_branch .Lm29_%=\n\t" \
    ".Lm30_%=:\n\t" \
    "buffer_load_dwordx4 v[216:219], %[l16], %[rs], s49 offen offset:2048 sc1\n\t" \
    "buffer_load_dwordx4 v[212:215], %[l16], %[rs], s49 offen offset:1024 sc1\n\t" \
    "buffer_load_dwordx4 v[208:211], %[l16], %[rs], s49 offen offset:0 sc1\n\t" \
    "buffer_load_dwordx4 v[204:207], %[l16], %[rs], s48 offen offset:3072 sc1\n\t" \
    "buffer_load_dwordx4 v[200:203], %[l16], %[rs], s48 offen offset:2048 sc1\n\t" \
    "buffer_load_dwordx4 v[196:199], %[l16], %[rs], s48 offen offset:1024 sc1\n\t" \
    "buffer_load_dwordx4 v[192:195], %[l16], %[rs], s48 offen offset:0 sc1\n\t" \
    ".Lm31_%=:\n\t" \
    LST1 \
    LJIT \
    "v_mfma_f32_16x16x32_bf16 v[224:227], v[28:31], v[220:223], 0\n\t" \
    "v_mfma_f32_16x16x32_bf16 v[228:231], v[60:63], v[220:223], 0\n\t" \
    "v_mfma_f32_16x16x32_bf16 v[232:235], v[92:95], v[220:223], 0\n\t" \
    "v_mfma_f32_16x16x32_bf16 v[236:239], v[124:127], v[220:223], 0\n\t" \
    "v_mfma_f32_16x16x32_bf16 v[240:243], v[156:159], v[220:223], 0\n\t" \
    "s_waitcnt vmcnt(6)\n\t" \
    ".Lm32_%=:\n\t" \
    "v_max_u32 v244, v216, v217\n\t" \
    "v_max3_u32 v244, v218, v219, v244\n\t" \
    "v_cmp_eq_u32 vcc, -1, v244\n\t" \
    "s_cbranch_vccnz .Lm33_%=\n\t" \
    ".Lm34_%=:\n\t" \
    "v_mfma_f32_16x16x32_bf16 v[224:227], v[24:27], v[216:219], v[224:227]\n\t" \
    "v_mfma_f32_16x16x32_bf16 v[228:231], v[56:59], v[216:219], v[228:231]\n\t" \
    "v_mfma_f32_16x16x32_bf16 v[232:235], v[88:91], v[216:219], v[232:235]\n\t" \
    "v_mfma_f32_16x16x32_bf16 v[236:239], v[120:123], v[216:219], v[236:239]\n\t" \
    "v_mfma_f32_16x16x32_bf16 v[240:243], v[152:155], v[216:219], v[240:243]\n\t" \
    "s_waitcnt vmcnt(5)\n\t" \
    ".Lm35_%=:\n\t" \
    "v_max_u32 v244, v212, v213\n\t" \
    "v_max3_u32 v244, v214, v215, v244\n\t" \
    "v_cmp_eq_u32 vcc, -1, v244\n\t" \
    "s_cbranch_vccnz .Lm36_%=\n\t" \
    ".Lm37_%=:\n\t" \
    "v_mfma_f32_16x16x32_bf16 v[224:227], v[20:23], v[212:215], v[224:227]\n\t" \
    "v_mfma_f32_16x16x32_bf16 v[228:231], v[52:55], v[212:215], v[228:231]\n\t" \
    "v_mfma_f32_16x16x32_bf16 v[232:235], v[84:87], v[212:215], v[232:235]\n\t" \
    "v_mfma_f32_16x16x32_bf16 v[236:239], v[116:119], v[212:215], v[236:239]\n\t" \
    "v_mfma_f32_16x16x32_bf16 v[240:243], v[148:151], v[212:215], v[240:243]\n\t" \
    "s_waitcnt vmcnt(4)\n\t" \
    ".Lm38_%=:\n\t" \
    "v_max_u32 v244, v208, v209\n\t" \
    "v_max3_u32 v244, v210, v211, v244\n\t" \
    "v_cmp_eq_u32 vcc, -1, v244\n\t" \
    "s_cbranch_vccnz .Lm39_%=\n\t" \
    ".Lm40_%=:\n\t" \
    "v_mfma_f32_16x16x32_bf16 v[224:227], v[16:19], v[208:211], v[224:227]\n\t" \
    "v_mfma_f32_16x16x32_bf16 v[228:231], v[48:51], v[208:211], v[228:231]\n\t" \
    "v_mfma_f32_16x16x32_bf16 v[232:235], v[80:83], v[208:211], v[232:235]\n\t" \
    "v_mfma_f32_16x16x32_bf16 v[236:239], v[112:115], v[208:211], v[236:239]\n\t" \
    "v_mfma_f32_16x16x32_bf16 v[240:243], v[144:147], v[208:211], v[240:243]\n\t" \
    "s_waitcnt vmcnt(3)\n\t" \
    ".Lm41_%=:\n\t" \
    "v_max_u32 v244, v204, v205\n\t" \
    "v_max3_u32 v244, v206, v207, v244\n\t" \
    "v_cmp_eq_u32 vcc, -1, v244\n\t" \
    "s_cbranch_vccnz .Lm42_%=\n\t" \
    ".Lm43_%=:\n\t" \
    "v_mfma_f32_16x16x32_bf16 v[224:227], v[12:15], v[204:207], v[224:227]\n\t" \
    "v_mfma_f32_16x16x32_bf16 v[228:231], v[44:47], v[204:207], v[228:231]\n\t" \
    "v_mfma_f32_16x16x32_bf16 v[232:235], v[76:79], v[204:207], v[232:235]\n\t" \
    "v_mfma_f32_16x16x32_bf16 v[236:239], v[108:111], v[204:207], v[236:239]\n\t" \
    "v_mfma_f32_16x16x32_bf16 v[240:243], v[140:143], v[204:207], v[240:243]\n\t" \
    "s_waitcnt vmcnt(2)\n\t" \
    ".Lm44_%=:\n\t" \
    "v_max_u32 v244, v200, v201\n\t" \
    "v_max3_u32 v244, v202, v203, v244\n\t" \
    "v_cmp_eq_u32 vcc, -1, v244\n\t" \
    "s_cbranch_vccnz .Lm45_%=\n\t" \
    ".Lm46_%=:\n\t" \
    "buffer_load_dwordx4 v[188:191], %[l16], %[rs], s47 offen offset:3072 sc1\n\t" \
    "v_mfma_f32_16x16x32_bf16 v[224:227], v[8:11], v[200:203], v[224:227]\n\t" \
    "v_mfma_f32_16x16x32_bf16 v[228:231], v[40:43], v[200:203], v[228:231]\n\t" \
    "v_mfma_f32_16x16x32_bf16 v[232:235], v[72:75], v[200:203], v[232:235]\n\t" \
    "v_mfma_f32_16x16x32_bf16 v[236:239], v[104:107], v[200:203], v[236:239]\n\t" \
    "v_mfma_f32_16x16x32_bf16 v[240:243], v[136:139], v[200:203], v[240:243]\n\t" \
    "s_waitcnt vmcnt(2)\n\t" \
    ".Lm47_%=:\n\t" \
    "v_max_u32 v244, v196, v197\n\t" \
    "v_max3_u32 v244, v198, v199, v244\n\t" \
    "v_cmp_eq_u32 vcc, -1, v244\n\t" \
    "s_cbranch_vccnz .Lm48_%=\n\t" \
    ".Lm49_%=:\n\t" \
    "v_mfma_f32_16x16x32_bf16 v[224:227], v[4:7], v[196:199], v[224:227]\n\t" \
    "v_mfma_f32_16x16x32_bf16 v[228:231], v[36:39], v[196:199], v[228:231]\n\t" \
    "v_mfma_f32_16x16x32_bf16 v[232:235], v[68:71], v[196:199], v[232:235]\n\t" \
    "v_mfma_f32_16x16x32_bf16 v[236:239], v[100:103], v[196:199], v[236:239]\n\t" \
    "v_mfma_f32_16x16x32_bf16 v[240:243], v[132:135], v[196:199], v[240:243]\n\t" \
    "s_waitcnt vmcnt(1)\n\t" \
    ".Lm50_%=:\n\t" \
    "v_max_u32 v244, v192, v193\n\t" \
    "v_max3_u32 v244, v194, v195, v244\n\t" \
    "v_cmp_eq_u32 vcc, -1, v244\n\t" \
    "s_cbranch_vccnz .Lm51_%=\n\t" \
    ".Lm52_%=:\n\t" \
    "v_mfma_f32_16x16x32_bf16 v[224:227], v[0:3], v[192:195], v[224:227]\n\t" \
    "v_mfma_f32_16x16x32_bf16 v[228:231], v[32:35], v[192:195], v[228:231]\n\t" \
    "v_mfma_f32_16x16x32_bf16 v[232:235], v[64:67], v[192:195], v[232:235]\n\t" \
    "v_mfma_f32_16x16x32_bf16 v[236:239], v[96:99], v[192:195], v[236:239]\n\t" \
    "v_mfma_f32_16x16x32_bf16 v[240:243], v[128:131], v[192:195], v[240:243]\n\t" \
    "s_add_u32 s40, s40, 1\n\t" \
    "v_mov_b32 v245, s40\n\t" \
    "ds_write_b128 %[ld], v[224:227] offset:30720\n\t" \
    "ds_write_b128 %[ld], v[228:231] offset:36864\n\t" \
    "ds_write_b128 %[ld], v[232:235] offset:43008\n\t" \
    "s_nop 0\n\t" \
    "ds_write_b128 %[ld], v[236:239] offset:49152\n\t" \
    "s_nop 1\n\t" \
    "ds_write_b128 %[ld], v[240:243] offset:55296\n\t" \
    "ds_write_b32 %[fa], v245\n\t" \
    LST2 \
    LJIT \
    "s_mov_b32 s43, 0\n\t" \
    "s_waitcnt vmcnt(0)\n\t" \
    "v_max_u32 v244, v188, v189\n\t" \
    "v_max3_u32 v244, v190, v191, v244\n\t" \
    "v_cmp_eq_u32 vcc, -1, v244\n\t" \
    "s_cbranch_vccnz .Lm53_%=\n\t" \
    "buffer_load_dwordx4 v[184:187], %[l16], %[rs], s47 offen offset:2048 sc1\n\t" \
    "buffer_load_dwordx4 v[180:183], %[l16], %[rs], s47 offen offset:1024 sc1\n\t" \
    "buffer_load_dwordx4 v[176:179], %[l16], %[rs], s47 offen offset:0 sc1\n\t" \
    "buffer_load_dwordx4 v[172:175], %[l16], %[rs], s46 offen offset:3072 sc1\n\t" \
    "buffer_load_dwordx4 v[168:171], %[l16], %[rs], s46 offen offset:2048 sc1\n\t" \
    "buffer_load_dwordx4 v[164:167], %[l16], %[rs], s46 offen offset:1024 sc1\n\t" \
    "buffer_load_dwordx4 v[160:163], %[l16], %[rs], s46 offen offset:0 sc1\n\t" \
    "s_mov_b32 s43, 1\n\t" \
    ".Lm53_%=:\n\t" \
    "v_sub_u32 %[ld], %[l2], %[ld]\n\t" \
    LFLUSHM \
    "s_nop 0\n\t" \
    "s_cmp_lg_u32 s40, 128\n\t" \
    "s_cbranch_scc1 .Lm1_%=\n\t" \
    "s_branch .Lend%=\n\t" \
    ".Lm7_%=:\n\t" \
    "s_cmp_eq_u32 s41, 0\n\t" \
    "s_cbranch_scc1 .Lm8_%=\n\t" \
    "s_sub_u32 s41, s41, 1\n\t" \
    "buffer_load_dwordx4 v[184:187], %[l16], %[rs], s47 offen offset:2048 sc1\n\t" \
    "s_waitcnt vmcnt(0)\n\t" \
    "s_branch .Lm6_%=\n\t" \
    ".Lm10_%=:\n\t" \
    "s_cmp_eq_u32 s41, 0\n\t" \
    "s_cbranch_scc1 .Lm11_%=\n\t" \
    "s_sub_u32 s41, s41, 1\n\t" \
    "buffer_load_dwordx4 v[180:183], %[l16], %[rs], s47 offen offset:1024 sc1\n\t" \
    "s_waitcnt vmcnt(0)\n\t" \
    "s_branch .Lm9_%=\n\t" \
    ".Lm13_%=:\n\t" \
    "s_cmp_eq_u32 s41, 0\n\t" \
    "s_cbranch_scc1 .Lm14_%=\n\t" \
    "s_sub_u32 s41, s41, 1\n\t" \
    "buffer_load_dwordx4 v[176:179], %[l16], %[rs], s47 offen offset:0 sc1\n\t" \
    "s_waitcnt vmcnt(0)\n\t" \
    "s_branch .Lm12_%=\n\t" \
    ".Lm16_%=:\n\t" \
    "s_cmp_eq_u32 s41, 0\n\t" \
    "s_cbranch_scc1 .Lm17_%=\n\t" \
    "s_sub_u32 s41, s41, 1\n\t" \
    "buffer_load_dwordx4 v[172:175], %[l16], %[rs], s46 offen offset:3072 sc1\n\t" \
    "s_waitcnt vmcnt(0)\n\t" \
    "s_branch .Lm15_%=\n\t" \
    ".Lm19_%=:\n\t" \
    "s_cmp_eq_u32 s41, 0\n\t" \
    "s_cbranch_scc1 .Lm20_%=\n\t" \
    "s_sub_u32 s41, s41, 1\n\t" \
    "buffer_load_dwordx4 v[168:171], %[l16], %[rs], s46 offen offset:2048 sc1\n\t" \
    "s_waitcnt vmcnt(0)\n\t" \
    "s_branch .Lm18_%=\n\t" \
    ".Lm22_%=:\n\t" \
    "s_cmp_eq_u32 s41, 0\n\t" \
    "s_cbranch_scc1 .Lm23_%=\n\t" \
    "s_sub_u32 s41, s41, 1\n\t" \
    "buffer_load_dwordx4 v[164:167], %[l16], %[rs], s46 offen offset:1024 sc1\n\t" \
    "s_waitcnt vmcnt(0)\n\t" \
    "s_branch .Lm21_%=\n\t" \
    ".Lm25_%=:\n\t" \
    "s_cmp_eq_u32 s41, 0\n\t" \
    "s_cbranch_scc1 .Lm26_%=\n\t" \
    "s_sub_u32 s41, s41, 1\n\t" \
    "buffer_load_dwordx4 v[160:163], %[l16], %[rs], s46 offen offset:0 sc1\n\t" \
    "s_waitcnt vmcnt(0)\n\t" \
    "s_branch .Lm24_%=\n\t" \
    ".Lm33_%=:\n\t" \
    "s_cmp_eq_u32 s41, 0\n\t" \
    "s_cbranch_scc1 .Lm34_%=\n\t" \
    "s_sub_u32 s41, s41, 1\n\t" \
    "buffer_load_dwordx4 v[216:219], %[l16], %[rs], s49 offen offset:2048 sc1\n\t" \
    "s_waitcnt vmcnt(0)\n\t" \
    "s_branch .Lm32_%=\n\t" \
    ".Lm36_%=:\n\t" \
    "s_cmp_eq_u32 s41, 0\n\t" \
    "s_cbranch_scc1 .Lm37_%=\n\t" \
    "s_sub_u32 s41, s41, 1\n\t" \
    "buffer_load_dwordx4 v[212:215], %[l16], %[rs], s49 offen offset:1024 sc1\n\t" \
    "s_waitcnt vmcnt(0)\n\t" \
    "s_branch .Lm35_%=\n\t" \
    ".Lm39_%=:\n\t" \
    "s_cmp_eq_u32 s41, 0\n\t" \
    "s_cbranch_scc1 .Lm40_%=\n\t" \
    "s_sub_u32 s41, s41, 1\n\t" \
    "buffer_load_dwordx4 v[208:211], %[l16], %[rs], s49 offen offset:0 sc1\n\t" \
    "s_waitcnt vmcnt(0)\n\t" \
    "s_branch .Lm38_%=\n\t" \
    ".Lm42_%=:\n\t" \
    "s_cmp_eq_u32 s41, 0\n\t" \
    "s_cbranch_scc1 .Lm43_%=\n\t" \
    "s_sub_u32 s41, s41, 1\n\t" \
    "buffer_load_dwordx4 v[204:207], %[l16], %[rs], s48 offen offset:3072 sc1\n\t" \
    "s_waitcnt vmcnt(0)\n\t" \
    "s_branch .Lm41_%=\n\t" \
    ".Lm45_%=:\n\t" \
    "s_cmp_eq_u32 s41, 0\n\t" \
    "s_cbranch_scc1 .Lm46_%=\n\t" \
    "s_sub_u32 s41, s41, 1\n\t" \
    "buffer_load_dwordx4 v[200:203], %[l16], %[rs], s48 offen offset:2048 sc1\n\t" \
    "s_waitcnt vmcnt(0)\n\t" \
    "s_branch .Lm44_%=\n\t" \
    ".Lm48_%=:\n\t" \
    "s_cmp_eq_u32 s41, 0\n\t" \
    "s_cbranch_scc1 .Lm49_%=\n\t" \
    "s_sub_u32 s41, s41, 1\n\t" \
    "buffer_load_dwordx4 v[196:199], %[l16], %[rs], s48 offen offset:1024 sc1\n\t" \
    "s_waitcnt vmcnt(0)\n\t" \
    "s_branch .Lm47_%=\n\t" \
    ".Lm51_%=:\n\t" \
    "s_cmp_eq_u32 s41, 0\n\t" \
    "s_cbranch_scc1 .Lm52_%=\n\t" \
    "s_sub_u32 s41, s41, 1\n\t" \
    "buffer_load_dwordx4 v[192:195], %[l16], %[rs], s48 offen offset:0 sc1\n\t" \
    "s_waitcnt vmcnt(0)\n\t" \
    "s_branch .Lm50_%=\n\t" \
    ".Lend%=:\n\t" \
    "s_waitcnt vmcnt(0) lgkmcnt(0)\n\t" \
    "s_mov_b32 %[bud], s41\n\t"
#define ASM_W4_FIN0 \
    "s_mov_b32 s40, %[mt]\n\t" \
    "s_mov_b32 s41, 0x40000\n\t" \
    LSTINIT \
    "s_mov_b32 s43, 0\n\t" \
    "v_mov_b32 v99, 0x3f803f80\n\t" \
    "s_setprio 3\n\t" \
    "s_add_u32 s46, s43, 0x2c00000\n\t" \
    "s_add_u32 s47, s43, 0x1c00000\n\t" \
    "buffer_load_dwordx2 v[120:121], %[t0], %[rs], s46 offen sc1\n\t" \
    "buffer_load_dwordx2 v[122:123], %[t0], %[rs], s47 offen sc1\n\t" \
    "buffer_load_dwordx2 v[124:125], %[t1], %[rs], s46 offen sc1\n\t" \
    "buffer_load_dwordx2 v[126:127], %[t1], %[rs], s47 offen sc1\n\t" \
    "buffer_load_dwordx2 v[128:129], %[t2], %[rs], s46 offen sc1\n\t" \
    "buffer_load_dwordx2 v[130:131], %[t2], %[rs], s47 offen sc1\n\t" \
    "buffer_load_dwordx2 v[132:133], %[t3], %[rs], s46 offen sc1\n\t" \
    "buffer_load_dwordx2 v[134:135], %[t3], %[rs], s47 offen sc1\n\t" \
    "buffer_load_dwordx2 v[136:137], %[t4], %[rs], s46 offen sc1\n\t" \
    "buffer_load_dwordx2 v[138:139], %[t4], %[rs], s47 offen sc1\n\t" \
    "s_waitcnt vmcnt(0)\n\t" \
    ".Lf01_%=:\n\t" \
    LST0 \
    LJIT \
    ".Lf02_%=:\n\t" \
    "s_waitcnt vmcnt(1)\n\t" \
    "v_max_u32 v80, v120, v121\n\t" \
    "v_max3_u32 v80, v122, v123, v80\n\t" \
    "v_max3_u32 v80, v124, v125, v80\n\t" \
    "v_max3_u32 v80, v126, v127, v80\n\t" \
    "v_max3_u32 v80, v128, v129, v80\n\t" \
    "v_max3_u32 v80, v130, v131, v80\n\t" \
    "v_max3_u32 v80, v132, v133, v80\n\t" \
    "v_max3_u32 v80, v134, v135, v80\n\t" \
    "v_max3_u32 v80, v136, v137, v80\n\t" \
    "v_max3_u32 v80, v138, v139, v80\n\t" \
    "v_cmp_eq_u32 vcc, -1, v80\n\t" \
    "s_cbranch_vccz .Lf03_%=\n\t" \
    "s_cmp_eq_u32 s41, 0\n\t" \
    "s_cbranch_scc1 .Lf03_%=\n\t" \
    "s_sub_u32 s41, s41, 1\n\t" \
    "s_sleep 8\n\t" \
    "s_add_u32 s46, s43, 0x2c00000\n\t" \
    "s_add_u32 s47, s43, 0x1c00000\n\t" \
    "buffer_load_dwordx2 v[120:121], %[t0], %[rs], s46 offen sc1\n\t" \
    "buffer_load_dwordx2 v[122:123], %[t0], %[rs], s47 offen sc1\n\t" \
    "buffer_load_dwordx2 v[124:125], %[t1], %[rs], s46 offen sc1\n\t" \
    "buffer_load_dwordx2 v[126:127], %[t1], %[rs], s47 offen sc1\n\t" \
    "buffer_load_dwordx2 v[128:129], %[t2], %[rs], s46 offen sc1\n\t" \
    "buffer_load_dwordx2 v[130:131], %[t2], %[rs], s47 offen sc1\n\t" \
    "buffer_load_dwordx2 v[132:133], %[t3], %[rs], s46 offen sc1\n\t" \
    "buffer_load_dwordx2 v[134:135], %[t3], %[rs], s47 offen sc1\n\t" \
    "buffer_load_dwordx2 v[136:137], %[t4], %[rs], s46 offen sc1\n\t" \
    "buffer_load_dwordx2 v[138:139], %[t4], %[rs], s47 offen sc1\n\t" \
    "s_waitcnt vmcnt(0)\n\t" \
    "s_branch .Lf02_%=\n\t" \
    ".Lf03_%=:\n\t" \
    "v_lshlrev_b32 v140, 16, v120\n\t" \
    "v_and_b32 v141, 0xffff0000, v120\n\t" \
    "v_lshlrev_b32 v142, 16, v121\n\t" \
    "v_and_b32 v143, 0xffff0000, v121\n\t" \
    "v_lshlrev_b32 v80, 16, v122\n\t" \
    "v_and_b32 v81, 0xffff0000, v122\n\t" \
    "v_lshlrev_b32 v82, 16, v123\n\t" \
    "v_and_b32 v83, 0xffff0000, v123\n\t" \
    "v_add_f32 v140, v140, v80\n\t" \
    "v_add_f32 v141, v141, v81\n\t" \
    "v_add_f32 v142, v142, v82\n\t" \
    "v_add_f32 v143, v143, v83\n\t" \
    "v_lshlrev_b32 v144, 16, v124\n\t" \
    "v_and_b32 v145, 0xffff0000, v124\n\t" \
    "v_lshlrev_b32 v146, 16, v125\n\t" \
    "v_and_b32 v147, 0xffff0000, v125\n\t" \
    "v_lshlrev_b32 v80, 16, v126\n\t" \
    "v_and_b32 v81, 0xffff0000, v126\n\t" \
    "v_lshlrev_b32 v82, 16, v127\n\t" \
    "v_and_b32 v83, 0xffff0000, v127\n\t" \
    "v_add_f32 v144, v144, v80\n\t" \
    "v_add_f32 v145, v145, v81\n\t" \
    "v_add_f32 v146, v146, v82\n\t" \
    "v_add_f32 v147, v147, v83\n\t" \
    "v_lshlrev_b32 v148, 16, v128\n\t" \
    "v_and_b32 v149, 0xffff0000, v128\n\t" \
    "v_lshlrev_b32 v150, 16, v129\n\t" \
    "v_and_b32 v151, 0xffff0000, v129\n\t" \
    "v_lshlrev_b32 v80, 16, v130\n\t" \
    "v_and_b32 v81, 0xffff0000, v130\n\t" \
    "v_lshlrev_b32 v82, 16, v131\n\t" \
    "v_and_b32 v83, 0xffff0000, v131\n\t" \
    "v_add_f32 v148, v148, v80\n\t" \
    "v_add_f32 v149, v149, v81\n\t" \
    "v_add_f32 v150, v150, v82\n\t" \
    "v_add_f32 v151, v151, v83\n\t" \
    "v_lshlrev_b32 v152, 16, v132\n\t" \
    "v_and_b32 v153, 0xffff0000, v132\n\t" \
    "v_lshlrev_b32 v154, 16, v133\n\t" \
    "v_and_b32 v155, 0xffff0000, v133\n\t" \
    "v_lshlrev_b32 v80, 16, v134\n\t" \
    "v_and_b32 v81, 0xffff0000, v134\n\t" \
    "v_lshlrev_b32 v82, 16, v135\n\t" \
    "v_and_b32 v83, 0xffff0000, v135\n\t" \
    "v_add_f32 v152, v152, v80\n\t" \
    "v_add_f32 v153, v153, v81\n\t" \
    "v_add_f32 v154, v154, v82\n\t" \
    "v_add_f32 v155, v155, v83\n\t" \
    "v_lshlrev_b32 v156, 16, v136\n\t" \
    "v_and_b32 v157, 0xffff0000, v136\n\t" \
    "v_lshlrev_b32 v158, 16, v137\n\t" \
    "v_and_b32 v159, 0xffff0000, v137\n\t" \
    "v_lshlrev_b32 v80, 16, v138\n\t" \
    "v_and_b32 v81, 0xffff0000, v138\n\t" \
    "v_lshlrev_b32 v82, 16, v139\n\t" \
    "v_and_b32 v83, 0xffff0000, v139\n\t" \
    "v_add_f32 v156, v156, v80\n\t" \
    "v_add_f32 v157, v157, v81\n\t" \
    "v_add_f32 v158, v158, v82\n\t" \
    "v_add_f32 v159, v159, v83\n\t" \
    "s_mov_b32 s42, s43\n\t" \
    "s_lshr_b32 s48, s40, 1\n\t" \
    "s_add_u32 s48, s48, 1\n\t" \
    "s_mul_i32 s43, s48, 0x14000\n\t" \
    "s_lshr_b32 s44, s48, 4\n\t" \
    "s_mul_i32 s44, s44, 0xc2000\n\t" \
    "s_add_u32 s43, s43, s44\n\t" \
    "s_lshr_b32 s44, s40, 3\n\t" \
    "s_lshl_b32 s44, s44, 14\n\t" \
    "s_and_b32 s45, s40, 7\n\t" \
    "s_lshl_b32 s45, s45, 10\n\t" \
    "s_add_u32 s49, s44, s45\n\t" \
    "s_add_u32 s49, s49, 0x1c0000\n\t" \
    "s_add_u32 s50, s49, %[co]\n\t" \
    "s_add_u32 s51, s42, 0x1400000\n\t" \
    "s_add_u32 s52, s42, 0xc00000\n\t" \
    "s_mov_b32 s44, s48\n\t" \
    "s_mov_b32 s45, s48\n\t" \
    "v_mov_b64 v[76:77], s[44:45]\n\t" \
    "v_mov_b64 v[78:79], s[44:45]\n\t" \
    "s_add_u32 s44, s40, 1\n\t" \
    "v_mov_b32 v98, s44\n\t" \
    LST1 \
    LJIT \
    ".Lf04_%=:\n\t" \
    "s_nop 7\n\t" \
    "ds_read_b32 v80, %[fa]\n\t" \
    "s_waitcnt lgkmcnt(0)\n\t" \
    "v_cmp_ge_u32 vcc, s40, v80\n\t" \
    "s_cbranch_vccnz .Lf04_%=\n\t" \
    LST2 \
    LJIT \
    "ds_read_b128 v[160:163], %[la] offset:0\n\t" \
    "ds_read_b128 v[164:167], %[la] offset:6144\n\t" \
    "ds_read_b128 v[168:171], %[la] offset:12288\n\t" \
    "ds_read_b128 v[172:175], %[la] offset:18432\n\t" \
    "ds_read_b128 v[176:179], %[la] offset:24576\n\t" \
    "ds_read_b128 v[180:183], %[la] offset:1024\n\t" \
    "ds_read_b128 v[184:187], %[la] offset:7168\n\t" \
    "ds_read_b128 v[188:191], %[la] offset:13312\n\t" \
    "ds_read_b128 v[192:195], %[la] offset:19456\n\t" \
    "ds_read_b128 v[196:199], %[la] offset:25600\n\t" \
    "ds_read_b128 v[200:203], %[la] offset:2048\n\t" \
    "ds_read_b128 v[204:207], %[la] offset:8192\n\t" \
    "ds_read_b128 v[208:211], %[la] offset:14336\n\t" \
    "ds_read_b128 v[212:215], %[la] offset:20480\n\t" \
    "ds_read_b128 v[216:219], %[la] offset:26624\n\t" \
    "s_waitcnt lgkmcnt(14)\n\t" \
    "v_pk_add_f32 v[100:101], v[140:141], v[160:161]\n\t" \
    "v_pk_add_f32 v[102:103], v[142:143], v[162:163]\n\t" \
    "ds_read_b128 v[220:223], %[la] offset:3072\n\t" \
    "s_waitcnt lgkmcnt(14)\n\t" \
    "v_pk_add_f32 v[104:105], v[144:145], v[164:165]\n\t" \
    "v_pk_add_f32 v[106:107], v[146:147], v[166:167]\n\t" \
    "ds_read_b128 v[224:227], %[la] offset:9216\n\t" \
    "s_waitcnt lgkmcnt(14)\n\t" \
    "v_pk_add_f32 v[108:109], v[148:149], v[168:169]\n\t" \
    "v_pk_add_f32 v[110:111], v[150:151], v[170:171]\n\t" \
    "ds_read_b128 v[228:231], %[la] offset:15360\n\t" \
    "s_waitcnt lgkmcnt(14)\n\t" \
    "v_pk_add_f32 v[112:113], v[152:153], v[172:173]\n\t" \
    "v_pk_add_f32 v[114:115], v[154:155], v[174:175]\n\t" \
    "ds_read_b128 v[232:235], %[la] offset:21504\n\t" \
    "s_waitcnt lgkmcnt(14)\n\t" \
    "v_pk_add_f32 v[116:117], v[156:157], v[176:177]\n\t" \
    "v_pk_add_f32 v[118:119], v[158:159], v[178:179]\n\t" \
    "ds_read_b128 v[236:239], %[la] offset:27648\n\t" \
    "s_waitcnt lgkmcnt(14)\n\t" \
    "v_pk_add_f32 v[100:101], v[100:101], v[180:181]\n\t" \
    "v_pk_add_f32 v[102:103], v[102:103], v[182:183]\n\t" \
    "ds_read_b128 v[160:163], %[la] offset:4096\n\t" \
    "s_waitcnt lgkmcnt(14)\n\t" \
    "v_pk_add_f32 v[104:105], v[104:105], v[184:185]\n\t" \
    "v_pk_add_f32 v[106:107], v[106:107], v[186:187]\n\t" \
    "ds_read_b128 v[164:167], %[la] offset:10240\n\t" \
    "s_waitcnt lgkmcnt(14)\n\t" \
    "v_pk_add_f32 v[108:109], v[108:109], v[188:189]\n\t" \
    "v_pk_add_f32 v[110:111], v[110:111], v[190:191]\n\t" \
    "ds_read_b128 v[168:171], %[la] offset:16384\n\t" \
    "s_waitcnt lgkmcnt(14)\n\t" \
    "v_pk_add_f32 v[112:113], v[112:113], v[192:193]\n\t" \
    "v_pk_add_f32 v[114:115], v[114:115], v[194:195]\n\t" \
    "ds_read_b128 v[172:175], %[la] offset:22528\n\t" \
    "s_waitcnt lgkmcnt(14)\n\t" \
    "v_pk_add_f32 v[116:117], v[116:117], v[196:197]\n\t" \
    "v_pk_add_f32 v[118:119], v[118:119], v[198:199]\n\t" \
    "ds_read_b128 v[176:179], %[la] offset:28672\n\t" \
    "s_waitcnt lgkmcnt(14)\n\t" \
    "v_pk_add_f32 v[100:101], v[100:101], v[200:201]\n\t" \
    "v_pk_add_f32 v[102:103], v[102:103], v[202:203]\n\t" \
    "ds_read_b128 v[180:183], %[la] offset:5120\n\t" \
    "s_waitcnt lgkmcnt(14)\n\t" \
    "v_pk_add_f32 v[104:105], v[104:105], v[204:205]\n\t" \
    "v_pk_add_f32 v[106:107], v[106:107], v[206:207]\n\t" \
    "ds_read_b128 v[184:187], %[la] offset:11264\n\t" \
    "s_waitcnt lgkmcnt(14)\n\t" \
    "v_pk_add_f32 v[108:109], v[108:109], v[208:209]\n\t" \
    "v_pk_add_f32 v[110:111], v[110:111], v[210:211]\n\t" \
    "ds_read_b128 v[188:191], %[la] offset:17408\n\t" \
    "s_waitcnt lgkmcnt(14)\n\t" \
    "v_pk_add_f32 v[112:113], v[112:113], v[212:213]\n\t" \
    "v_pk_add_f32 v[114:115], v[114:115], v[214:215]\n\t" \
    "ds_read_b128 v[192:195], %[la] offset:23552\n\t" \
    "s_waitcnt lgkmcnt(14)\n\t" \
    "v_pk_add_f32 v[116:117], v[116:117], v[216:217]\n\t" \
    "v_pk_add_f32 v[118:119], v[118:119], v[218:219]\n\t" \
    "ds_read_b128 v[196:199], %[la] offset:29696\n\t" \
    "s_waitcnt lgkmcnt(14)\n\t" \
    "v_pk_add_f32 v[100:101], v[100:101], v[220:221]\n\t" \
    "v_pk_add_f32 v[102:103], v[102:103], v[222:223]\n\t" \
    "s_waitcnt lgkmcnt(13)\n\t" \
    "v_pk_add_f32 v[104:105], v[104:105], v[224:225]\n\t" \
    "v_pk_add_f32 v[106:107], v[106:107], v[226:227]\n\t" \
    "s_waitcnt lgkmcnt(12)\n\t" \
    "v_pk_add_f32 v[108:109], v[108:109], v[228:229]\n\t" \
    "v_pk_add_f32 v[110:111], v[110:111], v[230:231]\n\t" \
    "s_waitcnt lgkmcnt(11)\n\t" \
    "v_pk_add_f32 v[112:113], v[112:113], v[232:233]\n\t" \
    "v_pk_add_f32 v[114:115], v[114:115], v[234:235]\n\t" \
    "s_waitcnt lgkmcnt(10)\n\t" \
    "v_pk_add_f32 v[116:117], v[116:117], v[236:237]\n\t" \
    "v_pk_add_f32 v[118:119], v[118:119], v[238:239]\n\t" \
    "s_waitcnt lgkmcnt(9)\n\t" \
    "v_pk_add_f32 v[100:101], v[100:101], v[160:161]\n\t" \
    "v_pk_add_f32 v[102:103], v[102:103], v[162:163]\n\t" \
    "s_waitcnt lgkmcnt(8)\n\t" \
    "v_pk_add_f32 v[104:105], v[104:105], v[164:165]\n\t" \
    "v_pk_add_f32 v[106:107], v[106:107], v[166:167]\n\t" \
    "s_waitcnt lgkmcnt(7)\n\t" \
    "v_pk_add_f32 v[108:109], v[108:109], v[168:169]\n\t" \
    "v_pk_add_f32 v[110:111], v[110:111], v[170:171]\n\t" \
    "s_waitcnt lgkmcnt(6)\n\t" \
    "v_pk_add_f32 v[112:113], v[112:113], v[172:173]\n\t" \
    "v_pk_add_f32 v[114:115], v[114:115], v[174:175]\n\t" \
    "s_waitcnt lgkmcnt(5)\n\t" \
    "v_pk_add_f32 v[116:117], v[116:117], v[176:177]\n\t" \
    "v_pk_add_f32 v[118:119], v[118:119], v[178:179]\n\t" \
    "s_waitcnt lgkmcnt(4)\n\t" \
    "v_pk_add_f32 v[100:101], v[100:101], v[180:181]\n\t" \
    "v_pk_add_f32 v[102:103], v[102:103], v[182:183]\n\t" \
    "s_waitcnt lgkmcnt(3)\n\t" \
    "v_pk_add_f32 v[104:105], v[104:105], v[184:185]\n\t" \
    "v_pk_add_f32 v[106:107], v[106:107], v[186:187]\n\t" \
    "s_waitcnt lgkmcnt(2)\n\t" \
    "v_pk_add_f32 v[108:109], v[108:109], v[188:189]\n\t" \
    "v_pk_add_f32 v[110:111], v[110:111], v[190:191]\n\t" \
    "s_waitcnt lgkmcnt(1)\n\t" \
    "v_pk_add_f32 v[112:113], v[112:113], v[192:193]\n\t" \
    "v_pk_add_f32 v[114:115], v[114:115], v[194:195]\n\t" \
    "s_waitcnt lgkmcnt(0)\n\t" \
    "v_pk_add_f32 v[116:117], v[116:117], v[196:197]\n\t" \
    "v_pk_add_f32 v[118:119], v[118:119], v[198:199]\n\t" \
    "ds_write_b32 %[fc], v98\n\t" \
    "v_sub_u32 %[la], %[l2], %[la]\n\t" \
    "v_cvt_pk_bf16_f32 v84, v100, v101\n\t" \
    "v_cvt_pk_bf16_f32 v85, v102, v103\n\t" \
    "v_cvt_pk_bf16_f32 v86, v104, v105\n\t" \
    "v_cvt_pk_bf16_f32 v87, v106, v107\n\t" \
    "v_cvt_pk_bf16_f32 v88, v108, v109\n\t" \
    "v_cvt_pk_bf16_f32 v89, v110, v111\n\t" \
    "v_cvt_pk_bf16_f32 v90, v112, v113\n\t" \
    "v_cvt_pk_bf16_f32 v91, v114, v115\n\t" \
    "v_cvt_pk_bf16_f32 v92, v116, v117\n\t" \
    "v_cvt_pk_bf16_f32 v93, v118, v119\n\t" \
    "v_mov_b32 v80, 0\n\t" \
    "v_mov_b32 v82, 0\n\t" \
    "v_dot2c_f32_bf16 v80, v84, v99\n\t" \
    "v_dot2c_f32_bf16 v82, v84, v84\n\t" \
    "v_dot2c_f32_bf16 v80, v85, v99\n\t" \
    "v_dot2c_f32_bf16 v82, v85, v85\n\t" \
    "v_dot2c_f32_bf16 v80, v86, v99\n\t" \
    "v_dot2c_f32_bf16 v82, v86, v86\n\t" \
    "v_dot2c_f32_bf16 v80, v87, v99\n\t" \
    "v_dot2c_f32_bf16 v82, v87, v87\n\t" \
    "v_dot2c_f32_bf16 v80, v88, v99\n\t" \
    "v_dot2c_f32_bf16 v82, v88, v88\n\t" \
    "v_dot2c_f32_bf16 v80, v89, v99\n\t" \
    "v_dot2c_f32_bf16 v82, v89, v89\n\t" \
    "v_dot2c_f32_bf16 v80, v90, v99\n\t" \
    "v_dot2c_f32_bf16 v82, v90, v90\n\t" \
    "v_dot2c_f32_bf16 v80, v91, v99\n\t" \
    "v_dot2c_f32_bf16 v82, v91, v91\n\t" \
    "v_dot2c_f32_bf16 v80, v92, v99\n\t" \
    "v_dot2c_f32_bf16 v82, v92, v92\n\t" \
    "v_dot2c_f32_bf16 v80, v93, v99\n\t" \
    "v_dot2c_f32_bf16 v82, v93, v93\n\t" \
    "s_nop 2\n\t" \
    "v_mov_b32 v81, v80\n\t" \
    "v_mov_b32 v83, v82\n\t" \
    "s_nop 1\n\t" \
    "v_permlane16_swap_b32 v80, v81\n\t" \
    "v_permlane16_swap_b32 v82, v83\n\t" \
    "v_add_f32 v80, v80, v81\n\t" \
    "v_add_f32 v82, v82, v83\n\t" \
    "v_mov_b32 v81, v80\n\t" \
    "v_mov_b32 v83, v82\n\t" \
    "s_nop 1\n\t" \
    "v_permlane32_swap_b32 v80, v81\n\t" \
    "v_permlane32_swap_b32 v82, v83\n\t" \
    "v_add_f32 v80, v80, v81\n\t" \
    "v_add_f32 v82, v82, v83\n\t" \
    "v_min_u32 v80, -2, v80\n\t" \
    "v_min_u32 v81, -2, v82\n\t" \
    "s_mov_b64 exec, 0xffff\n\t" \
    "buffer_store_dwordx2 v[80:81], %[l8], %[rs], s50 offen sc1\n\t" \
    "s_mov_b64 exec, -1\n\t" \
    LST3 \
    LJIT \
    "s_add_u32 s46, s43, 0x2c00000\n\t" \
    "s_add_u32 s47, s43, 0x1c00000\n\t" \
    "buffer_load_dwordx2 v[120:121], %[t0], %[rs], s46 offen sc1\n\t" \
    "buffer_load_dwordx2 v[122:123], %[t0], %[rs], s47 offen sc1\n\t" \
    "buffer_load_dwordx2 v[124:125], %[t1], %[rs], s46 offen sc1\n\t" \
    "buffer_load_dwordx2 v[126:127], %[t1], %[rs], s47 offen sc1\n\t" \
    "buffer_load_dwordx2 v[128:129], %[t2], %[rs], s46 offen sc1\n\t" \
    "buffer_load_dwordx2 v[130:131], %[t2], %[rs], s47 offen sc1\n\t" \
    "buffer_load_dwordx2 v[132:133], %[t3], %[rs], s46 offen sc1\n\t" \
    "buffer_load_dwordx2 v[134:135], %[t3], %[rs], s47 offen sc1\n\t" \
    "buffer_load_dwordx2 v[136:137], %[t4], %[rs], s46 offen sc1\n\t" \
    "buffer_load_dwordx2 v[138:139], %[t4], %[rs], s47 offen sc1\n\t" \
    "s_nop 0\n\t" \
    "v_permlane16_swap_b32 v84, v86\n\t" \
    "v_permlane16_swap_b32 v85, v87\n\t" \
    "v_permlane16_swap_b32 v88, v90\n\t" \
    "v_permlane16_swap_b32 v89, v91\n\t" \
    "s_sleep 4\n\t" \
    ".Lf05_%=:\n\t" \
    "buffer_load_dwordx2 v[94:95], %[l8], %[rs], s49 offen sc1\n\t" \
    "buffer_load_dwordx2 v[96:97], %[l8], %[rs], s49 offen offset:512 sc1\n\t" \
    "s_waitcnt vmcnt(0)\n\t" \
    "v_max_u32 v80, v94, v95\n\t" \
    "v_max3_u32 v80, v96, v97, v80\n\t" \
    "v_cmp_eq_u32 vcc, -1, v80\n\t" \
    "s_cbranch_vccz .Lf06_%=\n\t" \
    "s_cmp_eq_u32 s41, 0\n\t" \
    "s_cbranch_scc1 .Lf06_%=\n\t" \
    "s_sub_u32 s41, s41, 1\n\t" \
    "s_branch .Lf05_%=\n\t" \
    ".Lf06_%=:\n\t" \
    LST4 \
    LJIT \
    "v_add_f32 v80, v94, v96\n\t" \
    "v_add_f32 v82, v95, v97\n\t" \
    "v_mov_b32 v81, v80\n\t" \
    "v_mov_b32 v83, v82\n\t" \
    "s_nop 1\n\t" \
    "v_permlane16_swap_b32 v80, v81\n\t" \
    "v_permlane16_swap_b32 v82, v83\n\t" \
    "v_add_f32 v80, v80, v81\n\t" \
    "v_add_f32 v82, v82, v83\n\t" \
    "v_mov_b32 v81, v80\n\t" \
    "v_mov_b32 v83, v82\n\t" \
    "s_nop 1\n\t" \
    "v_permlane32_swap_b32 v80, v81\n\t" \
    "v_permlane32_swap_b32 v82, v83\n\t" \
    "v_add_f32 v80, v80, v81\n\t" \
    "v_add_f32 v82, v82, v83\n\t" \
    "v_mul_f32 v80, 0x3acccccd, v80\n\t" \
    "v_mul_f32 v82, 0x3acccccd, v82\n\t" \
    "v_fma_f32 v82, -v80, v80, v82\n\t" \
    "v_cvt_pk_bf16_f32 v80, v80, v80\n\t" \
    "v_and_b32 v80, 0xffff0000, v80\n\t" \
    "v_max_f32 v82, 0, v82\n\t" \
    "v_cvt_pk_bf16_f32 v82, v82, v82\n\t" \
    "v_and_b32 v82, 0xffff0000, v82\n\t" \
    "v_add_f32 v82, 0x3727c5ac, v82\n\t" \
    "v_cvt_pk_bf16_f32 v82, v82, v82\n\t" \
    "v_and_b32 v82, 0xffff0000, v82\n\t" \
    "v_sqrt_f32 v82, v82\n\t" \
    "s_nop 0\n\t" \
    "v_cvt_pk_bf16_f32 v82, v82, v82\n\t" \
    "v_and_b32 v82, 0xffff0000, v82\n\t" \
    "v_rcp_f32 v82, v82\n\t" \
    "s_nop 0\n\t" \
    "v_mul_f32 v80, -v80, v82\n\t" \
    "v_fma_f32 v100, v100, v82, v80\n\t" \
    "v_fma_f32 v101, v101, v82, v80\n\t" \
    "v_fma_f32 v102, v102, v82, v80\n\t" \
    "v_fma_f32 v103, v103, v82, v80\n\t" \
    "v_fma_f32 v104, v104, v82, v80\n\t" \
    "v_fma_f32 v105, v105, v82, v80\n\t" \
    "v_fma_f32 v106, v106, v82, v80\n\t" \
    "v_fma_f32 v107, v107, v82, v80\n\t" \
    "v_fma_f32 v108, v108, v82, v80\n\t" \
    "v_fma_f32 v109, v109, v82, v80\n\t" \
    "v_fma_f32 v110, v110, v82, v80\n\t" \
    "v_fma_f32 v111, v111, v82, v80\n\t" \
    "v_fma_f32 v112, v112, v82, v80\n\t" \
    "v_fma_f32 v113, v113, v82, v80\n\t" \
    "v_fma_f32 v114, v114, v82, v80\n\t" \
    "v_fma_f32 v115, v115, v82, v80\n\t" \
    "v_fma_f32 v116, v116, v82, v80\n\t" \
    "v_fma_f32 v117, v117, v82, v80\n\t" \
    "v_fma_f32 v118, v118, v82, v80\n\t" \
    "v_fma_f32 v119, v119, v82, v80\n\t" \
    "v_cvt_pk_bf16_f32 v64, v100, v101\n\t" \
    "v_cvt_pk_bf16_f32 v65, v102, v103\n\t" \
    "v_cvt_pk_bf16_f32 v66, v104, v105\n\t" \
    "v_cvt_pk_bf16_f32 v67, v106, v107\n\t" \
    "v_cvt_pk_bf16_f32 v68, v108, v109\n\t" \
    "v_cvt_pk_bf16_f32 v69, v110, v111\n\t" \
    "v_cvt_pk_bf16_f32 v70, v112, v113\n\t" \
    "v_cvt_pk_bf16_f32 v71, v114, v115\n\t" \
    "v_cvt_pk_bf16_f32 v72, v116, v117\n\t" \
    "v_cvt_pk_bf16_f32 v73, v118, v119\n\t" \
    "s_nop 0\n\t" \
    "v_permlane16_swap_b32 v64, v66\n\t" \
    "v_permlane16_swap_b32 v65, v67\n\t" \
    "v_permlane16_swap_b32 v68, v70\n\t" \
    "v_permlane16_swap_b32 v69, v71\n\t" \
    "buffer_store_dwordx4 v[64:67], %[o16], %[rn], s51 offen sc1\n\t" \
    "buffer_store_dwordx4 v[68:71], %[o17], %[rn], s51 offen sc1\n\t" \
    "buffer_store_dwordx2 v[72:73], %[t4], %[rn], s51 offen sc1\n\t" \
    "buffer_store_dwordx4 v[84:87], %[o16], %[rn], s52 offen sc1\n\t" \
    "buffer_store_dwordx4 v[88:91], %[o17], %[rn], s52 offen sc1\n\t" \
    "buffer_store_dwordx2 v[92:93], %[t4], %[rn], s52 offen sc1\n\t" \
    "s_waitcnt vmcnt(0)\n\t" \
    "buffer_store_dwordx4 v[76:79], %[fo], %[rn], %[fs] offen sc1\n\t" \
    LST5 \
    LJIT \
    LFLUSHF \
    "s_add_u32 s40, s40, 2\n\t" \
    "s_cmp_lt_u32 s40, 128\n\t" \
    "s_cbranch_scc1 .Lf01_%=\n\t" \
    "s_branch .Lend%=\n\t" \
    ".Lend%=:\n\t" \
    "s_waitcnt vmcnt(0) lgkmcnt(0)\n\t" \
    "s_mov_b32 %[bud], s41\n\t"
#define ASM_W4_FIN1 \
    "s_mov_b32 s40, %[mt]\n\t" \
    "s_mov_b32 s41, 0x40000\n\t" \
    LSTINIT \
    "s_mov_b32 s43, 0\n\t" \
    "v_mov_b32 v99, 0x3f803f80\n\t" \
    "s_setprio 3\n\t" \
    "s_add_u32 s46, s43, 0x2c00000\n\t" \
    "s_add_u32 s47, s43, 0x1c00000\n\t" \
    "buffer_load_dwordx2 v[120:121], %[t0], %[rs], s46 offen sc1\n\t" \
    "buffer_load_dwordx2 v[122:123], %[t0], %[rs], s47 offen sc1\n\t" \
    "buffer_load_dwordx2 v[124:125], %[t1], %[rs], s46 offen sc1\n\t" \
    "buffer_load_dwordx2 v[126:127], %[t1], %[rs], s47 offen sc1\n\t" \
    "buffer_load_dwordx2 v[128:129], %[t2], %[rs], s46 offen sc1\n\t" \
    "buffer_load_dwordx2 v[130:131], %[t2], %[rs], s47 offen sc1\n\t" \
    "buffer_load_dwordx2 v[132:133], %[t3], %[rs], s46 offen sc1\n\t" \
    "buffer_load_dwordx2 v[134:135], %[t3], %[rs], s47 offen sc1\n\t" \
    "buffer_load_dwordx2 v[136:137], %[t4], %[rs], s46 offen sc1\n\t" \
    "buffer_load_dwordx2 v[138:139], %[t4], %[rs], s47 offen sc1\n\t" \
    ".Lf11_%=:\n\t" \
    LST0 \
    LJIT \
    ".Lf12_%=:\n\t" \
    "s_waitcnt vmcnt(0)\n\t" \
    "v_max_u32 v80, v120, v121\n\t" \
    "v_max3_u32 v80, v122, v123, v80\n\t" \
    "v_max3_u32 v80, v124, v125, v80\n\t" \
    "v_max3_u32 v80, v126, v127, v80\n\t" \
    "v_max3_u32 v80, v128, v129, v80\n\t" \
    "v_max3_u32 v80, v130, v131, v80\n\t" \
    "v_max3_u32 v80, v132, v133, v80\n\t" \
    "v_max3_u32 v80, v134, v135, v80\n\t" \
    "v_max3_u32 v80, v136, v137, v80\n\t" \
    "v_max3_u32 v80, v138, v139, v80\n\t" \
    "v_cmp_eq_u32 vcc, -1, v80\n\t" \
    "s_cbranch_vccz .Lf13_%=\n\t" \
    "s_cmp_eq_u32 s41, 0\n\t" \
    "s_cbranch_scc1 .Lf13_%=\n\t" \
    "s_sub_u32 s41, s41, 1\n\t" \
    "s_sleep 8\n\t" \
    "s_add_u32 s46, s43, 0x2c00000\n\t" \
    "s_add_u32 s47, s43, 0x1c00000\n\t" \
    "buffer_load_dwordx2 v[120:121], %[t0], %[rs], s46 offen sc1\n\t" \
    "buffer_load_dwordx2 v[122:123], %[t0], %[rs], s47 offen sc1\n\t" \
    "buffer_load_dwordx2 v[124:125], %[t1], %[rs], s46 offen sc1\n\t" \
    "buffer_load_dwordx2 v[126:127], %[t1], %[rs], s47 offen sc1\n\t" \
    "buffer_load_dwordx2 v[128:129], %[t2], %[rs], s46 offen sc1\n\t" \
    "buffer_load_dwordx2 v[130:131], %[t2], %[rs], s47 offen sc1\n\t" \
    "buffer_load_dwordx2 v[132:133], %[t3], %[rs], s46 offen sc1\n\t" \
    "buffer_load_dwordx2 v[134:135], %[t3], %[rs], s47 offen sc1\n\t" \
    "buffer_load_dwordx2 v[136:137], %[t4], %[rs], s46 offen sc1\n\t" \
    "buffer_load_dwordx2 v[138:139], %[t4], %[rs], s47 offen sc1\n\t" \
    "s_waitcnt vmcnt(0)\n\t" \
    "s_branch .Lf12_%=\n\t" \
    ".Lf13_%=:\n\t" \
    "v_lshlrev_b32 v140, 16, v120\n\t" \
    "v_and_b32 v141, 0xffff0000, v120\n\t" \
    "v_lshlrev_b32 v142, 16, v121\n\t" \
    "v_and_b32 v143, 0xffff0000, v121\n\t" \
    "v_lshlrev_b32 v80, 16, v122\n\t" \
    "v_and_b32 v81, 0xffff0000, v122\n\t" \
    "v_lshlrev_b32 v82, 16, v123\n\t" \
    "v_and_b32 v83, 0xffff0000, v123\n\t" \
    "v_add_f32 v140, v140, v80\n\t" \
    "v_add_f32 v141, v141, v81\n\t" \
    "v_add_f32 v142, v142, v82\n\t" \
    "v_add_f32 v143, v143, v83\n\t" \
    "v_lshlrev_b32 v144, 16, v124\n\t" \
    "v_and_b32 v145, 0xffff0000, v124\n\t" \
    "v_lshlrev_b32 v146, 16, v125\n\t" \
    "v_and_b32 v147, 0xffff0000, v125\n\t" \
    "v_lshlrev_b32 v80, 16, v126\n\t" \
    "v_and_b32 v81, 0xffff0000, v126\n\t" \
    "v_lshlrev_b32 v82, 16, v127\n\t" \
    "v_and_b32 v83, 0xffff0000, v127\n\t" \
    "v_add_f32 v144, v144, v80\n\t" \
    "v_add_f32 v145, v145, v81\n\t" \
    "v_add_f32 v146, v146, v82\n\t" \
    "v_add_f32 v147, v147, v83\n\t" \
    "v_lshlrev_b32 v148, 16, v128\n\t" \
    "v_and_b32 v149, 0xffff0000, v128\n\t" \
    "v_lshlrev_b32 v150, 16, v129\n\t" \
    "v_and_b32 v151, 0xffff0000, v129\n\t" \
    "v_lshlrev_b32 v80, 16, v130\n\t" \
    "v_and_b32 v81, 0xffff0000, v130\n\t" \
    "v_lshlrev_b32 v82, 16, v131\n\t" \
    "v_and_b32 v83, 0xffff0000, v131\n\t" \
    "v_add_f32 v148, v148, v80\n\t" \
    "v_add_f32 v149, v149, v81\n\t" \
    "v_add_f32 v150, v150, v82\n\t" \
    "v_add_f32 v151, v151, v83\n\t" \
    "v_lshlrev_b32 v152, 16, v132\n\t" \
    "v_and_b32 v153, 0xffff0000, v132\n\t" \
    "v_lshlrev_b32 v154, 16, v133\n\t" \
    "v_and_b32 v155, 0xffff0000, v133\n\t" \
    "v_lshlrev_b32 v80, 16, v134\n\t" \
    "v_and_b32 v81, 0xffff0000, v134\n\t" \
    "v_lshlrev_b32 v82, 16, v135\n\t" \
    "v_and_b32 v83, 0xffff0000, v135\n\t" \
    "v_add_f32 v152, v152, v80\n\t" \
    "v_add_f32 v153, v153, v81\n\t" \
    "v_add_f32 v154, v154, v82\n\t" \
    "v_add_f32 v155, v155, v83\n\t" \
    "v_lshlrev_b32 v156, 16, v136\n\t" \
    "v_and_b32 v157, 0xffff0000, v136\n\t" \
    "v_lshlrev_b32 v158, 16, v137\n\t" \
    "v_and_b32 v159, 0xffff0000, v137\n\t" \
    "v_lshlrev_b32 v80, 16, v138\n\t" \
    "v_and_b32 v81, 0xffff0000, v138\n\t" \
    "v_lshlrev_b32 v82, 16, v139\n\t" \
    "v_and_b32 v83, 0xffff0000, v139\n\t" \
    "v_add_f32 v156, v156, v80\n\t" \
    "v_add_f32 v157, v157, v81\n\t" \
    "v_add_f32 v158, v158, v82\n\t" \
    "v_add_f32 v159, v159, v83\n\t" \
    "s_mov_b32 s42, s43\n\t" \
    "s_lshr_b32 s48, s40, 1\n\t" \
    "s_add_u32 s48, s48, 1\n\t" \
    "s_mul_i32 s43, s48, 0x14000\n\t" \
    "s_lshr_b32 s44, s48, 4\n\t" \
    "s_mul_i32 s44, s44, 0xc2000\n\t" \
    "s_add_u32 s43, s43, s44\n\t" \
    "s_mul_i32 s49, s40, 20480\n\t" \
    "s_add_u32 s49, s49, %[co]\n\t" \
    "s_add_u32 s44, s40, 1\n\t" \
    "v_mov_b32 v98, s44\n\t" \
    LST1 \
    LJIT \
    ".Lf14_%=:\n\t" \
    "s_nop 7\n\t" \
    "ds_read_b32 v80, %[fa]\n\t" \
    "s_waitcnt lgkmcnt(0)\n\t" \
    "v_cmp_ge_u32 vcc, s40, v80\n\t" \
    "s_cbranch_vccnz .Lf14_%=\n\t" \
    LST2 \
    LJIT \
    "ds_read_b128 v[160:163], %[la] offset:0\n\t" \
    "ds_read_b128 v[164:167], %[la] offset:6144\n\t" \
    "ds_read_b128 v[168:171], %[la] offset:12288\n\t" \
    "ds_read_b128 v[172:175], %[la] offset:18432\n\t" \
    "ds_read_b128 v[176:179], %[la] offset:24576\n\t" \
    "ds_read_b128 v[180:183], %[la] offset:1024\n\t" \
    "ds_read_b128 v[184:187], %[la] offset:7168\n\t" \
    "ds_read_b128 v[188:191], %[la] offset:13312\n\t" \
    "ds_read_b128 v[192:195], %[la] offset:19456\n\t" \
    "ds_read_b128 v[196:199], %[la] offset:25600\n\t" \
    "ds_read_b128 v[200:203], %[la] offset:2048\n\t" \
    "ds_read_b128 v[204:207], %[la] offset:8192\n\t" \
    "ds_read_b128 v[208:211], %[la] offset:14336\n\t" \
    "ds_read_b128 v[212:215], %[la] offset:20480\n\t" \
    "ds_read_b128 v[216:219], %[la] offset:26624\n\t" \
    "s_waitcnt lgkmcnt(14)\n\t" \
    "v_pk_add_f32 v[100:101], v[140:141], v[160:161]\n\t" \
    "v_pk_add_f32 v[102:103], v[142:143], v[162:163]\n\t" \
    "ds_read_b128 v[220:223], %[la] offset:3072\n\t" \
    "s_waitcnt lgkmcnt(14)\n\t" \
    "v_pk_add_f32 v[104:105], v[144:145], v[164:165]\n\t" \
    "v_pk_add_f32 v[106:107], v[146:147], v[166:167]\n\t" \
    "ds_read_b128 v[224:227], %[la] offset:9216\n\t" \
    "s_waitcnt lgkmcnt(14)\n\t" \
    "v_pk_add_f32 v[108:109], v[148:149], v[168:169]\n\t" \
    "v_pk_add_f32 v[110:111], v[150:151], v[170:171]\n\t" \
    "ds_read_b128 v[228:231], %[la] offset:15360\n\t" \
    "s_waitcnt lgkmcnt(14)\n\t" \
    "v_pk_add_f32 v[112:113], v[152:153], v[172:173]\n\t" \
    "v_pk_add_f32 v[114:115], v[154:155], v[174:175]\n\t" \
    "ds_read_b128 v[232:235], %[la] offset:21504\n\t" \
    "s_waitcnt lgkmcnt(14)\n\t" \
    "v_pk_add_f32 v[116:117], v[156:157], v[176:177]\n\t" \
    "v_pk_add_f32 v[118:119], v[158:159], v[178:179]\n\t" \
    "ds_read_b128 v[236:239], %[la] offset:27648\n\t" \
    "s_waitcnt lgkmcnt(14)\n\t" \
    "v_pk_add_f32 v[100:101], v[100:101], v[180:181]\n\t" \
    "v_pk_add_f32 v[102:103], v[102:103], v[182:183]\n\t" \
    "ds_read_b128 v[160:163], %[la] offset:4096\n\t" \
    "s_waitcnt lgkmcnt(14)\n\t" \
    "v_pk_add_f32 v[104:105], v[104:105], v[184:185]\n\t" \
    "v_pk_add_f32 v[106:107], v[106:107], v[186:187]\n\t" \
    "ds_read_b128 v[164:167], %[la] offset:10240\n\t" \
    "s_waitcnt lgkmcnt(14)\n\t" \
    "v_pk_add_f32 v[108:109], v[108:109], v[188:189]\n\t" \
    "v_pk_add_f32 v[110:111], v[110:111], v[190:191]\n\t" \
    "ds_read_b128 v[168:171], %[la] offset:16384\n\t" \
    "s_waitcnt lgkmcnt(14)\n\t" \
    "v_pk_add_f32 v[112:113], v[112:113], v[192:193]\n\t" \
    "v_pk_add_f32 v[114:115], v[114:115], v[194:195]\n\t" \
    "ds_read_b128 v[172:175], %[la] offset:22528\n\t" \
    "s_waitcnt lgkmcnt(14)\n\t" \
    "v_pk_add_f32 v[116:117], v[116:117], v[196:197]\n\t" \
    "v_pk_add_f32 v[118:119], v[118:119], v[198:199]\n\t" \
    "ds_read_b128 v[176:179], %[la] offset:28672\n\t" \
    "s_waitcnt lgkmcnt(14)\n\t" \
    "v_pk_add_f32 v[100:101], v[100:101], v[200:201]\n\t" \
    "v_pk_add_f32 v[102:103], v[102:103], v[202:203]\n\t" \
    "ds_read_b128 v[180:183], %[la] offset:5120\n\t" \
    "s_waitcnt lgkmcnt(14)\n\t" \
    "v_pk_add_f32 v[104:105], v[104:105], v[204:205]\n\t" \
    "v_pk_add_f32 v[106:107], v[106:107], v[206:207]\n\t" \
    "ds_read_b128 v[184:187], %[la] offset:11264\n\t" \
    "s_waitcnt lgkmcnt(14)\n\t" \
    "v_pk_add_f32 v[108:109], v[108:109], v[208:209]\n\t" \
    "v_pk_add_f32 v[110:111], v[110:111], v[210:211]\n\t" \
    "ds_read_b128 v[188:191], %[la] offset:17408\n\t" \
    "s_waitcnt lgkmcnt(14)\n\t" \
    "v_pk_add_f32 v[112:113], v[112:113], v[212:213]\n\t" \
    "v_pk_add_f32 v[114:115], v[114:115], v[214:215]\n\t" \
    "ds_read_b128 v[192:195], %[la] offset:23552\n\t" \
    "s_waitcnt lgkmcnt(14)\n\t" \
    "v_pk_add_f32 v[116:117], v[116:117], v[216:217]\n\t" \
    "v_pk_add_f32 v[118:119], v[118:119], v[218:219]\n\t" \
    "ds_read_b128 v[196:199], %[la] offset:29696\n\t" \
    "s_waitcnt lgkmcnt(14)\n\t" \
    "v_pk_add_f32 v[100:101], v[100:101], v[220:221]\n\t" \
    "v_pk_add_f32 v[102:103], v[102:103], v[222:223]\n\t" \
    "s_waitcnt lgkmcnt(13)\n\t" \
    "v_pk_add_f32 v[104:105], v[104:105], v[224:225]\n\t" \
    "v_pk_add_f32 v[106:107], v[106:107], v[226:227]\n\t" \
    "s_waitcnt lgkmcnt(12)\n\t" \
    "v_pk_add_f32 v[108:109], v[108:109], v[228:229]\n\t" \
    "v_pk_add_f32 v[110:111], v[110:111], v[230:231]\n\t" \
    "s_waitcnt lgkmcnt(11)\n\t" \
    "v_pk_add_f32 v[112:113], v[112:113], v[232:233]\n\t" \
    "v_pk_add_f32 v[114:115], v[114:115], v[234:235]\n\t" \
    "s_waitcnt lgkmcnt(10)\n\t" \
    "v_pk_add_f32 v[116:117], v[116:117], v[236:237]\n\t" \
    "v_pk_add_f32 v[118:119], v[118:119], v[238:239]\n\t" \
    "s_waitcnt lgkmcnt(9)\n\t" \
    "v_pk_add_f32 v[100:101], v[100:101], v[160:161]\n\t" \
    "v_pk_add_f32 v[102:103], v[102:103], v[162:163]\n\t" \
    "s_waitcnt lgkmcnt(8)\n\t" \
    "v_pk_add_f32 v[104:105], v[104:105], v[164:165]\n\t" \
    "v_pk_add_f32 v[106:107], v[106:107], v[166:167]\n\t" \
    "s_waitcnt lgkmcnt(7)\n\t" \
    "v_pk_add_f32 v[108:109], v[108:109], v[168:169]\n\t" \
    "v_pk_add_f32 v[110:111], v[110:111], v[170:171]\n\t" \
    "s_waitcnt lgkmcnt(6)\n\t" \
    "v_pk_add_f32 v[112:113], v[112:113], v[172:173]\n\t" \
    "v_pk_add_f32 v[114:115], v[114:115], v[174:175]\n\t" \
    "s_waitcnt lgkmcnt(5)\n\t" \
    "v_pk_add_f32 v[116:117], v[116:117], v[176:177]\n\t" \
    "v_pk_add_f32 v[118:119], v[118:119], v[178:179]\n\t" \
    "s_waitcnt lgkmcnt(4)\n\t" \
    "v_pk_add_f32 v[100:101], v[100:101], v[180:181]\n\t" \
    "v_pk_add_f32 v[102:103], v[102:103], v[182:183]\n\t" \
    "s_waitcnt lgkmcnt(3)\n\t" \
    "v_pk_add_f32 v[104:105], v[104:105], v[184:185]\n\t" \
    "v_pk_add_f32 v[106:107], v[106:107], v[186:187]\n\t" \
    "s_waitcnt lgkmcnt(2)\n\t" \
    "v_pk_add_f32 v[108:109], v[108:109], v[188:189]\n\t" \
    "v_pk_add_f32 v[110:111], v[110:111], v[190:191]\n\t" \
    "s_waitcnt lgkmcnt(1)\n\t" \
    "v_pk_add_f32 v[112:113], v[112:113], v[192:193]\n\t" \
    "v_pk_add_f32 v[114:115], v[114:115], v[194:195]\n\t" \
    "s_waitcnt lgkmcnt(0)\n\t" \
    "v_pk_add_f32 v[116:117], v[116:117], v[196:197]\n\t" \
    "v_pk_add_f32 v[118:119], v[118:119], v[198:199]\n\t" \
    "ds_write_b32 %[fc], v98\n\t" \
    "v_sub_u32 %[la], %[l2], %[la]\n\t" \
    "v_cvt_pk_bf16_f32 v84, v100, v101\n\t" \
    "v_cvt_pk_bf16_f32 v85, v102, v103\n\t" \
    "v_cvt_pk_bf16_f32 v86, v104, v105\n\t" \
    "v_cvt_pk_bf16_f32 v87, v106, v107\n\t" \
    "v_cvt_pk_bf16_f32 v88, v108, v109\n\t" \
    "v_cvt_pk_bf16_f32 v89, v110, v111\n\t" \
    "v_cvt_pk_bf16_f32 v90, v112, v113\n\t" \
    "v_cvt_pk_bf16_f32 v91, v114, v115\n\t" \
    "v_cvt_pk_bf16_f32 v92, v116, v117\n\t" \
    "v_cvt_pk_bf16_f32 v93, v118, v119\n\t" \
    "s_nop 0\n\t" \
    "v_permlane16_swap_b32 v84, v86\n\t" \
    "v_permlane16_swap_b32 v85, v87\n\t" \
    "v_permlane16_swap_b32 v88, v90\n\t" \
    "v_permlane16_swap_b32 v89, v91\n\t" \
    "buffer_store_dwordx4 v[84:87], %[q16], %[ro], s49 offen\n\t" \
    "buffer_store_dwordx4 v[88:91], %[q16], %[ro], s49 offen offset:64\n\t" \
    "buffer_store_dwordx2 v[92:93], %[q8], %[ro], s49 offen offset:128\n\t" \
    "s_add_u32 s46, s43, 0x2c00000\n\t" \
    "s_add_u32 s47, s43, 0x1c00000\n\t" \
    "buffer_load_dwordx2 v[120:121], %[t0], %[rs], s46 offen sc1\n\t" \
    "buffer_load_dwordx2 v[122:123], %[t0], %[rs], s47 offen sc1\n\t" \
    "buffer_load_dwordx2 v[124:125], %[t1], %[rs], s46 offen sc1\n\t" \
    "buffer_load_dwordx2 v[126:127], %[t1], %[rs], s47 offen sc1\n\t" \
    "buffer_load_dwordx2 v[128:129], %[t2], %[rs], s46 offen sc1\n\t" \
    "buffer_load_dwordx2 v[130:131], %[t2], %[rs], s47 offen sc1\n\t" \
    "buffer_load_dwordx2 v[132:133], %[t3], %[rs], s46 offen sc1\n\t" \
    "buffer_load_dwordx2 v[134:135], %[t3], %[rs], s47 offen sc1\n\t" \
    "buffer_load_dwordx2 v[136:137], %[t4], %[rs], s46 offen sc1\n\t" \
    "buffer_load_dwordx2 v[138:139], %[t4], %[rs], s47 offen sc1\n\t" \
    LST5 \
    LJIT \
    LFLUSHF \
    "s_add_u32 s40, s40, 2\n\t" \
    "s_cmp_lt_u32 s40, 128\n\t" \
    "s_cbranch_scc1 .Lf11_%=\n\t" \
    "s_branch .Lend%=\n\t" \
    ".Lend%=:\n\t" \
    "s_waitcnt vmcnt(0) lgkmcnt(0)\n\t" \
    "s_mov_b32 %[bud], s41\n\t"
#define ASM_W1_LOOP \
    "s_mov_b32 s40, 0\n\t" \
    "s_mov_b32 s41, 0x20000\n\t" \
    LSTINIT \
    "s_add_u32 m0, %[wm], 0x0\n\t" \
    "s_add_u32 s44, %[wo], 0x0\n\t" \
    "buffer_load_dwordx4 v176, %[wr], s44 offen nt lds\n\t" \
    "s_add_u32 m0, %[wm], 0x2000\n\t" \
    "s_add_u32 s44, %[wo], 0xa000\n\t" \
    "buffer_load_dwordx4 v176, %[wr], s44 offen nt lds\n\t" \
    "s_add_u32 m0, %[wm], 0x4000\n\t" \
    "s_add_u32 s44, %[wo], 0x14000\n\t" \
    "buffer_load_dwordx4 v176, %[wr], s44 offen nt lds\n\t" \
    "s_add_u32 m0, %[wm], 0x6000\n\t" \
    "s_add_u32 s44, %[wo], 0x1e000\n\t" \
    "buffer_load_dwordx4 v176, %[wr], s44 offen nt lds\n\t" \
    "s_add_u32 m0, %[wm], 0x8000\n\t" \
    "s_add_u32 s44, %[wo], 0x28000\n\t" \
    "buffer_load_dwordx4 v176, %[wr], s44 offen nt lds\n\t" \
    "s_add_u32 m0, %[wm], 0xa000\n\t" \
    "s_add_u32 s44, %[wo], 0x32000\n\t" \
    "buffer_load_dwordx4 v176, %[wr], s44 offen nt lds\n\t" \
    "s_add_u32 m0, %[wm], 0xc000\n\t" \
    "s_add_u32 s44, %[wo], 0x3c000\n\t" \
    "buffer_load_dwordx4 v176, %[wr], s44 offen nt lds\n\t" \
    "s_add_u32 m0, %[wm], 0xe000\n\t" \
    "s_add_u32 s44, %[wo], 0x46000\n\t" \
    "buffer_load_dwordx4 v176, %[wr], s44 offen nt lds\n\t" \
    "s_add_u32 m0, %[wm], 0x10000\n\t" \
    "s_add_u32 s44, %[wo], 0x50000\n\t" \
    "buffer_load_dwordx4 v176, %[wr], s44 offen nt lds\n\t" \
    "s_add_u32 m0, %[wm], 0x12000\n\t" \
    "s_add_u32 s44, %[wo], 0x5a000\n\t" \
    "buffer_load_dwordx4 v176, %[wr], s44 offen nt lds\n\t" \
    "s_add_u32 m0, %[wm], 0x14000\n\t" \
    "s_add_u32 s44, %[wo], 0x64000\n\t" \
    "buffer_load_dwordx4 v176, %[wr], s44 offen nt lds\n\t" \
    "s_add_u32 m0, %[wm], 0x16000\n\t" \
    "s_add_u32 s44, %[wo], 0x6e000\n\t" \
    "buffer_load_dwordx4 v176, %[wr], s44 offen nt lds\n\t" \
    "s_add_u32 m0, %[wm], 0x18000\n\t" \
    "s_add_u32 s44, %[wo], 0x78000\n\t" \
    "buffer_load_dwordx4 v176, %[wr], s44 offen nt lds\n\t" \
    "s_add_u32 m0, %[wm], 0x1a000\n\t" \
    "s_add_u32 s44, %[wo], 0x82000\n\t" \
    "buffer_load_dwordx4 v176, %[wr], s44 offen nt lds\n\t" \
    "s_add_u32 m0, %[wm], 0x1c000\n\t" \
    "s_add_u32 s44, %[wo], 0x8c000\n\t" \
    "buffer_load_dwordx4 v176, %[wr], s44 offen nt lds\n\t" \
    "s_add_u32 m0, %[wm], 0x1e000\n\t" \
    "s_add_u32 s44, %[wo], 0x96000\n\t" \
    "buffer_load_dwordx4 v176, %[wr], s44 offen nt lds\n\t" \
    "s_add_u32 m0, %[wm], 0x20000\n\t" \
    "s_add_u32 s44, %[wo], 0xa0000\n\t" \
    "buffer_load_dwordx4 v176, %[wr], s44 offen nt lds\n\t" \
    "s_add_u32 m0, %[wm], 0x22000\n\t" \
    "s_add_u32 s44, %[wo], 0xaa000\n\t" \
    "buffer_load_dwordx4 v176, %[wr], s44 offen nt lds\n\t" \
    "s_add_u32 m0, %[wm], 0x24000\n\t" \
    "s_add_u32 s44, %[wo], 0xb4000\n\t" \
    "buffer_load_dwordx4 v176, %[wr], s44 offen nt lds\n\t" \
    "s_add_u32 m0, %[wm], 0x26000\n\t" \
    "s_add_u32 s44, %[wo], 0xbe000\n\t" \
    "buffer_load_dwordx4 v176, %[wr], s44 offen nt lds\n\t" \
    "s_waitcnt vmcnt(0) lgkmcnt(0)\n\t" \
    "s_barrier \n\t" \
    "v_add_u32 v178, 0x0, v177\n\t" \
    "ds_read_b64_tr_b16 v[0:1], v178 offset:0\n\t" \
    "ds_read_b64_tr_b16 v[2:3], v178 offset:1024\n\t" \
    "ds_read_b64_tr_b16 v[4:5], v178 offset:8192\n\t" \
    "ds_read_b64_tr_b16 v[6:7], v178 offset:9216\n\t" \
    "ds_read_b64_tr_b16 v[8:9], v178 offset:16384\n\t" \
    "ds_read_b64_tr_b16 v[10:11], v178 offset:17408\n\t" \
    "ds_read_b64_tr_b16 v[12:13], v178 offset:24576\n\t" \
    "ds_read_b64_tr_b16 v[14:15], v178 offset:25600\n\t" \
    "v_add_u32 v178, 0x8000, v177\n\t" \
    "ds_read_b64_tr_b16 v[16:17], v178 offset:0\n\t" \
    "ds_read_b64_tr_b16 v[18:19], v178 offset:1024\n\t" \
    "ds_read_b64_tr_b16 v[20:21], v178 offset:8192\n\t" \
    "ds_read_b64_tr_b16 v[22:23], v178 offset:9216\n\t" \
    "ds_read_b64_tr_b16 v[24:25], v178 offset:16384\n\t" \
    "ds_read_b64_tr_b16 v[26:27], v178 offset:17408\n\t" \
    "ds_read_b64_tr_b16 v[28:29], v178 offset:24576\n\t" \
    "ds_read_b64_tr_b16 v[30:31], v178 offset:25600\n\t" \
    "v_add_u32 v178, 0x10000, v177\n\t" \
    "ds_read_b64_tr_b16 v[32:33], v178 offset:0\n\t" \
    "ds_read_b64_tr_b16 v[34:35], v178 offset:1024\n\t" \
    "ds_read_b64_tr_b16 v[36:37], v178 offset:8192\n\t" \
    "ds_read_b64_tr_b16 v[38:39], v178 offset:9216\n\t" \
    "ds_read_b64_tr_b16 v[40:41], v178 offset:16384\n\t" \
    "ds_read_b64_tr_b16 v[42:43], v178 offset:17408\n\t" \
    "ds_read_b64_tr_b16 v[44:45], v178 offset:24576\n\t" \
    "ds_read_b64_tr_b16 v[46:47], v178 offset:25600\n\t" \
    "v_add_u32 v178, 0x18000, v177\n\t" \
    "ds_read_b64_tr_b16 v[48:49], v178 offset:0\n\t" \
    "ds_read_b64_tr_b16 v[50:51], v178 offset:1024\n\t" \
    "ds_read_b64_tr_b16 v[52:53], v178 offset:8192\n\t" \
    "ds_read_b64_tr_b16 v[54:55], v178 offset:9216\n\t" \
    "ds_read_b64_tr_b16 v[56:57], v178 offset:16384\n\t" \
    "ds_read_b64_tr_b16 v[58:59], v178 offset:17408\n\t" \
    "ds_read_b64_tr_b16 v[60:61], v178 offset:24576\n\t" \
    "ds_read_b64_tr_b16 v[62:63], v178 offset:25600\n\t" \
    "v_add_u32 v178, 0x20000, v177\n\t" \
    "ds_read_b64_tr_b16 v[64:65], v178 offset:0\n\t" \
    "ds_read_b64_tr_b16 v[66:67], v178 offset:1024\n\t" \
    "ds_read_b64_tr_b16 v[68:69], v178 offset:8192\n\t" \
    "ds_read_b64_tr_b16 v[70:71], v178 offset:9216\n\t" \
    "ds_read_b64_tr_b16 v[72:73], v178 offset:16384\n\t" \
    "ds_read_b64_tr_b16 v[74:75], v178 offset:17408\n\t" \
    "ds_read_b64_tr_b16 v[76:77], v178 offset:24576\n\t" \
    "ds_read_b64_tr_b16 v[78:79], v178 offset:25600\n\t" \
    "v_mov_b32 v228, 0x3f803f80\n\t" \
    "v_mov_b32 v229, 0x3f803f80\n\t" \
    "v_mov_b32 v230, 0x3f803f80\n\t" \
    "v_mov_b32 v231, 0x3f803f80\n\t" \
    "v_mov_b64 v[216:217], 0\n\t" \
    "v_bfe_u32 v83, v253, 2, 5\n\t" \
    "v_add_u32 v83, 0x7000, v83\n\t" \
    "v_bfe_u32 v218, v253, 6, 1\n\t" \
    "s_waitcnt lgkmcnt(0)\n\t" \
    "s_barrier \n\t" \
    "ds_write_b32 v251, v228 offset:72\n\t" \
    "ds_write_b32 v251, v228 offset:2120\n\t" \
    "v_mfma_f32_16x16x32_bf16 v[160:163], v[0:3], v[228:231], 0\n\t" \
    "v_mfma_f32_16x16x32_bf16 v[160:163], v[4:7], v[228:231], v[160:163]\n\t" \
    "v_mfma_f32_16x16x32_bf16 v[160:163], v[8:11], v[228:231], v[160:163]\n\t" \
    "v_mfma_f32_16x16x32_bf16 v[160:163], v[12:15], v[228:231], v[160:163]\n\t" \
    "v_mfma_f32_16x16x32_bf16 v[160:163], v[16:19], v[228:231], v[160:163]\n\t" \
    "v_mfma_f32_16x16x32_bf16 v[160:163], v[20:23], v[228:231], v[160:163]\n\t" \
    "v_mfma_f32_16x16x32_bf16 v[160:163], v[24:27], v[228:231], v[160:163]\n\t" \
    "v_mfma_f32_16x16x32_bf16 v[160:163], v[28:31], v[228:231], v[160:163]\n\t" \
    "v_mfma_f32_16x16x32_bf16 v[160:163], v[32:35], v[228:231], v[160:163]\n\t" \
    "v_mfma_f32_16x16x32_bf16 v[160:163], v[36:39], v[228:231], v[160:163]\n\t" \
    "v_mfma_f32_16x16x32_bf16 v[160:163], v[40:43], v[228:231], v[160:163]\n\t" \
    "v_mfma_f32_16x16x32_bf16 v[160:163], v[44:47], v[228:231], v[160:163]\n\t" \
    "v_mfma_f32_16x16x32_bf16 v[160:163], v[48:51], v[228:231], v[160:163]\n\t" \
    "v_mfma_f32_16x16x32_bf16 v[160:163], v[52:55], v[228:231], v[160:163]\n\t" \
    "v_mfma_f32_16x16x32_bf16 v[160:163], v[56:59], v[228:231], v[160:163]\n\t" \
    "v_mfma_f32_16x16x32_bf16 v[160:163], v[60:63], v[228:231], v[160:163]\n\t" \
    "v_mfma_f32_16x16x32_bf16 v[160:163], v[64:67], v[228:231], v[160:163]\n\t" \
    "v_mfma_f32_16x16x32_bf16 v[160:163], v[68:71], v[228:231], v[160:163]\n\t" \
    "v_mfma_f32_16x16x32_bf16 v[160:163], v[72:75], v[228:231], v[160:163]\n\t" \
    "v_mfma_f32_16x16x32_bf16 v[160:163], v[76:79], v[228:231], v[160:163]\n\t" \
    "s_mov_b32 s52, 0x22222222\n\t" \
    "s_mov_b32 s53, 0x22222222\n\t" \
    "s_mov_b32 s54, 0x44444444\n\t" \
    "s_mov_b32 s55, 0x44444444\n\t" \
    "s_mov_b32 s56, 0x88888888\n\t" \
    "s_mov_b32 s57, 0x88888888\n\t" \
    "ds_write_b32 v83, v218\n\t" \
    "s_waitcnt lgkmcnt(0)\n\t" \
    "s_barrier \n\t" \
    ".Lw11_%=:\n\t" \
    "v_mov_b32 v248, 0\n\t" \
    "buffer_load_dword v248, v248, %[rs], %[go] offen sc1\n\t" \
    "s_waitcnt vmcnt(0)\n\t" \
    "v_cmp_ne_u32 vcc, 0, v248\n\t" \
    "s_cbranch_vccnz .Lw12_%=\n\t" \
    "s_cmp_eq_u32 s41, 0\n\t" \
    "s_cbranch_scc1 .Lw12_%=\n\t" \
    "s_sub_u32 s41, s41, 1\n\t" \
    "s_sleep 16\n\t" \
    "s_branch .Lw11_%=\n\t" \
    ".Lw12_%=:\n\t" \
    "s_sub_i32 s60, 0, %[ng]\n\t" \
    "s_sub_i32 s61, 1, %[ng]\n\t" \
    "s_add_i32 s62, s60, 6\n\t" \
    "s_add_i32 s63, s61, 6\n\t" \
    "s_mul_i32 s42, s61, 127\n\t" \
    "s_mul_i32 s44, %[ng], 0x733000\n\t" \
    "s_add_u32 s44, s44, 0x2400000\n\t" \
    "v_cmp_ne_u32 s[58:59], %[ng], v255\n\t" \
    "s_nop 1\n\t" \
    "s_mov_b64 exec, s[58:59]\n\t" \
    "buffer_store_dwordx2 v[216:217], v254, %[rs], s44 offen sc1\n\t" \
    "s_mov_b64 exec, -1\n\t" \
    "s_mov_b32 s43, 0\n\t" \
    "s_add_u32 s46, s43, %[pi]\n\t" \
    "s_cmp_gt_u32 %[wave], 3\n\t" \
    "s_cbranch_scc1 .Lw13_%=\n\t" \
    "s_add_u32 s47, s46, 0x0\n\t" \
    "buffer_load_dwordx4 v[208:211], v253, %[rs], s47 offen sc1\n\t" \
    "s_add_u32 s47, s46, 0x4000\n\t" \
    "buffer_load_dwordx4 v[212:215], v253, %[rs], s47 offen sc1\n\t" \
    "s_add_u32 s47, s46, 0x8000\n\t" \
    "buffer_load_dwordx4 v[216:219], v253, %[rs], s47 offen sc1\n\t" \
    "s_add_u32 s47, s46, 0xc000\n\t" \
    "buffer_load_dwordx4 v[220:223], v253, %[rs], s47 offen sc1\n\t" \
    "s_add_u32 s47, s46, 0x10000\n\t" \
    "buffer_load_dwordx4 v[224:227], v253, %[rs], s47 offen sc1\n\t" \
    ".Lw13_%=:\n\t" \
    "s_waitcnt vmcnt(0)\n\t" \
    "v_lshrrev_b32 v82, 1, v253\n\t" \
    ".Lw14_%=:\n\t" \
    "s_add_i32 s44, s40, s60\n\t" \
    "s_max_i32 s44, s44, 0\n\t" \
    "s_lshr_b32 s45, s44, 1\n\t" \
    "s_and_b32 s44, s44, 1\n\t" \
    "s_lshl_b32 s44, s44, 12\n\t" \
    "s_mul_i32 s49, s45, 0x14000\n\t" \
    "s_add_u32 s49, s49, s44\n\t" \
    "s_lshr_b32 s45, s45, 4\n\t" \
    "s_mul_i32 s45, s45, 0xc2000\n\t" \
    "s_add_u32 s49, s49, s45\n\t" \
    "s_add_i32 s44, s40, s61\n\t" \
    "s_max_i32 s44, s44, 0\n\t" \
    "s_lshr_b32 s45, s44, 1\n\t" \
    "s_and_b32 s44, s44, 1\n\t" \
    "s_lshl_b32 s44, s44, 12\n\t" \
    "s_mul_i32 s50, s45, 0x14000\n\t" \
    "s_add_u32 s50, s50, s44\n\t" \
    "s_lshr_b32 s45, s45, 4\n\t" \
    "s_mul_i32 s45, s45, 0xc2000\n\t" \
    "s_add_u32 s50, s50, s45\n\t" \
    "s_sub_u32 s50, s50, s49\n\t" \
    "s_add_u32 s49, s49, 0x2400000\n\t" \
    "v_mad_u32_u24 v80, v255, s50, v254\n\t" \
    "s_lshr_b32 s44, s40, 6\n\t" \
    "s_lshl_b32 s44, s44, 14\n\t" \
    "s_and_b32 s45, s40, 63\n\t" \
    "s_lshl_b32 s45, s45, 7\n\t" \
    "s_add_u32 s44, s44, s45\n\t" \
    "s_add_u32 s50, s44, 0x4d0000\n\t" \
    "s_add_i32 s44, s40, 1\n\t" \
    "s_max_i32 s44, s44, 0\n\t" \
    "s_lshr_b32 s45, s44, 1\n\t" \
    "s_and_b32 s44, s44, 1\n\t" \
    "s_lshl_b32 s44, s44, 12\n\t" \
    "s_mul_i32 s43, s45, 0x14000\n\t" \
    "s_add_u32 s43, s43, s44\n\t" \
    "s_lshr_b32 s45, s45, 4\n\t" \
    "s_mul_i32 s45, s45, 0xc2000\n\t" \
    "s_add_u32 s43, s43, s45\n\t" \
    LST0 \
    LJIT \
    "s_cmp_gt_u32 %[wave], 3\n\t" \
    "s_cbranch_scc1 .Lw17_%=\n\t" \
    "s_setprio 2\n\t" \
    "v_max_u32 v248, v208, v209\n\t" \
    "v_max3_u32 v248, v210, v211, v248\n\t" \
    "v_max3_u32 v248, v212, v213, v248\n\t" \
    "v_max3_u32 v248, v214, v215, v248\n\t" \
    "v_max3_u32 v248, v216, v217, v248\n\t" \
    "v_max3_u32 v248, v218, v219, v248\n\t" \
    "v_max3_u32 v248, v220, v221, v248\n\t" \
    "v_max3_u32 v248, v222, v223, v248\n\t" \
    "v_max3_u32 v248, v224, v225, v248\n\t" \
    "v_max3_u32 v248, v226, v227, v248\n\t" \
    "v_cmp_eq_u32 vcc, -1, v248\n\t" \
    "s_cbranch_vccz .Lw16_%=\n\t" \
    "s_and_b32 s44, s40, 1\n\t" \
    "s_cmp_eq_u32 s44, 1\n\t" \
    "s_cbranch_scc1 .Lw15_%=\n\t" \
    "s_sleep 10\n\t" \
    ".Lw15_%=:\n\t" \
    "v_max_u32 v248, v208, v209\n\t" \
    "v_max3_u32 v248, v210, v211, v248\n\t" \
    "v_cmp_eq_u32 vcc, -1, v248\n\t" \
    "s_cbranch_vccz .Lw18_%=\n\t" \
    "s_add_u32 s47, s46, 0x0\n\t" \
    "buffer_load_dwordx4 v[208:211], v253, %[rs], s47 offen sc1\n\t" \
    ".Lw18_%=:\n\t" \
    "v_max_u32 v248, v212, v213\n\t" \
    "v_max3_u32 v248, v214, v215, v248\n\t" \
    "v_cmp_eq_u32 vcc, -1, v248\n\t" \
    "s_cbranch_vccz .Lw19_%=\n\t" \
    "s_add_u32 s47, s46, 0x4000\n\t" \
    "buffer_load_dwordx4 v[212:215], v253, %[rs], s47 offen sc1\n\t" \
    ".Lw19_%=:\n\t" \
    "v_max_u32 v248, v216, v217\n\t" \
    "v_max3_u32 v248, v218, v219, v248\n\t" \
    "v_cmp_eq_u32 vcc, -1, v248\n\t" \
    "s_cbranch_vccz .Lw110_%=\n\t" \
    "s_add_u32 s47, s46, 0x8000\n\t" \
    "buffer_load_dwordx4 v[216:219], v253, %[rs], s47 offen sc1\n\t" \
    ".Lw110_%=:\n\t" \
    "v_max_u32 v248, v220, v221\n\t" \
    "v_max3_u32 v248, v222, v223, v248\n\t" \
    "v_cmp_eq_u32 vcc, -1, v248\n\t" \
    "s_cbranch_vccz .Lw111_%=\n\t" \
    "s_add_u32 s47, s46, 0xc000\n\t" \
    "buffer_load_dwordx4 v[220:223], v253, %[rs], s47 offen sc1\n\t" \
    ".Lw111_%=:\n\t" \
    "v_max_u32 v248, v224, v225\n\t" \
    "v_max3_u32 v248, v226, v227, v248\n\t" \
    "v_cmp_eq_u32 vcc, -1, v248\n\t" \
    "s_cbranch_vccz .Lw112_%=\n\t" \
    "s_add_u32 s47, s46, 0x10000\n\t" \
    "buffer_load_dwordx4 v[224:227], v253, %[rs], s47 offen sc1\n\t" \
    ".Lw112_%=:\n\t" \
    "s_waitcnt vmcnt(0)\n\t" \
    "v_max_u32 v248, v208, v209\n\t" \
    "v_max3_u32 v248, v210, v211, v248\n\t" \
    "v_max3_u32 v248, v212, v213, v248\n\t" \
    "v_max3_u32 v248, v214, v215, v248\n\t" \
    "v_max3_u32 v248, v216, v217, v248\n\t" \
    "v_max3_u32 v248, v218, v219, v248\n\t" \
    "v_max3_u32 v248, v220, v221, v248\n\t" \
    "v_max3_u32 v248, v222, v223, v248\n\t" \
    "v_max3_u32 v248, v224, v225, v248\n\t" \
    "v_max3_u32 v248, v226, v227, v248\n\t" \
    "v_cmp_eq_u32 vcc, -1, v248\n\t" \
    "s_cbranch_vccz .Lw16_%=\n\t" \
    "s_cmp_eq_u32 s41, 0\n\t" \
    "s_cbranch_scc1 .Lw16_%=\n\t" \
    "s_sub_u32 s41, s41, 1\n\t" \
    "s_branch .Lw15_%=\n\t" \
    ".Lw16_%=:\n\t" \
    LST1 \
    LJIT \
    "ds_write_b128 v250, v[208:211] offset:0\n\t" \
    "v_mfma_f32_16x16x32_bf16 v[236:239], v[208:211], v[208:211], 0\n\t" \
    "v_mfma_f32_16x16x32_bf16 v[232:235], v[228:231], v[208:211], 0\n\t" \
    "ds_write_b128 v250, v[212:215] offset:4096\n\t" \
    "v_mfma_f32_16x16x32_bf16 v[236:239], v[212:215], v[212:215], v[236:239]\n\t" \
    "v_mfma_f32_16x16x32_bf16 v[232:235], v[228:231], v[212:215], v[232:235]\n\t" \
    "ds_write_b128 v250, v[216:219] offset:8192\n\t" \
    "v_mfma_f32_16x16x32_bf16 v[236:239], v[216:219], v[216:219], v[236:239]\n\t" \
    "v_mfma_f32_16x16x32_bf16 v[232:235], v[228:231], v[216:219], v[232:235]\n\t" \
    "ds_write_b128 v250, v[220:223] offset:12288\n\t" \
    "v_mfma_f32_16x16x32_bf16 v[236:239], v[220:223], v[220:223], v[236:239]\n\t" \
    "v_mfma_f32_16x16x32_bf16 v[232:235], v[228:231], v[220:223], v[232:235]\n\t" \
    "ds_write_b128 v250, v[224:227] offset:16384\n\t" \
    "v_mfma_f32_16x16x32_bf16 v[236:239], v[224:227], v[224:227], v[236:239]\n\t" \
    "v_mfma_f32_16x16x32_bf16 v[232:235], v[228:231], v[224:227], v[232:235]\n\t" \
    "s_add_u32 s46, s43, %[pi]\n\t" \
    "s_nop 4\n\t" \
    "v_cndmask_b32 v236, v236, v237, s[52:53]\n\t" \
    "v_cndmask_b32 v236, v236, v238, s[54:55]\n\t" \
    "v_cndmask_b32 v236, v236, v239, s[56:57]\n\t" \
    "s_mov_b32 exec_lo, 0xf0000f\n\t" \
    "s_mov_b32 exec_hi, 0xf0000f00\n\t" \
    "ds_write2_b32 v252, v232, v236 offset1:1\n\t" \
    "s_mov_b64 exec, -1\n\t" \
    "s_add_u32 s44, s40, 1\n\t" \
    "v_mov_b32 v248, s44\n\t" \
    "s_lshl_b64 exec, 1, %[wave]\n\t" \
    "ds_write_b32 v83, v248\n\t" \
    "s_mov_b64 exec, -1\n\t" \
    ".Lw17_%=:\n\t" \
    "ds_read_b32 v248, v83\n\t" \
    "s_waitcnt lgkmcnt(0)\n\t" \
    "v_cmp_ge_u32 vcc, s40, v248\n\t" \
    "s_cbranch_vccz .Lw113_%=\n\t" \
    "s_cmp_lt_u32 %[wave], 4\n\t" \
    "s_cbranch_scc1 .Lw17_%=\n\t" \
    "s_sleep 1\n\t" \
    "s_branch .Lw17_%=\n\t" \
    ".Lw113_%=:\n\t" \
    "s_setprio 0\n\t" \
    LST2 \
    LJIT \
    "s_cmp_lg_u32 %[wave], 4\n\t" \
    "s_cbranch_scc1 .Lw114_%=\n\t" \
    "s_setprio 1\n\t" \
    "ds_read_b128 v[232:235], v251 offset:0\n\t" \
    "ds_read_b128 v[236:239], v251 offset:16\n\t" \
    "s_waitcnt lgkmcnt(0)\n\t" \
    "v_add_f32 v232, v232, v236\n\t" \
    "v_add_f32 v233, v233, v237\n\t" \
    "v_add_f32 v234, v234, v238\n\t" \
    "v_add_f32 v235, v235, v239\n\t" \
    "v_add_f32 v232, v232, v234\n\t" \
    "v_add_f32 v233, v233, v235\n\t" \
    "v_mul_f32 v232, 0x3acccccd, v232\n\t" \
    "v_mul_f32 v233, 0x3acccccd, v233\n\t" \
    "v_fma_f32 v233, -v232, v232, v233\n\t" \
    "v_mov_b32 v234, v232\n\t" \
    "v_max_f32 v233, 0, v233\n\t" \
    "v_cvt_pk_bf16_f32 v234, v234, v234\n\t" \
    "v_and_b32 v234, 0xffff0000, v234\n\t" \
    "v_mov_b32 v236, v233\n\t" \
    "v_cvt_pk_bf16_f32 v236, v236, v236\n\t" \
    "v_and_b32 v236, 0xffff0000, v236\n\t" \
    "v_add_f32 v236, 0x3727c5ac, v236\n\t" \
    "v_cvt_pk_bf16_f32 v236, v236, v236\n\t" \
    "v_and_b32 v236, 0xffff0000, v236\n\t" \
    "v_sqrt_f32 v236, v236\n\t" \
    "s_nop 0\n\t" \
    "v_cvt_pk_bf16_f32 v236, v236, v236\n\t" \
    "v_and_b32 v236, 0xffff0000, v236\n\t" \
    "v_rcp_f32 v235, v236\n\t" \
    "s_nop 0\n\t" \
    "v_sub_f32 v232, v232, v234\n\t" \
    "v_mul_f32 v233, v233, v235\n\t" \
    "v_mul_f32 v232, v232, v235\n\t" \
    "v_mul_f32 v233, v233, v235\n\t" \
    "v_cvt_pk_bf16_f32 v232, v232, v232\n\t" \
    "v_and_b32 v232, 0xffff0000, v232\n\t" \
    "v_cvt_pk_bf16_f32 v233, v233, v233\n\t" \
    "v_and_b32 v233, 0xffff0000, v233\n\t" \
    "v_add_f32 v233, 0x3727c5ac, v233\n\t" \
    "v_cvt_pk_bf16_f32 v233, v233, v233\n\t" \
    "v_and_b32 v233, 0xffff0000, v233\n\t" \
    "v_sqrt_f32 v233, v233\n\t" \
    "s_nop 0\n\t" \
    "v_cvt_pk_bf16_f32 v233, v233, v233\n\t" \
    "v_and_b32 v233, 0xffff0000, v233\n\t" \
    "v_rcp_f32 v233, v233\n\t" \
    "v_fma_f32 v232, v232, v236, v234\n\t" \
    "v_mul_f32 v237, v235, v233\n\t" \
    "v_mul_f32 v238, -v232, v237\n\t" \
    "v_min_u32 v234, -2, v234\n\t" \
    "v_min_u32 v235, -2, v235\n\t" \
    "v_mov_b32 v239, s40\n\t" \
    "ds_write2_b32 v251, v237, v238 offset0:16 offset1:17\n\t" \
    "ds_write_b32 v251, v239 offset:72\n\t" \
    "s_setprio 0\n\t" \
    "s_cmp_eq_u32 %[ss], 0\n\t" \
    "s_cbranch_scc1 .Lw115_%=\n\t" \
    "s_mov_b64 exec, 0xffff\n\t" \
    "buffer_store_dwordx2 v[234:235], v82, %[rs], s50 offen sc1\n\t" \
    "s_mov_b64 exec, -1\n\t" \
    ".Lw115_%=:\n\t" \
    ".Lw114_%=:\n\t" \
    "ds_read_b128 v[176:179], v249 offset:0\n\t" \
    "ds_read_b128 v[180:183], v249 offset:1024\n\t" \
    "ds_read_b128 v[184:187], v249 offset:2048\n\t" \
    "ds_read_b128 v[188:191], v249 offset:3072\n\t" \
    "ds_read_b128 v[192:195], v249 offset:4096\n\t" \
    "ds_read_b128 v[196:199], v249 offset:5120\n\t" \
    "ds_read_b128 v[200:203], v249 offset:6144\n\t" \
    "s_waitcnt lgkmcnt(6)\n\t" \
    "v_mfma_f32_16x16x32_bf16 v[168:171], v[0:3], v[176:179], 0\n\t" \
    "ds_read_b128 v[204:207], v249 offset:7168\n\t" \
    "s_waitcnt lgkmcnt(6)\n\t" \
    "v_mfma_f32_16x16x32_bf16 v[168:171], v[4:7], v[180:183], v[168:171]\n\t" \
    "ds_read_b128 v[176:179], v249 offset:8192\n\t" \
    "s_waitcnt lgkmcnt(6)\n\t" \
    "v_mfma_f32_16x16x32_bf16 v[168:171], v[8:11], v[184:187], v[168:171]\n\t" \
    "ds_read_b128 v[180:183], v249 offset:9216\n\t" \
    "s_waitcnt lgkmcnt(6)\n\t" \
    "v_mfma_f32_16x16x32_bf16 v[168:171], v[12:15], v[188:191], v[168:171]\n\t" \
    "ds_read_b128 v[184:187], v249 offset:10240\n\t" \
    "s_cmp_gt_u32 %[wave], 3\n\t" \
    "s_cbranch_scc1 .Lw116_%=\n\t" \
    "s_add_u32 s47, s46, 0x0\n\t" \
    "buffer_load_dwordx4 v[208:211], v253, %[rs], s47 offen sc1\n\t" \
    "s_add_u32 s47, s46, 0x4000\n\t" \
    "buffer_load_dwordx4 v[212:215], v253, %[rs], s47 offen sc1\n\t" \
    "s_add_u32 s47, s46, 0x8000\n\t" \
    "buffer_load_dwordx4 v[216:219], v253, %[rs], s47 offen sc1\n\t" \
    "s_add_u32 s47, s46, 0xc000\n\t" \
    "buffer_load_dwordx4 v[220:223], v253, %[rs], s47 offen sc1\n\t" \
    "s_add_u32 s47, s46, 0x10000\n\t" \
    "buffer_load_dwordx4 v[224:227], v253, %[rs], s47 offen sc1\n\t" \
    ".Lw116_%=:\n\t" \
    "s_waitcnt lgkmcnt(6)\n\t" \
    "v_mfma_f32_16x16x32_bf16 v[168:171], v[16:19], v[192:195], v[168:171]\n\t" \
    "ds_read_b128 v[188:191], v249 offset:11264\n\t" \
    "s_waitcnt lgkmcnt(6)\n\t" \
    "v_mfma_f32_16x16x32_bf16 v[168:171], v[20:23], v[196:199], v[168:171]\n\t" \
    "ds_read_b128 v[192:195], v249 offset:12288\n\t" \
    "s_waitcnt lgkmcnt(6)\n\t" \
    "v_mfma_f32_16x16x32_bf16 v[168:171], v[24:27], v[200:203], v[168:171]\n\t" \
    "ds_read_b128 v[196:199], v249 offset:13312\n\t" \
    "s_waitcnt lgkmcnt(6)\n\t" \
    "v_mfma_f32_16x16x32_bf16 v[168:171], v[28:31], v[204:207], v[168:171]\n\t" \
    "ds_read_b128 v[200:203], v249 offset:14336\n\t" \
    "s_waitcnt lgkmcnt(6)\n\t" \
    "v_mfma_f32_16x16x32_bf16 v[168:171], v[32:35], v[176:179], v[168:171]\n\t" \
    "ds_read_b128 v[204:207], v249 offset:15360\n\t" \
    "s_waitcnt lgkmcnt(6)\n\t" \
    "v_mfma_f32_16x16x32_bf16 v[168:171], v[36:39], v[180:183], v[168:171]\n\t" \
    "ds_read_b128 v[176:179], v249 offset:16384\n\t" \
    "s_waitcnt lgkmcnt(6)\n\t" \
    "v_mfma_f32_16x16x32_bf16 v[168:171], v[40:43], v[184:187], v[168:171]\n\t" \
    "ds_read_b128 v[180:183], v249 offset:17408\n\t" \
    "s_waitcnt lgkmcnt(6)\n\t" \
    "v_mfma_f32_16x16x32_bf16 v[168:171], v[44:47], v[188:191], v[168:171]\n\t" \
    "ds_read_b128 v[184:187], v249 offset:18432\n\t" \
    "s_waitcnt lgkmcnt(6)\n\t" \
    "v_mfma_f32_16x16x32_bf16 v[168:171], v[48:51], v[192:195], v[168:171]\n\t" \
    "ds_read_b128 v[188:191], v249 offset:19456\n\t" \
    "s_waitcnt lgkmcnt(6)\n\t" \
    "v_mfma_f32_16x16x32_bf16 v[168:171], v[52:55], v[196:199], v[168:171]\n\t" \
    "ds_read_b32 v236, v251 offset:72\n\t" \
    "ds_read_b64 v[238:239], v251 offset:64\n\t" \
    "s_waitcnt lgkmcnt(7)\n\t" \
    "v_mfma_f32_16x16x32_bf16 v[168:171], v[56:59], v[200:203], v[168:171]\n\t" \
    "s_waitcnt lgkmcnt(6)\n\t" \
    "v_mfma_f32_16x16x32_bf16 v[168:171], v[60:63], v[204:207], v[168:171]\n\t" \
    "s_waitcnt lgkmcnt(5)\n\t" \
    "v_mfma_f32_16x16x32_bf16 v[168:171], v[64:67], v[176:179], v[168:171]\n\t" \
    "s_waitcnt lgkmcnt(4)\n\t" \
    "v_mfma_f32_16x16x32_bf16 v[168:171], v[68:71], v[180:183], v[168:171]\n\t" \
    ".Lw117_%=:\n\t" \
    "s_waitcnt lgkmcnt(0)\n\t" \
    "v_cmp_ne_u32 vcc, s40, v236\n\t" \
    "s_cbranch_vccz .Lw118_%=\n\t" \
    "ds_read_b32 v236, v251 offset:72\n\t" \
    "ds_read_b64 v[238:239], v251 offset:64\n\t" \
    "s_branch .Lw117_%=\n\t" \
    ".Lw118_%=:\n\t" \
    "s_waitcnt lgkmcnt(0)\n\t" \
    "v_mul_f32 v240, v160, v239\n\t" \
    "v_mul_f32 v241, v161, v239\n\t" \
    "v_mfma_f32_16x16x32_bf16 v[168:171], v[72:75], v[184:187], v[168:171]\n\t" \
    "v_mul_f32 v242, v162, v239\n\t" \
    "v_mul_f32 v243, v163, v239\n\t" \
    "v_mfma_f32_16x16x32_bf16 v[168:171], v[76:79], v[188:191], v[168:171]\n\t" \
    "v_mov_b32 v237, 0xc0135761\n\t" \
    "s_cmp_lt_u32 %[wave], 4\n\t" \
    "s_cbranch_scc1 .Lw119_%=\n\t" \
    "s_add_u32 s44, s40, 2\n\t" \
    "v_mov_b32 v248, s44\n\t" \
    "s_lshl_b64 exec, 1, %[wave]\n\t" \
    "ds_write_b32 v83, v248\n\t" \
    "s_mov_b64 exec, -1\n\t" \
    ".Lw119_%=:\n\t" \
    "s_nop 1\n\t" \
    LST3 \
    LJIT \
    "s_nop 2\n\t" \
    "v_fma_f32 v168, v168, v238, v240\n\t" \
    "v_fma_f32 v169, v169, v238, v241\n\t" \
    "v_fma_f32 v170, v170, v238, v242\n\t" \
    "v_fma_f32 v171, v171, v238, v243\n\t" \
    "v_mul_f32 v176, v168, v168\n\t" \
    "v_mul_f32 v177, v169, v169\n\t" \
    "v_mul_f32 v178, v170, v170\n\t" \
    "v_mul_f32 v179, v171, v171\n\t" \
    "v_fmamk_f32 v176, v176, 0xbdd2d3e7, v237\n\t" \
    "v_fmamk_f32 v177, v177, 0xbdd2d3e7, v237\n\t" \
    "v_fmamk_f32 v178, v178, 0xbdd2d3e7, v237\n\t" \
    "v_fmamk_f32 v179, v179, 0xbdd2d3e7, v237\n\t" \
    "v_mul_f32 v176, v168, v176\n\t" \
    "v_mul_f32 v177, v169, v177\n\t" \
    "v_mul_f32 v178, v170, v178\n\t" \
    "v_mul_f32 v179, v171, v179\n\t" \
    "v_exp_f32 v176, v176\n\t" \
    "v_exp_f32 v177, v177\n\t" \
    "v_exp_f32 v178, v178\n\t" \
    "v_exp_f32 v179, v179\n\t" \
    "v_add_f32 v176, 1.0, v176\n\t" \
    "v_add_f32 v177, 1.0, v177\n\t" \
    "v_add_f32 v178, 1.0, v178\n\t" \
    "v_add_f32 v179, 1.0, v179\n\t" \
    "v_rcp_f32 v176, v176\n\t" \
    "v_rcp_f32 v177, v177\n\t" \
    "v_rcp_f32 v178, v178\n\t" \
    "v_rcp_f32 v179, v179\n\t" \
    "v_mul_f32 v168, v168, v176\n\t" \
    "v_mul_f32 v169, v169, v177\n\t" \
    "v_mul_f32 v170, v170, v178\n\t" \
    "v_mul_f32 v171, v171, v179\n\t" \
    "s_waitcnt vmcnt(0)\n\t" \
    "v_cvt_pk_bf16_f32 v184, v168, v169\n\t" \
    "v_cvt_pk_bf16_f32 v185, v170, v171\n\t" \
    "v_min_u32 v184, -2, v184\n\t" \
    "v_min_u32 v185, -2, v185\n\t" \
    "s_cmp_eq_u32 s40, s42\n\t" \
    "s_cselect_b64 s[44:45], s[58:59], 0\n\t" \
    "s_andn2_b64 exec, -1, s[44:45]\n\t" \
    "buffer_store_dwordx2 v[184:185], v80, %[rs], s49 offen sc1\n\t" \
    "s_mov_b64 exec, -1\n\t" \
    LST4 \
    LJIT \
    "s_cmp_ge_u32 s40, 122\n\t" \
    "s_cbranch_scc1 .Lw120_%=\n\t" \
    "s_add_i32 s44, s40, s62\n\t" \
    "s_max_i32 s44, s44, 0\n\t" \
    "s_lshr_b32 s45, s44, 1\n\t" \
    "s_and_b32 s44, s44, 1\n\t" \
    "s_lshl_b32 s44, s44, 12\n\t" \
    "s_mul_i32 s51, s45, 0x14000\n\t" \
    "s_add_u32 s51, s51, s44\n\t" \
    "s_lshr_b32 s45, s45, 4\n\t" \
    "s_mul_i32 s45, s45, 0xc2000\n\t" \
    "s_add_u32 s51, s51, s45\n\t" \
    "s_add_i32 s44, s40, s63\n\t" \
    "s_max_i32 s44, s44, 0\n\t" \
    "s_lshr_b32 s45, s44, 1\n\t" \
    "s_and_b32 s44, s44, 1\n\t" \
    "s_lshl_b32 s44, s44, 12\n\t" \
    "s_mul_i32 s50, s45, 0x14000\n\t" \
    "s_add_u32 s50, s50, s44\n\t" \
    "s_lshr_b32 s45, s45, 4\n\t" \
    "s_mul_i32 s45, s45, 0xc2000\n\t" \
    "s_add_u32 s50, s50, s45\n\t" \
    "s_sub_u32 s50, s50, s51\n\t" \
    "s_add_u32 s51, s51, 0x2400000\n\t" \
    "v_mad_u32_u24 v81, v255, s50, v254\n\t" \
    "v_mov_b64 v[188:189], -1\n\t" \
    "s_add_u32 s44, s40, 6\n\t" \
    "s_cmp_eq_u32 s44, s42\n\t" \
    "s_cselect_b64 s[44:45], s[58:59], 0\n\t" \
    "s_andn2_b64 exec, -1, s[44:45]\n\t" \
    "buffer_store_dwordx2 v[188:189], v81, %[rs], s51 offen sc1\n\t" \
    "s_mov_b64 exec, -1\n\t" \
    ".Lw120_%=:\n\t" \
    "v_xor_b32 v249, 0x8000, v249\n\t" \
    "v_xor_b32 v250, 0x8000, v250\n\t" \
    "v_xor_b32 v251, 0x800, v251\n\t" \
    "v_xor_b32 v252, 0x800, v252\n\t" \
    LFLUSHW \
    "s_add_u32 s40, s40, 1\n\t" \
    "s_cmp_lg_u32 s40, 128\n\t" \
    "s_cbranch_scc1 .Lw14_%=\n\t" \
    "s_branch .Lend%=\n\t" \
    ".Lend%=:\n\t" \
    "s_waitcnt vmcnt(0) lgkmcnt(0)\n\t" \
    "s_mov_b32 %[bud], s41\n\t"
#define ASM_W2_LOOP \
    "s_mov_b32 s40, 0\n\t" \
    "s_mov_b32 s41, 0x20000\n\t" \
    LSTINIT \
    "s_add_u32 m0, %[wm], 0x0\n\t" \
    "s_add_u32 s44, %[wo], 0x0\n\t" \
    "buffer_load_dwordx4 v176, %[wr], s44 offen nt lds\n\t" \
    "s_add_u32 m0, %[wm], 0x2000\n\t" \
    "s_add_u32 s44, %[wo], 0xa000\n\t" \
    "buffer_load_dwordx4 v176, %[wr], s44 offen nt lds\n\t" \
    "s_add_u32 m0, %[wm], 0x4000\n\t" \
    "s_add_u32 s44, %[wo], 0x14000\n\t" \
    "buffer_load_dwordx4 v176, %[wr], s44 offen nt lds\n\t" \
    "s_add_u32 m0, %[wm], 0x6000\n\t" \
    "s_add_u32 s44, %[wo], 0x1e000\n\t" \
    "buffer_load_dwordx4 v176, %[wr], s44 offen nt lds\n\t" \
    "s_add_u32 m0, %[wm], 0x8000\n\t" \
    "s_add_u32 s44, %[wo], 0x28000\n\t" \
    "buffer_load_dwordx4 v176, %[wr], s44 offen nt lds\n\t" \
    "s_add_u32 m0, %[wm], 0xa000\n\t" \
    "s_add_u32 s44, %[wo], 0x32000\n\t" \
    "buffer_load_dwordx4 v176, %[wr], s44 offen nt lds\n\t" \
    "s_add_u32 m0, %[wm], 0xc000\n\t" \
    "s_add_u32 s44, %[wo], 0x3c000\n\t" \
    "buffer_load_dwordx4 v176, %[wr], s44 offen nt lds\n\t" \
    "s_add_u32 m0, %[wm], 0xe000\n\t" \
    "s_add_u32 s44, %[wo], 0x46000\n\t" \
    "buffer_load_dwordx4 v176, %[wr], s44 offen nt lds\n\t" \
    "s_add_u32 m0, %[wm], 0x10000\n\t" \
    "s_add_u32 s44, %[wo], 0x50000\n\t" \
    "buffer_load_dwordx4 v176, %[wr], s44 offen nt lds\n\t" \
    "s_add_u32 m0, %[wm], 0x12000\n\t" \
    "s_add_u32 s44, %[wo], 0x5a000\n\t" \
    "buffer_load_dwordx4 v176, %[wr], s44 offen nt lds\n\t" \
    "s_add_u32 m0, %[wm], 0x14000\n\t" \
    "s_add_u32 s44, %[wo], 0x64000\n\t" \
    "buffer_load_dwordx4 v176, %[wr], s44 offen nt lds\n\t" \
    "s_add_u32 m0, %[wm], 0x16000\n\t" \
    "s_add_u32 s44, %[wo], 0x6e000\n\t" \
    "buffer_load_dwordx4 v176, %[wr], s44 offen nt lds\n\t" \
    "s_add_u32 m0, %[wm], 0x18000\n\t" \
    "s_add_u32 s44, %[wo], 0x78000\n\t" \
    "buffer_load_dwordx4 v176, %[wr], s44 offen nt lds\n\t" \
    "s_add_u32 m0, %[wm], 0x1a000\n\t" \
    "s_add_u32 s44, %[wo], 0x82000\n\t" \
    "buffer_load_dwordx4 v176, %[wr], s44 offen nt lds\n\t" \
    "s_add_u32 m0, %[wm], 0x1c000\n\t" \
    "s_add_u32 s44, %[wo], 0x8c000\n\t" \
    "buffer_load_dwordx4 v176, %[wr], s44 offen nt lds\n\t" \
    "s_add_u32 m0, %[wm], 0x1e000\n\t" \
    "s_add_u32 s44, %[wo], 0x96000\n\t" \
    "buffer_load_dwordx4 v176, %[wr], s44 offen nt lds\n\t" \
    "s_add_u32 m0, %[wm], 0x20000\n\t" \
    "s_add_u32 s44, %[wo], 0xa0000\n\t" \
    "buffer_load_dwordx4 v176, %[wr], s44 offen nt lds\n\t" \
    "s_add_u32 m0, %[wm], 0x22000\n\t" \
    "s_add_u32 s44, %[wo], 0xaa000\n\t" \
    "buffer_load_dwordx4 v176, %[wr], s44 offen nt lds\n\t" \
    "s_add_u32 m0, %[wm], 0x24000\n\t" \
    "s_add_u32 s44, %[wo], 0xb4000\n\t" \
    "buffer_load_dwordx4 v176, %[wr], s44 offen nt lds\n\t" \
    "s_add_u32 m0, %[wm], 0x26000\n\t" \
    "s_add_u32 s44, %[wo], 0xbe000\n\t" \
    "buffer_load_dwordx4 v176, %[wr], s44 offen nt lds\n\t" \
    "s_waitcnt vmcnt(0) lgkmcnt(0)\n\t" \
    "s_barrier \n\t" \
    "v_add_u32 v178, 0x0, v177\n\t" \
    "ds_read_b64_tr_b16 v[0:1], v178 offset:0\n\t" \
    "ds_read_b64_tr_b16 v[2:3], v178 offset:1024\n\t" \
    "ds_read_b64_tr_b16 v[4:5], v178 offset:8192\n\t" \
    "ds_read_b64_tr_b16 v[6:7], v178 offset:9216\n\t" \
    "ds_read_b64_tr_b16 v[8:9], v178 offset:16384\n\t" \
    "ds_read_b64_tr_b16 v[10:11], v178 offset:17408\n\t" \
    "ds_read_b64_tr_b16 v[12:13], v178 offset:24576\n\t" \
    "ds_read_b64_tr_b16 v[14:15], v178 offset:25600\n\t" \
    "v_add_u32 v178, 0x8000, v177\n\t" \
    "ds_read_b64_tr_b16 v[16:17], v178 offset:0\n\t" \
    "ds_read_b64_tr_b16 v[18:19], v178 offset:1024\n\t" \
    "ds_read_b64_tr_b16 v[20:21], v178 offset:8192\n\t" \
    "ds_read_b64_tr_b16 v[22:23], v178 offset:9216\n\t" \
    "ds_read_b64_tr_b16 v[24:25], v178 offset:16384\n\t" \
    "ds_read_b64_tr_b16 v[26:27], v178 offset:17408\n\t" \
    "ds_read_b64_tr_b16 v[28:29], v178 offset:24576\n\t" \
    "ds_read_b64_tr_b16 v[30:31], v178 offset:25600\n\t" \
    "v_add_u32 v178, 0x10000, v177\n\t" \
    "ds_read_b64_tr_b16 v[32:33], v178 offset:0\n\t" \
    "ds_read_b64_tr_b16 v[34:35], v178 offset:1024\n\t" \
    "ds_read_b64_tr_b16 v[36:37], v178 offset:8192\n\t" \
    "ds_read_b64_tr_b16 v[38:39], v178 offset:9216\n\t" \
    "ds_read_b64_tr_b16 v[40:41], v178 offset:16384\n\t" \
    "ds_read_b64_tr_b16 v[42:43], v178 offset:17408\n\t" \
    "ds_read_b64_tr_b16 v[44:45], v178 offset:24576\n\t" \
    "ds_read_b64_tr_b16 v[46:47], v178 offset:25600\n\t" \
    "v_add_u32 v178, 0x18000, v177\n\t" \
    "ds_read_b64_tr_b16 v[48:49], v178 offset:0\n\t" \
    "ds_read_b64_tr_b16 v[50:51], v178 offset:1024\n\t" \
    "ds_read_b64_tr_b16 v[52:53], v178 offset:8192\n\t" \
    "ds_read_b64_tr_b16 v[54:55], v178 offset:9216\n\t" \
    "ds_read_b64_tr_b16 v[56:57], v178 offset:16384\n\t" \
    "ds_read_b64_tr_b16 v[58:59], v178 offset:17408\n\t" \
    "ds_read_b64_tr_b16 v[60:61], v178 offset:24576\n\t" \
    "ds_read_b64_tr_b16 v[62:63], v178 offset:25600\n\t" \
    "v_add_u32 v178, 0x20000, v177\n\t" \
    "ds_read_b64_tr_b16 v[64:65], v178 offset:0\n\t" \
    "ds_read_b64_tr_b16 v[66:67], v178 offset:1024\n\t" \
    "ds_read_b64_tr_b16 v[68:69], v178 offset:8192\n\t" \
    "ds_read_b64_tr_b16 v[70:71], v178 offset:9216\n\t" \
    "ds_read_b64_tr_b16 v[72:73], v178 offset:16384\n\t" \
    "ds_read_b64_tr_b16 v[74:75], v178 offset:17408\n\t" \
    "ds_read_b64_tr_b16 v[76:77], v178 offset:24576\n\t" \
    "ds_read_b64_tr_b16 v[78:79], v178 offset:25600\n\t" \
    "v_mov_b32 v228, 0x3f803f80\n\t" \
    "v_mov_b32 v229, 0x3f803f80\n\t" \
    "v_mov_b32 v230, 0x3f803f80\n\t" \
    "v_mov_b32 v231, 0x3f803f80\n\t" \
    "v_mov_b64 v[216:217], 0\n\t" \
    "v_bfe_u32 v255, v253, 2, 5\n\t" \
    "v_add_u32 v255, 0x7000, v255\n\t" \
    "v_bfe_u32 v218, v253, 6, 1\n\t" \
    "s_waitcnt lgkmcnt(0)\n\t" \
    "s_barrier \n\t" \
    "ds_write_b32 v255, v218\n\t" \
    "s_waitcnt lgkmcnt(0)\n\t" \
    "s_barrier \n\t" \
    ".Lw21_%=:\n\t" \
    "v_mov_b32 v248, 0\n\t" \
    "buffer_load_dword v248, v248, %[rs], %[go] offen sc1\n\t" \
    "s_waitcnt vmcnt(0)\n\t" \
    "v_cmp_ne_u32 vcc, 0, v248\n\t" \
    "s_cbranch_vccnz .Lw22_%=\n\t" \
    "s_cmp_eq_u32 s41, 0\n\t" \
    "s_cbranch_scc1 .Lw22_%=\n\t" \
    "s_sub_u32 s41, s41, 1\n\t" \
    "s_sleep 16\n\t" \
    "s_branch .Lw21_%=\n\t" \
    ".Lw22_%=:\n\t" \
    "s_mov_b32 s43, 0\n\t" \
    "s_add_u32 s46, s43, %[pi]\n\t" \
    "s_cmp_gt_u32 %[wave], 3\n\t" \
    "s_cbranch_scc1 .Lw23_%=\n\t" \
    "s_add_u32 s47, s46, 0x0\n\t" \
    "buffer_load_dwordx4 v[208:211], v253, %[rs], s47 offen sc1\n\t" \
    "s_add_u32 s47, s46, 0x4000\n\t" \
    "buffer_load_dwordx4 v[212:215], v253, %[rs], s47 offen sc1\n\t" \
    "s_add_u32 s47, s46, 0x8000\n\t" \
    "buffer_load_dwordx4 v[216:219], v253, %[rs], s47 offen sc1\n\t" \
    "s_add_u32 s47, s46, 0xc000\n\t" \
    "buffer_load_dwordx4 v[220:223], v253, %[rs], s47 offen sc1\n\t" \
    "s_add_u32 s47, s46, 0x10000\n\t" \
    "buffer_load_dwordx4 v[224:227], v253, %[rs], s47 offen sc1\n\t" \
    ".Lw23_%=:\n\t" \
    "s_waitcnt vmcnt(0)\n\t" \
    ".Lw24_%=:\n\t" \
    "s_add_u32 s49, s43, 0x2c00000\n\t" \
    "s_add_u32 s50, s43, 0x1c00000\n\t" \
    "s_lshr_b32 s44, s40, 6\n\t" \
    "s_lshl_b32 s44, s44, 14\n\t" \
    "s_and_b32 s45, s40, 63\n\t" \
    "s_lshl_b32 s45, s45, 7\n\t" \
    "s_add_u32 s44, s44, s45\n\t" \
    "s_add_u32 s51, s44, 0x4d0000\n\t" \
    "s_add_i32 s44, s40, 1\n\t" \
    "s_max_i32 s44, s44, 0\n\t" \
    "s_lshr_b32 s45, s44, 1\n\t" \
    "s_and_b32 s44, s44, 1\n\t" \
    "s_lshl_b32 s44, s44, 12\n\t" \
    "s_mul_i32 s43, s45, 0x14000\n\t" \
    "s_add_u32 s43, s43, s44\n\t" \
    "s_lshr_b32 s45, s45, 4\n\t" \
    "s_mul_i32 s45, s45, 0xc2000\n\t" \
    "s_add_u32 s43, s43, s45\n\t" \
    LST0 \
    LJIT \
    "s_cmp_gt_u32 %[wave], 3\n\t" \
    "s_cbranch_scc1 .Lw27_%=\n\t" \
    "s_setprio 2\n\t" \
    "v_max_u32 v248, v208, v209\n\t" \
    "v_max3_u32 v248, v210, v211, v248\n\t" \
    "v_max3_u32 v248, v212, v213, v248\n\t" \
    "v_max3_u32 v248, v214, v215, v248\n\t" \
    "v_max3_u32 v248, v216, v217, v248\n\t" \
    "v_max3_u32 v248, v218, v219, v248\n\t" \
    "v_max3_u32 v248, v220, v221, v248\n\t" \
    "v_max3_u32 v248, v222, v223, v248\n\t" \
    "v_max3_u32 v248, v224, v225, v248\n\t" \
    "v_max3_u32 v248, v226, v227, v248\n\t" \
    "v_cmp_eq_u32 vcc, -1, v248\n\t" \
    "s_cbranch_vccz .Lw26_%=\n\t" \
    "s_and_b32 s44, s40, 1\n\t" \
    "s_cmp_eq_u32 s44, 0\n\t" \
    "s_cbranch_scc1 .Lw25_%=\n\t" \
    "s_sleep 6\n\t" \
    ".Lw25_%=:\n\t" \
    "v_max_u32 v248, v208, v209\n\t" \
    "v_max3_u32 v248, v210, v211, v248\n\t" \
    "v_cmp_eq_u32 vcc, -1, v248\n\t" \
    "s_cbranch_vccz .Lw28_%=\n\t" \
    "s_add_u32 s47, s46, 0x0\n\t" \
    "buffer_load_dwordx4 v[208:211], v253, %[rs], s47 offen sc1\n\t" \
    ".Lw28_%=:\n\t" \
    "v_max_u32 v248, v212, v213\n\t" \
    "v_max3_u32 v248, v214, v215, v248\n\t" \
    "v_cmp_eq_u32 vcc, -1, v248\n\t" \
    "s_cbranch_vccz .Lw29_%=\n\t" \
    "s_add_u32 s47, s46, 0x4000\n\t" \
    "buffer_load_dwordx4 v[212:215], v253, %[rs], s47 offen sc1\n\t" \
    ".Lw29_%=:\n\t" \
    "v_max_u32 v248, v216, v217\n\t" \
    "v_max3_u32 v248, v218, v219, v248\n\t" \
    "v_cmp_eq_u32 vcc, -1, v248\n\t" \
    "s_cbranch_vccz .Lw210_%=\n\t" \
    "s_add_u32 s47, s46, 0x8000\n\t" \
    "buffer_load_dwordx4 v[216:219], v253, %[rs], s47 offen sc1\n\t" \
    ".Lw210_%=:\n\t" \
    "v_max_u32 v248, v220, v221\n\t" \
    "v_max3_u32 v248, v222, v223, v248\n\t" \
    "v_cmp_eq_u32 vcc, -1, v248\n\t" \
    "s_cbranch_vccz .Lw211_%=\n\t" \
    "s_add_u32 s47, s46, 0xc000\n\t" \
    "buffer_load_dwordx4 v[220:223], v253, %[rs], s47 offen sc1\n\t" \
    ".Lw211_%=:\n\t" \
    "v_max_u32 v248, v224, v225\n\t" \
    "v_max3_u32 v248, v226, v227, v248\n\t" \
    "v_cmp_eq_u32 vcc, -1, v248\n\t" \
    "s_cbranch_vccz .Lw212_%=\n\t" \
    "s_add_u32 s47, s46, 0x10000\n\t" \
    "buffer_load_dwordx4 v[224:227], v253, %[rs], s47 offen sc1\n\t" \
    ".Lw212_%=:\n\t" \
    "s_waitcnt vmcnt(0)\n\t" \
    "v_max_u32 v248, v208, v209\n\t" \
    "v_max3_u32 v248, v210, v211, v248\n\t" \
    "v_max3_u32 v248, v212, v213, v248\n\t" \
    "v_max3_u32 v248, v214, v215, v248\n\t" \
    "v_max3_u32 v248, v216, v217, v248\n\t" \
    "v_max3_u32 v248, v218, v219, v248\n\t" \
    "v_max3_u32 v248, v220, v221, v248\n\t" \
    "v_max3_u32 v248, v222, v223, v248\n\t" \
    "v_max3_u32 v248, v224, v225, v248\n\t" \
    "v_max3_u32 v248, v226, v227, v248\n\t" \
    "v_cmp_eq_u32 vcc, -1, v248\n\t" \
    "s_cbranch_vccz .Lw26_%=\n\t" \
    "s_cmp_eq_u32 s41, 0\n\t" \
    "s_cbranch_scc1 .Lw26_%=\n\t" \
    "s_sub_u32 s41, s41, 1\n\t" \
    "s_branch .Lw25_%=\n\t" \
    ".Lw26_%=:\n\t" \
    LST1 \
    LJIT \
    "ds_write_b128 v250, v[208:211] offset:0\n\t" \
    "ds_write_b128 v250, v[212:215] offset:4096\n\t" \
    "ds_write_b128 v250, v[216:219] offset:8192\n\t" \
    "ds_write_b128 v250, v[220:223] offset:12288\n\t" \
    "ds_write_b128 v250, v[224:227] offset:16384\n\t" \
    "s_add_u32 s46, s43, %[pi]\n\t" \
    "s_add_u32 s44, s40, 1\n\t" \
    "v_mov_b32 v248, s44\n\t" \
    "s_lshl_b64 exec, 1, %[wave]\n\t" \
    "ds_write_b32 v255, v248\n\t" \
    "s_mov_b64 exec, -1\n\t" \
    ".Lw27_%=:\n\t" \
    "ds_read_b32 v248, v255\n\t" \
    "s_waitcnt lgkmcnt(0)\n\t" \
    "v_cmp_ge_u32 vcc, s40, v248\n\t" \
    "s_cbranch_vccz .Lw213_%=\n\t" \
    "s_cmp_lt_u32 %[wave], 4\n\t" \
    "s_cbranch_scc1 .Lw27_%=\n\t" \
    "s_sleep 1\n\t" \
    "s_branch .Lw27_%=\n\t" \
    ".Lw213_%=:\n\t" \
    "s_setprio 0\n\t" \
    LST2 \
    LJIT \
    "ds_read_b128 v[176:179], v249 offset:0\n\t" \
    "buffer_load_dwordx2 v[232:233], v254, %[rs], s50 offen sc1\n\t" \
    "buffer_load_dwordx2 v[234:235], v251, %[rs], s51 offen sc1\n\t" \
    "ds_read_b128 v[180:183], v249 offset:1024\n\t" \
    "ds_read_b128 v[184:187], v249 offset:2048\n\t" \
    "ds_read_b128 v[188:191], v249 offset:3072\n\t" \
    "ds_read_b128 v[192:195], v249 offset:4096\n\t" \
    "ds_read_b128 v[196:199], v249 offset:5120\n\t" \
    "ds_read_b128 v[200:203], v249 offset:6144\n\t" \
    "s_waitcnt lgkmcnt(6)\n\t" \
    "v_mfma_f32_16x16x32_bf16 v[168:171], v[0:3], v[176:179], 0\n\t" \
    "ds_read_b128 v[204:207], v249 offset:7168\n\t" \
    "s_waitcnt lgkmcnt(6)\n\t" \
    "v_mfma_f32_16x16x32_bf16 v[168:171], v[4:7], v[180:183], v[168:171]\n\t" \
    "ds_read_b128 v[176:179], v249 offset:8192\n\t" \
    "s_waitcnt lgkmcnt(6)\n\t" \
    "v_mfma_f32_16x16x32_bf16 v[168:171], v[8:11], v[184:187], v[168:171]\n\t" \
    "ds_read_b128 v[180:183], v249 offset:9216\n\t" \
    "s_waitcnt lgkmcnt(6)\n\t" \
    "v_mfma_f32_16x16x32_bf16 v[168:171], v[12:15], v[188:191], v[168:171]\n\t" \
    "ds_read_b128 v[184:187], v249 offset:10240\n\t" \
    "s_cmp_gt_u32 %[wave], 3\n\t" \
    "s_cbranch_scc1 .Lw214_%=\n\t" \
    "s_add_u32 s47, s46, 0x0\n\t" \
    "buffer_load_dwordx4 v[208:211], v253, %[rs], s47 offen sc1\n\t" \
    "s_add_u32 s47, s46, 0x4000\n\t" \
    "buffer_load_dwordx4 v[212:215], v253, %[rs], s47 offen sc1\n\t" \
    "s_add_u32 s47, s46, 0x8000\n\t" \
    "buffer_load_dwordx4 v[216:219], v253, %[rs], s47 offen sc1\n\t" \
    "s_add_u32 s47, s46, 0xc000\n\t" \
    "buffer_load_dwordx4 v[220:223], v253, %[rs], s47 offen sc1\n\t" \
    "s_add_u32 s47, s46, 0x10000\n\t" \
    "buffer_load_dwordx4 v[224:227], v253, %[rs], s47 offen sc1\n\t" \
    ".Lw214_%=:\n\t" \
    "s_waitcnt lgkmcnt(6)\n\t" \
    "v_mfma_f32_16x16x32_bf16 v[168:171], v[16:19], v[192:195], v[168:171]\n\t" \
    "ds_read_b128 v[188:191], v249 offset:11264\n\t" \
    "s_waitcnt lgkmcnt(6)\n\t" \
    "v_mfma_f32_16x16x32_bf16 v[168:171], v[20:23], v[196:199], v[168:171]\n\t" \
    "ds_read_b128 v[192:195], v249 offset:12288\n\t" \
    "s_waitcnt lgkmcnt(6)\n\t" \
    "v_mfma_f32_16x16x32_bf16 v[168:171], v[24:27], v[200:203], v[168:171]\n\t" \
    "ds_read_b128 v[196:199], v249 offset:13312\n\t" \
    "s_waitcnt lgkmcnt(6)\n\t" \
    "v_mfma_f32_16x16x32_bf16 v[168:171], v[28:31], v[204:207], v[168:171]\n\t" \
    "ds_read_b128 v[200:203], v249 offset:14336\n\t" \
    "s_waitcnt lgkmcnt(6)\n\t" \
    "v_mfma_f32_16x16x32_bf16 v[168:171], v[32:35], v[176:179], v[168:171]\n\t" \
    "ds_read_b128 v[204:207], v249 offset:15360\n\t" \
    "s_waitcnt lgkmcnt(6)\n\t" \
    "v_mfma_f32_16x16x32_bf16 v[168:171], v[36:39], v[180:183], v[168:171]\n\t" \
    "ds_read_b128 v[176:179], v249 offset:16384\n\t" \
    "s_waitcnt lgkmcnt(6)\n\t" \
    "v_mfma_f32_16x16x32_bf16 v[168:171], v[40:43], v[184:187], v[168:171]\n\t" \
    "ds_read_b128 v[180:183], v249 offset:17408\n\t" \
    "s_waitcnt lgkmcnt(6)\n\t" \
    "v_mfma_f32_16x16x32_bf16 v[168:171], v[44:47], v[188:191], v[168:171]\n\t" \
    "ds_read_b128 v[184:187], v249 offset:18432\n\t" \
    "s_waitcnt lgkmcnt(6)\n\t" \
    "v_mfma_f32_16x16x32_bf16 v[168:171], v[48:51], v[192:195], v[168:171]\n\t" \
    "ds_read_b128 v[188:191], v249 offset:19456\n\t" \
    "s_waitcnt lgkmcnt(6)\n\t" \
    "v_mfma_f32_16x16x32_bf16 v[168:171], v[52:55], v[196:199], v[168:171]\n\t" \
    "s_waitcnt lgkmcnt(5)\n\t" \
    "v_mfma_f32_16x16x32_bf16 v[168:171], v[56:59], v[200:203], v[168:171]\n\t" \
    "s_waitcnt lgkmcnt(4)\n\t" \
    "v_mfma_f32_16x16x32_bf16 v[168:171], v[60:63], v[204:207], v[168:171]\n\t" \
    "s_waitcnt lgkmcnt(3)\n\t" \
    "v_mfma_f32_16x16x32_bf16 v[168:171], v[64:67], v[176:179], v[168:171]\n\t" \
    "s_waitcnt lgkmcnt(2)\n\t" \
    "v_mfma_f32_16x16x32_bf16 v[168:171], v[68:71], v[180:183], v[168:171]\n\t" \
    "s_waitcnt lgkmcnt(1)\n\t" \
    "v_mfma_f32_16x16x32_bf16 v[168:171], v[72:75], v[184:187], v[168:171]\n\t" \
    "s_waitcnt lgkmcnt(0)\n\t" \
    "v_mfma_f32_16x16x32_bf16 v[168:171], v[76:79], v[188:191], v[168:171]\n\t" \
    "s_cmp_lt_u32 %[wave], 4\n\t" \
    "s_cbranch_scc1 .Lw215_%=\n\t" \
    "s_add_u32 s44, s40, 2\n\t" \
    "v_mov_b32 v248, s44\n\t" \
    "s_lshl_b64 exec, 1, %[wave]\n\t" \
    "ds_write_b32 v255, v248\n\t" \
    "s_mov_b64 exec, -1\n\t" \
    ".Lw215_%=:\n\t" \
    "s_nop 1\n\t" \
    LST3 \
    LJIT \
    "s_waitcnt vmcnt(0)\n\t" \
    ".Lw216_%=:\n\t" \
    "v_max_u32 v248, v234, v235\n\t" \
    "v_cmp_eq_u32 vcc, -1, v248\n\t" \
    "s_cbranch_vccz .Lw217_%=\n\t" \
    "s_cmp_eq_u32 s41, 0\n\t" \
    "s_cbranch_scc1 .Lw217_%=\n\t" \
    "s_sub_u32 s41, s41, 1\n\t" \
    "buffer_load_dwordx2 v[234:235], v251, %[rs], s51 offen sc1\n\t" \
    "s_waitcnt vmcnt(0)\n\t" \
    "s_branch .Lw216_%=\n\t" \
    ".Lw217_%=:\n\t" \
    "v_lshlrev_b32 v176, 16, v232\n\t" \
    "v_and_b32 v177, 0xffff0000, v232\n\t" \
    "v_lshlrev_b32 v178, 16, v233\n\t" \
    "v_and_b32 v179, 0xffff0000, v233\n\t" \
    "v_sub_f32 v176, v176, v234\n\t" \
    "v_sub_f32 v177, v177, v234\n\t" \
    "v_sub_f32 v178, v178, v234\n\t" \
    "v_sub_f32 v179, v179, v234\n\t" \
    "v_mul_f32 v176, v176, v235\n\t" \
    "v_mul_f32 v177, v177, v235\n\t" \
    "v_mul_f32 v178, v178, v235\n\t" \
    "v_mul_f32 v179, v179, v235\n\t" \
    "v_cvt_pk_bf16_f32 v180, v176, v177\n\t" \
    "v_cvt_pk_bf16_f32 v181, v178, v179\n\t" \
    "v_lshlrev_b32 v176, 16, v180\n\t" \
    "v_and_b32 v177, 0xffff0000, v180\n\t" \
    "v_lshlrev_b32 v178, 16, v181\n\t" \
    "v_and_b32 v179, 0xffff0000, v181\n\t" \
    "v_add_f32 v168, v176, v168\n\t" \
    "v_add_f32 v169, v177, v169\n\t" \
    "v_add_f32 v170, v178, v170\n\t" \
    "v_add_f32 v171, v179, v171\n\t" \
    "v_cvt_pk_bf16_f32 v184, v168, v169\n\t" \
    "v_cvt_pk_bf16_f32 v185, v170, v171\n\t" \
    "v_min_u32 v184, -2, v184\n\t" \
    "v_min_u32 v185, -2, v185\n\t" \
    "buffer_store_dwordx2 v[184:185], v254, %[rs], s49 offen sc1\n\t" \
    LST4 \
    LJIT \
    "s_cmp_ge_u32 s40, 122\n\t" \
    "s_cbranch_scc1 .Lw218_%=\n\t" \
    "s_add_i32 s44, s40, 6\n\t" \
    "s_max_i32 s44, s44, 0\n\t" \
    "s_lshr_b32 s45, s44, 1\n\t" \
    "s_and_b32 s44, s44, 1\n\t" \
    "s_lshl_b32 s44, s44, 12\n\t" \
    "s_mul_i32 s50, s45, 0x14000\n\t" \
    "s_add_u32 s50, s50, s44\n\t" \
    "s_lshr_b32 s45, s45, 4\n\t" \
    "s_mul_i32 s45, s45, 0xc2000\n\t" \
    "s_add_u32 s50, s50, s45\n\t" \
    "s_add_u32 s50, s50, 0x2c00000\n\t" \
    "v_mov_b64 v[188:189], -1\n\t" \
    "buffer_store_dwordx2 v[188:189], v254, %[rs], s50 offen sc1\n\t" \
    ".Lw218_%=:\n\t" \
    "v_xor_b32 v249, 0x8000, v249\n\t" \
    "v_xor_b32 v250, 0x8000, v250\n\t" \
    LFLUSHW \
    "s_add_u32 s40, s40, 1\n\t" \
    "s_cmp_lg_u32 s40, 128\n\t" \
    "s_cbranch_scc1 .Lw24_%=\n\t" \
    "s_branch .Lend%=\n\t" \
    ".Lend%=:\n\t" \
    "s_waitcnt vmcnt(0) lgkmcnt(0)\n\t" \
    "s_mov_b32 %[bud], s41\n\t"
#define ASM_W3_LOOP \
    "s_mov_b32 s40, 0\n\t" \
    "s_mov_b32 s41, 0x20000\n\t" \
    LSTINIT \
    "s_add_u32 m0, %[wm], 0x0\n\t" \
    "s_add_u32 s44, %[wo], 0x0\n\t" \
    "buffer_load_dwordx4 v176, %[wr], s44 offen nt lds\n\t" \
    "s_add_u32 m0, %[wm], 0x2000\n\t" \
    "s_add_u32 s44, %[wo], 0xc000\n\t" \
    "buffer_load_dwordx4 v176, %[wr], s44 offen nt lds\n\t" \
    "s_add_u32 m0, %[wm], 0x4000\n\t" \
    "s_add_u32 s44, %[wo], 0x18000\n\t" \
    "buffer_load_dwordx4 v176, %[wr], s44 offen nt lds\n\t" \
    "s_add_u32 m0, %[wm], 0x6000\n\t" \
    "s_add_u32 s44, %[wo], 0x24000\n\t" \
    "buffer_load_dwordx4 v176, %[wr], s44 offen nt lds\n\t" \
    "s_add_u32 m0, %[wm], 0x8000\n\t" \
    "s_add_u32 s44, %[wo], 0x30000\n\t" \
    "buffer_load_dwordx4 v176, %[wr], s44 offen nt lds\n\t" \
    "s_add_u32 m0, %[wm], 0xa000\n\t" \
    "s_add_u32 s44, %[wo], 0x3c000\n\t" \
    "buffer_load_dwordx4 v176, %[wr], s44 offen nt lds\n\t" \
    "s_add_u32 m0, %[wm], 0xc000\n\t" \
    "s_add_u32 s44, %[wo], 0x48000\n\t" \
    "buffer_load_dwordx4 v176, %[wr], s44 offen nt lds\n\t" \
    "s_add_u32 m0, %[wm], 0xe000\n\t" \
    "s_add_u32 s44, %[wo], 0x54000\n\t" \
    "buffer_load_dwordx4 v176, %[wr], s44 offen nt lds\n\t" \
    "s_add_u32 m0, %[wm], 0x10000\n\t" \
    "s_add_u32 s44, %[wo], 0x60000\n\t" \
    "buffer_load_dwordx4 v176, %[wr], s44 offen nt lds\n\t" \
    "s_add_u32 m0, %[wm], 0x12000\n\t" \
    "s_add_u32 s44, %[wo], 0x6c000\n\t" \
    "buffer_load_dwordx4 v176, %[wr], s44 offen nt lds\n\t" \
    "s_add_u32 m0, %[wm], 0x14000\n\t" \
    "s_add_u32 s44, %[wo], 0x78000\n\t" \
    "buffer_load_dwordx4 v176, %[wr], s44 offen nt lds\n\t" \
    "s_add_u32 m0, %[wm], 0x16000\n\t" \
    "s_add_u32 s44, %[wo], 0x84000\n\t" \
    "buffer_load_dwordx4 v176, %[wr], s44 offen nt lds\n\t" \
    "s_add_u32 m0, %[wm], 0x18000\n\t" \
    "s_add_u32 s44, %[wo], 0x90000\n\t" \
    "buffer_load_dwordx4 v176, %[wr], s44 offen nt lds\n\t" \
    "s_add_u32 m0, %[wm], 0x1a000\n\t" \
    "s_add_u32 s44, %[wo], 0x9c000\n\t" \
    "buffer_load_dwordx4 v176, %[wr], s44 offen nt lds\n\t" \
    "s_add_u32 m0, %[wm], 0x1c000\n\t" \
    "s_add_u32 s44, %[wo], 0xa8000\n\t" \
    "buffer_load_dwordx4 v176, %[wr], s44 offen nt lds\n\t" \
    "s_add_u32 m0, %[wm], 0x1e000\n\t" \
    "s_add_u32 s44, %[wo], 0xb4000\n\t" \
    "buffer_load_dwordx4 v176, %[wr], s44 offen nt lds\n\t" \
    "s_add_u32 m0, %[wm], 0x20000\n\t" \
    "s_add_u32 s44, %[wo], 0xc0000\n\t" \
    "buffer_load_dwordx4 v176, %[wr], s44 offen nt lds\n\t" \
    "s_add_u32 m0, %[wm], 0x22000\n\t" \
    "s_add_u32 s44, %[wo], 0xcc000\n\t" \
    "buffer_load_dwordx4 v176, %[wr], s44 offen nt lds\n\t" \
    "s_add_u32 m0, %[wm], 0x24000\n\t" \
    "s_add_u32 s44, %[wo], 0xd8000\n\t" \
    "buffer_load_dwordx4 v176, %[wr], s44 offen nt lds\n\t" \
    "s_add_u32 m0, %[wm], 0x26000\n\t" \
    "s_add_u32 s44, %[wo], 0xe4000\n\t" \
    "buffer_load_dwordx4 v176, %[wr], s44 offen nt lds\n\t" \
    "s_waitcnt vmcnt(0) lgkmcnt(0)\n\t" \
    "s_barrier \n\t" \
    "v_add_u32 v178, 0x0, v177\n\t" \
    "v_xor_b32 v179, 32, v178\n\t" \
    "ds_read_b64_tr_b16 v[0:1], v178 offset:0\n\t" \
    "ds_read_b64_tr_b16 v[2:3], v178 offset:2048\n\t" \
    "ds_read_b64_tr_b16 v[80:81], v179 offset:0\n\t" \
    "ds_read_b64_tr_b16 v[82:83], v179 offset:2048\n\t" \
    "ds_read_b64_tr_b16 v[4:5], v178 offset:16384\n\t" \
    "ds_read_b64_tr_b16 v[6:7], v178 offset:18432\n\t" \
    "ds_read_b64_tr_b16 v[84:85], v179 offset:16384\n\t" \
    "ds_read_b64_tr_b16 v[86:87], v179 offset:18432\n\t" \
    "v_add_u32 v178, 0x8000, v177\n\t" \
    "v_xor_b32 v179, 32, v178\n\t" \
    "ds_read_b64_tr_b16 v[8:9], v178 offset:0\n\t" \
    "ds_read_b64_tr_b16 v[10:11], v178 offset:2048\n\t" \
    "ds_read_b64_tr_b16 v[88:89], v179 offset:0\n\t" \
    "ds_read_b64_tr_b16 v[90:91], v179 offset:2048\n\t" \
    "ds_read_b64_tr_b16 v[12:13], v178 offset:16384\n\t" \
    "ds_read_b64_tr_b16 v[14:15], v178 offset:18432\n\t" \
    "ds_read_b64_tr_b16 v[92:93], v179 offset:16384\n\t" \
    "ds_read_b64_tr_b16 v[94:95], v179 offset:18432\n\t" \
    "v_add_u32 v178, 0x10000, v177\n\t" \
    "v_xor_b32 v179, 32, v178\n\t" \
    "ds_read_b64_tr_b16 v[16:17], v178 offset:0\n\t" \
    "ds_read_b64_tr_b16 v[18:19], v178 offset:2048\n\t" \
    "ds_read_b64_tr_b16 v[96:97], v179 offset:0\n\t" \
    "ds_read_b64_tr_b16 v[98:99], v179 offset:2048\n\t" \
    "ds_read_b64_tr_b16 v[20:21], v178 offset:16384\n\t" \
    "ds_read_b64_tr_b16 v[22:23], v178 offset:18432\n\t" \
    "ds_read_b64_tr_b16 v[100:101], v179 offset:16384\n\t" \
    "ds_read_b64_tr_b16 v[102:103], v179 offset:18432\n\t" \
    "v_add_u32 v178, 0x18000, v177\n\t" \
    "v_xor_b32 v179, 32, v178\n\t" \
    "ds_read_b64_tr_b16 v[24:25], v178 offset:0\n\t" \
    "ds_read_b64_tr_b16 v[26:27], v178 offset:2048\n\t" \
    "ds_read_b64_tr_b16 v[104:105], v179 offset:0\n\t" \
    "ds_read_b64_tr_b16 v[106:107], v179 offset:2048\n\t" \
    "ds_read_b64_tr_b16 v[28:29], v178 offset:16384\n\t" \
    "ds_read_b64_tr_b16 v[30:31], v178 offset:18432\n\t" \
    "ds_read_b64_tr_b16 v[108:109], v179 offset:16384\n\t" \
    "ds_read_b64_tr_b16 v[110:111], v179 offset:18432\n\t" \
    "v_add_u32 v178, 0x20000, v177\n\t" \
    "v_xor_b32 v179, 32, v178\n\t" \
    "ds_read_b64_tr_b16 v[32:33], v178 offset:0\n\t" \
    "ds_read_b64_tr_b16 v[34:35], v178 offset:2048\n\t" \
    "ds_read_b64_tr_b16 v[112:113], v179 offset:0\n\t" \
    "ds_read_b64_tr_b16 v[114:115], v179 offset:2048\n\t" \
    "ds_read_b64_tr_b16 v[36:37], v178 offset:16384\n\t" \
    "ds_read_b64_tr_b16 v[38:39], v178 offset:18432\n\t" \
    "ds_read_b64_tr_b16 v[116:117], v179 offset:16384\n\t" \
    "ds_read_b64_tr_b16 v[118:119], v179 offset:18432\n\t" \
    "s_waitcnt lgkmcnt(0)\n\t" \
    "s_barrier \n\t" \
    "s_add_u32 m0, %[wm], 0x0\n\t" \
    "s_add_u32 s44, %[wo], 0xf0000\n\t" \
    "buffer_load_dwordx4 v176, %[wr], s44 offen nt lds\n\t" \
    "s_add_u32 m0, %[wm], 0x2000\n\t" \
    "s_add_u32 s44, %[wo], 0xfc000\n\t" \
    "buffer_load_dwordx4 v176, %[wr], s44 offen nt lds\n\t" \
    "s_add_u32 m0, %[wm], 0x4000\n\t" \
    "s_add_u32 s44, %[wo], 0x108000\n\t" \
    "buffer_load_dwordx4 v176, %[wr], s44 offen nt lds\n\t" \
    "s_add_u32 m0, %[wm], 0x6000\n\t" \
    "s_add_u32 s44, %[wo], 0x114000\n\t" \
    "buffer_load_dwordx4 v176, %[wr], s44 offen nt lds\n\t" \
    "s_add_u32 m0, %[wm], 0x8000\n\t" \
    "s_add_u32 s44, %[wo], 0x120000\n\t" \
    "buffer_load_dwordx4 v176, %[wr], s44 offen nt lds\n\t" \
    "s_add_u32 m0, %[wm], 0xa000\n\t" \
    "s_add_u32 s44, %[wo], 0x12c000\n\t" \
    "buffer_load_dwordx4 v176, %[wr], s44 offen nt lds\n\t" \
    "s_add_u32 m0, %[wm], 0xc000\n\t" \
    "s_add_u32 s44, %[wo], 0x138000\n\t" \
    "buffer_load_dwordx4 v176, %[wr], s44 offen nt lds\n\t" \
    "s_add_u32 m0, %[wm], 0xe000\n\t" \
    "s_add_u32 s44, %[wo], 0x144000\n\t" \
    "buffer_load_dwordx4 v176, %[wr], s44 offen nt lds\n\t" \
    "s_add_u32 m0, %[wm], 0x10000\n\t" \
    "s_add_u32 s44, %[wo], 0x150000\n\t" \
    "buffer_load_dwordx4 v176, %[wr], s44 offen nt lds\n\t" \
    "s_add_u32 m0, %[wm], 0x12000\n\t" \
    "s_add_u32 s44, %[wo], 0x15c000\n\t" \
    "buffer_load_dwordx4 v176, %[wr], s44 offen nt lds\n\t" \
    "s_add_u32 m0, %[wm], 0x14000\n\t" \
    "s_add_u32 s44, %[wo], 0x168000\n\t" \
    "buffer_load_dwordx4 v176, %[wr], s44 offen nt lds\n\t" \
    "s_add_u32 m0, %[wm], 0x16000\n\t" \
    "s_add_u32 s44, %[wo], 0x174000\n\t" \
    "buffer_load_dwordx4 v176, %[wr], s44 offen nt lds\n\t" \
    "s_add_u32 m0, %[wm], 0x18000\n\t" \
    "s_add_u32 s44, %[wo], 0x180000\n\t" \
    "buffer_load_dwordx4 v176, %[wr], s44 offen nt lds\n\t" \
    "s_add_u32 m0, %[wm], 0x1a000\n\t" \
    "s_add_u32 s44, %[wo], 0x18c000\n\t" \
    "buffer_load_dwordx4 v176, %[wr], s44 offen nt lds\n\t" \
    "s_add_u32 m0, %[wm], 0x1c000\n\t" \
    "s_add_u32 s44, %[wo], 0x198000\n\t" \
    "buffer_load_dwordx4 v176, %[wr], s44 offen nt lds\n\t" \
    "s_add_u32 m0, %[wm], 0x1e000\n\t" \
    "s_add_u32 s44, %[wo], 0x1a4000\n\t" \
    "buffer_load_dwordx4 v176, %[wr], s44 offen nt lds\n\t" \
    "s_add_u32 m0, %[wm], 0x20000\n\t" \
    "s_add_u32 s44, %[wo], 0x1b0000\n\t" \
    "buffer_load_dwordx4 v176, %[wr], s44 offen nt lds\n\t" \
    "s_add_u32 m0, %[wm], 0x22000\n\t" \
    "s_add_u32 s44, %[wo], 0x1bc000\n\t" \
    "buffer_load_dwordx4 v176, %[wr], s44 offen nt lds\n\t" \
    "s_add_u32 m0, %[wm], 0x24000\n\t" \
    "s_add_u32 s44, %[wo], 0x1c8000\n\t" \
    "buffer_load_dwordx4 v176, %[wr], s44 offen nt lds\n\t" \
    "s_add_u32 m0, %[wm], 0x26000\n\t" \
    "s_add_u32 s44, %[wo], 0x1d4000\n\t" \
    "buffer_load_dwordx4 v176, %[wr], s44 offen nt lds\n\t" \
    "s_waitcnt vmcnt(0) lgkmcnt(0)\n\t" \
    "s_barrier \n\t" \
    "v_add_u32 v178, 0x0, v177\n\t" \
    "v_xor_b32 v179, 32, v178\n\t" \
    "ds_read_b64_tr_b16 v[40:41], v178 offset:0\n\t" \
    "ds_read_b64_tr_b16 v[42:43], v178 offset:2048\n\t" \
    "ds_read_b64_tr_b16 v[120:121], v179 offset:0\n\t" \
    "ds_read_b64_tr_b16 v[122:123], v179 offset:2048\n\t" \
    "ds_read_b64_tr_b16 v[44:45], v178 offset:16384\n\t" \
    "ds_read_b64_tr_b16 v[46:47], v178 offset:18432\n\t" \
    "ds_read_b64_tr_b16 v[124:125], v179 offset:16384\n\t" \
    "ds_read_b64_tr_b16 v[126:127], v179 offset:18432\n\t" \
    "v_add_u32 v178, 0x8000, v177\n\t" \
    "v_xor_b32 v179, 32, v178\n\t" \
    "ds_read_b64_tr_b16 v[48:49], v178 offset:0\n\t" \
    "ds_read_b64_tr_b16 v[50:51], v178 offset:2048\n\t" \
    "ds_read_b64_tr_b16 v[128:129], v179 offset:0\n\t" \
    "ds_read_b64_tr_b16 v[130:131], v179 offset:2048\n\t" \
    "ds_read_b64_tr_b16 v[52:53], v178 offset:16384\n\t" \
    "ds_read_b64_tr_b16 v[54:55], v178 offset:18432\n\t" \
    "ds_read_b64_tr_b16 v[132:133], v179 offset:16384\n\t" \
    "ds_read_b64_tr_b16 v[134:135], v179 offset:18432\n\t" \
    "v_add_u32 v178, 0x10000, v177\n\t" \
    "v_xor_b32 v179, 32, v178\n\t" \
    "ds_read_b64_tr_b16 v[56:57], v178 offset:0\n\t" \
    "ds_read_b64_tr_b16 v[58:59], v178 offset:2048\n\t" \
    "ds_read_b64_tr_b16 v[136:137], v179 offset:0\n\t" \
    "ds_read_b64_tr_b16 v[138:139], v179 offset:2048\n\t" \
    "ds_read_b64_tr_b16 v[60:61], v178 offset:16384\n\t" \
    "ds_read_b64_tr_b16 v[62:63], v178 offset:18432\n\t" \
    "ds_read_b64_tr_b16 v[140:141], v179 offset:16384\n\t" \
    "ds_read_b64_tr_b16 v[142:143], v179 offset:18432\n\t" \
    "v_add_u32 v178, 0x18000, v177\n\t" \
    "v_xor_b32 v179, 32, v178\n\t" \
    "ds_read_b64_tr_b16 v[64:65], v178 offset:0\n\t" \
    "ds_read_b64_tr_b16 v[66:67], v178 offset:2048\n\t" \
    "ds_read_b64_tr_b16 v[144:145], v179 offset:0\n\t" \
    "ds_read_b64_tr_b16 v[146:147], v179 offset:2048\n\t" \
    "ds_read_b64_tr_b16 v[68:69], v178 offset:16384\n\t" \
    "ds_read_b64_tr_b16 v[70:71], v178 offset:18432\n\t" \
    "ds_read_b64_tr_b16 v[148:149], v179 offset:16384\n\t" \
    "ds_read_b64_tr_b16 v[150:151], v179 offset:18432\n\t" \
    "v_add_u32 v178, 0x20000, v177\n\t" \
    "v_xor_b32 v179, 32, v178\n\t" \
    "ds_read_b64_tr_b16 v[72:73], v178 offset:0\n\t" \
    "ds_read_b64_tr_b16 v[74:75], v178 offset:2048\n\t" \
    "ds_read_b64_tr_b16 v[152:153], v179 offset:0\n\t" \
    "ds_read_b64_tr_b16 v[154:155], v179 offset:2048\n\t" \
    "ds_read_b64_tr_b16 v[76:77], v178 offset:16384\n\t" \
    "ds_read_b64_tr_b16 v[78:79], v178 offset:18432\n\t" \
    "ds_read_b64_tr_b16 v[156:157], v179 offset:16384\n\t" \
    "ds_read_b64_tr_b16 v[158:159], v179 offset:18432\n\t" \
    "v_mov_b32 v228, 0x3f803f80\n\t" \
    "v_mov_b32 v229, 0x3f803f80\n\t" \
    "v_mov_b32 v230, 0x3f803f80\n\t" \
    "v_mov_b32 v231, 0x3f803f80\n\t" \
    "v_mov_b64 v[216:217], 0\n\t" \
    "v_bfe_u32 v255, v253, 2, 5\n\t" \
    "v_add_u32 v255, 0x7000, v255\n\t" \
    "v_bfe_u32 v218, v253, 6, 1\n\t" \
    "s_waitcnt lgkmcnt(0)\n\t" \
    "s_barrier \n\t" \
    "ds_write_b32 v251, v228 offset:72\n\t" \
    "ds_write_b32 v251, v228 offset:2120\n\t" \
    "v_mfma_f32_16x16x32_bf16 v[160:163], v[0:3], v[228:231], 0\n\t" \
    "v_mfma_f32_16x16x32_bf16 v[164:167], v[80:83], v[228:231], 0\n\t" \
    "v_mfma_f32_16x16x32_bf16 v[160:163], v[4:7], v[228:231], v[160:163]\n\t" \
    "v_mfma_f32_16x16x32_bf16 v[164:167], v[84:87], v[228:231], v[164:167]\n\t" \
    "v_mfma_f32_16x16x32_bf16 v[160:163], v[8:11], v[228:231], v[160:163]\n\t" \
    "v_mfma_f32_16x16x32_bf16 v[164:167], v[88:91], v[228:231], v[164:167]\n\t" \
    "v_mfma_f32_16x16x32_bf16 v[160:163], v[12:15], v[228:231], v[160:163]\n\t" \
    "v_mfma_f32_16x16x32_bf16 v[164:167], v[92:95], v[228:231], v[164:167]\n\t" \
    "v_mfma_f32_16x16x32_bf16 v[160:163], v[16:19], v[228:231], v[160:163]\n\t" \
    "v_mfma_f32_16x16x32_bf16 v[164:167], v[96:99], v[228:231], v[164:167]\n\t" \
    "v_mfma_f32_16x16x32_bf16 v[160:163], v[20:23], v[228:231], v[160:163]\n\t" \
    "v_mfma_f32_16x16x32_bf16 v[164:167], v[100:103], v[228:231], v[164:167]\n\t" \
    "v_mfma_f32_16x16x32_bf16 v[160:163], v[24:27], v[228:231], v[160:163]\n\t" \
    "v_mfma_f32_16x16x32_bf16 v[164:167], v[104:107], v[228:231], v[164:167]\n\t" \
    "v_mfma_f32_16x16x32_bf16 v[160:163], v[28:31], v[228:231], v[160:163]\n\t" \
    "v_mfma_f32_16x16x32_bf16 v[164:167], v[108:111], v[228:231], v[164:167]\n\t" \
    "v_mfma_f32_16x16x32_bf16 v[160:163], v[32:35], v[228:231], v[160:163]\n\t" \
    "v_mfma_f32_16x16x32_bf16 v[164:167], v[112:115], v[228:231], v[164:167]\n\t" \
    "v_mfma_f32_16x16x32_bf16 v[160:163], v[36:39], v[228:231], v[160:163]\n\t" \
    "v_mfma_f32_16x16x32_bf16 v[164:167], v[116:119], v[228:231], v[164:167]\n\t" \
    "v_mfma_f32_16x16x32_bf16 v[160:163], v[40:43], v[228:231], v[160:163]\n\t" \
    "v_mfma_f32_16x16x32_bf16 v[164:167], v[120:123], v[228:231], v[164:167]\n\t" \
    "v_mfma_f32_16x16x32_bf16 v[160:163], v[44:47], v[228:231], v[160:163]\n\t" \
    "v_mfma_f32_16x16x32_bf16 v[164:167], v[124:127], v[228:231], v[164:167]\n\t" \
    "v_mfma_f32_16x16x32_bf16 v[160:163], v[48:51], v[228:231], v[160:163]\n\t" \
    "v_mfma_f32_16x16x32_bf16 v[164:167], v[128:131], v[228:231], v[164:167]\n\t" \
    "v_mfma_f32_16x16x32_bf16 v[160:163], v[52:55], v[228:231], v[160:163]\n\t" \
    "v_mfma_f32_16x16x32_bf16 v[164:167], v[132:135], v[228:231], v[164:167]\n\t" \
    "v_mfma_f32_16x16x32_bf16 v[160:163], v[56:59], v[228:231], v[160:163]\n\t" \
    "v_mfma_f32_16x16x32_bf16 v[164:167], v[136:139], v[228:231], v[164:167]\n\t" \
    "v_mfma_f32_16x16x32_bf16 v[160:163], v[60:63], v[228:231], v[160:163]\n\t" \
    "v_mfma_f32_16x16x32_bf16 v[164:167], v[140:143], v[228:231], v[164:167]\n\t" \
    "v_mfma_f32_16x16x32_bf16 v[160:163], v[64:67], v[228:231], v[160:163]\n\t" \
    "v_mfma_f32_16x16x32_bf16 v[164:167], v[144:147], v[228:231], v[164:167]\n\t" \
    "v_mfma_f32_16x16x32_bf16 v[160:163], v[68:71], v[228:231], v[160:163]\n\t" \
    "v_mfma_f32_16x16x32_bf16 v[164:167], v[148:151], v[228:231], v[164:167]\n\t" \
    "v_mfma_f32_16x16x32_bf16 v[160:163], v[72:75], v[228:231], v[160:163]\n\t" \
    "v_mfma_f32_16x16x32_bf16 v[164:167], v[152:155], v[228:231], v[164:167]\n\t" \
    "v_mfma_f32_16x16x32_bf16 v[160:163], v[76:79], v[228:231], v[160:163]\n\t" \
    "v_mfma_f32_16x16x32_bf16 v[164:167], v[156:159], v[228:231], v[164:167]\n\t" \
    "s_mov_b32 s52, 0x22222222\n\t" \
    "s_mov_b32 s53, 0x22222222\n\t" \
    "s_mov_b32 s54, 0x44444444\n\t" \
    "s_mov_b32 s55, 0x44444444\n\t" \
    "s_mov_b32 s56, 0x88888888\n\t" \
    "s_mov_b32 s57, 0x88888888\n\t" \
    "ds_write_b32 v255, v218\n\t" \
    "s_waitcnt lgkmcnt(0)\n\t" \
    "s_barrier \n\t" \
    ".Lw31_%=:\n\t" \
    "v_mov_b32 v248, 0\n\t" \
    "buffer_load_dword v248, v248, %[rs], %[go] offen sc1\n\t" \
    "s_waitcnt vmcnt(0)\n\t" \
    "v_cmp_ne_u32 vcc, 0, v248\n\t" \
    "s_cbranch_vccnz .Lw32_%=\n\t" \
    "s_cmp_eq_u32 s41, 0\n\t" \
    "s_cbranch_scc1 .Lw32_%=\n\t" \
    "s_sub_u32 s41, s41, 1\n\t" \
    "s_sleep 16\n\t" \
    "s_branch .Lw31_%=\n\t" \
    ".Lw32_%=:\n\t" \
    "s_mov_b32 s43, 0\n\t" \
    "s_add_u32 s46, s43, %[pi]\n\t" \
    "s_cmp_gt_u32 %[wave], 3\n\t" \
    "s_cbranch_scc1 .Lw33_%=\n\t" \
    "s_add_u32 s47, s46, 0x0\n\t" \
    "buffer_load_dwordx4 v[208:211], v253, %[rs], s47 offen sc1\n\t" \
    "s_add_u32 s47, s46, 0x4000\n\t" \
    "buffer_load_dwordx4 v[212:215], v253, %[rs], s47 offen sc1\n\t" \
    "s_add_u32 s47, s46, 0x8000\n\t" \
    "buffer_load_dwordx4 v[216:219], v253, %[rs], s47 offen sc1\n\t" \
    "s_add_u32 s47, s46, 0xc000\n\t" \
    "buffer_load_dwordx4 v[220:223], v253, %[rs], s47 offen sc1\n\t" \
    "s_add_u32 s47, s46, 0x10000\n\t" \
    "buffer_load_dwordx4 v[224:227], v253, %[rs], s47 offen sc1\n\t" \
    ".Lw33_%=:\n\t" \
    "s_waitcnt vmcnt(0)\n\t" \
    ".Lw34_%=:\n\t" \
    "s_add_i32 s44, s40, 0\n\t" \
    "s_max_i32 s44, s44, 0\n\t" \
    "s_lshr_b32 s45, s44, 1\n\t" \
    "s_and_b32 s44, s44, 1\n\t" \
    "s_lshl_b32 s44, s44, 12\n\t" \
    "s_mul_i32 s49, s45, 0x30000\n\t" \
    "s_add_u32 s49, s49, s44\n\t" \
    "s_lshr_b32 s45, s45, 3\n\t" \
    "s_mul_i32 s45, s45, 0x82000\n\t" \
    "s_add_u32 s49, s49, s45\n\t" \
    "s_add_u32 s49, s49, %[ng]\n\t" \
    "s_add_i32 s44, s40, 1\n\t" \
    "s_max_i32 s44, s44, 0\n\t" \
    "s_lshr_b32 s45, s44, 1\n\t" \
    "s_and_b32 s44, s44, 1\n\t" \
    "s_lshl_b32 s44, s44, 12\n\t" \
    "s_mul_i32 s43, s45, 0x14000\n\t" \
    "s_add_u32 s43, s43, s44\n\t" \
    "s_lshr_b32 s45, s45, 4\n\t" \
    "s_mul_i32 s45, s45, 0xc2000\n\t" \
    "s_add_u32 s43, s43, s45\n\t" \
    LST0 \
    LJIT \
    "s_cmp_gt_u32 %[wave], 3\n\t" \
    "s_cbranch_scc1 .Lw37_%=\n\t" \
    "s_setprio 2\n\t" \
    "v_max_u32 v248, v208, v209\n\t" \
    "v_max3_u32 v248, v210, v211, v248\n\t" \
    "v_max3_u32 v248, v212, v213, v248\n\t" \
    "v_max3_u32 v248, v214, v215, v248\n\t" \
    "v_max3_u32 v248, v216, v217, v248\n\t" \
    "v_max3_u32 v248, v218, v219, v248\n\t" \
    "v_max3_u32 v248, v220, v221, v248\n\t" \
    "v_max3_u32 v248, v222, v223, v248\n\t" \
    "v_max3_u32 v248, v224, v225, v248\n\t" \
    "v_max3_u32 v248, v226, v227, v248\n\t" \
    "v_cmp_eq_u32 vcc, -1, v248\n\t" \
    "s_cbranch_vccz .Lw36_%=\n\t" \
    "s_and_b32 s44, s40, 1\n\t" \
    "s_cmp_eq_u32 s44, 0\n\t" \
    "s_cbranch_scc1 .Lw35_%=\n\t" \
    "s_sleep 6\n\t" \
    ".Lw35_%=:\n\t" \
    "v_max_u32 v248, v208, v209\n\t" \
    "v_max3_u32 v248, v210, v211, v248\n\t" \
    "v_cmp_eq_u32 vcc, -1, v248\n\t" \
    "s_cbranch_vccz .Lw38_%=\n\t" \
    "s_add_u32 s47, s46, 0x0\n\t" \
    "buffer_load_dwordx4 v[208:211], v253, %[rs], s47 offen sc1\n\t" \
    ".Lw38_%=:\n\t" \
    "v_max_u32 v248, v212, v213\n\t" \
    "v_max3_u32 v248, v214, v215, v248\n\t" \
    "v_cmp_eq_u32 vcc, -1, v248\n\t" \
    "s_cbranch_vccz .Lw39_%=\n\t" \
    "s_add_u32 s47, s46, 0x4000\n\t" \
    "buffer_load_dwordx4 v[212:215], v253, %[rs], s47 offen sc1\n\t" \
    ".Lw39_%=:\n\t" \
    "v_max_u32 v248, v216, v217\n\t" \
    "v_max3_u32 v248, v218, v219, v248\n\t" \
    "v_cmp_eq_u32 vcc, -1, v248\n\t" \
    "s_cbranch_vccz .Lw310_%=\n\t" \
    "s_add_u32 s47, s46, 0x8000\n\t" \
    "buffer_load_dwordx4 v[216:219], v253, %[rs], s47 offen sc1\n\t" \
    ".Lw310_%=:\n\t" \
    "v_max_u32 v248, v220, v221\n\t" \
    "v_max3_u32 v248, v222, v223, v248\n\t" \
    "v_cmp_eq_u32 vcc, -1, v248\n\t" \
    "s_cbranch_vccz .Lw311_%=\n\t" \
    "s_add_u32 s47, s46, 0xc000\n\t" \
    "buffer_load_dwordx4 v[220:223], v253, %[rs], s47 offen sc1\n\t" \
    ".Lw311_%=:\n\t" \
    "v_max_u32 v248, v224, v225\n\t" \
    "v_max3_u32 v248, v226, v227, v248\n\t" \
    "v_cmp_eq_u32 vcc, -1, v248\n\t" \
    "s_cbranch_vccz .Lw312_%=\n\t" \
    "s_add_u32 s47, s46, 0x10000\n\t" \
    "buffer_load_dwordx4 v[224:227], v253, %[rs], s47 offen sc1\n\t" \
    ".Lw312_%=:\n\t" \
    "s_waitcnt vmcnt(0)\n\t" \
    "v_max_u32 v248, v208, v209\n\t" \
    "v_max3_u32 v248, v210, v211, v248\n\t" \
    "v_max3_u32 v248, v212, v213, v248\n\t" \
    "v_max3_u32 v248, v214, v215, v248\n\t" \
    "v_max3_u32 v248, v216, v217, v248\n\t" \
    "v_max3_u32 v248, v218, v219, v248\n\t" \
    "v_max3_u32 v248, v220, v221, v248\n\t" \
    "v_max3_u32 v248, v222, v223, v248\n\t" \
    "v_max3_u32 v248, v224, v225, v248\n\t" \
    "v_max3_u32 v248, v226, v227, v248\n\t" \
    "v_cmp_eq_u32 vcc, -1, v248\n\t" \
    "s_cbranch_vccz .Lw36_%=\n\t" \
    "s_cmp_eq_u32 s41, 0\n\t" \
    "s_cbranch_scc1 .Lw36_%=\n\t" \
    "s_sub_u32 s41, s41, 1\n\t" \
    "s_branch .Lw35_%=\n\t" \
    ".Lw36_%=:\n\t" \
    LST1 \
    LJIT \
    "ds_write_b128 v250, v[208:211] offset:0\n\t" \
    "v_mfma_f32_16x16x32_bf16 v[236:239], v[208:211], v[208:211], 0\n\t" \
    "v_mfma_f32_16x16x32_bf16 v[232:235], v[228:231], v[208:211], 0\n\t" \
    "ds_write_b128 v250, v[212:215] offset:4096\n\t" \
    "v_mfma_f32_16x16x32_bf16 v[236:239], v[212:215], v[212:215], v[236:239]\n\t" \
    "v_mfma_f32_16x16x32_bf16 v[232:235], v[228:231], v[212:215], v[232:235]\n\t" \
    "ds_write_b128 v250, v[216:219] offset:8192\n\t" \
    "v_mfma_f32_16x16x32_bf16 v[236:239], v[216:219], v[216:219], v[236:239]\n\t" \
    "v_mfma_f32_16x16x32_bf16 v[232:235], v[228:231], v[216:219], v[232:235]\n\t" \
    "ds_write_b128 v250, v[220:223] offset:12288\n\t" \
    "v_mfma_f32_16x16x32_bf16 v[236:239], v[220:223], v[220:223], v[236:239]\n\t" \
    "v_mfma_f32_16x16x32_bf16 v[232:235], v[228:231], v[220:223], v[232:235]\n\t" \
    "ds_write_b128 v250, v[224:227] offset:16384\n\t" \
    "v_mfma_f32_16x16x32_bf16 v[236:239], v[224:227], v[224:227], v[236:239]\n\t" \
    "v_mfma_f32_16x16x32_bf16 v[232:235], v[228:231], v[224:227], v[232:235]\n\t" \
    "s_add_u32 s46, s43, %[pi]\n\t" \
    "s_nop 4\n\t" \
    "v_cndmask_b32 v236, v236, v237, s[52:53]\n\t" \
    "v_cndmask_b32 v236, v236, v238, s[54:55]\n\t" \
    "v_cndmask_b32 v236, v236, v239, s[56:57]\n\t" \
    "s_mov_b32 exec_lo, 0xf0000f\n\t" \
    "s_mov_b32 exec_hi, 0xf0000f00\n\t" \
    "ds_write2_b32 v252, v232, v236 offset1:1\n\t" \
    "s_mov_b64 exec, -1\n\t" \
    "s_add_u32 s44, s40, 1\n\t" \
    "v_mov_b32 v248, s44\n\t" \
    "s_lshl_b64 exec, 1, %[wave]\n\t" \
    "ds_write_b32 v255, v248\n\t" \
    "s_mov_b64 exec, -1\n\t" \
    ".Lw37_%=:\n\t" \
    "ds_read_b32 v248, v255\n\t" \
    "s_waitcnt lgkmcnt(0)\n\t" \
    "v_cmp_ge_u32 vcc, s40, v248\n\t" \
    "s_cbranch_vccz .Lw313_%=\n\t" \
    "s_cmp_lt_u32 %[wave], 4\n\t" \
    "s_cbranch_scc1 .Lw37_%=\n\t" \
    "s_sleep 1\n\t" \
    "s_branch .Lw37_%=\n\t" \
    ".Lw313_%=:\n\t" \
    "s_setprio 0\n\t" \
    LST2 \
    LJIT \
    "s_cmp_lg_u32 %[wave], 4\n\t" \
    "s_cbranch_scc1 .Lw314_%=\n\t" \
    "s_setprio 1\n\t" \
    "ds_read_b128 v[232:235], v251 offset:0\n\t" \
    "ds_read_b128 v[236:239], v251 offset:16\n\t" \
    "s_waitcnt lgkmcnt(0)\n\t" \
    "v_add_f32 v232, v232, v236\n\t" \
    "v_add_f32 v233, v233, v237\n\t" \
    "v_add_f32 v234, v234, v238\n\t" \
    "v_add_f32 v235, v235, v239\n\t" \
    "v_add_f32 v232, v232, v234\n\t" \
    "v_add_f32 v233, v233, v235\n\t" \
    "v_mul_f32 v232, 0x3acccccd, v232\n\t" \
    "v_mul_f32 v233, 0x3acccccd, v233\n\t" \
    "v_fma_f32 v233, -v232, v232, v233\n\t" \
    "v_mov_b32 v234, v232\n\t" \
    "v_max_f32 v233, 0, v233\n\t" \
    "v_cvt_pk_bf16_f32 v234, v234, v234\n\t" \
    "v_and_b32 v234, 0xffff0000, v234\n\t" \
    "v_mov_b32 v236, v233\n\t" \
    "v_cvt_pk_bf16_f32 v236, v236, v236\n\t" \
    "v_and_b32 v236, 0xffff0000, v236\n\t" \
    "v_add_f32 v236, 0x3727c5ac, v236\n\t" \
    "v_cvt_pk_bf16_f32 v236, v236, v236\n\t" \
    "v_and_b32 v236, 0xffff0000, v236\n\t" \
    "v_sqrt_f32 v236, v236\n\t" \
    "s_nop 0\n\t" \
    "v_cvt_pk_bf16_f32 v236, v236, v236\n\t" \
    "v_and_b32 v236, 0xffff0000, v236\n\t" \
    "v_rcp_f32 v235, v236\n\t" \
    "s_nop 0\n\t" \
    "v_mov_b32 v237, v235\n\t" \
    "v_mul_f32 v238, -v234, v235\n\t" \
    "v_mov_b32 v239, s40\n\t" \
    "ds_write2_b32 v251, v237, v238 offset0:16 offset1:17\n\t" \
    "ds_write_b32 v251, v239 offset:72\n\t" \
    "s_setprio 0\n\t" \
    ".Lw314_%=:\n\t" \
    "ds_read_b128 v[176:179], v249 offset:0\n\t" \
    "ds_read_b128 v[180:183], v249 offset:1024\n\t" \
    "ds_read_b128 v[184:187], v249 offset:2048\n\t" \
    "ds_read_b128 v[188:191], v249 offset:3072\n\t" \
    "ds_read_b128 v[192:195], v249 offset:4096\n\t" \
    "ds_read_b128 v[196:199], v249 offset:5120\n\t" \
    "ds_read_b128 v[200:203], v249 offset:6144\n\t" \
    "s_waitcnt lgkmcnt(6)\n\t" \
    "v_mfma_f32_16x16x32_bf16 v[168:171], v[0:3], v[176:179], 0\n\t" \
    "ds_read_b128 v[204:207], v249 offset:7168\n\t" \
    "v_mfma_f32_16x16x32_bf16 v[172:175], v[80:83], v[176:179], 0\n\t" \
    "s_waitcnt lgkmcnt(6)\n\t" \
    "v_mfma_f32_16x16x32_bf16 v[168:171], v[4:7], v[180:183], v[168:171]\n\t" \
    "ds_read_b128 v[176:179], v249 offset:8192\n\t" \
    "v_mfma_f32_16x16x32_bf16 v[172:175], v[84:87], v[180:183], v[172:175]\n\t" \
    "s_waitcnt lgkmcnt(6)\n\t" \
    "v_mfma_f32_16x16x32_bf16 v[168:171], v[8:11], v[184:187], v[168:171]\n\t" \
    "ds_read_b128 v[180:183], v249 offset:9216\n\t" \
    "v_mfma_f32_16x16x32_bf16 v[172:175], v[88:91], v[184:187], v[172:175]\n\t" \
    "s_waitcnt lgkmcnt(6)\n\t" \
    "v_mfma_f32_16x16x32_bf16 v[168:171], v[12:15], v[188:191], v[168:171]\n\t" \
    "ds_read_b128 v[184:187], v249 offset:10240\n\t" \
    "v_mfma_f32_16x16x32_bf16 v[172:175], v[92:95], v[188:191], v[172:175]\n\t" \
    "s_waitcnt lgkmcnt(6)\n\t" \
    "v_mfma_f32_16x16x32_bf16 v[168:171], v[16:19], v[192:195], v[168:171]\n\t" \
    "ds_read_b128 v[188:191], v249 offset:11264\n\t" \
    "v_mfma_f32_16x16x32_bf16 v[172:175], v[96:99], v[192:195], v[172:175]\n\t" \
    "s_waitcnt lgkmcnt(6)\n\t" \
    "v_mfma_f32_16x16x32_bf16 v[168:171], v[20:23], v[196:199], v[168:171]\n\t" \
    "ds_read_b128 v[192:195], v249 offset:12288\n\t" \
    "v_mfma_f32_16x16x32_bf16 v[172:175], v[100:103], v[196:199], v[172:175]\n\t" \
    "s_waitcnt lgkmcnt(6)\n\t" \
    "v_mfma_f32_16x16x32_bf16 v[168:171], v[24:27], v[200:203], v[168:171]\n\t" \
    "ds_read_b128 v[196:199], v249 offset:13312\n\t" \
    "v_mfma_f32_16x16x32_bf16 v[172:175], v[104:107], v[200:203], v[172:175]\n\t" \
    "s_waitcnt lgkmcnt(6)\n\t" \
    "v_mfma_f32_16x16x32_bf16 v[168:171], v[28:31], v[204:207], v[168:171]\n\t" \
    "ds_read_b128 v[200:203], v249 offset:14336\n\t" \
    "v_mfma_f32_16x16x32_bf16 v[172:175], v[108:111], v[204:207], v[172:175]\n\t" \
    "s_waitcnt lgkmcnt(6)\n\t" \
    "v_mfma_f32_16x16x32_bf16 v[168:171], v[32:35], v[176:179], v[168:171]\n\t" \
    "ds_read_b128 v[204:207], v249 offset:15360\n\t" \
    "v_mfma_f32_16x16x32_bf16 v[172:175], v[112:115], v[176:179], v[172:175]\n\t" \
    "s_waitcnt lgkmcnt(6)\n\t" \
    "v_mfma_f32_16x16x32_bf16 v[168:171], v[36:39], v[180:183], v[168:171]\n\t" \
    "ds_read_b128 v[176:179], v249 offset:16384\n\t" \
    "v_mfma_f32_16x16x32_bf16 v[172:175], v[116:119], v[180:183], v[172:175]\n\t" \
    "s_waitcnt lgkmcnt(6)\n\t" \
    "v_mfma_f32_16x16x32_bf16 v[168:171], v[40:43], v[184:187], v[168:171]\n\t" \
    "ds_read_b128 v[180:183], v249 offset:17408\n\t" \
    "v_mfma_f32_16x16x32_bf16 v[172:175], v[120:123], v[184:187], v[172:175]\n\t" \
    "s_waitcnt lgkmcnt(6)\n\t" \
    "v_mfma_f32_16x16x32_bf16 v[168:171], v[44:47], v[188:191], v[168:171]\n\t" \
    "ds_read_b128 v[184:187], v249 offset:18432\n\t" \
    "v_mfma_f32_16x16x32_bf16 v[172:175], v[124:127], v[188:191], v[172:175]\n\t" \
    "s_waitcnt lgkmcnt(6)\n\t" \
    "v_mfma_f32_16x16x32_bf16 v[168:171], v[48:51], v[192:195], v[168:171]\n\t" \
    "ds_read_b128 v[188:191], v249 offset:19456\n\t" \
    "v_mfma_f32_16x16x32_bf16 v[172:175], v[128:131], v[192:195], v[172:175]\n\t" \
    "s_waitcnt lgkmcnt(6)\n\t" \
    "v_mfma_f32_16x16x32_bf16 v[168:171], v[52:55], v[196:199], v[168:171]\n\t" \
    "v_mfma_f32_16x16x32_bf16 v[172:175], v[132:135], v[196:199], v[172:175]\n\t" \
    "s_waitcnt lgkmcnt(5)\n\t" \
    "v_mfma_f32_16x16x32_bf16 v[168:171], v[56:59], v[200:203], v[168:171]\n\t" \
    "v_mfma_f32_16x16x32_bf16 v[172:175], v[136:139], v[200:203], v[172:175]\n\t" \
    "s_cmp_gt_u32 %[wave], 3\n\t" \
    "s_cbranch_scc1 .Lw316_%=\n\t" \
    "s_add_u32 s47, s46, 0x0\n\t" \
    "buffer_load_dwordx4 v[208:211], v253, %[rs], s47 offen sc1\n\t" \
    "s_add_u32 s47, s46, 0x4000\n\t" \
    "buffer_load_dwordx4 v[212:215], v253, %[rs], s47 offen sc1\n\t" \
    "s_add_u32 s47, s46, 0x8000\n\t" \
    "buffer_load_dwordx4 v[216:219], v253, %[rs], s47 offen sc1\n\t" \
    "s_add_u32 s47, s46, 0xc000\n\t" \
    "buffer_load_dwordx4 v[220:223], v253, %[rs], s47 offen sc1\n\t" \
    "s_add_u32 s47, s46, 0x10000\n\t" \
    "buffer_load_dwordx4 v[224:227], v253, %[rs], s47 offen sc1\n\t" \
    ".Lw316_%=:\n\t" \
    "s_waitcnt lgkmcnt(4)\n\t" \
    "v_mfma_f32_16x16x32_bf16 v[168:171], v[60:63], v[204:207], v[168:171]\n\t" \
    "v_mfma_f32_16x16x32_bf16 v[172:175], v[140:143], v[204:207], v[172:175]\n\t" \
    "ds_read_b32 v236, v251 offset:72\n\t" \
    "ds_read_b64 v[238:239], v251 offset:64\n\t" \
    "s_waitcnt lgkmcnt(5)\n\t" \
    "v_mfma_f32_16x16x32_bf16 v[168:171], v[64:67], v[176:179], v[168:171]\n\t" \
    "v_mfma_f32_16x16x32_bf16 v[172:175], v[144:147], v[176:179], v[172:175]\n\t" \
    "s_waitcnt lgkmcnt(4)\n\t" \
    "v_mfma_f32_16x16x32_bf16 v[168:171], v[68:71], v[180:183], v[168:171]\n\t" \
    "v_mfma_f32_16x16x32_bf16 v[172:175], v[148:151], v[180:183], v[172:175]\n\t" \
    ".Lw317_%=:\n\t" \
    "s_waitcnt lgkmcnt(0)\n\t" \
    "v_cmp_ne_u32 vcc, s40, v236\n\t" \
    "s_cbranch_vccz .Lw318_%=\n\t" \
    "ds_read_b32 v236, v251 offset:72\n\t" \
    "ds_read_b64 v[238:239], v251 offset:64\n\t" \
    "s_branch .Lw317_%=\n\t" \
    ".Lw318_%=:\n\t" \
    "s_waitcnt lgkmcnt(0)\n\t" \
    "v_mul_f32 v240, v160, v239\n\t" \
    "v_mul_f32 v241, v161, v239\n\t" \
    "v_mfma_f32_16x16x32_bf16 v[168:171], v[72:75], v[184:187], v[168:171]\n\t" \
    "v_mul_f32 v242, v162, v239\n\t" \
    "v_mul_f32 v243, v163, v239\n\t" \
    "v_mfma_f32_16x16x32_bf16 v[172:175], v[152:155], v[184:187], v[172:175]\n\t" \
    "v_mul_f32 v244, v164, v239\n\t" \
    "v_mul_f32 v245, v165, v239\n\t" \
    "v_mfma_f32_16x16x32_bf16 v[168:171], v[76:79], v[188:191], v[168:171]\n\t" \
    "v_mul_f32 v246, v166, v239\n\t" \
    "v_mul_f32 v247, v167, v239\n\t" \
    "v_mfma_f32_16x16x32_bf16 v[172:175], v[156:159], v[188:191], v[172:175]\n\t" \
    "v_mov_b32 v237, 0xc0135761\n\t" \
    "s_cmp_lt_u32 %[wave], 4\n\t" \
    "s_cbranch_scc1 .Lw319_%=\n\t" \
    "s_add_u32 s44, s40, 2\n\t" \
    "v_mov_b32 v248, s44\n\t" \
    "s_lshl_b64 exec, 1, %[wave]\n\t" \
    "ds_write_b32 v255, v248\n\t" \
    "s_mov_b64 exec, -1\n\t" \
    ".Lw319_%=:\n\t" \
    "s_nop 1\n\t" \
    LST3 \
    LJIT \
    "s_nop 2\n\t" \
    "v_fma_f32 v168, v168, v238, v240\n\t" \
    "v_fma_f32 v169, v169, v238, v241\n\t" \
    "v_fma_f32 v170, v170, v238, v242\n\t" \
    "v_fma_f32 v171, v171, v238, v243\n\t" \
    "v_fma_f32 v172, v172, v238, v244\n\t" \
    "v_fma_f32 v173, v173, v238, v245\n\t" \
    "v_fma_f32 v174, v174, v238, v246\n\t" \
    "v_fma_f32 v175, v175, v238, v247\n\t" \
    "v_mul_f32 v176, v168, v168\n\t" \
    "v_mul_f32 v177, v169, v169\n\t" \
    "v_mul_f32 v178, v170, v170\n\t" \
    "v_mul_f32 v179, v171, v171\n\t" \
    "v_mul_f32 v180, v172, v172\n\t" \
    "v_mul_f32 v181, v173, v173\n\t" \
    "v_mul_f32 v182, v174, v174\n\t" \
    "v_mul_f32 v183, v175, v175\n\t" \
    "v_fmamk_f32 v176, v176, 0xbdd2d3e7, v237\n\t" \
    "v_fmamk_f32 v177, v177, 0xbdd2d3e7, v237\n\t" \
    "v_fmamk_f32 v178, v178, 0xbdd2d3e7, v237\n\t" \
    "v_fmamk_f32 v179, v179, 0xbdd2d3e7, v237\n\t" \
    "v_fmamk_f32 v180, v180, 0xbdd2d3e7, v237\n\t" \
    "v_fmamk_f32 v181, v181, 0xbdd2d3e7, v237\n\t" \
    "v_fmamk_f32 v182, v182, 0xbdd2d3e7, v237\n\t" \
    "v_fmamk_f32 v183, v183, 0xbdd2d3e7, v237\n\t" \
    "v_mul_f32 v176, v168, v176\n\t" \
    "v_mul_f32 v177, v169, v177\n\t" \
    "v_mul_f32 v178, v170, v178\n\t" \
    "v_mul_f32 v179, v171, v179\n\t" \
    "v_mul_f32 v180, v172, v180\n\t" \
    "v_mul_f32 v181, v173, v181\n\t" \
    "v_mul_f32 v182, v174, v182\n\t" \
    "v_mul_f32 v183, v175, v183\n\t" \
    "v_exp_f32 v176, v176\n\t" \
    "v_exp_f32 v177, v177\n\t" \
    "v_exp_f32 v178, v178\n\t" \
    "v_exp_f32 v179, v179\n\t" \
    "v_exp_f32 v180, v180\n\t" \
    "v_exp_f32 v181, v181\n\t" \
    "v_exp_f32 v182, v182\n\t" \
    "v_exp_f32 v183, v183\n\t" \
    "v_add_f32 v176, 1.0, v176\n\t" \
    "v_add_f32 v177, 1.0, v177\n\t" \
    "v_add_f32 v178, 1.0, v178\n\t" \
    "v_add_f32 v179, 1.0, v179\n\t" \
    "v_add_f32 v180, 1.0, v180\n\t" \
    "v_add_f32 v181, 1.0, v181\n\t" \
    "v_add_f32 v182, 1.0, v182\n\t" \
    "v_add_f32 v183, 1.0, v183\n\t" \
    "v_rcp_f32 v176, v176\n\t" \
    "v_rcp_f32 v177, v177\n\t" \
    "v_rcp_f32 v178, v178\n\t" \
    "v_rcp_f32 v179, v179\n\t" \
    "v_rcp_f32 v180, v180\n\t" \
    "v_rcp_f32 v181, v181\n\t" \
    "v_rcp_f32 v182, v182\n\t" \
    "v_rcp_f32 v183, v183\n\t" \
    "v_mul_f32 v168, v168, v176\n\t" \
    "v_mul_f32 v169, v169, v177\n\t" \
    "v_mul_f32 v170, v170, v178\n\t" \
    "v_mul_f32 v171, v171, v179\n\t" \
    "v_mul_f32 v172, v172, v180\n\t" \
    "v_mul_f32 v173, v173, v181\n\t" \
    "v_mul_f32 v174, v174, v182\n\t" \
    "v_mul_f32 v175, v175, v183\n\t" \
    "s_waitcnt vmcnt(0)\n\t" \
    "v_cvt_pk_bf16_f32 v184, v168, v169\n\t" \
    "v_cvt_pk_bf16_f32 v185, v170, v171\n\t" \
    "v_cvt_pk_bf16_f32 v186, v172, v173\n\t" \
    "v_cvt_pk_bf16_f32 v187, v174, v175\n\t" \
    "v_min_u32 v184, -2, v184\n\t" \
    "v_min_u32 v185, -2, v185\n\t" \
    "v_min_u32 v186, -2, v186\n\t" \
    "v_min_u32 v187, -2, v187\n\t" \
    "s_nop 0\n\t" \
    "v_permlane16_swap_b32 v184, v186\n\t" \
    "v_permlane16_swap_b32 v185, v187\n\t" \
    "buffer_store_dwordx4 v[184:187], v254, %[rs], s49 offen sc1\n\t" \
    LST4 \
    LJIT \
    "s_cmp_ge_u32 s40, 122\n\t" \
    "s_cbranch_scc1 .Lw320_%=\n\t" \
    "s_add_i32 s44, s40, 6\n\t" \
    "s_max_i32 s44, s44, 0\n\t" \
    "s_lshr_b32 s45, s44, 1\n\t" \
    "s_and_b32 s44, s44, 1\n\t" \
    "s_lshl_b32 s44, s44, 12\n\t" \
    "s_mul_i32 s50, s45, 0x30000\n\t" \
    "s_add_u32 s50, s50, s44\n\t" \
    "s_lshr_b32 s45, s45, 3\n\t" \
    "s_mul_i32 s45, s45, 0x82000\n\t" \
    "s_add_u32 s50, s50, s45\n\t" \
    "s_add_u32 s50, s50, %[ng]\n\t" \
    "v_mov_b32 v188, -1\n\t" \
    "v_mov_b32 v189, -1\n\t" \
    "v_mov_b32 v190, -1\n\t" \
    "v_mov_b32 v191, -1\n\t" \
    "buffer_store_dwordx4 v[188:191], v253, %[rs], s50 offen sc1\n\t" \
    ".Lw320_%=:\n\t" \
    "v_xor_b32 v249, 0x8000, v249\n\t" \
    "v_xor_b32 v250, 0x8000, v250\n\t" \
    "v_xor_b32 v251, 0x800, v251\n\t" \
    "v_xor_b32 v252, 0x800, v252\n\t" \
    LFLUSHW \
    "s_add_u32 s40, s40, 1\n\t" \
    "s_cmp_lg_u32 s40, 128\n\t" \
    "s_cbranch_scc1 .Lw34_%=\n\t" \
    "s_branch .Lend%=\n\t" \
    ".Lend%=:\n\t" \
    "s_waitcnt vmcnt(0) lgkmcnt(0)\n\t" \
    "s_mov_b32 %[bud], s41\n\t"
using u32x8 = __attribute__((ext_vector_type(8))) unsigned;
using u32x16 = __attribute__((ext_vector_type(16))) unsigned;
using u32x32 = __attribute__((ext_vector_type(32))) unsigned;
__device__ __forceinline__ u32x32 cat32(u32x8 a, u32x8 b, u32x8 c, u32x8 d) {
    const u32x16 lo = __builtin_shufflevector(a, b, 0, 1, 2, 3, 4, 5, 6, 7, 8, 9, 10, 11, 12, 13, 14, 15);
    const u32x16 hi = __builtin_shufflevector(c, d, 0, 1, 2, 3, 4, 5, 6, 7, 8, 9, 10, 11, 12, 13, 14, 15);
    return __builtin_shufflevector(lo, hi, 0, 1, 2, 3, 4, 5, 6, 7, 8, 9, 10, 11, 12, 13, 14, 15, 16, 17, 18, 19, 20, 21, 22, 23, 24, 25, 26, 27, 28, 29, 30, 31);
}
__device__ __forceinline__ unsigned c1_dst(int wave, int kind) {
    const int ln = lane_id(), c = ln & 15, g = ln >> 4, hf = (wave >> 1) & 1;
    if (kind == 2) {
        const unsigned b = L_KM + (c >> 2) * 256 + (wave & 1) * 128 + g * 32;
        return (b + hf * 8 + (c & 3) * 2) | (b + 1024 + hf * 1024 + (c & 3) * 4) << 16;
    }
    const unsigned b = L_Q + (c >> 3) * 256 + g * 64 + ((c >> 2) & 1) * 8;
    return (b + (wave & 1) * (kind ? 512 : 1536) + ((c >> 1) & 1) * 32 + hf * 4 + (c & 1) * 2) | (b + 1024 + (wave & 3) * 512 + (c & 1) * 32 + ((c >> 1) & 1) * 4) << 16;
}
__device__ __forceinline__ void dma1(rsrc_t rs, unsigned vo, unsigned so, unsigned dst) {
    asm volatile("s_mov_b32 m0, %3\n\ts_nop 0\n\tbuffer_load_dwordx4 %0, %1, %2 offen nt lds" :: "v"(vo), "s"(rs), "s"(so), "s"(dst) : "memory");
}
using s16x4 = __attribute__((ext_vector_type(4))) short;
__device__ __forceinline__ u32x4 tr_frag(unsigned a, unsigned step) {
    const u32x2 x = __builtin_bit_cast(u32x2, __builtin_amdgcn_ds_read_tr16_b64_v4i16((LAS s16x4*)(size_t)a)), y = __builtin_bit_cast(u32x2, __builtin_amdgcn_ds_read_tr16_b64_v4i16((LAS s16x4*)(size_t)(a + step)));
    return (u32x4){x[0], x[1], y[0], y[1]};
}
#define LDELAY
#define LOOP_CLOB "memory", "vcc", "scc", "v0", "v1", "v2", "v3", "v4", "v5", "v6", "v7", "v8", "v9", "v10", "v11", "v12", "v13", "v14", "v15", "v16", "v17", "v18", "v19", "v20", "v21", "v22", "v23", "v24", "v25", "v26", "v27", "v28", "v29", "v30", "v31", "v32", "v33", "v34", "v35", "v36", "v37", "v38", "v39", "v40", "v41", "v42", "v43", "v44", "v45", "v46", "v47", "v48", "v49", "v50", "v51", "v52", "v53", "v54", "v55", "v56", "v57", "v58", "v59", "v60", "v61", "v62", "v63", "v64", "v65", "v66", "v67", "v68", "v69", "v70", "v71", "v72", "v73", "v74", "v75", "v76", "v77", "v78", "v79", "v80", "v81", "v82", "v83", "v84", "v85", "v86", "v87", "v88", "v89", "v90", "v91", "v92", "v93", "v94", "v95", "v96", "v97", "v98", "v99", "v100", "v101", "v102", "v103", "v104", "v105", "v106", "v107", "v108", "v109", "v110", "v111", "v112", "v113", "v114", "v115", "v116", "v117", "v118", "v119", "v120", "v121", "v122", "v123", "v124", "v125", "v126", "v127", "v128", "v129", "v130", "v131", "v132", "v133", "v134", "v135", "v136", "v137", "v138", "v139", "v140", "v141", "v142", "v143", "v144", "v145", "v146", "v147", "v148", "v149", "v150", "v151", "v152", "v153", "v154", "v155", "v156", "v157", "v158", "v159", "v160", "v161", "v162", "v163", "v164", "v165", "v166", "v167", "v168", "v169", "v170", "v171", "v172", "v173", "v174", "v175", "v176", "v177", "v178", "v179", "v180", "v181", "v182", "v183", "v184", "v185", "v186", "v187", "v188", "v189", "v190", "v191", "v192", "v193", "v194", "v195", "v196", "v197", "v198", "v199", "v200", "v201", "v202", "v203", "v204", "v205", "v206", "v207", "v208", "v209", "v210", "v211", "v212", "v213", "v214", "v215", "v216", "v217", "v218", "v219", "v220", "v221", "v222", "v223", "v224", "v225", "v226", "v227", "v228", "v229", "v230", "v231", "v232", "v233", "v234", "v235", "v236", "v237", "v238", "v239", "v246", "v247", "v248", "v249", "v250", "v251", "v252", "s40", "s41", "s42", "s43", "s44", "s45", "s46", "s47", "s48", "s49", "s50", "s51", "s52", "s53", "s54", "s55", "s56", "s57", "s58", "s59", "s60", "s61", "s62", "s63", "s64", "s65", "s66", "s67", "s68", "s69", "s70", "s71", "s72", "s73", "s74", "s75", "s76", "s77", "s78", "s79"
__device__ __forceinline__ unsigned UNI(unsigned x) {
    unsigned r;
    asm volatile("s_nop 0\n\tv_readfirstlane_b32 %0, %1\n\ts_nop 4" : "=s"(r) : "v"(x));
    return r;
}
__device__ __forceinline__ void note_timeout(const Params& P, unsigned bud) {
    if (bud == 0) __builtin_amdgcn_raw_buffer_store_b32(1u, mkrsrc(P.ws + FL0, 512), 0x100, 0, 16);
}

#define LOOP_IO [bud] "=s"(bud), "+{v253}"(l16), "+{v240}"(a[0]), "+{v241}"(a[1]), "+{v242}"(a[2]), "+{v243}"(a[3]), "+{v244}"(a[4]), "+{v245}"(a[5])
#define LOOP_IN "{v254}"(b2), "{v255}"(dsto), [wave] "s"(wave), [ws] "s"(P.ws), [mo] "s"(FL_MB + blockIdx.x * 128 + wave * 4), [sz] "s"(RG_L), [lx] "s"(L << 13), [wr] "s"(wr), [wm] "s"(wm), [fl] "s"(FL_XN + cpy * 2048), \
                [cj] "s"(REGS - 75 * WIN), [cs] "s"(5 * WIN), [mb] "s"(RG_XM + cpy * XM_COPY)
__device__ __forceinline__ void qkv_addr(unsigned (&a)[6], int w4, int ln, unsigned base) {
#pragma unroll
    for (int k = 0; k < 3; ++k) { const unsigned u = (w4 * 3 + k) * 64 + ln; a[k] = (u / 24) * (HD * 2) + (u % 24) * 16 - k * 1024; }
    a[3] = base + ((ln >> 4) * 8 + ((ln >> 2) & 3)) * 384 + (ln & 3) * 8 + w4 * 96;
    a[4] = a[5] = 0;
}
__device__ __forceinline__ void s_role(const Params& P, int wave, int L, int h, int cpy) {
    const int ln = lane_id();
    unsigned l16 = ln * 16, a[6];
    qkv_addr(a, wave & 3, ln, (wave >> 2) * 12288);
    const rsrc_t wr = mkrsrc((wave < 4 ? P.wq : P.wk) + h * DH, (DM * HD - h * DH) * 2);
    const unsigned wm = UNI(wave * 3072);
    const int mt = (wave >> 1) & 1, hf = wave & 1;
    const unsigned xs = D_XN + mt * 4096 + hf * WIN, x1 = (2 - hf) * WIN + hf * 2048, m0 = L_X + mt * 20480 + hf * 4096, m1 = L_X + mt * 20480 + 8192 + hf * 2048;
    const float SCL = 0.07216878364870322f * 1.4426950408889634f;
    const unsigned px = RG_PX + (h >> 2) * WIN;
    const unsigned pb = wave == 0 ? RG_XP + (h / 12) * WIN + (h % 12) * 640 - L_Q : px + (wave == 2 ? (h & 3) * 1024 : 5120 + ((h >> 1) & 1) * 1024 + (h & 1) * 256);
    unsigned b2 = l16 + (wave < 3 ? L_Q : wave == 3 ? L_QX : L_W + wave * 10240), dsto = c1_dst(wave, 1 + (wave >> 2)), bud;
    asm volatile(ASM_S_LOOP : LOOP_IO
                 : LOOP_IN, [xs] "s"(xs), [x1] "s"(x1), [m0] "s"(m0), [m1] "s"(m1), [qs] "s"(UNI(L_KX + hf * 1024 + mt * 8)), [q2] "s"(UNI(L_KX + 2048 + (wave & 3) * 1024)), [pb] "s"(UNI(pb)),
                   [pm] "s"(UNI(px + (wave == 2 ? 4096 : 7168) + (h & 3) * 256)), [scl] "s"(__float_as_uint(SCL)) LST_IN
                 : LOOP_CLOB);
    note_timeout(P, bud);
}

__device__ __forceinline__ void v_role(const Params& P, int wave, int L, int h, int cpy) {
    const int ln = lane_id(), w4 = wave & 3;
    unsigned l16 = ln * 16, a[6];
    if (wave < 4) qkv_addr(a, w4, ln, 0);
    else {
#pragma unroll
        for (int k = 0; k < 5; ++k) { const unsigned u = k * 64 + ln; a[k] = (u / 20) * (DM * 2) + (u % 20) * 16 - (k & 3) * 1024; }
        a[5] = L_W + w4 * 20480 + (ln >> 2) * 320 + (ln & 3) * 8;
    }
    const unsigned wo = wave < 4 ? h * DH : h * DH * DM + w4 * 160;
    const rsrc_t wr = mkrsrc((wave < 4 ? P.wv : P.wo) + wo, (DM * HD - wo) * 2);
    const unsigned wm = UNI(wave < 4 ? wave * 3072 : L_W + w4 * 20480);
    unsigned b2 = l16 + (wave < 3 ? L_Q : wave == 3 ? L_QX : L_W + wave * 10240), dsto = wave < 4 ? c1_dst(wave, 0) : (l16 >> 8) * WIN + (l16 & 255), bud;
    if (wave >= 4) l16 += L_Q;
    asm volatile(ASM_V_LOOP : LOOP_IO
                 : LOOP_IN, [xs] "s"(D_XN + w4 * WIN), [vm] "s"(VXD - 1), [vx] "s"(vxb(w4, 0) + (h >> 3) * WIN + (h & 7) * 1024 - L_Q),
                   [fo] "s"(FL_VO + (w4 >> 1) * WIN + ((w4 & 1) * 32 + h) * 128 - L_Q), [vo] "s"(RG_VM + w4 * 10 * WIN + (h >> 2) * 1024 + (h & 3) * 256) LST_IN
                 : LOOP_CLOB);
    note_timeout(P, bud);
}

#define D_W0 4
#define D_A1 8
#define D_R1 9
#define D_W1 12
__device__ __forceinline__ void delay(int n) { for (int i = 0; i < n; ++i) __builtin_amdgcn_s_sleep(64); }
__device__ __forceinline__ unsigned handshake(rsrc_t rw, unsigned mo) {
    wait_vm<0>();
    unsigned v = SENT;
    for (int spins = 0; spins < SPIN_CAP && v == SENT; ++spins) {
        __builtin_amdgcn_s_sleep(4);
        v = __builtin_amdgcn_readfirstlane(__builtin_amdgcn_raw_buffer_load_b32(rw, 0, mo, 16));
    }
    __builtin_amdgcn_raw_buffer_store_b32(1u, rw, lane_id() ? OOB : 32768u, mo, 16);
    return (v >> 13) & 1;
}
template <class F>
__device__ __forceinline__ void x_tile(const Params& P, int tile, F target) {
    const int ln = lane_id(), l15 = ln & 15, l4 = ln >> 4;
    u32x4 v[20];
    float s = 0.f, q = 0.f;
#pragma unroll
    for (int c = 0; c < 20; ++c) v[c] = *(const u32x4*)(P.x + (size_t)(tile * 16 + l15) * DM + c * 32 + l4 * 8);
#pragma unroll
    for (int c = 0; c < 20; ++c)
#pragma unroll
        for (int e = 0; e < 4; ++e) s += lo2f(v[c][e]) + hi2f(v[c][e]);
    sum2_rows(s, q);
#pragma unroll
    for (int c = 0; c < 20; ++c) asm volatile("" : "+v"(v[c]));
    const float mean = s * (1.0f / DM);
#pragma unroll
    for (int c = 0; c < 20; ++c)
#pragma unroll
        for (int e = 0; e < 4; ++e) { const float a = lo2f(v[c][e]) - mean, b = hi2f(v[c][e]) - mean; q += a * a + b * b; }
    sum2_rows(s, q);
#pragma unroll
    for (int c = 0; c < 20; ++c) asm volatile("" : "+v"(v[c]));
    const float m1 = rbf(mean), r = 1.0f / ln_den(q * (1.0f / DM));
    const unsigned sb = segb(tile >> 1) + (tile & 1) * 4096;
    u32x4 n[20];
#pragma unroll
    for (int c = 0; c < 20; ++c) {
#pragma unroll
        for (int e = 0; e < 4; ++e) { const unsigned d = pack2(lo2f(v[c][e]) - m1, hi2f(v[c][e]) - m1); n[c][e] = pack2(lo2f(d) * r, hi2f(d) * r); }
        asm volatile("" : "+v"(n[c]));
    }
    const rsrc_t rr = target();
#pragma unroll
    for (int c = 0; c < 20; ++c) {
        st128(n[c], rr, ln * 16 + (c & 3) * 1024, D_XN + sb + (c >> 2) * WIN);
        st128(v[c], rr, ln * 16 + (c & 3) * 1024, D_XR + sb + (c >> 2) * WIN);
    }
    wait_vm<0>();
}
__device__ __forceinline__ void wait_go(const Params& P, rsrc_t rd, int k) {
    for (int spins = 0;; ++spins) {
        if (__builtin_amdgcn_readfirstlane(__builtin_amdgcn_raw_buffer_load_b32(rd, 0, FL_VO + k * 128, 16))) break;
        __builtin_amdgcn_s_sleep(16);
        if (spin_fail(spins * 4 + 3, P)) break;
    }
    asm volatile("" ::: "memory");
}

constexpr unsigned R_RED = 0;
constexpr unsigned R_PART = 8192;
__device__ __forceinline__ void r_role(const Params& P, int wave, int L, int j, int cpy) {
    const int lane = lane_id(), l15 = lane & 15, l4 = lane >> 4;
    const unsigned l16 = lane * 16;
    const rsrc_t rr = mkrsrc(lbase(P, L), RG_L), rd = rr;
    const int sidx = (lane & 2) ? 0xEEEE : 0x4444;
    unsigned offp[3];
    auto hq = [&](int q) { return RG_XP + cpy * XP_COPY + (q / 3) * WIN + (q % 3) * 2560; };
#define ROT 1
    const int hg = (wave + ROT * j) & 7;
    offp[0] = hq(hg) + (lane >> 5) * 640 + 128 + (lane & 31) * 16;
    offp[1] = offp[0] + 1280;
    offp[2] = (lane & 2) ? OOB : hq(hg) + l4 * 640 + ((l15 >> 2) * 2 + (l15 & 1)) * 16;
    const int jq = j / 10, j10 = j % 10;
    const unsigned ae = wave < 3 ? ((j10 < 8 && wave * 1024 + l16 < 2560) ? hq(j10) + (jq - cpy) * XP_COPY + wave * 1024 + l16 : OOB)
                      : wave == 3 ? RG_XM + jq * XM_COPY + (j10 >> 3) * WIN + (j10 & 7) * 1024 + l16
                      : wave < 6 ? (j < 32 ? RG_PX + (j >> 2) * WIN + (j & 3) * 2048 + (wave & 1) * 1024 + l16 : OOB)
                      : wave == 7 ? D_XA + (j >> 3) * WIN + (j & 7) * 512 + (lane >> 5) * 4096 + (lane & 31) * 16 : OOB;
    const unsigned as = wave < 3 ? XP_SLOT : PX_SLOT;
    static_assert(PX_SLOT == XM_SLOT && AH == 3 && VXD == 4, "generated loop");
    const unsigned offv = RG_VM + j * WIN + hg * 1024 + l16;
    const unsigned offo = (wave < (int)NCP) ? RG_XM + wave * XM_COPY + (j >> 5) * WIN + (j & 31) * 256 + lane * 4 : OOB;
    {
        const u32x4 s4 = sent4();
#pragma unroll
        for (int k = 0; k < (int)NSLOT; ++k) {
            st128(s4, rr, offv, k * VM_SLOT);
            if (wave < 6 && (k || wave != 3)) st128(s4, rr, ae, k * as);
        }
        for (int u = j; u < 126; u += 40) st128(s4, rr, wave * 1024 + l16, u < 108 ? D_XA + (u < 60 ? u / 20 : 3) * 4 * REG + (u < 60 ? u % 20 : u - 60) * WIN : u < 124 ? RG_ST + (u - 108) * WIN : D_SS + (u - 124) * WIN);
        wait_vm<0>();
        bar();
        const int row = l4 * 2 + ((lane >> 2) & 1), col = 16 * j + 4 * (lane & 3) + 2 * ((lane >> 3) & 1);
        __builtin_amdgcn_raw_buffer_store_b32(nosent(*(const unsigned*)(P.mem0 + row * DM + col)), rr, offo, 0, 16);
    }
    auto red_addr = [](int fl) { return R_RED + fl * 80 + (fl >> 4) * 16; };
    const unsigned rw0 = red_addr(l4 * 16 + ((l15 >> 1) & 1) * 8 + (l15 >> 2)) + (l15 & 1) * 4 + (hg ^ (l15 >> 3)) * 8;
    const unsigned rrd = red_addr(lane);
    unsigned bud;
    asm volatile(ASM_R_LOOP : [bud] "=s"(bud)
                 : [wave] "s"(wave), [rs] "s"(rr), [l16] "v"(l16), [ov] "v"(offv), [o0] "v"(offp[0]), [o1] "v"(offp[1]), [o2] "v"(offp[2]), [oo] "v"(offo), [sidx] "v"(sidx), [rw] "v"(rw0), [rd] "v"(rrd),
                   [xo] "v"((l4 >> 1) * 256 + l15 * 16 + (l4 & 1) * 8), [pw] "v"(R_PART + hg * 1024 + l16), [pr] "v"(R_PART + (wave & 1) * 8192 + l16), [fo] "v"((lane & 3) * 128), [ae] "v"(ae),
                   [fs] "s"(UNI(FL_VO + (jq >> 1) * WIN + ((jq & 1) * 32 + hg * 4) * 128)), [vx] "s"(UNI(vxb(jq, 0) + (j10 * 4 + (hg >> 1)) * WIN + (hg & 1) * 4096)), [px] "s"(UNI(RG_PX + hg * WIN)),
                   [xr] "s"(UNI(D_XR + (j >> 3) * WIN + (wave & 1) * 4096 + (j & 7) * 512)), [ad] "s"(UNI(wave < 3 ? -1 : wave == 3 ? 0 : -2)), [as] "s"(UNI(as)) LST_IN
                 : "memory", "vcc", "scc", "v128", "v129", "v130", "v131", "v132", "v133", "v134", "v135", "v136", "v137", "v138", "v139", "v140", "v141", "v142", "v143", "v144", "v145", "v146", "v147", "v148", "v149", "v150", "v151", "v152", "v153", "v154", "v155", "v156", "v157", "v158", "v159", "v160", "v161", "v162", "v163", "v164", "v165", "v166", "v167", "v168", "v169", "v170", "v171", "v172", "v173", "v174", "v175", "v176", "v177", "v178", "v179", "v180", "v181", "v182", "v183", "v184", "v185", "v186", "v187", "v188", "v189", "v190", "v191", "v192", "v193", "v194", "v195", "v196", "v197", "v198", "v199", "v200", "v201", "v202", "v203", "v204", "v205", "v206", "v207", "v208", "v209", "v210", "v211", "v212", "v213", "v214", "v215", "v216", "v217", "v218", "v219", "v220", "v221", "v222", "v223", "v224", "v225", "v226", "v227", "v228", "v229", "v230", "v231", "v232", "v233", "v234", "v235", "v236", "v237", "v238", "v239", "v240", "v241", "v242", "v243", "v244", "v245", "v246", "v247", "v248", "v249", "v250", "v251", "v252", "v253", "v254", "v255", "s40", "s41", "s42", "s43", "s44", "s45", "s46", "s47", "s48", "s49", "s50", "s51", "s52", "s53", "s54", "s55", "s64", "s65", "s66", "s67", "s68", "s69", "s70", "s71", "s72", "s73", "s74", "s75", "s76", "s77", "s78", "s79");
    note_timeout(P, bud);
}

template <int WHICH>
__device__ __forceinline__ void w_role(const Params& P, int wave, int L, int cu) {
    constexpr int NT = (WHICH == 3) ? 2 : 1, N = (WHICH == 3) ? FF : DM, RB = NT * 256, RPI = 1024 / RB;
    const int ln = lane_id(), l15 = ln & 15, l4 = ln >> 4;
    const rsrc_t rd = mkrsrc(lbase(P, L), RG_L);
    const int nt0 = (cu * 8 + wave) * NT;
    const rsrc_t wr = mkrsrc((WHICH == 1 ? P.w1 : WHICH == 2 ? P.w2 : P.w3) + cu * (RB / 2), DM * N * 2 - cu * RB);
    auto swz = [](int row) { return ((row & 3) | ((row >> 1) & 4)) * 32; };
    const int dr = ln * 16 / RB, tr = l4 * 8 + ((ln >> 2) & 3);
    unsigned vo = dr * (N * 2) + ((ln * 16 % RB) ^ swz(wave * RPI + dr)), ta = tr * RB + ((wave * NT * 32 + (ln & 3) * 8) ^ swz(tr));
    const int grp = nt0 / 10, neg = grp & 1, sh = (WHICH == 1) ? (neg ? -1 : 1) * ((grp >> 1) + 1) : 0;
    unsigned ra = ln * 16, wa = ln * 16 + wave * 1024, sr = WHICH == 2 ? l15 * 8 : 24576 + l15 * 80, sw = sr + wave * 8, bud;
    const unsigned oo = WHICH == 3 ? (l4 & 1) * 512 + (l4 >> 1) * 256 + l15 * 16 : (nt0 >> 3) * WIN + (nt0 & 7) * 512 + (l4 >> 1) * 256 + ((l15 + sh) & 15) * 16 + (l4 & 1) * 8;
    unsigned up = ((unsigned)(l15 + sh) > 15u) ^ neg;
#define W_ARGS : [bud] "=s"(bud), "+{v176}"(vo), "+{v177}"(ta), "+{v249}"(ra), "+{v250}"(wa), "+{v251}"(sr), "+{v252}"(sw) \
               , "+{v255}"(up) : "{v253}"(ln * 16), "{v254}"(oo), [wave] "s"(wave), [rs] "s"(rd), [wr] "s"(wr), [wm] "s"(UNI(wave * 1024)), [wo] "s"(UNI(wave * RPI * N * 2)), [go] "s"(FL_VO + (WHICH * 8 + cu) * 128), \
                 [pi] "s"(UNI((WHICH == 1 ? D_XA : WHICH == 2 ? D_HS : D_X1) + wave * 1024)), \
                 [ng] "s"(UNI(WHICH == 3 ? D_H2 + (nt0 >> 3) * WIN + ((nt0 >> 1) & 3) * 1024 : neg)), [ss] "s"(UNI(cu == 0)) LST_IN LST_INW \
               : "memory", "vcc", "scc", "v0", "v1", "v2", "v3", "v4", "v5", "v6", "v7", "v8", "v9", "v10", "v11", "v12", "v13", "v14", "v15", "v16", "v17", "v18", "v19", "v20", "v21", "v22", "v23", "v24", "v25", "v26", "v27", "v28", "v29", "v30", "v31", "v32", "v33", "v34", "v35", "v36", "v37", "v38", "v39", "v40", "v41", "v42", "v43", "v44", "v45", "v46", "v47", "v48", "v49", "v50", "v51", "v52", "v53", "v54", "v55", "v56", "v57", "v58", "v59", "v60", "v61", "v62", "v63", "v64", "v65", "v66", "v67", "v68", "v69", "v70", "v71", "v72", "v73", "v74", "v75", "v76", "v77", "v78", "v79", "v80", "v81", "v82", "v83", "v84", "v85", "v86", "v87", "v88", "v89", "v90", "v91", "v92", "v93", "v94", "v95", "v96", "v97", "v98", "v99", "v100", "v101", "v102", "v103", "v104", "v105", "v106", "v107", "v108", "v109", "v110", "v111", "v112", "v113", "v114", "v115", "v116", "v117", "v118", "v119", "v120", "v121", "v122", "v123", "v124", "v125", "v126", "v127", "v128", "v129", "v130", "v131", "v132", "v133", "v134", "v135", "v136", "v137", "v138", "v139", "v140", "v141", "v142", "v143", "v144", "v145", "v146", "v147", "v148", "v149", "v150", "v151", "v152", "v153", "v154", "v155", "v156", "v157", "v158", "v159", "v160", "v161", "v162", "v163", "v164", "v165", "v166", "v167", "v168", "v169", "v170", "v171", "v172", "v173", "v174", "v175", "v178", "v179", "v180", "v181", "v182", "v183", "v184", "v185", "v186", "v187", "v188", "v189", "v190", "v191", "v192", "v193", "v194", "v195", "v196", "v197", "v198", "v199", "v200", "v201", "v202", "v203", "v204", "v205", "v206", "v207", "v208", "v209", "v210", "v211", "v212", "v213", "v214", "v215", "v216", "v217", "v218", "v219", "v220", "v221", "v222", "v223", "v224", "v225", "v226", "v227", "v228", "v229", "v230", "v231", "v232", "v233", "v234", "v235", "v236", "v237", "v238", "v239", "v240", "v241", "v242", "v243", "v244", "v245", "v246", "v247", "v248", "s40", "s41", "s42", "s43", "s44", "s45", "s46", "s47", "s48", "s49", "s50", "s51", "s52", "s53", "s54", "s55", "s56", "s57", "s58", "s59", "s60", "s61", "s62", "s63", "s64", "s65", "s66", "s67", "s68", "s69", "s70", "s71", "s72", "s73", "s74", "s75", "s76", "s77", "s78", "s79"
    if (WHICH == 1) asm volatile(ASM_W1_LOOP W_ARGS);
    if (WHICH == 2) asm volatile(ASM_W2_LOOP W_ARGS);
    if (WHICH == 3) asm volatile(ASM_W3_LOOP W_ARGS);
    note_timeout(P, bud);
}

constexpr unsigned W4_FL = 122880;
__device__ __forceinline__ unsigned lds_vld(unsigned a) { asm volatile("" ::: "memory"); const unsigned v = *(volatile LAS unsigned*)(size_t)a; asm volatile("" ::: "memory"); return v; }
__device__ __forceinline__ void lds_vst(unsigned a, unsigned v) { asm volatile("" ::: "memory"); *(volatile LAS unsigned*)(size_t)a = v; asm volatile("" ::: "memory"); }
__device__ __forceinline__ void w4_role(const Params& P, int wave, int L, int cu) {
    const int ln = lane_id(), l15 = ln & 15, l4 = ln >> 4;
    const unsigned l16 = ln * 16;
    const rsrc_t rd = mkrsrc(lbase(P, L), RG_L);
    unsigned bud;
    if (wave >= 6) wait_go(P, rd, 32 + cu);
    if (wave < 6) {
        u32x32 w[5];
        {
            const rsrc_t wr = mkrsrc(P.w4 + wave * 256 * DM + cu * 80, (FF * DM - wave * 256 * DM - cu * 80) * 2);
            const unsigned ta = wave * 20480 + (l4 * 8 + ((ln >> 2) & 3)) * 160 + (ln & 3) * 8;
            u32x4 f[5][8];
#pragma unroll
            for (int r = 0; r < 2; ++r) {
#pragma unroll
                for (int i = 0; i < 5; ++i) {
                    const unsigned u = i * 64 + ln, vo = (u / 10) * (DM * 2) + (u % 10) * 16;
#pragma unroll
                    for (int k = 0; k < 4; ++k) dma1(wr, vo, (r * 128 + k * 32) * (DM * 2), wave * 20480 + k * 5120 + i * 1024);
                }
                wait_vm<0>();
#pragma unroll
                for (int k = 0; k < 4; ++k)
#pragma unroll
                    for (int n = 0; n < 5; ++n) f[n][r * 4 + k] = tr_frag(ta + k * 5120 + n * 32, 640);
                asm volatile("s_waitcnt lgkmcnt(0)" ::: "memory");
                SB();
            }
#pragma unroll
            for (int n = 0; n < 5; ++n) {
                u32x8 g[4];
#pragma unroll
                for (int i = 0; i < 4; ++i) g[i] = __builtin_shufflevector(f[n][2 * i], f[n][2 * i + 1], 0, 1, 2, 3, 4, 5, 6, 7);
                w[n] = cat32(g[0], g[1], g[2], g[3]);
            }
            asm volatile("" : "+{v[0:31]}"(w[0]), "+{v[32:63]}"(w[1]), "+{v[64:95]}"(w[2]), "+{v[96:127]}"(w[3]), "+{v[128:159]}"(w[4]));
        }
        wait_go(P, rd, 32 + cu);
        bar();
        unsigned ld = l16 + wave * 1024;
        asm volatile(ASM_W4_MUL : [bud] "=s"(bud), [ld] "+v"(ld)
                     : "{v[0:31]}"(w[0]), "{v[32:63]}"(w[1]), "{v[64:95]}"(w[2]), "{v[96:127]}"(w[3]), "{v[128:159]}"(w[4]), [l2] "v"(2 * ld + 61440), [l16] "v"(l16), [fa] "v"(W4_FL + wave * 4), [rs] "s"(rd),
                       [hb] "s"(UNI(D_H2 + wave * 2 * WIN)) LST_IN
                     : "memory", "vcc", "scc", "v160", "v161", "v162", "v163", "v164", "v165", "v166", "v167", "v168", "v169", "v170", "v171", "v172", "v173", "v174", "v175", "v176", "v177", "v178", "v179", "v180", "v181", "v182", "v183", "v184", "v185", "v186", "v187", "v188", "v189", "v190", "v191", "v192", "v193", "v194", "v195", "v196", "v197", "v198", "v199", "v200", "v201", "v202", "v203", "v204", "v205", "v206", "v207", "v208", "v209", "v210", "v211", "v212", "v213", "v214", "v215", "v216", "v217", "v218", "v219", "v220", "v221", "v222", "v223", "v224", "v225", "v226", "v227", "v228", "v229", "v230", "v231", "v232", "v233", "v234", "v235", "v236", "v237", "v238", "v239", "v240", "v241", "v242", "v243", "v244", "v245", "v246", "v247", "s40", "s41", "s42", "s43", "s44", "s45", "s46", "s47", "s48", "s49", "s50", "s51", "s52", "s53", "s54", "s55", "s64", "s65", "s66", "s67", "s68", "s69", "s70", "s71", "s72", "s73", "s74", "s75", "s76", "s77", "s78", "s79");
    } else {
        const int mt = wave - 6;
        if (ln < 8) lds_vst(W4_FL + ln * 4, 0u);
        bar();
        auto tl = [&](int n) { return (unsigned)((cu * 5 + n) >> 3) * WIN + ((cu * 5 + n) & 7) * 512 + mt * 4096; };
        const unsigned o8 = (l4 >> 1) * 256 + l15 * 16 + (l4 & 1) * 8;
        unsigned la = mt * 30720 + l16;
        static_assert(RG_ST == 112 * WIN && D_XN == 10 * REG && D_XR == 6 * REG, "generated loop");
#define F_IN [l2] "v"(2 * la + 61440), [fa] "v"(W4_FL + (ln < 6 ? ln : 0) * 4), [fc] "v"(W4_FL + 24 + mt * 4), [rs] "s"(rd), [mt] "s"(mt), \
             [t0] "v"(o8 + tl(0)), [t1] "v"(o8 + tl(1)), [t2] "v"(o8 + tl(2)), [t3] "v"(o8 + tl(3)), [t4] "v"(o8 + tl(4))
#define F_CLOB "memory", "vcc", "scc", "v64", "v65", "v66", "v67", "v68", "v69", "v70", "v71", "v72", "v73", "v74", "v75", "v76", "v77", "v78", "v79", "v80", "v81", "v82", "v83", "v84", "v85", "v86", "v87", "v88", "v89", "v90", "v91", "v92", "v93", "v94", "v95", "v96", "v97", "v98", "v99", "v100", "v101", "v102", "v103", "v104", "v105", "v106", "v107", "v108", "v109", "v110", "v111", "v112", "v113", "v114", "v115", "v116", "v117", "v118", "v119", "v120", "v121", "v122", "v123", "v124", "v125", "v126", "v127", "v128", "v129", "v130", "v131", "v132", "v133", "v134", "v135", "v136", "v137", "v138", "v139", "v140", "v141", "v142", "v143", "v144", "v145", "v146", "v147", "v148", "v149", "v150", "v151", "v152", "v153", "v154", "v155", "v156", "v157", "v158", "v159", "v160", "v161", "v162", "v163", "v164", "v165", "v166", "v167", "v168", "v169", "v170", "v171", "v172", "v173", "v174", "v175", "v176", "v177", "v178", "v179", "v180", "v181", "v182", "v183", "v184", "v185", "v186", "v187", "v188", "v189", "v190", "v191", "v192", "v193", "v194", "v195", "v196", "v197", "v198", "v199", "v200", "v201", "v202", "v203", "v204", "v205", "v206", "v207", "v208", "v209", "v210", "v211", "v212", "v213", "v214", "v215", "v216", "v217", "v218", "v219", "v220", "v221", "v222", "v223", "v224", "v225", "v226", "v227", "v228", "v229", "v230", "v231", "v232", "v233", "v234", "v235", "v236", "v237", "v238", "v239", "s40", "s41", "s42", "s43", "s44", "s45", "s46", "s47", "s48", "s49", "s50", "s51", "s52", "s53", "s54", "s55", "s64", "s65", "s66", "s67", "s68", "s69", "s70", "s71", "s72", "s73", "s74", "s75", "s76", "s77", "s78", "s79"
        if (L) asm volatile(ASM_W4_FIN1 : [bud] "=s"(bud), [la] "+v"(la)
                            : F_IN, [ro] "s"(mkrsrc(P.out, ACT)), [co] "s"(cu * 160), [q8] "v"(l15 * (DM * 2) + l4 * 8), [q16] "v"(l15 * (DM * 2) + (l4 & 1) * 32 + (l4 >> 1) * 16) LST_IN : F_CLOB);
        else asm volatile(ASM_W4_FIN0 : [bud] "=s"(bud), [la] "+v"(la)
                          : F_IN, [rn] "s"(mkrsrc(lbase(P, 1), RG_L)), [co] "s"(cu * 128), [l8] "v"(ln * 8), [o16] "v"(tl(l4 & 1) + (l4 >> 1) * 256 + l15 * 16), [o17] "v"(tl(2 + (l4 & 1)) + (l4 >> 1) * 256 + l15 * 16),
                            [fo] "v"(ln < 32 ? (ln >> 3) * 2048 + (ln & 7) * 16 : OOB), [fs] "s"(FL_XN + (cu * 2 + mt) * 128) LST_IN : F_CLOB);
    }
    note_timeout(P, bud);
}

constexpr int LEADER = 252;
__device__ __forceinline__ unsigned leader(const Params& P, rsrc_t rw, int wave) {
    const int ln = lane_id();
    if (wave == 0) {
        const unsigned o = ln ? OOB : 0u;
        __builtin_amdgcn_raw_buffer_store_b32(0u, rw, o, CAL0, 16);
        __builtin_amdgcn_raw_buffer_store_b32(0u, rw, o, CAL0 + 8192, 16);
        unsigned m[2] = {~0u, ~0u};
#pragma unroll
        for (int r = 0; r < CALN; ++r) {
            unsigned long long a, c;
            asm volatile("s_waitcnt vmcnt(0) lgkmcnt(0)\n\ts_memtime %0\n\ts_waitcnt lgkmcnt(0)\n\tbuffer_store_dword %2, %2, %3, %4 offen sc1\n\ts_waitcnt vmcnt(0)\n\ts_memtime %1\n\ts_waitcnt lgkmcnt(0)"
                         : "=&s"(a), "=&s"(c) : "v"(o), "s"(rw), "s"(CAL0 + (r & 1) * 8192) : "memory");
            const unsigned d = (unsigned)(c - a);
            m[r & 1] = d < m[r & 1] ? d : m[r & 1];
        }
        lds_vst(0, m[0] < m[1] ? 1u : 0u);
    }
    bar();
    const unsigned par = __builtin_amdgcn_readfirstlane(lds_vld(0));
    const u32x4 s4 = sent4(), z4 = s4 ^ s4;
    for (int k = 0; k < 6; ++k) st128(z4, rw, ln * 16, FL_VO + (k * 8 + wave) * 1024);
    for (int k = 0; k < 10; ++k) st128(s4, rw, ln * 16, RG_XM + (wave >> 1) * XM_COPY + (wave & 1) * 8192 + (k >> 3) * WIN + (k & 7) * 1024);
    const unsigned mo = (wave * 32 + (ln >> 1)) * 128 + (ln & 1) * 16;
    st128(z4, rw, mo, FL_MB + 32768);
    if (wave == 0) st128(z4, rw, ln ? OOB : 256u, FL0);
    wait_vm<0>();
    bar();
    bool open = wave * 32 + (ln >> 1) != LEADER;
    for (int spins = 0; spins < SPIN_CAP; ++spins) {
        st128(s4 & (par << 13), rw, open ? mo : OOB, FL_MB);
        __builtin_amdgcn_s_sleep(20);
        const u32x4 d = LD128(rw, mo, FL_MB + 32768);
        open = open && !(d[0] && d[1] && d[2] && d[3]);
        if (!__any(open)) break;
    }
    bar();
    return par;
}
__global__ __launch_bounds__(512, 2) void mega_kernel(Params P) {
    const int wg = blockIdx.x;
    const int wave = __builtin_amdgcn_readfirstlane(threadIdx.x >> 6);
    const int L = (wg >> 2) & 1, x = wg & 3, r = wg >> 3, cpy = x;
    const int nr = (x & 1) ? 10 + RK : 10 - RK, j = (x >> 1) * 20 + (x & 1) * (10 - RK) + r - 16, i0 = (x >> 1) * 12 + (x & 1) * (6 + RK) + r - 16 - nr;
    const int i = (i0 * 5) % 24;
    const rsrc_t rw = mkrsrc(P.ws, RG_L);
    const bool lead = wg == LEADER;
    CJIT(30);
    if (!lead) __builtin_amdgcn_raw_buffer_store_b32(SENT, rw, lane_id() ? OOB : 0u, FL_MB + wg * 128 + wave * 4, 16);
    if (L && r < 16) delay(D_A1);
#define FIN() __builtin_amdgcn_endpgm()
#define HM 0
    const int r7 = r & 7;
    const int hs = HM == 1 ? r7 * 4 + x : HM == 3 ? (r7 >> 1) * 8 + x * 2 + (r7 & 1) : HM == 4 ? (r7 >> 2) * 16 + x * 4 + (r7 & 3) : x * 8 + r7;
    const int hv = HM == 2 ? (hs + 4) & 31 : HM == 5 ? (hs + 12) & 31 : hs;
    if (r < 8) { s_role(P, wave, L, hs, cpy); FIN(); }
    if (r < 16) { v_role(P, wave, L, hv, cpy); FIN(); }
    if (lead) P.par = leader(P, rw, wave);
    else {
        const unsigned mo = FL_MB + wg * 128 + wave * 4;
        const bool ln = r - 16 < nr && j < 16, first = ln && !L && wave == 7;
        if (!first) { P.par = handshake(rw, mo); if (L || r - 16 >= nr) delay(!L ? D_W0 : r - 16 < nr ? D_R1 : D_W1); }
        if (first || (ln && L && wave)) x_tile(P, (L ? wave * 16 : 0) + j, [&]() { if (first) P.par = handshake(rw, mo); return mkrsrc(lbase(P, 0), RG_L); });
        if (ln && L) bar();
        if (first || (ln && L && !wave)) {
            const rsrc_t r0 = mkrsrc(lbase(P, 0), RG_L);
            const int lane = lane_id();
            const unsigned fo = lane < 32 ? (lane >> 3) * 2048 + (lane & 7) * 16 : OOB;
            if (L) for (int spins = 0; spins < SPIN_CAP && __any(lane < 32 && LD128(r0, fo, FL_XN + j * 128)[0] == 0u); ++spins) __builtin_amdgcn_s_sleep(8);
            st128(sent4() & (L ? 64u : 8u), r0, fo, FL_XN + j * 128);
        }
    }
    CJIT(30);
    if (r - 16 < nr) { r_role(P, wave, L, j, cpy); FIN(); }
    if (i < 5) { w_role<1>(P, wave, L, i); FIN(); }
    if (i < 10) { w_role<2>(P, wave, L, i - 5); FIN(); }
    if (i < 16) { w_role<3>(P, wave, L, i - 10); FIN(); }
    w4_role(P, wave, L, i - 16);
    FIN();
}

extern "C" void kernel_launch(void* const* d_in, const int* in_sizes, int n_in, void* d_out, int out_size, void* d_ws,
                              size_t ws_size, hipStream_t stream) {
    char* ws = (char*)d_ws + (-(size_t)d_ws & (REG - 1));
    Params P;
    P.par = 0; P.pad = 0; P.ws = ws; P.out = (bf16_t*)d_out;
    P.x = (const bf16_t*)d_in[0]; P.mem0 = (const bf16_t*)d_in[1]; P.wq = (const bf16_t*)d_in[2]; P.wk = (const bf16_t*)d_in[3]; P.wv = (const bf16_t*)d_in[4]; P.wo = (const bf16_t*)d_in[5];
    P.w1 = (const bf16_t*)d_in[8]; P.w2 = (const bf16_t*)d_in[9]; P.w3 = (const bf16_t*)d_in[10]; P.w4 = (const bf16_t*)d_in[11];
    (void)hipFuncSetAttribute((const void*)mega_kernel, hipFuncAttributeMaxDynamicSharedMemorySize, MEGA_LDS);

    mega_kernel<<<256, 512, MEGA_LDS, stream>>>(P);
}
```

```cpp
#include <hip/hip_runtime.h>
#include <stdint.h>

typedef unsigned short bf16_t;
using bf16x2 = __attribute__((ext_vector_type(2))) __bf16;
using f32x2 = __attribute__((ext_vector_type(2))) float;
using u32x2 = __attribute__((ext_vector_type(2))) unsigned;
using u32x4 = __attribute__((ext_vector_type(4))) unsigned;
typedef __amdgpu_buffer_rsrc_t rsrc_t;

__device__ __forceinline__ unsigned pack2(float a, float b) {
    bf16x2 r = __builtin_convertvector((f32x2){a, b}, bf16x2);
    return __builtin_bit_cast(unsigned, r);
}
__device__ __forceinline__ float rbf(float f) { return __uint_as_float(pack2(f, 0.f) << 16); }
__device__ __forceinline__ float lo2f(unsigned u) { return __uint_as_float(u << 16); }
__device__ __forceinline__ float hi2f(unsigned u) { return __uint_as_float(u & 0xFFFF0000u); }
__device__ __forceinline__ rsrc_t mkrsrc(const void* p, unsigned bytes) {
    return __builtin_amdgcn_make_buffer_rsrc((void*)p, 0, bytes, 0x00020000);
}

constexpr int S_ = 2048, DM = 640, NH = 32, DH = 192, HD = NH * DH  , SEG = 32, NSEG = 64;
constexpr int FF = 1536;

constexpr unsigned NSLOT = 4, NCP = 4, VXD = 4;
constexpr unsigned WIN = 16384, REG = 2u << 20, REGS = REG + 8192;
constexpr unsigned XP_COPY = 3 * WIN, XP_SLOT = NCP * XP_COPY;
constexpr unsigned XM_COPY = 2 * WIN, XM_SLOT = NCP * XM_COPY;
constexpr unsigned VM_SLOT = REGS;
constexpr unsigned PX_SLOT = 8 * WIN;
constexpr unsigned RG_XM = 0, RG_XP = RG_XM + NSLOT * XM_SLOT, RG_PX = RG_XP + NSLOT * XP_SLOT, RG_ST = RG_PX + NSLOT * PX_SLOT, RG_VM = 2 * REG;
static_assert(RG_ST + 16 * WIN <= REG, "ring region");
constexpr unsigned FL0 = REG, FL_PAR = REG + 4096, FL_MB = REG + 65536;
constexpr unsigned FL_VO = RG_VM + 48 * WIN, FL_XN = FL_VO + 2 * WIN, D_SS = FL_VO + 4 * WIN, CAL0 = RG_VM + 64 * WIN;
constexpr unsigned ACT = S_ * DM * 2;
constexpr unsigned D_XR = 6 * REG, D_XN = 10 * REG, D_XA = 14 * REG, D_HS = 18 * REG, D_X1 = 22 * REG, D_H2 = 26 * REG, D_VX = 34 * REG;
__host__ __device__ constexpr unsigned segb(unsigned seg) { return seg * (5 * WIN) + (seg >> 4) * (REGS - 80 * WIN); }
__host__ __device__ constexpr unsigned segh(unsigned seg) { return seg * (12 * WIN) + (seg >> 3) * (REGS - 96 * WIN); }
#define CALN 4
#define RK 0
__host__ __device__ constexpr unsigned vxb(unsigned q, unsigned t) { return D_VX + q * (VXD / 2 * REG) + (t & (VXD - 1)) * (REG / 2) + (t & 2) * 4096; }
constexpr int AH = 3;
constexpr unsigned RG_L = D_VX + 2 * VXD * REG;

#define SB() __builtin_amdgcn_sched_barrier(0)
#define LAS __attribute__((address_space(3)))
constexpr unsigned OOB = 0x7FFFFF00u;
template <int N> __device__ __forceinline__ void wait_vm() { asm volatile("s_waitcnt vmcnt(%0)" :: "i"(N) : "memory"); }
__device__ __forceinline__ void bar() {
    asm volatile("s_waitcnt lgkmcnt(0)" ::: "memory");
    __builtin_amdgcn_s_barrier();
    asm volatile("" ::: "memory");
}
__device__ __forceinline__ int lane_id() {
    int ln;
    asm volatile("v_mbcnt_lo_u32_b32 %0, -1, 0\n\tv_mbcnt_hi_u32_b32 %0, -1, %0" : "=v"(ln));
    return ln;
}
__device__ __forceinline__ void sum2_rows(float& s, float& q) {
    unsigned a = __float_as_uint(s), b = a, c = __float_as_uint(q), d = c;
    asm volatile("s_nop 2\n\tv_permlane16_swap_b32 %0, %1\n\tv_permlane16_swap_b32 %2, %3\n\tv_add_f32 %0, %0, %1\n\tv_add_f32 %2, %2, %3\n\tv_mov_b32 %1, %0\n\tv_mov_b32 %3, %2\n\t"
                 "s_nop 1\n\tv_permlane32_swap_b32 %0, %1\n\tv_permlane32_swap_b32 %2, %3\n\tv_add_f32 %0, %0, %1\n\tv_add_f32 %2, %2, %3" : "+v"(a), "+v"(b), "+v"(c), "+v"(d));
    s = __uint_as_float(a); q = __uint_as_float(c);
}
template <int AUXV>
__device__ __forceinline__ void st128_(u32x4 v, rsrc_t r, unsigned voff, unsigned soff) {
    __builtin_amdgcn_raw_buffer_store_b128(v, r, voff, soff, AUXV);
    asm volatile("s_nop 1" :: "v"(v) : "memory");
}
#define st128(V, R, VO_, SO_) st128_<16>(V, R, VO_, SO_)
#define LD128(R, VO_, SO_) __builtin_amdgcn_raw_buffer_load_b128(R, VO_, SO_, 16)
__device__ __forceinline__ float ln_den(float var) { return rbf(__builtin_amdgcn_sqrtf(rbf(rbf(var) + 1e-5f))); }

struct Params {
    char* ws;
    const bf16_t *x, *mem0, *wq, *wk, *wv, *wo, *w1, *w2, *w3, *w4;
    bf16_t* out;
    unsigned par, pad;
};

#define SENT 0xFFFFFFFFu
constexpr int SPIN_CAP = 400000;
__device__ __forceinline__ bool spin_fail(int spins, const Params& P) {
    if ((spins & 1023) == 1023) {
        const rsrc_t rt = mkrsrc(P.ws + FL0, 512);
        unsigned o;
        asm volatile("v_mov_b32 %0, 0x100" : "=v"(o));
        if (__builtin_amdgcn_readfirstlane(__builtin_amdgcn_raw_buffer_load_b32(rt, o, 0, 16)) != 0u) return true;
        if (spins > SPIN_CAP) { __builtin_amdgcn_raw_buffer_store_b32(o, rt, o, 0, 16); return true; }
    }
    return false;
}
__device__ __forceinline__ unsigned nosent(unsigned d) { return d < 0xFFFFFFFEu ? d : 0xFFFFFFFEu; }
__device__ __forceinline__ u32x4 sent4() { unsigned sv; asm volatile("v_mov_b32 %0, -1" : "=v"(sv)); return (u32x4){sv, sv, sv, sv}; }
__device__ __forceinline__ char* lbase(const Params& P, int L) { return P.ws + ((P.par ^ L) << 13); }

constexpr unsigned L_X = 0;
constexpr unsigned L_MEM = 40960;
constexpr unsigned L_Q = 51200;
constexpr unsigned L_KM = 54272;
constexpr unsigned L_QX = 57344;
constexpr unsigned L_KX = 69632;
constexpr unsigned L_W = 81920;
constexpr int MEGA_LDS = 163840;

#define ASM_S_LOOP \
    "s_mov_b64 s[60:61], %[ws]\n\t" \
    "s_mov_b32 s62, %[sz]\n\t" \
    "s_mov_b32 s63, 0x20000\n\t" \
    "s_mov_b32 s40, 0\n\t" \
    "s_mov_b32 s41, 0x20000\n\t" \
    "s_cmp_eq_u32 %[wave], 0\n\t" \
    "s_cbranch_scc1 .Ls0_%=\n\t" \
    "s_cmp_eq_u32 %[wave], 1\n\t" \
    "s_cbranch_scc1 .Ls1_%=\n\t" \
    "s_cmp_eq_u32 %[wave], 2\n\t" \
    "s_cbranch_scc1 .Ls2_%=\n\t" \
    "s_cmp_eq_u32 %[wave], 3\n\t" \
    "s_cbranch_scc1 .Ls3_%=\n\t" \
    ".Ls4_%=:\n\t" \
    "s_mov_b32 s42, %[xs]\n\t" \
    "v_add_u32 v244, 0x18000, v243\n\t" \
    "s_waitcnt vmcnt(12) lgkmcnt(0)\n\t" \
    "s_barrier \n\t" \
    "s_add_u32 m0, %[wm], 0x12000\n\t" \
    "s_mov_b32 s44, 0x1e0000\n\t" \
    "buffer_load_dwordx4 v240, %[wr], s44 offen offset:0 nt lds\n\t" \
    "buffer_load_dwordx4 v241, %[wr], s44 offen offset:1024 nt lds\n\t" \
    "buffer_load_dwordx4 v242, %[wr], s44 offen offset:2048 nt lds\n\t" \
    "ds_read_b64_tr_b16 v[200:201], v244 offset:0\n\t" \
    "ds_read_b64_tr_b16 v[202:203], v244 offset:1536\n\t" \
    "ds_read_b64_tr_b16 v[0:1], v244 offset:32\n\t" \
    "ds_read_b64_tr_b16 v[2:3], v244 offset:1568\n\t" \
    "ds_read_b64_tr_b16 v[8:9], v244 offset:64\n\t" \
    "ds_read_b64_tr_b16 v[10:11], v244 offset:1600\n\t" \
    "v_add_u32 v244, 0x1e000, v243\n\t" \
    "s_waitcnt vmcnt(12) lgkmcnt(0)\n\t" \
    "s_barrier \n\t" \
    "s_add_u32 m0, %[wm], 0x18000\n\t" \
    "s_mov_b32 s44, 0x240000\n\t" \
    "buffer_load_dwordx4 v240, %[wr], s44 offen offset:0 nt lds\n\t" \
    "buffer_load_dwordx4 v241, %[wr], s44 offen offset:1024 nt lds\n\t" \
    "buffer_load_dwordx4 v242, %[wr], s44 offen offset:2048 nt lds\n\t" \
    "ds_read_b64_tr_b16 v[204:205], v244 offset:0\n\t" \
    "ds_read_b64_tr_b16 v[206:207], v244 offset:1536\n\t" \
    "ds_read_b64_tr_b16 v[4:5], v244 offset:32\n\t" \
    "ds_read_b64_tr_b16 v[6:7], v244 offset:1568\n\t" \
    "ds_read_b64_tr_b16 v[12:13], v244 offset:64\n\t" \
    "ds_read_b64_tr_b16 v[14:15], v244 offset:1600\n\t" \
    "v_add_u32 v244, 0x0, v243\n\t" \
    "s_waitcnt vmcnt(12) lgkmcnt(0)\n\t" \
    "s_barrier \n\t" \
    "s_add_u32 m0, %[wm], 0x1e000\n\t" \
    "s_mov_b32 s44, 0x2a0000\n\t" \
    "buffer_load_dwordx4 v240, %[wr], s44 offen offset:0 nt lds\n\t" \
    "buffer_load_dwordx4 v241, %[wr], s44 offen offset:1024 nt lds\n\t" \
    "buffer_load_dwordx4 v242, %[wr], s44 offen offset:2048 nt lds\n\t" \
    "ds_read_b64_tr_b16 v[16:17], v244 offset:0\n\t" \
    "ds_read_b64_tr_b16 v[18:19], v244 offset:1536\n\t" \
    "ds_read_b64_tr_b16 v[24:25], v244 offset:32\n\t" \
    "ds_read_b64_tr_b16 v[26:27], v244 offset:1568\n\t" \
    "ds_read_b64_tr_b16 v[32:33], v244 offset:64\n\t" \
    "ds_read_b64_tr_b16 v[34:35], v244 offset:1600\n\t" \
    "v_add_u32 v244, 0x6000, v243\n\t" \
    "s_waitcnt vmcnt(12) lgkmcnt(0)\n\t" \
    "s_barrier \n\t" \
    "s_add_u32 m0, %[wm], 0x0\n\t" \
    "s_mov_b32 s44, 0x300000\n\t" \
    "buffer_load_dwordx4 v240, %[wr], s44 offen offset:0 nt lds\n\t" \
    "buffer_load_dwordx4 v241, %[wr], s44 offen offset:1024 nt lds\n\t" \
    "buffer_load_dwordx4 v242, %[wr], s44 offen offset:2048 nt lds\n\t" \
    "ds_read_b64_tr_b16 v[20:21], v244 offset:0\n\t" \
    "ds_read_b64_tr_b16 v[22:23], v244 offset:1536\n\t" \
    "ds_read_b64_tr_b16 v[28:29], v244 offset:32\n\t" \
    "ds_read_b64_tr_b16 v[30:31], v244 offset:1568\n\t" \
    "ds_read_b64_tr_b16 v[36:37], v244 offset:64\n\t" \
    "ds_read_b64_tr_b16 v[38:39], v244 offset:1600\n\t" \
    "v_add_u32 v244, 0xc000, v243\n\t" \
    "s_waitcnt vmcnt(12) lgkmcnt(0)\n\t" \
    "s_barrier \n\t" \
    "s_add_u32 m0, %[wm], 0x6000\n\t" \
    "s_mov_b32 s44, 0x360000\n\t" \
    "buffer_load_dwordx4 v240, %[wr], s44 offen offset:0 nt lds\n\t" \
    "buffer_load_dwordx4 v241, %[wr], s44 offen offset:1024 nt lds\n\t" \
    "buffer_load_dwordx4 v242, %[wr], s44 offen offset:2048 nt lds\n\t" \
    "ds_read_b64_tr_b16 v[208:209], v244 offset:0\n\t" \
    "ds_read_b64_tr_b16 v[210:211], v244 offset:1536\n\t" \
    "ds_read_b64_tr_b16 v[40:41], v244 offset:32\n\t" \
    "ds_read_b64_tr_b16 v[42:43], v244 offset:1568\n\t" \
    "ds_read_b64_tr_b16 v[48:49], v244 offset:64\n\t" \
    "ds_read_b64_tr_b16 v[50:51], v244 offset:1600\n\t" \
    "v_add_u32 v244, 0x12000, v243\n\t" \
    "s_waitcnt vmcnt(12) lgkmcnt(0)\n\t" \
    "s_barrier \n\t" \
    "s_add_u32 m0, %[wm], 0xc000\n\t" \
    "s_mov_b32 s44, 0x3c0000\n\t" \
    "buffer_load_dwordx4 v240, %[wr], s44 offen offset:0 nt lds\n\t" \
    "buffer_load_dwordx4 v241, %[wr], s44 offen offset:1024 nt lds\n\t" \
    "buffer_load_dwordx4 v242, %[wr], s44 offen offset:2048 nt lds\n\t" \
    "ds_read_b64_tr_b16 v[212:213], v244 offset:0\n\t" \
    "ds_read_b64_tr_b16 v[214:215], v244 offset:1536\n\t" \
    "ds_read_b64_tr_b16 v[44:45], v244 offset:32\n\t" \
    "ds_read_b64_tr_b16 v[46:47], v244 offset:1568\n\t" \
    "ds_read_b64_tr_b16 v[52:53], v244 offset:64\n\t" \
    "ds_read_b64_tr_b16 v[54:55], v244 offset:1600\n\t" \
    "v_add_u32 v244, 0x18000, v243\n\t" \
    "s_waitcnt vmcnt(12) lgkmcnt(0)\n\t" \
    "s_barrier \n\t" \
    "s_add_u32 m0, %[wm], 0x12000\n\t" \
    "s_mov_b32 s44, 0x420000\n\t" \
    "buffer_load_dwordx4 v240, %[wr], s44 offen offset:0 nt lds\n\t" \
    "buffer_load_dwordx4 v241, %[wr], s44 offen offset:1024 nt lds\n\t" \
    "buffer_load_dwordx4 v242, %[wr], s44 offen offset:2048 nt lds\n\t" \
    "ds_read_b64_tr_b16 v[56:57], v244 offset:0\n\t" \
    "ds_read_b64_tr_b16 v[58:59], v244 offset:1536\n\t" \
    "ds_read_b64_tr_b16 v[64:65], v244 offset:32\n\t" \
    "ds_read_b64_tr_b16 v[66:67], v244 offset:1568\n\t" \
    "ds_read_b64_tr_b16 v[72:73], v244 offset:64\n\t" \
    "ds_read_b64_tr_b16 v[74:75], v244 offset:1600\n\t" \
    "v_add_u32 v244, 0x1e000, v243\n\t" \
    "s_waitcnt vmcnt(12) lgkmcnt(0)\n\t" \
    "s_barrier \n\t" \
    "s_add_u32 m0, %[wm], 0x18000\n\t" \
    "s_mov_b32 s44, 0x480000\n\t" \
    "buffer_load_dwordx4 v240, %[wr], s44 offen offset:0 nt lds\n\t" \
    "buffer_load_dwordx4 v241, %[wr], s44 offen offset:1024 nt lds\n\t" \
    "buffer_load_dwordx4 v242, %[wr], s44 offen offset:2048 nt lds\n\t" \
    "ds_read_b64_tr_b16 v[60:61], v244 offset:0\n\t" \
    "ds_read_b64_tr_b16 v[62:63], v244 offset:1536\n\t" \
    "ds_read_b64_tr_b16 v[68:69], v244 offset:32\n\t" \
    "ds_read_b64_tr_b16 v[70:71], v244 offset:1568\n\t" \
    "ds_read_b64_tr_b16 v[76:77], v244 offset:64\n\t" \
    "ds_read_b64_tr_b16 v[78:79], v244 offset:1600\n\t" \
    "v_add_u32 v244, 0x0, v243\n\t" \
    "s_waitcnt vmcnt(12) lgkmcnt(0)\n\t" \
    "s_barrier \n\t" \
    "v_mov_b32 v246, 0\n\t" \
    "buffer_load_dword v246, v246, s[60:63], %[mo] offen sc1\n\t" \
    "s_add_u32 m0, %[wm], 0x1e000\n\t" \
    "s_mov_b32 s44, 0x4e0000\n\t" \
    "buffer_load_dwordx4 v240, %[wr], s44 offen offset:0 nt lds\n\t" \
    "buffer_load_dwordx4 v241, %[wr], s44 offen offset:1024 nt lds\n\t" \
    "buffer_load_dwordx4 v242, %[wr], s44 offen offset:2048 nt lds\n\t" \
    "ds_read_b64_tr_b16 v[216:217], v244 offset:0\n\t" \
    "ds_read_b64_tr_b16 v[218:219], v244 offset:1536\n\t" \
    "ds_read_b64_tr_b16 v[80:81], v244 offset:32\n\t" \
    "ds_read_b64_tr_b16 v[82:83], v244 offset:1568\n\t" \
    "ds_read_b64_tr_b16 v[88:89], v244 offset:64\n\t" \
    "ds_read_b64_tr_b16 v[90:91], v244 offset:1600\n\t" \
    "v_add_u32 v244, 0x6000, v243\n\t" \
    "s_waitcnt vmcnt(12) lgkmcnt(0)\n\t" \
    "s_barrier \n\t" \
    "s_add_u32 m0, %[wm], 0x0\n\t" \
    "s_mov_b32 s44, 0x540000\n\t" \
    "buffer_load_dwordx4 v240, %[wr], s44 offen offset:0 nt lds\n\t" \
    "buffer_load_dwordx4 v241, %[wr], s44 offen offset:1024 nt lds\n\t" \
    "buffer_load_dwordx4 v242, %[wr], s44 offen offset:2048 nt lds\n\t" \
    "ds_read_b64_tr_b16 v[220:221], v244 offset:0\n\t" \
    "ds_read_b64_tr_b16 v[222:223], v244 offset:1536\n\t" \
    "ds_read_b64_tr_b16 v[84:85], v244 offset:32\n\t" \
    "ds_read_b64_tr_b16 v[86:87], v244 offset:1568\n\t" \
    "ds_read_b64_tr_b16 v[92:93], v244 offset:64\n\t" \
    "ds_read_b64_tr_b16 v[94:95], v244 offset:1600\n\t" \
    "v_add_u32 v244, 0xc000, v243\n\t" \
    "s_waitcnt vmcnt(12) lgkmcnt(0)\n\t" \
    "s_barrier \n\t" \
    "s_add_u32 m0, %[wm], 0x6000\n\t" \
    "s_mov_b32 s44, 0x5a0000\n\t" \
    "buffer_load_dwordx4 v240, %[wr], s44 offen offset:0 nt lds\n\t" \
    "buffer_load_dwordx4 v241, %[wr], s44 offen offset:1024 nt lds\n\t" \
    "buffer_load_dwordx4 v242, %[wr], s44 offen offset:2048 nt lds\n\t" \
    "ds_read_b64_tr_b16 v[96:97], v244 offset:0\n\t" \
    "ds_read_b64_tr_b16 v[98:99], v244 offset:1536\n\t" \
    "ds_read_b64_tr_b16 v[104:105], v244 offset:32\n\t" \
    "ds_read_b64_tr_b16 v[106:107], v244 offset:1568\n\t" \
    "ds_read_b64_tr_b16 v[112:113], v244 offset:64\n\t" \
    "ds_read_b64_tr_b16 v[114:115], v244 offset:1600\n\t" \
    "v_add_u32 v244, 0x12000, v243\n\t" \
    "s_waitcnt vmcnt(12) lgkmcnt(0)\n\t" \
    "s_barrier \n\t" \
    "s_add_u32 m0, %[wm], 0xc000\n\t" \
    "s_mov_b32 s44, 0x600000\n\t" \
    "buffer_load_dwordx4 v240, %[wr], s44 offen offset:0 nt lds\n\t" \
    "buffer_load_dwordx4 v241, %[wr], s44 offen offset:1024 nt lds\n\t" \
    "buffer_load_dwordx4 v242, %[wr], s44 offen offset:2048 nt lds\n\t" \
    "ds_read_b64_tr_b16 v[100:101], v244 offset:0\n\t" \
    "ds_read_b64_tr_b16 v[102:103], v244 offset:1536\n\t" \
    "ds_read_b64_tr_b16 v[108:109], v244 offset:32\n\t" \
    "ds_read_b64_tr_b16 v[110:111], v244 offset:1568\n\t" \
    "ds_read_b64_tr_b16 v[116:117], v244 offset:64\n\t" \
    "ds_read_b64_tr_b16 v[118:119], v244 offset:1600\n\t" \
    "v_add_u32 v244, 0x18000, v243\n\t" \
    "s_waitcnt vmcnt(12) lgkmcnt(0)\n\t" \
    "s_barrier \n\t" \
    "s_add_u32 m0, %[wm], 0x12000\n\t" \
    "s_mov_b32 s44, 0x660000\n\t" \
    "buffer_load_dwordx4 v240, %[wr], s44 offen offset:0 nt lds\n\t" \
    "buffer_load_dwordx4 v241, %[wr], s44 offen offset:1024 nt lds\n\t" \
    "buffer_load_dwordx4 v242, %[wr], s44 offen offset:2048 nt lds\n\t" \
    "ds_read_b64_tr_b16 v[224:225], v244 offset:0\n\t" \
    "ds_read_b64_tr_b16 v[226:227], v244 offset:1536\n\t" \
    "ds_read_b64_tr_b16 v[120:121], v244 offset:32\n\t" \
    "ds_read_b64_tr_b16 v[122:123], v244 offset:1568\n\t" \
    "ds_read_b64_tr_b16 v[128:129], v244 offset:64\n\t" \
    "ds_read_b64_tr_b16 v[130:131], v244 offset:1600\n\t" \
    "v_add_u32 v244, 0x1e000, v243\n\t" \
    "s_waitcnt vmcnt(12) lgkmcnt(0)\n\t" \
    "s_barrier \n\t" \
    ".Ls41_%=:\n\t" \
    "v_readfirstlane_b32 s43, v246\n\t" \
    "s_nop 0\n\t" \
    "s_cmp_lg_u32 s43, -1\n\t" \
    "s_cbranch_scc1 .Ls42_%=\n\t" \
    "s_cmp_eq_u32 s41, 0\n\t" \
    "s_cbranch_scc1 .Ls42_%=\n\t" \
    "s_sub_u32 s41, s41, 1\n\t" \
    "s_sleep 4\n\t" \
    "v_mov_b32 v246, 0\n\t" \
    "buffer_load_dword v246, v246, s[60:63], %[mo] offen sc1\n\t" \
    "s_waitcnt vmcnt(0)\n\t" \
    "s_branch .Ls41_%=\n\t" \
    ".Ls42_%=:\n\t" \
    "v_mov_b32 v246, 0x8000\n\t" \
    "s_mov_b64 exec, 1\n\t" \
    "buffer_store_dword v246, v246, s[60:63], %[mo] offen sc1\n\t" \
    "s_mov_b64 exec, -1\n\t" \
    "s_and_b32 s43, s43, 0x2000\n\t" \
    "s_xor_b32 s43, s43, %[lx]\n\t" \
    "s_add_u32 s60, s60, s43\n\t" \
    "s_addc_u32 s61, s61, 0\n\t" \
    "v_and_b32 v252, 0xf0, v253\n\t" \
    "v_lshlrev_b32 v252, 3, v252\n\t" \
    "buffer_load_dword v252, v252, s[60:63], %[fl] offen sc1\n\t" \
    "s_add_u32 m0, %[wm], 0x18000\n\t" \
    "s_mov_b32 s44, 0x6c0000\n\t" \
    "buffer_load_dwordx4 v240, %[wr], s44 offen offset:0 nt lds\n\t" \
    "buffer_load_dwordx4 v241, %[wr], s44 offen offset:1024 nt lds\n\t" \
    "buffer_load_dwordx4 v242, %[wr], s44 offen offset:2048 nt lds\n\t" \
    "ds_read_b64_tr_b16 v[228:229], v244 offset:0\n\t" \
    "ds_read_b64_tr_b16 v[230:231], v244 offset:1536\n\t" \
    "ds_read_b64_tr_b16 v[124:125], v244 offset:32\n\t" \
    "ds_read_b64_tr_b16 v[126:127], v244 offset:1568\n\t" \
    "ds_read_b64_tr_b16 v[132:133], v244 offset:64\n\t" \
    "ds_read_b64_tr_b16 v[134:135], v244 offset:1600\n\t" \
    "v_add_u32 v244, 0x0, v243\n\t" \
    "s_waitcnt vmcnt(12) lgkmcnt(0)\n\t" \
    "s_barrier \n\t" \
    "s_add_u32 m0, %[wm], 0x1e000\n\t" \
    "s_mov_b32 s44, 0x720000\n\t" \
    "buffer_load_dwordx4 v240, %[wr], s44 offen offset:0 nt lds\n\t" \
    "buffer_load_dwordx4 v241, %[wr], s44 offen offset:1024 nt lds\n\t" \
    "buffer_load_dwordx4 v242, %[wr], s44 offen offset:2048 nt lds\n\t" \
    "ds_read_b64_tr_b16 v[136:137], v244 offset:0\n\t" \
    "ds_read_b64_tr_b16 v[138:139], v244 offset:1536\n\t" \
    "ds_read_b64_tr_b16 v[144:145], v244 offset:32\n\t" \
    "ds_read_b64_tr_b16 v[146:147], v244 offset:1568\n\t" \
    "ds_read_b64_tr_b16 v[152:153], v244 offset:64\n\t" \
    "ds_read_b64_tr_b16 v[154:155], v244 offset:1600\n\t" \
    "v_add_u32 v244, 0x6000, v243\n\t" \
    "s_waitcnt vmcnt(12) lgkmcnt(0)\n\t" \
    "s_barrier \n\t" \
    "ds_read_b64_tr_b16 v[140:141], v244 offset:0\n\t" \
    "ds_read_b64_tr_b16 v[142:143], v244 offset:1536\n\t" \
    "ds_read_b64_tr_b16 v[148:149], v244 offset:32\n\t" \
    "ds_read_b64_tr_b16 v[150:151], v244 offset:1568\n\t" \
    "ds_read_b64_tr_b16 v[156:157], v244 offset:64\n\t" \
    "ds_read_b64_tr_b16 v[158:159], v244 offset:1600\n\t" \
    "v_add_u32 v244, 0xc000, v243\n\t" \
    "s_waitcnt vmcnt(9) lgkmcnt(0)\n\t" \
    "s_barrier \n\t" \
    "ds_read_b64_tr_b16 v[232:233], v244 offset:0\n\t" \
    "ds_read_b64_tr_b16 v[234:235], v244 offset:1536\n\t" \
    "ds_read_b64_tr_b16 v[160:161], v244 offset:32\n\t" \
    "ds_read_b64_tr_b16 v[162:163], v244 offset:1568\n\t" \
    "ds_read_b64_tr_b16 v[168:169], v244 offset:64\n\t" \
    "ds_read_b64_tr_b16 v[170:171], v244 offset:1600\n\t" \
    "v_add_u32 v244, 0x12000, v243\n\t" \
    "s_waitcnt vmcnt(6) lgkmcnt(0)\n\t" \
    "s_barrier \n\t" \
    "ds_read_b64_tr_b16 v[236:237], v244 offset:0\n\t" \
    "ds_read_b64_tr_b16 v[238:239], v244 offset:1536\n\t" \
    "ds_read_b64_tr_b16 v[164:165], v244 offset:32\n\t" \
    "ds_read_b64_tr_b16 v[166:167], v244 offset:1568\n\t" \
    "ds_read_b64_tr_b16 v[172:173], v244 offset:64\n\t" \
    "ds_read_b64_tr_b16 v[174:175], v244 offset:1600\n\t" \
    "v_add_u32 v244, 0x18000, v243\n\t" \
    "s_waitcnt vmcnt(3) lgkmcnt(0)\n\t" \
    "s_barrier \n\t" \
    "ds_read_b64_tr_b16 v[176:177], v244 offset:0\n\t" \
    "ds_read_b64_tr_b16 v[178:179], v244 offset:1536\n\t" \
    "ds_read_b64_tr_b16 v[184:185], v244 offset:32\n\t" \
    "ds_read_b64_tr_b16 v[186:187], v244 offset:1568\n\t" \
    "ds_read_b64_tr_b16 v[192:193], v244 offset:64\n\t" \
    "ds_read_b64_tr_b16 v[194:195], v244 offset:1600\n\t" \
    "s_mov_b32 s43, 0\n\t" \
    "v_cmp_eq_u32 vcc, 0, v252\n\t" \
    "s_cbranch_vccnz .Ls43_%=\n\t" \
    "s_mov_b32 m0, %[m0]\n\t" \
    "s_add_u32 s45, s42, %[x1]\n\t" \
    "buffer_load_dwordx4 v253, s[60:63], s42 offen offset:0 sc1 lds\n\t" \
    "buffer_load_dwordx4 v253, s[60:63], s42 offen offset:1024 sc1 lds\n\t" \
    "buffer_load_dwordx4 v253, s[60:63], s42 offen offset:2048 sc1 lds\n\t" \
    "buffer_load_dwordx4 v253, s[60:63], s42 offen offset:3072 sc1 lds\n\t" \
    "s_mov_b32 m0, %[m1]\n\t" \
    "s_nop 0\n\t" \
    "buffer_load_dwordx4 v253, s[60:63], s45 offen offset:0 sc1 lds\n\t" \
    "buffer_load_dwordx4 v253, s[60:63], s45 offen offset:1024 sc1 lds\n\t" \
    "s_mov_b32 s43, 1\n\t" \
    ".Ls43_%=:\n\t" \
    "v_add_u32 v244, 0x1e000, v243\n\t" \
    "s_waitcnt vmcnt(0) lgkmcnt(0)\n\t" \
    "s_barrier \n\t" \
    "ds_read_b64_tr_b16 v[180:181], v244 offset:0\n\t" \
    "ds_read_b64_tr_b16 v[182:183], v244 offset:1536\n\t" \
    "ds_read_b64_tr_b16 v[188:189], v244 offset:32\n\t" \
    "ds_read_b64_tr_b16 v[190:191], v244 offset:1568\n\t" \
    "ds_read_b64_tr_b16 v[196:197], v244 offset:64\n\t" \
    "ds_read_b64_tr_b16 v[198:199], v244 offset:1600\n\t" \
    "s_waitcnt lgkmcnt(0)\n\t" \
    "s_barrier \n\t" \
    "ds_write_b128 v254, v[200:203] offset:0\n\t" \
    "ds_write_b128 v254, v[204:207] offset:1024\n\t" \
    "ds_write_b128 v254, v[208:211] offset:2048\n\t" \
    "ds_write_b128 v254, v[212:215] offset:3072\n\t" \
    "ds_write_b128 v254, v[216:219] offset:4096\n\t" \
    "ds_write_b128 v254, v[220:223] offset:5120\n\t" \
    "ds_write_b128 v254, v[224:227] offset:6144\n\t" \
    "ds_write_b128 v254, v[228:231] offset:7168\n\t" \
    "ds_write_b128 v254, v[232:235] offset:8192\n\t" \
    "ds_write_b128 v254, v[236:239] offset:9216\n\t" \
    "s_cmp_lg_u32 s43, 0\n\t" \
    "s_cbranch_scc1 .Ls44_%=\n\t" \
    "s_mov_b32 s44, 0\n\t" \
    ".Ls45_%=:\n\t" \
    "v_cmp_ge_u32 vcc, s44, v252\n\t" \
    "s_cbranch_vccz .Ls46_%=\n\t" \
    "s_cmp_eq_u32 s41, 0\n\t" \
    "s_cbranch_scc1 .Ls46_%=\n\t" \
    "s_sub_u32 s41, s41, 1\n\t" \
    "s_sleep 8\n\t" \
    "v_and_b32 v252, 0xf0, v253\n\t" \
    "v_lshlrev_b32 v252, 3, v252\n\t" \
    "buffer_load_dword v252, v252, s[60:63], %[fl] offen sc1\n\t" \
    "s_waitcnt vmcnt(0)\n\t" \
    "s_branch .Ls45_%=\n\t" \
    ".Ls46_%=:\n\t" \
    "s_mov_b32 m0, %[m0]\n\t" \
    "s_add_u32 s45, s42, %[x1]\n\t" \
    "buffer_load_dwordx4 v253, s[60:63], s42 offen offset:0 sc1 lds\n\t" \
    "buffer_load_dwordx4 v253, s[60:63], s42 offen offset:1024 sc1 lds\n\t" \
    "buffer_load_dwordx4 v253, s[60:63], s42 offen offset:2048 sc1 lds\n\t" \
    "buffer_load_dwordx4 v253, s[60:63], s42 offen offset:3072 sc1 lds\n\t" \
    "s_mov_b32 m0, %[m1]\n\t" \
    "s_nop 0\n\t" \
    "buffer_load_dwordx4 v253, s[60:63], s45 offen offset:0 sc1 lds\n\t" \
    "buffer_load_dwordx4 v253, s[60:63], s45 offen offset:1024 sc1 lds\n\t" \
    ".Ls44_%=:\n\t" \
    "s_waitcnt vmcnt(0) lgkmcnt(0)\n\t" \
    "s_barrier \n\t" \
    "ds_read_b128 v[224:227], v253 offset:0\n\t" \
    "ds_read_b128 v[244:247], v254 offset:0\n\t" \
    "ds_read_b128 v[228:231], v253 offset:1024\n\t" \
    "ds_read_b128 v[232:235], v253 offset:20480\n\t" \
    "ds_read_b128 v[236:239], v253 offset:21504\n\t" \
    "ds_read_b128 v[248:251], v254 offset:1024\n\t" \
    ".Ls47_%=:\n\t" \
    "s_cmp_lg_u32 %[wave], 4\n\t" \
    "s_cbranch_scc1 .Ls48_%=\n\t" \
    "s_sleep 9\n\t" \
    ".Ls48_%=:\n\t" \
    "s_waitcnt lgkmcnt(5)\n\t" \
    "v_mfma_f32_16x16x32_bf16 v[204:207], v[0:3], v[224:227], 0\n\t" \
    "s_add_u32 m0, %[m0], 0x3000\n\t" \
    "s_waitcnt lgkmcnt(3)\n\t" \
    "v_mfma_f32_16x16x32_bf16 v[204:207], v[4:7], v[228:231], v[204:207]\n\t" \
    "ds_read_b128 v[240:243], v253 offset:2048\n\t" \
    "s_add_u32 s45, s42, 0xc000\n\t" \
    "v_mfma_f32_16x16x32_bf16 v[208:211], v[8:11], v[224:227], 0\n\t" \
    "buffer_load_dwordx4 v253, s[60:63], s45 offen offset:0 sc1 lds\n\t" \
    "v_mfma_f32_16x16x32_bf16 v[208:211], v[12:15], v[228:231], v[208:211]\n\t" \
    "v_mfma_f32_16x16x32_bf16 v[200:203], v[244:247], v[224:227], 0\n\t" \
    "buffer_load_dwordx4 v253, s[60:63], s45 offen offset:1024 sc1 lds\n\t" \
    "s_waitcnt lgkmcnt(1)\n\t" \
    "v_mfma_f32_16x16x32_bf16 v[200:203], v[248:251], v[228:231], v[200:203]\n\t" \
    "v_mfma_f32_16x16x32_bf16 v[216:219], v[0:3], v[232:235], 0\n\t" \
    "ds_read_b128 v[224:227], v253 offset:3072\n\t" \
    "buffer_load_dwordx4 v253, s[60:63], s45 offen offset:2048 sc1 lds\n\t" \
    "v_mfma_f32_16x16x32_bf16 v[216:219], v[4:7], v[236:239], v[216:219]\n\t" \
    "ds_read_b128 v[228:231], v253 offset:22528\n\t" \
    "v_mfma_f32_16x16x32_bf16 v[220:223], v[8:11], v[232:235], 0\n\t" \
    "buffer_load_dwordx4 v253, s[60:63], s45 offen offset:3072 sc1 lds\n\t" \
    "v_mfma_f32_16x16x32_bf16 v[220:223], v[12:15], v[236:239], v[220:223]\n\t" \
    "v_mfma_f32_16x16x32_bf16 v[212:215], v[244:247], v[232:235], 0\n\t" \
    "ds_read_b128 v[244:247], v254 offset:2048\n\t" \
    "v_mfma_f32_16x16x32_bf16 v[212:215], v[248:251], v[236:239], v[212:215]\n\t" \
    "ds_read_b128 v[248:251], v254 offset:3072\n\t" \
    "s_waitcnt lgkmcnt(4)\n\t" \
    "v_mfma_f32_16x16x32_bf16 v[204:207], v[24:27], v[240:243], v[204:207]\n\t" \
    "ds_read_b128 v[232:235], v253 offset:23552\n\t" \
    "s_waitcnt lgkmcnt(4)\n\t" \
    "v_mfma_f32_16x16x32_bf16 v[204:207], v[28:31], v[224:227], v[204:207]\n\t" \
    "ds_read_b128 v[236:239], v253 offset:4096\n\t" \
    "v_mfma_f32_16x16x32_bf16 v[208:211], v[32:35], v[240:243], v[208:211]\n\t" \
    "v_mfma_f32_16x16x32_bf16 v[208:211], v[36:39], v[224:227], v[208:211]\n\t" \
    "v_mfma_f32_16x16x32_bf16 v[200:203], v[16:19], v[240:243], v[200:203]\n\t" \
    "v_mfma_f32_16x16x32_bf16 v[200:203], v[20:23], v[224:227], v[200:203]\n\t" \
    "s_waitcnt lgkmcnt(4)\n\t" \
    "v_mfma_f32_16x16x32_bf16 v[216:219], v[24:27], v[228:231], v[216:219]\n\t" \
    "ds_read_b128 v[240:243], v253 offset:5120\n\t" \
    "s_waitcnt lgkmcnt(2)\n\t" \
    "v_mfma_f32_16x16x32_bf16 v[216:219], v[28:31], v[232:235], v[216:219]\n\t" \
    "ds_read_b128 v[224:227], v253 offset:24576\n\t" \
    "v_mfma_f32_16x16x32_bf16 v[220:223], v[32:35], v[228:231], v[220:223]\n\t" \
    "v_mfma_f32_16x16x32_bf16 v[220:223], v[36:39], v[232:235], v[220:223]\n\t" \
    "v_mfma_f32_16x16x32_bf16 v[212:215], v[16:19], v[228:231], v[212:215]\n\t" \
    "v_mfma_f32_16x16x32_bf16 v[212:215], v[20:23], v[232:235], v[212:215]\n\t" \
    "s_waitcnt lgkmcnt(2)\n\t" \
    "v_mfma_f32_16x16x32_bf16 v[204:207], v[40:43], v[236:239], v[204:207]\n\t" \
    "ds_read_b128 v[228:231], v253 offset:25600\n\t" \
    "s_waitcnt lgkmcnt(2)\n\t" \
    "v_mfma_f32_16x16x32_bf16 v[204:207], v[44:47], v[240:243], v[204:207]\n\t" \
    "ds_read_b128 v[232:235], v253 offset:6144\n\t" \
    "v_mfma_f32_16x16x32_bf16 v[208:211], v[48:51], v[236:239], v[208:211]\n\t" \
    "v_mfma_f32_16x16x32_bf16 v[208:211], v[52:55], v[240:243], v[208:211]\n\t" \
    "v_mfma_f32_16x16x32_bf16 v[200:203], v[244:247], v[236:239], v[200:203]\n\t" \
    "v_mfma_f32_16x16x32_bf16 v[200:203], v[248:251], v[240:243], v[200:203]\n\t" \
    "s_waitcnt lgkmcnt(2)\n\t" \
    "v_mfma_f32_16x16x32_bf16 v[216:219], v[40:43], v[224:227], v[216:219]\n\t" \
    "ds_read_b128 v[236:239], v253 offset:7168\n\t" \
    "s_waitcnt lgkmcnt(2)\n\t" \
    "v_mfma_f32_16x16x32_bf16 v[216:219], v[44:47], v[228:231], v[216:219]\n\t" \
    "ds_read_b128 v[240:243], v253 offset:26624\n\t" \
    "v_mfma_f32_16x16x32_bf16 v[220:223], v[48:51], v[224:227], v[220:223]\n\t" \
    "v_mfma_f32_16x16x32_bf16 v[220:223], v[52:55], v[228:231], v[220:223]\n\t" \
    "v_mfma_f32_16x16x32_bf16 v[212:215], v[244:247], v[224:227], v[212:215]\n\t" \
    "ds_read_b128 v[244:247], v254 offset:4096\n\t" \
    "v_mfma_f32_16x16x32_bf16 v[212:215], v[248:251], v[228:231], v[212:215]\n\t" \
    "ds_read_b128 v[248:251], v254 offset:5120\n\t" \
    "s_waitcnt lgkmcnt(4)\n\t" \
    "v_mfma_f32_16x16x32_bf16 v[204:207], v[64:67], v[232:235], v[204:207]\n\t" \
    "ds_read_b128 v[224:227], v253 offset:27648\n\t" \
    "s_waitcnt lgkmcnt(4)\n\t" \
    "v_mfma_f32_16x16x32_bf16 v[204:207], v[68:71], v[236:239], v[204:207]\n\t" \
    "ds_read_b128 v[228:231], v253 offset:8192\n\t" \
    "v_mfma_f32_16x16x32_bf16 v[208:211], v[72:75], v[232:235], v[208:211]\n\t" \
    "v_mfma_f32_16x16x32_bf16 v[208:211], v[76:79], v[236:239], v[208:211]\n\t" \
    "v_mfma_f32_16x16x32_bf16 v[200:203], v[56:59], v[232:235], v[200:203]\n\t" \
    "v_mfma_f32_16x16x32_bf16 v[200:203], v[60:63], v[236:239], v[200:203]\n\t" \
    "v_and_b32 v252, 0xf0, v253\n\t" \
    "v_lshlrev_b32 v252, 3, v252\n\t" \
    "buffer_load_dword v252, v252, s[60:63], %[fl] offen sc1\n\t" \
    "s_waitcnt lgkmcnt(4)\n\t" \
    "v_mfma_f32_16x16x32_bf16 v[216:219], v[64:67], v[240:243], v[216:219]\n\t" \
    "ds_read_b128 v[232:235], v253 offset:9216\n\t" \
    "s_waitcnt lgkmcnt(2)\n\t" \
    "v_mfma_f32_16x16x32_bf16 v[216:219], v[68:71], v[224:227], v[216:219]\n\t" \
    "ds_read_b128 v[236:239], v253 offset:28672\n\t" \
    "v_mfma_f32_16x16x32_bf16 v[220:223], v[72:75], v[240:243], v[220:223]\n\t" \
    "v_mfma_f32_16x16x32_bf16 v[220:223], v[76:79], v[224:227], v[220:223]\n\t" \
    "v_mfma_f32_16x16x32_bf16 v[212:215], v[56:59], v[240:243], v[212:215]\n\t" \
    "v_mfma_f32_16x16x32_bf16 v[212:215], v[60:63], v[224:227], v[212:215]\n\t" \
    "s_waitcnt lgkmcnt(2)\n\t" \
    "v_mfma_f32_16x16x32_bf16 v[204:207], v[80:83], v[228:231], v[204:207]\n\t" \
    "ds_read_b128 v[240:243], v253 offset:29696\n\t" \
    "s_waitcnt lgkmcnt(2)\n\t" \
    "v_mfma_f32_16x16x32_bf16 v[204:207], v[84:87], v[232:235], v[204:207]\n\t" \
    "ds_read_b128 v[224:227], v253 offset:10240\n\t" \
    "v_mfma_f32_16x16x32_bf16 v[208:211], v[88:91], v[228:231], v[208:211]\n\t" \
    "v_mfma_f32_16x16x32_bf16 v[208:211], v[92:95], v[232:235], v[208:211]\n\t" \
    "v_mfma_f32_16x16x32_bf16 v[200:203], v[244:247], v[228:231], v[200:203]\n\t" \
    "v_mfma_f32_16x16x32_bf16 v[200:203], v[248:251], v[232:235], v[200:203]\n\t" \
    "s_waitcnt lgkmcnt(2)\n\t" \
    "v_mfma_f32_16x16x32_bf16 v[216:219], v[80:83], v[236:239], v[216:219]\n\t" \
    "ds_read_b128 v[228:231], v253 offset:11264\n\t" \
    "s_add_u32 s44, s40, 1\n\t" \
    "s_waitcnt lgkmcnt(2)\n\t" \
    "v_mfma_f32_16x16x32_bf16 v[216:219], v[84:87], v[240:243], v[216:219]\n\t" \
    "ds_read_b128 v[232:235], v253 offset:30720\n\t" \
    "s_and_b32 s45, s44, 15\n\t" \
    "v_mfma_f32_16x16x32_bf16 v[220:223], v[88:91], v[236:239], v[220:223]\n\t" \
    "s_cmp_eq_u32 s45, 0\n\t" \
    "v_mfma_f32_16x16x32_bf16 v[220:223], v[92:95], v[240:243], v[220:223]\n\t" \
    "s_cselect_b32 s45, %[cj], %[cs]\n\t" \
    "v_mfma_f32_16x16x32_bf16 v[212:215], v[244:247], v[236:239], v[212:215]\n\t" \
    "ds_read_b128 v[244:247], v254 offset:6144\n\t" \
    "s_cmp_eq_u32 s44, 64\n\t" \
    "v_mfma_f32_16x16x32_bf16 v[212:215], v[248:251], v[240:243], v[212:215]\n\t" \
    "ds_read_b128 v[248:251], v254 offset:7168\n\t" \
    "s_cselect_b32 s45, 0, s45\n\t" \
    "ds_read_b128 v[236:239], v253 offset:31744\n\t" \
    "s_setprio 1\n\t" \
    "s_waitcnt vmcnt(0) lgkmcnt(0)\n\t" \
    "s_barrier \n\t" \
    "s_add_u32 s42, s42, s45\n\t" \
    "s_add_u32 s44, s40, 1\n\t" \
    "s_min_u32 s44, s44, 63\n\t" \
    ".Ls49_%=:\n\t" \
    "v_cmp_ge_u32 vcc, s44, v252\n\t" \
    "s_cbranch_vccz .Ls410_%=\n\t" \
    "s_cmp_eq_u32 s41, 0\n\t" \
    "s_cbranch_scc1 .Ls410_%=\n\t" \
    "s_sub_u32 s41, s41, 1\n\t" \
    "s_sleep 2\n\t" \
    "v_and_b32 v252, 0xf0, v253\n\t" \
    "v_lshlrev_b32 v252, 3, v252\n\t" \
    "buffer_load_dword v252, v252, s[60:63], %[fl] offen sc1\n\t" \
    "s_waitcnt vmcnt(0)\n\t" \
    "s_branch .Ls49_%=\n\t" \
    ".Ls410_%=:\n\t" \
    "s_mov_b32 m0, %[m0]\n\t" \
    "s_add_u32 s45, s42, %[x1]\n\t" \
    "buffer_load_dwordx4 v253, s[60:63], s42 offen offset:0 sc1 lds\n\t" \
    "buffer_load_dwordx4 v253, s[60:63], s42 offen offset:1024 sc1 lds\n\t" \
    "buffer_load_dwordx4 v253, s[60:63], s42 offen offset:2048 sc1 lds\n\t" \
    "buffer_load_dwordx4 v253, s[60:63], s42 offen offset:3072 sc1 lds\n\t" \
    "s_mov_b32 m0, %[m1]\n\t" \
    "s_nop 0\n\t" \
    "buffer_load_dwordx4 v253, s[60:63], s45 offen offset:0 sc1 lds\n\t" \
    "buffer_load_dwordx4 v253, s[60:63], s45 offen offset:1024 sc1 lds\n\t" \
    "v_mfma_f32_16x16x32_bf16 v[204:207], v[104:107], v[224:227], v[204:207]\n\t" \
    "v_mfma_f32_16x16x32_bf16 v[204:207], v[108:111], v[228:231], v[204:207]\n\t" \
    "ds_read_b128 v[240:243], v253 offset:12288\n\t" \
    "v_mfma_f32_16x16x32_bf16 v[208:211], v[112:115], v[224:227], v[208:211]\n\t" \
    "v_mfma_f32_16x16x32_bf16 v[208:211], v[116:119], v[228:231], v[208:211]\n\t" \
    "v_mfma_f32_16x16x32_bf16 v[200:203], v[96:99], v[224:227], v[200:203]\n\t" \
    "v_mfma_f32_16x16x32_bf16 v[200:203], v[100:103], v[228:231], v[200:203]\n\t" \
    "v_mfma_f32_16x16x32_bf16 v[216:219], v[104:107], v[232:235], v[216:219]\n\t" \
    "ds_read_b128 v[224:227], v253 offset:13312\n\t" \
    "v_mfma_f32_16x16x32_bf16 v[216:219], v[108:111], v[236:239], v[216:219]\n\t" \
    "ds_read_b128 v[228:231], v253 offset:32768\n\t" \
    "v_mfma_f32_16x16x32_bf16 v[220:223], v[112:115], v[232:235], v[220:223]\n\t" \
    "v_mfma_f32_16x16x32_bf16 v[220:223], v[116:119], v[236:239], v[220:223]\n\t" \
    "v_mfma_f32_16x16x32_bf16 v[212:215], v[96:99], v[232:235], v[212:215]\n\t" \
    "v_mfma_f32_16x16x32_bf16 v[212:215], v[100:103], v[236:239], v[212:215]\n\t" \
    "s_waitcnt lgkmcnt(2)\n\t" \
    "v_mfma_f32_16x16x32_bf16 v[204:207], v[120:123], v[240:243], v[204:207]\n\t" \
    "ds_read_b128 v[232:235], v253 offset:33792\n\t" \
    "s_waitcnt lgkmcnt(2)\n\t" \
    "v_mfma_f32_16x16x32_bf16 v[204:207], v[124:127], v[224:227], v[204:207]\n\t" \
    "ds_read_b128 v[236:239], v253 offset:16384\n\t" \
    "v_mfma_f32_16x16x32_bf16 v[208:211], v[128:131], v[240:243], v[208:211]\n\t" \
    "v_mfma_f32_16x16x32_bf16 v[208:211], v[132:135], v[224:227], v[208:211]\n\t" \
    "v_mfma_f32_16x16x32_bf16 v[200:203], v[244:247], v[240:243], v[200:203]\n\t" \
    "v_mfma_f32_16x16x32_bf16 v[200:203], v[248:251], v[224:227], v[200:203]\n\t" \
    "s_waitcnt lgkmcnt(2)\n\t" \
    "v_mfma_f32_16x16x32_bf16 v[216:219], v[120:123], v[228:231], v[216:219]\n\t" \
    "ds_read_b128 v[240:243], v253 offset:17408\n\t" \
    "s_waitcnt lgkmcnt(2)\n\t" \
    "v_mfma_f32_16x16x32_bf16 v[216:219], v[124:127], v[232:235], v[216:219]\n\t" \
    "ds_read_b128 v[224:227], v253 offset:36864\n\t" \
    "v_mfma_f32_16x16x32_bf16 v[220:223], v[128:131], v[228:231], v[220:223]\n\t" \
    "v_mfma_f32_16x16x32_bf16 v[220:223], v[132:135], v[232:235], v[220:223]\n\t" \
    "v_mfma_f32_16x16x32_bf16 v[212:215], v[244:247], v[228:231], v[212:215]\n\t" \
    "ds_read_b128 v[244:247], v254 offset:8192\n\t" \
    "v_mfma_f32_16x16x32_bf16 v[212:215], v[248:251], v[232:235], v[212:215]\n\t" \
    "ds_read_b128 v[248:251], v254 offset:9216\n\t" \
    "s_waitcnt lgkmcnt(4)\n\t" \
    "v_mfma_f32_16x16x32_bf16 v[204:207], v[160:163], v[236:239], v[204:207]\n\t" \
    "ds_read_b128 v[228:231], v253 offset:37888\n\t" \
    "s_waitcnt lgkmcnt(4)\n\t" \
    "v_mfma_f32_16x16x32_bf16 v[204:207], v[164:167], v[240:243], v[204:207]\n\t" \
    "ds_read_b128 v[232:235], v253 offset:14336\n\t" \
    "v_mfma_f32_16x16x32_bf16 v[208:211], v[168:171], v[236:239], v[208:211]\n\t" \
    "v_mfma_f32_16x16x32_bf16 v[208:211], v[172:175], v[240:243], v[208:211]\n\t" \
    "s_waitcnt lgkmcnt(3)\n\t" \
    "v_mfma_f32_16x16x32_bf16 v[200:203], v[244:247], v[236:239], v[200:203]\n\t" \
    "s_waitcnt lgkmcnt(2)\n\t" \
    "v_mfma_f32_16x16x32_bf16 v[200:203], v[248:251], v[240:243], v[200:203]\n\t" \
    "v_mfma_f32_16x16x32_bf16 v[216:219], v[160:163], v[224:227], v[216:219]\n\t" \
    "ds_read_b128 v[236:239], v253 offset:15360\n\t" \
    "v_add_u32 v252, %[q2], v253\n\t" \
    "s_waitcnt lgkmcnt(2)\n\t" \
    "v_mfma_f32_16x16x32_bf16 v[216:219], v[164:167], v[228:231], v[216:219]\n\t" \
    "ds_read_b128 v[240:243], v253 offset:18432\n\t" \
    "v_mfma_f32_16x16x32_bf16 v[220:223], v[168:171], v[224:227], v[220:223]\n\t" \
    "v_mfma_f32_16x16x32_bf16 v[220:223], v[172:175], v[228:231], v[220:223]\n\t" \
    "v_mfma_f32_16x16x32_bf16 v[212:215], v[244:247], v[224:227], v[212:215]\n\t" \
    "v_mfma_f32_16x16x32_bf16 v[212:215], v[248:251], v[228:231], v[212:215]\n\t" \
    "s_waitcnt lgkmcnt(2)\n\t" \
    "v_mfma_f32_16x16x32_bf16 v[204:207], v[144:147], v[232:235], v[204:207]\n\t" \
    "ds_read_b128 v[224:227], v253 offset:19456\n\t" \
    "v_mov_b64 v[244:245], 0\n\t" \
    "s_waitcnt lgkmcnt(2)\n\t" \
    "v_mfma_f32_16x16x32_bf16 v[204:207], v[148:151], v[236:239], v[204:207]\n\t" \
    "ds_read_b128 v[228:231], v253 offset:34816\n\t" \
    "v_mov_b64 v[246:247], 0\n\t" \
    "v_mfma_f32_16x16x32_bf16 v[208:211], v[152:155], v[232:235], v[208:211]\n\t" \
    "v_mov_b64 v[248:249], 0\n\t" \
    "v_mfma_f32_16x16x32_bf16 v[208:211], v[156:159], v[236:239], v[208:211]\n\t" \
    "v_mov_b64 v[250:251], 0\n\t" \
    "v_mfma_f32_16x16x32_bf16 v[200:203], v[136:139], v[232:235], v[200:203]\n\t" \
    "v_mfma_f32_16x16x32_bf16 v[200:203], v[140:143], v[236:239], v[200:203]\n\t" \
    "s_waitcnt lgkmcnt(2)\n\t" \
    "v_mfma_f32_16x16x32_bf16 v[204:207], v[184:187], v[240:243], v[204:207]\n\t" \
    "ds_read_b128 v[232:235], v253 offset:35840\n\t" \
    "s_waitcnt lgkmcnt(2)\n\t" \
    "v_mfma_f32_16x16x32_bf16 v[204:207], v[188:191], v[224:227], v[204:207]\n\t" \
    "ds_read_b128 v[236:239], v253 offset:38912\n\t" \
    "v_mfma_f32_16x16x32_bf16 v[208:211], v[192:195], v[240:243], v[208:211]\n\t" \
    "v_mfma_f32_16x16x32_bf16 v[208:211], v[196:199], v[224:227], v[208:211]\n\t" \
    "v_mfma_f32_16x16x32_bf16 v[200:203], v[176:179], v[240:243], v[200:203]\n\t" \
    "v_mfma_f32_16x16x32_bf16 v[200:203], v[180:183], v[224:227], v[200:203]\n\t" \
    "ds_read_b128 v[240:243], v253 offset:39936\n\t" \
    "s_waitcnt lgkmcnt(3)\n\t" \
    "v_mfma_f32_16x16x32_bf16 v[216:219], v[144:147], v[228:231], v[216:219]\n\t" \
    "s_waitcnt lgkmcnt(2)\n\t" \
    "v_mfma_f32_16x16x32_bf16 v[216:219], v[148:151], v[232:235], v[216:219]\n\t" \
    "v_cvt_pk_bf16_f32 v204, v204, v208\n\t" \
    "v_cvt_pk_bf16_f32 v205, v205, v209\n\t" \
    "s_waitcnt lgkmcnt(1)\n\t" \
    "v_mfma_f32_16x16x32_bf16 v[216:219], v[184:187], v[236:239], v[216:219]\n\t" \
    "v_cvt_pk_bf16_f32 v206, v206, v210\n\t" \
    "v_cvt_pk_bf16_f32 v207, v207, v211\n\t" \
    "s_waitcnt lgkmcnt(0)\n\t" \
    "v_mfma_f32_16x16x32_bf16 v[216:219], v[188:191], v[240:243], v[216:219]\n\t" \
    "ds_write_b128 v252, v[204:207] offset:0\n\t" \
    "v_mfma_f32_16x16x32_bf16 v[220:223], v[152:155], v[228:231], v[220:223]\n\t" \
    "v_mfma_f32_16x16x32_bf16 v[220:223], v[156:159], v[232:235], v[220:223]\n\t" \
    "v_add_u32 v252, %[qs], v253\n\t" \
    "v_cvt_pk_bf16_f32 v200, v200, v201\n\t" \
    "v_mfma_f32_16x16x32_bf16 v[220:223], v[192:195], v[236:239], v[220:223]\n\t" \
    "v_cvt_pk_bf16_f32 v201, v202, v203\n\t" \
    "ds_write_b64 v252, v[200:201] offset:0\n\t" \
    "v_mfma_f32_16x16x32_bf16 v[220:223], v[196:199], v[240:243], v[220:223]\n\t" \
    "v_mfma_f32_16x16x32_bf16 v[212:215], v[136:139], v[228:231], v[212:215]\n\t" \
    "v_mov_b64 v[208:209], 0\n\t" \
    "v_mfma_f32_16x16x32_bf16 v[212:215], v[140:143], v[232:235], v[212:215]\n\t" \
    "v_mov_b64 v[210:211], 0\n\t" \
    "v_mfma_f32_16x16x32_bf16 v[212:215], v[176:179], v[236:239], v[212:215]\n\t" \
    "v_add_u32 v252, %[q2], v253\n\t" \
    "v_cvt_pk_bf16_f32 v216, v216, v220\n\t" \
    "v_mfma_f32_16x16x32_bf16 v[212:215], v[180:183], v[240:243], v[212:215]\n\t" \
    "v_cvt_pk_bf16_f32 v217, v217, v221\n\t" \
    "v_cvt_pk_bf16_f32 v218, v218, v222\n\t" \
    "v_cvt_pk_bf16_f32 v219, v219, v223\n\t" \
    "ds_write_b128 v252, v[216:219] offset:6144\n\t" \
    "v_add_u32 v252, %[qs], v253\n\t" \
    "s_nop 2\n\t" \
    "v_cvt_pk_bf16_f32 v212, v212, v213\n\t" \
    "v_cvt_pk_bf16_f32 v213, v214, v215\n\t" \
    "ds_write_b64 v252, v[212:213] offset:6144\n\t" \
    "s_setprio 2\n\t" \
    "s_waitcnt vmcnt(0) lgkmcnt(0)\n\t" \
    "s_barrier \n\t" \
    "ds_read_b128 v[224:227], v253 offset:40960\n\t" \
    "ds_read_b128 v[228:231], v253 offset:41984\n\t" \
    "ds_read_b128 v[232:235], v253 offset:43008\n\t" \
    "ds_read_b128 v[236:239], v253 offset:44032\n\t" \
    "ds_read_b128 v[240:243], v253 offset:45056\n\t" \
    "ds_read_b128 v[200:203], v254 offset:0\n\t" \
    "ds_read_b128 v[204:207], v254 offset:1024\n\t" \
    "ds_read_b128 v[216:219], v254 offset:2048\n\t" \
    "ds_read_b128 v[220:223], v254 offset:3072\n\t" \
    "v_bfe_i32 v212, v253, 5, 1\n\t" \
    "v_and_b32 v212, 0xaaaa, v212\n\t" \
    "v_or_b32 v212, 0x4444, v212\n\t" \
    "v_and_b32 v213, 0xffff, v255\n\t" \
    "v_lshrrev_b32 v214, 16, v255\n\t" \
    "s_waitcnt lgkmcnt(8)\n\t" \
    "v_smfmac_f32_16x16x64_bf16 v[244:247], v[224:227], v[0:7], v212\n\t" \
    "v_smfmac_f32_16x16x64_bf16 v[248:251], v[224:227], v[8:15], v212\n\t" \
    "s_waitcnt lgkmcnt(2)\n\t" \
    "v_smfmac_f32_16x16x64_bf16 v[208:211], v[224:227], v[200:207], v212\n\t" \
    "ds_read_b128 v[200:203], v254 offset:4096\n\t" \
    "ds_read_b128 v[204:207], v254 offset:5120\n\t" \
    "ds_read_b128 v[224:227], v253 offset:46080\n\t" \
    "v_smfmac_f32_16x16x64_bf16 v[208:211], v[228:231], v[16:23], v212\n\t" \
    "v_smfmac_f32_16x16x64_bf16 v[244:247], v[228:231], v[24:31], v212\n\t" \
    "v_smfmac_f32_16x16x64_bf16 v[248:251], v[228:231], v[32:39], v212\n\t" \
    "ds_read_b128 v[228:231], v253 offset:47104\n\t" \
    "s_waitcnt lgkmcnt(4)\n\t" \
    "v_smfmac_f32_16x16x64_bf16 v[208:211], v[232:235], v[216:223], v212\n\t" \
    "ds_read_b128 v[216:219], v254 offset:6144\n\t" \
    "ds_read_b128 v[220:223], v254 offset:7168\n\t" \
    "v_smfmac_f32_16x16x64_bf16 v[244:247], v[232:235], v[40:47], v212\n\t" \
    "v_smfmac_f32_16x16x64_bf16 v[248:251], v[232:235], v[48:55], v212\n\t" \
    "ds_read_b128 v[232:235], v253 offset:48128\n\t" \
    "v_smfmac_f32_16x16x64_bf16 v[208:211], v[236:239], v[56:63], v212\n\t" \
    "v_smfmac_f32_16x16x64_bf16 v[244:247], v[236:239], v[64:71], v212\n\t" \
    "v_smfmac_f32_16x16x64_bf16 v[248:251], v[236:239], v[72:79], v212\n\t" \
    "ds_read_b128 v[236:239], v253 offset:49152\n\t" \
    "s_waitcnt lgkmcnt(6)\n\t" \
    "v_smfmac_f32_16x16x64_bf16 v[208:211], v[240:243], v[200:207], v212\n\t" \
    "ds_read_b128 v[200:203], v254 offset:8192\n\t" \
    "ds_read_b128 v[204:207], v254 offset:9216\n\t" \
    "v_smfmac_f32_16x16x64_bf16 v[244:247], v[240:243], v[80:87], v212\n\t" \
    "v_smfmac_f32_16x16x64_bf16 v[248:251], v[240:243], v[88:95], v212\n\t" \
    "ds_read_b128 v[240:243], v253 offset:50176\n\t" \
    "s_waitcnt lgkmcnt(8)\n\t" \
    "v_smfmac_f32_16x16x64_bf16 v[208:211], v[224:227], v[96:103], v212\n\t" \
    "v_smfmac_f32_16x16x64_bf16 v[244:247], v[224:227], v[104:111], v212\n\t" \
    "v_smfmac_f32_16x16x64_bf16 v[248:251], v[224:227], v[112:119], v212\n\t" \
    "s_waitcnt lgkmcnt(5)\n\t" \
    "v_smfmac_f32_16x16x64_bf16 v[208:211], v[228:231], v[216:223], v212\n\t" \
    "s_waitcnt lgkmcnt(4)\n\t" \
    "v_smfmac_f32_16x16x64_bf16 v[208:211], v[232:235], v[136:143], v212\n\t" \
    "s_waitcnt lgkmcnt(1)\n\t" \
    "v_smfmac_f32_16x16x64_bf16 v[208:211], v[236:239], v[200:207], v212\n\t" \
    "s_waitcnt lgkmcnt(0)\n\t" \
    "v_smfmac_f32_16x16x64_bf16 v[208:211], v[240:243], v[176:183], v212\n\t" \
    "v_smfmac_f32_16x16x64_bf16 v[244:247], v[228:231], v[120:127], v212\n\t" \
    "v_smfmac_f32_16x16x64_bf16 v[244:247], v[232:235], v[144:151], v212\n\t" \
    "v_smfmac_f32_16x16x64_bf16 v[244:247], v[236:239], v[160:167], v212\n\t" \
    "v_add_f32 v208, v208, v210\n\t" \
    "v_add_f32 v209, v209, v211\n\t" \
    "v_cvt_pk_bf16_f32 v208, v208, v209\n\t" \
    "v_smfmac_f32_16x16x64_bf16 v[244:247], v[240:243], v[184:191], v212\n\t" \
    "ds_write_b16 v213, v208 offset:0\n\t" \
    "ds_write_b16_d16_hi v213, v208 offset:16\n\t" \
    "v_smfmac_f32_16x16x64_bf16 v[248:251], v[228:231], v[128:135], v212\n\t" \
    "v_smfmac_f32_16x16x64_bf16 v[248:251], v[232:235], v[152:159], v212\n\t" \
    "v_smfmac_f32_16x16x64_bf16 v[248:251], v[236:239], v[168:175], v212\n\t" \
    "v_add_f32 v244, v244, v246\n\t" \
    "v_add_f32 v245, v245, v247\n\t" \
    "v_smfmac_f32_16x16x64_bf16 v[248:251], v[240:243], v[192:199], v212\n\t" \
    "s_nop 7\n\t" \
    "v_add_f32 v248, v248, v250\n\t" \
    "v_add_f32 v249, v249, v251\n\t" \
    "v_cvt_pk_bf16_f32 v244, v244, v248\n\t" \
    "v_cvt_pk_bf16_f32 v245, v245, v249\n\t" \
    "ds_write2_b32 v214, v244, v245 offset1:4\n\t" \
    "s_setprio 0\n\t" \
    "s_waitcnt lgkmcnt(0)\n\t" \
    "s_barrier \n\t" \
    "ds_read_b128 v[224:227], v253 offset:0\n\t" \
    "ds_read_b128 v[244:247], v254 offset:0\n\t" \
    "ds_read_b128 v[228:231], v253 offset:1024\n\t" \
    "ds_read_b128 v[232:235], v253 offset:20480\n\t" \
    "ds_read_b128 v[236:239], v253 offset:21504\n\t" \
    "ds_read_b128 v[248:251], v254 offset:1024\n\t" \
    "s_add_u32 s40, s40, 1\n\t" \
    "s_cmp_lg_u32 s40, 64\n\t" \
    "s_cbranch_scc1 .Ls47_%=\n\t" \
    "s_branch .Lend%=\n\t" \
    ".Ls3_%=:\n\t" \
    "v_add_u32 v244, 0x18000, v243\n\t" \
    "s_waitcnt vmcnt(12) lgkmcnt(0)\n\t" \
    "s_barrier \n\t" \
    "s_add_u32 m0, %[wm], 0x12000\n\t" \
    "s_mov_b32 s44, 0x1e0000\n\t" \
    "buffer_load_dwordx4 v240, %[wr], s44 offen offset:0 nt lds\n\t" \
    "buffer_load_dwordx4 v241, %[wr], s44 offen offset:1024 nt lds\n\t" \
    "buffer_load_dwordx4 v242, %[wr], s44 offen offset:2048 nt lds\n\t" \
    "ds_read_b64_tr_b16 v[208:209], v244 offset:0\n\t" \
    "ds_read_b64_tr_b16 v[210:211], v244 offset:1536\n\t" \
    "ds_read_b64_tr_b16 v[40:41], v244 offset:32\n\t" \
    "ds_read_b64_tr_b16 v[42:43], v244 offset:1568\n\t" \
    "ds_read_b64_tr_b16 v[48:49], v244 offset:64\n\t" \
    "ds_read_b64_tr_b16 v[50:51], v244 offset:1600\n\t" \
    "v_add_u32 v244, 0x1e000, v243\n\t" \
    "s_waitcnt vmcnt(12) lgkmcnt(0)\n\t" \
    "s_barrier \n\t" \
    "s_add_u32 m0, %[wm], 0x18000\n\t" \
    "s_mov_b32 s44, 0x240000\n\t" \
    "buffer_load_dwordx4 v240, %[wr], s44 offen offset:0 nt lds\n\t" \
    "buffer_load_dwordx4 v241, %[wr], s44 offen offset:1024 nt lds\n\t" \
    "buffer_load_dwordx4 v242, %[wr], s44 offen offset:2048 nt lds\n\t" \
    "ds_read_b64_tr_b16 v[212:213], v244 offset:0\n\t" \
    "ds_read_b64_tr_b16 v[214:215], v244 offset:1536\n\t" \
    "ds_read_b64_tr_b16 v[44:45], v244 offset:32\n\t" \
    "ds_read_b64_tr_b16 v[46:47], v244 offset:1568\n\t" \
    "ds_read_b64_tr_b16 v[52:53], v244 offset:64\n\t" \
    "ds_read_b64_tr_b16 v[54:55], v244 offset:1600\n\t" \
    "v_add_u32 v244, 0x0, v243\n\t" \
    "s_waitcnt vmcnt(12) lgkmcnt(0)\n\t" \
    "s_barrier \n\t" \
    "s_add_u32 m0, %[wm], 0x1e000\n\t" \
    "s_mov_b32 s44, 0x2a0000\n\t" \
    "buffer_load_dwordx4 v240, %[wr], s44 offen offset:0 nt lds\n\t" \
    "buffer_load_dwordx4 v241, %[wr], s44 offen offset:1024 nt lds\n\t" \
    "buffer_load_dwordx4 v242, %[wr], s44 offen offset:2048 nt lds\n\t" \
    "ds_read_b64_tr_b16 v[56:57], v244 offset:0\n\t" \
    "ds_read_b64_tr_b16 v[58:59], v244 offset:1536\n\t" \
    "ds_read_b64_tr_b16 v[64:65], v244 offset:32\n\t" \
    "ds_read_b64_tr_b16 v[66:67], v244 offset:1568\n\t" \
    "ds_read_b64_tr_b16 v[72:73], v244 offset:64\n\t" \
    "ds_read_b64_tr_b16 v[74:75], v244 offset:1600\n\t" \
    "v_add_u32 v244, 0x6000, v243\n\t" \
    "s_waitcnt vmcnt(12) lgkmcnt(0)\n\t" \
    "s_barrier \n\t" \
    "s_add_u32 m0, %[wm], 0x0\n\t" \
    "s_mov_b32 s44, 0x300000\n\t" \
    "buffer_load_dwordx4 v240, %[wr], s44 offen offset:0 nt lds\n\t" \
    "buffer_load_dwordx4 v241, %[wr], s44 offen offset:1024 nt lds\n\t" \
    "buffer_load_dwordx4 v242, %[wr], s44 offen offset:2048 nt lds\n\t" \
    "ds_read_b64_tr_b16 v[60:61], v244 offset:0\n\t" \
    "ds_read_b64_tr_b16 v[62:63], v244 offset:1536\n\t" \
    "ds_read_b64_tr_b16 v[68:69], v244 offset:32\n\t" \
    "ds_read_b64_tr_b16 v[70:71], v244 offset:1568\n\t" \
    "ds_read_b64_tr_b16 v[76:77], v244 offset:64\n\t" \
    "ds_read_b64_tr_b16 v[78:79], v244 offset:1600\n\t" \
    "v_add_u32 v244, 0xc000, v243\n\t" \
    "s_waitcnt vmcnt(12) lgkmcnt(0)\n\t" \
    "s_barrier \n\t" \
    "s_add_u32 m0, %[wm], 0x6000\n\t" \
    "s_mov_b32 s44, 0x360000\n\t" \
    "buffer_load_dwordx4 v240, %[wr], s44 offen offset:0 nt lds\n\t" \
    "buffer_load_dwordx4 v241, %[wr], s44 offen offset:1024 nt lds\n\t" \
    "buffer_load_dwordx4 v242, %[wr], s44 offen offset:2048 nt lds\n\t" \
    "ds_read_b64_tr_b16 v[216:217], v244 offset:0\n\t" \
    "ds_read_b64_tr_b16 v[218:219], v244 offset:1536\n\t" \
    "ds_read_b64_tr_b16 v[80:81], v244 offset:32\n\t" \
    "ds_read_b64_tr_b16 v[82:83], v244 offset:1568\n\t" \
    "ds_read_b64_tr_b16 v[88:89], v244 offset:64\n\t" \
    "ds_read_b64_tr_b16 v[90:91], v244 offset:1600\n\t" \
    "v_add_u32 v244, 0x12000, v243\n\t" \
    "s_waitcnt vmcnt(12) lgkmcnt(0)\n\t" \
    "s_barrier \n\t" \
    "s_add_u32 m0, %[wm], 0xc000\n\t" \
    "s_mov_b32 s44, 0x3c0000\n\t" \
    "buffer_load_dwordx4 v240, %[wr], s44 offen offset:0 nt lds\n\t" \
    "buffer_load_dwordx4 v241, %[wr], s44 offen offset:1024 nt lds\n\t" \
    "buffer_load_dwordx4 v242, %[wr], s44 offen offset:2048 nt lds\n\t" \
    "ds_read_b64_tr_b16 v[220:221], v244 offset:0\n\t" \
    "ds_read_b64_tr_b16 v[222:223], v244 offset:1536\n\t" \
    "ds_read_b64_tr_b16 v[84:85], v244 offset:32\n\t" \
    "ds_read_b64_tr_b16 v[86:87], v244 offset:1568\n\t" \
    "ds_read_b64_tr_b16 v[92:93], v244 offset:64\n\t" \
    "ds_read_b64_tr_b16 v[94:95], v244 offset:1600\n\t" \
    "v_add_u32 v244, 0x18000, v243\n\t" \
    "s_waitcnt vmcnt(12) lgkmcnt(0)\n\t" \
    "s_barrier \n\t" \
    "s_add_u32 m0, %[wm], 0x12000\n\t" \
    "s_mov_b32 s44, 0x420000\n\t" \
    "buffer_load_dwordx4 v240, %[wr], s44 offen offset:0 nt lds\n\t" \
    "buffer_load_dwordx4 v241, %[wr], s44 offen offset:1024 nt lds\n\t" \
    "buffer_load_dwordx4 v242, %[wr], s44 offen offset:2048 nt lds\n\t" \
    "ds_read_b64_tr_b16 v[96:97], v244 offset:0\n\t" \
    "ds_read_b64_tr_b16 v[98:99], v244 offset:1536\n\t" \
    "ds_read_b64_tr_b16 v[104:105], v244 offset:32\n\t" \
    "ds_read_b64_tr_b16 v[106:107], v244 offset:1568\n\t" \
    "ds_read_b64_tr_b16 v[112:113], v244 offset:64\n\t" \
    "ds_read_b64_tr_b16 v[114:115], v244 offset:1600\n\t" \
    "v_add_u32 v244, 0x1e000, v243\n\t" \
    "s_waitcnt vmcnt(12) lgkmcnt(0)\n\t" \
    "s_barrier \n\t" \
    "s_add_u32 m0, %[wm], 0x18000\n\t" \
    "s_mov_b32 s44, 0x480000\n\t" \
    "buffer_load_dwordx4 v240, %[wr], s44 offen offset:0 nt lds\n\t" \
    "buffer_load_dwordx4 v241, %[wr], s44 offen offset:1024 nt lds\n\t" \
    "buffer_load_dwordx4 v242, %[wr], s44 offen offset:2048 nt lds\n\t" \
    "ds_read_b64_tr_b16 v[100:101], v244 offset:0\n\t" \
    "ds_read_b64_tr_b16 v[102:103], v244 offset:1536\n\t" \
    "ds_read_b64_tr_b16 v[108:109], v244 offset:32\n\t" \
    "ds_read_b64_tr_b16 v[110:111], v244 offset:1568\n\t" \
    "ds_read_b64_tr_b16 v[116:117], v244 offset:64\n\t" \
    "ds_read_b64_tr_b16 v[118:119], v244 offset:1600\n\t" \
    "v_add_u32 v244, 0x0, v243\n\t" \
    "s_waitcnt vmcnt(12) lgkmcnt(0)\n\t" \
    "s_barrier \n\t" \
    "v_mov_b32 v246, 0\n\t" \
    "buffer_load_dword v246, v246, s[60:63], %[mo] offen sc1\n\t" \
    "s_add_u32 m0, %[wm], 0x1e000\n\t" \
    "s_mov_b32 s44, 0x4e0000\n\t" \
    "buffer_load_dwordx4 v240, %[wr], s44 offen offset:0 nt lds\n\t" \
    "buffer_load_dwordx4 v241, %[wr], s44 offen offset:1024 nt lds\n\t" \
    "buffer_load_dwordx4 v242, %[wr], s44 offen offset:2048 nt lds\n\t" \
    "ds_read_b64_tr_b16 v[224:225], v244 offset:0\n\t" \
    "ds_read_b64_tr_b16 v[226:227], v244 offset:1536\n\t" \
    "ds_read_b64_tr_b16 v[120:121], v244 offset:32\n\t" \
    "ds_read_b64_tr_b16 v[122:123], v244 offset:1568\n\t" \
    "ds_read_b64_tr_b16 v[128:129], v244 offset:64\n\t" \
    "ds_read_b64_tr_b16 v[130:131], v244 offset:1600\n\t" \
    "v_add_u32 v244, 0x6000, v243\n\t" \
    "s_waitcnt vmcnt(12) lgkmcnt(0)\n\t" \
    "s_barrier \n\t" \
    "s_add_u32 m0, %[wm], 0x0\n\t" \
    "s_mov_b32 s44, 0x540000\n\t" \
    "buffer_load_dwordx4 v240, %[wr], s44 offen offset:0 nt lds\n\t" \
    "buffer_load_dwordx4 v241, %[wr], s44 offen offset:1024 nt lds\n\t" \
    "buffer_load_dwordx4 v242, %[wr], s44 offen offset:2048 nt lds\n\t" \
    "ds_read_b64_tr_b16 v[228:229], v244 offset:0\n\t" \
    "ds_read_b64_tr_b16 v[230:231], v244 offset:1536\n\t" \
    "ds_read_b64_tr_b16 v[124:125], v244 offset:32\n\t" \
    "ds_read_b64_tr_b16 v[126:127], v244 offset:1568\n\t" \
    "ds_read_b64_tr_b16 v[132:133], v244 offset:64\n\t" \
    "ds_read_b64_tr_b16 v[134:135], v244 offset:1600\n\t" \
    "v_add_u32 v244, 0xc000, v243\n\t" \
    "s_waitcnt vmcnt(12) lgkmcnt(0)\n\t" \
    "s_barrier \n\t" \
    "s_add_u32 m0, %[wm], 0x6000\n\t" \
    "s_mov_b32 s44, 0x5a0000\n\t" \
    "buffer_load_dwordx4 v240, %[wr], s44 offen offset:0 nt lds\n\t" \
    "buffer_load_dwordx4 v241, %[wr], s44 offen offset:1024 nt lds\n\t" \
    "buffer_load_dwordx4 v242, %[wr], s44 offen offset:2048 nt lds\n\t" \
    "ds_read_b64_tr_b16 v[136:137], v244 offset:0\n\t" \
    "ds_read_b64_tr_b16 v[138:139], v244 offset:1536\n\t" \
    "ds_read_b64_tr_b16 v[144:145], v244 offset:32\n\t" \
    "ds_read_b64_tr_b16 v[146:147], v244 offset:1568\n\t" \
    "ds_read_b64_tr_b16 v[152:153], v244 offset:64\n\t" \
    "ds_read_b64_tr_b16 v[154:155], v244 offset:1600\n\t" \
    "v_add_u32 v244, 0x12000, v243\n\t" \
    "s_waitcnt vmcnt(12) lgkmcnt(0)\n\t" \
    "s_barrier \n\t" \
    "s_add_u32 m0, %[wm], 0xc000\n\t" \
    "s_mov_b32 s44, 0x600000\n\t" \
    "buffer_load_dwordx4 v240, %[wr], s44 offen offset:0 nt lds\n\t" \
    "buffer_load_dwordx4 v241, %[wr], s44 offen offset:1024 nt lds\n\t" \
    "buffer_load_dwordx4 v242, %[wr], s44 offen offset:2048 nt lds\n\t" \
    "ds_read_b64_tr_b16 v[140:141], v244 offset:0\n\t" \
    "ds_read_b64_tr_b16 v[142:143], v244 offset:1536\n\t" \
    "ds_read_b64_tr_b16 v[148:149], v244 offset:32\n\t" \
    "ds_read_b64_tr_b16 v[150:151], v244 offset:1568\n\t" \
    "ds_read_b64_tr_b16 v[156:157], v244 offset:64\n\t" \
    "ds_read_b64_tr_b16 v[158:159], v244 offset:1600\n\t" \
    "v_add_u32 v244, 0x18000, v243\n\t" \
    "s_waitcnt vmcnt(12) lgkmcnt(0)\n\t" \
    "s_barrier \n\t" \
    "s_add_u32 m0, %[wm], 0x12000\n\t" \
    "s_mov_b32 s44, 0x660000\n\t" \
    "buffer_load_dwordx4 v240, %[wr], s44 offen offset:0 nt lds\n\t" \
    "buffer_load_dwordx4 v241, %[wr], s44 offen offset:1024 nt lds\n\t" \
    "buffer_load_dwordx4 v242, %[wr], s44 offen offset:2048 nt lds\n\t" \
    "ds_read_b64_tr_b16 v[232:233], v244 offset:0\n\t" \
    "ds_read_b64_tr_b16 v[234:235], v244 offset:1536\n\t" \
    "ds_read_b64_tr_b16 v[160:161], v244 offset:32\n\t" \
    "ds_read_b64_tr_b16 v[162:163], v244 offset:1568\n\t" \
    "ds_read_b64_tr_b16 v[168:169], v244 offset:64\n\t" \
    "ds_read_b64_tr_b16 v[170:171], v244 offset:1600\n\t" \
    "v_add_u32 v244, 0x1e000, v243\n\t" \
    "s_waitcnt vmcnt(12) lgkmcnt(0)\n\t" \
    "s_barrier \n\t" \
    ".Ls31_%=:\n\t" \
    "v_readfirstlane_b32 s43, v246\n\t" \
    "s_nop 0\n\t" \
    "s_cmp_lg_u32 s43, -1\n\t" \
    "s_cbranch_scc1 .Ls32_%=\n\t" \
    "s_cmp_eq_u32 s41, 0\n\t" \
    "s_cbranch_scc1 .Ls32_%=\n\t" \
    "s_sub_u32 s41, s41, 1\n\t" \
    "s_sleep 4\n\t" \
    "v_mov_b32 v246, 0\n\t" \
    "buffer_load_dword v246, v246, s[60:63], %[mo] offen sc1\n\t" \
    "s_waitcnt vmcnt(0)\n\t" \
    "s_branch .Ls31_%=\n\t" \
    ".Ls32_%=:\n\t" \
    "v_mov_b32 v246, 0x8000\n\t" \
    "s_mov_b64 exec, 1\n\t" \
    "buffer_store_dword v246, v246, s[60:63], %[mo] offen sc1\n\t" \
    "s_mov_b64 exec, -1\n\t" \
    "s_and_b32 s43, s43, 0x2000\n\t" \
    "s_xor_b32 s43, s43, %[lx]\n\t" \
    "s_add_u32 s60, s60, s43\n\t" \
    "s_addc_u32 s61, s61, 0\n\t" \
    "s_add_u32 m0, %[wm], 0x18000\n\t" \
    "s_mov_b32 s44, 0x6c0000\n\t" \
    "buffer_load_dwordx4 v240, %[wr], s44 offen offset:0 nt lds\n\t" \
    "buffer_load_dwordx4 v241, %[wr], s44 offen offset:1024 nt lds\n\t" \
    "buffer_load_dwordx4 v242, %[wr], s44 offen offset:2048 nt lds\n\t" \
    "ds_read_b64_tr_b16 v[236:237], v244 offset:0\n\t" \
    "ds_read_b64_tr_b16 v[238:239], v244 offset:1536\n\t" \
    "ds_read_b64_tr_b16 v[164:165], v244 offset:32\n\t" \
    "ds_read_b64_tr_b16 v[166:167], v244 offset:1568\n\t" \
    "ds_read_b64_tr_b16 v[172:173], v244 offset:64\n\t" \
    "ds_read_b64_tr_b16 v[174:175], v244 offset:1600\n\t" \
    "v_add_u32 v244, 0x0, v243\n\t" \
    "s_waitcnt vmcnt(12) lgkmcnt(0)\n\t" \
    "s_barrier \n\t" \
    "s_add_u32 m0, %[wm], 0x1e000\n\t" \
    "s_mov_b32 s44, 0x720000\n\t" \
    "buffer_load_dwordx4 v240, %[wr], s44 offen offset:0 nt lds\n\t" \
    "buffer_load_dwordx4 v241, %[wr], s44 offen offset:1024 nt lds\n\t" \
    "buffer_load_dwordx4 v242, %[wr], s44 offen offset:2048 nt lds\n\t" \
    "ds_read_b64_tr_b16 v[176:177], v244 offset:0\n\t" \
    "ds_read_b64_tr_b16 v[178:179], v244 offset:1536\n\t" \
    "ds_read_b64_tr_b16 v[184:185], v244 offset:32\n\t" \
    "ds_read_b64_tr_b16 v[186:187], v244 offset:1568\n\t" \
    "ds_read_b64_tr_b16 v[192:193], v244 offset:64\n\t" \
    "ds_read_b64_tr_b16 v[194:195], v244 offset:1600\n\t" \
    "v_add_u32 v244, 0x6000, v243\n\t" \
    "s_waitcnt vmcnt(12) lgkmcnt(0)\n\t" \
    "s_barrier \n\t" \
    "ds_read_b64_tr_b16 v[180:181], v244 offset:0\n\t" \
    "ds_read_b64_tr_b16 v[182:183], v244 offset:1536\n\t" \
    "ds_read_b64_tr_b16 v[188:189], v244 offset:32\n\t" \
    "ds_read_b64_tr_b16 v[190:191], v244 offset:1568\n\t" \
    "ds_read_b64_tr_b16 v[196:197], v244 offset:64\n\t" \
    "ds_read_b64_tr_b16 v[198:199], v244 offset:1600\n\t" \
    "v_add_u32 v244, 0xc000, v243\n\t" \
    "s_waitcnt vmcnt(9) lgkmcnt(0)\n\t" \
    "s_barrier \n\t" \
    "ds_read_b64_tr_b16 v[200:201], v244 offset:0\n\t" \
    "ds_read_b64_tr_b16 v[202:203], v244 offset:1536\n\t" \
    "ds_read_b64_tr_b16 v[0:1], v244 offset:32\n\t" \
    "ds_read_b64_tr_b16 v[2:3], v244 offset:1568\n\t" \
    "ds_read_b64_tr_b16 v[8:9], v244 offset:64\n\t" \
    "ds_read_b64_tr_b16 v[10:11], v244 offset:1600\n\t" \
    "v_add_u32 v244, 0x12000, v243\n\t" \
    "s_waitcnt vmcnt(6) lgkmcnt(0)\n\t" \
    "s_barrier \n\t" \
    "ds_read_b64_tr_b16 v[204:205], v244 offset:0\n\t" \
    "ds_read_b64_tr_b16 v[206:207], v244 offset:1536\n\t" \
    "ds_read_b64_tr_b16 v[4:5], v244 offset:32\n\t" \
    "ds_read_b64_tr_b16 v[6:7], v244 offset:1568\n\t" \
    "ds_read_b64_tr_b16 v[12:13], v244 offset:64\n\t" \
    "ds_read_b64_tr_b16 v[14:15], v244 offset:1600\n\t" \
    "v_add_u32 v244, 0x18000, v243\n\t" \
    "s_waitcnt vmcnt(3) lgkmcnt(0)\n\t" \
    "s_barrier \n\t" \
    "ds_read_b64_tr_b16 v[16:17], v244 offset:0\n\t" \
    "ds_read_b64_tr_b16 v[18:19], v244 offset:1536\n\t" \
    "ds_read_b64_tr_b16 v[24:25], v244 offset:32\n\t" \
    "ds_read_b64_tr_b16 v[26:27], v244 offset:1568\n\t" \
    "ds_read_b64_tr_b16 v[32:33], v244 offset:64\n\t" \
    "ds_read_b64_tr_b16 v[34:35], v244 offset:1600\n\t" \
    "v_add_u32 v244, 0x1e000, v243\n\t" \
    "s_waitcnt vmcnt(0) lgkmcnt(0)\n\t" \
    "s_barrier \n\t" \
    "ds_read_b64_tr_b16 v[20:21], v244 offset:0\n\t" \
    "ds_read_b64_tr_b16 v[22:23], v244 offset:1536\n\t" \
    "ds_read_b64_tr_b16 v[28:29], v244 offset:32\n\t" \
    "ds_read_b64_tr_b16 v[30:31], v244 offset:1568\n\t" \
    "ds_read_b64_tr_b16 v[36:37], v244 offset:64\n\t" \
    "ds_read_b64_tr_b16 v[38:39], v244 offset:1600\n\t" \
    "s_waitcnt lgkmcnt(0)\n\t" \
    "s_barrier \n\t" \
    "ds_write_b128 v254, v[200:203] offset:55296\n\t" \
    "ds_write_b128 v254, v[204:207] offset:56320\n\t" \
    "ds_write_b128 v254, v[208:211] offset:57344\n\t" \
    "ds_write_b128 v254, v[212:215] offset:58368\n\t" \
    "ds_write_b128 v254, v[216:219] offset:59392\n\t" \
    "ds_write_b128 v254, v[220:223] offset:60416\n\t" \
    "ds_write_b128 v254, v[224:227] offset:61440\n\t" \
    "ds_write_b128 v254, v[228:231] offset:62464\n\t" \
    "ds_write_b128 v254, v[232:235] offset:63488\n\t" \
    "ds_write_b128 v254, v[236:239] offset:64512\n\t" \
    "s_waitcnt vmcnt(0) lgkmcnt(0)\n\t" \
    "s_barrier \n\t" \
    "ds_read_b128 v[208:211], v254 offset:24576\n\t" \
    "ds_read_b128 v[224:227], v253 offset:0\n\t" \
    "ds_read_b128 v[228:231], v253 offset:20480\n\t" \
    "ds_read_b128 v[212:215], v254 offset:25600\n\t" \
    "ds_read_b128 v[232:235], v253 offset:1024\n\t" \
    "ds_read_b128 v[236:239], v253 offset:21504\n\t" \
    "ds_read_b128 v[216:219], v254 offset:26624\n\t" \
    "ds_read_b128 v[240:243], v253 offset:2048\n\t" \
    "ds_read_b128 v[244:247], v253 offset:22528\n\t" \
    "ds_read_b128 v[220:223], v254 offset:27648\n\t" \
    "ds_read_b128 v[248:251], v253 offset:3072\n\t" \
    "s_waitcnt lgkmcnt(9)\n\t" \
    "v_mfma_f32_16x16x32_bf16 v[200:203], v[208:211], v[224:227], 0\n\t" \
    "ds_read_b128 v[224:227], v253 offset:23552\n\t" \
    "s_waitcnt lgkmcnt(9)\n\t" \
    "v_mfma_f32_16x16x32_bf16 v[204:207], v[208:211], v[228:231], 0\n\t" \
    "ds_read_b128 v[228:231], v253 offset:4096\n\t" \
    "ds_read_b128 v[208:211], v254 offset:28672\n\t" \
    "s_waitcnt lgkmcnt(9)\n\t" \
    "v_mfma_f32_16x16x32_bf16 v[200:203], v[212:215], v[232:235], v[200:203]\n\t" \
    "ds_read_b128 v[232:235], v253 offset:24576\n\t" \
    "s_waitcnt lgkmcnt(9)\n\t" \
    "v_mfma_f32_16x16x32_bf16 v[204:207], v[212:215], v[236:239], v[204:207]\n\t" \
    "ds_read_b128 v[236:239], v253 offset:5120\n\t" \
    "ds_read_b128 v[212:215], v254 offset:29696\n\t" \
    "s_waitcnt lgkmcnt(9)\n\t" \
    "v_mfma_f32_16x16x32_bf16 v[200:203], v[216:219], v[240:243], v[200:203]\n\t" \
    "ds_read_b128 v[240:243], v253 offset:25600\n\t" \
    ".Ls33_%=:\n\t" \
    "s_waitcnt lgkmcnt(9)\n\t" \
    "v_mfma_f32_16x16x32_bf16 v[204:207], v[216:219], v[244:247], v[204:207]\n\t" \
    "ds_read_b128 v[244:247], v253 offset:6144\n\t" \
    "ds_read_b128 v[216:219], v254 offset:30720\n\t" \
    "s_waitcnt lgkmcnt(9)\n\t" \
    "v_mfma_f32_16x16x32_bf16 v[200:203], v[220:223], v[248:251], v[200:203]\n\t" \
    "ds_read_b128 v[248:251], v253 offset:26624\n\t" \
    "s_waitcnt lgkmcnt(9)\n\t" \
    "v_mfma_f32_16x16x32_bf16 v[204:207], v[220:223], v[224:227], v[204:207]\n\t" \
    "ds_read_b128 v[224:227], v253 offset:7168\n\t" \
    "ds_read_b128 v[220:223], v254 offset:31744\n\t" \
    "s_waitcnt lgkmcnt(9)\n\t" \
    "v_mfma_f32_16x16x32_bf16 v[200:203], v[208:211], v[228:231], v[200:203]\n\t" \
    "ds_read_b128 v[228:231], v253 offset:27648\n\t" \
    "s_waitcnt lgkmcnt(9)\n\t" \
    "v_mfma_f32_16x16x32_bf16 v[204:207], v[208:211], v[232:235], v[204:207]\n\t" \
    "ds_read_b128 v[232:235], v253 offset:8192\n\t" \
    "ds_read_b128 v[208:211], v254 offset:32768\n\t" \
    "s_waitcnt lgkmcnt(9)\n\t" \
    "v_mfma_f32_16x16x32_bf16 v[200:203], v[212:215], v[236:239], v[200:203]\n\t" \
    "ds_read_b128 v[236:239], v253 offset:28672\n\t" \
    "s_waitcnt lgkmcnt(9)\n\t" \
    "v_mfma_f32_16x16x32_bf16 v[204:207], v[212:215], v[240:243], v[204:207]\n\t" \
    "ds_read_b128 v[240:243], v253 offset:9216\n\t" \
    "ds_read_b128 v[212:215], v254 offset:33792\n\t" \
    "s_waitcnt lgkmcnt(9)\n\t" \
    "v_mfma_f32_16x16x32_bf16 v[200:203], v[216:219], v[244:247], v[200:203]\n\t" \
    "ds_read_b128 v[244:247], v253 offset:29696\n\t" \
    "s_waitcnt lgkmcnt(9)\n\t" \
    "v_mfma_f32_16x16x32_bf16 v[204:207], v[216:219], v[248:251], v[204:207]\n\t" \
    "s_waitcnt lgkmcnt(7)\n\t" \
    "v_mfma_f32_16x16x32_bf16 v[200:203], v[220:223], v[224:227], v[200:203]\n\t" \
    "s_waitcnt lgkmcnt(6)\n\t" \
    "v_mfma_f32_16x16x32_bf16 v[204:207], v[220:223], v[228:231], v[204:207]\n\t" \
    "s_waitcnt lgkmcnt(4)\n\t" \
    "v_mfma_f32_16x16x32_bf16 v[200:203], v[208:211], v[232:235], v[200:203]\n\t" \
    "s_waitcnt lgkmcnt(3)\n\t" \
    "v_mfma_f32_16x16x32_bf16 v[204:207], v[208:211], v[236:239], v[204:207]\n\t" \
    "s_waitcnt lgkmcnt(1)\n\t" \
    "v_mfma_f32_16x16x32_bf16 v[200:203], v[212:215], v[240:243], v[200:203]\n\t" \
    "s_waitcnt lgkmcnt(0)\n\t" \
    "v_mfma_f32_16x16x32_bf16 v[204:207], v[212:215], v[244:247], v[204:207]\n\t" \
    "ds_read_b128 v[224:227], v253 offset:0\n\t" \
    "ds_read_b128 v[244:247], v254 offset:57344\n\t" \
    "ds_read_b128 v[228:231], v253 offset:1024\n\t" \
    "ds_read_b128 v[232:235], v253 offset:20480\n\t" \
    "ds_read_b128 v[236:239], v253 offset:21504\n\t" \
    "ds_read_b128 v[248:251], v254 offset:58368\n\t" \
    "s_nop 1\n\t" \
    "ds_write_b128 v253, v[200:203] offset:40960\n\t" \
    "ds_write_b128 v253, v[204:207] offset:41984\n\t" \
    "s_nop 1\n\t" \
    "s_waitcnt lgkmcnt(7)\n\t" \
    "v_mfma_f32_16x16x32_bf16 v[204:207], v[40:43], v[224:227], 0\n\t" \
    "s_waitcnt lgkmcnt(5)\n\t" \
    "v_mfma_f32_16x16x32_bf16 v[204:207], v[44:47], v[228:231], v[204:207]\n\t" \
    "ds_read_b128 v[240:243], v253 offset:2048\n\t" \
    "v_mfma_f32_16x16x32_bf16 v[208:211], v[48:51], v[224:227], 0\n\t" \
    "v_mfma_f32_16x16x32_bf16 v[208:211], v[52:55], v[228:231], v[208:211]\n\t" \
    "v_mfma_f32_16x16x32_bf16 v[200:203], v[244:247], v[224:227], 0\n\t" \
    "s_waitcnt lgkmcnt(3)\n\t" \
    "v_mfma_f32_16x16x32_bf16 v[200:203], v[248:251], v[228:231], v[200:203]\n\t" \
    "v_mfma_f32_16x16x32_bf16 v[216:219], v[40:43], v[232:235], 0\n\t" \
    "ds_read_b128 v[224:227], v253 offset:3072\n\t" \
    "v_mfma_f32_16x16x32_bf16 v[216:219], v[44:47], v[236:239], v[216:219]\n\t" \
    "ds_read_b128 v[228:231], v253 offset:22528\n\t" \
    "v_mfma_f32_16x16x32_bf16 v[220:223], v[48:51], v[232:235], 0\n\t" \
    "v_mfma_f32_16x16x32_bf16 v[220:223], v[52:55], v[236:239], v[220:223]\n\t" \
    "v_mfma_f32_16x16x32_bf16 v[212:215], v[244:247], v[232:235], 0\n\t" \
    "ds_read_b128 v[244:247], v254 offset:59392\n\t" \
    "v_mfma_f32_16x16x32_bf16 v[212:215], v[248:251], v[236:239], v[212:215]\n\t" \
    "ds_read_b128 v[248:251], v254 offset:60416\n\t" \
    "s_waitcnt lgkmcnt(4)\n\t" \
    "v_mfma_f32_16x16x32_bf16 v[204:207], v[64:67], v[240:243], v[204:207]\n\t" \
    "ds_read_b128 v[232:235], v253 offset:23552\n\t" \
    "s_waitcnt lgkmcnt(4)\n\t" \
    "v_mfma_f32_16x16x32_bf16 v[204:207], v[68:71], v[224:227], v[204:207]\n\t" \
    "ds_read_b128 v[236:239], v253 offset:4096\n\t" \
    "v_mfma_f32_16x16x32_bf16 v[208:211], v[72:75], v[240:243], v[208:211]\n\t" \
    "v_mfma_f32_16x16x32_bf16 v[208:211], v[76:79], v[224:227], v[208:211]\n\t" \
    "v_mfma_f32_16x16x32_bf16 v[200:203], v[56:59], v[240:243], v[200:203]\n\t" \
    "v_mfma_f32_16x16x32_bf16 v[200:203], v[60:63], v[224:227], v[200:203]\n\t" \
    "s_waitcnt lgkmcnt(4)\n\t" \
    "v_mfma_f32_16x16x32_bf16 v[216:219], v[64:67], v[228:231], v[216:219]\n\t" \
    "ds_read_b128 v[240:243], v253 offset:5120\n\t" \
    "s_waitcnt lgkmcnt(2)\n\t" \
    "v_mfma_f32_16x16x32_bf16 v[216:219], v[68:71], v[232:235], v[216:219]\n\t" \
    "ds_read_b128 v[224:227], v253 offset:24576\n\t" \
    "v_mfma_f32_16x16x32_bf16 v[220:223], v[72:75], v[228:231], v[220:223]\n\t" \
    "v_mfma_f32_16x16x32_bf16 v[220:223], v[76:79], v[232:235], v[220:223]\n\t" \
    "v_mfma_f32_16x16x32_bf16 v[212:215], v[56:59], v[228:231], v[212:215]\n\t" \
    "v_mfma_f32_16x16x32_bf16 v[212:215], v[60:63], v[232:235], v[212:215]\n\t" \
    "s_waitcnt lgkmcnt(2)\n\t" \
    "v_mfma_f32_16x16x32_bf16 v[204:207], v[80:83], v[236:239], v[204:207]\n\t" \
    "ds_read_b128 v[228:231], v253 offset:25600\n\t" \
    "s_waitcnt lgkmcnt(2)\n\t" \
    "v_mfma_f32_16x16x32_bf16 v[204:207], v[84:87], v[240:243], v[204:207]\n\t" \
    "ds_read_b128 v[232:235], v253 offset:6144\n\t" \
    "v_mfma_f32_16x16x32_bf16 v[208:211], v[88:91], v[236:239], v[208:211]\n\t" \
    "v_mfma_f32_16x16x32_bf16 v[208:211], v[92:95], v[240:243], v[208:211]\n\t" \
    "v_mfma_f32_16x16x32_bf16 v[200:203], v[244:247], v[236:239], v[200:203]\n\t" \
    "v_mfma_f32_16x16x32_bf16 v[200:203], v[248:251], v[240:243], v[200:203]\n\t" \
    "s_waitcnt lgkmcnt(2)\n\t" \
    "v_mfma_f32_16x16x32_bf16 v[216:219], v[80:83], v[224:227], v[216:219]\n\t" \
    "ds_read_b128 v[236:239], v253 offset:7168\n\t" \
    "s_waitcnt lgkmcnt(2)\n\t" \
    "v_mfma_f32_16x16x32_bf16 v[216:219], v[84:87], v[228:231], v[216:219]\n\t" \
    "ds_read_b128 v[240:243], v253 offset:26624\n\t" \
    "v_mfma_f32_16x16x32_bf16 v[220:223], v[88:91], v[224:227], v[220:223]\n\t" \
    "v_mfma_f32_16x16x32_bf16 v[220:223], v[92:95], v[228:231], v[220:223]\n\t" \
    "v_mfma_f32_16x16x32_bf16 v[212:215], v[244:247], v[224:227], v[212:215]\n\t" \
    "ds_read_b128 v[244:247], v254 offset:61440\n\t" \
    "v_mfma_f32_16x16x32_bf16 v[212:215], v[248:251], v[228:231], v[212:215]\n\t" \
    "ds_read_b128 v[248:251], v254 offset:62464\n\t" \
    "s_waitcnt lgkmcnt(4)\n\t" \
    "v_mfma_f32_16x16x32_bf16 v[204:207], v[104:107], v[232:235], v[204:207]\n\t" \
    "ds_read_b128 v[224:227], v253 offset:27648\n\t" \
    "s_waitcnt lgkmcnt(4)\n\t" \
    "v_mfma_f32_16x16x32_bf16 v[204:207], v[108:111], v[236:239], v[204:207]\n\t" \
    "ds_read_b128 v[228:231], v253 offset:8192\n\t" \
    "v_mfma_f32_16x16x32_bf16 v[208:211], v[112:115], v[232:235], v[208:211]\n\t" \
    "v_mfma_f32_16x16x32_bf16 v[208:211], v[116:119], v[236:239], v[208:211]\n\t" \
    "v_mfma_f32_16x16x32_bf16 v[200:203], v[96:99], v[232:235], v[200:203]\n\t" \
    "v_mfma_f32_16x16x32_bf16 v[200:203], v[100:103], v[236:239], v[200:203]\n\t" \
    "s_waitcnt lgkmcnt(4)\n\t" \
    "v_mfma_f32_16x16x32_bf16 v[216:219], v[104:107], v[240:243], v[216:219]\n\t" \
    "ds_read_b128 v[232:235], v253 offset:9216\n\t" \
    "s_waitcnt lgkmcnt(2)\n\t" \
    "v_mfma_f32_16x16x32_bf16 v[216:219], v[108:111], v[224:227], v[216:219]\n\t" \
    "ds_read_b128 v[236:239], v253 offset:28672\n\t" \
    "v_mfma_f32_16x16x32_bf16 v[220:223], v[112:115], v[240:243], v[220:223]\n\t" \
    "v_mfma_f32_16x16x32_bf16 v[220:223], v[116:119], v[224:227], v[220:223]\n\t" \
    "v_mfma_f32_16x16x32_bf16 v[212:215], v[96:99], v[240:243], v[212:215]\n\t" \
    "v_mfma_f32_16x16x32_bf16 v[212:215], v[100:103], v[224:227], v[212:215]\n\t" \
    "s_waitcnt lgkmcnt(2)\n\t" \
    "v_mfma_f32_16x16x32_bf16 v[204:207], v[120:123], v[228:231], v[204:207]\n\t" \
    "ds_read_b128 v[240:243], v253 offset:29696\n\t" \
    "s_waitcnt lgkmcnt(2)\n\t" \
    "v_mfma_f32_16x16x32_bf16 v[204:207], v[124:127], v[232:235], v[204:207]\n\t" \
    "ds_read_b128 v[224:227], v253 offset:10240\n\t" \
    "v_mfma_f32_16x16x32_bf16 v[208:211], v[128:131], v[228:231], v[208:211]\n\t" \
    "v_mfma_f32_16x16x32_bf16 v[208:211], v[132:135], v[232:235], v[208:211]\n\t" \
    "v_mfma_f32_16x16x32_bf16 v[200:203], v[244:247], v[228:231], v[200:203]\n\t" \
    "v_mfma_f32_16x16x32_bf16 v[200:203], v[248:251], v[232:235], v[200:203]\n\t" \
    "s_waitcnt lgkmcnt(2)\n\t" \
    "v_mfma_f32_16x16x32_bf16 v[216:219], v[120:123], v[236:239], v[216:219]\n\t" \
    "ds_read_b128 v[228:231], v253 offset:11264\n\t" \
    "s_waitcnt lgkmcnt(2)\n\t" \
    "v_mfma_f32_16x16x32_bf16 v[216:219], v[124:127], v[240:243], v[216:219]\n\t" \
    "ds_read_b128 v[232:235], v253 offset:30720\n\t" \
    "v_mfma_f32_16x16x32_bf16 v[220:223], v[128:131], v[236:239], v[220:223]\n\t" \
    "v_mfma_f32_16x16x32_bf16 v[220:223], v[132:135], v[240:243], v[220:223]\n\t" \
    "v_mfma_f32_16x16x32_bf16 v[212:215], v[244:247], v[236:239], v[212:215]\n\t" \
    "ds_read_b128 v[244:247], v254 offset:63488\n\t" \
    "v_mfma_f32_16x16x32_bf16 v[212:215], v[248:251], v[240:243], v[212:215]\n\t" \
    "ds_read_b128 v[248:251], v254 offset:64512\n\t" \
    "ds_read_b128 v[236:239], v253 offset:31744\n\t" \
    "s_setprio 2\n\t" \
    "s_waitcnt lgkmcnt(0)\n\t" \
    "s_barrier \n\t" \
    "v_mfma_f32_16x16x32_bf16 v[204:207], v[144:147], v[224:227], v[204:207]\n\t" \
    "v_mfma_f32_16x16x32_bf16 v[204:207], v[148:151], v[228:231], v[204:207]\n\t" \
    "ds_read_b128 v[240:243], v253 offset:12288\n\t" \
    "v_mfma_f32_16x16x32_bf16 v[208:211], v[152:155], v[224:227], v[208:211]\n\t" \
    "v_mfma_f32_16x16x32_bf16 v[208:211], v[156:159], v[228:231], v[208:211]\n\t" \
    "v_mfma_f32_16x16x32_bf16 v[200:203], v[136:139], v[224:227], v[200:203]\n\t" \
    "v_mfma_f32_16x16x32_bf16 v[200:203], v[140:143], v[228:231], v[200:203]\n\t" \
    "v_mfma_f32_16x16x32_bf16 v[216:219], v[144:147], v[232:235], v[216:219]\n\t" \
    "ds_read_b128 v[224:227], v253 offset:13312\n\t" \
    "s_and_b32 s44, s40, 3\n\t" \
    "v_mfma_f32_16x16x32_bf16 v[216:219], v[148:151], v[236:239], v[216:219]\n\t" \
    "ds_read_b128 v[228:231], v253 offset:32768\n\t" \
    "s_lshl_b32 s44, s44, 17\n\t" \
    "v_mfma_f32_16x16x32_bf16 v[220:223], v[152:155], v[232:235], v[220:223]\n\t" \
    "s_add_u32 s44, s44, %[mb]\n\t" \
    "v_mfma_f32_16x16x32_bf16 v[220:223], v[156:159], v[236:239], v[220:223]\n\t" \
    "s_add_u32 s46, s44, 0x4000\n\t" \
    "v_mfma_f32_16x16x32_bf16 v[212:215], v[136:139], v[232:235], v[212:215]\n\t" \
    "s_add_u32 s47, s44, 0x4400\n\t" \
    "v_mfma_f32_16x16x32_bf16 v[212:215], v[140:143], v[236:239], v[212:215]\n\t" \
    "s_waitcnt lgkmcnt(2)\n\t" \
    "v_mfma_f32_16x16x32_bf16 v[204:207], v[160:163], v[240:243], v[204:207]\n\t" \
    "ds_read_b128 v[232:235], v253 offset:33792\n\t" \
    "s_waitcnt lgkmcnt(2)\n\t" \
    "v_mfma_f32_16x16x32_bf16 v[204:207], v[164:167], v[224:227], v[204:207]\n\t" \
    "ds_read_b128 v[236:239], v253 offset:16384\n\t" \
    "v_mfma_f32_16x16x32_bf16 v[208:211], v[168:171], v[240:243], v[208:211]\n\t" \
    "v_mfma_f32_16x16x32_bf16 v[208:211], v[172:175], v[224:227], v[208:211]\n\t" \
    "v_mfma_f32_16x16x32_bf16 v[200:203], v[244:247], v[240:243], v[200:203]\n\t" \
    "v_mfma_f32_16x16x32_bf16 v[200:203], v[248:251], v[224:227], v[200:203]\n\t" \
    "s_waitcnt lgkmcnt(2)\n\t" \
    "v_mfma_f32_16x16x32_bf16 v[216:219], v[160:163], v[228:231], v[216:219]\n\t" \
    "ds_read_b128 v[240:243], v253 offset:17408\n\t" \
    "s_waitcnt lgkmcnt(2)\n\t" \
    "v_mfma_f32_16x16x32_bf16 v[216:219], v[164:167], v[232:235], v[216:219]\n\t" \
    "ds_read_b128 v[224:227], v253 offset:36864\n\t" \
    "v_mfma_f32_16x16x32_bf16 v[220:223], v[168:171], v[228:231], v[220:223]\n\t" \
    "v_mfma_f32_16x16x32_bf16 v[220:223], v[172:175], v[232:235], v[220:223]\n\t" \
    "v_mfma_f32_16x16x32_bf16 v[212:215], v[244:247], v[228:231], v[212:215]\n\t" \
    "ds_read_b128 v[244:247], v254 offset:55296\n\t" \
    "v_mfma_f32_16x16x32_bf16 v[212:215], v[248:251], v[232:235], v[212:215]\n\t" \
    "ds_read_b128 v[248:251], v254 offset:56320\n\t" \
    "s_waitcnt lgkmcnt(4)\n\t" \
    "v_mfma_f32_16x16x32_bf16 v[204:207], v[0:3], v[236:239], v[204:207]\n\t" \
    "ds_read_b128 v[228:231], v253 offset:37888\n\t" \
    "s_waitcnt lgkmcnt(4)\n\t" \
    "v_mfma_f32_16x16x32_bf16 v[204:207], v[4:7], v[240:243], v[204:207]\n\t" \
    "ds_read_b128 v[232:235], v253 offset:14336\n\t" \
    "v_mfma_f32_16x16x32_bf16 v[208:211], v[8:11], v[236:239], v[208:211]\n\t" \
    "v_mfma_f32_16x16x32_bf16 v[208:211], v[12:15], v[240:243], v[208:211]\n\t" \
    "s_waitcnt lgkmcnt(3)\n\t" \
    "v_mfma_f32_16x16x32_bf16 v[200:203], v[244:247], v[236:239], v[200:203]\n\t" \
    "s_waitcnt lgkmcnt(2)\n\t" \
    "v_mfma_f32_16x16x32_bf16 v[200:203], v[248:251], v[240:243], v[200:203]\n\t" \
    "v_mfma_f32_16x16x32_bf16 v[216:219], v[0:3], v[224:227], v[216:219]\n\t" \
    "ds_read_b128 v[236:239], v253 offset:15360\n\t" \
    "s_waitcnt lgkmcnt(2)\n\t" \
    "v_mfma_f32_16x16x32_bf16 v[216:219], v[4:7], v[228:231], v[216:219]\n\t" \
    "ds_read_b128 v[240:243], v253 offset:18432\n\t" \
    "v_mfma_f32_16x16x32_bf16 v[220:223], v[8:11], v[224:227], v[220:223]\n\t" \
    "v_mfma_f32_16x16x32_bf16 v[220:223], v[12:15], v[228:231], v[220:223]\n\t" \
    "v_mfma_f32_16x16x32_bf16 v[212:215], v[244:247], v[224:227], v[212:215]\n\t" \
    "v_mfma_f32_16x16x32_bf16 v[212:215], v[248:251], v[228:231], v[212:215]\n\t" \
    "s_waitcnt lgkmcnt(2)\n\t" \
    "v_mfma_f32_16x16x32_bf16 v[204:207], v[184:187], v[232:235], v[204:207]\n\t" \
    "ds_read_b128 v[224:227], v253 offset:19456\n\t" \
    "v_mov_b64 v[244:245], 0\n\t" \
    "s_waitcnt lgkmcnt(2)\n\t" \
    "v_mfma_f32_16x16x32_bf16 v[204:207], v[188:191], v[236:239], v[204:207]\n\t" \
    "ds_read_b128 v[228:231], v253 offset:34816\n\t" \
    "v_mov_b64 v[246:247], 0\n\t" \
    "v_mfma_f32_16x16x32_bf16 v[208:211], v[192:195], v[232:235], v[208:211]\n\t" \
    "v_mov_b64 v[248:249], 0\n\t" \
    "v_mfma_f32_16x16x32_bf16 v[208:211], v[196:199], v[236:239], v[208:211]\n\t" \
    "v_mov_b64 v[250:251], 0\n\t" \
    "v_mfma_f32_16x16x32_bf16 v[200:203], v[176:179], v[232:235], v[200:203]\n\t" \
    "v_mfma_f32_16x16x32_bf16 v[200:203], v[180:183], v[236:239], v[200:203]\n\t" \
    "s_waitcnt lgkmcnt(2)\n\t" \
    "v_mfma_f32_16x16x32_bf16 v[204:207], v[24:27], v[240:243], v[204:207]\n\t" \
    "ds_read_b128 v[232:235], v253 offset:35840\n\t" \
    "s_waitcnt lgkmcnt(2)\n\t" \
    "v_mfma_f32_16x16x32_bf16 v[204:207], v[28:31], v[224:227], v[204:207]\n\t" \
    "ds_read_b128 v[236:239], v253 offset:38912\n\t" \
    "v_mfma_f32_16x16x32_bf16 v[208:211], v[32:35], v[240:243], v[208:211]\n\t" \
    "v_mfma_f32_16x16x32_bf16 v[208:211], v[36:39], v[224:227], v[208:211]\n\t" \
    "v_mfma_f32_16x16x32_bf16 v[200:203], v[16:19], v[240:243], v[200:203]\n\t" \
    "v_mfma_f32_16x16x32_bf16 v[200:203], v[20:23], v[224:227], v[200:203]\n\t" \
    "ds_read_b128 v[240:243], v253 offset:39936\n\t" \
    "s_waitcnt lgkmcnt(3)\n\t" \
    "v_mfma_f32_16x16x32_bf16 v[216:219], v[184:187], v[228:231], v[216:219]\n\t" \
    "s_waitcnt lgkmcnt(2)\n\t" \
    "v_mfma_f32_16x16x32_bf16 v[216:219], v[188:191], v[232:235], v[216:219]\n\t" \
    "v_cvt_pk_bf16_f32 v204, v204, v208\n\t" \
    "v_cvt_pk_bf16_f32 v205, v205, v209\n\t" \
    "s_waitcnt lgkmcnt(1)\n\t" \
    "v_mfma_f32_16x16x32_bf16 v[216:219], v[24:27], v[236:239], v[216:219]\n\t" \
    "v_cvt_pk_bf16_f32 v206, v206, v210\n\t" \
    "v_cvt_pk_bf16_f32 v207, v207, v211\n\t" \
    "s_waitcnt lgkmcnt(0)\n\t" \
    "v_mfma_f32_16x16x32_bf16 v[216:219], v[28:31], v[240:243], v[216:219]\n\t" \
    "ds_write_b128 v254, v[204:207] offset:5120\n\t" \
    "v_mfma_f32_16x16x32_bf16 v[220:223], v[192:195], v[228:231], v[220:223]\n\t" \
    "v_mfma_f32_16x16x32_bf16 v[220:223], v[196:199], v[232:235], v[220:223]\n\t" \
    "v_cvt_pk_bf16_f32 v200, v200, v201\n\t" \
    "v_cvt_pk_bf16_f32 v201, v202, v203\n\t" \
    "v_mfma_f32_16x16x32_bf16 v[220:223], v[32:35], v[236:239], v[220:223]\n\t" \
    "ds_write_b64 v254, v[200:201] offset:1032\n\t" \
    "v_mfma_f32_16x16x32_bf16 v[220:223], v[36:39], v[240:243], v[220:223]\n\t" \
    "v_mfma_f32_16x16x32_bf16 v[212:215], v[176:179], v[228:231], v[212:215]\n\t" \
    "ds_read_b128 v[200:203], v254 offset:55296\n\t" \
    "ds_read_b128 v[204:207], v254 offset:56320\n\t" \
    "v_mfma_f32_16x16x32_bf16 v[212:215], v[180:183], v[232:235], v[212:215]\n\t" \
    "v_mov_b64 v[208:209], 0\n\t" \
    "v_mov_b64 v[210:211], 0\n\t" \
    "v_mfma_f32_16x16x32_bf16 v[212:215], v[16:19], v[236:239], v[212:215]\n\t" \
    "v_cvt_pk_bf16_f32 v216, v216, v220\n\t" \
    "v_cvt_pk_bf16_f32 v217, v217, v221\n\t" \
    "v_mfma_f32_16x16x32_bf16 v[212:215], v[20:23], v[240:243], v[212:215]\n\t" \
    "v_cvt_pk_bf16_f32 v218, v218, v222\n\t" \
    "v_cvt_pk_bf16_f32 v219, v219, v223\n\t" \
    "s_setprio 0\n\t" \
    "ds_write_b128 v254, v[216:219] offset:11264\n\t" \
    "ds_read_b128 v[216:219], v254 offset:57344\n\t" \
    "ds_read_b128 v[220:223], v254 offset:58368\n\t" \
    "s_nop 2\n\t" \
    "v_cvt_pk_bf16_f32 v212, v212, v213\n\t" \
    "v_cvt_pk_bf16_f32 v213, v214, v215\n\t" \
    "ds_write_b64 v254, v[212:213] offset:7176\n\t" \
    "s_sleep 3\n\t" \
    "s_mov_b32 m0, 0xc000\n\t" \
    "s_nop 0\n\t" \
    "buffer_load_dwordx4 v253, s[60:63], s46 offen sc1 lds\n\t" \
    "s_mov_b32 m0, 0xc400\n\t" \
    "s_nop 0\n\t" \
    "buffer_load_dwordx4 v253, s[60:63], s47 offen sc1 lds\n\t" \
    "s_waitcnt lgkmcnt(0)\n\t" \
    "v_bfe_i32 v212, v253, 5, 1\n\t" \
    "v_and_b32 v212, 0xaaaa, v212\n\t" \
    "v_or_b32 v212, 0x4444, v212\n\t" \
    "v_and_b32 v213, 0xffff, v255\n\t" \
    "v_lshrrev_b32 v214, 16, v255\n\t" \
    ".Ls34_%=:\n\t" \
    "s_waitcnt vmcnt(0)\n\t" \
    "ds_read_b128 v[224:227], v253 offset:49152\n\t" \
    "ds_read_b128 v[228:231], v253 offset:50176\n\t" \
    "s_waitcnt lgkmcnt(1)\n\t" \
    "v_max_u32 v215, v224, v225\n\t" \
    "v_max3_u32 v215, v226, v227, v215\n\t" \
    "s_waitcnt lgkmcnt(0)\n\t" \
    "v_max3_u32 v215, v228, v229, v215\n\t" \
    "v_max3_u32 v215, v230, v231, v215\n\t" \
    "v_cmp_eq_u32 vcc, -1, v215\n\t" \
    "s_cbranch_vccz .Ls35_%=\n\t" \
    "v_max_u32 v215, v224, v225\n\t" \
    "v_max3_u32 v215, v226, v227, v215\n\t" \
    "v_cmp_eq_u32 vcc, -1, v215\n\t" \
    "s_cbranch_vccz .Ls36_%=\n\t" \
    "s_mov_b32 m0, 0xc000\n\t" \
    "s_nop 0\n\t" \
    "buffer_load_dwordx4 v253, s[60:63], s46 offen sc1 lds\n\t" \
    ".Ls36_%=:\n\t" \
    "v_max_u32 v215, v228, v229\n\t" \
    "v_max3_u32 v215, v230, v231, v215\n\t" \
    "v_cmp_eq_u32 vcc, -1, v215\n\t" \
    "s_cbranch_vccz .Ls37_%=\n\t" \
    "s_mov_b32 m0, 0xc400\n\t" \
    "s_nop 0\n\t" \
    "buffer_load_dwordx4 v253, s[60:63], s47 offen sc1 lds\n\t" \
    ".Ls37_%=:\n\t" \
    "s_cmp_eq_u32 s41, 0\n\t" \
    "s_cbranch_scc1 .Ls35_%=\n\t" \
    "s_sub_u32 s41, s41, 1\n\t" \
    "s_branch .Ls34_%=\n\t" \
    ".Ls35_%=:\n\t" \
    "s_setprio 3\n\t" \
    "s_waitcnt lgkmcnt(0)\n\t" \
    "s_barrier \n\t" \
    "ds_read_b128 v[232:235], v253 offset:40960\n\t" \
    "ds_read_b128 v[236:239], v253 offset:41984\n\t" \
    "ds_read_b128 v[240:243], v253 offset:43008\n\t" \
    "v_smfmac_f32_16x16x64_bf16 v[244:247], v[224:227], v[0:7], v212\n\t" \
    "v_smfmac_f32_16x16x64_bf16 v[248:251], v[224:227], v[8:15], v212\n\t" \
    "v_smfmac_f32_16x16x64_bf16 v[208:211], v[224:227], v[200:207], v212\n\t" \
    "ds_read_b128 v[200:203], v254 offset:59392\n\t" \
    "ds_read_b128 v[204:207], v254 offset:60416\n\t" \
    "ds_read_b128 v[224:227], v253 offset:44032\n\t" \
    "v_smfmac_f32_16x16x64_bf16 v[208:211], v[228:231], v[16:23], v212\n\t" \
    "v_smfmac_f32_16x16x64_bf16 v[244:247], v[228:231], v[24:31], v212\n\t" \
    "v_smfmac_f32_16x16x64_bf16 v[248:251], v[228:231], v[32:39], v212\n\t" \
    "ds_read_b128 v[228:231], v253 offset:45056\n\t" \
    "s_waitcnt lgkmcnt(6)\n\t" \
    "v_smfmac_f32_16x16x64_bf16 v[208:211], v[232:235], v[216:223], v212\n\t" \
    "ds_read_b128 v[216:219], v254 offset:61440\n\t" \
    "ds_read_b128 v[220:223], v254 offset:62464\n\t" \
    "v_smfmac_f32_16x16x64_bf16 v[244:247], v[232:235], v[40:47], v212\n\t" \
    "v_smfmac_f32_16x16x64_bf16 v[248:251], v[232:235], v[48:55], v212\n\t" \
    "ds_read_b128 v[232:235], v253 offset:46080\n\t" \
    "s_waitcnt lgkmcnt(8)\n\t" \
    "v_smfmac_f32_16x16x64_bf16 v[208:211], v[236:239], v[56:63], v212\n\t" \
    "v_smfmac_f32_16x16x64_bf16 v[244:247], v[236:239], v[64:71], v212\n\t" \
    "v_smfmac_f32_16x16x64_bf16 v[248:251], v[236:239], v[72:79], v212\n\t" \
    "ds_read_b128 v[236:239], v253 offset:47104\n\t" \
    "s_waitcnt lgkmcnt(6)\n\t" \
    "v_smfmac_f32_16x16x64_bf16 v[208:211], v[240:243], v[200:207], v212\n\t" \
    "ds_read_b128 v[200:203], v254 offset:63488\n\t" \
    "ds_read_b128 v[204:207], v254 offset:64512\n\t" \
    "v_smfmac_f32_16x16x64_bf16 v[244:247], v[240:243], v[80:87], v212\n\t" \
    "v_smfmac_f32_16x16x64_bf16 v[248:251], v[240:243], v[88:95], v212\n\t" \
    "ds_read_b128 v[240:243], v253 offset:48128\n\t" \
    "s_waitcnt lgkmcnt(8)\n\t" \
    "v_smfmac_f32_16x16x64_bf16 v[208:211], v[224:227], v[96:103], v212\n\t" \
    "v_smfmac_f32_16x16x64_bf16 v[244:247], v[224:227], v[104:111], v212\n\t" \
    "v_smfmac_f32_16x16x64_bf16 v[248:251], v[224:227], v[112:119], v212\n\t" \
    "s_waitcnt lgkmcnt(5)\n\t" \
    "v_smfmac_f32_16x16x64_bf16 v[208:211], v[228:231], v[216:223], v212\n\t" \
    "s_waitcnt lgkmcnt(4)\n\t" \
    "v_smfmac_f32_16x16x64_bf16 v[208:211], v[232:235], v[136:143], v212\n\t" \
    "s_waitcnt lgkmcnt(1)\n\t" \
    "v_smfmac_f32_16x16x64_bf16 v[208:211], v[236:239], v[200:207], v212\n\t" \
    "s_waitcnt lgkmcnt(0)\n\t" \
    "v_smfmac_f32_16x16x64_bf16 v[208:211], v[240:243], v[176:183], v212\n\t" \
    "v_smfmac_f32_16x16x64_bf16 v[244:247], v[228:231], v[120:127], v212\n\t" \
    "v_smfmac_f32_16x16x64_bf16 v[244:247], v[232:235], v[144:151], v212\n\t" \
    "v_smfmac_f32_16x16x64_bf16 v[244:247], v[236:239], v[160:167], v212\n\t" \
    "v_add_f32 v208, v208, v210\n\t" \
    "v_add_f32 v209, v209, v211\n\t" \
    "v_cvt_pk_bf16_f32 v208, v208, v209\n\t" \
    "v_smfmac_f32_16x16x64_bf16 v[244:247], v[240:243], v[184:191], v212\n\t" \
    "ds_write_b16 v213, v208 offset:0\n\t" \
    "ds_write_b16_d16_hi v213, v208 offset:16\n\t" \
    "v_smfmac_f32_16x16x64_bf16 v[248:251], v[228:231], v[128:135], v212\n\t" \
    "v_smfmac_f32_16x16x64_bf16 v[248:251], v[232:235], v[152:159], v212\n\t" \
    "v_smfmac_f32_16x16x64_bf16 v[248:251], v[236:239], v[168:175], v212\n\t" \
    "v_add_f32 v244, v244, v246\n\t" \
    "v_add_f32 v245, v245, v247\n\t" \
    "v_smfmac_f32_16x16x64_bf16 v[248:251], v[240:243], v[192:199], v212\n\t" \
    "s_nop 7\n\t" \
    "v_add_f32 v248, v248, v250\n\t" \
    "v_add_f32 v249, v249, v251\n\t" \
    "v_cvt_pk_bf16_f32 v244, v244, v248\n\t" \
    "v_cvt_pk_bf16_f32 v245, v245, v249\n\t" \
    "ds_write2_b32 v214, v244, v245 offset1:4\n\t" \
    "s_setprio 0\n\t" \
    "s_sleep 1\n\t" \
    "ds_read_b128 v[208:211], v254 offset:24576\n\t" \
    "ds_read_b128 v[224:227], v253 offset:0\n\t" \
    "ds_read_b128 v[228:231], v253 offset:20480\n\t" \
    "ds_read_b128 v[212:215], v254 offset:25600\n\t" \
    "ds_read_b128 v[232:235], v253 offset:1024\n\t" \
    "ds_read_b128 v[236:239], v253 offset:21504\n\t" \
    "ds_read_b128 v[216:219], v254 offset:26624\n\t" \
    "ds_read_b128 v[240:243], v253 offset:2048\n\t" \
    "ds_read_b128 v[244:247], v253 offset:22528\n\t" \
    "ds_read_b128 v[220:223], v254 offset:27648\n\t" \
    "ds_read_b128 v[248:251], v253 offset:3072\n\t" \
    "s_waitcnt lgkmcnt(9)\n\t" \
    "v_mfma_f32_16x16x32_bf16 v[200:203], v[208:211], v[224:227], 0\n\t" \
    "ds_read_b128 v[224:227], v253 offset:23552\n\t" \
    "s_waitcnt lgkmcnt(9)\n\t" \
    "v_mfma_f32_16x16x32_bf16 v[204:207], v[208:211], v[228:231], 0\n\t" \
    "ds_read_b128 v[228:231], v253 offset:4096\n\t" \
    "ds_read_b128 v[208:211], v254 offset:28672\n\t" \
    "s_waitcnt lgkmcnt(9)\n\t" \
    "v_mfma_f32_16x16x32_bf16 v[200:203], v[212:215], v[232:235], v[200:203]\n\t" \
    "ds_read_b128 v[232:235], v253 offset:24576\n\t" \
    "s_waitcnt lgkmcnt(9)\n\t" \
    "v_mfma_f32_16x16x32_bf16 v[204:207], v[212:215], v[236:239], v[204:207]\n\t" \
    "ds_read_b128 v[236:239], v253 offset:5120\n\t" \
    "ds_read_b128 v[212:215], v254 offset:29696\n\t" \
    "s_waitcnt lgkmcnt(9)\n\t" \
    "v_mfma_f32_16x16x32_bf16 v[200:203], v[216:219], v[240:243], v[200:203]\n\t" \
    "ds_read_b128 v[240:243], v253 offset:25600\n\t" \
    "s_waitcnt lgkmcnt(0)\n\t" \
    "s_barrier \n\t" \
    "s_add_u32 s40, s40, 1\n\t" \
    "s_cmp_lg_u32 s40, 64\n\t" \
    "s_cbranch_scc1 .Ls33_%=\n\t" \
    "s_branch .Lend%=\n\t" \
    ".Ls2_%=:\n\t" \
    "v_add_u32 v244, 0x18000, v243\n\t" \
    "s_waitcnt vmcnt(12) lgkmcnt(0)\n\t" \
    "s_barrier \n\t" \
    "s_add_u32 m0, %[wm], 0x12000\n\t" \
    "s_mov_b32 s44, 0x1e0000\n\t" \
    "buffer_load_dwordx4 v240, %[wr], s44 offen offset:0 nt lds\n\t" \
    "buffer_load_dwordx4 v241, %[wr], s44 offen offset:1024 nt lds\n\t" \
    "buffer_load_dwordx4 v242, %[wr], s44 offen offset:2048 nt lds\n\t" \
    "ds_read_b64_tr_b16 v[208:209], v244 offset:0\n\t" \
    "ds_read_b64_tr_b16 v[210:211], v244 offset:1536\n\t" \
    "ds_read_b64_tr_b16 v[40:41], v244 offset:32\n\t" \
    "ds_read_b64_tr_b16 v[42:43], v244 offset:1568\n\t" \
    "ds_read_b64_tr_b16 v[48:49], v244 offset:64\n\t" \
    "ds_read_b64_tr_b16 v[50:51], v244 offset:1600\n\t" \
    "v_add_u32 v244, 0x1e000, v243\n\t" \
    "s_waitcnt vmcnt(12) lgkmcnt(0)\n\t" \
    "s_barrier \n\t" \
    "s_add_u32 m0, %[wm], 0x18000\n\t" \
    "s_mov_b32 s44, 0x240000\n\t" \
    "buffer_load_dwordx4 v240, %[wr], s44 offen offset:0 nt lds\n\t" \
    "buffer_load_dwordx4 v241, %[wr], s44 offen offset:1024 nt lds\n\t" \
    "buffer_load_dwordx4 v242, %[wr], s44 offen offset:2048 nt lds\n\t" \
    "ds_read_b64_tr_b16 v[212:213], v244 offset:0\n\t" \
    "ds_read_b64_tr_b16 v[214:215], v244 offset:1536\n\t" \
    "ds_read_b64_tr_b16 v[44:45], v244 offset:32\n\t" \
    "ds_read_b64_tr_b16 v[46:47], v244 offset:1568\n\t" \
    "ds_read_b64_tr_b16 v[52:53], v244 offset:64\n\t" \
    "ds_read_b64_tr_b16 v[54:55], v244 offset:1600\n\t" \
    "v_add_u32 v244, 0x0, v243\n\t" \
    "s_waitcnt vmcnt(12) lgkmcnt(0)\n\t" \
    "s_barrier \n\t" \
    "s_add_u32 m0, %[wm], 0x1e000\n\t" \
    "s_mov_b32 s44, 0x2a0000\n\t" \
    "buffer_load_dwordx4 v240, %[wr], s44 offen offset:0 nt lds\n\t" \
    "buffer_load_dwordx4 v241, %[wr], s44 offen offset:1024 nt lds\n\t" \
    "buffer_load_dwordx4 v242, %[wr], s44 offen offset:2048 nt lds\n\t" \
    "ds_read_b64_tr_b16 v[56:57], v244 offset:0\n\t" \
    "ds_read_b64_tr_b16 v[58:59], v244 offset:1536\n\t" \
    "ds_read_b64_tr_b16 v[64:65], v244 offset:32\n\t" \
    "ds_read_b64_tr_b16 v[66:67], v244 offset:1568\n\t" \
    "ds_read_b64_tr_b16 v[72:73], v244 offset:64\n\t" \
    "ds_read_b64_tr_b16 v[74:75], v244 offset:1600\n\t" \
    "v_add_u32 v244, 0x6000, v243\n\t" \
    "s_waitcnt vmcnt(12) lgkmcnt(0)\n\t" \
    "s_barrier \n\t" \
    "s_add_u32 m0, %[wm], 0x0\n\t" \
    "s_mov_b32 s44, 0x300000\n\t" \
    "buffer_load_dwordx4 v240, %[wr], s44 offen offset:0 nt lds\n\t" \
    "buffer_load_dwordx4 v241, %[wr], s44 offen offset:1024 nt lds\n\t" \
    "buffer_load_dwordx4 v242, %[wr], s44 offen offset:2048 nt lds\n\t" \
    "ds_read_b64_tr_b16 v[60:61], v244 offset:0\n\t" \
    "ds_read_b64_tr_b16 v[62:63], v244 offset:1536\n\t" \
    "ds_read_b64_tr_b16 v[68:69], v244 offset:32\n\t" \
    "ds_read_b64_tr_b16 v[70:71], v244 offset:1568\n\t" \
    "ds_read_b64_tr_b16 v[76:77], v244 offset:64\n\t" \
    "ds_read_b64_tr_b16 v[78:79], v244 offset:1600\n\t" \
    "v_add_u32 v244, 0xc000, v243\n\t" \
    "s_waitcnt vmcnt(12) lgkmcnt(0)\n\t" \
    "s_barrier \n\t" \
    "s_add_u32 m0, %[wm], 0x6000\n\t" \
    "s_mov_b32 s44, 0x360000\n\t" \
    "buffer_load_dwordx4 v240, %[wr], s44 offen offset:0 nt lds\n\t" \
    "buffer_load_dwordx4 v241, %[wr], s44 offen offset:1024 nt lds\n\t" \
    "buffer_load_dwordx4 v242, %[wr], s44 offen offset:2048 nt lds\n\t" \
    "ds_read_b64_tr_b16 v[216:217], v244 offset:0\n\t" \
    "ds_read_b64_tr_b16 v[218:219], v244 offset:1536\n\t" \
    "ds_read_b64_tr_b16 v[80:81], v244 offset:32\n\t" \
    "ds_read_b64_tr_b16 v[82:83], v244 offset:1568\n\t" \
    "ds_read_b64_tr_b16 v[88:89], v244 offset:64\n\t" \
    "ds_read_b64_tr_b16 v[90:91], v244 offset:1600\n\t" \
    "v_add_u32 v244, 0x12000, v243\n\t" \
    "s_waitcnt vmcnt(12) lgkmcnt(0)\n\t" \
    "s_barrier \n\t" \
    "s_add_u32 m0, %[wm], 0xc000\n\t" \
    "s_mov_b32 s44, 0x3c0000\n\t" \
    "buffer_load_dwordx4 v240, %[wr], s44 offen offset:0 nt lds\n\t" \
    "buffer_load_dwordx4 v241, %[wr], s44 offen offset:1024 nt lds\n\t" \
    "buffer_load_dwordx4 v242, %[wr], s44 offen offset:2048 nt lds\n\t" \
    "ds_read_b64_tr_b16 v[220:221], v244 offset:0\n\t" \
    "ds_read_b64_tr_b16 v[222:223], v244 offset:1536\n\t" \
    "ds_read_b64_tr_b16 v[84:85], v244 offset:32\n\t" \
    "ds_read_b64_tr_b16 v[86:87], v244 offset:1568\n\t" \
    "ds_read_b64_tr_b16 v[92:93], v244 offset:64\n\t" \
    "ds_read_b64_tr_b16 v[94:95], v244 offset:1600\n\t" \
    "v_add_u32 v244, 0x18000, v243\n\t" \
    "s_waitcnt vmcnt(12) lgkmcnt(0)\n\t" \
    "s_barrier \n\t" \
    "s_add_u32 m0, %[wm], 0x12000\n\t" \
    "s_mov_b32 s44, 0x420000\n\t" \
    "buffer_load_dwordx4 v240, %[wr], s44 offen offset:0 nt lds\n\t" \
    "buffer_load_dwordx4 v241, %[wr], s44 offen offset:1024 nt lds\n\t" \
    "buffer_load_dwordx4 v242, %[wr], s44 offen offset:2048 nt lds\n\t" \
    "ds_read_b64_tr_b16 v[96:97], v244 offset:0\n\t" \
    "ds_read_b64_tr_b16 v[98:99], v244 offset:1536\n\t" \
    "ds_read_b64_tr_b16 v[104:105], v244 offset:32\n\t" \
    "ds_read_b64_tr_b16 v[106:107], v244 offset:1568\n\t" \
    "ds_read_b64_tr_b16 v[112:113], v244 offset:64\n\t" \
    "ds_read_b64_tr_b16 v[114:115], v244 offset:1600\n\t" \
    "v_add_u32 v244, 0x1e000, v243\n\t" \
    "s_waitcnt vmcnt(12) lgkmcnt(0)\n\t" \
    "s_barrier \n\t" \
    "s_add_u32 m0, %[wm], 0x18000\n\t" \
    "s_mov_b32 s44, 0x480000\n\t" \
    "buffer_load_dwordx4 v240, %[wr], s44 offen offset:0 nt lds\n\t" \
    "buffer_load_dwordx4 v241, %[wr], s44 offen offset:1024 nt lds\n\t" \
    "buffer_load_dwordx4 v242, %[wr], s44 offen offset:2048 nt lds\n\t" \
    "ds_read_b64_tr_b16 v[100:101], v244 offset:0\n\t" \
    "ds_read_b64_tr_b16 v[102:103], v244 offset:1536\n\t" \
    "ds_read_b64_tr_b16 v[108:109], v244 offset:32\n\t" \
    "ds_read_b64_tr_b16 v[110:111], v244 offset:1568\n\t" \
    "ds_read_b64_tr_b16 v[116:117], v244 offset:64\n\t" \
    "ds_read_b64_tr_b16 v[118:119], v244 offset:1600\n\t" \
    "v_add_u32 v244, 0x0, v243\n\t" \
    "s_waitcnt vmcnt(12) lgkmcnt(0)\n\t" \
    "s_barrier \n\t" \
    "v_mov_b32 v246, 0\n\t" \
    "buffer_load_dword v246, v246, s[60:63], %[mo] offen sc1\n\t" \
    "s_add_u32 m0, %[wm], 0x1e000\n\t" \
    "s_mov_b32 s44, 0x4e0000\n\t" \
    "buffer_load_dwordx4 v240, %[wr], s44 offen offset:0 nt lds\n\t" \
    "buffer_load_dwordx4 v241, %[wr], s44 offen offset:1024 nt lds\n\t" \
    "buffer_load_dwordx4 v242, %[wr], s44 offen offset:2048 nt lds\n\t" \
    "ds_read_b64_tr_b16 v[200:201], v244 offset:0\n\t" \
    "ds_read_b64_tr_b16 v[202:203], v244 offset:1536\n\t" \
    "ds_read_b64_tr_b16 v[0:1], v244 offset:32\n\t" \
    "ds_read_b64_tr_b16 v[2:3], v244 offset:1568\n\t" \
    "ds_read_b64_tr_b16 v[8:9], v244 offset:64\n\t" \
    "ds_read_b64_tr_b16 v[10:11], v244 offset:1600\n\t" \
    "v_add_u32 v244, 0x6000, v243\n\t" \
    "s_waitcnt vmcnt(12) lgkmcnt(0)\n\t" \
    "s_barrier \n\t" \
    "s_add_u32 m0, %[wm], 0x0\n\t" \
    "s_mov_b32 s44, 0x540000\n\t" \
    "buffer_load_dwordx4 v240, %[wr], s44 offen offset:0 nt lds\n\t" \
    "buffer_load_dwordx4 v241, %[wr], s44 offen offset:1024 nt lds\n\t" \
    "buffer_load_dwordx4 v242, %[wr], s44 offen offset:2048 nt lds\n\t" \
    "ds_read_b64_tr_b16 v[204:205], v244 offset:0\n\t" \
    "ds_read_b64_tr_b16 v[206:207], v244 offset:1536\n\t" \
    "ds_read_b64_tr_b16 v[4:5], v244 offset:32\n\t" \
    "ds_read_b64_tr_b16 v[6:7], v244 offset:1568\n\t" \
    "ds_read_b64_tr_b16 v[12:13], v244 offset:64\n\t" \
    "ds_read_b64_tr_b16 v[14:15], v244 offset:1600\n\t" \
    "v_add_u32 v244, 0xc000, v243\n\t" \
    "s_waitcnt vmcnt(12) lgkmcnt(0)\n\t" \
    "s_barrier \n\t" \
    "s_add_u32 m0, %[wm], 0x6000\n\t" \
    "s_mov_b32 s44, 0x5a0000\n\t" \
    "buffer_load_dwordx4 v240, %[wr], s44 offen offset:0 nt lds\n\t" \
    "buffer_load_dwordx4 v241, %[wr], s44 offen offset:1024 nt lds\n\t" \
    "buffer_load_dwordx4 v242, %[wr], s44 offen offset:2048 nt lds\n\t" \
    "ds_read_b64_tr_b16 v[224:225], v244 offset:0\n\t" \
    "ds_read_b64_tr_b16 v[226:227], v244 offset:1536\n\t" \
    "ds_read_b64_tr_b16 v[120:121], v244 offset:32\n\t" \
    "ds_read_b64_tr_b16 v[122:123], v244 offset:1568\n\t" \
    "ds_read_b64_tr_b16 v[128:129], v244 offset:64\n\t" \
    "ds_read_b64_tr_b16 v[130:131], v244 offset:1600\n\t" \
    "v_add_u32 v244, 0x12000, v243\n\t" \
    "s_waitcnt vmcnt(12) lgkmcnt(0)\n\t" \
    "s_barrier \n\t" \
    "s_add_u32 m0, %[wm], 0xc000\n\t" \
    "s_mov_b32 s44, 0x600000\n\t" \
    "buffer_load_dwordx4 v240, %[wr], s44 offen offset:0 nt lds\n\t" \
    "buffer_load_dwordx4 v241, %[wr], s44 offen offset:1024 nt lds\n\t" \
    "buffer_load_dwordx4 v242, %[wr], s44 offen offset:2048 nt lds\n\t" \
    "ds_read_b64_tr_b16 v[228:229], v244 offset:0\n\t" \
    "ds_read_b64_tr_b16 v[230:231], v244 offset:1536\n\t" \
    "ds_read_b64_tr_b16 v[124:125], v244 offset:32\n\t" \
    "ds_read_b64_tr_b16 v[126:127], v244 offset:1568\n\t" \
    "ds_read_b64_tr_b16 v[132:133], v244 offset:64\n\t" \
    "ds_read_b64_tr_b16 v[134:135], v244 offset:1600\n\t" \
    "v_add_u32 v244, 0x18000, v243\n\t" \
    "s_waitcnt vmcnt(12) lgkmcnt(0)\n\t" \
    "s_barrier \n\t" \
    "s_add_u32 m0, %[wm], 0x12000\n\t" \
    "s_mov_b32 s44, 0x660000\n\t" \
    "buffer_load_dwordx4 v240, %[wr], s44 offen offset:0 nt lds\n\t" \
    "buffer_load_dwordx4 v241, %[wr], s44 offen offset:1024 nt lds\n\t" \
    "buffer_load_dwordx4 v242, %[wr], s44 offen offset:2048 nt lds\n\t" \
    "ds_read_b64_tr_b16 v[136:137], v244 offset:0\n\t" \
    "ds_read_b64_tr_b16 v[138:139], v244 offset:1536\n\t" \
    "ds_read_b64_tr_b16 v[144:145], v244 offset:32\n\t" \
    "ds_read_b64_tr_b16 v[146:147], v244 offset:1568\n\t" \
    "ds_read_b64_tr_b16 v[152:153], v244 offset:64\n\t" \
    "ds_read_b64_tr_b16 v[154:155], v244 offset:1600\n\t" \
    "v_add_u32 v244, 0x1e000, v243\n\t" \
    "s_waitcnt vmcnt(12) lgkmcnt(0)\n\t" \
    "s_barrier \n\t" \
    ".Ls21_%=:\n\t" \
    "v_readfirstlane_b32 s43, v246\n\t" \
    "s_nop 0\n\t" \
    "s_cmp_lg_u32 s43, -1\n\t" \
    "s_cbranch_scc1 .Ls22_%=\n\t" \
    "s_cmp_eq_u32 s41, 0\n\t" \
    "s_cbranch_scc1 .Ls22_%=\n\t" \
    "s_sub_u32 s41, s41, 1\n\t" \
    "s_sleep 4\n\t" \
    "v_mov_b32 v246, 0\n\t" \
    "buffer_load_dword v246, v246, s[60:63], %[mo] offen sc1\n\t" \
    "s_waitcnt vmcnt(0)\n\t" \
    "s_branch .Ls21_%=\n\t" \
    ".Ls22_%=:\n\t" \
    "v_mov_b32 v246, 0x8000\n\t" \
    "s_mov_b64 exec, 1\n\t" \
    "buffer_store_dword v246, v246, s[60:63], %[mo] offen sc1\n\t" \
    "s_mov_b64 exec, -1\n\t" \
    "s_and_b32 s43, s43, 0x2000\n\t" \
    "s_xor_b32 s43, s43, %[lx]\n\t" \
    "s_add_u32 s60, s60, s43\n\t" \
    "s_addc_u32 s61, s61, 0\n\t" \
    "s_add_u32 m0, %[wm], 0x18000\n\t" \
    "s_mov_b32 s44, 0x6c0000\n\t" \
    "buffer_load_dwordx4 v240, %[wr], s44 offen offset:0 nt lds\n\t" \
    "buffer_load_dwordx4 v241, %[wr], s44 offen offset:1024 nt lds\n\t" \
    "buffer_load_dwordx4 v242, %[wr], s44 offen offset:2048 nt lds\n\t" \
    "ds_read_b64_tr_b16 v[140:141], v244 offset:0\n\t" \
    "ds_read_b64_tr_b16 v[142:143], v244 offset:1536\n\t" \
    "ds_read_b64_tr_b16 v[148:149], v244 offset:32\n\t" \
    "ds_read_b64_tr_b16 v[150:151], v244 offset:1568\n\t" \
    "ds_read_b64_tr_b16 v[156:157], v244 offset:64\n\t" \
    "ds_read_b64_tr_b16 v[158:159], v244 offset:1600\n\t" \
    "v_add_u32 v244, 0x0, v243\n\t" \
    "s_waitcnt vmcnt(12) lgkmcnt(0)\n\t" \
    "s_barrier \n\t" \
    "s_add_u32 m0, %[wm], 0x1e000\n\t" \
    "s_mov_b32 s44, 0x720000\n\t" \
    "buffer_load_dwordx4 v240, %[wr], s44 offen offset:0 nt lds\n\t" \
    "buffer_load_dwordx4 v241, %[wr], s44 offen offset:1024 nt lds\n\t" \
    "buffer_load_dwordx4 v242, %[wr], s44 offen offset:2048 nt lds\n\t" \
    "ds_read_b64_tr_b16 v[16:17], v244 offset:0\n\t" \
    "ds_read_b64_tr_b16 v[18:19], v244 offset:1536\n\t" \
    "ds_read_b64_tr_b16 v[24:25], v244 offset:32\n\t" \
    "ds_read_b64_tr_b16 v[26:27], v244 offset:1568\n\t" \
    "ds_read_b64_tr_b16 v[32:33], v244 offset:64\n\t" \
    "ds_read_b64_tr_b16 v[34:35], v244 offset:1600\n\t" \
    "v_add_u32 v244, 0x6000, v243\n\t" \
    "s_waitcnt vmcnt(12) lgkmcnt(0)\n\t" \
    "s_barrier \n\t" \
    "ds_read_b64_tr_b16 v[20:21], v244 offset:0\n\t" \
    "ds_read_b64_tr_b16 v[22:23], v244 offset:1536\n\t" \
    "ds_read_b64_tr_b16 v[28:29], v244 offset:32\n\t" \
    "ds_read_b64_tr_b16 v[30:31], v244 offset:1568\n\t" \
    "ds_read_b64_tr_b16 v[36:37], v244 offset:64\n\t" \
    "ds_read_b64_tr_b16 v[38:39], v244 offset:1600\n\t" \
    "v_add_u32 v244, 0xc000, v243\n\t" \
    "s_waitcnt vmcnt(9) lgkmcnt(0)\n\t" \
    "s_barrier \n\t" \
    "ds_read_b64_tr_b16 v[232:233], v244 offset:0\n\t" \
    "ds_read_b64_tr_b16 v[234:235], v244 offset:1536\n\t" \
    "ds_read_b64_tr_b16 v[160:161], v244 offset:32\n\t" \
    "ds_read_b64_tr_b16 v[162:163], v244 offset:1568\n\t" \
    "ds_read_b64_tr_b16 v[168:169], v244 offset:64\n\t" \
    "ds_read_b64_tr_b16 v[170:171], v244 offset:1600\n\t" \
    "v_add_u32 v244, 0x12000, v243\n\t" \
    "s_waitcnt vmcnt(6) lgkmcnt(0)\n\t" \
    "s_barrier \n\t" \
    "ds_read_b64_tr_b16 v[236:237], v244 offset:0\n\t" \
    "ds_read_b64_tr_b16 v[238:239], v244 offset:1536\n\t" \
    "ds_read_b64_tr_b16 v[164:165], v244 offset:32\n\t" \
    "ds_read_b64_tr_b16 v[166:167], v244 offset:1568\n\t" \
    "ds_read_b64_tr_b16 v[172:173], v244 offset:64\n\t" \
    "ds_read_b64_tr_b16 v[174:175], v244 offset:1600\n\t" \
    "v_add_u32 v244, 0x18000, v243\n\t" \
    "s_waitcnt vmcnt(3) lgkmcnt(0)\n\t" \
    "s_barrier \n\t" \
    "ds_read_b64_tr_b16 v[176:177], v244 offset:0\n\t" \
    "ds_read_b64_tr_b16 v[178:179], v244 offset:1536\n\t" \
    "ds_read_b64_tr_b16 v[184:185], v244 offset:32\n\t" \
    "ds_read_b64_tr_b16 v[186:187], v244 offset:1568\n\t" \
    "ds_read_b64_tr_b16 v[192:193], v244 offset:64\n\t" \
    "ds_read_b64_tr_b16 v[194:195], v244 offset:1600\n\t" \
    "v_add_u32 v244, 0x1e000, v243\n\t" \
    "s_waitcnt vmcnt(0) lgkmcnt(0)\n\t" \
    "s_barrier \n\t" \
    "ds_read_b64_tr_b16 v[180:181], v244 offset:0\n\t" \
    "ds_read_b64_tr_b16 v[182:183], v244 offset:1536\n\t" \
    "ds_read_b64_tr_b16 v[188:189], v244 offset:32\n\t" \
    "ds_read_b64_tr_b16 v[190:191], v244 offset:1568\n\t" \
    "ds_read_b64_tr_b16 v[196:197], v244 offset:64\n\t" \
    "ds_read_b64_tr_b16 v[198:199], v244 offset:1600\n\t" \
    "s_waitcnt lgkmcnt(0)\n\t" \
    "s_barrier \n\t" \
    "ds_write_b128 v254, v[200:203] offset:51200\n\t" \
    "ds_write_b128 v254, v[204:207] offset:52224\n\t" \
    "ds_write_b128 v254, v[208:211] offset:53248\n\t" \
    "ds_write_b128 v254, v[212:215] offset:54272\n\t" \
    "ds_write_b128 v254, v[216:219] offset:55296\n\t" \
    "ds_write_b128 v254, v[220:223] offset:56320\n\t" \
    "ds_write_b128 v254, v[224:227] offset:57344\n\t" \
    "ds_write_b128 v254, v[228:231] offset:58368\n\t" \
    "ds_write_b128 v254, v[232:235] offset:59392\n\t" \
    "ds_write_b128 v254, v[236:239] offset:60416\n\t" \
    "s_waitcnt vmcnt(0) lgkmcnt(0)\n\t" \
    "s_barrier \n\t" \
    "ds_read_b128 v[224:227], v253 offset:0\n\t" \
    "ds_read_b128 v[244:247], v254 offset:53248\n\t" \
    "ds_read_b128 v[228:231], v253 offset:1024\n\t" \
    "ds_read_b128 v[232:235], v253 offset:20480\n\t" \
    "ds_read_b128 v[236:239], v253 offset:21504\n\t" \
    "ds_read_b128 v[248:251], v254 offset:54272\n\t" \
    ".Ls23_%=:\n\t" \
    "s_waitcnt lgkmcnt(5)\n\t" \
    "v_mfma_f32_16x16x32_bf16 v[204:207], v[40:43], v[224:227], 0\n\t" \
    "s_waitcnt lgkmcnt(3)\n\t" \
    "v_mfma_f32_16x16x32_bf16 v[204:207], v[44:47], v[228:231], v[204:207]\n\t" \
    "ds_read_b128 v[240:243], v253 offset:2048\n\t" \
    "v_mfma_f32_16x16x32_bf16 v[208:211], v[48:51], v[224:227], 0\n\t" \
    "v_mfma_f32_16x16x32_bf16 v[208:211], v[52:55], v[228:231], v[208:211]\n\t" \
    "v_mfma_f32_16x16x32_bf16 v[200:203], v[244:247], v[224:227], 0\n\t" \
    "s_waitcnt lgkmcnt(1)\n\t" \
    "v_mfma_f32_16x16x32_bf16 v[200:203], v[248:251], v[228:231], v[200:203]\n\t" \
    "v_mfma_f32_16x16x32_bf16 v[216:219], v[40:43], v[232:235], 0\n\t" \
    "ds_read_b128 v[224:227], v253 offset:3072\n\t" \
    "v_mfma_f32_16x16x32_bf16 v[216:219], v[44:47], v[236:239], v[216:219]\n\t" \
    "ds_read_b128 v[228:231], v253 offset:22528\n\t" \
    "v_mfma_f32_16x16x32_bf16 v[220:223], v[48:51], v[232:235], 0\n\t" \
    "v_mfma_f32_16x16x32_bf16 v[220:223], v[52:55], v[236:239], v[220:223]\n\t" \
    "v_mfma_f32_16x16x32_bf16 v[212:215], v[244:247], v[232:235], 0\n\t" \
    "ds_read_b128 v[244:247], v254 offset:55296\n\t" \
    "v_mfma_f32_16x16x32_bf16 v[212:215], v[248:251], v[236:239], v[212:215]\n\t" \
    "ds_read_b128 v[248:251], v254 offset:56320\n\t" \
    "s_waitcnt lgkmcnt(4)\n\t" \
    "v_mfma_f32_16x16x32_bf16 v[204:207], v[64:67], v[240:243], v[204:207]\n\t" \
    "ds_read_b128 v[232:235], v253 offset:23552\n\t" \
    "s_waitcnt lgkmcnt(4)\n\t" \
    "v_mfma_f32_16x16x32_bf16 v[204:207], v[68:71], v[224:227], v[204:207]\n\t" \
    "ds_read_b128 v[236:239], v253 offset:4096\n\t" \
    "v_mfma_f32_16x16x32_bf16 v[208:211], v[72:75], v[240:243], v[208:211]\n\t" \
    "v_mfma_f32_16x16x32_bf16 v[208:211], v[76:79], v[224:227], v[208:211]\n\t" \
    "v_mfma_f32_16x16x32_bf16 v[200:203], v[56:59], v[240:243], v[200:203]\n\t" \
    "v_mfma_f32_16x16x32_bf16 v[200:203], v[60:63], v[224:227], v[200:203]\n\t" \
    "s_waitcnt lgkmcnt(4)\n\t" \
    "v_mfma_f32_16x16x32_bf16 v[216:219], v[64:67], v[228:231], v[216:219]\n\t" \
    "ds_read_b128 v[240:243], v253 offset:5120\n\t" \
    "s_waitcnt lgkmcnt(2)\n\t" \
    "v_mfma_f32_16x16x32_bf16 v[216:219], v[68:71], v[232:235], v[216:219]\n\t" \
    "ds_read_b128 v[224:227], v253 offset:24576\n\t" \
    "v_mfma_f32_16x16x32_bf16 v[220:223], v[72:75], v[228:231], v[220:223]\n\t" \
    "v_mfma_f32_16x16x32_bf16 v[220:223], v[76:79], v[232:235], v[220:223]\n\t" \
    "v_mfma_f32_16x16x32_bf16 v[212:215], v[56:59], v[228:231], v[212:215]\n\t" \
    "v_mfma_f32_16x16x32_bf16 v[212:215], v[60:63], v[232:235], v[212:215]\n\t" \
    "s_waitcnt lgkmcnt(2)\n\t" \
    "v_mfma_f32_16x16x32_bf16 v[204:207], v[80:83], v[236:239], v[204:207]\n\t" \
    "ds_read_b128 v[228:231], v253 offset:25600\n\t" \
    "s_waitcnt lgkmcnt(2)\n\t" \
    "v_mfma_f32_16x16x32_bf16 v[204:207], v[84:87], v[240:243], v[204:207]\n\t" \
    "ds_read_b128 v[232:235], v253 offset:6144\n\t" \
    "v_mfma_f32_16x16x32_bf16 v[208:211], v[88:91], v[236:239], v[208:211]\n\t" \
    "v_mfma_f32_16x16x32_bf16 v[208:211], v[92:95], v[240:243], v[208:211]\n\t" \
    "v_mfma_f32_16x16x32_bf16 v[200:203], v[244:247], v[236:239], v[200:203]\n\t" \
    "v_mfma_f32_16x16x32_bf16 v[200:203], v[248:251], v[240:243], v[200:203]\n\t" \
    "s_waitcnt lgkmcnt(2)\n\t" \
    "v_mfma_f32_16x16x32_bf16 v[216:219], v[80:83], v[224:227], v[216:219]\n\t" \
    "ds_read_b128 v[236:239], v253 offset:7168\n\t" \
    "s_waitcnt lgkmcnt(2)\n\t" \
    "v_mfma_f32_16x16x32_bf16 v[216:219], v[84:87], v[228:231], v[216:219]\n\t" \
    "ds_read_b128 v[240:243], v253 offset:26624\n\t" \
    "v_mfma_f32_16x16x32_bf16 v[220:223], v[88:91], v[224:227], v[220:223]\n\t" \
    "v_mfma_f32_16x16x32_bf16 v[220:223], v[92:95], v[228:231], v[220:223]\n\t" \
    "v_mfma_f32_16x16x32_bf16 v[212:215], v[244:247], v[224:227], v[212:215]\n\t" \
    "ds_read_b128 v[244:247], v254 offset:51200\n\t" \
    "v_mfma_f32_16x16x32_bf16 v[212:215], v[248:251], v[228:231], v[212:215]\n\t" \
    "ds_read_b128 v[248:251], v254 offset:52224\n\t" \
    "s_waitcnt lgkmcnt(4)\n\t" \
    "v_mfma_f32_16x16x32_bf16 v[204:207], v[104:107], v[232:235], v[204:207]\n\t" \
    "ds_read_b128 v[224:227], v253 offset:27648\n\t" \
    "s_waitcnt lgkmcnt(4)\n\t" \
    "v_mfma_f32_16x16x32_bf16 v[204:207], v[108:111], v[236:239], v[204:207]\n\t" \
    "ds_read_b128 v[228:231], v253 offset:8192\n\t" \
    "v_mfma_f32_16x16x32_bf16 v[208:211], v[112:115], v[232:235], v[208:211]\n\t" \
    "v_mfma_f32_16x16x32_bf16 v[208:211], v[116:119], v[236:239], v[208:211]\n\t" \
    "v_mfma_f32_16x16x32_bf16 v[200:203], v[96:99], v[232:235], v[200:203]\n\t" \
    "v_mfma_f32_16x16x32_bf16 v[200:203], v[100:103], v[236:239], v[200:203]\n\t" \
    "s_waitcnt lgkmcnt(4)\n\t" \
    "v_mfma_f32_16x16x32_bf16 v[216:219], v[104:107], v[240:243], v[216:219]\n\t" \
    "ds_read_b128 v[232:235], v253 offset:9216\n\t" \
    "s_waitcnt lgkmcnt(2)\n\t" \
    "v_mfma_f32_16x16x32_bf16 v[216:219], v[108:111], v[224:227], v[216:219]\n\t" \
    "ds_read_b128 v[236:239], v253 offset:28672\n\t" \
    "v_mfma_f32_16x16x32_bf16 v[220:223], v[112:115], v[240:243], v[220:223]\n\t" \
    "v_mfma_f32_16x16x32_bf16 v[220:223], v[116:119], v[224:227], v[220:223]\n\t" \
    "v_mfma_f32_16x16x32_bf16 v[212:215], v[96:99], v[240:243], v[212:215]\n\t" \
    "v_mfma_f32_16x16x32_bf16 v[212:215], v[100:103], v[224:227], v[212:215]\n\t" \
    "s_waitcnt lgkmcnt(2)\n\t" \
    "v_mfma_f32_16x16x32_bf16 v[204:207], v[0:3], v[228:231], v[204:207]\n\t" \
    "ds_read_b128 v[240:243], v253 offset:29696\n\t" \
    "s_waitcnt lgkmcnt(2)\n\t" \
    "v_mfma_f32_16x16x32_bf16 v[204:207], v[4:7], v[232:235], v[204:207]\n\t" \
    "ds_read_b128 v[224:227], v253 offset:10240\n\t" \
    "v_mfma_f32_16x16x32_bf16 v[208:211], v[8:11], v[228:231], v[208:211]\n\t" \
    "v_mfma_f32_16x16x32_bf16 v[208:211], v[12:15], v[232:235], v[208:211]\n\t" \
    "v_mfma_f32_16x16x32_bf16 v[200:203], v[244:247], v[228:231], v[200:203]\n\t" \
    "v_mfma_f32_16x16x32_bf16 v[200:203], v[248:251], v[232:235], v[200:203]\n\t" \
    "s_waitcnt lgkmcnt(2)\n\t" \
    "v_mfma_f32_16x16x32_bf16 v[216:219], v[0:3], v[236:239], v[216:219]\n\t" \
    "ds_read_b128 v[228:231], v253 offset:11264\n\t" \
    "s_waitcnt lgkmcnt(2)\n\t" \
    "v_mfma_f32_16x16x32_bf16 v[216:219], v[4:7], v[240:243], v[216:219]\n\t" \
    "ds_read_b128 v[232:235], v253 offset:30720\n\t" \
    "v_mfma_f32_16x16x32_bf16 v[220:223], v[8:11], v[236:239], v[220:223]\n\t" \
    "v_mfma_f32_16x16x32_bf16 v[220:223], v[12:15], v[240:243], v[220:223]\n\t" \
    "v_mfma_f32_16x16x32_bf16 v[212:215], v[244:247], v[236:239], v[212:215]\n\t" \
    "ds_read_b128 v[244:247], v254 offset:57344\n\t" \
    "v_mfma_f32_16x16x32_bf16 v[212:215], v[248:251], v[240:243], v[212:215]\n\t" \
    "ds_read_b128 v[248:251], v254 offset:58368\n\t" \
    "ds_read_b128 v[236:239], v253 offset:31744\n\t" \
    "s_setprio 2\n\t" \
    "s_waitcnt lgkmcnt(0)\n\t" \
    "s_barrier \n\t" \
    "v_mfma_f32_16x16x32_bf16 v[204:207], v[120:123], v[224:227], v[204:207]\n\t" \
    "v_mfma_f32_16x16x32_bf16 v[204:207], v[124:127], v[228:231], v[204:207]\n\t" \
    "ds_read_b128 v[240:243], v253 offset:12288\n\t" \
    "v_mfma_f32_16x16x32_bf16 v[208:211], v[128:131], v[224:227], v[208:211]\n\t" \
    "v_mfma_f32_16x16x32_bf16 v[208:211], v[132:135], v[228:231], v[208:211]\n\t" \
    "v_mfma_f32_16x16x32_bf16 v[200:203], v[244:247], v[224:227], v[200:203]\n\t" \
    "v_mfma_f32_16x16x32_bf16 v[200:203], v[248:251], v[228:231], v[200:203]\n\t" \
    "v_mfma_f32_16x16x32_bf16 v[216:219], v[120:123], v[232:235], v[216:219]\n\t" \
    "ds_read_b128 v[224:227], v253 offset:13312\n\t" \
    "s_and_b32 s44, s40, 3\n\t" \
    "v_mfma_f32_16x16x32_bf16 v[216:219], v[124:127], v[236:239], v[216:219]\n\t" \
    "ds_read_b128 v[228:231], v253 offset:32768\n\t" \
    "s_lshl_b32 s44, s44, 17\n\t" \
    "v_mfma_f32_16x16x32_bf16 v[220:223], v[128:131], v[232:235], v[220:223]\n\t" \
    "s_add_u32 s44, s44, %[mb]\n\t" \
    "v_mfma_f32_16x16x32_bf16 v[220:223], v[132:135], v[236:239], v[220:223]\n\t" \
    "s_add_u32 s46, s44, 0x1000\n\t" \
    "v_mfma_f32_16x16x32_bf16 v[212:215], v[244:247], v[232:235], v[212:215]\n\t" \
    "ds_read_b128 v[244:247], v254 offset:59392\n\t" \
    "s_add_u32 s47, s44, 0x1c00\n\t" \
    "v_mfma_f32_16x16x32_bf16 v[212:215], v[248:251], v[236:239], v[212:215]\n\t" \
    "ds_read_b128 v[248:251], v254 offset:60416\n\t" \
    "s_waitcnt lgkmcnt(4)\n\t" \
    "v_mfma_f32_16x16x32_bf16 v[204:207], v[144:147], v[240:243], v[204:207]\n\t" \
    "ds_read_b128 v[232:235], v253 offset:33792\n\t" \
    "s_waitcnt lgkmcnt(4)\n\t" \
    "v_mfma_f32_16x16x32_bf16 v[204:207], v[148:151], v[224:227], v[204:207]\n\t" \
    "ds_read_b128 v[236:239], v253 offset:16384\n\t" \
    "v_mfma_f32_16x16x32_bf16 v[208:211], v[152:155], v[240:243], v[208:211]\n\t" \
    "v_mfma_f32_16x16x32_bf16 v[208:211], v[156:159], v[224:227], v[208:211]\n\t" \
    "v_mfma_f32_16x16x32_bf16 v[200:203], v[136:139], v[240:243], v[200:203]\n\t" \
    "v_mfma_f32_16x16x32_bf16 v[200:203], v[140:143], v[224:227], v[200:203]\n\t" \
    "s_waitcnt lgkmcnt(4)\n\t" \
    "v_mfma_f32_16x16x32_bf16 v[216:219], v[144:147], v[228:231], v[216:219]\n\t" \
    "ds_read_b128 v[240:243], v253 offset:17408\n\t" \
    "s_waitcnt lgkmcnt(2)\n\t" \
    "v_mfma_f32_16x16x32_bf16 v[216:219], v[148:151], v[232:235], v[216:219]\n\t" \
    "ds_read_b128 v[224:227], v253 offset:36864\n\t" \
    "v_mfma_f32_16x16x32_bf16 v[220:223], v[152:155], v[228:231], v[220:223]\n\t" \
    "v_mfma_f32_16x16x32_bf16 v[220:223], v[156:159], v[232:235], v[220:223]\n\t" \
    "v_mfma_f32_16x16x32_bf16 v[212:215], v[136:139], v[228:231], v[212:215]\n\t" \
    "v_mfma_f32_16x16x32_bf16 v[212:215], v[140:143], v[232:235], v[212:215]\n\t" \
    "s_waitcnt lgkmcnt(2)\n\t" \
    "v_mfma_f32_16x16x32_bf16 v[204:207], v[160:163], v[236:239], v[204:207]\n\t" \
    "ds_read_b128 v[228:231], v253 offset:37888\n\t" \
    "s_waitcnt lgkmcnt(2)\n\t" \
    "v_mfma_f32_16x16x32_bf16 v[204:207], v[164:167], v[240:243], v[204:207]\n\t" \
    "ds_read_b128 v[232:235], v253 offset:14336\n\t" \
    "v_mfma_f32_16x16x32_bf16 v[208:211], v[168:171], v[236:239], v[208:211]\n\t" \
    "v_mfma_f32_16x16x32_bf16 v[208:211], v[172:175], v[240:243], v[208:211]\n\t" \
    "v_mfma_f32_16x16x32_bf16 v[200:203], v[244:247], v[236:239], v[200:203]\n\t" \
    "v_mfma_f32_16x16x32_bf16 v[200:203], v[248:251], v[240:243], v[200:203]\n\t" \
    "s_waitcnt lgkmcnt(2)\n\t" \
    "v_mfma_f32_16x16x32_bf16 v[216:219], v[160:163], v[224:227], v[216:219]\n\t" \
    "ds_read_b128 v[236:239], v253 offset:15360\n\t" \
    "s_waitcnt lgkmcnt(2)\n\t" \
    "v_mfma_f32_16x16x32_bf16 v[216:219], v[164:167], v[228:231], v[216:219]\n\t" \
    "ds_read_b128 v[240:243], v253 offset:18432\n\t" \
    "v_mfma_f32_16x16x32_bf16 v[220:223], v[168:171], v[224:227], v[220:223]\n\t" \
    "v_mfma_f32_16x16x32_bf16 v[220:223], v[172:175], v[228:231], v[220:223]\n\t" \
    "v_mfma_f32_16x16x32_bf16 v[212:215], v[244:247], v[224:227], v[212:215]\n\t" \
    "v_mfma_f32_16x16x32_bf16 v[212:215], v[248:251], v[228:231], v[212:215]\n\t" \
    "s_waitcnt lgkmcnt(2)\n\t" \
    "v_mfma_f32_16x16x32_bf16 v[204:207], v[24:27], v[232:235], v[204:207]\n\t" \
    "ds_read_b128 v[224:227], v253 offset:19456\n\t" \
    "v_mov_b64 v[244:245], 0\n\t" \
    "s_waitcnt lgkmcnt(2)\n\t" \
    "v_mfma_f32_16x16x32_bf16 v[204:207], v[28:31], v[236:239], v[204:207]\n\t" \
    "ds_read_b128 v[228:231], v253 offset:34816\n\t" \
    "v_mov_b64 v[246:247], 0\n\t" \
    "v_mfma_f32_16x16x32_bf16 v[208:211], v[32:35], v[232:235], v[208:211]\n\t" \
    "v_mov_b64 v[248:249], 0\n\t" \
    "v_mfma_f32_16x16x32_bf16 v[208:211], v[36:39], v[236:239], v[208:211]\n\t" \
    "v_mov_b64 v[250:251], 0\n\t" \
    "v_mfma_f32_16x16x32_bf16 v[200:203], v[16:19], v[232:235], v[200:203]\n\t" \
    "v_mfma_f32_16x16x32_bf16 v[200:203], v[20:23], v[236:239], v[200:203]\n\t" \
    "s_waitcnt lgkmcnt(2)\n\t" \
    "v_mfma_f32_16x16x32_bf16 v[204:207], v[184:187], v[240:243], v[204:207]\n\t" \
    "ds_read_b128 v[232:235], v253 offset:35840\n\t" \
    "s_waitcnt lgkmcnt(2)\n\t" \
    "v_mfma_f32_16x16x32_bf16 v[204:207], v[188:191], v[224:227], v[204:207]\n\t" \
    "ds_read_b128 v[236:239], v253 offset:38912\n\t" \
    "v_mfma_f32_16x16x32_bf16 v[208:211], v[192:195], v[240:243], v[208:211]\n\t" \
    "v_mfma_f32_16x16x32_bf16 v[208:211], v[196:199], v[224:227], v[208:211]\n\t" \
    "v_mfma_f32_16x16x32_bf16 v[200:203], v[176:179], v[240:243], v[200:203]\n\t" \
    "v_mfma_f32_16x16x32_bf16 v[200:203], v[180:183], v[224:227], v[200:203]\n\t" \
    "ds_read_b128 v[240:243], v253 offset:39936\n\t" \
    "s_waitcnt lgkmcnt(3)\n\t" \
    "v_mfma_f32_16x16x32_bf16 v[216:219], v[24:27], v[228:231], v[216:219]\n\t" \
    "s_waitcnt lgkmcnt(2)\n\t" \
    "v_mfma_f32_16x16x32_bf16 v[216:219], v[28:31], v[232:235], v[216:219]\n\t" \
    "v_cvt_pk_bf16_f32 v204, v204, v208\n\t" \
    "v_cvt_pk_bf16_f32 v205, v205, v209\n\t" \
    "s_waitcnt lgkmcnt(1)\n\t" \
    "v_mfma_f32_16x16x32_bf16 v[216:219], v[184:187], v[236:239], v[216:219]\n\t" \
    "v_cvt_pk_bf16_f32 v206, v206, v210\n\t" \
    "v_cvt_pk_bf16_f32 v207, v207, v211\n\t" \
    "s_waitcnt lgkmcnt(0)\n\t" \
    "v_mfma_f32_16x16x32_bf16 v[216:219], v[188:191], v[240:243], v[216:219]\n\t" \
    "ds_write_b128 v254, v[204:207] offset:10240\n\t" \
    "v_mfma_f32_16x16x32_bf16 v[220:223], v[32:35], v[228:231], v[220:223]\n\t" \
    "v_mfma_f32_16x16x32_bf16 v[220:223], v[36:39], v[232:235], v[220:223]\n\t" \
    "v_cvt_pk_bf16_f32 v200, v200, v201\n\t" \
    "v_cvt_pk_bf16_f32 v201, v202, v203\n\t" \
    "v_mfma_f32_16x16x32_bf16 v[220:223], v[192:195], v[236:239], v[220:223]\n\t" \
    "ds_write_b64 v254, v[200:201] offset:6152\n\t" \
    "v_mfma_f32_16x16x32_bf16 v[220:223], v[196:199], v[240:243], v[220:223]\n\t" \
    "v_mfma_f32_16x16x32_bf16 v[212:215], v[16:19], v[228:231], v[212:215]\n\t" \
    "ds_read_b128 v[200:203], v254 offset:51200\n\t" \
    "ds_read_b128 v[204:207], v254 offset:52224\n\t" \
    "v_mfma_f32_16x16x32_bf16 v[212:215], v[20:23], v[232:235], v[212:215]\n\t" \
    "v_mov_b64 v[208:209], 0\n\t" \
    "v_mov_b64 v[210:211], 0\n\t" \
    "v_mfma_f32_16x16x32_bf16 v[212:215], v[176:179], v[236:239], v[212:215]\n\t" \
    "v_cvt_pk_bf16_f32 v216, v216, v220\n\t" \
    "v_cvt_pk_bf16_f32 v217, v217, v221\n\t" \
    "v_mfma_f32_16x16x32_bf16 v[212:215], v[180:183], v[240:243], v[212:215]\n\t" \
    "v_cvt_pk_bf16_f32 v218, v218, v222\n\t" \
    "v_cvt_pk_bf16_f32 v219, v219, v223\n\t" \
    "s_setprio 0\n\t" \
    "ds_write_b128 v254, v[216:219] offset:16384\n\t" \
    "ds_read_b128 v[216:219], v254 offset:53248\n\t" \
    "ds_read_b128 v[220:223], v254 offset:54272\n\t" \
    "s_nop 2\n\t" \
    "v_cvt_pk_bf16_f32 v212, v212, v213\n\t" \
    "v_cvt_pk_bf16_f32 v213, v214, v215\n\t" \
    "ds_write_b64 v254, v[212:213] offset:12296\n\t" \
    "s_sleep 3\n\t" \
    "s_mov_b32 m0, 0xb000\n\t" \
    "s_nop 0\n\t" \
    "buffer_load_dwordx4 v253, s[60:63], s46 offen sc1 lds\n\t" \
    "s_mov_b32 m0, 0xbc00\n\t" \
    "s_nop 0\n\t" \
    "buffer_load_dwordx4 v253, s[60:63], s47 offen sc1 lds\n\t" \
    "s_waitcnt lgkmcnt(0)\n\t" \
    "v_bfe_i32 v212, v253, 5, 1\n\t" \
    "v_and_b32 v212, 0xaaaa, v212\n\t" \
    "v_or_b32 v212, 0x4444, v212\n\t" \
    "v_and_b32 v213, 0xffff, v255\n\t" \
    "v_lshrrev_b32 v214, 16, v255\n\t" \
    ".Ls24_%=:\n\t" \
    "s_waitcnt vmcnt(0)\n\t" \
    "ds_read_b128 v[224:227], v253 offset:45056\n\t" \
    "ds_read_b128 v[228:231], v253 offset:48128\n\t" \
    "s_waitcnt lgkmcnt(1)\n\t" \
    "v_max_u32 v215, v224, v225\n\t" \
    "v_max3_u32 v215, v226, v227, v215\n\t" \
    "s_waitcnt lgkmcnt(0)\n\t" \
    "v_max3_u32 v215, v228, v229, v215\n\t" \
    "v_max3_u32 v215, v230, v231, v215\n\t" \
    "v_cmp_eq_u32 vcc, -1, v215\n\t" \
    "s_cbranch_vccz .Ls25_%=\n\t" \
    "v_max_u32 v215, v224, v225\n\t" \
    "v_max3_u32 v215, v226, v227, v215\n\t" \
    "v_cmp_eq_u32 vcc, -1, v215\n\t" \
    "s_cbranch_vccz .Ls26_%=\n\t" \
    "s_mov_b32 m0, 0xb000\n\t" \
    "s_nop 0\n\t" \
    "buffer_load_dwordx4 v253, s[60:63], s46 offen sc1 lds\n\t" \
    ".Ls26_%=:\n\t" \
    "v_max_u32 v215, v228, v229\n\t" \
    "v_max3_u32 v215, v230, v231, v215\n\t" \
    "v_cmp_eq_u32 vcc, -1, v215\n\t" \
    "s_cbranch_vccz .Ls27_%=\n\t" \
    "s_mov_b32 m0, 0xbc00\n\t" \
    "s_nop 0\n\t" \
    "buffer_load_dwordx4 v253, s[60:63], s47 offen sc1 lds\n\t" \
    ".Ls27_%=:\n\t" \
    "s_cmp_eq_u32 s41, 0\n\t" \
    "s_cbranch_scc1 .Ls25_%=\n\t" \
    "s_sub_u32 s41, s41, 1\n\t" \
    "s_branch .Ls24_%=\n\t" \
    ".Ls25_%=:\n\t" \
    "s_setprio 3\n\t" \
    "s_waitcnt lgkmcnt(0)\n\t" \
    "s_barrier \n\t" \
    "ds_read_b128 v[232:235], v253 offset:40960\n\t" \
    "ds_read_b128 v[236:239], v253 offset:41984\n\t" \
    "ds_read_b128 v[240:243], v253 offset:43008\n\t" \
    "v_smfmac_f32_16x16x64_bf16 v[244:247], v[224:227], v[0:7], v212\n\t" \
    "v_smfmac_f32_16x16x64_bf16 v[248:251], v[224:227], v[8:15], v212\n\t" \
    "v_smfmac_f32_16x16x64_bf16 v[208:211], v[224:227], v[200:207], v212\n\t" \
    "ds_read_b128 v[200:203], v254 offset:55296\n\t" \
    "ds_read_b128 v[204:207], v254 offset:56320\n\t" \
    "ds_read_b128 v[224:227], v253 offset:44032\n\t" \
    "v_smfmac_f32_16x16x64_bf16 v[208:211], v[228:231], v[16:23], v212\n\t" \
    "v_smfmac_f32_16x16x64_bf16 v[244:247], v[228:231], v[24:31], v212\n\t" \
    "v_smfmac_f32_16x16x64_bf16 v[248:251], v[228:231], v[32:39], v212\n\t" \
    "ds_read_b128 v[228:231], v253 offset:46080\n\t" \
    "s_waitcnt lgkmcnt(6)\n\t" \
    "v_smfmac_f32_16x16x64_bf16 v[208:211], v[232:235], v[216:223], v212\n\t" \
    "ds_read_b128 v[216:219], v254 offset:57344\n\t" \
    "ds_read_b128 v[220:223], v254 offset:58368\n\t" \
    "v_smfmac_f32_16x16x64_bf16 v[244:247], v[232:235], v[40:47], v212\n\t" \
    "v_smfmac_f32_16x16x64_bf16 v[248:251], v[232:235], v[48:55], v212\n\t" \
    "ds_read_b128 v[232:235], v253 offset:47104\n\t" \
    "s_waitcnt lgkmcnt(8)\n\t" \
    "v_smfmac_f32_16x16x64_bf16 v[208:211], v[236:239], v[56:63], v212\n\t" \
    "v_smfmac_f32_16x16x64_bf16 v[244:247], v[236:239], v[64:71], v212\n\t" \
    "v_smfmac_f32_16x16x64_bf16 v[248:251], v[236:239], v[72:79], v212\n\t" \
    "ds_read_b128 v[236:239], v253 offset:49152\n\t" \
    "s_waitcnt lgkmcnt(6)\n\t" \
    "v_smfmac_f32_16x16x64_bf16 v[208:211], v[240:243], v[200:207], v212\n\t" \
    "ds_read_b128 v[200:203], v254 offset:59392\n\t" \
    "ds_read_b128 v[204:207], v254 offset:60416\n\t" \
    "v_smfmac_f32_16x16x64_bf16 v[244:247], v[240:243], v[80:87], v212\n\t" \
    "v_smfmac_f32_16x16x64_bf16 v[248:251], v[240:243], v[88:95], v212\n\t" \
    "ds_read_b128 v[240:243], v253 offset:50176\n\t" \
    "s_waitcnt lgkmcnt(8)\n\t" \
    "v_smfmac_f32_16x16x64_bf16 v[208:211], v[224:227], v[96:103], v212\n\t" \
    "v_smfmac_f32_16x16x64_bf16 v[244:247], v[224:227], v[104:111], v212\n\t" \
    "v_smfmac_f32_16x16x64_bf16 v[248:251], v[224:227], v[112:119], v212\n\t" \
    "s_waitcnt lgkmcnt(5)\n\t" \
    "v_smfmac_f32_16x16x64_bf16 v[208:211], v[228:231], v[216:223], v212\n\t" \
    "s_waitcnt lgkmcnt(4)\n\t" \
    "v_smfmac_f32_16x16x64_bf16 v[208:211], v[232:235], v[136:143], v212\n\t" \
    "s_waitcnt lgkmcnt(1)\n\t" \
    "v_smfmac_f32_16x16x64_bf16 v[208:211], v[236:239], v[200:207], v212\n\t" \
    "s_waitcnt lgkmcnt(0)\n\t" \
    "v_smfmac_f32_16x16x64_bf16 v[208:211], v[240:243], v[176:183], v212\n\t" \
    "v_smfmac_f32_16x16x64_bf16 v[244:247], v[228:231], v[120:127], v212\n\t" \
    "v_smfmac_f32_16x16x64_bf16 v[244:247], v[232:235], v[144:151], v212\n\t" \
    "v_smfmac_f32_16x16x64_bf16 v[244:247], v[236:239], v[160:167], v212\n\t" \
    "v_add_f32 v208, v208, v210\n\t" \
    "v_add_f32 v209, v209, v211\n\t" \
    "v_cvt_pk_bf16_f32 v208, v208, v209\n\t" \
    "v_smfmac_f32_16x16x64_bf16 v[244:247], v[240:243], v[184:191], v212\n\t" \
    "ds_write_b16 v213, v208 offset:0\n\t" \
    "ds_write_b16_d16_hi v213, v208 offset:16\n\t" \
    "v_smfmac_f32_16x16x64_bf16 v[248:251], v[228:231], v[128:135], v212\n\t" \
    "v_smfmac_f32_16x16x64_bf16 v[248:251], v[232:235], v[152:159], v212\n\t" \
    "v_smfmac_f32_16x16x64_bf16 v[248:251], v[236:239], v[168:175], v212\n\t" \
    "v_add_f32 v244, v244, v246\n\t" \
    "v_add_f32 v245, v245, v247\n\t" \
    "v_smfmac_f32_16x16x64_bf16 v[248:251], v[240:243], v[192:199], v212\n\t" \
    "s_nop 7\n\t" \
    "v_add_f32 v248, v248, v250\n\t" \
    "v_add_f32 v249, v249, v251\n\t" \
    "v_cvt_pk_bf16_f32 v244, v244, v248\n\t" \
    "v_cvt_pk_bf16_f32 v245, v245, v249\n\t" \
    "ds_write2_b32 v214, v244, v245 offset1:4\n\t" \
    "s_setprio 0\n\t" \
    "s_sleep 1\n\t" \
    "ds_read_b128 v[200:203], v254 offset:12288\n\t" \
    "ds_read_b128 v[224:227], v254 offset:24576\n\t" \
    "ds_read_b128 v[204:207], v254 offset:13312\n\t" \
    "ds_read_b128 v[228:231], v254 offset:25600\n\t" \
    "ds_read_b128 v[208:211], v254 offset:14336\n\t" \
    "ds_read_b128 v[232:235], v254 offset:26624\n\t" \
    "ds_read_b128 v[212:215], v254 offset:15360\n\t" \
    "ds_read_b128 v[236:239], v254 offset:27648\n\t" \
    "ds_read_b128 v[216:219], v254 offset:16384\n\t" \
    "ds_read_b128 v[220:223], v254 offset:17408\n\t" \
    "s_and_b32 s44, s40, 3\n\t" \
    "s_lshl_b32 s44, s44, 17\n\t" \
    "s_add_u32 s50, s44, %[pb]\n\t" \
    "s_add_u32 s51, s44, %[pm]\n\t" \
    "s_mov_b32 s52, 0xf0000\n\t" \
    "s_mov_b32 s53, 0xfff00ff\n\t" \
    "s_mov_b32 s54, 0x1f0001\n\t" \
    "s_mov_b32 s55, 0x1fff01ff\n\t" \
    "s_mov_b32 s56, 0x3f0003\n\t" \
    "s_mov_b32 s57, 0x3fff03ff\n\t" \
    "s_mov_b32 s58, 0x7f0007\n\t" \
    "s_mov_b32 s59, 0x7fff07ff\n\t" \
    "s_waitcnt lgkmcnt(8)\n\t" \
    "v_mfma_f32_16x16x32_bf16 v[240:243], v[224:227], v[200:203], 0\n\t" \
    "ds_read_b128 v[224:227], v254 offset:28672\n\t" \
    "s_waitcnt lgkmcnt(7)\n\t" \
    "v_mfma_f32_16x16x32_bf16 v[240:243], v[228:231], v[204:207], v[240:243]\n\t" \
    "ds_read_b128 v[228:231], v254 offset:29696\n\t" \
    "s_waitcnt lgkmcnt(6)\n\t" \
    "v_mfma_f32_16x16x32_bf16 v[240:243], v[232:235], v[208:211], v[240:243]\n\t" \
    "ds_read_b128 v[232:235], v254 offset:18432\n\t" \
    "s_waitcnt lgkmcnt(5)\n\t" \
    "v_mfma_f32_16x16x32_bf16 v[240:243], v[236:239], v[212:215], v[240:243]\n\t" \
    "ds_read_b128 v[236:239], v254 offset:19456\n\t" \
    "v_and_b32 v252, 0xffffff7f, v254\n\t" \
    "s_waitcnt lgkmcnt(0)\n\t" \
    "s_barrier \n\t" \
    "v_mfma_f32_16x16x32_bf16 v[240:243], v[224:227], v[216:219], v[240:243]\n\t" \
    "ds_read_b128 v[224:227], v254 offset:20480\n\t" \
    "v_mfma_f32_16x16x32_bf16 v[240:243], v[228:231], v[220:223], v[240:243]\n\t" \
    "ds_read_b128 v[228:231], v254 offset:21504\n\t" \
    "v_mfma_f32_16x16x32_bf16 v[244:247], v[232:235], v[200:203], 0\n\t" \
    "ds_read_b128 v[232:235], v254 offset:22528\n\t" \
    "v_mfma_f32_16x16x32_bf16 v[244:247], v[236:239], v[204:207], v[244:247]\n\t" \
    "ds_read_b128 v[236:239], v254 offset:23552\n\t" \
    "s_waitcnt lgkmcnt(3)\n\t" \
    "v_mfma_f32_16x16x32_bf16 v[244:247], v[224:227], v[208:211], v[244:247]\n\t" \
    "ds_read_b128 v[224:227], v252 offset:3072\n\t" \
    "s_waitcnt lgkmcnt(3)\n\t" \
    "v_mfma_f32_16x16x32_bf16 v[244:247], v[228:231], v[212:215], v[244:247]\n\t" \
    "ds_read_b128 v[228:231], v252 offset:3200\n\t" \
    "s_waitcnt lgkmcnt(3)\n\t" \
    "v_mfma_f32_16x16x32_bf16 v[244:247], v[232:235], v[216:219], v[244:247]\n\t" \
    "ds_read_b128 v[232:235], v252 offset:4096\n\t" \
    "s_waitcnt lgkmcnt(3)\n\t" \
    "v_mfma_f32_16x16x32_bf16 v[244:247], v[236:239], v[220:223], v[244:247]\n\t" \
    "ds_read_b128 v[236:239], v252 offset:4224\n\t" \
    "s_waitcnt lgkmcnt(3)\n\t" \
    "v_mfma_f32_16x16x32_bf16 v[248:251], v[224:227], v[200:203], 0\n\t" \
    "ds_read_b128 v[224:227], v252 offset:5120\n\t" \
    "s_waitcnt lgkmcnt(3)\n\t" \
    "v_mfma_f32_16x16x32_bf16 v[248:251], v[228:231], v[204:207], v[248:251]\n\t" \
    "ds_read_b128 v[228:231], v252 offset:5248\n\t" \
    "s_waitcnt lgkmcnt(3)\n\t" \
    "v_mfma_f32_16x16x32_bf16 v[248:251], v[232:235], v[208:211], v[248:251]\n\t" \
    "s_waitcnt lgkmcnt(2)\n\t" \
    "v_mfma_f32_16x16x32_bf16 v[248:251], v[236:239], v[212:215], v[248:251]\n\t" \
    "s_waitcnt lgkmcnt(1)\n\t" \
    "v_mfma_f32_16x16x32_bf16 v[248:251], v[224:227], v[216:219], v[248:251]\n\t" \
    "s_waitcnt lgkmcnt(0)\n\t" \
    "v_mfma_f32_16x16x32_bf16 v[248:251], v[228:231], v[220:223], v[248:251]\n\t" \
    "v_mov_b32 v226, 0xff7fffff\n\t" \
    "v_cndmask_b32 v240, v240, v226, s[52:53]\n\t" \
    "v_cndmask_b32 v241, v241, v226, s[54:55]\n\t" \
    "v_cndmask_b32 v242, v242, v226, s[56:57]\n\t" \
    "v_cndmask_b32 v243, v243, v226, s[58:59]\n\t" \
    "v_max3_f32 v224, v240, v241, v242\n\t" \
    "v_max3_f32 v225, v244, v245, v246\n\t" \
    "v_max3_f32 v224, v224, v243, v247\n\t" \
    "v_lshrrev_b32 v200, 5, v253\n\t" \
    "v_and_b32 v201, 0xf0, v253\n\t" \
    "v_and_or_b32 v200, v200, 8, v201\n\t" \
    "v_max3_f32 v224, v224, v248, v249\n\t" \
    "v_max3_f32 v225, v225, v250, v251\n\t" \
    "v_max_f32 v224, v224, v225\n\t" \
    "v_mov_b32 v225, v224\n\t" \
    "s_nop 1\n\t" \
    "v_permlane16_swap_b32 v224, v225\n\t" \
    "v_max_f32 v224, v224, v225\n\t" \
    "v_mov_b32 v225, v224\n\t" \
    "s_nop 1\n\t" \
    "v_permlane32_swap_b32 v224, v225\n\t" \
    "v_max_f32 v224, v224, v225\n\t" \
    "v_mul_f32 v224, %[scl], v224\n\t" \
    "v_fma_f32 v240, v240, %[scl], -v224\n\t" \
    "v_fma_f32 v241, v241, %[scl], -v224\n\t" \
    "v_exp_f32 v240, v240\n\t" \
    "v_fma_f32 v242, v242, %[scl], -v224\n\t" \
    "v_exp_f32 v241, v241\n\t" \
    "v_fma_f32 v243, v243, %[scl], -v224\n\t" \
    "v_exp_f32 v242, v242\n\t" \
    "v_fma_f32 v244, v244, %[scl], -v224\n\t" \
    "v_exp_f32 v243, v243\n\t" \
    "v_fma_f32 v245, v245, %[scl], -v224\n\t" \
    "v_exp_f32 v244, v244\n\t" \
    "v_fma_f32 v246, v246, %[scl], -v224\n\t" \
    "v_exp_f32 v245, v245\n\t" \
    "v_fma_f32 v247, v247, %[scl], -v224\n\t" \
    "v_exp_f32 v246, v246\n\t" \
    "v_fma_f32 v248, v248, %[scl], -v224\n\t" \
    "v_exp_f32 v247, v247\n\t" \
    "v_fma_f32 v249, v249, %[scl], -v224\n\t" \
    "v_exp_f32 v248, v248\n\t" \
    "v_fma_f32 v250, v250, %[scl], -v224\n\t" \
    "v_exp_f32 v249, v249\n\t" \
    "v_fma_f32 v251, v251, %[scl], -v224\n\t" \
    "v_exp_f32 v250, v250\n\t" \
    "v_exp_f32 v251, v251\n\t" \
    "v_add_f32 v224, v240, v241\n\t" \
    "v_add_f32 v225, v242, v243\n\t" \
    "v_add_f32 v226, v244, v245\n\t" \
    "v_add_f32 v227, v246, v247\n\t" \
    "v_add_f32 v232, v248, v249\n\t" \
    "v_add_f32 v233, v250, v251\n\t" \
    "v_add_f32 v224, v224, v225\n\t" \
    "v_add_f32 v226, v226, v227\n\t" \
    "v_add_f32 v232, v232, v233\n\t" \
    "v_add_f32 v224, v224, v226\n\t" \
    "v_fma_f32 v224, v232, 0.5, v224\n\t" \
    "v_mov_b32 v225, v224\n\t" \
    "s_nop 1\n\t" \
    "v_permlane16_swap_b32 v224, v225\n\t" \
    "v_add_f32 v224, v224, v225\n\t" \
    "v_mov_b32 v225, v224\n\t" \
    "s_nop 1\n\t" \
    "v_permlane32_swap_b32 v224, v225\n\t" \
    "v_add_f32 v224, v224, v225\n\t" \
    "v_rcp_f32 v224, v224\n\t" \
    "s_nop 0\n\t" \
    "v_mul_f32 v240, v240, v224\n\t" \
    "v_mul_f32 v241, v241, v224\n\t" \
    "v_mul_f32 v242, v242, v224\n\t" \
    "v_mul_f32 v243, v243, v224\n\t" \
    "v_mul_f32 v244, v244, v224\n\t" \
    "v_mul_f32 v245, v245, v224\n\t" \
    "v_mul_f32 v246, v246, v224\n\t" \
    "v_mul_f32 v247, v247, v224\n\t" \
    "v_mul_f32 v248, v248, v224\n\t" \
    "v_mul_f32 v249, v249, v224\n\t" \
    "v_mul_f32 v250, v250, v224\n\t" \
    "v_mul_f32 v251, v251, v224\n\t" \
    "v_cvt_pk_bf16_f32 v204, |v244|, |v240|\n\t" \
    "v_cvt_pk_bf16_f32 v205, |v245|, |v241|\n\t" \
    "v_cvt_pk_bf16_f32 v206, |v246|, |v242|\n\t" \
    "v_cvt_pk_bf16_f32 v207, |v247|, |v243|\n\t" \
    "v_cvt_pk_bf16_f32 v208, |v248|, |v249|\n\t" \
    "v_cvt_pk_bf16_f32 v209, |v250|, |v251|\n\t" \
    "buffer_store_dwordx4 v[204:207], v253, s[60:63], s50 offen sc1\n\t" \
    "s_mov_b32 exec_lo, -1\n\t" \
    "s_mov_b32 exec_hi, 0\n\t" \
    "buffer_store_dwordx2 v[208:209], v200, s[60:63], s51 offen sc1\n\t" \
    "s_mov_b32 exec_hi, -1\n\t" \
    "ds_read_b128 v[224:227], v253 offset:0\n\t" \
    "ds_read_b128 v[244:247], v254 offset:53248\n\t" \
    "ds_read_b128 v[228:231], v253 offset:1024\n\t" \
    "ds_read_b128 v[232:235], v253 offset:20480\n\t" \
    "ds_read_b128 v[236:239], v253 offset:21504\n\t" \
    "ds_read_b128 v[248:251], v254 offset:54272\n\t" \
    "s_add_u32 s40, s40, 1\n\t" \
    "s_cmp_lg_u32 s40, 64\n\t" \
    "s_cbranch_scc1 .Ls23_%=\n\t" \
    "s_branch .Lend%=\n\t" \
    ".Ls1_%=:\n\t" \
    "v_add_u32 v244, 0x18000, v243\n\t" \
    "s_waitcnt vmcnt(12) lgkmcnt(0)\n\t" \
    "s_barrier \n\t" \
    "s_add_u32 m0, %[wm], 0x12000\n\t" \
    "s_mov_b32 s44, 0x1e0000\n\t" \
    "buffer_load_dwordx4 v240, %[wr], s44 offen offset:0 nt lds\n\t" \
    "buffer_load_dwordx4 v241, %[wr], s44 offen offset:1024 nt lds\n\t" \
    "buffer_load_dwordx4 v242, %[wr], s44 offen offset:2048 nt lds\n\t" \
    "ds_read_b64_tr_b16 v[56:57], v244 offset:0\n\t" \
    "ds_read_b64_tr_b16 v[58:59], v244 offset:1536\n\t" \
    "ds_read_b64_tr_b16 v[64:65], v244 offset:32\n\t" \
    "ds_read_b64_tr_b16 v[66:67], v244 offset:1568\n\t" \
    "ds_read_b64_tr_b16 v[72:73], v244 offset:64\n\t" \
    "ds_read_b64_tr_b16 v[74:75], v244 offset:1600\n\t" \
    "v_add_u32 v244, 0x1e000, v243\n\t" \
    "s_waitcnt vmcnt(12) lgkmcnt(0)\n\t" \
    "s_barrier \n\t" \
    "s_add_u32 m0, %[wm], 0x18000\n\t" \
    "s_mov_b32 s44, 0x240000\n\t" \
    "buffer_load_dwordx4 v240, %[wr], s44 offen offset:0 nt lds\n\t" \
    "buffer_load_dwordx4 v241, %[wr], s44 offen offset:1024 nt lds\n\t" \
    "buffer_load_dwordx4 v242, %[wr], s44 offen offset:2048 nt lds\n\t" \
    "ds_read_b64_tr_b16 v[60:61], v244 offset:0\n\t" \
    "ds_read_b64_tr_b16 v[62:63], v244 offset:1536\n\t" \
    "ds_read_b64_tr_b16 v[68:69], v244 offset:32\n\t" \
    "ds_read_b64_tr_b16 v[70:71], v244 offset:1568\n\t" \
    "ds_read_b64_tr_b16 v[76:77], v244 offset:64\n\t" \
    "ds_read_b64_tr_b16 v[78:79], v244 offset:1600\n\t" \
    "v_add_u32 v244, 0x0, v243\n\t" \
    "s_waitcnt vmcnt(12) lgkmcnt(0)\n\t" \
    "s_barrier \n\t" \
    "s_add_u32 m0, %[wm], 0x1e000\n\t" \
    "s_mov_b32 s44, 0x2a0000\n\t" \
    "buffer_load_dwordx4 v240, %[wr], s44 offen offset:0 nt lds\n\t" \
    "buffer_load_dwordx4 v241, %[wr], s44 offen offset:1024 nt lds\n\t" \
    "buffer_load_dwordx4 v242, %[wr], s44 offen offset:2048 nt lds\n\t" \
    "ds_read_b64_tr_b16 v[96:97], v244 offset:0\n\t" \
    "ds_read_b64_tr_b16 v[98:99], v244 offset:1536\n\t" \
    "ds_read_b64_tr_b16 v[104:105], v244 offset:32\n\t" \
    "ds_read_b64_tr_b16 v[106:107], v244 offset:1568\n\t" \
    "ds_read_b64_tr_b16 v[112:113], v244 offset:64\n\t" \
    "ds_read_b64_tr_b16 v[114:115], v244 offset:1600\n\t" \
    "v_add_u32 v244, 0x6000, v243\n\t" \
    "s_waitcnt vmcnt(12) lgkmcnt(0)\n\t" \
    "s_barrier \n\t" \
    "s_add_u32 m0, %[wm], 0x0\n\t" \
    "s_mov_b32 s44, 0x300000\n\t" \
    "buffer_load_dwordx4 v240, %[wr], s44 offen offset:0 nt lds\n\t" \
    "buffer_load_dwordx4 v241, %[wr], s44 offen offset:1024 nt lds\n\t" \
    "buffer_load_dwordx4 v242, %[wr], s44 offen offset:2048 nt lds\n\t" \
    "ds_read_b64_tr_b16 v[100:101], v244 offset:0\n\t" \
    "ds_read_b64_tr_b16 v[102:103], v244 offset:1536\n\t" \
    "ds_read_b64_tr_b16 v[108:109], v244 offset:32\n\t" \
    "ds_read_b64_tr_b16 v[110:111], v244 offset:1568\n\t" \
    "ds_read_b64_tr_b16 v[116:117], v244 offset:64\n\t" \
    "ds_read_b64_tr_b16 v[118:119], v244 offset:1600\n\t" \
    "v_add_u32 v244, 0xc000, v243\n\t" \
    "s_waitcnt vmcnt(12) lgkmcnt(0)\n\t" \
    "s_barrier \n\t" \
    "s_add_u32 m0, %[wm], 0x6000\n\t" \
    "s_mov_b32 s44, 0x360000\n\t" \
    "buffer_load_dwordx4 v240, %[wr], s44 offen offset:0 nt lds\n\t" \
    "buffer_load_dwordx4 v241, %[wr], s44 offen offset:1024 nt lds\n\t" \
    "buffer_load_dwordx4 v242, %[wr], s44 offen offset:2048 nt lds\n\t" \
    "ds_read_b64_tr_b16 v[200:201], v244 offset:0\n\t" \
    "ds_read_b64_tr_b16 v[202:203], v244 offset:1536\n\t" \
    "ds_read_b64_tr_b16 v[0:1], v244 offset:32\n\t" \
    "ds_read_b64_tr_b16 v[2:3], v244 offset:1568\n\t" \
    "ds_read_b64_tr_b16 v[8:9], v244 offset:64\n\t" \
    "ds_read_b64_tr_b16 v[10:11], v244 offset:1600\n\t" \
    "v_add_u32 v244, 0x12000, v243\n\t" \
    "s_waitcnt vmcnt(12) lgkmcnt(0)\n\t" \
    "s_barrier \n\t" \
    "s_add_u32 m0, %[wm], 0xc000\n\t" \
    "s_mov_b32 s44, 0x3c0000\n\t" \
    "buffer_load_dwordx4 v240, %[wr], s44 offen offset:0 nt lds\n\t" \
    "buffer_load_dwordx4 v241, %[wr], s44 offen offset:1024 nt lds\n\t" \
    "buffer_load_dwordx4 v242, %[wr], s44 offen offset:2048 nt lds\n\t" \
    "ds_read_b64_tr_b16 v[204:205], v244 offset:0\n\t" \
    "ds_read_b64_tr_b16 v[206:207], v244 offset:1536\n\t" \
    "ds_read_b64_tr_b16 v[4:5], v244 offset:32\n\t" \
    "ds_read_b64_tr_b16 v[6:7], v244 offset:1568\n\t" \
    "ds_read_b64_tr_b16 v[12:13], v244 offset:64\n\t" \
    "ds_read_b64_tr_b16 v[14:15], v244 offset:1600\n\t" \
    "v_add_u32 v244, 0x18000, v243\n\t" \
    "s_waitcnt vmcnt(12) lgkmcnt(0)\n\t" \
    "s_barrier \n\t" \
    "s_add_u32 m0, %[wm], 0x12000\n\t" \
    "s_mov_b32 s44, 0x420000\n\t" \
    "buffer_load_dwordx4 v240, %[wr], s44 offen offset:0 nt lds\n\t" \
    "buffer_load_dwordx4 v241, %[wr], s44 offen offset:1024 nt lds\n\t" \
    "buffer_load_dwordx4 v242, %[wr], s44 offen offset:2048 nt lds\n\t" \
    "ds_read_b64_tr_b16 v[208:209], v244 offset:0\n\t" \
    "ds_read_b64_tr_b16 v[210:211], v244 offset:1536\n\t" \
    "ds_read_b64_tr_b16 v[40:41], v244 offset:32\n\t" \
    "ds_read_b64_tr_b16 v[42:43], v244 offset:1568\n\t" \
    "ds_read_b64_tr_b16 v[48:49], v244 offset:64\n\t" \
    "ds_read_b64_tr_b16 v[50:51], v244 offset:1600\n\t" \
    "v_add_u32 v244, 0x1e000, v243\n\t" \
    "s_waitcnt vmcnt(12) lgkmcnt(0)\n\t" \
    "s_barrier \n\t" \
    "s_add_u32 m0, %[wm], 0x18000\n\t" \
    "s_mov_b32 s44, 0x480000\n\t" \
    "buffer_load_dwordx4 v240, %[wr], s44 offen offset:0 nt lds\n\t" \
    "buffer_load_dwordx4 v241, %[wr], s44 offen offset:1024 nt lds\n\t" \
    "buffer_load_dwordx4 v242, %[wr], s44 offen offset:2048 nt lds\n\t" \
    "ds_read_b64_tr_b16 v[212:213], v244 offset:0\n\t" \
    "ds_read_b64_tr_b16 v[214:215], v244 offset:1536\n\t" \
    "ds_read_b64_tr_b16 v[44:45], v244 offset:32\n\t" \
    "ds_read_b64_tr_b16 v[46:47], v244 offset:1568\n\t" \
    "ds_read_b64_tr_b16 v[52:53], v244 offset:64\n\t" \
    "ds_read_b64_tr_b16 v[54:55], v244 offset:1600\n\t" \
    "v_add_u32 v244, 0x0, v243\n\t" \
    "s_waitcnt vmcnt(12) lgkmcnt(0)\n\t" \
    "s_barrier \n\t" \
    "v_mov_b32 v246, 0\n\t" \
    "buffer_load_dword v246, v246, s[60:63], %[mo] offen sc1\n\t" \
    "s_add_u32 m0, %[wm], 0x1e000\n\t" \
    "s_mov_b32 s44, 0x4e0000\n\t" \
    "buffer_load_dwordx4 v240, %[wr], s44 offen offset:0 nt lds\n\t" \
    "buffer_load_dwordx4 v241, %[wr], s44 offen offset:1024 nt lds\n\t" \
    "buffer_load_dwordx4 v242, %[wr], s44 offen offset:2048 nt lds\n\t" \
    "ds_read_b64_tr_b16 v[136:137], v244 offset:0\n\t" \
    "ds_read_b64_tr_b16 v[138:139], v244 offset:1536\n\t" \
    "ds_read_b64_tr_b16 v[144:145], v244 offset:32\n\t" \
    "ds_read_b64_tr_b16 v[146:147], v244 offset:1568\n\t" \
    "ds_read_b64_tr_b16 v[152:153], v244 offset:64\n\t" \
    "ds_read_b64_tr_b16 v[154:155], v244 offset:1600\n\t" \
    "v_add_u32 v244, 0x6000, v243\n\t" \
    "s_waitcnt vmcnt(12) lgkmcnt(0)\n\t" \
    "s_barrier \n\t" \
    "s_add_u32 m0, %[wm], 0x0\n\t" \
    "s_mov_b32 s44, 0x540000\n\t" \
    "buffer_load_dwordx4 v240, %[wr], s44 offen offset:0 nt lds\n\t" \
    "buffer_load_dwordx4 v241, %[wr], s44 offen offset:1024 nt lds\n\t" \
    "buffer_load_dwordx4 v242, %[wr], s44 offen offset:2048 nt lds\n\t" \
    "ds_read_b64_tr_b16 v[140:141], v244 offset:0\n\t" \
    "ds_read_b64_tr_b16 v[142:143], v244 offset:1536\n\t" \
    "ds_read_b64_tr_b16 v[148:149], v244 offset:32\n\t" \
    "ds_read_b64_tr_b16 v[150:151], v244 offset:1568\n\t" \
    "ds_read_b64_tr_b16 v[156:157], v244 offset:64\n\t" \
    "ds_read_b64_tr_b16 v[158:159], v244 offset:1600\n\t" \
    "v_add_u32 v244, 0xc000, v243\n\t" \
    "s_waitcnt vmcnt(12) lgkmcnt(0)\n\t" \
    "s_barrier \n\t" \
    "s_add_u32 m0, %[wm], 0x6000\n\t" \
    "s_mov_b32 s44, 0x5a0000\n\t" \
    "buffer_load_dwordx4 v240, %[wr], s44 offen offset:0 nt lds\n\t" \
    "buffer_load_dwordx4 v241, %[wr], s44 offen offset:1024 nt lds\n\t" \
    "buffer_load_dwordx4 v242, %[wr], s44 offen offset:2048 nt lds\n\t" \
    "ds_read_b64_tr_b16 v[216:217], v244 offset:0\n\t" \
    "ds_read_b64_tr_b16 v[218:219], v244 offset:1536\n\t" \
    "ds_read_b64_tr_b16 v[80:81], v244 offset:32\n\t" \
    "ds_read_b64_tr_b16 v[82:83], v244 offset:1568\n\t" \
    "ds_read_b64_tr_b16 v[88:89], v244 offset:64\n\t" \
    "ds_read_b64_tr_b16 v[90:91], v244 offset:1600\n\t" \
    "v_add_u32 v244, 0x12000, v243\n\t" \
    "s_waitcnt vmcnt(12) lgkmcnt(0)\n\t" \
    "s_barrier \n\t" \
    "s_add_u32 m0, %[wm], 0xc000\n\t" \
    "s_mov_b32 s44, 0x600000\n\t" \
    "buffer_load_dwordx4 v240, %[wr], s44 offen offset:0 nt lds\n\t" \
    "buffer_load_dwordx4 v241, %[wr], s44 offen offset:1024 nt lds\n\t" \
    "buffer_load_dwordx4 v242, %[wr], s44 offen offset:2048 nt lds\n\t" \
    "ds_read_b64_tr_b16 v[220:221], v244 offset:0\n\t" \
    "ds_read_b64_tr_b16 v[222:223], v244 offset:1536\n\t" \
    "ds_read_b64_tr_b16 v[84:85], v244 offset:32\n\t" \
    "ds_read_b64_tr_b16 v[86:87], v244 offset:1568\n\t" \
    "ds_read_b64_tr_b16 v[92:93], v244 offset:64\n\t" \
    "ds_read_b64_tr_b16 v[94:95], v244 offset:1600\n\t" \
    "v_add_u32 v244, 0x18000, v243\n\t" \
    "s_waitcnt vmcnt(12) lgkmcnt(0)\n\t" \
    "s_barrier \n\t" \
    "s_add_u32 m0, %[wm], 0x12000\n\t" \
    "s_mov_b32 s44, 0x660000\n\t" \
    "buffer_load_dwordx4 v240, %[wr], s44 offen offset:0 nt lds\n\t" \
    "buffer_load_dwordx4 v241, %[wr], s44 offen offset:1024 nt lds\n\t" \
    "buffer_load_dwordx4 v242, %[wr], s44 offen offset:2048 nt lds\n\t" \
    "ds_read_b64_tr_b16 v[16:17], v244 offset:0\n\t" \
    "ds_read_b64_tr_b16 v[18:19], v244 offset:1536\n\t" \
    "ds_read_b64_tr_b16 v[24:25], v244 offset:32\n\t" \
    "ds_read_b64_tr_b16 v[26:27], v244 offset:1568\n\t" \
    "ds_read_b64_tr_b16 v[32:33], v244 offset:64\n\t" \
    "ds_read_b64_tr_b16 v[34:35], v244 offset:1600\n\t" \
    "v_add_u32 v244, 0x1e000, v243\n\t" \
    "s_waitcnt vmcnt(12) lgkmcnt(0)\n\t" \
    "s_barrier \n\t" \
    ".Ls11_%=:\n\t" \
    "v_readfirstlane_b32 s43, v246\n\t" \
    "s_nop 0\n\t" \
    "s_cmp_lg_u32 s43, -1\n\t" \
    "s_cbranch_scc1 .Ls12_%=\n\t" \
    "s_cmp_eq_u32 s41, 0\n\t" \
    "s_cbranch_scc1 .Ls12_%=\n\t" \
    "s_sub_u32 s41, s41, 1\n\t" \
    "s_sleep 4\n\t" \
    "v_mov_b32 v246, 0\n\t" \
    "buffer_load_dword v246, v246, s[60:63], %[mo] offen sc1\n\t" \
    "s_waitcnt vmcnt(0)\n\t" \
    "s_branch .Ls11_%=\n\t" \
    ".Ls12_%=:\n\t" \
    "v_mov_b32 v246, 0x8000\n\t" \
    "s_mov_b64 exec, 1\n\t" \
    "buffer_store_dword v246, v246, s[60:63], %[mo] offen sc1\n\t" \
    "s_mov_b64 exec, -1\n\t" \
    "s_and_b32 s43, s43, 0x2000\n\t" \
    "s_xor_b32 s43, s43, %[lx]\n\t" \
    "s_add_u32 s60, s60, s43\n\t" \
    "s_addc_u32 s61, s61, 0\n\t" \
    "s_add_u32 m0, %[wm], 0x18000\n\t" \
    "s_mov_b32 s44, 0x6c0000\n\t" \
    "buffer_load_dwordx4 v240, %[wr], s44 offen offset:0 nt lds\n\t" \
    "buffer_load_dwordx4 v241, %[wr], s44 offen offset:1024 nt lds\n\t" \
    "buffer_load_dwordx4 v242, %[wr], s44 offen offset:2048 nt lds\n\t" \
    "ds_read_b64_tr_b16 v[20:21], v244 offset:0\n\t" \
    "ds_read_b64_tr_b16 v[22:23], v244 offset:1536\n\t" \
    "ds_read_b64_tr_b16 v[28:29], v244 offset:32\n\t" \
    "ds_read_b64_tr_b16 v[30:31], v244 offset:1568\n\t" \
    "ds_read_b64_tr_b16 v[36:37], v244 offset:64\n\t" \
    "ds_read_b64_tr_b16 v[38:39], v244 offset:1600\n\t" \
    "v_add_u32 v244, 0x0, v243\n\t" \
    "s_waitcnt vmcnt(12) lgkmcnt(0)\n\t" \
    "s_barrier \n\t" \
    "s_add_u32 m0, %[wm], 0x1e000\n\t" \
    "s_mov_b32 s44, 0x720000\n\t" \
    "buffer_load_dwordx4 v240, %[wr], s44 offen offset:0 nt lds\n\t" \
    "buffer_load_dwordx4 v241, %[wr], s44 offen offset:1024 nt lds\n\t" \
    "buffer_load_dwordx4 v242, %[wr], s44 offen offset:2048 nt lds\n\t" \
    "ds_read_b64_tr_b16 v[224:225], v244 offset:0\n\t" \
    "ds_read_b64_tr_b16 v[226:227], v244 offset:1536\n\t" \
    "ds_read_b64_tr_b16 v[120:121], v244 offset:32\n\t" \
    "ds_read_b64_tr_b16 v[122:123], v244 offset:1568\n\t" \
    "ds_read_b64_tr_b16 v[128:129], v244 offset:64\n\t" \
    "ds_read_b64_tr_b16 v[130:131], v244 offset:1600\n\t" \
    "v_add_u32 v244, 0x6000, v243\n\t" \
    "s_waitcnt vmcnt(12) lgkmcnt(0)\n\t" \
    "s_barrier \n\t" \
    "ds_read_b64_tr_b16 v[228:229], v244 offset:0\n\t" \
    "ds_read_b64_tr_b16 v[230:231], v244 offset:1536\n\t" \
    "ds_read_b64_tr_b16 v[124:125], v244 offset:32\n\t" \
    "ds_read_b64_tr_b16 v[126:127], v244 offset:1568\n\t" \
    "ds_read_b64_tr_b16 v[132:133], v244 offset:64\n\t" \
    "ds_read_b64_tr_b16 v[134:135], v244 offset:1600\n\t" \
    "v_add_u32 v244, 0xc000, v243\n\t" \
    "s_waitcnt vmcnt(9) lgkmcnt(0)\n\t" \
    "s_barrier \n\t" \
    "ds_read_b64_tr_b16 v[232:233], v244 offset:0\n\t" \
    "ds_read_b64_tr_b16 v[234:235], v244 offset:1536\n\t" \
    "ds_read_b64_tr_b16 v[160:161], v244 offset:32\n\t" \
    "ds_read_b64_tr_b16 v[162:163], v244 offset:1568\n\t" \
    "ds_read_b64_tr_b16 v[168:169], v244 offset:64\n\t" \
    "ds_read_b64_tr_b16 v[170:171], v244 offset:1600\n\t" \
    "v_add_u32 v244, 0x12000, v243\n\t" \
    "s_waitcnt vmcnt(6) lgkmcnt(0)\n\t" \
    "s_barrier \n\t" \
    "ds_read_b64_tr_b16 v[236:237], v244 offset:0\n\t" \
    "ds_read_b64_tr_b16 v[238:239], v244 offset:1536\n\t" \
    "ds_read_b64_tr_b16 v[164:165], v244 offset:32\n\t" \
    "ds_read_b64_tr_b16 v[166:167], v244 offset:1568\n\t" \
    "ds_read_b64_tr_b16 v[172:173], v244 offset:64\n\t" \
    "ds_read_b64_tr_b16 v[174:175], v244 offset:1600\n\t" \
    "v_add_u32 v244, 0x18000, v243\n\t" \
    "s_waitcnt vmcnt(3) lgkmcnt(0)\n\t" \
    "s_barrier \n\t" \
    "ds_read_b64_tr_b16 v[176:177], v244 offset:0\n\t" \
    "ds_read_b64_tr_b16 v[178:179], v244 offset:1536\n\t" \
    "ds_read_b64_tr_b16 v[184:185], v244 offset:32\n\t" \
    "ds_read_b64_tr_b16 v[186:187], v244 offset:1568\n\t" \
    "ds_read_b64_tr_b16 v[192:193], v244 offset:64\n\t" \
    "ds_read_b64_tr_b16 v[194:195], v244 offset:1600\n\t" \
    "v_add_u32 v244, 0x1e000, v243\n\t" \
    "s_waitcnt vmcnt(0) lgkmcnt(0)\n\t" \
    "s_barrier \n\t" \
    "ds_read_b64_tr_b16 v[180:181], v244 offset:0\n\t" \
    "ds_read_b64_tr_b16 v[182:183], v244 offset:1536\n\t" \
    "ds_read_b64_tr_b16 v[188:189], v244 offset:32\n\t" \
    "ds_read_b64_tr_b16 v[190:191], v244 offset:1568\n\t" \
    "ds_read_b64_tr_b16 v[196:197], v244 offset:64\n\t" \
    "ds_read_b64_tr_b16 v[198:199], v244 offset:1600\n\t" \
    "s_waitcnt lgkmcnt(0)\n\t" \
    "s_barrier \n\t" \
    "ds_write_b128 v254, v[200:203] offset:40960\n\t" \
    "ds_write_b128 v254, v[204:207] offset:41984\n\t" \
    "ds_write_b128 v254, v[208:211] offset:43008\n\t" \
    "ds_write_b128 v254, v[212:215] offset:44032\n\t" \
    "ds_write_b128 v254, v[216:219] offset:45056\n\t" \
    "ds_write_b128 v254, v[220:223] offset:46080\n\t" \
    "ds_write_b128 v254, v[224:227] offset:47104\n\t" \
    "ds_write_b128 v254, v[228:231] offset:48128\n\t" \
    "ds_write_b128 v254, v[232:235] offset:49152\n\t" \
    "ds_write_b128 v254, v[236:239] offset:50176\n\t" \
    "s_waitcnt vmcnt(0) lgkmcnt(0)\n\t" \
    "s_barrier \n\t" \
    "ds_read_b128 v[224:227], v253 offset:0\n\t" \
    "ds_read_b128 v[244:247], v254 offset:40960\n\t" \
    "ds_read_b128 v[228:231], v253 offset:1024\n\t" \
    "ds_read_b128 v[232:235], v253 offset:20480\n\t" \
    "ds_read_b128 v[236:239], v253 offset:21504\n\t" \
    "ds_read_b128 v[248:251], v254 offset:41984\n\t" \
    ".Ls13_%=:\n\t" \
    "s_waitcnt lgkmcnt(5)\n\t" \
    "v_mfma_f32_16x16x32_bf16 v[204:207], v[64:67], v[224:227], 0\n\t" \
    "s_waitcnt lgkmcnt(3)\n\t" \
    "v_mfma_f32_16x16x32_bf16 v[204:207], v[68:71], v[228:231], v[204:207]\n\t" \
    "ds_read_b128 v[240:243], v253 offset:2048\n\t" \
    "v_mfma_f32_16x16x32_bf16 v[208:211], v[72:75], v[224:227], 0\n\t" \
    "v_mfma_f32_16x16x32_bf16 v[208:211], v[76:79], v[228:231], v[208:211]\n\t" \
    "v_mfma_f32_16x16x32_bf16 v[200:203], v[56:59], v[224:227], 0\n\t" \
    "v_mfma_f32_16x16x32_bf16 v[200:203], v[60:63], v[228:231], v[200:203]\n\t" \
    "s_waitcnt lgkmcnt(3)\n\t" \
    "v_mfma_f32_16x16x32_bf16 v[216:219], v[64:67], v[232:235], 0\n\t" \
    "ds_read_b128 v[224:227], v253 offset:3072\n\t" \
    "s_waitcnt lgkmcnt(3)\n\t" \
    "v_mfma_f32_16x16x32_bf16 v[216:219], v[68:71], v[236:239], v[216:219]\n\t" \
    "ds_read_b128 v[228:231], v253 offset:22528\n\t" \
    "v_mfma_f32_16x16x32_bf16 v[220:223], v[72:75], v[232:235], 0\n\t" \
    "v_mfma_f32_16x16x32_bf16 v[220:223], v[76:79], v[236:239], v[220:223]\n\t" \
    "v_mfma_f32_16x16x32_bf16 v[212:215], v[56:59], v[232:235], 0\n\t" \
    "v_mfma_f32_16x16x32_bf16 v[212:215], v[60:63], v[236:239], v[212:215]\n\t" \
    "s_waitcnt lgkmcnt(2)\n\t" \
    "v_mfma_f32_16x16x32_bf16 v[204:207], v[104:107], v[240:243], v[204:207]\n\t" \
    "ds_read_b128 v[232:235], v253 offset:23552\n\t" \
    "s_waitcnt lgkmcnt(2)\n\t" \
    "v_mfma_f32_16x16x32_bf16 v[204:207], v[108:111], v[224:227], v[204:207]\n\t" \
    "ds_read_b128 v[236:239], v253 offset:4096\n\t" \
    "v_mfma_f32_16x16x32_bf16 v[208:211], v[112:115], v[240:243], v[208:211]\n\t" \
    "v_mfma_f32_16x16x32_bf16 v[208:211], v[116:119], v[224:227], v[208:211]\n\t" \
    "v_mfma_f32_16x16x32_bf16 v[200:203], v[96:99], v[240:243], v[200:203]\n\t" \
    "v_mfma_f32_16x16x32_bf16 v[200:203], v[100:103], v[224:227], v[200:203]\n\t" \
    "s_waitcnt lgkmcnt(2)\n\t" \
    "v_mfma_f32_16x16x32_bf16 v[216:219], v[104:107], v[228:231], v[216:219]\n\t" \
    "ds_read_b128 v[240:243], v253 offset:5120\n\t" \
    "s_waitcnt lgkmcnt(2)\n\t" \
    "v_mfma_f32_16x16x32_bf16 v[216:219], v[108:111], v[232:235], v[216:219]\n\t" \
    "ds_read_b128 v[224:227], v253 offset:24576\n\t" \
    "v_mfma_f32_16x16x32_bf16 v[220:223], v[112:115], v[228:231], v[220:223]\n\t" \
    "v_mfma_f32_16x16x32_bf16 v[220:223], v[116:119], v[232:235], v[220:223]\n\t" \
    "v_mfma_f32_16x16x32_bf16 v[212:215], v[96:99], v[228:231], v[212:215]\n\t" \
    "v_mfma_f32_16x16x32_bf16 v[212:215], v[100:103], v[232:235], v[212:215]\n\t" \
    "s_waitcnt lgkmcnt(2)\n\t" \
    "v_mfma_f32_16x16x32_bf16 v[204:207], v[0:3], v[236:239], v[204:207]\n\t" \
    "ds_read_b128 v[228:231], v253 offset:25600\n\t" \
    "s_waitcnt lgkmcnt(2)\n\t" \
    "v_mfma_f32_16x16x32_bf16 v[204:207], v[4:7], v[240:243], v[204:207]\n\t" \
    "ds_read_b128 v[232:235], v253 offset:6144\n\t" \
    "v_mfma_f32_16x16x32_bf16 v[208:211], v[8:11], v[236:239], v[208:211]\n\t" \
    "v_mfma_f32_16x16x32_bf16 v[208:211], v[12:15], v[240:243], v[208:211]\n\t" \
    "v_mfma_f32_16x16x32_bf16 v[200:203], v[244:247], v[236:239], v[200:203]\n\t" \
    "v_mfma_f32_16x16x32_bf16 v[200:203], v[248:251], v[240:243], v[200:203]\n\t" \
    "s_waitcnt lgkmcnt(2)\n\t" \
    "v_mfma_f32_16x16x32_bf16 v[216:219], v[0:3], v[224:227], v[216:219]\n\t" \
    "ds_read_b128 v[236:239], v253 offset:7168\n\t" \
    "s_waitcnt lgkmcnt(2)\n\t" \
    "v_mfma_f32_16x16x32_bf16 v[216:219], v[4:7], v[228:231], v[216:219]\n\t" \
    "ds_read_b128 v[240:243], v253 offset:26624\n\t" \
    "v_mfma_f32_16x16x32_bf16 v[220:223], v[8:11], v[224:227], v[220:223]\n\t" \
    "v_mfma_f32_16x16x32_bf16 v[220:223], v[12:15], v[228:231], v[220:223]\n\t" \
    "v_mfma_f32_16x16x32_bf16 v[212:215], v[244:247], v[224:227], v[212:215]\n\t" \
    "ds_read_b128 v[244:247], v254 offset:43008\n\t" \
    "v_mfma_f32_16x16x32_bf16 v[212:215], v[248:251], v[228:231], v[212:215]\n\t" \
    "ds_read_b128 v[248:251], v254 offset:44032\n\t" \
    "s_waitcnt lgkmcnt(4)\n\t" \
    "v_mfma_f32_16x16x32_bf16 v[204:207], v[40:43], v[232:235], v[204:207]\n\t" \
    "ds_read_b128 v[224:227], v253 offset:27648\n\t" \
    "s_waitcnt lgkmcnt(4)\n\t" \
    "v_mfma_f32_16x16x32_bf16 v[204:207], v[44:47], v[236:239], v[204:207]\n\t" \
    "ds_read_b128 v[228:231], v253 offset:8192\n\t" \
    "v_mfma_f32_16x16x32_bf16 v[208:211], v[48:51], v[232:235], v[208:211]\n\t" \
    "v_mfma_f32_16x16x32_bf16 v[208:211], v[52:55], v[236:239], v[208:211]\n\t" \
    "s_waitcnt lgkmcnt(3)\n\t" \
    "v_mfma_f32_16x16x32_bf16 v[200:203], v[244:247], v[232:235], v[200:203]\n\t" \
    "s_waitcnt lgkmcnt(2)\n\t" \
    "v_mfma_f32_16x16x32_bf16 v[200:203], v[248:251], v[236:239], v[200:203]\n\t" \
    "v_mfma_f32_16x16x32_bf16 v[216:219], v[40:43], v[240:243], v[216:219]\n\t" \
    "ds_read_b128 v[232:235], v253 offset:9216\n\t" \
    "s_waitcnt lgkmcnt(2)\n\t" \
    "v_mfma_f32_16x16x32_bf16 v[216:219], v[44:47], v[224:227], v[216:219]\n\t" \
    "ds_read_b128 v[236:239], v253 offset:28672\n\t" \
    "v_mfma_f32_16x16x32_bf16 v[220:223], v[48:51], v[240:243], v[220:223]\n\t" \
    "v_mfma_f32_16x16x32_bf16 v[220:223], v[52:55], v[224:227], v[220:223]\n\t" \
    "v_mfma_f32_16x16x32_bf16 v[212:215], v[244:247], v[240:243], v[212:215]\n\t" \
    "ds_read_b128 v[244:247], v254 offset:45056\n\t" \
    "v_mfma_f32_16x16x32_bf16 v[212:215], v[248:251], v[224:227], v[212:215]\n\t" \
    "ds_read_b128 v[248:251], v254 offset:46080\n\t" \
    "s_waitcnt lgkmcnt(4)\n\t" \
    "v_mfma_f32_16x16x32_bf16 v[204:207], v[144:147], v[228:231], v[204:207]\n\t" \
    "ds_read_b128 v[240:243], v253 offset:29696\n\t" \
    "s_waitcnt lgkmcnt(4)\n\t" \
    "v_mfma_f32_16x16x32_bf16 v[204:207], v[148:151], v[232:235], v[204:207]\n\t" \
    "ds_read_b128 v[224:227], v253 offset:10240\n\t" \
    "v_mfma_f32_16x16x32_bf16 v[208:211], v[152:155], v[228:231], v[208:211]\n\t" \
    "v_mfma_f32_16x16x32_bf16 v[208:211], v[156:159], v[232:235], v[208:211]\n\t" \
    "v_mfma_f32_16x16x32_bf16 v[200:203], v[136:139], v[228:231], v[200:203]\n\t" \
    "v_mfma_f32_16x16x32_bf16 v[200:203], v[140:143], v[232:235], v[200:203]\n\t" \
    "s_waitcnt lgkmcnt(4)\n\t" \
    "v_mfma_f32_16x16x32_bf16 v[216:219], v[144:147], v[236:239], v[216:219]\n\t" \
    "ds_read_b128 v[228:231], v253 offset:11264\n\t" \
    "s_waitcnt lgkmcnt(2)\n\t" \
    "v_mfma_f32_16x16x32_bf16 v[216:219], v[148:151], v[240:243], v[216:219]\n\t" \
    "ds_read_b128 v[232:235], v253 offset:30720\n\t" \
    "v_mfma_f32_16x16x32_bf16 v[220:223], v[152:155], v[236:239], v[220:223]\n\t" \
    "v_mfma_f32_16x16x32_bf16 v[220:223], v[156:159], v[240:243], v[220:223]\n\t" \
    "v_mfma_f32_16x16x32_bf16 v[212:215], v[136:139], v[236:239], v[212:215]\n\t" \
    "v_mfma_f32_16x16x32_bf16 v[212:215], v[140:143], v[240:243], v[212:215]\n\t" \
    "ds_read_b128 v[236:239], v253 offset:31744\n\t" \
    "s_setprio 2\n\t" \
    "s_waitcnt lgkmcnt(0)\n\t" \
    "s_barrier \n\t" \
    "v_mfma_f32_16x16x32_bf16 v[204:207], v[80:83], v[224:227], v[204:207]\n\t" \
    "v_mfma_f32_16x16x32_bf16 v[204:207], v[84:87], v[228:231], v[204:207]\n\t" \
    "ds_read_b128 v[240:243], v253 offset:14336\n\t" \
    "v_mfma_f32_16x16x32_bf16 v[208:211], v[88:91], v[224:227], v[208:211]\n\t" \
    "v_mfma_f32_16x16x32_bf16 v[208:211], v[92:95], v[228:231], v[208:211]\n\t" \
    "v_mfma_f32_16x16x32_bf16 v[200:203], v[244:247], v[224:227], v[200:203]\n\t" \
    "v_mfma_f32_16x16x32_bf16 v[200:203], v[248:251], v[228:231], v[200:203]\n\t" \
    "v_mfma_f32_16x16x32_bf16 v[216:219], v[80:83], v[232:235], v[216:219]\n\t" \
    "ds_read_b128 v[224:227], v253 offset:15360\n\t" \
    "s_and_b32 s44, s40, 3\n\t" \
    "v_mfma_f32_16x16x32_bf16 v[216:219], v[84:87], v[236:239], v[216:219]\n\t" \
    "ds_read_b128 v[228:231], v253 offset:34816\n\t" \
    "s_lshl_b32 s44, s44, 17\n\t" \
    "v_mfma_f32_16x16x32_bf16 v[220:223], v[88:91], v[232:235], v[220:223]\n\t" \
    "s_add_u32 s44, s44, %[mb]\n\t" \
    "v_mfma_f32_16x16x32_bf16 v[220:223], v[92:95], v[236:239], v[220:223]\n\t" \
    "s_add_u32 s46, s44, 0x800\n\t" \
    "v_mfma_f32_16x16x32_bf16 v[212:215], v[244:247], v[232:235], v[212:215]\n\t" \
    "ds_read_b128 v[244:247], v254 offset:47104\n\t" \
    "s_add_u32 s47, s44, 0x1800\n\t" \
    "v_mfma_f32_16x16x32_bf16 v[212:215], v[248:251], v[236:239], v[212:215]\n\t" \
    "ds_read_b128 v[248:251], v254 offset:48128\n\t" \
    "s_add_u32 s48, s44, 0xc00\n\t" \
    "s_waitcnt lgkmcnt(4)\n\t" \
    "v_mfma_f32_16x16x32_bf16 v[204:207], v[120:123], v[240:243], v[204:207]\n\t" \
    "ds_read_b128 v[232:235], v253 offset:35840\n\t" \
    "s_waitcnt lgkmcnt(4)\n\t" \
    "v_mfma_f32_16x16x32_bf16 v[204:207], v[124:127], v[224:227], v[204:207]\n\t" \
    "ds_read_b128 v[236:239], v253 offset:16384\n\t" \
    "v_mfma_f32_16x16x32_bf16 v[208:211], v[128:131], v[240:243], v[208:211]\n\t" \
    "v_mfma_f32_16x16x32_bf16 v[208:211], v[132:135], v[224:227], v[208:211]\n\t" \
    "s_waitcnt lgkmcnt(3)\n\t" \
    "v_mfma_f32_16x16x32_bf16 v[200:203], v[244:247], v[240:243], v[200:203]\n\t" \
    "s_waitcnt lgkmcnt(2)\n\t" \
    "v_mfma_f32_16x16x32_bf16 v[200:203], v[248:251], v[224:227], v[200:203]\n\t" \
    "v_mfma_f32_16x16x32_bf16 v[216:219], v[120:123], v[228:231], v[216:219]\n\t" \
    "ds_read_b128 v[240:243], v253 offset:17408\n\t" \
    "s_waitcnt lgkmcnt(2)\n\t" \
    "v_mfma_f32_16x16x32_bf16 v[216:219], v[124:127], v[232:235], v[216:219]\n\t" \
    "ds_read_b128 v[224:227], v253 offset:36864\n\t" \
    "v_mfma_f32_16x16x32_bf16 v[220:223], v[128:131], v[228:231], v[220:223]\n\t" \
    "v_mfma_f32_16x16x32_bf16 v[220:223], v[132:135], v[232:235], v[220:223]\n\t" \
    "v_mfma_f32_16x16x32_bf16 v[212:215], v[244:247], v[228:231], v[212:215]\n\t" \
    "ds_read_b128 v[244:247], v254 offset:49152\n\t" \
    "v_mfma_f32_16x16x32_bf16 v[212:215], v[248:251], v[232:235], v[212:215]\n\t" \
    "ds_read_b128 v[248:251], v254 offset:50176\n\t" \
    "s_waitcnt lgkmcnt(4)\n\t" \
    "v_mfma_f32_16x16x32_bf16 v[204:207], v[160:163], v[236:239], v[204:207]\n\t" \
    "ds_read_b128 v[228:231], v253 offset:37888\n\t" \
    "s_waitcnt lgkmcnt(4)\n\t" \
    "v_mfma_f32_16x16x32_bf16 v[204:207], v[164:167], v[240:243], v[204:207]\n\t" \
    "ds_read_b128 v[232:235], v253 offset:12288\n\t" \
    "v_mfma_f32_16x16x32_bf16 v[208:211], v[168:171], v[236:239], v[208:211]\n\t" \
    "v_mfma_f32_16x16x32_bf16 v[208:211], v[172:175], v[240:243], v[208:211]\n\t" \
    "s_waitcnt lgkmcnt(3)\n\t" \
    "v_mfma_f32_16x16x32_bf16 v[200:203], v[244:247], v[236:239], v[200:203]\n\t" \
    "s_waitcnt lgkmcnt(2)\n\t" \
    "v_mfma_f32_16x16x32_bf16 v[200:203], v[248:251], v[240:243], v[200:203]\n\t" \
    "v_mfma_f32_16x16x32_bf16 v[216:219], v[160:163], v[224:227], v[216:219]\n\t" \
    "ds_read_b128 v[236:239], v253 offset:13312\n\t" \
    "s_waitcnt lgkmcnt(2)\n\t" \
    "v_mfma_f32_16x16x32_bf16 v[216:219], v[164:167], v[228:231], v[216:219]\n\t" \
    "ds_read_b128 v[240:243], v253 offset:18432\n\t" \
    "v_mfma_f32_16x16x32_bf16 v[220:223], v[168:171], v[224:227], v[220:223]\n\t" \
    "v_mfma_f32_16x16x32_bf16 v[220:223], v[172:175], v[228:231], v[220:223]\n\t" \
    "v_mfma_f32_16x16x32_bf16 v[212:215], v[244:247], v[224:227], v[212:215]\n\t" \
    "v_mfma_f32_16x16x32_bf16 v[212:215], v[248:251], v[228:231], v[212:215]\n\t" \
    "s_waitcnt lgkmcnt(2)\n\t" \
    "v_mfma_f32_16x16x32_bf16 v[204:207], v[24:27], v[232:235], v[204:207]\n\t" \
    "ds_read_b128 v[224:227], v253 offset:19456\n\t" \
    "v_mov_b64 v[244:245], 0\n\t" \
    "s_waitcnt lgkmcnt(2)\n\t" \
    "v_mfma_f32_16x16x32_bf16 v[204:207], v[28:31], v[236:239], v[204:207]\n\t" \
    "ds_read_b128 v[228:231], v253 offset:32768\n\t" \
    "v_mov_b64 v[246:247], 0\n\t" \
    "v_mfma_f32_16x16x32_bf16 v[208:211], v[32:35], v[232:235], v[208:211]\n\t" \
    "v_mov_b64 v[248:249], 0\n\t" \
    "v_mfma_f32_16x16x32_bf16 v[208:211], v[36:39], v[236:239], v[208:211]\n\t" \
    "v_mov_b64 v[250:251], 0\n\t" \
    "v_mfma_f32_16x16x32_bf16 v[200:203], v[16:19], v[232:235], v[200:203]\n\t" \
    "v_mfma_f32_16x16x32_bf16 v[200:203], v[20:23], v[236:239], v[200:203]\n\t" \
    "s_waitcnt lgkmcnt(2)\n\t" \
    "v_mfma_f32_16x16x32_bf16 v[204:207], v[184:187], v[240:243], v[204:207]\n\t" \
    "ds_read_b128 v[232:235], v253 offset:33792\n\t" \
    "s_waitcnt lgkmcnt(2)\n\t" \
    "v_mfma_f32_16x16x32_bf16 v[204:207], v[188:191], v[224:227], v[204:207]\n\t" \
    "ds_read_b128 v[236:239], v253 offset:38912\n\t" \
    "v_mfma_f32_16x16x32_bf16 v[208:211], v[192:195], v[240:243], v[208:211]\n\t" \
    "v_mfma_f32_16x16x32_bf16 v[208:211], v[196:199], v[224:227], v[208:211]\n\t" \
    "v_mfma_f32_16x16x32_bf16 v[200:203], v[176:179], v[240:243], v[200:203]\n\t" \
    "v_mfma_f32_16x16x32_bf16 v[200:203], v[180:183], v[224:227], v[200:203]\n\t" \
    "ds_read_b128 v[240:243], v253 offset:39936\n\t" \
    "s_waitcnt lgkmcnt(3)\n\t" \
    "v_mfma_f32_16x16x32_bf16 v[216:219], v[24:27], v[228:231], v[216:219]\n\t" \
    "s_waitcnt lgkmcnt(2)\n\t" \
    "v_mfma_f32_16x16x32_bf16 v[216:219], v[28:31], v[232:235], v[216:219]\n\t" \
    "v_cvt_pk_bf16_f32 v204, v204, v208\n\t" \
    "v_cvt_pk_bf16_f32 v205, v205, v209\n\t" \
    "s_waitcnt lgkmcnt(1)\n\t" \
    "v_mfma_f32_16x16x32_bf16 v[216:219], v[184:187], v[236:239], v[216:219]\n\t" \
    "v_cvt_pk_bf16_f32 v206, v206, v210\n\t" \
    "v_cvt_pk_bf16_f32 v207, v207, v211\n\t" \
    "s_waitcnt lgkmcnt(0)\n\t" \
    "v_mfma_f32_16x16x32_bf16 v[216:219], v[188:191], v[240:243], v[216:219]\n\t" \
    "ds_write_b128 v254, v[204:207] offset:9216\n\t" \
    "v_mfma_f32_16x16x32_bf16 v[220:223], v[32:35], v[228:231], v[220:223]\n\t" \
    "v_mfma_f32_16x16x32_bf16 v[220:223], v[36:39], v[232:235], v[220:223]\n\t" \
    "v_cvt_pk_bf16_f32 v200, v200, v201\n\t" \
    "v_cvt_pk_bf16_f32 v201, v202, v203\n\t" \
    "v_mfma_f32_16x16x32_bf16 v[220:223], v[192:195], v[236:239], v[220:223]\n\t" \
    "ds_write_b64 v254, v[200:201] offset:7168\n\t" \
    "v_mfma_f32_16x16x32_bf16 v[220:223], v[196:199], v[240:243], v[220:223]\n\t" \
    "v_mfma_f32_16x16x32_bf16 v[212:215], v[16:19], v[228:231], v[212:215]\n\t" \
    "ds_read_b128 v[200:203], v254 offset:40960\n\t" \
    "ds_read_b128 v[204:207], v254 offset:41984\n\t" \
    "v_mfma_f32_16x16x32_bf16 v[212:215], v[20:23], v[232:235], v[212:215]\n\t" \
    "v_mov_b64 v[208:209], 0\n\t" \
    "v_mov_b64 v[210:211], 0\n\t" \
    "v_mfma_f32_16x16x32_bf16 v[212:215], v[176:179], v[236:239], v[212:215]\n\t" \
    "v_cvt_pk_bf16_f32 v216, v216, v220\n\t" \
    "v_cvt_pk_bf16_f32 v217, v217, v221\n\t" \
    "v_mfma_f32_16x16x32_bf16 v[212:215], v[180:183], v[240:243], v[212:215]\n\t" \
    "v_cvt_pk_bf16_f32 v218, v218, v222\n\t" \
    "v_cvt_pk_bf16_f32 v219, v219, v223\n\t" \
    "s_setprio 0\n\t" \
    "ds_write_b128 v254, v[216:219] offset:15360\n\t" \
    "ds_read_b128 v[216:219], v254 offset:43008\n\t" \
    "ds_read_b128 v[220:223], v254 offset:44032\n\t" \
    "s_nop 2\n\t" \
    "v_cvt_pk_bf16_f32 v212, v212, v213\n\t" \
    "v_cvt_pk_bf16_f32 v213, v214, v215\n\t" \
    "ds_write_b64 v254, v[212:213] offset:13312\n\t" \
    "s_sleep 3\n\t" \
    "s_mov_b32 m0, 0xa800\n\t" \
    "s_nop 0\n\t" \
    "buffer_load_dwordx4 v253, s[60:63], s46 offen sc1 lds\n\t" \
    "s_mov_b32 m0, 0xb800\n\t" \
    "s_nop 0\n\t" \
    "buffer_load_dwordx4 v253, s[60:63], s47 offen sc1 lds\n\t" \
    "s_mov_b32 m0, 0xac00\n\t" \
    "s_nop 0\n\t" \
    "buffer_load_dwordx4 v253, s[60:63], s48 offen sc1 lds\n\t" \
    "s_waitcnt lgkmcnt(0)\n\t" \
    "v_bfe_i32 v212, v253, 5, 1\n\t" \
    "v_and_b32 v212, 0xaaaa, v212\n\t" \
    "v_or_b32 v212, 0x4444, v212\n\t" \
    "v_and_b32 v213, 0xffff, v255\n\t" \
    "v_lshrrev_b32 v214, 16, v255\n\t" \
    ".Ls14_%=:\n\t" \
    "s_waitcnt vmcnt(0)\n\t" \
    "ds_read_b128 v[224:227], v253 offset:43008\n\t" \
    "ds_read_b128 v[228:231], v253 offset:47104\n\t" \
    "ds_read_b128 v[232:235], v253 offset:44032\n\t" \
    "s_waitcnt lgkmcnt(2)\n\t" \
    "v_max_u32 v215, v224, v225\n\t" \
    "v_max3_u32 v215, v226, v227, v215\n\t" \
    "s_waitcnt lgkmcnt(1)\n\t" \
    "v_max3_u32 v215, v228, v229, v215\n\t" \
    "v_max3_u32 v215, v230, v231, v215\n\t" \
    "s_waitcnt lgkmcnt(0)\n\t" \
    "v_max3_u32 v215, v232, v233, v215\n\t" \
    "v_max3_u32 v215, v234, v235, v215\n\t" \
    "v_cmp_eq_u32 vcc, -1, v215\n\t" \
    "s_cbranch_vccz .Ls15_%=\n\t" \
    "v_max_u32 v215, v224, v225\n\t" \
    "v_max3_u32 v215, v226, v227, v215\n\t" \
    "v_cmp_eq_u32 vcc, -1, v215\n\t" \
    "s_cbranch_vccz .Ls16_%=\n\t" \
    "s_mov_b32 m0, 0xa800\n\t" \
    "s_nop 0\n\t" \
    "buffer_load_dwordx4 v253, s[60:63], s46 offen sc1 lds\n\t" \
    ".Ls16_%=:\n\t" \
    "v_max_u32 v215, v228, v229\n\t" \
    "v_max3_u32 v215, v230, v231, v215\n\t" \
    "v_cmp_eq_u32 vcc, -1, v215\n\t" \
    "s_cbranch_vccz .Ls17_%=\n\t" \
    "s_mov_b32 m0, 0xb800\n\t" \
    "s_nop 0\n\t" \
    "buffer_load_dwordx4 v253, s[60:63], s47 offen sc1 lds\n\t" \
    ".Ls17_%=:\n\t" \
    "v_max_u32 v215, v232, v233\n\t" \
    "v_max3_u32 v215, v234, v235, v215\n\t" \
    "v_cmp_eq_u32 vcc, -1, v215\n\t" \
    "s_cbranch_vccz .Ls18_%=\n\t" \
    "s_mov_b32 m0, 0xac00\n\t" \
    "s_nop 0\n\t" \
    "buffer_load_dwordx4 v253, s[60:63], s48 offen sc1 lds\n\t" \
    ".Ls18_%=:\n\t" \
    "s_cmp_eq_u32 s41, 0\n\t" \
    "s_cbranch_scc1 .Ls15_%=\n\t" \
    "s_sub_u32 s41, s41, 1\n\t" \
    "s_branch .Ls14_%=\n\t" \
    ".Ls15_%=:\n\t" \
    "s_setprio 3\n\t" \
    "s_waitcnt lgkmcnt(0)\n\t" \
    "s_barrier \n\t" \
    "ds_read_b128 v[236:239], v253 offset:40960\n\t" \
    "ds_read_b128 v[240:243], v253 offset:46080\n\t" \
    "v_smfmac_f32_16x16x64_bf16 v[244:247], v[224:227], v[0:7], v212\n\t" \
    "v_smfmac_f32_16x16x64_bf16 v[248:251], v[224:227], v[8:15], v212\n\t" \
    "v_smfmac_f32_16x16x64_bf16 v[208:211], v[224:227], v[200:207], v212\n\t" \
    "ds_read_b128 v[200:203], v254 offset:45056\n\t" \
    "ds_read_b128 v[204:207], v254 offset:46080\n\t" \
    "ds_read_b128 v[224:227], v253 offset:41984\n\t" \
    "v_smfmac_f32_16x16x64_bf16 v[208:211], v[228:231], v[16:23], v212\n\t" \
    "v_smfmac_f32_16x16x64_bf16 v[244:247], v[228:231], v[24:31], v212\n\t" \
    "v_smfmac_f32_16x16x64_bf16 v[248:251], v[228:231], v[32:39], v212\n\t" \
    "ds_read_b128 v[228:231], v253 offset:48128\n\t" \
    "v_smfmac_f32_16x16x64_bf16 v[208:211], v[232:235], v[216:223], v212\n\t" \
    "ds_read_b128 v[216:219], v254 offset:47104\n\t" \
    "ds_read_b128 v[220:223], v254 offset:48128\n\t" \
    "v_smfmac_f32_16x16x64_bf16 v[244:247], v[232:235], v[40:47], v212\n\t" \
    "v_smfmac_f32_16x16x64_bf16 v[248:251], v[232:235], v[48:55], v212\n\t" \
    "ds_read_b128 v[232:235], v253 offset:45056\n\t" \
    "s_waitcnt lgkmcnt(8)\n\t" \
    "v_smfmac_f32_16x16x64_bf16 v[208:211], v[236:239], v[56:63], v212\n\t" \
    "v_smfmac_f32_16x16x64_bf16 v[244:247], v[236:239], v[64:71], v212\n\t" \
    "v_smfmac_f32_16x16x64_bf16 v[248:251], v[236:239], v[72:79], v212\n\t" \
    "ds_read_b128 v[236:239], v253 offset:49152\n\t" \
    "s_waitcnt lgkmcnt(6)\n\t" \
    "v_smfmac_f32_16x16x64_bf16 v[208:211], v[240:243], v[200:207], v212\n\t" \
    "ds_read_b128 v[200:203], v254 offset:49152\n\t" \
    "ds_read_b128 v[204:207], v254 offset:50176\n\t" \
    "v_smfmac_f32_16x16x64_bf16 v[244:247], v[240:243], v[80:87], v212\n\t" \
    "v_smfmac_f32_16x16x64_bf16 v[248:251], v[240:243], v[88:95], v212\n\t" \
    "ds_read_b128 v[240:243], v253 offset:50176\n\t" \
    "s_waitcnt lgkmcnt(8)\n\t" \
    "v_smfmac_f32_16x16x64_bf16 v[208:211], v[224:227], v[96:103], v212\n\t" \
    "v_smfmac_f32_16x16x64_bf16 v[244:247], v[224:227], v[104:111], v212\n\t" \
    "v_smfmac_f32_16x16x64_bf16 v[248:251], v[224:227], v[112:119], v212\n\t" \
    "s_waitcnt lgkmcnt(5)\n\t" \
    "v_smfmac_f32_16x16x64_bf16 v[208:211], v[228:231], v[216:223], v212\n\t" \
    "s_waitcnt lgkmcnt(4)\n\t" \
    "v_smfmac_f32_16x16x64_bf16 v[208:211], v[232:235], v[136:143], v212\n\t" \
    "s_waitcnt lgkmcnt(1)\n\t" \
    "v_smfmac_f32_16x16x64_bf16 v[208:211], v[236:239], v[200:207], v212\n\t" \
    "s_waitcnt lgkmcnt(0)\n\t" \
    "v_smfmac_f32_16x16x64_bf16 v[208:211], v[240:243], v[176:183], v212\n\t" \
    "v_smfmac_f32_16x16x64_bf16 v[244:247], v[228:231], v[120:127], v212\n\t" \
    "v_smfmac_f32_16x16x64_bf16 v[244:247], v[232:235], v[144:151], v212\n\t" \
    "v_smfmac_f32_16x16x64_bf16 v[244:247], v[236:239], v[160:167], v212\n\t" \
    "v_add_f32 v208, v208, v210\n\t" \
    "v_add_f32 v209, v209, v211\n\t" \
    "v_cvt_pk_bf16_f32 v208, v208, v209\n\t" \
    "v_smfmac_f32_16x16x64_bf16 v[244:247], v[240:243], v[184:191], v212\n\t" \
    "ds_write_b16 v213, v208 offset:0\n\t" \
    "ds_write_b16_d16_hi v213, v208 offset:16\n\t" \
    "v_smfmac_f32_16x16x64_bf16 v[248:251], v[228:231], v[128:135], v212\n\t" \
    "v_smfmac_f32_16x16x64_bf16 v[248:251], v[232:235], v[152:159], v212\n\t" \
    "v_smfmac_f32_16x16x64_bf16 v[248:251], v[236:239], v[168:175], v212\n\t" \
    "v_add_f32 v244, v244, v246\n\t" \
    "v_add_f32 v245, v245, v247\n\t" \
    "v_smfmac_f32_16x16x64_bf16 v[248:251], v[240:243], v[192:199], v212\n\t" \
    "s_nop 7\n\t" \
    "v_add_f32 v248, v248, v250\n\t" \
    "v_add_f32 v249, v249, v251\n\t" \
    "v_cvt_pk_bf16_f32 v244, v244, v248\n\t" \
    "v_cvt_pk_bf16_f32 v245, v245, v249\n\t" \
    "ds_write2_b32 v214, v244, v245 offset1:4\n\t" \
    "s_setprio 0\n\t" \
    "s_sleep 1\n\t" \
    "ds_read_b128 v[200:203], v254 offset:6144\n\t" \
    "ds_read_b128 v[224:227], v254 offset:18432\n\t" \
    "ds_read_b128 v[204:207], v254 offset:7168\n\t" \
    "ds_read_b128 v[228:231], v254 offset:19456\n\t" \
    "ds_read_b128 v[208:211], v254 offset:8192\n\t" \
    "ds_read_b128 v[232:235], v254 offset:20480\n\t" \
    "ds_read_b128 v[212:215], v254 offset:9216\n\t" \
    "ds_read_b128 v[236:239], v254 offset:21504\n\t" \
    "ds_read_b128 v[216:219], v254 offset:10240\n\t" \
    "ds_read_b128 v[220:223], v254 offset:11264\n\t" \
    "s_and_b32 s44, s40, 3\n\t" \
    "s_lshl_b32 s44, s44, 17\n\t" \
    "s_add_u32 s50, s44, %[pb]\n\t" \
    "s_add_u32 s51, s44, %[pm]\n\t" \
    "s_mov_b32 s52, 0xf0000\n\t" \
    "s_mov_b32 s53, 0xfff00ff\n\t" \
    "s_mov_b32 s54, 0x1f0001\n\t" \
    "s_mov_b32 s55, 0x1fff01ff\n\t" \
    "s_mov_b32 s56, 0x3f0003\n\t" \
    "s_mov_b32 s57, 0x3fff03ff\n\t" \
    "s_mov_b32 s58, 0x7f0007\n\t" \
    "s_mov_b32 s59, 0x7fff07ff\n\t" \
    "s_waitcnt lgkmcnt(8)\n\t" \
    "v_mfma_f32_16x16x32_bf16 v[240:243], v[224:227], v[200:203], 0\n\t" \
    "ds_read_b128 v[224:227], v254 offset:22528\n\t" \
    "s_waitcnt lgkmcnt(7)\n\t" \
    "v_mfma_f32_16x16x32_bf16 v[240:243], v[228:231], v[204:207], v[240:243]\n\t" \
    "ds_read_b128 v[228:231], v254 offset:23552\n\t" \
    "s_waitcnt lgkmcnt(6)\n\t" \
    "v_mfma_f32_16x16x32_bf16 v[240:243], v[232:235], v[208:211], v[240:243]\n\t" \
    "s_waitcnt lgkmcnt(4)\n\t" \
    "v_mfma_f32_16x16x32_bf16 v[240:243], v[236:239], v[212:215], v[240:243]\n\t" \
    "v_and_b32 v252, 0xffffff7f, v254\n\t" \
    "s_waitcnt lgkmcnt(0)\n\t" \
    "s_barrier \n\t" \
    "ds_read_b128 v[232:235], v252 offset:3072\n\t" \
    "ds_read_b128 v[236:239], v252 offset:3200\n\t" \
    "v_mfma_f32_16x16x32_bf16 v[240:243], v[224:227], v[216:219], v[240:243]\n\t" \
    "ds_read_b128 v[224:227], v252 offset:4096\n\t" \
    "v_mfma_f32_16x16x32_bf16 v[240:243], v[228:231], v[220:223], v[240:243]\n\t" \
    "ds_read_b128 v[228:231], v252 offset:4224\n\t" \
    "s_waitcnt lgkmcnt(3)\n\t" \
    "v_mfma_f32_16x16x32_bf16 v[248:251], v[232:235], v[200:203], 0\n\t" \
    "ds_read_b128 v[232:235], v252 offset:5120\n\t" \
    "s_waitcnt lgkmcnt(3)\n\t" \
    "v_mfma_f32_16x16x32_bf16 v[248:251], v[236:239], v[204:207], v[248:251]\n\t" \
    "ds_read_b128 v[236:239], v252 offset:5248\n\t" \
    "s_waitcnt lgkmcnt(3)\n\t" \
    "v_mfma_f32_16x16x32_bf16 v[248:251], v[224:227], v[208:211], v[248:251]\n\t" \
    "s_waitcnt lgkmcnt(2)\n\t" \
    "v_mfma_f32_16x16x32_bf16 v[248:251], v[228:231], v[212:215], v[248:251]\n\t" \
    "s_waitcnt lgkmcnt(1)\n\t" \
    "v_mfma_f32_16x16x32_bf16 v[248:251], v[232:235], v[216:219], v[248:251]\n\t" \
    "s_waitcnt lgkmcnt(0)\n\t" \
    "v_mfma_f32_16x16x32_bf16 v[248:251], v[236:239], v[220:223], v[248:251]\n\t" \
    "v_mov_b32 v226, 0xff7fffff\n\t" \
    "v_cndmask_b32 v240, v240, v226, s[52:53]\n\t" \
    "v_cndmask_b32 v241, v241, v226, s[54:55]\n\t" \
    "v_cndmask_b32 v242, v242, v226, s[56:57]\n\t" \
    "v_cndmask_b32 v243, v243, v226, s[58:59]\n\t" \
    "v_max3_f32 v224, v240, v241, v242\n\t" \
    "v_mov_b32 v225, v243\n\t" \
    "v_lshrrev_b32 v200, 5, v253\n\t" \
    "v_and_b32 v201, 0xf0, v253\n\t" \
    "v_and_or_b32 v200, v200, 8, v201\n\t" \
    "v_max3_f32 v224, v224, v248, v249\n\t" \
    "v_max3_f32 v225, v225, v250, v251\n\t" \
    "v_max_f32 v224, v224, v225\n\t" \
    "v_mov_b32 v225, v224\n\t" \
    "s_nop 1\n\t" \
    "v_permlane16_swap_b32 v224, v225\n\t" \
    "v_max_f32 v224, v224, v225\n\t" \
    "v_mov_b32 v225, v224\n\t" \
    "s_nop 1\n\t" \
    "v_permlane32_swap_b32 v224, v225\n\t" \
    "v_max_f32 v224, v224, v225\n\t" \
    "v_mul_f32 v224, %[scl], v224\n\t" \
    "v_fma_f32 v240, v240, %[scl], -v224\n\t" \
    "v_fma_f32 v241, v241, %[scl], -v224\n\t" \
    "v_exp_f32 v240, v240\n\t" \
    "v_fma_f32 v242, v242, %[scl], -v224\n\t" \
    "v_exp_f32 v241, v241\n\t" \
    "v_fma_f32 v243, v243, %[scl], -v224\n\t" \
    "v_exp_f32 v242, v242\n\t" \
    "v_fma_f32 v248, v248, %[scl], -v224\n\t" \
    "v_exp_f32 v243, v243\n\t" \
    "v_fma_f32 v249, v249, %[scl], -v224\n\t" \
    "v_exp_f32 v248, v248\n\t" \
    "v_fma_f32 v250, v250, %[scl], -v224\n\t" \
    "v_exp_f32 v249, v249\n\t" \
    "v_fma_f32 v251, v251, %[scl], -v224\n\t" \
    "v_exp_f32 v250, v250\n\t" \
    "v_exp_f32 v251, v251\n\t" \
    "v_add_f32 v224, v240, v241\n\t" \
    "v_add_f32 v225, v242, v243\n\t" \
    "v_add_f32 v232, v248, v249\n\t" \
    "v_add_f32 v233, v250, v251\n\t" \
    "v_add_f32 v224, v224, v225\n\t" \
    "v_add_f32 v232, v232, v233\n\t" \
    "v_fma_f32 v224, v232, 0.5, v224\n\t" \
    "v_mov_b32 v225, v224\n\t" \
    "s_nop 1\n\t" \
    "v_permlane16_swap_b32 v224, v225\n\t" \
    "v_add_f32 v224, v224, v225\n\t" \
    "v_mov_b32 v225, v224\n\t" \
    "s_nop 1\n\t" \
    "v_permlane32_swap_b32 v224, v225\n\t" \
    "v_add_f32 v224, v224, v225\n\t" \
    "v_rcp_f32 v224, v224\n\t" \
    "s_nop 0\n\t" \
    "v_mul_f32 v240, v240, v224\n\t" \
    "v_mul_f32 v241, v241, v224\n\t" \
    "v_mul_f32 v242, v242, v224\n\t" \
    "v_mul_f32 v243, v243, v224\n\t" \
    "v_mul_f32 v248, v248, v224\n\t" \
    "v_mul_f32 v249, v249, v224\n\t" \
    "v_mul_f32 v250, v250, v224\n\t" \
    "v_mul_f32 v251, v251, v224\n\t" \
    "v_cvt_pk_bf16_f32 v204, |v240|, |v241|\n\t" \
    "v_cvt_pk_bf16_f32 v205, |v242|, |v243|\n\t" \
    "v_cvt_pk_bf16_f32 v208, |v248|, |v249|\n\t" \
    "v_cvt_pk_bf16_f32 v209, |v250|, |v251|\n\t" \
    "v_permlane16_swap_b32 v204, v206\n\t" \
    "v_permlane16_swap_b32 v205, v207\n\t" \
    "s_mov_b32 exec_lo, 0xffff\n\t" \
    "s_mov_b32 exec_hi, 0xffff\n\t" \
    "buffer_store_dwordx4 v[204:207], v253, s[60:63], s50 offen sc1\n\t" \
    "s_mov_b32 exec_lo, -1\n\t" \
    "s_mov_b32 exec_hi, 0\n\t" \
    "buffer_store_dwordx2 v[208:209], v200, s[60:63], s51 offen sc1\n\t" \
    "s_mov_b32 exec_hi, -1\n\t" \
    "ds_read_b128 v[224:227], v253 offset:0\n\t" \
    "ds_read_b128 v[244:247], v254 offset:40960\n\t" \
    "ds_read_b128 v[228:231], v253 offset:1024\n\t" \
    "ds_read_b128 v[232:235], v253 offset:20480\n\t" \
    "ds_read_b128 v[236:239], v253 offset:21504\n\t" \
    "ds_read_b128 v[248:251], v254 offset:41984\n\t" \
    "s_add_u32 s40, s40, 1\n\t" \
    "s_cmp_lg_u32 s40, 64\n\t" \
    "s_cbranch_scc1 .Ls13_%=\n\t" \
    "s_branch .Lend%=\n\t" \
    ".Ls0_%=:\n\t" \
    "v_add_u32 v244, 0x18000, v243\n\t" \
    "s_waitcnt vmcnt(12) lgkmcnt(0)\n\t" \
    "s_barrier \n\t" \
    "s_add_u32 m0, %[wm], 0x12000\n\t" \
    "s_mov_b32 s44, 0x1e0000\n\t" \
    "buffer_load_dwordx4 v240, %[wr], s44 offen offset:0 nt lds\n\t" \
    "buffer_load_dwordx4 v241, %[wr], s44 offen offset:1024 nt lds\n\t" \
    "buffer_load_dwordx4 v242, %[wr], s44 offen offset:2048 nt lds\n\t" \
    "ds_read_b64_tr_b16 v[200:201], v244 offset:0\n\t" \
    "ds_read_b64_tr_b16 v[202:203], v244 offset:1536\n\t" \
    "ds_read_b64_tr_b16 v[0:1], v244 offset:32\n\t" \
    "ds_read_b64_tr_b16 v[2:3], v244 offset:1568\n\t" \
    "ds_read_b64_tr_b16 v[8:9], v244 offset:64\n\t" \
    "ds_read_b64_tr_b16 v[10:11], v244 offset:1600\n\t" \
    "v_add_u32 v244, 0x1e000, v243\n\t" \
    "s_waitcnt vmcnt(12) lgkmcnt(0)\n\t" \
    "s_barrier \n\t" \
    "s_add_u32 m0, %[wm], 0x18000\n\t" \
    "s_mov_b32 s44, 0x240000\n\t" \
    "buffer_load_dwordx4 v240, %[wr], s44 offen offset:0 nt lds\n\t" \
    "buffer_load_dwordx4 v241, %[wr], s44 offen offset:1024 nt lds\n\t" \
    "buffer_load_dwordx4 v242, %[wr], s44 offen offset:2048 nt lds\n\t" \
    "ds_read_b64_tr_b16 v[204:205], v244 offset:0\n\t" \
    "ds_read_b64_tr_b16 v[206:207], v244 offset:1536\n\t" \
    "ds_read_b64_tr_b16 v[4:5], v244 offset:32\n\t" \
    "ds_read_b64_tr_b16 v[6:7], v244 offset:1568\n\t" \
    "ds_read_b64_tr_b16 v[12:13], v244 offset:64\n\t" \
    "ds_read_b64_tr_b16 v[14:15], v244 offset:1600\n\t" \
    "v_add_u32 v244, 0x0, v243\n\t" \
    "s_waitcnt vmcnt(12) lgkmcnt(0)\n\t" \
    "s_barrier \n\t" \
    "s_add_u32 m0, %[wm], 0x1e000\n\t" \
    "s_mov_b32 s44, 0x2a0000\n\t" \
    "buffer_load_dwordx4 v240, %[wr], s44 offen offset:0 nt lds\n\t" \
    "buffer_load_dwordx4 v241, %[wr], s44 offen offset:1024 nt lds\n\t" \
    "buffer_load_dwordx4 v242, %[wr], s44 offen offset:2048 nt lds\n\t" \
    "ds_read_b64_tr_b16 v[208:209], v244 offset:0\n\t" \
    "ds_read_b64_tr_b16 v[210:211], v244 offset:1536\n\t" \
    "ds_read_b64_tr_b16 v[40:41], v244 offset:32\n\t" \
    "ds_read_b64_tr_b16 v[42:43], v244 offset:1568\n\t" \
    "ds_read_b64_tr_b16 v[48:49], v244 offset:64\n\t" \
    "ds_read_b64_tr_b16 v[50:51], v244 offset:1600\n\t" \
    "v_add_u32 v244, 0x6000, v243\n\t" \
    "s_waitcnt vmcnt(12) lgkmcnt(0)\n\t" \
    "s_barrier \n\t" \
    "s_add_u32 m0, %[wm], 0x0\n\t" \
    "s_mov_b32 s44, 0x300000\n\t" \
    "buffer_load_dwordx4 v240, %[wr], s44 offen offset:0 nt lds\n\t" \
    "buffer_load_dwordx4 v241, %[wr], s44 offen offset:1024 nt lds\n\t" \
    "buffer_load_dwordx4 v242, %[wr], s44 offen offset:2048 nt lds\n\t" \
    "ds_read_b64_tr_b16 v[212:213], v244 offset:0\n\t" \
    "ds_read_b64_tr_b16 v[214:215], v244 offset:1536\n\t" \
    "ds_read_b64_tr_b16 v[44:45], v244 offset:32\n\t" \
    "ds_read_b64_tr_b16 v[46:47], v244 offset:1568\n\t" \
    "ds_read_b64_tr_b16 v[52:53], v244 offset:64\n\t" \
    "ds_read_b64_tr_b16 v[54:55], v244 offset:1600\n\t" \
    "v_add_u32 v244, 0xc000, v243\n\t" \
    "s_waitcnt vmcnt(12) lgkmcnt(0)\n\t" \
    "s_barrier \n\t" \
    "s_add_u32 m0, %[wm], 0x6000\n\t" \
    "s_mov_b32 s44, 0x360000\n\t" \
    "buffer_load_dwordx4 v240, %[wr], s44 offen offset:0 nt lds\n\t" \
    "buffer_load_dwordx4 v241, %[wr], s44 offen offset:1024 nt lds\n\t" \
    "buffer_load_dwordx4 v242, %[wr], s44 offen offset:2048 nt lds\n\t" \
    "ds_read_b64_tr_b16 v[216:217], v244 offset:0\n\t" \
    "ds_read_b64_tr_b16 v[218:219], v244 offset:1536\n\t" \
    "ds_read_b64_tr_b16 v[80:81], v244 offset:32\n\t" \
    "ds_read_b64_tr_b16 v[82:83], v244 offset:1568\n\t" \
    "ds_read_b64_tr_b16 v[88:89], v244 offset:64\n\t" \
    "ds_read_b64_tr_b16 v[90:91], v244 offset:1600\n\t" \
    "v_add_u32 v244, 0x12000, v243\n\t" \
    "s_waitcnt vmcnt(12) lgkmcnt(0)\n\t" \
    "s_barrier \n\t" \
    "s_add_u32 m0, %[wm], 0xc000\n\t" \
    "s_mov_b32 s44, 0x3c0000\n\t" \
    "buffer_load_dwordx4 v240, %[wr], s44 offen offset:0 nt lds\n\t" \
    "buffer_load_dwordx4 v241, %[wr], s44 offen offset:1024 nt lds\n\t" \
    "buffer_load_dwordx4 v242, %[wr], s44 offen offset:2048 nt lds\n\t" \
    "ds_read_b64_tr_b16 v[220:221], v244 offset:0\n\t" \
    "ds_read_b64_tr_b16 v[222:223], v244 offset:1536\n\t" \
    "ds_read_b64_tr_b16 v[84:85], v244 offset:32\n\t" \
    "ds_read_b64_tr_b16 v[86:87], v244 offset:1568\n\t" \
    "ds_read_b64_tr_b16 v[92:93], v244 offset:64\n\t" \
    "ds_read_b64_tr_b16 v[94:95], v244 offset:1600\n\t" \
    "v_add_u32 v244, 0x18000, v243\n\t" \
    "s_waitcnt vmcnt(12) lgkmcnt(0)\n\t" \
    "s_barrier \n\t" \
    "s_add_u32 m0, %[wm], 0x12000\n\t" \
    "s_mov_b32 s44, 0x420000\n\t" \
    "buffer_load_dwordx4 v240, %[wr], s44 offen offset:0 nt lds\n\t" \
    "buffer_load_dwordx4 v241, %[wr], s44 offen offset:1024 nt lds\n\t" \
    "buffer_load_dwordx4 v242, %[wr], s44 offen offset:2048 nt lds\n\t" \
    "ds_read_b64_tr_b16 v[224:225], v244 offset:0\n\t" \
    "ds_read_b64_tr_b16 v[226:227], v244 offset:1536\n\t" \
    "ds_read_b64_tr_b16 v[120:121], v244 offset:32\n\t" \
    "ds_read_b64_tr_b16 v[122:123], v244 offset:1568\n\t" \
    "ds_read_b64_tr_b16 v[128:129], v244 offset:64\n\t" \
    "ds_read_b64_tr_b16 v[130:131], v244 offset:1600\n\t" \
    "v_add_u32 v244, 0x1e000, v243\n\t" \
    "s_waitcnt vmcnt(12) lgkmcnt(0)\n\t" \
    "s_barrier \n\t" \
    "s_add_u32 m0, %[wm], 0x18000\n\t" \
    "s_mov_b32 s44, 0x480000\n\t" \
    "buffer_load_dwordx4 v240, %[wr], s44 offen offset:0 nt lds\n\t" \
    "buffer_load_dwordx4 v241, %[wr], s44 offen offset:1024 nt lds\n\t" \
    "buffer_load_dwordx4 v242, %[wr], s44 offen offset:2048 nt lds\n\t" \
    "ds_read_b64_tr_b16 v[228:229], v244 offset:0\n\t" \
    "ds_read_b64_tr_b16 v[230:231], v244 offset:1536\n\t" \
    "ds_read_b64_tr_b16 v[124:125], v244 offset:32\n\t" \
    "ds_read_b64_tr_b16 v[126:127], v244 offset:1568\n\t" \
    "ds_read_b64_tr_b16 v[132:133], v244 offset:64\n\t" \
    "ds_read_b64_tr_b16 v[134:135], v244 offset:1600\n\t" \
    "v_add_u32 v244, 0x0, v243\n\t" \
    "s_waitcnt vmcnt(12) lgkmcnt(0)\n\t" \
    "s_barrier \n\t" \
    "v_mov_b32 v246, 0\n\t" \
    "buffer_load_dword v246, v246, s[60:63], %[mo] offen sc1\n\t" \
    "s_add_u32 m0, %[wm], 0x1e000\n\t" \
    "s_mov_b32 s44, 0x4e0000\n\t" \
    "buffer_load_dwordx4 v240, %[wr], s44 offen offset:0 nt lds\n\t" \
    "buffer_load_dwordx4 v241, %[wr], s44 offen offset:1024 nt lds\n\t" \
    "buffer_load_dwordx4 v242, %[wr], s44 offen offset:2048 nt lds\n\t" \
    "ds_read_b64_tr_b16 v[232:233], v244 offset:0\n\t" \
    "ds_read_b64_tr_b16 v[234:235], v244 offset:1536\n\t" \
    "ds_read_b64_tr_b16 v[160:161], v244 offset:32\n\t" \
    "ds_read_b64_tr_b16 v[162:163], v244 offset:1568\n\t" \
    "ds_read_b64_tr_b16 v[168:169], v244 offset:64\n\t" \
    "ds_read_b64_tr_b16 v[170:171], v244 offset:1600\n\t" \
    "v_add_u32 v244, 0x6000, v243\n\t" \
    "s_waitcnt vmcnt(12) lgkmcnt(0)\n\t" \
    "s_barrier \n\t" \
    "s_add_u32 m0, %[wm], 0x0\n\t" \
    "s_mov_b32 s44, 0x540000\n\t" \
    "buffer_load_dwordx4 v240, %[wr], s44 offen offset:0 nt lds\n\t" \
    "buffer_load_dwordx4 v241, %[wr], s44 offen offset:1024 nt lds\n\t" \
    "buffer_load_dwordx4 v242, %[wr], s44 offen offset:2048 nt lds\n\t" \
    "ds_read_b64_tr_b16 v[236:237], v244 offset:0\n\t" \
    "ds_read_b64_tr_b16 v[238:239], v244 offset:1536\n\t" \
    "ds_read_b64_tr_b16 v[164:165], v244 offset:32\n\t" \
    "ds_read_b64_tr_b16 v[166:167], v244 offset:1568\n\t" \
    "ds_read_b64_tr_b16 v[172:173], v244 offset:64\n\t" \
    "ds_read_b64_tr_b16 v[174:175], v244 offset:1600\n\t" \
    "v_add_u32 v244, 0xc000, v243\n\t" \
    "s_waitcnt vmcnt(12) lgkmcnt(0)\n\t" \
    "s_barrier \n\t" \
    "s_add_u32 m0, %[wm], 0x6000\n\t" \
    "s_mov_b32 s44, 0x5a0000\n\t" \
    "buffer_load_dwordx4 v240, %[wr], s44 offen offset:0 nt lds\n\t" \
    "buffer_load_dwordx4 v241, %[wr], s44 offen offset:1024 nt lds\n\t" \
    "buffer_load_dwordx4 v242, %[wr], s44 offen offset:2048 nt lds\n\t" \
    "ds_read_b64_tr_b16 v[16:17], v244 offset:0\n\t" \
    "ds_read_b64_tr_b16 v[18:19], v244 offset:1536\n\t" \
    "ds_read_b64_tr_b16 v[24:25], v244 offset:32\n\t" \
    "ds_read_b64_tr_b16 v[26:27], v244 offset:1568\n\t" \
    "ds_read_b64_tr_b16 v[32:33], v244 offset:64\n\t" \
    "ds_read_b64_tr_b16 v[34:35], v244 offset:1600\n\t" \
    "v_add_u32 v244, 0x12000, v243\n\t" \
    "s_waitcnt vmcnt(12) lgkmcnt(0)\n\t" \
    "s_barrier \n\t" \
    "s_add_u32 m0, %[wm], 0xc000\n\t" \
    "s_mov_b32 s44, 0x600000\n\t" \
    "buffer_load_dwordx4 v240, %[wr], s44 offen offset:0 nt lds\n\t" \
    "buffer_load_dwordx4 v241, %[wr], s44 offen offset:1024 nt lds\n\t" \
    "buffer_load_dwordx4 v242, %[wr], s44 offen offset:2048 nt lds\n\t" \
    "ds_read_b64_tr_b16 v[20:21], v244 offset:0\n\t" \
    "ds_read_b64_tr_b16 v[22:23], v244 offset:1536\n\t" \
    "ds_read_b64_tr_b16 v[28:29], v244 offset:32\n\t" \
    "ds_read_b64_tr_b16 v[30:31], v244 offset:1568\n\t" \
    "ds_read_b64_tr_b16 v[36:37], v244 offset:64\n\t" \
    "ds_read_b64_tr_b16 v[38:39], v244 offset:1600\n\t" \
    "v_add_u32 v244, 0x18000, v243\n\t" \
    "s_waitcnt vmcnt(12) lgkmcnt(0)\n\t" \
    "s_barrier \n\t" \
    "s_add_u32 m0, %[wm], 0x12000\n\t" \
    "s_mov_b32 s44, 0x660000\n\t" \
    "buffer_load_dwordx4 v240, %[wr], s44 offen offset:0 nt lds\n\t" \
    "buffer_load_dwordx4 v241, %[wr], s44 offen offset:1024 nt lds\n\t" \
    "buffer_load_dwordx4 v242, %[wr], s44 offen offset:2048 nt lds\n\t" \
    "ds_read_b64_tr_b16 v[56:57], v244 offset:0\n\t" \
    "ds_read_b64_tr_b16 v[58:59], v244 offset:1536\n\t" \
    "ds_read_b64_tr_b16 v[64:65], v244 offset:32\n\t" \
    "ds_read_b64_tr_b16 v[66:67], v244 offset:1568\n\t" \
    "ds_read_b64_tr_b16 v[72:73], v244 offset:64\n\t" \
    "ds_read_b64_tr_b16 v[74:75], v244 offset:1600\n\t" \
    "v_add_u32 v244, 0x1e000, v243\n\t" \
    "s_waitcnt vmcnt(12) lgkmcnt(0)\n\t" \
    "s_barrier \n\t" \
    ".Ls01_%=:\n\t" \
    "v_readfirstlane_b32 s43, v246\n\t" \
    "s_nop 0\n\t" \
    "s_cmp_lg_u32 s43, -1\n\t" \
    "s_cbranch_scc1 .Ls02_%=\n\t" \
    "s_cmp_eq_u32 s41, 0\n\t" \
    "s_cbranch_scc1 .Ls02_%=\n\t" \
    "s_sub_u32 s41, s41, 1\n\t" \
    "s_sleep 4\n\t" \
    "v_mov_b32 v246, 0\n\t" \
    "buffer_load_dword v246, v246, s[60:63], %[mo] offen sc1\n\t" \
    "s_waitcnt vmcnt(0)\n\t" \
    "s_branch .Ls01_%=\n\t" \
    ".Ls02_%=:\n\t" \
    "v_mov_b32 v246, 0x8000\n\t" \
    "s_mov_b64 exec, 1\n\t" \
    "buffer_store_dword v246, v246, s[60:63], %[mo] offen sc1\n\t" \
    "s_mov_b64 exec, -1\n\t" \
    "s_and_b32 s43, s43, 0x2000\n\t" \
    "s_xor_b32 s43, s43, %[lx]\n\t" \
    "s_add_u32 s60, s60, s43\n\t" \
    "s_addc_u32 s61, s61, 0\n\t" \
    "s_add_u32 m0, %[wm], 0x18000\n\t" \
    "s_mov_b32 s44, 0x6c0000\n\t" \
    "buffer_load_dwordx4 v240, %[wr], s44 offen offset:0 nt lds\n\t" \
    "buffer_load_dwordx4 v241, %[wr], s44 offen offset:1024 nt lds\n\t" \
    "buffer_load_dwordx4 v242, %[wr], s44 offen offset:2048 nt lds\n\t" \
    "ds_read_b64_tr_b16 v[60:61], v244 offset:0\n\t" \
    "ds_read_b64_tr_b16 v[62:63], v244 offset:1536\n\t" \
    "ds_read_b64_tr_b16 v[68:69], v244 offset:32\n\t" \
    "ds_read_b64_tr_b16 v[70:71], v244 offset:1568\n\t" \
    "ds_read_b64_tr_b16 v[76:77], v244 offset:64\n\t" \
    "ds_read_b64_tr_b16 v[78:79], v244 offset:1600\n\t" \
    "v_add_u32 v244, 0x0, v243\n\t" \
    "s_waitcnt vmcnt(12) lgkmcnt(0)\n\t" \
    "s_barrier \n\t" \
    "s_add_u32 m0, %[wm], 0x1e000\n\t" \
    "s_mov_b32 s44, 0x720000\n\t" \
    "buffer_load_dwordx4 v240, %[wr], s44 offen offset:0 nt lds\n\t" \
    "buffer_load_dwordx4 v241, %[wr], s44 offen offset:1024 nt lds\n\t" \
    "buffer_load_dwordx4 v242, %[wr], s44 offen offset:2048 nt lds\n\t" \
    "ds_read_b64_tr_b16 v[96:97], v244 offset:0\n\t" \
    "ds_read_b64_tr_b16 v[98:99], v244 offset:1536\n\t" \
    "ds_read_b64_tr_b16 v[104:105], v244 offset:32\n\t" \
    "ds_read_b64_tr_b16 v[106:107], v244 offset:1568\n\t" \
    "ds_read_b64_tr_b16 v[112:113], v244 offset:64\n\t" \
    "ds_read_b64_tr_b16 v[114:115], v244 offset:1600\n\t" \
    "v_add_u32 v244, 0x6000, v243\n\t" \
    "s_waitcnt vmcnt(12) lgkmcnt(0)\n\t" \
    "s_barrier \n\t" \
    "ds_read_b64_tr_b16 v[100:101], v244 offset:0\n\t" \
    "ds_read_b64_tr_b16 v[102:103], v244 offset:1536\n\t" \
    "ds_read_b64_tr_b16 v[108:109], v244 offset:32\n\t" \
    "ds_read_b64_tr_b16 v[110:111], v244 offset:1568\n\t" \
    "ds_read_b64_tr_b16 v[116:117], v244 offset:64\n\t" \
    "ds_read_b64_tr_b16 v[118:119], v244 offset:1600\n\t" \
    "v_add_u32 v244, 0xc000, v243\n\t" \
    "s_waitcnt vmcnt(9) lgkmcnt(0)\n\t" \
    "s_barrier \n\t" \
    "ds_read_b64_tr_b16 v[136:137], v244 offset:0\n\t" \
    "ds_read_b64_tr_b16 v[138:139], v244 offset:1536\n\t" \
    "ds_read_b64_tr_b16 v[144:145], v244 offset:32\n\t" \
    "ds_read_b64_tr_b16 v[146:147], v244 offset:1568\n\t" \
    "ds_read_b64_tr_b16 v[152:153], v244 offset:64\n\t" \
    "ds_read_b64_tr_b16 v[154:155], v244 offset:1600\n\t" \
    "v_add_u32 v244, 0x12000, v243\n\t" \
    "s_waitcnt vmcnt(6) lgkmcnt(0)\n\t" \
    "s_barrier \n\t" \
    "ds_read_b64_tr_b16 v[140:141], v244 offset:0\n\t" \
    "ds_read_b64_tr_b16 v[142:143], v244 offset:1536\n\t" \
    "ds_read_b64_tr_b16 v[148:149], v244 offset:32\n\t" \
    "ds_read_b64_tr_b16 v[150:151], v244 offset:1568\n\t" \
    "ds_read_b64_tr_b16 v[156:157], v244 offset:64\n\t" \
    "ds_read_b64_tr_b16 v[158:159], v244 offset:1600\n\t" \
    "v_add_u32 v244, 0x18000, v243\n\t" \
    "s_waitcnt vmcnt(3) lgkmcnt(0)\n\t" \
    "s_barrier \n\t" \
    "ds_read_b64_tr_b16 v[176:177], v244 offset:0\n\t" \
    "ds_read_b64_tr_b16 v[178:179], v244 offset:1536\n\t" \
    "ds_read_b64_tr_b16 v[184:185], v244 offset:32\n\t" \
    "ds_read_b64_tr_b16 v[186:187], v244 offset:1568\n\t" \
    "ds_read_b64_tr_b16 v[192:193], v244 offset:64\n\t" \
    "ds_read_b64_tr_b16 v[194:195], v244 offset:1600\n\t" \
    "v_add_u32 v244, 0x1e000, v243\n\t" \
    "s_waitcnt vmcnt(0) lgkmcnt(0)\n\t" \
    "s_barrier \n\t" \
    "ds_read_b64_tr_b16 v[180:181], v244 offset:0\n\t" \
    "ds_read_b64_tr_b16 v[182:183], v244 offset:1536\n\t" \
    "ds_read_b64_tr_b16 v[188:189], v244 offset:32\n\t" \
    "ds_read_b64_tr_b16 v[190:191], v244 offset:1568\n\t" \
    "ds_read_b64_tr_b16 v[196:197], v244 offset:64\n\t" \
    "ds_read_b64_tr_b16 v[198:199], v244 offset:1600\n\t" \
    "s_waitcnt lgkmcnt(0)\n\t" \
    "s_barrier \n\t" \
    "ds_write_b128 v254, v[200:203] offset:30720\n\t" \
    "ds_write_b128 v254, v[204:207] offset:31744\n\t" \
    "ds_write_b128 v254, v[208:211] offset:32768\n\t" \
    "ds_write_b128 v254, v[212:215] offset:33792\n\t" \
    "ds_write_b128 v254, v[216:219] offset:34816\n\t" \
    "ds_write_b128 v254, v[220:223] offset:35840\n\t" \
    "ds_write_b128 v254, v[224:227] offset:36864\n\t" \
    "ds_write_b128 v254, v[228:231] offset:37888\n\t" \
    "ds_write_b128 v254, v[232:235] offset:38912\n\t" \
    "ds_write_b128 v254, v[236:239] offset:39936\n\t" \
    "s_waitcnt vmcnt(0) lgkmcnt(0)\n\t" \
    "s_barrier \n\t" \
    "ds_read_b128 v[224:227], v253 offset:0\n\t" \
    "ds_read_b128 v[228:231], v253 offset:1024\n\t" \
    "ds_read_b128 v[232:235], v253 offset:20480\n\t" \
    "ds_read_b128 v[236:239], v253 offset:21504\n\t" \
    ".Ls03_%=:\n\t" \
    "s_waitcnt lgkmcnt(3)\n\t" \
    "v_mfma_f32_16x16x32_bf16 v[204:207], v[0:3], v[224:227], 0\n\t" \
    "s_waitcnt lgkmcnt(2)\n\t" \
    "v_mfma_f32_16x16x32_bf16 v[204:207], v[4:7], v[228:231], v[204:207]\n\t" \
    "ds_read_b128 v[240:243], v253 offset:2048\n\t" \
    "v_mfma_f32_16x16x32_bf16 v[208:211], v[8:11], v[224:227], 0\n\t" \
    "v_mfma_f32_16x16x32_bf16 v[208:211], v[12:15], v[228:231], v[208:211]\n\t" \
    "s_waitcnt lgkmcnt(2)\n\t" \
    "v_mfma_f32_16x16x32_bf16 v[216:219], v[0:3], v[232:235], 0\n\t" \
    "ds_read_b128 v[224:227], v253 offset:3072\n\t" \
    "s_waitcnt lgkmcnt(2)\n\t" \
    "v_mfma_f32_16x16x32_bf16 v[216:219], v[4:7], v[236:239], v[216:219]\n\t" \
    "ds_read_b128 v[228:231], v253 offset:22528\n\t" \
    "v_mfma_f32_16x16x32_bf16 v[220:223], v[8:11], v[232:235], 0\n\t" \
    "v_mfma_f32_16x16x32_bf16 v[220:223], v[12:15], v[236:239], v[220:223]\n\t" \
    "s_waitcnt lgkmcnt(2)\n\t" \
    "v_mfma_f32_16x16x32_bf16 v[204:207], v[40:43], v[240:243], v[204:207]\n\t" \
    "ds_read_b128 v[232:235], v253 offset:23552\n\t" \
    "s_waitcnt lgkmcnt(2)\n\t" \
    "v_mfma_f32_16x16x32_bf16 v[204:207], v[44:47], v[224:227], v[204:207]\n\t" \
    "ds_read_b128 v[236:239], v253 offset:4096\n\t" \
    "v_mfma_f32_16x16x32_bf16 v[208:211], v[48:51], v[240:243], v[208:211]\n\t" \
    "v_mfma_f32_16x16x32_bf16 v[208:211], v[52:55], v[224:227], v[208:211]\n\t" \
    "s_waitcnt lgkmcnt(2)\n\t" \
    "v_mfma_f32_16x16x32_bf16 v[216:219], v[40:43], v[228:231], v[216:219]\n\t" \
    "ds_read_b128 v[240:243], v253 offset:5120\n\t" \
    "s_waitcnt lgkmcnt(2)\n\t" \
    "v_mfma_f32_16x16x32_bf16 v[216:219], v[44:47], v[232:235], v[216:219]\n\t" \
    "ds_read_b128 v[224:227], v253 offset:24576\n\t" \
    "v_mfma_f32_16x16x32_bf16 v[220:223], v[48:51], v[228:231], v[220:223]\n\t" \
    "v_mfma_f32_16x16x32_bf16 v[220:223], v[52:55], v[232:235], v[220:223]\n\t" \
    "s_waitcnt lgkmcnt(2)\n\t" \
    "v_mfma_f32_16x16x32_bf16 v[204:207], v[80:83], v[236:239], v[204:207]\n\t" \
    "ds_read_b128 v[228:231], v253 offset:25600\n\t" \
    "s_waitcnt lgkmcnt(2)\n\t" \
    "v_mfma_f32_16x16x32_bf16 v[204:207], v[84:87], v[240:243], v[204:207]\n\t" \
    "ds_read_b128 v[232:235], v253 offset:6144\n\t" \
    "v_mfma_f32_16x16x32_bf16 v[208:211], v[88:91], v[236:239], v[208:211]\n\t" \
    "v_mfma_f32_16x16x32_bf16 v[208:211], v[92:95], v[240:243], v[208:211]\n\t" \
    "s_waitcnt lgkmcnt(2)\n\t" \
    "v_mfma_f32_16x16x32_bf16 v[216:219], v[80:83], v[224:227], v[216:219]\n\t" \
    "ds_read_b128 v[236:239], v253 offset:7168\n\t" \
    "s_waitcnt lgkmcnt(2)\n\t" \
    "v_mfma_f32_16x16x32_bf16 v[216:219], v[84:87], v[228:231], v[216:219]\n\t" \
    "ds_read_b128 v[240:243], v253 offset:26624\n\t" \
    "v_mfma_f32_16x16x32_bf16 v[220:223], v[88:91], v[224:227], v[220:223]\n\t" \
    "v_mfma_f32_16x16x32_bf16 v[220:223], v[92:95], v[228:231], v[220:223]\n\t" \
    "s_waitcnt lgkmcnt(2)\n\t" \
    "v_mfma_f32_16x16x32_bf16 v[204:207], v[120:123], v[232:235], v[204:207]\n\t" \
    "ds_read_b128 v[224:227], v253 offset:27648\n\t" \
    "s_waitcnt lgkmcnt(2)\n\t" \
    "v_mfma_f32_16x16x32_bf16 v[204:207], v[124:127], v[236:239], v[204:207]\n\t" \
    "ds_read_b128 v[228:231], v253 offset:8192\n\t" \
    "v_mfma_f32_16x16x32_bf16 v[208:211], v[128:131], v[232:235], v[208:211]\n\t" \
    "v_mfma_f32_16x16x32_bf16 v[208:211], v[132:135], v[236:239], v[208:211]\n\t" \
    "s_waitcnt lgkmcnt(2)\n\t" \
    "v_mfma_f32_16x16x32_bf16 v[216:219], v[120:123], v[240:243], v[216:219]\n\t" \
    "ds_read_b128 v[232:235], v253 offset:9216\n\t" \
    "s_waitcnt lgkmcnt(2)\n\t" \
    "v_mfma_f32_16x16x32_bf16 v[216:219], v[124:127], v[224:227], v[216:219]\n\t" \
    "ds_read_b128 v[236:239], v253 offset:28672\n\t" \
    "v_mfma_f32_16x16x32_bf16 v[220:223], v[128:131], v[240:243], v[220:223]\n\t" \
    "v_mfma_f32_16x16x32_bf16 v[220:223], v[132:135], v[224:227], v[220:223]\n\t" \
    "s_waitcnt lgkmcnt(2)\n\t" \
    "v_mfma_f32_16x16x32_bf16 v[204:207], v[160:163], v[228:231], v[204:207]\n\t" \
    "ds_read_b128 v[240:243], v253 offset:29696\n\t" \
    "s_waitcnt lgkmcnt(2)\n\t" \
    "v_mfma_f32_16x16x32_bf16 v[204:207], v[164:167], v[232:235], v[204:207]\n\t" \
    "ds_read_b128 v[224:227], v253 offset:10240\n\t" \
    "v_mfma_f32_16x16x32_bf16 v[208:211], v[168:171], v[228:231], v[208:211]\n\t" \
    "v_mfma_f32_16x16x32_bf16 v[208:211], v[172:175], v[232:235], v[208:211]\n\t" \
    "s_waitcnt lgkmcnt(2)\n\t" \
    "v_mfma_f32_16x16x32_bf16 v[216:219], v[160:163], v[236:239], v[216:219]\n\t" \
    "ds_read_b128 v[228:231], v253 offset:11264\n\t" \
    "s_waitcnt lgkmcnt(2)\n\t" \
    "v_mfma_f32_16x16x32_bf16 v[216:219], v[164:167], v[240:243], v[216:219]\n\t" \
    "ds_read_b128 v[232:235], v253 offset:30720\n\t" \
    "v_mfma_f32_16x16x32_bf16 v[220:223], v[168:171], v[236:239], v[220:223]\n\t" \
    "v_mfma_f32_16x16x32_bf16 v[220:223], v[172:175], v[240:243], v[220:223]\n\t" \
    "ds_read_b128 v[236:239], v253 offset:31744\n\t" \
    "s_setprio 2\n\t" \
    "s_waitcnt lgkmcnt(0)\n\t" \
    "s_barrier \n\t" \
    "ds_read_b128 v[200:203], v253 offset:40960\n\t" \
    "ds_read_b128 v[212:215], v253 offset:41984\n\t" \
    "v_mfma_f32_16x16x32_bf16 v[204:207], v[24:27], v[224:227], v[204:207]\n\t" \
    "v_mfma_f32_16x16x32_bf16 v[204:207], v[28:31], v[228:231], v[204:207]\n\t" \
    "ds_read_b128 v[240:243], v253 offset:12288\n\t" \
    "v_mfma_f32_16x16x32_bf16 v[208:211], v[32:35], v[224:227], v[208:211]\n\t" \
    "v_mfma_f32_16x16x32_bf16 v[208:211], v[36:39], v[228:231], v[208:211]\n\t" \
    "s_waitcnt lgkmcnt(2)\n\t" \
    "v_mfma_f32_16x16x32_bf16 v[200:203], v[16:19], v[224:227], v[200:203]\n\t" \
    "v_mfma_f32_16x16x32_bf16 v[200:203], v[20:23], v[228:231], v[200:203]\n\t" \
    "v_mfma_f32_16x16x32_bf16 v[216:219], v[24:27], v[232:235], v[216:219]\n\t" \
    "ds_read_b128 v[224:227], v253 offset:13312\n\t" \
    "s_and_b32 s44, s40, 3\n\t" \
    "v_mfma_f32_16x16x32_bf16 v[216:219], v[28:31], v[236:239], v[216:219]\n\t" \
    "ds_read_b128 v[228:231], v253 offset:32768\n\t" \
    "s_lshl_b32 s44, s44, 17\n\t" \
    "v_mfma_f32_16x16x32_bf16 v[220:223], v[32:35], v[232:235], v[220:223]\n\t" \
    "s_add_u32 s44, s44, %[mb]\n\t" \
    "v_mfma_f32_16x16x32_bf16 v[220:223], v[36:39], v[236:239], v[220:223]\n\t" \
    "s_add_u32 s46, s44, 0x0\n\t" \
    "s_waitcnt lgkmcnt(3)\n\t" \
    "v_mfma_f32_16x16x32_bf16 v[212:215], v[16:19], v[232:235], v[212:215]\n\t" \
    "s_add_u32 s47, s44, 0x1400\n\t" \
    "v_mfma_f32_16x16x32_bf16 v[212:215], v[20:23], v[236:239], v[212:215]\n\t" \
    "s_add_u32 s48, s44, 0x400\n\t" \
    "s_waitcnt lgkmcnt(2)\n\t" \
    "v_mfma_f32_16x16x32_bf16 v[204:207], v[64:67], v[240:243], v[204:207]\n\t" \
    "ds_read_b128 v[232:235], v253 offset:33792\n\t" \
    "s_waitcnt lgkmcnt(2)\n\t" \
    "v_mfma_f32_16x16x32_bf16 v[204:207], v[68:71], v[224:227], v[204:207]\n\t" \
    "ds_read_b128 v[236:239], v253 offset:14336\n\t" \
    "v_mfma_f32_16x16x32_bf16 v[208:211], v[72:75], v[240:243], v[208:211]\n\t" \
    "v_mfma_f32_16x16x32_bf16 v[208:211], v[76:79], v[224:227], v[208:211]\n\t" \
    "v_mfma_f32_16x16x32_bf16 v[200:203], v[56:59], v[240:243], v[200:203]\n\t" \
    "v_mfma_f32_16x16x32_bf16 v[200:203], v[60:63], v[224:227], v[200:203]\n\t" \
    "s_waitcnt lgkmcnt(2)\n\t" \
    "v_mfma_f32_16x16x32_bf16 v[216:219], v[64:67], v[228:231], v[216:219]\n\t" \
    "ds_read_b128 v[240:243], v253 offset:15360\n\t" \
    "s_waitcnt lgkmcnt(2)\n\t" \
    "v_mfma_f32_16x16x32_bf16 v[216:219], v[68:71], v[232:235], v[216:219]\n\t" \
    "ds_read_b128 v[224:227], v253 offset:34816\n\t" \
    "v_mfma_f32_16x16x32_bf16 v[220:223], v[72:75], v[228:231], v[220:223]\n\t" \
    "v_mfma_f32_16x16x32_bf16 v[220:223], v[76:79], v[232:235], v[220:223]\n\t" \
    "v_mfma_f32_16x16x32_bf16 v[212:215], v[56:59], v[228:231], v[212:215]\n\t" \
    "v_mfma_f32_16x16x32_bf16 v[212:215], v[60:63], v[232:235], v[212:215]\n\t" \
    "s_waitcnt lgkmcnt(2)\n\t" \
    "v_mfma_f32_16x16x32_bf16 v[204:207], v[104:107], v[236:239], v[204:207]\n\t" \
    "ds_read_b128 v[228:231], v253 offset:35840\n\t" \
    "s_waitcnt lgkmcnt(2)\n\t" \
    "v_mfma_f32_16x16x32_bf16 v[204:207], v[108:111], v[240:243], v[204:207]\n\t" \
    "ds_read_b128 v[232:235], v253 offset:16384\n\t" \
    "v_mfma_f32_16x16x32_bf16 v[208:211], v[112:115], v[236:239], v[208:211]\n\t" \
    "v_mfma_f32_16x16x32_bf16 v[208:211], v[116:119], v[240:243], v[208:211]\n\t" \
    "v_mfma_f32_16x16x32_bf16 v[200:203], v[96:99], v[236:239], v[200:203]\n\t" \
    "v_mfma_f32_16x16x32_bf16 v[200:203], v[100:103], v[240:243], v[200:203]\n\t" \
    "s_waitcnt lgkmcnt(2)\n\t" \
    "v_mfma_f32_16x16x32_bf16 v[216:219], v[104:107], v[224:227], v[216:219]\n\t" \
    "ds_read_b128 v[236:239], v253 offset:17408\n\t" \
    "s_waitcnt lgkmcnt(2)\n\t" \
    "v_mfma_f32_16x16x32_bf16 v[216:219], v[108:111], v[228:231], v[216:219]\n\t" \
    "ds_read_b128 v[240:243], v253 offset:18432\n\t" \
    "v_mfma_f32_16x16x32_bf16 v[220:223], v[112:115], v[224:227], v[220:223]\n\t" \
    "v_mfma_f32_16x16x32_bf16 v[220:223], v[116:119], v[228:231], v[220:223]\n\t" \
    "v_mfma_f32_16x16x32_bf16 v[212:215], v[96:99], v[224:227], v[212:215]\n\t" \
    "v_mfma_f32_16x16x32_bf16 v[212:215], v[100:103], v[228:231], v[212:215]\n\t" \
    "s_waitcnt lgkmcnt(2)\n\t" \
    "v_mfma_f32_16x16x32_bf16 v[204:207], v[144:147], v[232:235], v[204:207]\n\t" \
    "ds_read_b128 v[224:227], v253 offset:19456\n\t" \
    "v_mov_b64 v[244:245], 0\n\t" \
    "s_waitcnt lgkmcnt(2)\n\t" \
    "v_mfma_f32_16x16x32_bf16 v[204:207], v[148:151], v[236:239], v[204:207]\n\t" \
    "ds_read_b128 v[228:231], v253 offset:36864\n\t" \
    "v_mov_b64 v[246:247], 0\n\t" \
    "v_mfma_f32_16x16x32_bf16 v[208:211], v[152:155], v[232:235], v[208:211]\n\t" \
    "v_mov_b64 v[248:249], 0\n\t" \
    "v_mfma_f32_16x16x32_bf16 v[208:211], v[156:159], v[236:239], v[208:211]\n\t" \
    "v_mov_b64 v[250:251], 0\n\t" \
    "v_mfma_f32_16x16x32_bf16 v[200:203], v[136:139], v[232:235], v[200:203]\n\t" \
    "v_mfma_f32_16x16x32_bf16 v[200:203], v[140:143], v[236:239], v[200:203]\n\t" \
    "s_waitcnt lgkmcnt(2)\n\t" \
    "v_mfma_f32_16x16x32_bf16 v[204:207], v[184:187], v[240:243], v[204:207]\n\t" \
    "ds_read_b128 v[232:235], v253 offset:37888\n\t" \
    "s_waitcnt lgkmcnt(2)\n\t" \
    "v_mfma_f32_16x16x32_bf16 v[204:207], v[188:191], v[224:227], v[204:207]\n\t" \
    "ds_read_b128 v[236:239], v253 offset:38912\n\t" \
    "v_mfma_f32_16x16x32_bf16 v[208:211], v[192:195], v[240:243], v[208:211]\n\t" \
    "v_mfma_f32_16x16x32_bf16 v[208:211], v[196:199], v[224:227], v[208:211]\n\t" \
    "v_mfma_f32_16x16x32_bf16 v[200:203], v[176:179], v[240:243], v[200:203]\n\t" \
    "v_mfma_f32_16x16x32_bf16 v[200:203], v[180:183], v[224:227], v[200:203]\n\t" \
    "ds_read_b128 v[240:243], v253 offset:39936\n\t" \
    "s_waitcnt lgkmcnt(3)\n\t" \
    "v_mfma_f32_16x16x32_bf16 v[216:219], v[144:147], v[228:231], v[216:219]\n\t" \
    "s_waitcnt lgkmcnt(2)\n\t" \
    "v_mfma_f32_16x16x32_bf16 v[216:219], v[148:151], v[232:235], v[216:219]\n\t" \
    "v_cvt_pk_bf16_f32 v204, v204, v208\n\t" \
    "v_cvt_pk_bf16_f32 v205, v205, v209\n\t" \
    "s_waitcnt lgkmcnt(1)\n\t" \
    "v_mfma_f32_16x16x32_bf16 v[216:219], v[184:187], v[236:239], v[216:219]\n\t" \
    "v_cvt_pk_bf16_f32 v206, v206, v210\n\t" \
    "v_cvt_pk_bf16_f32 v207, v207, v211\n\t" \
    "s_waitcnt lgkmcnt(0)\n\t" \
    "v_mfma_f32_16x16x32_bf16 v[216:219], v[188:191], v[240:243], v[216:219]\n\t" \
    "ds_write_b128 v254, v[204:207] offset:8192\n\t" \
    "v_mfma_f32_16x16x32_bf16 v[220:223], v[152:155], v[228:231], v[220:223]\n\t" \
    "v_mfma_f32_16x16x32_bf16 v[220:223], v[156:159], v[232:235], v[220:223]\n\t" \
    "v_cvt_pk_bf16_f32 v200, v200, v201\n\t" \
    "v_cvt_pk_bf16_f32 v201, v202, v203\n\t" \
    "v_mfma_f32_16x16x32_bf16 v[220:223], v[192:195], v[236:239], v[220:223]\n\t" \
    "ds_write_b64 v254, v[200:201] offset:6144\n\t" \
    "v_mfma_f32_16x16x32_bf16 v[220:223], v[196:199], v[240:243], v[220:223]\n\t" \
    "v_mfma_f32_16x16x32_bf16 v[212:215], v[136:139], v[228:231], v[212:215]\n\t" \
    "ds_read_b128 v[200:203], v254 offset:30720\n\t" \
    "ds_read_b128 v[204:207], v254 offset:31744\n\t" \
    "v_mfma_f32_16x16x32_bf16 v[212:215], v[140:143], v[232:235], v[212:215]\n\t" \
    "v_mov_b64 v[208:209], 0\n\t" \
    "v_mov_b64 v[210:211], 0\n\t" \
    "v_mfma_f32_16x16x32_bf16 v[212:215], v[176:179], v[236:239], v[212:215]\n\t" \
    "v_cvt_pk_bf16_f32 v216, v216, v220\n\t" \
    "v_cvt_pk_bf16_f32 v217, v217, v221\n\t" \
    "v_mfma_f32_16x16x32_bf16 v[212:215], v[180:183], v[240:243], v[212:215]\n\t" \
    "v_cvt_pk_bf16_f32 v218, v218, v222\n\t" \
    "v_cvt_pk_bf16_f32 v219, v219, v223\n\t" \
    "s_setprio 0\n\t" \
    "ds_write_b128 v254, v[216:219] offset:14336\n\t" \
    "ds_read_b128 v[216:219], v254 offset:32768\n\t" \
    "ds_read_b128 v[220:223], v254 offset:33792\n\t" \
    "s_nop 2\n\t" \
    "v_cvt_pk_bf16_f32 v212, v212, v213\n\t" \
    "v_cvt_pk_bf16_f32 v213, v214, v215\n\t" \
    "ds_write_b64 v254, v[212:213] offset:12288\n\t" \
    "s_sleep 3\n\t" \
    "s_mov_b32 m0, 0xa000\n\t" \
    "s_nop 0\n\t" \
    "buffer_load_dwordx4 v253, s[60:63], s46 offen sc1 lds\n\t" \
    "s_mov_b32 m0, 0xb400\n\t" \
    "s_nop 0\n\t" \
    "buffer_load_dwordx4 v253, s[60:63], s47 offen sc1 lds\n\t" \
    "s_mov_b32 m0, 0xa400\n\t" \
    "s_nop 0\n\t" \
    "buffer_load_dwordx4 v253, s[60:63], s48 offen sc1 lds\n\t" \
    "s_waitcnt lgkmcnt(0)\n\t" \
    "v_bfe_i32 v212, v253, 5, 1\n\t" \
    "v_and_b32 v212, 0xaaaa, v212\n\t" \
    "v_or_b32 v212, 0x4444, v212\n\t" \
    "v_and_b32 v213, 0xffff, v255\n\t" \
    "v_lshrrev_b32 v214, 16, v255\n\t" \
    ".Ls04_%=:\n\t" \
    "s_waitcnt vmcnt(0)\n\t" \
    "ds_read_b128 v[224:227], v253 offset:40960\n\t" \
    "ds_read_b128 v[228:231], v253 offset:46080\n\t" \
    "ds_read_b128 v[232:235], v253 offset:41984\n\t" \
    "s_waitcnt lgkmcnt(2)\n\t" \
    "v_max_u32 v215, v224, v225\n\t" \
    "v_max3_u32 v215, v226, v227, v215\n\t" \
    "s_waitcnt lgkmcnt(1)\n\t" \
    "v_max3_u32 v215, v228, v229, v215\n\t" \
    "v_max3_u32 v215, v230, v231, v215\n\t" \
    "s_waitcnt lgkmcnt(0)\n\t" \
    "v_max3_u32 v215, v232, v233, v215\n\t" \
    "v_max3_u32 v215, v234, v235, v215\n\t" \
    "v_cmp_eq_u32 vcc, -1, v215\n\t" \
    "s_cbranch_vccz .Ls05_%=\n\t" \
    "v_max_u32 v215, v224, v225\n\t" \
    "v_max3_u32 v215, v226, v227, v215\n\t" \
    "v_cmp_eq_u32 vcc, -1, v215\n\t" \
    "s_cbranch_vccz .Ls06_%=\n\t" \
    "s_mov_b32 m0, 0xa000\n\t" \
    "s_nop 0\n\t" \
    "buffer_load_dwordx4 v253, s[60:63], s46 offen sc1 lds\n\t" \
    ".Ls06_%=:\n\t" \
    "v_max_u32 v215, v228, v229\n\t" \
    "v_max3_u32 v215, v230, v231, v215\n\t" \
    "v_cmp_eq_u32 vcc, -1, v215\n\t" \
    "s_cbranch_vccz .Ls07_%=\n\t" \
    "s_mov_b32 m0, 0xb400\n\t" \
    "s_nop 0\n\t" \
    "buffer_load_dwordx4 v253, s[60:63], s47 offen sc1 lds\n\t" \
    ".Ls07_%=:\n\t" \
    "v_max_u32 v215, v232, v233\n\t" \
    "v_max3_u32 v215, v234, v235, v215\n\t" \
    "v_cmp_eq_u32 vcc, -1, v215\n\t" \
    "s_cbranch_vccz .Ls08_%=\n\t" \
    "s_mov_b32 m0, 0xa400\n\t" \
    "s_nop 0\n\t" \
    "buffer_load_dwordx4 v253, s[60:63], s48 offen sc1 lds\n\t" \
    ".Ls08_%=:\n\t" \
    "s_cmp_eq_u32 s41, 0\n\t" \
    "s_cbranch_scc1 .Ls05_%=\n\t" \
    "s_sub_u32 s41, s41, 1\n\t" \
    "s_branch .Ls04_%=\n\t" \
    ".Ls05_%=:\n\t" \
    "s_setprio 3\n\t" \
    "s_waitcnt lgkmcnt(0)\n\t" \
    "s_barrier \n\t" \
    "ds_read_b128 v[236:239], v253 offset:47104\n\t" \
    "ds_read_b128 v[240:243], v253 offset:43008\n\t" \
    "v_smfmac_f32_16x16x64_bf16 v[244:247], v[224:227], v[0:7], v212\n\t" \
    "v_smfmac_f32_16x16x64_bf16 v[248:251], v[224:227], v[8:15], v212\n\t" \
    "v_smfmac_f32_16x16x64_bf16 v[208:211], v[224:227], v[200:207], v212\n\t" \
    "ds_read_b128 v[200:203], v254 offset:34816\n\t" \
    "ds_read_b128 v[204:207], v254 offset:35840\n\t" \
    "ds_read_b128 v[224:227], v253 offset:48128\n\t" \
    "v_smfmac_f32_16x16x64_bf16 v[208:211], v[228:231], v[16:23], v212\n\t" \
    "v_smfmac_f32_16x16x64_bf16 v[244:247], v[228:231], v[24:31], v212\n\t" \
    "v_smfmac_f32_16x16x64_bf16 v[248:251], v[228:231], v[32:39], v212\n\t" \
    "ds_read_b128 v[228:231], v253 offset:44032\n\t" \
    "v_smfmac_f32_16x16x64_bf16 v[208:211], v[232:235], v[216:223], v212\n\t" \
    "ds_read_b128 v[216:219], v254 offset:36864\n\t" \
    "ds_read_b128 v[220:223], v254 offset:37888\n\t" \
    "v_smfmac_f32_16x16x64_bf16 v[244:247], v[232:235], v[40:47], v212\n\t" \
    "v_smfmac_f32_16x16x64_bf16 v[248:251], v[232:235], v[48:55], v212\n\t" \
    "ds_read_b128 v[232:235], v253 offset:49152\n\t" \
    "s_waitcnt lgkmcnt(8)\n\t" \
    "v_smfmac_f32_16x16x64_bf16 v[208:211], v[236:239], v[56:63], v212\n\t" \
    "v_smfmac_f32_16x16x64_bf16 v[244:247], v[236:239], v[64:71], v212\n\t" \
    "v_smfmac_f32_16x16x64_bf16 v[248:251], v[236:239], v[72:79], v212\n\t" \
    "ds_read_b128 v[236:239], v253 offset:45056\n\t" \
    "s_waitcnt lgkmcnt(6)\n\t" \
    "v_smfmac_f32_16x16x64_bf16 v[208:211], v[240:243], v[200:207], v212\n\t" \
    "ds_read_b128 v[200:203], v254 offset:38912\n\t" \
    "ds_read_b128 v[204:207], v254 offset:39936\n\t" \
    "v_smfmac_f32_16x16x64_bf16 v[244:247], v[240:243], v[80:87], v212\n\t" \
    "v_smfmac_f32_16x16x64_bf16 v[248:251], v[240:243], v[88:95], v212\n\t" \
    "ds_read_b128 v[240:243], v253 offset:50176\n\t" \
    "s_waitcnt lgkmcnt(8)\n\t" \
    "v_smfmac_f32_16x16x64_bf16 v[208:211], v[224:227], v[96:103], v212\n\t" \
    "v_smfmac_f32_16x16x64_bf16 v[244:247], v[224:227], v[104:111], v212\n\t" \
    "v_smfmac_f32_16x16x64_bf16 v[248:251], v[224:227], v[112:119], v212\n\t" \
    "s_waitcnt lgkmcnt(5)\n\t" \
    "v_smfmac_f32_16x16x64_bf16 v[208:211], v[228:231], v[216:223], v212\n\t" \
    "s_waitcnt lgkmcnt(4)\n\t" \
    "v_smfmac_f32_16x16x64_bf16 v[208:211], v[232:235], v[136:143], v212\n\t" \
    "s_waitcnt lgkmcnt(1)\n\t" \
    "v_smfmac_f32_16x16x64_bf16 v[208:211], v[236:239], v[200:207], v212\n\t" \
    "s_waitcnt lgkmcnt(0)\n\t" \
    "v_smfmac_f32_16x16x64_bf16 v[208:211], v[240:243], v[176:183], v212\n\t" \
    "v_smfmac_f32_16x16x64_bf16 v[244:247], v[228:231], v[120:127], v212\n\t" \
    "v_smfmac_f32_16x16x64_bf16 v[244:247], v[232:235], v[144:151], v212\n\t" \
    "v_smfmac_f32_16x16x64_bf16 v[244:247], v[236:239], v[160:167], v212\n\t" \
    "v_add_f32 v208, v208, v210\n\t" \
    "v_add_f32 v209, v209, v211\n\t" \
    "v_cvt_pk_bf16_f32 v208, v208, v209\n\t" \
    "v_smfmac_f32_16x16x64_bf16 v[244:247], v[240:243], v[184:191], v212\n\t" \
    "ds_write_b16 v213, v208 offset:0\n\t" \
    "ds_write_b16_d16_hi v213, v208 offset:16\n\t" \
    "v_smfmac_f32_16x16x64_bf16 v[248:251], v[228:231], v[128:135], v212\n\t" \
    "v_smfmac_f32_16x16x64_bf16 v[248:251], v[232:235], v[152:159], v212\n\t" \
    "v_smfmac_f32_16x16x64_bf16 v[248:251], v[236:239], v[168:175], v212\n\t" \
    "v_add_f32 v244, v244, v246\n\t" \
    "v_add_f32 v245, v245, v247\n\t" \
    "v_smfmac_f32_16x16x64_bf16 v[248:251], v[240:243], v[192:199], v212\n\t" \
    "s_nop 7\n\t" \
    "v_add_f32 v248, v248, v250\n\t" \
    "v_add_f32 v249, v249, v251\n\t" \
    "v_cvt_pk_bf16_f32 v244, v244, v248\n\t" \
    "v_cvt_pk_bf16_f32 v245, v245, v249\n\t" \
    "ds_write2_b32 v214, v244, v245 offset1:4\n\t" \
    "s_setprio 0\n\t" \
    "s_sleep 2\n\t" \
    "ds_read_b128 v[200:203], v254 offset:18432\n\t" \
    "ds_read_b128 v[204:207], v254 offset:19456\n\t" \
    "ds_read_b128 v[214:217], v254 offset:24576\n\t" \
    "ds_read_b128 v[218:221], v254 offset:25600\n\t" \
    "ds_read_b128 v[222:225], v254 offset:20480\n\t" \
    "ds_read_b128 v[226:229], v254 offset:21504\n\t" \
    "ds_read_b128 v[230:233], v254 offset:26624\n\t" \
    "ds_read_b128 v[234:237], v254 offset:27648\n\t" \
    "v_and_b32 v213, 0xffffff7f, v254\n\t" \
    "v_mov_b64 v[242:243], 0\n\t" \
    "v_mov_b64 v[244:245], 0\n\t" \
    "v_mov_b64 v[246:247], 0\n\t" \
    "v_mov_b64 v[248:249], 0\n\t" \
    "v_mov_b64 v[250:251], 0\n\t" \
    "v_mov_b64 v[252:253], 0\n\t" \
    "s_and_b32 s44, s40, 3\n\t" \
    "s_mul_i32 s44, s44, 0x30000\n\t" \
    "s_add_u32 s50, s44, %[pb]\n\t" \
    "s_add_u32 s51, s50, 0xc000\n\t" \
    "s_add_u32 s52, s50, 0x18000\n\t" \
    "s_add_u32 s53, s50, 0x24000\n\t" \
    "s_setprio 3\n\t" \
    "s_waitcnt lgkmcnt(0)\n\t" \
    "s_barrier \n\t" \
    "ds_read_b128 v[208:211], v254 offset:0\n\t" \
    "ds_read_b128 v[238:241], v254 offset:1024\n\t" \
    "s_waitcnt lgkmcnt(1)\n\t" \
    "v_smfmac_f32_16x16x64_bf16 v[242:245], v[208:211], v[200:207], v212\n\t" \
    "ds_read_b128 v[200:203], v213 offset:3072\n\t" \
    "ds_read_b128 v[204:207], v213 offset:3200\n\t" \
    "v_smfmac_f32_16x16x64_bf16 v[246:249], v[208:211], v[214:221], v212\n\t" \
    "ds_read_b128 v[214:217], v213 offset:4096\n\t" \
    "ds_read_b128 v[218:221], v213 offset:4224\n\t" \
    "s_waitcnt lgkmcnt(4)\n\t" \
    "v_smfmac_f32_16x16x64_bf16 v[242:245], v[238:241], v[222:229], v212\n\t" \
    "ds_read_b128 v[222:225], v254 offset:22528\n\t" \
    "ds_read_b128 v[226:229], v254 offset:23552\n\t" \
    "v_smfmac_f32_16x16x64_bf16 v[246:249], v[238:241], v[230:237], v212\n\t" \
    "ds_read_b128 v[230:233], v254 offset:28672\n\t" \
    "ds_read_b128 v[234:237], v254 offset:29696\n\t" \
    "s_waitcnt lgkmcnt(6)\n\t" \
    "v_smfmac_f32_16x16x64_bf16 v[250:253], v[208:211], v[200:207], v212\n\t" \
    "ds_read_b128 v[208:211], v254 offset:2048\n\t" \
    "ds_read_b128 v[200:203], v213 offset:5120\n\t" \
    "ds_read_b128 v[204:207], v213 offset:5248\n\t" \
    "s_waitcnt lgkmcnt(7)\n\t" \
    "v_smfmac_f32_16x16x64_bf16 v[250:253], v[238:241], v[214:221], v212\n\t" \
    "v_lshrrev_b32 v238, 3, v254\n\t" \
    "v_and_b32 v238, 0x6e, v238\n\t" \
    "v_lshrrev_b32 v239, 16, v255\n\t" \
    "s_waitcnt lgkmcnt(2)\n\t" \
    "v_smfmac_f32_16x16x64_bf16 v[242:245], v[208:211], v[222:229], v212\n\t" \
    "v_smfmac_f32_16x16x64_bf16 v[246:249], v[208:211], v[230:237], v212\n\t" \
    "s_waitcnt lgkmcnt(0)\n\t" \
    "v_smfmac_f32_16x16x64_bf16 v[250:253], v[208:211], v[200:207], v212\n\t" \
    "s_nop 1\n\t" \
    "v_add_f32 v242, v242, v244\n\t" \
    "v_add_f32 v243, v243, v245\n\t" \
    "s_nop 0\n\t" \
    "v_add_f32 v246, v246, v248\n\t" \
    "v_add_f32 v247, v247, v249\n\t" \
    "s_nop 0\n\t" \
    "v_add_f32 v250, v250, v252\n\t" \
    "v_add_f32 v251, v251, v253\n\t" \
    "v_max3_f32 v244, v242, v246, v250\n\t" \
    "v_max3_f32 v245, v243, v247, v251\n\t" \
    "s_nop 0\n\t" \
    "v_max_f32_dpp v244, v244, v244 quad_perm:[1,0,3,2] row_mask:0xf bank_mask:0xf\n\t" \
    "v_max_f32_dpp v245, v245, v245 quad_perm:[1,0,3,2] row_mask:0xf bank_mask:0xf\n\t" \
    "s_nop 0\n\t" \
    "v_max_f32_dpp v244, v244, v244 quad_perm:[2,3,0,1] row_mask:0xf bank_mask:0xf\n\t" \
    "v_max_f32_dpp v245, v245, v245 quad_perm:[2,3,0,1] row_mask:0xf bank_mask:0xf\n\t" \
    "s_nop 0\n\t" \
    "v_max_f32_dpp v244, v244, v244 row_half_mirror row_mask:0xf bank_mask:0xf\n\t" \
    "v_max_f32_dpp v245, v245, v245 row_half_mirror row_mask:0xf bank_mask:0xf\n\t" \
    "s_nop 0\n\t" \
    "v_max_f32_dpp v244, v244, v244 row_mirror row_mask:0xf bank_mask:0xf\n\t" \
    "v_max_f32_dpp v245, v245, v245 row_mirror row_mask:0xf bank_mask:0xf\n\t" \
    "v_mul_f32 v244, %[scl], v244\n\t" \
    "v_mul_f32 v245, %[scl], v245\n\t" \
    "v_fma_f32 v242, v242, %[scl], -v244\n\t" \
    "v_fma_f32 v243, v243, %[scl], -v245\n\t" \
    "v_fma_f32 v246, v246, %[scl], -v244\n\t" \
    "v_fma_f32 v247, v247, %[scl], -v245\n\t" \
    "v_exp_f32 v242, v242\n\t" \
    "v_exp_f32 v243, v243\n\t" \
    "v_fma_f32 v250, v250, %[scl], -v244\n\t" \
    "v_fma_f32 v251, v251, %[scl], -v245\n\t" \
    "v_exp_f32 v246, v246\n\t" \
    "v_exp_f32 v247, v247\n\t" \
    "v_exp_f32 v250, v250\n\t" \
    "v_exp_f32 v251, v251\n\t" \
    "v_add_f32 v244, v242, v246\n\t" \
    "v_add_f32 v245, v243, v247\n\t" \
    "v_fma_f32 v244, v250, 0.5, v244\n\t" \
    "v_fma_f32 v245, v251, 0.5, v245\n\t" \
    "s_nop 0\n\t" \
    "v_add_f32_dpp v244, v244, v244 quad_perm:[1,0,3,2] row_mask:0xf bank_mask:0xf\n\t" \
    "v_add_f32_dpp v245, v245, v245 quad_perm:[1,0,3,2] row_mask:0xf bank_mask:0xf\n\t" \
    "s_nop 0\n\t" \
    "v_add_f32_dpp v244, v244, v244 quad_perm:[2,3,0,1] row_mask:0xf bank_mask:0xf\n\t" \
    "v_add_f32_dpp v245, v245, v245 quad_perm:[2,3,0,1] row_mask:0xf bank_mask:0xf\n\t" \
    "s_nop 0\n\t" \
    "v_add_f32_dpp v244, v244, v244 row_half_mirror row_mask:0xf bank_mask:0xf\n\t" \
    "v_add_f32_dpp v245, v245, v245 row_half_mirror row_mask:0xf bank_mask:0xf\n\t" \
    "s_nop 0\n\t" \
    "v_add_f32_dpp v244, v244, v244 row_mirror row_mask:0xf bank_mask:0xf\n\t" \
    "v_add_f32_dpp v245, v245, v245 row_mirror row_mask:0xf bank_mask:0xf\n\t" \
    "v_rcp_f32 v244, v244\n\t" \
    "v_rcp_f32 v245, v245\n\t" \
    "s_wakeup\n\t" \
    "v_mul_f32 v250, v250, v244\n\t" \
    "v_mul_f32 v251, v251, v245\n\t" \
    "v_mul_f32 v242, v242, v244\n\t" \
    "v_mul_f32 v243, v243, v245\n\t" \
    "v_mul_f32 v246, v246, v244\n\t" \
    "v_mul_f32 v247, v247, v245\n\t" \
    "v_cvt_pk_bf16_f32 v250, |v250|, |v251|\n\t" \
    "ds_write_b16 v238, v250 offset:52096\n\t" \
    "ds_write_b16_d16_hi v238, v250 offset:52112\n\t" \
    "v_cvt_pk_bf16_f32 v242, |v242|, |v246|\n\t" \
    "v_cvt_pk_bf16_f32 v243, |v243|, |v247|\n\t" \
    "ds_write2_b32 v239, v242, v243 offset1:4\n\t" \
    "ds_read_b128 v[200:203], v254 offset:896\n\t" \
    "v_and_b32 v253, 0x3ff, v254\n\t" \
    "s_mov_b32 exec_hi, 0xff\n\t" \
    "s_waitcnt lgkmcnt(0)\n\t" \
    "buffer_store_dwordx4 v[200:203], v254, s[60:63], s50 offen sc1\n\t" \
    "buffer_store_dwordx4 v[200:203], v254, s[60:63], s51 offen sc1\n\t" \
    "buffer_store_dwordx4 v[200:203], v254, s[60:63], s52 offen sc1\n\t" \
    "buffer_store_dwordx4 v[200:203], v254, s[60:63], s53 offen sc1\n\t" \
    "s_mov_b32 exec_hi, -1\n\t" \
    "s_setprio 0\n\t" \
    "ds_read_b128 v[224:227], v253 offset:0\n\t" \
    "ds_read_b128 v[228:231], v253 offset:1024\n\t" \
    "ds_read_b128 v[232:235], v253 offset:20480\n\t" \
    "ds_read_b128 v[236:239], v253 offset:21504\n\t" \
    "s_add_u32 s40, s40, 1\n\t" \
    "s_cmp_lg_u32 s40, 64\n\t" \
    "s_cbranch_scc1 .Ls03_%=\n\t" \
    "s_branch .Lend%=\n\t" \
    ".Lend%=:\n\t" \
    "s_waitcnt vmcnt(0) lgkmcnt(0)\n\t" \
    "s_mov_b32 %[bud], s41\n\t"
#define ASM_V_LOOP \
    "s_mov_b64 s[60:61], %[ws]\n\t" \
    "s_mov_b32 s62, %[sz]\n\t" \
    "s_mov_b32 s63, 0x20000\n\t" \
    "s_mov_b32 s40, 0\n\t" \
    "s_mov_b32 s41, 0x20000\n\t" \
    "s_cmp_eq_u32 %[wave], 0\n\t" \
    "s_cbranch_scc1 .Lv0_%=\n\t" \
    "s_cmp_eq_u32 %[wave], 1\n\t" \
    "s_cbranch_scc1 .Lv1_%=\n\t" \
    "s_cmp_eq_u32 %[wave], 2\n\t" \
    "s_cbranch_scc1 .Lv2_%=\n\t" \
    "s_cmp_eq_u32 %[wave], 3\n\t" \
    "s_cbranch_scc1 .Lv3_%=\n\t" \
    ".Lv4_%=:\n\t" \
    "s_barrier \n\t" \
    "s_waitcnt vmcnt(15)\n\t" \
    "ds_read_b64_tr_b16 v[200:201], v245 offset:0\n\t" \
    "ds_read_b64_tr_b16 v[16:17], v245 offset:32\n\t" \
    "ds_read_b64_tr_b16 v[208:209], v245 offset:64\n\t" \
    "ds_read_b64_tr_b16 v[56:57], v245 offset:96\n\t" \
    "ds_read_b64_tr_b16 v[216:217], v245 offset:128\n\t" \
    "ds_read_b64_tr_b16 v[96:97], v245 offset:160\n\t" \
    "ds_read_b64_tr_b16 v[224:225], v245 offset:192\n\t" \
    "ds_read_b64_tr_b16 v[136:137], v245 offset:224\n\t" \
    "ds_read_b64_tr_b16 v[232:233], v245 offset:256\n\t" \
    "ds_read_b64_tr_b16 v[176:177], v245 offset:288\n\t" \
    "s_waitcnt lgkmcnt(0)\n\t" \
    "s_add_u32 m0, %[wm], 0x0\n\t" \
    "s_mov_b32 s44, 0x14000\n\t" \
    "buffer_load_dwordx4 v240, %[wr], s44 offen offset:0 nt lds\n\t" \
    "buffer_load_dwordx4 v241, %[wr], s44 offen offset:1024 nt lds\n\t" \
    "buffer_load_dwordx4 v242, %[wr], s44 offen offset:2048 nt lds\n\t" \
    "buffer_load_dwordx4 v243, %[wr], s44 offen offset:3072 nt lds\n\t" \
    "s_add_u32 m0, %[wm], 0x1000\n\t" \
    "s_nop 0\n\t" \
    "buffer_load_dwordx4 v244, %[wr], s44 offen nt lds\n\t" \
    "s_barrier \n\t" \
    "s_barrier \n\t" \
    "s_waitcnt vmcnt(15)\n\t" \
    "ds_read_b64_tr_b16 v[204:205], v245 offset:5120\n\t" \
    "ds_read_b64_tr_b16 v[20:21], v245 offset:5152\n\t" \
    "ds_read_b64_tr_b16 v[212:213], v245 offset:5184\n\t" \
    "ds_read_b64_tr_b16 v[60:61], v245 offset:5216\n\t" \
    "ds_read_b64_tr_b16 v[220:221], v245 offset:5248\n\t" \
    "ds_read_b64_tr_b16 v[100:101], v245 offset:5280\n\t" \
    "ds_read_b64_tr_b16 v[228:229], v245 offset:5312\n\t" \
    "ds_read_b64_tr_b16 v[140:141], v245 offset:5344\n\t" \
    "ds_read_b64_tr_b16 v[236:237], v245 offset:5376\n\t" \
    "ds_read_b64_tr_b16 v[180:181], v245 offset:5408\n\t" \
    "s_waitcnt lgkmcnt(0)\n\t" \
    "s_add_u32 m0, %[wm], 0x1400\n\t" \
    "s_mov_b32 s44, 0x19000\n\t" \
    "buffer_load_dwordx4 v240, %[wr], s44 offen offset:0 nt lds\n\t" \
    "buffer_load_dwordx4 v241, %[wr], s44 offen offset:1024 nt lds\n\t" \
    "buffer_load_dwordx4 v242, %[wr], s44 offen offset:2048 nt lds\n\t" \
    "buffer_load_dwordx4 v243, %[wr], s44 offen offset:3072 nt lds\n\t" \
    "s_add_u32 m0, %[wm], 0x2400\n\t" \
    "s_nop 0\n\t" \
    "buffer_load_dwordx4 v244, %[wr], s44 offen nt lds\n\t" \
    "s_barrier \n\t" \
    "s_barrier \n\t" \
    "s_waitcnt vmcnt(15)\n\t" \
    "ds_read_b64_tr_b16 v[0:1], v245 offset:10240\n\t" \
    "ds_read_b64_tr_b16 v[24:25], v245 offset:10272\n\t" \
    "ds_read_b64_tr_b16 v[40:41], v245 offset:10304\n\t" \
    "ds_read_b64_tr_b16 v[64:65], v245 offset:10336\n\t" \
    "ds_read_b64_tr_b16 v[80:81], v245 offset:10368\n\t" \
    "ds_read_b64_tr_b16 v[104:105], v245 offset:10400\n\t" \
    "ds_read_b64_tr_b16 v[120:121], v245 offset:10432\n\t" \
    "ds_read_b64_tr_b16 v[144:145], v245 offset:10464\n\t" \
    "ds_read_b64_tr_b16 v[160:161], v245 offset:10496\n\t" \
    "ds_read_b64_tr_b16 v[184:185], v245 offset:10528\n\t" \
    "s_waitcnt lgkmcnt(0)\n\t" \
    "s_add_u32 m0, %[wm], 0x2800\n\t" \
    "s_mov_b32 s44, 0x1e000\n\t" \
    "buffer_load_dwordx4 v240, %[wr], s44 offen offset:0 nt lds\n\t" \
    "buffer_load_dwordx4 v241, %[wr], s44 offen offset:1024 nt lds\n\t" \
    "buffer_load_dwordx4 v242, %[wr], s44 offen offset:2048 nt lds\n\t" \
    "buffer_load_dwordx4 v243, %[wr], s44 offen offset:3072 nt lds\n\t" \
    "s_add_u32 m0, %[wm], 0x3800\n\t" \
    "s_nop 0\n\t" \
    "buffer_load_dwordx4 v244, %[wr], s44 offen nt lds\n\t" \
    "s_barrier \n\t" \
    "s_waitcnt vmcnt(15)\n\t" \
    "ds_read_b64_tr_b16 v[4:5], v245 offset:15360\n\t" \
    "ds_read_b64_tr_b16 v[28:29], v245 offset:15392\n\t" \
    "ds_read_b64_tr_b16 v[44:45], v245 offset:15424\n\t" \
    "ds_read_b64_tr_b16 v[68:69], v245 offset:15456\n\t" \
    "ds_read_b64_tr_b16 v[84:85], v245 offset:15488\n\t" \
    "ds_read_b64_tr_b16 v[108:109], v245 offset:15520\n\t" \
    "ds_read_b64_tr_b16 v[124:125], v245 offset:15552\n\t" \
    "ds_read_b64_tr_b16 v[148:149], v245 offset:15584\n\t" \
    "ds_read_b64_tr_b16 v[164:165], v245 offset:15616\n\t" \
    "ds_read_b64_tr_b16 v[188:189], v245 offset:15648\n\t" \
    "s_waitcnt lgkmcnt(0)\n\t" \
    "s_add_u32 m0, %[wm], 0x3c00\n\t" \
    "s_mov_b32 s44, 0x23000\n\t" \
    "buffer_load_dwordx4 v240, %[wr], s44 offen offset:0 nt lds\n\t" \
    "buffer_load_dwordx4 v241, %[wr], s44 offen offset:1024 nt lds\n\t" \
    "buffer_load_dwordx4 v242, %[wr], s44 offen offset:2048 nt lds\n\t" \
    "buffer_load_dwordx4 v243, %[wr], s44 offen offset:3072 nt lds\n\t" \
    "s_add_u32 m0, %[wm], 0x4c00\n\t" \
    "s_nop 0\n\t" \
    "buffer_load_dwordx4 v244, %[wr], s44 offen nt lds\n\t" \
    "s_barrier \n\t" \
    "s_barrier \n\t" \
    "s_waitcnt vmcnt(15)\n\t" \
    "ds_read_b64_tr_b16 v[8:9], v245 offset:0\n\t" \
    "ds_read_b64_tr_b16 v[32:33], v245 offset:32\n\t" \
    "ds_read_b64_tr_b16 v[48:49], v245 offset:64\n\t" \
    "ds_read_b64_tr_b16 v[72:73], v245 offset:96\n\t" \
    "ds_read_b64_tr_b16 v[88:89], v245 offset:128\n\t" \
    "ds_read_b64_tr_b16 v[112:113], v245 offset:160\n\t" \
    "ds_read_b64_tr_b16 v[128:129], v245 offset:192\n\t" \
    "ds_read_b64_tr_b16 v[152:153], v245 offset:224\n\t" \
    "ds_read_b64_tr_b16 v[168:169], v245 offset:256\n\t" \
    "ds_read_b64_tr_b16 v[192:193], v245 offset:288\n\t" \
    "s_waitcnt lgkmcnt(0)\n\t" \
    "s_add_u32 m0, %[wm], 0x0\n\t" \
    "s_mov_b32 s44, 0x28000\n\t" \
    "buffer_load_dwordx4 v240, %[wr], s44 offen offset:0 nt lds\n\t" \
    "buffer_load_dwordx4 v241, %[wr], s44 offen offset:1024 nt lds\n\t" \
    "buffer_load_dwordx4 v242, %[wr], s44 offen offset:2048 nt lds\n\t" \
    "buffer_load_dwordx4 v243, %[wr], s44 offen offset:3072 nt lds\n\t" \
    "s_add_u32 m0, %[wm], 0x1000\n\t" \
    "s_nop 0\n\t" \
    "buffer_load_dwordx4 v244, %[wr], s44 offen nt lds\n\t" \
    "s_barrier \n\t" \
    "s_barrier \n\t" \
    "s_waitcnt vmcnt(15)\n\t" \
    "ds_read_b64_tr_b16 v[12:13], v245 offset:5120\n\t" \
    "ds_read_b64_tr_b16 v[36:37], v245 offset:5152\n\t" \
    "ds_read_b64_tr_b16 v[52:53], v245 offset:5184\n\t" \
    "ds_read_b64_tr_b16 v[76:77], v245 offset:5216\n\t" \
    "ds_read_b64_tr_b16 v[92:93], v245 offset:5248\n\t" \
    "ds_read_b64_tr_b16 v[116:117], v245 offset:5280\n\t" \
    "ds_read_b64_tr_b16 v[132:133], v245 offset:5312\n\t" \
    "ds_read_b64_tr_b16 v[156:157], v245 offset:5344\n\t" \
    "ds_read_b64_tr_b16 v[172:173], v245 offset:5376\n\t" \
    "ds_read_b64_tr_b16 v[196:197], v245 offset:5408\n\t" \
    "s_waitcnt lgkmcnt(0)\n\t" \
    "s_add_u32 m0, %[wm], 0x1400\n\t" \
    "s_mov_b32 s44, 0x2d000\n\t" \
    "buffer_load_dwordx4 v240, %[wr], s44 offen offset:0 nt lds\n\t" \
    "buffer_load_dwordx4 v241, %[wr], s44 offen offset:1024 nt lds\n\t" \
    "buffer_load_dwordx4 v242, %[wr], s44 offen offset:2048 nt lds\n\t" \
    "buffer_load_dwordx4 v243, %[wr], s44 offen offset:3072 nt lds\n\t" \
    "s_add_u32 m0, %[wm], 0x2400\n\t" \
    "s_nop 0\n\t" \
    "buffer_load_dwordx4 v244, %[wr], s44 offen nt lds\n\t" \
    "s_barrier \n\t" \
    "s_waitcnt vmcnt(15)\n\t" \
    "ds_read_b64_tr_b16 v[202:203], v245 offset:10240\n\t" \
    "ds_read_b64_tr_b16 v[18:19], v245 offset:10272\n\t" \
    "ds_read_b64_tr_b16 v[210:211], v245 offset:10304\n\t" \
    "ds_read_b64_tr_b16 v[58:59], v245 offset:10336\n\t" \
    "ds_read_b64_tr_b16 v[218:219], v245 offset:10368\n\t" \
    "ds_read_b64_tr_b16 v[98:99], v245 offset:10400\n\t" \
    "ds_read_b64_tr_b16 v[226:227], v245 offset:10432\n\t" \
    "ds_read_b64_tr_b16 v[138:139], v245 offset:10464\n\t" \
    "ds_read_b64_tr_b16 v[234:235], v245 offset:10496\n\t" \
    "ds_read_b64_tr_b16 v[178:179], v245 offset:10528\n\t" \
    "v_mov_b32 v246, 0\n\t" \
    "buffer_load_dword v246, v246, s[60:63], %[mo] offen sc1\n\t" \
    "s_waitcnt lgkmcnt(0)\n\t" \
    "s_add_u32 m0, %[wm], 0x2800\n\t" \
    "s_mov_b32 s44, 0x32000\n\t" \
    "buffer_load_dwordx4 v240, %[wr], s44 offen offset:0 nt lds\n\t" \
    "buffer_load_dwordx4 v241, %[wr], s44 offen offset:1024 nt lds\n\t" \
    "buffer_load_dwordx4 v242, %[wr], s44 offen offset:2048 nt lds\n\t" \
    "buffer_load_dwordx4 v243, %[wr], s44 offen offset:3072 nt lds\n\t" \
    "s_add_u32 m0, %[wm], 0x3800\n\t" \
    "s_nop 0\n\t" \
    "buffer_load_dwordx4 v244, %[wr], s44 offen nt lds\n\t" \
    "s_barrier \n\t" \
    "s_barrier \n\t" \
    "s_waitcnt vmcnt(15)\n\t" \
    "ds_read_b64_tr_b16 v[206:207], v245 offset:15360\n\t" \
    "ds_read_b64_tr_b16 v[22:23], v245 offset:15392\n\t" \
    "ds_read_b64_tr_b16 v[214:215], v245 offset:15424\n\t" \
    "ds_read_b64_tr_b16 v[62:63], v245 offset:15456\n\t" \
    "ds_read_b64_tr_b16 v[222:223], v245 offset:15488\n\t" \
    "ds_read_b64_tr_b16 v[102:103], v245 offset:15520\n\t" \
    "ds_read_b64_tr_b16 v[230:231], v245 offset:15552\n\t" \
    "ds_read_b64_tr_b16 v[142:143], v245 offset:15584\n\t" \
    "ds_read_b64_tr_b16 v[238:239], v245 offset:15616\n\t" \
    "ds_read_b64_tr_b16 v[182:183], v245 offset:15648\n\t" \
    "s_waitcnt lgkmcnt(0)\n\t" \
    "s_add_u32 m0, %[wm], 0x3c00\n\t" \
    "s_mov_b32 s44, 0x37000\n\t" \
    "buffer_load_dwordx4 v240, %[wr], s44 offen offset:0 nt lds\n\t" \
    "buffer_load_dwordx4 v241, %[wr], s44 offen offset:1024 nt lds\n\t" \
    "buffer_load_dwordx4 v242, %[wr], s44 offen offset:2048 nt lds\n\t" \
    "buffer_load_dwordx4 v243, %[wr], s44 offen offset:3072 nt lds\n\t" \
    "s_add_u32 m0, %[wm], 0x4c00\n\t" \
    "s_nop 0\n\t" \
    "buffer_load_dwordx4 v244, %[wr], s44 offen nt lds\n\t" \
    "s_barrier \n\t" \
    "s_barrier \n\t" \
    "s_waitcnt vmcnt(15)\n\t" \
    "ds_read_b64_tr_b16 v[2:3], v245 offset:0\n\t" \
    "ds_read_b64_tr_b16 v[26:27], v245 offset:32\n\t" \
    "ds_read_b64_tr_b16 v[42:43], v245 offset:64\n\t" \
    "ds_read_b64_tr_b16 v[66:67], v245 offset:96\n\t" \
    "ds_read_b64_tr_b16 v[82:83], v245 offset:128\n\t" \
    "ds_read_b64_tr_b16 v[106:107], v245 offset:160\n\t" \
    "ds_read_b64_tr_b16 v[122:123], v245 offset:192\n\t" \
    "ds_read_b64_tr_b16 v[146:147], v245 offset:224\n\t" \
    "ds_read_b64_tr_b16 v[162:163], v245 offset:256\n\t" \
    "ds_read_b64_tr_b16 v[186:187], v245 offset:288\n\t" \
    "s_barrier \n\t" \
    "s_waitcnt vmcnt(10)\n\t" \
    "ds_read_b64_tr_b16 v[6:7], v245 offset:5120\n\t" \
    "ds_read_b64_tr_b16 v[30:31], v245 offset:5152\n\t" \
    "ds_read_b64_tr_b16 v[46:47], v245 offset:5184\n\t" \
    "ds_read_b64_tr_b16 v[70:71], v245 offset:5216\n\t" \
    "ds_read_b64_tr_b16 v[86:87], v245 offset:5248\n\t" \
    "ds_read_b64_tr_b16 v[110:111], v245 offset:5280\n\t" \
    "ds_read_b64_tr_b16 v[126:127], v245 offset:5312\n\t" \
    "ds_read_b64_tr_b16 v[150:151], v245 offset:5344\n\t" \
    "ds_read_b64_tr_b16 v[166:167], v245 offset:5376\n\t" \
    "ds_read_b64_tr_b16 v[190:191], v245 offset:5408\n\t" \
    "s_barrier \n\t" \
    "s_barrier \n\t" \
    "s_waitcnt vmcnt(5)\n\t" \
    "ds_read_b64_tr_b16 v[10:11], v245 offset:10240\n\t" \
    "ds_read_b64_tr_b16 v[34:35], v245 offset:10272\n\t" \
    "ds_read_b64_tr_b16 v[50:51], v245 offset:10304\n\t" \
    "ds_read_b64_tr_b16 v[74:75], v245 offset:10336\n\t" \
    "ds_read_b64_tr_b16 v[90:91], v245 offset:10368\n\t" \
    "ds_read_b64_tr_b16 v[114:115], v245 offset:10400\n\t" \
    "ds_read_b64_tr_b16 v[130:131], v245 offset:10432\n\t" \
    "ds_read_b64_tr_b16 v[154:155], v245 offset:10464\n\t" \
    "ds_read_b64_tr_b16 v[170:171], v245 offset:10496\n\t" \
    "ds_read_b64_tr_b16 v[194:195], v245 offset:10528\n\t" \
    ".Lv41_%=:\n\t" \
    "v_readfirstlane_b32 s43, v246\n\t" \
    "s_nop 0\n\t" \
    "s_cmp_lg_u32 s43, -1\n\t" \
    "s_cbranch_scc1 .Lv42_%=\n\t" \
    "s_cmp_eq_u32 s41, 0\n\t" \
    "s_cbranch_scc1 .Lv42_%=\n\t" \
    "s_sub_u32 s41, s41, 1\n\t" \
    "s_sleep 4\n\t" \
    "v_mov_b32 v246, 0\n\t" \
    "buffer_load_dword v246, v246, s[60:63], %[mo] offen sc1\n\t" \
    "s_waitcnt vmcnt(0)\n\t" \
    "s_branch .Lv41_%=\n\t" \
    ".Lv42_%=:\n\t" \
    "v_mov_b32 v246, 0x8000\n\t" \
    "s_mov_b64 exec, 1\n\t" \
    "buffer_store_dword v246, v246, s[60:63], %[mo] offen sc1\n\t" \
    "s_mov_b64 exec, -1\n\t" \
    "s_and_b32 s43, s43, 0x2000\n\t" \
    "s_xor_b32 s43, s43, %[lx]\n\t" \
    "s_add_u32 s60, s60, s43\n\t" \
    "s_addc_u32 s61, s61, 0\n\t" \
    "s_barrier \n\t" \
    "s_barrier \n\t" \
    "s_waitcnt vmcnt(0)\n\t" \
    "ds_read_b64_tr_b16 v[14:15], v245 offset:15360\n\t" \
    "ds_read_b64_tr_b16 v[38:39], v245 offset:15392\n\t" \
    "ds_read_b64_tr_b16 v[54:55], v245 offset:15424\n\t" \
    "ds_read_b64_tr_b16 v[78:79], v245 offset:15456\n\t" \
    "ds_read_b64_tr_b16 v[94:95], v245 offset:15488\n\t" \
    "ds_read_b64_tr_b16 v[118:119], v245 offset:15520\n\t" \
    "ds_read_b64_tr_b16 v[134:135], v245 offset:15552\n\t" \
    "ds_read_b64_tr_b16 v[158:159], v245 offset:15584\n\t" \
    "ds_read_b64_tr_b16 v[174:175], v245 offset:15616\n\t" \
    "ds_read_b64_tr_b16 v[198:199], v245 offset:15648\n\t" \
    "s_waitcnt lgkmcnt(0)\n\t" \
    "s_barrier \n\t" \
    "ds_write_b128 v254, v[200:203] offset:0\n\t" \
    "ds_write_b128 v254, v[204:207] offset:1024\n\t" \
    "ds_write_b128 v254, v[208:211] offset:2048\n\t" \
    "ds_write_b128 v254, v[212:215] offset:3072\n\t" \
    "ds_write_b128 v254, v[216:219] offset:4096\n\t" \
    "ds_write_b128 v254, v[220:223] offset:5120\n\t" \
    "ds_write_b128 v254, v[224:227] offset:6144\n\t" \
    "ds_write_b128 v254, v[228:231] offset:7168\n\t" \
    "ds_write_b128 v254, v[232:235] offset:8192\n\t" \
    "ds_write_b128 v254, v[236:239] offset:9216\n\t" \
    "s_waitcnt vmcnt(0) lgkmcnt(0)\n\t" \
    "s_barrier \n\t" \
    "v_mov_b64 v[200:201], 0\n\t" \
    "v_mov_b64 v[202:203], 0\n\t" \
    "ds_write_b128 v253, v[200:203] offset:3072\n\t" \
    "ds_write_b128 v253, v[200:203] offset:4096\n\t" \
    "v_bfe_i32 v252, v253, 5, 1\n\t" \
    "v_and_b32 v252, 0xaaaa, v252\n\t" \
    "v_or_b32 v252, 0x4444, v252\n\t" \
    ".Lv43_%=:\n\t" \
    "ds_read_b128 v[248:251], v254 offset:0\n\t" \
    "s_and_b32 s44, s40, %[vm]\n\t" \
    "s_lshl_b32 s44, s44, 20\n\t" \
    "s_and_b32 s45, s40, 2\n\t" \
    "s_lshl_b32 s45, s45, 12\n\t" \
    "s_add_u32 s44, s44, s45\n\t" \
    "s_add_u32 s42, s44, %[vx]\n\t" \
    "s_waitcnt lgkmcnt(0)\n\t" \
    "s_barrier \n\t" \
    "ds_read_b128 v[224:227], v253 offset:6144\n\t" \
    "ds_read_b128 v[232:235], v253 offset:8192\n\t" \
    "ds_read_b128 v[236:239], v253 offset:9216\n\t" \
    "ds_read_b128 v[240:243], v253 offset:10240\n\t" \
    "ds_read_b128 v[244:247], v253 offset:11264\n\t" \
    "ds_read_b128 v[228:231], v253 offset:7168\n\t" \
    "s_waitcnt lgkmcnt(5)\n\t" \
    "v_mfma_f32_16x16x32_bf16 v[200:203], v[224:227], v[16:19], 0\n\t" \
    "s_waitcnt lgkmcnt(4)\n\t" \
    "v_mfma_f32_16x16x32_bf16 v[200:203], v[232:235], v[24:27], v[200:203]\n\t" \
    "s_waitcnt lgkmcnt(3)\n\t" \
    "v_mfma_f32_16x16x32_bf16 v[200:203], v[236:239], v[28:31], v[200:203]\n\t" \
    "s_waitcnt lgkmcnt(2)\n\t" \
    "v_mfma_f32_16x16x32_bf16 v[200:203], v[240:243], v[32:35], v[200:203]\n\t" \
    "s_waitcnt lgkmcnt(1)\n\t" \
    "v_mfma_f32_16x16x32_bf16 v[200:203], v[244:247], v[36:39], v[200:203]\n\t" \
    "s_waitcnt lgkmcnt(0)\n\t" \
    "v_mfma_f32_16x16x32_bf16 v[200:203], v[228:231], v[20:23], v[200:203]\n\t" \
    "v_mfma_f32_16x16x32_bf16 v[204:207], v[224:227], v[248:251], 0\n\t" \
    "ds_read_b128 v[248:251], v254 offset:1024\n\t" \
    "v_mfma_f32_16x16x32_bf16 v[204:207], v[232:235], v[0:3], v[204:207]\n\t" \
    "v_mfma_f32_16x16x32_bf16 v[204:207], v[236:239], v[4:7], v[204:207]\n\t" \
    "v_mfma_f32_16x16x32_bf16 v[204:207], v[240:243], v[8:11], v[204:207]\n\t" \
    "v_mfma_f32_16x16x32_bf16 v[204:207], v[244:247], v[12:15], v[204:207]\n\t" \
    "s_waitcnt lgkmcnt(0)\n\t" \
    "v_mfma_f32_16x16x32_bf16 v[204:207], v[228:231], v[248:251], v[204:207]\n\t" \
    "ds_read_b128 v[248:251], v254 offset:2048\n\t" \
    "s_barrier \n\t" \
    "v_mfma_f32_16x16x32_bf16 v[208:211], v[224:227], v[56:59], 0\n\t" \
    "v_mfma_f32_16x16x32_bf16 v[208:211], v[232:235], v[64:67], v[208:211]\n\t" \
    "v_mfma_f32_16x16x32_bf16 v[208:211], v[236:239], v[68:71], v[208:211]\n\t" \
    "v_mfma_f32_16x16x32_bf16 v[208:211], v[240:243], v[72:75], v[208:211]\n\t" \
    "v_mfma_f32_16x16x32_bf16 v[208:211], v[244:247], v[76:79], v[208:211]\n\t" \
    "v_mfma_f32_16x16x32_bf16 v[208:211], v[228:231], v[60:63], v[208:211]\n\t" \
    "s_waitcnt lgkmcnt(0)\n\t" \
    "v_mfma_f32_16x16x32_bf16 v[212:215], v[224:227], v[248:251], 0\n\t" \
    "ds_read_b128 v[248:251], v254 offset:3072\n\t" \
    "ds_read_b128 v[224:227], v253 offset:12288\n\t" \
    "v_mfma_f32_16x16x32_bf16 v[212:215], v[232:235], v[40:43], v[212:215]\n\t" \
    "ds_read_b128 v[232:235], v253 offset:14336\n\t" \
    "v_mfma_f32_16x16x32_bf16 v[212:215], v[236:239], v[44:47], v[212:215]\n\t" \
    "ds_read_b128 v[236:239], v253 offset:15360\n\t" \
    "v_mfma_f32_16x16x32_bf16 v[212:215], v[240:243], v[48:51], v[212:215]\n\t" \
    "ds_read_b128 v[240:243], v253 offset:16384\n\t" \
    "v_mfma_f32_16x16x32_bf16 v[212:215], v[244:247], v[52:55], v[212:215]\n\t" \
    "ds_read_b128 v[244:247], v253 offset:17408\n\t" \
    "s_waitcnt lgkmcnt(5)\n\t" \
    "v_mfma_f32_16x16x32_bf16 v[212:215], v[228:231], v[248:251], v[212:215]\n\t" \
    "ds_read_b128 v[248:251], v254 offset:4096\n\t" \
    "ds_read_b128 v[228:231], v253 offset:13312\n\t" \
    "s_waitcnt lgkmcnt(6)\n\t" \
    "v_mfma_f32_16x16x32_bf16 v[216:219], v[224:227], v[16:19], 0\n\t" \
    "s_waitcnt lgkmcnt(5)\n\t" \
    "v_mfma_f32_16x16x32_bf16 v[216:219], v[232:235], v[24:27], v[216:219]\n\t" \
    "s_waitcnt lgkmcnt(4)\n\t" \
    "v_mfma_f32_16x16x32_bf16 v[216:219], v[236:239], v[28:31], v[216:219]\n\t" \
    "s_waitcnt lgkmcnt(3)\n\t" \
    "v_mfma_f32_16x16x32_bf16 v[216:219], v[240:243], v[32:35], v[216:219]\n\t" \
    "s_waitcnt lgkmcnt(2)\n\t" \
    "v_mfma_f32_16x16x32_bf16 v[216:219], v[244:247], v[36:39], v[216:219]\n\t" \
    "s_waitcnt lgkmcnt(0)\n\t" \
    "v_mfma_f32_16x16x32_bf16 v[216:219], v[228:231], v[20:23], v[216:219]\n\t" \
    "v_mfma_f32_16x16x32_bf16 v[220:223], v[224:227], v[248:251], 0\n\t" \
    "ds_read_b128 v[248:251], v254 offset:5120\n\t" \
    "v_mfma_f32_16x16x32_bf16 v[220:223], v[232:235], v[80:83], v[220:223]\n\t" \
    "v_mfma_f32_16x16x32_bf16 v[220:223], v[236:239], v[84:87], v[220:223]\n\t" \
    "v_cvt_pk_bf16_f32 v200, v200, v216\n\t" \
    "v_cvt_pk_bf16_f32 v201, v201, v217\n\t" \
    "v_mfma_f32_16x16x32_bf16 v[220:223], v[240:243], v[88:91], v[220:223]\n\t" \
    "v_cvt_pk_bf16_f32 v202, v202, v218\n\t" \
    "v_cvt_pk_bf16_f32 v203, v203, v219\n\t" \
    "v_mfma_f32_16x16x32_bf16 v[220:223], v[244:247], v[92:95], v[220:223]\n\t" \
    "s_add_u32 s44, s42, 0x10000\n\t" \
    "s_waitcnt lgkmcnt(0)\n\t" \
    "v_mfma_f32_16x16x32_bf16 v[220:223], v[228:231], v[248:251], v[220:223]\n\t" \
    "ds_read_b128 v[248:251], v254 offset:0\n\t" \
    "buffer_store_dwordx4 v[200:203], v253, s[60:63], s44 offen sc1\n\t" \
    "v_mfma_f32_16x16x32_bf16 v[216:219], v[224:227], v[56:59], 0\n\t" \
    "v_mfma_f32_16x16x32_bf16 v[216:219], v[232:235], v[64:67], v[216:219]\n\t" \
    "v_mfma_f32_16x16x32_bf16 v[216:219], v[236:239], v[68:71], v[216:219]\n\t" \
    "v_mfma_f32_16x16x32_bf16 v[216:219], v[240:243], v[72:75], v[216:219]\n\t" \
    "v_mfma_f32_16x16x32_bf16 v[216:219], v[244:247], v[76:79], v[216:219]\n\t" \
    "v_mfma_f32_16x16x32_bf16 v[216:219], v[228:231], v[60:63], v[216:219]\n\t" \
    "s_waitcnt lgkmcnt(0)\n\t" \
    "v_mfma_f32_16x16x32_bf16 v[200:203], v[224:227], v[248:251], 0\n\t" \
    "ds_read_b128 v[248:251], v254 offset:1024\n\t" \
    "v_mfma_f32_16x16x32_bf16 v[200:203], v[232:235], v[0:3], v[200:203]\n\t" \
    "v_mfma_f32_16x16x32_bf16 v[200:203], v[236:239], v[4:7], v[200:203]\n\t" \
    "v_cvt_pk_bf16_f32 v208, v208, v216\n\t" \
    "v_cvt_pk_bf16_f32 v209, v209, v217\n\t" \
    "v_mfma_f32_16x16x32_bf16 v[200:203], v[240:243], v[8:11], v[200:203]\n\t" \
    "v_cvt_pk_bf16_f32 v210, v210, v218\n\t" \
    "v_cvt_pk_bf16_f32 v211, v211, v219\n\t" \
    "v_mfma_f32_16x16x32_bf16 v[200:203], v[244:247], v[12:15], v[200:203]\n\t" \
    "s_add_u32 s44, s42, 0x30000\n\t" \
    "s_waitcnt lgkmcnt(0)\n\t" \
    "v_mfma_f32_16x16x32_bf16 v[200:203], v[228:231], v[248:251], v[200:203]\n\t" \
    "ds_read_b128 v[248:251], v254 offset:2048\n\t" \
    "buffer_store_dwordx4 v[208:211], v253, s[60:63], s44 offen sc1\n\t" \
    "v_mfma_f32_16x16x32_bf16 v[216:219], v[224:227], v[96:99], 0\n\t" \
    "v_mfma_f32_16x16x32_bf16 v[216:219], v[232:235], v[104:107], v[216:219]\n\t" \
    "v_mfma_f32_16x16x32_bf16 v[216:219], v[236:239], v[108:111], v[216:219]\n\t" \
    "v_cvt_pk_bf16_f32 v204, v204, v200\n\t" \
    "v_mfma_f32_16x16x32_bf16 v[216:219], v[240:243], v[112:115], v[216:219]\n\t" \
    "v_cvt_pk_bf16_f32 v205, v205, v201\n\t" \
    "v_cvt_pk_bf16_f32 v206, v206, v202\n\t" \
    "v_mfma_f32_16x16x32_bf16 v[216:219], v[244:247], v[116:119], v[216:219]\n\t" \
    "v_cvt_pk_bf16_f32 v207, v207, v203\n\t" \
    "v_mfma_f32_16x16x32_bf16 v[216:219], v[228:231], v[100:103], v[216:219]\n\t" \
    "s_add_u32 s44, s42, 0x0\n\t" \
    "buffer_store_dwordx4 v[204:207], v253, s[60:63], s44 offen sc1\n\t" \
    "s_waitcnt lgkmcnt(0)\n\t" \
    "v_mfma_f32_16x16x32_bf16 v[208:211], v[224:227], v[248:251], 0\n\t" \
    "ds_read_b128 v[248:251], v254 offset:3072\n\t" \
    "v_mfma_f32_16x16x32_bf16 v[208:211], v[232:235], v[40:43], v[208:211]\n\t" \
    "v_mfma_f32_16x16x32_bf16 v[208:211], v[236:239], v[44:47], v[208:211]\n\t" \
    "v_mfma_f32_16x16x32_bf16 v[208:211], v[240:243], v[48:51], v[208:211]\n\t" \
    "v_mfma_f32_16x16x32_bf16 v[208:211], v[244:247], v[52:55], v[208:211]\n\t" \
    "s_waitcnt lgkmcnt(0)\n\t" \
    "v_mfma_f32_16x16x32_bf16 v[208:211], v[228:231], v[248:251], v[208:211]\n\t" \
    "ds_read_b128 v[248:251], v254 offset:6144\n\t" \
    "v_mfma_f32_16x16x32_bf16 v[200:203], v[224:227], v[136:139], 0\n\t" \
    "v_mfma_f32_16x16x32_bf16 v[200:203], v[232:235], v[144:147], v[200:203]\n\t" \
    "v_mfma_f32_16x16x32_bf16 v[200:203], v[236:239], v[148:151], v[200:203]\n\t" \
    "v_cvt_pk_bf16_f32 v212, v212, v208\n\t" \
    "v_cvt_pk_bf16_f32 v213, v213, v209\n\t" \
    "v_mfma_f32_16x16x32_bf16 v[200:203], v[240:243], v[152:155], v[200:203]\n\t" \
    "v_cvt_pk_bf16_f32 v214, v214, v210\n\t" \
    "v_cvt_pk_bf16_f32 v215, v215, v211\n\t" \
    "v_mfma_f32_16x16x32_bf16 v[200:203], v[244:247], v[156:159], v[200:203]\n\t" \
    "s_add_u32 s44, s42, 0x20000\n\t" \
    "v_mfma_f32_16x16x32_bf16 v[200:203], v[228:231], v[140:143], v[200:203]\n\t" \
    "buffer_store_dwordx4 v[212:215], v253, s[60:63], s44 offen sc1\n\t" \
    "s_waitcnt lgkmcnt(0)\n\t" \
    "v_mfma_f32_16x16x32_bf16 v[204:207], v[224:227], v[248:251], 0\n\t" \
    "ds_read_b128 v[248:251], v254 offset:7168\n\t" \
    "ds_read_b128 v[224:227], v253 offset:6144\n\t" \
    "v_mfma_f32_16x16x32_bf16 v[204:207], v[232:235], v[120:123], v[204:207]\n\t" \
    "ds_read_b128 v[232:235], v253 offset:8192\n\t" \
    "v_mfma_f32_16x16x32_bf16 v[204:207], v[236:239], v[124:127], v[204:207]\n\t" \
    "ds_read_b128 v[236:239], v253 offset:9216\n\t" \
    "v_mfma_f32_16x16x32_bf16 v[204:207], v[240:243], v[128:131], v[204:207]\n\t" \
    "ds_read_b128 v[240:243], v253 offset:10240\n\t" \
    "v_mfma_f32_16x16x32_bf16 v[204:207], v[244:247], v[132:135], v[204:207]\n\t" \
    "ds_read_b128 v[244:247], v253 offset:11264\n\t" \
    "s_waitcnt lgkmcnt(5)\n\t" \
    "v_mfma_f32_16x16x32_bf16 v[204:207], v[228:231], v[248:251], v[204:207]\n\t" \
    "ds_read_b128 v[248:251], v254 offset:4096\n\t" \
    "ds_read_b128 v[228:231], v253 offset:7168\n\t" \
    "s_waitcnt lgkmcnt(6)\n\t" \
    "v_mfma_f32_16x16x32_bf16 v[208:211], v[224:227], v[96:99], 0\n\t" \
    "s_waitcnt lgkmcnt(5)\n\t" \
    "v_mfma_f32_16x16x32_bf16 v[208:211], v[232:235], v[104:107], v[208:211]\n\t" \
    "s_waitcnt lgkmcnt(4)\n\t" \
    "v_mfma_f32_16x16x32_bf16 v[208:211], v[236:239], v[108:111], v[208:211]\n\t" \
    "s_waitcnt lgkmcnt(3)\n\t" \
    "v_mfma_f32_16x16x32_bf16 v[208:211], v[240:243], v[112:115], v[208:211]\n\t" \
    "s_waitcnt lgkmcnt(2)\n\t" \
    "v_mfma_f32_16x16x32_bf16 v[208:211], v[244:247], v[116:119], v[208:211]\n\t" \
    "s_waitcnt lgkmcnt(0)\n\t" \
    "v_mfma_f32_16x16x32_bf16 v[208:211], v[228:231], v[100:103], v[208:211]\n\t" \
    "v_mfma_f32_16x16x32_bf16 v[212:215], v[224:227], v[248:251], 0\n\t" \
    "ds_read_b128 v[248:251], v254 offset:5120\n\t" \
    "v_mfma_f32_16x16x32_bf16 v[212:215], v[232:235], v[80:83], v[212:215]\n\t" \
    "v_mfma_f32_16x16x32_bf16 v[212:215], v[236:239], v[84:87], v[212:215]\n\t" \
    "v_cvt_pk_bf16_f32 v216, v208, v216\n\t" \
    "v_cvt_pk_bf16_f32 v217, v209, v217\n\t" \
    "v_mfma_f32_16x16x32_bf16 v[212:215], v[240:243], v[88:91], v[212:215]\n\t" \
    "v_cvt_pk_bf16_f32 v218, v210, v218\n\t" \
    "v_cvt_pk_bf16_f32 v219, v211, v219\n\t" \
    "v_mfma_f32_16x16x32_bf16 v[212:215], v[244:247], v[92:95], v[212:215]\n\t" \
    "s_add_u32 s44, s42, 0x50000\n\t" \
    "s_waitcnt lgkmcnt(0)\n\t" \
    "v_mfma_f32_16x16x32_bf16 v[212:215], v[228:231], v[248:251], v[212:215]\n\t" \
    "ds_read_b128 v[248:251], v254 offset:6144\n\t" \
    "buffer_store_dwordx4 v[216:219], v253, s[60:63], s44 offen sc1\n\t" \
    "v_mfma_f32_16x16x32_bf16 v[208:211], v[224:227], v[176:179], 0\n\t" \
    "v_mfma_f32_16x16x32_bf16 v[208:211], v[232:235], v[184:187], v[208:211]\n\t" \
    "v_mfma_f32_16x16x32_bf16 v[208:211], v[236:239], v[188:191], v[208:211]\n\t" \
    "v_cvt_pk_bf16_f32 v220, v212, v220\n\t" \
    "v_mfma_f32_16x16x32_bf16 v[208:211], v[240:243], v[192:195], v[208:211]\n\t" \
    "v_cvt_pk_bf16_f32 v221, v213, v221\n\t" \
    "v_cvt_pk_bf16_f32 v222, v214, v222\n\t" \
    "v_mfma_f32_16x16x32_bf16 v[208:211], v[244:247], v[196:199], v[208:211]\n\t" \
    "v_cvt_pk_bf16_f32 v223, v215, v223\n\t" \
    "v_mfma_f32_16x16x32_bf16 v[208:211], v[228:231], v[180:183], v[208:211]\n\t" \
    "s_add_u32 s44, s42, 0x40000\n\t" \
    "buffer_store_dwordx4 v[220:223], v253, s[60:63], s44 offen sc1\n\t" \
    "s_waitcnt lgkmcnt(0)\n\t" \
    "v_mfma_f32_16x16x32_bf16 v[216:219], v[224:227], v[248:251], 0\n\t" \
    "ds_read_b128 v[248:251], v254 offset:7168\n\t" \
    "v_mfma_f32_16x16x32_bf16 v[216:219], v[232:235], v[120:123], v[216:219]\n\t" \
    "v_mfma_f32_16x16x32_bf16 v[216:219], v[236:239], v[124:127], v[216:219]\n\t" \
    "v_mfma_f32_16x16x32_bf16 v[216:219], v[240:243], v[128:131], v[216:219]\n\t" \
    "v_mfma_f32_16x16x32_bf16 v[216:219], v[244:247], v[132:135], v[216:219]\n\t" \
    "s_waitcnt lgkmcnt(0)\n\t" \
    "v_mfma_f32_16x16x32_bf16 v[216:219], v[228:231], v[248:251], v[216:219]\n\t" \
    "ds_read_b128 v[248:251], v254 offset:8192\n\t" \
    "v_mfma_f32_16x16x32_bf16 v[212:215], v[224:227], v[136:139], 0\n\t" \
    "v_mfma_f32_16x16x32_bf16 v[212:215], v[232:235], v[144:147], v[212:215]\n\t" \
    "v_mfma_f32_16x16x32_bf16 v[212:215], v[236:239], v[148:151], v[212:215]\n\t" \
    "v_cvt_pk_bf16_f32 v204, v216, v204\n\t" \
    "v_cvt_pk_bf16_f32 v205, v217, v205\n\t" \
    "v_mfma_f32_16x16x32_bf16 v[212:215], v[240:243], v[152:155], v[212:215]\n\t" \
    "v_cvt_pk_bf16_f32 v206, v218, v206\n\t" \
    "v_cvt_pk_bf16_f32 v207, v219, v207\n\t" \
    "v_mfma_f32_16x16x32_bf16 v[212:215], v[244:247], v[156:159], v[212:215]\n\t" \
    "s_add_u32 s44, s42, 0x60000\n\t" \
    "v_mfma_f32_16x16x32_bf16 v[212:215], v[228:231], v[140:143], v[212:215]\n\t" \
    "buffer_store_dwordx4 v[204:207], v253, s[60:63], s44 offen sc1\n\t" \
    "s_waitcnt lgkmcnt(0)\n\t" \
    "v_mfma_f32_16x16x32_bf16 v[220:223], v[224:227], v[248:251], 0\n\t" \
    "ds_read_b128 v[248:251], v254 offset:9216\n\t" \
    "ds_read_b128 v[224:227], v253 offset:12288\n\t" \
    "v_mfma_f32_16x16x32_bf16 v[220:223], v[232:235], v[160:163], v[220:223]\n\t" \
    "ds_read_b128 v[232:235], v253 offset:14336\n\t" \
    "v_mfma_f32_16x16x32_bf16 v[220:223], v[236:239], v[164:167], v[220:223]\n\t" \
    "ds_read_b128 v[236:239], v253 offset:15360\n\t" \
    "v_cvt_pk_bf16_f32 v200, v212, v200\n\t" \
    "v_mfma_f32_16x16x32_bf16 v[220:223], v[240:243], v[168:171], v[220:223]\n\t" \
    "ds_read_b128 v[240:243], v253 offset:16384\n\t" \
    "v_cvt_pk_bf16_f32 v201, v213, v201\n\t" \
    "v_cvt_pk_bf16_f32 v202, v214, v202\n\t" \
    "v_mfma_f32_16x16x32_bf16 v[220:223], v[244:247], v[172:175], v[220:223]\n\t" \
    "ds_read_b128 v[244:247], v253 offset:17408\n\t" \
    "v_cvt_pk_bf16_f32 v203, v215, v203\n\t" \
    "s_waitcnt lgkmcnt(5)\n\t" \
    "v_mfma_f32_16x16x32_bf16 v[220:223], v[228:231], v[248:251], v[220:223]\n\t" \
    "ds_read_b128 v[248:251], v254 offset:8192\n\t" \
    "ds_read_b128 v[228:231], v253 offset:13312\n\t" \
    "s_add_u32 s44, s42, 0x70000\n\t" \
    "buffer_store_dwordx4 v[200:203], v253, s[60:63], s44 offen sc1\n\t" \
    "s_waitcnt lgkmcnt(6)\n\t" \
    "v_mfma_f32_16x16x32_bf16 v[216:219], v[224:227], v[176:179], 0\n\t" \
    "s_waitcnt lgkmcnt(5)\n\t" \
    "v_mfma_f32_16x16x32_bf16 v[216:219], v[232:235], v[184:187], v[216:219]\n\t" \
    "s_waitcnt lgkmcnt(4)\n\t" \
    "v_mfma_f32_16x16x32_bf16 v[216:219], v[236:239], v[188:191], v[216:219]\n\t" \
    "s_waitcnt lgkmcnt(3)\n\t" \
    "v_mfma_f32_16x16x32_bf16 v[216:219], v[240:243], v[192:195], v[216:219]\n\t" \
    "s_waitcnt lgkmcnt(2)\n\t" \
    "v_mfma_f32_16x16x32_bf16 v[216:219], v[244:247], v[196:199], v[216:219]\n\t" \
    "s_waitcnt lgkmcnt(0)\n\t" \
    "v_mfma_f32_16x16x32_bf16 v[216:219], v[228:231], v[180:183], v[216:219]\n\t" \
    "v_mfma_f32_16x16x32_bf16 v[204:207], v[224:227], v[248:251], 0\n\t" \
    "ds_read_b128 v[248:251], v254 offset:9216\n\t" \
    "v_mfma_f32_16x16x32_bf16 v[204:207], v[232:235], v[160:163], v[204:207]\n\t" \
    "v_mfma_f32_16x16x32_bf16 v[204:207], v[236:239], v[164:167], v[204:207]\n\t" \
    "v_cvt_pk_bf16_f32 v208, v208, v216\n\t" \
    "v_cvt_pk_bf16_f32 v209, v209, v217\n\t" \
    "v_mfma_f32_16x16x32_bf16 v[204:207], v[240:243], v[168:171], v[204:207]\n\t" \
    "v_cvt_pk_bf16_f32 v210, v210, v218\n\t" \
    "v_cvt_pk_bf16_f32 v211, v211, v219\n\t" \
    "v_mfma_f32_16x16x32_bf16 v[204:207], v[244:247], v[172:175], v[204:207]\n\t" \
    "s_add_u32 s44, s42, 0x90000\n\t" \
    "s_waitcnt lgkmcnt(0)\n\t" \
    "v_mfma_f32_16x16x32_bf16 v[204:207], v[228:231], v[248:251], v[204:207]\n\t" \
    "buffer_store_dwordx4 v[208:211], v253, s[60:63], s44 offen sc1\n\t" \
    "s_nop 6\n\t" \
    "v_cvt_pk_bf16_f32 v220, v220, v204\n\t" \
    "v_cvt_pk_bf16_f32 v221, v221, v205\n\t" \
    "v_cvt_pk_bf16_f32 v222, v222, v206\n\t" \
    "v_cvt_pk_bf16_f32 v223, v223, v207\n\t" \
    "s_add_u32 s44, s42, 0x80000\n\t" \
    "buffer_store_dwordx4 v[220:223], v253, s[60:63], s44 offen sc1\n\t" \
    "s_add_u32 s44, s40, 1\n\t" \
    "s_mov_b32 s45, s44\n\t" \
    "v_mov_b64 v[200:201], s[44:45]\n\t" \
    "v_mov_b64 v[202:203], s[44:45]\n\t" \
    "s_waitcnt lgkmcnt(0)\n\t" \
    "s_barrier \n\t" \
    "ds_read_b128 v[220:223], v253 offset:3072\n\t" \
    "ds_read_b128 v[224:227], v253 offset:3072\n\t" \
    "ds_read_b128 v[228:231], v253 offset:3072\n\t" \
    "ds_read_b128 v[232:235], v253 offset:3072\n\t" \
    "ds_read_b128 v[236:239], v253 offset:3072\n\t" \
    "ds_read_b128 v[240:243], v253 offset:3072\n\t" \
    "ds_read_b128 v[244:247], v253 offset:3072\n\t" \
    "ds_read_b128 v[248:251], v253 offset:3072\n\t" \
    "ds_read_b128 v[212:215], v254 offset:0\n\t" \
    "ds_read_b128 v[216:219], v254 offset:1024\n\t" \
    "s_and_b32 s44, s40, 3\n\t" \
    "s_mul_i32 s44, s44, 0x202000\n\t" \
    "s_add_u32 s42, s44, %[vo]\n\t" \
    "s_mov_b64 exec, 0xff\n\t" \
    "s_waitcnt vmcnt(0)\n\t" \
    "buffer_store_dwordx4 v[200:203], v253, s[60:63], %[fo] offen sc1\n\t" \
    "s_mov_b64 exec, -1\n\t" \
    "s_setprio 3\n\t" \
    "s_waitcnt lgkmcnt(0)\n\t" \
    "s_barrier \n\t" \
    "ds_read_b128 v[204:207], v253 offset:1024\n\t" \
    "ds_read_b128 v[208:211], v253 offset:2048\n\t" \
    "ds_read_b128 v[200:203], v253 offset:0\n\t" \
    "s_waitcnt lgkmcnt(2)\n\t" \
    "v_smfmac_f32_16x16x64_bf16 v[224:227], v[204:207], v[24:31], v252\n\t" \
    "v_smfmac_f32_16x16x64_bf16 v[220:223], v[204:207], v[0:7], v252\n\t" \
    "s_waitcnt lgkmcnt(1)\n\t" \
    "v_smfmac_f32_16x16x64_bf16 v[224:227], v[208:211], v[32:39], v252\n\t" \
    "v_smfmac_f32_16x16x64_bf16 v[220:223], v[208:211], v[8:15], v252\n\t" \
    "s_waitcnt lgkmcnt(0)\n\t" \
    "v_smfmac_f32_16x16x64_bf16 v[224:227], v[200:203], v[16:23], v252\n\t" \
    "v_smfmac_f32_16x16x64_bf16 v[220:223], v[200:203], v[212:219], v252\n\t" \
    "ds_read_b128 v[212:215], v254 offset:2048\n\t" \
    "ds_read_b128 v[216:219], v254 offset:3072\n\t" \
    "v_smfmac_f32_16x16x64_bf16 v[232:235], v[204:207], v[64:71], v252\n\t" \
    "v_smfmac_f32_16x16x64_bf16 v[228:231], v[204:207], v[40:47], v252\n\t" \
    "v_smfmac_f32_16x16x64_bf16 v[232:235], v[208:211], v[72:79], v252\n\t" \
    "v_add_f32 v220, v220, v222\n\t" \
    "v_add_f32 v221, v221, v223\n\t" \
    "v_cvt_pk_bf16_f32 v220, v220, v221\n\t" \
    "v_smfmac_f32_16x16x64_bf16 v[228:231], v[208:211], v[48:55], v252\n\t" \
    "v_min_u32 v220, -2, v220\n\t" \
    "v_add_f32 v224, v224, v226\n\t" \
    "v_add_f32 v225, v225, v227\n\t" \
    "v_smfmac_f32_16x16x64_bf16 v[232:235], v[200:203], v[56:63], v252\n\t" \
    "v_cvt_pk_bf16_f32 v221, v224, v225\n\t" \
    "v_min_u32 v221, -2, v221\n\t" \
    "s_waitcnt lgkmcnt(0)\n\t" \
    "v_smfmac_f32_16x16x64_bf16 v[228:231], v[200:203], v[212:219], v252\n\t" \
    "ds_read_b128 v[212:215], v254 offset:4096\n\t" \
    "ds_read_b128 v[216:219], v254 offset:5120\n\t" \
    "v_smfmac_f32_16x16x64_bf16 v[240:243], v[204:207], v[104:111], v252\n\t" \
    "v_smfmac_f32_16x16x64_bf16 v[236:239], v[204:207], v[80:87], v252\n\t" \
    "v_smfmac_f32_16x16x64_bf16 v[240:243], v[208:211], v[112:119], v252\n\t" \
    "v_add_f32 v228, v228, v230\n\t" \
    "v_add_f32 v229, v229, v231\n\t" \
    "v_cvt_pk_bf16_f32 v222, v228, v229\n\t" \
    "v_smfmac_f32_16x16x64_bf16 v[236:239], v[208:211], v[88:95], v252\n\t" \
    "v_min_u32 v222, -2, v222\n\t" \
    "v_add_f32 v232, v232, v234\n\t" \
    "v_add_f32 v233, v233, v235\n\t" \
    "v_smfmac_f32_16x16x64_bf16 v[240:243], v[200:203], v[96:103], v252\n\t" \
    "v_cvt_pk_bf16_f32 v223, v232, v233\n\t" \
    "v_min_u32 v223, -2, v223\n\t" \
    "v_permlane32_swap_b32 v220, v222\n\t" \
    "s_waitcnt lgkmcnt(0)\n\t" \
    "v_smfmac_f32_16x16x64_bf16 v[236:239], v[200:203], v[212:219], v252\n\t" \
    "ds_read_b128 v[212:215], v254 offset:6144\n\t" \
    "ds_read_b128 v[216:219], v254 offset:7168\n\t" \
    "s_nop 0\n\t" \
    "v_permlane32_swap_b32 v221, v223\n\t" \
    "s_nop 1\n\t" \
    "v_permlane16_swap_b32 v220, v221\n\t" \
    "v_permlane16_swap_b32 v222, v223\n\t" \
    "buffer_store_dwordx4 v[220:223], v255, s[60:63], s42 offen sc1\n\t" \
    "s_add_u32 s42, s42, 0x10000\n\t" \
    "ds_read_b128 v[224:227], v253 offset:3072\n\t" \
    "ds_read_b128 v[228:231], v253 offset:3072\n\t" \
    "v_smfmac_f32_16x16x64_bf16 v[248:251], v[204:207], v[144:151], v252\n\t" \
    "v_smfmac_f32_16x16x64_bf16 v[244:247], v[204:207], v[120:127], v252\n\t" \
    "v_smfmac_f32_16x16x64_bf16 v[248:251], v[208:211], v[152:159], v252\n\t" \
    "v_add_f32 v236, v236, v238\n\t" \
    "v_add_f32 v237, v237, v239\n\t" \
    "v_cvt_pk_bf16_f32 v236, v236, v237\n\t" \
    "v_smfmac_f32_16x16x64_bf16 v[244:247], v[208:211], v[128:135], v252\n\t" \
    "v_min_u32 v236, -2, v236\n\t" \
    "v_add_f32 v240, v240, v242\n\t" \
    "v_add_f32 v241, v241, v243\n\t" \
    "v_smfmac_f32_16x16x64_bf16 v[248:251], v[200:203], v[136:143], v252\n\t" \
    "v_cvt_pk_bf16_f32 v237, v240, v241\n\t" \
    "v_min_u32 v237, -2, v237\n\t" \
    "s_waitcnt lgkmcnt(2)\n\t" \
    "v_smfmac_f32_16x16x64_bf16 v[244:247], v[200:203], v[212:219], v252\n\t" \
    "ds_read_b128 v[212:215], v254 offset:8192\n\t" \
    "ds_read_b128 v[216:219], v254 offset:9216\n\t" \
    "s_waitcnt lgkmcnt(2)\n\t" \
    "v_smfmac_f32_16x16x64_bf16 v[228:231], v[204:207], v[184:191], v252\n\t" \
    "v_smfmac_f32_16x16x64_bf16 v[224:227], v[204:207], v[160:167], v252\n\t" \
    "v_smfmac_f32_16x16x64_bf16 v[228:231], v[208:211], v[192:199], v252\n\t" \
    "v_and_b32 v232, 0x7fff, v255\n\t" \
    "v_lshrrev_b32 v233, 15, v255\n\t" \
    "v_lshl_add_u32 v232, v233, 3, v232\n\t" \
    "v_smfmac_f32_16x16x64_bf16 v[224:227], v[208:211], v[168:175], v252\n\t" \
    "v_add_f32 v244, v244, v246\n\t" \
    "v_add_f32 v245, v245, v247\n\t" \
    "v_cvt_pk_bf16_f32 v238, v244, v245\n\t" \
    "v_smfmac_f32_16x16x64_bf16 v[228:231], v[200:203], v[176:183], v252\n\t" \
    "v_min_u32 v238, -2, v238\n\t" \
    "v_add_f32 v248, v248, v250\n\t" \
    "v_add_f32 v249, v249, v251\n\t" \
    "s_waitcnt lgkmcnt(0)\n\t" \
    "v_smfmac_f32_16x16x64_bf16 v[224:227], v[200:203], v[212:219], v252\n\t" \
    "v_cvt_pk_bf16_f32 v239, v248, v249\n\t" \
    "v_min_u32 v239, -2, v239\n\t" \
    "v_permlane32_swap_b32 v236, v238\n\t" \
    "s_nop 0\n\t" \
    "v_permlane32_swap_b32 v237, v239\n\t" \
    "s_nop 1\n\t" \
    "v_permlane16_swap_b32 v236, v237\n\t" \
    "v_permlane16_swap_b32 v238, v239\n\t" \
    "buffer_store_dwordx4 v[236:239], v255, s[60:63], s42 offen sc1\n\t" \
    "s_add_u32 s42, s42, 0x10000\n\t" \
    "v_add_f32 v228, v228, v230\n\t" \
    "v_add_f32 v229, v229, v231\n\t" \
    "v_add_f32 v224, v224, v226\n\t" \
    "v_add_f32 v225, v225, v227\n\t" \
    "v_cvt_pk_bf16_f32 v229, v228, v229\n\t" \
    "v_cvt_pk_bf16_f32 v228, v224, v225\n\t" \
    "v_min_u32 v229, -2, v229\n\t" \
    "v_min_u32 v228, -2, v228\n\t" \
    "s_nop 1\n\t" \
    "v_permlane16_swap_b32 v228, v229\n\t" \
    "buffer_store_dwordx2 v[228:229], v232, s[60:63], s42 offen sc1\n\t" \
    "s_setprio 0\n\t" \
    "s_add_u32 s40, s40, 1\n\t" \
    "s_cmp_lg_u32 s40, 64\n\t" \
    "s_cbranch_scc1 .Lv43_%=\n\t" \
    "s_branch .Lend%=\n\t" \
    ".Lv3_%=:\n\t" \
    "s_mov_b32 s42, %[xs]\n\t" \
    "v_add_u32 v244, 0xc000, v243\n\t" \
    "s_waitcnt vmcnt(12) lgkmcnt(0)\n\t" \
    "s_barrier \n\t" \
    "s_add_u32 m0, %[wm], 0x9000\n\t" \
    "s_mov_b32 s44, 0x1e0000\n\t" \
    "buffer_load_dwordx4 v240, %[wr], s44 offen offset:0 nt lds\n\t" \
    "buffer_load_dwordx4 v241, %[wr], s44 offen offset:1024 nt lds\n\t" \
    "buffer_load_dwordx4 v242, %[wr], s44 offen offset:2048 nt lds\n\t" \
    "ds_read_b64_tr_b16 v[208:209], v244 offset:0\n\t" \
    "ds_read_b64_tr_b16 v[210:211], v244 offset:1536\n\t" \
    "ds_read_b64_tr_b16 v[40:41], v244 offset:32\n\t" \
    "ds_read_b64_tr_b16 v[42:43], v244 offset:1568\n\t" \
    "ds_read_b64_tr_b16 v[48:49], v244 offset:64\n\t" \
    "ds_read_b64_tr_b16 v[50:51], v244 offset:1600\n\t" \
    "v_add_u32 v244, 0xf000, v243\n\t" \
    "s_waitcnt vmcnt(12) lgkmcnt(0)\n\t" \
    "s_barrier \n\t" \
    "s_add_u32 m0, %[wm], 0xc000\n\t" \
    "s_mov_b32 s44, 0x240000\n\t" \
    "buffer_load_dwordx4 v240, %[wr], s44 offen offset:0 nt lds\n\t" \
    "buffer_load_dwordx4 v241, %[wr], s44 offen offset:1024 nt lds\n\t" \
    "buffer_load_dwordx4 v242, %[wr], s44 offen offset:2048 nt lds\n\t" \
    "ds_read_b64_tr_b16 v[212:213], v244 offset:0\n\t" \
    "ds_read_b64_tr_b16 v[214:215], v244 offset:1536\n\t" \
    "ds_read_b64_tr_b16 v[44:45], v244 offset:32\n\t" \
    "ds_read_b64_tr_b16 v[46:47], v244 offset:1568\n\t" \
    "ds_read_b64_tr_b16 v[52:53], v244 offset:64\n\t" \
    "ds_read_b64_tr_b16 v[54:55], v244 offset:1600\n\t" \
    "v_add_u32 v244, 0x0, v243\n\t" \
    "s_waitcnt vmcnt(12) lgkmcnt(0)\n\t" \
    "s_barrier \n\t" \
    "s_add_u32 m0, %[wm], 0xf000\n\t" \
    "s_mov_b32 s44, 0x2a0000\n\t" \
    "buffer_load_dwordx4 v240, %[wr], s44 offen offset:0 nt lds\n\t" \
    "buffer_load_dwordx4 v241, %[wr], s44 offen offset:1024 nt lds\n\t" \
    "buffer_load_dwordx4 v242, %[wr], s44 offen offset:2048 nt lds\n\t" \
    "ds_read_b64_tr_b16 v[56:57], v244 offset:0\n\t" \
    "ds_read_b64_tr_b16 v[58:59], v244 offset:1536\n\t" \
    "ds_read_b64_tr_b16 v[64:65], v244 offset:32\n\t" \
    "ds_read_b64_tr_b16 v[66:67], v244 offset:1568\n\t" \
    "ds_read_b64_tr_b16 v[72:73], v244 offset:64\n\t" \
    "ds_read_b64_tr_b16 v[74:75], v244 offset:1600\n\t" \
    "v_add_u32 v244, 0x3000, v243\n\t" \
    "s_waitcnt vmcnt(12) lgkmcnt(0)\n\t" \
    "s_barrier \n\t" \
    "s_add_u32 m0, %[wm], 0x0\n\t" \
    "s_mov_b32 s44, 0x300000\n\t" \
    "buffer_load_dwordx4 v240, %[wr], s44 offen offset:0 nt lds\n\t" \
    "buffer_load_dwordx4 v241, %[wr], s44 offen offset:1024 nt lds\n\t" \
    "buffer_load_dwordx4 v242, %[wr], s44 offen offset:2048 nt lds\n\t" \
    "ds_read_b64_tr_b16 v[60:61], v244 offset:0\n\t" \
    "ds_read_b64_tr_b16 v[62:63], v244 offset:1536\n\t" \
    "ds_read_b64_tr_b16 v[68:69], v244 offset:32\n\t" \
    "ds_read_b64_tr_b16 v[70:71], v244 offset:1568\n\t" \
    "ds_read_b64_tr_b16 v[76:77], v244 offset:64\n\t" \
    "ds_read_b64_tr_b16 v[78:79], v244 offset:1600\n\t" \
    "v_add_u32 v244, 0x6000, v243\n\t" \
    "s_waitcnt vmcnt(12) lgkmcnt(0)\n\t" \
    "s_barrier \n\t" \
    "s_add_u32 m0, %[wm], 0x3000\n\t" \
    "s_mov_b32 s44, 0x360000\n\t" \
    "buffer_load_dwordx4 v240, %[wr], s44 offen offset:0 nt lds\n\t" \
    "buffer_load_dwordx4 v241, %[wr], s44 offen offset:1024 nt lds\n\t" \
    "buffer_load_dwordx4 v242, %[wr], s44 offen offset:2048 nt lds\n\t" \
    "ds_read_b64_tr_b16 v[216:217], v244 offset:0\n\t" \
    "ds_read_b64_tr_b16 v[218:219], v244 offset:1536\n\t" \
    "ds_read_b64_tr_b16 v[80:81], v244 offset:32\n\t" \
    "ds_read_b64_tr_b16 v[82:83], v244 offset:1568\n\t" \
    "ds_read_b64_tr_b16 v[88:89], v244 offset:64\n\t" \
    "ds_read_b64_tr_b16 v[90:91], v244 offset:1600\n\t" \
    "v_add_u32 v244, 0x9000, v243\n\t" \
    "s_waitcnt vmcnt(12) lgkmcnt(0)\n\t" \
    "s_barrier \n\t" \
    "s_add_u32 m0, %[wm], 0x6000\n\t" \
    "s_mov_b32 s44, 0x3c0000\n\t" \
    "buffer_load_dwordx4 v240, %[wr], s44 offen offset:0 nt lds\n\t" \
    "buffer_load_dwordx4 v241, %[wr], s44 offen offset:1024 nt lds\n\t" \
    "buffer_load_dwordx4 v242, %[wr], s44 offen offset:2048 nt lds\n\t" \
    "ds_read_b64_tr_b16 v[220:221], v244 offset:0\n\t" \
    "ds_read_b64_tr_b16 v[222:223], v244 offset:1536\n\t" \
    "ds_read_b64_tr_b16 v[84:85], v244 offset:32\n\t" \
    "ds_read_b64_tr_b16 v[86:87], v244 offset:1568\n\t" \
    "ds_read_b64_tr_b16 v[92:93], v244 offset:64\n\t" \
    "ds_read_b64_tr_b16 v[94:95], v244 offset:1600\n\t" \
    "v_add_u32 v244, 0xc000, v243\n\t" \
    "s_waitcnt vmcnt(12) lgkmcnt(0)\n\t" \
    "s_barrier \n\t" \
    "s_add_u32 m0, %[wm], 0x9000\n\t" \
    "s_mov_b32 s44, 0x420000\n\t" \
    "buffer_load_dwordx4 v240, %[wr], s44 offen offset:0 nt lds\n\t" \
    "buffer_load_dwordx4 v241, %[wr], s44 offen offset:1024 nt lds\n\t" \
    "buffer_load_dwordx4 v242, %[wr], s44 offen offset:2048 nt lds\n\t" \
    "ds_read_b64_tr_b16 v[96:97], v244 offset:0\n\t" \
    "ds_read_b64_tr_b16 v[98:99], v244 offset:1536\n\t" \
    "ds_read_b64_tr_b16 v[104:105], v244 offset:32\n\t" \
    "ds_read_b64_tr_b16 v[106:107], v244 offset:1568\n\t" \
    "ds_read_b64_tr_b16 v[112:113], v244 offset:64\n\t" \
    "ds_read_b64_tr_b16 v[114:115], v244 offset:1600\n\t" \
    "v_add_u32 v244, 0xf000, v243\n\t" \
    "s_waitcnt vmcnt(12) lgkmcnt(0)\n\t" \
    "s_barrier \n\t" \
    "s_add_u32 m0, %[wm], 0xc000\n\t" \
    "s_mov_b32 s44, 0x480000\n\t" \
    "buffer_load_dwordx4 v240, %[wr], s44 offen offset:0 nt lds\n\t" \
    "buffer_load_dwordx4 v241, %[wr], s44 offen offset:1024 nt lds\n\t" \
    "buffer_load_dwordx4 v242, %[wr], s44 offen offset:2048 nt lds\n\t" \
    "ds_read_b64_tr_b16 v[100:101], v244 offset:0\n\t" \
    "ds_read_b64_tr_b16 v[102:103], v244 offset:1536\n\t" \
    "ds_read_b64_tr_b16 v[108:109], v244 offset:32\n\t" \
    "ds_read_b64_tr_b16 v[110:111], v244 offset:1568\n\t" \
    "ds_read_b64_tr_b16 v[116:117], v244 offset:64\n\t" \
    "ds_read_b64_tr_b16 v[118:119], v244 offset:1600\n\t" \
    "v_add_u32 v244, 0x0, v243\n\t" \
    "s_waitcnt vmcnt(12) lgkmcnt(0)\n\t" \
    "s_barrier \n\t" \
    "v_mov_b32 v246, 0\n\t" \
    "buffer_load_dword v246, v246, s[60:63], %[mo] offen sc1\n\t" \
    "s_add_u32 m0, %[wm], 0xf000\n\t" \
    "s_mov_b32 s44, 0x4e0000\n\t" \
    "buffer_load_dwordx4 v240, %[wr], s44 offen offset:0 nt lds\n\t" \
    "buffer_load_dwordx4 v241, %[wr], s44 offen offset:1024 nt lds\n\t" \
    "buffer_load_dwordx4 v242, %[wr], s44 offen offset:2048 nt lds\n\t" \
    "ds_read_b64_tr_b16 v[224:225], v244 offset:0\n\t" \
    "ds_read_b64_tr_b16 v[226:227], v244 offset:1536\n\t" \
    "ds_read_b64_tr_b16 v[120:121], v244 offset:32\n\t" \
    "ds_read_b64_tr_b16 v[122:123], v244 offset:1568\n\t" \
    "ds_read_b64_tr_b16 v[128:129], v244 offset:64\n\t" \
    "ds_read_b64_tr_b16 v[130:131], v244 offset:1600\n\t" \
    "v_add_u32 v244, 0x3000, v243\n\t" \
    "s_waitcnt vmcnt(12) lgkmcnt(0)\n\t" \
    "s_barrier \n\t" \
    "s_add_u32 m0, %[wm], 0x0\n\t" \
    "s_mov_b32 s44, 0x540000\n\t" \
    "buffer_load_dwordx4 v240, %[wr], s44 offen offset:0 nt lds\n\t" \
    "buffer_load_dwordx4 v241, %[wr], s44 offen offset:1024 nt lds\n\t" \
    "buffer_load_dwordx4 v242, %[wr], s44 offen offset:2048 nt lds\n\t" \
    "ds_read_b64_tr_b16 v[228:229], v244 offset:0\n\t" \
    "ds_read_b64_tr_b16 v[230:231], v244 offset:1536\n\t" \
    "ds_read_b64_tr_b16 v[124:125], v244 offset:32\n\t" \
    "ds_read_b64_tr_b16 v[126:127], v244 offset:1568\n\t" \
    "ds_read_b64_tr_b16 v[132:133], v244 offset:64\n\t" \
    "ds_read_b64_tr_b16 v[134:135], v244 offset:1600\n\t" \
    "v_add_u32 v244, 0x6000, v243\n\t" \
    "s_waitcnt vmcnt(12) lgkmcnt(0)\n\t" \
    "s_barrier \n\t" \
    "s_add_u32 m0, %[wm], 0x3000\n\t" \
    "s_mov_b32 s44, 0x5a0000\n\t" \
    "buffer_load_dwordx4 v240, %[wr], s44 offen offset:0 nt lds\n\t" \
    "buffer_load_dwordx4 v241, %[wr], s44 offen offset:1024 nt lds\n\t" \
    "buffer_load_dwordx4 v242, %[wr], s44 offen offset:2048 nt lds\n\t" \
    "ds_read_b64_tr_b16 v[136:137], v244 offset:0\n\t" \
    "ds_read_b64_tr_b16 v[138:139], v244 offset:1536\n\t" \
    "ds_read_b64_tr_b16 v[144:145], v244 offset:32\n\t" \
    "ds_read_b64_tr_b16 v[146:147], v244 offset:1568\n\t" \
    "ds_read_b64_tr_b16 v[152:153], v244 offset:64\n\t" \
    "ds_read_b64_tr_b16 v[154:155], v244 offset:1600\n\t" \
    "v_add_u32 v244, 0x9000, v243\n\t" \
    "s_waitcnt vmcnt(12) lgkmcnt(0)\n\t" \
    "s_barrier \n\t" \
    "s_add_u32 m0, %[wm], 0x6000\n\t" \
    "s_mov_b32 s44, 0x600000\n\t" \
    "buffer_load_dwordx4 v240, %[wr], s44 offen offset:0 nt lds\n\t" \
    "buffer_load_dwordx4 v241, %[wr], s44 offen offset:1024 nt lds\n\t" \
    "buffer_load_dwordx4 v242, %[wr], s44 offen offset:2048 nt lds\n\t" \
    "ds_read_b64_tr_b16 v[140:141], v244 offset:0\n\t" \
    "ds_read_b64_tr_b16 v[142:143], v244 offset:1536\n\t" \
    "ds_read_b64_tr_b16 v[148:149], v244 offset:32\n\t" \
    "ds_read_b64_tr_b16 v[150:151], v244 offset:1568\n\t" \
    "ds_read_b64_tr_b16 v[156:157], v244 offset:64\n\t" \
    "ds_read_b64_tr_b16 v[158:159], v244 offset:1600\n\t" \
    "v_add_u32 v244, 0xc000, v243\n\t" \
    "s_waitcnt vmcnt(12) lgkmcnt(0)\n\t" \
    "s_barrier \n\t" \
    "s_add_u32 m0, %[wm], 0x9000\n\t" \
    "s_mov_b32 s44, 0x660000\n\t" \
    "buffer_load_dwordx4 v240, %[wr], s44 offen offset:0 nt lds\n\t" \
    "buffer_load_dwordx4 v241, %[wr], s44 offen offset:1024 nt lds\n\t" \
    "buffer_load_dwordx4 v242, %[wr], s44 offen offset:2048 nt lds\n\t" \
    "ds_read_b64_tr_b16 v[232:233], v244 offset:0\n\t" \
    "ds_read_b64_tr_b16 v[234:235], v244 offset:1536\n\t" \
    "ds_read_b64_tr_b16 v[160:161], v244 offset:32\n\t" \
    "ds_read_b64_tr_b16 v[162:163], v244 offset:1568\n\t" \
    "ds_read_b64_tr_b16 v[168:169], v244 offset:64\n\t" \
    "ds_read_b64_tr_b16 v[170:171], v244 offset:1600\n\t" \
    "v_add_u32 v244, 0xf000, v243\n\t" \
    "s_waitcnt vmcnt(12) lgkmcnt(0)\n\t" \
    "s_barrier \n\t" \
    ".Lv31_%=:\n\t" \
    "v_readfirstlane_b32 s43, v246\n\t" \
    "s_nop 0\n\t" \
    "s_cmp_lg_u32 s43, -1\n\t" \
    "s_cbranch_scc1 .Lv32_%=\n\t" \
    "s_cmp_eq_u32 s41, 0\n\t" \
    "s_cbranch_scc1 .Lv32_%=\n\t" \
    "s_sub_u32 s41, s41, 1\n\t" \
    "s_sleep 4\n\t" \
    "v_mov_b32 v246, 0\n\t" \
    "buffer_load_dword v246, v246, s[60:63], %[mo] offen sc1\n\t" \
    "s_waitcnt vmcnt(0)\n\t" \
    "s_branch .Lv31_%=\n\t" \
    ".Lv32_%=:\n\t" \
    "v_mov_b32 v246, 0x8000\n\t" \
    "s_mov_b64 exec, 1\n\t" \
    "buffer_store_dword v246, v246, s[60:63], %[mo] offen sc1\n\t" \
    "s_mov_b64 exec, -1\n\t" \
    "s_and_b32 s43, s43, 0x2000\n\t" \
    "s_xor_b32 s43, s43, %[lx]\n\t" \
    "s_add_u32 s60, s60, s43\n\t" \
    "s_addc_u32 s61, s61, 0\n\t" \
    "v_and_b32 v252, 0xf0, v253\n\t" \
    "v_lshlrev_b32 v252, 3, v252\n\t" \
    "buffer_load_dword v252, v252, s[60:63], %[fl] offen sc1\n\t" \
    "s_add_u32 m0, %[wm], 0xc000\n\t" \
    "s_mov_b32 s44, 0x6c0000\n\t" \
    "buffer_load_dwordx4 v240, %[wr], s44 offen offset:0 nt lds\n\t" \
    "buffer_load_dwordx4 v241, %[wr], s44 offen offset:1024 nt lds\n\t" \
    "buffer_load_dwordx4 v242, %[wr], s44 offen offset:2048 nt lds\n\t" \
    "ds_read_b64_tr_b16 v[236:237], v244 offset:0\n\t" \
    "ds_read_b64_tr_b16 v[238:239], v244 offset:1536\n\t" \
    "ds_read_b64_tr_b16 v[164:165], v244 offset:32\n\t" \
    "ds_read_b64_tr_b16 v[166:167], v244 offset:1568\n\t" \
    "ds_read_b64_tr_b16 v[172:173], v244 offset:64\n\t" \
    "ds_read_b64_tr_b16 v[174:175], v244 offset:1600\n\t" \
    "v_add_u32 v244, 0x0, v243\n\t" \
    "s_waitcnt vmcnt(12) lgkmcnt(0)\n\t" \
    "s_barrier \n\t" \
    "s_add_u32 m0, %[wm], 0xf000\n\t" \
    "s_mov_b32 s44, 0x720000\n\t" \
    "buffer_load_dwordx4 v240, %[wr], s44 offen offset:0 nt lds\n\t" \
    "buffer_load_dwordx4 v241, %[wr], s44 offen offset:1024 nt lds\n\t" \
    "buffer_load_dwordx4 v242, %[wr], s44 offen offset:2048 nt lds\n\t" \
    "ds_read_b64_tr_b16 v[176:177], v244 offset:0\n\t" \
    "ds_read_b64_tr_b16 v[178:179], v244 offset:1536\n\t" \
    "ds_read_b64_tr_b16 v[184:185], v244 offset:32\n\t" \
    "ds_read_b64_tr_b16 v[186:187], v244 offset:1568\n\t" \
    "ds_read_b64_tr_b16 v[192:193], v244 offset:64\n\t" \
    "ds_read_b64_tr_b16 v[194:195], v244 offset:1600\n\t" \
    "v_add_u32 v244, 0x3000, v243\n\t" \
    "s_waitcnt vmcnt(12) lgkmcnt(0)\n\t" \
    "s_barrier \n\t" \
    "ds_read_b64_tr_b16 v[180:181], v244 offset:0\n\t" \
    "ds_read_b64_tr_b16 v[182:183], v244 offset:1536\n\t" \
    "ds_read_b64_tr_b16 v[188:189], v244 offset:32\n\t" \
    "ds_read_b64_tr_b16 v[190:191], v244 offset:1568\n\t" \
    "ds_read_b64_tr_b16 v[196:197], v244 offset:64\n\t" \
    "ds_read_b64_tr_b16 v[198:199], v244 offset:1600\n\t" \
    "v_add_u32 v244, 0x6000, v243\n\t" \
    "s_waitcnt vmcnt(9) lgkmcnt(0)\n\t" \
    "s_barrier \n\t" \
    "ds_read_b64_tr_b16 v[200:201], v244 offset:0\n\t" \
    "ds_read_b64_tr_b16 v[202:203], v244 offset:1536\n\t" \
    "ds_read_b64_tr_b16 v[0:1], v244 offset:32\n\t" \
    "ds_read_b64_tr_b16 v[2:3], v244 offset:1568\n\t" \
    "ds_read_b64_tr_b16 v[8:9], v244 offset:64\n\t" \
    "ds_read_b64_tr_b16 v[10:11], v244 offset:1600\n\t" \
    "v_add_u32 v244, 0x9000, v243\n\t" \
    "s_waitcnt vmcnt(6) lgkmcnt(0)\n\t" \
    "s_barrier \n\t" \
    "ds_read_b64_tr_b16 v[204:205], v244 offset:0\n\t" \
    "ds_read_b64_tr_b16 v[206:207], v244 offset:1536\n\t" \
    "ds_read_b64_tr_b16 v[4:5], v244 offset:32\n\t" \
    "ds_read_b64_tr_b16 v[6:7], v244 offset:1568\n\t" \
    "ds_read_b64_tr_b16 v[12:13], v244 offset:64\n\t" \
    "ds_read_b64_tr_b16 v[14:15], v244 offset:1600\n\t" \
    "v_add_u32 v244, 0xc000, v243\n\t" \
    "s_waitcnt vmcnt(3) lgkmcnt(0)\n\t" \
    "s_barrier \n\t" \
    "ds_read_b64_tr_b16 v[16:17], v244 offset:0\n\t" \
    "ds_read_b64_tr_b16 v[18:19], v244 offset:1536\n\t" \
    "ds_read_b64_tr_b16 v[24:25], v244 offset:32\n\t" \
    "ds_read_b64_tr_b16 v[26:27], v244 offset:1568\n\t" \
    "ds_read_b64_tr_b16 v[32:33], v244 offset:64\n\t" \
    "ds_read_b64_tr_b16 v[34:35], v244 offset:1600\n\t" \
    "s_mov_b32 s43, 0\n\t" \
    "v_cmp_eq_u32 vcc, 0, v252\n\t" \
    "s_cbranch_vccnz .Lv33_%=\n\t" \
    "s_mov_b32 m0, 0x3000\n\t" \
    "s_add_u32 s49, s42, 0x0\n\t" \
    "buffer_load_dwordx4 v253, s[60:63], s49 offen offset:0 sc1 lds\n\t" \
    "buffer_load_dwordx4 v253, s[60:63], s49 offen offset:1024 sc1 lds\n\t" \
    "buffer_load_dwordx4 v253, s[60:63], s49 offen offset:2048 sc1 lds\n\t" \
    "buffer_load_dwordx4 v253, s[60:63], s49 offen offset:3072 sc1 lds\n\t" \
    "s_mov_b32 m0, 0x4c00\n\t" \
    "s_add_u32 s49, s42, 0x4c00\n\t" \
    "buffer_load_dwordx4 v253, s[60:63], s49 offen offset:0 sc1 lds\n\t" \
    "s_mov_b32 m0, 0x8000\n\t" \
    "s_add_u32 s49, s42, 0x1000\n\t" \
    "buffer_load_dwordx4 v253, s[60:63], s49 offen offset:0 sc1 lds\n\t" \
    "buffer_load_dwordx4 v253, s[60:63], s49 offen offset:1024 sc1 lds\n\t" \
    "buffer_load_dwordx4 v253, s[60:63], s49 offen offset:2048 sc1 lds\n\t" \
    "buffer_load_dwordx4 v253, s[60:63], s49 offen offset:3072 sc1 lds\n\t" \
    "s_mov_b32 m0, 0x9c00\n\t" \
    "s_add_u32 s49, s42, 0x5c00\n\t" \
    "buffer_load_dwordx4 v253, s[60:63], s49 offen offset:0 sc1 lds\n\t" \
    "s_mov_b32 s43, 1\n\t" \
    ".Lv33_%=:\n\t" \
    "v_add_u32 v244, 0xf000, v243\n\t" \
    "s_waitcnt vmcnt(0) lgkmcnt(0)\n\t" \
    "s_barrier \n\t" \
    "ds_read_b64_tr_b16 v[20:21], v244 offset:0\n\t" \
    "ds_read_b64_tr_b16 v[22:23], v244 offset:1536\n\t" \
    "ds_read_b64_tr_b16 v[28:29], v244 offset:32\n\t" \
    "ds_read_b64_tr_b16 v[30:31], v244 offset:1568\n\t" \
    "ds_read_b64_tr_b16 v[36:37], v244 offset:64\n\t" \
    "ds_read_b64_tr_b16 v[38:39], v244 offset:1600\n\t" \
    "s_waitcnt lgkmcnt(0)\n\t" \
    "s_barrier \n\t" \
    "ds_write_b128 v254, v[200:203] offset:55296\n\t" \
    "ds_write_b128 v254, v[204:207] offset:56320\n\t" \
    "ds_write_b128 v254, v[208:211] offset:57344\n\t" \
    "ds_write_b128 v254, v[212:215] offset:58368\n\t" \
    "ds_write_b128 v254, v[216:219] offset:59392\n\t" \
    "ds_write_b128 v254, v[220:223] offset:60416\n\t" \
    "ds_write_b128 v254, v[224:227] offset:61440\n\t" \
    "ds_write_b128 v254, v[228:231] offset:62464\n\t" \
    "ds_write_b128 v254, v[232:235] offset:63488\n\t" \
    "ds_write_b128 v254, v[236:239] offset:64512\n\t" \
    "s_cmp_lg_u32 s43, 0\n\t" \
    "s_cbranch_scc1 .Lv34_%=\n\t" \
    "s_mov_b32 s44, 0\n\t" \
    ".Lv35_%=:\n\t" \
    "v_cmp_ge_u32 vcc, s44, v252\n\t" \
    "s_cbranch_vccz .Lv36_%=\n\t" \
    "s_cmp_eq_u32 s41, 0\n\t" \
    "s_cbranch_scc1 .Lv36_%=\n\t" \
    "s_sub_u32 s41, s41, 1\n\t" \
    "s_sleep 8\n\t" \
    "v_and_b32 v252, 0xf0, v253\n\t" \
    "v_lshlrev_b32 v252, 3, v252\n\t" \
    "buffer_load_dword v252, v252, s[60:63], %[fl] offen sc1\n\t" \
    "s_waitcnt vmcnt(0)\n\t" \
    "s_branch .Lv35_%=\n\t" \
    ".Lv36_%=:\n\t" \
    "s_mov_b32 m0, 0x3000\n\t" \
    "s_add_u32 s49, s42, 0x0\n\t" \
    "buffer_load_dwordx4 v253, s[60:63], s49 offen offset:0 sc1 lds\n\t" \
    "buffer_load_dwordx4 v253, s[60:63], s49 offen offset:1024 sc1 lds\n\t" \
    "buffer_load_dwordx4 v253, s[60:63], s49 offen offset:2048 sc1 lds\n\t" \
    "buffer_load_dwordx4 v253, s[60:63], s49 offen offset:3072 sc1 lds\n\t" \
    "s_mov_b32 m0, 0x4c00\n\t" \
    "s_add_u32 s49, s42, 0x4c00\n\t" \
    "buffer_load_dwordx4 v253, s[60:63], s49 offen offset:0 sc1 lds\n\t" \
    "s_mov_b32 m0, 0x8000\n\t" \
    "s_add_u32 s49, s42, 0x1000\n\t" \
    "buffer_load_dwordx4 v253, s[60:63], s49 offen offset:0 sc1 lds\n\t" \
    "buffer_load_dwordx4 v253, s[60:63], s49 offen offset:1024 sc1 lds\n\t" \
    "buffer_load_dwordx4 v253, s[60:63], s49 offen offset:2048 sc1 lds\n\t" \
    "buffer_load_dwordx4 v253, s[60:63], s49 offen offset:3072 sc1 lds\n\t" \
    "s_mov_b32 m0, 0x9c00\n\t" \
    "s_add_u32 s49, s42, 0x5c00\n\t" \
    "buffer_load_dwordx4 v253, s[60:63], s49 offen offset:0 sc1 lds\n\t" \
    ".Lv34_%=:\n\t" \
    "s_waitcnt vmcnt(0) lgkmcnt(0)\n\t" \
    "s_barrier \n\t" \
    "ds_read_b128 v[224:227], v253 offset:16384\n\t" \
    "ds_read_b128 v[244:247], v254 offset:55296\n\t" \
    "ds_read_b128 v[228:231], v253 offset:17408\n\t" \
    "ds_read_b128 v[232:235], v253 offset:36864\n\t" \
    "ds_read_b128 v[236:239], v253 offset:37888\n\t" \
    "ds_read_b128 v[248:251], v254 offset:56320\n\t" \
    ".Lv37_%=:\n\t" \
    "s_waitcnt lgkmcnt(5)\n\t" \
    "v_mfma_f32_16x16x32_bf16 v[204:207], v[0:3], v[224:227], 0\n\t" \
    "s_waitcnt lgkmcnt(3)\n\t" \
    "v_mfma_f32_16x16x32_bf16 v[204:207], v[4:7], v[228:231], v[204:207]\n\t" \
    "ds_read_b128 v[240:243], v253 offset:18432\n\t" \
    "v_mfma_f32_16x16x32_bf16 v[208:211], v[8:11], v[224:227], 0\n\t" \
    "v_mfma_f32_16x16x32_bf16 v[208:211], v[12:15], v[228:231], v[208:211]\n\t" \
    "v_mfma_f32_16x16x32_bf16 v[200:203], v[244:247], v[224:227], 0\n\t" \
    "s_waitcnt lgkmcnt(1)\n\t" \
    "v_mfma_f32_16x16x32_bf16 v[200:203], v[248:251], v[228:231], v[200:203]\n\t" \
    "v_mfma_f32_16x16x32_bf16 v[216:219], v[0:3], v[232:235], 0\n\t" \
    "ds_read_b128 v[224:227], v253 offset:19456\n\t" \
    "v_mfma_f32_16x16x32_bf16 v[216:219], v[4:7], v[236:239], v[216:219]\n\t" \
    "ds_read_b128 v[228:231], v253 offset:0\n\t" \
    "v_mfma_f32_16x16x32_bf16 v[220:223], v[8:11], v[232:235], 0\n\t" \
    "v_mfma_f32_16x16x32_bf16 v[220:223], v[12:15], v[236:239], v[220:223]\n\t" \
    "v_mfma_f32_16x16x32_bf16 v[212:215], v[244:247], v[232:235], 0\n\t" \
    "ds_read_b128 v[244:247], v254 offset:57344\n\t" \
    "v_mfma_f32_16x16x32_bf16 v[212:215], v[248:251], v[236:239], v[212:215]\n\t" \
    "ds_read_b128 v[248:251], v254 offset:58368\n\t" \
    "s_waitcnt lgkmcnt(4)\n\t" \
    "v_mfma_f32_16x16x32_bf16 v[204:207], v[24:27], v[240:243], v[204:207]\n\t" \
    "ds_read_b128 v[232:235], v253 offset:1024\n\t" \
    "s_waitcnt lgkmcnt(4)\n\t" \
    "v_mfma_f32_16x16x32_bf16 v[204:207], v[28:31], v[224:227], v[204:207]\n\t" \
    "ds_read_b128 v[236:239], v253 offset:20480\n\t" \
    "v_mfma_f32_16x16x32_bf16 v[208:211], v[32:35], v[240:243], v[208:211]\n\t" \
    "v_mfma_f32_16x16x32_bf16 v[208:211], v[36:39], v[224:227], v[208:211]\n\t" \
    "v_mfma_f32_16x16x32_bf16 v[200:203], v[16:19], v[240:243], v[200:203]\n\t" \
    "v_mfma_f32_16x16x32_bf16 v[200:203], v[20:23], v[224:227], v[200:203]\n\t" \
    "s_waitcnt lgkmcnt(4)\n\t" \
    "v_mfma_f32_16x16x32_bf16 v[204:207], v[40:43], v[228:231], v[204:207]\n\t" \
    "ds_read_b128 v[240:243], v253 offset:21504\n\t" \
    "s_waitcnt lgkmcnt(2)\n\t" \
    "v_mfma_f32_16x16x32_bf16 v[204:207], v[44:47], v[232:235], v[204:207]\n\t" \
    "ds_read_b128 v[224:227], v253 offset:2048\n\t" \
    "v_mfma_f32_16x16x32_bf16 v[208:211], v[48:51], v[228:231], v[208:211]\n\t" \
    "v_mfma_f32_16x16x32_bf16 v[208:211], v[52:55], v[232:235], v[208:211]\n\t" \
    "v_mfma_f32_16x16x32_bf16 v[200:203], v[244:247], v[228:231], v[200:203]\n\t" \
    "v_mfma_f32_16x16x32_bf16 v[200:203], v[248:251], v[232:235], v[200:203]\n\t" \
    "s_waitcnt lgkmcnt(2)\n\t" \
    "v_mfma_f32_16x16x32_bf16 v[216:219], v[40:43], v[236:239], v[216:219]\n\t" \
    "ds_read_b128 v[228:231], v253 offset:3072\n\t" \
    "s_add_u32 s44, s40, 1\n\t" \
    "s_waitcnt lgkmcnt(2)\n\t" \
    "v_mfma_f32_16x16x32_bf16 v[216:219], v[44:47], v[240:243], v[216:219]\n\t" \
    "ds_read_b128 v[232:235], v253 offset:4096\n\t" \
    "s_and_b32 s45, s44, 15\n\t" \
    "v_mfma_f32_16x16x32_bf16 v[220:223], v[48:51], v[236:239], v[220:223]\n\t" \
    "s_cmp_eq_u32 s45, 0\n\t" \
    "v_mfma_f32_16x16x32_bf16 v[220:223], v[52:55], v[240:243], v[220:223]\n\t" \
    "s_cselect_b32 s45, %[cj], %[cs]\n\t" \
    "v_mfma_f32_16x16x32_bf16 v[212:215], v[244:247], v[236:239], v[212:215]\n\t" \
    "ds_read_b128 v[244:247], v254 offset:59392\n\t" \
    "s_cmp_eq_u32 s44, 64\n\t" \
    "v_mfma_f32_16x16x32_bf16 v[212:215], v[248:251], v[240:243], v[212:215]\n\t" \
    "ds_read_b128 v[248:251], v254 offset:60416\n\t" \
    "s_cselect_b32 s45, 0, s45\n\t" \
    "s_waitcnt lgkmcnt(4)\n\t" \
    "v_mfma_f32_16x16x32_bf16 v[204:207], v[64:67], v[224:227], v[204:207]\n\t" \
    "ds_read_b128 v[236:239], v253 offset:5120\n\t" \
    "s_add_u32 s42, s42, s45\n\t" \
    "s_waitcnt lgkmcnt(4)\n\t" \
    "v_mfma_f32_16x16x32_bf16 v[204:207], v[68:71], v[228:231], v[204:207]\n\t" \
    "ds_read_b128 v[240:243], v253 offset:24576\n\t" \
    "v_mfma_f32_16x16x32_bf16 v[208:211], v[72:75], v[224:227], v[208:211]\n\t" \
    "v_mfma_f32_16x16x32_bf16 v[208:211], v[76:79], v[228:231], v[208:211]\n\t" \
    "v_mfma_f32_16x16x32_bf16 v[200:203], v[56:59], v[224:227], v[200:203]\n\t" \
    "v_mfma_f32_16x16x32_bf16 v[200:203], v[60:63], v[228:231], v[200:203]\n\t" \
    "s_waitcnt lgkmcnt(4)\n\t" \
    "v_mfma_f32_16x16x32_bf16 v[204:207], v[80:83], v[232:235], v[204:207]\n\t" \
    "ds_read_b128 v[224:227], v253 offset:25600\n\t" \
    "s_waitcnt lgkmcnt(2)\n\t" \
    "v_mfma_f32_16x16x32_bf16 v[204:207], v[84:87], v[236:239], v[204:207]\n\t" \
    "ds_read_b128 v[228:231], v253 offset:6144\n\t" \
    "v_mfma_f32_16x16x32_bf16 v[208:211], v[88:91], v[232:235], v[208:211]\n\t" \
    "v_mfma_f32_16x16x32_bf16 v[208:211], v[92:95], v[236:239], v[208:211]\n\t" \
    "v_mfma_f32_16x16x32_bf16 v[200:203], v[244:247], v[232:235], v[200:203]\n\t" \
    "v_mfma_f32_16x16x32_bf16 v[200:203], v[248:251], v[236:239], v[200:203]\n\t" \
    "s_waitcnt lgkmcnt(2)\n\t" \
    "v_mfma_f32_16x16x32_bf16 v[216:219], v[80:83], v[240:243], v[216:219]\n\t" \
    "ds_read_b128 v[232:235], v253 offset:7168\n\t" \
    "s_and_b32 s44, s40, 3\n\t" \
    "s_waitcnt lgkmcnt(2)\n\t" \
    "v_mfma_f32_16x16x32_bf16 v[216:219], v[84:87], v[224:227], v[216:219]\n\t" \
    "ds_read_b128 v[236:239], v253 offset:8192\n\t" \
    "s_lshl_b32 s44, s44, 17\n\t" \
    "v_mfma_f32_16x16x32_bf16 v[220:223], v[88:91], v[240:243], v[220:223]\n\t" \
    "s_add_u32 s44, s44, %[mb]\n\t" \
    "v_mfma_f32_16x16x32_bf16 v[220:223], v[92:95], v[224:227], v[220:223]\n\t" \
    "s_add_u32 s46, s44, 0x4000\n\t" \
    "v_mfma_f32_16x16x32_bf16 v[212:215], v[244:247], v[240:243], v[212:215]\n\t" \
    "ds_read_b128 v[244:247], v254 offset:61440\n\t" \
    "s_add_u32 s47, s44, 0x4400\n\t" \
    "v_mfma_f32_16x16x32_bf16 v[212:215], v[248:251], v[224:227], v[212:215]\n\t" \
    "ds_read_b128 v[248:251], v254 offset:62464\n\t" \
    "s_waitcnt lgkmcnt(4)\n\t" \
    "v_mfma_f32_16x16x32_bf16 v[204:207], v[104:107], v[228:231], v[204:207]\n\t" \
    "ds_read_b128 v[240:243], v253 offset:9216\n\t" \
    "s_waitcnt lgkmcnt(4)\n\t" \
    "v_mfma_f32_16x16x32_bf16 v[204:207], v[108:111], v[232:235], v[204:207]\n\t" \
    "ds_read_b128 v[224:227], v253 offset:28672\n\t" \
    "v_mfma_f32_16x16x32_bf16 v[208:211], v[112:115], v[228:231], v[208:211]\n\t" \
    "v_mfma_f32_16x16x32_bf16 v[208:211], v[116:119], v[232:235], v[208:211]\n\t" \
    "v_mfma_f32_16x16x32_bf16 v[200:203], v[96:99], v[228:231], v[200:203]\n\t" \
    "v_mfma_f32_16x16x32_bf16 v[200:203], v[100:103], v[232:235], v[200:203]\n\t" \
    "s_waitcnt lgkmcnt(4)\n\t" \
    "v_mfma_f32_16x16x32_bf16 v[204:207], v[120:123], v[236:239], v[204:207]\n\t" \
    "ds_read_b128 v[228:231], v253 offset:29696\n\t" \
    "s_waitcnt lgkmcnt(2)\n\t" \
    "v_mfma_f32_16x16x32_bf16 v[204:207], v[124:127], v[240:243], v[204:207]\n\t" \
    "ds_read_b128 v[232:235], v253 offset:10240\n\t" \
    "v_mfma_f32_16x16x32_bf16 v[208:211], v[128:131], v[236:239], v[208:211]\n\t" \
    "v_mfma_f32_16x16x32_bf16 v[208:211], v[132:135], v[240:243], v[208:211]\n\t" \
    "v_mfma_f32_16x16x32_bf16 v[200:203], v[244:247], v[236:239], v[200:203]\n\t" \
    "v_mfma_f32_16x16x32_bf16 v[200:203], v[248:251], v[240:243], v[200:203]\n\t" \
    "s_waitcnt lgkmcnt(2)\n\t" \
    "v_mfma_f32_16x16x32_bf16 v[216:219], v[120:123], v[224:227], v[216:219]\n\t" \
    "ds_read_b128 v[236:239], v253 offset:11264\n\t" \
    "s_waitcnt lgkmcnt(2)\n\t" \
    "v_mfma_f32_16x16x32_bf16 v[216:219], v[124:127], v[228:231], v[216:219]\n\t" \
    "ds_read_b128 v[240:243], v253 offset:12288\n\t" \
    "v_mfma_f32_16x16x32_bf16 v[220:223], v[128:131], v[224:227], v[220:223]\n\t" \
    "v_mfma_f32_16x16x32_bf16 v[220:223], v[132:135], v[228:231], v[220:223]\n\t" \
    "v_mfma_f32_16x16x32_bf16 v[212:215], v[244:247], v[224:227], v[212:215]\n\t" \
    "ds_read_b128 v[244:247], v254 offset:63488\n\t" \
    "v_mfma_f32_16x16x32_bf16 v[212:215], v[248:251], v[228:231], v[212:215]\n\t" \
    "ds_read_b128 v[248:251], v254 offset:64512\n\t" \
    "v_and_b32 v252, 0xf0, v253\n\t" \
    "v_lshlrev_b32 v252, 3, v252\n\t" \
    "buffer_load_dword v252, v252, s[60:63], %[fl] offen sc1\n\t" \
    "s_waitcnt lgkmcnt(4)\n\t" \
    "v_mfma_f32_16x16x32_bf16 v[204:207], v[144:147], v[232:235], v[204:207]\n\t" \
    "ds_read_b128 v[224:227], v253 offset:13312\n\t" \
    "s_waitcnt lgkmcnt(4)\n\t" \
    "v_mfma_f32_16x16x32_bf16 v[204:207], v[148:151], v[236:239], v[204:207]\n\t" \
    "ds_read_b128 v[228:231], v253 offset:32768\n\t" \
    "v_mfma_f32_16x16x32_bf16 v[208:211], v[152:155], v[232:235], v[208:211]\n\t" \
    "v_mfma_f32_16x16x32_bf16 v[208:211], v[156:159], v[236:239], v[208:211]\n\t" \
    "v_mfma_f32_16x16x32_bf16 v[200:203], v[136:139], v[232:235], v[200:203]\n\t" \
    "v_mfma_f32_16x16x32_bf16 v[200:203], v[140:143], v[236:239], v[200:203]\n\t" \
    "s_waitcnt lgkmcnt(4)\n\t" \
    "v_mfma_f32_16x16x32_bf16 v[204:207], v[160:163], v[240:243], v[204:207]\n\t" \
    "ds_read_b128 v[232:235], v253 offset:33792\n\t" \
    "s_waitcnt lgkmcnt(2)\n\t" \
    "v_mfma_f32_16x16x32_bf16 v[204:207], v[164:167], v[224:227], v[204:207]\n\t" \
    "ds_read_b128 v[236:239], v253 offset:14336\n\t" \
    "v_mfma_f32_16x16x32_bf16 v[208:211], v[168:171], v[240:243], v[208:211]\n\t" \
    "v_mfma_f32_16x16x32_bf16 v[208:211], v[172:175], v[224:227], v[208:211]\n\t" \
    "v_mfma_f32_16x16x32_bf16 v[200:203], v[244:247], v[240:243], v[200:203]\n\t" \
    "v_mfma_f32_16x16x32_bf16 v[200:203], v[248:251], v[224:227], v[200:203]\n\t" \
    "s_waitcnt lgkmcnt(2)\n\t" \
    "v_mfma_f32_16x16x32_bf16 v[216:219], v[160:163], v[228:231], v[216:219]\n\t" \
    "ds_read_b128 v[240:243], v253 offset:15360\n\t" \
    "s_waitcnt lgkmcnt(2)\n\t" \
    "v_mfma_f32_16x16x32_bf16 v[216:219], v[164:167], v[232:235], v[216:219]\n\t" \
    "ds_read_b128 v[224:227], v253 offset:38912\n\t" \
    "v_mfma_f32_16x16x32_bf16 v[220:223], v[168:171], v[228:231], v[220:223]\n\t" \
    "v_mfma_f32_16x16x32_bf16 v[220:223], v[172:175], v[232:235], v[220:223]\n\t" \
    "v_mfma_f32_16x16x32_bf16 v[212:215], v[244:247], v[228:231], v[212:215]\n\t" \
    "v_mfma_f32_16x16x32_bf16 v[212:215], v[248:251], v[232:235], v[212:215]\n\t" \
    "s_waitcnt lgkmcnt(2)\n\t" \
    "v_mfma_f32_16x16x32_bf16 v[204:207], v[184:187], v[236:239], v[204:207]\n\t" \
    "ds_read_b128 v[228:231], v253 offset:39936\n\t" \
    "s_waitcnt lgkmcnt(2)\n\t" \
    "v_mfma_f32_16x16x32_bf16 v[204:207], v[188:191], v[240:243], v[204:207]\n\t" \
    "ds_read_b128 v[232:235], v253 offset:22528\n\t" \
    "v_mfma_f32_16x16x32_bf16 v[208:211], v[192:195], v[236:239], v[208:211]\n\t" \
    "v_mfma_f32_16x16x32_bf16 v[208:211], v[196:199], v[240:243], v[208:211]\n\t" \
    "v_mfma_f32_16x16x32_bf16 v[200:203], v[176:179], v[236:239], v[200:203]\n\t" \
    "v_mfma_f32_16x16x32_bf16 v[200:203], v[180:183], v[240:243], v[200:203]\n\t" \
    "s_waitcnt lgkmcnt(2)\n\t" \
    "v_mfma_f32_16x16x32_bf16 v[216:219], v[24:27], v[224:227], v[216:219]\n\t" \
    "ds_read_b128 v[236:239], v253 offset:23552\n\t" \
    "s_waitcnt lgkmcnt(2)\n\t" \
    "v_mfma_f32_16x16x32_bf16 v[216:219], v[28:31], v[228:231], v[216:219]\n\t" \
    "ds_read_b128 v[240:243], v253 offset:26624\n\t" \
    "v_mfma_f32_16x16x32_bf16 v[220:223], v[32:35], v[224:227], v[220:223]\n\t" \
    "v_cvt_pk_bf16_f32 v204, v204, v205\n\t" \
    "v_mfma_f32_16x16x32_bf16 v[220:223], v[36:39], v[228:231], v[220:223]\n\t" \
    "v_cvt_pk_bf16_f32 v205, v206, v207\n\t" \
    "v_mfma_f32_16x16x32_bf16 v[212:215], v[16:19], v[224:227], v[212:215]\n\t" \
    "ds_write_b64 v254, v[204:205] offset:4104\n\t" \
    "v_mfma_f32_16x16x32_bf16 v[212:215], v[20:23], v[228:231], v[212:215]\n\t" \
    "v_cvt_pk_bf16_f32 v208, v208, v209\n\t" \
    "s_waitcnt lgkmcnt(3)\n\t" \
    "v_mfma_f32_16x16x32_bf16 v[216:219], v[64:67], v[232:235], v[216:219]\n\t" \
    "ds_read_b128 v[224:227], v253 offset:27648\n\t" \
    "v_cvt_pk_bf16_f32 v209, v210, v211\n\t" \
    "s_waitcnt lgkmcnt(3)\n\t" \
    "v_mfma_f32_16x16x32_bf16 v[216:219], v[68:71], v[236:239], v[216:219]\n\t" \
    "ds_read_b128 v[228:231], v253 offset:30720\n\t" \
    "ds_write_b64 v254, v[208:209] offset:5128\n\t" \
    "v_mfma_f32_16x16x32_bf16 v[220:223], v[72:75], v[232:235], v[220:223]\n\t" \
    "v_cvt_pk_bf16_f32 v200, v200, v201\n\t" \
    "v_mfma_f32_16x16x32_bf16 v[220:223], v[76:79], v[236:239], v[220:223]\n\t" \
    "v_cvt_pk_bf16_f32 v201, v202, v203\n\t" \
    "v_mfma_f32_16x16x32_bf16 v[212:215], v[56:59], v[232:235], v[212:215]\n\t" \
    "ds_write_b64 v254, v[200:201] offset:3080\n\t" \
    "v_mfma_f32_16x16x32_bf16 v[212:215], v[60:63], v[236:239], v[212:215]\n\t" \
    "s_waitcnt lgkmcnt(5)\n\t" \
    "v_mfma_f32_16x16x32_bf16 v[216:219], v[104:107], v[240:243], v[216:219]\n\t" \
    "ds_read_b128 v[232:235], v253 offset:31744\n\t" \
    "s_waitcnt lgkmcnt(4)\n\t" \
    "v_mfma_f32_16x16x32_bf16 v[216:219], v[108:111], v[224:227], v[216:219]\n\t" \
    "ds_read_b128 v[236:239], v253 offset:34816\n\t" \
    "v_mfma_f32_16x16x32_bf16 v[220:223], v[112:115], v[240:243], v[220:223]\n\t" \
    "v_mfma_f32_16x16x32_bf16 v[220:223], v[116:119], v[224:227], v[220:223]\n\t" \
    "v_mfma_f32_16x16x32_bf16 v[212:215], v[96:99], v[240:243], v[212:215]\n\t" \
    "v_mfma_f32_16x16x32_bf16 v[212:215], v[100:103], v[224:227], v[212:215]\n\t" \
    "ds_read_b128 v[240:243], v253 offset:35840\n\t" \
    "s_waitcnt lgkmcnt(3)\n\t" \
    "s_barrier \n\t" \
    "v_mfma_f32_16x16x32_bf16 v[216:219], v[144:147], v[228:231], v[216:219]\n\t" \
    "s_waitcnt lgkmcnt(2)\n\t" \
    "v_mfma_f32_16x16x32_bf16 v[216:219], v[148:151], v[232:235], v[216:219]\n\t" \
    "s_waitcnt lgkmcnt(1)\n\t" \
    "v_mfma_f32_16x16x32_bf16 v[216:219], v[184:187], v[236:239], v[216:219]\n\t" \
    "s_waitcnt lgkmcnt(0)\n\t" \
    "v_mfma_f32_16x16x32_bf16 v[216:219], v[188:191], v[240:243], v[216:219]\n\t" \
    "v_mfma_f32_16x16x32_bf16 v[220:223], v[152:155], v[228:231], v[220:223]\n\t" \
    "v_mfma_f32_16x16x32_bf16 v[220:223], v[156:159], v[232:235], v[220:223]\n\t" \
    "v_mfma_f32_16x16x32_bf16 v[220:223], v[192:195], v[236:239], v[220:223]\n\t" \
    "v_cvt_pk_bf16_f32 v216, v216, v217\n\t" \
    "v_cvt_pk_bf16_f32 v217, v218, v219\n\t" \
    "v_mfma_f32_16x16x32_bf16 v[220:223], v[196:199], v[240:243], v[220:223]\n\t" \
    "ds_write_b64 v254, v[216:217] offset:10248\n\t" \
    "v_mfma_f32_16x16x32_bf16 v[212:215], v[136:139], v[228:231], v[212:215]\n\t" \
    "v_mfma_f32_16x16x32_bf16 v[212:215], v[140:143], v[232:235], v[212:215]\n\t" \
    "v_mfma_f32_16x16x32_bf16 v[212:215], v[176:179], v[236:239], v[212:215]\n\t" \
    "v_cvt_pk_bf16_f32 v220, v220, v221\n\t" \
    "v_cvt_pk_bf16_f32 v221, v222, v223\n\t" \
    "v_mfma_f32_16x16x32_bf16 v[212:215], v[180:183], v[240:243], v[212:215]\n\t" \
    "ds_write_b64 v254, v[220:221] offset:11272\n\t" \
    "s_nop 6\n\t" \
    "v_cvt_pk_bf16_f32 v212, v212, v213\n\t" \
    "v_cvt_pk_bf16_f32 v213, v214, v215\n\t" \
    "ds_write_b64 v254, v[212:213] offset:9224\n\t" \
    "s_waitcnt vmcnt(0) lgkmcnt(0)\n\t" \
    "s_barrier \n\t" \
    "s_setprio 2\n\t" \
    "s_add_u32 s44, s40, 1\n\t" \
    "s_min_u32 s44, s44, 63\n\t" \
    ".Lv38_%=:\n\t" \
    "v_cmp_ge_u32 vcc, s44, v252\n\t" \
    "s_cbranch_vccz .Lv39_%=\n\t" \
    "s_cmp_eq_u32 s41, 0\n\t" \
    "s_cbranch_scc1 .Lv39_%=\n\t" \
    "s_sub_u32 s41, s41, 1\n\t" \
    "s_sleep 2\n\t" \
    "v_and_b32 v252, 0xf0, v253\n\t" \
    "v_lshlrev_b32 v252, 3, v252\n\t" \
    "buffer_load_dword v252, v252, s[60:63], %[fl] offen sc1\n\t" \
    "s_waitcnt vmcnt(0)\n\t" \
    "s_branch .Lv38_%=\n\t" \
    ".Lv39_%=:\n\t" \
    "s_mov_b32 m0, 0x3000\n\t" \
    "s_add_u32 s49, s42, 0x0\n\t" \
    "buffer_load_dwordx4 v253, s[60:63], s49 offen offset:0 sc1 lds\n\t" \
    "s_sleep 1\n\t" \
    "buffer_load_dwordx4 v253, s[60:63], s49 offen offset:1024 sc1 lds\n\t" \
    "s_sleep 1\n\t" \
    "buffer_load_dwordx4 v253, s[60:63], s49 offen offset:2048 sc1 lds\n\t" \
    "s_sleep 1\n\t" \
    "buffer_load_dwordx4 v253, s[60:63], s49 offen offset:3072 sc1 lds\n\t" \
    "s_sleep 1\n\t" \
    "s_mov_b32 m0, 0x4c00\n\t" \
    "s_add_u32 s49, s42, 0x4c00\n\t" \
    "buffer_load_dwordx4 v253, s[60:63], s49 offen offset:0 sc1 lds\n\t" \
    "s_sleep 1\n\t" \
    "s_mov_b32 m0, 0x8000\n\t" \
    "s_add_u32 s49, s42, 0x1000\n\t" \
    "buffer_load_dwordx4 v253, s[60:63], s49 offen offset:0 sc1 lds\n\t" \
    "s_sleep 1\n\t" \
    "buffer_load_dwordx4 v253, s[60:63], s49 offen offset:1024 sc1 lds\n\t" \
    "s_sleep 1\n\t" \
    "buffer_load_dwordx4 v253, s[60:63], s49 offen offset:2048 sc1 lds\n\t" \
    "s_sleep 1\n\t" \
    "buffer_load_dwordx4 v253, s[60:63], s49 offen offset:3072 sc1 lds\n\t" \
    "s_sleep 1\n\t" \
    "s_mov_b32 m0, 0x9c00\n\t" \
    "s_add_u32 s49, s42, 0x5c00\n\t" \
    "buffer_load_dwordx4 v253, s[60:63], s49 offen offset:0 sc1 lds\n\t" \
    "s_sleep 1\n\t" \
    "ds_read_b128 v[200:203], v254 offset:55296\n\t" \
    "ds_read_b128 v[204:207], v254 offset:56320\n\t" \
    "ds_read_b128 v[216:219], v254 offset:57344\n\t" \
    "ds_read_b128 v[220:223], v254 offset:58368\n\t" \
    "v_mov_b64 v[208:209], 0\n\t" \
    "v_mov_b64 v[210:211], 0\n\t" \
    "v_mov_b64 v[244:245], 0\n\t" \
    "v_mov_b64 v[246:247], 0\n\t" \
    "v_mov_b64 v[248:249], 0\n\t" \
    "v_mov_b64 v[250:251], 0\n\t" \
    "s_sleep 5\n\t" \
    "s_mov_b32 m0, 0xc000\n\t" \
    "s_nop 0\n\t" \
    "buffer_load_dwordx4 v253, s[60:63], s46 offen sc1 lds\n\t" \
    "s_mov_b32 m0, 0xc400\n\t" \
    "s_nop 0\n\t" \
    "buffer_load_dwordx4 v253, s[60:63], s47 offen sc1 lds\n\t" \
    "s_waitcnt lgkmcnt(0)\n\t" \
    "v_bfe_i32 v212, v253, 5, 1\n\t" \
    "v_and_b32 v212, 0xaaaa, v212\n\t" \
    "v_or_b32 v212, 0x4444, v212\n\t" \
    "v_and_b32 v213, 0xffff, v255\n\t" \
    "v_lshrrev_b32 v214, 16, v255\n\t" \
    ".Lv310_%=:\n\t" \
    "s_waitcnt vmcnt(0)\n\t" \
    "ds_read_b128 v[224:227], v253 offset:49152\n\t" \
    "ds_read_b128 v[228:231], v253 offset:50176\n\t" \
    "s_waitcnt lgkmcnt(1)\n\t" \
    "v_max_u32 v215, v224, v225\n\t" \
    "v_max3_u32 v215, v226, v227, v215\n\t" \
    "s_waitcnt lgkmcnt(0)\n\t" \
    "v_max3_u32 v215, v228, v229, v215\n\t" \
    "v_max3_u32 v215, v230, v231, v215\n\t" \
    "v_cmp_eq_u32 vcc, -1, v215\n\t" \
    "s_cbranch_vccz .Lv311_%=\n\t" \
    "v_max_u32 v215, v224, v225\n\t" \
    "v_max3_u32 v215, v226, v227, v215\n\t" \
    "v_cmp_eq_u32 vcc, -1, v215\n\t" \
    "s_cbranch_vccz .Lv312_%=\n\t" \
    "s_mov_b32 m0, 0xc000\n\t" \
    "s_nop 0\n\t" \
    "buffer_load_dwordx4 v253, s[60:63], s46 offen sc1 lds\n\t" \
    ".Lv312_%=:\n\t" \
    "v_max_u32 v215, v228, v229\n\t" \
    "v_max3_u32 v215, v230, v231, v215\n\t" \
    "v_cmp_eq_u32 vcc, -1, v215\n\t" \
    "s_cbranch_vccz .Lv313_%=\n\t" \
    "s_mov_b32 m0, 0xc400\n\t" \
    "s_nop 0\n\t" \
    "buffer_load_dwordx4 v253, s[60:63], s47 offen sc1 lds\n\t" \
    ".Lv313_%=:\n\t" \
    "s_cmp_eq_u32 s41, 0\n\t" \
    "s_cbranch_scc1 .Lv311_%=\n\t" \
    "s_sub_u32 s41, s41, 1\n\t" \
    "s_branch .Lv310_%=\n\t" \
    ".Lv311_%=:\n\t" \
    "s_waitcnt lgkmcnt(0)\n\t" \
    "s_barrier \n\t" \
    "ds_read_b128 v[232:235], v253 offset:40960\n\t" \
    "ds_read_b128 v[236:239], v253 offset:41984\n\t" \
    "ds_read_b128 v[240:243], v253 offset:43008\n\t" \
    "v_smfmac_f32_16x16x64_bf16 v[244:247], v[224:227], v[0:7], v212\n\t" \
    "v_smfmac_f32_16x16x64_bf16 v[248:251], v[224:227], v[8:15], v212\n\t" \
    "v_smfmac_f32_16x16x64_bf16 v[208:211], v[224:227], v[200:207], v212\n\t" \
    "ds_read_b128 v[200:203], v254 offset:59392\n\t" \
    "ds_read_b128 v[204:207], v254 offset:60416\n\t" \
    "ds_read_b128 v[224:227], v253 offset:44032\n\t" \
    "v_smfmac_f32_16x16x64_bf16 v[208:211], v[228:231], v[16:23], v212\n\t" \
    "v_smfmac_f32_16x16x64_bf16 v[244:247], v[228:231], v[24:31], v212\n\t" \
    "v_smfmac_f32_16x16x64_bf16 v[248:251], v[228:231], v[32:39], v212\n\t" \
    "ds_read_b128 v[228:231], v253 offset:45056\n\t" \
    "s_waitcnt lgkmcnt(6)\n\t" \
    "v_smfmac_f32_16x16x64_bf16 v[208:211], v[232:235], v[216:223], v212\n\t" \
    "ds_read_b128 v[216:219], v254 offset:61440\n\t" \
    "ds_read_b128 v[220:223], v254 offset:62464\n\t" \
    "v_smfmac_f32_16x16x64_bf16 v[244:247], v[232:235], v[40:47], v212\n\t" \
    "v_smfmac_f32_16x16x64_bf16 v[248:251], v[232:235], v[48:55], v212\n\t" \
    "ds_read_b128 v[232:235], v253 offset:46080\n\t" \
    "s_waitcnt lgkmcnt(8)\n\t" \
    "v_smfmac_f32_16x16x64_bf16 v[208:211], v[236:239], v[56:63], v212\n\t" \
    "v_smfmac_f32_16x16x64_bf16 v[244:247], v[236:239], v[64:71], v212\n\t" \
    "v_smfmac_f32_16x16x64_bf16 v[248:251], v[236:239], v[72:79], v212\n\t" \
    "ds_read_b128 v[236:239], v253 offset:47104\n\t" \
    "s_waitcnt lgkmcnt(6)\n\t" \
    "v_smfmac_f32_16x16x64_bf16 v[208:211], v[240:243], v[200:207], v212\n\t" \
    "ds_read_b128 v[200:203], v254 offset:63488\n\t" \
    "ds_read_b128 v[204:207], v254 offset:64512\n\t" \
    "v_smfmac_f32_16x16x64_bf16 v[244:247], v[240:243], v[80:87], v212\n\t" \
    "v_smfmac_f32_16x16x64_bf16 v[248:251], v[240:243], v[88:95], v212\n\t" \
    "ds_read_b128 v[240:243], v253 offset:48128\n\t" \
    "s_waitcnt lgkmcnt(8)\n\t" \
    "v_smfmac_f32_16x16x64_bf16 v[208:211], v[224:227], v[96:103], v212\n\t" \
    "v_smfmac_f32_16x16x64_bf16 v[244:247], v[224:227], v[104:111], v212\n\t" \
    "v_smfmac_f32_16x16x64_bf16 v[248:251], v[224:227], v[112:119], v212\n\t" \
    "s_waitcnt lgkmcnt(7)\n\t" \
    "v_smfmac_f32_16x16x64_bf16 v[244:247], v[228:231], v[120:127], v212\n\t" \
    "s_waitcnt lgkmcnt(4)\n\t" \
    "v_smfmac_f32_16x16x64_bf16 v[244:247], v[232:235], v[144:151], v212\n\t" \
    "s_waitcnt lgkmcnt(3)\n\t" \
    "v_smfmac_f32_16x16x64_bf16 v[244:247], v[236:239], v[160:167], v212\n\t" \
    "s_waitcnt lgkmcnt(0)\n\t" \
    "v_smfmac_f32_16x16x64_bf16 v[244:247], v[240:243], v[184:191], v212\n\t" \
    "v_smfmac_f32_16x16x64_bf16 v[248:251], v[228:231], v[128:135], v212\n\t" \
    "v_smfmac_f32_16x16x64_bf16 v[248:251], v[232:235], v[152:159], v212\n\t" \
    "v_smfmac_f32_16x16x64_bf16 v[248:251], v[236:239], v[168:175], v212\n\t" \
    "v_add_f32 v244, v244, v246\n\t" \
    "v_add_f32 v245, v245, v247\n\t" \
    "v_cvt_pk_bf16_f32 v244, v244, v245\n\t" \
    "v_smfmac_f32_16x16x64_bf16 v[248:251], v[240:243], v[192:199], v212\n\t" \
    "ds_write_b16 v213, v244 offset:512\n\t" \
    "ds_write_b16_d16_hi v213, v244 offset:528\n\t" \
    "v_smfmac_f32_16x16x64_bf16 v[208:211], v[228:231], v[216:223], v212\n\t" \
    "v_smfmac_f32_16x16x64_bf16 v[208:211], v[232:235], v[136:143], v212\n\t" \
    "v_smfmac_f32_16x16x64_bf16 v[208:211], v[236:239], v[200:207], v212\n\t" \
    "v_add_f32 v248, v248, v250\n\t" \
    "v_add_f32 v249, v249, v251\n\t" \
    "v_cvt_pk_bf16_f32 v248, v248, v249\n\t" \
    "v_smfmac_f32_16x16x64_bf16 v[208:211], v[240:243], v[176:183], v212\n\t" \
    "ds_write_b16 v213, v248 offset:1024\n\t" \
    "ds_write_b16_d16_hi v213, v248 offset:1040\n\t" \
    "s_nop 5\n\t" \
    "v_add_f32 v208, v208, v210\n\t" \
    "v_add_f32 v209, v209, v211\n\t" \
    "v_cvt_pk_bf16_f32 v208, v208, v209\n\t" \
    "ds_write_b16 v213, v208 offset:0\n\t" \
    "ds_write_b16_d16_hi v213, v208 offset:16\n\t" \
    "s_setprio 0\n\t" \
    "s_waitcnt lgkmcnt(0)\n\t" \
    "s_barrier \n\t" \
    "ds_read_b128 v[224:227], v253 offset:16384\n\t" \
    "ds_read_b128 v[244:247], v254 offset:55296\n\t" \
    "ds_read_b128 v[228:231], v253 offset:17408\n\t" \
    "ds_read_b128 v[232:235], v253 offset:36864\n\t" \
    "ds_read_b128 v[236:239], v253 offset:37888\n\t" \
    "ds_read_b128 v[248:251], v254 offset:56320\n\t" \
    "s_add_u32 s40, s40, 1\n\t" \
    "s_cmp_lg_u32 s40, 64\n\t" \
    "s_cbranch_scc1 .Lv37_%=\n\t" \
    "s_branch .Lend%=\n\t" \
    ".Lv2_%=:\n\t" \
    "s_mov_b32 s42, %[xs]\n\t" \
    "v_add_u32 v244, 0xc000, v243\n\t" \
    "s_waitcnt vmcnt(12) lgkmcnt(0)\n\t" \
    "s_barrier \n\t" \
    "s_add_u32 m0, %[wm], 0x9000\n\t" \
    "s_mov_b32 s44, 0x1e0000\n\t" \
    "buffer_load_dwordx4 v240, %[wr], s44 offen offset:0 nt lds\n\t" \
    "buffer_load_dwordx4 v241, %[wr], s44 offen offset:1024 nt lds\n\t" \
    "buffer_load_dwordx4 v242, %[wr], s44 offen offset:2048 nt lds\n\t" \
    "ds_read_b64_tr_b16 v[208:209], v244 offset:0\n\t" \
    "ds_read_b64_tr_b16 v[210:211], v244 offset:1536\n\t" \
    "ds_read_b64_tr_b16 v[40:41], v244 offset:32\n\t" \
    "ds_read_b64_tr_b16 v[42:43], v244 offset:1568\n\t" \
    "ds_read_b64_tr_b16 v[48:49], v244 offset:64\n\t" \
    "ds_read_b64_tr_b16 v[50:51], v244 offset:1600\n\t" \
    "v_add_u32 v244, 0xf000, v243\n\t" \
    "s_waitcnt vmcnt(12) lgkmcnt(0)\n\t" \
    "s_barrier \n\t" \
    "s_add_u32 m0, %[wm], 0xc000\n\t" \
    "s_mov_b32 s44, 0x240000\n\t" \
    "buffer_load_dwordx4 v240, %[wr], s44 offen offset:0 nt lds\n\t" \
    "buffer_load_dwordx4 v241, %[wr], s44 offen offset:1024 nt lds\n\t" \
    "buffer_load_dwordx4 v242, %[wr], s44 offen offset:2048 nt lds\n\t" \
    "ds_read_b64_tr_b16 v[212:213], v244 offset:0\n\t" \
    "ds_read_b64_tr_b16 v[214:215], v244 offset:1536\n\t" \
    "ds_read_b64_tr_b16 v[44:45], v244 offset:32\n\t" \
    "ds_read_b64_tr_b16 v[46:47], v244 offset:1568\n\t" \
    "ds_read_b64_tr_b16 v[52:53], v244 offset:64\n\t" \
    "ds_read_b64_tr_b16 v[54:55], v244 offset:1600\n\t" \
    "v_add_u32 v244, 0x0, v243\n\t" \
    "s_waitcnt vmcnt(12) lgkmcnt(0)\n\t" \
    "s_barrier \n\t" \
    "s_add_u32 m0, %[wm], 0xf000\n\t" \
    "s_mov_b32 s44, 0x2a0000\n\t" \
    "buffer_load_dwordx4 v240, %[wr], s44 offen offset:0 nt lds\n\t" \
    "buffer_load_dwordx4 v241, %[wr], s44 offen offset:1024 nt lds\n\t" \
    "buffer_load_dwordx4 v242, %[wr], s44 offen offset:2048 nt lds\n\t" \
    "ds_read_b64_tr_b16 v[56:57], v244 offset:0\n\t" \
    "ds_read_b64_tr_b16 v[58:59], v244 offset:1536\n\t" \
    "ds_read_b64_tr_b16 v[64:65], v244 offset:32\n\t" \
    "ds_read_b64_tr_b16 v[66:67], v244 offset:1568\n\t" \
    "ds_read_b64_tr_b16 v[72:73], v244 offset:64\n\t" \
    "ds_read_b64_tr_b16 v[74:75], v244 offset:1600\n\t" \
    "v_add_u32 v244, 0x3000, v243\n\t" \
    "s_waitcnt vmcnt(12) lgkmcnt(0)\n\t" \
    "s_barrier \n\t" \
    "s_add_u32 m0, %[wm], 0x0\n\t" \
    "s_mov_b32 s44, 0x300000\n\t" \
    "buffer_load_dwordx4 v240, %[wr], s44 offen offset:0 nt lds\n\t" \
    "buffer_load_dwordx4 v241, %[wr], s44 offen offset:1024 nt lds\n\t" \
    "buffer_load_dwordx4 v242, %[wr], s44 offen offset:2048 nt lds\n\t" \
    "ds_read_b64_tr_b16 v[60:61], v244 offset:0\n\t" \
    "ds_read_b64_tr_b16 v[62:63], v244 offset:1536\n\t" \
    "ds_read_b64_tr_b16 v[68:69], v244 offset:32\n\t" \
    "ds_read_b64_tr_b16 v[70:71], v244 offset:1568\n\t" \
    "ds_read_b64_tr_b16 v[76:77], v244 offset:64\n\t" \
    "ds_read_b64_tr_b16 v[78:79], v244 offset:1600\n\t" \
    "v_add_u32 v244, 0x6000, v243\n\t" \
    "s_waitcnt vmcnt(12) lgkmcnt(0)\n\t" \
    "s_barrier \n\t" \
    "s_add_u32 m0, %[wm], 0x3000\n\t" \
    "s_mov_b32 s44, 0x360000\n\t" \
    "buffer_load_dwordx4 v240, %[wr], s44 offen offset:0 nt lds\n\t" \
    "buffer_load_dwordx4 v241, %[wr], s44 offen offset:1024 nt lds\n\t" \
    "buffer_load_dwordx4 v242, %[wr], s44 offen offset:2048 nt lds\n\t" \
    "ds_read_b64_tr_b16 v[216:217], v244 offset:0\n\t" \
    "ds_read_b64_tr_b16 v[218:219], v244 offset:1536\n\t" \
    "ds_read_b64_tr_b16 v[80:81], v244 offset:32\n\t" \
    "ds_read_b64_tr_b16 v[82:83], v244 offset:1568\n\t" \
    "ds_read_b64_tr_b16 v[88:89], v244 offset:64\n\t" \
    "ds_read_b64_tr_b16 v[90:91], v244 offset:1600\n\t" \
    "v_add_u32 v244, 0x9000, v243\n\t" \
    "s_waitcnt vmcnt(12) lgkmcnt(0)\n\t" \
    "s_barrier \n\t" \
    "s_add_u32 m0, %[wm], 0x6000\n\t" \
    "s_mov_b32 s44, 0x3c0000\n\t" \
    "buffer_load_dwordx4 v240, %[wr], s44 offen offset:0 nt lds\n\t" \
    "buffer_load_dwordx4 v241, %[wr], s44 offen offset:1024 nt lds\n\t" \
    "buffer_load_dwordx4 v242, %[wr], s44 offen offset:2048 nt lds\n\t" \
    "ds_read_b64_tr_b16 v[220:221], v244 offset:0\n\t" \
    "ds_read_b64_tr_b16 v[222:223], v244 offset:1536\n\t" \
    "ds_read_b64_tr_b16 v[84:85], v244 offset:32\n\t" \
    "ds_read_b64_tr_b16 v[86:87], v244 offset:1568\n\t" \
    "ds_read_b64_tr_b16 v[92:93], v244 offset:64\n\t" \
    "ds_read_b64_tr_b16 v[94:95], v244 offset:1600\n\t" \
    "v_add_u32 v244, 0xc000, v243\n\t" \
    "s_waitcnt vmcnt(12) lgkmcnt(0)\n\t" \
    "s_barrier \n\t" \
    "s_add_u32 m0, %[wm], 0x9000\n\t" \
    "s_mov_b32 s44, 0x420000\n\t" \
    "buffer_load_dwordx4 v240, %[wr], s44 offen offset:0 nt lds\n\t" \
    "buffer_load_dwordx4 v241, %[wr], s44 offen offset:1024 nt lds\n\t" \
    "buffer_load_dwordx4 v242, %[wr], s44 offen offset:2048 nt lds\n\t" \
    "ds_read_b64_tr_b16 v[96:97], v244 offset:0\n\t" \
    "ds_read_b64_tr_b16 v[98:99], v244 offset:1536\n\t" \
    "ds_read_b64_tr_b16 v[104:105], v244 offset:32\n\t" \
    "ds_read_b64_tr_b16 v[106:107], v244 offset:1568\n\t" \
    "ds_read_b64_tr_b16 v[112:113], v244 offset:64\n\t" \
    "ds_read_b64_tr_b16 v[114:115], v244 offset:1600\n\t" \
    "v_add_u32 v244, 0xf000, v243\n\t" \
    "s_waitcnt vmcnt(12) lgkmcnt(0)\n\t" \
    "s_barrier \n\t" \
    "s_add_u32 m0, %[wm], 0xc000\n\t" \
    "s_mov_b32 s44, 0x480000\n\t" \
    "buffer_load_dwordx4 v240, %[wr], s44 offen offset:0 nt lds\n\t" \
    "buffer_load_dwordx4 v241, %[wr], s44 offen offset:1024 nt lds\n\t" \
    "buffer_load_dwordx4 v242, %[wr], s44 offen offset:2048 nt lds\n\t" \
    "ds_read_b64_tr_b16 v[100:101], v244 offset:0\n\t" \
    "ds_read_b64_tr_b16 v[102:103], v244 offset:1536\n\t" \
    "ds_read_b64_tr_b16 v[108:109], v244 offset:32\n\t" \
    "ds_read_b64_tr_b16 v[110:111], v244 offset:1568\n\t" \
    "ds_read_b64_tr_b16 v[116:117], v244 offset:64\n\t" \
    "ds_read_b64_tr_b16 v[118:119], v244 offset:1600\n\t" \
    "v_add_u32 v244, 0x0, v243\n\t" \
    "s_waitcnt vmcnt(12) lgkmcnt(0)\n\t" \
    "s_barrier \n\t" \
    "v_mov_b32 v246, 0\n\t" \
    "buffer_load_dword v246, v246, s[60:63], %[mo] offen sc1\n\t" \
    "s_add_u32 m0, %[wm], 0xf000\n\t" \
    "s_mov_b32 s44, 0x4e0000\n\t" \
    "buffer_load_dwordx4 v240, %[wr], s44 offen offset:0 nt lds\n\t" \
    "buffer_load_dwordx4 v241, %[wr], s44 offen offset:1024 nt lds\n\t" \
    "buffer_load_dwordx4 v242, %[wr], s44 offen offset:2048 nt lds\n\t" \
    "ds_read_b64_tr_b16 v[200:201], v244 offset:0\n\t" \
    "ds_read_b64_tr_b16 v[202:203], v244 offset:1536\n\t" \
    "ds_read_b64_tr_b16 v[0:1], v244 offset:32\n\t" \
    "ds_read_b64_tr_b16 v[2:3], v244 offset:1568\n\t" \
    "ds_read_b64_tr_b16 v[8:9], v244 offset:64\n\t" \
    "ds_read_b64_tr_b16 v[10:11], v244 offset:1600\n\t" \
    "v_add_u32 v244, 0x3000, v243\n\t" \
    "s_waitcnt vmcnt(12) lgkmcnt(0)\n\t" \
    "s_barrier \n\t" \
    "s_add_u32 m0, %[wm], 0x0\n\t" \
    "s_mov_b32 s44, 0x540000\n\t" \
    "buffer_load_dwordx4 v240, %[wr], s44 offen offset:0 nt lds\n\t" \
    "buffer_load_dwordx4 v241, %[wr], s44 offen offset:1024 nt lds\n\t" \
    "buffer_load_dwordx4 v242, %[wr], s44 offen offset:2048 nt lds\n\t" \
    "ds_read_b64_tr_b16 v[204:205], v244 offset:0\n\t" \
    "ds_read_b64_tr_b16 v[206:207], v244 offset:1536\n\t" \
    "ds_read_b64_tr_b16 v[4:5], v244 offset:32\n\t" \
    "ds_read_b64_tr_b16 v[6:7], v244 offset:1568\n\t" \
    "ds_read_b64_tr_b16 v[12:13], v244 offset:64\n\t" \
    "ds_read_b64_tr_b16 v[14:15], v244 offset:1600\n\t" \
    "v_add_u32 v244, 0x6000, v243\n\t" \
    "s_waitcnt vmcnt(12) lgkmcnt(0)\n\t" \
    "s_barrier \n\t" \
    "s_add_u32 m0, %[wm], 0x3000\n\t" \
    "s_mov_b32 s44, 0x5a0000\n\t" \
    "buffer_load_dwordx4 v240, %[wr], s44 offen offset:0 nt lds\n\t" \
    "buffer_load_dwordx4 v241, %[wr], s44 offen offset:1024 nt lds\n\t" \
    "buffer_load_dwordx4 v242, %[wr], s44 offen offset:2048 nt lds\n\t" \
    "ds_read_b64_tr_b16 v[224:225], v244 offset:0\n\t" \
    "ds_read_b64_tr_b16 v[226:227], v244 offset:1536\n\t" \
    "ds_read_b64_tr_b16 v[120:121], v244 offset:32\n\t" \
    "ds_read_b64_tr_b16 v[122:123], v244 offset:1568\n\t" \
    "ds_read_b64_tr_b16 v[128:129], v244 offset:64\n\t" \
    "ds_read_b64_tr_b16 v[130:131], v244 offset:1600\n\t" \
    "v_add_u32 v244, 0x9000, v243\n\t" \
    "s_waitcnt vmcnt(12) lgkmcnt(0)\n\t" \
    "s_barrier \n\t" \
    "s_add_u32 m0, %[wm], 0x6000\n\t" \
    "s_mov_b32 s44, 0x600000\n\t" \
    "buffer_load_dwordx4 v240, %[wr], s44 offen offset:0 nt lds\n\t" \
    "buffer_load_dwordx4 v241, %[wr], s44 offen offset:1024 nt lds\n\t" \
    "buffer_load_dwordx4 v242, %[wr], s44 offen offset:2048 nt lds\n\t" \
    "ds_read_b64_tr_b16 v[228:229], v244 offset:0\n\t" \
    "ds_read_b64_tr_b16 v[230:231], v244 offset:1536\n\t" \
    "ds_read_b64_tr_b16 v[124:125], v244 offset:32\n\t" \
    "ds_read_b64_tr_b16 v[126:127], v244 offset:1568\n\t" \
    "ds_read_b64_tr_b16 v[132:133], v244 offset:64\n\t" \
    "ds_read_b64_tr_b16 v[134:135], v244 offset:1600\n\t" \
    "v_add_u32 v244, 0xc000, v243\n\t" \
    "s_waitcnt vmcnt(12) lgkmcnt(0)\n\t" \
    "s_barrier \n\t" \
    "s_add_u32 m0, %[wm], 0x9000\n\t" \
    "s_mov_b32 s44, 0x660000\n\t" \
    "buffer_load_dwordx4 v240, %[wr], s44 offen offset:0 nt lds\n\t" \
    "buffer_load_dwordx4 v241, %[wr], s44 offen offset:1024 nt lds\n\t" \
    "buffer_load_dwordx4 v242, %[wr], s44 offen offset:2048 nt lds\n\t" \
    "ds_read_b64_tr_b16 v[136:137], v244 offset:0\n\t" \
    "ds_read_b64_tr_b16 v[138:139], v244 offset:1536\n\t" \
    "ds_read_b64_tr_b16 v[144:145], v244 offset:32\n\t" \
    "ds_read_b64_tr_b16 v[146:147], v244 offset:1568\n\t" \
    "ds_read_b64_tr_b16 v[152:153], v244 offset:64\n\t" \
    "ds_read_b64_tr_b16 v[154:155], v244 offset:1600\n\t" \
    "v_add_u32 v244, 0xf000, v243\n\t" \
    "s_waitcnt vmcnt(12) lgkmcnt(0)\n\t" \
    "s_barrier \n\t" \
    ".Lv21_%=:\n\t" \
    "v_readfirstlane_b32 s43, v246\n\t" \
    "s_nop 0\n\t" \
    "s_cmp_lg_u32 s43, -1\n\t" \
    "s_cbranch_scc1 .Lv22_%=\n\t" \
    "s_cmp_eq_u32 s41, 0\n\t" \
    "s_cbranch_scc1 .Lv22_%=\n\t" \
    "s_sub_u32 s41, s41, 1\n\t" \
    "s_sleep 4\n\t" \
    "v_mov_b32 v246, 0\n\t" \
    "buffer_load_dword v246, v246, s[60:63], %[mo] offen sc1\n\t" \
    "s_waitcnt vmcnt(0)\n\t" \
    "s_branch .Lv21_%=\n\t" \
    ".Lv22_%=:\n\t" \
    "v_mov_b32 v246, 0x8000\n\t" \
    "s_mov_b64 exec, 1\n\t" \
    "buffer_store_dword v246, v246, s[60:63], %[mo] offen sc1\n\t" \
    "s_mov_b64 exec, -1\n\t" \
    "s_and_b32 s43, s43, 0x2000\n\t" \
    "s_xor_b32 s43, s43, %[lx]\n\t" \
    "s_add_u32 s60, s60, s43\n\t" \
    "s_addc_u32 s61, s61, 0\n\t" \
    "v_and_b32 v252, 0xf0, v253\n\t" \
    "v_lshlrev_b32 v252, 3, v252\n\t" \
    "buffer_load_dword v252, v252, s[60:63], %[fl] offen sc1\n\t" \
    "s_add_u32 m0, %[wm], 0xc000\n\t" \
    "s_mov_b32 s44, 0x6c0000\n\t" \
    "buffer_load_dwordx4 v240, %[wr], s44 offen offset:0 nt lds\n\t" \
    "buffer_load_dwordx4 v241, %[wr], s44 offen offset:1024 nt lds\n\t" \
    "buffer_load_dwordx4 v242, %[wr], s44 offen offset:2048 nt lds\n\t" \
    "ds_read_b64_tr_b16 v[140:141], v244 offset:0\n\t" \
    "ds_read_b64_tr_b16 v[142:143], v244 offset:1536\n\t" \
    "ds_read_b64_tr_b16 v[148:149], v244 offset:32\n\t" \
    "ds_read_b64_tr_b16 v[150:151], v244 offset:1568\n\t" \
    "ds_read_b64_tr_b16 v[156:157], v244 offset:64\n\t" \
    "ds_read_b64_tr_b16 v[158:159], v244 offset:1600\n\t" \
    "v_add_u32 v244, 0x0, v243\n\t" \
    "s_waitcnt vmcnt(12) lgkmcnt(0)\n\t" \
    "s_barrier \n\t" \
    "s_add_u32 m0, %[wm], 0xf000\n\t" \
    "s_mov_b32 s44, 0x720000\n\t" \
    "buffer_load_dwordx4 v240, %[wr], s44 offen offset:0 nt lds\n\t" \
    "buffer_load_dwordx4 v241, %[wr], s44 offen offset:1024 nt lds\n\t" \
    "buffer_load_dwordx4 v242, %[wr], s44 offen offset:2048 nt lds\n\t" \
    "ds_read_b64_tr_b16 v[16:17], v244 offset:0\n\t" \
    "ds_read_b64_tr_b16 v[18:19], v244 offset:1536\n\t" \
    "ds_read_b64_tr_b16 v[24:25], v244 offset:32\n\t" \
    "ds_read_b64_tr_b16 v[26:27], v244 offset:1568\n\t" \
    "ds_read_b64_tr_b16 v[32:33], v244 offset:64\n\t" \
    "ds_read_b64_tr_b16 v[34:35], v244 offset:1600\n\t" \
    "v_add_u32 v244, 0x3000, v243\n\t" \
    "s_waitcnt vmcnt(12) lgkmcnt(0)\n\t" \
    "s_barrier \n\t" \
    "ds_read_b64_tr_b16 v[20:21], v244 offset:0\n\t" \
    "ds_read_b64_tr_b16 v[22:23], v244 offset:1536\n\t" \
    "ds_read_b64_tr_b16 v[28:29], v244 offset:32\n\t" \
    "ds_read_b64_tr_b16 v[30:31], v244 offset:1568\n\t" \
    "ds_read_b64_tr_b16 v[36:37], v244 offset:64\n\t" \
    "ds_read_b64_tr_b16 v[38:39], v244 offset:1600\n\t" \
    "v_add_u32 v244, 0x6000, v243\n\t" \
    "s_waitcnt vmcnt(9) lgkmcnt(0)\n\t" \
    "s_barrier \n\t" \
    "ds_read_b64_tr_b16 v[232:233], v244 offset:0\n\t" \
    "ds_read_b64_tr_b16 v[234:235], v244 offset:1536\n\t" \
    "ds_read_b64_tr_b16 v[160:161], v244 offset:32\n\t" \
    "ds_read_b64_tr_b16 v[162:163], v244 offset:1568\n\t" \
    "ds_read_b64_tr_b16 v[168:169], v244 offset:64\n\t" \
    "ds_read_b64_tr_b16 v[170:171], v244 offset:1600\n\t" \
    "v_add_u32 v244, 0x9000, v243\n\t" \
    "s_waitcnt vmcnt(6) lgkmcnt(0)\n\t" \
    "s_barrier \n\t" \
    "ds_read_b64_tr_b16 v[236:237], v244 offset:0\n\t" \
    "ds_read_b64_tr_b16 v[238:239], v244 offset:1536\n\t" \
    "ds_read_b64_tr_b16 v[164:165], v244 offset:32\n\t" \
    "ds_read_b64_tr_b16 v[166:167], v244 offset:1568\n\t" \
    "ds_read_b64_tr_b16 v[172:173], v244 offset:64\n\t" \
    "ds_read_b64_tr_b16 v[174:175], v244 offset:1600\n\t" \
    "v_add_u32 v244, 0xc000, v243\n\t" \
    "s_waitcnt vmcnt(3) lgkmcnt(0)\n\t" \
    "s_barrier \n\t" \
    "ds_read_b64_tr_b16 v[176:177], v244 offset:0\n\t" \
    "ds_read_b64_tr_b16 v[178:179], v244 offset:1536\n\t" \
    "ds_read_b64_tr_b16 v[184:185], v244 offset:32\n\t" \
    "ds_read_b64_tr_b16 v[186:187], v244 offset:1568\n\t" \
    "ds_read_b64_tr_b16 v[192:193], v244 offset:64\n\t" \
    "ds_read_b64_tr_b16 v[194:195], v244 offset:1600\n\t" \
    "s_mov_b32 s43, 0\n\t" \
    "v_cmp_eq_u32 vcc, 0, v252\n\t" \
    "s_cbranch_vccnz .Lv23_%=\n\t" \
    "s_mov_b32 m0, 0x2000\n\t" \
    "s_add_u32 s49, s42, 0x0\n\t" \
    "buffer_load_dwordx4 v253, s[60:63], s49 offen offset:0 sc1 lds\n\t" \
    "buffer_load_dwordx4 v253, s[60:63], s49 offen offset:1024 sc1 lds\n\t" \
    "buffer_load_dwordx4 v253, s[60:63], s49 offen offset:2048 sc1 lds\n\t" \
    "buffer_load_dwordx4 v253, s[60:63], s49 offen offset:3072 sc1 lds\n\t" \
    "s_mov_b32 m0, 0x4800\n\t" \
    "s_add_u32 s49, s42, 0x8800\n\t" \
    "buffer_load_dwordx4 v253, s[60:63], s49 offen offset:0 sc1 lds\n\t" \
    "s_mov_b32 m0, 0x7000\n\t" \
    "s_add_u32 s49, s42, 0x1000\n\t" \
    "buffer_load_dwordx4 v253, s[60:63], s49 offen offset:0 sc1 lds\n\t" \
    "buffer_load_dwordx4 v253, s[60:63], s49 offen offset:1024 sc1 lds\n\t" \
    "buffer_load_dwordx4 v253, s[60:63], s49 offen offset:2048 sc1 lds\n\t" \
    "buffer_load_dwordx4 v253, s[60:63], s49 offen offset:3072 sc1 lds\n\t" \
    "s_mov_b32 m0, 0x9800\n\t" \
    "s_add_u32 s49, s42, 0x9800\n\t" \
    "buffer_load_dwordx4 v253, s[60:63], s49 offen offset:0 sc1 lds\n\t" \
    "s_mov_b32 s43, 1\n\t" \
    ".Lv23_%=:\n\t" \
    "v_add_u32 v244, 0xf000, v243\n\t" \
    "s_waitcnt vmcnt(0) lgkmcnt(0)\n\t" \
    "s_barrier \n\t" \
    "ds_read_b64_tr_b16 v[180:181], v244 offset:0\n\t" \
    "ds_read_b64_tr_b16 v[182:183], v244 offset:1536\n\t" \
    "ds_read_b64_tr_b16 v[188:189], v244 offset:32\n\t" \
    "ds_read_b64_tr_b16 v[190:191], v244 offset:1568\n\t" \
    "ds_read_b64_tr_b16 v[196:197], v244 offset:64\n\t" \
    "ds_read_b64_tr_b16 v[198:199], v244 offset:1600\n\t" \
    "s_waitcnt lgkmcnt(0)\n\t" \
    "s_barrier \n\t" \
    "ds_write_b128 v254, v[200:203] offset:51200\n\t" \
    "ds_write_b128 v254, v[204:207] offset:52224\n\t" \
    "ds_write_b128 v254, v[208:211] offset:53248\n\t" \
    "ds_write_b128 v254, v[212:215] offset:54272\n\t" \
    "ds_write_b128 v254, v[216:219] offset:55296\n\t" \
    "ds_write_b128 v254, v[220:223] offset:56320\n\t" \
    "ds_write_b128 v254, v[224:227] offset:57344\n\t" \
    "ds_write_b128 v254, v[228:231] offset:58368\n\t" \
    "ds_write_b128 v254, v[232:235] offset:59392\n\t" \
    "ds_write_b128 v254, v[236:239] offset:60416\n\t" \
    "s_cmp_lg_u32 s43, 0\n\t" \
    "s_cbranch_scc1 .Lv24_%=\n\t" \
    "s_mov_b32 s44, 0\n\t" \
    ".Lv25_%=:\n\t" \
    "v_cmp_ge_u32 vcc, s44, v252\n\t" \
    "s_cbranch_vccz .Lv26_%=\n\t" \
    "s_cmp_eq_u32 s41, 0\n\t" \
    "s_cbranch_scc1 .Lv26_%=\n\t" \
    "s_sub_u32 s41, s41, 1\n\t" \
    "s_sleep 8\n\t" \
    "v_and_b32 v252, 0xf0, v253\n\t" \
    "v_lshlrev_b32 v252, 3, v252\n\t" \
    "buffer_load_dword v252, v252, s[60:63], %[fl] offen sc1\n\t" \
    "s_waitcnt vmcnt(0)\n\t" \
    "s_branch .Lv25_%=\n\t" \
    ".Lv26_%=:\n\t" \
    "s_mov_b32 m0, 0x2000\n\t" \
    "s_add_u32 s49, s42, 0x0\n\t" \
    "buffer_load_dwordx4 v253, s[60:63], s49 offen offset:0 sc1 lds\n\t" \
    "buffer_load_dwordx4 v253, s[60:63], s49 offen offset:1024 sc1 lds\n\t" \
    "buffer_load_dwordx4 v253, s[60:63], s49 offen offset:2048 sc1 lds\n\t" \
    "buffer_load_dwordx4 v253, s[60:63], s49 offen offset:3072 sc1 lds\n\t" \
    "s_mov_b32 m0, 0x4800\n\t" \
    "s_add_u32 s49, s42, 0x8800\n\t" \
    "buffer_load_dwordx4 v253, s[60:63], s49 offen offset:0 sc1 lds\n\t" \
    "s_mov_b32 m0, 0x7000\n\t" \
    "s_add_u32 s49, s42, 0x1000\n\t" \
    "buffer_load_dwordx4 v253, s[60:63], s49 offen offset:0 sc1 lds\n\t" \
    "buffer_load_dwordx4 v253, s[60:63], s49 offen offset:1024 sc1 lds\n\t" \
    "buffer_load_dwordx4 v253, s[60:63], s49 offen offset:2048 sc1 lds\n\t" \
    "buffer_load_dwordx4 v253, s[60:63], s49 offen offset:3072 sc1 lds\n\t" \
    "s_mov_b32 m0, 0x9800\n\t" \
    "s_add_u32 s49, s42, 0x9800\n\t" \
    "buffer_load_dwordx4 v253, s[60:63], s49 offen offset:0 sc1 lds\n\t" \
    ".Lv24_%=:\n\t" \
    "s_waitcnt vmcnt(0) lgkmcnt(0)\n\t" \
    "s_barrier \n\t" \
    "ds_read_b128 v[224:227], v253 offset:8192\n\t" \
    "ds_read_b128 v[244:247], v254 offset:51200\n\t" \
    "ds_read_b128 v[228:231], v253 offset:9216\n\t" \
    "ds_read_b128 v[232:235], v253 offset:28672\n\t" \
    "ds_read_b128 v[236:239], v253 offset:29696\n\t" \
    "ds_read_b128 v[248:251], v254 offset:52224\n\t" \
    ".Lv27_%=:\n\t" \
    "s_waitcnt lgkmcnt(5)\n\t" \
    "v_mfma_f32_16x16x32_bf16 v[204:207], v[0:3], v[224:227], 0\n\t" \
    "s_waitcnt lgkmcnt(3)\n\t" \
    "v_mfma_f32_16x16x32_bf16 v[204:207], v[4:7], v[228:231], v[204:207]\n\t" \
    "ds_read_b128 v[240:243], v253 offset:14336\n\t" \
    "v_mfma_f32_16x16x32_bf16 v[208:211], v[8:11], v[224:227], 0\n\t" \
    "v_mfma_f32_16x16x32_bf16 v[208:211], v[12:15], v[228:231], v[208:211]\n\t" \
    "v_mfma_f32_16x16x32_bf16 v[200:203], v[244:247], v[224:227], 0\n\t" \
    "s_waitcnt lgkmcnt(1)\n\t" \
    "v_mfma_f32_16x16x32_bf16 v[200:203], v[248:251], v[228:231], v[200:203]\n\t" \
    "v_mfma_f32_16x16x32_bf16 v[216:219], v[0:3], v[232:235], 0\n\t" \
    "ds_read_b128 v[224:227], v253 offset:15360\n\t" \
    "v_mfma_f32_16x16x32_bf16 v[216:219], v[4:7], v[236:239], v[216:219]\n\t" \
    "ds_read_b128 v[228:231], v253 offset:0\n\t" \
    "v_mfma_f32_16x16x32_bf16 v[220:223], v[8:11], v[232:235], 0\n\t" \
    "v_mfma_f32_16x16x32_bf16 v[220:223], v[12:15], v[236:239], v[220:223]\n\t" \
    "v_mfma_f32_16x16x32_bf16 v[212:215], v[244:247], v[232:235], 0\n\t" \
    "ds_read_b128 v[244:247], v254 offset:53248\n\t" \
    "v_mfma_f32_16x16x32_bf16 v[212:215], v[248:251], v[236:239], v[212:215]\n\t" \
    "ds_read_b128 v[248:251], v254 offset:54272\n\t" \
    "s_waitcnt lgkmcnt(4)\n\t" \
    "v_mfma_f32_16x16x32_bf16 v[204:207], v[24:27], v[240:243], v[204:207]\n\t" \
    "ds_read_b128 v[232:235], v253 offset:1024\n\t" \
    "s_waitcnt lgkmcnt(4)\n\t" \
    "v_mfma_f32_16x16x32_bf16 v[204:207], v[28:31], v[224:227], v[204:207]\n\t" \
    "ds_read_b128 v[236:239], v253 offset:20480\n\t" \
    "v_mfma_f32_16x16x32_bf16 v[208:211], v[32:35], v[240:243], v[208:211]\n\t" \
    "v_mfma_f32_16x16x32_bf16 v[208:211], v[36:39], v[224:227], v[208:211]\n\t" \
    "v_mfma_f32_16x16x32_bf16 v[200:203], v[16:19], v[240:243], v[200:203]\n\t" \
    "v_mfma_f32_16x16x32_bf16 v[200:203], v[20:23], v[224:227], v[200:203]\n\t" \
    "s_waitcnt lgkmcnt(4)\n\t" \
    "v_mfma_f32_16x16x32_bf16 v[204:207], v[40:43], v[228:231], v[204:207]\n\t" \
    "ds_read_b128 v[240:243], v253 offset:21504\n\t" \
    "s_waitcnt lgkmcnt(2)\n\t" \
    "v_mfma_f32_16x16x32_bf16 v[204:207], v[44:47], v[232:235], v[204:207]\n\t" \
    "ds_read_b128 v[224:227], v253 offset:2048\n\t" \
    "v_mfma_f32_16x16x32_bf16 v[208:211], v[48:51], v[228:231], v[208:211]\n\t" \
    "v_mfma_f32_16x16x32_bf16 v[208:211], v[52:55], v[232:235], v[208:211]\n\t" \
    "v_mfma_f32_16x16x32_bf16 v[200:203], v[244:247], v[228:231], v[200:203]\n\t" \
    "v_mfma_f32_16x16x32_bf16 v[200:203], v[248:251], v[232:235], v[200:203]\n\t" \
    "s_waitcnt lgkmcnt(2)\n\t" \
    "v_mfma_f32_16x16x32_bf16 v[216:219], v[40:43], v[236:239], v[216:219]\n\t" \
    "ds_read_b128 v[228:231], v253 offset:3072\n\t" \
    "s_add_u32 s44, s40, 1\n\t" \
    "s_waitcnt lgkmcnt(2)\n\t" \
    "v_mfma_f32_16x16x32_bf16 v[216:219], v[44:47], v[240:243], v[216:219]\n\t" \
    "ds_read_b128 v[232:235], v253 offset:4096\n\t" \
    "s_and_b32 s45, s44, 15\n\t" \
    "v_mfma_f32_16x16x32_bf16 v[220:223], v[48:51], v[236:239], v[220:223]\n\t" \
    "s_cmp_eq_u32 s45, 0\n\t" \
    "v_mfma_f32_16x16x32_bf16 v[220:223], v[52:55], v[240:243], v[220:223]\n\t" \
    "s_cselect_b32 s45, %[cj], %[cs]\n\t" \
    "v_mfma_f32_16x16x32_bf16 v[212:215], v[244:247], v[236:239], v[212:215]\n\t" \
    "ds_read_b128 v[244:247], v254 offset:55296\n\t" \
    "s_cmp_eq_u32 s44, 64\n\t" \
    "v_mfma_f32_16x16x32_bf16 v[212:215], v[248:251], v[240:243], v[212:215]\n\t" \
    "ds_read_b128 v[248:251], v254 offset:56320\n\t" \
    "s_cselect_b32 s45, 0, s45\n\t" \
    "s_waitcnt lgkmcnt(4)\n\t" \
    "v_mfma_f32_16x16x32_bf16 v[204:207], v[64:67], v[224:227], v[204:207]\n\t" \
    "ds_read_b128 v[236:239], v253 offset:5120\n\t" \
    "s_add_u32 s42, s42, s45\n\t" \
    "s_waitcnt lgkmcnt(4)\n\t" \
    "v_mfma_f32_16x16x32_bf16 v[204:207], v[68:71], v[228:231], v[204:207]\n\t" \
    "ds_read_b128 v[240:243], v253 offset:24576\n\t" \
    "v_mfma_f32_16x16x32_bf16 v[208:211], v[72:75], v[224:227], v[208:211]\n\t" \
    "v_mfma_f32_16x16x32_bf16 v[208:211], v[76:79], v[228:231], v[208:211]\n\t" \
    "v_mfma_f32_16x16x32_bf16 v[200:203], v[56:59], v[224:227], v[200:203]\n\t" \
    "v_mfma_f32_16x16x32_bf16 v[200:203], v[60:63], v[228:231], v[200:203]\n\t" \
    "s_waitcnt lgkmcnt(4)\n\t" \
    "v_mfma_f32_16x16x32_bf16 v[204:207], v[80:83], v[232:235], v[204:207]\n\t" \
    "ds_read_b128 v[224:227], v253 offset:25600\n\t" \
    "s_waitcnt lgkmcnt(2)\n\t" \
    "v_mfma_f32_16x16x32_bf16 v[204:207], v[84:87], v[236:239], v[204:207]\n\t" \
    "ds_read_b128 v[228:231], v253 offset:6144\n\t" \
    "v_mfma_f32_16x16x32_bf16 v[208:211], v[88:91], v[232:235], v[208:211]\n\t" \
    "v_mfma_f32_16x16x32_bf16 v[208:211], v[92:95], v[236:239], v[208:211]\n\t" \
    "v_mfma_f32_16x16x32_bf16 v[200:203], v[244:247], v[232:235], v[200:203]\n\t" \
    "v_mfma_f32_16x16x32_bf16 v[200:203], v[248:251], v[236:239], v[200:203]\n\t" \
    "s_waitcnt lgkmcnt(2)\n\t" \
    "v_mfma_f32_16x16x32_bf16 v[216:219], v[80:83], v[240:243], v[216:219]\n\t" \
    "ds_read_b128 v[232:235], v253 offset:7168\n\t" \
    "s_and_b32 s44, s40, 3\n\t" \
    "s_waitcnt lgkmcnt(2)\n\t" \
    "v_mfma_f32_16x16x32_bf16 v[216:219], v[84:87], v[224:227], v[216:219]\n\t" \
    "ds_read_b128 v[236:239], v253 offset:10240\n\t" \
    "s_lshl_b32 s44, s44, 17\n\t" \
    "v_mfma_f32_16x16x32_bf16 v[220:223], v[88:91], v[240:243], v[220:223]\n\t" \
    "s_add_u32 s44, s44, %[mb]\n\t" \
    "v_mfma_f32_16x16x32_bf16 v[220:223], v[92:95], v[224:227], v[220:223]\n\t" \
    "s_add_u32 s46, s44, 0x1000\n\t" \
    "v_mfma_f32_16x16x32_bf16 v[212:215], v[244:247], v[240:243], v[212:215]\n\t" \
    "ds_read_b128 v[244:247], v254 offset:57344\n\t" \
    "s_add_u32 s47, s44, 0x1c00\n\t" \
    "v_mfma_f32_16x16x32_bf16 v[212:215], v[248:251], v[224:227], v[212:215]\n\t" \
    "ds_read_b128 v[248:251], v254 offset:58368\n\t" \
    "s_waitcnt lgkmcnt(4)\n\t" \
    "v_mfma_f32_16x16x32_bf16 v[204:207], v[104:107], v[228:231], v[204:207]\n\t" \
    "ds_read_b128 v[240:243], v253 offset:11264\n\t" \
    "s_waitcnt lgkmcnt(4)\n\t" \
    "v_mfma_f32_16x16x32_bf16 v[204:207], v[108:111], v[232:235], v[204:207]\n\t" \
    "ds_read_b128 v[224:227], v253 offset:30720\n\t" \
    "v_mfma_f32_16x16x32_bf16 v[208:211], v[112:115], v[228:231], v[208:211]\n\t" \
    "v_mfma_f32_16x16x32_bf16 v[208:211], v[116:119], v[232:235], v[208:211]\n\t" \
    "v_mfma_f32_16x16x32_bf16 v[200:203], v[96:99], v[228:231], v[200:203]\n\t" \
    "v_mfma_f32_16x16x32_bf16 v[200:203], v[100:103], v[232:235], v[200:203]\n\t" \
    "s_waitcnt lgkmcnt(4)\n\t" \
    "v_mfma_f32_16x16x32_bf16 v[204:207], v[120:123], v[236:239], v[204:207]\n\t" \
    "ds_read_b128 v[228:231], v253 offset:31744\n\t" \
    "s_waitcnt lgkmcnt(2)\n\t" \
    "v_mfma_f32_16x16x32_bf16 v[204:207], v[124:127], v[240:243], v[204:207]\n\t" \
    "ds_read_b128 v[232:235], v253 offset:12288\n\t" \
    "v_mfma_f32_16x16x32_bf16 v[208:211], v[128:131], v[236:239], v[208:211]\n\t" \
    "v_mfma_f32_16x16x32_bf16 v[208:211], v[132:135], v[240:243], v[208:211]\n\t" \
    "v_mfma_f32_16x16x32_bf16 v[200:203], v[244:247], v[236:239], v[200:203]\n\t" \
    "v_mfma_f32_16x16x32_bf16 v[200:203], v[248:251], v[240:243], v[200:203]\n\t" \
    "s_waitcnt lgkmcnt(2)\n\t" \
    "v_mfma_f32_16x16x32_bf16 v[216:219], v[120:123], v[224:227], v[216:219]\n\t" \
    "ds_read_b128 v[236:239], v253 offset:13312\n\t" \
    "s_waitcnt lgkmcnt(2)\n\t" \
    "v_mfma_f32_16x16x32_bf16 v[216:219], v[124:127], v[228:231], v[216:219]\n\t" \
    "ds_read_b128 v[240:243], v253 offset:16384\n\t" \
    "v_mfma_f32_16x16x32_bf16 v[220:223], v[128:131], v[224:227], v[220:223]\n\t" \
    "v_mfma_f32_16x16x32_bf16 v[220:223], v[132:135], v[228:231], v[220:223]\n\t" \
    "v_mfma_f32_16x16x32_bf16 v[212:215], v[244:247], v[224:227], v[212:215]\n\t" \
    "ds_read_b128 v[244:247], v254 offset:59392\n\t" \
    "v_mfma_f32_16x16x32_bf16 v[212:215], v[248:251], v[228:231], v[212:215]\n\t" \
    "ds_read_b128 v[248:251], v254 offset:60416\n\t" \
    "v_and_b32 v252, 0xf0, v253\n\t" \
    "v_lshlrev_b32 v252, 3, v252\n\t" \
    "buffer_load_dword v252, v252, s[60:63], %[fl] offen sc1\n\t" \
    "s_waitcnt lgkmcnt(4)\n\t" \
    "v_mfma_f32_16x16x32_bf16 v[204:207], v[144:147], v[232:235], v[204:207]\n\t" \
    "ds_read_b128 v[224:227], v253 offset:17408\n\t" \
    "s_waitcnt lgkmcnt(4)\n\t" \
    "v_mfma_f32_16x16x32_bf16 v[204:207], v[148:151], v[236:239], v[204:207]\n\t" \
    "ds_read_b128 v[228:231], v253 offset:36864\n\t" \
    "v_mfma_f32_16x16x32_bf16 v[208:211], v[152:155], v[232:235], v[208:211]\n\t" \
    "v_mfma_f32_16x16x32_bf16 v[208:211], v[156:159], v[236:239], v[208:211]\n\t" \
    "v_mfma_f32_16x16x32_bf16 v[200:203], v[136:139], v[232:235], v[200:203]\n\t" \
    "v_mfma_f32_16x16x32_bf16 v[200:203], v[140:143], v[236:239], v[200:203]\n\t" \
    "s_waitcnt lgkmcnt(4)\n\t" \
    "v_mfma_f32_16x16x32_bf16 v[204:207], v[160:163], v[240:243], v[204:207]\n\t" \
    "ds_read_b128 v[232:235], v253 offset:37888\n\t" \
    "s_waitcnt lgkmcnt(2)\n\t" \
    "v_mfma_f32_16x16x32_bf16 v[204:207], v[164:167], v[224:227], v[204:207]\n\t" \
    "ds_read_b128 v[236:239], v253 offset:18432\n\t" \
    "v_mfma_f32_16x16x32_bf16 v[208:211], v[168:171], v[240:243], v[208:211]\n\t" \
    "v_mfma_f32_16x16x32_bf16 v[208:211], v[172:175], v[224:227], v[208:211]\n\t" \
    "v_mfma_f32_16x16x32_bf16 v[200:203], v[244:247], v[240:243], v[200:203]\n\t" \
    "v_mfma_f32_16x16x32_bf16 v[200:203], v[248:251], v[224:227], v[200:203]\n\t" \
    "s_waitcnt lgkmcnt(2)\n\t" \
    "v_mfma_f32_16x16x32_bf16 v[216:219], v[160:163], v[228:231], v[216:219]\n\t" \
    "ds_read_b128 v[240:243], v253 offset:19456\n\t" \
    "s_waitcnt lgkmcnt(2)\n\t" \
    "v_mfma_f32_16x16x32_bf16 v[216:219], v[164:167], v[232:235], v[216:219]\n\t" \
    "ds_read_b128 v[224:227], v253 offset:34816\n\t" \
    "v_mfma_f32_16x16x32_bf16 v[220:223], v[168:171], v[228:231], v[220:223]\n\t" \
    "v_mfma_f32_16x16x32_bf16 v[220:223], v[172:175], v[232:235], v[220:223]\n\t" \
    "v_mfma_f32_16x16x32_bf16 v[212:215], v[244:247], v[228:231], v[212:215]\n\t" \
    "v_mfma_f32_16x16x32_bf16 v[212:215], v[248:251], v[232:235], v[212:215]\n\t" \
    "s_waitcnt lgkmcnt(2)\n\t" \
    "v_mfma_f32_16x16x32_bf16 v[204:207], v[184:187], v[236:239], v[204:207]\n\t" \
    "ds_read_b128 v[228:231], v253 offset:35840\n\t" \
    "s_waitcnt lgkmcnt(2)\n\t" \
    "v_mfma_f32_16x16x32_bf16 v[204:207], v[188:191], v[240:243], v[204:207]\n\t" \
    "ds_read_b128 v[232:235], v253 offset:22528\n\t" \
    "v_mfma_f32_16x16x32_bf16 v[208:211], v[192:195], v[236:239], v[208:211]\n\t" \
    "v_mfma_f32_16x16x32_bf16 v[208:211], v[196:199], v[240:243], v[208:211]\n\t" \
    "v_mfma_f32_16x16x32_bf16 v[200:203], v[176:179], v[236:239], v[200:203]\n\t" \
    "v_mfma_f32_16x16x32_bf16 v[200:203], v[180:183], v[240:243], v[200:203]\n\t" \
    "s_waitcnt lgkmcnt(2)\n\t" \
    "v_mfma_f32_16x16x32_bf16 v[216:219], v[24:27], v[224:227], v[216:219]\n\t" \
    "ds_read_b128 v[236:239], v253 offset:23552\n\t" \
    "s_waitcnt lgkmcnt(2)\n\t" \
    "v_mfma_f32_16x16x32_bf16 v[216:219], v[28:31], v[228:231], v[216:219]\n\t" \
    "ds_read_b128 v[240:243], v253 offset:26624\n\t" \
    "v_mfma_f32_16x16x32_bf16 v[220:223], v[32:35], v[224:227], v[220:223]\n\t" \
    "v_cvt_pk_bf16_f32 v204, v204, v205\n\t" \
    "v_mfma_f32_16x16x32_bf16 v[220:223], v[36:39], v[228:231], v[220:223]\n\t" \
    "v_cvt_pk_bf16_f32 v205, v206, v207\n\t" \
    "v_mfma_f32_16x16x32_bf16 v[212:215], v[16:19], v[224:227], v[212:215]\n\t" \
    "ds_write_b64 v254, v[204:205] offset:7176\n\t" \
    "v_mfma_f32_16x16x32_bf16 v[212:215], v[20:23], v[228:231], v[212:215]\n\t" \
    "v_cvt_pk_bf16_f32 v208, v208, v209\n\t" \
    "s_waitcnt lgkmcnt(3)\n\t" \
    "v_mfma_f32_16x16x32_bf16 v[216:219], v[64:67], v[232:235], v[216:219]\n\t" \
    "ds_read_b128 v[224:227], v253 offset:27648\n\t" \
    "v_cvt_pk_bf16_f32 v209, v210, v211\n\t" \
    "s_waitcnt lgkmcnt(3)\n\t" \
    "v_mfma_f32_16x16x32_bf16 v[216:219], v[68:71], v[236:239], v[216:219]\n\t" \
    "ds_read_b128 v[228:231], v253 offset:32768\n\t" \
    "ds_write_b64 v254, v[208:209] offset:8200\n\t" \
    "v_mfma_f32_16x16x32_bf16 v[220:223], v[72:75], v[232:235], v[220:223]\n\t" \
    "v_cvt_pk_bf16_f32 v200, v200, v201\n\t" \
    "v_mfma_f32_16x16x32_bf16 v[220:223], v[76:79], v[236:239], v[220:223]\n\t" \
    "v_cvt_pk_bf16_f32 v201, v202, v203\n\t" \
    "v_mfma_f32_16x16x32_bf16 v[212:215], v[56:59], v[232:235], v[212:215]\n\t" \
    "ds_write_b64 v254, v[200:201] offset:6152\n\t" \
    "v_mfma_f32_16x16x32_bf16 v[212:215], v[60:63], v[236:239], v[212:215]\n\t" \
    "s_waitcnt lgkmcnt(5)\n\t" \
    "v_mfma_f32_16x16x32_bf16 v[216:219], v[104:107], v[240:243], v[216:219]\n\t" \
    "ds_read_b128 v[232:235], v253 offset:33792\n\t" \
    "s_waitcnt lgkmcnt(4)\n\t" \
    "v_mfma_f32_16x16x32_bf16 v[216:219], v[108:111], v[224:227], v[216:219]\n\t" \
    "ds_read_b128 v[236:239], v253 offset:38912\n\t" \
    "v_mfma_f32_16x16x32_bf16 v[220:223], v[112:115], v[240:243], v[220:223]\n\t" \
    "v_mfma_f32_16x16x32_bf16 v[220:223], v[116:119], v[224:227], v[220:223]\n\t" \
    "v_mfma_f32_16x16x32_bf16 v[212:215], v[96:99], v[240:243], v[212:215]\n\t" \
    "v_mfma_f32_16x16x32_bf16 v[212:215], v[100:103], v[224:227], v[212:215]\n\t" \
    "ds_read_b128 v[240:243], v253 offset:39936\n\t" \
    "s_waitcnt lgkmcnt(3)\n\t" \
    "s_barrier \n\t" \
    "v_mfma_f32_16x16x32_bf16 v[216:219], v[144:147], v[228:231], v[216:219]\n\t" \
    "s_waitcnt lgkmcnt(2)\n\t" \
    "v_mfma_f32_16x16x32_bf16 v[216:219], v[148:151], v[232:235], v[216:219]\n\t" \
    "s_waitcnt lgkmcnt(1)\n\t" \
    "v_mfma_f32_16x16x32_bf16 v[216:219], v[184:187], v[236:239], v[216:219]\n\t" \
    "s_waitcnt lgkmcnt(0)\n\t" \
    "v_mfma_f32_16x16x32_bf16 v[216:219], v[188:191], v[240:243], v[216:219]\n\t" \
    "v_mfma_f32_16x16x32_bf16 v[220:223], v[152:155], v[228:231], v[220:223]\n\t" \
    "v_mfma_f32_16x16x32_bf16 v[220:223], v[156:159], v[232:235], v[220:223]\n\t" \
    "v_mfma_f32_16x16x32_bf16 v[220:223], v[192:195], v[236:239], v[220:223]\n\t" \
    "v_cvt_pk_bf16_f32 v216, v216, v217\n\t" \
    "v_cvt_pk_bf16_f32 v217, v218, v219\n\t" \
    "v_mfma_f32_16x16x32_bf16 v[220:223], v[196:199], v[240:243], v[220:223]\n\t" \
    "ds_write_b64 v254, v[216:217] offset:13320\n\t" \
    "v_mfma_f32_16x16x32_bf16 v[212:215], v[136:139], v[228:231], v[212:215]\n\t" \
    "v_mfma_f32_16x16x32_bf16 v[212:215], v[140:143], v[232:235], v[212:215]\n\t" \
    "v_mfma_f32_16x16x32_bf16 v[212:215], v[176:179], v[236:239], v[212:215]\n\t" \
    "v_cvt_pk_bf16_f32 v220, v220, v221\n\t" \
    "v_cvt_pk_bf16_f32 v221, v222, v223\n\t" \
    "v_mfma_f32_16x16x32_bf16 v[212:215], v[180:183], v[240:243], v[212:215]\n\t" \
    "ds_write_b64 v254, v[220:221] offset:14344\n\t" \
    "s_nop 6\n\t" \
    "v_cvt_pk_bf16_f32 v212, v212, v213\n\t" \
    "v_cvt_pk_bf16_f32 v213, v214, v215\n\t" \
    "ds_write_b64 v254, v[212:213] offset:12296\n\t" \
    "s_waitcnt vmcnt(0) lgkmcnt(0)\n\t" \
    "s_barrier \n\t" \
    "s_setprio 2\n\t" \
    "s_add_u32 s44, s40, 1\n\t" \
    "s_min_u32 s44, s44, 63\n\t" \
    ".Lv28_%=:\n\t" \
    "v_cmp_ge_u32 vcc, s44, v252\n\t" \
    "s_cbranch_vccz .Lv29_%=\n\t" \
    "s_cmp_eq_u32 s41, 0\n\t" \
    "s_cbranch_scc1 .Lv29_%=\n\t" \
    "s_sub_u32 s41, s41, 1\n\t" \
    "s_sleep 2\n\t" \
    "v_and_b32 v252, 0xf0, v253\n\t" \
    "v_lshlrev_b32 v252, 3, v252\n\t" \
    "buffer_load_dword v252, v252, s[60:63], %[fl] offen sc1\n\t" \
    "s_waitcnt vmcnt(0)\n\t" \
    "s_branch .Lv28_%=\n\t" \
    ".Lv29_%=:\n\t" \
    "s_mov_b32 m0, 0x2000\n\t" \
    "s_add_u32 s49, s42, 0x0\n\t" \
    "buffer_load_dwordx4 v253, s[60:63], s49 offen offset:0 sc1 lds\n\t" \
    "s_sleep 1\n\t" \
    "buffer_load_dwordx4 v253, s[60:63], s49 offen offset:1024 sc1 lds\n\t" \
    "s_sleep 1\n\t" \
    "buffer_load_dwordx4 v253, s[60:63], s49 offen offset:2048 sc1 lds\n\t" \
    "s_sleep 1\n\t" \
    "buffer_load_dwordx4 v253, s[60:63], s49 offen offset:3072 sc1 lds\n\t" \
    "s_sleep 1\n\t" \
    "s_mov_b32 m0, 0x4800\n\t" \
    "s_add_u32 s49, s42, 0x8800\n\t" \
    "buffer_load_dwordx4 v253, s[60:63], s49 offen offset:0 sc1 lds\n\t" \
    "s_sleep 1\n\t" \
    "s_mov_b32 m0, 0x7000\n\t" \
    "s_add_u32 s49, s42, 0x1000\n\t" \
    "buffer_load_dwordx4 v253, s[60:63], s49 offen offset:0 sc1 lds\n\t" \
    "s_sleep 1\n\t" \
    "buffer_load_dwordx4 v253, s[60:63], s49 offen offset:1024 sc1 lds\n\t" \
    "s_sleep 1\n\t" \
    "buffer_load_dwordx4 v253, s[60:63], s49 offen offset:2048 sc1 lds\n\t" \
    "s_sleep 1\n\t" \
    "buffer_load_dwordx4 v253, s[60:63], s49 offen offset:3072 sc1 lds\n\t" \
    "s_sleep 1\n\t" \
    "s_mov_b32 m0, 0x9800\n\t" \
    "s_add_u32 s49, s42, 0x9800\n\t" \
    "buffer_load_dwordx4 v253, s[60:63], s49 offen offset:0 sc1 lds\n\t" \
    "s_sleep 1\n\t" \
    "ds_read_b128 v[200:203], v254 offset:51200\n\t" \
    "ds_read_b128 v[204:207], v254 offset:52224\n\t" \
    "ds_read_b128 v[216:219], v254 offset:53248\n\t" \
    "ds_read_b128 v[220:223], v254 offset:54272\n\t" \
    "v_mov_b64 v[208:209], 0\n\t" \
    "v_mov_b64 v[210:211], 0\n\t" \
    "v_mov_b64 v[244:245], 0\n\t" \
    "v_mov_b64 v[246:247], 0\n\t" \
    "v_mov_b64 v[248:249], 0\n\t" \
    "v_mov_b64 v[250:251], 0\n\t" \
    "s_sleep 5\n\t" \
    "s_mov_b32 m0, 0xb000\n\t" \
    "s_nop 0\n\t" \
    "buffer_load_dwordx4 v253, s[60:63], s46 offen sc1 lds\n\t" \
    "s_mov_b32 m0, 0xbc00\n\t" \
    "s_nop 0\n\t" \
    "buffer_load_dwordx4 v253, s[60:63], s47 offen sc1 lds\n\t" \
    "s_waitcnt lgkmcnt(0)\n\t" \
    "v_bfe_i32 v212, v253, 5, 1\n\t" \
    "v_and_b32 v212, 0xaaaa, v212\n\t" \
    "v_or_b32 v212, 0x4444, v212\n\t" \
    "v_and_b32 v213, 0xffff, v255\n\t" \
    "v_lshrrev_b32 v214, 16, v255\n\t" \
    ".Lv210_%=:\n\t" \
    "s_waitcnt vmcnt(0)\n\t" \
    "ds_read_b128 v[224:227], v253 offset:45056\n\t" \
    "ds_read_b128 v[228:231], v253 offset:48128\n\t" \
    "s_waitcnt lgkmcnt(1)\n\t" \
    "v_max_u32 v215, v224, v225\n\t" \
    "v_max3_u32 v215, v226, v227, v215\n\t" \
    "s_waitcnt lgkmcnt(0)\n\t" \
    "v_max3_u32 v215, v228, v229, v215\n\t" \
    "v_max3_u32 v215, v230, v231, v215\n\t" \
    "v_cmp_eq_u32 vcc, -1, v215\n\t" \
    "s_cbranch_vccz .Lv211_%=\n\t" \
    "v_max_u32 v215, v224, v225\n\t" \
    "v_max3_u32 v215, v226, v227, v215\n\t" \
    "v_cmp_eq_u32 vcc, -1, v215\n\t" \
    "s_cbranch_vccz .Lv212_%=\n\t" \
    "s_mov_b32 m0, 0xb000\n\t" \
    "s_nop 0\n\t" \
    "buffer_load_dwordx4 v253, s[60:63], s46 offen sc1 lds\n\t" \
    ".Lv212_%=:\n\t" \
    "v_max_u32 v215, v228, v229\n\t" \
    "v_max3_u32 v215, v230, v231, v215\n\t" \
    "v_cmp_eq_u32 vcc, -1, v215\n\t" \
    "s_cbranch_vccz .Lv213_%=\n\t" \
    "s_mov_b32 m0, 0xbc00\n\t" \
    "s_nop 0\n\t" \
    "buffer_load_dwordx4 v253, s[60:63], s47 offen sc1 lds\n\t" \
    ".Lv213_%=:\n\t" \
    "s_cmp_eq_u32 s41, 0\n\t" \
    "s_cbranch_scc1 .Lv211_%=\n\t" \
    "s_sub_u32 s41, s41, 1\n\t" \
    "s_branch .Lv210_%=\n\t" \
    ".Lv211_%=:\n\t" \
    "s_waitcnt lgkmcnt(0)\n\t" \
    "s_barrier \n\t" \
    "ds_read_b128 v[232:235], v253 offset:40960\n\t" \
    "ds_read_b128 v[236:239], v253 offset:41984\n\t" \
    "ds_read_b128 v[240:243], v253 offset:43008\n\t" \
    "v_smfmac_f32_16x16x64_bf16 v[244:247], v[224:227], v[0:7], v212\n\t" \
    "v_smfmac_f32_16x16x64_bf16 v[248:251], v[224:227], v[8:15], v212\n\t" \
    "v_smfmac_f32_16x16x64_bf16 v[208:211], v[224:227], v[200:207], v212\n\t" \
    "ds_read_b128 v[200:203], v254 offset:55296\n\t" \
    "ds_read_b128 v[204:207], v254 offset:56320\n\t" \
    "ds_read_b128 v[224:227], v253 offset:44032\n\t" \
    "v_smfmac_f32_16x16x64_bf16 v[208:211], v[228:231], v[16:23], v212\n\t" \
    "v_smfmac_f32_16x16x64_bf16 v[244:247], v[228:231], v[24:31], v212\n\t" \
    "v_smfmac_f32_16x16x64_bf16 v[248:251], v[228:231], v[32:39], v212\n\t" \
    "ds_read_b128 v[228:231], v253 offset:46080\n\t" \
    "s_waitcnt lgkmcnt(6)\n\t" \
    "v_smfmac_f32_16x16x64_bf16 v[208:211], v[232:235], v[216:223], v212\n\t" \
    "ds_read_b128 v[216:219], v254 offset:57344\n\t" \
    "ds_read_b128 v[220:223], v254 offset:58368\n\t" \
    "v_smfmac_f32_16x16x64_bf16 v[244:247], v[232:235], v[40:47], v212\n\t" \
    "v_smfmac_f32_16x16x64_bf16 v[248:251], v[232:235], v[48:55], v212\n\t" \
    "ds_read_b128 v[232:235], v253 offset:47104\n\t" \
    "s_waitcnt lgkmcnt(8)\n\t" \
    "v_smfmac_f32_16x16x64_bf16 v[208:211], v[236:239], v[56:63], v212\n\t" \
    "v_smfmac_f32_16x16x64_bf16 v[244:247], v[236:239], v[64:71], v212\n\t" \
    "v_smfmac_f32_16x16x64_bf16 v[248:251], v[236:239], v[72:79], v212\n\t" \
    "ds_read_b128 v[236:239], v253 offset:49152\n\t" \
    "s_waitcnt lgkmcnt(6)\n\t" \
    "v_smfmac_f32_16x16x64_bf16 v[208:211], v[240:243], v[200:207], v212\n\t" \
    "ds_read_b128 v[200:203], v254 offset:59392\n\t" \
    "ds_read_b128 v[204:207], v254 offset:60416\n\t" \
    "v_smfmac_f32_16x16x64_bf16 v[244:247], v[240:243], v[80:87], v212\n\t" \
    "v_smfmac_f32_16x16x64_bf16 v[248:251], v[240:243], v[88:95], v212\n\t" \
    "ds_read_b128 v[240:243], v253 offset:50176\n\t" \
    "s_waitcnt lgkmcnt(8)\n\t" \
    "v_smfmac_f32_16x16x64_bf16 v[208:211], v[224:227], v[96:103], v212\n\t" \
    "v_smfmac_f32_16x16x64_bf16 v[244:247], v[224:227], v[104:111], v212\n\t" \
    "v_smfmac_f32_16x16x64_bf16 v[248:251], v[224:227], v[112:119], v212\n\t" \
    "s_waitcnt lgkmcnt(7)\n\t" \
    "v_smfmac_f32_16x16x64_bf16 v[244:247], v[228:231], v[120:127], v212\n\t" \
    "s_waitcnt lgkmcnt(4)\n\t" \
    "v_smfmac_f32_16x16x64_bf16 v[244:247], v[232:235], v[144:151], v212\n\t" \
    "s_waitcnt lgkmcnt(3)\n\t" \
    "v_smfmac_f32_16x16x64_bf16 v[244:247], v[236:239], v[160:167], v212\n\t" \
    "s_waitcnt lgkmcnt(0)\n\t" \
    "v_smfmac_f32_16x16x64_bf16 v[244:247], v[240:243], v[184:191], v212\n\t" \
    "v_smfmac_f32_16x16x64_bf16 v[248:251], v[228:231], v[128:135], v212\n\t" \
    "v_smfmac_f32_16x16x64_bf16 v[248:251], v[232:235], v[152:159], v212\n\t" \
    "v_smfmac_f32_16x16x64_bf16 v[248:251], v[236:239], v[168:175], v212\n\t" \
    "v_add_f32 v244, v244, v246\n\t" \
    "v_add_f32 v245, v245, v247\n\t" \
    "v_cvt_pk_bf16_f32 v244, v244, v245\n\t" \
    "v_smfmac_f32_16x16x64_bf16 v[248:251], v[240:243], v[192:199], v212\n\t" \
    "ds_write_b16 v213, v244 offset:512\n\t" \
    "ds_write_b16_d16_hi v213, v244 offset:528\n\t" \
    "v_smfmac_f32_16x16x64_bf16 v[208:211], v[228:231], v[216:223], v212\n\t" \
    "v_smfmac_f32_16x16x64_bf16 v[208:211], v[232:235], v[136:143], v212\n\t" \
    "v_smfmac_f32_16x16x64_bf16 v[208:211], v[236:239], v[200:207], v212\n\t" \
    "v_add_f32 v248, v248, v250\n\t" \
    "v_add_f32 v249, v249, v251\n\t" \
    "v_cvt_pk_bf16_f32 v248, v248, v249\n\t" \
    "v_smfmac_f32_16x16x64_bf16 v[208:211], v[240:243], v[176:183], v212\n\t" \
    "ds_write_b16 v213, v248 offset:1024\n\t" \
    "ds_write_b16_d16_hi v213, v248 offset:1040\n\t" \
    "s_nop 5\n\t" \
    "v_add_f32 v208, v208, v210\n\t" \
    "v_add_f32 v209, v209, v211\n\t" \
    "v_cvt_pk_bf16_f32 v208, v208, v209\n\t" \
    "ds_write_b16 v213, v208 offset:0\n\t" \
    "ds_write_b16_d16_hi v213, v208 offset:16\n\t" \
    "s_setprio 0\n\t" \
    "s_waitcnt lgkmcnt(0)\n\t" \
    "s_barrier \n\t" \
    "ds_read_b128 v[224:227], v253 offset:8192\n\t" \
    "ds_read_b128 v[244:247], v254 offset:51200\n\t" \
    "ds_read_b128 v[228:231], v253 offset:9216\n\t" \
    "ds_read_b128 v[232:235], v253 offset:28672\n\t" \
    "ds_read_b128 v[236:239], v253 offset:29696\n\t" \
    "ds_read_b128 v[248:251], v254 offset:52224\n\t" \
    "s_add_u32 s40, s40, 1\n\t" \
    "s_cmp_lg_u32 s40, 64\n\t" \
    "s_cbranch_scc1 .Lv27_%=\n\t" \
    "s_branch .Lend%=\n\t" \
    ".Lv1_%=:\n\t" \
    "s_mov_b32 s42, %[xs]\n\t" \
    "v_add_u32 v244, 0xc000, v243\n\t" \
    "s_waitcnt vmcnt(12) lgkmcnt(0)\n\t" \
    "s_barrier \n\t" \
    "s_add_u32 m0, %[wm], 0x9000\n\t" \
    "s_mov_b32 s44, 0x1e0000\n\t" \
    "buffer_load_dwordx4 v240, %[wr], s44 offen offset:0 nt lds\n\t" \
    "buffer_load_dwordx4 v241, %[wr], s44 offen offset:1024 nt lds\n\t" \
    "buffer_load_dwordx4 v242, %[wr], s44 offen offset:2048 nt lds\n\t" \
    "ds_read_b64_tr_b16 v[56:57], v244 offset:0\n\t" \
    "ds_read_b64_tr_b16 v[58:59], v244 offset:1536\n\t" \
    "ds_read_b64_tr_b16 v[64:65], v244 offset:32\n\t" \
    "ds_read_b64_tr_b16 v[66:67], v244 offset:1568\n\t" \
    "ds_read_b64_tr_b16 v[72:73], v244 offset:64\n\t" \
    "ds_read_b64_tr_b16 v[74:75], v244 offset:1600\n\t" \
    "v_add_u32 v244, 0xf000, v243\n\t" \
    "s_waitcnt vmcnt(12) lgkmcnt(0)\n\t" \
    "s_barrier \n\t" \
    "s_add_u32 m0, %[wm], 0xc000\n\t" \
    "s_mov_b32 s44, 0x240000\n\t" \
    "buffer_load_dwordx4 v240, %[wr], s44 offen offset:0 nt lds\n\t" \
    "buffer_load_dwordx4 v241, %[wr], s44 offen offset:1024 nt lds\n\t" \
    "buffer_load_dwordx4 v242, %[wr], s44 offen offset:2048 nt lds\n\t" \
    "ds_read_b64_tr_b16 v[60:61], v244 offset:0\n\t" \
    "ds_read_b64_tr_b16 v[62:63], v244 offset:1536\n\t" \
    "ds_read_b64_tr_b16 v[68:69], v244 offset:32\n\t" \
    "ds_read_b64_tr_b16 v[70:71], v244 offset:1568\n\t" \
    "ds_read_b64_tr_b16 v[76:77], v244 offset:64\n\t" \
    "ds_read_b64_tr_b16 v[78:79], v244 offset:1600\n\t" \
    "v_add_u32 v244, 0x0, v243\n\t" \
    "s_waitcnt vmcnt(12) lgkmcnt(0)\n\t" \
    "s_barrier \n\t" \
    "s_add_u32 m0, %[wm], 0xf000\n\t" \
    "s_mov_b32 s44, 0x2a0000\n\t" \
    "buffer_load_dwordx4 v240, %[wr], s44 offen offset:0 nt lds\n\t" \
    "buffer_load_dwordx4 v241, %[wr], s44 offen offset:1024 nt lds\n\t" \
    "buffer_load_dwordx4 v242, %[wr], s44 offen offset:2048 nt lds\n\t" \
    "ds_read_b64_tr_b16 v[96:97], v244 offset:0\n\t" \
    "ds_read_b64_tr_b16 v[98:99], v244 offset:1536\n\t" \
    "ds_read_b64_tr_b16 v[104:105], v244 offset:32\n\t" \
    "ds_read_b64_tr_b16 v[106:107], v244 offset:1568\n\t" \
    "ds_read_b64_tr_b16 v[112:113], v244 offset:64\n\t" \
    "ds_read_b64_tr_b16 v[114:115], v244 offset:1600\n\t" \
    "v_add_u32 v244, 0x3000, v243\n\t" \
    "s_waitcnt vmcnt(12) lgkmcnt(0)\n\t" \
    "s_barrier \n\t" \
    "s_add_u32 m0, %[wm], 0x0\n\t" \
    "s_mov_b32 s44, 0x300000\n\t" \
    "buffer_load_dwordx4 v240, %[wr], s44 offen offset:0 nt lds\n\t" \
    "buffer_load_dwordx4 v241, %[wr], s44 offen offset:1024 nt lds\n\t" \
    "buffer_load_dwordx4 v242, %[wr], s44 offen offset:2048 nt lds\n\t" \
    "ds_read_b64_tr_b16 v[100:101], v244 offset:0\n\t" \
    "ds_read_b64_tr_b16 v[102:103], v244 offset:1536\n\t" \
    "ds_read_b64_tr_b16 v[108:109], v244 offset:32\n\t" \
    "ds_read_b64_tr_b16 v[110:111], v244 offset:1568\n\t" \
    "ds_read_b64_tr_b16 v[116:117], v244 offset:64\n\t" \
    "ds_read_b64_tr_b16 v[118:119], v244 offset:1600\n\t" \
    "v_add_u32 v244, 0x6000, v243\n\t" \
    "s_waitcnt vmcnt(12) lgkmcnt(0)\n\t" \
    "s_barrier \n\t" \
    "s_add_u32 m0, %[wm], 0x3000\n\t" \
    "s_mov_b32 s44, 0x360000\n\t" \
    "buffer_load_dwordx4 v240, %[wr], s44 offen offset:0 nt lds\n\t" \
    "buffer_load_dwordx4 v241, %[wr], s44 offen offset:1024 nt lds\n\t" \
    "buffer_load_dwordx4 v242, %[wr], s44 offen offset:2048 nt lds\n\t" \
    "ds_read_b64_tr_b16 v[200:201], v244 offset:0\n\t" \
    "ds_read_b64_tr_b16 v[202:203], v244 offset:1536\n\t" \
    "ds_read_b64_tr_b16 v[0:1], v244 offset:32\n\t" \
    "ds_read_b64_tr_b16 v[2:3], v244 offset:1568\n\t" \
    "ds_read_b64_tr_b16 v[8:9], v244 offset:64\n\t" \
    "ds_read_b64_tr_b16 v[10:11], v244 offset:1600\n\t" \
    "v_add_u32 v244, 0x9000, v243\n\t" \
    "s_waitcnt vmcnt(12) lgkmcnt(0)\n\t" \
    "s_barrier \n\t" \
    "s_add_u32 m0, %[wm], 0x6000\n\t" \
    "s_mov_b32 s44, 0x3c0000\n\t" \
    "buffer_load_dwordx4 v240, %[wr], s44 offen offset:0 nt lds\n\t" \
    "buffer_load_dwordx4 v241, %[wr], s44 offen offset:1024 nt lds\n\t" \
    "buffer_load_dwordx4 v242, %[wr], s44 offen offset:2048 nt lds\n\t" \
    "ds_read_b64_tr_b16 v[204:205], v244 offset:0\n\t" \
    "ds_read_b64_tr_b16 v[206:207], v244 offset:1536\n\t" \
    "ds_read_b64_tr_b16 v[4:5], v244 offset:32\n\t" \
    "ds_read_b64_tr_b16 v[6:7], v244 offset:1568\n\t" \
    "ds_read_b64_tr_b16 v[12:13], v244 offset:64\n\t" \
    "ds_read_b64_tr_b16 v[14:15], v244 offset:1600\n\t" \
    "v_add_u32 v244, 0xc000, v243\n\t" \
    "s_waitcnt vmcnt(12) lgkmcnt(0)\n\t" \
    "s_barrier \n\t" \
    "s_add_u32 m0, %[wm], 0x9000\n\t" \
    "s_mov_b32 s44, 0x420000\n\t" \
    "buffer_load_dwordx4 v240, %[wr], s44 offen offset:0 nt lds\n\t" \
    "buffer_load_dwordx4 v241, %[wr], s44 offen offset:1024 nt lds\n\t" \
    "buffer_load_dwordx4 v242, %[wr], s44 offen offset:2048 nt lds\n\t" \
    "ds_read_b64_tr_b16 v[208:209], v244 offset:0\n\t" \
    "ds_read_b64_tr_b16 v[210:211], v244 offset:1536\n\t" \
    "ds_read_b64_tr_b16 v[40:41], v244 offset:32\n\t" \
    "ds_read_b64_tr_b16 v[42:43], v244 offset:1568\n\t" \
    "ds_read_b64_tr_b16 v[48:49], v244 offset:64\n\t" \
    "ds_read_b64_tr_b16 v[50:51], v244 offset:1600\n\t" \
    "v_add_u32 v244, 0xf000, v243\n\t" \
    "s_waitcnt vmcnt(12) lgkmcnt(0)\n\t" \
    "s_barrier \n\t" \
    "s_add_u32 m0, %[wm], 0xc000\n\t" \
    "s_mov_b32 s44, 0x480000\n\t" \
    "buffer_load_dwordx4 v240, %[wr], s44 offen offset:0 nt lds\n\t" \
    "buffer_load_dwordx4 v241, %[wr], s44 offen offset:1024 nt lds\n\t" \
    "buffer_load_dwordx4 v242, %[wr], s44 offen offset:2048 nt lds\n\t" \
    "ds_read_b64_tr_b16 v[212:213], v244 offset:0\n\t" \
    "ds_read_b64_tr_b16 v[214:215], v244 offset:1536\n\t" \
    "ds_read_b64_tr_b16 v[44:45], v244 offset:32\n\t" \
    "ds_read_b64_tr_b16 v[46:47], v244 offset:1568\n\t" \
    "ds_read_b64_tr_b16 v[52:53], v244 offset:64\n\t" \
    "ds_read_b64_tr_b16 v[54:55], v244 offset:1600\n\t" \
    "v_add_u32 v244, 0x0, v243\n\t" \
    "s_waitcnt vmcnt(12) lgkmcnt(0)\n\t" \
    "s_barrier \n\t" \
    "v_mov_b32 v246, 0\n\t" \
    "buffer_load_dword v246, v246, s[60:63], %[mo] offen sc1\n\t" \
    "s_add_u32 m0, %[wm], 0xf000\n\t" \
    "s_mov_b32 s44, 0x4e0000\n\t" \
    "buffer_load_dwordx4 v240, %[wr], s44 offen offset:0 nt lds\n\t" \
    "buffer_load_dwordx4 v241, %[wr], s44 offen offset:1024 nt lds\n\t" \
    "buffer_load_dwordx4 v242, %[wr], s44 offen offset:2048 nt lds\n\t" \
    "ds_read_b64_tr_b16 v[136:137], v244 offset:0\n\t" \
    "ds_read_b64_tr_b16 v[138:139], v244 offset:1536\n\t" \
    "ds_read_b64_tr_b16 v[144:145], v244 offset:32\n\t" \
    "ds_read_b64_tr_b16 v[146:147], v244 offset:1568\n\t" \
    "ds_read_b64_tr_b16 v[152:153], v244 offset:64\n\t" \
    "ds_read_b64_tr_b16 v[154:155], v244 offset:1600\n\t" \
    "v_add_u32 v244, 0x3000, v243\n\t" \
    "s_waitcnt vmcnt(12) lgkmcnt(0)\n\t" \
    "s_barrier \n\t" \
    "s_add_u32 m0, %[wm], 0x0\n\t" \
    "s_mov_b32 s44, 0x540000\n\t" \
    "buffer_load_dwordx4 v240, %[wr], s44 offen offset:0 nt lds\n\t" \
    "buffer_load_dwordx4 v241, %[wr], s44 offen offset:1024 nt lds\n\t" \
    "buffer_load_dwordx4 v242, %[wr], s44 offen offset:2048 nt lds\n\t" \
    "ds_read_b64_tr_b16 v[140:141], v244 offset:0\n\t" \
    "ds_read_b64_tr_b16 v[142:143], v244 offset:1536\n\t" \
    "ds_read_b64_tr_b16 v[148:149], v244 offset:32\n\t" \
    "ds_read_b64_tr_b16 v[150:151], v244 offset:1568\n\t" \
    "ds_read_b64_tr_b16 v[156:157], v244 offset:64\n\t" \
    "ds_read_b64_tr_b16 v[158:159], v244 offset:1600\n\t" \
    "v_add_u32 v244, 0x6000, v243\n\t" \
    "s_waitcnt vmcnt(12) lgkmcnt(0)\n\t" \
    "s_barrier \n\t" \
    "s_add_u32 m0, %[wm], 0x3000\n\t" \
    "s_mov_b32 s44, 0x5a0000\n\t" \
    "buffer_load_dwordx4 v240, %[wr], s44 offen offset:0 nt lds\n\t" \
    "buffer_load_dwordx4 v241, %[wr], s44 offen offset:1024 nt lds\n\t" \
    "buffer_load_dwordx4 v242, %[wr], s44 offen offset:2048 nt lds\n\t" \
    "ds_read_b64_tr_b16 v[216:217], v244 offset:0\n\t" \
    "ds_read_b64_tr_b16 v[218:219], v244 offset:1536\n\t" \
    "ds_read_b64_tr_b16 v[80:81], v244 offset:32\n\t" \
    "ds_read_b64_tr_b16 v[82:83], v244 offset:1568\n\t" \
    "ds_read_b64_tr_b16 v[88:89], v244 offset:64\n\t" \
    "ds_read_b64_tr_b16 v[90:91], v244 offset:1600\n\t" \
    "v_add_u32 v244, 0x9000, v243\n\t" \
    "s_waitcnt vmcnt(12) lgkmcnt(0)\n\t" \
    "s_barrier \n\t" \
    "s_add_u32 m0, %[wm], 0x6000\n\t" \
    "s_mov_b32 s44, 0x600000\n\t" \
    "buffer_load_dwordx4 v240, %[wr], s44 offen offset:0 nt lds\n\t" \
    "buffer_load_dwordx4 v241, %[wr], s44 offen offset:1024 nt lds\n\t" \
    "buffer_load_dwordx4 v242, %[wr], s44 offen offset:2048 nt lds\n\t" \
    "ds_read_b64_tr_b16 v[220:221], v244 offset:0\n\t" \
    "ds_read_b64_tr_b16 v[222:223], v244 offset:1536\n\t" \
    "ds_read_b64_tr_b16 v[84:85], v244 offset:32\n\t" \
    "ds_read_b64_tr_b16 v[86:87], v244 offset:1568\n\t" \
    "ds_read_b64_tr_b16 v[92:93], v244 offset:64\n\t" \
    "ds_read_b64_tr_b16 v[94:95], v244 offset:1600\n\t" \
    "v_add_u32 v244, 0xc000, v243\n\t" \
    "s_waitcnt vmcnt(12) lgkmcnt(0)\n\t" \
    "s_barrier \n\t" \
    "s_add_u32 m0, %[wm], 0x9000\n\t" \
    "s_mov_b32 s44, 0x660000\n\t" \
    "buffer_load_dwordx4 v240, %[wr], s44 offen offset:0 nt lds\n\t" \
    "buffer_load_dwordx4 v241, %[wr], s44 offen offset:1024 nt lds\n\t" \
    "buffer_load_dwordx4 v242, %[wr], s44 offen offset:2048 nt lds\n\t" \
    "ds_read_b64_tr_b16 v[16:17], v244 offset:0\n\t" \
    "ds_read_b64_tr_b16 v[18:19], v244 offset:1536\n\t" \
    "ds_read_b64_tr_b16 v[24:25], v244 offset:32\n\t" \
    "ds_read_b64_tr_b16 v[26:27], v244 offset:1568\n\t" \
    "ds_read_b64_tr_b16 v[32:33], v244 offset:64\n\t" \
    "ds_read_b64_tr_b16 v[34:35], v244 offset:1600\n\t" \
    "v_add_u32 v244, 0xf000, v243\n\t" \
    "s_waitcnt vmcnt(12) lgkmcnt(0)\n\t" \
    "s_barrier \n\t" \
    ".Lv11_%=:\n\t" \
    "v_readfirstlane_b32 s43, v246\n\t" \
    "s_nop 0\n\t" \
    "s_cmp_lg_u32 s43, -1\n\t" \
    "s_cbranch_scc1 .Lv12_%=\n\t" \
    "s_cmp_eq_u32 s41, 0\n\t" \
    "s_cbranch_scc1 .Lv12_%=\n\t" \
    "s_sub_u32 s41, s41, 1\n\t" \
    "s_sleep 4\n\t" \
    "v_mov_b32 v246, 0\n\t" \
    "buffer_load_dword v246, v246, s[60:63], %[mo] offen sc1\n\t" \
    "s_waitcnt vmcnt(0)\n\t" \
    "s_branch .Lv11_%=\n\t" \
    ".Lv12_%=:\n\t" \
    "v_mov_b32 v246, 0x8000\n\t" \
    "s_mov_b64 exec, 1\n\t" \
    "buffer_store_dword v246, v246, s[60:63], %[mo] offen sc1\n\t" \
    "s_mov_b64 exec, -1\n\t" \
    "s_and_b32 s43, s43, 0x2000\n\t" \
    "s_xor_b32 s43, s43, %[lx]\n\t" \
    "s_add_u32 s60, s60, s43\n\t" \
    "s_addc_u32 s61, s61, 0\n\t" \
    "v_and_b32 v252, 0xf0, v253\n\t" \
    "v_lshlrev_b32 v252, 3, v252\n\t" \
    "buffer_load_dword v252, v252, s[60:63], %[fl] offen sc1\n\t" \
    "s_add_u32 m0, %[wm], 0xc000\n\t" \
    "s_mov_b32 s44, 0x6c0000\n\t" \
    "buffer_load_dwordx4 v240, %[wr], s44 offen offset:0 nt lds\n\t" \
    "buffer_load_dwordx4 v241, %[wr], s44 offen offset:1024 nt lds\n\t" \
    "buffer_load_dwordx4 v242, %[wr], s44 offen offset:2048 nt lds\n\t" \
    "ds_read_b64_tr_b16 v[20:21], v244 offset:0\n\t" \
    "ds_read_b64_tr_b16 v[22:23], v244 offset:1536\n\t" \
    "ds_read_b64_tr_b16 v[28:29], v244 offset:32\n\t" \
    "ds_read_b64_tr_b16 v[30:31], v244 offset:1568\n\t" \
    "ds_read_b64_tr_b16 v[36:37], v244 offset:64\n\t" \
    "ds_read_b64_tr_b16 v[38:39], v244 offset:1600\n\t" \
    "v_add_u32 v244, 0x0, v243\n\t" \
    "s_waitcnt vmcnt(12) lgkmcnt(0)\n\t" \
    "s_barrier \n\t" \
    "s_add_u32 m0, %[wm], 0xf000\n\t" \
    "s_mov_b32 s44, 0x720000\n\t" \
    "buffer_load_dwordx4 v240, %[wr], s44 offen offset:0 nt lds\n\t" \
    "buffer_load_dwordx4 v241, %[wr], s44 offen offset:1024 nt lds\n\t" \
    "buffer_load_dwordx4 v242, %[wr], s44 offen offset:2048 nt lds\n\t" \
    "ds_read_b64_tr_b16 v[224:225], v244 offset:0\n\t" \
    "ds_read_b64_tr_b16 v[226:227], v244 offset:1536\n\t" \
    "ds_read_b64_tr_b16 v[120:121], v244 offset:32\n\t" \
    "ds_read_b64_tr_b16 v[122:123], v244 offset:1568\n\t" \
    "ds_read_b64_tr_b16 v[128:129], v244 offset:64\n\t" \
    "ds_read_b64_tr_b16 v[130:131], v244 offset:1600\n\t" \
    "v_add_u32 v244, 0x3000, v243\n\t" \
    "s_waitcnt vmcnt(12) lgkmcnt(0)\n\t" \
    "s_barrier \n\t" \
    "ds_read_b64_tr_b16 v[228:229], v244 offset:0\n\t" \
    "ds_read_b64_tr_b16 v[230:231], v244 offset:1536\n\t" \
    "ds_read_b64_tr_b16 v[124:125], v244 offset:32\n\t" \
    "ds_read_b64_tr_b16 v[126:127], v244 offset:1568\n\t" \
    "ds_read_b64_tr_b16 v[132:133], v244 offset:64\n\t" \
    "ds_read_b64_tr_b16 v[134:135], v244 offset:1600\n\t" \
    "v_add_u32 v244, 0x6000, v243\n\t" \
    "s_waitcnt vmcnt(9) lgkmcnt(0)\n\t" \
    "s_barrier \n\t" \
    "ds_read_b64_tr_b16 v[232:233], v244 offset:0\n\t" \
    "ds_read_b64_tr_b16 v[234:235], v244 offset:1536\n\t" \
    "ds_read_b64_tr_b16 v[160:161], v244 offset:32\n\t" \
    "ds_read_b64_tr_b16 v[162:163], v244 offset:1568\n\t" \
    "ds_read_b64_tr_b16 v[168:169], v244 offset:64\n\t" \
    "ds_read_b64_tr_b16 v[170:171], v244 offset:1600\n\t" \
    "v_add_u32 v244, 0x9000, v243\n\t" \
    "s_waitcnt vmcnt(6) lgkmcnt(0)\n\t" \
    "s_barrier \n\t" \
    "ds_read_b64_tr_b16 v[236:237], v244 offset:0\n\t" \
    "ds_read_b64_tr_b16 v[238:239], v244 offset:1536\n\t" \
    "ds_read_b64_tr_b16 v[164:165], v244 offset:32\n\t" \
    "ds_read_b64_tr_b16 v[166:167], v244 offset:1568\n\t" \
    "ds_read_b64_tr_b16 v[172:173], v244 offset:64\n\t" \
    "ds_read_b64_tr_b16 v[174:175], v244 offset:1600\n\t" \
    "v_add_u32 v244, 0xc000, v243\n\t" \
    "s_waitcnt vmcnt(3) lgkmcnt(0)\n\t" \
    "s_barrier \n\t" \
    "ds_read_b64_tr_b16 v[176:177], v244 offset:0\n\t" \
    "ds_read_b64_tr_b16 v[178:179], v244 offset:1536\n\t" \
    "ds_read_b64_tr_b16 v[184:185], v244 offset:32\n\t" \
    "ds_read_b64_tr_b16 v[186:187], v244 offset:1568\n\t" \
    "ds_read_b64_tr_b16 v[192:193], v244 offset:64\n\t" \
    "ds_read_b64_tr_b16 v[194:195], v244 offset:1600\n\t" \
    "s_mov_b32 s43, 0\n\t" \
    "v_cmp_eq_u32 vcc, 0, v252\n\t" \
    "s_cbranch_vccnz .Lv13_%=\n\t" \
    "s_mov_b32 m0, 0x1000\n\t" \
    "s_add_u32 s49, s42, 0x0\n\t" \
    "buffer_load_dwordx4 v253, s[60:63], s49 offen offset:0 sc1 lds\n\t" \
    "buffer_load_dwordx4 v253, s[60:63], s49 offen offset:1024 sc1 lds\n\t" \
    "buffer_load_dwordx4 v253, s[60:63], s49 offen offset:2048 sc1 lds\n\t" \
    "buffer_load_dwordx4 v253, s[60:63], s49 offen offset:3072 sc1 lds\n\t" \
    "s_mov_b32 m0, 0x4400\n\t" \
    "s_add_u32 s49, s42, 0xc400\n\t" \
    "buffer_load_dwordx4 v253, s[60:63], s49 offen offset:0 sc1 lds\n\t" \
    "s_mov_b32 m0, 0x6000\n\t" \
    "s_add_u32 s49, s42, 0x1000\n\t" \
    "buffer_load_dwordx4 v253, s[60:63], s49 offen offset:0 sc1 lds\n\t" \
    "buffer_load_dwordx4 v253, s[60:63], s49 offen offset:1024 sc1 lds\n\t" \
    "buffer_load_dwordx4 v253, s[60:63], s49 offen offset:2048 sc1 lds\n\t" \
    "buffer_load_dwordx4 v253, s[60:63], s49 offen offset:3072 sc1 lds\n\t" \
    "s_mov_b32 m0, 0x9400\n\t" \
    "s_add_u32 s49, s42, 0xd400\n\t" \
    "buffer_load_dwordx4 v253, s[60:63], s49 offen offset:0 sc1 lds\n\t" \
    "s_mov_b32 s43, 1\n\t" \
    ".Lv13_%=:\n\t" \
    "v_add_u32 v244, 0xf000, v243\n\t" \
    "s_waitcnt vmcnt(0) lgkmcnt(0)\n\t" \
    "s_barrier \n\t" \
    "ds_read_b64_tr_b16 v[180:181], v244 offset:0\n\t" \
    "ds_read_b64_tr_b16 v[182:183], v244 offset:1536\n\t" \
    "ds_read_b64_tr_b16 v[188:189], v244 offset:32\n\t" \
    "ds_read_b64_tr_b16 v[190:191], v244 offset:1568\n\t" \
    "ds_read_b64_tr_b16 v[196:197], v244 offset:64\n\t" \
    "ds_read_b64_tr_b16 v[198:199], v244 offset:1600\n\t" \
    "s_waitcnt lgkmcnt(0)\n\t" \
    "s_barrier \n\t" \
    "ds_write_b128 v254, v[200:203] offset:40960\n\t" \
    "ds_write_b128 v254, v[204:207] offset:41984\n\t" \
    "ds_write_b128 v254, v[208:211] offset:43008\n\t" \
    "ds_write_b128 v254, v[212:215] offset:44032\n\t" \
    "ds_write_b128 v254, v[216:219] offset:45056\n\t" \
    "ds_write_b128 v254, v[220:223] offset:46080\n\t" \
    "ds_write_b128 v254, v[224:227] offset:47104\n\t" \
    "ds_write_b128 v254, v[228:231] offset:48128\n\t" \
    "ds_write_b128 v254, v[232:235] offset:49152\n\t" \
    "ds_write_b128 v254, v[236:239] offset:50176\n\t" \
    "s_cmp_lg_u32 s43, 0\n\t" \
    "s_cbranch_scc1 .Lv14_%=\n\t" \
    "s_mov_b32 s44, 0\n\t" \
    ".Lv15_%=:\n\t" \
    "v_cmp_ge_u32 vcc, s44, v252\n\t" \
    "s_cbranch_vccz .Lv16_%=\n\t" \
    "s_cmp_eq_u32 s41, 0\n\t" \
    "s_cbranch_scc1 .Lv16_%=\n\t" \
    "s_sub_u32 s41, s41, 1\n\t" \
    "s_sleep 8\n\t" \
    "v_and_b32 v252, 0xf0, v253\n\t" \
    "v_lshlrev_b32 v252, 3, v252\n\t" \
    "buffer_load_dword v252, v252, s[60:63], %[fl] offen sc1\n\t" \
    "s_waitcnt vmcnt(0)\n\t" \
    "s_branch .Lv15_%=\n\t" \
    ".Lv16_%=:\n\t" \
    "s_mov_b32 m0, 0x1000\n\t" \
    "s_add_u32 s49, s42, 0x0\n\t" \
    "buffer_load_dwordx4 v253, s[60:63], s49 offen offset:0 sc1 lds\n\t" \
    "buffer_load_dwordx4 v253, s[60:63], s49 offen offset:1024 sc1 lds\n\t" \
    "buffer_load_dwordx4 v253, s[60:63], s49 offen offset:2048 sc1 lds\n\t" \
    "buffer_load_dwordx4 v253, s[60:63], s49 offen offset:3072 sc1 lds\n\t" \
    "s_mov_b32 m0, 0x4400\n\t" \
    "s_add_u32 s49, s42, 0xc400\n\t" \
    "buffer_load_dwordx4 v253, s[60:63], s49 offen offset:0 sc1 lds\n\t" \
    "s_mov_b32 m0, 0x6000\n\t" \
    "s_add_u32 s49, s42, 0x1000\n\t" \
    "buffer_load_dwordx4 v253, s[60:63], s49 offen offset:0 sc1 lds\n\t" \
    "buffer_load_dwordx4 v253, s[60:63], s49 offen offset:1024 sc1 lds\n\t" \
    "buffer_load_dwordx4 v253, s[60:63], s49 offen offset:2048 sc1 lds\n\t" \
    "buffer_load_dwordx4 v253, s[60:63], s49 offen offset:3072 sc1 lds\n\t" \
    "s_mov_b32 m0, 0x9400\n\t" \
    "s_add_u32 s49, s42, 0xd400\n\t" \
    "buffer_load_dwordx4 v253, s[60:63], s49 offen offset:0 sc1 lds\n\t" \
    ".Lv14_%=:\n\t" \
    "s_waitcnt vmcnt(0) lgkmcnt(0)\n\t" \
    "s_barrier \n\t" \
    "ds_read_b128 v[224:227], v253 offset:4096\n\t" \
    "ds_read_b128 v[244:247], v254 offset:40960\n\t" \
    "ds_read_b128 v[228:231], v253 offset:5120\n\t" \
    "ds_read_b128 v[232:235], v253 offset:24576\n\t" \
    "ds_read_b128 v[236:239], v253 offset:25600\n\t" \
    "ds_read_b128 v[248:251], v254 offset:41984\n\t" \
    ".Lv17_%=:\n\t" \
    "s_waitcnt lgkmcnt(5)\n\t" \
    "v_mfma_f32_16x16x32_bf16 v[204:207], v[0:3], v[224:227], 0\n\t" \
    "s_waitcnt lgkmcnt(3)\n\t" \
    "v_mfma_f32_16x16x32_bf16 v[204:207], v[4:7], v[228:231], v[204:207]\n\t" \
    "ds_read_b128 v[240:243], v253 offset:12288\n\t" \
    "v_mfma_f32_16x16x32_bf16 v[208:211], v[8:11], v[224:227], 0\n\t" \
    "v_mfma_f32_16x16x32_bf16 v[208:211], v[12:15], v[228:231], v[208:211]\n\t" \
    "v_mfma_f32_16x16x32_bf16 v[200:203], v[244:247], v[224:227], 0\n\t" \
    "s_waitcnt lgkmcnt(1)\n\t" \
    "v_mfma_f32_16x16x32_bf16 v[200:203], v[248:251], v[228:231], v[200:203]\n\t" \
    "v_mfma_f32_16x16x32_bf16 v[216:219], v[0:3], v[232:235], 0\n\t" \
    "ds_read_b128 v[224:227], v253 offset:13312\n\t" \
    "v_mfma_f32_16x16x32_bf16 v[216:219], v[4:7], v[236:239], v[216:219]\n\t" \
    "ds_read_b128 v[228:231], v253 offset:6144\n\t" \
    "v_mfma_f32_16x16x32_bf16 v[220:223], v[8:11], v[232:235], 0\n\t" \
    "v_mfma_f32_16x16x32_bf16 v[220:223], v[12:15], v[236:239], v[220:223]\n\t" \
    "v_mfma_f32_16x16x32_bf16 v[212:215], v[244:247], v[232:235], 0\n\t" \
    "ds_read_b128 v[244:247], v254 offset:43008\n\t" \
    "v_mfma_f32_16x16x32_bf16 v[212:215], v[248:251], v[236:239], v[212:215]\n\t" \
    "ds_read_b128 v[248:251], v254 offset:44032\n\t" \
    "s_waitcnt lgkmcnt(4)\n\t" \
    "v_mfma_f32_16x16x32_bf16 v[204:207], v[24:27], v[240:243], v[204:207]\n\t" \
    "ds_read_b128 v[232:235], v253 offset:7168\n\t" \
    "s_waitcnt lgkmcnt(4)\n\t" \
    "v_mfma_f32_16x16x32_bf16 v[204:207], v[28:31], v[224:227], v[204:207]\n\t" \
    "ds_read_b128 v[236:239], v253 offset:26624\n\t" \
    "v_mfma_f32_16x16x32_bf16 v[208:211], v[32:35], v[240:243], v[208:211]\n\t" \
    "v_mfma_f32_16x16x32_bf16 v[208:211], v[36:39], v[224:227], v[208:211]\n\t" \
    "v_mfma_f32_16x16x32_bf16 v[200:203], v[16:19], v[240:243], v[200:203]\n\t" \
    "v_mfma_f32_16x16x32_bf16 v[200:203], v[20:23], v[224:227], v[200:203]\n\t" \
    "s_waitcnt lgkmcnt(4)\n\t" \
    "v_mfma_f32_16x16x32_bf16 v[204:207], v[40:43], v[228:231], v[204:207]\n\t" \
    "ds_read_b128 v[240:243], v253 offset:27648\n\t" \
    "s_waitcnt lgkmcnt(2)\n\t" \
    "v_mfma_f32_16x16x32_bf16 v[204:207], v[44:47], v[232:235], v[204:207]\n\t" \
    "ds_read_b128 v[224:227], v253 offset:0\n\t" \
    "v_mfma_f32_16x16x32_bf16 v[208:211], v[48:51], v[228:231], v[208:211]\n\t" \
    "v_mfma_f32_16x16x32_bf16 v[208:211], v[52:55], v[232:235], v[208:211]\n\t" \
    "v_mfma_f32_16x16x32_bf16 v[200:203], v[244:247], v[228:231], v[200:203]\n\t" \
    "v_mfma_f32_16x16x32_bf16 v[200:203], v[248:251], v[232:235], v[200:203]\n\t" \
    "s_waitcnt lgkmcnt(2)\n\t" \
    "v_mfma_f32_16x16x32_bf16 v[216:219], v[40:43], v[236:239], v[216:219]\n\t" \
    "ds_read_b128 v[228:231], v253 offset:1024\n\t" \
    "s_add_u32 s44, s40, 1\n\t" \
    "s_waitcnt lgkmcnt(2)\n\t" \
    "v_mfma_f32_16x16x32_bf16 v[216:219], v[44:47], v[240:243], v[216:219]\n\t" \
    "ds_read_b128 v[232:235], v253 offset:10240\n\t" \
    "s_and_b32 s45, s44, 15\n\t" \
    "v_mfma_f32_16x16x32_bf16 v[220:223], v[48:51], v[236:239], v[220:223]\n\t" \
    "s_cmp_eq_u32 s45, 0\n\t" \
    "v_mfma_f32_16x16x32_bf16 v[220:223], v[52:55], v[240:243], v[220:223]\n\t" \
    "s_cselect_b32 s45, %[cj], %[cs]\n\t" \
    "v_mfma_f32_16x16x32_bf16 v[212:215], v[244:247], v[236:239], v[212:215]\n\t" \
    "ds_read_b128 v[244:247], v254 offset:45056\n\t" \
    "s_cmp_eq_u32 s44, 64\n\t" \
    "v_mfma_f32_16x16x32_bf16 v[212:215], v[248:251], v[240:243], v[212:215]\n\t" \
    "ds_read_b128 v[248:251], v254 offset:46080\n\t" \
    "s_cselect_b32 s45, 0, s45\n\t" \
    "s_waitcnt lgkmcnt(4)\n\t" \
    "v_mfma_f32_16x16x32_bf16 v[204:207], v[64:67], v[224:227], v[204:207]\n\t" \
    "ds_read_b128 v[236:239], v253 offset:11264\n\t" \
    "s_add_u32 s42, s42, s45\n\t" \
    "s_waitcnt lgkmcnt(4)\n\t" \
    "v_mfma_f32_16x16x32_bf16 v[204:207], v[68:71], v[228:231], v[204:207]\n\t" \
    "ds_read_b128 v[240:243], v253 offset:30720\n\t" \
    "v_mfma_f32_16x16x32_bf16 v[208:211], v[72:75], v[224:227], v[208:211]\n\t" \
    "v_mfma_f32_16x16x32_bf16 v[208:211], v[76:79], v[228:231], v[208:211]\n\t" \
    "v_mfma_f32_16x16x32_bf16 v[200:203], v[56:59], v[224:227], v[200:203]\n\t" \
    "v_mfma_f32_16x16x32_bf16 v[200:203], v[60:63], v[228:231], v[200:203]\n\t" \
    "s_waitcnt lgkmcnt(4)\n\t" \
    "v_mfma_f32_16x16x32_bf16 v[204:207], v[80:83], v[232:235], v[204:207]\n\t" \
    "ds_read_b128 v[224:227], v253 offset:31744\n\t" \
    "s_waitcnt lgkmcnt(2)\n\t" \
    "v_mfma_f32_16x16x32_bf16 v[204:207], v[84:87], v[236:239], v[204:207]\n\t" \
    "ds_read_b128 v[228:231], v253 offset:2048\n\t" \
    "v_mfma_f32_16x16x32_bf16 v[208:211], v[88:91], v[232:235], v[208:211]\n\t" \
    "v_mfma_f32_16x16x32_bf16 v[208:211], v[92:95], v[236:239], v[208:211]\n\t" \
    "v_mfma_f32_16x16x32_bf16 v[200:203], v[244:247], v[232:235], v[200:203]\n\t" \
    "v_mfma_f32_16x16x32_bf16 v[200:203], v[248:251], v[236:239], v[200:203]\n\t" \
    "s_waitcnt lgkmcnt(2)\n\t" \
    "v_mfma_f32_16x16x32_bf16 v[216:219], v[80:83], v[240:243], v[216:219]\n\t" \
    "ds_read_b128 v[232:235], v253 offset:3072\n\t" \
    "s_and_b32 s44, s40, 3\n\t" \
    "s_waitcnt lgkmcnt(2)\n\t" \
    "v_mfma_f32_16x16x32_bf16 v[216:219], v[84:87], v[224:227], v[216:219]\n\t" \
    "ds_read_b128 v[236:239], v253 offset:14336\n\t" \
    "s_lshl_b32 s44, s44, 17\n\t" \
    "v_mfma_f32_16x16x32_bf16 v[220:223], v[88:91], v[240:243], v[220:223]\n\t" \
    "s_add_u32 s44, s44, %[mb]\n\t" \
    "v_mfma_f32_16x16x32_bf16 v[220:223], v[92:95], v[224:227], v[220:223]\n\t" \
    "s_add_u32 s46, s44, 0x800\n\t" \
    "v_mfma_f32_16x16x32_bf16 v[212:215], v[244:247], v[240:243], v[212:215]\n\t" \
    "ds_read_b128 v[244:247], v254 offset:47104\n\t" \
    "s_add_u32 s47, s44, 0x1800\n\t" \
    "v_mfma_f32_16x16x32_bf16 v[212:215], v[248:251], v[224:227], v[212:215]\n\t" \
    "ds_read_b128 v[248:251], v254 offset:48128\n\t" \
    "s_add_u32 s48, s44, 0xc00\n\t" \
    "s_waitcnt lgkmcnt(4)\n\t" \
    "v_mfma_f32_16x16x32_bf16 v[204:207], v[104:107], v[228:231], v[204:207]\n\t" \
    "ds_read_b128 v[240:243], v253 offset:15360\n\t" \
    "s_waitcnt lgkmcnt(4)\n\t" \
    "v_mfma_f32_16x16x32_bf16 v[204:207], v[108:111], v[232:235], v[204:207]\n\t" \
    "ds_read_b128 v[224:227], v253 offset:34816\n\t" \
    "v_mfma_f32_16x16x32_bf16 v[208:211], v[112:115], v[228:231], v[208:211]\n\t" \
    "v_mfma_f32_16x16x32_bf16 v[208:211], v[116:119], v[232:235], v[208:211]\n\t" \
    "v_mfma_f32_16x16x32_bf16 v[200:203], v[96:99], v[228:231], v[200:203]\n\t" \
    "v_mfma_f32_16x16x32_bf16 v[200:203], v[100:103], v[232:235], v[200:203]\n\t" \
    "s_waitcnt lgkmcnt(4)\n\t" \
    "v_mfma_f32_16x16x32_bf16 v[204:207], v[120:123], v[236:239], v[204:207]\n\t" \
    "ds_read_b128 v[228:231], v253 offset:35840\n\t" \
    "s_waitcnt lgkmcnt(2)\n\t" \
    "v_mfma_f32_16x16x32_bf16 v[204:207], v[124:127], v[240:243], v[204:207]\n\t" \
    "ds_read_b128 v[232:235], v253 offset:8192\n\t" \
    "v_mfma_f32_16x16x32_bf16 v[208:211], v[128:131], v[236:239], v[208:211]\n\t" \
    "v_mfma_f32_16x16x32_bf16 v[208:211], v[132:135], v[240:243], v[208:211]\n\t" \
    "v_mfma_f32_16x16x32_bf16 v[200:203], v[244:247], v[236:239], v[200:203]\n\t" \
    "v_mfma_f32_16x16x32_bf16 v[200:203], v[248:251], v[240:243], v[200:203]\n\t" \
    "s_waitcnt lgkmcnt(2)\n\t" \
    "v_mfma_f32_16x16x32_bf16 v[216:219], v[120:123], v[224:227], v[216:219]\n\t" \
    "ds_read_b128 v[236:239], v253 offset:9216\n\t" \
    "s_waitcnt lgkmcnt(2)\n\t" \
    "v_mfma_f32_16x16x32_bf16 v[216:219], v[124:127], v[228:231], v[216:219]\n\t" \
    "ds_read_b128 v[240:243], v253 offset:16384\n\t" \
    "v_mfma_f32_16x16x32_bf16 v[220:223], v[128:131], v[224:227], v[220:223]\n\t" \
    "v_mfma_f32_16x16x32_bf16 v[220:223], v[132:135], v[228:231], v[220:223]\n\t" \
    "v_mfma_f32_16x16x32_bf16 v[212:215], v[244:247], v[224:227], v[212:215]\n\t" \
    "ds_read_b128 v[244:247], v254 offset:49152\n\t" \
    "v_mfma_f32_16x16x32_bf16 v[212:215], v[248:251], v[228:231], v[212:215]\n\t" \
    "ds_read_b128 v[248:251], v254 offset:50176\n\t" \
    "v_and_b32 v252, 0xf0, v253\n\t" \
    "v_lshlrev_b32 v252, 3, v252\n\t" \
    "buffer_load_dword v252, v252, s[60:63], %[fl] offen sc1\n\t" \
    "s_waitcnt lgkmcnt(4)\n\t" \
    "v_mfma_f32_16x16x32_bf16 v[204:207], v[144:147], v[232:235], v[204:207]\n\t" \
    "ds_read_b128 v[224:227], v253 offset:17408\n\t" \
    "s_waitcnt lgkmcnt(4)\n\t" \
    "v_mfma_f32_16x16x32_bf16 v[204:207], v[148:151], v[236:239], v[204:207]\n\t" \
    "ds_read_b128 v[228:231], v253 offset:36864\n\t" \
    "v_mfma_f32_16x16x32_bf16 v[208:211], v[152:155], v[232:235], v[208:211]\n\t" \
    "v_mfma_f32_16x16x32_bf16 v[208:211], v[156:159], v[236:239], v[208:211]\n\t" \
    "v_mfma_f32_16x16x32_bf16 v[200:203], v[136:139], v[232:235], v[200:203]\n\t" \
    "v_mfma_f32_16x16x32_bf16 v[200:203], v[140:143], v[236:239], v[200:203]\n\t" \
    "s_waitcnt lgkmcnt(4)\n\t" \
    "v_mfma_f32_16x16x32_bf16 v[204:207], v[160:163], v[240:243], v[204:207]\n\t" \
    "ds_read_b128 v[232:235], v253 offset:37888\n\t" \
    "s_waitcnt lgkmcnt(2)\n\t" \
    "v_mfma_f32_16x16x32_bf16 v[204:207], v[164:167], v[224:227], v[204:207]\n\t" \
    "ds_read_b128 v[236:239], v253 offset:18432\n\t" \
    "v_mfma_f32_16x16x32_bf16 v[208:211], v[168:171], v[240:243], v[208:211]\n\t" \
    "v_mfma_f32_16x16x32_bf16 v[208:211], v[172:175], v[224:227], v[208:211]\n\t" \
    "v_mfma_f32_16x16x32_bf16 v[200:203], v[244:247], v[240:243], v[200:203]\n\t" \
    "v_mfma_f32_16x16x32_bf16 v[200:203], v[248:251], v[224:227], v[200:203]\n\t" \
    "s_waitcnt lgkmcnt(2)\n\t" \
    "v_mfma_f32_16x16x32_bf16 v[216:219], v[160:163], v[228:231], v[216:219]\n\t" \
    "ds_read_b128 v[240:243], v253 offset:19456\n\t" \
    "s_waitcnt lgkmcnt(2)\n\t" \
    "v_mfma_f32_16x16x32_bf16 v[216:219], v[164:167], v[232:235], v[216:219]\n\t" \
    "ds_read_b128 v[224:227], v253 offset:32768\n\t" \
    "v_mfma_f32_16x16x32_bf16 v[220:223], v[168:171], v[228:231], v[220:223]\n\t" \
    "v_mfma_f32_16x16x32_bf16 v[220:223], v[172:175], v[232:235], v[220:223]\n\t" \
    "v_mfma_f32_16x16x32_bf16 v[212:215], v[244:247], v[228:231], v[212:215]\n\t" \
    "v_mfma_f32_16x16x32_bf16 v[212:215], v[248:251], v[232:235], v[212:215]\n\t" \
    "s_waitcnt lgkmcnt(2)\n\t" \
    "v_mfma_f32_16x16x32_bf16 v[204:207], v[184:187], v[236:239], v[204:207]\n\t" \
    "ds_read_b128 v[228:231], v253 offset:33792\n\t" \
    "s_waitcnt lgkmcnt(2)\n\t" \
    "v_mfma_f32_16x16x32_bf16 v[204:207], v[188:191], v[240:243], v[204:207]\n\t" \
    "ds_read_b128 v[232:235], v253 offset:20480\n\t" \
    "v_mfma_f32_16x16x32_bf16 v[208:211], v[192:195], v[236:239], v[208:211]\n\t" \
    "v_mfma_f32_16x16x32_bf16 v[208:211], v[196:199], v[240:243], v[208:211]\n\t" \
    "v_mfma_f32_16x16x32_bf16 v[200:203], v[176:179], v[236:239], v[200:203]\n\t" \
    "v_mfma_f32_16x16x32_bf16 v[200:203], v[180:183], v[240:243], v[200:203]\n\t" \
    "s_waitcnt lgkmcnt(2)\n\t" \
    "v_mfma_f32_16x16x32_bf16 v[216:219], v[24:27], v[224:227], v[216:219]\n\t" \
    "ds_read_b128 v[236:239], v253 offset:21504\n\t" \
    "s_waitcnt lgkmcnt(2)\n\t" \
    "v_mfma_f32_16x16x32_bf16 v[216:219], v[28:31], v[228:231], v[216:219]\n\t" \
    "ds_read_b128 v[240:243], v253 offset:22528\n\t" \
    "v_mfma_f32_16x16x32_bf16 v[220:223], v[32:35], v[224:227], v[220:223]\n\t" \
    "v_cvt_pk_bf16_f32 v204, v204, v205\n\t" \
    "v_mfma_f32_16x16x32_bf16 v[220:223], v[36:39], v[228:231], v[220:223]\n\t" \
    "v_cvt_pk_bf16_f32 v205, v206, v207\n\t" \
    "v_mfma_f32_16x16x32_bf16 v[212:215], v[16:19], v[224:227], v[212:215]\n\t" \
    "ds_write_b64 v254, v[204:205] offset:10240\n\t" \
    "v_mfma_f32_16x16x32_bf16 v[212:215], v[20:23], v[228:231], v[212:215]\n\t" \
    "v_cvt_pk_bf16_f32 v208, v208, v209\n\t" \
    "s_waitcnt lgkmcnt(3)\n\t" \
    "v_mfma_f32_16x16x32_bf16 v[216:219], v[64:67], v[232:235], v[216:219]\n\t" \
    "ds_read_b128 v[224:227], v253 offset:23552\n\t" \
    "v_cvt_pk_bf16_f32 v209, v210, v211\n\t" \
    "s_waitcnt lgkmcnt(3)\n\t" \
    "v_mfma_f32_16x16x32_bf16 v[216:219], v[68:71], v[236:239], v[216:219]\n\t" \
    "ds_read_b128 v[228:231], v253 offset:28672\n\t" \
    "ds_write_b64 v254, v[208:209] offset:11264\n\t" \
    "v_mfma_f32_16x16x32_bf16 v[220:223], v[72:75], v[232:235], v[220:223]\n\t" \
    "v_cvt_pk_bf16_f32 v200, v200, v201\n\t" \
    "v_mfma_f32_16x16x32_bf16 v[220:223], v[76:79], v[236:239], v[220:223]\n\t" \
    "v_cvt_pk_bf16_f32 v201, v202, v203\n\t" \
    "v_mfma_f32_16x16x32_bf16 v[212:215], v[56:59], v[232:235], v[212:215]\n\t" \
    "ds_write_b64 v254, v[200:201] offset:9216\n\t" \
    "v_mfma_f32_16x16x32_bf16 v[212:215], v[60:63], v[236:239], v[212:215]\n\t" \
    "s_waitcnt lgkmcnt(5)\n\t" \
    "v_mfma_f32_16x16x32_bf16 v[216:219], v[104:107], v[240:243], v[216:219]\n\t" \
    "ds_read_b128 v[232:235], v253 offset:29696\n\t" \
    "s_waitcnt lgkmcnt(4)\n\t" \
    "v_mfma_f32_16x16x32_bf16 v[216:219], v[108:111], v[224:227], v[216:219]\n\t" \
    "ds_read_b128 v[236:239], v253 offset:38912\n\t" \
    "v_mfma_f32_16x16x32_bf16 v[220:223], v[112:115], v[240:243], v[220:223]\n\t" \
    "v_mfma_f32_16x16x32_bf16 v[220:223], v[116:119], v[224:227], v[220:223]\n\t" \
    "v_mfma_f32_16x16x32_bf16 v[212:215], v[96:99], v[240:243], v[212:215]\n\t" \
    "v_mfma_f32_16x16x32_bf16 v[212:215], v[100:103], v[224:227], v[212:215]\n\t" \
    "ds_read_b128 v[240:243], v253 offset:39936\n\t" \
    "s_waitcnt lgkmcnt(3)\n\t" \
    "s_barrier \n\t" \
    "v_mfma_f32_16x16x32_bf16 v[216:219], v[144:147], v[228:231], v[216:219]\n\t" \
    "s_waitcnt lgkmcnt(2)\n\t" \
    "v_mfma_f32_16x16x32_bf16 v[216:219], v[148:151], v[232:235], v[216:219]\n\t" \
    "s_waitcnt lgkmcnt(1)\n\t" \
    "v_mfma_f32_16x16x32_bf16 v[216:219], v[184:187], v[236:239], v[216:219]\n\t" \
    "s_waitcnt lgkmcnt(0)\n\t" \
    "v_mfma_f32_16x16x32_bf16 v[216:219], v[188:191], v[240:243], v[216:219]\n\t" \
    "v_mfma_f32_16x16x32_bf16 v[220:223], v[152:155], v[228:231], v[220:223]\n\t" \
    "v_mfma_f32_16x16x32_bf16 v[220:223], v[156:159], v[232:235], v[220:223]\n\t" \
    "v_mfma_f32_16x16x32_bf16 v[220:223], v[192:195], v[236:239], v[220:223]\n\t" \
    "v_cvt_pk_bf16_f32 v216, v216, v217\n\t" \
    "v_cvt_pk_bf16_f32 v217, v218, v219\n\t" \
    "v_mfma_f32_16x16x32_bf16 v[220:223], v[196:199], v[240:243], v[220:223]\n\t" \
    "ds_write_b64 v254, v[216:217] offset:16384\n\t" \
    "v_mfma_f32_16x16x32_bf16 v[212:215], v[136:139], v[228:231], v[212:215]\n\t" \
    "v_mfma_f32_16x16x32_bf16 v[212:215], v[140:143], v[232:235], v[212:215]\n\t" \
    "v_mfma_f32_16x16x32_bf16 v[212:215], v[176:179], v[236:239], v[212:215]\n\t" \
    "v_cvt_pk_bf16_f32 v220, v220, v221\n\t" \
    "v_cvt_pk_bf16_f32 v221, v222, v223\n\t" \
    "v_mfma_f32_16x16x32_bf16 v[212:215], v[180:183], v[240:243], v[212:215]\n\t" \
    "ds_write_b64 v254, v[220:221] offset:17408\n\t" \
    "s_nop 6\n\t" \
    "v_cvt_pk_bf16_f32 v212, v212, v213\n\t" \
    "v_cvt_pk_bf16_f32 v213, v214, v215\n\t" \
    "ds_write_b64 v254, v[212:213] offset:15360\n\t" \
    "s_waitcnt vmcnt(0) lgkmcnt(0)\n\t" \
    "s_barrier \n\t" \
    "s_setprio 2\n\t" \
    "s_add_u32 s44, s40, 1\n\t" \
    "s_min_u32 s44, s44, 63\n\t" \
    ".Lv18_%=:\n\t" \
    "v_cmp_ge_u32 vcc, s44, v252\n\t" \
    "s_cbranch_vccz .Lv19_%=\n\t" \
    "s_cmp_eq_u32 s41, 0\n\t" \
    "s_cbranch_scc1 .Lv19_%=\n\t" \
    "s_sub_u32 s41, s41, 1\n\t" \
    "s_sleep 2\n\t" \
    "v_and_b32 v252, 0xf0, v253\n\t" \
    "v_lshlrev_b32 v252, 3, v252\n\t" \
    "buffer_load_dword v252, v252, s[60:63], %[fl] offen sc1\n\t" \
    "s_waitcnt vmcnt(0)\n\t" \
    "s_branch .Lv18_%=\n\t" \
    ".Lv19_%=:\n\t" \
    "s_mov_b32 m0, 0x1000\n\t" \
    "s_add_u32 s49, s42, 0x0\n\t" \
    "buffer_load_dwordx4 v253, s[60:63], s49 offen offset:0 sc1 lds\n\t" \
    "s_sleep 1\n\t" \
    "buffer_load_dwordx4 v253, s[60:63], s49 offen offset:1024 sc1 lds\n\t" \
    "s_sleep 1\n\t" \
    "buffer_load_dwordx4 v253, s[60:63], s49 offen offset:2048 sc1 lds\n\t" \
    "s_sleep 1\n\t" \
    "buffer_load_dwordx4 v253, s[60:63], s49 offen offset:3072 sc1 lds\n\t" \
    "s_sleep 1\n\t" \
    "s_mov_b32 m0, 0x4400\n\t" \
    "s_add_u32 s49, s42, 0xc400\n\t" \
    "buffer_load_dwordx4 v253, s[60:63], s49 offen offset:0 sc1 lds\n\t" \
    "s_sleep 1\n\t" \
    "s_mov_b32 m0, 0x6000\n\t" \
    "s_add_u32 s49, s42, 0x1000\n\t" \
    "buffer_load_dwordx4 v253, s[60:63], s49 offen offset:0 sc1 lds\n\t" \
    "s_sleep 1\n\t" \
    "buffer_load_dwordx4 v253, s[60:63], s49 offen offset:1024 sc1 lds\n\t" \
    "s_sleep 1\n\t" \
    "buffer_load_dwordx4 v253, s[60:63], s49 offen offset:2048 sc1 lds\n\t" \
    "s_sleep 1\n\t" \
    "buffer_load_dwordx4 v253, s[60:63], s49 offen offset:3072 sc1 lds\n\t" \
    "s_sleep 1\n\t" \
    "s_mov_b32 m0, 0x9400\n\t" \
    "s_add_u32 s49, s42, 0xd400\n\t" \
    "buffer_load_dwordx4 v253, s[60:63], s49 offen offset:0 sc1 lds\n\t" \
    "s_sleep 1\n\t" \
    "ds_read_b128 v[200:203], v254 offset:40960\n\t" \
    "ds_read_b128 v[204:207], v254 offset:41984\n\t" \
    "ds_read_b128 v[216:219], v254 offset:43008\n\t" \
    "ds_read_b128 v[220:223], v254 offset:44032\n\t" \
    "v_mov_b64 v[208:209], 0\n\t" \
    "v_mov_b64 v[210:211], 0\n\t" \
    "v_mov_b64 v[244:245], 0\n\t" \
    "v_mov_b64 v[246:247], 0\n\t" \
    "v_mov_b64 v[248:249], 0\n\t" \
    "v_mov_b64 v[250:251], 0\n\t" \
    "s_sleep 5\n\t" \
    "s_mov_b32 m0, 0xa800\n\t" \
    "s_nop 0\n\t" \
    "buffer_load_dwordx4 v253, s[60:63], s46 offen sc1 lds\n\t" \
    "s_mov_b32 m0, 0xb800\n\t" \
    "s_nop 0\n\t" \
    "buffer_load_dwordx4 v253, s[60:63], s47 offen sc1 lds\n\t" \
    "s_mov_b32 m0, 0xac00\n\t" \
    "s_nop 0\n\t" \
    "buffer_load_dwordx4 v253, s[60:63], s48 offen sc1 lds\n\t" \
    "s_waitcnt lgkmcnt(0)\n\t" \
    "v_bfe_i32 v212, v253, 5, 1\n\t" \
    "v_and_b32 v212, 0xaaaa, v212\n\t" \
    "v_or_b32 v212, 0x4444, v212\n\t" \
    "v_and_b32 v213, 0xffff, v255\n\t" \
    "v_lshrrev_b32 v214, 16, v255\n\t" \
    ".Lv110_%=:\n\t" \
    "s_waitcnt vmcnt(0)\n\t" \
    "ds_read_b128 v[224:227], v253 offset:43008\n\t" \
    "ds_read_b128 v[228:231], v253 offset:47104\n\t" \
    "ds_read_b128 v[232:235], v253 offset:44032\n\t" \
    "s_waitcnt lgkmcnt(2)\n\t" \
    "v_max_u32 v215, v224, v225\n\t" \
    "v_max3_u32 v215, v226, v227, v215\n\t" \
    "s_waitcnt lgkmcnt(1)\n\t" \
    "v_max3_u32 v215, v228, v229, v215\n\t" \
    "v_max3_u32 v215, v230, v231, v215\n\t" \
    "s_waitcnt lgkmcnt(0)\n\t" \
    "v_max3_u32 v215, v232, v233, v215\n\t" \
    "v_max3_u32 v215, v234, v235, v215\n\t" \
    "v_cmp_eq_u32 vcc, -1, v215\n\t" \
    "s_cbranch_vccz .Lv111_%=\n\t" \
    "v_max_u32 v215, v224, v225\n\t" \
    "v_max3_u32 v215, v226, v227, v215\n\t" \
    "v_cmp_eq_u32 vcc, -1, v215\n\t" \
    "s_cbranch_vccz .Lv112_%=\n\t" \
    "s_mov_b32 m0, 0xa800\n\t" \
    "s_nop 0\n\t" \
    "buffer_load_dwordx4 v253, s[60:63], s46 offen sc1 lds\n\t" \
    ".Lv112_%=:\n\t" \
    "v_max_u32 v215, v228, v229\n\t" \
    "v_max3_u32 v215, v230, v231, v215\n\t" \
    "v_cmp_eq_u32 vcc, -1, v215\n\t" \
    "s_cbranch_vccz .Lv113_%=\n\t" \
    "s_mov_b32 m0, 0xb800\n\t" \
    "s_nop 0\n\t" \
    "buffer_load_dwordx4 v253, s[60:63], s47 offen sc1 lds\n\t" \
    ".Lv113_%=:\n\t" \
    "v_max_u32 v215, v232, v233\n\t" \
    "v_max3_u32 v215, v234, v235, v215\n\t" \
    "v_cmp_eq_u32 vcc, -1, v215\n\t" \
    "s_cbranch_vccz .Lv114_%=\n\t" \
    "s_mov_b32 m0, 0xac00\n\t" \
    "s_nop 0\n\t" \
    "buffer_load_dwordx4 v253, s[60:63], s48 offen sc1 lds\n\t" \
    ".Lv114_%=:\n\t" \
    "s_cmp_eq_u32 s41, 0\n\t" \
    "s_cbranch_scc1 .Lv111_%=\n\t" \
    "s_sub_u32 s41, s41, 1\n\t" \
    "s_branch .Lv110_%=\n\t" \
    ".Lv111_%=:\n\t" \
    "s_waitcnt lgkmcnt(0)\n\t" \
    "s_barrier \n\t" \
    "ds_read_b128 v[236:239], v253 offset:40960\n\t" \
    "ds_read_b128 v[240:243], v253 offset:46080\n\t" \
    "v_smfmac_f32_16x16x64_bf16 v[244:247], v[224:227], v[0:7], v212\n\t" \
    "v_smfmac_f32_16x16x64_bf16 v[248:251], v[224:227], v[8:15], v212\n\t" \
    "v_smfmac_f32_16x16x64_bf16 v[208:211], v[224:227], v[200:207], v212\n\t" \
    "ds_read_b128 v[200:203], v254 offset:45056\n\t" \
    "ds_read_b128 v[204:207], v254 offset:46080\n\t" \
    "ds_read_b128 v[224:227], v253 offset:41984\n\t" \
    "v_smfmac_f32_16x16x64_bf16 v[208:211], v[228:231], v[16:23], v212\n\t" \
    "v_smfmac_f32_16x16x64_bf16 v[244:247], v[228:231], v[24:31], v212\n\t" \
    "v_smfmac_f32_16x16x64_bf16 v[248:251], v[228:231], v[32:39], v212\n\t" \
    "ds_read_b128 v[228:231], v253 offset:48128\n\t" \
    "v_smfmac_f32_16x16x64_bf16 v[208:211], v[232:235], v[216:223], v212\n\t" \
    "ds_read_b128 v[216:219], v254 offset:47104\n\t" \
    "ds_read_b128 v[220:223], v254 offset:48128\n\t" \
    "v_smfmac_f32_16x16x64_bf16 v[244:247], v[232:235], v[40:47], v212\n\t" \
    "v_smfmac_f32_16x16x64_bf16 v[248:251], v[232:235], v[48:55], v212\n\t" \
    "ds_read_b128 v[232:235], v253 offset:45056\n\t" \
    "s_waitcnt lgkmcnt(8)\n\t" \
    "v_smfmac_f32_16x16x64_bf16 v[208:211], v[236:239], v[56:63], v212\n\t" \
    "v_smfmac_f32_16x16x64_bf16 v[244:247], v[236:239], v[64:71], v212\n\t" \
    "v_smfmac_f32_16x16x64_bf16 v[248:251], v[236:239], v[72:79], v212\n\t" \
    "ds_read_b128 v[236:239], v253 offset:49152\n\t" \
    "s_waitcnt lgkmcnt(6)\n\t" \
    "v_smfmac_f32_16x16x64_bf16 v[208:211], v[240:243], v[200:207], v212\n\t" \
    "ds_read_b128 v[200:203], v254 offset:49152\n\t" \
    "ds_read_b128 v[204:207], v254 offset:50176\n\t" \
    "v_smfmac_f32_16x16x64_bf16 v[244:247], v[240:243], v[80:87], v212\n\t" \
    "v_smfmac_f32_16x16x64_bf16 v[248:251], v[240:243], v[88:95], v212\n\t" \
    "ds_read_b128 v[240:243], v253 offset:50176\n\t" \
    "s_waitcnt lgkmcnt(8)\n\t" \
    "v_smfmac_f32_16x16x64_bf16 v[208:211], v[224:227], v[96:103], v212\n\t" \
    "v_smfmac_f32_16x16x64_bf16 v[244:247], v[224:227], v[104:111], v212\n\t" \
    "v_smfmac_f32_16x16x64_bf16 v[248:251], v[224:227], v[112:119], v212\n\t" \
    "s_waitcnt lgkmcnt(7)\n\t" \
    "v_smfmac_f32_16x16x64_bf16 v[244:247], v[228:231], v[120:127], v212\n\t" \
    "s_waitcnt lgkmcnt(4)\n\t" \
    "v_smfmac_f32_16x16x64_bf16 v[244:247], v[232:235], v[144:151], v212\n\t" \
    "s_waitcnt lgkmcnt(3)\n\t" \
    "v_smfmac_f32_16x16x64_bf16 v[244:247], v[236:239], v[160:167], v212\n\t" \
    "s_waitcnt lgkmcnt(0)\n\t" \
    "v_smfmac_f32_16x16x64_bf16 v[244:247], v[240:243], v[184:191], v212\n\t" \
    "v_smfmac_f32_16x16x64_bf16 v[248:251], v[228:231], v[128:135], v212\n\t" \
    "v_smfmac_f32_16x16x64_bf16 v[248:251], v[232:235], v[152:159], v212\n\t" \
    "v_smfmac_f32_16x16x64_bf16 v[248:251], v[236:239], v[168:175], v212\n\t" \
    "v_add_f32 v244, v244, v246\n\t" \
    "v_add_f32 v245, v245, v247\n\t" \
    "v_cvt_pk_bf16_f32 v244, v244, v245\n\t" \
    "v_smfmac_f32_16x16x64_bf16 v[248:251], v[240:243], v[192:199], v212\n\t" \
    "ds_write_b16 v213, v244 offset:512\n\t" \
    "ds_write_b16_d16_hi v213, v244 offset:528\n\t" \
    "v_smfmac_f32_16x16x64_bf16 v[208:211], v[228:231], v[216:223], v212\n\t" \
    "v_smfmac_f32_16x16x64_bf16 v[208:211], v[232:235], v[136:143], v212\n\t" \
    "v_smfmac_f32_16x16x64_bf16 v[208:211], v[236:239], v[200:207], v212\n\t" \
    "v_add_f32 v248, v248, v250\n\t" \
    "v_add_f32 v249, v249, v251\n\t" \
    "v_cvt_pk_bf16_f32 v248, v248, v249\n\t" \
    "v_smfmac_f32_16x16x64_bf16 v[208:211], v[240:243], v[176:183], v212\n\t" \
    "ds_write_b16 v213, v248 offset:1024\n\t" \
    "ds_write_b16_d16_hi v213, v248 offset:1040\n\t" \
    "s_nop 5\n\t" \
    "v_add_f32 v208, v208, v210\n\t" \
    "v_add_f32 v209, v209, v211\n\t" \
    "v_cvt_pk_bf16_f32 v208, v208, v209\n\t" \
    "ds_write_b16 v213, v208 offset:0\n\t" \
    "ds_write_b16_d16_hi v213, v208 offset:16\n\t" \
    "s_setprio 0\n\t" \
    "s_waitcnt lgkmcnt(0)\n\t" \
    "s_barrier \n\t" \
    "ds_read_b128 v[224:227], v253 offset:4096\n\t" \
    "ds_read_b128 v[244:247], v254 offset:40960\n\t" \
    "ds_read_b128 v[228:231], v253 offset:5120\n\t" \
    "ds_read_b128 v[232:235], v253 offset:24576\n\t" \
    "ds_read_b128 v[236:239], v253 offset:25600\n\t" \
    "ds_read_b128 v[248:251], v254 offset:41984\n\t" \
    "s_add_u32 s40, s40, 1\n\t" \
    "s_cmp_lg_u32 s40, 64\n\t" \
    "s_cbranch_scc1 .Lv17_%=\n\t" \
    "s_branch .Lend%=\n\t" \
    ".Lv0_%=:\n\t" \
    "s_mov_b32 s42, %[xs]\n\t" \
    "v_add_u32 v244, 0xc000, v243\n\t" \
    "s_waitcnt vmcnt(12) lgkmcnt(0)\n\t" \
    "s_barrier \n\t" \
    "s_add_u32 m0, %[wm], 0x9000\n\t" \
    "s_mov_b32 s44, 0x1e0000\n\t" \
    "buffer_load_dwordx4 v240, %[wr], s44 offen offset:0 nt lds\n\t" \
    "buffer_load_dwordx4 v241, %[wr], s44 offen offset:1024 nt lds\n\t" \
    "buffer_load_dwordx4 v242, %[wr], s44 offen offset:2048 nt lds\n\t" \
    "ds_read_b64_tr_b16 v[200:201], v244 offset:0\n\t" \
    "ds_read_b64_tr_b16 v[202:203], v244 offset:1536\n\t" \
    "ds_read_b64_tr_b16 v[0:1], v244 offset:32\n\t" \
    "ds_read_b64_tr_b16 v[2:3], v244 offset:1568\n\t" \
    "ds_read_b64_tr_b16 v[8:9], v244 offset:64\n\t" \
    "ds_read_b64_tr_b16 v[10:11], v244 offset:1600\n\t" \
    "v_add_u32 v244, 0xf000, v243\n\t" \
    "s_waitcnt vmcnt(12) lgkmcnt(0)\n\t" \
    "s_barrier \n\t" \
    "s_add_u32 m0, %[wm], 0xc000\n\t" \
    "s_mov_b32 s44, 0x240000\n\t" \
    "buffer_load_dwordx4 v240, %[wr], s44 offen offset:0 nt lds\n\t" \
    "buffer_load_dwordx4 v241, %[wr], s44 offen offset:1024 nt lds\n\t" \
    "buffer_load_dwordx4 v242, %[wr], s44 offen offset:2048 nt lds\n\t" \
    "ds_read_b64_tr_b16 v[204:205], v244 offset:0\n\t" \
    "ds_read_b64_tr_b16 v[206:207], v244 offset:1536\n\t" \
    "ds_read_b64_tr_b16 v[4:5], v244 offset:32\n\t" \
    "ds_read_b64_tr_b16 v[6:7], v244 offset:1568\n\t" \
    "ds_read_b64_tr_b16 v[12:13], v244 offset:64\n\t" \
    "ds_read_b64_tr_b16 v[14:15], v244 offset:1600\n\t" \
    "v_add_u32 v244, 0x0, v243\n\t" \
    "s_waitcnt vmcnt(12) lgkmcnt(0)\n\t" \
    "s_barrier \n\t" \
    "s_add_u32 m0, %[wm], 0xf000\n\t" \
    "s_mov_b32 s44, 0x2a0000\n\t" \
    "buffer_load_dwordx4 v240, %[wr], s44 offen offset:0 nt lds\n\t" \
    "buffer_load_dwordx4 v241, %[wr], s44 offen offset:1024 nt lds\n\t" \
    "buffer_load_dwordx4 v242, %[wr], s44 offen offset:2048 nt lds\n\t" \
    "ds_read_b64_tr_b16 v[208:209], v244 offset:0\n\t" \
    "ds_read_b64_tr_b16 v[210:211], v244 offset:1536\n\t" \
    "ds_read_b64_tr_b16 v[40:41], v244 offset:32\n\t" \
    "ds_read_b64_tr_b16 v[42:43], v244 offset:1568\n\t" \
    "ds_read_b64_tr_b16 v[48:49], v244 offset:64\n\t" \
    "ds_read_b64_tr_b16 v[50:51], v244 offset:1600\n\t" \
    "v_add_u32 v244, 0x3000, v243\n\t" \
    "s_waitcnt vmcnt(12) lgkmcnt(0)\n\t" \
    "s_barrier \n\t" \
    "s_add_u32 m0, %[wm], 0x0\n\t" \
    "s_mov_b32 s44, 0x300000\n\t" \
    "buffer_load_dwordx4 v240, %[wr], s44 offen offset:0 nt lds\n\t" \
    "buffer_load_dwordx4 v241, %[wr], s44 offen offset:1024 nt lds\n\t" \
    "buffer_load_dwordx4 v242, %[wr], s44 offen offset:2048 nt lds\n\t" \
    "ds_read_b64_tr_b16 v[212:213], v244 offset:0\n\t" \
    "ds_read_b64_tr_b16 v[214:215], v244 offset:1536\n\t" \
    "ds_read_b64_tr_b16 v[44:45], v244 offset:32\n\t" \
    "ds_read_b64_tr_b16 v[46:47], v244 offset:1568\n\t" \
    "ds_read_b64_tr_b16 v[52:53], v244 offset:64\n\t" \
    "ds_read_b64_tr_b16 v[54:55], v244 offset:1600\n\t" \
    "v_add_u32 v244, 0x6000, v243\n\t" \
    "s_waitcnt vmcnt(12) lgkmcnt(0)\n\t" \
    "s_barrier \n\t" \
    "s_add_u32 m0, %[wm], 0x3000\n\t" \
    "s_mov_b32 s44, 0x360000\n\t" \
    "buffer_load_dwordx4 v240, %[wr], s44 offen offset:0 nt lds\n\t" \
    "buffer_load_dwordx4 v241, %[wr], s44 offen offset:1024 nt lds\n\t" \
    "buffer_load_dwordx4 v242, %[wr], s44 offen offset:2048 nt lds\n\t" \
    "ds_read_b64_tr_b16 v[216:217], v244 offset:0\n\t" \
    "ds_read_b64_tr_b16 v[218:219], v244 offset:1536\n\t" \
    "ds_read_b64_tr_b16 v[80:81], v244 offset:32\n\t" \
    "ds_read_b64_tr_b16 v[82:83], v244 offset:1568\n\t" \
    "ds_read_b64_tr_b16 v[88:89], v244 offset:64\n\t" \
    "ds_read_b64_tr_b16 v[90:91], v244 offset:1600\n\t" \
    "v_add_u32 v244, 0x9000, v243\n\t" \
    "s_waitcnt vmcnt(12) lgkmcnt(0)\n\t" \
    "s_barrier \n\t" \
    "s_add_u32 m0, %[wm], 0x6000\n\t" \
    "s_mov_b32 s44, 0x3c0000\n\t" \
    "buffer_load_dwordx4 v240, %[wr], s44 offen offset:0 nt lds\n\t" \
    "buffer_load_dwordx4 v241, %[wr], s44 offen offset:1024 nt lds\n\t" \
    "buffer_load_dwordx4 v242, %[wr], s44 offen offset:2048 nt lds\n\t" \
    "ds_read_b64_tr_b16 v[220:221], v244 offset:0\n\t" \
    "ds_read_b64_tr_b16 v[222:223], v244 offset:1536\n\t" \
    "ds_read_b64_tr_b16 v[84:85], v244 offset:32\n\t" \
    "ds_read_b64_tr_b16 v[86:87], v244 offset:1568\n\t" \
    "ds_read_b64_tr_b16 v[92:93], v244 offset:64\n\t" \
    "ds_read_b64_tr_b16 v[94:95], v244 offset:1600\n\t" \
    "v_add_u32 v244, 0xc000, v243\n\t" \
    "s_waitcnt vmcnt(12) lgkmcnt(0)\n\t" \
    "s_barrier \n\t" \
    "s_add_u32 m0, %[wm], 0x9000\n\t" \
    "s_mov_b32 s44, 0x420000\n\t" \
    "buffer_load_dwordx4 v240, %[wr], s44 offen offset:0 nt lds\n\t" \
    "buffer_load_dwordx4 v241, %[wr], s44 offen offset:1024 nt lds\n\t" \
    "buffer_load_dwordx4 v242, %[wr], s44 offen offset:2048 nt lds\n\t" \
    "ds_read_b64_tr_b16 v[224:225], v244 offset:0\n\t" \
    "ds_read_b64_tr_b16 v[226:227], v244 offset:1536\n\t" \
    "ds_read_b64_tr_b16 v[120:121], v244 offset:32\n\t" \
    "ds_read_b64_tr_b16 v[122:123], v244 offset:1568\n\t" \
    "ds_read_b64_tr_b16 v[128:129], v244 offset:64\n\t" \
    "ds_read_b64_tr_b16 v[130:131], v244 offset:1600\n\t" \
    "v_add_u32 v244, 0xf000, v243\n\t" \
    "s_waitcnt vmcnt(12) lgkmcnt(0)\n\t" \
    "s_barrier \n\t" \
    "s_add_u32 m0, %[wm], 0xc000\n\t" \
    "s_mov_b32 s44, 0x480000\n\t" \
    "buffer_load_dwordx4 v240, %[wr], s44 offen offset:0 nt lds\n\t" \
    "buffer_load_dwordx4 v241, %[wr], s44 offen offset:1024 nt lds\n\t" \
    "buffer_load_dwordx4 v242, %[wr], s44 offen offset:2048 nt lds\n\t" \
    "ds_read_b64_tr_b16 v[228:229], v244 offset:0\n\t" \
    "ds_read_b64_tr_b16 v[230:231], v244 offset:1536\n\t" \
    "ds_read_b64_tr_b16 v[124:125], v244 offset:32\n\t" \
    "ds_read_b64_tr_b16 v[126:127], v244 offset:1568\n\t" \
    "ds_read_b64_tr_b16 v[132:133], v244 offset:64\n\t" \
    "ds_read_b64_tr_b16 v[134:135], v244 offset:1600\n\t" \
    "v_add_u32 v244, 0x0, v243\n\t" \
    "s_waitcnt vmcnt(12) lgkmcnt(0)\n\t" \
    "s_barrier \n\t" \
    "v_mov_b32 v246, 0\n\t" \
    "buffer_load_dword v246, v246, s[60:63], %[mo] offen sc1\n\t" \
    "s_add_u32 m0, %[wm], 0xf000\n\t" \
    "s_mov_b32 s44, 0x4e0000\n\t" \
    "buffer_load_dwordx4 v240, %[wr], s44 offen offset:0 nt lds\n\t" \
    "buffer_load_dwordx4 v241, %[wr], s44 offen offset:1024 nt lds\n\t" \
    "buffer_load_dwordx4 v242, %[wr], s44 offen offset:2048 nt lds\n\t" \
    "ds_read_b64_tr_b16 v[232:233], v244 offset:0\n\t" \
    "ds_read_b64_tr_b16 v[234:235], v244 offset:1536\n\t" \
    "ds_read_b64_tr_b16 v[160:161], v244 offset:32\n\t" \
    "ds_read_b64_tr_b16 v[162:163], v244 offset:1568\n\t" \
    "ds_read_b64_tr_b16 v[168:169], v244 offset:64\n\t" \
    "ds_read_b64_tr_b16 v[170:171], v244 offset:1600\n\t" \
    "v_add_u32 v244, 0x3000, v243\n\t" \
    "s_waitcnt vmcnt(12) lgkmcnt(0)\n\t" \
    "s_barrier \n\t" \
    "s_add_u32 m0, %[wm], 0x0\n\t" \
    "s_mov_b32 s44, 0x540000\n\t" \
    "buffer_load_dwordx4 v240, %[wr], s44 offen offset:0 nt lds\n\t" \
    "buffer_load_dwordx4 v241, %[wr], s44 offen offset:1024 nt lds\n\t" \
    "buffer_load_dwordx4 v242, %[wr], s44 offen offset:2048 nt lds\n\t" \
    "ds_read_b64_tr_b16 v[236:237], v244 offset:0\n\t" \
    "ds_read_b64_tr_b16 v[238:239], v244 offset:1536\n\t" \
    "ds_read_b64_tr_b16 v[164:165], v244 offset:32\n\t" \
    "ds_read_b64_tr_b16 v[166:167], v244 offset:1568\n\t" \
    "ds_read_b64_tr_b16 v[172:173], v244 offset:64\n\t" \
    "ds_read_b64_tr_b16 v[174:175], v244 offset:1600\n\t" \
    "v_add_u32 v244, 0x6000, v243\n\t" \
    "s_waitcnt vmcnt(12) lgkmcnt(0)\n\t" \
    "s_barrier \n\t" \
    "s_add_u32 m0, %[wm], 0x3000\n\t" \
    "s_mov_b32 s44, 0x5a0000\n\t" \
    "buffer_load_dwordx4 v240, %[wr], s44 offen offset:0 nt lds\n\t" \
    "buffer_load_dwordx4 v241, %[wr], s44 offen offset:1024 nt lds\n\t" \
    "buffer_load_dwordx4 v242, %[wr], s44 offen offset:2048 nt lds\n\t" \
    "ds_read_b64_tr_b16 v[16:17], v244 offset:0\n\t" \
    "ds_read_b64_tr_b16 v[18:19], v244 offset:1536\n\t" \
    "ds_read_b64_tr_b16 v[24:25], v244 offset:32\n\t" \
    "ds_read_b64_tr_b16 v[26:27], v244 offset:1568\n\t" \
    "ds_read_b64_tr_b16 v[32:33], v244 offset:64\n\t" \
    "ds_read_b64_tr_b16 v[34:35], v244 offset:1600\n\t" \
    "v_add_u32 v244, 0x9000, v243\n\t" \
    "s_waitcnt vmcnt(12) lgkmcnt(0)\n\t" \
    "s_barrier \n\t" \
    "s_add_u32 m0, %[wm], 0x6000\n\t" \
    "s_mov_b32 s44, 0x600000\n\t" \
    "buffer_load_dwordx4 v240, %[wr], s44 offen offset:0 nt lds\n\t" \
    "buffer_load_dwordx4 v241, %[wr], s44 offen offset:1024 nt lds\n\t" \
    "buffer_load_dwordx4 v242, %[wr], s44 offen offset:2048 nt lds\n\t" \
    "ds_read_b64_tr_b16 v[20:21], v244 offset:0\n\t" \
    "ds_read_b64_tr_b16 v[22:23], v244 offset:1536\n\t" \
    "ds_read_b64_tr_b16 v[28:29], v244 offset:32\n\t" \
    "ds_read_b64_tr_b16 v[30:31], v244 offset:1568\n\t" \
    "ds_read_b64_tr_b16 v[36:37], v244 offset:64\n\t" \
    "ds_read_b64_tr_b16 v[38:39], v244 offset:1600\n\t" \
    "v_add_u32 v244, 0xc000, v243\n\t" \
    "s_waitcnt vmcnt(12) lgkmcnt(0)\n\t" \
    "s_barrier \n\t" \
    "s_add_u32 m0, %[wm], 0x9000\n\t" \
    "s_mov_b32 s44, 0x660000\n\t" \
    "buffer_load_dwordx4 v240, %[wr], s44 offen offset:0 nt lds\n\t" \
    "buffer_load_dwordx4 v241, %[wr], s44 offen offset:1024 nt lds\n\t" \
    "buffer_load_dwordx4 v242, %[wr], s44 offen offset:2048 nt lds\n\t" \
    "ds_read_b64_tr_b16 v[56:57], v244 offset:0\n\t" \
    "ds_read_b64_tr_b16 v[58:59], v244 offset:1536\n\t" \
    "ds_read_b64_tr_b16 v[64:65], v244 offset:32\n\t" \
    "ds_read_b64_tr_b16 v[66:67], v244 offset:1568\n\t" \
    "ds_read_b64_tr_b16 v[72:73], v244 offset:64\n\t" \
    "ds_read_b64_tr_b16 v[74:75], v244 offset:1600\n\t" \
    "v_add_u32 v244, 0xf000, v243\n\t" \
    "s_waitcnt vmcnt(12) lgkmcnt(0)\n\t" \
    "s_barrier \n\t" \
    ".Lv01_%=:\n\t" \
    "v_readfirstlane_b32 s43, v246\n\t" \
    "s_nop 0\n\t" \
    "s_cmp_lg_u32 s43, -1\n\t" \
    "s_cbranch_scc1 .Lv02_%=\n\t" \
    "s_cmp_eq_u32 s41, 0\n\t" \
    "s_cbranch_scc1 .Lv02_%=\n\t" \
    "s_sub_u32 s41, s41, 1\n\t" \
    "s_sleep 4\n\t" \
    "v_mov_b32 v246, 0\n\t" \
    "buffer_load_dword v246, v246, s[60:63], %[mo] offen sc1\n\t" \
    "s_waitcnt vmcnt(0)\n\t" \
    "s_branch .Lv01_%=\n\t" \
    ".Lv02_%=:\n\t" \
    "v_mov_b32 v246, 0x8000\n\t" \
    "s_mov_b64 exec, 1\n\t" \
    "buffer_store_dword v246, v246, s[60:63], %[mo] offen sc1\n\t" \
    "s_mov_b64 exec, -1\n\t" \
    "s_and_b32 s43, s43, 0x2000\n\t" \
    "s_xor_b32 s43, s43, %[lx]\n\t" \
    "s_add_u32 s60, s60, s43\n\t" \
    "s_addc_u32 s61, s61, 0\n\t" \
    "v_and_b32 v252, 0xf0, v253\n\t" \
    "v_lshlrev_b32 v252, 3, v252\n\t" \
    "buffer_load_dword v252, v252, s[60:63], %[fl] offen sc1\n\t" \
    "s_add_u32 m0, %[wm], 0xc000\n\t" \
    "s_mov_b32 s44, 0x6c0000\n\t" \
    "buffer_load_dwordx4 v240, %[wr], s44 offen offset:0 nt lds\n\t" \
    "buffer_load_dwordx4 v241, %[wr], s44 offen offset:1024 nt lds\n\t" \
    "buffer_load_dwordx4 v242, %[wr], s44 offen offset:2048 nt lds\n\t" \
    "ds_read_b64_tr_b16 v[60:61], v244 offset:0\n\t" \
    "ds_read_b64_tr_b16 v[62:63], v244 offset:1536\n\t" \
    "ds_read_b64_tr_b16 v[68:69], v244 offset:32\n\t" \
    "ds_read_b64_tr_b16 v[70:71], v244 offset:1568\n\t" \
    "ds_read_b64_tr_b16 v[76:77], v244 offset:64\n\t" \
    "ds_read_b64_tr_b16 v[78:79], v244 offset:1600\n\t" \
    "v_add_u32 v244, 0x0, v243\n\t" \
    "s_waitcnt vmcnt(12) lgkmcnt(0)\n\t" \
    "s_barrier \n\t" \
    "s_add_u32 m0, %[wm], 0xf000\n\t" \
    "s_mov_b32 s44, 0x720000\n\t" \
    "buffer_load_dwordx4 v240, %[wr], s44 offen offset:0 nt lds\n\t" \
    "buffer_load_dwordx4 v241, %[wr], s44 offen offset:1024 nt lds\n\t" \
    "buffer_load_dwordx4 v242, %[wr], s44 offen offset:2048 nt lds\n\t" \
    "ds_read_b64_tr_b16 v[96:97], v244 offset:0\n\t" \
    "ds_read_b64_tr_b16 v[98:99], v244 offset:1536\n\t" \
    "ds_read_b64_tr_b16 v[104:105], v244 offset:32\n\t" \
    "ds_read_b64_tr_b16 v[106:107], v244 offset:1568\n\t" \
    "ds_read_b64_tr_b16 v[112:113], v244 offset:64\n\t" \
    "ds_read_b64_tr_b16 v[114:115], v244 offset:1600\n\t" \
    "v_add_u32 v244, 0x3000, v243\n\t" \
    "s_waitcnt vmcnt(12) lgkmcnt(0)\n\t" \
    "s_barrier \n\t" \
    "ds_read_b64_tr_b16 v[100:101], v244 offset:0\n\t" \
    "ds_read_b64_tr_b16 v[102:103], v244 offset:1536\n\t" \
    "ds_read_b64_tr_b16 v[108:109], v244 offset:32\n\t" \
    "ds_read_b64_tr_b16 v[110:111], v244 offset:1568\n\t" \
    "ds_read_b64_tr_b16 v[116:117], v244 offset:64\n\t" \
    "ds_read_b64_tr_b16 v[118:119], v244 offset:1600\n\t" \
    "v_add_u32 v244, 0x6000, v243\n\t" \
    "s_waitcnt vmcnt(9) lgkmcnt(0)\n\t" \
    "s_barrier \n\t" \
    "ds_read_b64_tr_b16 v[136:137], v244 offset:0\n\t" \
    "ds_read_b64_tr_b16 v[138:139], v244 offset:1536\n\t" \
    "ds_read_b64_tr_b16 v[144:145], v244 offset:32\n\t" \
    "ds_read_b64_tr_b16 v[146:147], v244 offset:1568\n\t" \
    "ds_read_b64_tr_b16 v[152:153], v244 offset:64\n\t" \
    "ds_read_b64_tr_b16 v[154:155], v244 offset:1600\n\t" \
    "v_add_u32 v244, 0x9000, v243\n\t" \
    "s_waitcnt vmcnt(6) lgkmcnt(0)\n\t" \
    "s_barrier \n\t" \
    "ds_read_b64_tr_b16 v[140:141], v244 offset:0\n\t" \
    "ds_read_b64_tr_b16 v[142:143], v244 offset:1536\n\t" \
    "ds_read_b64_tr_b16 v[148:149], v244 offset:32\n\t" \
    "ds_read_b64_tr_b16 v[150:151], v244 offset:1568\n\t" \
    "ds_read_b64_tr_b16 v[156:157], v244 offset:64\n\t" \
    "ds_read_b64_tr_b16 v[158:159], v244 offset:1600\n\t" \
    "v_add_u32 v244, 0xc000, v243\n\t" \
    "s_waitcnt vmcnt(3) lgkmcnt(0)\n\t" \
    "s_barrier \n\t" \
    "ds_read_b64_tr_b16 v[176:177], v244 offset:0\n\t" \
    "ds_read_b64_tr_b16 v[178:179], v244 offset:1536\n\t" \
    "ds_read_b64_tr_b16 v[184:185], v244 offset:32\n\t" \
    "ds_read_b64_tr_b16 v[186:187], v244 offset:1568\n\t" \
    "ds_read_b64_tr_b16 v[192:193], v244 offset:64\n\t" \
    "ds_read_b64_tr_b16 v[194:195], v244 offset:1600\n\t" \
    "s_mov_b32 s43, 0\n\t" \
    "v_cmp_eq_u32 vcc, 0, v252\n\t" \
    "s_cbranch_vccnz .Lv03_%=\n\t" \
    "s_mov_b32 m0, 0x0\n\t" \
    "s_add_u32 s49, s42, 0x0\n\t" \
    "buffer_load_dwordx4 v253, s[60:63], s49 offen offset:0 sc1 lds\n\t" \
    "buffer_load_dwordx4 v253, s[60:63], s49 offen offset:1024 sc1 lds\n\t" \
    "buffer_load_dwordx4 v253, s[60:63], s49 offen offset:2048 sc1 lds\n\t" \
    "buffer_load_dwordx4 v253, s[60:63], s49 offen offset:3072 sc1 lds\n\t" \
    "s_mov_b32 m0, 0x4000\n\t" \
    "s_add_u32 s49, s42, 0x10000\n\t" \
    "buffer_load_dwordx4 v253, s[60:63], s49 offen offset:0 sc1 lds\n\t" \
    "s_mov_b32 m0, 0x5000\n\t" \
    "s_add_u32 s49, s42, 0x1000\n\t" \
    "buffer_load_dwordx4 v253, s[60:63], s49 offen offset:0 sc1 lds\n\t" \
    "buffer_load_dwordx4 v253, s[60:63], s49 offen offset:1024 sc1 lds\n\t" \
    "buffer_load_dwordx4 v253, s[60:63], s49 offen offset:2048 sc1 lds\n\t" \
    "buffer_load_dwordx4 v253, s[60:63], s49 offen offset:3072 sc1 lds\n\t" \
    "s_mov_b32 m0, 0x9000\n\t" \
    "s_add_u32 s49, s42, 0x11000\n\t" \
    "buffer_load_dwordx4 v253, s[60:63], s49 offen offset:0 sc1 lds\n\t" \
    "s_mov_b32 s43, 1\n\t" \
    ".Lv03_%=:\n\t" \
    "v_add_u32 v244, 0xf000, v243\n\t" \
    "s_waitcnt vmcnt(0) lgkmcnt(0)\n\t" \
    "s_barrier \n\t" \
    "ds_read_b64_tr_b16 v[180:181], v244 offset:0\n\t" \
    "ds_read_b64_tr_b16 v[182:183], v244 offset:1536\n\t" \
    "ds_read_b64_tr_b16 v[188:189], v244 offset:32\n\t" \
    "ds_read_b64_tr_b16 v[190:191], v244 offset:1568\n\t" \
    "ds_read_b64_tr_b16 v[196:197], v244 offset:64\n\t" \
    "ds_read_b64_tr_b16 v[198:199], v244 offset:1600\n\t" \
    "s_waitcnt lgkmcnt(0)\n\t" \
    "s_barrier \n\t" \
    "ds_write_b128 v254, v[200:203] offset:30720\n\t" \
    "ds_write_b128 v254, v[204:207] offset:31744\n\t" \
    "ds_write_b128 v254, v[208:211] offset:32768\n\t" \
    "ds_write_b128 v254, v[212:215] offset:33792\n\t" \
    "ds_write_b128 v254, v[216:219] offset:34816\n\t" \
    "ds_write_b128 v254, v[220:223] offset:35840\n\t" \
    "ds_write_b128 v254, v[224:227] offset:36864\n\t" \
    "ds_write_b128 v254, v[228:231] offset:37888\n\t" \
    "ds_write_b128 v254, v[232:235] offset:38912\n\t" \
    "ds_write_b128 v254, v[236:239] offset:39936\n\t" \
    "s_cmp_lg_u32 s43, 0\n\t" \
    "s_cbranch_scc1 .Lv04_%=\n\t" \
    "s_mov_b32 s44, 0\n\t" \
    ".Lv05_%=:\n\t" \
    "v_cmp_ge_u32 vcc, s44, v252\n\t" \
    "s_cbranch_vccz .Lv06_%=\n\t" \
    "s_cmp_eq_u32 s41, 0\n\t" \
    "s_cbranch_scc1 .Lv06_%=\n\t" \
    "s_sub_u32 s41, s41, 1\n\t" \
    "s_sleep 8\n\t" \
    "v_and_b32 v252, 0xf0, v253\n\t" \
    "v_lshlrev_b32 v252, 3, v252\n\t" \
    "buffer_load_dword v252, v252, s[60:63], %[fl] offen sc1\n\t" \
    "s_waitcnt vmcnt(0)\n\t" \
    "s_branch .Lv05_%=\n\t" \
    ".Lv06_%=:\n\t" \
    "s_mov_b32 m0, 0x0\n\t" \
    "s_add_u32 s49, s42, 0x0\n\t" \
    "buffer_load_dwordx4 v253, s[60:63], s49 offen offset:0 sc1 lds\n\t" \
    "buffer_load_dwordx4 v253, s[60:63], s49 offen offset:1024 sc1 lds\n\t" \
    "buffer_load_dwordx4 v253, s[60:63], s49 offen offset:2048 sc1 lds\n\t" \
    "buffer_load_dwordx4 v253, s[60:63], s49 offen offset:3072 sc1 lds\n\t" \
    "s_mov_b32 m0, 0x4000\n\t" \
    "s_add_u32 s49, s42, 0x10000\n\t" \
    "buffer_load_dwordx4 v253, s[60:63], s49 offen offset:0 sc1 lds\n\t" \
    "s_mov_b32 m0, 0x5000\n\t" \
    "s_add_u32 s49, s42, 0x1000\n\t" \
    "buffer_load_dwordx4 v253, s[60:63], s49 offen offset:0 sc1 lds\n\t" \
    "buffer_load_dwordx4 v253, s[60:63], s49 offen offset:1024 sc1 lds\n\t" \
    "buffer_load_dwordx4 v253, s[60:63], s49 offen offset:2048 sc1 lds\n\t" \
    "buffer_load_dwordx4 v253, s[60:63], s49 offen offset:3072 sc1 lds\n\t" \
    "s_mov_b32 m0, 0x9000\n\t" \
    "s_add_u32 s49, s42, 0x11000\n\t" \
    "buffer_load_dwordx4 v253, s[60:63], s49 offen offset:0 sc1 lds\n\t" \
    ".Lv04_%=:\n\t" \
    "s_waitcnt vmcnt(0) lgkmcnt(0)\n\t" \
    "s_barrier \n\t" \
    "ds_read_b128 v[224:227], v253 offset:0\n\t" \
    "ds_read_b128 v[244:247], v254 offset:30720\n\t" \
    "ds_read_b128 v[228:231], v253 offset:1024\n\t" \
    "ds_read_b128 v[232:235], v253 offset:20480\n\t" \
    "ds_read_b128 v[236:239], v253 offset:21504\n\t" \
    "ds_read_b128 v[248:251], v254 offset:31744\n\t" \
    ".Lv07_%=:\n\t" \
    "s_waitcnt lgkmcnt(5)\n\t" \
    "v_mfma_f32_16x16x32_bf16 v[204:207], v[0:3], v[224:227], 0\n\t" \
    "s_waitcnt lgkmcnt(3)\n\t" \
    "v_mfma_f32_16x16x32_bf16 v[204:207], v[4:7], v[228:231], v[204:207]\n\t" \
    "ds_read_b128 v[240:243], v253 offset:10240\n\t" \
    "v_mfma_f32_16x16x32_bf16 v[208:211], v[8:11], v[224:227], 0\n\t" \
    "v_mfma_f32_16x16x32_bf16 v[208:211], v[12:15], v[228:231], v[208:211]\n\t" \
    "v_mfma_f32_16x16x32_bf16 v[200:203], v[244:247], v[224:227], 0\n\t" \
    "s_waitcnt lgkmcnt(1)\n\t" \
    "v_mfma_f32_16x16x32_bf16 v[200:203], v[248:251], v[228:231], v[200:203]\n\t" \
    "v_mfma_f32_16x16x32_bf16 v[216:219], v[0:3], v[232:235], 0\n\t" \
    "ds_read_b128 v[224:227], v253 offset:11264\n\t" \
    "v_mfma_f32_16x16x32_bf16 v[216:219], v[4:7], v[236:239], v[216:219]\n\t" \
    "ds_read_b128 v[228:231], v253 offset:2048\n\t" \
    "v_mfma_f32_16x16x32_bf16 v[220:223], v[8:11], v[232:235], 0\n\t" \
    "v_mfma_f32_16x16x32_bf16 v[220:223], v[12:15], v[236:239], v[220:223]\n\t" \
    "v_mfma_f32_16x16x32_bf16 v[212:215], v[244:247], v[232:235], 0\n\t" \
    "ds_read_b128 v[244:247], v254 offset:32768\n\t" \
    "v_mfma_f32_16x16x32_bf16 v[212:215], v[248:251], v[236:239], v[212:215]\n\t" \
    "ds_read_b128 v[248:251], v254 offset:33792\n\t" \
    "s_waitcnt lgkmcnt(4)\n\t" \
    "v_mfma_f32_16x16x32_bf16 v[204:207], v[24:27], v[240:243], v[204:207]\n\t" \
    "ds_read_b128 v[232:235], v253 offset:3072\n\t" \
    "s_waitcnt lgkmcnt(4)\n\t" \
    "v_mfma_f32_16x16x32_bf16 v[204:207], v[28:31], v[224:227], v[204:207]\n\t" \
    "ds_read_b128 v[236:239], v253 offset:22528\n\t" \
    "v_mfma_f32_16x16x32_bf16 v[208:211], v[32:35], v[240:243], v[208:211]\n\t" \
    "v_mfma_f32_16x16x32_bf16 v[208:211], v[36:39], v[224:227], v[208:211]\n\t" \
    "v_mfma_f32_16x16x32_bf16 v[200:203], v[16:19], v[240:243], v[200:203]\n\t" \
    "v_mfma_f32_16x16x32_bf16 v[200:203], v[20:23], v[224:227], v[200:203]\n\t" \
    "s_waitcnt lgkmcnt(4)\n\t" \
    "v_mfma_f32_16x16x32_bf16 v[204:207], v[40:43], v[228:231], v[204:207]\n\t" \
    "ds_read_b128 v[240:243], v253 offset:23552\n\t" \
    "s_waitcnt lgkmcnt(2)\n\t" \
    "v_mfma_f32_16x16x32_bf16 v[204:207], v[44:47], v[232:235], v[204:207]\n\t" \
    "ds_read_b128 v[224:227], v253 offset:12288\n\t" \
    "v_mfma_f32_16x16x32_bf16 v[208:211], v[48:51], v[228:231], v[208:211]\n\t" \
    "v_mfma_f32_16x16x32_bf16 v[208:211], v[52:55], v[232:235], v[208:211]\n\t" \
    "v_mfma_f32_16x16x32_bf16 v[200:203], v[244:247], v[228:231], v[200:203]\n\t" \
    "v_mfma_f32_16x16x32_bf16 v[200:203], v[248:251], v[232:235], v[200:203]\n\t" \
    "s_waitcnt lgkmcnt(2)\n\t" \
    "v_mfma_f32_16x16x32_bf16 v[216:219], v[40:43], v[236:239], v[216:219]\n\t" \
    "ds_read_b128 v[228:231], v253 offset:13312\n\t" \
    "s_add_u32 s44, s40, 1\n\t" \
    "s_waitcnt lgkmcnt(2)\n\t" \
    "v_mfma_f32_16x16x32_bf16 v[216:219], v[44:47], v[240:243], v[216:219]\n\t" \
    "ds_read_b128 v[232:235], v253 offset:4096\n\t" \
    "s_and_b32 s45, s44, 15\n\t" \
    "v_mfma_f32_16x16x32_bf16 v[220:223], v[48:51], v[236:239], v[220:223]\n\t" \
    "s_cmp_eq_u32 s45, 0\n\t" \
    "v_mfma_f32_16x16x32_bf16 v[220:223], v[52:55], v[240:243], v[220:223]\n\t" \
    "s_cselect_b32 s45, %[cj], %[cs]\n\t" \
    "v_mfma_f32_16x16x32_bf16 v[212:215], v[244:247], v[236:239], v[212:215]\n\t" \
    "ds_read_b128 v[244:247], v254 offset:34816\n\t" \
    "s_cmp_eq_u32 s44, 64\n\t" \
    "v_mfma_f32_16x16x32_bf16 v[212:215], v[248:251], v[240:243], v[212:215]\n\t" \
    "ds_read_b128 v[248:251], v254 offset:35840\n\t" \
    "s_cselect_b32 s45, 0, s45\n\t" \
    "s_waitcnt lgkmcnt(4)\n\t" \
    "v_mfma_f32_16x16x32_bf16 v[204:207], v[64:67], v[224:227], v[204:207]\n\t" \
    "ds_read_b128 v[236:239], v253 offset:5120\n\t" \
    "s_add_u32 s42, s42, s45\n\t" \
    "s_waitcnt lgkmcnt(4)\n\t" \
    "v_mfma_f32_16x16x32_bf16 v[204:207], v[68:71], v[228:231], v[204:207]\n\t" \
    "ds_read_b128 v[240:243], v253 offset:24576\n\t" \
    "v_mfma_f32_16x16x32_bf16 v[208:211], v[72:75], v[224:227], v[208:211]\n\t" \
    "v_mfma_f32_16x16x32_bf16 v[208:211], v[76:79], v[228:231], v[208:211]\n\t" \
    "v_mfma_f32_16x16x32_bf16 v[200:203], v[56:59], v[224:227], v[200:203]\n\t" \
    "v_mfma_f32_16x16x32_bf16 v[200:203], v[60:63], v[228:231], v[200:203]\n\t" \
    "s_waitcnt lgkmcnt(4)\n\t" \
    "v_mfma_f32_16x16x32_bf16 v[204:207], v[80:83], v[232:235], v[204:207]\n\t" \
    "ds_read_b128 v[224:227], v253 offset:25600\n\t" \
    "s_waitcnt lgkmcnt(2)\n\t" \
    "v_mfma_f32_16x16x32_bf16 v[204:207], v[84:87], v[236:239], v[204:207]\n\t" \
    "ds_read_b128 v[228:231], v253 offset:14336\n\t" \
    "v_mfma_f32_16x16x32_bf16 v[208:211], v[88:91], v[232:235], v[208:211]\n\t" \
    "v_mfma_f32_16x16x32_bf16 v[208:211], v[92:95], v[236:239], v[208:211]\n\t" \
    "v_mfma_f32_16x16x32_bf16 v[200:203], v[244:247], v[232:235], v[200:203]\n\t" \
    "v_mfma_f32_16x16x32_bf16 v[200:203], v[248:251], v[236:239], v[200:203]\n\t" \
    "s_waitcnt lgkmcnt(2)\n\t" \
    "v_mfma_f32_16x16x32_bf16 v[216:219], v[80:83], v[240:243], v[216:219]\n\t" \
    "ds_read_b128 v[232:235], v253 offset:15360\n\t" \
    "s_and_b32 s44, s40, 3\n\t" \
    "s_waitcnt lgkmcnt(2)\n\t" \
    "v_mfma_f32_16x16x32_bf16 v[216:219], v[84:87], v[224:227], v[216:219]\n\t" \
    "ds_read_b128 v[236:239], v253 offset:6144\n\t" \
    "s_lshl_b32 s44, s44, 17\n\t" \
    "v_mfma_f32_16x16x32_bf16 v[220:223], v[88:91], v[240:243], v[220:223]\n\t" \
    "s_add_u32 s44, s44, %[mb]\n\t" \
    "v_mfma_f32_16x16x32_bf16 v[220:223], v[92:95], v[224:227], v[220:223]\n\t" \
    "s_add_u32 s46, s44, 0x0\n\t" \
    "v_mfma_f32_16x16x32_bf16 v[212:215], v[244:247], v[240:243], v[212:215]\n\t" \
    "ds_read_b128 v[244:247], v254 offset:36864\n\t" \
    "s_add_u32 s47, s44, 0x1400\n\t" \
    "v_mfma_f32_16x16x32_bf16 v[212:215], v[248:251], v[224:227], v[212:215]\n\t" \
    "ds_read_b128 v[248:251], v254 offset:37888\n\t" \
    "s_add_u32 s48, s44, 0x400\n\t" \
    "s_waitcnt lgkmcnt(4)\n\t" \
    "v_mfma_f32_16x16x32_bf16 v[204:207], v[104:107], v[228:231], v[204:207]\n\t" \
    "ds_read_b128 v[240:243], v253 offset:7168\n\t" \
    "s_waitcnt lgkmcnt(4)\n\t" \
    "v_mfma_f32_16x16x32_bf16 v[204:207], v[108:111], v[232:235], v[204:207]\n\t" \
    "ds_read_b128 v[224:227], v253 offset:26624\n\t" \
    "v_mfma_f32_16x16x32_bf16 v[208:211], v[112:115], v[228:231], v[208:211]\n\t" \
    "v_mfma_f32_16x16x32_bf16 v[208:211], v[116:119], v[232:235], v[208:211]\n\t" \
    "v_mfma_f32_16x16x32_bf16 v[200:203], v[96:99], v[228:231], v[200:203]\n\t" \
    "v_mfma_f32_16x16x32_bf16 v[200:203], v[100:103], v[232:235], v[200:203]\n\t" \
    "s_waitcnt lgkmcnt(4)\n\t" \
    "v_mfma_f32_16x16x32_bf16 v[204:207], v[120:123], v[236:239], v[204:207]\n\t" \
    "ds_read_b128 v[228:231], v253 offset:27648\n\t" \
    "s_waitcnt lgkmcnt(2)\n\t" \
    "v_mfma_f32_16x16x32_bf16 v[204:207], v[124:127], v[240:243], v[204:207]\n\t" \
    "ds_read_b128 v[232:235], v253 offset:16384\n\t" \
    "v_mfma_f32_16x16x32_bf16 v[208:211], v[128:131], v[236:239], v[208:211]\n\t" \
    "v_mfma_f32_16x16x32_bf16 v[208:211], v[132:135], v[240:243], v[208:211]\n\t" \
    "v_mfma_f32_16x16x32_bf16 v[200:203], v[244:247], v[236:239], v[200:203]\n\t" \
    "v_mfma_f32_16x16x32_bf16 v[200:203], v[248:251], v[240:243], v[200:203]\n\t" \
    "s_waitcnt lgkmcnt(2)\n\t" \
    "v_mfma_f32_16x16x32_bf16 v[216:219], v[120:123], v[224:227], v[216:219]\n\t" \
    "ds_read_b128 v[236:239], v253 offset:17408\n\t" \
    "s_waitcnt lgkmcnt(2)\n\t" \
    "v_mfma_f32_16x16x32_bf16 v[216:219], v[124:127], v[228:231], v[216:219]\n\t" \
    "ds_read_b128 v[240:243], v253 offset:8192\n\t" \
    "v_mfma_f32_16x16x32_bf16 v[220:223], v[128:131], v[224:227], v[220:223]\n\t" \
    "v_mfma_f32_16x16x32_bf16 v[220:223], v[132:135], v[228:231], v[220:223]\n\t" \
    "v_mfma_f32_16x16x32_bf16 v[212:215], v[244:247], v[224:227], v[212:215]\n\t" \
    "ds_read_b128 v[244:247], v254 offset:38912\n\t" \
    "v_mfma_f32_16x16x32_bf16 v[212:215], v[248:251], v[228:231], v[212:215]\n\t" \
    "ds_read_b128 v[248:251], v254 offset:39936\n\t" \
    "v_and_b32 v252, 0xf0, v253\n\t" \
    "v_lshlrev_b32 v252, 3, v252\n\t" \
    "buffer_load_dword v252, v252, s[60:63], %[fl] offen sc1\n\t" \
    "s_waitcnt lgkmcnt(4)\n\t" \
    "v_mfma_f32_16x16x32_bf16 v[204:207], v[144:147], v[232:235], v[204:207]\n\t" \
    "ds_read_b128 v[224:227], v253 offset:9216\n\t" \
    "s_waitcnt lgkmcnt(4)\n\t" \
    "v_mfma_f32_16x16x32_bf16 v[204:207], v[148:151], v[236:239], v[204:207]\n\t" \
    "ds_read_b128 v[228:231], v253 offset:28672\n\t" \
    "v_mfma_f32_16x16x32_bf16 v[208:211], v[152:155], v[232:235], v[208:211]\n\t" \
    "v_mfma_f32_16x16x32_bf16 v[208:211], v[156:159], v[236:239], v[208:211]\n\t" \
    "v_mfma_f32_16x16x32_bf16 v[200:203], v[136:139], v[232:235], v[200:203]\n\t" \
    "v_mfma_f32_16x16x32_bf16 v[200:203], v[140:143], v[236:239], v[200:203]\n\t" \
    "s_waitcnt lgkmcnt(4)\n\t" \
    "v_mfma_f32_16x16x32_bf16 v[204:207], v[160:163], v[240:243], v[204:207]\n\t" \
    "ds_read_b128 v[232:235], v253 offset:29696\n\t" \
    "s_waitcnt lgkmcnt(2)\n\t" \
    "v_mfma_f32_16x16x32_bf16 v[204:207], v[164:167], v[224:227], v[204:207]\n\t" \
    "ds_read_b128 v[236:239], v253 offset:18432\n\t" \
    "v_mfma_f32_16x16x32_bf16 v[208:211], v[168:171], v[240:243], v[208:211]\n\t" \
    "v_mfma_f32_16x16x32_bf16 v[208:211], v[172:175], v[224:227], v[208:211]\n\t" \
    "v_mfma_f32_16x16x32_bf16 v[200:203], v[244:247], v[240:243], v[200:203]\n\t" \
    "v_mfma_f32_16x16x32_bf16 v[200:203], v[248:251], v[224:227], v[200:203]\n\t" \
    "s_waitcnt lgkmcnt(2)\n\t" \
    "v_mfma_f32_16x16x32_bf16 v[216:219], v[160:163], v[228:231], v[216:219]\n\t" \
    "ds_read_b128 v[240:243], v253 offset:19456\n\t" \
    "s_waitcnt lgkmcnt(2)\n\t" \
    "v_mfma_f32_16x16x32_bf16 v[216:219], v[164:167], v[232:235], v[216:219]\n\t" \
    "ds_read_b128 v[224:227], v253 offset:30720\n\t" \
    "v_mfma_f32_16x16x32_bf16 v[220:223], v[168:171], v[228:231], v[220:223]\n\t" \
    "v_mfma_f32_16x16x32_bf16 v[220:223], v[172:175], v[232:235], v[220:223]\n\t" \
    "v_mfma_f32_16x16x32_bf16 v[212:215], v[244:247], v[228:231], v[212:215]\n\t" \
    "v_mfma_f32_16x16x32_bf16 v[212:215], v[248:251], v[232:235], v[212:215]\n\t" \
    "s_waitcnt lgkmcnt(2)\n\t" \
    "v_mfma_f32_16x16x32_bf16 v[204:207], v[184:187], v[236:239], v[204:207]\n\t" \
    "ds_read_b128 v[228:231], v253 offset:31744\n\t" \
    "s_waitcnt lgkmcnt(2)\n\t" \
    "v_mfma_f32_16x16x32_bf16 v[204:207], v[188:191], v[240:243], v[204:207]\n\t" \
    "ds_read_b128 v[232:235], v253 offset:32768\n\t" \
    "v_mfma_f32_16x16x32_bf16 v[208:211], v[192:195], v[236:239], v[208:211]\n\t" \
    "v_mfma_f32_16x16x32_bf16 v[208:211], v[196:199], v[240:243], v[208:211]\n\t" \
    "v_mfma_f32_16x16x32_bf16 v[200:203], v[176:179], v[236:239], v[200:203]\n\t" \
    "v_mfma_f32_16x16x32_bf16 v[200:203], v[180:183], v[240:243], v[200:203]\n\t" \
    "s_waitcnt lgkmcnt(2)\n\t" \
    "v_mfma_f32_16x16x32_bf16 v[216:219], v[24:27], v[224:227], v[216:219]\n\t" \
    "ds_read_b128 v[236:239], v253 offset:33792\n\t" \
    "s_waitcnt lgkmcnt(2)\n\t" \
    "v_mfma_f32_16x16x32_bf16 v[216:219], v[28:31], v[228:231], v[216:219]\n\t" \
    "ds_read_b128 v[240:243], v253 offset:34816\n\t" \
    "v_mfma_f32_16x16x32_bf16 v[220:223], v[32:35], v[224:227], v[220:223]\n\t" \
    "v_cvt_pk_bf16_f32 v204, v204, v205\n\t" \
    "v_mfma_f32_16x16x32_bf16 v[220:223], v[36:39], v[228:231], v[220:223]\n\t" \
    "v_cvt_pk_bf16_f32 v205, v206, v207\n\t" \
    "v_mfma_f32_16x16x32_bf16 v[212:215], v[16:19], v[224:227], v[212:215]\n\t" \
    "ds_write_b64 v254, v[204:205] offset:7168\n\t" \
    "v_mfma_f32_16x16x32_bf16 v[212:215], v[20:23], v[228:231], v[212:215]\n\t" \
    "v_cvt_pk_bf16_f32 v208, v208, v209\n\t" \
    "s_waitcnt lgkmcnt(3)\n\t" \
    "v_mfma_f32_16x16x32_bf16 v[216:219], v[64:67], v[232:235], v[216:219]\n\t" \
    "ds_read_b128 v[224:227], v253 offset:35840\n\t" \
    "v_cvt_pk_bf16_f32 v209, v210, v211\n\t" \
    "s_waitcnt lgkmcnt(3)\n\t" \
    "v_mfma_f32_16x16x32_bf16 v[216:219], v[68:71], v[236:239], v[216:219]\n\t" \
    "ds_read_b128 v[228:231], v253 offset:36864\n\t" \
    "ds_write_b64 v254, v[208:209] offset:8192\n\t" \
    "v_mfma_f32_16x16x32_bf16 v[220:223], v[72:75], v[232:235], v[220:223]\n\t" \
    "v_cvt_pk_bf16_f32 v200, v200, v201\n\t" \
    "v_mfma_f32_16x16x32_bf16 v[220:223], v[76:79], v[236:239], v[220:223]\n\t" \
    "v_cvt_pk_bf16_f32 v201, v202, v203\n\t" \
    "v_mfma_f32_16x16x32_bf16 v[212:215], v[56:59], v[232:235], v[212:215]\n\t" \
    "ds_write_b64 v254, v[200:201] offset:6144\n\t" \
    "v_mfma_f32_16x16x32_bf16 v[212:215], v[60:63], v[236:239], v[212:215]\n\t" \
    "s_waitcnt lgkmcnt(5)\n\t" \
    "v_mfma_f32_16x16x32_bf16 v[216:219], v[104:107], v[240:243], v[216:219]\n\t" \
    "ds_read_b128 v[232:235], v253 offset:37888\n\t" \
    "s_waitcnt lgkmcnt(4)\n\t" \
    "v_mfma_f32_16x16x32_bf16 v[216:219], v[108:111], v[224:227], v[216:219]\n\t" \
    "ds_read_b128 v[236:239], v253 offset:38912\n\t" \
    "v_mfma_f32_16x16x32_bf16 v[220:223], v[112:115], v[240:243], v[220:223]\n\t" \
    "v_mfma_f32_16x16x32_bf16 v[220:223], v[116:119], v[224:227], v[220:223]\n\t" \
    "v_mfma_f32_16x16x32_bf16 v[212:215], v[96:99], v[240:243], v[212:215]\n\t" \
    "v_mfma_f32_16x16x32_bf16 v[212:215], v[100:103], v[224:227], v[212:215]\n\t" \
    "ds_read_b128 v[240:243], v253 offset:39936\n\t" \
    "s_waitcnt lgkmcnt(3)\n\t" \
    "s_barrier \n\t" \
    "v_mfma_f32_16x16x32_bf16 v[216:219], v[144:147], v[228:231], v[216:219]\n\t" \
    "s_waitcnt lgkmcnt(2)\n\t" \
    "v_mfma_f32_16x16x32_bf16 v[216:219], v[148:151], v[232:235], v[216:219]\n\t" \
    "s_waitcnt lgkmcnt(1)\n\t" \
    "v_mfma_f32_16x16x32_bf16 v[216:219], v[184:187], v[236:239], v[216:219]\n\t" \
    "s_waitcnt lgkmcnt(0)\n\t" \
    "v_mfma_f32_16x16x32_bf16 v[216:219], v[188:191], v[240:243], v[216:219]\n\t" \
    "v_mfma_f32_16x16x32_bf16 v[220:223], v[152:155], v[228:231], v[220:223]\n\t" \
    "v_mfma_f32_16x16x32_bf16 v[220:223], v[156:159], v[232:235], v[220:223]\n\t" \
    "v_mfma_f32_16x16x32_bf16 v[220:223], v[192:195], v[236:239], v[220:223]\n\t" \
    "v_cvt_pk_bf16_f32 v216, v216, v217\n\t" \
    "v_cvt_pk_bf16_f32 v217, v218, v219\n\t" \
    "v_mfma_f32_16x16x32_bf16 v[220:223], v[196:199], v[240:243], v[220:223]\n\t" \
    "ds_write_b64 v254, v[216:217] offset:13312\n\t" \
    "v_mfma_f32_16x16x32_bf16 v[212:215], v[136:139], v[228:231], v[212:215]\n\t" \
    "v_mfma_f32_16x16x32_bf16 v[212:215], v[140:143], v[232:235], v[212:215]\n\t" \
    "v_mfma_f32_16x16x32_bf16 v[212:215], v[176:179], v[236:239], v[212:215]\n\t" \
    "v_cvt_pk_bf16_f32 v220, v220, v221\n\t" \
    "v_cvt_pk_bf16_f32 v221, v222, v223\n\t" \
    "v_mfma_f32_16x16x32_bf16 v[212:215], v[180:183], v[240:243], v[212:215]\n\t" \
    "ds_write_b64 v254, v[220:221] offset:14336\n\t" \
    "s_nop 6\n\t" \
    "v_cvt_pk_bf16_f32 v212, v212, v213\n\t" \
    "v_cvt_pk_bf16_f32 v213, v214, v215\n\t" \
    "ds_write_b64 v254, v[212:213] offset:12288\n\t" \
    "s_waitcnt vmcnt(0) lgkmcnt(0)\n\t" \
    "s_barrier \n\t" \
    "s_setprio 2\n\t" \
    "s_add_u32 s44, s40, 1\n\t" \
    "s_min_u32 s44, s44, 63\n\t" \
    ".Lv08_%=:\n\t" \
    "v_cmp_ge_u32 vcc, s44, v252\n\t" \
    "s_cbranch_vccz .Lv09_%=\n\t" \
    "s_cmp_eq_u32 s41, 0\n\t" \
    "s_cbranch_scc1 .Lv09_%=\n\t" \
    "s_sub_u32 s41, s41, 1\n\t" \
    "s_sleep 2\n\t" \
    "v_and_b32 v252, 0xf0, v253\n\t" \
    "v_lshlrev_b32 v252, 3, v252\n\t" \
    "buffer_load_dword v252, v252, s[60:63], %[fl] offen sc1\n\t" \
    "s_waitcnt vmcnt(0)\n\t" \
    "s_branch .Lv08_%=\n\t" \
    ".Lv09_%=:\n\t" \
    "s_mov_b32 m0, 0x0\n\t" \
    "s_add_u32 s49, s42, 0x0\n\t" \
    "buffer_load_dwordx4 v253, s[60:63], s49 offen offset:0 sc1 lds\n\t" \
    "s_sleep 1\n\t" \
    "buffer_load_dwordx4 v253, s[60:63], s49 offen offset:1024 sc1 lds\n\t" \
    "s_sleep 1\n\t" \
    "buffer_load_dwordx4 v253, s[60:63], s49 offen offset:2048 sc1 lds\n\t" \
    "s_sleep 1\n\t" \
    "buffer_load_dwordx4 v253, s[60:63], s49 offen offset:3072 sc1 lds\n\t" \
    "s_sleep 1\n\t" \
    "s_mov_b32 m0, 0x4000\n\t" \
    "s_add_u32 s49, s42, 0x10000\n\t" \
    "buffer_load_dwordx4 v253, s[60:63], s49 offen offset:0 sc1 lds\n\t" \
    "s_sleep 1\n\t" \
    "s_mov_b32 m0, 0x5000\n\t" \
    "s_add_u32 s49, s42, 0x1000\n\t" \
    "buffer_load_dwordx4 v253, s[60:63], s49 offen offset:0 sc1 lds\n\t" \
    "s_sleep 1\n\t" \
    "buffer_load_dwordx4 v253, s[60:63], s49 offen offset:1024 sc1 lds\n\t" \
    "s_sleep 1\n\t" \
    "buffer_load_dwordx4 v253, s[60:63], s49 offen offset:2048 sc1 lds\n\t" \
    "s_sleep 1\n\t" \
    "buffer_load_dwordx4 v253, s[60:63], s49 offen offset:3072 sc1 lds\n\t" \
    "s_sleep 1\n\t" \
    "s_mov_b32 m0, 0x9000\n\t" \
    "s_add_u32 s49, s42, 0x11000\n\t" \
    "buffer_load_dwordx4 v253, s[60:63], s49 offen offset:0 sc1 lds\n\t" \
    "s_sleep 1\n\t" \
    "ds_read_b128 v[200:203], v254 offset:30720\n\t" \
    "ds_read_b128 v[204:207], v254 offset:31744\n\t" \
    "ds_read_b128 v[216:219], v254 offset:32768\n\t" \
    "ds_read_b128 v[220:223], v254 offset:33792\n\t" \
    "v_mov_b64 v[208:209], 0\n\t" \
    "v_mov_b64 v[210:211], 0\n\t" \
    "v_mov_b64 v[244:245], 0\n\t" \
    "v_mov_b64 v[246:247], 0\n\t" \
    "v_mov_b64 v[248:249], 0\n\t" \
    "v_mov_b64 v[250:251], 0\n\t" \
    "s_sleep 5\n\t" \
    "s_mov_b32 m0, 0xa000\n\t" \
    "s_nop 0\n\t" \
    "buffer_load_dwordx4 v253, s[60:63], s46 offen sc1 lds\n\t" \
    "s_mov_b32 m0, 0xb400\n\t" \
    "s_nop 0\n\t" \
    "buffer_load_dwordx4 v253, s[60:63], s47 offen sc1 lds\n\t" \
    "s_mov_b32 m0, 0xa400\n\t" \
    "s_nop 0\n\t" \
    "buffer_load_dwordx4 v253, s[60:63], s48 offen sc1 lds\n\t" \
    "s_waitcnt lgkmcnt(0)\n\t" \
    "v_bfe_i32 v212, v253, 5, 1\n\t" \
    "v_and_b32 v212, 0xaaaa, v212\n\t" \
    "v_or_b32 v212, 0x4444, v212\n\t" \
    "v_and_b32 v213, 0xffff, v255\n\t" \
    "v_lshrrev_b32 v214, 16, v255\n\t" \
    ".Lv010_%=:\n\t" \
    "s_waitcnt vmcnt(0)\n\t" \
    "ds_read_b128 v[224:227], v253 offset:40960\n\t" \
    "ds_read_b128 v[228:231], v253 offset:46080\n\t" \
    "ds_read_b128 v[232:235], v253 offset:41984\n\t" \
    "s_waitcnt lgkmcnt(2)\n\t" \
    "v_max_u32 v215, v224, v225\n\t" \
    "v_max3_u32 v215, v226, v227, v215\n\t" \
    "s_waitcnt lgkmcnt(1)\n\t" \
    "v_max3_u32 v215, v228, v229, v215\n\t" \
    "v_max3_u32 v215, v230, v231, v215\n\t" \
    "s_waitcnt lgkmcnt(0)\n\t" \
    "v_max3_u32 v215, v232, v233, v215\n\t" \
    "v_max3_u32 v215, v234, v235, v215\n\t" \
    "v_cmp_eq_u32 vcc, -1, v215\n\t" \
    "s_cbranch_vccz .Lv011_%=\n\t" \
    "v_max_u32 v215, v224, v225\n\t" \
    "v_max3_u32 v215, v226, v227, v215\n\t" \
    "v_cmp_eq_u32 vcc, -1, v215\n\t" \
    "s_cbranch_vccz .Lv012_%=\n\t" \
    "s_mov_b32 m0, 0xa000\n\t" \
    "s_nop 0\n\t" \
    "buffer_load_dwordx4 v253, s[60:63], s46 offen sc1 lds\n\t" \
    ".Lv012_%=:\n\t" \
    "v_max_u32 v215, v228, v229\n\t" \
    "v_max3_u32 v215, v230, v231, v215\n\t" \
    "v_cmp_eq_u32 vcc, -1, v215\n\t" \
    "s_cbranch_vccz .Lv013_%=\n\t" \
    "s_mov_b32 m0, 0xb400\n\t" \
    "s_nop 0\n\t" \
    "buffer_load_dwordx4 v253, s[60:63], s47 offen sc1 lds\n\t" \
    ".Lv013_%=:\n\t" \
    "v_max_u32 v215, v232, v233\n\t" \
    "v_max3_u32 v215, v234, v235, v215\n\t" \
    "v_cmp_eq_u32 vcc, -1, v215\n\t" \
    "s_cbranch_vccz .Lv014_%=\n\t" \
    "s_mov_b32 m0, 0xa400\n\t" \
    "s_nop 0\n\t" \
    "buffer_load_dwordx4 v253, s[60:63], s48 offen sc1 lds\n\t" \
    ".Lv014_%=:\n\t" \
    "s_cmp_eq_u32 s41, 0\n\t" \
    "s_cbranch_scc1 .Lv011_%=\n\t" \
    "s_sub_u32 s41, s41, 1\n\t" \
    "s_branch .Lv010_%=\n\t" \
    ".Lv011_%=:\n\t" \
    "s_waitcnt lgkmcnt(0)\n\t" \
    "s_barrier \n\t" \
    "ds_read_b128 v[236:239], v253 offset:47104\n\t" \
    "ds_read_b128 v[240:243], v253 offset:43008\n\t" \
    "v_smfmac_f32_16x16x64_bf16 v[244:247], v[224:227], v[0:7], v212\n\t" \
    "v_smfmac_f32_16x16x64_bf16 v[248:251], v[224:227], v[8:15], v212\n\t" \
    "v_smfmac_f32_16x16x64_bf16 v[208:211], v[224:227], v[200:207], v212\n\t" \
    "ds_read_b128 v[200:203], v254 offset:34816\n\t" \
    "ds_read_b128 v[204:207], v254 offset:35840\n\t" \
    "ds_read_b128 v[224:227], v253 offset:48128\n\t" \
    "v_smfmac_f32_16x16x64_bf16 v[208:211], v[228:231], v[16:23], v212\n\t" \
    "v_smfmac_f32_16x16x64_bf16 v[244:247], v[228:231], v[24:31], v212\n\t" \
    "v_smfmac_f32_16x16x64_bf16 v[248:251], v[228:231], v[32:39], v212\n\t" \
    "ds_read_b128 v[228:231], v253 offset:44032\n\t" \
    "v_smfmac_f32_16x16x64_bf16 v[208:211], v[232:235], v[216:223], v212\n\t" \
    "ds_read_b128 v[216:219], v254 offset:36864\n\t" \
    "ds_read_b128 v[220:223], v254 offset:37888\n\t" \
    "v_smfmac_f32_16x16x64_bf16 v[244:247], v[232:235], v[40:47], v212\n\t" \
    "v_smfmac_f32_16x16x64_bf16 v[248:251], v[232:235], v[48:55], v212\n\t" \
    "ds_read_b128 v[232:235], v253 offset:49152\n\t" \
    "s_waitcnt lgkmcnt(8)\n\t" \
    "v_smfmac_f32_16x16x64_bf16 v[208:211], v[236:239], v[56:63], v212\n\t" \
    "v_smfmac_f32_16x16x64_bf16 v[244:247], v[236:239], v[64:71], v212\n\t" \
    "v_smfmac_f32_16x16x64_bf16 v[248:251], v[236:239], v[72:79], v212\n\t" \
    "ds_read_b128 v[236:239], v253 offset:45056\n\t" \
    "s_waitcnt lgkmcnt(6)\n\t" \
    "v_smfmac_f32_16x16x64_bf16 v[208:211], v[240:243], v[200:207], v212\n\t" \
    "ds_read_b128 v[200:203], v254 offset:38912\n\t" \
    "ds_read_b128 v[204:207], v254 offset:39936\n\t" \
    "v_smfmac_f32_16x16x64_bf16 v[244:247], v[240:243], v[80:87], v212\n\t" \
    "v_smfmac_f32_16x16x64_bf16 v[248:251], v[240:243], v[88:95], v212\n\t" \
    "ds_read_b128 v[240:243], v253 offset:50176\n\t" \
    "s_waitcnt lgkmcnt(8)\n\t" \
    "v_smfmac_f32_16x16x64_bf16 v[208:211], v[224:227], v[96:103], v212\n\t" \
    "v_smfmac_f32_16x16x64_bf16 v[244:247], v[224:227], v[104:111], v212\n\t" \
    "v_smfmac_f32_16x16x64_bf16 v[248:251], v[224:227], v[112:119], v212\n\t" \
    "s_waitcnt lgkmcnt(7)\n\t" \
    "v_smfmac_f32_16x16x64_bf16 v[244:247], v[228:231], v[120:127], v212\n\t" \
    "s_waitcnt lgkmcnt(4)\n\t" \
    "v_smfmac_f32_16x16x64_bf16 v[244:247], v[232:235], v[144:151], v212\n\t" \
    "s_waitcnt lgkmcnt(3)\n\t" \
    "v_smfmac_f32_16x16x64_bf16 v[244:247], v[236:239], v[160:167], v212\n\t" \
    "s_waitcnt lgkmcnt(0)\n\t" \
    "v_smfmac_f32_16x16x64_bf16 v[244:247], v[240:243], v[184:191], v212\n\t" \
    "v_smfmac_f32_16x16x64_bf16 v[248:251], v[228:231], v[128:135], v212\n\t" \
    "v_smfmac_f32_16x16x64_bf16 v[248:251], v[232:235], v[152:159], v212\n\t" \
    "v_smfmac_f32_16x16x64_bf16 v[248:251], v[236:239], v[168:175], v212\n\t" \
    "v_add_f32 v244, v244, v246\n\t" \
    "v_add_f32 v245, v245, v247\n\t" \
    "v_cvt_pk_bf16_f32 v244, v244, v245\n\t" \
    "v_smfmac_f32_16x16x64_bf16 v[248:251], v[240:243], v[192:199], v212\n\t" \
    "ds_write_b16 v213, v244 offset:512\n\t" \
    "ds_write_b16_d16_hi v213, v244 offset:528\n\t" \
    "v_smfmac_f32_16x16x64_bf16 v[208:211], v[228:231], v[216:223], v212\n\t" \
    "v_smfmac_f32_16x16x64_bf16 v[208:211], v[232:235], v[136:143], v212\n\t" \
    "v_smfmac_f32_16x16x64_bf16 v[208:211], v[236:239], v[200:207], v212\n\t" \
    "v_add_f32 v248, v248, v250\n\t" \
    "v_add_f32 v249, v249, v251\n\t" \
    "v_cvt_pk_bf16_f32 v248, v248, v249\n\t" \
    "v_smfmac_f32_16x16x64_bf16 v[208:211], v[240:243], v[176:183], v212\n\t" \
    "ds_write_b16 v213, v248 offset:1024\n\t" \
    "ds_write_b16_d16_hi v213, v248 offset:1040\n\t" \
    "s_nop 5\n\t" \
    "v_add_f32 v208, v208, v210\n\t" \
    "v_add_f32 v209, v209, v211\n\t" \
    "v_cvt_pk_bf16_f32 v208, v208, v209\n\t" \
    "ds_write_b16 v213, v208 offset:0\n\t" \
    "ds_write_b16_d16_hi v213, v208 offset:16\n\t" \
    "s_setprio 0\n\t" \
    "s_waitcnt lgkmcnt(0)\n\t" \
    "s_barrier \n\t" \
    "ds_read_b128 v[224:227], v253 offset:0\n\t" \
    "ds_read_b128 v[244:247], v254 offset:30720\n\t" \
    "ds_read_b128 v[228:231], v253 offset:1024\n\t" \
    "ds_read_b128 v[232:235], v253 offset:20480\n\t" \
    "ds_read_b128 v[236:239], v253 offset:21504\n\t" \
    "ds_read_b128 v[248:251], v254 offset:31744\n\t" \
    "s_add_u32 s40, s40, 1\n\t" \
    "s_cmp_lg_u32 s40, 64\n\t" \
    "s_cbranch_scc1 .Lv07_%=\n\t" \
    "s_branch .Lend%=\n\t" \
    ".Lend%=:\n\t" \
    "s_waitcnt vmcnt(0) lgkmcnt(0)\n\t" \
    "s_mov_b32 %[bud], s41\n\t"
#define ASM_R_LOOP \
    "s_mov_b32 s40, 0\n\t" \
    "s_mov_b32 s41, 0x40000\n\t" \
    "v_mov_b64 v[252:253], -1\n\t" \
    "v_mov_b64 v[254:255], -1\n\t" \
    "v_mov_b32 v248, 0\n\t" \
    "s_mov_b32 s53, 0x05040100\n\t" \
    ".Lr1_%=:\n\t" \
    "s_mov_b32 s43, s40\n\t" \
    "s_and_b32 s44, s40, 3\n\t" \
    "s_lshl_b32 s42, s44, 20\n\t" \
    "s_and_b32 s45, s40, 2\n\t" \
    "s_lshl_b32 s45, s45, 12\n\t" \
    "s_add_u32 s42, s42, s45\n\t" \
    "s_add_u32 s42, s42, %[vx]\n\t" \
    "s_mul_i32 s46, s44, 0x202000\n\t" \
    "s_mul_i32 s47, s44, 0x30000\n\t" \
    "s_lshl_b32 s49, s44, 17\n\t" \
    "s_add_u32 s49, s49, %[px]\n\t" \
    "s_add_u32 s50, s49, 0x1000\n\t" \
    "s_add_u32 s44, s40, 1\n\t" \
    "s_and_b32 s44, s44, 3\n\t" \
    "s_lshl_b32 s48, s44, 17\n\t" \
    "s_mul_i32 s52, s40, 0x14000\n\t" \
    "s_lshr_b32 s44, s40, 4\n\t" \
    "s_mul_i32 s44, s44, 0xc2000\n\t" \
    "s_add_u32 s52, s52, s44\n\t" \
    "s_add_u32 s52, s52, %[xr]\n\t" \
    "s_add_i32 s44, s40, %[ad]\n\t" \
    "s_and_b32 s45, s44, 3\n\t" \
    "s_mul_i32 s51, s45, %[as]\n\t" \
    "s_cmp_lt_i32 s44, 0\n\t" \
    "s_cselect_b32 s55, 0x40000000, 0\n\t" \
    "s_add_u32 s44, s40, 3\n\t" \
    "s_mul_i32 s54, s44, 0x14000\n\t" \
    "s_lshr_b32 s45, s44, 4\n\t" \
    "s_mul_i32 s45, s45, 0xc2000\n\t" \
    "s_add_u32 s54, s54, s45\n\t" \
    "s_cmp_eq_u32 %[wave], 7\n\t" \
    "s_cselect_b32 s51, s54, s51\n\t" \
    "s_cselect_b32 s55, 0, s55\n\t" \
    "s_cselect_b32 s44, 60, 62\n\t" \
    "s_cmp_gt_u32 s40, s44\n\t" \
    "s_cselect_b32 s55, 0x40000000, s55\n\t" \
    "v_or_b32 v197, s55, %[ae]\n\t" \
    "v_mov_b64 v[180:181], -1\n\t" \
    "v_mov_b64 v[182:183], -1\n\t" \
    "v_mov_b64 v[184:185], -1\n\t" \
    "v_mov_b64 v[186:187], -1\n\t" \
    "v_mov_b64 v[188:189], -1\n\t" \
    "v_mov_b64 v[190:191], -1\n\t" \
    "v_mov_b64 v[192:193], -1\n\t" \
    "v_mov_b64 v[194:195], -1\n\t" \
    "s_waitcnt vmcnt(0)\n\t" \
    ".Lr2_%=:\n\t" \
    "buffer_load_dword v248, %[fo], %[rs], %[fs] offen sc1\n\t" \
    "s_waitcnt vmcnt(0)\n\t" \
    "v_cmp_ge_u32 vcc, s43, v248\n\t" \
    "s_cbranch_vccz .Lr3_%=\n\t" \
    "s_sleep 2\n\t" \
    "s_cmp_eq_u32 s41, 0\n\t" \
    "s_cbranch_scc1 .Lr3_%=\n\t" \
    "s_sub_u32 s41, s41, 1\n\t" \
    "s_branch .Lr2_%=\n\t" \
    ".Lr3_%=:\n\t" \
    "buffer_load_dwordx4 v[160:163], %[l16], %[rs], s42 offen offset:0 sc1\n\t" \
    "buffer_load_dwordx4 v[164:167], %[l16], %[rs], s42 offen offset:1024 sc1\n\t" \
    "buffer_load_dwordx4 v[168:171], %[l16], %[rs], s42 offen offset:2048 sc1\n\t" \
    "buffer_load_dwordx4 v[172:175], %[l16], %[rs], s42 offen offset:3072 sc1\n\t" \
    "s_sleep 8\n\t" \
    "s_cmp_lg_u32 s40, 63\n\t" \
    "s_cbranch_scc1 .Lr5_%=\n\t" \
    "buffer_load_dwordx4 v[180:183], %[ov], %[rs], s46 offen sc1\n\t" \
    ".Lr6_%=:\n\t" \
    "s_waitcnt vmcnt(12)\n\t" \
    "v_max_u32 v196, v180, v181\n\t" \
    "v_max3_u32 v196, v182, v183, v196\n\t" \
    "v_cmp_eq_u32 vcc, -1, v196\n\t" \
    "s_cbranch_vccz .Lr7_%=\n\t" \
    "s_nop 7\n\t" \
    "v_max_u32 v196, v180, v181\n\t" \
    "v_max3_u32 v196, v182, v183, v196\n\t" \
    "v_cmp_eq_u32 vcc, -1, v196\n\t" \
    "s_cbranch_vccz .Lr7_%=\n\t" \
    "s_nop 7\n\t" \
    "v_max_u32 v196, v180, v181\n\t" \
    "v_max3_u32 v196, v182, v183, v196\n\t" \
    "v_cmp_eq_u32 vcc, -1, v196\n\t" \
    "s_cbranch_vccz .Lr7_%=\n\t" \
    "s_nop 7\n\t" \
    "v_max_u32 v196, v180, v181\n\t" \
    "v_max3_u32 v196, v182, v183, v196\n\t" \
    "v_cmp_eq_u32 vcc, -1, v196\n\t" \
    "s_cbranch_vccz .Lr7_%=\n\t" \
    "s_nop 7\n\t" \
    "s_cmp_eq_u32 s41, 0\n\t" \
    "s_cbranch_scc1 .Lr7_%=\n\t" \
    "s_sub_u32 s41, s41, 1\n\t" \
    "v_max_u32 v196, v180, v181\n\t" \
    "v_max3_u32 v196, v182, v183, v196\n\t" \
    "v_cmp_eq_u32 vcc, -1, v196\n\t" \
    "s_cbranch_vccz .Lr8_%=\n\t" \
    "buffer_load_dwordx4 v[180:183], %[ov], %[rs], s46 offen sc1\n\t" \
    ".Lr8_%=:\n\t" \
    "s_branch .Lr6_%=\n\t" \
    ".Lr7_%=:\n\t" \
    "s_branch .Lr4_%=\n\t" \
    ".Lr5_%=:\n\t" \
    "buffer_load_dwordx4 v[184:187], %[o0], %[rs], s47 offen sc1\n\t" \
    "buffer_load_dwordx4 v[188:191], %[o1], %[rs], s47 offen sc1\n\t" \
    "buffer_load_dwordx4 v[192:195], %[o2], %[rs], s47 offen sc1\n\t" \
    "buffer_load_dwordx4 v[180:183], %[ov], %[rs], s46 offen sc1\n\t" \
    ".Lr9_%=:\n\t" \
    "s_waitcnt vmcnt(12)\n\t" \
    "v_max_u32 v196, v184, v185\n\t" \
    "v_max3_u32 v196, v186, v187, v196\n\t" \
    "v_max3_u32 v196, v188, v189, v196\n\t" \
    "v_max3_u32 v196, v190, v191, v196\n\t" \
    "v_max3_u32 v196, v192, v193, v196\n\t" \
    "v_max3_u32 v196, v194, v195, v196\n\t" \
    "v_max3_u32 v196, v180, v181, v196\n\t" \
    "v_max3_u32 v196, v182, v183, v196\n\t" \
    "v_cmp_eq_u32 vcc, -1, v196\n\t" \
    "s_cbranch_vccz .Lr10_%=\n\t" \
    "s_nop 7\n\t" \
    "v_max_u32 v196, v184, v185\n\t" \
    "v_max3_u32 v196, v186, v187, v196\n\t" \
    "v_max3_u32 v196, v188, v189, v196\n\t" \
    "v_max3_u32 v196, v190, v191, v196\n\t" \
    "v_max3_u32 v196, v192, v193, v196\n\t" \
    "v_max3_u32 v196, v194, v195, v196\n\t" \
    "v_max3_u32 v196, v180, v181, v196\n\t" \
    "v_max3_u32 v196, v182, v183, v196\n\t" \
    "v_cmp_eq_u32 vcc, -1, v196\n\t" \
    "s_cbranch_vccz .Lr10_%=\n\t" \
    "s_nop 7\n\t" \
    "v_max_u32 v196, v184, v185\n\t" \
    "v_max3_u32 v196, v186, v187, v196\n\t" \
    "v_max3_u32 v196, v188, v189, v196\n\t" \
    "v_max3_u32 v196, v190, v191, v196\n\t" \
    "v_max3_u32 v196, v192, v193, v196\n\t" \
    "v_max3_u32 v196, v194, v195, v196\n\t" \
    "v_max3_u32 v196, v180, v181, v196\n\t" \
    "v_max3_u32 v196, v182, v183, v196\n\t" \
    "v_cmp_eq_u32 vcc, -1, v196\n\t" \
    "s_cbranch_vccz .Lr10_%=\n\t" \
    "s_nop 7\n\t" \
    "v_max_u32 v196, v184, v185\n\t" \
    "v_max3_u32 v196, v186, v187, v196\n\t" \
    "v_max3_u32 v196, v188, v189, v196\n\t" \
    "v_max3_u32 v196, v190, v191, v196\n\t" \
    "v_max3_u32 v196, v192, v193, v196\n\t" \
    "v_max3_u32 v196, v194, v195, v196\n\t" \
    "v_max3_u32 v196, v180, v181, v196\n\t" \
    "v_max3_u32 v196, v182, v183, v196\n\t" \
    "v_cmp_eq_u32 vcc, -1, v196\n\t" \
    "s_cbranch_vccz .Lr10_%=\n\t" \
    "s_nop 7\n\t" \
    "v_max_u32 v196, v184, v185\n\t" \
    "v_max3_u32 v196, v186, v187, v196\n\t" \
    "v_max3_u32 v196, v188, v189, v196\n\t" \
    "v_max3_u32 v196, v190, v191, v196\n\t" \
    "v_max3_u32 v196, v192, v193, v196\n\t" \
    "v_max3_u32 v196, v194, v195, v196\n\t" \
    "v_max3_u32 v196, v180, v181, v196\n\t" \
    "v_max3_u32 v196, v182, v183, v196\n\t" \
    "v_cmp_eq_u32 vcc, -1, v196\n\t" \
    "s_cbranch_vccz .Lr10_%=\n\t" \
    "s_nop 7\n\t" \
    "v_max_u32 v196, v184, v185\n\t" \
    "v_max3_u32 v196, v186, v187, v196\n\t" \
    "v_max3_u32 v196, v188, v189, v196\n\t" \
    "v_max3_u32 v196, v190, v191, v196\n\t" \
    "v_max3_u32 v196, v192, v193, v196\n\t" \
    "v_max3_u32 v196, v194, v195, v196\n\t" \
    "v_max3_u32 v196, v180, v181, v196\n\t" \
    "v_max3_u32 v196, v182, v183, v196\n\t" \
    "v_cmp_eq_u32 vcc, -1, v196\n\t" \
    "s_cbranch_vccz .Lr10_%=\n\t" \
    "s_nop 7\n\t" \
    "v_max_u32 v196, v184, v185\n\t" \
    "v_max3_u32 v196, v186, v187, v196\n\t" \
    "v_max3_u32 v196, v188, v189, v196\n\t" \
    "v_max3_u32 v196, v190, v191, v196\n\t" \
    "v_max3_u32 v196, v192, v193, v196\n\t" \
    "v_max3_u32 v196, v194, v195, v196\n\t" \
    "v_max3_u32 v196, v180, v181, v196\n\t" \
    "v_max3_u32 v196, v182, v183, v196\n\t" \
    "v_cmp_eq_u32 vcc, -1, v196\n\t" \
    "s_cbranch_vccz .Lr10_%=\n\t" \
    "s_nop 7\n\t" \
    "v_max_u32 v196, v184, v185\n\t" \
    "v_max3_u32 v196, v186, v187, v196\n\t" \
    "v_max3_u32 v196, v188, v189, v196\n\t" \
    "v_max3_u32 v196, v190, v191, v196\n\t" \
    "v_max3_u32 v196, v192, v193, v196\n\t" \
    "v_max3_u32 v196, v194, v195, v196\n\t" \
    "v_max3_u32 v196, v180, v181, v196\n\t" \
    "v_max3_u32 v196, v182, v183, v196\n\t" \
    "v_cmp_eq_u32 vcc, -1, v196\n\t" \
    "s_cbranch_vccz .Lr10_%=\n\t" \
    "s_nop 7\n\t" \
    "s_cmp_eq_u32 s41, 0\n\t" \
    "s_cbranch_scc1 .Lr10_%=\n\t" \
    "s_sub_u32 s41, s41, 1\n\t" \
    "v_max_u32 v196, v184, v185\n\t" \
    "v_max3_u32 v196, v186, v187, v196\n\t" \
    "v_cmp_eq_u32 vcc, -1, v196\n\t" \
    "s_cbranch_vccz .Lr11_%=\n\t" \
    "buffer_load_dwordx4 v[184:187], %[o0], %[rs], s47 offen sc1\n\t" \
    ".Lr11_%=:\n\t" \
    "v_max_u32 v196, v188, v189\n\t" \
    "v_max3_u32 v196, v190, v191, v196\n\t" \
    "v_cmp_eq_u32 vcc, -1, v196\n\t" \
    "s_cbranch_vccz .Lr12_%=\n\t" \
    "buffer_load_dwordx4 v[188:191], %[o1], %[rs], s47 offen sc1\n\t" \
    ".Lr12_%=:\n\t" \
    "v_max_u32 v196, v192, v193\n\t" \
    "v_max3_u32 v196, v194, v195, v196\n\t" \
    "v_cmp_eq_u32 vcc, -1, v196\n\t" \
    "s_cbranch_vccz .Lr13_%=\n\t" \
    "buffer_load_dwordx4 v[192:195], %[o2], %[rs], s47 offen sc1\n\t" \
    ".Lr13_%=:\n\t" \
    "v_max_u32 v196, v180, v181\n\t" \
    "v_max3_u32 v196, v182, v183, v196\n\t" \
    "v_cmp_eq_u32 vcc, -1, v196\n\t" \
    "s_cbranch_vccz .Lr14_%=\n\t" \
    "buffer_load_dwordx4 v[180:183], %[ov], %[rs], s46 offen sc1\n\t" \
    ".Lr14_%=:\n\t" \
    "s_branch .Lr9_%=\n\t" \
    ".Lr10_%=:\n\t" \
    "v_mfma_f32_16x16x32_bf16 v[176:179], v[192:195], v[180:183], 0\n\t" \
    "v_smfmac_f32_16x16x64_bf16 v[176:179], v[184:187], v[160:167], %[sidx]\n\t" \
    "v_smfmac_f32_16x16x64_bf16 v[176:179], v[188:191], v[168:175], %[sidx]\n\t" \
    "s_cmp_gt_u32 %[wave], 3\n\t" \
    "s_nop 7\n\t" \
    "v_add_f32 v176, v176, v178\n\t" \
    "v_add_f32 v177, v177, v179\n\t" \
    "ds_write2_b32 %[rw], v176, v177 offset1:80\n\t" \
    "s_waitcnt lgkmcnt(0)\n\t" \
    "s_barrier \n\t" \
    "s_cbranch_scc1 .Lr4_%=\n\t" \
    "ds_read_b128 v[128:131], %[rd] offset:0\n\t" \
    "ds_read_b128 v[132:135], %[rd] offset:16\n\t" \
    "ds_read_b128 v[136:139], %[rd] offset:32\n\t" \
    "ds_read_b128 v[140:143], %[rd] offset:48\n\t" \
    "s_waitcnt lgkmcnt(2)\n\t" \
    "v_pk_add_f32 v[128:129], v[128:129], v[130:131]\n\t" \
    "v_pk_add_f32 v[132:133], v[132:133], v[134:135]\n\t" \
    "s_waitcnt lgkmcnt(0)\n\t" \
    "v_pk_add_f32 v[136:137], v[136:137], v[138:139]\n\t" \
    "v_pk_add_f32 v[140:141], v[140:141], v[142:143]\n\t" \
    "v_pk_add_f32 v[128:129], v[128:129], v[132:133]\n\t" \
    "v_pk_add_f32 v[136:137], v[136:137], v[140:141]\n\t" \
    "v_pk_add_f32 v[128:129], v[128:129], v[136:137]\n\t" \
    "v_cvt_pk_bf16_f32 v128, v128, v129\n\t" \
    "v_min_u32 v128, -2, v128\n\t" \
    "buffer_store_dword v128, %[oo], %[rs], s48 offen sc1\n\t" \
    ".Lr4_%=:\n\t" \
    "s_cmp_lt_u32 %[wave], 4\n\t" \
    "s_cbranch_scc1 .Lr16_%=\n\t" \
    "s_sleep 4\n\t" \
    ".Lr16_%=:\n\t" \
    "s_sleep 2\n\t" \
    "s_cmp_lt_u32 %[wave], 6\n\t" \
    "s_cbranch_scc1 .Lr15_%=\n\t" \
    "buffer_load_dwordx2 v[250:251], %[xo], %[rs], s52 offen sc1\n\t" \
    ".Lr15_%=:\n\t" \
    "buffer_load_dwordx4 v[220:223], %[l16], %[rs], s50 offen offset:1024 sc1\n\t" \
    "buffer_load_dwordx4 v[224:227], %[l16], %[rs], s50 offen offset:2048 sc1\n\t" \
    "buffer_load_dwordx4 v[228:231], %[l16], %[rs], s50 offen offset:3072 sc1\n\t" \
    "v_perm_b32 v232, v161, v160, s53\n\t" \
    "v_perm_b32 v233, v163, v162, s53\n\t" \
    "v_perm_b32 v234, v165, v164, s53\n\t" \
    "v_perm_b32 v235, v167, v166, s53\n\t" \
    "v_perm_b32 v236, v169, v168, s53\n\t" \
    "v_perm_b32 v237, v171, v170, s53\n\t" \
    "v_perm_b32 v238, v173, v172, s53\n\t" \
    "v_perm_b32 v239, v175, v174, s53\n\t" \
    "s_nop 1\n\t" \
    "v_permlane16_swap_b32 v232, v234\n\t" \
    "v_permlane16_swap_b32 v233, v235\n\t" \
    "v_permlane16_swap_b32 v236, v238\n\t" \
    "v_permlane16_swap_b32 v237, v239\n\t" \
    "s_waitcnt vmcnt(0)\n\t" \
    ".Lr17_%=:\n\t" \
    "v_max_u32 v196, v220, v221\n\t" \
    "v_max3_u32 v196, v222, v223, v196\n\t" \
    "v_max3_u32 v196, v224, v225, v196\n\t" \
    "v_max3_u32 v196, v226, v227, v196\n\t" \
    "v_max3_u32 v196, v228, v229, v196\n\t" \
    "v_max3_u32 v196, v230, v231, v196\n\t" \
    "v_cmp_eq_u32 vcc, -1, v196\n\t" \
    "s_cbranch_vccz .Lr18_%=\n\t" \
    "s_cmp_eq_u32 s41, 0\n\t" \
    "s_cbranch_scc1 .Lr18_%=\n\t" \
    "s_sub_u32 s41, s41, 1\n\t" \
    "v_max_u32 v196, v220, v221\n\t" \
    "v_max3_u32 v196, v222, v223, v196\n\t" \
    "v_cmp_eq_u32 vcc, -1, v196\n\t" \
    "s_cbranch_vccz .Lr19_%=\n\t" \
    "buffer_load_dwordx4 v[220:223], %[l16], %[rs], s50 offen offset:1024 sc1\n\t" \
    ".Lr19_%=:\n\t" \
    "v_max_u32 v196, v224, v225\n\t" \
    "v_max3_u32 v196, v226, v227, v196\n\t" \
    "v_cmp_eq_u32 vcc, -1, v196\n\t" \
    "s_cbranch_vccz .Lr20_%=\n\t" \
    "buffer_load_dwordx4 v[224:227], %[l16], %[rs], s50 offen offset:2048 sc1\n\t" \
    ".Lr20_%=:\n\t" \
    "v_max_u32 v196, v228, v229\n\t" \
    "v_max3_u32 v196, v230, v231, v196\n\t" \
    "v_cmp_eq_u32 vcc, -1, v196\n\t" \
    "s_cbranch_vccz .Lr21_%=\n\t" \
    "buffer_load_dwordx4 v[228:231], %[l16], %[rs], s50 offen offset:3072 sc1\n\t" \
    ".Lr21_%=:\n\t" \
    "s_waitcnt vmcnt(0)\n\t" \
    "s_branch .Lr17_%=\n\t" \
    ".Lr18_%=:\n\t" \
    "v_mfma_f32_16x16x32_bf16 v[240:243], v[232:235], v[220:223], 0\n\t" \
    "v_mfma_f32_16x16x32_bf16 v[240:243], v[236:239], v[224:227], v[240:243]\n\t" \
    "v_mfma_f32_16x16x32_bf16 v[240:243], v[180:183], v[228:231], v[240:243]\n\t" \
    "s_nop 7\n\t" \
    "ds_write_b128 %[pw], v[240:243]\n\t" \
    "s_waitcnt lgkmcnt(0)\n\t" \
    "s_barrier \n\t" \
    "buffer_load_dwordx4 v[200:203], %[l16], %[rs], s49 offen offset:0 sc1\n\t" \
    "buffer_load_dwordx4 v[204:207], %[l16], %[rs], s49 offen offset:1024 sc1\n\t" \
    "buffer_load_dwordx4 v[208:211], %[l16], %[rs], s49 offen offset:2048 sc1\n\t" \
    "buffer_load_dwordx4 v[212:215], %[l16], %[rs], s49 offen offset:3072 sc1\n\t" \
    "buffer_load_dwordx4 v[216:219], %[l16], %[rs], s50 offen offset:0 sc1\n\t" \
    "s_cmp_lg_u32 %[wave], 6\n\t" \
    "s_cbranch_scc1 .Lr22_%=\n\t" \
    "ds_read_b128 v[128:131], %[pr] offset:0\n\t" \
    "ds_read_b128 v[132:135], %[pr] offset:1024\n\t" \
    "ds_read_b128 v[136:139], %[pr] offset:2048\n\t" \
    "ds_read_b128 v[140:143], %[pr] offset:3072\n\t" \
    "ds_read_b128 v[144:147], %[pr] offset:4096\n\t" \
    "ds_read_b128 v[148:151], %[pr] offset:5120\n\t" \
    "ds_read_b128 v[152:155], %[pr] offset:6144\n\t" \
    "ds_read_b128 v[156:159], %[pr] offset:7168\n\t" \
    "v_lshlrev_b32 v176, 16, v250\n\t" \
    "v_and_b32 v177, 0xffff0000, v250\n\t" \
    "v_lshlrev_b32 v178, 16, v251\n\t" \
    "v_and_b32 v179, 0xffff0000, v251\n\t" \
    "s_waitcnt lgkmcnt(6)\n\t" \
    "v_pk_add_f32 v[128:129], v[128:129], v[132:133]\n\t" \
    "v_pk_add_f32 v[130:131], v[130:131], v[134:135]\n\t" \
    "s_waitcnt lgkmcnt(4)\n\t" \
    "v_pk_add_f32 v[136:137], v[136:137], v[140:141]\n\t" \
    "v_pk_add_f32 v[138:139], v[138:139], v[142:143]\n\t" \
    "v_pk_add_f32 v[128:129], v[128:129], v[136:137]\n\t" \
    "v_pk_add_f32 v[130:131], v[130:131], v[138:139]\n\t" \
    "s_waitcnt lgkmcnt(2)\n\t" \
    "v_pk_add_f32 v[144:145], v[144:145], v[148:149]\n\t" \
    "v_pk_add_f32 v[146:147], v[146:147], v[150:151]\n\t" \
    "s_waitcnt lgkmcnt(0)\n\t" \
    "v_pk_add_f32 v[152:153], v[152:153], v[156:157]\n\t" \
    "v_pk_add_f32 v[154:155], v[154:155], v[158:159]\n\t" \
    "v_pk_add_f32 v[144:145], v[144:145], v[152:153]\n\t" \
    "v_pk_add_f32 v[146:147], v[146:147], v[154:155]\n\t" \
    "v_pk_add_f32 v[128:129], v[128:129], v[144:145]\n\t" \
    "v_pk_add_f32 v[130:131], v[130:131], v[146:147]\n\t" \
    "v_pk_add_f32 v[128:129], v[128:129], v[176:177]\n\t" \
    "v_pk_add_f32 v[130:131], v[130:131], v[178:179]\n\t" \
    "v_cvt_pk_bf16_f32 v128, v128, v129\n\t" \
    "v_cvt_pk_bf16_f32 v129, v130, v131\n\t" \
    "v_min_u32 v128, -2, v128\n\t" \
    "v_min_u32 v129, -2, v129\n\t" \
    "s_add_u32 s44, s52, 0x1000000\n\t" \
    "buffer_store_dwordx2 v[128:129], %[xo], %[rs], s44 offen sc1\n\t" \
    "s_waitcnt vmcnt(1)\n\t" \
    "s_branch .Lr23_%=\n\t" \
    ".Lr22_%=:\n\t" \
    "s_waitcnt vmcnt(0)\n\t" \
    ".Lr23_%=:\n\t" \
    ".Lr24_%=:\n\t" \
    "v_max_u32 v196, v200, v201\n\t" \
    "v_max3_u32 v196, v202, v203, v196\n\t" \
    "v_max3_u32 v196, v204, v205, v196\n\t" \
    "v_max3_u32 v196, v206, v207, v196\n\t" \
    "v_max3_u32 v196, v208, v209, v196\n\t" \
    "v_max3_u32 v196, v210, v211, v196\n\t" \
    "v_max3_u32 v196, v212, v213, v196\n\t" \
    "v_max3_u32 v196, v214, v215, v196\n\t" \
    "v_max3_u32 v196, v216, v217, v196\n\t" \
    "v_max3_u32 v196, v218, v219, v196\n\t" \
    "v_cmp_eq_u32 vcc, -1, v196\n\t" \
    "s_cbranch_vccz .Lr25_%=\n\t" \
    "s_cmp_eq_u32 s41, 0\n\t" \
    "s_cbranch_scc1 .Lr25_%=\n\t" \
    "s_sub_u32 s41, s41, 1\n\t" \
    "v_max_u32 v196, v200, v201\n\t" \
    "v_max3_u32 v196, v202, v203, v196\n\t" \
    "v_cmp_eq_u32 vcc, -1, v196\n\t" \
    "s_cbranch_vccz .Lr26_%=\n\t" \
    "buffer_load_dwordx4 v[200:203], %[l16], %[rs], s49 offen offset:0 sc1\n\t" \
    ".Lr26_%=:\n\t" \
    "v_max_u32 v196, v204, v205\n\t" \
    "v_max3_u32 v196, v206, v207, v196\n\t" \
    "v_cmp_eq_u32 vcc, -1, v196\n\t" \
    "s_cbranch_vccz .Lr27_%=\n\t" \
    "buffer_load_dwordx4 v[204:207], %[l16], %[rs], s49 offen offset:1024 sc1\n\t" \
    ".Lr27_%=:\n\t" \
    "v_max_u32 v196, v208, v209\n\t" \
    "v_max3_u32 v196, v210, v211, v196\n\t" \
    "v_cmp_eq_u32 vcc, -1, v196\n\t" \
    "s_cbranch_vccz .Lr28_%=\n\t" \
    "buffer_load_dwordx4 v[208:211], %[l16], %[rs], s49 offen offset:2048 sc1\n\t" \
    ".Lr28_%=:\n\t" \
    "v_max_u32 v196, v212, v213\n\t" \
    "v_max3_u32 v196, v214, v215, v196\n\t" \
    "v_cmp_eq_u32 vcc, -1, v196\n\t" \
    "s_cbranch_vccz .Lr29_%=\n\t" \
    "buffer_load_dwordx4 v[212:215], %[l16], %[rs], s49 offen offset:3072 sc1\n\t" \
    ".Lr29_%=:\n\t" \
    "v_max_u32 v196, v216, v217\n\t" \
    "v_max3_u32 v196, v218, v219, v196\n\t" \
    "v_cmp_eq_u32 vcc, -1, v196\n\t" \
    "s_cbranch_vccz .Lr30_%=\n\t" \
    "buffer_load_dwordx4 v[216:219], %[l16], %[rs], s50 offen offset:0 sc1\n\t" \
    ".Lr30_%=:\n\t" \
    "s_waitcnt vmcnt(0)\n\t" \
    "s_branch .Lr24_%=\n\t" \
    ".Lr25_%=:\n\t" \
    "v_mfma_f32_16x16x32_bf16 v[244:247], v[160:163], v[200:203], 0\n\t" \
    "v_mfma_f32_16x16x32_bf16 v[244:247], v[164:167], v[204:207], v[244:247]\n\t" \
    "v_mfma_f32_16x16x32_bf16 v[244:247], v[168:171], v[208:211], v[244:247]\n\t" \
    "v_mfma_f32_16x16x32_bf16 v[244:247], v[172:175], v[212:215], v[244:247]\n\t" \
    "v_mfma_f32_16x16x32_bf16 v[244:247], v[180:183], v[216:219], v[244:247]\n\t" \
    "s_nop 7\n\t" \
    "ds_write_b128 %[pw], v[244:247] offset:8192\n\t" \
    "s_waitcnt lgkmcnt(0)\n\t" \
    "s_barrier \n\t" \
    "s_cmp_lg_u32 %[wave], 7\n\t" \
    "s_cbranch_scc1 .Lr31_%=\n\t" \
    "ds_read_b128 v[128:131], %[pr] offset:0\n\t" \
    "ds_read_b128 v[132:135], %[pr] offset:1024\n\t" \
    "ds_read_b128 v[136:139], %[pr] offset:2048\n\t" \
    "ds_read_b128 v[140:143], %[pr] offset:3072\n\t" \
    "ds_read_b128 v[144:147], %[pr] offset:4096\n\t" \
    "ds_read_b128 v[148:151], %[pr] offset:5120\n\t" \
    "ds_read_b128 v[152:155], %[pr] offset:6144\n\t" \
    "ds_read_b128 v[156:159], %[pr] offset:7168\n\t" \
    "v_lshlrev_b32 v176, 16, v250\n\t" \
    "v_and_b32 v177, 0xffff0000, v250\n\t" \
    "v_lshlrev_b32 v178, 16, v251\n\t" \
    "v_and_b32 v179, 0xffff0000, v251\n\t" \
    "s_waitcnt lgkmcnt(6)\n\t" \
    "v_pk_add_f32 v[128:129], v[128:129], v[132:133]\n\t" \
    "v_pk_add_f32 v[130:131], v[130:131], v[134:135]\n\t" \
    "s_waitcnt lgkmcnt(4)\n\t" \
    "v_pk_add_f32 v[136:137], v[136:137], v[140:141]\n\t" \
    "v_pk_add_f32 v[138:139], v[138:139], v[142:143]\n\t" \
    "v_pk_add_f32 v[128:129], v[128:129], v[136:137]\n\t" \
    "v_pk_add_f32 v[130:131], v[130:131], v[138:139]\n\t" \
    "s_waitcnt lgkmcnt(2)\n\t" \
    "v_pk_add_f32 v[144:145], v[144:145], v[148:149]\n\t" \
    "v_pk_add_f32 v[146:147], v[146:147], v[150:151]\n\t" \
    "s_waitcnt lgkmcnt(0)\n\t" \
    "v_pk_add_f32 v[152:153], v[152:153], v[156:157]\n\t" \
    "v_pk_add_f32 v[154:155], v[154:155], v[158:159]\n\t" \
    "v_pk_add_f32 v[144:145], v[144:145], v[152:153]\n\t" \
    "v_pk_add_f32 v[146:147], v[146:147], v[154:155]\n\t" \
    "v_pk_add_f32 v[128:129], v[128:129], v[144:145]\n\t" \
    "v_pk_add_f32 v[130:131], v[130:131], v[146:147]\n\t" \
    "v_pk_add_f32 v[128:129], v[128:129], v[176:177]\n\t" \
    "v_pk_add_f32 v[130:131], v[130:131], v[178:179]\n\t" \
    "v_cvt_pk_bf16_f32 v128, v128, v129\n\t" \
    "v_cvt_pk_bf16_f32 v129, v130, v131\n\t" \
    "v_min_u32 v128, -2, v128\n\t" \
    "v_min_u32 v129, -2, v129\n\t" \
    "s_add_u32 s44, s52, 0x1000000\n\t" \
    "buffer_store_dwordx2 v[128:129], %[xo], %[rs], s44 offen sc1\n\t" \
    ".Lr31_%=:\n\t" \
    "buffer_store_dwordx4 v[252:255], %[ov], %[rs], s46 offen sc1\n\t" \
    "buffer_store_dwordx4 v[252:255], v197, %[rs], s51 offen sc1\n\t" \
    "s_add_u32 s40, s40, 1\n\t" \
    "s_cmp_lg_u32 s40, 64\n\t" \
    "s_cbranch_scc1 .Lr1_%=\n\t" \
    "s_branch .Lend%=\n\t" \
    ".Lend%=:\n\t" \
    "s_waitcnt vmcnt(0) lgkmcnt(0)\n\t" \
    "s_mov_b32 %[bud], s41\n\t"
#define ASM_W4_MUL \
    "s_mov_b32 s40, 0\n\t" \
    "s_mov_b32 s41, 0x40000\n\t" \
    "s_mov_b32 s43, 0\n\t" \
    "s_mov_b32 s46, %[hb]\n\t" \
    "s_add_u32 s47, s46, 0x4000\n\t" \
    "v_mov_b64 v[188:189], -1\n\t" \
    "v_mov_b32 v246, 0x1e000\n\t" \
    ".Lm1_%=:\n\t" \
    ".Lm2_%=:\n\t" \
    "ds_read_b32 v245, v246 offset:24\n\t" \
    "s_sub_i32 s44, s40, 3\n\t" \
    "s_waitcnt lgkmcnt(0)\n\t" \
    "v_cmp_gt_i32 vcc, s44, v245\n\t" \
    "s_cbranch_vccnz .Lm2_%=\n\t" \
    "s_add_u32 s44, s40, 1\n\t" \
    "s_lshr_b32 s45, s44, 1\n\t" \
    "s_and_b32 s44, s44, 1\n\t" \
    "s_lshl_b32 s44, s44, 12\n\t" \
    "s_mul_i32 s48, s45, 0x30000\n\t" \
    "s_add_u32 s48, s48, s44\n\t" \
    "s_lshr_b32 s45, s45, 3\n\t" \
    "s_mul_i32 s45, s45, 0x82000\n\t" \
    "s_add_u32 s48, s48, s45\n\t" \
    "s_add_u32 s48, s48, %[hb]\n\t" \
    "s_add_u32 s49, s48, 0x4000\n\t" \
    "s_cmp_lg_u32 s43, 0\n\t" \
    "s_cbranch_scc1 .Lm5_%=\n\t" \
    ".Lm3_%=:\n\t" \
    "v_max_u32 v244, v188, v189\n\t" \
    "v_max3_u32 v244, v190, v191, v244\n\t" \
    "v_cmp_eq_u32 vcc, -1, v244\n\t" \
    "s_cbranch_vccz .Lm4_%=\n\t" \
    "s_cmp_eq_u32 s41, 0\n\t" \
    "s_cbranch_scc1 .Lm4_%=\n\t" \
    "s_sub_u32 s41, s41, 1\n\t" \
    "s_sleep 3\n\t" \
    "buffer_load_dwordx4 v[188:191], %[l16], %[rs], s47 offen offset:3072 sc1\n\t" \
    "s_waitcnt vmcnt(0)\n\t" \
    "s_branch .Lm3_%=\n\t" \
    ".Lm4_%=:\n\t" \
    "buffer_load_dwordx4 v[184:187], %[l16], %[rs], s47 offen offset:2048 sc1\n\t" \
    "buffer_load_dwordx4 v[180:183], %[l16], %[rs], s47 offen offset:1024 sc1\n\t" \
    "buffer_load_dwordx4 v[176:179], %[l16], %[rs], s47 offen offset:0 sc1\n\t" \
    "buffer_load_dwordx4 v[172:175], %[l16], %[rs], s46 offen offset:3072 sc1\n\t" \
    "buffer_load_dwordx4 v[168:171], %[l16], %[rs], s46 offen offset:2048 sc1\n\t" \
    "buffer_load_dwordx4 v[164:167], %[l16], %[rs], s46 offen offset:1024 sc1\n\t" \
    "buffer_load_dwordx4 v[160:163], %[l16], %[rs], s46 offen offset:0 sc1\n\t" \
    ".Lm5_%=:\n\t" \
    "v_mfma_f32_16x16x32_bf16 v[224:227], v[28:31], v[188:191], 0\n\t" \
    "v_mfma_f32_16x16x32_bf16 v[228:231], v[60:63], v[188:191], 0\n\t" \
    "v_mfma_f32_16x16x32_bf16 v[232:235], v[92:95], v[188:191], 0\n\t" \
    "v_mfma_f32_16x16x32_bf16 v[236:239], v[124:127], v[188:191], 0\n\t" \
    "v_mfma_f32_16x16x32_bf16 v[240:243], v[156:159], v[188:191], 0\n\t" \
    "s_waitcnt vmcnt(6)\n\t" \
    ".Lm6_%=:\n\t" \
    "v_max_u32 v244, v184, v185\n\t" \
    "v_max3_u32 v244, v186, v187, v244\n\t" \
    "v_cmp_eq_u32 vcc, -1, v244\n\t" \
    "s_cbranch_vccnz .Lm7_%=\n\t" \
    ".Lm8_%=:\n\t" \
    "v_mfma_f32_16x16x32_bf16 v[224:227], v[24:27], v[184:187], v[224:227]\n\t" \
    "v_mfma_f32_16x16x32_bf16 v[228:231], v[56:59], v[184:187], v[228:231]\n\t" \
    "v_mfma_f32_16x16x32_bf16 v[232:235], v[88:91], v[184:187], v[232:235]\n\t" \
    "v_mfma_f32_16x16x32_bf16 v[236:239], v[120:123], v[184:187], v[236:239]\n\t" \
    "v_mfma_f32_16x16x32_bf16 v[240:243], v[152:155], v[184:187], v[240:243]\n\t" \
    "s_waitcnt vmcnt(5)\n\t" \
    ".Lm9_%=:\n\t" \
    "v_max_u32 v244, v180, v181\n\t" \
    "v_max3_u32 v244, v182, v183, v244\n\t" \
    "v_cmp_eq_u32 vcc, -1, v244\n\t" \
    "s_cbranch_vccnz .Lm10_%=\n\t" \
    ".Lm11_%=:\n\t" \
    "v_mfma_f32_16x16x32_bf16 v[224:227], v[20:23], v[180:183], v[224:227]\n\t" \
    "v_mfma_f32_16x16x32_bf16 v[228:231], v[52:55], v[180:183], v[228:231]\n\t" \
    "v_mfma_f32_16x16x32_bf16 v[232:235], v[84:87], v[180:183], v[232:235]\n\t" \
    "v_mfma_f32_16x16x32_bf16 v[236:239], v[116:119], v[180:183], v[236:239]\n\t" \
    "v_mfma_f32_16x16x32_bf16 v[240:243], v[148:151], v[180:183], v[240:243]\n\t" \
    "s_waitcnt vmcnt(4)\n\t" \
    ".Lm12_%=:\n\t" \
    "v_max_u32 v244, v176, v177\n\t" \
    "v_max3_u32 v244, v178, v179, v244\n\t" \
    "v_cmp_eq_u32 vcc, -1, v244\n\t" \
    "s_cbranch_vccnz .Lm13_%=\n\t" \
    ".Lm14_%=:\n\t" \
    "v_mfma_f32_16x16x32_bf16 v[224:227], v[16:19], v[176:179], v[224:227]\n\t" \
    "v_mfma_f32_16x16x32_bf16 v[228:231], v[48:51], v[176:179], v[228:231]\n\t" \
    "v_mfma_f32_16x16x32_bf16 v[232:235], v[80:83], v[176:179], v[232:235]\n\t" \
    "v_mfma_f32_16x16x32_bf16 v[236:239], v[112:115], v[176:179], v[236:239]\n\t" \
    "v_mfma_f32_16x16x32_bf16 v[240:243], v[144:147], v[176:179], v[240:243]\n\t" \
    "s_waitcnt vmcnt(3)\n\t" \
    ".Lm15_%=:\n\t" \
    "v_max_u32 v244, v172, v173\n\t" \
    "v_max3_u32 v244, v174, v175, v244\n\t" \
    "v_cmp_eq_u32 vcc, -1, v244\n\t" \
    "s_cbranch_vccnz .Lm16_%=\n\t" \
    ".Lm17_%=:\n\t" \
    "v_mfma_f32_16x16x32_bf16 v[224:227], v[12:15], v[172:175], v[224:227]\n\t" \
    "v_mfma_f32_16x16x32_bf16 v[228:231], v[44:47], v[172:175], v[228:231]\n\t" \
    "v_mfma_f32_16x16x32_bf16 v[232:235], v[76:79], v[172:175], v[232:235]\n\t" \
    "v_mfma_f32_16x16x32_bf16 v[236:239], v[108:111], v[172:175], v[236:239]\n\t" \
    "v_mfma_f32_16x16x32_bf16 v[240:243], v[140:143], v[172:175], v[240:243]\n\t" \
    "s_waitcnt vmcnt(2)\n\t" \
    ".Lm18_%=:\n\t" \
    "v_max_u32 v244, v168, v169\n\t" \
    "v_max3_u32 v244, v170, v171, v244\n\t" \
    "v_cmp_eq_u32 vcc, -1, v244\n\t" \
    "s_cbranch_vccnz .Lm19_%=\n\t" \
    ".Lm20_%=:\n\t" \
    "buffer_load_dwordx4 v[220:223], %[l16], %[rs], s49 offen offset:3072 sc1\n\t" \
    "v_mfma_f32_16x16x32_bf16 v[224:227], v[8:11], v[168:171], v[224:227]\n\t" \
    "v_mfma_f32_16x16x32_bf16 v[228:231], v[40:43], v[168:171], v[228:231]\n\t" \
    "v_mfma_f32_16x16x32_bf16 v[232:235], v[72:75], v[168:171], v[232:235]\n\t" \
    "v_mfma_f32_16x16x32_bf16 v[236:239], v[104:107], v[168:171], v[236:239]\n\t" \
    "v_mfma_f32_16x16x32_bf16 v[240:243], v[136:139], v[168:171], v[240:243]\n\t" \
    "s_waitcnt vmcnt(2)\n\t" \
    ".Lm21_%=:\n\t" \
    "v_max_u32 v244, v164, v165\n\t" \
    "v_max3_u32 v244, v166, v167, v244\n\t" \
    "v_cmp_eq_u32 vcc, -1, v244\n\t" \
    "s_cbranch_vccnz .Lm22_%=\n\t" \
    ".Lm23_%=:\n\t" \
    "v_mfma_f32_16x16x32_bf16 v[224:227], v[4:7], v[164:167], v[224:227]\n\t" \
    "v_mfma_f32_16x16x32_bf16 v[228:231], v[36:39], v[164:167], v[228:231]\n\t" \
    "v_mfma_f32_16x16x32_bf16 v[232:235], v[68:71], v[164:167], v[232:235]\n\t" \
    "v_mfma_f32_16x16x32_bf16 v[236:239], v[100:103], v[164:167], v[236:239]\n\t" \
    "v_mfma_f32_16x16x32_bf16 v[240:243], v[132:135], v[164:167], v[240:243]\n\t" \
    "s_waitcnt vmcnt(1)\n\t" \
    ".Lm24_%=:\n\t" \
    "v_max_u32 v244, v160, v161\n\t" \
    "v_max3_u32 v244, v162, v163, v244\n\t" \
    "v_cmp_eq_u32 vcc, -1, v244\n\t" \
    "s_cbranch_vccnz .Lm25_%=\n\t" \
    ".Lm26_%=:\n\t" \
    "v_mfma_f32_16x16x32_bf16 v[224:227], v[0:3], v[160:163], v[224:227]\n\t" \
    "v_mfma_f32_16x16x32_bf16 v[228:231], v[32:35], v[160:163], v[228:231]\n\t" \
    "v_mfma_f32_16x16x32_bf16 v[232:235], v[64:67], v[160:163], v[232:235]\n\t" \
    "v_mfma_f32_16x16x32_bf16 v[236:239], v[96:99], v[160:163], v[236:239]\n\t" \
    "v_mfma_f32_16x16x32_bf16 v[240:243], v[128:131], v[160:163], v[240:243]\n\t" \
    "s_add_u32 s40, s40, 1\n\t" \
    "v_mov_b32 v245, s40\n\t" \
    "ds_write_b128 %[ld], v[224:227] offset:0\n\t" \
    "ds_write_b128 %[ld], v[228:231] offset:6144\n\t" \
    "ds_write_b128 %[ld], v[232:235] offset:12288\n\t" \
    "s_nop 0\n\t" \
    "ds_write_b128 %[ld], v[236:239] offset:18432\n\t" \
    "s_nop 1\n\t" \
    "ds_write_b128 %[ld], v[240:243] offset:24576\n\t" \
    "ds_write_b32 %[fa], v245\n\t" \
    "s_mov_b32 s43, 0\n\t" \
    "s_waitcnt vmcnt(0)\n\t" \
    "v_max_u32 v244, v220, v221\n\t" \
    "v_max3_u32 v244, v222, v223, v244\n\t" \
    "v_cmp_eq_u32 vcc, -1, v244\n\t" \
    "s_cbranch_vccnz .Lm27_%=\n\t" \
    "buffer_load_dwordx4 v[216:219], %[l16], %[rs], s49 offen offset:2048 sc1\n\t" \
    "buffer_load_dwordx4 v[212:215], %[l16], %[rs], s49 offen offset:1024 sc1\n\t" \
    "buffer_load_dwordx4 v[208:211], %[l16], %[rs], s49 offen offset:0 sc1\n\t" \
    "buffer_load_dwordx4 v[204:207], %[l16], %[rs], s48 offen offset:3072 sc1\n\t" \
    "buffer_load_dwordx4 v[200:203], %[l16], %[rs], s48 offen offset:2048 sc1\n\t" \
    "buffer_load_dwordx4 v[196:199], %[l16], %[rs], s48 offen offset:1024 sc1\n\t" \
    "buffer_load_dwordx4 v[192:195], %[l16], %[rs], s48 offen offset:0 sc1\n\t" \
    "s_mov_b32 s43, 1\n\t" \
    ".Lm27_%=:\n\t" \
    ".Lm28_%=:\n\t" \
    "ds_read_b32 v245, v246 offset:28\n\t" \
    "s_sub_i32 s44, s40, 3\n\t" \
    "s_waitcnt lgkmcnt(0)\n\t" \
    "v_cmp_gt_i32 vcc, s44, v245\n\t" \
    "s_cbranch_vccnz .Lm28_%=\n\t" \
    "s_add_u32 s44, s40, 1\n\t" \
    "s_lshr_b32 s45, s44, 1\n\t" \
    "s_and_b32 s44, s44, 1\n\t" \
    "s_lshl_b32 s44, s44, 12\n\t" \
    "s_mul_i32 s46, s45, 0x30000\n\t" \
    "s_add_u32 s46, s46, s44\n\t" \
    "s_lshr_b32 s45, s45, 3\n\t" \
    "s_mul_i32 s45, s45, 0x82000\n\t" \
    "s_add_u32 s46, s46, s45\n\t" \
    "s_add_u32 s46, s46, %[hb]\n\t" \
    "s_add_u32 s47, s46, 0x4000\n\t" \
    "s_cmp_lg_u32 s43, 0\n\t" \
    "s_cbranch_scc1 .Lm31_%=\n\t" \
    ".Lm29_%=:\n\t" \
    "v_max_u32 v244, v220, v221\n\t" \
    "v_max3_u32 v244, v222, v223, v244\n\t" \
    "v_cmp_eq_u32 vcc, -1, v244\n\t" \
    "s_cbranch_vccz .Lm30_%=\n\t" \
    "s_cmp_eq_u32 s41, 0\n\t" \
    "s_cbranch_scc1 .Lm30_%=\n\t" \
    "s_sub_u32 s41, s41, 1\n\t" \
    "s_sleep 3\n\t" \
    "buffer_load_dwordx4 v[220:223], %[l16], %[rs], s49 offen offset:3072 sc1\n\t" \
    "s_waitcnt vmcnt(0)\n\t" \
    "s_branch .Lm29_%=\n\t" \
    ".Lm30_%=:\n\t" \
    "buffer_load_dwordx4 v[216:219], %[l16], %[rs], s49 offen offset:2048 sc1\n\t" \
    "buffer_load_dwordx4 v[212:215], %[l16], %[rs], s49 offen offset:1024 sc1\n\t" \
    "buffer_load_dwordx4 v[208:211], %[l16], %[rs], s49 offen offset:0 sc1\n\t" \
    "buffer_load_dwordx4 v[204:207], %[l16], %[rs], s48 offen offset:3072 sc1\n\t" \
    "buffer_load_dwordx4 v[200:203], %[l16], %[rs], s48 offen offset:2048 sc1\n\t" \
    "buffer_load_dwordx4 v[196:199], %[l16], %[rs], s48 offen offset:1024 sc1\n\t" \
    "buffer_load_dwordx4 v[192:195], %[l16], %[rs], s48 offen offset:0 sc1\n\t" \
    ".Lm31_%=:\n\t" \
    "v_mfma_f32_16x16x32_bf16 v[224:227], v[28:31], v[220:223], 0\n\t" \
    "v_mfma_f32_16x16x32_bf16 v[228:231], v[60:63], v[220:223], 0\n\t" \
    "v_mfma_f32_16x16x32_bf16 v[232:235], v[92:95], v[220:223], 0\n\t" \
    "v_mfma_f32_16x16x32_bf16 v[236:239], v[124:127], v[220:223], 0\n\t" \
    "v_mfma_f32_16x16x32_bf16 v[240:243], v[156:159], v[220:223], 0\n\t" \
    "s_waitcnt vmcnt(6)\n\t" \
    ".Lm32_%=:\n\t" \
    "v_max_u32 v244, v216, v217\n\t" \
    "v_max3_u32 v244, v218, v219, v244\n\t" \
    "v_cmp_eq_u32 vcc, -1, v244\n\t" \
    "s_cbranch_vccnz .Lm33_%=\n\t" \
    ".Lm34_%=:\n\t" \
    "v_mfma_f32_16x16x32_bf16 v[224:227], v[24:27], v[216:219], v[224:227]\n\t" \
    "v_mfma_f32_16x16x32_bf16 v[228:231], v[56:59], v[216:219], v[228:231]\n\t" \
    "v_mfma_f32_16x16x32_bf16 v[232:235], v[88:91], v[216:219], v[232:235]\n\t" \
    "v_mfma_f32_16x16x32_bf16 v[236:239], v[120:123], v[216:219], v[236:239]\n\t" \
    "v_mfma_f32_16x16x32_bf16 v[240:243], v[152:155], v[216:219], v[240:243]\n\t" \
    "s_waitcnt vmcnt(5)\n\t" \
    ".Lm35_%=:\n\t" \
    "v_max_u32 v244, v212, v213\n\t" \
    "v_max3_u32 v244, v214, v215, v244\n\t" \
    "v_cmp_eq_u32 vcc, -1, v244\n\t" \
    "s_cbranch_vccnz .Lm36_%=\n\t" \
    ".Lm37_%=:\n\t" \
    "v_mfma_f32_16x16x32_bf16 v[224:227], v[20:23], v[212:215], v[224:227]\n\t" \
    "v_mfma_f32_16x16x32_bf16 v[228:231], v[52:55], v[212:215], v[228:231]\n\t" \
    "v_mfma_f32_16x16x32_bf16 v[232:235], v[84:87], v[212:215], v[232:235]\n\t" \
    "v_mfma_f32_16x16x32_bf16 v[236:239], v[116:119], v[212:215], v[236:239]\n\t" \
    "v_mfma_f32_16x16x32_bf16 v[240:243], v[148:151], v[212:215], v[240:243]\n\t" \
    "s_waitcnt vmcnt(4)\n\t" \
    ".Lm38_%=:\n\t" \
    "v_max_u32 v244, v208, v209\n\t" \
    "v_max3_u32 v244, v210, v211, v244\n\t" \
    "v_cmp_eq_u32 vcc, -1, v244\n\t" \
    "s_cbranch_vccnz .Lm39_%=\n\t" \
    ".Lm40_%=:\n\t" \
    "v_mfma_f32_16x16x32_bf16 v[224:227], v[16:19], v[208:211], v[224:227]\n\t" \
    "v_mfma_f32_16x16x32_bf16 v[228:231], v[48:51], v[208:211], v[228:231]\n\t" \
    "v_mfma_f32_16x16x32_bf16 v[232:235], v[80:83], v[208:211], v[232:235]\n\t" \
    "v_mfma_f32_16x16x32_bf16 v[236:239], v[112:115], v[208:211], v[236:239]\n\t" \
    "v_mfma_f32_16x16x32_bf16 v[240:243], v[144:147], v[208:211], v[240:243]\n\t" \
    "s_waitcnt vmcnt(3)\n\t" \
    ".Lm41_%=:\n\t" \
    "v_max_u32 v244, v204, v205\n\t" \
    "v_max3_u32 v244, v206, v207, v244\n\t" \
    "v_cmp_eq_u32 vcc, -1, v244\n\t" \
    "s_cbranch_vccnz .Lm42_%=\n\t" \
    ".Lm43_%=:\n\t" \
    "v_mfma_f32_16x16x32_bf16 v[224:227], v[12:15], v[204:207], v[224:227]\n\t" \
    "v_mfma_f32_16x16x32_bf16 v[228:231], v[44:47], v[204:207], v[228:231]\n\t" \
    "v_mfma_f32_16x16x32_bf16 v[232:235], v[76:79], v[204:207], v[232:235]\n\t" \
    "v_mfma_f32_16x16x32_bf16 v[236:239], v[108:111], v[204:207], v[236:239]\n\t" \
    "v_mfma_f32_16x16x32_bf16 v[240:243], v[140:143], v[204:207], v[240:243]\n\t" \
    "s_waitcnt vmcnt(2)\n\t" \
    ".Lm44_%=:\n\t" \
    "v_max_u32 v244, v200, v201\n\t" \
    "v_max3_u32 v244, v202, v203, v244\n\t" \
    "v_cmp_eq_u32 vcc, -1, v244\n\t" \
    "s_cbranch_vccnz .Lm45_%=\n\t" \
    ".Lm46_%=:\n\t" \
    "buffer_load_dwordx4 v[188:191], %[l16], %[rs], s47 offen offset:3072 sc1\n\t" \
    "v_mfma_f32_16x16x32_bf16 v[224:227], v[8:11], v[200:203], v[224:227]\n\t" \
    "v_mfma_f32_16x16x32_bf16 v[228:231], v[40:43], v[200:203], v[228:231]\n\t" \
    "v_mfma_f32_16x16x32_bf16 v[232:235], v[72:75], v[200:203], v[232:235]\n\t" \
    "v_mfma_f32_16x16x32_bf16 v[236:239], v[104:107], v[200:203], v[236:239]\n\t" \
    "v_mfma_f32_16x16x32_bf16 v[240:243], v[136:139], v[200:203], v[240:243]\n\t" \
    "s_waitcnt vmcnt(2)\n\t" \
    ".Lm47_%=:\n\t" \
    "v_max_u32 v244, v196, v197\n\t" \
    "v_max3_u32 v244, v198, v199, v244\n\t" \
    "v_cmp_eq_u32 vcc, -1, v244\n\t" \
    "s_cbranch_vccnz .Lm48_%=\n\t" \
    ".Lm49_%=:\n\t" \
    "v_mfma_f32_16x16x32_bf16 v[224:227], v[4:7], v[196:199], v[224:227]\n\t" \
    "v_mfma_f32_16x16x32_bf16 v[228:231], v[36:39], v[196:199], v[228:231]\n\t" \
    "v_mfma_f32_16x16x32_bf16 v[232:235], v[68:71], v[196:199], v[232:235]\n\t" \
    "v_mfma_f32_16x16x32_bf16 v[236:239], v[100:103], v[196:199], v[236:239]\n\t" \
    "v_mfma_f32_16x16x32_bf16 v[240:243], v[132:135], v[196:199], v[240:243]\n\t" \
    "s_waitcnt vmcnt(1)\n\t" \
    ".Lm50_%=:\n\t" \
    "v_max_u32 v244, v192, v193\n\t" \
    "v_max3_u32 v244, v194, v195, v244\n\t" \
    "v_cmp_eq_u32 vcc, -1, v244\n\t" \
    "s_cbranch_vccnz .Lm51_%=\n\t" \
    ".Lm52_%=:\n\t" \
    "v_mfma_f32_16x16x32_bf16 v[224:227], v[0:3], v[192:195], v[224:227]\n\t" \
    "v_mfma_f32_16x16x32_bf16 v[228:231], v[32:35], v[192:195], v[228:231]\n\t" \
    "v_mfma_f32_16x16x32_bf16 v[232:235], v[64:67], v[192:195], v[232:235]\n\t" \
    "v_mfma_f32_16x16x32_bf16 v[236:239], v[96:99], v[192:195], v[236:239]\n\t" \
    "v_mfma_f32_16x16x32_bf16 v[240:243], v[128:131], v[192:195], v[240:243]\n\t" \
    "s_add_u32 s40, s40, 1\n\t" \
    "v_mov_b32 v245, s40\n\t" \
    "ds_write_b128 %[ld], v[224:227] offset:30720\n\t" \
    "ds_write_b128 %[ld], v[228:231] offset:36864\n\t" \
    "ds_write_b128 %[ld], v[232:235] offset:43008\n\t" \
    "s_nop 0\n\t" \
    "ds_write_b128 %[ld], v[236:239] offset:49152\n\t" \
    "s_nop 1\n\t" \
    "ds_write_b128 %[ld], v[240:243] offset:55296\n\t" \
    "ds_write_b32 %[fa], v245\n\t" \
    "s_mov_b32 s43, 0\n\t" \
    "s_waitcnt vmcnt(0)\n\t" \
    "v_max_u32 v244, v188, v189\n\t" \
    "v_max3_u32 v244, v190, v191, v244\n\t" \
    "v_cmp_eq_u32 vcc, -1, v244\n\t" \
    "s_cbranch_vccnz .Lm53_%=\n\t" \
    "buffer_load_dwordx4 v[184:187], %[l16], %[rs], s47 offen offset:2048 sc1\n\t" \
    "buffer_load_dwordx4 v[180:183], %[l16], %[rs], s47 offen offset:1024 sc1\n\t" \
    "buffer_load_dwordx4 v[176:179], %[l16], %[rs], s47 offen offset:0 sc1\n\t" \
    "buffer_load_dwordx4 v[172:175], %[l16], %[rs], s46 offen offset:3072 sc1\n\t" \
    "buffer_load_dwordx4 v[168:171], %[l16], %[rs], s46 offen offset:2048 sc1\n\t" \
    "buffer_load_dwordx4 v[164:167], %[l16], %[rs], s46 offen offset:1024 sc1\n\t" \
    "buffer_load_dwordx4 v[160:163], %[l16], %[rs], s46 offen offset:0 sc1\n\t" \
    "s_mov_b32 s43, 1\n\t" \
    ".Lm53_%=:\n\t" \
    "v_sub_u32 %[ld], %[l2], %[ld]\n\t" \
    "s_nop 0\n\t" \
    "s_cmp_lg_u32 s40, 128\n\t" \
    "s_cbranch_scc1 .Lm1_%=\n\t" \
    "s_branch .Lend%=\n\t" \
    ".Lm7_%=:\n\t" \
    "s_cmp_eq_u32 s41, 0\n\t" \
    "s_cbranch_scc1 .Lm8_%=\n\t" \
    "s_sub_u32 s41, s41, 1\n\t" \
    "buffer_load_dwordx4 v[184:187], %[l16], %[rs], s47 offen offset:2048 sc1\n\t" \
    "s_waitcnt vmcnt(0)\n\t" \
    "s_branch .Lm6_%=\n\t" \
    ".Lm10_%=:\n\t" \
    "s_cmp_eq_u32 s41, 0\n\t" \
    "s_cbranch_scc1 .Lm11_%=\n\t" \
    "s_sub_u32 s41, s41, 1\n\t" \
    "buffer_load_dwordx4 v[180:183], %[l16], %[rs], s47 offen offset:1024 sc1\n\t" \
    "s_waitcnt vmcnt(0)\n\t" \
    "s_branch .Lm9_%=\n\t" \
    ".Lm13_%=:\n\t" \
    "s_cmp_eq_u32 s41, 0\n\t" \
    "s_cbranch_scc1 .Lm14_%=\n\t" \
    "s_sub_u32 s41, s41, 1\n\t" \
    "buffer_load_dwordx4 v[176:179], %[l16], %[rs], s47 offen offset:0 sc1\n\t" \
    "s_waitcnt vmcnt(0)\n\t" \
    "s_branch .Lm12_%=\n\t" \
    ".Lm16_%=:\n\t" \
    "s_cmp_eq_u32 s41, 0\n\t" \
    "s_cbranch_scc1 .Lm17_%=\n\t" \
    "s_sub_u32 s41, s41, 1\n\t" \
    "buffer_load_dwordx4 v[172:175], %[l16], %[rs], s46 offen offset:3072 sc1\n\t" \
    "s_waitcnt vmcnt(0)\n\t" \
    "s_branch .Lm15_%=\n\t" \
    ".Lm19_%=:\n\t" \
    "s_cmp_eq_u32 s41, 0\n\t" \
    "s_cbranch_scc1 .Lm20_%=\n\t" \
    "s_sub_u32 s41, s41, 1\n\t" \
    "buffer_load_dwordx4 v[168:171], %[l16], %[rs], s46 offen offset:2048 sc1\n\t" \
    "s_waitcnt vmcnt(0)\n\t" \
    "s_branch .Lm18_%=\n\t" \
    ".Lm22_%=:\n\t" \
    "s_cmp_eq_u32 s41, 0\n\t" \
    "s_cbranch_scc1 .Lm23_%=\n\t" \
    "s_sub_u32 s41, s41, 1\n\t" \
    "buffer_load_dwordx4 v[164:167], %[l16], %[rs], s46 offen offset:1024 sc1\n\t" \
    "s_waitcnt vmcnt(0)\n\t" \
    "s_branch .Lm21_%=\n\t" \
    ".Lm25_%=:\n\t" \
    "s_cmp_eq_u32 s41, 0\n\t" \
    "s_cbranch_scc1 .Lm26_%=\n\t" \
    "s_sub_u32 s41, s41, 1\n\t" \
    "buffer_load_dwordx4 v[160:163], %[l16], %[rs], s46 offen offset:0 sc1\n\t" \
    "s_waitcnt vmcnt(0)\n\t" \
    "s_branch .Lm24_%=\n\t" \
    ".Lm33_%=:\n\t" \
    "s_cmp_eq_u32 s41, 0\n\t" \
    "s_cbranch_scc1 .Lm34_%=\n\t" \
    "s_sub_u32 s41, s41, 1\n\t" \
    "buffer_load_dwordx4 v[216:219], %[l16], %[rs], s49 offen offset:2048 sc1\n\t" \
    "s_waitcnt vmcnt(0)\n\t" \
    "s_branch .Lm32_%=\n\t" \
    ".Lm36_%=:\n\t" \
    "s_cmp_eq_u32 s41, 0\n\t" \
    "s_cbranch_scc1 .Lm37_%=\n\t" \
    "s_sub_u32 s41, s41, 1\n\t" \
    "buffer_load_dwordx4 v[212:215], %[l16], %[rs], s49 offen offset:1024 sc1\n\t" \
    "s_waitcnt vmcnt(0)\n\t" \
    "s_branch .Lm35_%=\n\t" \
    ".Lm39_%=:\n\t" \
    "s_cmp_eq_u32 s41, 0\n\t" \
    "s_cbranch_scc1 .Lm40_%=\n\t" \
    "s_sub_u32 s41, s41, 1\n\t" \
    "buffer_load_dwordx4 v[208:211], %[l16], %[rs], s49 offen offset:0 sc1\n\t" \
    "s_waitcnt vmcnt(0)\n\t" \
    "s_branch .Lm38_%=\n\t" \
    ".Lm42_%=:\n\t" \
    "s_cmp_eq_u32 s41, 0\n\t" \
    "s_cbranch_scc1 .Lm43_%=\n\t" \
    "s_sub_u32 s41, s41, 1\n\t" \
    "buffer_load_dwordx4 v[204:207], %[l16], %[rs], s48 offen offset:3072 sc1\n\t" \
    "s_waitcnt vmcnt(0)\n\t" \
    "s_branch .Lm41_%=\n\t" \
    ".Lm45_%=:\n\t" \
    "s_cmp_eq_u32 s41, 0\n\t" \
    "s_cbranch_scc1 .Lm46_%=\n\t" \
    "s_sub_u32 s41, s41, 1\n\t" \
    "buffer_load_dwordx4 v[200:203], %[l16], %[rs], s48 offen offset:2048 sc1\n\t" \
    "s_waitcnt vmcnt(0)\n\t" \
    "s_branch .Lm44_%=\n\t" \
    ".Lm48_%=:\n\t" \
    "s_cmp_eq_u32 s41, 0\n\t" \
    "s_cbranch_scc1 .Lm49_%=\n\t" \
    "s_sub_u32 s41, s41, 1\n\t" \
    "buffer_load_dwordx4 v[196:199], %[l16], %[rs], s48 offen offset:1024 sc1\n\t" \
    "s_waitcnt vmcnt(0)\n\t" \
    "s_branch .Lm47_%=\n\t" \
    ".Lm51_%=:\n\t" \
    "s_cmp_eq_u32 s41, 0\n\t" \
    "s_cbranch_scc1 .Lm52_%=\n\t" \
    "s_sub_u32 s41, s41, 1\n\t" \
    "buffer_load_dwordx4 v[192:195], %[l16], %[rs], s48 offen offset:0 sc1\n\t" \
    "s_waitcnt vmcnt(0)\n\t" \
    "s_branch .Lm50_%=\n\t" \
    ".Lend%=:\n\t" \
    "s_waitcnt vmcnt(0) lgkmcnt(0)\n\t" \
    "s_mov_b32 %[bud], s41\n\t"
#define ASM_W4_FIN0 \
    "s_mov_b32 s40, %[mt]\n\t" \
    "s_mov_b32 s41, 0x40000\n\t" \
    "s_mov_b32 s43, 0\n\t" \
    "v_mov_b32 v99, 0x3f803f80\n\t" \
    "s_setprio 3\n\t" \
    "s_add_u32 s46, s43, 0x2c00000\n\t" \
    "s_add_u32 s47, s43, 0x1c00000\n\t" \
    "buffer_load_dwordx2 v[120:121], %[t0], %[rs], s46 offen sc1\n\t" \
    "buffer_load_dwordx2 v[122:123], %[t0], %[rs], s47 offen sc1\n\t" \
    "buffer_load_dwordx2 v[124:125], %[t1], %[rs], s46 offen sc1\n\t" \
    "buffer_load_dwordx2 v[126:127], %[t1], %[rs], s47 offen sc1\n\t" \
    "buffer_load_dwordx2 v[128:129], %[t2], %[rs], s46 offen sc1\n\t" \
    "buffer_load_dwordx2 v[130:131], %[t2], %[rs], s47 offen sc1\n\t" \
    "buffer_load_dwordx2 v[132:133], %[t3], %[rs], s46 offen sc1\n\t" \
    "buffer_load_dwordx2 v[134:135], %[t3], %[rs], s47 offen sc1\n\t" \
    "buffer_load_dwordx2 v[136:137], %[t4], %[rs], s46 offen sc1\n\t" \
    "buffer_load_dwordx2 v[138:139], %[t4], %[rs], s47 offen sc1\n\t" \
    "s_waitcnt vmcnt(0)\n\t" \
    ".Lf01_%=:\n\t" \
    ".Lf02_%=:\n\t" \
    "s_waitcnt vmcnt(1)\n\t" \
    "v_max_u32 v80, v120, v121\n\t" \
    "v_max3_u32 v80, v122, v123, v80\n\t" \
    "v_max3_u32 v80, v124, v125, v80\n\t" \
    "v_max3_u32 v80, v126, v127, v80\n\t" \
    "v_max3_u32 v80, v128, v129, v80\n\t" \
    "v_max3_u32 v80, v130, v131, v80\n\t" \
    "v_max3_u32 v80, v132, v133, v80\n\t" \
    "v_max3_u32 v80, v134, v135, v80\n\t" \
    "v_max3_u32 v80, v136, v137, v80\n\t" \
    "v_max3_u32 v80, v138, v139, v80\n\t" \
    "v_cmp_eq_u32 vcc, -1, v80\n\t" \
    "s_cbranch_vccz .Lf03_%=\n\t" \
    "s_cmp_eq_u32 s41, 0\n\t" \
    "s_cbranch_scc1 .Lf03_%=\n\t" \
    "s_sub_u32 s41, s41, 1\n\t" \
    "s_sleep 8\n\t" \
    "s_add_u32 s46, s43, 0x2c00000\n\t" \
    "s_add_u32 s47, s43, 0x1c00000\n\t" \
    "buffer_load_dwordx2 v[120:121], %[t0], %[rs], s46 offen sc1\n\t" \
    "buffer_load_dwordx2 v[122:123], %[t0], %[rs], s47 offen sc1\n\t" \
    "buffer_load_dwordx2 v[124:125], %[t1], %[rs], s46 offen sc1\n\t" \
    "buffer_load_dwordx2 v[126:127], %[t1], %[rs], s47 offen sc1\n\t" \
    "buffer_load_dwordx2 v[128:129], %[t2], %[rs], s46 offen sc1\n\t" \
    "buffer_load_dwordx2 v[130:131], %[t2], %[rs], s47 offen sc1\n\t" \
    "buffer_load_dwordx2 v[132:133], %[t3], %[rs], s46 offen sc1\n\t" \
    "buffer_load_dwordx2 v[134:135], %[t3], %[rs], s47 offen sc1\n\t" \
    "buffer_load_dwordx2 v[136:137], %[t4], %[rs], s46 offen sc1\n\t" \
    "buffer_load_dwordx2 v[138:139], %[t4], %[rs], s47 offen sc1\n\t" \
    "s_waitcnt vmcnt(0)\n\t" \
    "s_branch .Lf02_%=\n\t" \
    ".Lf03_%=:\n\t" \
    "v_lshlrev_b32 v140, 16, v120\n\t" \
    "v_and_b32 v141, 0xffff0000, v120\n\t" \
    "v_lshlrev_b32 v142, 16, v121\n\t" \
    "v_and_b32 v143, 0xffff0000, v121\n\t" \
    "v_lshlrev_b32 v80, 16, v122\n\t" \
    "v_and_b32 v81, 0xffff0000, v122\n\t" \
    "v_lshlrev_b32 v82, 16, v123\n\t" \
    "v_and_b32 v83, 0xffff0000, v123\n\t" \
    "v_add_f32 v140, v140, v80\n\t" \
    "v_add_f32 v141, v141, v81\n\t" \
    "v_add_f32 v142, v142, v82\n\t" \
    "v_add_f32 v143, v143, v83\n\t" \
    "v_lshlrev_b32 v144, 16, v124\n\t" \
    "v_and_b32 v145, 0xffff0000, v124\n\t" \
    "v_lshlrev_b32 v146, 16, v125\n\t" \
    "v_and_b32 v147, 0xffff0000, v125\n\t" \
    "v_lshlrev_b32 v80, 16, v126\n\t" \
    "v_and_b32 v81, 0xffff0000, v126\n\t" \
    "v_lshlrev_b32 v82, 16, v127\n\t" \
    "v_and_b32 v83, 0xffff0000, v127\n\t" \
    "v_add_f32 v144, v144, v80\n\t" \
    "v_add_f32 v145, v145, v81\n\t" \
    "v_add_f32 v146, v146, v82\n\t" \
    "v_add_f32 v147, v147, v83\n\t" \
    "v_lshlrev_b32 v148, 16, v128\n\t" \
    "v_and_b32 v149, 0xffff0000, v128\n\t" \
    "v_lshlrev_b32 v150, 16, v129\n\t" \
    "v_and_b32 v151, 0xffff0000, v129\n\t" \
    "v_lshlrev_b32 v80, 16, v130\n\t" \
    "v_and_b32 v81, 0xffff0000, v130\n\t" \
    "v_lshlrev_b32 v82, 16, v131\n\t" \
    "v_and_b32 v83, 0xffff0000, v131\n\t" \
    "v_add_f32 v148, v148, v80\n\t" \
    "v_add_f32 v149, v149, v81\n\t" \
    "v_add_f32 v150, v150, v82\n\t" \
    "v_add_f32 v151, v151, v83\n\t" \
    "v_lshlrev_b32 v152, 16, v132\n\t" \
    "v_and_b32 v153, 0xffff0000, v132\n\t" \
    "v_lshlrev_b32 v154, 16, v133\n\t" \
    "v_and_b32 v155, 0xffff0000, v133\n\t" \
    "v_lshlrev_b32 v80, 16, v134\n\t" \
    "v_and_b32 v81, 0xffff0000, v134\n\t" \
    "v_lshlrev_b32 v82, 16, v135\n\t" \
    "v_and_b32 v83, 0xffff0000, v135\n\t" \
    "v_add_f32 v152, v152, v80\n\t" \
    "v_add_f32 v153, v153, v81\n\t" \
    "v_add_f32 v154, v154, v82\n\t" \
    "v_add_f32 v155, v155, v83\n\t" \
    "v_lshlrev_b32 v156, 16, v136\n\t" \
    "v_and_b32 v157, 0xffff0000, v136\n\t" \
    "v_lshlrev_b32 v158, 16, v137\n\t" \
    "v_and_b32 v159, 0xffff0000, v137\n\t" \
    "v_lshlrev_b32 v80, 16, v138\n\t" \
    "v_and_b32 v81, 0xffff0000, v138\n\t" \
    "v_lshlrev_b32 v82, 16, v139\n\t" \
    "v_and_b32 v83, 0xffff0000, v139\n\t" \
    "v_add_f32 v156, v156, v80\n\t" \
    "v_add_f32 v157, v157, v81\n\t" \
    "v_add_f32 v158, v158, v82\n\t" \
    "v_add_f32 v159, v159, v83\n\t" \
    "s_mov_b32 s42, s43\n\t" \
    "s_lshr_b32 s48, s40, 1\n\t" \
    "s_add_u32 s48, s48, 1\n\t" \
    "s_mul_i32 s43, s48, 0x14000\n\t" \
    "s_lshr_b32 s44, s48, 4\n\t" \
    "s_mul_i32 s44, s44, 0xc2000\n\t" \
    "s_add_u32 s43, s43, s44\n\t" \
    "s_lshr_b32 s44, s40, 3\n\t" \
    "s_lshl_b32 s44, s44, 14\n\t" \
    "s_and_b32 s45, s40, 7\n\t" \
    "s_lshl_b32 s45, s45, 10\n\t" \
    "s_add_u32 s49, s44, s45\n\t" \
    "s_add_u32 s49, s49, 0x1c0000\n\t" \
    "s_add_u32 s50, s49, %[co]\n\t" \
    "s_add_u32 s51, s42, 0x1400000\n\t" \
    "s_add_u32 s52, s42, 0xc00000\n\t" \
    "s_mov_b32 s44, s48\n\t" \
    "s_mov_b32 s45, s48\n\t" \
    "v_mov_b64 v[76:77], s[44:45]\n\t" \
    "v_mov_b64 v[78:79], s[44:45]\n\t" \
    "s_add_u32 s44, s40, 1\n\t" \
    "v_mov_b32 v98, s44\n\t" \
    ".Lf04_%=:\n\t" \
    "s_nop 7\n\t" \
    "ds_read_b32 v80, %[fa]\n\t" \
    "s_waitcnt lgkmcnt(0)\n\t" \
    "v_cmp_ge_u32 vcc, s40, v80\n\t" \
    "s_cbranch_vccnz .Lf04_%=\n\t" \
    "ds_read_b128 v[160:163], %[la] offset:0\n\t" \
    "ds_read_b128 v[164:167], %[la] offset:6144\n\t" \
    "ds_read_b128 v[168:171], %[la] offset:12288\n\t" \
    "ds_read_b128 v[172:175], %[la] offset:18432\n\t" \
    "ds_read_b128 v[176:179], %[la] offset:24576\n\t" \
    "ds_read_b128 v[180:183], %[la] offset:1024\n\t" \
    "ds_read_b128 v[184:187], %[la] offset:7168\n\t" \
    "ds_read_b128 v[188:191], %[la] offset:13312\n\t" \
    "ds_read_b128 v[192:195], %[la] offset:19456\n\t" \
    "ds_read_b128 v[196:199], %[la] offset:25600\n\t" \
    "ds_read_b128 v[200:203], %[la] offset:2048\n\t" \
    "ds_read_b128 v[204:207], %[la] offset:8192\n\t" \
    "ds_read_b128 v[208:211], %[la] offset:14336\n\t" \
    "ds_read_b128 v[212:215], %[la] offset:20480\n\t" \
    "ds_read_b128 v[216:219], %[la] offset:26624\n\t" \
    "s_waitcnt lgkmcnt(14)\n\t" \
    "v_pk_add_f32 v[100:101], v[140:141], v[160:161]\n\t" \
    "v_pk_add_f32 v[102:103], v[142:143], v[162:163]\n\t" \
    "ds_read_b128 v[220:223], %[la] offset:3072\n\t" \
    "s_waitcnt lgkmcnt(14)\n\t" \
    "v_pk_add_f32 v[104:105], v[144:145], v[164:165]\n\t" \
    "v_pk_add_f32 v[106:107], v[146:147], v[166:167]\n\t" \
    "ds_read_b128 v[224:227], %[la] offset:9216\n\t" \
    "s_waitcnt lgkmcnt(14)\n\t" \
    "v_pk_add_f32 v[108:109], v[148:149], v[168:169]\n\t" \
    "v_pk_add_f32 v[110:111], v[150:151], v[170:171]\n\t" \
    "ds_read_b128 v[228:231], %[la] offset:15360\n\t" \
    "s_waitcnt lgkmcnt(14)\n\t" \
    "v_pk_add_f32 v[112:113], v[152:153], v[172:173]\n\t" \
    "v_pk_add_f32 v[114:115], v[154:155], v[174:175]\n\t" \
    "ds_read_b128 v[232:235], %[la] offset:21504\n\t" \
    "s_waitcnt lgkmcnt(14)\n\t" \
    "v_pk_add_f32 v[116:117], v[156:157], v[176:177]\n\t" \
    "v_pk_add_f32 v[118:119], v[158:159], v[178:179]\n\t" \
    "ds_read_b128 v[236:239], %[la] offset:27648\n\t" \
    "s_waitcnt lgkmcnt(14)\n\t" \
    "v_pk_add_f32 v[100:101], v[100:101], v[180:181]\n\t" \
    "v_pk_add_f32 v[102:103], v[102:103], v[182:183]\n\t" \
    "ds_read_b128 v[160:163], %[la] offset:4096\n\t" \
    "s_waitcnt lgkmcnt(14)\n\t" \
    "v_pk_add_f32 v[104:105], v[104:105], v[184:185]\n\t" \
    "v_pk_add_f32 v[106:107], v[106:107], v[186:187]\n\t" \
    "ds_read_b128 v[164:167], %[la] offset:10240\n\t" \
    "s_waitcnt lgkmcnt(14)\n\t" \
    "v_pk_add_f32 v[108:109], v[108:109], v[188:189]\n\t" \
    "v_pk_add_f32 v[110:111], v[110:111], v[190:191]\n\t" \
    "ds_read_b128 v[168:171], %[la] offset:16384\n\t" \
    "s_waitcnt lgkmcnt(14)\n\t" \
    "v_pk_add_f32 v[112:113], v[112:113], v[192:193]\n\t" \
    "v_pk_add_f32 v[114:115], v[114:115], v[194:195]\n\t" \
    "ds_read_b128 v[172:175], %[la] offset:22528\n\t" \
    "s_waitcnt lgkmcnt(14)\n\t" \
    "v_pk_add_f32 v[116:117], v[116:117], v[196:197]\n\t" \
    "v_pk_add_f32 v[118:119], v[118:119], v[198:199]\n\t" \
    "ds_read_b128 v[176:179], %[la] offset:28672\n\t" \
    "s_waitcnt lgkmcnt(14)\n\t" \
    "v_pk_add_f32 v[100:101], v[100:101], v[200:201]\n\t" \
    "v_pk_add_f32 v[102:103], v[102:103], v[202:203]\n\t" \
    "ds_read_b128 v[180:183], %[la] offset:5120\n\t" \
    "s_waitcnt lgkmcnt(14)\n\t" \
    "v_pk_add_f32 v[104:105], v[104:105], v[204:205]\n\t" \
    "v_pk_add_f32 v[106:107], v[106:107], v[206:207]\n\t" \
    "ds_read_b128 v[184:187], %[la] offset:11264\n\t" \
    "s_waitcnt lgkmcnt(14)\n\t" \
    "v_pk_add_f32 v[108:109], v[108:109], v[208:209]\n\t" \
    "v_pk_add_f32 v[110:111], v[110:111], v[210:211]\n\t" \
    "ds_read_b128 v[188:191], %[la] offset:17408\n\t" \
    "s_waitcnt lgkmcnt(14)\n\t" \
    "v_pk_add_f32 v[112:113], v[112:113], v[212:213]\n\t" \
    "v_pk_add_f32 v[114:115], v[114:115], v[214:215]\n\t" \
    "ds_read_b128 v[192:195], %[la] offset:23552\n\t" \
    "s_waitcnt lgkmcnt(14)\n\t" \
    "v_pk_add_f32 v[116:117], v[116:117], v[216:217]\n\t" \
    "v_pk_add_f32 v[118:119], v[118:119], v[218:219]\n\t" \
    "ds_read_b128 v[196:199], %[la] offset:29696\n\t" \
    "s_waitcnt lgkmcnt(14)\n\t" \
    "v_pk_add_f32 v[100:101], v[100:101], v[220:221]\n\t" \
    "v_pk_add_f32 v[102:103], v[102:103], v[222:223]\n\t" \
    "s_waitcnt lgkmcnt(13)\n\t" \
    "v_pk_add_f32 v[104:105], v[104:105], v[224:225]\n\t" \
    "v_pk_add_f32 v[106:107], v[106:107], v[226:227]\n\t" \
    "s_waitcnt lgkmcnt(12)\n\t" \
    "v_pk_add_f32 v[108:109], v[108:109], v[228:229]\n\t" \
    "v_pk_add_f32 v[110:111], v[110:111], v[230:231]\n\t" \
    "s_waitcnt lgkmcnt(11)\n\t" \
    "v_pk_add_f32 v[112:113], v[112:113], v[232:233]\n\t" \
    "v_pk_add_f32 v[114:115], v[114:115], v[234:235]\n\t" \
    "s_waitcnt lgkmcnt(10)\n\t" \
    "v_pk_add_f32 v[116:117], v[116:117], v[236:237]\n\t" \
    "v_pk_add_f32 v[118:119], v[118:119], v[238:239]\n\t" \
    "s_waitcnt lgkmcnt(9)\n\t" \
    "v_pk_add_f32 v[100:101], v[100:101], v[160:161]\n\t" \
    "v_pk_add_f32 v[102:103], v[102:103], v[162:163]\n\t" \
    "s_waitcnt lgkmcnt(8)\n\t" \
    "v_pk_add_f32 v[104:105], v[104:105], v[164:165]\n\t" \
    "v_pk_add_f32 v[106:107], v[106:107], v[166:167]\n\t" \
    "s_waitcnt lgkmcnt(7)\n\t" \
    "v_pk_add_f32 v[108:109], v[108:109], v[168:169]\n\t" \
    "v_pk_add_f32 v[110:111], v[110:111], v[170:171]\n\t" \
    "s_waitcnt lgkmcnt(6)\n\t" \
    "v_pk_add_f32 v[112:113], v[112:113], v[172:173]\n\t" \
    "v_pk_add_f32 v[114:115], v[114:115], v[174:175]\n\t" \
    "s_waitcnt lgkmcnt(5)\n\t" \
    "v_pk_add_f32 v[116:117], v[116:117], v[176:177]\n\t" \
    "v_pk_add_f32 v[118:119], v[118:119], v[178:179]\n\t" \
    "s_waitcnt lgkmcnt(4)\n\t" \
    "v_pk_add_f32 v[100:101], v[100:101], v[180:181]\n\t" \
    "v_pk_add_f32 v[102:103], v[102:103], v[182:183]\n\t" \
    "s_waitcnt lgkmcnt(3)\n\t" \
    "v_pk_add_f32 v[104:105], v[104:105], v[184:185]\n\t" \
    "v_pk_add_f32 v[106:107], v[106:107], v[186:187]\n\t" \
    "s_waitcnt lgkmcnt(2)\n\t" \
    "v_pk_add_f32 v[108:109], v[108:109], v[188:189]\n\t" \
    "v_pk_add_f32 v[110:111], v[110:111], v[190:191]\n\t" \
    "s_waitcnt lgkmcnt(1)\n\t" \
    "v_pk_add_f32 v[112:113], v[112:113], v[192:193]\n\t" \
    "v_pk_add_f32 v[114:115], v[114:115], v[194:195]\n\t" \
    "s_waitcnt lgkmcnt(0)\n\t" \
    "v_pk_add_f32 v[116:117], v[116:117], v[196:197]\n\t" \
    "v_pk_add_f32 v[118:119], v[118:119], v[198:199]\n\t" \
    "ds_write_b32 %[fc], v98\n\t" \
    "v_sub_u32 %[la], %[l2], %[la]\n\t" \
    "v_cvt_pk_bf16_f32 v84, v100, v101\n\t" \
    "v_cvt_pk_bf16_f32 v85, v102, v103\n\t" \
    "v_cvt_pk_bf16_f32 v86, v104, v105\n\t" \
    "v_cvt_pk_bf16_f32 v87, v106, v107\n\t" \
    "v_cvt_pk_bf16_f32 v88, v108, v109\n\t" \
    "v_cvt_pk_bf16_f32 v89, v110, v111\n\t" \
    "v_cvt_pk_bf16_f32 v90, v112, v113\n\t" \
    "v_cvt_pk_bf16_f32 v91, v114, v115\n\t" \
    "v_cvt_pk_bf16_f32 v92, v116, v117\n\t" \
    "v_cvt_pk_bf16_f32 v93, v118, v119\n\t" \
    "v_mov_b32 v80, 0\n\t" \
    "v_mov_b32 v82, 0\n\t" \
    "v_dot2c_f32_bf16 v80, v84, v99\n\t" \
    "v_dot2c_f32_bf16 v82, v84, v84\n\t" \
    "v_dot2c_f32_bf16 v80, v85, v99\n\t" \
    "v_dot2c_f32_bf16 v82, v85, v85\n\t" \
    "v_dot2c_f32_bf16 v80, v86, v99\n\t" \
    "v_dot2c_f32_bf16 v82, v86, v86\n\t" \
    "v_dot2c_f32_bf16 v80, v87, v99\n\t" \
    "v_dot2c_f32_bf16 v82, v87, v87\n\t" \
    "v_dot2c_f32_bf16 v80, v88, v99\n\t" \
    "v_dot2c_f32_bf16 v82, v88, v88\n\t" \
    "v_dot2c_f32_bf16 v80, v89, v99\n\t" \
    "v_dot2c_f32_bf16 v82, v89, v89\n\t" \
    "v_dot2c_f32_bf16 v80, v90, v99\n\t" \
    "v_dot2c_f32_bf16 v82, v90, v90\n\t" \
    "v_dot2c_f32_bf16 v80, v91, v99\n\t" \
    "v_dot2c_f32_bf16 v82, v91, v91\n\t" \
    "v_dot2c_f32_bf16 v80, v92, v99\n\t" \
    "v_dot2c_f32_bf16 v82, v92, v92\n\t" \
    "v_dot2c_f32_bf16 v80, v93, v99\n\t" \
    "v_dot2c_f32_bf16 v82, v93, v93\n\t" \
    "s_nop 2\n\t" \
    "v_mov_b32 v81, v80\n\t" \
    "v_mov_b32 v83, v82\n\t" \
    "s_nop 1\n\t" \
    "v_permlane16_swap_b32 v80, v81\n\t" \
    "v_permlane16_swap_b32 v82, v83\n\t" \
    "v_add_f32 v80, v80, v81\n\t" \
    "v_add_f32 v82, v82, v83\n\t" \
    "v_mov_b32 v81, v80\n\t" \
    "v_mov_b32 v83, v82\n\t" \
    "s_nop 1\n\t" \
    "v_permlane32_swap_b32 v80, v81\n\t" \
    "v_permlane32_swap_b32 v82, v83\n\t" \
    "v_add_f32 v80, v80, v81\n\t" \
    "v_add_f32 v82, v82, v83\n\t" \
    "v_min_u32 v80, -2, v80\n\t" \
    "v_min_u32 v81, -2, v82\n\t" \
    "s_mov_b64 exec, 0xffff\n\t" \
    "buffer_store_dwordx2 v[80:81], %[l8], %[rs], s50 offen sc1\n\t" \
    "s_mov_b64 exec, -1\n\t" \
    "s_add_u32 s46, s43, 0x2c00000\n\t" \
    "s_add_u32 s47, s43, 0x1c00000\n\t" \
    "buffer_load_dwordx2 v[120:121], %[t0], %[rs], s46 offen sc1\n\t" \
    "buffer_load_dwordx2 v[122:123], %[t0], %[rs], s47 offen sc1\n\t" \
    "buffer_load_dwordx2 v[124:125], %[t1], %[rs], s46 offen sc1\n\t" \
    "buffer_load_dwordx2 v[126:127], %[t1], %[rs], s47 offen sc1\n\t" \
    "buffer_load_dwordx2 v[128:129], %[t2], %[rs], s46 offen sc1\n\t" \
    "buffer_load_dwordx2 v[130:131], %[t2], %[rs], s47 offen sc1\n\t" \
    "buffer_load_dwordx2 v[132:133], %[t3], %[rs], s46 offen sc1\n\t" \
    "buffer_load_dwordx2 v[134:135], %[t3], %[rs], s47 offen sc1\n\t" \
    "buffer_load_dwordx2 v[136:137], %[t4], %[rs], s46 offen sc1\n\t" \
    "buffer_load_dwordx2 v[138:139], %[t4], %[rs], s47 offen sc1\n\t" \
    "s_nop 0\n\t" \
    "v_permlane16_swap_b32 v84, v86\n\t" \
    "v_permlane16_swap_b32 v85, v87\n\t" \
    "v_permlane16_swap_b32 v88, v90\n\t" \
    "v_permlane16_swap_b32 v89, v91\n\t" \
    "s_sleep 4\n\t" \
    ".Lf05_%=:\n\t" \
    "buffer_load_dwordx2 v[94:95], %[l8], %[rs], s49 offen sc1\n\t" \
    "buffer_load_dwordx2 v[96:97], %[l8], %[rs], s49 offen offset:512 sc1\n\t" \
    "s_waitcnt vmcnt(0)\n\t" \
    "v_max_u32 v80, v94, v95\n\t" \
    "v_max3_u32 v80, v96, v97, v80\n\t" \
    "v_cmp_eq_u32 vcc, -1, v80\n\t" \
    "s_cbranch_vccz .Lf06_%=\n\t" \
    "s_cmp_eq_u32 s41, 0\n\t" \
    "s_cbranch_scc1 .Lf06_%=\n\t" \
    "s_sub_u32 s41, s41, 1\n\t" \
    "s_branch .Lf05_%=\n\t" \
    ".Lf06_%=:\n\t" \
    "v_add_f32 v80, v94, v96\n\t" \
    "v_add_f32 v82, v95, v97\n\t" \
    "v_mov_b32 v81, v80\n\t" \
    "v_mov_b32 v83, v82\n\t" \
    "s_nop 1\n\t" \
    "v_permlane16_swap_b32 v80, v81\n\t" \
    "v_permlane16_swap_b32 v82, v83\n\t" \
    "v_add_f32 v80, v80, v81\n\t" \
    "v_add_f32 v82, v82, v83\n\t" \
    "v_mov_b32 v81, v80\n\t" \
    "v_mov_b32 v83, v82\n\t" \
    "s_nop 1\n\t" \
    "v_permlane32_swap_b32 v80, v81\n\t" \
    "v_permlane32_swap_b32 v82, v83\n\t" \
    "v_add_f32 v80, v80, v81\n\t" \
    "v_add_f32 v82, v82, v83\n\t" \
    "v_mul_f32 v80, 0x3acccccd, v80\n\t" \
    "v_mul_f32 v82, 0x3acccccd, v82\n\t" \
    "v_fma_f32 v82, -v80, v80, v82\n\t" \
    "v_cvt_pk_bf16_f32 v80, v80, v80\n\t" \
    "v_and_b32 v80, 0xffff0000, v80\n\t" \
    "v_max_f32 v82, 0, v82\n\t" \
    "v_cvt_pk_bf16_f32 v82, v82, v82\n\t" \
    "v_and_b32 v82, 0xffff0000, v82\n\t" \
    "v_add_f32 v82, 0x3727c5ac, v82\n\t" \
    "v_cvt_pk_bf16_f32 v82, v82, v82\n\t" \
    "v_and_b32 v82, 0xffff0000, v82\n\t" \
    "v_sqrt_f32 v82, v82\n\t" \
    "s_nop 0\n\t" \
    "v_cvt_pk_bf16_f32 v82, v82, v82\n\t" \
    "v_and_b32 v82, 0xffff0000, v82\n\t" \
    "v_rcp_f32 v82, v82\n\t" \
    "s_nop 0\n\t" \
    "v_mul_f32 v80, -v80, v82\n\t" \
    "v_fma_f32 v100, v100, v82, v80\n\t" \
    "v_fma_f32 v101, v101, v82, v80\n\t" \
    "v_fma_f32 v102, v102, v82, v80\n\t" \
    "v_fma_f32 v103, v103, v82, v80\n\t" \
    "v_fma_f32 v104, v104, v82, v80\n\t" \
    "v_fma_f32 v105, v105, v82, v80\n\t" \
    "v_fma_f32 v106, v106, v82, v80\n\t" \
    "v_fma_f32 v107, v107, v82, v80\n\t" \
    "v_fma_f32 v108, v108, v82, v80\n\t" \
    "v_fma_f32 v109, v109, v82, v80\n\t" \
    "v_fma_f32 v110, v110, v82, v80\n\t" \
    "v_fma_f32 v111, v111, v82, v80\n\t" \
    "v_fma_f32 v112, v112, v82, v80\n\t" \
    "v_fma_f32 v113, v113, v82, v80\n\t" \
    "v_fma_f32 v114, v114, v82, v80\n\t" \
    "v_fma_f32 v115, v115, v82, v80\n\t" \
    "v_fma_f32 v116, v116, v82, v80\n\t" \
    "v_fma_f32 v117, v117, v82, v80\n\t" \
    "v_fma_f32 v118, v118, v82, v80\n\t" \
    "v_fma_f32 v119, v119, v82, v80\n\t" \
    "v_cvt_pk_bf16_f32 v64, v100, v101\n\t" \
    "v_cvt_pk_bf16_f32 v65, v102, v103\n\t" \
    "v_cvt_pk_bf16_f32 v66, v104, v105\n\t" \
    "v_cvt_pk_bf16_f32 v67, v106, v107\n\t" \
    "v_cvt_pk_bf16_f32 v68, v108, v109\n\t" \
    "v_cvt_pk_bf16_f32 v69, v110, v111\n\t" \
    "v_cvt_pk_bf16_f32 v70, v112, v113\n\t" \
    "v_cvt_pk_bf16_f32 v71, v114, v115\n\t" \
    "v_cvt_pk_bf16_f32 v72, v116, v117\n\t" \
    "v_cvt_pk_bf16_f32 v73, v118, v119\n\t" \
    "s_nop 0\n\t" \
    "v_permlane16_swap_b32 v64, v66\n\t" \
    "v_permlane16_swap_b32 v65, v67\n\t" \
    "v_permlane16_swap_b32 v68, v70\n\t" \
    "v_permlane16_swap_b32 v69, v71\n\t" \
    "buffer_store_dwordx4 v[64:67], %[o16], %[rn], s51 offen sc1\n\t" \
    "buffer_store_dwordx4 v[68:71], %[o17], %[rn], s51 offen sc1\n\t" \
    "buffer_store_dwordx2 v[72:73], %[t4], %[rn], s51 offen sc1\n\t" \
    "buffer_store_dwordx4 v[84:87], %[o16], %[rn], s52 offen sc1\n\t" \
    "buffer_store_dwordx4 v[88:91], %[o17], %[rn], s52 offen sc1\n\t" \
    "buffer_store_dwordx2 v[92:93], %[t4], %[rn], s52 offen sc1\n\t" \
    "s_waitcnt vmcnt(0)\n\t" \
    "buffer_store_dwordx4 v[76:79], %[fo], %[rn], %[fs] offen sc1\n\t" \
    "s_add_u32 s40, s40, 2\n\t" \
    "s_cmp_lt_u32 s40, 128\n\t" \
    "s_cbranch_scc1 .Lf01_%=\n\t" \
    "s_branch .Lend%=\n\t" \
    ".Lend%=:\n\t" \
    "s_waitcnt vmcnt(0) lgkmcnt(0)\n\t" \
    "s_mov_b32 %[bud], s41\n\t"
#define ASM_W4_FIN1 \
    "s_mov_b32 s40, %[mt]\n\t" \
    "s_mov_b32 s41, 0x40000\n\t" \
    "s_mov_b32 s43, 0\n\t" \
    "v_mov_b32 v99, 0x3f803f80\n\t" \
    "s_setprio 3\n\t" \
    "s_add_u32 s46, s43, 0x2c00000\n\t" \
    "s_add_u32 s47, s43, 0x1c00000\n\t" \
    "buffer_load_dwordx2 v[120:121], %[t0], %[rs], s46 offen sc1\n\t" \
    "buffer_load_dwordx2 v[122:123], %[t0], %[rs], s47 offen sc1\n\t" \
    "buffer_load_dwordx2 v[124:125], %[t1], %[rs], s46 offen sc1\n\t" \
    "buffer_load_dwordx2 v[126:127], %[t1], %[rs], s47 offen sc1\n\t" \
    "buffer_load_dwordx2 v[128:129], %[t2], %[rs], s46 offen sc1\n\t" \
    "buffer_load_dwordx2 v[130:131], %[t2], %[rs], s47 offen sc1\n\t" \
    "buffer_load_dwordx2 v[132:133], %[t3], %[rs], s46 offen sc1\n\t" \
    "buffer_load_dwordx2 v[134:135], %[t3], %[rs], s47 offen sc1\n\t" \
    "buffer_load_dwordx2 v[136:137], %[t4], %[rs], s46 offen sc1\n\t" \
    "buffer_load_dwordx2 v[138:139], %[t4], %[rs], s47 offen sc1\n\t" \
    ".Lf11_%=:\n\t" \
    ".Lf12_%=:\n\t" \
    "s_waitcnt vmcnt(0)\n\t" \
    "v_max_u32 v80, v120, v121\n\t" \
    "v_max3_u32 v80, v122, v123, v80\n\t" \
    "v_max3_u32 v80, v124, v125, v80\n\t" \
    "v_max3_u32 v80, v126, v127, v80\n\t" \
    "v_max3_u32 v80, v128, v129, v80\n\t" \
    "v_max3_u32 v80, v130, v131, v80\n\t" \
    "v_max3_u32 v80, v132, v133, v80\n\t" \
    "v_max3_u32 v80, v134, v135, v80\n\t" \
    "v_max3_u32 v80, v136, v137, v80\n\t" \
    "v_max3_u32 v80, v138, v139, v80\n\t" \
    "v_cmp_eq_u32 vcc, -1, v80\n\t" \
    "s_cbranch_vccz .Lf13_%=\n\t" \
    "s_cmp_eq_u32 s41, 0\n\t" \
    "s_cbranch_scc1 .Lf13_%=\n\t" \
    "s_sub_u32 s41, s41, 1\n\t" \
    "s_sleep 8\n\t" \
    "s_add_u32 s46, s43, 0x2c00000\n\t" \
    "s_add_u32 s47, s43, 0x1c00000\n\t" \
    "buffer_load_dwordx2 v[120:121], %[t0], %[rs], s46 offen sc1\n\t" \
    "buffer_load_dwordx2 v[122:123], %[t0], %[rs], s47 offen sc1\n\t" \
    "buffer_load_dwordx2 v[124:125], %[t1], %[rs], s46 offen sc1\n\t" \
    "buffer_load_dwordx2 v[126:127], %[t1], %[rs], s47 offen sc1\n\t" \
    "buffer_load_dwordx2 v[128:129], %[t2], %[rs], s46 offen sc1\n\t" \
    "buffer_load_dwordx2 v[130:131], %[t2], %[rs], s47 offen sc1\n\t" \
    "buffer_load_dwordx2 v[132:133], %[t3], %[rs], s46 offen sc1\n\t" \
    "buffer_load_dwordx2 v[134:135], %[t3], %[rs], s47 offen sc1\n\t" \
    "buffer_load_dwordx2 v[136:137], %[t4], %[rs], s46 offen sc1\n\t" \
    "buffer_load_dwordx2 v[138:139], %[t4], %[rs], s47 offen sc1\n\t" \
    "s_waitcnt vmcnt(0)\n\t" \
    "s_branch .Lf12_%=\n\t" \
    ".Lf13_%=:\n\t" \
    "v_lshlrev_b32 v140, 16, v120\n\t" \
    "v_and_b32 v141, 0xffff0000, v120\n\t" \
    "v_lshlrev_b32 v142, 16, v121\n\t" \
    "v_and_b32 v143, 0xffff0000, v121\n\t" \
    "v_lshlrev_b32 v80, 16, v122\n\t" \
    "v_and_b32 v81, 0xffff0000, v122\n\t" \
    "v_lshlrev_b32 v82, 16, v123\n\t" \
    "v_and_b32 v83, 0xffff0000, v123\n\t" \
    "v_add_f32 v140, v140, v80\n\t" \
    "v_add_f32 v141, v141, v81\n\t" \
    "v_add_f32 v142, v142, v82\n\t" \
    "v_add_f32 v143, v143, v83\n\t" \
    "v_lshlrev_b32 v144, 16, v124\n\t" \
    "v_and_b32 v145, 0xffff0000, v124\n\t" \
    "v_lshlrev_b32 v146, 16, v125\n\t" \
    "v_and_b32 v147, 0xffff0000, v125\n\t" \
    "v_lshlrev_b32 v80, 16, v126\n\t" \
    "v_and_b32 v81, 0xffff0000, v126\n\t" \
    "v_lshlrev_b32 v82, 16, v127\n\t" \
    "v_and_b32 v83, 0xffff0000, v127\n\t" \
    "v_add_f32 v144, v144, v80\n\t" \
    "v_add_f32 v145, v145, v81\n\t" \
    "v_add_f32 v146, v146, v82\n\t" \
    "v_add_f32 v147, v147, v83\n\t" \
    "v_lshlrev_b32 v148, 16, v128\n\t" \
    "v_and_b32 v149, 0xffff0000, v128\n\t" \
    "v_lshlrev_b32 v150, 16, v129\n\t" \
    "v_and_b32 v151, 0xffff0000, v129\n\t" \
    "v_lshlrev_b32 v80, 16, v130\n\t" \
    "v_and_b32 v81, 0xffff0000, v130\n\t" \
    "v_lshlrev_b32 v82, 16, v131\n\t" \
    "v_and_b32 v83, 0xffff0000, v131\n\t" \
    "v_add_f32 v148, v148, v80\n\t" \
    "v_add_f32 v149, v149, v81\n\t" \
    "v_add_f32 v150, v150, v82\n\t" \
    "v_add_f32 v151, v151, v83\n\t" \
    "v_lshlrev_b32 v152, 16, v132\n\t" \
    "v_and_b32 v153, 0xffff0000, v132\n\t" \
    "v_lshlrev_b32 v154, 16, v133\n\t" \
    "v_and_b32 v155, 0xffff0000, v133\n\t" \
    "v_lshlrev_b32 v80, 16, v134\n\t" \
    "v_and_b32 v81, 0xffff0000, v134\n\t" \
    "v_lshlrev_b32 v82, 16, v135\n\t" \
    "v_and_b32 v83, 0xffff0000, v135\n\t" \
    "v_add_f32 v152, v152, v80\n\t" \
    "v_add_f32 v153, v153, v81\n\t" \
    "v_add_f32 v154, v154, v82\n\t" \
    "v_add_f32 v155, v155, v83\n\t" \
    "v_lshlrev_b32 v156, 16, v136\n\t" \
    "v_and_b32 v157, 0xffff0000, v136\n\t" \
    "v_lshlrev_b32 v158, 16, v137\n\t" \
    "v_and_b32 v159, 0xffff0000, v137\n\t" \
    "v_lshlrev_b32 v80, 16, v138\n\t" \
    "v_and_b32 v81, 0xffff0000, v138\n\t" \
    "v_lshlrev_b32 v82, 16, v139\n\t" \
    "v_and_b32 v83, 0xffff0000, v139\n\t" \
    "v_add_f32 v156, v156, v80\n\t" \
    "v_add_f32 v157, v157, v81\n\t" \
    "v_add_f32 v158, v158, v82\n\t" \
    "v_add_f32 v159, v159, v83\n\t" \
    "s_mov_b32 s42, s43\n\t" \
    "s_lshr_b32 s48, s40, 1\n\t" \
    "s_add_u32 s48, s48, 1\n\t" \
    "s_mul_i32 s43, s48, 0x14000\n\t" \
    "s_lshr_b32 s44, s48, 4\n\t" \
    "s_mul_i32 s44, s44, 0xc2000\n\t" \
    "s_add_u32 s43, s43, s44\n\t" \
    "s_mul_i32 s49, s40, 20480\n\t" \
    "s_add_u32 s49, s49, %[co]\n\t" \
    "s_add_u32 s44, s40, 1\n\t" \
    "v_mov_b32 v98, s44\n\t" \
    ".Lf14_%=:\n\t" \
    "s_nop 7\n\t" \
    "ds_read_b32 v80, %[fa]\n\t" \
    "s_waitcnt lgkmcnt(0)\n\t" \
    "v_cmp_ge_u32 vcc, s40, v80\n\t" \
    "s_cbranch_vccnz .Lf14_%=\n\t" \
    "ds_read_b128 v[160:163], %[la] offset:0\n\t" \
    "ds_read_b128 v[164:167], %[la] offset:6144\n\t" \
    "ds_read_b128 v[168:171], %[la] offset:12288\n\t" \
    "ds_read_b128 v[172:175], %[la] offset:18432\n\t" \
    "ds_read_b128 v[176:179], %[la] offset:24576\n\t" \
    "ds_read_b128 v[180:183], %[la] offset:1024\n\t" \
    "ds_read_b128 v[184:187], %[la] offset:7168\n\t" \
    "ds_read_b128 v[188:191], %[la] offset:13312\n\t" \
    "ds_read_b128 v[192:195], %[la] offset:19456\n\t" \
    "ds_read_b128 v[196:199], %[la] offset:25600\n\t" \
    "ds_read_b128 v[200:203], %[la] offset:2048\n\t" \
    "ds_read_b128 v[204:207], %[la] offset:8192\n\t" \
    "ds_read_b128 v[208:211], %[la] offset:14336\n\t" \
    "ds_read_b128 v[212:215], %[la] offset:20480\n\t" \
    "ds_read_b128 v[216:219], %[la] offset:26624\n\t" \
    "s_waitcnt lgkmcnt(14)\n\t" \
    "v_pk_add_f32 v[100:101], v[140:141], v[160:161]\n\t" \
    "v_pk_add_f32 v[102:103], v[142:143], v[162:163]\n\t" \
    "ds_read_b128 v[220:223], %[la] offset:3072\n\t" \
    "s_waitcnt lgkmcnt(14)\n\t" \
    "v_pk_add_f32 v[104:105], v[144:145], v[164:165]\n\t" \
    "v_pk_add_f32 v[106:107], v[146:147], v[166:167]\n\t" \
    "ds_read_b128 v[224:227], %[la] offset:9216\n\t" \
    "s_waitcnt lgkmcnt(14)\n\t" \
    "v_pk_add_f32 v[108:109], v[148:149], v[168:169]\n\t" \
    "v_pk_add_f32 v[110:111], v[150:151], v[170:171]\n\t" \
    "ds_read_b128 v[228:231], %[la] offset:15360\n\t" \
    "s_waitcnt lgkmcnt(14)\n\t" \
    "v_pk_add_f32 v[112:113], v[152:153], v[172:173]\n\t" \
    "v_pk_add_f32 v[114:115], v[154:155], v[174:175]\n\t" \
    "ds_read_b128 v[232:235], %[la] offset:21504\n\t" \
    "s_waitcnt lgkmcnt(14)\n\t" \
    "v_pk_add_f32 v[116:117], v[156:157], v[176:177]\n\t" \
    "v_pk_add_f32 v[118:119], v[158:159], v[178:179]\n\t" \
    "ds_read_b128 v[236:239], %[la] offset:27648\n\t" \
    "s_waitcnt lgkmcnt(14)\n\t" \
    "v_pk_add_f32 v[100:101], v[100:101], v[180:181]\n\t" \
    "v_pk_add_f32 v[102:103], v[102:103], v[182:183]\n\t" \
    "ds_read_b128 v[160:163], %[la] offset:4096\n\t" \
    "s_waitcnt lgkmcnt(14)\n\t" \
    "v_pk_add_f32 v[104:105], v[104:105], v[184:185]\n\t" \
    "v_pk_add_f32 v[106:107], v[106:107], v[186:187]\n\t" \
    "ds_read_b128 v[164:167], %[la] offset:10240\n\t" \
    "s_waitcnt lgkmcnt(14)\n\t" \
    "v_pk_add_f32 v[108:109], v[108:109], v[188:189]\n\t" \
    "v_pk_add_f32 v[110:111], v[110:111], v[190:191]\n\t" \
    "ds_read_b128 v[168:171], %[la] offset:16384\n\t" \
    "s_waitcnt lgkmcnt(14)\n\t" \
    "v_pk_add_f32 v[112:113], v[112:113], v[192:193]\n\t" \
    "v_pk_add_f32 v[114:115], v[114:115], v[194:195]\n\t" \
    "ds_read_b128 v[172:175], %[la] offset:22528\n\t" \
    "s_waitcnt lgkmcnt(14)\n\t" \
    "v_pk_add_f32 v[116:117], v[116:117], v[196:197]\n\t" \
    "v_pk_add_f32 v[118:119], v[118:119], v[198:199]\n\t" \
    "ds_read_b128 v[176:179], %[la] offset:28672\n\t" \
    "s_waitcnt lgkmcnt(14)\n\t" \
    "v_pk_add_f32 v[100:101], v[100:101], v[200:201]\n\t" \
    "v_pk_add_f32 v[102:103], v[102:103], v[202:203]\n\t" \
    "ds_read_b128 v[180:183], %[la] offset:5120\n\t" \
    "s_waitcnt lgkmcnt(14)\n\t" \
    "v_pk_add_f32 v[104:105], v[104:105], v[204:205]\n\t" \
    "v_pk_add_f32 v[106:107], v[106:107], v[206:207]\n\t" \
    "ds_read_b128 v[184:187], %[la] offset:11264\n\t" \
    "s_waitcnt lgkmcnt(14)\n\t" \
    "v_pk_add_f32 v[108:109], v[108:109], v[208:209]\n\t" \
    "v_pk_add_f32 v[110:111], v[110:111], v[210:211]\n\t" \
    "ds_read_b128 v[188:191], %[la] offset:17408\n\t" \
    "s_waitcnt lgkmcnt(14)\n\t" \
    "v_pk_add_f32 v[112:113], v[112:113], v[212:213]\n\t" \
    "v_pk_add_f32 v[114:115], v[114:115], v[214:215]\n\t" \
    "ds_read_b128 v[192:195], %[la] offset:23552\n\t" \
    "s_waitcnt lgkmcnt(14)\n\t" \
    "v_pk_add_f32 v[116:117], v[116:117], v[216:217]\n\t" \
    "v_pk_add_f32 v[118:119], v[118:119], v[218:219]\n\t" \
    "ds_read_b128 v[196:199], %[la] offset:29696\n\t" \
    "s_waitcnt lgkmcnt(14)\n\t" \
    "v_pk_add_f32 v[100:101], v[100:101], v[220:221]\n\t" \
    "v_pk_add_f32 v[102:103], v[102:103], v[222:223]\n\t" \
    "s_waitcnt lgkmcnt(13)\n\t" \
    "v_pk_add_f32 v[104:105], v[104:105], v[224:225]\n\t" \
    "v_pk_add_f32 v[106:107], v[106:107], v[226:227]\n\t" \
    "s_waitcnt lgkmcnt(12)\n\t" \
    "v_pk_add_f32 v[108:109], v[108:109], v[228:229]\n\t" \
    "v_pk_add_f32 v[110:111], v[110:111], v[230:231]\n\t" \
    "s_waitcnt lgkmcnt(11)\n\t" \
    "v_pk_add_f32 v[112:113], v[112:113], v[232:233]\n\t" \
    "v_pk_add_f32 v[114:115], v[114:115], v[234:235]\n\t" \
    "s_waitcnt lgkmcnt(10)\n\t" \
    "v_pk_add_f32 v[116:117], v[116:117], v[236:237]\n\t" \
    "v_pk_add_f32 v[118:119], v[118:119], v[238:239]\n\t" \
    "s_waitcnt lgkmcnt(9)\n\t" \
    "v_pk_add_f32 v[100:101], v[100:101], v[160:161]\n\t" \
    "v_pk_add_f32 v[102:103], v[102:103], v[162:163]\n\t" \
    "s_waitcnt lgkmcnt(8)\n\t" \
    "v_pk_add_f32 v[104:105], v[104:105], v[164:165]\n\t" \
    "v_pk_add_f32 v[106:107], v[106:107], v[166:167]\n\t" \
    "s_waitcnt lgkmcnt(7)\n\t" \
    "v_pk_add_f32 v[108:109], v[108:109], v[168:169]\n\t" \
    "v_pk_add_f32 v[110:111], v[110:111], v[170:171]\n\t" \
    "s_waitcnt lgkmcnt(6)\n\t" \
    "v_pk_add_f32 v[112:113], v[112:113], v[172:173]\n\t" \
    "v_pk_add_f32 v[114:115], v[114:115], v[174:175]\n\t" \
    "s_waitcnt lgkmcnt(5)\n\t" \
    "v_pk_add_f32 v[116:117], v[116:117], v[176:177]\n\t" \
    "v_pk_add_f32 v[118:119], v[118:119], v[178:179]\n\t" \
    "s_waitcnt lgkmcnt(4)\n\t" \
    "v_pk_add_f32 v[100:101], v[100:101], v[180:181]\n\t" \
    "v_pk_add_f32 v[102:103], v[102:103], v[182:183]\n\t" \
    "s_waitcnt lgkmcnt(3)\n\t" \
    "v_pk_add_f32 v[104:105], v[104:105], v[184:185]\n\t" \
    "v_pk_add_f32 v[106:107], v[106:107], v[186:187]\n\t" \
    "s_waitcnt lgkmcnt(2)\n\t" \
    "v_pk_add_f32 v[108:109], v[108:109], v[188:189]\n\t" \
    "v_pk_add_f32 v[110:111], v[110:111], v[190:191]\n\t" \
    "s_waitcnt lgkmcnt(1)\n\t" \
    "v_pk_add_f32 v[112:113], v[112:113], v[192:193]\n\t" \
    "v_pk_add_f32 v[114:115], v[114:115], v[194:195]\n\t" \
    "s_waitcnt lgkmcnt(0)\n\t" \
    "v_pk_add_f32 v[116:117], v[116:117], v[196:197]\n\t" \
    "v_pk_add_f32 v[118:119], v[118:119], v[198:199]\n\t" \
    "ds_write_b32 %[fc], v98\n\t" \
    "v_sub_u32 %[la], %[l2], %[la]\n\t" \
    "v_cvt_pk_bf16_f32 v84, v100, v101\n\t" \
    "v_cvt_pk_bf16_f32 v85, v102, v103\n\t" \
    "v_cvt_pk_bf16_f32 v86, v104, v105\n\t" \
    "v_cvt_pk_bf16_f32 v87, v106, v107\n\t" \
    "v_cvt_pk_bf16_f32 v88, v108, v109\n\t" \
    "v_cvt_pk_bf16_f32 v89, v110, v111\n\t" \
    "v_cvt_pk_bf16_f32 v90, v112, v113\n\t" \
    "v_cvt_pk_bf16_f32 v91, v114, v115\n\t" \
    "v_cvt_pk_bf16_f32 v92, v116, v117\n\t" \
    "v_cvt_pk_bf16_f32 v93, v118, v119\n\t" \
    "s_nop 0\n\t" \
    "v_permlane16_swap_b32 v84, v86\n\t" \
    "v_permlane16_swap_b32 v85, v87\n\t" \
    "v_permlane16_swap_b32 v88, v90\n\t" \
    "v_permlane16_swap_b32 v89, v91\n\t" \
    "buffer_store_dwordx4 v[84:87], %[q16], %[ro], s49 offen\n\t" \
    "buffer_store_dwordx4 v[88:91], %[q16], %[ro], s49 offen offset:64\n\t" \
    "buffer_store_dwordx2 v[92:93], %[q8], %[ro], s49 offen offset:128\n\t" \
    "s_add_u32 s46, s43, 0x2c00000\n\t" \
    "s_add_u32 s47, s43, 0x1c00000\n\t" \
    "buffer_load_dwordx2 v[120:121], %[t0], %[rs], s46 offen sc1\n\t" \
    "buffer_load_dwordx2 v[122:123], %[t0], %[rs], s47 offen sc1\n\t" \
    "buffer_load_dwordx2 v[124:125], %[t1], %[rs], s46 offen sc1\n\t" \
    "buffer_load_dwordx2 v[126:127], %[t1], %[rs], s47 offen sc1\n\t" \
    "buffer_load_dwordx2 v[128:129], %[t2], %[rs], s46 offen sc1\n\t" \
    "buffer_load_dwordx2 v[130:131], %[t2], %[rs], s47 offen sc1\n\t" \
    "buffer_load_dwordx2 v[132:133], %[t3], %[rs], s46 offen sc1\n\t" \
    "buffer_load_dwordx2 v[134:135], %[t3], %[rs], s47 offen sc1\n\t" \
    "buffer_load_dwordx2 v[136:137], %[t4], %[rs], s46 offen sc1\n\t" \
    "buffer_load_dwordx2 v[138:139], %[t4], %[rs], s47 offen sc1\n\t" \
    "s_add_u32 s40, s40, 2\n\t" \
    "s_cmp_lt_u32 s40, 128\n\t" \
    "s_cbranch_scc1 .Lf11_%=\n\t" \
    "s_branch .Lend%=\n\t" \
    ".Lend%=:\n\t" \
    "s_waitcnt vmcnt(0) lgkmcnt(0)\n\t" \
    "s_mov_b32 %[bud], s41\n\t"
#define ASM_W1_LOOP \
    "s_mov_b32 s40, 0\n\t" \
    "s_mov_b32 s41, 0x20000\n\t" \
    "s_add_u32 m0, %[wm], 0x0\n\t" \
    "s_add_u32 s44, %[wo], 0x0\n\t" \
    "buffer_load_dwordx4 v176, %[wr], s44 offen nt lds\n\t" \
    "s_add_u32 m0, %[wm], 0x2000\n\t" \
    "s_add_u32 s44, %[wo], 0xa000\n\t" \
    "buffer_load_dwordx4 v176, %[wr], s44 offen nt lds\n\t" \
    "s_add_u32 m0, %[wm], 0x4000\n\t" \
    "s_add_u32 s44, %[wo], 0x14000\n\t" \
    "buffer_load_dwordx4 v176, %[wr], s44 offen nt lds\n\t" \
    "s_add_u32 m0, %[wm], 0x6000\n\t" \
    "s_add_u32 s44, %[wo], 0x1e000\n\t" \
    "buffer_load_dwordx4 v176, %[wr], s44 offen nt lds\n\t" \
    "s_add_u32 m0, %[wm], 0x8000\n\t" \
    "s_add_u32 s44, %[wo], 0x28000\n\t" \
    "buffer_load_dwordx4 v176, %[wr], s44 offen nt lds\n\t" \
    "s_add_u32 m0, %[wm], 0xa000\n\t" \
    "s_add_u32 s44, %[wo], 0x32000\n\t" \
    "buffer_load_dwordx4 v176, %[wr], s44 offen nt lds\n\t" \
    "s_add_u32 m0, %[wm], 0xc000\n\t" \
    "s_add_u32 s44, %[wo], 0x3c000\n\t" \
    "buffer_load_dwordx4 v176, %[wr], s44 offen nt lds\n\t" \
    "s_add_u32 m0, %[wm], 0xe000\n\t" \
    "s_add_u32 s44, %[wo], 0x46000\n\t" \
    "buffer_load_dwordx4 v176, %[wr], s44 offen nt lds\n\t" \
    "s_add_u32 m0, %[wm], 0x10000\n\t" \
    "s_add_u32 s44, %[wo], 0x50000\n\t" \
    "buffer_load_dwordx4 v176, %[wr], s44 offen nt lds\n\t" \
    "s_add_u32 m0, %[wm], 0x12000\n\t" \
    "s_add_u32 s44, %[wo], 0x5a000\n\t" \
    "buffer_load_dwordx4 v176, %[wr], s44 offen nt lds\n\t" \
    "s_add_u32 m0, %[wm], 0x14000\n\t" \
    "s_add_u32 s44, %[wo], 0x64000\n\t" \
    "buffer_load_dwordx4 v176, %[wr], s44 offen nt lds\n\t" \
    "s_add_u32 m0, %[wm], 0x16000\n\t" \
    "s_add_u32 s44, %[wo], 0x6e000\n\t" \
    "buffer_load_dwordx4 v176, %[wr], s44 offen nt lds\n\t" \
    "s_add_u32 m0, %[wm], 0x18000\n\t" \
    "s_add_u32 s44, %[wo], 0x78000\n\t" \
    "buffer_load_dwordx4 v176, %[wr], s44 offen nt lds\n\t" \
    "s_add_u32 m0, %[wm], 0x1a000\n\t" \
    "s_add_u32 s44, %[wo], 0x82000\n\t" \
    "buffer_load_dwordx4 v176, %[wr], s44 offen nt lds\n\t" \
    "s_add_u32 m0, %[wm], 0x1c000\n\t" \
    "s_add_u32 s44, %[wo], 0x8c000\n\t" \
    "buffer_load_dwordx4 v176, %[wr], s44 offen nt lds\n\t" \
    "s_add_u32 m0, %[wm], 0x1e000\n\t" \
    "s_add_u32 s44, %[wo], 0x96000\n\t" \
    "buffer_load_dwordx4 v176, %[wr], s44 offen nt lds\n\t" \
    "s_add_u32 m0, %[wm], 0x20000\n\t" \
    "s_add_u32 s44, %[wo], 0xa0000\n\t" \
    "buffer_load_dwordx4 v176, %[wr], s44 offen nt lds\n\t" \
    "s_add_u32 m0, %[wm], 0x22000\n\t" \
    "s_add_u32 s44, %[wo], 0xaa000\n\t" \
    "buffer_load_dwordx4 v176, %[wr], s44 offen nt lds\n\t" \
    "s_add_u32 m0, %[wm], 0x24000\n\t" \
    "s_add_u32 s44, %[wo], 0xb4000\n\t" \
    "buffer_load_dwordx4 v176, %[wr], s44 offen nt lds\n\t" \
    "s_add_u32 m0, %[wm], 0x26000\n\t" \
    "s_add_u32 s44, %[wo], 0xbe000\n\t" \
    "buffer_load_dwordx4 v176, %[wr], s44 offen nt lds\n\t" \
    "s_waitcnt vmcnt(0) lgkmcnt(0)\n\t" \
    "s_barrier \n\t" \
    "v_add_u32 v178, 0x0, v177\n\t" \
    "ds_read_b64_tr_b16 v[0:1], v178 offset:0\n\t" \
    "ds_read_b64_tr_b16 v[2:3], v178 offset:1024\n\t" \
    "ds_read_b64_tr_b16 v[4:5], v178 offset:8192\n\t" \
    "ds_read_b64_tr_b16 v[6:7], v178 offset:9216\n\t" \
    "ds_read_b64_tr_b16 v[8:9], v178 offset:16384\n\t" \
    "ds_read_b64_tr_b16 v[10:11], v178 offset:17408\n\t" \
    "ds_read_b64_tr_b16 v[12:13], v178 offset:24576\n\t" \
    "ds_read_b64_tr_b16 v[14:15], v178 offset:25600\n\t" \
    "v_add_u32 v178, 0x8000, v177\n\t" \
    "ds_read_b64_tr_b16 v[16:17], v178 offset:0\n\t" \
    "ds_read_b64_tr_b16 v[18:19], v178 offset:1024\n\t" \
    "ds_read_b64_tr_b16 v[20:21], v178 offset:8192\n\t" \
    "ds_read_b64_tr_b16 v[22:23], v178 offset:9216\n\t" \
    "ds_read_b64_tr_b16 v[24:25], v178 offset:16384\n\t" \
    "ds_read_b64_tr_b16 v[26:27], v178 offset:17408\n\t" \
    "ds_read_b64_tr_b16 v[28:29], v178 offset:24576\n\t" \
    "ds_read_b64_tr_b16 v[30:31], v178 offset:25600\n\t" \
    "v_add_u32 v178, 0x10000, v177\n\t" \
    "ds_read_b64_tr_b16 v[32:33], v178 offset:0\n\t" \
    "ds_read_b64_tr_b16 v[34:35], v178 offset:1024\n\t" \
    "ds_read_b64_tr_b16 v[36:37], v178 offset:8192\n\t" \
    "ds_read_b64_tr_b16 v[38:39], v178 offset:9216\n\t" \
    "ds_read_b64_tr_b16 v[40:41], v178 offset:16384\n\t" \
    "ds_read_b64_tr_b16 v[42:43], v178 offset:17408\n\t" \
    "ds_read_b64_tr_b16 v[44:45], v178 offset:24576\n\t" \
    "ds_read_b64_tr_b16 v[46:47], v178 offset:25600\n\t" \
    "v_add_u32 v178, 0x18000, v177\n\t" \
    "ds_read_b64_tr_b16 v[48:49], v178 offset:0\n\t" \
    "ds_read_b64_tr_b16 v[50:51], v178 offset:1024\n\t" \
    "ds_read_b64_tr_b16 v[52:53], v178 offset:8192\n\t" \
    "ds_read_b64_tr_b16 v[54:55], v178 offset:9216\n\t" \
    "ds_read_b64_tr_b16 v[56:57], v178 offset:16384\n\t" \
    "ds_read_b64_tr_b16 v[58:59], v178 offset:17408\n\t" \
    "ds_read_b64_tr_b16 v[60:61], v178 offset:24576\n\t" \
    "ds_read_b64_tr_b16 v[62:63], v178 offset:25600\n\t" \
    "v_add_u32 v178, 0x20000, v177\n\t" \
    "ds_read_b64_tr_b16 v[64:65], v178 offset:0\n\t" \
    "ds_read_b64_tr_b16 v[66:67], v178 offset:1024\n\t" \
    "ds_read_b64_tr_b16 v[68:69], v178 offset:8192\n\t" \
    "ds_read_b64_tr_b16 v[70:71], v178 offset:9216\n\t" \
    "ds_read_b64_tr_b16 v[72:73], v178 offset:16384\n\t" \
    "ds_read_b64_tr_b16 v[74:75], v178 offset:17408\n\t" \
    "ds_read_b64_tr_b16 v[76:77], v178 offset:24576\n\t" \
    "ds_read_b64_tr_b16 v[78:79], v178 offset:25600\n\t" \
    "v_mov_b32 v228, 0x3f803f80\n\t" \
    "v_mov_b32 v229, 0x3f803f80\n\t" \
    "v_mov_b32 v230, 0x3f803f80\n\t" \
    "v_mov_b32 v231, 0x3f803f80\n\t" \
    "v_mov_b64 v[216:217], 0\n\t" \
    "v_mov_b32 v219, 0\n\t" \
    "v_bfe_u32 v83, v253, 2, 5\n\t" \
    "v_add_u32 v83, 0x7000, v83\n\t" \
    "v_mov_b32 v218, 0\n\t" \
    "s_waitcnt lgkmcnt(0)\n\t" \
    "s_barrier \n\t" \
    "ds_write_b32 v251, v228 offset:72\n\t" \
    "ds_write_b32 v251, v228 offset:2120\n\t" \
    "v_mfma_f32_16x16x32_bf16 v[160:163], v[0:3], v[228:231], 0\n\t" \
    "v_mfma_f32_16x16x32_bf16 v[160:163], v[4:7], v[228:231], v[160:163]\n\t" \
    "v_mfma_f32_16x16x32_bf16 v[160:163], v[8:11], v[228:231], v[160:163]\n\t" \
    "v_mfma_f32_16x16x32_bf16 v[160:163], v[12:15], v[228:231], v[160:163]\n\t" \
    "v_mfma_f32_16x16x32_bf16 v[160:163], v[16:19], v[228:231], v[160:163]\n\t" \
    "v_mfma_f32_16x16x32_bf16 v[160:163], v[20:23], v[228:231], v[160:163]\n\t" \
    "v_mfma_f32_16x16x32_bf16 v[160:163], v[24:27], v[228:231], v[160:163]\n\t" \
    "v_mfma_f32_16x16x32_bf16 v[160:163], v[28:31], v[228:231], v[160:163]\n\t" \
    "v_mfma_f32_16x16x32_bf16 v[160:163], v[32:35], v[228:231], v[160:163]\n\t" \
    "v_mfma_f32_16x16x32_bf16 v[160:163], v[36:39], v[228:231], v[160:163]\n\t" \
    "v_mfma_f32_16x16x32_bf16 v[160:163], v[40:43], v[228:231], v[160:163]\n\t" \
    "v_mfma_f32_16x16x32_bf16 v[160:163], v[44:47], v[228:231], v[160:163]\n\t" \
    "v_mfma_f32_16x16x32_bf16 v[160:163], v[48:51], v[228:231], v[160:163]\n\t" \
    "v_mfma_f32_16x16x32_bf16 v[160:163], v[52:55], v[228:231], v[160:163]\n\t" \
    "v_mfma_f32_16x16x32_bf16 v[160:163], v[56:59], v[228:231], v[160:163]\n\t" \
    "v_mfma_f32_16x16x32_bf16 v[160:163], v[60:63], v[228:231], v[160:163]\n\t" \
    "v_mfma_f32_16x16x32_bf16 v[160:163], v[64:67], v[228:231], v[160:163]\n\t" \
    "v_mfma_f32_16x16x32_bf16 v[160:163], v[68:71], v[228:231], v[160:163]\n\t" \
    "v_mfma_f32_16x16x32_bf16 v[160:163], v[72:75], v[228:231], v[160:163]\n\t" \
    "v_mfma_f32_16x16x32_bf16 v[160:163], v[76:79], v[228:231], v[160:163]\n\t" \
    "s_mov_b32 s52, 0x22222222\n\t" \
    "s_mov_b32 s53, 0x22222222\n\t" \
    "s_mov_b32 s54, 0x44444444\n\t" \
    "s_mov_b32 s55, 0x44444444\n\t" \
    "s_mov_b32 s56, 0x88888888\n\t" \
    "s_mov_b32 s57, 0x88888888\n\t" \
    "ds_write_b32 v83, v218\n\t" \
    "s_waitcnt lgkmcnt(0)\n\t" \
    "s_barrier \n\t" \
    ".Lw11_%=:\n\t" \
    "v_mov_b32 v248, 0\n\t" \
    "buffer_load_dword v248, v248, %[rs], %[go] offen sc1\n\t" \
    "s_waitcnt vmcnt(0)\n\t" \
    "v_cmp_ne_u32 vcc, 0, v248\n\t" \
    "s_cbranch_vccnz .Lw12_%=\n\t" \
    "s_cmp_eq_u32 s41, 0\n\t" \
    "s_cbranch_scc1 .Lw12_%=\n\t" \
    "s_sub_u32 s41, s41, 1\n\t" \
    "s_sleep 16\n\t" \
    "s_branch .Lw11_%=\n\t" \
    ".Lw12_%=:\n\t" \
    "s_sub_i32 s60, 0, %[ng]\n\t" \
    "s_sub_i32 s61, 1, %[ng]\n\t" \
    "s_add_i32 s62, s60, 6\n\t" \
    "s_add_i32 s63, s61, 6\n\t" \
    "s_mul_i32 s42, s61, 127\n\t" \
    "s_mul_i32 s44, %[ng], 0x733000\n\t" \
    "s_add_u32 s44, s44, 0x2400000\n\t" \
    "v_cmp_ne_u32 s[58:59], %[ng], v255\n\t" \
    "s_nop 1\n\t" \
    "s_mov_b64 exec, s[58:59]\n\t" \
    "buffer_store_dwordx2 v[216:217], v254, %[rs], s44 offen sc1\n\t" \
    "s_mov_b64 exec, -1\n\t" \
    "s_lshr_b32 s48, %[wave], 2\n\t" \
    "s_mul_i32 s48, s48, 0x3000\n\t" \
    "s_add_u32 s48, s48, %[pi]\n\t" \
    "s_mov_b32 s43, 0\n\t" \
    "s_add_u32 s46, s43, s48\n\t" \
    "s_add_u32 s47, s46, 0x0\n\t" \
    "buffer_load_dwordx4 v[208:211], v253, %[rs], s47 offen sc1\n\t" \
    "s_add_u32 s47, s46, 0x8000\n\t" \
    "buffer_load_dwordx4 v[212:215], v253, %[rs], s47 offen sc1\n\t" \
    "s_cmp_gt_u32 %[wave], 3\n\t" \
    "s_cbranch_scc1 .Lw13_%=\n\t" \
    "s_add_u32 s47, s46, 0x10000\n\t" \
    "buffer_load_dwordx4 v[216:219], v253, %[rs], s47 offen sc1\n\t" \
    ".Lw13_%=:\n\t" \
    "s_waitcnt vmcnt(0)\n\t" \
    "v_lshrrev_b32 v82, 1, v253\n\t" \
    ".Lw14_%=:\n\t" \
    "s_add_i32 s44, s40, s60\n\t" \
    "s_max_i32 s44, s44, 0\n\t" \
    "s_lshr_b32 s45, s44, 1\n\t" \
    "s_and_b32 s44, s44, 1\n\t" \
    "s_lshl_b32 s44, s44, 12\n\t" \
    "s_mul_i32 s49, s45, 0x14000\n\t" \
    "s_add_u32 s49, s49, s44\n\t" \
    "s_lshr_b32 s45, s45, 4\n\t" \
    "s_mul_i32 s45, s45, 0xc2000\n\t" \
    "s_add_u32 s49, s49, s45\n\t" \
    "s_add_i32 s44, s40, s61\n\t" \
    "s_max_i32 s44, s44, 0\n\t" \
    "s_lshr_b32 s45, s44, 1\n\t" \
    "s_and_b32 s44, s44, 1\n\t" \
    "s_lshl_b32 s44, s44, 12\n\t" \
    "s_mul_i32 s50, s45, 0x14000\n\t" \
    "s_add_u32 s50, s50, s44\n\t" \
    "s_lshr_b32 s45, s45, 4\n\t" \
    "s_mul_i32 s45, s45, 0xc2000\n\t" \
    "s_add_u32 s50, s50, s45\n\t" \
    "s_sub_u32 s50, s50, s49\n\t" \
    "s_add_u32 s49, s49, 0x2400000\n\t" \
    "v_mad_u32_u24 v80, v255, s50, v254\n\t" \
    "s_lshr_b32 s44, s40, 6\n\t" \
    "s_lshl_b32 s44, s44, 14\n\t" \
    "s_and_b32 s45, s40, 63\n\t" \
    "s_lshl_b32 s45, s45, 7\n\t" \
    "s_add_u32 s44, s44, s45\n\t" \
    "s_add_u32 s50, s44, 0x4d0000\n\t" \
    "s_add_i32 s44, s40, 1\n\t" \
    "s_max_i32 s44, s44, 0\n\t" \
    "s_lshr_b32 s45, s44, 1\n\t" \
    "s_and_b32 s44, s44, 1\n\t" \
    "s_lshl_b32 s44, s44, 12\n\t" \
    "s_mul_i32 s43, s45, 0x14000\n\t" \
    "s_add_u32 s43, s43, s44\n\t" \
    "s_lshr_b32 s45, s45, 4\n\t" \
    "s_mul_i32 s45, s45, 0xc2000\n\t" \
    "s_add_u32 s43, s43, s45\n\t" \
    "s_setprio 2\n\t" \
    "v_max_u32 v248, v208, v209\n\t" \
    "v_max3_u32 v248, v210, v211, v248\n\t" \
    "v_max3_u32 v248, v212, v213, v248\n\t" \
    "v_max3_u32 v248, v214, v215, v248\n\t" \
    "v_max3_u32 v248, v216, v217, v248\n\t" \
    "v_max3_u32 v248, v218, v219, v248\n\t" \
    "v_cmp_eq_u32 vcc, -1, v248\n\t" \
    "s_cbranch_vccz .Lw16_%=\n\t" \
    "s_and_b32 s44, s40, 1\n\t" \
    "s_cmp_eq_u32 s44, 1\n\t" \
    "s_cbranch_scc1 .Lw15_%=\n\t" \
    "s_sleep 10\n\t" \
    ".Lw15_%=:\n\t" \
    "v_max_u32 v248, v208, v209\n\t" \
    "v_max3_u32 v248, v210, v211, v248\n\t" \
    "v_cmp_eq_u32 vcc, -1, v248\n\t" \
    "s_cbranch_vccz .Lw18_%=\n\t" \
    "s_add_u32 s47, s46, 0x0\n\t" \
    "buffer_load_dwordx4 v[208:211], v253, %[rs], s47 offen sc1\n\t" \
    ".Lw18_%=:\n\t" \
    "v_max_u32 v248, v212, v213\n\t" \
    "v_max3_u32 v248, v214, v215, v248\n\t" \
    "v_cmp_eq_u32 vcc, -1, v248\n\t" \
    "s_cbranch_vccz .Lw19_%=\n\t" \
    "s_add_u32 s47, s46, 0x8000\n\t" \
    "buffer_load_dwordx4 v[212:215], v253, %[rs], s47 offen sc1\n\t" \
    ".Lw19_%=:\n\t" \
    "v_max_u32 v248, v216, v217\n\t" \
    "v_max3_u32 v248, v218, v219, v248\n\t" \
    "v_cmp_eq_u32 vcc, -1, v248\n\t" \
    "s_cbranch_vccz .Lw110_%=\n\t" \
    "s_add_u32 s47, s46, 0x10000\n\t" \
    "buffer_load_dwordx4 v[216:219], v253, %[rs], s47 offen sc1\n\t" \
    ".Lw110_%=:\n\t" \
    "s_waitcnt vmcnt(0)\n\t" \
    "v_max_u32 v248, v208, v209\n\t" \
    "v_max3_u32 v248, v210, v211, v248\n\t" \
    "v_max3_u32 v248, v212, v213, v248\n\t" \
    "v_max3_u32 v248, v214, v215, v248\n\t" \
    "v_max3_u32 v248, v216, v217, v248\n\t" \
    "v_max3_u32 v248, v218, v219, v248\n\t" \
    "v_cmp_eq_u32 vcc, -1, v248\n\t" \
    "s_cbranch_vccz .Lw16_%=\n\t" \
    "s_cmp_eq_u32 s41, 0\n\t" \
    "s_cbranch_scc1 .Lw16_%=\n\t" \
    "s_sub_u32 s41, s41, 1\n\t" \
    "s_branch .Lw15_%=\n\t" \
    ".Lw16_%=:\n\t" \
    "ds_write_b128 v250, v[208:211] offset:0\n\t" \
    "v_mfma_f32_16x16x32_bf16 v[236:239], v[208:211], v[208:211], 0\n\t" \
    "v_mfma_f32_16x16x32_bf16 v[232:235], v[228:231], v[208:211], 0\n\t" \
    "ds_write_b128 v250, v[212:215] offset:8192\n\t" \
    "v_mfma_f32_16x16x32_bf16 v[236:239], v[212:215], v[212:215], v[236:239]\n\t" \
    "v_mfma_f32_16x16x32_bf16 v[232:235], v[228:231], v[212:215], v[232:235]\n\t" \
    "s_cmp_gt_u32 %[wave], 3\n\t" \
    "s_cbranch_scc1 .Lw111_%=\n\t" \
    "ds_write_b128 v250, v[216:219] offset:16384\n\t" \
    ".Lw111_%=:\n\t" \
    "v_mfma_f32_16x16x32_bf16 v[236:239], v[216:219], v[216:219], v[236:239]\n\t" \
    "v_mfma_f32_16x16x32_bf16 v[232:235], v[228:231], v[216:219], v[232:235]\n\t" \
    "s_add_u32 s46, s43, s48\n\t" \
    "s_nop 4\n\t" \
    "v_cndmask_b32 v236, v236, v237, s[52:53]\n\t" \
    "v_cndmask_b32 v236, v236, v238, s[54:55]\n\t" \
    "v_cndmask_b32 v236, v236, v239, s[56:57]\n\t" \
    "s_mov_b32 exec_lo, 0xf0000f\n\t" \
    "s_mov_b32 exec_hi, 0xf0000f00\n\t" \
    "ds_write2_b32 v252, v232, v236 offset1:1\n\t" \
    "s_mov_b64 exec, -1\n\t" \
    "s_waitcnt lgkmcnt(0)\n\t" \
    "s_barrier \n\t" \
    "s_setprio 0\n\t" \
    "s_cmp_lg_u32 %[wave], 4\n\t" \
    "s_cbranch_scc1 .Lw112_%=\n\t" \
    "s_setprio 1\n\t" \
    "ds_read_b128 v[232:235], v251 offset:0\n\t" \
    "ds_read_b128 v[236:239], v251 offset:16\n\t" \
    "ds_read_b128 v[240:243], v251 offset:32\n\t" \
    "ds_read_b128 v[244:247], v251 offset:48\n\t" \
    "s_waitcnt lgkmcnt(0)\n\t" \
    "v_add_f32 v232, v232, v240\n\t" \
    "v_add_f32 v233, v233, v241\n\t" \
    "v_add_f32 v234, v234, v242\n\t" \
    "v_add_f32 v235, v235, v243\n\t" \
    "v_add_f32 v236, v236, v244\n\t" \
    "v_add_f32 v237, v237, v245\n\t" \
    "v_add_f32 v238, v238, v246\n\t" \
    "v_add_f32 v239, v239, v247\n\t" \
    "v_add_f32 v232, v232, v236\n\t" \
    "v_add_f32 v233, v233, v237\n\t" \
    "v_add_f32 v234, v234, v238\n\t" \
    "v_add_f32 v235, v235, v239\n\t" \
    "v_add_f32 v232, v232, v234\n\t" \
    "v_add_f32 v233, v233, v235\n\t" \
    "v_mul_f32 v232, 0x3acccccd, v232\n\t" \
    "v_mul_f32 v233, 0x3acccccd, v233\n\t" \
    "v_fma_f32 v233, -v232, v232, v233\n\t" \
    "v_mov_b32 v234, v232\n\t" \
    "v_max_f32 v233, 0, v233\n\t" \
    "v_cvt_pk_bf16_f32 v234, v234, v234\n\t" \
    "v_and_b32 v234, 0xffff0000, v234\n\t" \
    "v_mov_b32 v236, v233\n\t" \
    "v_cvt_pk_bf16_f32 v236, v236, v236\n\t" \
    "v_and_b32 v236, 0xffff0000, v236\n\t" \
    "v_add_f32 v236, 0x3727c5ac, v236\n\t" \
    "v_cvt_pk_bf16_f32 v236, v236, v236\n\t" \
    "v_and_b32 v236, 0xffff0000, v236\n\t" \
    "v_sqrt_f32 v236, v236\n\t" \
    "s_nop 0\n\t" \
    "v_cvt_pk_bf16_f32 v236, v236, v236\n\t" \
    "v_and_b32 v236, 0xffff0000, v236\n\t" \
    "v_rcp_f32 v235, v236\n\t" \
    "s_nop 0\n\t" \
    "v_sub_f32 v232, v232, v234\n\t" \
    "v_mul_f32 v233, v233, v235\n\t" \
    "v_mul_f32 v232, v232, v235\n\t" \
    "v_mul_f32 v233, v233, v235\n\t" \
    "v_cvt_pk_bf16_f32 v232, v232, v232\n\t" \
    "v_and_b32 v232, 0xffff0000, v232\n\t" \
    "v_cvt_pk_bf16_f32 v233, v233, v233\n\t" \
    "v_and_b32 v233, 0xffff0000, v233\n\t" \
    "v_add_f32 v233, 0x3727c5ac, v233\n\t" \
    "v_cvt_pk_bf16_f32 v233, v233, v233\n\t" \
    "v_and_b32 v233, 0xffff0000, v233\n\t" \
    "v_sqrt_f32 v233, v233\n\t" \
    "s_nop 0\n\t" \
    "v_cvt_pk_bf16_f32 v233, v233, v233\n\t" \
    "v_and_b32 v233, 0xffff0000, v233\n\t" \
    "v_rcp_f32 v233, v233\n\t" \
    "v_fma_f32 v232, v232, v236, v234\n\t" \
    "v_mul_f32 v237, v235, v233\n\t" \
    "v_mul_f32 v238, -v232, v237\n\t" \
    "v_min_u32 v234, -2, v234\n\t" \
    "v_min_u32 v235, -2, v235\n\t" \
    "v_mov_b32 v239, s40\n\t" \
    "ds_write2_b32 v251, v237, v238 offset0:16 offset1:17\n\t" \
    "ds_write_b32 v251, v239 offset:72\n\t" \
    "s_setprio 0\n\t" \
    "s_cmp_eq_u32 %[ss], 0\n\t" \
    "s_cbranch_scc1 .Lw113_%=\n\t" \
    "s_mov_b64 exec, 0xffff\n\t" \
    "buffer_store_dwordx2 v[234:235], v82, %[rs], s50 offen sc1\n\t" \
    "s_mov_b64 exec, -1\n\t" \
    ".Lw113_%=:\n\t" \
    ".Lw112_%=:\n\t" \
    "ds_read_b128 v[176:179], v249 offset:0\n\t" \
    "ds_read_b128 v[180:183], v249 offset:1024\n\t" \
    "ds_read_b128 v[184:187], v249 offset:2048\n\t" \
    "ds_read_b128 v[188:191], v249 offset:3072\n\t" \
    "ds_read_b128 v[192:195], v249 offset:4096\n\t" \
    "ds_read_b128 v[196:199], v249 offset:5120\n\t" \
    "ds_read_b128 v[200:203], v249 offset:6144\n\t" \
    "s_waitcnt lgkmcnt(6)\n\t" \
    "v_mfma_f32_16x16x32_bf16 v[168:171], v[0:3], v[176:179], 0\n\t" \
    "ds_read_b128 v[204:207], v249 offset:7168\n\t" \
    "s_waitcnt lgkmcnt(6)\n\t" \
    "v_mfma_f32_16x16x32_bf16 v[168:171], v[4:7], v[180:183], v[168:171]\n\t" \
    "ds_read_b128 v[176:179], v249 offset:8192\n\t" \
    "s_waitcnt lgkmcnt(6)\n\t" \
    "v_mfma_f32_16x16x32_bf16 v[168:171], v[8:11], v[184:187], v[168:171]\n\t" \
    "ds_read_b128 v[180:183], v249 offset:9216\n\t" \
    "s_waitcnt lgkmcnt(6)\n\t" \
    "v_mfma_f32_16x16x32_bf16 v[168:171], v[12:15], v[188:191], v[168:171]\n\t" \
    "ds_read_b128 v[184:187], v249 offset:10240\n\t" \
    "s_add_u32 s47, s46, 0x0\n\t" \
    "buffer_load_dwordx4 v[208:211], v253, %[rs], s47 offen sc1\n\t" \
    "s_add_u32 s47, s46, 0x8000\n\t" \
    "buffer_load_dwordx4 v[212:215], v253, %[rs], s47 offen sc1\n\t" \
    "s_cmp_gt_u32 %[wave], 3\n\t" \
    "s_cbranch_scc1 .Lw114_%=\n\t" \
    "s_add_u32 s47, s46, 0x10000\n\t" \
    "buffer_load_dwordx4 v[216:219], v253, %[rs], s47 offen sc1\n\t" \
    ".Lw114_%=:\n\t" \
    "s_waitcnt lgkmcnt(6)\n\t" \
    "v_mfma_f32_16x16x32_bf16 v[168:171], v[16:19], v[192:195], v[168:171]\n\t" \
    "ds_read_b128 v[188:191], v249 offset:11264\n\t" \
    "s_waitcnt lgkmcnt(6)\n\t" \
    "v_mfma_f32_16x16x32_bf16 v[168:171], v[20:23], v[196:199], v[168:171]\n\t" \
    "ds_read_b128 v[192:195], v249 offset:12288\n\t" \
    "s_waitcnt lgkmcnt(6)\n\t" \
    "v_mfma_f32_16x16x32_bf16 v[168:171], v[24:27], v[200:203], v[168:171]\n\t" \
    "ds_read_b128 v[196:199], v249 offset:13312\n\t" \
    "s_waitcnt lgkmcnt(6)\n\t" \
    "v_mfma_f32_16x16x32_bf16 v[168:171], v[28:31], v[204:207], v[168:171]\n\t" \
    "ds_read_b128 v[200:203], v249 offset:14336\n\t" \
    "s_waitcnt lgkmcnt(6)\n\t" \
    "v_mfma_f32_16x16x32_bf16 v[168:171], v[32:35], v[176:179], v[168:171]\n\t" \
    "ds_read_b128 v[204:207], v249 offset:15360\n\t" \
    "s_waitcnt lgkmcnt(6)\n\t" \
    "v_mfma_f32_16x16x32_bf16 v[168:171], v[36:39], v[180:183], v[168:171]\n\t" \
    "ds_read_b128 v[176:179], v249 offset:16384\n\t" \
    "s_waitcnt lgkmcnt(6)\n\t" \
    "v_mfma_f32_16x16x32_bf16 v[168:171], v[40:43], v[184:187], v[168:171]\n\t" \
    "ds_read_b128 v[180:183], v249 offset:17408\n\t" \
    "s_waitcnt lgkmcnt(6)\n\t" \
    "v_mfma_f32_16x16x32_bf16 v[168:171], v[44:47], v[188:191], v[168:171]\n\t" \
    "ds_read_b128 v[184:187], v249 offset:18432\n\t" \
    "s_waitcnt lgkmcnt(6)\n\t" \
    "v_mfma_f32_16x16x32_bf16 v[168:171], v[48:51], v[192:195], v[168:171]\n\t" \
    "ds_read_b128 v[188:191], v249 offset:19456\n\t" \
    "s_waitcnt lgkmcnt(6)\n\t" \
    "v_mfma_f32_16x16x32_bf16 v[168:171], v[52:55], v[196:199], v[168:171]\n\t" \
    "ds_read_b32 v236, v251 offset:72\n\t" \
    "ds_read_b64 v[238:239], v251 offset:64\n\t" \
    "s_waitcnt lgkmcnt(7)\n\t" \
    "v_mfma_f32_16x16x32_bf16 v[168:171], v[56:59], v[200:203], v[168:171]\n\t" \
    "s_waitcnt lgkmcnt(6)\n\t" \
    "v_mfma_f32_16x16x32_bf16 v[168:171], v[60:63], v[204:207], v[168:171]\n\t" \
    "s_waitcnt lgkmcnt(5)\n\t" \
    "v_mfma_f32_16x16x32_bf16 v[168:171], v[64:67], v[176:179], v[168:171]\n\t" \
    "s_waitcnt lgkmcnt(4)\n\t" \
    "v_mfma_f32_16x16x32_bf16 v[168:171], v[68:71], v[180:183], v[168:171]\n\t" \
    ".Lw115_%=:\n\t" \
    "s_waitcnt lgkmcnt(0)\n\t" \
    "v_cmp_ne_u32 vcc, s40, v236\n\t" \
    "s_cbranch_vccz .Lw116_%=\n\t" \
    "ds_read_b32 v236, v251 offset:72\n\t" \
    "ds_read_b64 v[238:239], v251 offset:64\n\t" \
    "s_branch .Lw115_%=\n\t" \
    ".Lw116_%=:\n\t" \
    "s_waitcnt lgkmcnt(0)\n\t" \
    "v_mul_f32 v240, v160, v239\n\t" \
    "v_mul_f32 v241, v161, v239\n\t" \
    "v_mfma_f32_16x16x32_bf16 v[168:171], v[72:75], v[184:187], v[168:171]\n\t" \
    "v_mul_f32 v242, v162, v239\n\t" \
    "v_mul_f32 v243, v163, v239\n\t" \
    "v_mfma_f32_16x16x32_bf16 v[168:171], v[76:79], v[188:191], v[168:171]\n\t" \
    "v_mov_b32 v237, 0xc0135761\n\t" \
    "s_nop 6\n\t" \
    "v_fma_f32 v168, v168, v238, v240\n\t" \
    "v_fma_f32 v169, v169, v238, v241\n\t" \
    "v_fma_f32 v170, v170, v238, v242\n\t" \
    "v_fma_f32 v171, v171, v238, v243\n\t" \
    "v_mul_f32 v176, v168, v168\n\t" \
    "v_mul_f32 v177, v169, v169\n\t" \
    "v_mul_f32 v178, v170, v170\n\t" \
    "v_mul_f32 v179, v171, v171\n\t" \
    "v_fmamk_f32 v176, v176, 0xbdd2d3e7, v237\n\t" \
    "v_fmamk_f32 v177, v177, 0xbdd2d3e7, v237\n\t" \
    "v_fmamk_f32 v178, v178, 0xbdd2d3e7, v237\n\t" \
    "v_fmamk_f32 v179, v179, 0xbdd2d3e7, v237\n\t" \
    "v_mul_f32 v176, v168, v176\n\t" \
    "v_mul_f32 v177, v169, v177\n\t" \
    "v_mul_f32 v178, v170, v178\n\t" \
    "v_mul_f32 v179, v171, v179\n\t" \
    "v_exp_f32 v176, v176\n\t" \
    "v_exp_f32 v177, v177\n\t" \
    "v_exp_f32 v178, v178\n\t" \
    "v_exp_f32 v179, v179\n\t" \
    "v_add_f32 v176, 1.0, v176\n\t" \
    "v_add_f32 v177, 1.0, v177\n\t" \
    "v_add_f32 v178, 1.0, v178\n\t" \
    "v_add_f32 v179, 1.0, v179\n\t" \
    "v_rcp_f32 v176, v176\n\t" \
    "v_rcp_f32 v177, v177\n\t" \
    "v_rcp_f32 v178, v178\n\t" \
    "v_rcp_f32 v179, v179\n\t" \
    "v_mul_f32 v168, v168, v176\n\t" \
    "v_mul_f32 v169, v169, v177\n\t" \
    "v_mul_f32 v170, v170, v178\n\t" \
    "v_mul_f32 v171, v171, v179\n\t" \
    "s_waitcnt vmcnt(0)\n\t" \
    "v_cvt_pk_bf16_f32 v184, v168, v169\n\t" \
    "v_cvt_pk_bf16_f32 v185, v170, v171\n\t" \
    "v_min_u32 v184, -2, v184\n\t" \
    "v_min_u32 v185, -2, v185\n\t" \
    "s_cmp_eq_u32 s40, s42\n\t" \
    "s_cselect_b64 s[44:45], s[58:59], 0\n\t" \
    "s_andn2_b64 exec, -1, s[44:45]\n\t" \
    "buffer_store_dwordx2 v[184:185], v80, %[rs], s49 offen sc1\n\t" \
    "s_mov_b64 exec, -1\n\t" \
    "s_cmp_ge_u32 s40, 122\n\t" \
    "s_cbranch_scc1 .Lw117_%=\n\t" \
    "s_add_i32 s44, s40, s62\n\t" \
    "s_max_i32 s44, s44, 0\n\t" \
    "s_lshr_b32 s45, s44, 1\n\t" \
    "s_and_b32 s44, s44, 1\n\t" \
    "s_lshl_b32 s44, s44, 12\n\t" \
    "s_mul_i32 s51, s45, 0x14000\n\t" \
    "s_add_u32 s51, s51, s44\n\t" \
    "s_lshr_b32 s45, s45, 4\n\t" \
    "s_mul_i32 s45, s45, 0xc2000\n\t" \
    "s_add_u32 s51, s51, s45\n\t" \
    "s_add_i32 s44, s40, s63\n\t" \
    "s_max_i32 s44, s44, 0\n\t" \
    "s_lshr_b32 s45, s44, 1\n\t" \
    "s_and_b32 s44, s44, 1\n\t" \
    "s_lshl_b32 s44, s44, 12\n\t" \
    "s_mul_i32 s50, s45, 0x14000\n\t" \
    "s_add_u32 s50, s50, s44\n\t" \
    "s_lshr_b32 s45, s45, 4\n\t" \
    "s_mul_i32 s45, s45, 0xc2000\n\t" \
    "s_add_u32 s50, s50, s45\n\t" \
    "s_sub_u32 s50, s50, s51\n\t" \
    "s_add_u32 s51, s51, 0x2400000\n\t" \
    "v_mad_u32_u24 v81, v255, s50, v254\n\t" \
    "v_mov_b64 v[188:189], -1\n\t" \
    "s_add_u32 s44, s40, 6\n\t" \
    "s_cmp_eq_u32 s44, s42\n\t" \
    "s_cselect_b64 s[44:45], s[58:59], 0\n\t" \
    "s_andn2_b64 exec, -1, s[44:45]\n\t" \
    "buffer_store_dwordx2 v[188:189], v81, %[rs], s51 offen sc1\n\t" \
    "s_mov_b64 exec, -1\n\t" \
    ".Lw117_%=:\n\t" \
    "v_xor_b32 v249, 0x8000, v249\n\t" \
    "v_xor_b32 v250, 0x8000, v250\n\t" \
    "v_xor_b32 v251, 0x800, v251\n\t" \
    "v_xor_b32 v252, 0x800, v252\n\t" \
    "s_add_u32 s40, s40, 1\n\t" \
    "s_cmp_lg_u32 s40, 128\n\t" \
    "s_cbranch_scc1 .Lw14_%=\n\t" \
    "s_branch .Lend%=\n\t" \
    ".Lend%=:\n\t" \
    "s_waitcnt vmcnt(0) lgkmcnt(0)\n\t" \
    "s_mov_b32 %[bud], s41\n\t"
#define ASM_W2_LOOP \
    "s_mov_b32 s40, 0\n\t" \
    "s_mov_b32 s41, 0x20000\n\t" \
    "s_add_u32 m0, %[wm], 0x0\n\t" \
    "s_add_u32 s44, %[wo], 0x0\n\t" \
    "buffer_load_dwordx4 v176, %[wr], s44 offen nt lds\n\t" \
    "s_add_u32 m0, %[wm], 0x2000\n\t" \
    "s_add_u32 s44, %[wo], 0xa000\n\t" \
    "buffer_load_dwordx4 v176, %[wr], s44 offen nt lds\n\t" \
    "s_add_u32 m0, %[wm], 0x4000\n\t" \
    "s_add_u32 s44, %[wo], 0x14000\n\t" \
    "buffer_load_dwordx4 v176, %[wr], s44 offen nt lds\n\t" \
    "s_add_u32 m0, %[wm], 0x6000\n\t" \
    "s_add_u32 s44, %[wo], 0x1e000\n\t" \
    "buffer_load_dwordx4 v176, %[wr], s44 offen nt lds\n\t" \
    "s_add_u32 m0, %[wm], 0x8000\n\t" \
    "s_add_u32 s44, %[wo], 0x28000\n\t" \
    "buffer_load_dwordx4 v176, %[wr], s44 offen nt lds\n\t" \
    "s_add_u32 m0, %[wm], 0xa000\n\t" \
    "s_add_u32 s44, %[wo], 0x32000\n\t" \
    "buffer_load_dwordx4 v176, %[wr], s44 offen nt lds\n\t" \
    "s_add_u32 m0, %[wm], 0xc000\n\t" \
    "s_add_u32 s44, %[wo], 0x3c000\n\t" \
    "buffer_load_dwordx4 v176, %[wr], s44 offen nt lds\n\t" \
    "s_add_u32 m0, %[wm], 0xe000\n\t" \
    "s_add_u32 s44, %[wo], 0x46000\n\t" \
    "buffer_load_dwordx4 v176, %[wr], s44 offen nt lds\n\t" \
    "s_add_u32 m0, %[wm], 0x10000\n\t" \
    "s_add_u32 s44, %[wo], 0x50000\n\t" \
    "buffer_load_dwordx4 v176, %[wr], s44 offen nt lds\n\t" \
    "s_add_u32 m0, %[wm], 0x12000\n\t" \
    "s_add_u32 s44, %[wo], 0x5a000\n\t" \
    "buffer_load_dwordx4 v176, %[wr], s44 offen nt lds\n\t" \
    "s_add_u32 m0, %[wm], 0x14000\n\t" \
    "s_add_u32 s44, %[wo], 0x64000\n\t" \
    "buffer_load_dwordx4 v176, %[wr], s44 offen nt lds\n\t" \
    "s_add_u32 m0, %[wm], 0x16000\n\t" \
    "s_add_u32 s44, %[wo], 0x6e000\n\t" \
    "buffer_load_dwordx4 v176, %[wr], s44 offen nt lds\n\t" \
    "s_add_u32 m0, %[wm], 0x18000\n\t" \
    "s_add_u32 s44, %[wo], 0x78000\n\t" \
    "buffer_load_dwordx4 v176, %[wr], s44 offen nt lds\n\t" \
    "s_add_u32 m0, %[wm], 0x1a000\n\t" \
    "s_add_u32 s44, %[wo], 0x82000\n\t" \
    "buffer_load_dwordx4 v176, %[wr], s44 offen nt lds\n\t" \
    "s_add_u32 m0, %[wm], 0x1c000\n\t" \
    "s_add_u32 s44, %[wo], 0x8c000\n\t" \
    "buffer_load_dwordx4 v176, %[wr], s44 offen nt lds\n\t" \
    "s_add_u32 m0, %[wm], 0x1e000\n\t" \
    "s_add_u32 s44, %[wo], 0x96000\n\t" \
    "buffer_load_dwordx4 v176, %[wr], s44 offen nt lds\n\t" \
    "s_add_u32 m0, %[wm], 0x20000\n\t" \
    "s_add_u32 s44, %[wo], 0xa0000\n\t" \
    "buffer_load_dwordx4 v176, %[wr], s44 offen nt lds\n\t" \
    "s_add_u32 m0, %[wm], 0x22000\n\t" \
    "s_add_u32 s44, %[wo], 0xaa000\n\t" \
    "buffer_load_dwordx4 v176, %[wr], s44 offen nt lds\n\t" \
    "s_add_u32 m0, %[wm], 0x24000\n\t" \
    "s_add_u32 s44, %[wo], 0xb4000\n\t" \
    "buffer_load_dwordx4 v176, %[wr], s44 offen nt lds\n\t" \
    "s_add_u32 m0, %[wm], 0x26000\n\t" \
    "s_add_u32 s44, %[wo], 0xbe000\n\t" \
    "buffer_load_dwordx4 v176, %[wr], s44 offen nt lds\n\t" \
    "s_waitcnt vmcnt(0) lgkmcnt(0)\n\t" \
    "s_barrier \n\t" \
    "v_add_u32 v178, 0x0, v177\n\t" \
    "ds_read_b64_tr_b16 v[0:1], v178 offset:0\n\t" \
    "ds_read_b64_tr_b16 v[2:3], v178 offset:1024\n\t" \
    "ds_read_b64_tr_b16 v[4:5], v178 offset:8192\n\t" \
    "ds_read_b64_tr_b16 v[6:7], v178 offset:9216\n\t" \
    "ds_read_b64_tr_b16 v[8:9], v178 offset:16384\n\t" \
    "ds_read_b64_tr_b16 v[10:11], v178 offset:17408\n\t" \
    "ds_read_b64_tr_b16 v[12:13], v178 offset:24576\n\t" \
    "ds_read_b64_tr_b16 v[14:15], v178 offset:25600\n\t" \
    "v_add_u32 v178, 0x8000, v177\n\t" \
    "ds_read_b64_tr_b16 v[16:17], v178 offset:0\n\t" \
    "ds_read_b64_tr_b16 v[18:19], v178 offset:1024\n\t" \
    "ds_read_b64_tr_b16 v[20:21], v178 offset:8192\n\t" \
    "ds_read_b64_tr_b16 v[22:23], v178 offset:9216\n\t" \
    "ds_read_b64_tr_b16 v[24:25], v178 offset:16384\n\t" \
    "ds_read_b64_tr_b16 v[26:27], v178 offset:17408\n\t" \
    "ds_read_b64_tr_b16 v[28:29], v178 offset:24576\n\t" \
    "ds_read_b64_tr_b16 v[30:31], v178 offset:25600\n\t" \
    "v_add_u32 v178, 0x10000, v177\n\t" \
    "ds_read_b64_tr_b16 v[32:33], v178 offset:0\n\t" \
    "ds_read_b64_tr_b16 v[34:35], v178 offset:1024\n\t" \
    "ds_read_b64_tr_b16 v[36:37], v178 offset:8192\n\t" \
    "ds_read_b64_tr_b16 v[38:39], v178 offset:9216\n\t" \
    "ds_read_b64_tr_b16 v[40:41], v178 offset:16384\n\t" \
    "ds_read_b64_tr_b16 v[42:43], v178 offset:17408\n\t" \
    "ds_read_b64_tr_b16 v[44:45], v178 offset:24576\n\t" \
    "ds_read_b64_tr_b16 v[46:47], v178 offset:25600\n\t" \
    "v_add_u32 v178, 0x18000, v177\n\t" \
    "ds_read_b64_tr_b16 v[48:49], v178 offset:0\n\t" \
    "ds_read_b64_tr_b16 v[50:51], v178 offset:1024\n\t" \
    "ds_read_b64_tr_b16 v[52:53], v178 offset:8192\n\t" \
    "ds_read_b64_tr_b16 v[54:55], v178 offset:9216\n\t" \
    "ds_read_b64_tr_b16 v[56:57], v178 offset:16384\n\t" \
    "ds_read_b64_tr_b16 v[58:59], v178 offset:17408\n\t" \
    "ds_read_b64_tr_b16 v[60:61], v178 offset:24576\n\t" \
    "ds_read_b64_tr_b16 v[62:63], v178 offset:25600\n\t" \
    "v_add_u32 v178, 0x20000, v177\n\t" \
    "ds_read_b64_tr_b16 v[64:65], v178 offset:0\n\t" \
    "ds_read_b64_tr_b16 v[66:67], v178 offset:1024\n\t" \
    "ds_read_b64_tr_b16 v[68:69], v178 offset:8192\n\t" \
    "ds_read_b64_tr_b16 v[70:71], v178 offset:9216\n\t" \
    "ds_read_b64_tr_b16 v[72:73], v178 offset:16384\n\t" \
    "ds_read_b64_tr_b16 v[74:75], v178 offset:17408\n\t" \
    "ds_read_b64_tr_b16 v[76:77], v178 offset:24576\n\t" \
    "ds_read_b64_tr_b16 v[78:79], v178 offset:25600\n\t" \
    "v_mov_b32 v228, 0x3f803f80\n\t" \
    "v_mov_b32 v229, 0x3f803f80\n\t" \
    "v_mov_b32 v230, 0x3f803f80\n\t" \
    "v_mov_b32 v231, 0x3f803f80\n\t" \
    "v_mov_b64 v[216:217], 0\n\t" \
    "v_mov_b32 v219, 0\n\t" \
    "v_bfe_u32 v255, v253, 2, 5\n\t" \
    "v_add_u32 v255, 0x7000, v255\n\t" \
    "v_mov_b32 v218, 0\n\t" \
    "s_waitcnt lgkmcnt(0)\n\t" \
    "s_barrier \n\t" \
    "ds_write_b32 v255, v218\n\t" \
    "s_waitcnt lgkmcnt(0)\n\t" \
    "s_barrier \n\t" \
    ".Lw21_%=:\n\t" \
    "v_mov_b32 v248, 0\n\t" \
    "buffer_load_dword v248, v248, %[rs], %[go] offen sc1\n\t" \
    "s_waitcnt vmcnt(0)\n\t" \
    "v_cmp_ne_u32 vcc, 0, v248\n\t" \
    "s_cbranch_vccnz .Lw22_%=\n\t" \
    "s_cmp_eq_u32 s41, 0\n\t" \
    "s_cbranch_scc1 .Lw22_%=\n\t" \
    "s_sub_u32 s41, s41, 1\n\t" \
    "s_sleep 16\n\t" \
    "s_branch .Lw21_%=\n\t" \
    ".Lw22_%=:\n\t" \
    "s_lshr_b32 s48, %[wave], 2\n\t" \
    "s_mul_i32 s48, s48, 0x3000\n\t" \
    "s_add_u32 s48, s48, %[pi]\n\t" \
    "s_mov_b32 s43, 0\n\t" \
    "s_add_u32 s46, s43, s48\n\t" \
    "s_add_u32 s47, s46, 0x0\n\t" \
    "buffer_load_dwordx4 v[208:211], v253, %[rs], s47 offen sc1\n\t" \
    "s_add_u32 s47, s46, 0x8000\n\t" \
    "buffer_load_dwordx4 v[212:215], v253, %[rs], s47 offen sc1\n\t" \
    "s_cmp_gt_u32 %[wave], 3\n\t" \
    "s_cbranch_scc1 .Lw23_%=\n\t" \
    "s_add_u32 s47, s46, 0x10000\n\t" \
    "buffer_load_dwordx4 v[216:219], v253, %[rs], s47 offen sc1\n\t" \
    ".Lw23_%=:\n\t" \
    "s_waitcnt vmcnt(0)\n\t" \
    ".Lw24_%=:\n\t" \
    "s_add_u32 s49, s43, 0x2c00000\n\t" \
    "s_add_u32 s50, s43, 0x1c00000\n\t" \
    "s_lshr_b32 s44, s40, 6\n\t" \
    "s_lshl_b32 s44, s44, 14\n\t" \
    "s_and_b32 s45, s40, 63\n\t" \
    "s_lshl_b32 s45, s45, 7\n\t" \
    "s_add_u32 s44, s44, s45\n\t" \
    "s_add_u32 s51, s44, 0x4d0000\n\t" \
    "s_add_i32 s44, s40, 1\n\t" \
    "s_max_i32 s44, s44, 0\n\t" \
    "s_lshr_b32 s45, s44, 1\n\t" \
    "s_and_b32 s44, s44, 1\n\t" \
    "s_lshl_b32 s44, s44, 12\n\t" \
    "s_mul_i32 s43, s45, 0x14000\n\t" \
    "s_add_u32 s43, s43, s44\n\t" \
    "s_lshr_b32 s45, s45, 4\n\t" \
    "s_mul_i32 s45, s45, 0xc2000\n\t" \
    "s_add_u32 s43, s43, s45\n\t" \
    "s_setprio 2\n\t" \
    "v_max_u32 v248, v208, v209\n\t" \
    "v_max3_u32 v248, v210, v211, v248\n\t" \
    "v_max3_u32 v248, v212, v213, v248\n\t" \
    "v_max3_u32 v248, v214, v215, v248\n\t" \
    "v_max3_u32 v248, v216, v217, v248\n\t" \
    "v_max3_u32 v248, v218, v219, v248\n\t" \
    "v_cmp_eq_u32 vcc, -1, v248\n\t" \
    "s_cbranch_vccz .Lw26_%=\n\t" \
    "s_and_b32 s44, s40, 1\n\t" \
    "s_cmp_eq_u32 s44, 0\n\t" \
    "s_cbranch_scc1 .Lw25_%=\n\t" \
    "s_sleep 6\n\t" \
    ".Lw25_%=:\n\t" \
    "v_max_u32 v248, v208, v209\n\t" \
    "v_max3_u32 v248, v210, v211, v248\n\t" \
    "v_cmp_eq_u32 vcc, -1, v248\n\t" \
    "s_cbranch_vccz .Lw28_%=\n\t" \
    "s_add_u32 s47, s46, 0x0\n\t" \
    "buffer_load_dwordx4 v[208:211], v253, %[rs], s47 offen sc1\n\t" \
    ".Lw28_%=:\n\t" \
    "v_max_u32 v248, v212, v213\n\t" \
    "v_max3_u32 v248, v214, v215, v248\n\t" \
    "v_cmp_eq_u32 vcc, -1, v248\n\t" \
    "s_cbranch_vccz .Lw29_%=\n\t" \
    "s_add_u32 s47, s46, 0x8000\n\t" \
    "buffer_load_dwordx4 v[212:215], v253, %[rs], s47 offen sc1\n\t" \
    ".Lw29_%=:\n\t" \
    "v_max_u32 v248, v216, v217\n\t" \
    "v_max3_u32 v248, v218, v219, v248\n\t" \
    "v_cmp_eq_u32 vcc, -1, v248\n\t" \
    "s_cbranch_vccz .Lw210_%=\n\t" \
    "s_add_u32 s47, s46, 0x10000\n\t" \
    "buffer_load_dwordx4 v[216:219], v253, %[rs], s47 offen sc1\n\t" \
    ".Lw210_%=:\n\t" \
    "s_waitcnt vmcnt(0)\n\t" \
    "v_max_u32 v248, v208, v209\n\t" \
    "v_max3_u32 v248, v210, v211, v248\n\t" \
    "v_max3_u32 v248, v212, v213, v248\n\t" \
    "v_max3_u32 v248, v214, v215, v248\n\t" \
    "v_max3_u32 v248, v216, v217, v248\n\t" \
    "v_max3_u32 v248, v218, v219, v248\n\t" \
    "v_cmp_eq_u32 vcc, -1, v248\n\t" \
    "s_cbranch_vccz .Lw26_%=\n\t" \
    "s_cmp_eq_u32 s41, 0\n\t" \
    "s_cbranch_scc1 .Lw26_%=\n\t" \
    "s_sub_u32 s41, s41, 1\n\t" \
    "s_branch .Lw25_%=\n\t" \
    ".Lw26_%=:\n\t" \
    "ds_write_b128 v250, v[208:211] offset:0\n\t" \
    "ds_write_b128 v250, v[212:215] offset:8192\n\t" \
    "s_cmp_gt_u32 %[wave], 3\n\t" \
    "s_cbranch_scc1 .Lw211_%=\n\t" \
    "ds_write_b128 v250, v[216:219] offset:16384\n\t" \
    ".Lw211_%=:\n\t" \
    "s_add_u32 s46, s43, s48\n\t" \
    "s_waitcnt lgkmcnt(0)\n\t" \
    "s_barrier \n\t" \
    "s_setprio 0\n\t" \
    "ds_read_b128 v[176:179], v249 offset:0\n\t" \
    "buffer_load_dwordx2 v[232:233], v254, %[rs], s50 offen sc1\n\t" \
    "buffer_load_dwordx2 v[234:235], v251, %[rs], s51 offen sc1\n\t" \
    "ds_read_b128 v[180:183], v249 offset:1024\n\t" \
    "ds_read_b128 v[184:187], v249 offset:2048\n\t" \
    "ds_read_b128 v[188:191], v249 offset:3072\n\t" \
    "ds_read_b128 v[192:195], v249 offset:4096\n\t" \
    "ds_read_b128 v[196:199], v249 offset:5120\n\t" \
    "ds_read_b128 v[200:203], v249 offset:6144\n\t" \
    "s_waitcnt lgkmcnt(6)\n\t" \
    "v_mfma_f32_16x16x32_bf16 v[168:171], v[0:3], v[176:179], 0\n\t" \
    "ds_read_b128 v[204:207], v249 offset:7168\n\t" \
    "s_waitcnt lgkmcnt(6)\n\t" \
    "v_mfma_f32_16x16x32_bf16 v[168:171], v[4:7], v[180:183], v[168:171]\n\t" \
    "ds_read_b128 v[176:179], v249 offset:8192\n\t" \
    "s_waitcnt lgkmcnt(6)\n\t" \
    "v_mfma_f32_16x16x32_bf16 v[168:171], v[8:11], v[184:187], v[168:171]\n\t" \
    "ds_read_b128 v[180:183], v249 offset:9216\n\t" \
    "s_waitcnt lgkmcnt(6)\n\t" \
    "v_mfma_f32_16x16x32_bf16 v[168:171], v[12:15], v[188:191], v[168:171]\n\t" \
    "ds_read_b128 v[184:187], v249 offset:10240\n\t" \
    "s_add_u32 s47, s46, 0x0\n\t" \
    "buffer_load_dwordx4 v[208:211], v253, %[rs], s47 offen sc1\n\t" \
    "s_add_u32 s47, s46, 0x8000\n\t" \
    "buffer_load_dwordx4 v[212:215], v253, %[rs], s47 offen sc1\n\t" \
    "s_cmp_gt_u32 %[wave], 3\n\t" \
    "s_cbranch_scc1 .Lw212_%=\n\t" \
    "s_add_u32 s47, s46, 0x10000\n\t" \
    "buffer_load_dwordx4 v[216:219], v253, %[rs], s47 offen sc1\n\t" \
    ".Lw212_%=:\n\t" \
    "s_waitcnt lgkmcnt(6)\n\t" \
    "v_mfma_f32_16x16x32_bf16 v[168:171], v[16:19], v[192:195], v[168:171]\n\t" \
    "ds_read_b128 v[188:191], v249 offset:11264\n\t" \
    "s_waitcnt lgkmcnt(6)\n\t" \
    "v_mfma_f32_16x16x32_bf16 v[168:171], v[20:23], v[196:199], v[168:171]\n\t" \
    "ds_read_b128 v[192:195], v249 offset:12288\n\t" \
    "s_waitcnt lgkmcnt(6)\n\t" \
    "v_mfma_f32_16x16x32_bf16 v[168:171], v[24:27], v[200:203], v[168:171]\n\t" \
    "ds_read_b128 v[196:199], v249 offset:13312\n\t" \
    "s_waitcnt lgkmcnt(6)\n\t" \
    "v_mfma_f32_16x16x32_bf16 v[168:171], v[28:31], v[204:207], v[168:171]\n\t" \
    "ds_read_b128 v[200:203], v249 offset:14336\n\t" \
    "s_waitcnt lgkmcnt(6)\n\t" \
    "v_mfma_f32_16x16x32_bf16 v[168:171], v[32:35], v[176:179], v[168:171]\n\t" \
    "ds_read_b128 v[204:207], v249 offset:15360\n\t" \
    "s_waitcnt lgkmcnt(6)\n\t" \
    "v_mfma_f32_16x16x32_bf16 v[168:171], v[36:39], v[180:183], v[168:171]\n\t" \
    "ds_read_b128 v[176:179], v249 offset:16384\n\t" \
    "s_waitcnt lgkmcnt(6)\n\t" \
    "v_mfma_f32_16x16x32_bf16 v[168:171], v[40:43], v[184:187], v[168:171]\n\t" \
    "ds_read_b128 v[180:183], v249 offset:17408\n\t" \
    "s_waitcnt lgkmcnt(6)\n\t" \
    "v_mfma_f32_16x16x32_bf16 v[168:171], v[44:47], v[188:191], v[168:171]\n\t" \
    "ds_read_b128 v[184:187], v249 offset:18432\n\t" \
    "s_waitcnt lgkmcnt(6)\n\t" \
    "v_mfma_f32_16x16x32_bf16 v[168:171], v[48:51], v[192:195], v[168:171]\n\t" \
    "ds_read_b128 v[188:191], v249 offset:19456\n\t" \
    "s_waitcnt lgkmcnt(6)\n\t" \
    "v_mfma_f32_16x16x32_bf16 v[168:171], v[52:55], v[196:199], v[168:171]\n\t" \
    "s_waitcnt lgkmcnt(5)\n\t" \
    "v_mfma_f32_16x16x32_bf16 v[168:171], v[56:59], v[200:203], v[168:171]\n\t" \
    "s_waitcnt lgkmcnt(4)\n\t" \
    "v_mfma_f32_16x16x32_bf16 v[168:171], v[60:63], v[204:207], v[168:171]\n\t" \
    "s_waitcnt lgkmcnt(3)\n\t" \
    "v_mfma_f32_16x16x32_bf16 v[168:171], v[64:67], v[176:179], v[168:171]\n\t" \
    "s_waitcnt lgkmcnt(2)\n\t" \
    "v_mfma_f32_16x16x32_bf16 v[168:171], v[68:71], v[180:183], v[168:171]\n\t" \
    "s_waitcnt lgkmcnt(1)\n\t" \
    "v_mfma_f32_16x16x32_bf16 v[168:171], v[72:75], v[184:187], v[168:171]\n\t" \
    "s_waitcnt lgkmcnt(0)\n\t" \
    "v_mfma_f32_16x16x32_bf16 v[168:171], v[76:79], v[188:191], v[168:171]\n\t" \
    "s_waitcnt vmcnt(0)\n\t" \
    ".Lw213_%=:\n\t" \
    "v_max_u32 v248, v234, v235\n\t" \
    "v_cmp_eq_u32 vcc, -1, v248\n\t" \
    "s_cbranch_vccz .Lw214_%=\n\t" \
    "s_cmp_eq_u32 s41, 0\n\t" \
    "s_cbranch_scc1 .Lw214_%=\n\t" \
    "s_sub_u32 s41, s41, 1\n\t" \
    "buffer_load_dwordx2 v[234:235], v251, %[rs], s51 offen sc1\n\t" \
    "s_waitcnt vmcnt(0)\n\t" \
    "s_branch .Lw213_%=\n\t" \
    ".Lw214_%=:\n\t" \
    "v_lshlrev_b32 v176, 16, v232\n\t" \
    "v_and_b32 v177, 0xffff0000, v232\n\t" \
    "v_lshlrev_b32 v178, 16, v233\n\t" \
    "v_and_b32 v179, 0xffff0000, v233\n\t" \
    "v_sub_f32 v176, v176, v234\n\t" \
    "v_sub_f32 v177, v177, v234\n\t" \
    "v_sub_f32 v178, v178, v234\n\t" \
    "v_sub_f32 v179, v179, v234\n\t" \
    "v_mul_f32 v176, v176, v235\n\t" \
    "v_mul_f32 v177, v177, v235\n\t" \
    "v_mul_f32 v178, v178, v235\n\t" \
    "v_mul_f32 v179, v179, v235\n\t" \
    "v_cvt_pk_bf16_f32 v180, v176, v177\n\t" \
    "v_cvt_pk_bf16_f32 v181, v178, v179\n\t" \
    "v_lshlrev_b32 v176, 16, v180\n\t" \
    "v_and_b32 v177, 0xffff0000, v180\n\t" \
    "v_lshlrev_b32 v178, 16, v181\n\t" \
    "v_and_b32 v179, 0xffff0000, v181\n\t" \
    "v_add_f32 v168, v176, v168\n\t" \
    "v_add_f32 v169, v177, v169\n\t" \
    "v_add_f32 v170, v178, v170\n\t" \
    "v_add_f32 v171, v179, v171\n\t" \
    "v_cvt_pk_bf16_f32 v184, v168, v169\n\t" \
    "v_cvt_pk_bf16_f32 v185, v170, v171\n\t" \
    "v_min_u32 v184, -2, v184\n\t" \
    "v_min_u32 v185, -2, v185\n\t" \
    "buffer_store_dwordx2 v[184:185], v254, %[rs], s49 offen sc1\n\t" \
    "s_cmp_ge_u32 s40, 122\n\t" \
    "s_cbranch_scc1 .Lw215_%=\n\t" \
    "s_add_i32 s44, s40, 6\n\t" \
    "s_max_i32 s44, s44, 0\n\t" \
    "s_lshr_b32 s45, s44, 1\n\t" \
    "s_and_b32 s44, s44, 1\n\t" \
    "s_lshl_b32 s44, s44, 12\n\t" \
    "s_mul_i32 s50, s45, 0x14000\n\t" \
    "s_add_u32 s50, s50, s44\n\t" \
    "s_lshr_b32 s45, s45, 4\n\t" \
    "s_mul_i32 s45, s45, 0xc2000\n\t" \
    "s_add_u32 s50, s50, s45\n\t" \
    "s_add_u32 s50, s50, 0x2c00000\n\t" \
    "v_mov_b64 v[188:189], -1\n\t" \
    "buffer_store_dwordx2 v[188:189], v254, %[rs], s50 offen sc1\n\t" \
    ".Lw215_%=:\n\t" \
    "v_xor_b32 v249, 0x8000, v249\n\t" \
    "v_xor_b32 v250, 0x8000, v250\n\t" \
    "s_add_u32 s40, s40, 1\n\t" \
    "s_cmp_lg_u32 s40, 128\n\t" \
    "s_cbranch_scc1 .Lw24_%=\n\t" \
    "s_branch .Lend%=\n\t" \
    ".Lend%=:\n\t" \
    "s_waitcnt vmcnt(0) lgkmcnt(0)\n\t" \
    "s_mov_b32 %[bud], s41\n\t"
#define ASM_W3_LOOP \
    "s_mov_b32 s40, 0\n\t" \
    "s_mov_b32 s41, 0x20000\n\t" \
    "s_add_u32 m0, %[wm], 0x0\n\t" \
    "s_add_u32 s44, %[wo], 0x0\n\t" \
    "buffer_load_dwordx4 v176, %[wr], s44 offen nt lds\n\t" \
    "s_add_u32 m0, %[wm], 0x2000\n\t" \
    "s_add_u32 s44, %[wo], 0xc000\n\t" \
    "buffer_load_dwordx4 v176, %[wr], s44 offen nt lds\n\t" \
    "s_add_u32 m0, %[wm], 0x4000\n\t" \
    "s_add_u32 s44, %[wo], 0x18000\n\t" \
    "buffer_load_dwordx4 v176, %[wr], s44 offen nt lds\n\t" \
    "s_add_u32 m0, %[wm], 0x6000\n\t" \
    "s_add_u32 s44, %[wo], 0x24000\n\t" \
    "buffer_load_dwordx4 v176, %[wr], s44 offen nt lds\n\t" \
    "s_add_u32 m0, %[wm], 0x8000\n\t" \
    "s_add_u32 s44, %[wo], 0x30000\n\t" \
    "buffer_load_dwordx4 v176, %[wr], s44 offen nt lds\n\t" \
    "s_add_u32 m0, %[wm], 0xa000\n\t" \
    "s_add_u32 s44, %[wo], 0x3c000\n\t" \
    "buffer_load_dwordx4 v176, %[wr], s44 offen nt lds\n\t" \
    "s_add_u32 m0, %[wm], 0xc000\n\t" \
    "s_add_u32 s44, %[wo], 0x48000\n\t" \
    "buffer_load_dwordx4 v176, %[wr], s44 offen nt lds\n\t" \
    "s_add_u32 m0, %[wm], 0xe000\n\t" \
    "s_add_u32 s44, %[wo], 0x54000\n\t" \
    "buffer_load_dwordx4 v176, %[wr], s44 offen nt lds\n\t" \
    "s_add_u32 m0, %[wm], 0x10000\n\t" \
    "s_add_u32 s44, %[wo], 0x60000\n\t" \
    "buffer_load_dwordx4 v176, %[wr], s44 offen nt lds\n\t" \
    "s_add_u32 m0, %[wm], 0x12000\n\t" \
    "s_add_u32 s44, %[wo], 0x6c000\n\t" \
    "buffer_load_dwordx4 v176, %[wr], s44 offen nt lds\n\t" \
    "s_add_u32 m0, %[wm], 0x14000\n\t" \
    "s_add_u32 s44, %[wo], 0x78000\n\t" \
    "buffer_load_dwordx4 v176, %[wr], s44 offen nt lds\n\t" \
    "s_add_u32 m0, %[wm], 0x16000\n\t" \
    "s_add_u32 s44, %[wo], 0x84000\n\t" \
    "buffer_load_dwordx4 v176, %[wr], s44 offen nt lds\n\t" \
    "s_add_u32 m0, %[wm], 0x18000\n\t" \
    "s_add_u32 s44, %[wo], 0x90000\n\t" \
    "buffer_load_dwordx4 v176, %[wr], s44 offen nt lds\n\t" \
    "s_add_u32 m0, %[wm], 0x1a000\n\t" \
    "s_add_u32 s44, %[wo], 0x9c000\n\t" \
    "buffer_load_dwordx4 v176, %[wr], s44 offen nt lds\n\t" \
    "s_add_u32 m0, %[wm], 0x1c000\n\t" \
    "s_add_u32 s44, %[wo], 0xa8000\n\t" \
    "buffer_load_dwordx4 v176, %[wr], s44 offen nt lds\n\t" \
    "s_add_u32 m0, %[wm], 0x1e000\n\t" \
    "s_add_u32 s44, %[wo], 0xb4000\n\t" \
    "buffer_load_dwordx4 v176, %[wr], s44 offen nt lds\n\t" \
    "s_add_u32 m0, %[wm], 0x20000\n\t" \
    "s_add_u32 s44, %[wo], 0xc0000\n\t" \
    "buffer_load_dwordx4 v176, %[wr], s44 offen nt lds\n\t" \
    "s_add_u32 m0, %[wm], 0x22000\n\t" \
    "s_add_u32 s44, %[wo], 0xcc000\n\t" \
    "buffer_load_dwordx4 v176, %[wr], s44 offen nt lds\n\t" \
    "s_add_u32 m0, %[wm], 0x24000\n\t" \
    "s_add_u32 s44, %[wo], 0xd8000\n\t" \
    "buffer_load_dwordx4 v176, %[wr], s44 offen nt lds\n\t" \
    "s_add_u32 m0, %[wm], 0x26000\n\t" \
    "s_add_u32 s44, %[wo], 0xe4000\n\t" \
    "buffer_load_dwordx4 v176, %[wr], s44 offen nt lds\n\t" \
    "s_waitcnt vmcnt(0) lgkmcnt(0)\n\t" \
    "s_barrier \n\t" \
    "v_add_u32 v178, 0x0, v177\n\t" \
    "v_xor_b32 v179, 32, v178\n\t" \
    "ds_read_b64_tr_b16 v[0:1], v178 offset:0\n\t" \
    "ds_read_b64_tr_b16 v[2:3], v178 offset:2048\n\t" \
    "ds_read_b64_tr_b16 v[80:81], v179 offset:0\n\t" \
    "ds_read_b64_tr_b16 v[82:83], v179 offset:2048\n\t" \
    "ds_read_b64_tr_b16 v[4:5], v178 offset:16384\n\t" \
    "ds_read_b64_tr_b16 v[6:7], v178 offset:18432\n\t" \
    "ds_read_b64_tr_b16 v[84:85], v179 offset:16384\n\t" \
    "ds_read_b64_tr_b16 v[86:87], v179 offset:18432\n\t" \
    "v_add_u32 v178, 0x8000, v177\n\t" \
    "v_xor_b32 v179, 32, v178\n\t" \
    "ds_read_b64_tr_b16 v[8:9], v178 offset:0\n\t" \
    "ds_read_b64_tr_b16 v[10:11], v178 offset:2048\n\t" \
    "ds_read_b64_tr_b16 v[88:89], v179 offset:0\n\t" \
    "ds_read_b64_tr_b16 v[90:91], v179 offset:2048\n\t" \
    "ds_read_b64_tr_b16 v[12:13], v178 offset:16384\n\t" \
    "ds_read_b64_tr_b16 v[14:15], v178 offset:18432\n\t" \
    "ds_read_b64_tr_b16 v[92:93], v179 offset:16384\n\t" \
    "ds_read_b64_tr_b16 v[94:95], v179 offset:18432\n\t" \
    "v_add_u32 v178, 0x10000, v177\n\t" \
    "v_xor_b32 v179, 32, v178\n\t" \
    "ds_read_b64_tr_b16 v[16:17], v178 offset:0\n\t" \
    "ds_read_b64_tr_b16 v[18:19], v178 offset:2048\n\t" \
    "ds_read_b64_tr_b16 v[96:97], v179 offset:0\n\t" \
    "ds_read_b64_tr_b16 v[98:99], v179 offset:2048\n\t" \
    "ds_read_b64_tr_b16 v[20:21], v178 offset:16384\n\t" \
    "ds_read_b64_tr_b16 v[22:23], v178 offset:18432\n\t" \
    "ds_read_b64_tr_b16 v[100:101], v179 offset:16384\n\t" \
    "ds_read_b64_tr_b16 v[102:103], v179 offset:18432\n\t" \
    "v_add_u32 v178, 0x18000, v177\n\t" \
    "v_xor_b32 v179, 32, v178\n\t" \
    "ds_read_b64_tr_b16 v[24:25], v178 offset:0\n\t" \
    "ds_read_b64_tr_b16 v[26:27], v178 offset:2048\n\t" \
    "ds_read_b64_tr_b16 v[104:105], v179 offset:0\n\t" \
    "ds_read_b64_tr_b16 v[106:107], v179 offset:2048\n\t" \
    "ds_read_b64_tr_b16 v[28:29], v178 offset:16384\n\t" \
    "ds_read_b64_tr_b16 v[30:31], v178 offset:18432\n\t" \
    "ds_read_b64_tr_b16 v[108:109], v179 offset:16384\n\t" \
    "ds_read_b64_tr_b16 v[110:111], v179 offset:18432\n\t" \
    "v_add_u32 v178, 0x20000, v177\n\t" \
    "v_xor_b32 v179, 32, v178\n\t" \
    "ds_read_b64_tr_b16 v[32:33], v178 offset:0\n\t" \
    "ds_read_b64_tr_b16 v[34:35], v178 offset:2048\n\t" \
    "ds_read_b64_tr_b16 v[112:113], v179 offset:0\n\t" \
    "ds_read_b64_tr_b16 v[114:115], v179 offset:2048\n\t" \
    "ds_read_b64_tr_b16 v[36:37], v178 offset:16384\n\t" \
    "ds_read_b64_tr_b16 v[38:39], v178 offset:18432\n\t" \
    "ds_read_b64_tr_b16 v[116:117], v179 offset:16384\n\t" \
    "ds_read_b64_tr_b16 v[118:119], v179 offset:18432\n\t" \
    "s_waitcnt lgkmcnt(0)\n\t" \
    "s_barrier \n\t" \
    "s_add_u32 m0, %[wm], 0x0\n\t" \
    "s_add_u32 s44, %[wo], 0xf0000\n\t" \
    "buffer_load_dwordx4 v176, %[wr], s44 offen nt lds\n\t" \
    "s_add_u32 m0, %[wm], 0x2000\n\t" \
    "s_add_u32 s44, %[wo], 0xfc000\n\t" \
    "buffer_load_dwordx4 v176, %[wr], s44 offen nt lds\n\t" \
    "s_add_u32 m0, %[wm], 0x4000\n\t" \
    "s_add_u32 s44, %[wo], 0x108000\n\t" \
    "buffer_load_dwordx4 v176, %[wr], s44 offen nt lds\n\t" \
    "s_add_u32 m0, %[wm], 0x6000\n\t" \
    "s_add_u32 s44, %[wo], 0x114000\n\t" \
    "buffer_load_dwordx4 v176, %[wr], s44 offen nt lds\n\t" \
    "s_add_u32 m0, %[wm], 0x8000\n\t" \
    "s_add_u32 s44, %[wo], 0x120000\n\t" \
    "buffer_load_dwordx4 v176, %[wr], s44 offen nt lds\n\t" \
    "s_add_u32 m0, %[wm], 0xa000\n\t" \
    "s_add_u32 s44, %[wo], 0x12c000\n\t" \
    "buffer_load_dwordx4 v176, %[wr], s44 offen nt lds\n\t" \
    "s_add_u32 m0, %[wm], 0xc000\n\t" \
    "s_add_u32 s44, %[wo], 0x138000\n\t" \
    "buffer_load_dwordx4 v176, %[wr], s44 offen nt lds\n\t" \
    "s_add_u32 m0, %[wm], 0xe000\n\t" \
    "s_add_u32 s44, %[wo], 0x144000\n\t" \
    "buffer_load_dwordx4 v176, %[wr], s44 offen nt lds\n\t" \
    "s_add_u32 m0, %[wm], 0x10000\n\t" \
    "s_add_u32 s44, %[wo], 0x150000\n\t" \
    "buffer_load_dwordx4 v176, %[wr], s44 offen nt lds\n\t" \
    "s_add_u32 m0, %[wm], 0x12000\n\t" \
    "s_add_u32 s44, %[wo], 0x15c000\n\t" \
    "buffer_load_dwordx4 v176, %[wr], s44 offen nt lds\n\t" \
    "s_add_u32 m0, %[wm], 0x14000\n\t" \
    "s_add_u32 s44, %[wo], 0x168000\n\t" \
    "buffer_load_dwordx4 v176, %[wr], s44 offen nt lds\n\t" \
    "s_add_u32 m0, %[wm], 0x16000\n\t" \
    "s_add_u32 s44, %[wo], 0x174000\n\t" \
    "buffer_load_dwordx4 v176, %[wr], s44 offen nt lds\n\t" \
    "s_add_u32 m0, %[wm], 0x18000\n\t" \
    "s_add_u32 s44, %[wo], 0x180000\n\t" \
    "buffer_load_dwordx4 v176, %[wr], s44 offen nt lds\n\t" \
    "s_add_u32 m0, %[wm], 0x1a000\n\t" \
    "s_add_u32 s44, %[wo], 0x18c000\n\t" \
    "buffer_load_dwordx4 v176, %[wr], s44 offen nt lds\n\t" \
    "s_add_u32 m0, %[wm], 0x1c000\n\t" \
    "s_add_u32 s44, %[wo], 0x198000\n\t" \
    "buffer_load_dwordx4 v176, %[wr], s44 offen nt lds\n\t" \
    "s_add_u32 m0, %[wm], 0x1e000\n\t" \
    "s_add_u32 s44, %[wo], 0x1a4000\n\t" \
    "buffer_load_dwordx4 v176, %[wr], s44 offen nt lds\n\t" \
    "s_add_u32 m0, %[wm], 0x20000\n\t" \
    "s_add_u32 s44, %[wo], 0x1b0000\n\t" \
    "buffer_load_dwordx4 v176, %[wr], s44 offen nt lds\n\t" \
    "s_add_u32 m0, %[wm], 0x22000\n\t" \
    "s_add_u32 s44, %[wo], 0x1bc000\n\t" \
    "buffer_load_dwordx4 v176, %[wr], s44 offen nt lds\n\t" \
    "s_add_u32 m0, %[wm], 0x24000\n\t" \
    "s_add_u32 s44, %[wo], 0x1c8000\n\t" \
    "buffer_load_dwordx4 v176, %[wr], s44 offen nt lds\n\t" \
    "s_add_u32 m0, %[wm], 0x26000\n\t" \
    "s_add_u32 s44, %[wo], 0x1d4000\n\t" \
    "buffer_load_dwordx4 v176, %[wr], s44 offen nt lds\n\t" \
    "s_waitcnt vmcnt(0) lgkmcnt(0)\n\t" \
    "s_barrier \n\t" \
    "v_add_u32 v178, 0x0, v177\n\t" \
    "v_xor_b32 v179, 32, v178\n\t" \
    "ds_read_b64_tr_b16 v[40:41], v178 offset:0\n\t" \
    "ds_read_b64_tr_b16 v[42:43], v178 offset:2048\n\t" \
    "ds_read_b64_tr_b16 v[120:121], v179 offset:0\n\t" \
    "ds_read_b64_tr_b16 v[122:123], v179 offset:2048\n\t" \
    "ds_read_b64_tr_b16 v[44:45], v178 offset:16384\n\t" \
    "ds_read_b64_tr_b16 v[46:47], v178 offset:18432\n\t" \
    "ds_read_b64_tr_b16 v[124:125], v179 offset:16384\n\t" \
    "ds_read_b64_tr_b16 v[126:127], v179 offset:18432\n\t" \
    "v_add_u32 v178, 0x8000, v177\n\t" \
    "v_xor_b32 v179, 32, v178\n\t" \
    "ds_read_b64_tr_b16 v[48:49], v178 offset:0\n\t" \
    "ds_read_b64_tr_b16 v[50:51], v178 offset:2048\n\t" \
    "ds_read_b64_tr_b16 v[128:129], v179 offset:0\n\t" \
    "ds_read_b64_tr_b16 v[130:131], v179 offset:2048\n\t" \
    "ds_read_b64_tr_b16 v[52:53], v178 offset:16384\n\t" \
    "ds_read_b64_tr_b16 v[54:55], v178 offset:18432\n\t" \
    "ds_read_b64_tr_b16 v[132:133], v179 offset:16384\n\t" \
    "ds_read_b64_tr_b16 v[134:135], v179 offset:18432\n\t" \
    "v_add_u32 v178, 0x10000, v177\n\t" \
    "v_xor_b32 v179, 32, v178\n\t" \
    "ds_read_b64_tr_b16 v[56:57], v178 offset:0\n\t" \
    "ds_read_b64_tr_b16 v[58:59], v178 offset:2048\n\t" \
    "ds_read_b64_tr_b16 v[136:137], v179 offset:0\n\t" \
    "ds_read_b64_tr_b16 v[138:139], v179 offset:2048\n\t" \
    "ds_read_b64_tr_b16 v[60:61], v178 offset:16384\n\t" \
    "ds_read_b64_tr_b16 v[62:63], v178 offset:18432\n\t" \
    "ds_read_b64_tr_b16 v[140:141], v179 offset:16384\n\t" \
    "ds_read_b64_tr_b16 v[142:143], v179 offset:18432\n\t" \
    "v_add_u32 v178, 0x18000, v177\n\t" \
    "v_xor_b32 v179, 32, v178\n\t" \
    "ds_read_b64_tr_b16 v[64:65], v178 offset:0\n\t" \
    "ds_read_b64_tr_b16 v[66:67], v178 offset:2048\n\t" \
    "ds_read_b64_tr_b16 v[144:145], v179 offset:0\n\t" \
    "ds_read_b64_tr_b16 v[146:147], v179 offset:2048\n\t" \
    "ds_read_b64_tr_b16 v[68:69], v178 offset:16384\n\t" \
    "ds_read_b64_tr_b16 v[70:71], v178 offset:18432\n\t" \
    "ds_read_b64_tr_b16 v[148:149], v179 offset:16384\n\t" \
    "ds_read_b64_tr_b16 v[150:151], v179 offset:18432\n\t" \
    "v_add_u32 v178, 0x20000, v177\n\t" \
    "v_xor_b32 v179, 32, v178\n\t" \
    "ds_read_b64_tr_b16 v[72:73], v178 offset:0\n\t" \
    "ds_read_b64_tr_b16 v[74:75], v178 offset:2048\n\t" \
    "ds_read_b64_tr_b16 v[152:153], v179 offset:0\n\t" \
    "ds_read_b64_tr_b16 v[154:155], v179 offset:2048\n\t" \
    "ds_read_b64_tr_b16 v[76:77], v178 offset:16384\n\t" \
    "ds_read_b64_tr_b16 v[78:79], v178 offset:18432\n\t" \
    "ds_read_b64_tr_b16 v[156:157], v179 offset:16384\n\t" \
    "ds_read_b64_tr_b16 v[158:159], v179 offset:18432\n\t" \
    "v_mov_b32 v228, 0x3f803f80\n\t" \
    "v_mov_b32 v229, 0x3f803f80\n\t" \
    "v_mov_b32 v230, 0x3f803f80\n\t" \
    "v_mov_b32 v231, 0x3f803f80\n\t" \
    "v_mov_b64 v[216:217], 0\n\t" \
    "v_mov_b32 v219, 0\n\t" \
    "v_bfe_u32 v255, v253, 2, 5\n\t" \
    "v_add_u32 v255, 0x7000, v255\n\t" \
    "v_bfe_u32 v218, v253, 6, 1\n\t" \
    "s_waitcnt lgkmcnt(0)\n\t" \
    "s_barrier \n\t" \
    "ds_write_b32 v251, v228 offset:72\n\t" \
    "ds_write_b32 v251, v228 offset:2120\n\t" \
    "v_mfma_f32_16x16x32_bf16 v[160:163], v[0:3], v[228:231], 0\n\t" \
    "v_mfma_f32_16x16x32_bf16 v[164:167], v[80:83], v[228:231], 0\n\t" \
    "v_mfma_f32_16x16x32_bf16 v[160:163], v[4:7], v[228:231], v[160:163]\n\t" \
    "v_mfma_f32_16x16x32_bf16 v[164:167], v[84:87], v[228:231], v[164:167]\n\t" \
    "v_mfma_f32_16x16x32_bf16 v[160:163], v[8:11], v[228:231], v[160:163]\n\t" \
    "v_mfma_f32_16x16x32_bf16 v[164:167], v[88:91], v[228:231], v[164:167]\n\t" \
    "v_mfma_f32_16x16x32_bf16 v[160:163], v[12:15], v[228:231], v[160:163]\n\t" \
    "v_mfma_f32_16x16x32_bf16 v[164:167], v[92:95], v[228:231], v[164:167]\n\t" \
    "v_mfma_f32_16x16x32_bf16 v[160:163], v[16:19], v[228:231], v[160:163]\n\t" \
    "v_mfma_f32_16x16x32_bf16 v[164:167], v[96:99], v[228:231], v[164:167]\n\t" \
    "v_mfma_f32_16x16x32_bf16 v[160:163], v[20:23], v[228:231], v[160:163]\n\t" \
    "v_mfma_f32_16x16x32_bf16 v[164:167], v[100:103], v[228:231], v[164:167]\n\t" \
    "v_mfma_f32_16x16x32_bf16 v[160:163], v[24:27], v[228:231], v[160:163]\n\t" \
    "v_mfma_f32_16x16x32_bf16 v[164:167], v[104:107], v[228:231], v[164:167]\n\t" \
    "v_mfma_f32_16x16x32_bf16 v[160:163], v[28:31], v[228:231], v[160:163]\n\t" \
    "v_mfma_f32_16x16x32_bf16 v[164:167], v[108:111], v[228:231], v[164:167]\n\t" \
    "v_mfma_f32_16x16x32_bf16 v[160:163], v[32:35], v[228:231], v[160:163]\n\t" \
    "v_mfma_f32_16x16x32_bf16 v[164:167], v[112:115], v[228:231], v[164:167]\n\t" \
    "v_mfma_f32_16x16x32_bf16 v[160:163], v[36:39], v[228:231], v[160:163]\n\t" \
    "v_mfma_f32_16x16x32_bf16 v[164:167], v[116:119], v[228:231], v[164:167]\n\t" \
    "v_mfma_f32_16x16x32_bf16 v[160:163], v[40:43], v[228:231], v[160:163]\n\t" \
    "v_mfma_f32_16x16x32_bf16 v[164:167], v[120:123], v[228:231], v[164:167]\n\t" \
    "v_mfma_f32_16x16x32_bf16 v[160:163], v[44:47], v[228:231], v[160:163]\n\t" \
    "v_mfma_f32_16x16x32_bf16 v[164:167], v[124:127], v[228:231], v[164:167]\n\t" \
    "v_mfma_f32_16x16x32_bf16 v[160:163], v[48:51], v[228:231], v[160:163]\n\t" \
    "v_mfma_f32_16x16x32_bf16 v[164:167], v[128:131], v[228:231], v[164:167]\n\t" \
    "v_mfma_f32_16x16x32_bf16 v[160:163], v[52:55], v[228:231], v[160:163]\n\t" \
    "v_mfma_f32_16x16x32_bf16 v[164:167], v[132:135], v[228:231], v[164:167]\n\t" \
    "v_mfma_f32_16x16x32_bf16 v[160:163], v[56:59], v[228:231], v[160:163]\n\t" \
    "v_mfma_f32_16x16x32_bf16 v[164:167], v[136:139], v[228:231], v[164:167]\n\t" \
    "v_mfma_f32_16x16x32_bf16 v[160:163], v[60:63], v[228:231], v[160:163]\n\t" \
    "v_mfma_f32_16x16x32_bf16 v[164:167], v[140:143], v[228:231], v[164:167]\n\t" \
    "v_mfma_f32_16x16x32_bf16 v[160:163], v[64:67], v[228:231], v[160:163]\n\t" \
    "v_mfma_f32_16x16x32_bf16 v[164:167], v[144:147], v[228:231], v[164:167]\n\t" \
    "v_mfma_f32_16x16x32_bf16 v[160:163], v[68:71], v[228:231], v[160:163]\n\t" \
    "v_mfma_f32_16x16x32_bf16 v[164:167], v[148:151], v[228:231], v[164:167]\n\t" \
    "v_mfma_f32_16x16x32_bf16 v[160:163], v[72:75], v[228:231], v[160:163]\n\t" \
    "v_mfma_f32_16x16x32_bf16 v[164:167], v[152:155], v[228:231], v[164:167]\n\t" \
    "v_mfma_f32_16x16x32_bf16 v[160:163], v[76:79], v[228:231], v[160:163]\n\t" \
    "v_mfma_f32_16x16x32_bf16 v[164:167], v[156:159], v[228:231], v[164:167]\n\t" \
    "s_mov_b32 s52, 0x22222222\n\t" \
    "s_mov_b32 s53, 0x22222222\n\t" \
    "s_mov_b32 s54, 0x44444444\n\t" \
    "s_mov_b32 s55, 0x44444444\n\t" \
    "s_mov_b32 s56, 0x88888888\n\t" \
    "s_mov_b32 s57, 0x88888888\n\t" \
    "ds_write_b32 v255, v218\n\t" \
    "s_waitcnt lgkmcnt(0)\n\t" \
    "s_barrier \n\t" \
    ".Lw31_%=:\n\t" \
    "v_mov_b32 v248, 0\n\t" \
    "buffer_load_dword v248, v248, %[rs], %[go] offen sc1\n\t" \
    "s_waitcnt vmcnt(0)\n\t" \
    "v_cmp_ne_u32 vcc, 0, v248\n\t" \
    "s_cbranch_vccnz .Lw32_%=\n\t" \
    "s_cmp_eq_u32 s41, 0\n\t" \
    "s_cbranch_scc1 .Lw32_%=\n\t" \
    "s_sub_u32 s41, s41, 1\n\t" \
    "s_sleep 16\n\t" \
    "s_branch .Lw31_%=\n\t" \
    ".Lw32_%=:\n\t" \
    "s_mov_b32 s43, 0\n\t" \
    "s_add_u32 s46, s43, %[pi]\n\t" \
    "s_cmp_gt_u32 %[wave], 3\n\t" \
    "s_cbranch_scc1 .Lw33_%=\n\t" \
    "s_add_u32 s47, s46, 0x0\n\t" \
    "buffer_load_dwordx4 v[208:211], v253, %[rs], s47 offen sc1\n\t" \
    "s_add_u32 s47, s46, 0x4000\n\t" \
    "buffer_load_dwordx4 v[212:215], v253, %[rs], s47 offen sc1\n\t" \
    "s_add_u32 s47, s46, 0x8000\n\t" \
    "buffer_load_dwordx4 v[216:219], v253, %[rs], s47 offen sc1\n\t" \
    "s_add_u32 s47, s46, 0xc000\n\t" \
    "buffer_load_dwordx4 v[220:223], v253, %[rs], s47 offen sc1\n\t" \
    "s_add_u32 s47, s46, 0x10000\n\t" \
    "buffer_load_dwordx4 v[224:227], v253, %[rs], s47 offen sc1\n\t" \
    ".Lw33_%=:\n\t" \
    "s_waitcnt vmcnt(0)\n\t" \
    ".Lw34_%=:\n\t" \
    "s_add_i32 s44, s40, 0\n\t" \
    "s_max_i32 s44, s44, 0\n\t" \
    "s_lshr_b32 s45, s44, 1\n\t" \
    "s_and_b32 s44, s44, 1\n\t" \
    "s_lshl_b32 s44, s44, 12\n\t" \
    "s_mul_i32 s49, s45, 0x30000\n\t" \
    "s_add_u32 s49, s49, s44\n\t" \
    "s_lshr_b32 s45, s45, 3\n\t" \
    "s_mul_i32 s45, s45, 0x82000\n\t" \
    "s_add_u32 s49, s49, s45\n\t" \
    "s_add_u32 s49, s49, %[ng]\n\t" \
    "s_add_i32 s44, s40, 1\n\t" \
    "s_max_i32 s44, s44, 0\n\t" \
    "s_lshr_b32 s45, s44, 1\n\t" \
    "s_and_b32 s44, s44, 1\n\t" \
    "s_lshl_b32 s44, s44, 12\n\t" \
    "s_mul_i32 s43, s45, 0x14000\n\t" \
    "s_add_u32 s43, s43, s44\n\t" \
    "s_lshr_b32 s45, s45, 4\n\t" \
    "s_mul_i32 s45, s45, 0xc2000\n\t" \
    "s_add_u32 s43, s43, s45\n\t" \
    "s_cmp_gt_u32 %[wave], 3\n\t" \
    "s_cbranch_scc1 .Lw37_%=\n\t" \
    "s_setprio 2\n\t" \
    "v_max_u32 v248, v208, v209\n\t" \
    "v_max3_u32 v248, v210, v211, v248\n\t" \
    "v_max3_u32 v248, v212, v213, v248\n\t" \
    "v_max3_u32 v248, v214, v215, v248\n\t" \
    "v_max3_u32 v248, v216, v217, v248\n\t" \
    "v_max3_u32 v248, v218, v219, v248\n\t" \
    "v_max3_u32 v248, v220, v221, v248\n\t" \
    "v_max3_u32 v248, v222, v223, v248\n\t" \
    "v_max3_u32 v248, v224, v225, v248\n\t" \
    "v_max3_u32 v248, v226, v227, v248\n\t" \
    "v_cmp_eq_u32 vcc, -1, v248\n\t" \
    "s_cbranch_vccz .Lw36_%=\n\t" \
    ".Lw35_%=:\n\t" \
    "v_max_u32 v248, v208, v209\n\t" \
    "v_max3_u32 v248, v210, v211, v248\n\t" \
    "v_cmp_eq_u32 vcc, -1, v248\n\t" \
    "s_cbranch_vccz .Lw38_%=\n\t" \
    "s_add_u32 s47, s46, 0x0\n\t" \
    "buffer_load_dwordx4 v[208:211], v253, %[rs], s47 offen sc1\n\t" \
    ".Lw38_%=:\n\t" \
    "v_max_u32 v248, v212, v213\n\t" \
    "v_max3_u32 v248, v214, v215, v248\n\t" \
    "v_cmp_eq_u32 vcc, -1, v248\n\t" \
    "s_cbranch_vccz .Lw39_%=\n\t" \
    "s_add_u32 s47, s46, 0x4000\n\t" \
    "buffer_load_dwordx4 v[212:215], v253, %[rs], s47 offen sc1\n\t" \
    ".Lw39_%=:\n\t" \
    "v_max_u32 v248, v216, v217\n\t" \
    "v_max3_u32 v248, v218, v219, v248\n\t" \
    "v_cmp_eq_u32 vcc, -1, v248\n\t" \
    "s_cbranch_vccz .Lw310_%=\n\t" \
    "s_add_u32 s47, s46, 0x8000\n\t" \
    "buffer_load_dwordx4 v[216:219], v253, %[rs], s47 offen sc1\n\t" \
    ".Lw310_%=:\n\t" \
    "v_max_u32 v248, v220, v221\n\t" \
    "v_max3_u32 v248, v222, v223, v248\n\t" \
    "v_cmp_eq_u32 vcc, -1, v248\n\t" \
    "s_cbranch_vccz .Lw311_%=\n\t" \
    "s_add_u32 s47, s46, 0xc000\n\t" \
    "buffer_load_dwordx4 v[220:223], v253, %[rs], s47 offen sc1\n\t" \
    ".Lw311_%=:\n\t" \
    "v_max_u32 v248, v224, v225\n\t" \
    "v_max3_u32 v248, v226, v227, v248\n\t" \
    "v_cmp_eq_u32 vcc, -1, v248\n\t" \
    "s_cbranch_vccz .Lw312_%=\n\t" \
    "s_add_u32 s47, s46, 0x10000\n\t" \
    "buffer_load_dwordx4 v[224:227], v253, %[rs], s47 offen sc1\n\t" \
    ".Lw312_%=:\n\t" \
    "s_waitcnt vmcnt(0)\n\t" \
    "v_max_u32 v248, v208, v209\n\t" \
    "v_max3_u32 v248, v210, v211, v248\n\t" \
    "v_max3_u32 v248, v212, v213, v248\n\t" \
    "v_max3_u32 v248, v214, v215, v248\n\t" \
    "v_max3_u32 v248, v216, v217, v248\n\t" \
    "v_max3_u32 v248, v218, v219, v248\n\t" \
    "v_max3_u32 v248, v220, v221, v248\n\t" \
    "v_max3_u32 v248, v222, v223, v248\n\t" \
    "v_max3_u32 v248, v224, v225, v248\n\t" \
    "v_max3_u32 v248, v226, v227, v248\n\t" \
    "v_cmp_eq_u32 vcc, -1, v248\n\t" \
    "s_cbranch_vccz .Lw36_%=\n\t" \
    "s_cmp_eq_u32 s41, 0\n\t" \
    "s_cbranch_scc1 .Lw36_%=\n\t" \
    "s_sub_u32 s41, s41, 1\n\t" \
    "s_branch .Lw35_%=\n\t" \
    ".Lw36_%=:\n\t" \
    "ds_write_b128 v250, v[208:211] offset:0\n\t" \
    "v_mfma_f32_16x16x32_bf16 v[236:239], v[208:211], v[208:211], 0\n\t" \
    "v_mfma_f32_16x16x32_bf16 v[232:235], v[228:231], v[208:211], 0\n\t" \
    "ds_write_b128 v250, v[212:215] offset:4096\n\t" \
    "v_mfma_f32_16x16x32_bf16 v[236:239], v[212:215], v[212:215], v[236:239]\n\t" \
    "v_mfma_f32_16x16x32_bf16 v[232:235], v[228:231], v[212:215], v[232:235]\n\t" \
    "ds_write_b128 v250, v[216:219] offset:8192\n\t" \
    "v_mfma_f32_16x16x32_bf16 v[236:239], v[216:219], v[216:219], v[236:239]\n\t" \
    "v_mfma_f32_16x16x32_bf16 v[232:235], v[228:231], v[216:219], v[232:235]\n\t" \
    "ds_write_b128 v250, v[220:223] offset:12288\n\t" \
    "v_mfma_f32_16x16x32_bf16 v[236:239], v[220:223], v[220:223], v[236:239]\n\t" \
    "v_mfma_f32_16x16x32_bf16 v[232:235], v[228:231], v[220:223], v[232:235]\n\t" \
    "ds_write_b128 v250, v[224:227] offset:16384\n\t" \
    "v_mfma_f32_16x16x32_bf16 v[236:239], v[224:227], v[224:227], v[236:239]\n\t" \
    "v_mfma_f32_16x16x32_bf16 v[232:235], v[228:231], v[224:227], v[232:235]\n\t" \
    "s_add_u32 s46, s43, %[pi]\n\t" \
    "s_nop 4\n\t" \
    "v_cndmask_b32 v236, v236, v237, s[52:53]\n\t" \
    "v_cndmask_b32 v236, v236, v238, s[54:55]\n\t" \
    "v_cndmask_b32 v236, v236, v239, s[56:57]\n\t" \
    "s_mov_b32 exec_lo, 0xf0000f\n\t" \
    "s_mov_b32 exec_hi, 0xf0000f00\n\t" \
    "ds_write2_b32 v252, v232, v236 offset1:1\n\t" \
    "s_mov_b64 exec, -1\n\t" \
    "s_add_u32 s44, s40, 1\n\t" \
    "v_mov_b32 v248, s44\n\t" \
    "s_lshl_b64 exec, 1, %[wave]\n\t" \
    "ds_write_b32 v255, v248\n\t" \
    "s_mov_b64 exec, -1\n\t" \
    ".Lw37_%=:\n\t" \
    "ds_read_b32 v248, v255\n\t" \
    "s_waitcnt lgkmcnt(0)\n\t" \
    "v_cmp_ge_u32 vcc, s40, v248\n\t" \
    "s_cbranch_vccz .Lw313_%=\n\t" \
    "s_cmp_lt_u32 %[wave], 4\n\t" \
    "s_cbranch_scc1 .Lw37_%=\n\t" \
    "s_sleep 1\n\t" \
    "s_branch .Lw37_%=\n\t" \
    ".Lw313_%=:\n\t" \
    "s_setprio 0\n\t" \
    "s_cmp_lg_u32 %[wave], 4\n\t" \
    "s_cbranch_scc1 .Lw314_%=\n\t" \
    "s_setprio 1\n\t" \
    "ds_read_b128 v[232:235], v251 offset:0\n\t" \
    "ds_read_b128 v[236:239], v251 offset:16\n\t" \
    "s_waitcnt lgkmcnt(0)\n\t" \
    "v_add_f32 v232, v232, v236\n\t" \
    "v_add_f32 v233, v233, v237\n\t" \
    "v_add_f32 v234, v234, v238\n\t" \
    "v_add_f32 v235, v235, v239\n\t" \
    "v_add_f32 v232, v232, v234\n\t" \
    "v_add_f32 v233, v233, v235\n\t" \
    "v_mul_f32 v232, 0x3acccccd, v232\n\t" \
    "v_mul_f32 v233, 0x3acccccd, v233\n\t" \
    "v_fma_f32 v233, -v232, v232, v233\n\t" \
    "v_mov_b32 v234, v232\n\t" \
    "v_max_f32 v233, 0, v233\n\t" \
    "v_cvt_pk_bf16_f32 v234, v234, v234\n\t" \
    "v_and_b32 v234, 0xffff0000, v234\n\t" \
    "v_mov_b32 v236, v233\n\t" \
    "v_cvt_pk_bf16_f32 v236, v236, v236\n\t" \
    "v_and_b32 v236, 0xffff0000, v236\n\t" \
    "v_add_f32 v236, 0x3727c5ac, v236\n\t" \
    "v_cvt_pk_bf16_f32 v236, v236, v236\n\t" \
    "v_and_b32 v236, 0xffff0000, v236\n\t" \
    "v_sqrt_f32 v236, v236\n\t" \
    "s_nop 0\n\t" \
    "v_cvt_pk_bf16_f32 v236, v236, v236\n\t" \
    "v_and_b32 v236, 0xffff0000, v236\n\t" \
    "v_rcp_f32 v235, v236\n\t" \
    "s_nop 0\n\t" \
    "v_mov_b32 v237, v235\n\t" \
    "v_mul_f32 v238, -v234, v235\n\t" \
    "v_mov_b32 v239, s40\n\t" \
    "ds_write2_b32 v251, v237, v238 offset0:16 offset1:17\n\t" \
    "ds_write_b32 v251, v239 offset:72\n\t" \
    "s_setprio 0\n\t" \
    ".Lw314_%=:\n\t" \
    "ds_read_b128 v[176:179], v249 offset:0\n\t" \
    "ds_read_b128 v[180:183], v249 offset:1024\n\t" \
    "ds_read_b128 v[184:187], v249 offset:2048\n\t" \
    "ds_read_b128 v[188:191], v249 offset:3072\n\t" \
    "ds_read_b128 v[192:195], v249 offset:4096\n\t" \
    "ds_read_b128 v[196:199], v249 offset:5120\n\t" \
    "ds_read_b128 v[200:203], v249 offset:6144\n\t" \
    "s_waitcnt lgkmcnt(6)\n\t" \
    "v_mfma_f32_16x16x32_bf16 v[168:171], v[0:3], v[176:179], 0\n\t" \
    "ds_read_b128 v[204:207], v249 offset:7168\n\t" \
    "v_mfma_f32_16x16x32_bf16 v[172:175], v[80:83], v[176:179], 0\n\t" \
    "s_waitcnt lgkmcnt(6)\n\t" \
    "v_mfma_f32_16x16x32_bf16 v[168:171], v[4:7], v[180:183], v[168:171]\n\t" \
    "ds_read_b128 v[176:179], v249 offset:8192\n\t" \
    "v_mfma_f32_16x16x32_bf16 v[172:175], v[84:87], v[180:183], v[172:175]\n\t" \
    "s_waitcnt lgkmcnt(6)\n\t" \
    "v_mfma_f32_16x16x32_bf16 v[168:171], v[8:11], v[184:187], v[168:171]\n\t" \
    "ds_read_b128 v[180:183], v249 offset:9216\n\t" \
    "v_mfma_f32_16x16x32_bf16 v[172:175], v[88:91], v[184:187], v[172:175]\n\t" \
    "s_waitcnt lgkmcnt(6)\n\t" \
    "v_mfma_f32_16x16x32_bf16 v[168:171], v[12:15], v[188:191], v[168:171]\n\t" \
    "ds_read_b128 v[184:187], v249 offset:10240\n\t" \
    "v_mfma_f32_16x16x32_bf16 v[172:175], v[92:95], v[188:191], v[172:175]\n\t" \
    "s_waitcnt lgkmcnt(6)\n\t" \
    "v_mfma_f32_16x16x32_bf16 v[168:171], v[16:19], v[192:195], v[168:171]\n\t" \
    "ds_read_b128 v[188:191], v249 offset:11264\n\t" \
    "v_mfma_f32_16x16x32_bf16 v[172:175], v[96:99], v[192:195], v[172:175]\n\t" \
    "s_waitcnt lgkmcnt(6)\n\t" \
    "v_mfma_f32_16x16x32_bf16 v[168:171], v[20:23], v[196:199], v[168:171]\n\t" \
    "ds_read_b128 v[192:195], v249 offset:12288\n\t" \
    "v_mfma_f32_16x16x32_bf16 v[172:175], v[100:103], v[196:199], v[172:175]\n\t" \
    "s_waitcnt lgkmcnt(6)\n\t" \
    "v_mfma_f32_16x16x32_bf16 v[168:171], v[24:27], v[200:203], v[168:171]\n\t" \
    "ds_read_b128 v[196:199], v249 offset:13312\n\t" \
    "v_mfma_f32_16x16x32_bf16 v[172:175], v[104:107], v[200:203], v[172:175]\n\t" \
    "s_waitcnt lgkmcnt(6)\n\t" \
    "v_mfma_f32_16x16x32_bf16 v[168:171], v[28:31], v[204:207], v[168:171]\n\t" \
    "ds_read_b128 v[200:203], v249 offset:14336\n\t" \
    "v_mfma_f32_16x16x32_bf16 v[172:175], v[108:111], v[204:207], v[172:175]\n\t" \
    "s_waitcnt lgkmcnt(6)\n\t" \
    "v_mfma_f32_16x16x32_bf16 v[168:171], v[32:35], v[176:179], v[168:171]\n\t" \
    "ds_read_b128 v[204:207], v249 offset:15360\n\t" \
    "v_mfma_f32_16x16x32_bf16 v[172:175], v[112:115], v[176:179], v[172:175]\n\t" \
    "s_waitcnt lgkmcnt(6)\n\t" \
    "v_mfma_f32_16x16x32_bf16 v[168:171], v[36:39], v[180:183], v[168:171]\n\t" \
    "ds_read_b128 v[176:179], v249 offset:16384\n\t" \
    "v_mfma_f32_16x16x32_bf16 v[172:175], v[116:119], v[180:183], v[172:175]\n\t" \
    "s_waitcnt lgkmcnt(6)\n\t" \
    "v_mfma_f32_16x16x32_bf16 v[168:171], v[40:43], v[184:187], v[168:171]\n\t" \
    "ds_read_b128 v[180:183], v249 offset:17408\n\t" \
    "v_mfma_f32_16x16x32_bf16 v[172:175], v[120:123], v[184:187], v[172:175]\n\t" \
    "s_waitcnt lgkmcnt(6)\n\t" \
    "v_mfma_f32_16x16x32_bf16 v[168:171], v[44:47], v[188:191], v[168:171]\n\t" \
    "ds_read_b128 v[184:187], v249 offset:18432\n\t" \
    "v_mfma_f32_16x16x32_bf16 v[172:175], v[124:127], v[188:191], v[172:175]\n\t" \
    "s_waitcnt lgkmcnt(6)\n\t" \
    "v_mfma_f32_16x16x32_bf16 v[168:171], v[48:51], v[192:195], v[168:171]\n\t" \
    "ds_read_b128 v[188:191], v249 offset:19456\n\t" \
    "v_mfma_f32_16x16x32_bf16 v[172:175], v[128:131], v[192:195], v[172:175]\n\t" \
    "s_waitcnt lgkmcnt(6)\n\t" \
    "v_mfma_f32_16x16x32_bf16 v[168:171], v[52:55], v[196:199], v[168:171]\n\t" \
    "v_mfma_f32_16x16x32_bf16 v[172:175], v[132:135], v[196:199], v[172:175]\n\t" \
    "s_waitcnt lgkmcnt(5)\n\t" \
    "v_mfma_f32_16x16x32_bf16 v[168:171], v[56:59], v[200:203], v[168:171]\n\t" \
    "v_mfma_f32_16x16x32_bf16 v[172:175], v[136:139], v[200:203], v[172:175]\n\t" \
    "s_cmp_gt_u32 %[wave], 3\n\t" \
    "s_cbranch_scc1 .Lw316_%=\n\t" \
    "s_add_u32 s47, s46, 0x0\n\t" \
    "buffer_load_dwordx4 v[208:211], v253, %[rs], s47 offen sc1\n\t" \
    "s_add_u32 s47, s46, 0x4000\n\t" \
    "buffer_load_dwordx4 v[212:215], v253, %[rs], s47 offen sc1\n\t" \
    "s_add_u32 s47, s46, 0x8000\n\t" \
    "buffer_load_dwordx4 v[216:219], v253, %[rs], s47 offen sc1\n\t" \
    "s_add_u32 s47, s46, 0xc000\n\t" \
    "buffer_load_dwordx4 v[220:223], v253, %[rs], s47 offen sc1\n\t" \
    "s_add_u32 s47, s46, 0x10000\n\t" \
    "buffer_load_dwordx4 v[224:227], v253, %[rs], s47 offen sc1\n\t" \
    ".Lw316_%=:\n\t" \
    "s_waitcnt lgkmcnt(4)\n\t" \
    "v_mfma_f32_16x16x32_bf16 v[168:171], v[60:63], v[204:207], v[168:171]\n\t" \
    "v_mfma_f32_16x16x32_bf16 v[172:175], v[140:143], v[204:207], v[172:175]\n\t" \
    "ds_read_b32 v236, v251 offset:72\n\t" \
    "ds_read_b64 v[238:239], v251 offset:64\n\t" \
    "s_waitcnt lgkmcnt(5)\n\t" \
    "v_mfma_f32_16x16x32_bf16 v[168:171], v[64:67], v[176:179], v[168:171]\n\t" \
    "v_mfma_f32_16x16x32_bf16 v[172:175], v[144:147], v[176:179], v[172:175]\n\t" \
    "s_waitcnt lgkmcnt(4)\n\t" \
    "v_mfma_f32_16x16x32_bf16 v[168:171], v[68:71], v[180:183], v[168:171]\n\t" \
    "v_mfma_f32_16x16x32_bf16 v[172:175], v[148:151], v[180:183], v[172:175]\n\t" \
    ".Lw317_%=:\n\t" \
    "s_waitcnt lgkmcnt(0)\n\t" \
    "v_cmp_ne_u32 vcc, s40, v236\n\t" \
    "s_cbranch_vccz .Lw318_%=\n\t" \
    "ds_read_b32 v236, v251 offset:72\n\t" \
    "ds_read_b64 v[238:239], v251 offset:64\n\t" \
    "s_branch .Lw317_%=\n\t" \
    ".Lw318_%=:\n\t" \
    "s_waitcnt lgkmcnt(0)\n\t" \
    "v_mul_f32 v240, v160, v239\n\t" \
    "v_mul_f32 v241, v161, v239\n\t" \
    "v_mfma_f32_16x16x32_bf16 v[168:171], v[72:75], v[184:187], v[168:171]\n\t" \
    "v_mul_f32 v242, v162, v239\n\t" \
    "v_mul_f32 v243, v163, v239\n\t" \
    "v_mfma_f32_16x16x32_bf16 v[172:175], v[152:155], v[184:187], v[172:175]\n\t" \
    "v_mul_f32 v244, v164, v239\n\t" \
    "v_mul_f32 v245, v165, v239\n\t" \
    "v_mfma_f32_16x16x32_bf16 v[168:171], v[76:79], v[188:191], v[168:171]\n\t" \
    "v_mul_f32 v246, v166, v239\n\t" \
    "v_mul_f32 v247, v167, v239\n\t" \
    "v_mfma_f32_16x16x32_bf16 v[172:175], v[156:159], v[188:191], v[172:175]\n\t" \
    "v_mov_b32 v237, 0xc0135761\n\t" \
    "s_cmp_lt_u32 %[wave], 4\n\t" \
    "s_cbranch_scc1 .Lw319_%=\n\t" \
    "s_add_u32 s44, s40, 2\n\t" \
    "v_mov_b32 v248, s44\n\t" \
    "s_lshl_b64 exec, 1, %[wave]\n\t" \
    "ds_write_b32 v255, v248\n\t" \
    "s_mov_b64 exec, -1\n\t" \
    ".Lw319_%=:\n\t" \
    "s_nop 1\n\t" \
    "s_nop 2\n\t" \
    "v_fma_f32 v168, v168, v238, v240\n\t" \
    "v_fma_f32 v169, v169, v238, v241\n\t" \
    "v_fma_f32 v170, v170, v238, v242\n\t" \
    "v_fma_f32 v171, v171, v238, v243\n\t" \
    "v_fma_f32 v172, v172, v238, v244\n\t" \
    "v_fma_f32 v173, v173, v238, v245\n\t" \
    "v_fma_f32 v174, v174, v238, v246\n\t" \
    "v_fma_f32 v175, v175, v238, v247\n\t" \
    "v_mul_f32 v176, v168, v168\n\t" \
    "v_mul_f32 v177, v169, v169\n\t" \
    "v_mul_f32 v178, v170, v170\n\t" \
    "v_mul_f32 v179, v171, v171\n\t" \
    "v_mul_f32 v180, v172, v172\n\t" \
    "v_mul_f32 v181, v173, v173\n\t" \
    "v_mul_f32 v182, v174, v174\n\t" \
    "v_mul_f32 v183, v175, v175\n\t" \
    "v_fmamk_f32 v176, v176, 0xbdd2d3e7, v237\n\t" \
    "v_fmamk_f32 v177, v177, 0xbdd2d3e7, v237\n\t" \
    "v_fmamk_f32 v178, v178, 0xbdd2d3e7, v237\n\t" \
    "v_fmamk_f32 v179, v179, 0xbdd2d3e7, v237\n\t" \
    "v_fmamk_f32 v180, v180, 0xbdd2d3e7, v237\n\t" \
    "v_fmamk_f32 v181, v181, 0xbdd2d3e7, v237\n\t" \
    "v_fmamk_f32 v182, v182, 0xbdd2d3e7, v237\n\t" \
    "v_fmamk_f32 v183, v183, 0xbdd2d3e7, v237\n\t" \
    "v_mul_f32 v176, v168, v176\n\t" \
    "v_mul_f32 v177, v169, v177\n\t" \
    "v_mul_f32 v178, v170, v178\n\t" \
    "v_mul_f32 v179, v171, v179\n\t" \
    "v_mul_f32 v180, v172, v180\n\t" \
    "v_mul_f32 v181, v173, v181\n\t" \
    "v_mul_f32 v182, v174, v182\n\t" \
    "v_mul_f32 v183, v175, v183\n\t" \
    "v_exp_f32 v176, v176\n\t" \
    "v_exp_f32 v177, v177\n\t" \
    "v_exp_f32 v178, v178\n\t" \
    "v_exp_f32 v179, v179\n\t" \
    "v_exp_f32 v180, v180\n\t" \
    "v_exp_f32 v181, v181\n\t" \
    "v_exp_f32 v182, v182\n\t" \
    "v_exp_f32 v183, v183\n\t" \
    "v_add_f32 v176, 1.0, v176\n\t" \
    "v_add_f32 v177, 1.0, v177\n\t" \
    "v_add_f32 v178, 1.0, v178\n\t" \
    "v_add_f32 v179, 1.0, v179\n\t" \
    "v_add_f32 v180, 1.0, v180\n\t" \
    "v_add_f32 v181, 1.0, v181\n\t" \
    "v_add_f32 v182, 1.0, v182\n\t" \
    "v_add_f32 v183, 1.0, v183\n\t" \
    "v_rcp_f32 v176, v176\n\t" \
    "v_rcp_f32 v177, v177\n\t" \
    "v_rcp_f32 v178, v178\n\t" \
    "v_rcp_f32 v179, v179\n\t" \
    "v_rcp_f32 v180, v180\n\t" \
    "v_rcp_f32 v181, v181\n\t" \
    "v_rcp_f32 v182, v182\n\t" \
    "v_rcp_f32 v183, v183\n\t" \
    "v_mul_f32 v168, v168, v176\n\t" \
    "v_mul_f32 v169, v169, v177\n\t" \
    "v_mul_f32 v170, v170, v178\n\t" \
    "v_mul_f32 v171, v171, v179\n\t" \
    "v_mul_f32 v172, v172, v180\n\t" \
    "v_mul_f32 v173, v173, v181\n\t" \
    "v_mul_f32 v174, v174, v182\n\t" \
    "v_mul_f32 v175, v175, v183\n\t" \
    "s_waitcnt vmcnt(0)\n\t" \
    "v_cvt_pk_bf16_f32 v184, v168, v169\n\t" \
    "v_cvt_pk_bf16_f32 v185, v170, v171\n\t" \
    "v_cvt_pk_bf16_f32 v186, v172, v173\n\t" \
    "v_cvt_pk_bf16_f32 v187, v174, v175\n\t" \
    "v_min_u32 v184, -2, v184\n\t" \
    "v_min_u32 v185, -2, v185\n\t" \
    "v_min_u32 v186, -2, v186\n\t" \
    "v_min_u32 v187, -2, v187\n\t" \
    "s_nop 0\n\t" \
    "v_permlane16_swap_b32 v184, v186\n\t" \
    "v_permlane16_swap_b32 v185, v187\n\t" \
    "buffer_store_dwordx4 v[184:187], v254, %[rs], s49 offen sc1\n\t" \
    "s_cmp_ge_u32 s40, 122\n\t" \
    "s_cbranch_scc1 .Lw320_%=\n\t" \
    "s_add_i32 s44, s40, 6\n\t" \
    "s_max_i32 s44, s44, 0\n\t" \
    "s_lshr_b32 s45, s44, 1\n\t" \
    "s_and_b32 s44, s44, 1\n\t" \
    "s_lshl_b32 s44, s44, 12\n\t" \
    "s_mul_i32 s50, s45, 0x30000\n\t" \
    "s_add_u32 s50, s50, s44\n\t" \
    "s_lshr_b32 s45, s45, 3\n\t" \
    "s_mul_i32 s45, s45, 0x82000\n\t" \
    "s_add_u32 s50, s50, s45\n\t" \
    "s_add_u32 s50, s50, %[ng]\n\t" \
    "v_mov_b32 v188, -1\n\t" \
    "v_mov_b32 v189, -1\n\t" \
    "v_mov_b32 v190, -1\n\t" \
    "v_mov_b32 v191, -1\n\t" \
    "buffer_store_dwordx4 v[188:191], v253, %[rs], s50 offen sc1\n\t" \
    ".Lw320_%=:\n\t" \
    "v_xor_b32 v249, 0x8000, v249\n\t" \
    "v_xor_b32 v250, 0x8000, v250\n\t" \
    "v_xor_b32 v251, 0x800, v251\n\t" \
    "v_xor_b32 v252, 0x800, v252\n\t" \
    "s_add_u32 s40, s40, 1\n\t" \
    "s_cmp_lg_u32 s40, 128\n\t" \
    "s_cbranch_scc1 .Lw34_%=\n\t" \
    "s_branch .Lend%=\n\t" \
    ".Lend%=:\n\t" \
    "s_waitcnt vmcnt(0) lgkmcnt(0)\n\t" \
    "s_mov_b32 %[bud], s41\n\t"
using u32x8 = __attribute__((ext_vector_type(8))) unsigned;
using u32x16 = __attribute__((ext_vector_type(16))) unsigned;
using u32x32 = __attribute__((ext_vector_type(32))) unsigned;
__device__ __forceinline__ u32x32 cat32(u32x8 a, u32x8 b, u32x8 c, u32x8 d) {
    const u32x16 lo = __builtin_shufflevector(a, b, 0, 1, 2, 3, 4, 5, 6, 7, 8, 9, 10, 11, 12, 13, 14, 15);
    const u32x16 hi = __builtin_shufflevector(c, d, 0, 1, 2, 3, 4, 5, 6, 7, 8, 9, 10, 11, 12, 13, 14, 15);
    return __builtin_shufflevector(lo, hi, 0, 1, 2, 3, 4, 5, 6, 7, 8, 9, 10, 11, 12, 13, 14, 15, 16, 17, 18, 19, 20, 21, 22, 23, 24, 25, 26, 27, 28, 29, 30, 31);
}
__device__ __forceinline__ unsigned c1_dst(int wave, int kind) {
    const int ln = lane_id(), c = ln & 15, g = ln >> 4, hf = (wave >> 1) & 1;
    if (kind == 2) {
        const unsigned b = L_KM + (c >> 2) * 256 + (wave & 1) * 128 + g * 32;
        return (b + hf * 8 + (c & 3) * 2) | (b + 1024 + hf * 1024 + (c & 3) * 4) << 16;
    }
    const unsigned b = L_Q + (c >> 3) * 256 + g * 64 + ((c >> 2) & 1) * 8;
    return (b + (wave & 1) * (kind ? 512 : 1536) + ((c >> 1) & 1) * 32 + hf * 4 + (c & 1) * 2) | (b + 1024 + (wave & 3) * 512 + (c & 1) * 32 + ((c >> 1) & 1) * 4) << 16;
}
__device__ __forceinline__ void dma1(rsrc_t rs, unsigned vo, unsigned so, unsigned dst) {
    asm volatile("s_mov_b32 m0, %3\n\ts_nop 0\n\tbuffer_load_dwordx4 %0, %1, %2 offen nt lds" :: "v"(vo), "s"(rs), "s"(so), "s"(dst) : "memory");
}
using s16x4 = __attribute__((ext_vector_type(4))) short;
__device__ __forceinline__ u32x4 tr_frag(unsigned a, unsigned step) {
    const u32x2 x = __builtin_bit_cast(u32x2, __builtin_amdgcn_ds_read_tr16_b64_v4i16((LAS s16x4*)(size_t)a)), y = __builtin_bit_cast(u32x2, __builtin_amdgcn_ds_read_tr16_b64_v4i16((LAS s16x4*)(size_t)(a + step)));
    return (u32x4){x[0], x[1], y[0], y[1]};
}
#define LOOP_CLOB "memory", "vcc", "scc", "v0", "v1", "v2", "v3", "v4", "v5", "v6", "v7", "v8", "v9", "v10", "v11", "v12", "v13", "v14", "v15", "v16", "v17", "v18", "v19", "v20", "v21", "v22", "v23", "v24", "v25", "v26", "v27", "v28", "v29", "v30", "v31", "v32", "v33", "v34", "v35", "v36", "v37", "v38", "v39", "v40", "v41", "v42", "v43", "v44", "v45", "v46", "v47", "v48", "v49", "v50", "v51", "v52", "v53", "v54", "v55", "v56", "v57", "v58", "v59", "v60", "v61", "v62", "v63", "v64", "v65", "v66", "v67", "v68", "v69", "v70", "v71", "v72", "v73", "v74", "v75", "v76", "v77", "v78", "v79", "v80", "v81", "v82", "v83", "v84", "v85", "v86", "v87", "v88", "v89", "v90", "v91", "v92", "v93", "v94", "v95", "v96", "v97", "v98", "v99", "v100", "v101", "v102", "v103", "v104", "v105", "v106", "v107", "v108", "v109", "v110", "v111", "v112", "v113", "v114", "v115", "v116", "v117", "v118", "v119", "v120", "v121", "v122", "v123", "v124", "v125", "v126", "v127", "v128", "v129", "v130", "v131", "v132", "v133", "v134", "v135", "v136", "v137", "v138", "v139", "v140", "v141", "v142", "v143", "v144", "v145", "v146", "v147", "v148", "v149", "v150", "v151", "v152", "v153", "v154", "v155", "v156", "v157", "v158", "v159", "v160", "v161", "v162", "v163", "v164", "v165", "v166", "v167", "v168", "v169", "v170", "v171", "v172", "v173", "v174", "v175", "v176", "v177", "v178", "v179", "v180", "v181", "v182", "v183", "v184", "v185", "v186", "v187", "v188", "v189", "v190", "v191", "v192", "v193", "v194", "v195", "v196", "v197", "v198", "v199", "v200", "v201", "v202", "v203", "v204", "v205", "v206", "v207", "v208", "v209", "v210", "v211", "v212", "v213", "v214", "v215", "v216", "v217", "v218", "v219", "v220", "v221", "v222", "v223", "v224", "v225", "v226", "v227", "v228", "v229", "v230", "v231", "v232", "v233", "v234", "v235", "v236", "v237", "v238", "v239", "v246", "v247", "v248", "v249", "v250", "v251", "v252", "s40", "s41", "s42", "s43", "s44", "s45", "s46", "s47", "s48", "s49", "s50", "s51", "s52", "s53", "s54", "s55", "s56", "s57", "s58", "s59", "s60", "s61", "s62", "s63", "s64", "s65", "s66", "s67", "s68", "s69", "s70", "s71", "s72", "s73", "s74", "s75", "s76", "s77", "s78", "s79"
__device__ __forceinline__ unsigned UNI(unsigned x) {
    unsigned r;
    asm volatile("s_nop 0\n\tv_readfirstlane_b32 %0, %1\n\ts_nop 4" : "=s"(r) : "v"(x));
    return r;
}
__device__ __forceinline__ void note_timeout(const Params& P, unsigned bud) {
    if (bud == 0) __builtin_amdgcn_raw_buffer_store_b32(1u, mkrsrc(P.ws + FL0, 512), 0x100, 0, 16);
}

#define LOOP_IO [bud] "=s"(bud), "+{v253}"(l16), "+{v240}"(W.a[0]), "+{v241}"(W.a[1]), "+{v242}"(W.a[2]), "+{v243}"(W.a[3]), "+{v244}"(W.a[4]), "+{v245}"(W.a[5])
#define LOOP_IN "{v254}"(b2), "{v255}"(dsto), [wave] "s"(wave), [ws] "s"(P.ws), [mo] "s"(FL_MB + blockIdx.x * 128 + wave * 4), [sz] "s"(RG_L), [lx] "s"(L << 13), [wr] "s"(wdesc(P, wave, isv, h)), [wm] "s"(UNI(wstage(wave, isv))), [fl] "s"(FL_XN + cpy * 2048), \
                [cj] "s"(REGS - 75 * WIN), [cs] "s"(5 * WIN), [mb] "s"(RG_XM + cpy * XM_COPY)
struct WLoad { unsigned a[6]; };
__device__ __forceinline__ rsrc_t wdesc(const Params& P, int wave, bool isv, int h) {
    const unsigned wo = (isv && wave >= 4) ? h * DH * DM + (wave & 3) * 160 : h * DH;
    typedef const __attribute__((address_space(4))) unsigned long long* kptr;
    static_assert(__builtin_offsetof(Params, wq) == 24 && __builtin_offsetof(Params, wo) == 48, "argument order");
    const unsigned long long w = ((kptr)__builtin_amdgcn_kernarg_segment_ptr())[3 + (isv ? 2 : 0) + (wave >> 2)];
    return mkrsrc((const bf16_t*)w + wo, (DM * HD - wo) * 2);
}
__device__ __forceinline__ unsigned wstage(int wave, bool isv) { return (isv && wave >= 4) ? L_W + (wave & 3) * 20480 : wave * 3072; }
template <bool START>
__device__ __forceinline__ void wload_start(WLoad& W, const Params& P, int wave, bool isv, int h) {
    const int ln = lane_id(), w4 = wave & 3;
    const rsrc_t wr = wdesc(P, wave, isv, h);
    const unsigned wm = UNI(wstage(wave, isv));
    if (isv && wave >= 4) {
#pragma unroll
        for (int k = 0; k < 5; ++k) { const unsigned u = k * 64 + ln; W.a[k] = (u / 20) * (DM * 2) + (u % 20) * 16 - (k & 3) * 1024; }
#pragma unroll 1
        for (int k = 0; k < (START ? 4 : 0); ++k)
            asm volatile("s_mov_b32 m0, %7\n\ts_nop 0\n\tbuffer_load_dwordx4 %0, %5, %6 offen nt lds\n\tbuffer_load_dwordx4 %1, %5, %6 offen offset:1024 nt lds\n\tbuffer_load_dwordx4 %2, %5, %6 offen offset:2048 nt lds\n\t"
                         "buffer_load_dwordx4 %3, %5, %6 offen offset:3072 nt lds\n\ts_mov_b32 m0, %8\n\ts_nop 0\n\tbuffer_load_dwordx4 %4, %5, %6 offen nt lds"
                         :: "v"(W.a[0]), "v"(W.a[1]), "v"(W.a[2]), "v"(W.a[3]), "v"(W.a[4]), "s"(wr), "s"(k * 20480), "s"(wm + k * 5120), "s"(wm + k * 5120 + 4096) : "memory");
        W.a[5] = L_W + w4 * 20480 + (ln >> 2) * 320 + (ln & 3) * 8;
    } else {
        const unsigned sl = isv ? 12288 : 24576;
#pragma unroll
        for (int k = 0; k < 3; ++k) { const unsigned u = (w4 * 3 + k) * 64 + ln; W.a[k] = (u / 24) * (HD * 2) + (u % 24) * 16 - k * 1024; }
#pragma unroll 1
        for (int k = 0; k < (START ? 5 : 0); ++k)
            asm volatile("s_mov_b32 m0, %5\n\ts_nop 0\n\tbuffer_load_dwordx4 %0, %3, %4 offen nt lds\n\tbuffer_load_dwordx4 %1, %3, %4 offen offset:1024 nt lds\n\tbuffer_load_dwordx4 %2, %3, %4 offen offset:2048 nt lds"
                         :: "v"(W.a[0]), "v"(W.a[1]), "v"(W.a[2]), "s"(wr), "s"(k * 32 * HD * 2), "s"(wm + (k < 2 ? k + 4 : k - 2) * sl) : "memory");
        W.a[3] = (wave >> 2) * 12288 + ((ln >> 4) * 8 + ((ln >> 2) & 3)) * 384 + (ln & 3) * 8 + w4 * 96;
        W.a[4] = 0;
        W.a[5] = 0;
    }
}
__device__ __forceinline__ void s_role(const Params& P, int wave, int L, int h, int cpy) {
    const int ln = lane_id();
    unsigned l16 = ln * 16;
    constexpr bool isv = false;
    WLoad W;
    wload_start<false>(W, P, wave, isv, h);
    const int mt = (wave >> 1) & 1, hf = wave & 1;
    const unsigned xs = D_XN + mt * 4096 + hf * WIN, x1 = (2 - hf) * WIN + hf * 2048, m0 = L_X + mt * 20480 + hf * 4096, m1 = L_X + mt * 20480 + 8192 + hf * 2048;
    const float SCL = 0.07216878364870322f * 1.4426950408889634f;
    const unsigned px = RG_PX + (h >> 2) * WIN;
    const unsigned pb = wave == 0 ? RG_XP + (h / 12) * WIN + (h % 12) * 640 - L_Q : px + (wave == 2 ? (h & 3) * 1024 : 5120 + ((h >> 1) & 1) * 1024 + (h & 1) * 256);
    unsigned b2 = l16 + (wave < 3 ? L_Q : wave == 3 ? L_QX : L_W + wave * 10240), dsto = c1_dst(wave, 1 + (wave >> 2)), bud;
    asm volatile(ASM_S_LOOP : LOOP_IO
                 : LOOP_IN, [xs] "s"(xs), [x1] "s"(x1), [m0] "s"(m0), [m1] "s"(m1), [qs] "s"(UNI(L_KX + hf * 1024 + mt * 8)), [q2] "s"(UNI(L_KX + 2048 + (wave & 3) * 1024)), [pb] "s"(UNI(pb)),
                   [pm] "s"(UNI(px + (wave == 2 ? 4096 : 7168) + (h & 3) * 256)), [scl] "s"(__float_as_uint(SCL))
                 : LOOP_CLOB);
    note_timeout(P, bud);
}

__device__ __forceinline__ void v_role(const Params& P, int wave, int L, int h, int cpy) {
    const int ln = lane_id(), w4 = wave & 3;
    unsigned l16 = ln * 16;
    constexpr bool isv = true;
    WLoad W;
    wload_start<false>(W, P, wave, isv, h);
    unsigned b2 = l16 + (wave < 3 ? L_Q : wave == 3 ? L_QX : L_W + wave * 10240), dsto = wave < 4 ? c1_dst(wave, 0) : (l16 >> 8) * WIN + (l16 & 255), bud;
    if (wave >= 4) l16 += L_Q;
    asm volatile(ASM_V_LOOP : LOOP_IO
                 : LOOP_IN, [xs] "s"(D_XN + w4 * WIN), [vm] "s"(VXD - 1), [vx] "s"(vxb(w4, 0) + (h >> 3) * WIN + (h & 7) * 1024 - L_Q),
                   [fo] "s"(FL_VO + (w4 >> 1) * WIN + ((w4 & 1) * 32 + h) * 128 - L_Q), [vo] "s"(RG_VM + w4 * 10 * WIN + (h >> 2) * 1024 + (h & 3) * 256)
                 : LOOP_CLOB);
    note_timeout(P, bud);
}

#define D_W0 4
#define D_A1 8
#define D_R1 9
#define D_W1 12
__device__ __forceinline__ void delay(int n) { for (int i = 0; i < n; ++i) __builtin_amdgcn_s_sleep(64); }
__device__ __forceinline__ unsigned handshake(rsrc_t rw, unsigned mo) {
    wait_vm<0>();
    unsigned v = SENT;
    for (int spins = 0; spins < SPIN_CAP && v == SENT; ++spins) {
        asm volatile("s_sleep 4" ::: "memory");
        v = __builtin_amdgcn_readfirstlane(__builtin_amdgcn_raw_buffer_load_b32(rw, 0, mo, 16));
    }
    __builtin_amdgcn_raw_buffer_store_b32(1u, rw, lane_id() ? OOB : 32768u, mo, 16);
    return (v >> 13) & 1;
}
template <class F>
__device__ __forceinline__ void x_tile(const Params& P, int tile, F target) {
    const int ln = lane_id(), l15 = ln & 15, l4 = ln >> 4;
    u32x4 v[20];
    float s = 0.f, q = 0.f;
#pragma unroll
    for (int c = 0; c < 20; ++c) v[c] = *(const u32x4*)(P.x + (size_t)(tile * 16 + l15) * DM + c * 32 + l4 * 8);
#pragma unroll
    for (int c = 0; c < 20; ++c)
#pragma unroll
        for (int e = 0; e < 4; ++e) s += lo2f(v[c][e]) + hi2f(v[c][e]);
    sum2_rows(s, q);
#pragma unroll
    for (int c = 0; c < 20; ++c) asm volatile("" : "+v"(v[c]));
    const float mean = s * (1.0f / DM);
#pragma unroll
    for (int c = 0; c < 20; ++c)
#pragma unroll
        for (int e = 0; e < 4; ++e) { const float a = lo2f(v[c][e]) - mean, b = hi2f(v[c][e]) - mean; q += a * a + b * b; }
    sum2_rows(s, q);
#pragma unroll
    for (int c = 0; c < 20; ++c) asm volatile("" : "+v"(v[c]));
    const float m1 = rbf(mean), r = 1.0f / ln_den(q * (1.0f / DM));
    const unsigned sb = segb(tile >> 1) + (tile & 1) * 4096;
    u32x4 n[20];
#pragma unroll
    for (int c = 0; c < 20; ++c) {
#pragma unroll
        for (int e = 0; e < 4; ++e) { const unsigned d = pack2(lo2f(v[c][e]) - m1, hi2f(v[c][e]) - m1); n[c][e] = pack2(lo2f(d) * r, hi2f(d) * r); }
        asm volatile("" : "+v"(n[c]));
    }
    const rsrc_t rr = target();
#pragma unroll
    for (int c = 0; c < 20; ++c) {
        st128(n[c], rr, ln * 16 + (c & 3) * 1024, D_XN + sb + (c >> 2) * WIN);
        st128(v[c], rr, ln * 16 + (c & 3) * 1024, D_XR + sb + (c >> 2) * WIN);
    }
    wait_vm<0>();
}
__device__ __forceinline__ void wait_go(const Params& P, rsrc_t rd, int k) {
    for (int spins = 0;; ++spins) {
        if (__builtin_amdgcn_readfirstlane(__builtin_amdgcn_raw_buffer_load_b32(rd, 0, FL_VO + k * 128, 16))) break;
        asm volatile("s_sleep 16" ::: "memory");
        if (spin_fail(spins * 4 + 3, P)) break;
    }
    asm volatile("" ::: "memory");
}

constexpr unsigned R_RED = 0;
constexpr unsigned R_PART = 8192;
__device__ __forceinline__ void r_role(const Params& P, int wave, int L, int j, int cpy) {
    const int lane = lane_id(), l15 = lane & 15, l4 = lane >> 4;
    const unsigned l16 = lane * 16;
    const rsrc_t rr = mkrsrc(lbase(P, L), RG_L);
    const int sidx = (lane & 2) ? 0xEEEE : 0x4444;
    unsigned offp[3];
    auto hq = [&](int q) { return RG_XP + cpy * XP_COPY + (q / 3) * WIN + (q % 3) * 2560; };
#define ROT 1
    const int hg = (wave + ROT * j) & 7;
    offp[0] = hq(hg) + (lane >> 5) * 640 + 128 + (lane & 31) * 16;
    offp[1] = offp[0] + 1280;
    offp[2] = (lane & 2) ? OOB : hq(hg) + l4 * 640 + ((l15 >> 2) * 2 + (l15 & 1)) * 16;
    const int jq = j / 10, j10 = j % 10;
    const unsigned ae = wave < 3 ? ((j10 < 8 && wave * 1024 + l16 < 2560) ? hq(j10) + (jq - cpy) * XP_COPY + wave * 1024 + l16 : OOB)
                      : wave == 3 ? RG_XM + jq * XM_COPY + (j10 >> 3) * WIN + (j10 & 7) * 1024 + l16
                      : wave < 6 ? (j < 32 ? RG_PX + (j >> 2) * WIN + (j & 3) * 2048 + (wave & 1) * 1024 + l16 : OOB)
                      : wave == 7 ? D_XA + (j >> 3) * WIN + (j & 7) * 512 + (lane >> 5) * 4096 + (lane & 31) * 16 : OOB;
    const unsigned as = wave < 3 ? XP_SLOT : PX_SLOT;
    static_assert(PX_SLOT == XM_SLOT && AH == 3 && VXD == 4, "generated loop");
    const unsigned offv = RG_VM + j * WIN + hg * 1024 + l16;
    const unsigned offo = (wave < (int)NCP) ? RG_XM + wave * XM_COPY + (j >> 5) * WIN + (j & 31) * 256 + lane * 4 : OOB;
    {
        const u32x4 s4 = sent4();
#pragma unroll
        for (int k = 0; k < (int)NSLOT; ++k) {
            st128(s4, rr, offv, k * VM_SLOT);
            if (wave < 6 && (k || wave != 3)) st128(s4, rr, ae, k * as);
        }
        for (int u = j; u < 126; u += 40) st128(s4, rr, wave * 1024 + l16, u < 108 ? D_XA + (u < 60 ? u / 20 : 3) * 4 * REG + (u < 60 ? u % 20 : u - 60) * WIN : u < 124 ? RG_ST + (u - 108) * WIN : D_SS + (u - 124) * WIN);
        wait_vm<0>();
        bar();
        const int row = l4 * 2 + ((lane >> 2) & 1), col = 16 * j + 4 * (lane & 3) + 2 * ((lane >> 3) & 1);
        __builtin_amdgcn_raw_buffer_store_b32(nosent(*(const unsigned*)(P.mem0 + row * DM + col)), rr, offo, 0, 16);
    }
    auto red_addr = [](int fl) { return R_RED + fl * 80 + (fl >> 4) * 16; };
    const unsigned rw0 = red_addr(l4 * 16 + ((l15 >> 1) & 1) * 8 + (l15 >> 2)) + (l15 & 1) * 4 + (hg ^ (l15 >> 3)) * 8;
    const unsigned rrd = red_addr(lane);
    unsigned bud;
    asm volatile(ASM_R_LOOP : [bud] "=s"(bud)
                 : [wave] "s"(wave), [rs] "s"(rr), [l16] "v"(l16), [ov] "v"(offv), [o0] "v"(offp[0]), [o1] "v"(offp[1]), [o2] "v"(offp[2]), [oo] "v"(offo), [sidx] "v"(sidx), [rw] "v"(rw0), [rd] "v"(rrd),
                   [xo] "v"((l4 >> 1) * 256 + l15 * 16 + (l4 & 1) * 8), [pw] "v"(R_PART + hg * 1024 + l16), [pr] "v"(R_PART + (wave & 1) * 8192 + l16), [fo] "v"((lane & 3) * 128), [ae] "v"(ae),
                   [fs] "s"(UNI(FL_VO + (jq >> 1) * WIN + ((jq & 1) * 32 + hg * 4) * 128)), [vx] "s"(UNI(vxb(jq, 0) + (j10 * 4 + (hg >> 1)) * WIN + (hg & 1) * 4096)), [px] "s"(UNI(RG_PX + hg * WIN)),
                   [xr] "s"(UNI(D_XR + (j >> 3) * WIN + (wave & 1) * 4096 + (j & 7) * 512)), [ad] "s"(UNI(wave < 3 ? -1 : wave == 3 ? 0 : -2)), [as] "s"(UNI(as))
                 : "memory", "vcc", "scc", "v128", "v129", "v130", "v131", "v132", "v133", "v134", "v135", "v136", "v137", "v138", "v139", "v140", "v141", "v142", "v143", "v144", "v145", "v146", "v147", "v148", "v149", "v150", "v151", "v152", "v153", "v154", "v155", "v156", "v157", "v158", "v159", "v160", "v161", "v162", "v163", "v164", "v165", "v166", "v167", "v168", "v169", "v170", "v171", "v172", "v173", "v174", "v175", "v176", "v177", "v178", "v179", "v180", "v181", "v182", "v183", "v184", "v185", "v186", "v187", "v188", "v189", "v190", "v191", "v192", "v193", "v194", "v195", "v196", "v197", "v198", "v199", "v200", "v201", "v202", "v203", "v204", "v205", "v206", "v207", "v208", "v209", "v210", "v211", "v212", "v213", "v214", "v215", "v216", "v217", "v218", "v219", "v220", "v221", "v222", "v223", "v224", "v225", "v226", "v227", "v228", "v229", "v230", "v231", "v232", "v233", "v234", "v235", "v236", "v237", "v238", "v239", "v240", "v241", "v242", "v243", "v244", "v245", "v246", "v247", "v248", "v249", "v250", "v251", "v252", "v253", "v254", "v255", "s40", "s41", "s42", "s43", "s44", "s45", "s46", "s47", "s48", "s49", "s50", "s51", "s52", "s53", "s54", "s55", "s64", "s65", "s66", "s67", "s68", "s69", "s70", "s71", "s72", "s73", "s74", "s75", "s76", "s77", "s78", "s79");
    note_timeout(P, bud);
}

template <int WHICH>
__device__ __forceinline__ void w_role(const Params& P, int wave, int L, int cu) {
    constexpr int NT = (WHICH == 3) ? 2 : 1, N = (WHICH == 3) ? FF : DM, RB = NT * 256, RPI = 1024 / RB;
    const int ln = lane_id(), l15 = ln & 15, l4 = ln >> 4;
    const rsrc_t rd = mkrsrc(lbase(P, L), RG_L);
    const int nt0 = (cu * 8 + wave) * NT;
    const rsrc_t wr = mkrsrc((WHICH == 1 ? P.w1 : WHICH == 2 ? P.w2 : P.w3) + cu * (RB / 2), DM * N * 2 - cu * RB);
    auto swz = [](int row) { return ((row & 3) | ((row >> 1) & 4)) * 32; };
    const int dr = ln * 16 / RB, tr = l4 * 8 + ((ln >> 2) & 3);
    unsigned vo = dr * (N * 2) + ((ln * 16 % RB) ^ swz(wave * RPI + dr)), ta = tr * RB + ((wave * NT * 32 + (ln & 3) * 8) ^ swz(tr));
    const int grp = nt0 / 10, neg = grp & 1, sh = (WHICH == 1) ? (neg ? -1 : 1) * ((grp >> 1) + 1) : 0;
    unsigned ra = ln * 16, wa = ln * 16 + wave * 1024, sr = WHICH == 2 ? l15 * 8 : 24576 + l15 * 80, sw = sr + wave * 8, bud;
    const unsigned oo = WHICH == 3 ? (l4 & 1) * 512 + (l4 >> 1) * 256 + l15 * 16 : (nt0 >> 3) * WIN + (nt0 & 7) * 512 + (l4 >> 1) * 256 + ((l15 + sh) & 15) * 16 + (l4 & 1) * 8;
    unsigned up = ((unsigned)(l15 + sh) > 15u) ^ neg;
#define W_ARGS : [bud] "=s"(bud), "+{v176}"(vo), "+{v177}"(ta), "+{v249}"(ra), "+{v250}"(wa), "+{v251}"(sr), "+{v252}"(sw) \
               , "+{v255}"(up) : "{v253}"(ln * 16), "{v254}"(oo), [wave] "s"(wave), [rs] "s"(rd), [wr] "s"(wr), [wm] "s"(UNI(wave * 1024)), [wo] "s"(UNI(wave * RPI * N * 2)), [go] "s"(FL_VO + (WHICH * 8 + cu) * 128), \
                 [pi] "s"(UNI((WHICH == 1 ? D_XA : WHICH == 2 ? D_HS : D_X1) + wave * 1024)), \
                 [ng] "s"(UNI(WHICH == 3 ? D_H2 + (nt0 >> 3) * WIN + ((nt0 >> 1) & 3) * 1024 : neg)), [ss] "s"(UNI(cu == 0)) \
               : "memory", "vcc", "scc", "v0", "v1", "v2", "v3", "v4", "v5", "v6", "v7", "v8", "v9", "v10", "v11", "v12", "v13", "v14", "v15", "v16", "v17", "v18", "v19", "v20", "v21", "v22", "v23", "v24", "v25", "v26", "v27", "v28", "v29", "v30", "v31", "v32", "v33", "v34", "v35", "v36", "v37", "v38", "v39", "v40", "v41", "v42", "v43", "v44", "v45", "v46", "v47", "v48", "v49", "v50", "v51", "v52", "v53", "v54", "v55", "v56", "v57", "v58", "v59", "v60", "v61", "v62", "v63", "v64", "v65", "v66", "v67", "v68", "v69", "v70", "v71", "v72", "v73", "v74", "v75", "v76", "v77", "v78", "v79", "v80", "v81", "v82", "v83", "v84", "v85", "v86", "v87", "v88", "v89", "v90", "v91", "v92", "v93", "v94", "v95", "v96", "v97", "v98", "v99", "v100", "v101", "v102", "v103", "v104", "v105", "v106", "v107", "v108", "v109", "v110", "v111", "v112", "v113", "v114", "v115", "v116", "v117", "v118", "v119", "v120", "v121", "v122", "v123", "v124", "v125", "v126", "v127", "v128", "v129", "v130", "v131", "v132", "v133", "v134", "v135", "v136", "v137", "v138", "v139", "v140", "v141", "v142", "v143", "v144", "v145", "v146", "v147", "v148", "v149", "v150", "v151", "v152", "v153", "v154", "v155", "v156", "v157", "v158", "v159", "v160", "v161", "v162", "v163", "v164", "v165", "v166", "v167", "v168", "v169", "v170", "v171", "v172", "v173", "v174", "v175", "v178", "v179", "v180", "v181", "v182", "v183", "v184", "v185", "v186", "v187", "v188", "v189", "v190", "v191", "v192", "v193", "v194", "v195", "v196", "v197", "v198", "v199", "v200", "v201", "v202", "v203", "v204", "v205", "v206", "v207", "v208", "v209", "v210", "v211", "v212", "v213", "v214", "v215", "v216", "v217", "v218", "v219", "v220", "v221", "v222", "v223", "v224", "v225", "v226", "v227", "v228", "v229", "v230", "v231", "v232", "v233", "v234", "v235", "v236", "v237", "v238", "v239", "v240", "v241", "v242", "v243", "v244", "v245", "v246", "v247", "v248", "s40", "s41", "s42", "s43", "s44", "s45", "s46", "s47", "s48", "s49", "s50", "s51", "s52", "s53", "s54", "s55", "s56", "s57", "s58", "s59", "s60", "s61", "s62", "s63", "s64", "s65", "s66", "s67", "s68", "s69", "s70", "s71", "s72", "s73", "s74", "s75", "s76", "s77", "s78", "s79"
    if (WHICH == 1) asm volatile(ASM_W1_LOOP W_ARGS);
    if (WHICH == 2) asm volatile(ASM_W2_LOOP W_ARGS);
    if (WHICH == 3) asm volatile(ASM_W3_LOOP W_ARGS);
    note_timeout(P, bud);
}

constexpr unsigned W4_FL = 122880;
__device__ __forceinline__ unsigned lds_vld(unsigned a) { asm volatile("" ::: "memory"); const unsigned v = *(volatile LAS unsigned*)(size_t)a; asm volatile("" ::: "memory"); return v; }
__device__ __forceinline__ void lds_vst(unsigned a, unsigned v) { asm volatile("" ::: "memory"); *(volatile LAS unsigned*)(size_t)a = v; asm volatile("" ::: "memory"); }
__device__ __forceinline__ void w4_role(const Params& P, int wave, int L, int cu) {
    const int ln = lane_id(), l15 = ln & 15, l4 = ln >> 4;
    const unsigned l16 = ln * 16;
    const rsrc_t rd = mkrsrc(lbase(P, L), RG_L);
    unsigned bud;
    if (wave >= 6) wait_go(P, rd, 32 + cu);
    if (wave < 6) {
        u32x32 w[5];
        {
            const rsrc_t wr = mkrsrc(P.w4 + wave * 256 * DM + cu * 80, (FF * DM - wave * 256 * DM - cu * 80) * 2);
            const unsigned ta = wave * 20480 + (l4 * 8 + ((ln >> 2) & 3)) * 160 + (ln & 3) * 8;
            u32x4 f[5][8];
#pragma unroll
            for (int r = 0; r < 2; ++r) {
#pragma unroll
                for (int i = 0; i < 5; ++i) {
                    const unsigned u = i * 64 + ln, vo = (u / 10) * (DM * 2) + (u % 10) * 16;
#pragma unroll
                    for (int k = 0; k < 4; ++k) dma1(wr, vo, (r * 128 + k * 32) * (DM * 2), wave * 20480 + k * 5120 + i * 1024);
                }
                wait_vm<0>();
#pragma unroll
                for (int k = 0; k < 4; ++k)
#pragma unroll
                    for (int n = 0; n < 5; ++n) f[n][r * 4 + k] = tr_frag(ta + k * 5120 + n * 32, 640);
                asm volatile("s_waitcnt lgkmcnt(0)" ::: "memory");
                SB();
            }
#pragma unroll
            for (int n = 0; n < 5; ++n) {
                u32x8 g[4];
#pragma unroll
                for (int i = 0; i < 4; ++i) g[i] = __builtin_shufflevector(f[n][2 * i], f[n][2 * i + 1], 0, 1, 2, 3, 4, 5, 6, 7);
                w[n] = cat32(g[0], g[1], g[2], g[3]);
            }
            asm volatile("" : "+{v[0:31]}"(w[0]), "+{v[32:63]}"(w[1]), "+{v[64:95]}"(w[2]), "+{v[96:127]}"(w[3]), "+{v[128:159]}"(w[4]));
        }
        wait_go(P, rd, 32 + cu);
        bar();
        unsigned ld = l16 + wave * 1024;
        asm volatile(ASM_W4_MUL : [bud] "=s"(bud), [ld] "+v"(ld)
                     : "{v[0:31]}"(w[0]), "{v[32:63]}"(w[1]), "{v[64:95]}"(w[2]), "{v[96:127]}"(w[3]), "{v[128:159]}"(w[4]), [l2] "v"(2 * ld + 61440), [l16] "v"(l16), [fa] "v"(W4_FL + wave * 4), [rs] "s"(rd),
                       [hb] "s"(UNI(D_H2 + wave * 2 * WIN)), [wave] "s"(wave), [ro] "s"(UNI(wave * 5120)), [fp] "v"(W4_FL + (ln < 6 ? ln : 0) * 4)
                     : "memory", "vcc", "scc", "v160", "v161", "v162", "v163", "v164", "v165", "v166", "v167", "v168", "v169", "v170", "v171", "v172", "v173", "v174", "v175", "v176", "v177", "v178", "v179", "v180", "v181", "v182", "v183", "v184", "v185", "v186", "v187", "v188", "v189", "v190", "v191", "v192", "v193", "v194", "v195", "v196", "v197", "v198", "v199", "v200", "v201", "v202", "v203", "v204", "v205", "v206", "v207", "v208", "v209", "v210", "v211", "v212", "v213", "v214", "v215", "v216", "v217", "v218", "v219", "v220", "v221", "v222", "v223", "v224", "v225", "v226", "v227", "v228", "v229", "v230", "v231", "v232", "v233", "v234", "v235", "v236", "v237", "v238", "v239", "v240", "v241", "v242", "v243", "v244", "v245", "v246", "v247", "s40", "s41", "s42", "s43", "s44", "s45", "s46", "s47", "s48", "s49", "s50", "s51", "s52", "s53", "s54", "s55", "s64", "s65", "s66", "s67", "s68", "s69", "s70", "s71", "s72", "s73", "s74", "s75", "s76", "s77", "s78", "s79");
    } else {
        const int mt = wave - 6;
        if (ln < 16) lds_vst(W4_FL + ln * 4, 0u);
        bar();
        auto tl = [&](int n) { return (unsigned)((cu * 5 + n) >> 3) * WIN + ((cu * 5 + n) & 7) * 512 + mt * 4096; };
        const unsigned o8 = (l4 >> 1) * 256 + l15 * 16 + (l4 & 1) * 8;
        unsigned la = mt * 30720 + l16;
        static_assert(RG_ST == 112 * WIN && D_XN == 10 * REG && D_XR == 6 * REG, "generated loop");
#define F_IN [l2] "v"(2 * la + 61440), [fa] "v"(W4_FL + (ln < 6 ? ln : 0) * 4), [fb] "v"(W4_FL + 32 + (ln < 5 ? ln : 0) * 4), [fc] "v"(W4_FL + 24 + mt * 4), [rs] "s"(rd), [mt] "s"(mt), \
             [t0] "v"(o8 + tl(0)), [t1] "v"(o8 + tl(1)), [t2] "v"(o8 + tl(2)), [t3] "v"(o8 + tl(3)), [t4] "v"(o8 + tl(4))
#define F_CLOB "memory", "vcc", "scc", "v64", "v65", "v66", "v67", "v68", "v69", "v70", "v71", "v72", "v73", "v74", "v75", "v76", "v77", "v78", "v79", "v80", "v81", "v82", "v83", "v84", "v85", "v86", "v87", "v88", "v89", "v90", "v91", "v92", "v93", "v94", "v95", "v96", "v97", "v98", "v99", "v100", "v101", "v102", "v103", "v104", "v105", "v106", "v107", "v108", "v109", "v110", "v111", "v112", "v113", "v114", "v115", "v116", "v117", "v118", "v119", "v120", "v121", "v122", "v123", "v124", "v125", "v126", "v127", "v128", "v129", "v130", "v131", "v132", "v133", "v134", "v135", "v136", "v137", "v138", "v139", "v140", "v141", "v142", "v143", "v144", "v145", "v146", "v147", "v148", "v149", "v150", "v151", "v152", "v153", "v154", "v155", "v156", "v157", "v158", "v159", "v160", "v161", "v162", "v163", "v164", "v165", "v166", "v167", "v168", "v169", "v170", "v171", "v172", "v173", "v174", "v175", "v176", "v177", "v178", "v179", "v180", "v181", "v182", "v183", "v184", "v185", "v186", "v187", "v188", "v189", "v190", "v191", "v192", "v193", "v194", "v195", "v196", "v197", "v198", "v199", "v200", "v201", "v202", "v203", "v204", "v205", "v206", "v207", "v208", "v209", "v210", "v211", "v212", "v213", "v214", "v215", "v216", "v217", "v218", "v219", "v220", "v221", "v222", "v223", "v224", "v225", "v226", "v227", "v228", "v229", "v230", "v231", "v232", "v233", "v234", "v235", "v236", "v237", "v238", "v239", "s40", "s41", "s42", "s43", "s44", "s45", "s46", "s47", "s48", "s49", "s50", "s51", "s52", "s53", "s54", "s55", "s64", "s65", "s66", "s67", "s68", "s69", "s70", "s71", "s72", "s73", "s74", "s75", "s76", "s77", "s78", "s79"
        if (L) asm volatile(ASM_W4_FIN1 : [bud] "=s"(bud), [la] "+v"(la)
                            : F_IN, [ro] "s"(mkrsrc(P.out, ACT)), [co] "s"(cu * 160), [q8] "v"(l15 * (DM * 2) + l4 * 8), [q16] "v"(l15 * (DM * 2) + (l4 & 1) * 32 + (l4 >> 1) * 16) : F_CLOB);
        else asm volatile(ASM_W4_FIN0 : [bud] "=s"(bud), [la] "+v"(la)
                          : F_IN, [rn] "s"(mkrsrc(lbase(P, 1), RG_L)), [co] "s"(cu * 128), [l8] "v"(ln * 8), [o16] "v"(tl(l4 & 1) + (l4 >> 1) * 256 + l15 * 16), [o17] "v"(tl(2 + (l4 & 1)) + (l4 >> 1) * 256 + l15 * 16),
                            [fo] "v"(ln < 32 ? (ln >> 3) * 2048 + (ln & 7) * 16 : OOB), [fs] "s"(FL_XN + (cu * 2 + mt) * 128) : F_CLOB);
    }
    note_timeout(P, bud);
}

constexpr int LEADER = 252;
__device__ __forceinline__ unsigned leader(const Params& P, rsrc_t rw, int wave) {
    const int ln = lane_id();
    if (wave == 0) {
        const unsigned o = ln ? OOB : 0u;
        __builtin_amdgcn_raw_buffer_store_b32(0u, rw, o, CAL0, 16);
        __builtin_amdgcn_raw_buffer_store_b32(0u, rw, o, CAL0 + 8192, 16);
        unsigned m[2] = {~0u, ~0u};
#pragma unroll
        for (int r = 0; r < CALN; ++r) {
            unsigned long long a, c;
            asm volatile("s_waitcnt vmcnt(0) lgkmcnt(0)\n\ts_memtime %0\n\ts_waitcnt lgkmcnt(0)\n\tbuffer_store_dword %2, %2, %3, %4 offen sc1\n\ts_waitcnt vmcnt(0)\n\ts_memtime %1\n\ts_waitcnt lgkmcnt(0)"
                         : "=&s"(a), "=&s"(c) : "v"(o), "s"(rw), "s"(CAL0 + (r & 1) * 8192) : "memory");
            const unsigned d = (unsigned)(c - a);
            m[r & 1] = d < m[r & 1] ? d : m[r & 1];
        }
        lds_vst(0, m[0] < m[1] ? 1u : 0u);
    }
    bar();
    const unsigned par = __builtin_amdgcn_readfirstlane(lds_vld(0));
    const u32x4 s4 = sent4(), z4 = s4 ^ s4;
    for (int k = 0; k < 6; ++k) st128(z4, rw, ln * 16, FL_VO + (k * 8 + wave) * 1024);
    for (int k = 0; k < 10; ++k) st128(s4, rw, ln * 16, RG_XM + (wave >> 1) * XM_COPY + (wave & 1) * 8192 + (k >> 3) * WIN + (k & 7) * 1024);
    const unsigned mo = (wave * 32 + (ln >> 1)) * 128 + (ln & 1) * 16;
    st128(z4, rw, mo, FL_MB + 32768);
    if (wave == 0) st128(z4, rw, ln ? OOB : 256u, FL0);
    wait_vm<0>();
    bar();
    bool open = wave * 32 + (ln >> 1) != LEADER;
    for (int spins = 0; spins < SPIN_CAP; ++spins) {
        st128(s4 & (par << 13), rw, open ? mo : OOB, FL_MB);
        __builtin_amdgcn_s_sleep(20);
        const u32x4 d = LD128(rw, mo, FL_MB + 32768);
        open = open && !(d[0] && d[1] && d[2] && d[3]);
        if (!__any(open)) break;
    }
    bar();
    return par;
}
__global__ __launch_bounds__(512, 2) void mega_kernel(Params P) {
    const int wg = blockIdx.x;
    const int wave = __builtin_amdgcn_readfirstlane(threadIdx.x >> 6);
    const int L = (wg >> 2) & 1, x = wg & 3, r = wg >> 3, cpy = x;
    if (__builtin_expect(r < 16, 1)) {
        WLoad W;
        if (L) delay(D_A1);
        wload_start<true>(W, P, wave, r >= 8, x * 8 + (r & 7));
    }
    const int nr = (x & 1) ? 10 + RK : 10 - RK, j = (x >> 1) * 20 + (x & 1) * (10 - RK) + r - 16, i0 = (x >> 1) * 12 + (x & 1) * (6 + RK) + r - 16 - nr;
    const int i = (i0 * 5) % 24;
    const rsrc_t rw = mkrsrc(P.ws, RG_L);
    const bool lead = wg == LEADER;
    if (!lead) __builtin_amdgcn_raw_buffer_store_b32(SENT, rw, lane_id() ? OOB : 0u, FL_MB + wg * 128 + wave * 4, 16);
#define FIN() __builtin_amdgcn_endpgm()
    if (r < 8) { s_role(P, wave, L, x * 8 + r, cpy); FIN(); }
    if (r < 16) { v_role(P, wave, L, x * 8 + r - 8, cpy); FIN(); }
    if (lead) P.par = leader(P, rw, wave);
    else {
        const unsigned mo = FL_MB + wg * 128 + wave * 4;
        const bool ln = r - 16 < nr && j < 16, first = ln && !L && wave == 7;
        if (!first) { P.par = handshake(rw, mo); if (L || r - 16 >= nr) delay(!L ? D_W0 : r - 16 < nr ? D_R1 : D_W1); }
        if (first || (ln && L && wave)) x_tile(P, (L ? wave * 16 : 0) + j, [&]() { if (first) P.par = handshake(rw, mo); return mkrsrc(lbase(P, 0), RG_L); });
        if (ln && L) bar();
        if (first || (ln && L && !wave)) {
            const rsrc_t r0 = mkrsrc(lbase(P, 0), RG_L);
            const int lane = lane_id();
            const unsigned fo = lane < 32 ? (lane >> 3) * 2048 + (lane & 7) * 16 : OOB;
            if (L) for (int spins = 0; spins < SPIN_CAP && __any(lane < 32 && LD128(r0, fo, FL_XN + j * 128)[0] == 0u); ++spins) asm volatile("s_sleep 8" ::: "memory");
            st128(sent4() & (L ? 64u : 8u), r0, fo, FL_XN + j * 128);
        }
    }
    if (r - 16 < nr) { r_role(P, wave, L, j, cpy); FIN(); }
    if (i < 5) { w_role<1>(P, wave, L, i); FIN(); }
    if (i < 10) { w_role<2>(P, wave, L, i - 5); FIN(); }
    if (i < 16) { w_role<3>(P, wave, L, i - 10); FIN(); }
    w4_role(P, wave, L, i - 16);
    FIN();
}

extern "C" void kernel_launch(void* const* d_in, const int* in_sizes, int n_in, void* d_out, int out_size, void* d_ws,
                              size_t ws_size, hipStream_t stream) {
    char* ws = (char*)d_ws + (-(size_t)d_ws & (REG - 1));
    Params P;
    P.par = 0; P.pad = 0; P.ws = ws; P.out = (bf16_t*)d_out;
    P.x = (const bf16_t*)d_in[0]; P.mem0 = (const bf16_t*)d_in[1]; P.wq = (const bf16_t*)d_in[2]; P.wk = (const bf16_t*)d_in[3]; P.wv = (const bf16_t*)d_in[4]; P.wo = (const bf16_t*)d_in[5];
    P.w1 = (const bf16_t*)d_in[8]; P.w2 = (const bf16_t*)d_in[9]; P.w3 = (const bf16_t*)d_in[10]; P.w4 = (const bf16_t*)d_in[11];
    (void)hipFuncSetAttribute((const void*)mega_kernel, hipFuncAttributeMaxDynamicSharedMemorySize, MEGA_LDS);

    mega_kernel<<<256, 512, MEGA_LDS, stream>>>(P);
}
```

```cpp
#define PRET 1
#include <hip/hip_runtime.h>
#include <stdint.h>

typedef unsigned short bf16_t;
using bf16x2 = __attribute__((ext_vector_type(2))) __bf16;
using f32x2 = __attribute__((ext_vector_type(2))) float;
using u32x2 = __attribute__((ext_vector_type(2))) unsigned;
using u32x4 = __attribute__((ext_vector_type(4))) unsigned;
typedef __amdgpu_buffer_rsrc_t rsrc_t;

__device__ __forceinline__ unsigned pack2(float a, float b) {
    bf16x2 r = __builtin_convertvector((f32x2){a, b}, bf16x2);
    return __builtin_bit_cast(unsigned, r);
}
__device__ __forceinline__ float rbf(float f) { return __uint_as_float(pack2(f, 0.f) << 16); }
__device__ __forceinline__ float lo2f(unsigned u) { return __uint_as_float(u << 16); }
__device__ __forceinline__ float hi2f(unsigned u) { return __uint_as_float(u & 0xFFFF0000u); }
__device__ __forceinline__ rsrc_t mkrsrc(const void* p, unsigned bytes) {
    return __builtin_amdgcn_make_buffer_rsrc((void*)p, 0, bytes, 0x00020000);
}

constexpr int S_ = 2048, DM = 640, NH = 32, DH = 192, HD = NH * DH  , SEG = 32, NSEG = 64;
constexpr int FF = 1536;

constexpr unsigned NSLOT = 4, NCP = 4, VXD = 4;
constexpr unsigned WIN = 16384, REG = 2u << 20, REGS = REG + 8192;
constexpr unsigned XP_COPY = 3 * WIN, XP_SLOT = NCP * XP_COPY;
constexpr unsigned XM_COPY = 2 * WIN, XM_SLOT = NCP * XM_COPY;
constexpr unsigned VM_SLOT = REGS;
constexpr unsigned PX_SLOT = 8 * WIN;
constexpr unsigned RG_XM = 0, RG_XP = RG_XM + NSLOT * XM_SLOT, RG_PX = RG_XP + NSLOT * XP_SLOT, RG_ST = RG_PX + NSLOT * PX_SLOT, RG_VM = 2 * REG;
static_assert(RG_ST + 16 * WIN <= REG, "ring region");
constexpr unsigned FL0 = REG, FL_PAR = REG + 4096, FL_MB = REG + 65536;
constexpr unsigned FL_VO = RG_VM + 48 * WIN, FL_XN = FL_VO + 2 * WIN, D_SS = FL_VO + 4 * WIN, CAL0 = RG_VM + 64 * WIN;
constexpr unsigned ACT = S_ * DM * 2;
constexpr unsigned D_XR = 6 * REG, D_XN = 10 * REG, D_XA = 14 * REG, D_HS = 18 * REG, D_X1 = 22 * REG, D_H2 = 26 * REG, D_VX = 34 * REG;
__host__ __device__ constexpr unsigned segb(unsigned seg) { return seg * (5 * WIN) + (seg >> 4) * (REGS - 80 * WIN); }
__host__ __device__ constexpr unsigned segh(unsigned seg) { return seg * (12 * WIN) + (seg >> 3) * (REGS - 96 * WIN); }
#define CALN 4
#define RK 0
__host__ __device__ constexpr unsigned vxb(unsigned q, unsigned t) { return D_VX + q * (VXD / 2 * REG) + (t & (VXD - 1)) * (REG / 2) + (t & 2) * 4096; }
constexpr int AH = 3;
constexpr unsigned RG_L = D_VX + 2 * VXD * REG;

#define SB() __builtin_amdgcn_sched_barrier(0)
#define LAS __attribute__((address_space(3)))
constexpr unsigned OOB = 0x7FFFFF00u;
template <int N> __device__ __forceinline__ void wait_vm() { asm volatile("s_waitcnt vmcnt(%0)" :: "i"(N) : "memory"); }
__device__ __forceinline__ void bar() {
    asm volatile("s_waitcnt lgkmcnt(0)" ::: "memory");
    __builtin_amdgcn_s_barrier();
    asm volatile("" ::: "memory");
}
__device__ __forceinline__ int lane_id() {
    int ln;
    asm volatile("v_mbcnt_lo_u32_b32 %0, -1, 0\n\tv_mbcnt_hi_u32_b32 %0, -1, %0" : "=v"(ln));
    return ln;
}
__device__ __forceinline__ void sum2_rows(float& s, float& q) {
    unsigned a = __float_as_uint(s), b = a, c = __float_as_uint(q), d = c;
    asm volatile("s_nop 2\n\tv_permlane16_swap_b32 %0, %1\n\tv_permlane16_swap_b32 %2, %3\n\tv_add_f32 %0, %0, %1\n\tv_add_f32 %2, %2, %3\n\tv_mov_b32 %1, %0\n\tv_mov_b32 %3, %2\n\t"
                 "s_nop 1\n\tv_permlane32_swap_b32 %0, %1\n\tv_permlane32_swap_b32 %2, %3\n\tv_add_f32 %0, %0, %1\n\tv_add_f32 %2, %2, %3" : "+v"(a), "+v"(b), "+v"(c), "+v"(d));
    s = __uint_as_float(a); q = __uint_as_float(c);
}
template <int AUXV>
__device__ __forceinline__ void st128_(u32x4 v, rsrc_t r, unsigned voff, unsigned soff) {
    __builtin_amdgcn_raw_buffer_store_b128(v, r, voff, soff, AUXV);
    asm volatile("s_nop 1" :: "v"(v) : "memory");
}
#define st128(V, R, VO_, SO_) st128_<16>(V, R, VO_, SO_)
#define LD128(R, VO_, SO_) __builtin_amdgcn_raw_buffer_load_b128(R, VO_, SO_, 16)
__device__ __forceinline__ float ln_den(float var) { return rbf(__builtin_amdgcn_sqrtf(rbf(rbf(var) + 1e-5f))); }

struct Params {
    char* ws;
    const bf16_t *x, *mem0, *wq, *wk, *wv, *wo, *w1, *w2, *w3, *w4;
    bf16_t* out;
    unsigned par, pad;
};

#define SENT 0xFFFFFFFFu
constexpr int SPIN_CAP = 400000;
__device__ __forceinline__ bool spin_fail(int spins, const Params& P) {
    if ((spins & 1023) == 1023) {
        const rsrc_t rt = mkrsrc(P.ws + FL0, 512);
        unsigned o;
        asm volatile("v_mov_b32 %0, 0x100" : "=v"(o));
        if (__builtin_amdgcn_readfirstlane(__builtin_amdgcn_raw_buffer_load_b32(rt, o, 0, 16)) != 0u) return true;
        if (spins > SPIN_CAP) { __builtin_amdgcn_raw_buffer_store_b32(o, rt, o, 0, 16); return true; }
    }
    return false;
}
__device__ __forceinline__ unsigned nosent(unsigned d) { return d < 0xFFFFFFFEu ? d : 0xFFFFFFFEu; }
__device__ __forceinline__ u32x4 sent4() { unsigned sv; asm volatile("v_mov_b32 %0, -1" : "=v"(sv)); return (u32x4){sv, sv, sv, sv}; }
__device__ __forceinline__ char* lbase(const Params& P, int L) { return P.ws + ((P.par ^ L) << 13); }

#define LST0
#define LST1
#define LST2
#define LST3
#define LST4
#define LST5
#define LST6
#define LST7
#define LSTINIT
#define LFLUSH200
#define LFLUSH212
#define LFLUSHM
#define LFLUSHF
#define LFLUSHR
#define LFLUSHW
#define LST_INW
#define LST_IN

#define LJIT
#define CJIT(m)
constexpr unsigned L_X = 0;
constexpr unsigned L_MEM = 40960;
constexpr unsigned L_Q = 51200;
constexpr unsigned L_KM = 54272;
constexpr unsigned L_QX = 57344;
constexpr unsigned L_KX = 69632;
constexpr unsigned L_W = 81920;
constexpr int MEGA_LDS = 163840;

#define ASM_S_LOOP \
    "s_mov_b64 s[60:61], %[ws]\n\t" \
    "s_mov_b32 s62, %[sz]\n\t" \
    "s_mov_b32 s63, 0x20000\n\t" \
    "s_mov_b32 s40, 0\n\t" \
    "s_mov_b32 s41, 0x20000\n\t" \
    LSTINIT \
    "s_cmp_eq_u32 %[wave], 0\n\t" \
    "s_cbranch_scc1 .Ls0_%=\n\t" \
    "s_cmp_eq_u32 %[wave], 1\n\t" \
    "s_cbranch_scc1 .Ls1_%=\n\t" \
    "s_cmp_eq_u32 %[wave], 2\n\t" \
    "s_cbranch_scc1 .Ls2_%=\n\t" \
    "s_cmp_eq_u32 %[wave], 3\n\t" \
    "s_cbranch_scc1 .Ls3_%=\n\t" \
    ".Ls4_%=:\n\t" \
    "s_mov_b32 s42, %[xs]\n\t" \
    "v_add_u32 v244, 0x18000, v243\n\t" \
    "s_waitcnt vmcnt(12) lgkmcnt(0)\n\t" \
    "s_barrier \n\t" \
    "s_add_u32 m0, %[wm], 0x12000\n\t" \
    "s_mov_b32 s44, 0x1e0000\n\t" \
    "buffer_load_dwordx4 v240, %[wr], s44 offen offset:0 nt lds\n\t" \
    "buffer_load_dwordx4 v241, %[wr], s44 offen offset:1024 nt lds\n\t" \
    "buffer_load_dwordx4 v242, %[wr], s44 offen offset:2048 nt lds\n\t" \
    "ds_read_b64_tr_b16 v[200:201], v244 offset:0\n\t" \
    "ds_read_b64_tr_b16 v[202:203], v244 offset:1536\n\t" \
    "ds_read_b64_tr_b16 v[0:1], v244 offset:32\n\t" \
    "ds_read_b64_tr_b16 v[2:3], v244 offset:1568\n\t" \
    "ds_read_b64_tr_b16 v[8:9], v244 offset:64\n\t" \
    "ds_read_b64_tr_b16 v[10:11], v244 offset:1600\n\t" \
    "v_add_u32 v244, 0x1e000, v243\n\t" \
    "s_waitcnt vmcnt(12) lgkmcnt(0)\n\t" \
    "s_barrier \n\t" \
    "s_add_u32 m0, %[wm], 0x18000\n\t" \
    "s_mov_b32 s44, 0x240000\n\t" \
    "buffer_load_dwordx4 v240, %[wr], s44 offen offset:0 nt lds\n\t" \
    "buffer_load_dwordx4 v241, %[wr], s44 offen offset:1024 nt lds\n\t" \
    "buffer_load_dwordx4 v242, %[wr], s44 offen offset:2048 nt lds\n\t" \
    "ds_read_b64_tr_b16 v[204:205], v244 offset:0\n\t" \
    "ds_read_b64_tr_b16 v[206:207], v244 offset:1536\n\t" \
    "ds_read_b64_tr_b16 v[4:5], v244 offset:32\n\t" \
    "ds_read_b64_tr_b16 v[6:7], v244 offset:1568\n\t" \
    "ds_read_b64_tr_b16 v[12:13], v244 offset:64\n\t" \
    "ds_read_b64_tr_b16 v[14:15], v244 offset:1600\n\t" \
    "v_add_u32 v244, 0x0, v243\n\t" \
    "s_waitcnt vmcnt(12) lgkmcnt(0)\n\t" \
    "s_barrier \n\t" \
    "s_add_u32 m0, %[wm], 0x1e000\n\t" \
    "s_mov_b32 s44, 0x2a0000\n\t" \
    "buffer_load_dwordx4 v240, %[wr], s44 offen offset:0 nt lds\n\t" \
    "buffer_load_dwordx4 v241, %[wr], s44 offen offset:1024 nt lds\n\t" \
    "buffer_load_dwordx4 v242, %[wr], s44 offen offset:2048 nt lds\n\t" \
    "ds_read_b64_tr_b16 v[16:17], v244 offset:0\n\t" \
    "ds_read_b64_tr_b16 v[18:19], v244 offset:1536\n\t" \
    "ds_read_b64_tr_b16 v[24:25], v244 offset:32\n\t" \
    "ds_read_b64_tr_b16 v[26:27], v244 offset:1568\n\t" \
    "ds_read_b64_tr_b16 v[32:33], v244 offset:64\n\t" \
    "ds_read_b64_tr_b16 v[34:35], v244 offset:1600\n\t" \
    "v_add_u32 v244, 0x6000, v243\n\t" \
    "s_waitcnt vmcnt(12) lgkmcnt(0)\n\t" \
    "s_barrier \n\t" \
    "s_add_u32 m0, %[wm], 0x0\n\t" \
    "s_mov_b32 s44, 0x300000\n\t" \
    "buffer_load_dwordx4 v240, %[wr], s44 offen offset:0 nt lds\n\t" \
    "buffer_load_dwordx4 v241, %[wr], s44 offen offset:1024 nt lds\n\t" \
    "buffer_load_dwordx4 v242, %[wr], s44 offen offset:2048 nt lds\n\t" \
    "ds_read_b64_tr_b16 v[20:21], v244 offset:0\n\t" \
    "ds_read_b64_tr_b16 v[22:23], v244 offset:1536\n\t" \
    "ds_read_b64_tr_b16 v[28:29], v244 offset:32\n\t" \
    "ds_read_b64_tr_b16 v[30:31], v244 offset:1568\n\t" \
    "ds_read_b64_tr_b16 v[36:37], v244 offset:64\n\t" \
    "ds_read_b64_tr_b16 v[38:39], v244 offset:1600\n\t" \
    "v_add_u32 v244, 0xc000, v243\n\t" \
    "s_waitcnt vmcnt(12) lgkmcnt(0)\n\t" \
    "s_barrier \n\t" \
    "s_add_u32 m0, %[wm], 0x6000\n\t" \
    "s_mov_b32 s44, 0x360000\n\t" \
    "buffer_load_dwordx4 v240, %[wr], s44 offen offset:0 nt lds\n\t" \
    "buffer_load_dwordx4 v241, %[wr], s44 offen offset:1024 nt lds\n\t" \
    "buffer_load_dwordx4 v242, %[wr], s44 offen offset:2048 nt lds\n\t" \
    "ds_read_b64_tr_b16 v[208:209], v244 offset:0\n\t" \
    "ds_read_b64_tr_b16 v[210:211], v244 offset:1536\n\t" \
    "ds_read_b64_tr_b16 v[40:41], v244 offset:32\n\t" \
    "ds_read_b64_tr_b16 v[42:43], v244 offset:1568\n\t" \
    "ds_read_b64_tr_b16 v[48:49], v244 offset:64\n\t" \
    "ds_read_b64_tr_b16 v[50:51], v244 offset:1600\n\t" \
    "v_add_u32 v244, 0x12000, v243\n\t" \
    "s_waitcnt vmcnt(12) lgkmcnt(0)\n\t" \
    "s_barrier \n\t" \
    "s_add_u32 m0, %[wm], 0xc000\n\t" \
    "s_mov_b32 s44, 0x3c0000\n\t" \
    "buffer_load_dwordx4 v240, %[wr], s44 offen offset:0 nt lds\n\t" \
    "buffer_load_dwordx4 v241, %[wr], s44 offen offset:1024 nt lds\n\t" \
    "buffer_load_dwordx4 v242, %[wr], s44 offen offset:2048 nt lds\n\t" \
    "ds_read_b64_tr_b16 v[212:213], v244 offset:0\n\t" \
    "ds_read_b64_tr_b16 v[214:215], v244 offset:1536\n\t" \
    "ds_read_b64_tr_b16 v[44:45], v244 offset:32\n\t" \
    "ds_read_b64_tr_b16 v[46:47], v244 offset:1568\n\t" \
    "ds_read_b64_tr_b16 v[52:53], v244 offset:64\n\t" \
    "ds_read_b64_tr_b16 v[54:55], v244 offset:1600\n\t" \
    "v_add_u32 v244, 0x18000, v243\n\t" \
    "s_waitcnt vmcnt(12) lgkmcnt(0)\n\t" \
    "s_barrier \n\t" \
    "s_add_u32 m0, %[wm], 0x12000\n\t" \
    "s_mov_b32 s44, 0x420000\n\t" \
    "buffer_load_dwordx4 v240, %[wr], s44 offen offset:0 nt lds\n\t" \
    "buffer_load_dwordx4 v241, %[wr], s44 offen offset:1024 nt lds\n\t" \
    "buffer_load_dwordx4 v242, %[wr], s44 offen offset:2048 nt lds\n\t" \
    "ds_read_b64_tr_b16 v[56:57], v244 offset:0\n\t" \
    "ds_read_b64_tr_b16 v[58:59], v244 offset:1536\n\t" \
    "ds_read_b64_tr_b16 v[64:65], v244 offset:32\n\t" \
    "ds_read_b64_tr_b16 v[66:67], v244 offset:1568\n\t" \
    "ds_read_b64_tr_b16 v[72:73], v244 offset:64\n\t" \
    "ds_read_b64_tr_b16 v[74:75], v244 offset:1600\n\t" \
    "v_add_u32 v244, 0x1e000, v243\n\t" \
    "s_waitcnt vmcnt(12) lgkmcnt(0)\n\t" \
    "s_barrier \n\t" \
    "s_add_u32 m0, %[wm], 0x18000\n\t" \
    "s_mov_b32 s44, 0x480000\n\t" \
    "buffer_load_dwordx4 v240, %[wr], s44 offen offset:0 nt lds\n\t" \
    "buffer_load_dwordx4 v241, %[wr], s44 offen offset:1024 nt lds\n\t" \
    "buffer_load_dwordx4 v242, %[wr], s44 offen offset:2048 nt lds\n\t" \
    "ds_read_b64_tr_b16 v[60:61], v244 offset:0\n\t" \
    "ds_read_b64_tr_b16 v[62:63], v244 offset:1536\n\t" \
    "ds_read_b64_tr_b16 v[68:69], v244 offset:32\n\t" \
    "ds_read_b64_tr_b16 v[70:71], v244 offset:1568\n\t" \
    "ds_read_b64_tr_b16 v[76:77], v244 offset:64\n\t" \
    "ds_read_b64_tr_b16 v[78:79], v244 offset:1600\n\t" \
    "v_add_u32 v244, 0x0, v243\n\t" \
    "s_waitcnt vmcnt(12) lgkmcnt(0)\n\t" \
    "s_barrier \n\t" \
    "v_mov_b32 v246, 0\n\t" \
    "buffer_load_dword v246, v246, s[60:63], %[mo] offen sc1\n\t" \
    "s_add_u32 m0, %[wm], 0x1e000\n\t" \
    "s_mov_b32 s44, 0x4e0000\n\t" \
    "buffer_load_dwordx4 v240, %[wr], s44 offen offset:0 nt lds\n\t" \
    "buffer_load_dwordx4 v241, %[wr], s44 offen offset:1024 nt lds\n\t" \
    "buffer_load_dwordx4 v242, %[wr], s44 offen offset:2048 nt lds\n\t" \
    "ds_read_b64_tr_b16 v[216:217], v244 offset:0\n\t" \
    "ds_read_b64_tr_b16 v[218:219], v244 offset:1536\n\t" \
    "ds_read_b64_tr_b16 v[80:81], v244 offset:32\n\t" \
    "ds_read_b64_tr_b16 v[82:83], v244 offset:1568\n\t" \
    "ds_read_b64_tr_b16 v[88:89], v244 offset:64\n\t" \
    "ds_read_b64_tr_b16 v[90:91], v244 offset:1600\n\t" \
    "v_add_u32 v244, 0x6000, v243\n\t" \
    "s_waitcnt vmcnt(12) lgkmcnt(0)\n\t" \
    "s_barrier \n\t" \
    "s_add_u32 m0, %[wm], 0x0\n\t" \
    "s_mov_b32 s44, 0x540000\n\t" \
    "buffer_load_dwordx4 v240, %[wr], s44 offen offset:0 nt lds\n\t" \
    "buffer_load_dwordx4 v241, %[wr], s44 offen offset:1024 nt lds\n\t" \
    "buffer_load_dwordx4 v242, %[wr], s44 offen offset:2048 nt lds\n\t" \
    "ds_read_b64_tr_b16 v[220:221], v244 offset:0\n\t" \
    "ds_read_b64_tr_b16 v[222:223], v244 offset:1536\n\t" \
    "ds_read_b64_tr_b16 v[84:85], v244 offset:32\n\t" \
    "ds_read_b64_tr_b16 v[86:87], v244 offset:1568\n\t" \
    "ds_read_b64_tr_b16 v[92:93], v244 offset:64\n\t" \
    "ds_read_b64_tr_b16 v[94:95], v244 offset:1600\n\t" \
    "v_add_u32 v244, 0xc000, v243\n\t" \
    "s_waitcnt vmcnt(12) lgkmcnt(0)\n\t" \
    "s_barrier \n\t" \
    "s_add_u32 m0, %[wm], 0x6000\n\t" \
    "s_mov_b32 s44, 0x5a0000\n\t" \
    "buffer_load_dwordx4 v240, %[wr], s44 offen offset:0 nt lds\n\t" \
    "buffer_load_dwordx4 v241, %[wr], s44 offen offset:1024 nt lds\n\t" \
    "buffer_load_dwordx4 v242, %[wr], s44 offen offset:2048 nt lds\n\t" \
    "ds_read_b64_tr_b16 v[96:97], v244 offset:0\n\t" \
    "ds_read_b64_tr_b16 v[98:99], v244 offset:1536\n\t" \
    "ds_read_b64_tr_b16 v[104:105], v244 offset:32\n\t" \
    "ds_read_b64_tr_b16 v[106:107], v244 offset:1568\n\t" \
    "ds_read_b64_tr_b16 v[112:113], v244 offset:64\n\t" \
    "ds_read_b64_tr_b16 v[114:115], v244 offset:1600\n\t" \
    "v_add_u32 v244, 0x12000, v243\n\t" \
    "s_waitcnt vmcnt(12) lgkmcnt(0)\n\t" \
    "s_barrier \n\t" \
    "s_add_u32 m0, %[wm], 0xc000\n\t" \
    "s_mov_b32 s44, 0x600000\n\t" \
    "buffer_load_dwordx4 v240, %[wr], s44 offen offset:0 nt lds\n\t" \
    "buffer_load_dwordx4 v241, %[wr], s44 offen offset:1024 nt lds\n\t" \
    "buffer_load_dwordx4 v242, %[wr], s44 offen offset:2048 nt lds\n\t" \
    "ds_read_b64_tr_b16 v[100:101], v244 offset:0\n\t" \
    "ds_read_b64_tr_b16 v[102:103], v244 offset:1536\n\t" \
    "ds_read_b64_tr_b16 v[108:109], v244 offset:32\n\t" \
    "ds_read_b64_tr_b16 v[110:111], v244 offset:1568\n\t" \
    "ds_read_b64_tr_b16 v[116:117], v244 offset:64\n\t" \
    "ds_read_b64_tr_b16 v[118:119], v244 offset:1600\n\t" \
    "v_add_u32 v244, 0x18000, v243\n\t" \
    "s_waitcnt vmcnt(12) lgkmcnt(0)\n\t" \
    "s_barrier \n\t" \
    "s_add_u32 m0, %[wm], 0x12000\n\t" \
    "s_mov_b32 s44, 0x660000\n\t" \
    "buffer_load_dwordx4 v240, %[wr], s44 offen offset:0 nt lds\n\t" \
    "buffer_load_dwordx4 v241, %[wr], s44 offen offset:1024 nt lds\n\t" \
    "buffer_load_dwordx4 v242, %[wr], s44 offen offset:2048 nt lds\n\t" \
    "ds_read_b64_tr_b16 v[224:225], v244 offset:0\n\t" \
    "ds_read_b64_tr_b16 v[226:227], v244 offset:1536\n\t" \
    "ds_read_b64_tr_b16 v[120:121], v244 offset:32\n\t" \
    "ds_read_b64_tr_b16 v[122:123], v244 offset:1568\n\t" \
    "ds_read_b64_tr_b16 v[128:129], v244 offset:64\n\t" \
    "ds_read_b64_tr_b16 v[130:131], v244 offset:1600\n\t" \
    "v_add_u32 v244, 0x1e000, v243\n\t" \
    "s_waitcnt vmcnt(12) lgkmcnt(0)\n\t" \
    "s_barrier \n\t" \
    ".Ls41_%=:\n\t" \
    "v_readfirstlane_b32 s43, v246\n\t" \
    "s_nop 0\n\t" \
    "s_cmp_lg_u32 s43, -1\n\t" \
    "s_cbranch_scc1 .Ls42_%=\n\t" \
    "s_cmp_eq_u32 s41, 0\n\t" \
    "s_cbranch_scc1 .Ls42_%=\n\t" \
    "s_sub_u32 s41, s41, 1\n\t" \
    "s_sleep 4\n\t" \
    "v_mov_b32 v246, 0\n\t" \
    "buffer_load_dword v246, v246, s[60:63], %[mo] offen sc1\n\t" \
    "s_waitcnt vmcnt(0)\n\t" \
    "s_branch .Ls41_%=\n\t" \
    ".Ls42_%=:\n\t" \
    "v_mov_b32 v246, 0x8000\n\t" \
    "s_mov_b64 exec, 1\n\t" \
    "buffer_store_dword v246, v246, s[60:63], %[mo] offen sc1\n\t" \
    "s_mov_b64 exec, -1\n\t" \
    "s_and_b32 s43, s43, 0x2000\n\t" \
    "s_xor_b32 s43, s43, %[lx]\n\t" \
    "s_add_u32 s60, s60, s43\n\t" \
    "s_addc_u32 s61, s61, 0\n\t" \
    "v_and_b32 v252, 0xf0, v253\n\t" \
    "v_lshlrev_b32 v252, 3, v252\n\t" \
    "buffer_load_dword v252, v252, s[60:63], %[fl] offen sc1\n\t" \
    "s_add_u32 m0, %[wm], 0x18000\n\t" \
    "s_mov_b32 s44, 0x6c0000\n\t" \
    "buffer_load_dwordx4 v240, %[wr], s44 offen offset:0 nt lds\n\t" \
    "buffer_load_dwordx4 v241, %[wr], s44 offen offset:1024 nt lds\n\t" \
    "buffer_load_dwordx4 v242, %[wr], s44 offen offset:2048 nt lds\n\t" \
    "ds_read_b64_tr_b16 v[228:229], v244 offset:0\n\t" \
    "ds_read_b64_tr_b16 v[230:231], v244 offset:1536\n\t" \
    "ds_read_b64_tr_b16 v[124:125], v244 offset:32\n\t" \
    "ds_read_b64_tr_b16 v[126:127], v244 offset:1568\n\t" \
    "ds_read_b64_tr_b16 v[132:133], v244 offset:64\n\t" \
    "ds_read_b64_tr_b16 v[134:135], v244 offset:1600\n\t" \
    "v_add_u32 v244, 0x0, v243\n\t" \
    "s_waitcnt vmcnt(12) lgkmcnt(0)\n\t" \
    "s_barrier \n\t" \
    "s_add_u32 m0, %[wm], 0x1e000\n\t" \
    "s_mov_b32 s44, 0x720000\n\t" \
    "buffer_load_dwordx4 v240, %[wr], s44 offen offset:0 nt lds\n\t" \
    "buffer_load_dwordx4 v241, %[wr], s44 offen offset:1024 nt lds\n\t" \
    "buffer_load_dwordx4 v242, %[wr], s44 offen offset:2048 nt lds\n\t" \
    "ds_read_b64_tr_b16 v[136:137], v244 offset:0\n\t" \
    "ds_read_b64_tr_b16 v[138:139], v244 offset:1536\n\t" \
    "ds_read_b64_tr_b16 v[144:145], v244 offset:32\n\t" \
    "ds_read_b64_tr_b16 v[146:147], v244 offset:1568\n\t" \
    "ds_read_b64_tr_b16 v[152:153], v244 offset:64\n\t" \
    "ds_read_b64_tr_b16 v[154:155], v244 offset:1600\n\t" \
    "v_add_u32 v244, 0x6000, v243\n\t" \
    "s_waitcnt vmcnt(12) lgkmcnt(0)\n\t" \
    "s_barrier \n\t" \
    "ds_read_b64_tr_b16 v[140:141], v244 offset:0\n\t" \
    "ds_read_b64_tr_b16 v[142:143], v244 offset:1536\n\t" \
    "ds_read_b64_tr_b16 v[148:149], v244 offset:32\n\t" \
    "ds_read_b64_tr_b16 v[150:151], v244 offset:1568\n\t" \
    "ds_read_b64_tr_b16 v[156:157], v244 offset:64\n\t" \
    "ds_read_b64_tr_b16 v[158:159], v244 offset:1600\n\t" \
    "v_add_u32 v244, 0xc000, v243\n\t" \
    "s_waitcnt vmcnt(9) lgkmcnt(0)\n\t" \
    "s_barrier \n\t" \
    "ds_read_b64_tr_b16 v[232:233], v244 offset:0\n\t" \
    "ds_read_b64_tr_b16 v[234:235], v244 offset:1536\n\t" \
    "ds_read_b64_tr_b16 v[160:161], v244 offset:32\n\t" \
    "ds_read_b64_tr_b16 v[162:163], v244 offset:1568\n\t" \
    "ds_read_b64_tr_b16 v[168:169], v244 offset:64\n\t" \
    "ds_read_b64_tr_b16 v[170:171], v244 offset:1600\n\t" \
    "v_add_u32 v244, 0x12000, v243\n\t" \
    "s_waitcnt vmcnt(6) lgkmcnt(0)\n\t" \
    "s_barrier \n\t" \
    "ds_read_b64_tr_b16 v[236:237], v244 offset:0\n\t" \
    "ds_read_b64_tr_b16 v[238:239], v244 offset:1536\n\t" \
    "ds_read_b64_tr_b16 v[164:165], v244 offset:32\n\t" \
    "ds_read_b64_tr_b16 v[166:167], v244 offset:1568\n\t" \
    "ds_read_b64_tr_b16 v[172:173], v244 offset:64\n\t" \
    "ds_read_b64_tr_b16 v[174:175], v244 offset:1600\n\t" \
    "v_add_u32 v244, 0x18000, v243\n\t" \
    "s_waitcnt vmcnt(3) lgkmcnt(0)\n\t" \
    "s_barrier \n\t" \
    "ds_read_b64_tr_b16 v[176:177], v244 offset:0\n\t" \
    "ds_read_b64_tr_b16 v[178:179], v244 offset:1536\n\t" \
    "ds_read_b64_tr_b16 v[184:185], v244 offset:32\n\t" \
    "ds_read_b64_tr_b16 v[186:187], v244 offset:1568\n\t" \
    "ds_read_b64_tr_b16 v[192:193], v244 offset:64\n\t" \
    "ds_read_b64_tr_b16 v[194:195], v244 offset:1600\n\t" \
    "s_mov_b32 s43, 0\n\t" \
    "v_cmp_eq_u32 vcc, 0, v252\n\t" \
    "s_cbranch_vccnz .Ls43_%=\n\t" \
    "s_mov_b32 m0, %[m0]\n\t" \
    "s_add_u32 s45, s42, %[x1]\n\t" \
    "buffer_load_dwordx4 v253, s[60:63], s42 offen offset:0 sc1 lds\n\t" \
    "buffer_load_dwordx4 v253, s[60:63], s42 offen offset:1024 sc1 lds\n\t" \
    "buffer_load_dwordx4 v253, s[60:63], s42 offen offset:2048 sc1 lds\n\t" \
    "buffer_load_dwordx4 v253, s[60:63], s42 offen offset:3072 sc1 lds\n\t" \
    "s_mov_b32 m0, %[m1]\n\t" \
    "s_nop 0\n\t" \
    "buffer_load_dwordx4 v253, s[60:63], s45 offen offset:0 sc1 lds\n\t" \
    "buffer_load_dwordx4 v253, s[60:63], s45 offen offset:1024 sc1 lds\n\t" \
    "s_mov_b32 s43, 1\n\t" \
    ".Ls43_%=:\n\t" \
    "v_add_u32 v244, 0x1e000, v243\n\t" \
    "s_waitcnt vmcnt(0) lgkmcnt(0)\n\t" \
    "s_barrier \n\t" \
    "ds_read_b64_tr_b16 v[180:181], v244 offset:0\n\t" \
    "ds_read_b64_tr_b16 v[182:183], v244 offset:1536\n\t" \
    "ds_read_b64_tr_b16 v[188:189], v244 offset:32\n\t" \
    "ds_read_b64_tr_b16 v[190:191], v244 offset:1568\n\t" \
    "ds_read_b64_tr_b16 v[196:197], v244 offset:64\n\t" \
    "ds_read_b64_tr_b16 v[198:199], v244 offset:1600\n\t" \
    "s_waitcnt lgkmcnt(0)\n\t" \
    "s_barrier \n\t" \
    LST0 \
    LJIT \
    "ds_write_b128 v254, v[200:203] offset:0\n\t" \
    "ds_write_b128 v254, v[204:207] offset:1024\n\t" \
    "ds_write_b128 v254, v[208:211] offset:2048\n\t" \
    "ds_write_b128 v254, v[212:215] offset:3072\n\t" \
    "ds_write_b128 v254, v[216:219] offset:4096\n\t" \
    "ds_write_b128 v254, v[220:223] offset:5120\n\t" \
    "ds_write_b128 v254, v[224:227] offset:6144\n\t" \
    "ds_write_b128 v254, v[228:231] offset:7168\n\t" \
    "ds_write_b128 v254, v[232:235] offset:8192\n\t" \
    "ds_write_b128 v254, v[236:239] offset:9216\n\t" \
    "s_cmp_lg_u32 s43, 0\n\t" \
    "s_cbranch_scc1 .Ls44_%=\n\t" \
    "s_mov_b32 s44, 0\n\t" \
    ".Ls45_%=:\n\t" \
    "v_cmp_ge_u32 vcc, s44, v252\n\t" \
    "s_cbranch_vccz .Ls46_%=\n\t" \
    "s_cmp_eq_u32 s41, 0\n\t" \
    "s_cbranch_scc1 .Ls46_%=\n\t" \
    "s_sub_u32 s41, s41, 1\n\t" \
    "s_sleep 8\n\t" \
    "v_and_b32 v252, 0xf0, v253\n\t" \
    "v_lshlrev_b32 v252, 3, v252\n\t" \
    "buffer_load_dword v252, v252, s[60:63], %[fl] offen sc1\n\t" \
    "s_waitcnt vmcnt(0)\n\t" \
    "s_branch .Ls45_%=\n\t" \
    ".Ls46_%=:\n\t" \
    "s_mov_b32 m0, %[m0]\n\t" \
    "s_add_u32 s45, s42, %[x1]\n\t" \
    "buffer_load_dwordx4 v253, s[60:63], s42 offen offset:0 sc1 lds\n\t" \
    "buffer_load_dwordx4 v253, s[60:63], s42 offen offset:1024 sc1 lds\n\t" \
    "buffer_load_dwordx4 v253, s[60:63], s42 offen offset:2048 sc1 lds\n\t" \
    "buffer_load_dwordx4 v253, s[60:63], s42 offen offset:3072 sc1 lds\n\t" \
    "s_mov_b32 m0, %[m1]\n\t" \
    "s_nop 0\n\t" \
    "buffer_load_dwordx4 v253, s[60:63], s45 offen offset:0 sc1 lds\n\t" \
    "buffer_load_dwordx4 v253, s[60:63], s45 offen offset:1024 sc1 lds\n\t" \
    ".Ls44_%=:\n\t" \
    "s_waitcnt vmcnt(0) lgkmcnt(0)\n\t" \
    "s_barrier \n\t" \
    "ds_read_b128 v[224:227], v253 offset:0\n\t" \
    "ds_read_b128 v[244:247], v254 offset:0\n\t" \
    "ds_read_b128 v[228:231], v253 offset:1024\n\t" \
    "ds_read_b128 v[232:235], v253 offset:20480\n\t" \
    "ds_read_b128 v[236:239], v253 offset:21504\n\t" \
    "ds_read_b128 v[248:251], v254 offset:1024\n\t" \
    ".Ls47_%=:\n\t" \
    "s_cmp_lg_u32 %[wave], 4\n\t" \
    "s_cbranch_scc1 .Ls48_%=\n\t" \
    "s_sleep 9\n\t" \
    ".Ls48_%=:\n\t" \
    "s_waitcnt lgkmcnt(5)\n\t" \
    "v_mfma_f32_16x16x32_bf16 v[204:207], v[0:3], v[224:227], 0\n\t" \
    "s_add_u32 m0, %[m0], 0x3000\n\t" \
    "s_waitcnt lgkmcnt(3)\n\t" \
    "v_mfma_f32_16x16x32_bf16 v[204:207], v[4:7], v[228:231], v[204:207]\n\t" \
    "ds_read_b128 v[240:243], v253 offset:2048\n\t" \
    "s_add_u32 s45, s42, 0xc000\n\t" \
    "v_mfma_f32_16x16x32_bf16 v[208:211], v[8:11], v[224:227], 0\n\t" \
    "buffer_load_dwordx4 v253, s[60:63], s45 offen offset:0 sc1 lds\n\t" \
    "v_mfma_f32_16x16x32_bf16 v[208:211], v[12:15], v[228:231], v[208:211]\n\t" \
    "v_mfma_f32_16x16x32_bf16 v[200:203], v[244:247], v[224:227], 0\n\t" \
    "buffer_load_dwordx4 v253, s[60:63], s45 offen offset:1024 sc1 lds\n\t" \
    "s_waitcnt lgkmcnt(1)\n\t" \
    "v_mfma_f32_16x16x32_bf16 v[200:203], v[248:251], v[228:231], v[200:203]\n\t" \
    "v_mfma_f32_16x16x32_bf16 v[216:219], v[0:3], v[232:235], 0\n\t" \
    "ds_read_b128 v[224:227], v253 offset:3072\n\t" \
    "buffer_load_dwordx4 v253, s[60:63], s45 offen offset:2048 sc1 lds\n\t" \
    "v_mfma_f32_16x16x32_bf16 v[216:219], v[4:7], v[236:239], v[216:219]\n\t" \
    "ds_read_b128 v[228:231], v253 offset:22528\n\t" \
    "v_mfma_f32_16x16x32_bf16 v[220:223], v[8:11], v[232:235], 0\n\t" \
    "buffer_load_dwordx4 v253, s[60:63], s45 offen offset:3072 sc1 lds\n\t" \
    "v_mfma_f32_16x16x32_bf16 v[220:223], v[12:15], v[236:239], v[220:223]\n\t" \
    "v_mfma_f32_16x16x32_bf16 v[212:215], v[244:247], v[232:235], 0\n\t" \
    "ds_read_b128 v[244:247], v254 offset:2048\n\t" \
    "v_mfma_f32_16x16x32_bf16 v[212:215], v[248:251], v[236:239], v[212:215]\n\t" \
    "ds_read_b128 v[248:251], v254 offset:3072\n\t" \
    "s_waitcnt lgkmcnt(4)\n\t" \
    "v_mfma_f32_16x16x32_bf16 v[204:207], v[24:27], v[240:243], v[204:207]\n\t" \
    "ds_read_b128 v[232:235], v253 offset:23552\n\t" \
    "s_waitcnt lgkmcnt(4)\n\t" \
    "v_mfma_f32_16x16x32_bf16 v[204:207], v[28:31], v[224:227], v[204:207]\n\t" \
    "ds_read_b128 v[236:239], v253 offset:4096\n\t" \
    "v_mfma_f32_16x16x32_bf16 v[208:211], v[32:35], v[240:243], v[208:211]\n\t" \
    "v_mfma_f32_16x16x32_bf16 v[208:211], v[36:39], v[224:227], v[208:211]\n\t" \
    "v_mfma_f32_16x16x32_bf16 v[200:203], v[16:19], v[240:243], v[200:203]\n\t" \
    "v_mfma_f32_16x16x32_bf16 v[200:203], v[20:23], v[224:227], v[200:203]\n\t" \
    "s_waitcnt lgkmcnt(4)\n\t" \
    "v_mfma_f32_16x16x32_bf16 v[216:219], v[24:27], v[228:231], v[216:219]\n\t" \
    "ds_read_b128 v[240:243], v253 offset:5120\n\t" \
    "s_waitcnt lgkmcnt(2)\n\t" \
    "v_mfma_f32_16x16x32_bf16 v[216:219], v[28:31], v[232:235], v[216:219]\n\t" \
    "ds_read_b128 v[224:227], v253 offset:24576\n\t" \
    "v_mfma_f32_16x16x32_bf16 v[220:223], v[32:35], v[228:231], v[220:223]\n\t" \
    "v_mfma_f32_16x16x32_bf16 v[220:223], v[36:39], v[232:235], v[220:223]\n\t" \
    "v_mfma_f32_16x16x32_bf16 v[212:215], v[16:19], v[228:231], v[212:215]\n\t" \
    "v_mfma_f32_16x16x32_bf16 v[212:215], v[20:23], v[232:235], v[212:215]\n\t" \
    "s_waitcnt lgkmcnt(2)\n\t" \
    "v_mfma_f32_16x16x32_bf16 v[204:207], v[40:43], v[236:239], v[204:207]\n\t" \
    "ds_read_b128 v[228:231], v253 offset:25600\n\t" \
    "s_waitcnt lgkmcnt(2)\n\t" \
    "v_mfma_f32_16x16x32_bf16 v[204:207], v[44:47], v[240:243], v[204:207]\n\t" \
    "ds_read_b128 v[232:235], v253 offset:6144\n\t" \
    "v_mfma_f32_16x16x32_bf16 v[208:211], v[48:51], v[236:239], v[208:211]\n\t" \
    "v_mfma_f32_16x16x32_bf16 v[208:211], v[52:55], v[240:243], v[208:211]\n\t" \
    "v_mfma_f32_16x16x32_bf16 v[200:203], v[244:247], v[236:239], v[200:203]\n\t" \
    "v_mfma_f32_16x16x32_bf16 v[200:203], v[248:251], v[240:243], v[200:203]\n\t" \
    "s_waitcnt lgkmcnt(2)\n\t" \
    "v_mfma_f32_16x16x32_bf16 v[216:219], v[40:43], v[224:227], v[216:219]\n\t" \
    "ds_read_b128 v[236:239], v253 offset:7168\n\t" \
    "s_waitcnt lgkmcnt(2)\n\t" \
    "v_mfma_f32_16x16x32_bf16 v[216:219], v[44:47], v[228:231], v[216:219]\n\t" \
    "ds_read_b128 v[240:243], v253 offset:26624\n\t" \
    "v_mfma_f32_16x16x32_bf16 v[220:223], v[48:51], v[224:227], v[220:223]\n\t" \
    "v_mfma_f32_16x16x32_bf16 v[220:223], v[52:55], v[228:231], v[220:223]\n\t" \
    "v_mfma_f32_16x16x32_bf16 v[212:215], v[244:247], v[224:227], v[212:215]\n\t" \
    "ds_read_b128 v[244:247], v254 offset:4096\n\t" \
    "v_mfma_f32_16x16x32_bf16 v[212:215], v[248:251], v[228:231], v[212:215]\n\t" \
    "ds_read_b128 v[248:251], v254 offset:5120\n\t" \
    "s_waitcnt lgkmcnt(4)\n\t" \
    "v_mfma_f32_16x16x32_bf16 v[204:207], v[64:67], v[232:235], v[204:207]\n\t" \
    "ds_read_b128 v[224:227], v253 offset:27648\n\t" \
    "s_waitcnt lgkmcnt(4)\n\t" \
    "v_mfma_f32_16x16x32_bf16 v[204:207], v[68:71], v[236:239], v[204:207]\n\t" \
    "ds_read_b128 v[228:231], v253 offset:8192\n\t" \
    "v_mfma_f32_16x16x32_bf16 v[208:211], v[72:75], v[232:235], v[208:211]\n\t" \
    "v_mfma_f32_16x16x32_bf16 v[208:211], v[76:79], v[236:239], v[208:211]\n\t" \
    "v_mfma_f32_16x16x32_bf16 v[200:203], v[56:59], v[232:235], v[200:203]\n\t" \
    "v_mfma_f32_16x16x32_bf16 v[200:203], v[60:63], v[236:239], v[200:203]\n\t" \
    "v_and_b32 v252, 0xf0, v253\n\t" \
    "v_lshlrev_b32 v252, 3, v252\n\t" \
    "buffer_load_dword v252, v252, s[60:63], %[fl] offen sc1\n\t" \
    "s_waitcnt lgkmcnt(4)\n\t" \
    "v_mfma_f32_16x16x32_bf16 v[216:219], v[64:67], v[240:243], v[216:219]\n\t" \
    "ds_read_b128 v[232:235], v253 offset:9216\n\t" \
    "s_waitcnt lgkmcnt(2)\n\t" \
    "v_mfma_f32_16x16x32_bf16 v[216:219], v[68:71], v[224:227], v[216:219]\n\t" \
    "ds_read_b128 v[236:239], v253 offset:28672\n\t" \
    "v_mfma_f32_16x16x32_bf16 v[220:223], v[72:75], v[240:243], v[220:223]\n\t" \
    "v_mfma_f32_16x16x32_bf16 v[220:223], v[76:79], v[224:227], v[220:223]\n\t" \
    "v_mfma_f32_16x16x32_bf16 v[212:215], v[56:59], v[240:243], v[212:215]\n\t" \
    "v_mfma_f32_16x16x32_bf16 v[212:215], v[60:63], v[224:227], v[212:215]\n\t" \
    "s_waitcnt lgkmcnt(2)\n\t" \
    "v_mfma_f32_16x16x32_bf16 v[204:207], v[80:83], v[228:231], v[204:207]\n\t" \
    "ds_read_b128 v[240:243], v253 offset:29696\n\t" \
    "s_waitcnt lgkmcnt(2)\n\t" \
    "v_mfma_f32_16x16x32_bf16 v[204:207], v[84:87], v[232:235], v[204:207]\n\t" \
    "ds_read_b128 v[224:227], v253 offset:10240\n\t" \
    "v_mfma_f32_16x16x32_bf16 v[208:211], v[88:91], v[228:231], v[208:211]\n\t" \
    "v_mfma_f32_16x16x32_bf16 v[208:211], v[92:95], v[232:235], v[208:211]\n\t" \
    "v_mfma_f32_16x16x32_bf16 v[200:203], v[244:247], v[228:231], v[200:203]\n\t" \
    "v_mfma_f32_16x16x32_bf16 v[200:203], v[248:251], v[232:235], v[200:203]\n\t" \
    "s_waitcnt lgkmcnt(2)\n\t" \
    "v_mfma_f32_16x16x32_bf16 v[216:219], v[80:83], v[236:239], v[216:219]\n\t" \
    "ds_read_b128 v[228:231], v253 offset:11264\n\t" \
    "s_add_u32 s44, s40, 1\n\t" \
    "s_waitcnt lgkmcnt(2)\n\t" \
    "v_mfma_f32_16x16x32_bf16 v[216:219], v[84:87], v[240:243], v[216:219]\n\t" \
    "ds_read_b128 v[232:235], v253 offset:30720\n\t" \
    "s_and_b32 s45, s44, 15\n\t" \
    "v_mfma_f32_16x16x32_bf16 v[220:223], v[88:91], v[236:239], v[220:223]\n\t" \
    "s_cmp_eq_u32 s45, 0\n\t" \
    "v_mfma_f32_16x16x32_bf16 v[220:223], v[92:95], v[240:243], v[220:223]\n\t" \
    "s_cselect_b32 s45, %[cj], %[cs]\n\t" \
    "v_mfma_f32_16x16x32_bf16 v[212:215], v[244:247], v[236:239], v[212:215]\n\t" \
    "ds_read_b128 v[244:247], v254 offset:6144\n\t" \
    "s_cmp_eq_u32 s44, 64\n\t" \
    "v_mfma_f32_16x16x32_bf16 v[212:215], v[248:251], v[240:243], v[212:215]\n\t" \
    "ds_read_b128 v[248:251], v254 offset:7168\n\t" \
    "s_cselect_b32 s45, 0, s45\n\t" \
    "ds_read_b128 v[236:239], v253 offset:31744\n\t" \
    "s_setprio 1\n\t" \
    "s_waitcnt vmcnt(0) lgkmcnt(0)\n\t" \
    "s_barrier \n\t" \
    LST7 \
    LJIT \
    "s_add_u32 s42, s42, s45\n\t" \
    "s_add_u32 s44, s40, 1\n\t" \
    "s_min_u32 s44, s44, 63\n\t" \
    ".Ls49_%=:\n\t" \
    "v_cmp_ge_u32 vcc, s44, v252\n\t" \
    "s_cbranch_vccz .Ls410_%=\n\t" \
    "s_cmp_eq_u32 s41, 0\n\t" \
    "s_cbranch_scc1 .Ls410_%=\n\t" \
    "s_sub_u32 s41, s41, 1\n\t" \
    "s_sleep 2\n\t" \
    "v_and_b32 v252, 0xf0, v253\n\t" \
    "v_lshlrev_b32 v252, 3, v252\n\t" \
    "buffer_load_dword v252, v252, s[60:63], %[fl] offen sc1\n\t" \
    "s_waitcnt vmcnt(0)\n\t" \
    "s_branch .Ls49_%=\n\t" \
    ".Ls410_%=:\n\t" \
    "s_mov_b32 m0, %[m0]\n\t" \
    "s_add_u32 s45, s42, %[x1]\n\t" \
    "buffer_load_dwordx4 v253, s[60:63], s42 offen offset:0 sc1 lds\n\t" \
    "buffer_load_dwordx4 v253, s[60:63], s42 offen offset:1024 sc1 lds\n\t" \
    "buffer_load_dwordx4 v253, s[60:63], s42 offen offset:2048 sc1 lds\n\t" \
    "buffer_load_dwordx4 v253, s[60:63], s42 offen offset:3072 sc1 lds\n\t" \
    "s_mov_b32 m0, %[m1]\n\t" \
    "s_nop 0\n\t" \
    "buffer_load_dwordx4 v253, s[60:63], s45 offen offset:0 sc1 lds\n\t" \
    "buffer_load_dwordx4 v253, s[60:63], s45 offen offset:1024 sc1 lds\n\t" \
    "v_mfma_f32_16x16x32_bf16 v[204:207], v[104:107], v[224:227], v[204:207]\n\t" \
    "v_mfma_f32_16x16x32_bf16 v[204:207], v[108:111], v[228:231], v[204:207]\n\t" \
    "ds_read_b128 v[240:243], v253 offset:12288\n\t" \
    "v_mfma_f32_16x16x32_bf16 v[208:211], v[112:115], v[224:227], v[208:211]\n\t" \
    "v_mfma_f32_16x16x32_bf16 v[208:211], v[116:119], v[228:231], v[208:211]\n\t" \
    "v_mfma_f32_16x16x32_bf16 v[200:203], v[96:99], v[224:227], v[200:203]\n\t" \
    "v_mfma_f32_16x16x32_bf16 v[200:203], v[100:103], v[228:231], v[200:203]\n\t" \
    "v_mfma_f32_16x16x32_bf16 v[216:219], v[104:107], v[232:235], v[216:219]\n\t" \
    "ds_read_b128 v[224:227], v253 offset:13312\n\t" \
    "v_mfma_f32_16x16x32_bf16 v[216:219], v[108:111], v[236:239], v[216:219]\n\t" \
    "ds_read_b128 v[228:231], v253 offset:32768\n\t" \
    "v_mfma_f32_16x16x32_bf16 v[220:223], v[112:115], v[232:235], v[220:223]\n\t" \
    "v_mfma_f32_16x16x32_bf16 v[220:223], v[116:119], v[236:239], v[220:223]\n\t" \
    "v_mfma_f32_16x16x32_bf16 v[212:215], v[96:99], v[232:235], v[212:215]\n\t" \
    "v_mfma_f32_16x16x32_bf16 v[212:215], v[100:103], v[236:239], v[212:215]\n\t" \
    "s_waitcnt lgkmcnt(2)\n\t" \
    "v_mfma_f32_16x16x32_bf16 v[204:207], v[120:123], v[240:243], v[204:207]\n\t" \
    "ds_read_b128 v[232:235], v253 offset:33792\n\t" \
    "s_waitcnt lgkmcnt(2)\n\t" \
    "v_mfma_f32_16x16x32_bf16 v[204:207], v[124:127], v[224:227], v[204:207]\n\t" \
    "ds_read_b128 v[236:239], v253 offset:16384\n\t" \
    "v_mfma_f32_16x16x32_bf16 v[208:211], v[128:131], v[240:243], v[208:211]\n\t" \
    "v_mfma_f32_16x16x32_bf16 v[208:211], v[132:135], v[224:227], v[208:211]\n\t" \
    "v_mfma_f32_16x16x32_bf16 v[200:203], v[244:247], v[240:243], v[200:203]\n\t" \
    "v_mfma_f32_16x16x32_bf16 v[200:203], v[248:251], v[224:227], v[200:203]\n\t" \
    "s_waitcnt lgkmcnt(2)\n\t" \
    "v_mfma_f32_16x16x32_bf16 v[216:219], v[120:123], v[228:231], v[216:219]\n\t" \
    "ds_read_b128 v[240:243], v253 offset:17408\n\t" \
    "s_waitcnt lgkmcnt(2)\n\t" \
    "v_mfma_f32_16x16x32_bf16 v[216:219], v[124:127], v[232:235], v[216:219]\n\t" \
    "ds_read_b128 v[224:227], v253 offset:36864\n\t" \
    "v_mfma_f32_16x16x32_bf16 v[220:223], v[128:131], v[228:231], v[220:223]\n\t" \
    "v_mfma_f32_16x16x32_bf16 v[220:223], v[132:135], v[232:235], v[220:223]\n\t" \
    "v_mfma_f32_16x16x32_bf16 v[212:215], v[244:247], v[228:231], v[212:215]\n\t" \
    "ds_read_b128 v[244:247], v254 offset:8192\n\t" \
    "v_mfma_f32_16x16x32_bf16 v[212:215], v[248:251], v[232:235], v[212:215]\n\t" \
    "ds_read_b128 v[248:251], v254 offset:9216\n\t" \
    "s_waitcnt lgkmcnt(4)\n\t" \
    "v_mfma_f32_16x16x32_bf16 v[204:207], v[160:163], v[236:239], v[204:207]\n\t" \
    "ds_read_b128 v[228:231], v253 offset:37888\n\t" \
    "s_waitcnt lgkmcnt(4)\n\t" \
    "v_mfma_f32_16x16x32_bf16 v[204:207], v[164:167], v[240:243], v[204:207]\n\t" \
    "ds_read_b128 v[232:235], v253 offset:14336\n\t" \
    "v_mfma_f32_16x16x32_bf16 v[208:211], v[168:171], v[236:239], v[208:211]\n\t" \
    "v_mfma_f32_16x16x32_bf16 v[208:211], v[172:175], v[240:243], v[208:211]\n\t" \
    "s_waitcnt lgkmcnt(3)\n\t" \
    "v_mfma_f32_16x16x32_bf16 v[200:203], v[244:247], v[236:239], v[200:203]\n\t" \
    "s_waitcnt lgkmcnt(2)\n\t" \
    "v_mfma_f32_16x16x32_bf16 v[200:203], v[248:251], v[240:243], v[200:203]\n\t" \
    "v_mfma_f32_16x16x32_bf16 v[216:219], v[160:163], v[224:227], v[216:219]\n\t" \
    "ds_read_b128 v[236:239], v253 offset:15360\n\t" \
    "v_add_u32 v252, %[q2], v253\n\t" \
    "s_waitcnt lgkmcnt(2)\n\t" \
    "v_mfma_f32_16x16x32_bf16 v[216:219], v[164:167], v[228:231], v[216:219]\n\t" \
    "ds_read_b128 v[240:243], v253 offset:18432\n\t" \
    "v_mfma_f32_16x16x32_bf16 v[220:223], v[168:171], v[224:227], v[220:223]\n\t" \
    "v_mfma_f32_16x16x32_bf16 v[220:223], v[172:175], v[228:231], v[220:223]\n\t" \
    "v_mfma_f32_16x16x32_bf16 v[212:215], v[244:247], v[224:227], v[212:215]\n\t" \
    "v_mfma_f32_16x16x32_bf16 v[212:215], v[248:251], v[228:231], v[212:215]\n\t" \
    "s_waitcnt lgkmcnt(2)\n\t" \
    "v_mfma_f32_16x16x32_bf16 v[204:207], v[144:147], v[232:235], v[204:207]\n\t" \
    "ds_read_b128 v[224:227], v253 offset:19456\n\t" \
    "v_mov_b64 v[244:245], 0\n\t" \
    "s_waitcnt lgkmcnt(2)\n\t" \
    "v_mfma_f32_16x16x32_bf16 v[204:207], v[148:151], v[236:239], v[204:207]\n\t" \
    "ds_read_b128 v[228:231], v253 offset:34816\n\t" \
    "v_mov_b64 v[246:247], 0\n\t" \
    "v_mfma_f32_16x16x32_bf16 v[208:211], v[152:155], v[232:235], v[208:211]\n\t" \
    "v_mov_b64 v[248:249], 0\n\t" \
    "v_mfma_f32_16x16x32_bf16 v[208:211], v[156:159], v[236:239], v[208:211]\n\t" \
    "v_mov_b64 v[250:251], 0\n\t" \
    "v_mfma_f32_16x16x32_bf16 v[200:203], v[136:139], v[232:235], v[200:203]\n\t" \
    "v_mfma_f32_16x16x32_bf16 v[200:203], v[140:143], v[236:239], v[200:203]\n\t" \
    "s_waitcnt lgkmcnt(2)\n\t" \
    "v_mfma_f32_16x16x32_bf16 v[204:207], v[184:187], v[240:243], v[204:207]\n\t" \
    "ds_read_b128 v[232:235], v253 offset:35840\n\t" \
    "s_waitcnt lgkmcnt(2)\n\t" \
    "v_mfma_f32_16x16x32_bf16 v[204:207], v[188:191], v[224:227], v[204:207]\n\t" \
    "ds_read_b128 v[236:239], v253 offset:38912\n\t" \
    "v_mfma_f32_16x16x32_bf16 v[208:211], v[192:195], v[240:243], v[208:211]\n\t" \
    "v_mfma_f32_16x16x32_bf16 v[208:211], v[196:199], v[224:227], v[208:211]\n\t" \
    "v_mfma_f32_16x16x32_bf16 v[200:203], v[176:179], v[240:243], v[200:203]\n\t" \
    "v_mfma_f32_16x16x32_bf16 v[200:203], v[180:183], v[224:227], v[200:203]\n\t" \
    "ds_read_b128 v[240:243], v253 offset:39936\n\t" \
    "s_waitcnt lgkmcnt(3)\n\t" \
    "v_mfma_f32_16x16x32_bf16 v[216:219], v[144:147], v[228:231], v[216:219]\n\t" \
    "s_waitcnt lgkmcnt(2)\n\t" \
    "v_mfma_f32_16x16x32_bf16 v[216:219], v[148:151], v[232:235], v[216:219]\n\t" \
    "v_cvt_pk_bf16_f32 v204, v204, v208\n\t" \
    "v_cvt_pk_bf16_f32 v205, v205, v209\n\t" \
    "s_waitcnt lgkmcnt(1)\n\t" \
    "v_mfma_f32_16x16x32_bf16 v[216:219], v[184:187], v[236:239], v[216:219]\n\t" \
    "v_cvt_pk_bf16_f32 v206, v206, v210\n\t" \
    "v_cvt_pk_bf16_f32 v207, v207, v211\n\t" \
    "s_waitcnt lgkmcnt(0)\n\t" \
    "v_mfma_f32_16x16x32_bf16 v[216:219], v[188:191], v[240:243], v[216:219]\n\t" \
    "ds_write_b128 v252, v[204:207] offset:0\n\t" \
    "v_mfma_f32_16x16x32_bf16 v[220:223], v[152:155], v[228:231], v[220:223]\n\t" \
    "v_mfma_f32_16x16x32_bf16 v[220:223], v[156:159], v[232:235], v[220:223]\n\t" \
    "v_add_u32 v252, %[qs], v253\n\t" \
    "v_cvt_pk_bf16_f32 v200, v200, v201\n\t" \
    "v_mfma_f32_16x16x32_bf16 v[220:223], v[192:195], v[236:239], v[220:223]\n\t" \
    "v_cvt_pk_bf16_f32 v201, v202, v203\n\t" \
    "ds_write_b64 v252, v[200:201] offset:0\n\t" \
    "v_mfma_f32_16x16x32_bf16 v[220:223], v[196:199], v[240:243], v[220:223]\n\t" \
    "v_mfma_f32_16x16x32_bf16 v[212:215], v[136:139], v[228:231], v[212:215]\n\t" \
    "v_mov_b64 v[208:209], 0\n\t" \
    "v_mfma_f32_16x16x32_bf16 v[212:215], v[140:143], v[232:235], v[212:215]\n\t" \
    "v_mov_b64 v[210:211], 0\n\t" \
    "v_mfma_f32_16x16x32_bf16 v[212:215], v[176:179], v[236:239], v[212:215]\n\t" \
    "v_add_u32 v252, %[q2], v253\n\t" \
    "v_cvt_pk_bf16_f32 v216, v216, v220\n\t" \
    "v_mfma_f32_16x16x32_bf16 v[212:215], v[180:183], v[240:243], v[212:215]\n\t" \
    "v_cvt_pk_bf16_f32 v217, v217, v221\n\t" \
    "v_cvt_pk_bf16_f32 v218, v218, v222\n\t" \
    "v_cvt_pk_bf16_f32 v219, v219, v223\n\t" \
    "ds_write_b128 v252, v[216:219] offset:6144\n\t" \
    "v_add_u32 v252, %[qs], v253\n\t" \
    "s_nop 2\n\t" \
    "v_cvt_pk_bf16_f32 v212, v212, v213\n\t" \
    "v_cvt_pk_bf16_f32 v213, v214, v215\n\t" \
    "ds_write_b64 v252, v[212:213] offset:6144\n\t" \
    LST6 \
    LJIT \
    "s_setprio 2\n\t" \
    "s_waitcnt vmcnt(0) lgkmcnt(0)\n\t" \
    "s_barrier \n\t" \
    LST3 \
    LJIT \
    "ds_read_b128 v[224:227], v253 offset:40960\n\t" \
    "ds_read_b128 v[228:231], v253 offset:41984\n\t" \
    "ds_read_b128 v[232:235], v253 offset:43008\n\t" \
    "ds_read_b128 v[236:239], v253 offset:44032\n\t" \
    "ds_read_b128 v[240:243], v253 offset:45056\n\t" \
    "ds_read_b128 v[200:203], v254 offset:0\n\t" \
    "ds_read_b128 v[204:207], v254 offset:1024\n\t" \
    "ds_read_b128 v[216:219], v254 offset:2048\n\t" \
    "ds_read_b128 v[220:223], v254 offset:3072\n\t" \
    LFLUSH212 \
    "v_bfe_i32 v212, v253, 5, 1\n\t" \
    "v_and_b32 v212, 0xaaaa, v212\n\t" \
    "v_or_b32 v212, 0x4444, v212\n\t" \
    "v_and_b32 v213, 0xffff, v255\n\t" \
    "v_lshrrev_b32 v214, 16, v255\n\t" \
    "s_waitcnt lgkmcnt(8)\n\t" \
    "v_smfmac_f32_16x16x64_bf16 v[244:247], v[224:227], v[0:7], v212\n\t" \
    "v_smfmac_f32_16x16x64_bf16 v[248:251], v[224:227], v[8:15], v212\n\t" \
    "s_waitcnt lgkmcnt(2)\n\t" \
    "v_smfmac_f32_16x16x64_bf16 v[208:211], v[224:227], v[200:207], v212\n\t" \
    "ds_read_b128 v[200:203], v254 offset:4096\n\t" \
    "ds_read_b128 v[204:207], v254 offset:5120\n\t" \
    "ds_read_b128 v[224:227], v253 offset:46080\n\t" \
    "v_smfmac_f32_16x16x64_bf16 v[208:211], v[228:231], v[16:23], v212\n\t" \
    "v_smfmac_f32_16x16x64_bf16 v[244:247], v[228:231], v[24:31], v212\n\t" \
    "v_smfmac_f32_16x16x64_bf16 v[248:251], v[228:231], v[32:39], v212\n\t" \
    "ds_read_b128 v[228:231], v253 offset:47104\n\t" \
    "s_waitcnt lgkmcnt(4)\n\t" \
    "v_smfmac_f32_16x16x64_bf16 v[208:211], v[232:235], v[216:223], v212\n\t" \
    "ds_read_b128 v[216:219], v254 offset:6144\n\t" \
    "ds_read_b128 v[220:223], v254 offset:7168\n\t" \
    "v_smfmac_f32_16x16x64_bf16 v[244:247], v[232:235], v[40:47], v212\n\t" \
    "v_smfmac_f32_16x16x64_bf16 v[248:251], v[232:235], v[48:55], v212\n\t" \
    "ds_read_b128 v[232:235], v253 offset:48128\n\t" \
    "v_smfmac_f32_16x16x64_bf16 v[208:211], v[236:239], v[56:63], v212\n\t" \
    "v_smfmac_f32_16x16x64_bf16 v[244:247], v[236:239], v[64:71], v212\n\t" \
    "v_smfmac_f32_16x16x64_bf16 v[248:251], v[236:239], v[72:79], v212\n\t" \
    "ds_read_b128 v[236:239], v253 offset:49152\n\t" \
    "s_waitcnt lgkmcnt(6)\n\t" \
    "v_smfmac_f32_16x16x64_bf16 v[208:211], v[240:243], v[200:207], v212\n\t" \
    "ds_read_b128 v[200:203], v254 offset:8192\n\t" \
    "ds_read_b128 v[204:207], v254 offset:9216\n\t" \
    "v_smfmac_f32_16x16x64_bf16 v[244:247], v[240:243], v[80:87], v212\n\t" \
    "v_smfmac_f32_16x16x64_bf16 v[248:251], v[240:243], v[88:95], v212\n\t" \
    "ds_read_b128 v[240:243], v253 offset:50176\n\t" \
    "s_waitcnt lgkmcnt(8)\n\t" \
    "v_smfmac_f32_16x16x64_bf16 v[208:211], v[224:227], v[96:103], v212\n\t" \
    "v_smfmac_f32_16x16x64_bf16 v[244:247], v[224:227], v[104:111], v212\n\t" \
    "v_smfmac_f32_16x16x64_bf16 v[248:251], v[224:227], v[112:119], v212\n\t" \
    "s_waitcnt lgkmcnt(5)\n\t" \
    "v_smfmac_f32_16x16x64_bf16 v[208:211], v[228:231], v[216:223], v212\n\t" \
    "s_waitcnt lgkmcnt(4)\n\t" \
    "v_smfmac_f32_16x16x64_bf16 v[208:211], v[232:235], v[136:143], v212\n\t" \
    "s_waitcnt lgkmcnt(1)\n\t" \
    "v_smfmac_f32_16x16x64_bf16 v[208:211], v[236:239], v[200:207], v212\n\t" \
    "s_waitcnt lgkmcnt(0)\n\t" \
    "v_smfmac_f32_16x16x64_bf16 v[208:211], v[240:243], v[176:183], v212\n\t" \
    "v_smfmac_f32_16x16x64_bf16 v[244:247], v[228:231], v[120:127], v212\n\t" \
    "v_smfmac_f32_16x16x64_bf16 v[244:247], v[232:235], v[144:151], v212\n\t" \
    "v_smfmac_f32_16x16x64_bf16 v[244:247], v[236:239], v[160:167], v212\n\t" \
    "v_add_f32 v208, v208, v210\n\t" \
    "v_add_f32 v209, v209, v211\n\t" \
    "v_cvt_pk_bf16_f32 v208, v208, v209\n\t" \
    "v_smfmac_f32_16x16x64_bf16 v[244:247], v[240:243], v[184:191], v212\n\t" \
    "ds_write_b16 v213, v208 offset:0\n\t" \
    "ds_write_b16_d16_hi v213, v208 offset:16\n\t" \
    "v_smfmac_f32_16x16x64_bf16 v[248:251], v[228:231], v[128:135], v212\n\t" \
    "v_smfmac_f32_16x16x64_bf16 v[248:251], v[232:235], v[152:159], v212\n\t" \
    "v_smfmac_f32_16x16x64_bf16 v[248:251], v[236:239], v[168:175], v212\n\t" \
    "v_add_f32 v244, v244, v246\n\t" \
    "v_add_f32 v245, v245, v247\n\t" \
    "v_smfmac_f32_16x16x64_bf16 v[248:251], v[240:243], v[192:199], v212\n\t" \
    "s_nop 7\n\t" \
    "v_add_f32 v248, v248, v250\n\t" \
    "v_add_f32 v249, v249, v251\n\t" \
    "v_cvt_pk_bf16_f32 v244, v244, v248\n\t" \
    "v_cvt_pk_bf16_f32 v245, v245, v249\n\t" \
    "ds_write2_b32 v214, v244, v245 offset1:4\n\t" \
    "s_setprio 0\n\t" \
    "s_waitcnt lgkmcnt(0)\n\t" \
    "s_barrier \n\t" \
    LST4 \
    LJIT \
    "ds_read_b128 v[224:227], v253 offset:0\n\t" \
    "ds_read_b128 v[244:247], v254 offset:0\n\t" \
    "ds_read_b128 v[228:231], v253 offset:1024\n\t" \
    "ds_read_b128 v[232:235], v253 offset:20480\n\t" \
    "ds_read_b128 v[236:239], v253 offset:21504\n\t" \
    "ds_read_b128 v[248:251], v254 offset:1024\n\t" \
    "s_add_u32 s40, s40, 1\n\t" \
    "s_cmp_lg_u32 s40, 64\n\t" \
    "s_cbranch_scc1 .Ls47_%=\n\t" \
    "s_branch .Lend%=\n\t" \
    ".Ls3_%=:\n\t" \
    "v_add_u32 v244, 0x18000, v243\n\t" \
    "s_waitcnt vmcnt(12) lgkmcnt(0)\n\t" \
    "s_barrier \n\t" \
    "s_add_u32 m0, %[wm], 0x12000\n\t" \
    "s_mov_b32 s44, 0x1e0000\n\t" \
    "buffer_load_dwordx4 v240, %[wr], s44 offen offset:0 nt lds\n\t" \
    "buffer_load_dwordx4 v241, %[wr], s44 offen offset:1024 nt lds\n\t" \
    "buffer_load_dwordx4 v242, %[wr], s44 offen offset:2048 nt lds\n\t" \
    "ds_read_b64_tr_b16 v[208:209], v244 offset:0\n\t" \
    "ds_read_b64_tr_b16 v[210:211], v244 offset:1536\n\t" \
    "ds_read_b64_tr_b16 v[40:41], v244 offset:32\n\t" \
    "ds_read_b64_tr_b16 v[42:43], v244 offset:1568\n\t" \
    "ds_read_b64_tr_b16 v[48:49], v244 offset:64\n\t" \
    "ds_read_b64_tr_b16 v[50:51], v244 offset:1600\n\t" \
    "v_add_u32 v244, 0x1e000, v243\n\t" \
    "s_waitcnt vmcnt(12) lgkmcnt(0)\n\t" \
    "s_barrier \n\t" \
    "s_add_u32 m0, %[wm], 0x18000\n\t" \
    "s_mov_b32 s44, 0x240000\n\t" \
    "buffer_load_dwordx4 v240, %[wr], s44 offen offset:0 nt lds\n\t" \
    "buffer_load_dwordx4 v241, %[wr], s44 offen offset:1024 nt lds\n\t" \
    "buffer_load_dwordx4 v242, %[wr], s44 offen offset:2048 nt lds\n\t" \
    "ds_read_b64_tr_b16 v[212:213], v244 offset:0\n\t" \
    "ds_read_b64_tr_b16 v[214:215], v244 offset:1536\n\t" \
    "ds_read_b64_tr_b16 v[44:45], v244 offset:32\n\t" \
    "ds_read_b64_tr_b16 v[46:47], v244 offset:1568\n\t" \
    "ds_read_b64_tr_b16 v[52:53], v244 offset:64\n\t" \
    "ds_read_b64_tr_b16 v[54:55], v244 offset:1600\n\t" \
    "v_add_u32 v244, 0x0, v243\n\t" \
    "s_waitcnt vmcnt(12) lgkmcnt(0)\n\t" \
    "s_barrier \n\t" \
    "s_add_u32 m0, %[wm], 0x1e000\n\t" \
    "s_mov_b32 s44, 0x2a0000\n\t" \
    "buffer_load_dwordx4 v240, %[wr], s44 offen offset:0 nt lds\n\t" \
    "buffer_load_dwordx4 v241, %[wr], s44 offen offset:1024 nt lds\n\t" \
    "buffer_load_dwordx4 v242, %[wr], s44 offen offset:2048 nt lds\n\t" \
    "ds_read_b64_tr_b16 v[56:57], v244 offset:0\n\t" \
    "ds_read_b64_tr_b16 v[58:59], v244 offset:1536\n\t" \
    "ds_read_b64_tr_b16 v[64:65], v244 offset:32\n\t" \
    "ds_read_b64_tr_b16 v[66:67], v244 offset:1568\n\t" \
    "ds_read_b64_tr_b16 v[72:73], v244 offset:64\n\t" \
    "ds_read_b64_tr_b16 v[74:75], v244 offset:1600\n\t" \
    "v_add_u32 v244, 0x6000, v243\n\t" \
    "s_waitcnt vmcnt(12) lgkmcnt(0)\n\t" \
    "s_barrier \n\t" \
    "s_add_u32 m0, %[wm], 0x0\n\t" \
    "s_mov_b32 s44, 0x300000\n\t" \
    "buffer_load_dwordx4 v240, %[wr], s44 offen offset:0 nt lds\n\t" \
    "buffer_load_dwordx4 v241, %[wr], s44 offen offset:1024 nt lds\n\t" \
    "buffer_load_dwordx4 v242, %[wr], s44 offen offset:2048 nt lds\n\t" \
    "ds_read_b64_tr_b16 v[60:61], v244 offset:0\n\t" \
    "ds_read_b64_tr_b16 v[62:63], v244 offset:1536\n\t" \
    "ds_read_b64_tr_b16 v[68:69], v244 offset:32\n\t" \
    "ds_read_b64_tr_b16 v[70:71], v244 offset:1568\n\t" \
    "ds_read_b64_tr_b16 v[76:77], v244 offset:64\n\t" \
    "ds_read_b64_tr_b16 v[78:79], v244 offset:1600\n\t" \
    "v_add_u32 v244, 0xc000, v243\n\t" \
    "s_waitcnt vmcnt(12) lgkmcnt(0)\n\t" \
    "s_barrier \n\t" \
    "s_add_u32 m0, %[wm], 0x6000\n\t" \
    "s_mov_b32 s44, 0x360000\n\t" \
    "buffer_load_dwordx4 v240, %[wr], s44 offen offset:0 nt lds\n\t" \
    "buffer_load_dwordx4 v241, %[wr], s44 offen offset:1024 nt lds\n\t" \
    "buffer_load_dwordx4 v242, %[wr], s44 offen offset:2048 nt lds\n\t" \
    "ds_read_b64_tr_b16 v[216:217], v244 offset:0\n\t" \
    "ds_read_b64_tr_b16 v[218:219], v244 offset:1536\n\t" \
    "ds_read_b64_tr_b16 v[80:81], v244 offset:32\n\t" \
    "ds_read_b64_tr_b16 v[82:83], v244 offset:1568\n\t" \
    "ds_read_b64_tr_b16 v[88:89], v244 offset:64\n\t" \
    "ds_read_b64_tr_b16 v[90:91], v244 offset:1600\n\t" \
    "v_add_u32 v244, 0x12000, v243\n\t" \
    "s_waitcnt vmcnt(12) lgkmcnt(0)\n\t" \
    "s_barrier \n\t" \
    "s_add_u32 m0, %[wm], 0xc000\n\t" \
    "s_mov_b32 s44, 0x3c0000\n\t" \
    "buffer_load_dwordx4 v240, %[wr], s44 offen offset:0 nt lds\n\t" \
    "buffer_load_dwordx4 v241, %[wr], s44 offen offset:1024 nt lds\n\t" \
    "buffer_load_dwordx4 v242, %[wr], s44 offen offset:2048 nt lds\n\t" \
    "ds_read_b64_tr_b16 v[220:221], v244 offset:0\n\t" \
    "ds_read_b64_tr_b16 v[222:223], v244 offset:1536\n\t" \
    "ds_read_b64_tr_b16 v[84:85], v244 offset:32\n\t" \
    "ds_read_b64_tr_b16 v[86:87], v244 offset:1568\n\t" \
    "ds_read_b64_tr_b16 v[92:93], v244 offset:64\n\t" \
    "ds_read_b64_tr_b16 v[94:95], v244 offset:1600\n\t" \
    "v_add_u32 v244, 0x18000, v243\n\t" \
    "s_waitcnt vmcnt(12) lgkmcnt(0)\n\t" \
    "s_barrier \n\t" \
    "s_add_u32 m0, %[wm], 0x12000\n\t" \
    "s_mov_b32 s44, 0x420000\n\t" \
    "buffer_load_dwordx4 v240, %[wr], s44 offen offset:0 nt lds\n\t" \
    "buffer_load_dwordx4 v241, %[wr], s44 offen offset:1024 nt lds\n\t" \
    "buffer_load_dwordx4 v242, %[wr], s44 offen offset:2048 nt lds\n\t" \
    "ds_read_b64_tr_b16 v[96:97], v244 offset:0\n\t" \
    "ds_read_b64_tr_b16 v[98:99], v244 offset:1536\n\t" \
    "ds_read_b64_tr_b16 v[104:105], v244 offset:32\n\t" \
    "ds_read_b64_tr_b16 v[106:107], v244 offset:1568\n\t" \
    "ds_read_b64_tr_b16 v[112:113], v244 offset:64\n\t" \
    "ds_read_b64_tr_b16 v[114:115], v244 offset:1600\n\t" \
    "v_add_u32 v244, 0x1e000, v243\n\t" \
    "s_waitcnt vmcnt(12) lgkmcnt(0)\n\t" \
    "s_barrier \n\t" \
    "s_add_u32 m0, %[wm], 0x18000\n\t" \
    "s_mov_b32 s44, 0x480000\n\t" \
    "buffer_load_dwordx4 v240, %[wr], s44 offen offset:0 nt lds\n\t" \
    "buffer_load_dwordx4 v241, %[wr], s44 offen offset:1024 nt lds\n\t" \
    "buffer_load_dwordx4 v242, %[wr], s44 offen offset:2048 nt lds\n\t" \
    "ds_read_b64_tr_b16 v[100:101], v244 offset:0\n\t" \
    "ds_read_b64_tr_b16 v[102:103], v244 offset:1536\n\t" \
    "ds_read_b64_tr_b16 v[108:109], v244 offset:32\n\t" \
    "ds_read_b64_tr_b16 v[110:111], v244 offset:1568\n\t" \
    "ds_read_b64_tr_b16 v[116:117], v244 offset:64\n\t" \
    "ds_read_b64_tr_b16 v[118:119], v244 offset:1600\n\t" \
    "v_add_u32 v244, 0x0, v243\n\t" \
    "s_waitcnt vmcnt(12) lgkmcnt(0)\n\t" \
    "s_barrier \n\t" \
    "v_mov_b32 v246, 0\n\t" \
    "buffer_load_dword v246, v246, s[60:63], %[mo] offen sc1\n\t" \
    "s_add_u32 m0, %[wm], 0x1e000\n\t" \
    "s_mov_b32 s44, 0x4e0000\n\t" \
    "buffer_load_dwordx4 v240, %[wr], s44 offen offset:0 nt lds\n\t" \
    "buffer_load_dwordx4 v241, %[wr], s44 offen offset:1024 nt lds\n\t" \
    "buffer_load_dwordx4 v242, %[wr], s44 offen offset:2048 nt lds\n\t" \
    "ds_read_b64_tr_b16 v[224:225], v244 offset:0\n\t" \
    "ds_read_b64_tr_b16 v[226:227], v244 offset:1536\n\t" \
    "ds_read_b64_tr_b16 v[120:121], v244 offset:32\n\t" \
    "ds_read_b64_tr_b16 v[122:123], v244 offset:1568\n\t" \
    "ds_read_b64_tr_b16 v[128:129], v244 offset:64\n\t" \
    "ds_read_b64_tr_b16 v[130:131], v244 offset:1600\n\t" \
    "v_add_u32 v244, 0x6000, v243\n\t" \
    "s_waitcnt vmcnt(12) lgkmcnt(0)\n\t" \
    "s_barrier \n\t" \
    "s_add_u32 m0, %[wm], 0x0\n\t" \
    "s_mov_b32 s44, 0x540000\n\t" \
    "buffer_load_dwordx4 v240, %[wr], s44 offen offset:0 nt lds\n\t" \
    "buffer_load_dwordx4 v241, %[wr], s44 offen offset:1024 nt lds\n\t" \
    "buffer_load_dwordx4 v242, %[wr], s44 offen offset:2048 nt lds\n\t" \
    "ds_read_b64_tr_b16 v[228:229], v244 offset:0\n\t" \
    "ds_read_b64_tr_b16 v[230:231], v244 offset:1536\n\t" \
    "ds_read_b64_tr_b16 v[124:125], v244 offset:32\n\t" \
    "ds_read_b64_tr_b16 v[126:127], v244 offset:1568\n\t" \
    "ds_read_b64_tr_b16 v[132:133], v244 offset:64\n\t" \
    "ds_read_b64_tr_b16 v[134:135], v244 offset:1600\n\t" \
    "v_add_u32 v244, 0xc000, v243\n\t" \
    "s_waitcnt vmcnt(12) lgkmcnt(0)\n\t" \
    "s_barrier \n\t" \
    "s_add_u32 m0, %[wm], 0x6000\n\t" \
    "s_mov_b32 s44, 0x5a0000\n\t" \
    "buffer_load_dwordx4 v240, %[wr], s44 offen offset:0 nt lds\n\t" \
    "buffer_load_dwordx4 v241, %[wr], s44 offen offset:1024 nt lds\n\t" \
    "buffer_load_dwordx4 v242, %[wr], s44 offen offset:2048 nt lds\n\t" \
    "ds_read_b64_tr_b16 v[136:137], v244 offset:0\n\t" \
    "ds_read_b64_tr_b16 v[138:139], v244 offset:1536\n\t" \
    "ds_read_b64_tr_b16 v[144:145], v244 offset:32\n\t" \
    "ds_read_b64_tr_b16 v[146:147], v244 offset:1568\n\t" \
    "ds_read_b64_tr_b16 v[152:153], v244 offset:64\n\t" \
    "ds_read_b64_tr_b16 v[154:155], v244 offset:1600\n\t" \
    "v_add_u32 v244, 0x12000, v243\n\t" \
    "s_waitcnt vmcnt(12) lgkmcnt(0)\n\t" \
    "s_barrier \n\t" \
    "s_add_u32 m0, %[wm], 0xc000\n\t" \
    "s_mov_b32 s44, 0x600000\n\t" \
    "buffer_load_dwordx4 v240, %[wr], s44 offen offset:0 nt lds\n\t" \
    "buffer_load_dwordx4 v241, %[wr], s44 offen offset:1024 nt lds\n\t" \
    "buffer_load_dwordx4 v242, %[wr], s44 offen offset:2048 nt lds\n\t" \
    "ds_read_b64_tr_b16 v[140:141], v244 offset:0\n\t" \
    "ds_read_b64_tr_b16 v[142:143], v244 offset:1536\n\t" \
    "ds_read_b64_tr_b16 v[148:149], v244 offset:32\n\t" \
    "ds_read_b64_tr_b16 v[150:151], v244 offset:1568\n\t" \
    "ds_read_b64_tr_b16 v[156:157], v244 offset:64\n\t" \
    "ds_read_b64_tr_b16 v[158:159], v244 offset:1600\n\t" \
    "v_add_u32 v244, 0x18000, v243\n\t" \
    "s_waitcnt vmcnt(12) lgkmcnt(0)\n\t" \
    "s_barrier \n\t" \
    "s_add_u32 m0, %[wm], 0x12000\n\t" \
    "s_mov_b32 s44, 0x660000\n\t" \
    "buffer_load_dwordx4 v240, %[wr], s44 offen offset:0 nt lds\n\t" \
    "buffer_load_dwordx4 v241, %[wr], s44 offen offset:1024 nt lds\n\t" \
    "buffer_load_dwordx4 v242, %[wr], s44 offen offset:2048 nt lds\n\t" \
    "ds_read_b64_tr_b16 v[232:233], v244 offset:0\n\t" \
    "ds_read_b64_tr_b16 v[234:235], v244 offset:1536\n\t" \
    "ds_read_b64_tr_b16 v[160:161], v244 offset:32\n\t" \
    "ds_read_b64_tr_b16 v[162:163], v244 offset:1568\n\t" \
    "ds_read_b64_tr_b16 v[168:169], v244 offset:64\n\t" \
    "ds_read_b64_tr_b16 v[170:171], v244 offset:1600\n\t" \
    "v_add_u32 v244, 0x1e000, v243\n\t" \
    "s_waitcnt vmcnt(12) lgkmcnt(0)\n\t" \
    "s_barrier \n\t" \
    ".Ls31_%=:\n\t" \
    "v_readfirstlane_b32 s43, v246\n\t" \
    "s_nop 0\n\t" \
    "s_cmp_lg_u32 s43, -1\n\t" \
    "s_cbranch_scc1 .Ls32_%=\n\t" \
    "s_cmp_eq_u32 s41, 0\n\t" \
    "s_cbranch_scc1 .Ls32_%=\n\t" \
    "s_sub_u32 s41, s41, 1\n\t" \
    "s_sleep 4\n\t" \
    "v_mov_b32 v246, 0\n\t" \
    "buffer_load_dword v246, v246, s[60:63], %[mo] offen sc1\n\t" \
    "s_waitcnt vmcnt(0)\n\t" \
    "s_branch .Ls31_%=\n\t" \
    ".Ls32_%=:\n\t" \
    "v_mov_b32 v246, 0x8000\n\t" \
    "s_mov_b64 exec, 1\n\t" \
    "buffer_store_dword v246, v246, s[60:63], %[mo] offen sc1\n\t" \
    "s_mov_b64 exec, -1\n\t" \
    "s_and_b32 s43, s43, 0x2000\n\t" \
    "s_xor_b32 s43, s43, %[lx]\n\t" \
    "s_add_u32 s60, s60, s43\n\t" \
    "s_addc_u32 s61, s61, 0\n\t" \
    "s_add_u32 m0, %[wm], 0x18000\n\t" \
    "s_mov_b32 s44, 0x6c0000\n\t" \
    "buffer_load_dwordx4 v240, %[wr], s44 offen offset:0 nt lds\n\t" \
    "buffer_load_dwordx4 v241, %[wr], s44 offen offset:1024 nt lds\n\t" \
    "buffer_load_dwordx4 v242, %[wr], s44 offen offset:2048 nt lds\n\t" \
    "ds_read_b64_tr_b16 v[236:237], v244 offset:0\n\t" \
    "ds_read_b64_tr_b16 v[238:239], v244 offset:1536\n\t" \
    "ds_read_b64_tr_b16 v[164:165], v244 offset:32\n\t" \
    "ds_read_b64_tr_b16 v[166:167], v244 offset:1568\n\t" \
    "ds_read_b64_tr_b16 v[172:173], v244 offset:64\n\t" \
    "ds_read_b64_tr_b16 v[174:175], v244 offset:1600\n\t" \
    "v_add_u32 v244, 0x0, v243\n\t" \
    "s_waitcnt vmcnt(12) lgkmcnt(0)\n\t" \
    "s_barrier \n\t" \
    "s_add_u32 m0, %[wm], 0x1e000\n\t" \
    "s_mov_b32 s44, 0x720000\n\t" \
    "buffer_load_dwordx4 v240, %[wr], s44 offen offset:0 nt lds\n\t" \
    "buffer_load_dwordx4 v241, %[wr], s44 offen offset:1024 nt lds\n\t" \
    "buffer_load_dwordx4 v242, %[wr], s44 offen offset:2048 nt lds\n\t" \
    "ds_read_b64_tr_b16 v[176:177], v244 offset:0\n\t" \
    "ds_read_b64_tr_b16 v[178:179], v244 offset:1536\n\t" \
    "ds_read_b64_tr_b16 v[184:185], v244 offset:32\n\t" \
    "ds_read_b64_tr_b16 v[186:187], v244 offset:1568\n\t" \
    "ds_read_b64_tr_b16 v[192:193], v244 offset:64\n\t" \
    "ds_read_b64_tr_b16 v[194:195], v244 offset:1600\n\t" \
    "v_add_u32 v244, 0x6000, v243\n\t" \
    "s_waitcnt vmcnt(12) lgkmcnt(0)\n\t" \
    "s_barrier \n\t" \
    "ds_read_b64_tr_b16 v[180:181], v244 offset:0\n\t" \
    "ds_read_b64_tr_b16 v[182:183], v244 offset:1536\n\t" \
    "ds_read_b64_tr_b16 v[188:189], v244 offset:32\n\t" \
    "ds_read_b64_tr_b16 v[190:191], v244 offset:1568\n\t" \
    "ds_read_b64_tr_b16 v[196:197], v244 offset:64\n\t" \
    "ds_read_b64_tr_b16 v[198:199], v244 offset:1600\n\t" \
    "v_add_u32 v244, 0xc000, v243\n\t" \
    "s_waitcnt vmcnt(9) lgkmcnt(0)\n\t" \
    "s_barrier \n\t" \
    "ds_read_b64_tr_b16 v[200:201], v244 offset:0\n\t" \
    "ds_read_b64_tr_b16 v[202:203], v244 offset:1536\n\t" \
    "ds_read_b64_tr_b16 v[0:1], v244 offset:32\n\t" \
    "ds_read_b64_tr_b16 v[2:3], v244 offset:1568\n\t" \
    "ds_read_b64_tr_b16 v[8:9], v244 offset:64\n\t" \
    "ds_read_b64_tr_b16 v[10:11], v244 offset:1600\n\t" \
    "v_add_u32 v244, 0x12000, v243\n\t" \
    "s_waitcnt vmcnt(6) lgkmcnt(0)\n\t" \
    "s_barrier \n\t" \
    "ds_read_b64_tr_b16 v[204:205], v244 offset:0\n\t" \
    "ds_read_b64_tr_b16 v[206:207], v244 offset:1536\n\t" \
    "ds_read_b64_tr_b16 v[4:5], v244 offset:32\n\t" \
    "ds_read_b64_tr_b16 v[6:7], v244 offset:1568\n\t" \
    "ds_read_b64_tr_b16 v[12:13], v244 offset:64\n\t" \
    "ds_read_b64_tr_b16 v[14:15], v244 offset:1600\n\t" \
    "v_add_u32 v244, 0x18000, v243\n\t" \
    "s_waitcnt vmcnt(3) lgkmcnt(0)\n\t" \
    "s_barrier \n\t" \
    "ds_read_b64_tr_b16 v[16:17], v244 offset:0\n\t" \
    "ds_read_b64_tr_b16 v[18:19], v244 offset:1536\n\t" \
    "ds_read_b64_tr_b16 v[24:25], v244 offset:32\n\t" \
    "ds_read_b64_tr_b16 v[26:27], v244 offset:1568\n\t" \
    "ds_read_b64_tr_b16 v[32:33], v244 offset:64\n\t" \
    "ds_read_b64_tr_b16 v[34:35], v244 offset:1600\n\t" \
    "v_add_u32 v244, 0x1e000, v243\n\t" \
    "s_waitcnt vmcnt(0) lgkmcnt(0)\n\t" \
    "s_barrier \n\t" \
    "ds_read_b64_tr_b16 v[20:21], v244 offset:0\n\t" \
    "ds_read_b64_tr_b16 v[22:23], v244 offset:1536\n\t" \
    "ds_read_b64_tr_b16 v[28:29], v244 offset:32\n\t" \
    "ds_read_b64_tr_b16 v[30:31], v244 offset:1568\n\t" \
    "ds_read_b64_tr_b16 v[36:37], v244 offset:64\n\t" \
    "ds_read_b64_tr_b16 v[38:39], v244 offset:1600\n\t" \
    "s_waitcnt lgkmcnt(0)\n\t" \
    "s_barrier \n\t" \
    LST0 \
    LJIT \
    "ds_write_b128 v254, v[200:203] offset:55296\n\t" \
    "ds_write_b128 v254, v[204:207] offset:56320\n\t" \
    "ds_write_b128 v254, v[208:211] offset:57344\n\t" \
    "ds_write_b128 v254, v[212:215] offset:58368\n\t" \
    "ds_write_b128 v254, v[216:219] offset:59392\n\t" \
    "ds_write_b128 v254, v[220:223] offset:60416\n\t" \
    "ds_write_b128 v254, v[224:227] offset:61440\n\t" \
    "ds_write_b128 v254, v[228:231] offset:62464\n\t" \
    "ds_write_b128 v254, v[232:235] offset:63488\n\t" \
    "ds_write_b128 v254, v[236:239] offset:64512\n\t" \
    "s_waitcnt vmcnt(0) lgkmcnt(0)\n\t" \
    "s_barrier \n\t" \
    "ds_read_b128 v[208:211], v254 offset:24576\n\t" \
    "ds_read_b128 v[224:227], v253 offset:0\n\t" \
    "ds_read_b128 v[228:231], v253 offset:20480\n\t" \
    "ds_read_b128 v[212:215], v254 offset:25600\n\t" \
    "ds_read_b128 v[232:235], v253 offset:1024\n\t" \
    "ds_read_b128 v[236:239], v253 offset:21504\n\t" \
    "ds_read_b128 v[216:219], v254 offset:26624\n\t" \
    "ds_read_b128 v[240:243], v253 offset:2048\n\t" \
    "ds_read_b128 v[244:247], v253 offset:22528\n\t" \
    "ds_read_b128 v[220:223], v254 offset:27648\n\t" \
    "ds_read_b128 v[248:251], v253 offset:3072\n\t" \
    "s_waitcnt lgkmcnt(9)\n\t" \
    "v_mfma_f32_16x16x32_bf16 v[200:203], v[208:211], v[224:227], 0\n\t" \
    "ds_read_b128 v[224:227], v253 offset:23552\n\t" \
    "s_waitcnt lgkmcnt(9)\n\t" \
    "v_mfma_f32_16x16x32_bf16 v[204:207], v[208:211], v[228:231], 0\n\t" \
    "ds_read_b128 v[228:231], v253 offset:4096\n\t" \
    "ds_read_b128 v[208:211], v254 offset:28672\n\t" \
    "s_waitcnt lgkmcnt(9)\n\t" \
    "v_mfma_f32_16x16x32_bf16 v[200:203], v[212:215], v[232:235], v[200:203]\n\t" \
    "ds_read_b128 v[232:235], v253 offset:24576\n\t" \
    "s_waitcnt lgkmcnt(9)\n\t" \
    "v_mfma_f32_16x16x32_bf16 v[204:207], v[212:215], v[236:239], v[204:207]\n\t" \
    "ds_read_b128 v[236:239], v253 offset:5120\n\t" \
    "ds_read_b128 v[212:215], v254 offset:29696\n\t" \
    "s_waitcnt lgkmcnt(9)\n\t" \
    "v_mfma_f32_16x16x32_bf16 v[200:203], v[216:219], v[240:243], v[200:203]\n\t" \
    "ds_read_b128 v[240:243], v253 offset:25600\n\t" \
    ".Ls33_%=:\n\t" \
    "s_waitcnt lgkmcnt(9)\n\t" \
    "v_mfma_f32_16x16x32_bf16 v[204:207], v[216:219], v[244:247], v[204:207]\n\t" \
    "ds_read_b128 v[244:247], v253 offset:6144\n\t" \
    "ds_read_b128 v[216:219], v254 offset:30720\n\t" \
    "s_waitcnt lgkmcnt(9)\n\t" \
    "v_mfma_f32_16x16x32_bf16 v[200:203], v[220:223], v[248:251], v[200:203]\n\t" \
    "ds_read_b128 v[248:251], v253 offset:26624\n\t" \
    "s_waitcnt lgkmcnt(9)\n\t" \
    "v_mfma_f32_16x16x32_bf16 v[204:207], v[220:223], v[224:227], v[204:207]\n\t" \
    "ds_read_b128 v[224:227], v253 offset:7168\n\t" \
    "ds_read_b128 v[220:223], v254 offset:31744\n\t" \
    "s_waitcnt lgkmcnt(9)\n\t" \
    "v_mfma_f32_16x16x32_bf16 v[200:203], v[208:211], v[228:231], v[200:203]\n\t" \
    "ds_read_b128 v[228:231], v253 offset:27648\n\t" \
    "s_waitcnt lgkmcnt(9)\n\t" \
    "v_mfma_f32_16x16x32_bf16 v[204:207], v[208:211], v[232:235], v[204:207]\n\t" \
    "ds_read_b128 v[232:235], v253 offset:8192\n\t" \
    "ds_read_b128 v[208:211], v254 offset:32768\n\t" \
    "s_waitcnt lgkmcnt(9)\n\t" \
    "v_mfma_f32_16x16x32_bf16 v[200:203], v[212:215], v[236:239], v[200:203]\n\t" \
    "ds_read_b128 v[236:239], v253 offset:28672\n\t" \
    "s_waitcnt lgkmcnt(9)\n\t" \
    "v_mfma_f32_16x16x32_bf16 v[204:207], v[212:215], v[240:243], v[204:207]\n\t" \
    "ds_read_b128 v[240:243], v253 offset:9216\n\t" \
    "ds_read_b128 v[212:215], v254 offset:33792\n\t" \
    "s_waitcnt lgkmcnt(9)\n\t" \
    "v_mfma_f32_16x16x32_bf16 v[200:203], v[216:219], v[244:247], v[200:203]\n\t" \
    "ds_read_b128 v[244:247], v253 offset:29696\n\t" \
    "s_waitcnt lgkmcnt(9)\n\t" \
    "v_mfma_f32_16x16x32_bf16 v[204:207], v[216:219], v[248:251], v[204:207]\n\t" \
    "s_waitcnt lgkmcnt(7)\n\t" \
    "v_mfma_f32_16x16x32_bf16 v[200:203], v[220:223], v[224:227], v[200:203]\n\t" \
    "s_waitcnt lgkmcnt(6)\n\t" \
    "v_mfma_f32_16x16x32_bf16 v[204:207], v[220:223], v[228:231], v[204:207]\n\t" \
    "s_waitcnt lgkmcnt(4)\n\t" \
    "v_mfma_f32_16x16x32_bf16 v[200:203], v[208:211], v[232:235], v[200:203]\n\t" \
    "s_waitcnt lgkmcnt(3)\n\t" \
    "v_mfma_f32_16x16x32_bf16 v[204:207], v[208:211], v[236:239], v[204:207]\n\t" \
    "s_waitcnt lgkmcnt(1)\n\t" \
    "v_mfma_f32_16x16x32_bf16 v[200:203], v[212:215], v[240:243], v[200:203]\n\t" \
    "s_waitcnt lgkmcnt(0)\n\t" \
    "v_mfma_f32_16x16x32_bf16 v[204:207], v[212:215], v[244:247], v[204:207]\n\t" \
    "ds_read_b128 v[224:227], v253 offset:0\n\t" \
    "ds_read_b128 v[244:247], v254 offset:57344\n\t" \
    "ds_read_b128 v[228:231], v253 offset:1024\n\t" \
    "ds_read_b128 v[232:235], v253 offset:20480\n\t" \
    "ds_read_b128 v[236:239], v253 offset:21504\n\t" \
    "ds_read_b128 v[248:251], v254 offset:58368\n\t" \
    "s_nop 1\n\t" \
    "ds_write_b128 v253, v[200:203] offset:40960\n\t" \
    "ds_write_b128 v253, v[204:207] offset:41984\n\t" \
    "s_nop 1\n\t" \
    "s_waitcnt lgkmcnt(7)\n\t" \
    "v_mfma_f32_16x16x32_bf16 v[204:207], v[40:43], v[224:227], 0\n\t" \
    "s_waitcnt lgkmcnt(5)\n\t" \
    "v_mfma_f32_16x16x32_bf16 v[204:207], v[44:47], v[228:231], v[204:207]\n\t" \
    "ds_read_b128 v[240:243], v253 offset:2048\n\t" \
    "v_mfma_f32_16x16x32_bf16 v[208:211], v[48:51], v[224:227], 0\n\t" \
    "v_mfma_f32_16x16x32_bf16 v[208:211], v[52:55], v[228:231], v[208:211]\n\t" \
    "v_mfma_f32_16x16x32_bf16 v[200:203], v[244:247], v[224:227], 0\n\t" \
    "s_waitcnt lgkmcnt(3)\n\t" \
    "v_mfma_f32_16x16x32_bf16 v[200:203], v[248:251], v[228:231], v[200:203]\n\t" \
    "v_mfma_f32_16x16x32_bf16 v[216:219], v[40:43], v[232:235], 0\n\t" \
    "ds_read_b128 v[224:227], v253 offset:3072\n\t" \
    "v_mfma_f32_16x16x32_bf16 v[216:219], v[44:47], v[236:239], v[216:219]\n\t" \
    "ds_read_b128 v[228:231], v253 offset:22528\n\t" \
    "v_mfma_f32_16x16x32_bf16 v[220:223], v[48:51], v[232:235], 0\n\t" \
    "v_mfma_f32_16x16x32_bf16 v[220:223], v[52:55], v[236:239], v[220:223]\n\t" \
    "v_mfma_f32_16x16x32_bf16 v[212:215], v[244:247], v[232:235], 0\n\t" \
    "ds_read_b128 v[244:247], v254 offset:59392\n\t" \
    "v_mfma_f32_16x16x32_bf16 v[212:215], v[248:251], v[236:239], v[212:215]\n\t" \
    "ds_read_b128 v[248:251], v254 offset:60416\n\t" \
    "s_waitcnt lgkmcnt(4)\n\t" \
    "v_mfma_f32_16x16x32_bf16 v[204:207], v[64:67], v[240:243], v[204:207]\n\t" \
    "ds_read_b128 v[232:235], v253 offset:23552\n\t" \
    "s_waitcnt lgkmcnt(4)\n\t" \
    "v_mfma_f32_16x16x32_bf16 v[204:207], v[68:71], v[224:227], v[204:207]\n\t" \
    "ds_read_b128 v[236:239], v253 offset:4096\n\t" \
    "v_mfma_f32_16x16x32_bf16 v[208:211], v[72:75], v[240:243], v[208:211]\n\t" \
    "v_mfma_f32_16x16x32_bf16 v[208:211], v[76:79], v[224:227], v[208:211]\n\t" \
    "v_mfma_f32_16x16x32_bf16 v[200:203], v[56:59], v[240:243], v[200:203]\n\t" \
    "v_mfma_f32_16x16x32_bf16 v[200:203], v[60:63], v[224:227], v[200:203]\n\t" \
    "s_waitcnt lgkmcnt(4)\n\t" \
    "v_mfma_f32_16x16x32_bf16 v[216:219], v[64:67], v[228:231], v[216:219]\n\t" \
    "ds_read_b128 v[240:243], v253 offset:5120\n\t" \
    "s_waitcnt lgkmcnt(2)\n\t" \
    "v_mfma_f32_16x16x32_bf16 v[216:219], v[68:71], v[232:235], v[216:219]\n\t" \
    "ds_read_b128 v[224:227], v253 offset:24576\n\t" \
    "v_mfma_f32_16x16x32_bf16 v[220:223], v[72:75], v[228:231], v[220:223]\n\t" \
    "v_mfma_f32_16x16x32_bf16 v[220:223], v[76:79], v[232:235], v[220:223]\n\t" \
    "v_mfma_f32_16x16x32_bf16 v[212:215], v[56:59], v[228:231], v[212:215]\n\t" \
    "v_mfma_f32_16x16x32_bf16 v[212:215], v[60:63], v[232:235], v[212:215]\n\t" \
    "s_waitcnt lgkmcnt(2)\n\t" \
    "v_mfma_f32_16x16x32_bf16 v[204:207], v[80:83], v[236:239], v[204:207]\n\t" \
    "ds_read_b128 v[228:231], v253 offset:25600\n\t" \
    "s_waitcnt lgkmcnt(2)\n\t" \
    "v_mfma_f32_16x16x32_bf16 v[204:207], v[84:87], v[240:243], v[204:207]\n\t" \
    "ds_read_b128 v[232:235], v253 offset:6144\n\t" \
    "v_mfma_f32_16x16x32_bf16 v[208:211], v[88:91], v[236:239], v[208:211]\n\t" \
    "v_mfma_f32_16x16x32_bf16 v[208:211], v[92:95], v[240:243], v[208:211]\n\t" \
    "v_mfma_f32_16x16x32_bf16 v[200:203], v[244:247], v[236:239], v[200:203]\n\t" \
    "v_mfma_f32_16x16x32_bf16 v[200:203], v[248:251], v[240:243], v[200:203]\n\t" \
    "s_waitcnt lgkmcnt(2)\n\t" \
    "v_mfma_f32_16x16x32_bf16 v[216:219], v[80:83], v[224:227], v[216:219]\n\t" \
    "ds_read_b128 v[236:239], v253 offset:7168\n\t" \
    "s_waitcnt lgkmcnt(2)\n\t" \
    "v_mfma_f32_16x16x32_bf16 v[216:219], v[84:87], v[228:231], v[216:219]\n\t" \
    "ds_read_b128 v[240:243], v253 offset:26624\n\t" \
    "v_mfma_f32_16x16x32_bf16 v[220:223], v[88:91], v[224:227], v[220:223]\n\t" \
    "v_mfma_f32_16x16x32_bf16 v[220:223], v[92:95], v[228:231], v[220:223]\n\t" \
    "v_mfma_f32_16x16x32_bf16 v[212:215], v[244:247], v[224:227], v[212:215]\n\t" \
    "ds_read_b128 v[244:247], v254 offset:61440\n\t" \
    "v_mfma_f32_16x16x32_bf16 v[212:215], v[248:251], v[228:231], v[212:215]\n\t" \
    "ds_read_b128 v[248:251], v254 offset:62464\n\t" \
    "s_waitcnt lgkmcnt(4)\n\t" \
    "v_mfma_f32_16x16x32_bf16 v[204:207], v[104:107], v[232:235], v[204:207]\n\t" \
    "ds_read_b128 v[224:227], v253 offset:27648\n\t" \
    "s_waitcnt lgkmcnt(4)\n\t" \
    "v_mfma_f32_16x16x32_bf16 v[204:207], v[108:111], v[236:239], v[204:207]\n\t" \
    "ds_read_b128 v[228:231], v253 offset:8192\n\t" \
    "v_mfma_f32_16x16x32_bf16 v[208:211], v[112:115], v[232:235], v[208:211]\n\t" \
    "v_mfma_f32_16x16x32_bf16 v[208:211], v[116:119], v[236:239], v[208:211]\n\t" \
    "v_mfma_f32_16x16x32_bf16 v[200:203], v[96:99], v[232:235], v[200:203]\n\t" \
    "v_mfma_f32_16x16x32_bf16 v[200:203], v[100:103], v[236:239], v[200:203]\n\t" \
    "s_waitcnt lgkmcnt(4)\n\t" \
    "v_mfma_f32_16x16x32_bf16 v[216:219], v[104:107], v[240:243], v[216:219]\n\t" \
    "ds_read_b128 v[232:235], v253 offset:9216\n\t" \
    "s_waitcnt lgkmcnt(2)\n\t" \
    "v_mfma_f32_16x16x32_bf16 v[216:219], v[108:111], v[224:227], v[216:219]\n\t" \
    "ds_read_b128 v[236:239], v253 offset:28672\n\t" \
    "v_mfma_f32_16x16x32_bf16 v[220:223], v[112:115], v[240:243], v[220:223]\n\t" \
    "v_mfma_f32_16x16x32_bf16 v[220:223], v[116:119], v[224:227], v[220:223]\n\t" \
    "v_mfma_f32_16x16x32_bf16 v[212:215], v[96:99], v[240:243], v[212:215]\n\t" \
    "v_mfma_f32_16x16x32_bf16 v[212:215], v[100:103], v[224:227], v[212:215]\n\t" \
    "s_waitcnt lgkmcnt(2)\n\t" \
    "v_mfma_f32_16x16x32_bf16 v[204:207], v[120:123], v[228:231], v[204:207]\n\t" \
    "ds_read_b128 v[240:243], v253 offset:29696\n\t" \
    "s_waitcnt lgkmcnt(2)\n\t" \
    "v_mfma_f32_16x16x32_bf16 v[204:207], v[124:127], v[232:235], v[204:207]\n\t" \
    "ds_read_b128 v[224:227], v253 offset:10240\n\t" \
    "v_mfma_f32_16x16x32_bf16 v[208:211], v[128:131], v[228:231], v[208:211]\n\t" \
    "v_mfma_f32_16x16x32_bf16 v[208:211], v[132:135], v[232:235], v[208:211]\n\t" \
    "v_mfma_f32_16x16x32_bf16 v[200:203], v[244:247], v[228:231], v[200:203]\n\t" \
    "v_mfma_f32_16x16x32_bf16 v[200:203], v[248:251], v[232:235], v[200:203]\n\t" \
    "s_waitcnt lgkmcnt(2)\n\t" \
    "v_mfma_f32_16x16x32_bf16 v[216:219], v[120:123], v[236:239], v[216:219]\n\t" \
    "ds_read_b128 v[228:231], v253 offset:11264\n\t" \
    "s_waitcnt lgkmcnt(2)\n\t" \
    "v_mfma_f32_16x16x32_bf16 v[216:219], v[124:127], v[240:243], v[216:219]\n\t" \
    "ds_read_b128 v[232:235], v253 offset:30720\n\t" \
    "v_mfma_f32_16x16x32_bf16 v[220:223], v[128:131], v[236:239], v[220:223]\n\t" \
    "v_mfma_f32_16x16x32_bf16 v[220:223], v[132:135], v[240:243], v[220:223]\n\t" \
    "v_mfma_f32_16x16x32_bf16 v[212:215], v[244:247], v[236:239], v[212:215]\n\t" \
    "ds_read_b128 v[244:247], v254 offset:63488\n\t" \
    "v_mfma_f32_16x16x32_bf16 v[212:215], v[248:251], v[240:243], v[212:215]\n\t" \
    "ds_read_b128 v[248:251], v254 offset:64512\n\t" \
    "ds_read_b128 v[236:239], v253 offset:31744\n\t" \
    "s_setprio 2\n\t" \
    "s_waitcnt lgkmcnt(0)\n\t" \
    "s_barrier \n\t" \
    LST7 \
    LJIT \
    "v_mfma_f32_16x16x32_bf16 v[204:207], v[144:147], v[224:227], v[204:207]\n\t" \
    "v_mfma_f32_16x16x32_bf16 v[204:207], v[148:151], v[228:231], v[204:207]\n\t" \
    "ds_read_b128 v[240:243], v253 offset:12288\n\t" \
    "v_mfma_f32_16x16x32_bf16 v[208:211], v[152:155], v[224:227], v[208:211]\n\t" \
    "v_mfma_f32_16x16x32_bf16 v[208:211], v[156:159], v[228:231], v[208:211]\n\t" \
    "v_mfma_f32_16x16x32_bf16 v[200:203], v[136:139], v[224:227], v[200:203]\n\t" \
    "v_mfma_f32_16x16x32_bf16 v[200:203], v[140:143], v[228:231], v[200:203]\n\t" \
    "v_mfma_f32_16x16x32_bf16 v[216:219], v[144:147], v[232:235], v[216:219]\n\t" \
    "ds_read_b128 v[224:227], v253 offset:13312\n\t" \
    "s_and_b32 s44, s40, 3\n\t" \
    "v_mfma_f32_16x16x32_bf16 v[216:219], v[148:151], v[236:239], v[216:219]\n\t" \
    "ds_read_b128 v[228:231], v253 offset:32768\n\t" \
    "s_lshl_b32 s44, s44, 17\n\t" \
    "v_mfma_f32_16x16x32_bf16 v[220:223], v[152:155], v[232:235], v[220:223]\n\t" \
    "s_add_u32 s44, s44, %[mb]\n\t" \
    "v_mfma_f32_16x16x32_bf16 v[220:223], v[156:159], v[236:239], v[220:223]\n\t" \
    "s_add_u32 s46, s44, 0x4000\n\t" \
    "v_mfma_f32_16x16x32_bf16 v[212:215], v[136:139], v[232:235], v[212:215]\n\t" \
    "s_add_u32 s47, s44, 0x4400\n\t" \
    "v_mfma_f32_16x16x32_bf16 v[212:215], v[140:143], v[236:239], v[212:215]\n\t" \
    "s_waitcnt lgkmcnt(2)\n\t" \
    "v_mfma_f32_16x16x32_bf16 v[204:207], v[160:163], v[240:243], v[204:207]\n\t" \
    "ds_read_b128 v[232:235], v253 offset:33792\n\t" \
    "s_waitcnt lgkmcnt(2)\n\t" \
    "v_mfma_f32_16x16x32_bf16 v[204:207], v[164:167], v[224:227], v[204:207]\n\t" \
    "ds_read_b128 v[236:239], v253 offset:16384\n\t" \
    "v_mfma_f32_16x16x32_bf16 v[208:211], v[168:171], v[240:243], v[208:211]\n\t" \
    "v_mfma_f32_16x16x32_bf16 v[208:211], v[172:175], v[224:227], v[208:211]\n\t" \
    "v_mfma_f32_16x16x32_bf16 v[200:203], v[244:247], v[240:243], v[200:203]\n\t" \
    "v_mfma_f32_16x16x32_bf16 v[200:203], v[248:251], v[224:227], v[200:203]\n\t" \
    "s_waitcnt lgkmcnt(2)\n\t" \
    "v_mfma_f32_16x16x32_bf16 v[216:219], v[160:163], v[228:231], v[216:219]\n\t" \
    "ds_read_b128 v[240:243], v253 offset:17408\n\t" \
    "s_waitcnt lgkmcnt(2)\n\t" \
    "v_mfma_f32_16x16x32_bf16 v[216:219], v[164:167], v[232:235], v[216:219]\n\t" \
    "ds_read_b128 v[224:227], v253 offset:36864\n\t" \
    "v_mfma_f32_16x16x32_bf16 v[220:223], v[168:171], v[228:231], v[220:223]\n\t" \
    "v_mfma_f32_16x16x32_bf16 v[220:223], v[172:175], v[232:235], v[220:223]\n\t" \
    "v_mfma_f32_16x16x32_bf16 v[212:215], v[244:247], v[228:231], v[212:215]\n\t" \
    "ds_read_b128 v[244:247], v254 offset:55296\n\t" \
    "v_mfma_f32_16x16x32_bf16 v[212:215], v[248:251], v[232:235], v[212:215]\n\t" \
    "ds_read_b128 v[248:251], v254 offset:56320\n\t" \
    "s_waitcnt lgkmcnt(4)\n\t" \
    "v_mfma_f32_16x16x32_bf16 v[204:207], v[0:3], v[236:239], v[204:207]\n\t" \
    "ds_read_b128 v[228:231], v253 offset:37888\n\t" \
    "s_waitcnt lgkmcnt(4)\n\t" \
    "v_mfma_f32_16x16x32_bf16 v[204:207], v[4:7], v[240:243], v[204:207]\n\t" \
    "ds_read_b128 v[232:235], v253 offset:14336\n\t" \
    "v_mfma_f32_16x16x32_bf16 v[208:211], v[8:11], v[236:239], v[208:211]\n\t" \
    "v_mfma_f32_16x16x32_bf16 v[208:211], v[12:15], v[240:243], v[208:211]\n\t" \
    "s_waitcnt lgkmcnt(3)\n\t" \
    "v_mfma_f32_16x16x32_bf16 v[200:203], v[244:247], v[236:239], v[200:203]\n\t" \
    "s_waitcnt lgkmcnt(2)\n\t" \
    "v_mfma_f32_16x16x32_bf16 v[200:203], v[248:251], v[240:243], v[200:203]\n\t" \
    "v_mfma_f32_16x16x32_bf16 v[216:219], v[0:3], v[224:227], v[216:219]\n\t" \
    "ds_read_b128 v[236:239], v253 offset:15360\n\t" \
    "s_waitcnt lgkmcnt(2)\n\t" \
    "v_mfma_f32_16x16x32_bf16 v[216:219], v[4:7], v[228:231], v[216:219]\n\t" \
    "ds_read_b128 v[240:243], v253 offset:18432\n\t" \
    "v_mfma_f32_16x16x32_bf16 v[220:223], v[8:11], v[224:227], v[220:223]\n\t" \
    "v_mfma_f32_16x16x32_bf16 v[220:223], v[12:15], v[228:231], v[220:223]\n\t" \
    "v_mfma_f32_16x16x32_bf16 v[212:215], v[244:247], v[224:227], v[212:215]\n\t" \
    "v_mfma_f32_16x16x32_bf16 v[212:215], v[248:251], v[228:231], v[212:215]\n\t" \
    "s_waitcnt lgkmcnt(2)\n\t" \
    "v_mfma_f32_16x16x32_bf16 v[204:207], v[184:187], v[232:235], v[204:207]\n\t" \
    "ds_read_b128 v[224:227], v253 offset:19456\n\t" \
    "v_mov_b64 v[244:245], 0\n\t" \
    "s_waitcnt lgkmcnt(2)\n\t" \
    "v_mfma_f32_16x16x32_bf16 v[204:207], v[188:191], v[236:239], v[204:207]\n\t" \
    "ds_read_b128 v[228:231], v253 offset:34816\n\t" \
    "v_mov_b64 v[246:247], 0\n\t" \
    "v_mfma_f32_16x16x32_bf16 v[208:211], v[192:195], v[232:235], v[208:211]\n\t" \
    "v_mov_b64 v[248:249], 0\n\t" \
    "v_mfma_f32_16x16x32_bf16 v[208:211], v[196:199], v[236:239], v[208:211]\n\t" \
    "v_mov_b64 v[250:251], 0\n\t" \
    "v_mfma_f32_16x16x32_bf16 v[200:203], v[176:179], v[232:235], v[200:203]\n\t" \
    "v_mfma_f32_16x16x32_bf16 v[200:203], v[180:183], v[236:239], v[200:203]\n\t" \
    "s_waitcnt lgkmcnt(2)\n\t" \
    "v_mfma_f32_16x16x32_bf16 v[204:207], v[24:27], v[240:243], v[204:207]\n\t" \
    "ds_read_b128 v[232:235], v253 offset:35840\n\t" \
    "s_waitcnt lgkmcnt(2)\n\t" \
    "v_mfma_f32_16x16x32_bf16 v[204:207], v[28:31], v[224:227], v[204:207]\n\t" \
    "ds_read_b128 v[236:239], v253 offset:38912\n\t" \
    "v_mfma_f32_16x16x32_bf16 v[208:211], v[32:35], v[240:243], v[208:211]\n\t" \
    "v_mfma_f32_16x16x32_bf16 v[208:211], v[36:39], v[224:227], v[208:211]\n\t" \
    "v_mfma_f32_16x16x32_bf16 v[200:203], v[16:19], v[240:243], v[200:203]\n\t" \
    "v_mfma_f32_16x16x32_bf16 v[200:203], v[20:23], v[224:227], v[200:203]\n\t" \
    "ds_read_b128 v[240:243], v253 offset:39936\n\t" \
    "s_waitcnt lgkmcnt(3)\n\t" \
    "v_mfma_f32_16x16x32_bf16 v[216:219], v[184:187], v[228:231], v[216:219]\n\t" \
    "s_waitcnt lgkmcnt(2)\n\t" \
    "v_mfma_f32_16x16x32_bf16 v[216:219], v[188:191], v[232:235], v[216:219]\n\t" \
    "v_cvt_pk_bf16_f32 v204, v204, v208\n\t" \
    "v_cvt_pk_bf16_f32 v205, v205, v209\n\t" \
    "s_waitcnt lgkmcnt(1)\n\t" \
    "v_mfma_f32_16x16x32_bf16 v[216:219], v[24:27], v[236:239], v[216:219]\n\t" \
    "v_cvt_pk_bf16_f32 v206, v206, v210\n\t" \
    "v_cvt_pk_bf16_f32 v207, v207, v211\n\t" \
    "s_waitcnt lgkmcnt(0)\n\t" \
    "v_mfma_f32_16x16x32_bf16 v[216:219], v[28:31], v[240:243], v[216:219]\n\t" \
    "ds_write_b128 v254, v[204:207] offset:5120\n\t" \
    "v_mfma_f32_16x16x32_bf16 v[220:223], v[192:195], v[228:231], v[220:223]\n\t" \
    "v_mfma_f32_16x16x32_bf16 v[220:223], v[196:199], v[232:235], v[220:223]\n\t" \
    "v_cvt_pk_bf16_f32 v200, v200, v201\n\t" \
    "v_cvt_pk_bf16_f32 v201, v202, v203\n\t" \
    "v_mfma_f32_16x16x32_bf16 v[220:223], v[32:35], v[236:239], v[220:223]\n\t" \
    "ds_write_b64 v254, v[200:201] offset:1032\n\t" \
    "v_mfma_f32_16x16x32_bf16 v[220:223], v[36:39], v[240:243], v[220:223]\n\t" \
    "v_mfma_f32_16x16x32_bf16 v[212:215], v[176:179], v[228:231], v[212:215]\n\t" \
    "ds_read_b128 v[200:203], v254 offset:55296\n\t" \
    "ds_read_b128 v[204:207], v254 offset:56320\n\t" \
    "v_mfma_f32_16x16x32_bf16 v[212:215], v[180:183], v[232:235], v[212:215]\n\t" \
    "v_mov_b64 v[208:209], 0\n\t" \
    "v_mov_b64 v[210:211], 0\n\t" \
    "v_mfma_f32_16x16x32_bf16 v[212:215], v[16:19], v[236:239], v[212:215]\n\t" \
    "v_cvt_pk_bf16_f32 v216, v216, v220\n\t" \
    "v_cvt_pk_bf16_f32 v217, v217, v221\n\t" \
    "v_mfma_f32_16x16x32_bf16 v[212:215], v[20:23], v[240:243], v[212:215]\n\t" \
    "v_cvt_pk_bf16_f32 v218, v218, v222\n\t" \
    "v_cvt_pk_bf16_f32 v219, v219, v223\n\t" \
    "s_setprio 0\n\t" \
    "ds_write_b128 v254, v[216:219] offset:11264\n\t" \
    "ds_read_b128 v[216:219], v254 offset:57344\n\t" \
    "ds_read_b128 v[220:223], v254 offset:58368\n\t" \
    "s_nop 2\n\t" \
    "v_cvt_pk_bf16_f32 v212, v212, v213\n\t" \
    "v_cvt_pk_bf16_f32 v213, v214, v215\n\t" \
    "ds_write_b64 v254, v[212:213] offset:7176\n\t" \
    LST6 \
    LJIT \
    "s_sleep 3\n\t" \
    "s_mov_b32 m0, 0xc000\n\t" \
    "s_nop 0\n\t" \
    "buffer_load_dwordx4 v253, s[60:63], s46 offen sc1 lds\n\t" \
    "s_mov_b32 m0, 0xc400\n\t" \
    "s_nop 0\n\t" \
    "buffer_load_dwordx4 v253, s[60:63], s47 offen sc1 lds\n\t" \
    "s_waitcnt lgkmcnt(0)\n\t" \
    LFLUSH212 \
    "v_bfe_i32 v212, v253, 5, 1\n\t" \
    "v_and_b32 v212, 0xaaaa, v212\n\t" \
    "v_or_b32 v212, 0x4444, v212\n\t" \
    "v_and_b32 v213, 0xffff, v255\n\t" \
    "v_lshrrev_b32 v214, 16, v255\n\t" \
    ".Ls34_%=:\n\t" \
    "s_waitcnt vmcnt(0)\n\t" \
    "ds_read_b128 v[224:227], v253 offset:49152\n\t" \
    "ds_read_b128 v[228:231], v253 offset:50176\n\t" \
    "s_waitcnt lgkmcnt(1)\n\t" \
    "v_max_u32 v215, v224, v225\n\t" \
    "v_max3_u32 v215, v226, v227, v215\n\t" \
    "s_waitcnt lgkmcnt(0)\n\t" \
    "v_max3_u32 v215, v228, v229, v215\n\t" \
    "v_max3_u32 v215, v230, v231, v215\n\t" \
    "v_cmp_eq_u32 vcc, -1, v215\n\t" \
    "s_cbranch_vccz .Ls35_%=\n\t" \
    "v_max_u32 v215, v224, v225\n\t" \
    "v_max3_u32 v215, v226, v227, v215\n\t" \
    "v_cmp_eq_u32 vcc, -1, v215\n\t" \
    "s_cbranch_vccz .Ls36_%=\n\t" \
    "s_mov_b32 m0, 0xc000\n\t" \
    "s_nop 0\n\t" \
    "buffer_load_dwordx4 v253, s[60:63], s46 offen sc1 lds\n\t" \
    ".Ls36_%=:\n\t" \
    "v_max_u32 v215, v228, v229\n\t" \
    "v_max3_u32 v215, v230, v231, v215\n\t" \
    "v_cmp_eq_u32 vcc, -1, v215\n\t" \
    "s_cbranch_vccz .Ls37_%=\n\t" \
    "s_mov_b32 m0, 0xc400\n\t" \
    "s_nop 0\n\t" \
    "buffer_load_dwordx4 v253, s[60:63], s47 offen sc1 lds\n\t" \
    ".Ls37_%=:\n\t" \
    "s_cmp_eq_u32 s41, 0\n\t" \
    "s_cbranch_scc1 .Ls35_%=\n\t" \
    "s_sub_u32 s41, s41, 1\n\t" \
    "s_branch .Ls34_%=\n\t" \
    ".Ls35_%=:\n\t" \
    LST2 \
    LJIT \
    "s_setprio 3\n\t" \
    "s_waitcnt lgkmcnt(0)\n\t" \
    "s_barrier \n\t" \
    LST3 \
    LJIT \
    "ds_read_b128 v[232:235], v253 offset:40960\n\t" \
    "ds_read_b128 v[236:239], v253 offset:41984\n\t" \
    "ds_read_b128 v[240:243], v253 offset:43008\n\t" \
    "v_smfmac_f32_16x16x64_bf16 v[244:247], v[224:227], v[0:7], v212\n\t" \
    "v_smfmac_f32_16x16x64_bf16 v[248:251], v[224:227], v[8:15], v212\n\t" \
    "v_smfmac_f32_16x16x64_bf16 v[208:211], v[224:227], v[200:207], v212\n\t" \
    "ds_read_b128 v[200:203], v254 offset:59392\n\t" \
    "ds_read_b128 v[204:207], v254 offset:60416\n\t" \
    "ds_read_b128 v[224:227], v253 offset:44032\n\t" \
    "v_smfmac_f32_16x16x64_bf16 v[208:211], v[228:231], v[16:23], v212\n\t" \
    "v_smfmac_f32_16x16x64_bf16 v[244:247], v[228:231], v[24:31], v212\n\t" \
    "v_smfmac_f32_16x16x64_bf16 v[248:251], v[228:231], v[32:39], v212\n\t" \
    "ds_read_b128 v[228:231], v253 offset:45056\n\t" \
    "s_waitcnt lgkmcnt(6)\n\t" \
    "v_smfmac_f32_16x16x64_bf16 v[208:211], v[232:235], v[216:223], v212\n\t" \
    "ds_read_b128 v[216:219], v254 offset:61440\n\t" \
    "ds_read_b128 v[220:223], v254 offset:62464\n\t" \
    "v_smfmac_f32_16x16x64_bf16 v[244:247], v[232:235], v[40:47], v212\n\t" \
    "v_smfmac_f32_16x16x64_bf16 v[248:251], v[232:235], v[48:55], v212\n\t" \
    "ds_read_b128 v[232:235], v253 offset:46080\n\t" \
    "s_waitcnt lgkmcnt(8)\n\t" \
    "v_smfmac_f32_16x16x64_bf16 v[208:211], v[236:239], v[56:63], v212\n\t" \
    "v_smfmac_f32_16x16x64_bf16 v[244:247], v[236:239], v[64:71], v212\n\t" \
    "v_smfmac_f32_16x16x64_bf16 v[248:251], v[236:239], v[72:79], v212\n\t" \
    "ds_read_b128 v[236:239], v253 offset:47104\n\t" \
    "s_waitcnt lgkmcnt(6)\n\t" \
    "v_smfmac_f32_16x16x64_bf16 v[208:211], v[240:243], v[200:207], v212\n\t" \
    "ds_read_b128 v[200:203], v254 offset:63488\n\t" \
    "ds_read_b128 v[204:207], v254 offset:64512\n\t" \
    "v_smfmac_f32_16x16x64_bf16 v[244:247], v[240:243], v[80:87], v212\n\t" \
    "v_smfmac_f32_16x16x64_bf16 v[248:251], v[240:243], v[88:95], v212\n\t" \
    "ds_read_b128 v[240:243], v253 offset:48128\n\t" \
    "s_waitcnt lgkmcnt(8)\n\t" \
    "v_smfmac_f32_16x16x64_bf16 v[208:211], v[224:227], v[96:103], v212\n\t" \
    "v_smfmac_f32_16x16x64_bf16 v[244:247], v[224:227], v[104:111], v212\n\t" \
    "v_smfmac_f32_16x16x64_bf16 v[248:251], v[224:227], v[112:119], v212\n\t" \
    "s_waitcnt lgkmcnt(5)\n\t" \
    "v_smfmac_f32_16x16x64_bf16 v[208:211], v[228:231], v[216:223], v212\n\t" \
    "s_waitcnt lgkmcnt(4)\n\t" \
    "v_smfmac_f32_16x16x64_bf16 v[208:211], v[232:235], v[136:143], v212\n\t" \
    "s_waitcnt lgkmcnt(1)\n\t" \
    "v_smfmac_f32_16x16x64_bf16 v[208:211], v[236:239], v[200:207], v212\n\t" \
    "s_waitcnt lgkmcnt(0)\n\t" \
    "v_smfmac_f32_16x16x64_bf16 v[208:211], v[240:243], v[176:183], v212\n\t" \
    "v_smfmac_f32_16x16x64_bf16 v[244:247], v[228:231], v[120:127], v212\n\t" \
    "v_smfmac_f32_16x16x64_bf16 v[244:247], v[232:235], v[144:151], v212\n\t" \
    "v_smfmac_f32_16x16x64_bf16 v[244:247], v[236:239], v[160:167], v212\n\t" \
    "v_add_f32 v208, v208, v210\n\t" \
    "v_add_f32 v209, v209, v211\n\t" \
    "v_cvt_pk_bf16_f32 v208, v208, v209\n\t" \
    "v_smfmac_f32_16x16x64_bf16 v[244:247], v[240:243], v[184:191], v212\n\t" \
    "ds_write_b16 v213, v208 offset:0\n\t" \
    "ds_write_b16_d16_hi v213, v208 offset:16\n\t" \
    "v_smfmac_f32_16x16x64_bf16 v[248:251], v[228:231], v[128:135], v212\n\t" \
    "v_smfmac_f32_16x16x64_bf16 v[248:251], v[232:235], v[152:159], v212\n\t" \
    "v_smfmac_f32_16x16x64_bf16 v[248:251], v[236:239], v[168:175], v212\n\t" \
    "v_add_f32 v244, v244, v246\n\t" \
    "v_add_f32 v245, v245, v247\n\t" \
    "v_smfmac_f32_16x16x64_bf16 v[248:251], v[240:243], v[192:199], v212\n\t" \
    "s_nop 7\n\t" \
    "v_add_f32 v248, v248, v250\n\t" \
    "v_add_f32 v249, v249, v251\n\t" \
    "v_cvt_pk_bf16_f32 v244, v244, v248\n\t" \
    "v_cvt_pk_bf16_f32 v245, v245, v249\n\t" \
    "ds_write2_b32 v214, v244, v245 offset1:4\n\t" \
    "s_setprio 0\n\t" \
    "s_sleep 1\n\t" \
    "ds_read_b128 v[208:211], v254 offset:24576\n\t" \
    "ds_read_b128 v[224:227], v253 offset:0\n\t" \
    "ds_read_b128 v[228:231], v253 offset:20480\n\t" \
    "ds_read_b128 v[212:215], v254 offset:25600\n\t" \
    "ds_read_b128 v[232:235], v253 offset:1024\n\t" \
    "ds_read_b128 v[236:239], v253 offset:21504\n\t" \
    "ds_read_b128 v[216:219], v254 offset:26624\n\t" \
    "ds_read_b128 v[240:243], v253 offset:2048\n\t" \
    "ds_read_b128 v[244:247], v253 offset:22528\n\t" \
    "ds_read_b128 v[220:223], v254 offset:27648\n\t" \
    "ds_read_b128 v[248:251], v253 offset:3072\n\t" \
    "s_waitcnt lgkmcnt(9)\n\t" \
    "v_mfma_f32_16x16x32_bf16 v[200:203], v[208:211], v[224:227], 0\n\t" \
    "ds_read_b128 v[224:227], v253 offset:23552\n\t" \
    "s_waitcnt lgkmcnt(9)\n\t" \
    "v_mfma_f32_16x16x32_bf16 v[204:207], v[208:211], v[228:231], 0\n\t" \
    "ds_read_b128 v[228:231], v253 offset:4096\n\t" \
    "ds_read_b128 v[208:211], v254 offset:28672\n\t" \
    "s_waitcnt lgkmcnt(9)\n\t" \
    "v_mfma_f32_16x16x32_bf16 v[200:203], v[212:215], v[232:235], v[200:203]\n\t" \
    "ds_read_b128 v[232:235], v253 offset:24576\n\t" \
    "s_waitcnt lgkmcnt(9)\n\t" \
    "v_mfma_f32_16x16x32_bf16 v[204:207], v[212:215], v[236:239], v[204:207]\n\t" \
    "ds_read_b128 v[236:239], v253 offset:5120\n\t" \
    "ds_read_b128 v[212:215], v254 offset:29696\n\t" \
    "s_waitcnt lgkmcnt(9)\n\t" \
    "v_mfma_f32_16x16x32_bf16 v[200:203], v[216:219], v[240:243], v[200:203]\n\t" \
    "ds_read_b128 v[240:243], v253 offset:25600\n\t" \
    "s_waitcnt lgkmcnt(0)\n\t" \
    "s_barrier \n\t" \
    LST4 \
    LJIT \
    "s_add_u32 s40, s40, 1\n\t" \
    "s_cmp_lg_u32 s40, 64\n\t" \
    "s_cbranch_scc1 .Ls33_%=\n\t" \
    "s_branch .Lend%=\n\t" \
    ".Ls2_%=:\n\t" \
    "v_add_u32 v244, 0x18000, v243\n\t" \
    "s_waitcnt vmcnt(12) lgkmcnt(0)\n\t" \
    "s_barrier \n\t" \
    "s_add_u32 m0, %[wm], 0x12000\n\t" \
    "s_mov_b32 s44, 0x1e0000\n\t" \
    "buffer_load_dwordx4 v240, %[wr], s44 offen offset:0 nt lds\n\t" \
    "buffer_load_dwordx4 v241, %[wr], s44 offen offset:1024 nt lds\n\t" \
    "buffer_load_dwordx4 v242, %[wr], s44 offen offset:2048 nt lds\n\t" \
    "ds_read_b64_tr_b16 v[208:209], v244 offset:0\n\t" \
    "ds_read_b64_tr_b16 v[210:211], v244 offset:1536\n\t" \
    "ds_read_b64_tr_b16 v[40:41], v244 offset:32\n\t" \
    "ds_read_b64_tr_b16 v[42:43], v244 offset:1568\n\t" \
    "ds_read_b64_tr_b16 v[48:49], v244 offset:64\n\t" \
    "ds_read_b64_tr_b16 v[50:51], v244 offset:1600\n\t" \
    "v_add_u32 v244, 0x1e000, v243\n\t" \
    "s_waitcnt vmcnt(12) lgkmcnt(0)\n\t" \
    "s_barrier \n\t" \
    "s_add_u32 m0, %[wm], 0x18000\n\t" \
    "s_mov_b32 s44, 0x240000\n\t" \
    "buffer_load_dwordx4 v240, %[wr], s44 offen offset:0 nt lds\n\t" \
    "buffer_load_dwordx4 v241, %[wr], s44 offen offset:1024 nt lds\n\t" \
    "buffer_load_dwordx4 v242, %[wr], s44 offen offset:2048 nt lds\n\t" \
    "ds_read_b64_tr_b16 v[212:213], v244 offset:0\n\t" \
    "ds_read_b64_tr_b16 v[214:215], v244 offset:1536\n\t" \
    "ds_read_b64_tr_b16 v[44:45], v244 offset:32\n\t" \
    "ds_read_b64_tr_b16 v[46:47], v244 offset:1568\n\t" \
    "ds_read_b64_tr_b16 v[52:53], v244 offset:64\n\t" \
    "ds_read_b64_tr_b16 v[54:55], v244 offset:1600\n\t" \
    "v_add_u32 v244, 0x0, v243\n\t" \
    "s_waitcnt vmcnt(12) lgkmcnt(0)\n\t" \
    "s_barrier \n\t" \
    "s_add_u32 m0, %[wm], 0x1e000\n\t" \
    "s_mov_b32 s44, 0x2a0000\n\t" \
    "buffer_load_dwordx4 v240, %[wr], s44 offen offset:0 nt lds\n\t" \
    "buffer_load_dwordx4 v241, %[wr], s44 offen offset:1024 nt lds\n\t" \
    "buffer_load_dwordx4 v242, %[wr], s44 offen offset:2048 nt lds\n\t" \
    "ds_read_b64_tr_b16 v[56:57], v244 offset:0\n\t" \
    "ds_read_b64_tr_b16 v[58:59], v244 offset:1536\n\t" \
    "ds_read_b64_tr_b16 v[64:65], v244 offset:32\n\t" \
    "ds_read_b64_tr_b16 v[66:67], v244 offset:1568\n\t" \
    "ds_read_b64_tr_b16 v[72:73], v244 offset:64\n\t" \
    "ds_read_b64_tr_b16 v[74:75], v244 offset:1600\n\t" \
    "v_add_u32 v244, 0x6000, v243\n\t" \
    "s_waitcnt vmcnt(12) lgkmcnt(0)\n\t" \
    "s_barrier \n\t" \
    "s_add_u32 m0, %[wm], 0x0\n\t" \
    "s_mov_b32 s44, 0x300000\n\t" \
    "buffer_load_dwordx4 v240, %[wr], s44 offen offset:0 nt lds\n\t" \
    "buffer_load_dwordx4 v241, %[wr], s44 offen offset:1024 nt lds\n\t" \
    "buffer_load_dwordx4 v242, %[wr], s44 offen offset:2048 nt lds\n\t" \
    "ds_read_b64_tr_b16 v[60:61], v244 offset:0\n\t" \
    "ds_read_b64_tr_b16 v[62:63], v244 offset:1536\n\t" \
    "ds_read_b64_tr_b16 v[68:69], v244 offset:32\n\t" \
    "ds_read_b64_tr_b16 v[70:71], v244 offset:1568\n\t" \
    "ds_read_b64_tr_b16 v[76:77], v244 offset:64\n\t" \
    "ds_read_b64_tr_b16 v[78:79], v244 offset:1600\n\t" \
    "v_add_u32 v244, 0xc000, v243\n\t" \
    "s_waitcnt vmcnt(12) lgkmcnt(0)\n\t" \
    "s_barrier \n\t" \
    "s_add_u32 m0, %[wm], 0x6000\n\t" \
    "s_mov_b32 s44, 0x360000\n\t" \
    "buffer_load_dwordx4 v240, %[wr], s44 offen offset:0 nt lds\n\t" \
    "buffer_load_dwordx4 v241, %[wr], s44 offen offset:1024 nt lds\n\t" \
    "buffer_load_dwordx4 v242, %[wr], s44 offen offset:2048 nt lds\n\t" \
    "ds_read_b64_tr_b16 v[216:217], v244 offset:0\n\t" \
    "ds_read_b64_tr_b16 v[218:219], v244 offset:1536\n\t" \
    "ds_read_b64_tr_b16 v[80:81], v244 offset:32\n\t" \
    "ds_read_b64_tr_b16 v[82:83], v244 offset:1568\n\t" \
    "ds_read_b64_tr_b16 v[88:89], v244 offset:64\n\t" \
    "ds_read_b64_tr_b16 v[90:91], v244 offset:1600\n\t" \
    "v_add_u32 v244, 0x12000, v243\n\t" \
    "s_waitcnt vmcnt(12) lgkmcnt(0)\n\t" \
    "s_barrier \n\t" \
    "s_add_u32 m0, %[wm], 0xc000\n\t" \
    "s_mov_b32 s44, 0x3c0000\n\t" \
    "buffer_load_dwordx4 v240, %[wr], s44 offen offset:0 nt lds\n\t" \
    "buffer_load_dwordx4 v241, %[wr], s44 offen offset:1024 nt lds\n\t" \
    "buffer_load_dwordx4 v242, %[wr], s44 offen offset:2048 nt lds\n\t" \
    "ds_read_b64_tr_b16 v[220:221], v244 offset:0\n\t" \
    "ds_read_b64_tr_b16 v[222:223], v244 offset:1536\n\t" \
    "ds_read_b64_tr_b16 v[84:85], v244 offset:32\n\t" \
    "ds_read_b64_tr_b16 v[86:87], v244 offset:1568\n\t" \
    "ds_read_b64_tr_b16 v[92:93], v244 offset:64\n\t" \
    "ds_read_b64_tr_b16 v[94:95], v244 offset:1600\n\t" \
    "v_add_u32 v244, 0x18000, v243\n\t" \
    "s_waitcnt vmcnt(12) lgkmcnt(0)\n\t" \
    "s_barrier \n\t" \
    "s_add_u32 m0, %[wm], 0x12000\n\t" \
    "s_mov_b32 s44, 0x420000\n\t" \
    "buffer_load_dwordx4 v240, %[wr], s44 offen offset:0 nt lds\n\t" \
    "buffer_load_dwordx4 v241, %[wr], s44 offen offset:1024 nt lds\n\t" \
    "buffer_load_dwordx4 v242, %[wr], s44 offen offset:2048 nt lds\n\t" \
    "ds_read_b64_tr_b16 v[96:97], v244 offset:0\n\t" \
    "ds_read_b64_tr_b16 v[98:99], v244 offset:1536\n\t" \
    "ds_read_b64_tr_b16 v[104:105], v244 offset:32\n\t" \
    "ds_read_b64_tr_b16 v[106:107], v244 offset:1568\n\t" \
    "ds_read_b64_tr_b16 v[112:113], v244 offset:64\n\t" \
    "ds_read_b64_tr_b16 v[114:115], v244 offset:1600\n\t" \
    "v_add_u32 v244, 0x1e000, v243\n\t" \
    "s_waitcnt vmcnt(12) lgkmcnt(0)\n\t" \
    "s_barrier \n\t" \
    "s_add_u32 m0, %[wm], 0x18000\n\t" \
    "s_mov_b32 s44, 0x480000\n\t" \
    "buffer_load_dwordx4 v240, %[wr], s44 offen offset:0 nt lds\n\t" \
    "buffer_load_dwordx4 v241, %[wr], s44 offen offset:1024 nt lds\n\t" \
    "buffer_load_dwordx4 v242, %[wr], s44 offen offset:2048 nt lds\n\t" \
    "ds_read_b64_tr_b16 v[100:101], v244 offset:0\n\t" \
    "ds_read_b64_tr_b16 v[102:103], v244 offset:1536\n\t" \
    "ds_read_b64_tr_b16 v[108:109], v244 offset:32\n\t" \
    "ds_read_b64_tr_b16 v[110:111], v244 offset:1568\n\t" \
    "ds_read_b64_tr_b16 v[116:117], v244 offset:64\n\t" \
    "ds_read_b64_tr_b16 v[118:119], v244 offset:1600\n\t" \
    "v_add_u32 v244, 0x0, v243\n\t" \
    "s_waitcnt vmcnt(12) lgkmcnt(0)\n\t" \
    "s_barrier \n\t" \
    "v_mov_b32 v246, 0\n\t" \
    "buffer_load_dword v246, v246, s[60:63], %[mo] offen sc1\n\t" \
    "s_add_u32 m0, %[wm], 0x1e000\n\t" \
    "s_mov_b32 s44, 0x4e0000\n\t" \
    "buffer_load_dwordx4 v240, %[wr], s44 offen offset:0 nt lds\n\t" \
    "buffer_load_dwordx4 v241, %[wr], s44 offen offset:1024 nt lds\n\t" \
    "buffer_load_dwordx4 v242, %[wr], s44 offen offset:2048 nt lds\n\t" \
    "ds_read_b64_tr_b16 v[200:201], v244 offset:0\n\t" \
    "ds_read_b64_tr_b16 v[202:203], v244 offset:1536\n\t" \
    "ds_read_b64_tr_b16 v[0:1], v244 offset:32\n\t" \
    "ds_read_b64_tr_b16 v[2:3], v244 offset:1568\n\t" \
    "ds_read_b64_tr_b16 v[8:9], v244 offset:64\n\t" \
    "ds_read_b64_tr_b16 v[10:11], v244 offset:1600\n\t" \
    "v_add_u32 v244, 0x6000, v243\n\t" \
    "s_waitcnt vmcnt(12) lgkmcnt(0)\n\t" \
    "s_barrier \n\t" \
    "s_add_u32 m0, %[wm], 0x0\n\t" \
    "s_mov_b32 s44, 0x540000\n\t" \
    "buffer_load_dwordx4 v240, %[wr], s44 offen offset:0 nt lds\n\t" \
    "buffer_load_dwordx4 v241, %[wr], s44 offen offset:1024 nt lds\n\t" \
    "buffer_load_dwordx4 v242, %[wr], s44 offen offset:2048 nt lds\n\t" \
    "ds_read_b64_tr_b16 v[204:205], v244 offset:0\n\t" \
    "ds_read_b64_tr_b16 v[206:207], v244 offset:1536\n\t" \
    "ds_read_b64_tr_b16 v[4:5], v244 offset:32\n\t" \
    "ds_read_b64_tr_b16 v[6:7], v244 offset:1568\n\t" \
    "ds_read_b64_tr_b16 v[12:13], v244 offset:64\n\t" \
    "ds_read_b64_tr_b16 v[14:15], v244 offset:1600\n\t" \
    "v_add_u32 v244, 0xc000, v243\n\t" \
    "s_waitcnt vmcnt(12) lgkmcnt(0)\n\t" \
    "s_barrier \n\t" \
    "s_add_u32 m0, %[wm], 0x6000\n\t" \
    "s_mov_b32 s44, 0x5a0000\n\t" \
    "buffer_load_dwordx4 v240, %[wr], s44 offen offset:0 nt lds\n\t" \
    "buffer_load_dwordx4 v241, %[wr], s44 offen offset:1024 nt lds\n\t" \
    "buffer_load_dwordx4 v242, %[wr], s44 offen offset:2048 nt lds\n\t" \
    "ds_read_b64_tr_b16 v[224:225], v244 offset:0\n\t" \
    "ds_read_b64_tr_b16 v[226:227], v244 offset:1536\n\t" \
    "ds_read_b64_tr_b16 v[120:121], v244 offset:32\n\t" \
    "ds_read_b64_tr_b16 v[122:123], v244 offset:1568\n\t" \
    "ds_read_b64_tr_b16 v[128:129], v244 offset:64\n\t" \
    "ds_read_b64_tr_b16 v[130:131], v244 offset:1600\n\t" \
    "v_add_u32 v244, 0x12000, v243\n\t" \
    "s_waitcnt vmcnt(12) lgkmcnt(0)\n\t" \
    "s_barrier \n\t" \
    "s_add_u32 m0, %[wm], 0xc000\n\t" \
    "s_mov_b32 s44, 0x600000\n\t" \
    "buffer_load_dwordx4 v240, %[wr], s44 offen offset:0 nt lds\n\t" \
    "buffer_load_dwordx4 v241, %[wr], s44 offen offset:1024 nt lds\n\t" \
    "buffer_load_dwordx4 v242, %[wr], s44 offen offset:2048 nt lds\n\t" \
    "ds_read_b64_tr_b16 v[228:229], v244 offset:0\n\t" \
    "ds_read_b64_tr_b16 v[230:231], v244 offset:1536\n\t" \
    "ds_read_b64_tr_b16 v[124:125], v244 offset:32\n\t" \
    "ds_read_b64_tr_b16 v[126:127], v244 offset:1568\n\t" \
    "ds_read_b64_tr_b16 v[132:133], v244 offset:64\n\t" \
    "ds_read_b64_tr_b16 v[134:135], v244 offset:1600\n\t" \
    "v_add_u32 v244, 0x18000, v243\n\t" \
    "s_waitcnt vmcnt(12) lgkmcnt(0)\n\t" \
    "s_barrier \n\t" \
    "s_add_u32 m0, %[wm], 0x12000\n\t" \
    "s_mov_b32 s44, 0x660000\n\t" \
    "buffer_load_dwordx4 v240, %[wr], s44 offen offset:0 nt lds\n\t" \
    "buffer_load_dwordx4 v241, %[wr], s44 offen offset:1024 nt lds\n\t" \
    "buffer_load_dwordx4 v242, %[wr], s44 offen offset:2048 nt lds\n\t" \
    "ds_read_b64_tr_b16 v[136:137], v244 offset:0\n\t" \
    "ds_read_b64_tr_b16 v[138:139], v244 offset:1536\n\t" \
    "ds_read_b64_tr_b16 v[144:145], v244 offset:32\n\t" \
    "ds_read_b64_tr_b16 v[146:147], v244 offset:1568\n\t" \
    "ds_read_b64_tr_b16 v[152:153], v244 offset:64\n\t" \
    "ds_read_b64_tr_b16 v[154:155], v244 offset:1600\n\t" \
    "v_add_u32 v244, 0x1e000, v243\n\t" \
    "s_waitcnt vmcnt(12) lgkmcnt(0)\n\t" \
    "s_barrier \n\t" \
    ".Ls21_%=:\n\t" \
    "v_readfirstlane_b32 s43, v246\n\t" \
    "s_nop 0\n\t" \
    "s_cmp_lg_u32 s43, -1\n\t" \
    "s_cbranch_scc1 .Ls22_%=\n\t" \
    "s_cmp_eq_u32 s41, 0\n\t" \
    "s_cbranch_scc1 .Ls22_%=\n\t" \
    "s_sub_u32 s41, s41, 1\n\t" \
    "s_sleep 4\n\t" \
    "v_mov_b32 v246, 0\n\t" \
    "buffer_load_dword v246, v246, s[60:63], %[mo] offen sc1\n\t" \
    "s_waitcnt vmcnt(0)\n\t" \
    "s_branch .Ls21_%=\n\t" \
    ".Ls22_%=:\n\t" \
    "v_mov_b32 v246, 0x8000\n\t" \
    "s_mov_b64 exec, 1\n\t" \
    "buffer_store_dword v246, v246, s[60:63], %[mo] offen sc1\n\t" \
    "s_mov_b64 exec, -1\n\t" \
    "s_and_b32 s43, s43, 0x2000\n\t" \
    "s_xor_b32 s43, s43, %[lx]\n\t" \
    "s_add_u32 s60, s60, s43\n\t" \
    "s_addc_u32 s61, s61, 0\n\t" \
    "s_add_u32 m0, %[wm], 0x18000\n\t" \
    "s_mov_b32 s44, 0x6c0000\n\t" \
    "buffer_load_dwordx4 v240, %[wr], s44 offen offset:0 nt lds\n\t" \
    "buffer_load_dwordx4 v241, %[wr], s44 offen offset:1024 nt lds\n\t" \
    "buffer_load_dwordx4 v242, %[wr], s44 offen offset:2048 nt lds\n\t" \
    "ds_read_b64_tr_b16 v[140:141], v244 offset:0\n\t" \
    "ds_read_b64_tr_b16 v[142:143], v244 offset:1536\n\t" \
    "ds_read_b64_tr_b16 v[148:149], v244 offset:32\n\t" \
    "ds_read_b64_tr_b16 v[150:151], v244 offset:1568\n\t" \
    "ds_read_b64_tr_b16 v[156:157], v244 offset:64\n\t" \
    "ds_read_b64_tr_b16 v[158:159], v244 offset:1600\n\t" \
    "v_add_u32 v244, 0x0, v243\n\t" \
    "s_waitcnt vmcnt(12) lgkmcnt(0)\n\t" \
    "s_barrier \n\t" \
    "s_add_u32 m0, %[wm], 0x1e000\n\t" \
    "s_mov_b32 s44, 0x720000\n\t" \
    "buffer_load_dwordx4 v240, %[wr], s44 offen offset:0 nt lds\n\t" \
    "buffer_load_dwordx4 v241, %[wr], s44 offen offset:1024 nt lds\n\t" \
    "buffer_load_dwordx4 v242, %[wr], s44 offen offset:2048 nt lds\n\t" \
    "ds_read_b64_tr_b16 v[16:17], v244 offset:0\n\t" \
    "ds_read_b64_tr_b16 v[18:19], v244 offset:1536\n\t" \
    "ds_read_b64_tr_b16 v[24:25], v244 offset:32\n\t" \
    "ds_read_b64_tr_b16 v[26:27], v244 offset:1568\n\t" \
    "ds_read_b64_tr_b16 v[32:33], v244 offset:64\n\t" \
    "ds_read_b64_tr_b16 v[34:35], v244 offset:1600\n\t" \
    "v_add_u32 v244, 0x6000, v243\n\t" \
    "s_waitcnt vmcnt(12) lgkmcnt(0)\n\t" \
    "s_barrier \n\t" \
    "ds_read_b64_tr_b16 v[20:21], v244 offset:0\n\t" \
    "ds_read_b64_tr_b16 v[22:23], v244 offset:1536\n\t" \
    "ds_read_b64_tr_b16 v[28:29], v244 offset:32\n\t" \
    "ds_read_b64_tr_b16 v[30:31], v244 offset:1568\n\t" \
    "ds_read_b64_tr_b16 v[36:37], v244 offset:64\n\t" \
    "ds_read_b64_tr_b16 v[38:39], v244 offset:1600\n\t" \
    "v_add_u32 v244, 0xc000, v243\n\t" \
    "s_waitcnt vmcnt(9) lgkmcnt(0)\n\t" \
    "s_barrier \n\t" \
    "ds_read_b64_tr_b16 v[232:233], v244 offset:0\n\t" \
    "ds_read_b64_tr_b16 v[234:235], v244 offset:1536\n\t" \
    "ds_read_b64_tr_b16 v[160:161], v244 offset:32\n\t" \
    "ds_read_b64_tr_b16 v[162:163], v244 offset:1568\n\t" \
    "ds_read_b64_tr_b16 v[168:169], v244 offset:64\n\t" \
    "ds_read_b64_tr_b16 v[170:171], v244 offset:1600\n\t" \
    "v_add_u32 v244, 0x12000, v243\n\t" \
    "s_waitcnt vmcnt(6) lgkmcnt(0)\n\t" \
    "s_barrier \n\t" \
    "ds_read_b64_tr_b16 v[236:237], v244 offset:0\n\t" \
    "ds_read_b64_tr_b16 v[238:239], v244 offset:1536\n\t" \
    "ds_read_b64_tr_b16 v[164:165], v244 offset:32\n\t" \
    "ds_read_b64_tr_b16 v[166:167], v244 offset:1568\n\t" \
    "ds_read_b64_tr_b16 v[172:173], v244 offset:64\n\t" \
    "ds_read_b64_tr_b16 v[174:175], v244 offset:1600\n\t" \
    "v_add_u32 v244, 0x18000, v243\n\t" \
    "s_waitcnt vmcnt(3) lgkmcnt(0)\n\t" \
    "s_barrier \n\t" \
    "ds_read_b64_tr_b16 v[176:177], v244 offset:0\n\t" \
    "ds_read_b64_tr_b16 v[178:179], v244 offset:1536\n\t" \
    "ds_read_b64_tr_b16 v[184:185], v244 offset:32\n\t" \
    "ds_read_b64_tr_b16 v[186:187], v244 offset:1568\n\t" \
    "ds_read_b64_tr_b16 v[192:193], v244 offset:64\n\t" \
    "ds_read_b64_tr_b16 v[194:195], v244 offset:1600\n\t" \
    "v_add_u32 v244, 0x1e000, v243\n\t" \
    "s_waitcnt vmcnt(0) lgkmcnt(0)\n\t" \
    "s_barrier \n\t" \
    "ds_read_b64_tr_b16 v[180:181], v244 offset:0\n\t" \
    "ds_read_b64_tr_b16 v[182:183], v244 offset:1536\n\t" \
    "ds_read_b64_tr_b16 v[188:189], v244 offset:32\n\t" \
    "ds_read_b64_tr_b16 v[190:191], v244 offset:1568\n\t" \
    "ds_read_b64_tr_b16 v[196:197], v244 offset:64\n\t" \
    "ds_read_b64_tr_b16 v[198:199], v244 offset:1600\n\t" \
    "s_waitcnt lgkmcnt(0)\n\t" \
    "s_barrier \n\t" \
    LST0 \
    LJIT \
    "ds_write_b128 v254, v[200:203] offset:51200\n\t" \
    "ds_write_b128 v254, v[204:207] offset:52224\n\t" \
    "ds_write_b128 v254, v[208:211] offset:53248\n\t" \
    "ds_write_b128 v254, v[212:215] offset:54272\n\t" \
    "ds_write_b128 v254, v[216:219] offset:55296\n\t" \
    "ds_write_b128 v254, v[220:223] offset:56320\n\t" \
    "ds_write_b128 v254, v[224:227] offset:57344\n\t" \
    "ds_write_b128 v254, v[228:231] offset:58368\n\t" \
    "ds_write_b128 v254, v[232:235] offset:59392\n\t" \
    "ds_write_b128 v254, v[236:239] offset:60416\n\t" \
    "s_waitcnt vmcnt(0) lgkmcnt(0)\n\t" \
    "s_barrier \n\t" \
    "ds_read_b128 v[224:227], v253 offset:0\n\t" \
    "ds_read_b128 v[244:247], v254 offset:53248\n\t" \
    "ds_read_b128 v[228:231], v253 offset:1024\n\t" \
    "ds_read_b128 v[232:235], v253 offset:20480\n\t" \
    "ds_read_b128 v[236:239], v253 offset:21504\n\t" \
    "ds_read_b128 v[248:251], v254 offset:54272\n\t" \
    ".Ls23_%=:\n\t" \
    "s_waitcnt lgkmcnt(5)\n\t" \
    "v_mfma_f32_16x16x32_bf16 v[204:207], v[40:43], v[224:227], 0\n\t" \
    "s_waitcnt lgkmcnt(3)\n\t" \
    "v_mfma_f32_16x16x32_bf16 v[204:207], v[44:47], v[228:231], v[204:207]\n\t" \
    "ds_read_b128 v[240:243], v253 offset:2048\n\t" \
    "v_mfma_f32_16x16x32_bf16 v[208:211], v[48:51], v[224:227], 0\n\t" \
    "v_mfma_f32_16x16x32_bf16 v[208:211], v[52:55], v[228:231], v[208:211]\n\t" \
    "v_mfma_f32_16x16x32_bf16 v[200:203], v[244:247], v[224:227], 0\n\t" \
    "s_waitcnt lgkmcnt(1)\n\t" \
    "v_mfma_f32_16x16x32_bf16 v[200:203], v[248:251], v[228:231], v[200:203]\n\t" \
    "v_mfma_f32_16x16x32_bf16 v[216:219], v[40:43], v[232:235], 0\n\t" \
    "ds_read_b128 v[224:227], v253 offset:3072\n\t" \
    "v_mfma_f32_16x16x32_bf16 v[216:219], v[44:47], v[236:239], v[216:219]\n\t" \
    "ds_read_b128 v[228:231], v253 offset:22528\n\t" \
    "v_mfma_f32_16x16x32_bf16 v[220:223], v[48:51], v[232:235], 0\n\t" \
    "v_mfma_f32_16x16x32_bf16 v[220:223], v[52:55], v[236:239], v[220:223]\n\t" \
    "v_mfma_f32_16x16x32_bf16 v[212:215], v[244:247], v[232:235], 0\n\t" \
    "ds_read_b128 v[244:247], v254 offset:55296\n\t" \
    "v_mfma_f32_16x16x32_bf16 v[212:215], v[248:251], v[236:239], v[212:215]\n\t" \
    "ds_read_b128 v[248:251], v254 offset:56320\n\t" \
    "s_waitcnt lgkmcnt(4)\n\t" \
    "v_mfma_f32_16x16x32_bf16 v[204:207], v[64:67], v[240:243], v[204:207]\n\t" \
    "ds_read_b128 v[232:235], v253 offset:23552\n\t" \
    "s_waitcnt lgkmcnt(4)\n\t" \
    "v_mfma_f32_16x16x32_bf16 v[204:207], v[68:71], v[224:227], v[204:207]\n\t" \
    "ds_read_b128 v[236:239], v253 offset:4096\n\t" \
    "v_mfma_f32_16x16x32_bf16 v[208:211], v[72:75], v[240:243], v[208:211]\n\t" \
    "v_mfma_f32_16x16x32_bf16 v[208:211], v[76:79], v[224:227], v[208:211]\n\t" \
    "v_mfma_f32_16x16x32_bf16 v[200:203], v[56:59], v[240:243], v[200:203]\n\t" \
    "v_mfma_f32_16x16x32_bf16 v[200:203], v[60:63], v[224:227], v[200:203]\n\t" \
    "s_waitcnt lgkmcnt(4)\n\t" \
    "v_mfma_f32_16x16x32_bf16 v[216:219], v[64:67], v[228:231], v[216:219]\n\t" \
    "ds_read_b128 v[240:243], v253 offset:5120\n\t" \
    "s_waitcnt lgkmcnt(2)\n\t" \
    "v_mfma_f32_16x16x32_bf16 v[216:219], v[68:71], v[232:235], v[216:219]\n\t" \
    "ds_read_b128 v[224:227], v253 offset:24576\n\t" \
    "v_mfma_f32_16x16x32_bf16 v[220:223], v[72:75], v[228:231], v[220:223]\n\t" \
    "v_mfma_f32_16x16x32_bf16 v[220:223], v[76:79], v[232:235], v[220:223]\n\t" \
    "v_mfma_f32_16x16x32_bf16 v[212:215], v[56:59], v[228:231], v[212:215]\n\t" \
    "v_mfma_f32_16x16x32_bf16 v[212:215], v[60:63], v[232:235], v[212:215]\n\t" \
    "s_waitcnt lgkmcnt(2)\n\t" \
    "v_mfma_f32_16x16x32_bf16 v[204:207], v[80:83], v[236:239], v[204:207]\n\t" \
    "ds_read_b128 v[228:231], v253 offset:25600\n\t" \
    "s_waitcnt lgkmcnt(2)\n\t" \
    "v_mfma_f32_16x16x32_bf16 v[204:207], v[84:87], v[240:243], v[204:207]\n\t" \
    "ds_read_b128 v[232:235], v253 offset:6144\n\t" \
    "v_mfma_f32_16x16x32_bf16 v[208:211], v[88:91], v[236:239], v[208:211]\n\t" \
    "v_mfma_f32_16x16x32_bf16 v[208:211], v[92:95], v[240:243], v[208:211]\n\t" \
    "v_mfma_f32_16x16x32_bf16 v[200:203], v[244:247], v[236:239], v[200:203]\n\t" \
    "v_mfma_f32_16x16x32_bf16 v[200:203], v[248:251], v[240:243], v[200:203]\n\t" \
    "s_waitcnt lgkmcnt(2)\n\t" \
    "v_mfma_f32_16x16x32_bf16 v[216:219], v[80:83], v[224:227], v[216:219]\n\t" \
    "ds_read_b128 v[236:239], v253 offset:7168\n\t" \
    "s_waitcnt lgkmcnt(2)\n\t" \
    "v_mfma_f32_16x16x32_bf16 v[216:219], v[84:87], v[228:231], v[216:219]\n\t" \
    "ds_read_b128 v[240:243], v253 offset:26624\n\t" \
    "v_mfma_f32_16x16x32_bf16 v[220:223], v[88:91], v[224:227], v[220:223]\n\t" \
    "v_mfma_f32_16x16x32_bf16 v[220:223], v[92:95], v[228:231], v[220:223]\n\t" \
    "v_mfma_f32_16x16x32_bf16 v[212:215], v[244:247], v[224:227], v[212:215]\n\t" \
    "ds_read_b128 v[244:247], v254 offset:51200\n\t" \
    "v_mfma_f32_16x16x32_bf16 v[212:215], v[248:251], v[228:231], v[212:215]\n\t" \
    "ds_read_b128 v[248:251], v254 offset:52224\n\t" \
    "s_waitcnt lgkmcnt(4)\n\t" \
    "v_mfma_f32_16x16x32_bf16 v[204:207], v[104:107], v[232:235], v[204:207]\n\t" \
    "ds_read_b128 v[224:227], v253 offset:27648\n\t" \
    "s_waitcnt lgkmcnt(4)\n\t" \
    "v_mfma_f32_16x16x32_bf16 v[204:207], v[108:111], v[236:239], v[204:207]\n\t" \
    "ds_read_b128 v[228:231], v253 offset:8192\n\t" \
    "v_mfma_f32_16x16x32_bf16 v[208:211], v[112:115], v[232:235], v[208:211]\n\t" \
    "v_mfma_f32_16x16x32_bf16 v[208:211], v[116:119], v[236:239], v[208:211]\n\t" \
    "v_mfma_f32_16x16x32_bf16 v[200:203], v[96:99], v[232:235], v[200:203]\n\t" \
    "v_mfma_f32_16x16x32_bf16 v[200:203], v[100:103], v[236:239], v[200:203]\n\t" \
    "s_waitcnt lgkmcnt(4)\n\t" \
    "v_mfma_f32_16x16x32_bf16 v[216:219], v[104:107], v[240:243], v[216:219]\n\t" \
    "ds_read_b128 v[232:235], v253 offset:9216\n\t" \
    "s_waitcnt lgkmcnt(2)\n\t" \
    "v_mfma_f32_16x16x32_bf16 v[216:219], v[108:111], v[224:227], v[216:219]\n\t" \
    "ds_read_b128 v[236:239], v253 offset:28672\n\t" \
    "v_mfma_f32_16x16x32_bf16 v[220:223], v[112:115], v[240:243], v[220:223]\n\t" \
    "v_mfma_f32_16x16x32_bf16 v[220:223], v[116:119], v[224:227], v[220:223]\n\t" \
    "v_mfma_f32_16x16x32_bf16 v[212:215], v[96:99], v[240:243], v[212:215]\n\t" \
    "v_mfma_f32_16x16x32_bf16 v[212:215], v[100:103], v[224:227], v[212:215]\n\t" \
    "s_waitcnt lgkmcnt(2)\n\t" \
    "v_mfma_f32_16x16x32_bf16 v[204:207], v[0:3], v[228:231], v[204:207]\n\t" \
    "ds_read_b128 v[240:243], v253 offset:29696\n\t" \
    "s_waitcnt lgkmcnt(2)\n\t" \
    "v_mfma_f32_16x16x32_bf16 v[204:207], v[4:7], v[232:235], v[204:207]\n\t" \
    "ds_read_b128 v[224:227], v253 offset:10240\n\t" \
    "v_mfma_f32_16x16x32_bf16 v[208:211], v[8:11], v[228:231], v[208:211]\n\t" \
    "v_mfma_f32_16x16x32_bf16 v[208:211], v[12:15], v[232:235], v[208:211]\n\t" \
    "v_mfma_f32_16x16x32_bf16 v[200:203], v[244:247], v[228:231], v[200:203]\n\t" \
    "v_mfma_f32_16x16x32_bf16 v[200:203], v[248:251], v[232:235], v[200:203]\n\t" \
    "s_waitcnt lgkmcnt(2)\n\t" \
    "v_mfma_f32_16x16x32_bf16 v[216:219], v[0:3], v[236:239], v[216:219]\n\t" \
    "ds_read_b128 v[228:231], v253 offset:11264\n\t" \
    "s_waitcnt lgkmcnt(2)\n\t" \
    "v_mfma_f32_16x16x32_bf16 v[216:219], v[4:7], v[240:243], v[216:219]\n\t" \
    "ds_read_b128 v[232:235], v253 offset:30720\n\t" \
    "v_mfma_f32_16x16x32_bf16 v[220:223], v[8:11], v[236:239], v[220:223]\n\t" \
    "v_mfma_f32_16x16x32_bf16 v[220:223], v[12:15], v[240:243], v[220:223]\n\t" \
    "v_mfma_f32_16x16x32_bf16 v[212:215], v[244:247], v[236:239], v[212:215]\n\t" \
    "ds_read_b128 v[244:247], v254 offset:57344\n\t" \
    "v_mfma_f32_16x16x32_bf16 v[212:215], v[248:251], v[240:243], v[212:215]\n\t" \
    "ds_read_b128 v[248:251], v254 offset:58368\n\t" \
    "ds_read_b128 v[236:239], v253 offset:31744\n\t" \
    "s_setprio 2\n\t" \
    "s_waitcnt lgkmcnt(0)\n\t" \
    "s_barrier \n\t" \
    LST7 \
    LJIT \
    "v_mfma_f32_16x16x32_bf16 v[204:207], v[120:123], v[224:227], v[204:207]\n\t" \
    "v_mfma_f32_16x16x32_bf16 v[204:207], v[124:127], v[228:231], v[204:207]\n\t" \
    "ds_read_b128 v[240:243], v253 offset:12288\n\t" \
    "v_mfma_f32_16x16x32_bf16 v[208:211], v[128:131], v[224:227], v[208:211]\n\t" \
    "v_mfma_f32_16x16x32_bf16 v[208:211], v[132:135], v[228:231], v[208:211]\n\t" \
    "v_mfma_f32_16x16x32_bf16 v[200:203], v[244:247], v[224:227], v[200:203]\n\t" \
    "v_mfma_f32_16x16x32_bf16 v[200:203], v[248:251], v[228:231], v[200:203]\n\t" \
    "v_mfma_f32_16x16x32_bf16 v[216:219], v[120:123], v[232:235], v[216:219]\n\t" \
    "ds_read_b128 v[224:227], v253 offset:13312\n\t" \
    "s_and_b32 s44, s40, 3\n\t" \
    "v_mfma_f32_16x16x32_bf16 v[216:219], v[124:127], v[236:239], v[216:219]\n\t" \
    "ds_read_b128 v[228:231], v253 offset:32768\n\t" \
    "s_lshl_b32 s44, s44, 17\n\t" \
    "v_mfma_f32_16x16x32_bf16 v[220:223], v[128:131], v[232:235], v[220:223]\n\t" \
    "s_add_u32 s44, s44, %[mb]\n\t" \
    "v_mfma_f32_16x16x32_bf16 v[220:223], v[132:135], v[236:239], v[220:223]\n\t" \
    "s_add_u32 s46, s44, 0x1000\n\t" \
    "v_mfma_f32_16x16x32_bf16 v[212:215], v[244:247], v[232:235], v[212:215]\n\t" \
    "ds_read_b128 v[244:247], v254 offset:59392\n\t" \
    "s_add_u32 s47, s44, 0x1c00\n\t" \
    "v_mfma_f32_16x16x32_bf16 v[212:215], v[248:251], v[236:239], v[212:215]\n\t" \
    "ds_read_b128 v[248:251], v254 offset:60416\n\t" \
    "s_waitcnt lgkmcnt(4)\n\t" \
    "v_mfma_f32_16x16x32_bf16 v[204:207], v[144:147], v[240:243], v[204:207]\n\t" \
    "ds_read_b128 v[232:235], v253 offset:33792\n\t" \
    "s_waitcnt lgkmcnt(4)\n\t" \
    "v_mfma_f32_16x16x32_bf16 v[204:207], v[148:151], v[224:227], v[204:207]\n\t" \
    "ds_read_b128 v[236:239], v253 offset:16384\n\t" \
    "v_mfma_f32_16x16x32_bf16 v[208:211], v[152:155], v[240:243], v[208:211]\n\t" \
    "v_mfma_f32_16x16x32_bf16 v[208:211], v[156:159], v[224:227], v[208:211]\n\t" \
    "v_mfma_f32_16x16x32_bf16 v[200:203], v[136:139], v[240:243], v[200:203]\n\t" \
    "v_mfma_f32_16x16x32_bf16 v[200:203], v[140:143], v[224:227], v[200:203]\n\t" \
    "s_waitcnt lgkmcnt(4)\n\t" \
    "v_mfma_f32_16x16x32_bf16 v[216:219], v[144:147], v[228:231], v[216:219]\n\t" \
    "ds_read_b128 v[240:243], v253 offset:17408\n\t" \
    "s_waitcnt lgkmcnt(2)\n\t" \
    "v_mfma_f32_16x16x32_bf16 v[216:219], v[148:151], v[232:235], v[216:219]\n\t" \
    "ds_read_b128 v[224:227], v253 offset:36864\n\t" \
    "v_mfma_f32_16x16x32_bf16 v[220:223], v[152:155], v[228:231], v[220:223]\n\t" \
    "v_mfma_f32_16x16x32_bf16 v[220:223], v[156:159], v[232:235], v[220:223]\n\t" \
    "v_mfma_f32_16x16x32_bf16 v[212:215], v[136:139], v[228:231], v[212:215]\n\t" \
    "v_mfma_f32_16x16x32_bf16 v[212:215], v[140:143], v[232:235], v[212:215]\n\t" \
    "s_waitcnt lgkmcnt(2)\n\t" \
    "v_mfma_f32_16x16x32_bf16 v[204:207], v[160:163], v[236:239], v[204:207]\n\t" \
    "ds_read_b128 v[228:231], v253 offset:37888\n\t" \
    "s_waitcnt lgkmcnt(2)\n\t" \
    "v_mfma_f32_16x16x32_bf16 v[204:207], v[164:167], v[240:243], v[204:207]\n\t" \
    "ds_read_b128 v[232:235], v253 offset:14336\n\t" \
    "v_mfma_f32_16x16x32_bf16 v[208:211], v[168:171], v[236:239], v[208:211]\n\t" \
    "v_mfma_f32_16x16x32_bf16 v[208:211], v[172:175], v[240:243], v[208:211]\n\t" \
    "v_mfma_f32_16x16x32_bf16 v[200:203], v[244:247], v[236:239], v[200:203]\n\t" \
    "v_mfma_f32_16x16x32_bf16 v[200:203], v[248:251], v[240:243], v[200:203]\n\t" \
    "s_waitcnt lgkmcnt(2)\n\t" \
    "v_mfma_f32_16x16x32_bf16 v[216:219], v[160:163], v[224:227], v[216:219]\n\t" \
    "ds_read_b128 v[236:239], v253 offset:15360\n\t" \
    "s_waitcnt lgkmcnt(2)\n\t" \
    "v_mfma_f32_16x16x32_bf16 v[216:219], v[164:167], v[228:231], v[216:219]\n\t" \
    "ds_read_b128 v[240:243], v253 offset:18432\n\t" \
    "v_mfma_f32_16x16x32_bf16 v[220:223], v[168:171], v[224:227], v[220:223]\n\t" \
    "v_mfma_f32_16x16x32_bf16 v[220:223], v[172:175], v[228:231], v[220:223]\n\t" \
    "v_mfma_f32_16x16x32_bf16 v[212:215], v[244:247], v[224:227], v[212:215]\n\t" \
    "v_mfma_f32_16x16x32_bf16 v[212:215], v[248:251], v[228:231], v[212:215]\n\t" \
    "s_waitcnt lgkmcnt(2)\n\t" \
    "v_mfma_f32_16x16x32_bf16 v[204:207], v[24:27], v[232:235], v[204:207]\n\t" \
    "ds_read_b128 v[224:227], v253 offset:19456\n\t" \
    "v_mov_b64 v[244:245], 0\n\t" \
    "s_waitcnt lgkmcnt(2)\n\t" \
    "v_mfma_f32_16x16x32_bf16 v[204:207], v[28:31], v[236:239], v[204:207]\n\t" \
    "ds_read_b128 v[228:231], v253 offset:34816\n\t" \
    "v_mov_b64 v[246:247], 0\n\t" \
    "v_mfma_f32_16x16x32_bf16 v[208:211], v[32:35], v[232:235], v[208:211]\n\t" \
    "v_mov_b64 v[248:249], 0\n\t" \
    "v_mfma_f32_16x16x32_bf16 v[208:211], v[36:39], v[236:239], v[208:211]\n\t" \
    "v_mov_b64 v[250:251], 0\n\t" \
    "v_mfma_f32_16x16x32_bf16 v[200:203], v[16:19], v[232:235], v[200:203]\n\t" \
    "v_mfma_f32_16x16x32_bf16 v[200:203], v[20:23], v[236:239], v[200:203]\n\t" \
    "s_waitcnt lgkmcnt(2)\n\t" \
    "v_mfma_f32_16x16x32_bf16 v[204:207], v[184:187], v[240:243], v[204:207]\n\t" \
    "ds_read_b128 v[232:235], v253 offset:35840\n\t" \
    "s_waitcnt lgkmcnt(2)\n\t" \
    "v_mfma_f32_16x16x32_bf16 v[204:207], v[188:191], v[224:227], v[204:207]\n\t" \
    "ds_read_b128 v[236:239], v253 offset:38912\n\t" \
    "v_mfma_f32_16x16x32_bf16 v[208:211], v[192:195], v[240:243], v[208:211]\n\t" \
    "v_mfma_f32_16x16x32_bf16 v[208:211], v[196:199], v[224:227], v[208:211]\n\t" \
    "v_mfma_f32_16x16x32_bf16 v[200:203], v[176:179], v[240:243], v[200:203]\n\t" \
    "v_mfma_f32_16x16x32_bf16 v[200:203], v[180:183], v[224:227], v[200:203]\n\t" \
    "ds_read_b128 v[240:243], v253 offset:39936\n\t" \
    "s_waitcnt lgkmcnt(3)\n\t" \
    "v_mfma_f32_16x16x32_bf16 v[216:219], v[24:27], v[228:231], v[216:219]\n\t" \
    "s_waitcnt lgkmcnt(2)\n\t" \
    "v_mfma_f32_16x16x32_bf16 v[216:219], v[28:31], v[232:235], v[216:219]\n\t" \
    "v_cvt_pk_bf16_f32 v204, v204, v208\n\t" \
    "v_cvt_pk_bf16_f32 v205, v205, v209\n\t" \
    "s_waitcnt lgkmcnt(1)\n\t" \
    "v_mfma_f32_16x16x32_bf16 v[216:219], v[184:187], v[236:239], v[216:219]\n\t" \
    "v_cvt_pk_bf16_f32 v206, v206, v210\n\t" \
    "v_cvt_pk_bf16_f32 v207, v207, v211\n\t" \
    "s_waitcnt lgkmcnt(0)\n\t" \
    "v_mfma_f32_16x16x32_bf16 v[216:219], v[188:191], v[240:243], v[216:219]\n\t" \
    "ds_write_b128 v254, v[204:207] offset:10240\n\t" \
    "v_mfma_f32_16x16x32_bf16 v[220:223], v[32:35], v[228:231], v[220:223]\n\t" \
    "v_mfma_f32_16x16x32_bf16 v[220:223], v[36:39], v[232:235], v[220:223]\n\t" \
    "v_cvt_pk_bf16_f32 v200, v200, v201\n\t" \
    "v_cvt_pk_bf16_f32 v201, v202, v203\n\t" \
    "v_mfma_f32_16x16x32_bf16 v[220:223], v[192:195], v[236:239], v[220:223]\n\t" \
    "ds_write_b64 v254, v[200:201] offset:6152\n\t" \
    "v_mfma_f32_16x16x32_bf16 v[220:223], v[196:199], v[240:243], v[220:223]\n\t" \
    "v_mfma_f32_16x16x32_bf16 v[212:215], v[16:19], v[228:231], v[212:215]\n\t" \
    "ds_read_b128 v[200:203], v254 offset:51200\n\t" \
    "ds_read_b128 v[204:207], v254 offset:52224\n\t" \
    "v_mfma_f32_16x16x32_bf16 v[212:215], v[20:23], v[232:235], v[212:215]\n\t" \
    "v_mov_b64 v[208:209], 0\n\t" \
    "v_mov_b64 v[210:211], 0\n\t" \
    "v_mfma_f32_16x16x32_bf16 v[212:215], v[176:179], v[236:239], v[212:215]\n\t" \
    "v_cvt_pk_bf16_f32 v216, v216, v220\n\t" \
    "v_cvt_pk_bf16_f32 v217, v217, v221\n\t" \
    "v_mfma_f32_16x16x32_bf16 v[212:215], v[180:183], v[240:243], v[212:215]\n\t" \
    "v_cvt_pk_bf16_f32 v218, v218, v222\n\t" \
    "v_cvt_pk_bf16_f32 v219, v219, v223\n\t" \
    "s_setprio 0\n\t" \
    "ds_write_b128 v254, v[216:219] offset:16384\n\t" \
    "ds_read_b128 v[216:219], v254 offset:53248\n\t" \
    "ds_read_b128 v[220:223], v254 offset:54272\n\t" \
    "s_nop 2\n\t" \
    "v_cvt_pk_bf16_f32 v212, v212, v213\n\t" \
    "v_cvt_pk_bf16_f32 v213, v214, v215\n\t" \
    "ds_write_b64 v254, v[212:213] offset:12296\n\t" \
    LST6 \
    LJIT \
    "s_sleep 3\n\t" \
    "s_mov_b32 m0, 0xb000\n\t" \
    "s_nop 0\n\t" \
    "buffer_load_dwordx4 v253, s[60:63], s46 offen sc1 lds\n\t" \
    "s_mov_b32 m0, 0xbc00\n\t" \
    "s_nop 0\n\t" \
    "buffer_load_dwordx4 v253, s[60:63], s47 offen sc1 lds\n\t" \
    "s_waitcnt lgkmcnt(0)\n\t" \
    LFLUSH212 \
    "v_bfe_i32 v212, v253, 5, 1\n\t" \
    "v_and_b32 v212, 0xaaaa, v212\n\t" \
    "v_or_b32 v212, 0x4444, v212\n\t" \
    "v_and_b32 v213, 0xffff, v255\n\t" \
    "v_lshrrev_b32 v214, 16, v255\n\t" \
    ".Ls24_%=:\n\t" \
    "s_waitcnt vmcnt(0)\n\t" \
    "ds_read_b128 v[224:227], v253 offset:45056\n\t" \
    "ds_read_b128 v[228:231], v253 offset:48128\n\t" \
    "s_waitcnt lgkmcnt(1)\n\t" \
    "v_max_u32 v215, v224, v225\n\t" \
    "v_max3_u32 v215, v226, v227, v215\n\t" \
    "s_waitcnt lgkmcnt(0)\n\t" \
    "v_max3_u32 v215, v228, v229, v215\n\t" \
    "v_max3_u32 v215, v230, v231, v215\n\t" \
    "v_cmp_eq_u32 vcc, -1, v215\n\t" \
    "s_cbranch_vccz .Ls25_%=\n\t" \
    "v_max_u32 v215, v224, v225\n\t" \
    "v_max3_u32 v215, v226, v227, v215\n\t" \
    "v_cmp_eq_u32 vcc, -1, v215\n\t" \
    "s_cbranch_vccz .Ls26_%=\n\t" \
    "s_mov_b32 m0, 0xb000\n\t" \
    "s_nop 0\n\t" \
    "buffer_load_dwordx4 v253, s[60:63], s46 offen sc1 lds\n\t" \
    ".Ls26_%=:\n\t" \
    "v_max_u32 v215, v228, v229\n\t" \
    "v_max3_u32 v215, v230, v231, v215\n\t" \
    "v_cmp_eq_u32 vcc, -1, v215\n\t" \
    "s_cbranch_vccz .Ls27_%=\n\t" \
    "s_mov_b32 m0, 0xbc00\n\t" \
    "s_nop 0\n\t" \
    "buffer_load_dwordx4 v253, s[60:63], s47 offen sc1 lds\n\t" \
    ".Ls27_%=:\n\t" \
    "s_cmp_eq_u32 s41, 0\n\t" \
    "s_cbranch_scc1 .Ls25_%=\n\t" \
    "s_sub_u32 s41, s41, 1\n\t" \
    "s_branch .Ls24_%=\n\t" \
    ".Ls25_%=:\n\t" \
    LST2 \
    LJIT \
    "s_setprio 3\n\t" \
    "s_waitcnt lgkmcnt(0)\n\t" \
    "s_barrier \n\t" \
    LST3 \
    LJIT \
    "ds_read_b128 v[232:235], v253 offset:40960\n\t" \
    "ds_read_b128 v[236:239], v253 offset:41984\n\t" \
    "ds_read_b128 v[240:243], v253 offset:43008\n\t" \
    "v_smfmac_f32_16x16x64_bf16 v[244:247], v[224:227], v[0:7], v212\n\t" \
    "v_smfmac_f32_16x16x64_bf16 v[248:251], v[224:227], v[8:15], v212\n\t" \
    "v_smfmac_f32_16x16x64_bf16 v[208:211], v[224:227], v[200:207], v212\n\t" \
    "ds_read_b128 v[200:203], v254 offset:55296\n\t" \
    "ds_read_b128 v[204:207], v254 offset:56320\n\t" \
    "ds_read_b128 v[224:227], v253 offset:44032\n\t" \
    "v_smfmac_f32_16x16x64_bf16 v[208:211], v[228:231], v[16:23], v212\n\t" \
    "v_smfmac_f32_16x16x64_bf16 v[244:247], v[228:231], v[24:31], v212\n\t" \
    "v_smfmac_f32_16x16x64_bf16 v[248:251], v[228:231], v[32:39], v212\n\t" \
    "ds_read_b128 v[228:231], v253 offset:46080\n\t" \
    "s_waitcnt lgkmcnt(6)\n\t" \
    "v_smfmac_f32_16x16x64_bf16 v[208:211], v[232:235], v[216:223], v212\n\t" \
    "ds_read_b128 v[216:219], v254 offset:57344\n\t" \
    "ds_read_b128 v[220:223], v254 offset:58368\n\t" \
    "v_smfmac_f32_16x16x64_bf16 v[244:247], v[232:235], v[40:47], v212\n\t" \
    "v_smfmac_f32_16x16x64_bf16 v[248:251], v[232:235], v[48:55], v212\n\t" \
    "ds_read_b128 v[232:235], v253 offset:47104\n\t" \
    "s_waitcnt lgkmcnt(8)\n\t" \
    "v_smfmac_f32_16x16x64_bf16 v[208:211], v[236:239], v[56:63], v212\n\t" \
    "v_smfmac_f32_16x16x64_bf16 v[244:247], v[236:239], v[64:71], v212\n\t" \
    "v_smfmac_f32_16x16x64_bf16 v[248:251], v[236:239], v[72:79], v212\n\t" \
    "ds_read_b128 v[236:239], v253 offset:49152\n\t" \
    "s_waitcnt lgkmcnt(6)\n\t" \
    "v_smfmac_f32_16x16x64_bf16 v[208:211], v[240:243], v[200:207], v212\n\t" \
    "ds_read_b128 v[200:203], v254 offset:59392\n\t" \
    "ds_read_b128 v[204:207], v254 offset:60416\n\t" \
    "v_smfmac_f32_16x16x64_bf16 v[244:247], v[240:243], v[80:87], v212\n\t" \
    "v_smfmac_f32_16x16x64_bf16 v[248:251], v[240:243], v[88:95], v212\n\t" \
    "ds_read_b128 v[240:243], v253 offset:50176\n\t" \
    "s_waitcnt lgkmcnt(8)\n\t" \
    "v_smfmac_f32_16x16x64_bf16 v[208:211], v[224:227], v[96:103], v212\n\t" \
    "v_smfmac_f32_16x16x64_bf16 v[244:247], v[224:227], v[104:111], v212\n\t" \
    "v_smfmac_f32_16x16x64_bf16 v[248:251], v[224:227], v[112:119], v212\n\t" \
    "s_waitcnt lgkmcnt(5)\n\t" \
    "v_smfmac_f32_16x16x64_bf16 v[208:211], v[228:231], v[216:223], v212\n\t" \
    "s_waitcnt lgkmcnt(4)\n\t" \
    "v_smfmac_f32_16x16x64_bf16 v[208:211], v[232:235], v[136:143], v212\n\t" \
    "s_waitcnt lgkmcnt(1)\n\t" \
    "v_smfmac_f32_16x16x64_bf16 v[208:211], v[236:239], v[200:207], v212\n\t" \
    "s_waitcnt lgkmcnt(0)\n\t" \
    "v_smfmac_f32_16x16x64_bf16 v[208:211], v[240:243], v[176:183], v212\n\t" \
    "v_smfmac_f32_16x16x64_bf16 v[244:247], v[228:231], v[120:127], v212\n\t" \
    "v_smfmac_f32_16x16x64_bf16 v[244:247], v[232:235], v[144:151], v212\n\t" \
    "v_smfmac_f32_16x16x64_bf16 v[244:247], v[236:239], v[160:167], v212\n\t" \
    "v_add_f32 v208, v208, v210\n\t" \
    "v_add_f32 v209, v209, v211\n\t" \
    "v_cvt_pk_bf16_f32 v208, v208, v209\n\t" \
    "v_smfmac_f32_16x16x64_bf16 v[244:247], v[240:243], v[184:191], v212\n\t" \
    "ds_write_b16 v213, v208 offset:0\n\t" \
    "ds_write_b16_d16_hi v213, v208 offset:16\n\t" \
    "v_smfmac_f32_16x16x64_bf16 v[248:251], v[228:231], v[128:135], v212\n\t" \
    "v_smfmac_f32_16x16x64_bf16 v[248:251], v[232:235], v[152:159], v212\n\t" \
    "v_smfmac_f32_16x16x64_bf16 v[248:251], v[236:239], v[168:175], v212\n\t" \
    "v_add_f32 v244, v244, v246\n\t" \
    "v_add_f32 v245, v245, v247\n\t" \
    "v_smfmac_f32_16x16x64_bf16 v[248:251], v[240:243], v[192:199], v212\n\t" \
    "s_nop 7\n\t" \
    "v_add_f32 v248, v248, v250\n\t" \
    "v_add_f32 v249, v249, v251\n\t" \
    "v_cvt_pk_bf16_f32 v244, v244, v248\n\t" \
    "v_cvt_pk_bf16_f32 v245, v245, v249\n\t" \
    "ds_write2_b32 v214, v244, v245 offset1:4\n\t" \
    "s_setprio 0\n\t" \
    "s_sleep 1\n\t" \
    "ds_read_b128 v[200:203], v254 offset:12288\n\t" \
    "ds_read_b128 v[224:227], v254 offset:24576\n\t" \
    "ds_read_b128 v[204:207], v254 offset:13312\n\t" \
    "ds_read_b128 v[228:231], v254 offset:25600\n\t" \
    "ds_read_b128 v[208:211], v254 offset:14336\n\t" \
    "ds_read_b128 v[232:235], v254 offset:26624\n\t" \
    "ds_read_b128 v[212:215], v254 offset:15360\n\t" \
    "ds_read_b128 v[236:239], v254 offset:27648\n\t" \
    "ds_read_b128 v[216:219], v254 offset:16384\n\t" \
    "ds_read_b128 v[220:223], v254 offset:17408\n\t" \
    "s_and_b32 s44, s40, 3\n\t" \
    "s_lshl_b32 s44, s44, 17\n\t" \
    "s_add_u32 s50, s44, %[pb]\n\t" \
    "s_add_u32 s51, s44, %[pm]\n\t" \
    "s_mov_b32 s52, 0xf0000\n\t" \
    "s_mov_b32 s53, 0xfff00ff\n\t" \
    "s_mov_b32 s54, 0x1f0001\n\t" \
    "s_mov_b32 s55, 0x1fff01ff\n\t" \
    "s_mov_b32 s56, 0x3f0003\n\t" \
    "s_mov_b32 s57, 0x3fff03ff\n\t" \
    "s_mov_b32 s58, 0x7f0007\n\t" \
    "s_mov_b32 s59, 0x7fff07ff\n\t" \
    "s_waitcnt lgkmcnt(8)\n\t" \
    "v_mfma_f32_16x16x32_bf16 v[240:243], v[224:227], v[200:203], 0\n\t" \
    "ds_read_b128 v[224:227], v254 offset:28672\n\t" \
    "s_waitcnt lgkmcnt(7)\n\t" \
    "v_mfma_f32_16x16x32_bf16 v[240:243], v[228:231], v[204:207], v[240:243]\n\t" \
    "ds_read_b128 v[228:231], v254 offset:29696\n\t" \
    "s_waitcnt lgkmcnt(6)\n\t" \
    "v_mfma_f32_16x16x32_bf16 v[240:243], v[232:235], v[208:211], v[240:243]\n\t" \
    "ds_read_b128 v[232:235], v254 offset:18432\n\t" \
    "s_waitcnt lgkmcnt(5)\n\t" \
    "v_mfma_f32_16x16x32_bf16 v[240:243], v[236:239], v[212:215], v[240:243]\n\t" \
    "ds_read_b128 v[236:239], v254 offset:19456\n\t" \
    "v_and_b32 v252, 0xffffff7f, v254\n\t" \
    "s_waitcnt lgkmcnt(0)\n\t" \
    "s_barrier \n\t" \
    LST4 \
    LJIT \
    "v_mfma_f32_16x16x32_bf16 v[240:243], v[224:227], v[216:219], v[240:243]\n\t" \
    "ds_read_b128 v[224:227], v254 offset:20480\n\t" \
    "v_mfma_f32_16x16x32_bf16 v[240:243], v[228:231], v[220:223], v[240:243]\n\t" \
    "ds_read_b128 v[228:231], v254 offset:21504\n\t" \
    "v_mfma_f32_16x16x32_bf16 v[244:247], v[232:235], v[200:203], 0\n\t" \
    "ds_read_b128 v[232:235], v254 offset:22528\n\t" \
    "v_mfma_f32_16x16x32_bf16 v[244:247], v[236:239], v[204:207], v[244:247]\n\t" \
    "ds_read_b128 v[236:239], v254 offset:23552\n\t" \
    "s_waitcnt lgkmcnt(3)\n\t" \
    "v_mfma_f32_16x16x32_bf16 v[244:247], v[224:227], v[208:211], v[244:247]\n\t" \
    "ds_read_b128 v[224:227], v252 offset:3072\n\t" \
    "s_waitcnt lgkmcnt(3)\n\t" \
    "v_mfma_f32_16x16x32_bf16 v[244:247], v[228:231], v[212:215], v[244:247]\n\t" \
    "ds_read_b128 v[228:231], v252 offset:3200\n\t" \
    "s_waitcnt lgkmcnt(3)\n\t" \
    "v_mfma_f32_16x16x32_bf16 v[244:247], v[232:235], v[216:219], v[244:247]\n\t" \
    "ds_read_b128 v[232:235], v252 offset:4096\n\t" \
    "s_waitcnt lgkmcnt(3)\n\t" \
    "v_mfma_f32_16x16x32_bf16 v[244:247], v[236:239], v[220:223], v[244:247]\n\t" \
    "ds_read_b128 v[236:239], v252 offset:4224\n\t" \
    "s_waitcnt lgkmcnt(3)\n\t" \
    "v_mfma_f32_16x16x32_bf16 v[248:251], v[224:227], v[200:203], 0\n\t" \
    "ds_read_b128 v[224:227], v252 offset:5120\n\t" \
    "s_waitcnt lgkmcnt(3)\n\t" \
    "v_mfma_f32_16x16x32_bf16 v[248:251], v[228:231], v[204:207], v[248:251]\n\t" \
    "ds_read_b128 v[228:231], v252 offset:5248\n\t" \
    "s_waitcnt lgkmcnt(3)\n\t" \
    "v_mfma_f32_16x16x32_bf16 v[248:251], v[232:235], v[208:211], v[248:251]\n\t" \
    "s_waitcnt lgkmcnt(2)\n\t" \
    "v_mfma_f32_16x16x32_bf16 v[248:251], v[236:239], v[212:215], v[248:251]\n\t" \
    "s_waitcnt lgkmcnt(1)\n\t" \
    "v_mfma_f32_16x16x32_bf16 v[248:251], v[224:227], v[216:219], v[248:251]\n\t" \
    "s_waitcnt lgkmcnt(0)\n\t" \
    "v_mfma_f32_16x16x32_bf16 v[248:251], v[228:231], v[220:223], v[248:251]\n\t" \
    "v_mov_b32 v226, 0xff7fffff\n\t" \
    "v_cndmask_b32 v240, v240, v226, s[52:53]\n\t" \
    "v_cndmask_b32 v241, v241, v226, s[54:55]\n\t" \
    "v_cndmask_b32 v242, v242, v226, s[56:57]\n\t" \
    "v_cndmask_b32 v243, v243, v226, s[58:59]\n\t" \
    "v_max3_f32 v224, v240, v241, v242\n\t" \
    "v_max3_f32 v225, v244, v245, v246\n\t" \
    "v_max3_f32 v224, v224, v243, v247\n\t" \
    "v_lshrrev_b32 v200, 5, v253\n\t" \
    "v_and_b32 v201, 0xf0, v253\n\t" \
    "v_and_or_b32 v200, v200, 8, v201\n\t" \
    "v_max3_f32 v224, v224, v248, v249\n\t" \
    "v_max3_f32 v225, v225, v250, v251\n\t" \
    "v_max_f32 v224, v224, v225\n\t" \
    "v_mov_b32 v225, v224\n\t" \
    "s_nop 1\n\t" \
    "v_permlane16_swap_b32 v224, v225\n\t" \
    "v_max_f32 v224, v224, v225\n\t" \
    "v_mov_b32 v225, v224\n\t" \
    "s_nop 1\n\t" \
    "v_permlane32_swap_b32 v224, v225\n\t" \
    "v_max_f32 v224, v224, v225\n\t" \
    "v_mul_f32 v224, %[scl], v224\n\t" \
    "v_fma_f32 v240, v240, %[scl], -v224\n\t" \
    "v_fma_f32 v241, v241, %[scl], -v224\n\t" \
    "v_exp_f32 v240, v240\n\t" \
    "v_fma_f32 v242, v242, %[scl], -v224\n\t" \
    "v_exp_f32 v241, v241\n\t" \
    "v_fma_f32 v243, v243, %[scl], -v224\n\t" \
    "v_exp_f32 v242, v242\n\t" \
    "v_fma_f32 v244, v244, %[scl], -v224\n\t" \
    "v_exp_f32 v243, v243\n\t" \
    "v_fma_f32 v245, v245, %[scl], -v224\n\t" \
    "v_exp_f32 v244, v244\n\t" \
    "v_fma_f32 v246, v246, %[scl], -v224\n\t" \
    "v_exp_f32 v245, v245\n\t" \
    "v_fma_f32 v247, v247, %[scl], -v224\n\t" \
    "v_exp_f32 v246, v246\n\t" \
    "v_fma_f32 v248, v248, %[scl], -v224\n\t" \
    "v_exp_f32 v247, v247\n\t" \
    "v_fma_f32 v249, v249, %[scl], -v224\n\t" \
    "v_exp_f32 v248, v248\n\t" \
    "v_fma_f32 v250, v250, %[scl], -v224\n\t" \
    "v_exp_f32 v249, v249\n\t" \
    "v_fma_f32 v251, v251, %[scl], -v224\n\t" \
    "v_exp_f32 v250, v250\n\t" \
    "v_exp_f32 v251, v251\n\t" \
    "v_add_f32 v224, v240, v241\n\t" \
    "v_add_f32 v225, v242, v243\n\t" \
    "v_add_f32 v226, v244, v245\n\t" \
    "v_add_f32 v227, v246, v247\n\t" \
    "v_add_f32 v232, v248, v249\n\t" \
    "v_add_f32 v233, v250, v251\n\t" \
    "v_add_f32 v224, v224, v225\n\t" \
    "v_add_f32 v226, v226, v227\n\t" \
    "v_add_f32 v232, v232, v233\n\t" \
    "v_add_f32 v224, v224, v226\n\t" \
    "v_fma_f32 v224, v232, 0.5, v224\n\t" \
    "v_mov_b32 v225, v224\n\t" \
    "s_nop 1\n\t" \
    "v_permlane16_swap_b32 v224, v225\n\t" \
    "v_add_f32 v224, v224, v225\n\t" \
    "v_mov_b32 v225, v224\n\t" \
    "s_nop 1\n\t" \
    "v_permlane32_swap_b32 v224, v225\n\t" \
    "v_add_f32 v224, v224, v225\n\t" \
    "v_rcp_f32 v224, v224\n\t" \
    "s_nop 0\n\t" \
    "v_mul_f32 v240, v240, v224\n\t" \
    "v_mul_f32 v241, v241, v224\n\t" \
    "v_mul_f32 v242, v242, v224\n\t" \
    "v_mul_f32 v243, v243, v224\n\t" \
    "v_mul_f32 v244, v244, v224\n\t" \
    "v_mul_f32 v245, v245, v224\n\t" \
    "v_mul_f32 v246, v246, v224\n\t" \
    "v_mul_f32 v247, v247, v224\n\t" \
    "v_mul_f32 v248, v248, v224\n\t" \
    "v_mul_f32 v249, v249, v224\n\t" \
    "v_mul_f32 v250, v250, v224\n\t" \
    "v_mul_f32 v251, v251, v224\n\t" \
    "v_cvt_pk_bf16_f32 v204, |v244|, |v240|\n\t" \
    "v_cvt_pk_bf16_f32 v205, |v245|, |v241|\n\t" \
    "v_cvt_pk_bf16_f32 v206, |v246|, |v242|\n\t" \
    "v_cvt_pk_bf16_f32 v207, |v247|, |v243|\n\t" \
    "v_cvt_pk_bf16_f32 v208, |v248|, |v249|\n\t" \
    "v_cvt_pk_bf16_f32 v209, |v250|, |v251|\n\t" \
    "buffer_store_dwordx4 v[204:207], v253, s[60:63], s50 offen sc1\n\t" \
    "s_mov_b32 exec_lo, -1\n\t" \
    "s_mov_b32 exec_hi, 0\n\t" \
    "buffer_store_dwordx2 v[208:209], v200, s[60:63], s51 offen sc1\n\t" \
    "s_mov_b32 exec_hi, -1\n\t" \
    LST5 \
    LJIT \
    "ds_read_b128 v[224:227], v253 offset:0\n\t" \
    "ds_read_b128 v[244:247], v254 offset:53248\n\t" \
    "ds_read_b128 v[228:231], v253 offset:1024\n\t" \
    "ds_read_b128 v[232:235], v253 offset:20480\n\t" \
    "ds_read_b128 v[236:239], v253 offset:21504\n\t" \
    "ds_read_b128 v[248:251], v254 offset:54272\n\t" \
    "s_add_u32 s40, s40, 1\n\t" \
    "s_cmp_lg_u32 s40, 64\n\t" \
    "s_cbranch_scc1 .Ls23_%=\n\t" \
    "s_branch .Lend%=\n\t" \
    ".Ls1_%=:\n\t" \
    "v_add_u32 v244, 0x18000, v243\n\t" \
    "s_waitcnt vmcnt(12) lgkmcnt(0)\n\t" \
    "s_barrier \n\t" \
    "s_add_u32 m0, %[wm], 0x12000\n\t" \
    "s_mov_b32 s44, 0x1e0000\n\t" \
    "buffer_load_dwordx4 v240, %[wr], s44 offen offset:0 nt lds\n\t" \
    "buffer_load_dwordx4 v241, %[wr], s44 offen offset:1024 nt lds\n\t" \
    "buffer_load_dwordx4 v242, %[wr], s44 offen offset:2048 nt lds\n\t" \
    "ds_read_b64_tr_b16 v[56:57], v244 offset:0\n\t" \
    "ds_read_b64_tr_b16 v[58:59], v244 offset:1536\n\t" \
    "ds_read_b64_tr_b16 v[64:65], v244 offset:32\n\t" \
    "ds_read_b64_tr_b16 v[66:67], v244 offset:1568\n\t" \
    "ds_read_b64_tr_b16 v[72:73], v244 offset:64\n\t" \
    "ds_read_b64_tr_b16 v[74:75], v244 offset:1600\n\t" \
    "v_add_u32 v244, 0x1e000, v243\n\t" \
    "s_waitcnt vmcnt(12) lgkmcnt(0)\n\t" \
    "s_barrier \n\t" \
    "s_add_u32 m0, %[wm], 0x18000\n\t" \
    "s_mov_b32 s44, 0x240000\n\t" \
    "buffer_load_dwordx4 v240, %[wr], s44 offen offset:0 nt lds\n\t" \
    "buffer_load_dwordx4 v241, %[wr], s44 offen offset:1024 nt lds\n\t" \
    "buffer_load_dwordx4 v242, %[wr], s44 offen offset:2048 nt lds\n\t" \
    "ds_read_b64_tr_b16 v[60:61], v244 offset:0\n\t" \
    "ds_read_b64_tr_b16 v[62:63], v244 offset:1536\n\t" \
    "ds_read_b64_tr_b16 v[68:69], v244 offset:32\n\t" \
    "ds_read_b64_tr_b16 v[70:71], v244 offset:1568\n\t" \
    "ds_read_b64_tr_b16 v[76:77], v244 offset:64\n\t" \
    "ds_read_b64_tr_b16 v[78:79], v244 offset:1600\n\t" \
    "v_add_u32 v244, 0x0, v243\n\t" \
    "s_waitcnt vmcnt(12) lgkmcnt(0)\n\t" \
    "s_barrier \n\t" \
    "s_add_u32 m0, %[wm], 0x1e000\n\t" \
    "s_mov_b32 s44, 0x2a0000\n\t" \
    "buffer_load_dwordx4 v240, %[wr], s44 offen offset:0 nt lds\n\t" \
    "buffer_load_dwordx4 v241, %[wr], s44 offen offset:1024 nt lds\n\t" \
    "buffer_load_dwordx4 v242, %[wr], s44 offen offset:2048 nt lds\n\t" \
    "ds_read_b64_tr_b16 v[96:97], v244 offset:0\n\t" \
    "ds_read_b64_tr_b16 v[98:99], v244 offset:1536\n\t" \
    "ds_read_b64_tr_b16 v[104:105], v244 offset:32\n\t" \
    "ds_read_b64_tr_b16 v[106:107], v244 offset:1568\n\t" \
    "ds_read_b64_tr_b16 v[112:113], v244 offset:64\n\t" \
    "ds_read_b64_tr_b16 v[114:115], v244 offset:1600\n\t" \
    "v_add_u32 v244, 0x6000, v243\n\t" \
    "s_waitcnt vmcnt(12) lgkmcnt(0)\n\t" \
    "s_barrier \n\t" \
    "s_add_u32 m0, %[wm], 0x0\n\t" \
    "s_mov_b32 s44, 0x300000\n\t" \
    "buffer_load_dwordx4 v240, %[wr], s44 offen offset:0 nt lds\n\t" \
    "buffer_load_dwordx4 v241, %[wr], s44 offen offset:1024 nt lds\n\t" \
    "buffer_load_dwordx4 v242, %[wr], s44 offen offset:2048 nt lds\n\t" \
    "ds_read_b64_tr_b16 v[100:101], v244 offset:0\n\t" \
    "ds_read_b64_tr_b16 v[102:103], v244 offset:1536\n\t" \
    "ds_read_b64_tr_b16 v[108:109], v244 offset:32\n\t" \
    "ds_read_b64_tr_b16 v[110:111], v244 offset:1568\n\t" \
    "ds_read_b64_tr_b16 v[116:117], v244 offset:64\n\t" \
    "ds_read_b64_tr_b16 v[118:119], v244 offset:1600\n\t" \
    "v_add_u32 v244, 0xc000, v243\n\t" \
    "s_waitcnt vmcnt(12) lgkmcnt(0)\n\t" \
    "s_barrier \n\t" \
    "s_add_u32 m0, %[wm], 0x6000\n\t" \
    "s_mov_b32 s44, 0x360000\n\t" \
    "buffer_load_dwordx4 v240, %[wr], s44 offen offset:0 nt lds\n\t" \
    "buffer_load_dwordx4 v241, %[wr], s44 offen offset:1024 nt lds\n\t" \
    "buffer_load_dwordx4 v242, %[wr], s44 offen offset:2048 nt lds\n\t" \
    "ds_read_b64_tr_b16 v[200:201], v244 offset:0\n\t" \
    "ds_read_b64_tr_b16 v[202:203], v244 offset:1536\n\t" \
    "ds_read_b64_tr_b16 v[0:1], v244 offset:32\n\t" \
    "ds_read_b64_tr_b16 v[2:3], v244 offset:1568\n\t" \
    "ds_read_b64_tr_b16 v[8:9], v244 offset:64\n\t" \
    "ds_read_b64_tr_b16 v[10:11], v244 offset:1600\n\t" \
    "v_add_u32 v244, 0x12000, v243\n\t" \
    "s_waitcnt vmcnt(12) lgkmcnt(0)\n\t" \
    "s_barrier \n\t" \
    "s_add_u32 m0, %[wm], 0xc000\n\t" \
    "s_mov_b32 s44, 0x3c0000\n\t" \
    "buffer_load_dwordx4 v240, %[wr], s44 offen offset:0 nt lds\n\t" \
    "buffer_load_dwordx4 v241, %[wr], s44 offen offset:1024 nt lds\n\t" \
    "buffer_load_dwordx4 v242, %[wr], s44 offen offset:2048 nt lds\n\t" \
    "ds_read_b64_tr_b16 v[204:205], v244 offset:0\n\t" \
    "ds_read_b64_tr_b16 v[206:207], v244 offset:1536\n\t" \
    "ds_read_b64_tr_b16 v[4:5], v244 offset:32\n\t" \
    "ds_read_b64_tr_b16 v[6:7], v244 offset:1568\n\t" \
    "ds_read_b64_tr_b16 v[12:13], v244 offset:64\n\t" \
    "ds_read_b64_tr_b16 v[14:15], v244 offset:1600\n\t" \
    "v_add_u32 v244, 0x18000, v243\n\t" \
    "s_waitcnt vmcnt(12) lgkmcnt(0)\n\t" \
    "s_barrier \n\t" \
    "s_add_u32 m0, %[wm], 0x12000\n\t" \
    "s_mov_b32 s44, 0x420000\n\t" \
    "buffer_load_dwordx4 v240, %[wr], s44 offen offset:0 nt lds\n\t" \
    "buffer_load_dwordx4 v241, %[wr], s44 offen offset:1024 nt lds\n\t" \
    "buffer_load_dwordx4 v242, %[wr], s44 offen offset:2048 nt lds\n\t" \
    "ds_read_b64_tr_b16 v[208:209], v244 offset:0\n\t" \
    "ds_read_b64_tr_b16 v[210:211], v244 offset:1536\n\t" \
    "ds_read_b64_tr_b16 v[40:41], v244 offset:32\n\t" \
    "ds_read_b64_tr_b16 v[42:43], v244 offset:1568\n\t" \
    "ds_read_b64_tr_b16 v[48:49], v244 offset:64\n\t" \
    "ds_read_b64_tr_b16 v[50:51], v244 offset:1600\n\t" \
    "v_add_u32 v244, 0x1e000, v243\n\t" \
    "s_waitcnt vmcnt(12) lgkmcnt(0)\n\t" \
    "s_barrier \n\t" \
    "s_add_u32 m0, %[wm], 0x18000\n\t" \
    "s_mov_b32 s44, 0x480000\n\t" \
    "buffer_load_dwordx4 v240, %[wr], s44 offen offset:0 nt lds\n\t" \
    "buffer_load_dwordx4 v241, %[wr], s44 offen offset:1024 nt lds\n\t" \
    "buffer_load_dwordx4 v242, %[wr], s44 offen offset:2048 nt lds\n\t" \
    "ds_read_b64_tr_b16 v[212:213], v244 offset:0\n\t" \
    "ds_read_b64_tr_b16 v[214:215], v244 offset:1536\n\t" \
    "ds_read_b64_tr_b16 v[44:45], v244 offset:32\n\t" \
    "ds_read_b64_tr_b16 v[46:47], v244 offset:1568\n\t" \
    "ds_read_b64_tr_b16 v[52:53], v244 offset:64\n\t" \
    "ds_read_b64_tr_b16 v[54:55], v244 offset:1600\n\t" \
    "v_add_u32 v244, 0x0, v243\n\t" \
    "s_waitcnt vmcnt(12) lgkmcnt(0)\n\t" \
    "s_barrier \n\t" \
    "v_mov_b32 v246, 0\n\t" \
    "buffer_load_dword v246, v246, s[60:63], %[mo] offen sc1\n\t" \
    "s_add_u32 m0, %[wm], 0x1e000\n\t" \
    "s_mov_b32 s44, 0x4e0000\n\t" \
    "buffer_load_dwordx4 v240, %[wr], s44 offen offset:0 nt lds\n\t" \
    "buffer_load_dwordx4 v241, %[wr], s44 offen offset:1024 nt lds\n\t" \
    "buffer_load_dwordx4 v242, %[wr], s44 offen offset:2048 nt lds\n\t" \
    "ds_read_b64_tr_b16 v[136:137], v244 offset:0\n\t" \
    "ds_read_b64_tr_b16 v[138:139], v244 offset:1536\n\t" \
    "ds_read_b64_tr_b16 v[144:145], v244 offset:32\n\t" \
    "ds_read_b64_tr_b16 v[146:147], v244 offset:1568\n\t" \
    "ds_read_b64_tr_b16 v[152:153], v244 offset:64\n\t" \
    "ds_read_b64_tr_b16 v[154:155], v244 offset:1600\n\t" \
    "v_add_u32 v244, 0x6000, v243\n\t" \
    "s_waitcnt vmcnt(12) lgkmcnt(0)\n\t" \
    "s_barrier \n\t" \
    "s_add_u32 m0, %[wm], 0x0\n\t" \
    "s_mov_b32 s44, 0x540000\n\t" \
    "buffer_load_dwordx4 v240, %[wr], s44 offen offset:0 nt lds\n\t" \
    "buffer_load_dwordx4 v241, %[wr], s44 offen offset:1024 nt lds\n\t" \
    "buffer_load_dwordx4 v242, %[wr], s44 offen offset:2048 nt lds\n\t" \
    "ds_read_b64_tr_b16 v[140:141], v244 offset:0\n\t" \
    "ds_read_b64_tr_b16 v[142:143], v244 offset:1536\n\t" \
    "ds_read_b64_tr_b16 v[148:149], v244 offset:32\n\t" \
    "ds_read_b64_tr_b16 v[150:151], v244 offset:1568\n\t" \
    "ds_read_b64_tr_b16 v[156:157], v244 offset:64\n\t" \
    "ds_read_b64_tr_b16 v[158:159], v244 offset:1600\n\t" \
    "v_add_u32 v244, 0xc000, v243\n\t" \
    "s_waitcnt vmcnt(12) lgkmcnt(0)\n\t" \
    "s_barrier \n\t" \
    "s_add_u32 m0, %[wm], 0x6000\n\t" \
    "s_mov_b32 s44, 0x5a0000\n\t" \
    "buffer_load_dwordx4 v240, %[wr], s44 offen offset:0 nt lds\n\t" \
    "buffer_load_dwordx4 v241, %[wr], s44 offen offset:1024 nt lds\n\t" \
    "buffer_load_dwordx4 v242, %[wr], s44 offen offset:2048 nt lds\n\t" \
    "ds_read_b64_tr_b16 v[216:217], v244 offset:0\n\t" \
    "ds_read_b64_tr_b16 v[218:219], v244 offset:1536\n\t" \
    "ds_read_b64_tr_b16 v[80:81], v244 offset:32\n\t" \
    "ds_read_b64_tr_b16 v[82:83], v244 offset:1568\n\t" \
    "ds_read_b64_tr_b16 v[88:89], v244 offset:64\n\t" \
    "ds_read_b64_tr_b16 v[90:91], v244 offset:1600\n\t" \
    "v_add_u32 v244, 0x12000, v243\n\t" \
    "s_waitcnt vmcnt(12) lgkmcnt(0)\n\t" \
    "s_barrier \n\t" \
    "s_add_u32 m0, %[wm], 0xc000\n\t" \
    "s_mov_b32 s44, 0x600000\n\t" \
    "buffer_load_dwordx4 v240, %[wr], s44 offen offset:0 nt lds\n\t" \
    "buffer_load_dwordx4 v241, %[wr], s44 offen offset:1024 nt lds\n\t" \
    "buffer_load_dwordx4 v242, %[wr], s44 offen offset:2048 nt lds\n\t" \
    "ds_read_b64_tr_b16 v[220:221], v244 offset:0\n\t" \
    "ds_read_b64_tr_b16 v[222:223], v244 offset:1536\n\t" \
    "ds_read_b64_tr_b16 v[84:85], v244 offset:32\n\t" \
    "ds_read_b64_tr_b16 v[86:87], v244 offset:1568\n\t" \
    "ds_read_b64_tr_b16 v[92:93], v244 offset:64\n\t" \
    "ds_read_b64_tr_b16 v[94:95], v244 offset:1600\n\t" \
    "v_add_u32 v244, 0x18000, v243\n\t" \
    "s_waitcnt vmcnt(12) lgkmcnt(0)\n\t" \
    "s_barrier \n\t" \
    "s_add_u32 m0, %[wm], 0x12000\n\t" \
    "s_mov_b32 s44, 0x660000\n\t" \
    "buffer_load_dwordx4 v240, %[wr], s44 offen offset:0 nt lds\n\t" \
    "buffer_load_dwordx4 v241, %[wr], s44 offen offset:1024 nt lds\n\t" \
    "buffer_load_dwordx4 v242, %[wr], s44 offen offset:2048 nt lds\n\t" \
    "ds_read_b64_tr_b16 v[16:17], v244 offset:0\n\t" \
    "ds_read_b64_tr_b16 v[18:19], v244 offset:1536\n\t" \
    "ds_read_b64_tr_b16 v[24:25], v244 offset:32\n\t" \
    "ds_read_b64_tr_b16 v[26:27], v244 offset:1568\n\t" \
    "ds_read_b64_tr_b16 v[32:33], v244 offset:64\n\t" \
    "ds_read_b64_tr_b16 v[34:35], v244 offset:1600\n\t" \
    "v_add_u32 v244, 0x1e000, v243\n\t" \
    "s_waitcnt vmcnt(12) lgkmcnt(0)\n\t" \
    "s_barrier \n\t" \
    ".Ls11_%=:\n\t" \
    "v_readfirstlane_b32 s43, v246\n\t" \
    "s_nop 0\n\t" \
    "s_cmp_lg_u32 s43, -1\n\t" \
    "s_cbranch_scc1 .Ls12_%=\n\t" \
    "s_cmp_eq_u32 s41, 0\n\t" \
    "s_cbranch_scc1 .Ls12_%=\n\t" \
    "s_sub_u32 s41, s41, 1\n\t" \
    "s_sleep 4\n\t" \
    "v_mov_b32 v246, 0\n\t" \
    "buffer_load_dword v246, v246, s[60:63], %[mo] offen sc1\n\t" \
    "s_waitcnt vmcnt(0)\n\t" \
    "s_branch .Ls11_%=\n\t" \
    ".Ls12_%=:\n\t" \
    "v_mov_b32 v246, 0x8000\n\t" \
    "s_mov_b64 exec, 1\n\t" \
    "buffer_store_dword v246, v246, s[60:63], %[mo] offen sc1\n\t" \
    "s_mov_b64 exec, -1\n\t" \
    "s_and_b32 s43, s43, 0x2000\n\t" \
    "s_xor_b32 s43, s43, %[lx]\n\t" \
    "s_add_u32 s60, s60, s43\n\t" \
    "s_addc_u32 s61, s61, 0\n\t" \
    "s_add_u32 m0, %[wm], 0x18000\n\t" \
    "s_mov_b32 s44, 0x6c0000\n\t" \
    "buffer_load_dwordx4 v240, %[wr], s44 offen offset:0 nt lds\n\t" \
    "buffer_load_dwordx4 v241, %[wr], s44 offen offset:1024 nt lds\n\t" \
    "buffer_load_dwordx4 v242, %[wr], s44 offen offset:2048 nt lds\n\t" \
    "ds_read_b64_tr_b16 v[20:21], v244 offset:0\n\t" \
    "ds_read_b64_tr_b16 v[22:23], v244 offset:1536\n\t" \
    "ds_read_b64_tr_b16 v[28:29], v244 offset:32\n\t" \
    "ds_read_b64_tr_b16 v[30:31], v244 offset:1568\n\t" \
    "ds_read_b64_tr_b16 v[36:37], v244 offset:64\n\t" \
    "ds_read_b64_tr_b16 v[38:39], v244 offset:1600\n\t" \
    "v_add_u32 v244, 0x0, v243\n\t" \
    "s_waitcnt vmcnt(12) lgkmcnt(0)\n\t" \
    "s_barrier \n\t" \
    "s_add_u32 m0, %[wm], 0x1e000\n\t" \
    "s_mov_b32 s44, 0x720000\n\t" \
    "buffer_load_dwordx4 v240, %[wr], s44 offen offset:0 nt lds\n\t" \
    "buffer_load_dwordx4 v241, %[wr], s44 offen offset:1024 nt lds\n\t" \
    "buffer_load_dwordx4 v242, %[wr], s44 offen offset:2048 nt lds\n\t" \
    "ds_read_b64_tr_b16 v[224:225], v244 offset:0\n\t" \
    "ds_read_b64_tr_b16 v[226:227], v244 offset:1536\n\t" \
    "ds_read_b64_tr_b16 v[120:121], v244 offset:32\n\t" \
    "ds_read_b64_tr_b16 v[122:123], v244 offset:1568\n\t" \
    "ds_read_b64_tr_b16 v[128:129], v244 offset:64\n\t" \
    "ds_read_b64_tr_b16 v[130:131], v244 offset:1600\n\t" \
    "v_add_u32 v244, 0x6000, v243\n\t" \
    "s_waitcnt vmcnt(12) lgkmcnt(0)\n\t" \
    "s_barrier \n\t" \
    "ds_read_b64_tr_b16 v[228:229], v244 offset:0\n\t" \
    "ds_read_b64_tr_b16 v[230:231], v244 offset:1536\n\t" \
    "ds_read_b64_tr_b16 v[124:125], v244 offset:32\n\t" \
    "ds_read_b64_tr_b16 v[126:127], v244 offset:1568\n\t" \
    "ds_read_b64_tr_b16 v[132:133], v244 offset:64\n\t" \
    "ds_read_b64_tr_b16 v[134:135], v244 offset:1600\n\t" \
    "v_add_u32 v244, 0xc000, v243\n\t" \
    "s_waitcnt vmcnt(9) lgkmcnt(0)\n\t" \
    "s_barrier \n\t" \
    "ds_read_b64_tr_b16 v[232:233], v244 offset:0\n\t" \
    "ds_read_b64_tr_b16 v[234:235], v244 offset:1536\n\t" \
    "ds_read_b64_tr_b16 v[160:161], v244 offset:32\n\t" \
    "ds_read_b64_tr_b16 v[162:163], v244 offset:1568\n\t" \
    "ds_read_b64_tr_b16 v[168:169], v244 offset:64\n\t" \
    "ds_read_b64_tr_b16 v[170:171], v244 offset:1600\n\t" \
    "v_add_u32 v244, 0x12000, v243\n\t" \
    "s_waitcnt vmcnt(6) lgkmcnt(0)\n\t" \
    "s_barrier \n\t" \
    "ds_read_b64_tr_b16 v[236:237], v244 offset:0\n\t" \
    "ds_read_b64_tr_b16 v[238:239], v244 offset:1536\n\t" \
    "ds_read_b64_tr_b16 v[164:165], v244 offset:32\n\t" \
    "ds_read_b64_tr_b16 v[166:167], v244 offset:1568\n\t" \
    "ds_read_b64_tr_b16 v[172:173], v244 offset:64\n\t" \
    "ds_read_b64_tr_b16 v[174:175], v244 offset:1600\n\t" \
    "v_add_u32 v244, 0x18000, v243\n\t" \
    "s_waitcnt vmcnt(3) lgkmcnt(0)\n\t" \
    "s_barrier \n\t" \
    "ds_read_b64_tr_b16 v[176:177], v244 offset:0\n\t" \
    "ds_read_b64_tr_b16 v[178:179], v244 offset:1536\n\t" \
    "ds_read_b64_tr_b16 v[184:185], v244 offset:32\n\t" \
    "ds_read_b64_tr_b16 v[186:187], v244 offset:1568\n\t" \
    "ds_read_b64_tr_b16 v[192:193], v244 offset:64\n\t" \
    "ds_read_b64_tr_b16 v[194:195], v244 offset:1600\n\t" \
    "v_add_u32 v244, 0x1e000, v243\n\t" \
    "s_waitcnt vmcnt(0) lgkmcnt(0)\n\t" \
    "s_barrier \n\t" \
    "ds_read_b64_tr_b16 v[180:181], v244 offset:0\n\t" \
    "ds_read_b64_tr_b16 v[182:183], v244 offset:1536\n\t" \
    "ds_read_b64_tr_b16 v[188:189], v244 offset:32\n\t" \
    "ds_read_b64_tr_b16 v[190:191], v244 offset:1568\n\t" \
    "ds_read_b64_tr_b16 v[196:197], v244 offset:64\n\t" \
    "ds_read_b64_tr_b16 v[198:199], v244 offset:1600\n\t" \
    "s_waitcnt lgkmcnt(0)\n\t" \
    "s_barrier \n\t" \
    LST0 \
    LJIT \
    "ds_write_b128 v254, v[200:203] offset:40960\n\t" \
    "ds_write_b128 v254, v[204:207] offset:41984\n\t" \
    "ds_write_b128 v254, v[208:211] offset:43008\n\t" \
    "ds_write_b128 v254, v[212:215] offset:44032\n\t" \
    "ds_write_b128 v254, v[216:219] offset:45056\n\t" \
    "ds_write_b128 v254, v[220:223] offset:46080\n\t" \
    "ds_write_b128 v254, v[224:227] offset:47104\n\t" \
    "ds_write_b128 v254, v[228:231] offset:48128\n\t" \
    "ds_write_b128 v254, v[232:235] offset:49152\n\t" \
    "ds_write_b128 v254, v[236:239] offset:50176\n\t" \
    "s_waitcnt vmcnt(0) lgkmcnt(0)\n\t" \
    "s_barrier \n\t" \
    "ds_read_b128 v[224:227], v253 offset:0\n\t" \
    "ds_read_b128 v[244:247], v254 offset:40960\n\t" \
    "ds_read_b128 v[228:231], v253 offset:1024\n\t" \
    "ds_read_b128 v[232:235], v253 offset:20480\n\t" \
    "ds_read_b128 v[236:239], v253 offset:21504\n\t" \
    "ds_read_b128 v[248:251], v254 offset:41984\n\t" \
    ".Ls13_%=:\n\t" \
    "s_waitcnt lgkmcnt(5)\n\t" \
    "v_mfma_f32_16x16x32_bf16 v[204:207], v[64:67], v[224:227], 0\n\t" \
    "s_waitcnt lgkmcnt(3)\n\t" \
    "v_mfma_f32_16x16x32_bf16 v[204:207], v[68:71], v[228:231], v[204:207]\n\t" \
    "ds_read_b128 v[240:243], v253 offset:2048\n\t" \
    "v_mfma_f32_16x16x32_bf16 v[208:211], v[72:75], v[224:227], 0\n\t" \
    "v_mfma_f32_16x16x32_bf16 v[208:211], v[76:79], v[228:231], v[208:211]\n\t" \
    "v_mfma_f32_16x16x32_bf16 v[200:203], v[56:59], v[224:227], 0\n\t" \
    "v_mfma_f32_16x16x32_bf16 v[200:203], v[60:63], v[228:231], v[200:203]\n\t" \
    "s_waitcnt lgkmcnt(3)\n\t" \
    "v_mfma_f32_16x16x32_bf16 v[216:219], v[64:67], v[232:235], 0\n\t" \
    "ds_read_b128 v[224:227], v253 offset:3072\n\t" \
    "s_waitcnt lgkmcnt(3)\n\t" \
    "v_mfma_f32_16x16x32_bf16 v[216:219], v[68:71], v[236:239], v[216:219]\n\t" \
    "ds_read_b128 v[228:231], v253 offset:22528\n\t" \
    "v_mfma_f32_16x16x32_bf16 v[220:223], v[72:75], v[232:235], 0\n\t" \
    "v_mfma_f32_16x16x32_bf16 v[220:223], v[76:79], v[236:239], v[220:223]\n\t" \
    "v_mfma_f32_16x16x32_bf16 v[212:215], v[56:59], v[232:235], 0\n\t" \
    "v_mfma_f32_16x16x32_bf16 v[212:215], v[60:63], v[236:239], v[212:215]\n\t" \
    "s_waitcnt lgkmcnt(2)\n\t" \
    "v_mfma_f32_16x16x32_bf16 v[204:207], v[104:107], v[240:243], v[204:207]\n\t" \
    "ds_read_b128 v[232:235], v253 offset:23552\n\t" \
    "s_waitcnt lgkmcnt(2)\n\t" \
    "v_mfma_f32_16x16x32_bf16 v[204:207], v[108:111], v[224:227], v[204:207]\n\t" \
    "ds_read_b128 v[236:239], v253 offset:4096\n\t" \
    "v_mfma_f32_16x16x32_bf16 v[208:211], v[112:115], v[240:243], v[208:211]\n\t" \
    "v_mfma_f32_16x16x32_bf16 v[208:211], v[116:119], v[224:227], v[208:211]\n\t" \
    "v_mfma_f32_16x16x32_bf16 v[200:203], v[96:99], v[240:243], v[200:203]\n\t" \
    "v_mfma_f32_16x16x32_bf16 v[200:203], v[100:103], v[224:227], v[200:203]\n\t" \
    "s_waitcnt lgkmcnt(2)\n\t" \
    "v_mfma_f32_16x16x32_bf16 v[216:219], v[104:107], v[228:231], v[216:219]\n\t" \
    "ds_read_b128 v[240:243], v253 offset:5120\n\t" \
    "s_waitcnt lgkmcnt(2)\n\t" \
    "v_mfma_f32_16x16x32_bf16 v[216:219], v[108:111], v[232:235], v[216:219]\n\t" \
    "ds_read_b128 v[224:227], v253 offset:24576\n\t" \
    "v_mfma_f32_16x16x32_bf16 v[220:223], v[112:115], v[228:231], v[220:223]\n\t" \
    "v_mfma_f32_16x16x32_bf16 v[220:223], v[116:119], v[232:235], v[220:223]\n\t" \
    "v_mfma_f32_16x16x32_bf16 v[212:215], v[96:99], v[228:231], v[212:215]\n\t" \
    "v_mfma_f32_16x16x32_bf16 v[212:215], v[100:103], v[232:235], v[212:215]\n\t" \
    "s_waitcnt lgkmcnt(2)\n\t" \
    "v_mfma_f32_16x16x32_bf16 v[204:207], v[0:3], v[236:239], v[204:207]\n\t" \
    "ds_read_b128 v[228:231], v253 offset:25600\n\t" \
    "s_waitcnt lgkmcnt(2)\n\t" \
    "v_mfma_f32_16x16x32_bf16 v[204:207], v[4:7], v[240:243], v[204:207]\n\t" \
    "ds_read_b128 v[232:235], v253 offset:6144\n\t" \
    "v_mfma_f32_16x16x32_bf16 v[208:211], v[8:11], v[236:239], v[208:211]\n\t" \
    "v_mfma_f32_16x16x32_bf16 v[208:211], v[12:15], v[240:243], v[208:211]\n\t" \
    "v_mfma_f32_16x16x32_bf16 v[200:203], v[244:247], v[236:239], v[200:203]\n\t" \
    "v_mfma_f32_16x16x32_bf16 v[200:203], v[248:251], v[240:243], v[200:203]\n\t" \
    "s_waitcnt lgkmcnt(2)\n\t" \
    "v_mfma_f32_16x16x32_bf16 v[216:219], v[0:3], v[224:227], v[216:219]\n\t" \
    "ds_read_b128 v[236:239], v253 offset:7168\n\t" \
    "s_waitcnt lgkmcnt(2)\n\t" \
    "v_mfma_f32_16x16x32_bf16 v[216:219], v[4:7], v[228:231], v[216:219]\n\t" \
    "ds_read_b128 v[240:243], v253 offset:26624\n\t" \
    "v_mfma_f32_16x16x32_bf16 v[220:223], v[8:11], v[224:227], v[220:223]\n\t" \
    "v_mfma_f32_16x16x32_bf16 v[220:223], v[12:15], v[228:231], v[220:223]\n\t" \
    "v_mfma_f32_16x16x32_bf16 v[212:215], v[244:247], v[224:227], v[212:215]\n\t" \
    "ds_read_b128 v[244:247], v254 offset:43008\n\t" \
    "v_mfma_f32_16x16x32_bf16 v[212:215], v[248:251], v[228:231], v[212:215]\n\t" \
    "ds_read_b128 v[248:251], v254 offset:44032\n\t" \
    "s_waitcnt lgkmcnt(4)\n\t" \
    "v_mfma_f32_16x16x32_bf16 v[204:207], v[40:43], v[232:235], v[204:207]\n\t" \
    "ds_read_b128 v[224:227], v253 offset:27648\n\t" \
    "s_waitcnt lgkmcnt(4)\n\t" \
    "v_mfma_f32_16x16x32_bf16 v[204:207], v[44:47], v[236:239], v[204:207]\n\t" \
    "ds_read_b128 v[228:231], v253 offset:8192\n\t" \
    "v_mfma_f32_16x16x32_bf16 v[208:211], v[48:51], v[232:235], v[208:211]\n\t" \
    "v_mfma_f32_16x16x32_bf16 v[208:211], v[52:55], v[236:239], v[208:211]\n\t" \
    "s_waitcnt lgkmcnt(3)\n\t" \
    "v_mfma_f32_16x16x32_bf16 v[200:203], v[244:247], v[232:235], v[200:203]\n\t" \
    "s_waitcnt lgkmcnt(2)\n\t" \
    "v_mfma_f32_16x16x32_bf16 v[200:203], v[248:251], v[236:239], v[200:203]\n\t" \
    "v_mfma_f32_16x16x32_bf16 v[216:219], v[40:43], v[240:243], v[216:219]\n\t" \
    "ds_read_b128 v[232:235], v253 offset:9216\n\t" \
    "s_waitcnt lgkmcnt(2)\n\t" \
    "v_mfma_f32_16x16x32_bf16 v[216:219], v[44:47], v[224:227], v[216:219]\n\t" \
    "ds_read_b128 v[236:239], v253 offset:28672\n\t" \
    "v_mfma_f32_16x16x32_bf16 v[220:223], v[48:51], v[240:243], v[220:223]\n\t" \
    "v_mfma_f32_16x16x32_bf16 v[220:223], v[52:55], v[224:227], v[220:223]\n\t" \
    "v_mfma_f32_16x16x32_bf16 v[212:215], v[244:247], v[240:243], v[212:215]\n\t" \
    "ds_read_b128 v[244:247], v254 offset:45056\n\t" \
    "v_mfma_f32_16x16x32_bf16 v[212:215], v[248:251], v[224:227], v[212:215]\n\t" \
    "ds_read_b128 v[248:251], v254 offset:46080\n\t" \
    "s_waitcnt lgkmcnt(4)\n\t" \
    "v_mfma_f32_16x16x32_bf16 v[204:207], v[144:147], v[228:231], v[204:207]\n\t" \
    "ds_read_b128 v[240:243], v253 offset:29696\n\t" \
    "s_waitcnt lgkmcnt(4)\n\t" \
    "v_mfma_f32_16x16x32_bf16 v[204:207], v[148:151], v[232:235], v[204:207]\n\t" \
    "ds_read_b128 v[224:227], v253 offset:10240\n\t" \
    "v_mfma_f32_16x16x32_bf16 v[208:211], v[152:155], v[228:231], v[208:211]\n\t" \
    "v_mfma_f32_16x16x32_bf16 v[208:211], v[156:159], v[232:235], v[208:211]\n\t" \
    "v_mfma_f32_16x16x32_bf16 v[200:203], v[136:139], v[228:231], v[200:203]\n\t" \
    "v_mfma_f32_16x16x32_bf16 v[200:203], v[140:143], v[232:235], v[200:203]\n\t" \
    "s_waitcnt lgkmcnt(4)\n\t" \
    "v_mfma_f32_16x16x32_bf16 v[216:219], v[144:147], v[236:239], v[216:219]\n\t" \
    "ds_read_b128 v[228:231], v253 offset:11264\n\t" \
    "s_waitcnt lgkmcnt(2)\n\t" \
    "v_mfma_f32_16x16x32_bf16 v[216:219], v[148:151], v[240:243], v[216:219]\n\t" \
    "ds_read_b128 v[232:235], v253 offset:30720\n\t" \
    "v_mfma_f32_16x16x32_bf16 v[220:223], v[152:155], v[236:239], v[220:223]\n\t" \
    "v_mfma_f32_16x16x32_bf16 v[220:223], v[156:159], v[240:243], v[220:223]\n\t" \
    "v_mfma_f32_16x16x32_bf16 v[212:215], v[136:139], v[236:239], v[212:215]\n\t" \
    "v_mfma_f32_16x16x32_bf16 v[212:215], v[140:143], v[240:243], v[212:215]\n\t" \
    "ds_read_b128 v[236:239], v253 offset:31744\n\t" \
    "s_setprio 2\n\t" \
    "s_waitcnt lgkmcnt(0)\n\t" \
    "s_barrier \n\t" \
    LST7 \
    LJIT \
    "v_mfma_f32_16x16x32_bf16 v[204:207], v[80:83], v[224:227], v[204:207]\n\t" \
    "v_mfma_f32_16x16x32_bf16 v[204:207], v[84:87], v[228:231], v[204:207]\n\t" \
    "ds_read_b128 v[240:243], v253 offset:14336\n\t" \
    "v_mfma_f32_16x16x32_bf16 v[208:211], v[88:91], v[224:227], v[208:211]\n\t" \
    "v_mfma_f32_16x16x32_bf16 v[208:211], v[92:95], v[228:231], v[208:211]\n\t" \
    "v_mfma_f32_16x16x32_bf16 v[200:203], v[244:247], v[224:227], v[200:203]\n\t" \
    "v_mfma_f32_16x16x32_bf16 v[200:203], v[248:251], v[228:231], v[200:203]\n\t" \
    "v_mfma_f32_16x16x32_bf16 v[216:219], v[80:83], v[232:235], v[216:219]\n\t" \
    "ds_read_b128 v[224:227], v253 offset:15360\n\t" \
    "s_and_b32 s44, s40, 3\n\t" \
    "v_mfma_f32_16x16x32_bf16 v[216:219], v[84:87], v[236:239], v[216:219]\n\t" \
    "ds_read_b128 v[228:231], v253 offset:34816\n\t" \
    "s_lshl_b32 s44, s44, 17\n\t" \
    "v_mfma_f32_16x16x32_bf16 v[220:223], v[88:91], v[232:235], v[220:223]\n\t" \
    "s_add_u32 s44, s44, %[mb]\n\t" \
    "v_mfma_f32_16x16x32_bf16 v[220:223], v[92:95], v[236:239], v[220:223]\n\t" \
    "s_add_u32 s46, s44, 0x800\n\t" \
    "v_mfma_f32_16x16x32_bf16 v[212:215], v[244:247], v[232:235], v[212:215]\n\t" \
    "ds_read_b128 v[244:247], v254 offset:47104\n\t" \
    "s_add_u32 s47, s44, 0x1800\n\t" \
    "v_mfma_f32_16x16x32_bf16 v[212:215], v[248:251], v[236:239], v[212:215]\n\t" \
    "ds_read_b128 v[248:251], v254 offset:48128\n\t" \
    "s_add_u32 s48, s44, 0xc00\n\t" \
    "s_waitcnt lgkmcnt(4)\n\t" \
    "v_mfma_f32_16x16x32_bf16 v[204:207], v[120:123], v[240:243], v[204:207]\n\t" \
    "ds_read_b128 v[232:235], v253 offset:35840\n\t" \
    "s_waitcnt lgkmcnt(4)\n\t" \
    "v_mfma_f32_16x16x32_bf16 v[204:207], v[124:127], v[224:227], v[204:207]\n\t" \
    "ds_read_b128 v[236:239], v253 offset:16384\n\t" \
    "v_mfma_f32_16x16x32_bf16 v[208:211], v[128:131], v[240:243], v[208:211]\n\t" \
    "v_mfma_f32_16x16x32_bf16 v[208:211], v[132:135], v[224:227], v[208:211]\n\t" \
    "s_waitcnt lgkmcnt(3)\n\t" \
    "v_mfma_f32_16x16x32_bf16 v[200:203], v[244:247], v[240:243], v[200:203]\n\t" \
    "s_waitcnt lgkmcnt(2)\n\t" \
    "v_mfma_f32_16x16x32_bf16 v[200:203], v[248:251], v[224:227], v[200:203]\n\t" \
    "v_mfma_f32_16x16x32_bf16 v[216:219], v[120:123], v[228:231], v[216:219]\n\t" \
    "ds_read_b128 v[240:243], v253 offset:17408\n\t" \
    "s_waitcnt lgkmcnt(2)\n\t" \
    "v_mfma_f32_16x16x32_bf16 v[216:219], v[124:127], v[232:235], v[216:219]\n\t" \
    "ds_read_b128 v[224:227], v253 offset:36864\n\t" \
    "v_mfma_f32_16x16x32_bf16 v[220:223], v[128:131], v[228:231], v[220:223]\n\t" \
    "v_mfma_f32_16x16x32_bf16 v[220:223], v[132:135], v[232:235], v[220:223]\n\t" \
    "v_mfma_f32_16x16x32_bf16 v[212:215], v[244:247], v[228:231], v[212:215]\n\t" \
    "ds_read_b128 v[244:247], v254 offset:49152\n\t" \
    "v_mfma_f32_16x16x32_bf16 v[212:215], v[248:251], v[232:235], v[212:215]\n\t" \
    "ds_read_b128 v[248:251], v254 offset:50176\n\t" \
    "s_waitcnt lgkmcnt(4)\n\t" \
    "v_mfma_f32_16x16x32_bf16 v[204:207], v[160:163], v[236:239], v[204:207]\n\t" \
    "ds_read_b128 v[228:231], v253 offset:37888\n\t" \
    "s_waitcnt lgkmcnt(4)\n\t" \
    "v_mfma_f32_16x16x32_bf16 v[204:207], v[164:167], v[240:243], v[204:207]\n\t" \
    "ds_read_b128 v[232:235], v253 offset:12288\n\t" \
    "v_mfma_f32_16x16x32_bf16 v[208:211], v[168:171], v[236:239], v[208:211]\n\t" \
    "v_mfma_f32_16x16x32_bf16 v[208:211], v[172:175], v[240:243], v[208:211]\n\t" \
    "s_waitcnt lgkmcnt(3)\n\t" \
    "v_mfma_f32_16x16x32_bf16 v[200:203], v[244:247], v[236:239], v[200:203]\n\t" \
    "s_waitcnt lgkmcnt(2)\n\t" \
    "v_mfma_f32_16x16x32_bf16 v[200:203], v[248:251], v[240:243], v[200:203]\n\t" \
    "v_mfma_f32_16x16x32_bf16 v[216:219], v[160:163], v[224:227], v[216:219]\n\t" \
    "ds_read_b128 v[236:239], v253 offset:13312\n\t" \
    "s_waitcnt lgkmcnt(2)\n\t" \
    "v_mfma_f32_16x16x32_bf16 v[216:219], v[164:167], v[228:231], v[216:219]\n\t" \
    "ds_read_b128 v[240:243], v253 offset:18432\n\t" \
    "v_mfma_f32_16x16x32_bf16 v[220:223], v[168:171], v[224:227], v[220:223]\n\t" \
    "v_mfma_f32_16x16x32_bf16 v[220:223], v[172:175], v[228:231], v[220:223]\n\t" \
    "v_mfma_f32_16x16x32_bf16 v[212:215], v[244:247], v[224:227], v[212:215]\n\t" \
    "v_mfma_f32_16x16x32_bf16 v[212:215], v[248:251], v[228:231], v[212:215]\n\t" \
    "s_waitcnt lgkmcnt(2)\n\t" \
    "v_mfma_f32_16x16x32_bf16 v[204:207], v[24:27], v[232:235], v[204:207]\n\t" \
    "ds_read_b128 v[224:227], v253 offset:19456\n\t" \
    "v_mov_b64 v[244:245], 0\n\t" \
    "s_waitcnt lgkmcnt(2)\n\t" \
    "v_mfma_f32_16x16x32_bf16 v[204:207], v[28:31], v[236:239], v[204:207]\n\t" \
    "ds_read_b128 v[228:231], v253 offset:32768\n\t" \
    "v_mov_b64 v[246:247], 0\n\t" \
    "v_mfma_f32_16x16x32_bf16 v[208:211], v[32:35], v[232:235], v[208:211]\n\t" \
    "v_mov_b64 v[248:249], 0\n\t" \
    "v_mfma_f32_16x16x32_bf16 v[208:211], v[36:39], v[236:239], v[208:211]\n\t" \
    "v_mov_b64 v[250:251], 0\n\t" \
    "v_mfma_f32_16x16x32_bf16 v[200:203], v[16:19], v[232:235], v[200:203]\n\t" \
    "v_mfma_f32_16x16x32_bf16 v[200:203], v[20:23], v[236:239], v[200:203]\n\t" \
    "s_waitcnt lgkmcnt(2)\n\t" \
    "v_mfma_f32_16x16x32_bf16 v[204:207], v[184:187], v[240:243], v[204:207]\n\t" \
    "ds_read_b128 v[232:235], v253 offset:33792\n\t" \
    "s_waitcnt lgkmcnt(2)\n\t" \
    "v_mfma_f32_16x16x32_bf16 v[204:207], v[188:191], v[224:227], v[204:207]\n\t" \
    "ds_read_b128 v[236:239], v253 offset:38912\n\t" \
    "v_mfma_f32_16x16x32_bf16 v[208:211], v[192:195], v[240:243], v[208:211]\n\t" \
    "v_mfma_f32_16x16x32_bf16 v[208:211], v[196:199], v[224:227], v[208:211]\n\t" \
    "v_mfma_f32_16x16x32_bf16 v[200:203], v[176:179], v[240:243], v[200:203]\n\t" \
    "v_mfma_f32_16x16x32_bf16 v[200:203], v[180:183], v[224:227], v[200:203]\n\t" \
    "ds_read_b128 v[240:243], v253 offset:39936\n\t" \
    "s_waitcnt lgkmcnt(3)\n\t" \
    "v_mfma_f32_16x16x32_bf16 v[216:219], v[24:27], v[228:231], v[216:219]\n\t" \
    "s_waitcnt lgkmcnt(2)\n\t" \
    "v_mfma_f32_16x16x32_bf16 v[216:219], v[28:31], v[232:235], v[216:219]\n\t" \
    "v_cvt_pk_bf16_f32 v204, v204, v208\n\t" \
    "v_cvt_pk_bf16_f32 v205, v205, v209\n\t" \
    "s_waitcnt lgkmcnt(1)\n\t" \
    "v_mfma_f32_16x16x32_bf16 v[216:219], v[184:187], v[236:239], v[216:219]\n\t" \
    "v_cvt_pk_bf16_f32 v206, v206, v210\n\t" \
    "v_cvt_pk_bf16_f32 v207, v207, v211\n\t" \
    "s_waitcnt lgkmcnt(0)\n\t" \
    "v_mfma_f32_16x16x32_bf16 v[216:219], v[188:191], v[240:243], v[216:219]\n\t" \
    "ds_write_b128 v254, v[204:207] offset:9216\n\t" \
    "v_mfma_f32_16x16x32_bf16 v[220:223], v[32:35], v[228:231], v[220:223]\n\t" \
    "v_mfma_f32_16x16x32_bf16 v[220:223], v[36:39], v[232:235], v[220:223]\n\t" \
    "v_cvt_pk_bf16_f32 v200, v200, v201\n\t" \
    "v_cvt_pk_bf16_f32 v201, v202, v203\n\t" \
    "v_mfma_f32_16x16x32_bf16 v[220:223], v[192:195], v[236:239], v[220:223]\n\t" \
    "ds_write_b64 v254, v[200:201] offset:7168\n\t" \
    "v_mfma_f32_16x16x32_bf16 v[220:223], v[196:199], v[240:243], v[220:223]\n\t" \
    "v_mfma_f32_16x16x32_bf16 v[212:215], v[16:19], v[228:231], v[212:215]\n\t" \
    "ds_read_b128 v[200:203], v254 offset:40960\n\t" \
    "ds_read_b128 v[204:207], v254 offset:41984\n\t" \
    "v_mfma_f32_16x16x32_bf16 v[212:215], v[20:23], v[232:235], v[212:215]\n\t" \
    "v_mov_b64 v[208:209], 0\n\t" \
    "v_mov_b64 v[210:211], 0\n\t" \
    "v_mfma_f32_16x16x32_bf16 v[212:215], v[176:179], v[236:239], v[212:215]\n\t" \
    "v_cvt_pk_bf16_f32 v216, v216, v220\n\t" \
    "v_cvt_pk_bf16_f32 v217, v217, v221\n\t" \
    "v_mfma_f32_16x16x32_bf16 v[212:215], v[180:183], v[240:243], v[212:215]\n\t" \
    "v_cvt_pk_bf16_f32 v218, v218, v222\n\t" \
    "v_cvt_pk_bf16_f32 v219, v219, v223\n\t" \
    "s_setprio 0\n\t" \
    "ds_write_b128 v254, v[216:219] offset:15360\n\t" \
    "ds_read_b128 v[216:219], v254 offset:43008\n\t" \
    "ds_read_b128 v[220:223], v254 offset:44032\n\t" \
    "s_nop 2\n\t" \
    "v_cvt_pk_bf16_f32 v212, v212, v213\n\t" \
    "v_cvt_pk_bf16_f32 v213, v214, v215\n\t" \
    "ds_write_b64 v254, v[212:213] offset:13312\n\t" \
    LST6 \
    LJIT \
    "s_sleep 3\n\t" \
    "s_mov_b32 m0, 0xa800\n\t" \
    "s_nop 0\n\t" \
    "buffer_load_dwordx4 v253, s[60:63], s46 offen sc1 lds\n\t" \
    "s_mov_b32 m0, 0xb800\n\t" \
    "s_nop 0\n\t" \
    "buffer_load_dwordx4 v253, s[60:63], s47 offen sc1 lds\n\t" \
    "s_mov_b32 m0, 0xac00\n\t" \
    "s_nop 0\n\t" \
    "buffer_load_dwordx4 v253, s[60:63], s48 offen sc1 lds\n\t" \
    "s_waitcnt lgkmcnt(0)\n\t" \
    LFLUSH212 \
    "v_bfe_i32 v212, v253, 5, 1\n\t" \
    "v_and_b32 v212, 0xaaaa, v212\n\t" \
    "v_or_b32 v212, 0x4444, v212\n\t" \
    "v_and_b32 v213, 0xffff, v255\n\t" \
    "v_lshrrev_b32 v214, 16, v255\n\t" \
    ".Ls14_%=:\n\t" \
    "s_waitcnt vmcnt(0)\n\t" \
    "ds_read_b128 v[224:227], v253 offset:43008\n\t" \
    "ds_read_b128 v[228:231], v253 offset:47104\n\t" \
    "ds_read_b128 v[232:235], v253 offset:44032\n\t" \
    "s_waitcnt lgkmcnt(2)\n\t" \
    "v_max_u32 v215, v224, v225\n\t" \
    "v_max3_u32 v215, v226, v227, v215\n\t" \
    "s_waitcnt lgkmcnt(1)\n\t" \
    "v_max3_u32 v215, v228, v229, v215\n\t" \
    "v_max3_u32 v215, v230, v231, v215\n\t" \
    "s_waitcnt lgkmcnt(0)\n\t" \
    "v_max3_u32 v215, v232, v233, v215\n\t" \
    "v_max3_u32 v215, v234, v235, v215\n\t" \
    "v_cmp_eq_u32 vcc, -1, v215\n\t" \
    "s_cbranch_vccz .Ls15_%=\n\t" \
    "v_max_u32 v215, v224, v225\n\t" \
    "v_max3_u32 v215, v226, v227, v215\n\t" \
    "v_cmp_eq_u32 vcc, -1, v215\n\t" \
    "s_cbranch_vccz .Ls16_%=\n\t" \
    "s_mov_b32 m0, 0xa800\n\t" \
    "s_nop 0\n\t" \
    "buffer_load_dwordx4 v253, s[60:63], s46 offen sc1 lds\n\t" \
    ".Ls16_%=:\n\t" \
    "v_max_u32 v215, v228, v229\n\t" \
    "v_max3_u32 v215, v230, v231, v215\n\t" \
    "v_cmp_eq_u32 vcc, -1, v215\n\t" \
    "s_cbranch_vccz .Ls17_%=\n\t" \
    "s_mov_b32 m0, 0xb800\n\t" \
    "s_nop 0\n\t" \
    "buffer_load_dwordx4 v253, s[60:63], s47 offen sc1 lds\n\t" \
    ".Ls17_%=:\n\t" \
    "v_max_u32 v215, v232, v233\n\t" \
    "v_max3_u32 v215, v234, v235, v215\n\t" \
    "v_cmp_eq_u32 vcc, -1, v215\n\t" \
    "s_cbranch_vccz .Ls18_%=\n\t" \
    "s_mov_b32 m0, 0xac00\n\t" \
    "s_nop 0\n\t" \
    "buffer_load_dwordx4 v253, s[60:63], s48 offen sc1 lds\n\t" \
    ".Ls18_%=:\n\t" \
    "s_cmp_eq_u32 s41, 0\n\t" \
    "s_cbranch_scc1 .Ls15_%=\n\t" \
    "s_sub_u32 s41, s41, 1\n\t" \
    "s_branch .Ls14_%=\n\t" \
    ".Ls15_%=:\n\t" \
    LST2 \
    LJIT \
    "s_setprio 3\n\t" \
    "s_waitcnt lgkmcnt(0)\n\t" \
    "s_barrier \n\t" \
    LST3 \
    LJIT \
    "ds_read_b128 v[236:239], v253 offset:40960\n\t" \
    "ds_read_b128 v[240:243], v253 offset:46080\n\t" \
    "v_smfmac_f32_16x16x64_bf16 v[244:247], v[224:227], v[0:7], v212\n\t" \
    "v_smfmac_f32_16x16x64_bf16 v[248:251], v[224:227], v[8:15], v212\n\t" \
    "v_smfmac_f32_16x16x64_bf16 v[208:211], v[224:227], v[200:207], v212\n\t" \
    "ds_read_b128 v[200:203], v254 offset:45056\n\t" \
    "ds_read_b128 v[204:207], v254 offset:46080\n\t" \
    "ds_read_b128 v[224:227], v253 offset:41984\n\t" \
    "v_smfmac_f32_16x16x64_bf16 v[208:211], v[228:231], v[16:23], v212\n\t" \
    "v_smfmac_f32_16x16x64_bf16 v[244:247], v[228:231], v[24:31], v212\n\t" \
    "v_smfmac_f32_16x16x64_bf16 v[248:251], v[228:231], v[32:39], v212\n\t" \
    "ds_read_b128 v[228:231], v253 offset:48128\n\t" \
    "v_smfmac_f32_16x16x64_bf16 v[208:211], v[232:235], v[216:223], v212\n\t" \
    "ds_read_b128 v[216:219], v254 offset:47104\n\t" \
    "ds_read_b128 v[220:223], v254 offset:48128\n\t" \
    "v_smfmac_f32_16x16x64_bf16 v[244:247], v[232:235], v[40:47], v212\n\t" \
    "v_smfmac_f32_16x16x64_bf16 v[248:251], v[232:235], v[48:55], v212\n\t" \
    "ds_read_b128 v[232:235], v253 offset:45056\n\t" \
    "s_waitcnt lgkmcnt(8)\n\t" \
    "v_smfmac_f32_16x16x64_bf16 v[208:211], v[236:239], v[56:63], v212\n\t" \
    "v_smfmac_f32_16x16x64_bf16 v[244:247], v[236:239], v[64:71], v212\n\t" \
    "v_smfmac_f32_16x16x64_bf16 v[248:251], v[236:239], v[72:79], v212\n\t" \
    "ds_read_b128 v[236:239], v253 offset:49152\n\t" \
    "s_waitcnt lgkmcnt(6)\n\t" \
    "v_smfmac_f32_16x16x64_bf16 v[208:211], v[240:243], v[200:207], v212\n\t" \
    "ds_read_b128 v[200:203], v254 offset:49152\n\t" \
    "ds_read_b128 v[204:207], v254 offset:50176\n\t" \
    "v_smfmac_f32_16x16x64_bf16 v[244:247], v[240:243], v[80:87], v212\n\t" \
    "v_smfmac_f32_16x16x64_bf16 v[248:251], v[240:243], v[88:95], v212\n\t" \
    "ds_read_b128 v[240:243], v253 offset:50176\n\t" \
    "s_waitcnt lgkmcnt(8)\n\t" \
    "v_smfmac_f32_16x16x64_bf16 v[208:211], v[224:227], v[96:103], v212\n\t" \
    "v_smfmac_f32_16x16x64_bf16 v[244:247], v[224:227], v[104:111], v212\n\t" \
    "v_smfmac_f32_16x16x64_bf16 v[248:251], v[224:227], v[112:119], v212\n\t" \
    "s_waitcnt lgkmcnt(5)\n\t" \
    "v_smfmac_f32_16x16x64_bf16 v[208:211], v[228:231], v[216:223], v212\n\t" \
    "s_waitcnt lgkmcnt(4)\n\t" \
    "v_smfmac_f32_16x16x64_bf16 v[208:211], v[232:235], v[136:143], v212\n\t" \
    "s_waitcnt lgkmcnt(1)\n\t" \
    "v_smfmac_f32_16x16x64_bf16 v[208:211], v[236:239], v[200:207], v212\n\t" \
    "s_waitcnt lgkmcnt(0)\n\t" \
    "v_smfmac_f32_16x16x64_bf16 v[208:211], v[240:243], v[176:183], v212\n\t" \
    "v_smfmac_f32_16x16x64_bf16 v[244:247], v[228:231], v[120:127], v212\n\t" \
    "v_smfmac_f32_16x16x64_bf16 v[244:247], v[232:235], v[144:151], v212\n\t" \
    "v_smfmac_f32_16x16x64_bf16 v[244:247], v[236:239], v[160:167], v212\n\t" \
    "v_add_f32 v208, v208, v210\n\t" \
    "v_add_f32 v209, v209, v211\n\t" \
    "v_cvt_pk_bf16_f32 v208, v208, v209\n\t" \
    "v_smfmac_f32_16x16x64_bf16 v[244:247], v[240:243], v[184:191], v212\n\t" \
    "ds_write_b16 v213, v208 offset:0\n\t" \
    "ds_write_b16_d16_hi v213, v208 offset:16\n\t" \
    "v_smfmac_f32_16x16x64_bf16 v[248:251], v[228:231], v[128:135], v212\n\t" \
    "v_smfmac_f32_16x16x64_bf16 v[248:251], v[232:235], v[152:159], v212\n\t" \
    "v_smfmac_f32_16x16x64_bf16 v[248:251], v[236:239], v[168:175], v212\n\t" \
    "v_add_f32 v244, v244, v246\n\t" \
    "v_add_f32 v245, v245, v247\n\t" \
    "v_smfmac_f32_16x16x64_bf16 v[248:251], v[240:243], v[192:199], v212\n\t" \
    "s_nop 7\n\t" \
    "v_add_f32 v248, v248, v250\n\t" \
    "v_add_f32 v249, v249, v251\n\t" \
    "v_cvt_pk_bf16_f32 v244, v244, v248\n\t" \
    "v_cvt_pk_bf16_f32 v245, v245, v249\n\t" \
    "ds_write2_b32 v214, v244, v245 offset1:4\n\t" \
    "s_setprio 0\n\t" \
    "s_sleep 1\n\t" \
    "ds_read_b128 v[200:203], v254 offset:6144\n\t" \
    "ds_read_b128 v[224:227], v254 offset:18432\n\t" \
    "ds_read_b128 v[204:207], v254 offset:7168\n\t" \
    "ds_read_b128 v[228:231], v254 offset:19456\n\t" \
    "ds_read_b128 v[208:211], v254 offset:8192\n\t" \
    "ds_read_b128 v[232:235], v254 offset:20480\n\t" \
    "ds_read_b128 v[212:215], v254 offset:9216\n\t" \
    "ds_read_b128 v[236:239], v254 offset:21504\n\t" \
    "ds_read_b128 v[216:219], v254 offset:10240\n\t" \
    "ds_read_b128 v[220:223], v254 offset:11264\n\t" \
    "s_and_b32 s44, s40, 3\n\t" \
    "s_lshl_b32 s44, s44, 17\n\t" \
    "s_add_u32 s50, s44, %[pb]\n\t" \
    "s_add_u32 s51, s44, %[pm]\n\t" \
    "s_mov_b32 s52, 0xf0000\n\t" \
    "s_mov_b32 s53, 0xfff00ff\n\t" \
    "s_mov_b32 s54, 0x1f0001\n\t" \
    "s_mov_b32 s55, 0x1fff01ff\n\t" \
    "s_mov_b32 s56, 0x3f0003\n\t" \
    "s_mov_b32 s57, 0x3fff03ff\n\t" \
    "s_mov_b32 s58, 0x7f0007\n\t" \
    "s_mov_b32 s59, 0x7fff07ff\n\t" \
    "s_waitcnt lgkmcnt(8)\n\t" \
    "v_mfma_f32_16x16x32_bf16 v[240:243], v[224:227], v[200:203], 0\n\t" \
    "ds_read_b128 v[224:227], v254 offset:22528\n\t" \
    "s_waitcnt lgkmcnt(7)\n\t" \
    "v_mfma_f32_16x16x32_bf16 v[240:243], v[228:231], v[204:207], v[240:243]\n\t" \
    "ds_read_b128 v[228:231], v254 offset:23552\n\t" \
    "s_waitcnt lgkmcnt(6)\n\t" \
    "v_mfma_f32_16x16x32_bf16 v[240:243], v[232:235], v[208:211], v[240:243]\n\t" \
    "s_waitcnt lgkmcnt(4)\n\t" \
    "v_mfma_f32_16x16x32_bf16 v[240:243], v[236:239], v[212:215], v[240:243]\n\t" \
    "v_and_b32 v252, 0xffffff7f, v254\n\t" \
    "s_waitcnt lgkmcnt(0)\n\t" \
    "s_barrier \n\t" \
    LST4 \
    LJIT \
    "ds_read_b128 v[232:235], v252 offset:3072\n\t" \
    "ds_read_b128 v[236:239], v252 offset:3200\n\t" \
    "v_mfma_f32_16x16x32_bf16 v[240:243], v[224:227], v[216:219], v[240:243]\n\t" \
    "ds_read_b128 v[224:227], v252 offset:4096\n\t" \
    "v_mfma_f32_16x16x32_bf16 v[240:243], v[228:231], v[220:223], v[240:243]\n\t" \
    "ds_read_b128 v[228:231], v252 offset:4224\n\t" \
    "s_waitcnt lgkmcnt(3)\n\t" \
    "v_mfma_f32_16x16x32_bf16 v[248:251], v[232:235], v[200:203], 0\n\t" \
    "ds_read_b128 v[232:235], v252 offset:5120\n\t" \
    "s_waitcnt lgkmcnt(3)\n\t" \
    "v_mfma_f32_16x16x32_bf16 v[248:251], v[236:239], v[204:207], v[248:251]\n\t" \
    "ds_read_b128 v[236:239], v252 offset:5248\n\t" \
    "s_waitcnt lgkmcnt(3)\n\t" \
    "v_mfma_f32_16x16x32_bf16 v[248:251], v[224:227], v[208:211], v[248:251]\n\t" \
    "s_waitcnt lgkmcnt(2)\n\t" \
    "v_mfma_f32_16x16x32_bf16 v[248:251], v[228:231], v[212:215], v[248:251]\n\t" \
    "s_waitcnt lgkmcnt(1)\n\t" \
    "v_mfma_f32_16x16x32_bf16 v[248:251], v[232:235], v[216:219], v[248:251]\n\t" \
    "s_waitcnt lgkmcnt(0)\n\t" \
    "v_mfma_f32_16x16x32_bf16 v[248:251], v[236:239], v[220:223], v[248:251]\n\t" \
    "v_mov_b32 v226, 0xff7fffff\n\t" \
    "v_cndmask_b32 v240, v240, v226, s[52:53]\n\t" \
    "v_cndmask_b32 v241, v241, v226, s[54:55]\n\t" \
    "v_cndmask_b32 v242, v242, v226, s[56:57]\n\t" \
    "v_cndmask_b32 v243, v243, v226, s[58:59]\n\t" \
    "v_max3_f32 v224, v240, v241, v242\n\t" \
    "v_mov_b32 v225, v243\n\t" \
    "v_lshrrev_b32 v200, 5, v253\n\t" \
    "v_and_b32 v201, 0xf0, v253\n\t" \
    "v_and_or_b32 v200, v200, 8, v201\n\t" \
    "v_max3_f32 v224, v224, v248, v249\n\t" \
    "v_max3_f32 v225, v225, v250, v251\n\t" \
    "v_max_f32 v224, v224, v225\n\t" \
    "v_mov_b32 v225, v224\n\t" \
    "s_nop 1\n\t" \
    "v_permlane16_swap_b32 v224, v225\n\t" \
    "v_max_f32 v224, v224, v225\n\t" \
    "v_mov_b32 v225, v224\n\t" \
    "s_nop 1\n\t" \
    "v_permlane32_swap_b32 v224, v225\n\t" \
    "v_max_f32 v224, v224, v225\n\t" \
    "v_mul_f32 v224, %[scl], v224\n\t" \
    "v_fma_f32 v240, v240, %[scl], -v224\n\t" \
    "v_fma_f32 v241, v241, %[scl], -v224\n\t" \
    "v_exp_f32 v240, v240\n\t" \
    "v_fma_f32 v242, v242, %[scl], -v224\n\t" \
    "v_exp_f32 v241, v241\n\t" \
    "v_fma_f32 v243, v243, %[scl], -v224\n\t" \
    "v_exp_f32 v242, v242\n\t" \
    "v_fma_f32 v248, v248, %[scl], -v224\n\t" \
    "v_exp_f32 v243, v243\n\t" \
    "v_fma_f32 v249, v249, %[scl], -v224\n\t" \
    "v_exp_f32 v248, v248\n\t" \
    "v_fma_f32 v250, v250, %[scl], -v224\n\t" \
    "v_exp_f32 v249, v249\n\t" \
    "v_fma_f32 v251, v251, %[scl], -v224\n\t" \
    "v_exp_f32 v250, v250\n\t" \
    "v_exp_f32 v251, v251\n\t" \
    "v_add_f32 v224, v240, v241\n\t" \
    "v_add_f32 v225, v242, v243\n\t" \
    "v_add_f32 v232, v248, v249\n\t" \
    "v_add_f32 v233, v250, v251\n\t" \
    "v_add_f32 v224, v224, v225\n\t" \
    "v_add_f32 v232, v232, v233\n\t" \
    "v_fma_f32 v224, v232, 0.5, v224\n\t" \
    "v_mov_b32 v225, v224\n\t" \
    "s_nop 1\n\t" \
    "v_permlane16_swap_b32 v224, v225\n\t" \
    "v_add_f32 v224, v224, v225\n\t" \
    "v_mov_b32 v225, v224\n\t" \
    "s_nop 1\n\t" \
    "v_permlane32_swap_b32 v224, v225\n\t" \
    "v_add_f32 v224, v224, v225\n\t" \
    "v_rcp_f32 v224, v224\n\t" \
    "s_nop 0\n\t" \
    "v_mul_f32 v240, v240, v224\n\t" \
    "v_mul_f32 v241, v241, v224\n\t" \
    "v_mul_f32 v242, v242, v224\n\t" \
    "v_mul_f32 v243, v243, v224\n\t" \
    "v_mul_f32 v248, v248, v224\n\t" \
    "v_mul_f32 v249, v249, v224\n\t" \
    "v_mul_f32 v250, v250, v224\n\t" \
    "v_mul_f32 v251, v251, v224\n\t" \
    "v_cvt_pk_bf16_f32 v204, |v240|, |v241|\n\t" \
    "v_cvt_pk_bf16_f32 v205, |v242|, |v243|\n\t" \
    "v_cvt_pk_bf16_f32 v208, |v248|, |v249|\n\t" \
    "v_cvt_pk_bf16_f32 v209, |v250|, |v251|\n\t" \
    "v_permlane16_swap_b32 v204, v206\n\t" \
    "v_permlane16_swap_b32 v205, v207\n\t" \
    "s_mov_b32 exec_lo, 0xffff\n\t" \
    "s_mov_b32 exec_hi, 0xffff\n\t" \
    "buffer_store_dwordx4 v[204:207], v253, s[60:63], s50 offen sc1\n\t" \
    "s_mov_b32 exec_lo, -1\n\t" \
    "s_mov_b32 exec_hi, 0\n\t" \
    "buffer_store_dwordx2 v[208:209], v200, s[60:63], s51 offen sc1\n\t" \
    "s_mov_b32 exec_hi, -1\n\t" \
    LST5 \
    LJIT \
    "ds_read_b128 v[224:227], v253 offset:0\n\t" \
    "ds_read_b128 v[244:247], v254 offset:40960\n\t" \
    "ds_read_b128 v[228:231], v253 offset:1024\n\t" \
    "ds_read_b128 v[232:235], v253 offset:20480\n\t" \
    "ds_read_b128 v[236:239], v253 offset:21504\n\t" \
    "ds_read_b128 v[248:251], v254 offset:41984\n\t" \
    "s_add_u32 s40, s40, 1\n\t" \
    "s_cmp_lg_u32 s40, 64\n\t" \
    "s_cbranch_scc1 .Ls13_%=\n\t" \
    "s_branch .Lend%=\n\t" \
    ".Ls0_%=:\n\t" \
    "v_add_u32 v244, 0x18000, v243\n\t" \
    "s_waitcnt vmcnt(12) lgkmcnt(0)\n\t" \
    "s_barrier \n\t" \
    "s_add_u32 m0, %[wm], 0x12000\n\t" \
    "s_mov_b32 s44, 0x1e0000\n\t" \
    "buffer_load_dwordx4 v240, %[wr], s44 offen offset:0 nt lds\n\t" \
    "buffer_load_dwordx4 v241, %[wr], s44 offen offset:1024 nt lds\n\t" \
    "buffer_load_dwordx4 v242, %[wr], s44 offen offset:2048 nt lds\n\t" \
    "ds_read_b64_tr_b16 v[200:201], v244 offset:0\n\t" \
    "ds_read_b64_tr_b16 v[202:203], v244 offset:1536\n\t" \
    "ds_read_b64_tr_b16 v[0:1], v244 offset:32\n\t" \
    "ds_read_b64_tr_b16 v[2:3], v244 offset:1568\n\t" \
    "ds_read_b64_tr_b16 v[8:9], v244 offset:64\n\t" \
    "ds_read_b64_tr_b16 v[10:11], v244 offset:1600\n\t" \
    "v_add_u32 v244, 0x1e000, v243\n\t" \
    "s_waitcnt vmcnt(12) lgkmcnt(0)\n\t" \
    "s_barrier \n\t" \
    "s_add_u32 m0, %[wm], 0x18000\n\t" \
    "s_mov_b32 s44, 0x240000\n\t" \
    "buffer_load_dwordx4 v240, %[wr], s44 offen offset:0 nt lds\n\t" \
    "buffer_load_dwordx4 v241, %[wr], s44 offen offset:1024 nt lds\n\t" \
    "buffer_load_dwordx4 v242, %[wr], s44 offen offset:2048 nt lds\n\t" \
    "ds_read_b64_tr_b16 v[204:205], v244 offset:0\n\t" \
    "ds_read_b64_tr_b16 v[206:207], v244 offset:1536\n\t" \
    "ds_read_b64_tr_b16 v[4:5], v244 offset:32\n\t" \
    "ds_read_b64_tr_b16 v[6:7], v244 offset:1568\n\t" \
    "ds_read_b64_tr_b16 v[12:13], v244 offset:64\n\t" \
    "ds_read_b64_tr_b16 v[14:15], v244 offset:1600\n\t" \
    "v_add_u32 v244, 0x0, v243\n\t" \
    "s_waitcnt vmcnt(12) lgkmcnt(0)\n\t" \
    "s_barrier \n\t" \
    "s_add_u32 m0, %[wm], 0x1e000\n\t" \
    "s_mov_b32 s44, 0x2a0000\n\t" \
    "buffer_load_dwordx4 v240, %[wr], s44 offen offset:0 nt lds\n\t" \
    "buffer_load_dwordx4 v241, %[wr], s44 offen offset:1024 nt lds\n\t" \
    "buffer_load_dwordx4 v242, %[wr], s44 offen offset:2048 nt lds\n\t" \
    "ds_read_b64_tr_b16 v[208:209], v244 offset:0\n\t" \
    "ds_read_b64_tr_b16 v[210:211], v244 offset:1536\n\t" \
    "ds_read_b64_tr_b16 v[40:41], v244 offset:32\n\t" \
    "ds_read_b64_tr_b16 v[42:43], v244 offset:1568\n\t" \
    "ds_read_b64_tr_b16 v[48:49], v244 offset:64\n\t" \
    "ds_read_b64_tr_b16 v[50:51], v244 offset:1600\n\t" \
    "v_add_u32 v244, 0x6000, v243\n\t" \
    "s_waitcnt vmcnt(12) lgkmcnt(0)\n\t" \
    "s_barrier \n\t" \
    "s_add_u32 m0, %[wm], 0x0\n\t" \
    "s_mov_b32 s44, 0x300000\n\t" \
    "buffer_load_dwordx4 v240, %[wr], s44 offen offset:0 nt lds\n\t" \
    "buffer_load_dwordx4 v241, %[wr], s44 offen offset:1024 nt lds\n\t" \
    "buffer_load_dwordx4 v242, %[wr], s44 offen offset:2048 nt lds\n\t" \
    "ds_read_b64_tr_b16 v[212:213], v244 offset:0\n\t" \
    "ds_read_b64_tr_b16 v[214:215], v244 offset:1536\n\t" \
    "ds_read_b64_tr_b16 v[44:45], v244 offset:32\n\t" \
    "ds_read_b64_tr_b16 v[46:47], v244 offset:1568\n\t" \
    "ds_read_b64_tr_b16 v[52:53], v244 offset:64\n\t" \
    "ds_read_b64_tr_b16 v[54:55], v244 offset:1600\n\t" \
    "v_add_u32 v244, 0xc000, v243\n\t" \
    "s_waitcnt vmcnt(12) lgkmcnt(0)\n\t" \
    "s_barrier \n\t" \
    "s_add_u32 m0, %[wm], 0x6000\n\t" \
    "s_mov_b32 s44, 0x360000\n\t" \
    "buffer_load_dwordx4 v240, %[wr], s44 offen offset:0 nt lds\n\t" \
    "buffer_load_dwordx4 v241, %[wr], s44 offen offset:1024 nt lds\n\t" \
    "buffer_load_dwordx4 v242, %[wr], s44 offen offset:2048 nt lds\n\t" \
    "ds_read_b64_tr_b16 v[216:217], v244 offset:0\n\t" \
    "ds_read_b64_tr_b16 v[218:219], v244 offset:1536\n\t" \
    "ds_read_b64_tr_b16 v[80:81], v244 offset:32\n\t" \
    "ds_read_b64_tr_b16 v[82:83], v244 offset:1568\n\t" \
    "ds_read_b64_tr_b16 v[88:89], v244 offset:64\n\t" \
    "ds_read_b64_tr_b16 v[90:91], v244 offset:1600\n\t" \
    "v_add_u32 v244, 0x12000, v243\n\t" \
    "s_waitcnt vmcnt(12) lgkmcnt(0)\n\t" \
    "s_barrier \n\t" \
    "s_add_u32 m0, %[wm], 0xc000\n\t" \
    "s_mov_b32 s44, 0x3c0000\n\t" \
    "buffer_load_dwordx4 v240, %[wr], s44 offen offset:0 nt lds\n\t" \
    "buffer_load_dwordx4 v241, %[wr], s44 offen offset:1024 nt lds\n\t" \
    "buffer_load_dwordx4 v242, %[wr], s44 offen offset:2048 nt lds\n\t" \
    "ds_read_b64_tr_b16 v[220:221], v244 offset:0\n\t" \
    "ds_read_b64_tr_b16 v[222:223], v244 offset:1536\n\t" \
    "ds_read_b64_tr_b16 v[84:85], v244 offset:32\n\t" \
    "ds_read_b64_tr_b16 v[86:87], v244 offset:1568\n\t" \
    "ds_read_b64_tr_b16 v[92:93], v244 offset:64\n\t" \
    "ds_read_b64_tr_b16 v[94:95], v244 offset:1600\n\t" \
    "v_add_u32 v244, 0x18000, v243\n\t" \
    "s_waitcnt vmcnt(12) lgkmcnt(0)\n\t" \
    "s_barrier \n\t" \
    "s_add_u32 m0, %[wm], 0x12000\n\t" \
    "s_mov_b32 s44, 0x420000\n\t" \
    "buffer_load_dwordx4 v240, %[wr], s44 offen offset:0 nt lds\n\t" \
    "buffer_load_dwordx4 v241, %[wr], s44 offen offset:1024 nt lds\n\t" \
    "buffer_load_dwordx4 v242, %[wr], s44 offen offset:2048 nt lds\n\t" \
    "ds_read_b64_tr_b16 v[224:225], v244 offset:0\n\t" \
    "ds_read_b64_tr_b16 v[226:227], v244 offset:1536\n\t" \
    "ds_read_b64_tr_b16 v[120:121], v244 offset:32\n\t" \
    "ds_read_b64_tr_b16 v[122:123], v244 offset:1568\n\t" \
    "ds_read_b64_tr_b16 v[128:129], v244 offset:64\n\t" \
    "ds_read_b64_tr_b16 v[130:131], v244 offset:1600\n\t" \
    "v_add_u32 v244, 0x1e000, v243\n\t" \
    "s_waitcnt vmcnt(12) lgkmcnt(0)\n\t" \
    "s_barrier \n\t" \
    "s_add_u32 m0, %[wm], 0x18000\n\t" \
    "s_mov_b32 s44, 0x480000\n\t" \
    "buffer_load_dwordx4 v240, %[wr], s44 offen offset:0 nt lds\n\t" \
    "buffer_load_dwordx4 v241, %[wr], s44 offen offset:1024 nt lds\n\t" \
    "buffer_load_dwordx4 v242, %[wr], s44 offen offset:2048 nt lds\n\t" \
    "ds_read_b64_tr_b16 v[228:229], v244 offset:0\n\t" \
    "ds_read_b64_tr_b16 v[230:231], v244 offset:1536\n\t" \
    "ds_read_b64_tr_b16 v[124:125], v244 offset:32\n\t" \
    "ds_read_b64_tr_b16 v[126:127], v244 offset:1568\n\t" \
    "ds_read_b64_tr_b16 v[132:133], v244 offset:64\n\t" \
    "ds_read_b64_tr_b16 v[134:135], v244 offset:1600\n\t" \
    "v_add_u32 v244, 0x0, v243\n\t" \
    "s_waitcnt vmcnt(12) lgkmcnt(0)\n\t" \
    "s_barrier \n\t" \
    "v_mov_b32 v246, 0\n\t" \
    "buffer_load_dword v246, v246, s[60:63], %[mo] offen sc1\n\t" \
    "s_add_u32 m0, %[wm], 0x1e000\n\t" \
    "s_mov_b32 s44, 0x4e0000\n\t" \
    "buffer_load_dwordx4 v240, %[wr], s44 offen offset:0 nt lds\n\t" \
    "buffer_load_dwordx4 v241, %[wr], s44 offen offset:1024 nt lds\n\t" \
    "buffer_load_dwordx4 v242, %[wr], s44 offen offset:2048 nt lds\n\t" \
    "ds_read_b64_tr_b16 v[232:233], v244 offset:0\n\t" \
    "ds_read_b64_tr_b16 v[234:235], v244 offset:1536\n\t" \
    "ds_read_b64_tr_b16 v[160:161], v244 offset:32\n\t" \
    "ds_read_b64_tr_b16 v[162:163], v244 offset:1568\n\t" \
    "ds_read_b64_tr_b16 v[168:169], v244 offset:64\n\t" \
    "ds_read_b64_tr_b16 v[170:171], v244 offset:1600\n\t" \
    "v_add_u32 v244, 0x6000, v243\n\t" \
    "s_waitcnt vmcnt(12) lgkmcnt(0)\n\t" \
    "s_barrier \n\t" \
    "s_add_u32 m0, %[wm], 0x0\n\t" \
    "s_mov_b32 s44, 0x540000\n\t" \
    "buffer_load_dwordx4 v240, %[wr], s44 offen offset:0 nt lds\n\t" \
    "buffer_load_dwordx4 v241, %[wr], s44 offen offset:1024 nt lds\n\t" \
    "buffer_load_dwordx4 v242, %[wr], s44 offen offset:2048 nt lds\n\t" \
    "ds_read_b64_tr_b16 v[236:237], v244 offset:0\n\t" \
    "ds_read_b64_tr_b16 v[238:239], v244 offset:1536\n\t" \
    "ds_read_b64_tr_b16 v[164:165], v244 offset:32\n\t" \
    "ds_read_b64_tr_b16 v[166:167], v244 offset:1568\n\t" \
    "ds_read_b64_tr_b16 v[172:173], v244 offset:64\n\t" \
    "ds_read_b64_tr_b16 v[174:175], v244 offset:1600\n\t" \
    "v_add_u32 v244, 0xc000, v243\n\t" \
    "s_waitcnt vmcnt(12) lgkmcnt(0)\n\t" \
    "s_barrier \n\t" \
    "s_add_u32 m0, %[wm], 0x6000\n\t" \
    "s_mov_b32 s44, 0x5a0000\n\t" \
    "buffer_load_dwordx4 v240, %[wr], s44 offen offset:0 nt lds\n\t" \
    "buffer_load_dwordx4 v241, %[wr], s44 offen offset:1024 nt lds\n\t" \
    "buffer_load_dwordx4 v242, %[wr], s44 offen offset:2048 nt lds\n\t" \
    "ds_read_b64_tr_b16 v[16:17], v244 offset:0\n\t" \
    "ds_read_b64_tr_b16 v[18:19], v244 offset:1536\n\t" \
    "ds_read_b64_tr_b16 v[24:25], v244 offset:32\n\t" \
    "ds_read_b64_tr_b16 v[26:27], v244 offset:1568\n\t" \
    "ds_read_b64_tr_b16 v[32:33], v244 offset:64\n\t" \
    "ds_read_b64_tr_b16 v[34:35], v244 offset:1600\n\t" \
    "v_add_u32 v244, 0x12000, v243\n\t" \
    "s_waitcnt vmcnt(12) lgkmcnt(0)\n\t" \
    "s_barrier \n\t" \
    "s_add_u32 m0, %[wm], 0xc000\n\t" \
    "s_mov_b32 s44, 0x600000\n\t" \
    "buffer_load_dwordx4 v240, %[wr], s44 offen offset:0 nt lds\n\t" \
    "buffer_load_dwordx4 v241, %[wr], s44 offen offset:1024 nt lds\n\t" \
    "buffer_load_dwordx4 v242, %[wr], s44 offen offset:2048 nt lds\n\t" \
    "ds_read_b64_tr_b16 v[20:21], v244 offset:0\n\t" \
    "ds_read_b64_tr_b16 v[22:23], v244 offset:1536\n\t" \
    "ds_read_b64_tr_b16 v[28:29], v244 offset:32\n\t" \
    "ds_read_b64_tr_b16 v[30:31], v244 offset:1568\n\t" \
    "ds_read_b64_tr_b16 v[36:37], v244 offset:64\n\t" \
    "ds_read_b64_tr_b16 v[38:39], v244 offset:1600\n\t" \
    "v_add_u32 v244, 0x18000, v243\n\t" \
    "s_waitcnt vmcnt(12) lgkmcnt(0)\n\t" \
    "s_barrier \n\t" \
    "s_add_u32 m0, %[wm], 0x12000\n\t" \
    "s_mov_b32 s44, 0x660000\n\t" \
    "buffer_load_dwordx4 v240, %[wr], s44 offen offset:0 nt lds\n\t" \
    "buffer_load_dwordx4 v241, %[wr], s44 offen offset:1024 nt lds\n\t" \
    "buffer_load_dwordx4 v242, %[wr], s44 offen offset:2048 nt lds\n\t" \
    "ds_read_b64_tr_b16 v[56:57], v244 offset:0\n\t" \
    "ds_read_b64_tr_b16 v[58:59], v244 offset:1536\n\t" \
    "ds_read_b64_tr_b16 v[64:65], v244 offset:32\n\t" \
    "ds_read_b64_tr_b16 v[66:67], v244 offset:1568\n\t" \
    "ds_read_b64_tr_b16 v[72:73], v244 offset:64\n\t" \
    "ds_read_b64_tr_b16 v[74:75], v244 offset:1600\n\t" \
    "v_add_u32 v244, 0x1e000, v243\n\t" \
    "s_waitcnt vmcnt(12) lgkmcnt(0)\n\t" \
    "s_barrier \n\t" \
    ".Ls01_%=:\n\t" \
    "v_readfirstlane_b32 s43, v246\n\t" \
    "s_nop 0\n\t" \
    "s_cmp_lg_u32 s43, -1\n\t" \
    "s_cbranch_scc1 .Ls02_%=\n\t" \
    "s_cmp_eq_u32 s41, 0\n\t" \
    "s_cbranch_scc1 .Ls02_%=\n\t" \
    "s_sub_u32 s41, s41, 1\n\t" \
    "s_sleep 4\n\t" \
    "v_mov_b32 v246, 0\n\t" \
    "buffer_load_dword v246, v246, s[60:63], %[mo] offen sc1\n\t" \
    "s_waitcnt vmcnt(0)\n\t" \
    "s_branch .Ls01_%=\n\t" \
    ".Ls02_%=:\n\t" \
    "v_mov_b32 v246, 0x8000\n\t" \
    "s_mov_b64 exec, 1\n\t" \
    "buffer_store_dword v246, v246, s[60:63], %[mo] offen sc1\n\t" \
    "s_mov_b64 exec, -1\n\t" \
    "s_and_b32 s43, s43, 0x2000\n\t" \
    "s_xor_b32 s43, s43, %[lx]\n\t" \
    "s_add_u32 s60, s60, s43\n\t" \
    "s_addc_u32 s61, s61, 0\n\t" \
    "s_add_u32 m0, %[wm], 0x18000\n\t" \
    "s_mov_b32 s44, 0x6c0000\n\t" \
    "buffer_load_dwordx4 v240, %[wr], s44 offen offset:0 nt lds\n\t" \
    "buffer_load_dwordx4 v241, %[wr], s44 offen offset:1024 nt lds\n\t" \
    "buffer_load_dwordx4 v242, %[wr], s44 offen offset:2048 nt lds\n\t" \
    "ds_read_b64_tr_b16 v[60:61], v244 offset:0\n\t" \
    "ds_read_b64_tr_b16 v[62:63], v244 offset:1536\n\t" \
    "ds_read_b64_tr_b16 v[68:69], v244 offset:32\n\t" \
    "ds_read_b64_tr_b16 v[70:71], v244 offset:1568\n\t" \
    "ds_read_b64_tr_b16 v[76:77], v244 offset:64\n\t" \
    "ds_read_b64_tr_b16 v[78:79], v244 offset:1600\n\t" \
    "v_add_u32 v244, 0x0, v243\n\t" \
    "s_waitcnt vmcnt(12) lgkmcnt(0)\n\t" \
    "s_barrier \n\t" \
    "s_add_u32 m0, %[wm], 0x1e000\n\t" \
    "s_mov_b32 s44, 0x720000\n\t" \
    "buffer_load_dwordx4 v240, %[wr], s44 offen offset:0 nt lds\n\t" \
    "buffer_load_dwordx4 v241, %[wr], s44 offen offset:1024 nt lds\n\t" \
    "buffer_load_dwordx4 v242, %[wr], s44 offen offset:2048 nt lds\n\t" \
    "ds_read_b64_tr_b16 v[96:97], v244 offset:0\n\t" \
    "ds_read_b64_tr_b16 v[98:99], v244 offset:1536\n\t" \
    "ds_read_b64_tr_b16 v[104:105], v244 offset:32\n\t" \
    "ds_read_b64_tr_b16 v[106:107], v244 offset:1568\n\t" \
    "ds_read_b64_tr_b16 v[112:113], v244 offset:64\n\t" \
    "ds_read_b64_tr_b16 v[114:115], v244 offset:1600\n\t" \
    "v_add_u32 v244, 0x6000, v243\n\t" \
    "s_waitcnt vmcnt(12) lgkmcnt(0)\n\t" \
    "s_barrier \n\t" \
    "ds_read_b64_tr_b16 v[100:101], v244 offset:0\n\t" \
    "ds_read_b64_tr_b16 v[102:103], v244 offset:1536\n\t" \
    "ds_read_b64_tr_b16 v[108:109], v244 offset:32\n\t" \
    "ds_read_b64_tr_b16 v[110:111], v244 offset:1568\n\t" \
    "ds_read_b64_tr_b16 v[116:117], v244 offset:64\n\t" \
    "ds_read_b64_tr_b16 v[118:119], v244 offset:1600\n\t" \
    "v_add_u32 v244, 0xc000, v243\n\t" \
    "s_waitcnt vmcnt(9) lgkmcnt(0)\n\t" \
    "s_barrier \n\t" \
    "ds_read_b64_tr_b16 v[136:137], v244 offset:0\n\t" \
    "ds_read_b64_tr_b16 v[138:139], v244 offset:1536\n\t" \
    "ds_read_b64_tr_b16 v[144:145], v244 offset:32\n\t" \
    "ds_read_b64_tr_b16 v[146:147], v244 offset:1568\n\t" \
    "ds_read_b64_tr_b16 v[152:153], v244 offset:64\n\t" \
    "ds_read_b64_tr_b16 v[154:155], v244 offset:1600\n\t" \
    "v_add_u32 v244, 0x12000, v243\n\t" \
    "s_waitcnt vmcnt(6) lgkmcnt(0)\n\t" \
    "s_barrier \n\t" \
    "ds_read_b64_tr_b16 v[140:141], v244 offset:0\n\t" \
    "ds_read_b64_tr_b16 v[142:143], v244 offset:1536\n\t" \
    "ds_read_b64_tr_b16 v[148:149], v244 offset:32\n\t" \
    "ds_read_b64_tr_b16 v[150:151], v244 offset:1568\n\t" \
    "ds_read_b64_tr_b16 v[156:157], v244 offset:64\n\t" \
    "ds_read_b64_tr_b16 v[158:159], v244 offset:1600\n\t" \
    "v_add_u32 v244, 0x18000, v243\n\t" \
    "s_waitcnt vmcnt(3) lgkmcnt(0)\n\t" \
    "s_barrier \n\t" \
    "ds_read_b64_tr_b16 v[176:177], v244 offset:0\n\t" \
    "ds_read_b64_tr_b16 v[178:179], v244 offset:1536\n\t" \
    "ds_read_b64_tr_b16 v[184:185], v244 offset:32\n\t" \
    "ds_read_b64_tr_b16 v[186:187], v244 offset:1568\n\t" \
    "ds_read_b64_tr_b16 v[192:193], v244 offset:64\n\t" \
    "ds_read_b64_tr_b16 v[194:195], v244 offset:1600\n\t" \
    "v_add_u32 v244, 0x1e000, v243\n\t" \
    "s_waitcnt vmcnt(0) lgkmcnt(0)\n\t" \
    "s_barrier \n\t" \
    "ds_read_b64_tr_b16 v[180:181], v244 offset:0\n\t" \
    "ds_read_b64_tr_b16 v[182:183], v244 offset:1536\n\t" \
    "ds_read_b64_tr_b16 v[188:189], v244 offset:32\n\t" \
    "ds_read_b64_tr_b16 v[190:191], v244 offset:1568\n\t" \
    "ds_read_b64_tr_b16 v[196:197], v244 offset:64\n\t" \
    "ds_read_b64_tr_b16 v[198:199], v244 offset:1600\n\t" \
    "s_waitcnt lgkmcnt(0)\n\t" \
    "s_barrier \n\t" \
    LST0 \
    LJIT \
    "ds_write_b128 v254, v[200:203] offset:30720\n\t" \
    "ds_write_b128 v254, v[204:207] offset:31744\n\t" \
    "ds_write_b128 v254, v[208:211] offset:32768\n\t" \
    "ds_write_b128 v254, v[212:215] offset:33792\n\t" \
    "ds_write_b128 v254, v[216:219] offset:34816\n\t" \
    "ds_write_b128 v254, v[220:223] offset:35840\n\t" \
    "ds_write_b128 v254, v[224:227] offset:36864\n\t" \
    "ds_write_b128 v254, v[228:231] offset:37888\n\t" \
    "ds_write_b128 v254, v[232:235] offset:38912\n\t" \
    "ds_write_b128 v254, v[236:239] offset:39936\n\t" \
    "s_waitcnt vmcnt(0) lgkmcnt(0)\n\t" \
    "s_barrier \n\t" \
    "ds_read_b128 v[224:227], v253 offset:0\n\t" \
    "ds_read_b128 v[228:231], v253 offset:1024\n\t" \
    "ds_read_b128 v[232:235], v253 offset:20480\n\t" \
    "ds_read_b128 v[236:239], v253 offset:21504\n\t" \
    ".Ls03_%=:\n\t" \
    "s_waitcnt lgkmcnt(3)\n\t" \
    "v_mfma_f32_16x16x32_bf16 v[204:207], v[0:3], v[224:227], 0\n\t" \
    "s_waitcnt lgkmcnt(2)\n\t" \
    "v_mfma_f32_16x16x32_bf16 v[204:207], v[4:7], v[228:231], v[204:207]\n\t" \
    "ds_read_b128 v[240:243], v253 offset:2048\n\t" \
    "v_mfma_f32_16x16x32_bf16 v[208:211], v[8:11], v[224:227], 0\n\t" \
    "v_mfma_f32_16x16x32_bf16 v[208:211], v[12:15], v[228:231], v[208:211]\n\t" \
    "s_waitcnt lgkmcnt(2)\n\t" \
    "v_mfma_f32_16x16x32_bf16 v[216:219], v[0:3], v[232:235], 0\n\t" \
    "ds_read_b128 v[224:227], v253 offset:3072\n\t" \
    "s_waitcnt lgkmcnt(2)\n\t" \
    "v_mfma_f32_16x16x32_bf16 v[216:219], v[4:7], v[236:239], v[216:219]\n\t" \
    "ds_read_b128 v[228:231], v253 offset:22528\n\t" \
    "v_mfma_f32_16x16x32_bf16 v[220:223], v[8:11], v[232:235], 0\n\t" \
    "v_mfma_f32_16x16x32_bf16 v[220:223], v[12:15], v[236:239], v[220:223]\n\t" \
    "s_waitcnt lgkmcnt(2)\n\t" \
    "v_mfma_f32_16x16x32_bf16 v[204:207], v[40:43], v[240:243], v[204:207]\n\t" \
    "ds_read_b128 v[232:235], v253 offset:23552\n\t" \
    "s_waitcnt lgkmcnt(2)\n\t" \
    "v_mfma_f32_16x16x32_bf16 v[204:207], v[44:47], v[224:227], v[204:207]\n\t" \
    "ds_read_b128 v[236:239], v253 offset:4096\n\t" \
    "v_mfma_f32_16x16x32_bf16 v[208:211], v[48:51], v[240:243], v[208:211]\n\t" \
    "v_mfma_f32_16x16x32_bf16 v[208:211], v[52:55], v[224:227], v[208:211]\n\t" \
    "s_waitcnt lgkmcnt(2)\n\t" \
    "v_mfma_f32_16x16x32_bf16 v[216:219], v[40:43], v[228:231], v[216:219]\n\t" \
    "ds_read_b128 v[240:243], v253 offset:5120\n\t" \
    "s_waitcnt lgkmcnt(2)\n\t" \
    "v_mfma_f32_16x16x32_bf16 v[216:219], v[44:47], v[232:235], v[216:219]\n\t" \
    "ds_read_b128 v[224:227], v253 offset:24576\n\t" \
    "v_mfma_f32_16x16x32_bf16 v[220:223], v[48:51], v[228:231], v[220:223]\n\t" \
    "v_mfma_f32_16x16x32_bf16 v[220:223], v[52:55], v[232:235], v[220:223]\n\t" \
    "s_waitcnt lgkmcnt(2)\n\t" \
    "v_mfma_f32_16x16x32_bf16 v[204:207], v[80:83], v[236:239], v[204:207]\n\t" \
    "ds_read_b128 v[228:231], v253 offset:25600\n\t" \
    "s_waitcnt lgkmcnt(2)\n\t" \
    "v_mfma_f32_16x16x32_bf16 v[204:207], v[84:87], v[240:243], v[204:207]\n\t" \
    "ds_read_b128 v[232:235], v253 offset:6144\n\t" \
    "v_mfma_f32_16x16x32_bf16 v[208:211], v[88:91], v[236:239], v[208:211]\n\t" \
    "v_mfma_f32_16x16x32_bf16 v[208:211], v[92:95], v[240:243], v[208:211]\n\t" \
    "s_waitcnt lgkmcnt(2)\n\t" \
    "v_mfma_f32_16x16x32_bf16 v[216:219], v[80:83], v[224:227], v[216:219]\n\t" \
    "ds_read_b128 v[236:239], v253 offset:7168\n\t" \
    "s_waitcnt lgkmcnt(2)\n\t" \
    "v_mfma_f32_16x16x32_bf16 v[216:219], v[84:87], v[228:231], v[216:219]\n\t" \
    "ds_read_b128 v[240:243], v253 offset:26624\n\t" \
    "v_mfma_f32_16x16x32_bf16 v[220:223], v[88:91], v[224:227], v[220:223]\n\t" \
    "v_mfma_f32_16x16x32_bf16 v[220:223], v[92:95], v[228:231], v[220:223]\n\t" \
    "s_waitcnt lgkmcnt(2)\n\t" \
    "v_mfma_f32_16x16x32_bf16 v[204:207], v[120:123], v[232:235], v[204:207]\n\t" \
    "ds_read_b128 v[224:227], v253 offset:27648\n\t" \
    "s_waitcnt lgkmcnt(2)\n\t" \
    "v_mfma_f32_16x16x32_bf16 v[204:207], v[124:127], v[236:239], v[204:207]\n\t" \
    "ds_read_b128 v[228:231], v253 offset:8192\n\t" \
    "v_mfma_f32_16x16x32_bf16 v[208:211], v[128:131], v[232:235], v[208:211]\n\t" \
    "v_mfma_f32_16x16x32_bf16 v[208:211], v[132:135], v[236:239], v[208:211]\n\t" \
    "s_waitcnt lgkmcnt(2)\n\t" \
    "v_mfma_f32_16x16x32_bf16 v[216:219], v[120:123], v[240:243], v[216:219]\n\t" \
    "ds_read_b128 v[232:235], v253 offset:9216\n\t" \
    "s_waitcnt lgkmcnt(2)\n\t" \
    "v_mfma_f32_16x16x32_bf16 v[216:219], v[124:127], v[224:227], v[216:219]\n\t" \
    "ds_read_b128 v[236:239], v253 offset:28672\n\t" \
    "v_mfma_f32_16x16x32_bf16 v[220:223], v[128:131], v[240:243], v[220:223]\n\t" \
    "v_mfma_f32_16x16x32_bf16 v[220:223], v[132:135], v[224:227], v[220:223]\n\t" \
    "s_waitcnt lgkmcnt(2)\n\t" \
    "v_mfma_f32_16x16x32_bf16 v[204:207], v[160:163], v[228:231], v[204:207]\n\t" \
    "ds_read_b128 v[240:243], v253 offset:29696\n\t" \
    "s_waitcnt lgkmcnt(2)\n\t" \
    "v_mfma_f32_16x16x32_bf16 v[204:207], v[164:167], v[232:235], v[204:207]\n\t" \
    "ds_read_b128 v[224:227], v253 offset:10240\n\t" \
    "v_mfma_f32_16x16x32_bf16 v[208:211], v[168:171], v[228:231], v[208:211]\n\t" \
    "v_mfma_f32_16x16x32_bf16 v[208:211], v[172:175], v[232:235], v[208:211]\n\t" \
    "s_waitcnt lgkmcnt(2)\n\t" \
    "v_mfma_f32_16x16x32_bf16 v[216:219], v[160:163], v[236:239], v[216:219]\n\t" \
    "ds_read_b128 v[228:231], v253 offset:11264\n\t" \
    "s_waitcnt lgkmcnt(2)\n\t" \
    "v_mfma_f32_16x16x32_bf16 v[216:219], v[164:167], v[240:243], v[216:219]\n\t" \
    "ds_read_b128 v[232:235], v253 offset:30720\n\t" \
    "v_mfma_f32_16x16x32_bf16 v[220:223], v[168:171], v[236:239], v[220:223]\n\t" \
    "v_mfma_f32_16x16x32_bf16 v[220:223], v[172:175], v[240:243], v[220:223]\n\t" \
    "ds_read_b128 v[236:239], v253 offset:31744\n\t" \
    "s_setprio 2\n\t" \
    "s_waitcnt lgkmcnt(0)\n\t" \
    "s_barrier \n\t" \
    LST7 \
    LJIT \
    "ds_read_b128 v[200:203], v253 offset:40960\n\t" \
    "ds_read_b128 v[212:215], v253 offset:41984\n\t" \
    "v_mfma_f32_16x16x32_bf16 v[204:207], v[24:27], v[224:227], v[204:207]\n\t" \
    "v_mfma_f32_16x16x32_bf16 v[204:207], v[28:31], v[228:231], v[204:207]\n\t" \
    "ds_read_b128 v[240:243], v253 offset:12288\n\t" \
    "v_mfma_f32_16x16x32_bf16 v[208:211], v[32:35], v[224:227], v[208:211]\n\t" \
    "v_mfma_f32_16x16x32_bf16 v[208:211], v[36:39], v[228:231], v[208:211]\n\t" \
    "s_waitcnt lgkmcnt(2)\n\t" \
    "v_mfma_f32_16x16x32_bf16 v[200:203], v[16:19], v[224:227], v[200:203]\n\t" \
    "v_mfma_f32_16x16x32_bf16 v[200:203], v[20:23], v[228:231], v[200:203]\n\t" \
    "v_mfma_f32_16x16x32_bf16 v[216:219], v[24:27], v[232:235], v[216:219]\n\t" \
    "ds_read_b128 v[224:227], v253 offset:13312\n\t" \
    "s_and_b32 s44, s40, 3\n\t" \
    "v_mfma_f32_16x16x32_bf16 v[216:219], v[28:31], v[236:239], v[216:219]\n\t" \
    "ds_read_b128 v[228:231], v253 offset:32768\n\t" \
    "s_lshl_b32 s44, s44, 17\n\t" \
    "v_mfma_f32_16x16x32_bf16 v[220:223], v[32:35], v[232:235], v[220:223]\n\t" \
    "s_add_u32 s44, s44, %[mb]\n\t" \
    "v_mfma_f32_16x16x32_bf16 v[220:223], v[36:39], v[236:239], v[220:223]\n\t" \
    "s_add_u32 s46, s44, 0x0\n\t" \
    "s_waitcnt lgkmcnt(3)\n\t" \
    "v_mfma_f32_16x16x32_bf16 v[212:215], v[16:19], v[232:235], v[212:215]\n\t" \
    "s_add_u32 s47, s44, 0x1400\n\t" \
    "v_mfma_f32_16x16x32_bf16 v[212:215], v[20:23], v[236:239], v[212:215]\n\t" \
    "s_add_u32 s48, s44, 0x400\n\t" \
    "s_waitcnt lgkmcnt(2)\n\t" \
    "v_mfma_f32_16x16x32_bf16 v[204:207], v[64:67], v[240:243], v[204:207]\n\t" \
    "ds_read_b128 v[232:235], v253 offset:33792\n\t" \
    "s_waitcnt lgkmcnt(2)\n\t" \
    "v_mfma_f32_16x16x32_bf16 v[204:207], v[68:71], v[224:227], v[204:207]\n\t" \
    "ds_read_b128 v[236:239], v253 offset:14336\n\t" \
    "v_mfma_f32_16x16x32_bf16 v[208:211], v[72:75], v[240:243], v[208:211]\n\t" \
    "v_mfma_f32_16x16x32_bf16 v[208:211], v[76:79], v[224:227], v[208:211]\n\t" \
    "v_mfma_f32_16x16x32_bf16 v[200:203], v[56:59], v[240:243], v[200:203]\n\t" \
    "v_mfma_f32_16x16x32_bf16 v[200:203], v[60:63], v[224:227], v[200:203]\n\t" \
    "s_waitcnt lgkmcnt(2)\n\t" \
    "v_mfma_f32_16x16x32_bf16 v[216:219], v[64:67], v[228:231], v[216:219]\n\t" \
    "ds_read_b128 v[240:243], v253 offset:15360\n\t" \
    "s_waitcnt lgkmcnt(2)\n\t" \
    "v_mfma_f32_16x16x32_bf16 v[216:219], v[68:71], v[232:235], v[216:219]\n\t" \
    "ds_read_b128 v[224:227], v253 offset:34816\n\t" \
    "v_mfma_f32_16x16x32_bf16 v[220:223], v[72:75], v[228:231], v[220:223]\n\t" \
    "v_mfma_f32_16x16x32_bf16 v[220:223], v[76:79], v[232:235], v[220:223]\n\t" \
    "v_mfma_f32_16x16x32_bf16 v[212:215], v[56:59], v[228:231], v[212:215]\n\t" \
    "v_mfma_f32_16x16x32_bf16 v[212:215], v[60:63], v[232:235], v[212:215]\n\t" \
    "s_waitcnt lgkmcnt(2)\n\t" \
    "v_mfma_f32_16x16x32_bf16 v[204:207], v[104:107], v[236:239], v[204:207]\n\t" \
    "ds_read_b128 v[228:231], v253 offset:35840\n\t" \
    "s_waitcnt lgkmcnt(2)\n\t" \
    "v_mfma_f32_16x16x32_bf16 v[204:207], v[108:111], v[240:243], v[204:207]\n\t" \
    "ds_read_b128 v[232:235], v253 offset:16384\n\t" \
    "v_mfma_f32_16x16x32_bf16 v[208:211], v[112:115], v[236:239], v[208:211]\n\t" \
    "v_mfma_f32_16x16x32_bf16 v[208:211], v[116:119], v[240:243], v[208:211]\n\t" \
    "v_mfma_f32_16x16x32_bf16 v[200:203], v[96:99], v[236:239], v[200:203]\n\t" \
    "v_mfma_f32_16x16x32_bf16 v[200:203], v[100:103], v[240:243], v[200:203]\n\t" \
    "s_waitcnt lgkmcnt(2)\n\t" \
    "v_mfma_f32_16x16x32_bf16 v[216:219], v[104:107], v[224:227], v[216:219]\n\t" \
    "ds_read_b128 v[236:239], v253 offset:17408\n\t" \
    "s_waitcnt lgkmcnt(2)\n\t" \
    "v_mfma_f32_16x16x32_bf16 v[216:219], v[108:111], v[228:231], v[216:219]\n\t" \
    "ds_read_b128 v[240:243], v253 offset:18432\n\t" \
    "v_mfma_f32_16x16x32_bf16 v[220:223], v[112:115], v[224:227], v[220:223]\n\t" \
    "v_mfma_f32_16x16x32_bf16 v[220:223], v[116:119], v[228:231], v[220:223]\n\t" \
    "v_mfma_f32_16x16x32_bf16 v[212:215], v[96:99], v[224:227], v[212:215]\n\t" \
    "v_mfma_f32_16x16x32_bf16 v[212:215], v[100:103], v[228:231], v[212:215]\n\t" \
    "s_waitcnt lgkmcnt(2)\n\t" \
    "v_mfma_f32_16x16x32_bf16 v[204:207], v[144:147], v[232:235], v[204:207]\n\t" \
    "ds_read_b128 v[224:227], v253 offset:19456\n\t" \
    "v_mov_b64 v[244:245], 0\n\t" \
    "s_waitcnt lgkmcnt(2)\n\t" \
    "v_mfma_f32_16x16x32_bf16 v[204:207], v[148:151], v[236:239], v[204:207]\n\t" \
    "ds_read_b128 v[228:231], v253 offset:36864\n\t" \
    "v_mov_b64 v[246:247], 0\n\t" \
    "v_mfma_f32_16x16x32_bf16 v[208:211], v[152:155], v[232:235], v[208:211]\n\t" \
    "v_mov_b64 v[248:249], 0\n\t" \
    "v_mfma_f32_16x16x32_bf16 v[208:211], v[156:159], v[236:239], v[208:211]\n\t" \
    "v_mov_b64 v[250:251], 0\n\t" \
    "v_mfma_f32_16x16x32_bf16 v[200:203], v[136:139], v[232:235], v[200:203]\n\t" \
    "v_mfma_f32_16x16x32_bf16 v[200:203], v[140:143], v[236:239], v[200:203]\n\t" \
    "s_waitcnt lgkmcnt(2)\n\t" \
    "v_mfma_f32_16x16x32_bf16 v[204:207], v[184:187], v[240:243], v[204:207]\n\t" \
    "ds_read_b128 v[232:235], v253 offset:37888\n\t" \
    "s_waitcnt lgkmcnt(2)\n\t" \
    "v_mfma_f32_16x16x32_bf16 v[204:207], v[188:191], v[224:227], v[204:207]\n\t" \
    "ds_read_b128 v[236:239], v253 offset:38912\n\t" \
    "v_mfma_f32_16x16x32_bf16 v[208:211], v[192:195], v[240:243], v[208:211]\n\t" \
    "v_mfma_f32_16x16x32_bf16 v[208:211], v[196:199], v[224:227], v[208:211]\n\t" \
    "v_mfma_f32_16x16x32_bf16 v[200:203], v[176:179], v[240:243], v[200:203]\n\t" \
    "v_mfma_f32_16x16x32_bf16 v[200:203], v[180:183], v[224:227], v[200:203]\n\t" \
    "ds_read_b128 v[240:243], v253 offset:39936\n\t" \
    "s_waitcnt lgkmcnt(3)\n\t" \
    "v_mfma_f32_16x16x32_bf16 v[216:219], v[144:147], v[228:231], v[216:219]\n\t" \
    "s_waitcnt lgkmcnt(2)\n\t" \
    "v_mfma_f32_16x16x32_bf16 v[216:219], v[148:151], v[232:235], v[216:219]\n\t" \
    "v_cvt_pk_bf16_f32 v204, v204, v208\n\t" \
    "v_cvt_pk_bf16_f32 v205, v205, v209\n\t" \
    "s_waitcnt lgkmcnt(1)\n\t" \
    "v_mfma_f32_16x16x32_bf16 v[216:219], v[184:187], v[236:239], v[216:219]\n\t" \
    "v_cvt_pk_bf16_f32 v206, v206, v210\n\t" \
    "v_cvt_pk_bf16_f32 v207, v207, v211\n\t" \
    "s_waitcnt lgkmcnt(0)\n\t" \
    "v_mfma_f32_16x16x32_bf16 v[216:219], v[188:191], v[240:243], v[216:219]\n\t" \
    "ds_write_b128 v254, v[204:207] offset:8192\n\t" \
    "v_mfma_f32_16x16x32_bf16 v[220:223], v[152:155], v[228:231], v[220:223]\n\t" \
    "v_mfma_f32_16x16x32_bf16 v[220:223], v[156:159], v[232:235], v[220:223]\n\t" \
    "v_cvt_pk_bf16_f32 v200, v200, v201\n\t" \
    "v_cvt_pk_bf16_f32 v201, v202, v203\n\t" \
    "v_mfma_f32_16x16x32_bf16 v[220:223], v[192:195], v[236:239], v[220:223]\n\t" \
    "ds_write_b64 v254, v[200:201] offset:6144\n\t" \
    "v_mfma_f32_16x16x32_bf16 v[220:223], v[196:199], v[240:243], v[220:223]\n\t" \
    "v_mfma_f32_16x16x32_bf16 v[212:215], v[136:139], v[228:231], v[212:215]\n\t" \
    "ds_read_b128 v[200:203], v254 offset:30720\n\t" \
    "ds_read_b128 v[204:207], v254 offset:31744\n\t" \
    "v_mfma_f32_16x16x32_bf16 v[212:215], v[140:143], v[232:235], v[212:215]\n\t" \
    "v_mov_b64 v[208:209], 0\n\t" \
    "v_mov_b64 v[210:211], 0\n\t" \
    "v_mfma_f32_16x16x32_bf16 v[212:215], v[176:179], v[236:239], v[212:215]\n\t" \
    "v_cvt_pk_bf16_f32 v216, v216, v220\n\t" \
    "v_cvt_pk_bf16_f32 v217, v217, v221\n\t" \
    "v_mfma_f32_16x16x32_bf16 v[212:215], v[180:183], v[240:243], v[212:215]\n\t" \
    "v_cvt_pk_bf16_f32 v218, v218, v222\n\t" \
    "v_cvt_pk_bf16_f32 v219, v219, v223\n\t" \
    "s_setprio 0\n\t" \
    "ds_write_b128 v254, v[216:219] offset:14336\n\t" \
    "ds_read_b128 v[216:219], v254 offset:32768\n\t" \
    "ds_read_b128 v[220:223], v254 offset:33792\n\t" \
    "s_nop 2\n\t" \
    "v_cvt_pk_bf16_f32 v212, v212, v213\n\t" \
    "v_cvt_pk_bf16_f32 v213, v214, v215\n\t" \
    "ds_write_b64 v254, v[212:213] offset:12288\n\t" \
    LST6 \
    LJIT \
    "s_sleep 3\n\t" \
    "s_mov_b32 m0, 0xa000\n\t" \
    "s_nop 0\n\t" \
    "buffer_load_dwordx4 v253, s[60:63], s46 offen sc1 lds\n\t" \
    "s_mov_b32 m0, 0xb400\n\t" \
    "s_nop 0\n\t" \
    "buffer_load_dwordx4 v253, s[60:63], s47 offen sc1 lds\n\t" \
    "s_mov_b32 m0, 0xa400\n\t" \
    "s_nop 0\n\t" \
    "buffer_load_dwordx4 v253, s[60:63], s48 offen sc1 lds\n\t" \
    "s_waitcnt lgkmcnt(0)\n\t" \
    LFLUSH212 \
    "v_bfe_i32 v212, v253, 5, 1\n\t" \
    "v_and_b32 v212, 0xaaaa, v212\n\t" \
    "v_or_b32 v212, 0x4444, v212\n\t" \
    "v_and_b32 v213, 0xffff, v255\n\t" \
    "v_lshrrev_b32 v214, 16, v255\n\t" \
    ".Ls04_%=:\n\t" \
    "s_waitcnt vmcnt(0)\n\t" \
    "ds_read_b128 v[224:227], v253 offset:40960\n\t" \
    "ds_read_b128 v[228:231], v253 offset:46080\n\t" \
    "ds_read_b128 v[232:235], v253 offset:41984\n\t" \
    "s_waitcnt lgkmcnt(2)\n\t" \
    "v_max_u32 v215, v224, v225\n\t" \
    "v_max3_u32 v215, v226, v227, v215\n\t" \
    "s_waitcnt lgkmcnt(1)\n\t" \
    "v_max3_u32 v215, v228, v229, v215\n\t" \
    "v_max3_u32 v215, v230, v231, v215\n\t" \
    "s_waitcnt lgkmcnt(0)\n\t" \
    "v_max3_u32 v215, v232, v233, v215\n\t" \
    "v_max3_u32 v215, v234, v235, v215\n\t" \
    "v_cmp_eq_u32 vcc, -1, v215\n\t" \
    "s_cbranch_vccz .Ls05_%=\n\t" \
    "v_max_u32 v215, v224, v225\n\t" \
    "v_max3_u32 v215, v226, v227, v215\n\t" \
    "v_cmp_eq_u32 vcc, -1, v215\n\t" \
    "s_cbranch_vccz .Ls06_%=\n\t" \
    "s_mov_b32 m0, 0xa000\n\t" \
    "s_nop 0\n\t" \
    "buffer_load_dwordx4 v253, s[60:63], s46 offen sc1 lds\n\t" \
    ".Ls06_%=:\n\t" \
    "v_max_u32 v215, v228, v229\n\t" \
    "v_max3_u32 v215, v230, v231, v215\n\t" \
    "v_cmp_eq_u32 vcc, -1, v215\n\t" \
    "s_cbranch_vccz .Ls07_%=\n\t" \
    "s_mov_b32 m0, 0xb400\n\t" \
    "s_nop 0\n\t" \
    "buffer_load_dwordx4 v253, s[60:63], s47 offen sc1 lds\n\t" \
    ".Ls07_%=:\n\t" \
    "v_max_u32 v215, v232, v233\n\t" \
    "v_max3_u32 v215, v234, v235, v215\n\t" \
    "v_cmp_eq_u32 vcc, -1, v215\n\t" \
    "s_cbranch_vccz .Ls08_%=\n\t" \
    "s_mov_b32 m0, 0xa400\n\t" \
    "s_nop 0\n\t" \
    "buffer_load_dwordx4 v253, s[60:63], s48 offen sc1 lds\n\t" \
    ".Ls08_%=:\n\t" \
    "s_cmp_eq_u32 s41, 0\n\t" \
    "s_cbranch_scc1 .Ls05_%=\n\t" \
    "s_sub_u32 s41, s41, 1\n\t" \
    "s_branch .Ls04_%=\n\t" \
    ".Ls05_%=:\n\t" \
    LST2 \
    LJIT \
    "s_setprio 3\n\t" \
    "s_waitcnt lgkmcnt(0)\n\t" \
    "s_barrier \n\t" \
    LST3 \
    LJIT \
    "ds_read_b128 v[236:239], v253 offset:47104\n\t" \
    "ds_read_b128 v[240:243], v253 offset:43008\n\t" \
    "v_smfmac_f32_16x16x64_bf16 v[244:247], v[224:227], v[0:7], v212\n\t" \
    "v_smfmac_f32_16x16x64_bf16 v[248:251], v[224:227], v[8:15], v212\n\t" \
    "v_smfmac_f32_16x16x64_bf16 v[208:211], v[224:227], v[200:207], v212\n\t" \
    "ds_read_b128 v[200:203], v254 offset:34816\n\t" \
    "ds_read_b128 v[204:207], v254 offset:35840\n\t" \
    "ds_read_b128 v[224:227], v253 offset:48128\n\t" \
    "v_smfmac_f32_16x16x64_bf16 v[208:211], v[228:231], v[16:23], v212\n\t" \
    "v_smfmac_f32_16x16x64_bf16 v[244:247], v[228:231], v[24:31], v212\n\t" \
    "v_smfmac_f32_16x16x64_bf16 v[248:251], v[228:231], v[32:39], v212\n\t" \
    "ds_read_b128 v[228:231], v253 offset:44032\n\t" \
    "v_smfmac_f32_16x16x64_bf16 v[208:211], v[232:235], v[216:223], v212\n\t" \
    "ds_read_b128 v[216:219], v254 offset:36864\n\t" \
    "ds_read_b128 v[220:223], v254 offset:37888\n\t" \
    "v_smfmac_f32_16x16x64_bf16 v[244:247], v[232:235], v[40:47], v212\n\t" \
    "v_smfmac_f32_16x16x64_bf16 v[248:251], v[232:235], v[48:55], v212\n\t" \
    "ds_read_b128 v[232:235], v253 offset:49152\n\t" \
    "s_waitcnt lgkmcnt(8)\n\t" \
    "v_smfmac_f32_16x16x64_bf16 v[208:211], v[236:239], v[56:63], v212\n\t" \
    "v_smfmac_f32_16x16x64_bf16 v[244:247], v[236:239], v[64:71], v212\n\t" \
    "v_smfmac_f32_16x16x64_bf16 v[248:251], v[236:239], v[72:79], v212\n\t" \
    "ds_read_b128 v[236:239], v253 offset:45056\n\t" \
    "s_waitcnt lgkmcnt(6)\n\t" \
    "v_smfmac_f32_16x16x64_bf16 v[208:211], v[240:243], v[200:207], v212\n\t" \
    "ds_read_b128 v[200:203], v254 offset:38912\n\t" \
    "ds_read_b128 v[204:207], v254 offset:39936\n\t" \
    "v_smfmac_f32_16x16x64_bf16 v[244:247], v[240:243], v[80:87], v212\n\t" \
    "v_smfmac_f32_16x16x64_bf16 v[248:251], v[240:243], v[88:95], v212\n\t" \
    "ds_read_b128 v[240:243], v253 offset:50176\n\t" \
    "s_waitcnt lgkmcnt(8)\n\t" \
    "v_smfmac_f32_16x16x64_bf16 v[208:211], v[224:227], v[96:103], v212\n\t" \
    "v_smfmac_f32_16x16x64_bf16 v[244:247], v[224:227], v[104:111], v212\n\t" \
    "v_smfmac_f32_16x16x64_bf16 v[248:251], v[224:227], v[112:119], v212\n\t" \
    "s_waitcnt lgkmcnt(5)\n\t" \
    "v_smfmac_f32_16x16x64_bf16 v[208:211], v[228:231], v[216:223], v212\n\t" \
    "s_waitcnt lgkmcnt(4)\n\t" \
    "v_smfmac_f32_16x16x64_bf16 v[208:211], v[232:235], v[136:143], v212\n\t" \
    "s_waitcnt lgkmcnt(1)\n\t" \
    "v_smfmac_f32_16x16x64_bf16 v[208:211], v[236:239], v[200:207], v212\n\t" \
    "s_waitcnt lgkmcnt(0)\n\t" \
    "v_smfmac_f32_16x16x64_bf16 v[208:211], v[240:243], v[176:183], v212\n\t" \
    "v_smfmac_f32_16x16x64_bf16 v[244:247], v[228:231], v[120:127], v212\n\t" \
    "v_smfmac_f32_16x16x64_bf16 v[244:247], v[232:235], v[144:151], v212\n\t" \
    "v_smfmac_f32_16x16x64_bf16 v[244:247], v[236:239], v[160:167], v212\n\t" \
    "v_add_f32 v208, v208, v210\n\t" \
    "v_add_f32 v209, v209, v211\n\t" \
    "v_cvt_pk_bf16_f32 v208, v208, v209\n\t" \
    "v_smfmac_f32_16x16x64_bf16 v[244:247], v[240:243], v[184:191], v212\n\t" \
    "ds_write_b16 v213, v208 offset:0\n\t" \
    "ds_write_b16_d16_hi v213, v208 offset:16\n\t" \
    "v_smfmac_f32_16x16x64_bf16 v[248:251], v[228:231], v[128:135], v212\n\t" \
    "v_smfmac_f32_16x16x64_bf16 v[248:251], v[232:235], v[152:159], v212\n\t" \
    "v_smfmac_f32_16x16x64_bf16 v[248:251], v[236:239], v[168:175], v212\n\t" \
    "v_add_f32 v244, v244, v246\n\t" \
    "v_add_f32 v245, v245, v247\n\t" \
    "v_smfmac_f32_16x16x64_bf16 v[248:251], v[240:243], v[192:199], v212\n\t" \
    "s_nop 7\n\t" \
    "v_add_f32 v248, v248, v250\n\t" \
    "v_add_f32 v249, v249, v251\n\t" \
    "v_cvt_pk_bf16_f32 v244, v244, v248\n\t" \
    "v_cvt_pk_bf16_f32 v245, v245, v249\n\t" \
    "ds_write2_b32 v214, v244, v245 offset1:4\n\t" \
    "s_setprio 0\n\t" \
    "s_sleep 2\n\t" \
    "ds_read_b128 v[200:203], v254 offset:18432\n\t" \
    "ds_read_b128 v[204:207], v254 offset:19456\n\t" \
    "ds_read_b128 v[214:217], v254 offset:24576\n\t" \
    "ds_read_b128 v[218:221], v254 offset:25600\n\t" \
    "ds_read_b128 v[222:225], v254 offset:20480\n\t" \
    "ds_read_b128 v[226:229], v254 offset:21504\n\t" \
    "ds_read_b128 v[230:233], v254 offset:26624\n\t" \
    "ds_read_b128 v[234:237], v254 offset:27648\n\t" \
    "v_and_b32 v213, 0xffffff7f, v254\n\t" \
    "v_mov_b64 v[242:243], 0\n\t" \
    "v_mov_b64 v[244:245], 0\n\t" \
    "v_mov_b64 v[246:247], 0\n\t" \
    "v_mov_b64 v[248:249], 0\n\t" \
    "v_mov_b64 v[250:251], 0\n\t" \
    "v_mov_b64 v[252:253], 0\n\t" \
    "s_and_b32 s44, s40, 3\n\t" \
    "s_mul_i32 s44, s44, 0x30000\n\t" \
    "s_add_u32 s50, s44, %[pb]\n\t" \
    "s_add_u32 s51, s50, 0xc000\n\t" \
    "s_add_u32 s52, s50, 0x18000\n\t" \
    "s_add_u32 s53, s50, 0x24000\n\t" \
    "s_setprio 3\n\t" \
    "s_waitcnt lgkmcnt(0)\n\t" \
    "s_barrier \n\t" \
    LST4 \
    LJIT \
    "ds_read_b128 v[208:211], v254 offset:0\n\t" \
    "ds_read_b128 v[238:241], v254 offset:1024\n\t" \
    "s_waitcnt lgkmcnt(1)\n\t" \
    "v_smfmac_f32_16x16x64_bf16 v[242:245], v[208:211], v[200:207], v212\n\t" \
    "ds_read_b128 v[200:203], v213 offset:3072\n\t" \
    "ds_read_b128 v[204:207], v213 offset:3200\n\t" \
    "v_smfmac_f32_16x16x64_bf16 v[246:249], v[208:211], v[214:221], v212\n\t" \
    "ds_read_b128 v[214:217], v213 offset:4096\n\t" \
    "ds_read_b128 v[218:221], v213 offset:4224\n\t" \
    "s_waitcnt lgkmcnt(4)\n\t" \
    "v_smfmac_f32_16x16x64_bf16 v[242:245], v[238:241], v[222:229], v212\n\t" \
    "ds_read_b128 v[222:225], v254 offset:22528\n\t" \
    "ds_read_b128 v[226:229], v254 offset:23552\n\t" \
    "v_smfmac_f32_16x16x64_bf16 v[246:249], v[238:241], v[230:237], v212\n\t" \
    "ds_read_b128 v[230:233], v254 offset:28672\n\t" \
    "ds_read_b128 v[234:237], v254 offset:29696\n\t" \
    "s_waitcnt lgkmcnt(6)\n\t" \
    "v_smfmac_f32_16x16x64_bf16 v[250:253], v[208:211], v[200:207], v212\n\t" \
    "ds_read_b128 v[208:211], v254 offset:2048\n\t" \
    "ds_read_b128 v[200:203], v213 offset:5120\n\t" \
    "ds_read_b128 v[204:207], v213 offset:5248\n\t" \
    "s_waitcnt lgkmcnt(7)\n\t" \
    "v_smfmac_f32_16x16x64_bf16 v[250:253], v[238:241], v[214:221], v212\n\t" \
    "v_lshrrev_b32 v238, 3, v254\n\t" \
    "v_and_b32 v238, 0x6e, v238\n\t" \
    "v_lshrrev_b32 v239, 16, v255\n\t" \
    "s_waitcnt lgkmcnt(2)\n\t" \
    "v_smfmac_f32_16x16x64_bf16 v[242:245], v[208:211], v[222:229], v212\n\t" \
    "v_smfmac_f32_16x16x64_bf16 v[246:249], v[208:211], v[230:237], v212\n\t" \
    "s_waitcnt lgkmcnt(0)\n\t" \
    "v_smfmac_f32_16x16x64_bf16 v[250:253], v[208:211], v[200:207], v212\n\t" \
    "s_nop 1\n\t" \
    "v_add_f32 v242, v242, v244\n\t" \
    "v_add_f32 v243, v243, v245\n\t" \
    "s_nop 0\n\t" \
    "v_add_f32 v246, v246, v248\n\t" \
    "v_add_f32 v247, v247, v249\n\t" \
    "s_nop 0\n\t" \
    "v_add_f32 v250, v250, v252\n\t" \
    "v_add_f32 v251, v251, v253\n\t" \
    "v_max3_f32 v244, v242, v246, v250\n\t" \
    "v_max3_f32 v245, v243, v247, v251\n\t" \
    "s_nop 0\n\t" \
    "v_max_f32_dpp v244, v244, v244 quad_perm:[1,0,3,2] row_mask:0xf bank_mask:0xf\n\t" \
    "v_max_f32_dpp v245, v245, v245 quad_perm:[1,0,3,2] row_mask:0xf bank_mask:0xf\n\t" \
    "s_nop 0\n\t" \
    "v_max_f32_dpp v244, v244, v244 quad_perm:[2,3,0,1] row_mask:0xf bank_mask:0xf\n\t" \
    "v_max_f32_dpp v245, v245, v245 quad_perm:[2,3,0,1] row_mask:0xf bank_mask:0xf\n\t" \
    "s_nop 0\n\t" \
    "v_max_f32_dpp v244, v244, v244 row_half_mirror row_mask:0xf bank_mask:0xf\n\t" \
    "v_max_f32_dpp v245, v245, v245 row_half_mirror row_mask:0xf bank_mask:0xf\n\t" \
    "s_nop 0\n\t" \
    "v_max_f32_dpp v244, v244, v244 row_mirror row_mask:0xf bank_mask:0xf\n\t" \
    "v_max_f32_dpp v245, v245, v245 row_mirror row_mask:0xf bank_mask:0xf\n\t" \
    "v_mul_f32 v244, %[scl], v244\n\t" \
    "v_mul_f32 v245, %[scl], v245\n\t" \
    "v_fma_f32 v242, v242, %[scl], -v244\n\t" \
    "v_fma_f32 v243, v243, %[scl], -v245\n\t" \
    "v_fma_f32 v246, v246, %[scl], -v244\n\t" \
    "v_fma_f32 v247, v247, %[scl], -v245\n\t" \
    "v_exp_f32 v242, v242\n\t" \
    "v_exp_f32 v243, v243\n\t" \
    "v_fma_f32 v250, v250, %[scl], -v244\n\t" \
    "v_fma_f32 v251, v251, %[scl], -v245\n\t" \
    "v_exp_f32 v246, v246\n\t" \
    "v_exp_f32 v247, v247\n\t" \
    "v_exp_f32 v250, v250\n\t" \
    "v_exp_f32 v251, v251\n\t" \
    "v_add_f32 v244, v242, v246\n\t" \
    "v_add_f32 v245, v243, v247\n\t" \
    "v_fma_f32 v244, v250, 0.5, v244\n\t" \
    "v_fma_f32 v245, v251, 0.5, v245\n\t" \
    "s_nop 0\n\t" \
    "v_add_f32_dpp v244, v244, v244 quad_perm:[1,0,3,2] row_mask:0xf bank_mask:0xf\n\t" \
    "v_add_f32_dpp v245, v245, v245 quad_perm:[1,0,3,2] row_mask:0xf bank_mask:0xf\n\t" \
    "s_nop 0\n\t" \
    "v_add_f32_dpp v244, v244, v244 quad_perm:[2,3,0,1] row_mask:0xf bank_mask:0xf\n\t" \
    "v_add_f32_dpp v245, v245, v245 quad_perm:[2,3,0,1] row_mask:0xf bank_mask:0xf\n\t" \
    "s_nop 0\n\t" \
    "v_add_f32_dpp v244, v244, v244 row_half_mirror row_mask:0xf bank_mask:0xf\n\t" \
    "v_add_f32_dpp v245, v245, v245 row_half_mirror row_mask:0xf bank_mask:0xf\n\t" \
    "s_nop 0\n\t" \
    "v_add_f32_dpp v244, v244, v244 row_mirror row_mask:0xf bank_mask:0xf\n\t" \
    "v_add_f32_dpp v245, v245, v245 row_mirror row_mask:0xf bank_mask:0xf\n\t" \
    "v_rcp_f32 v244, v244\n\t" \
    "v_rcp_f32 v245, v245\n\t" \
    "s_wakeup\n\t" \
    "v_mul_f32 v250, v250, v244\n\t" \
    "v_mul_f32 v251, v251, v245\n\t" \
    "v_mul_f32 v242, v242, v244\n\t" \
    "v_mul_f32 v243, v243, v245\n\t" \
    "v_mul_f32 v246, v246, v244\n\t" \
    "v_mul_f32 v247, v247, v245\n\t" \
    "v_cvt_pk_bf16_f32 v250, |v250|, |v251|\n\t" \
    "ds_write_b16 v238, v250 offset:52096\n\t" \
    "ds_write_b16_d16_hi v238, v250 offset:52112\n\t" \
    "v_cvt_pk_bf16_f32 v242, |v242|, |v246|\n\t" \
    "v_cvt_pk_bf16_f32 v243, |v243|, |v247|\n\t" \
    "ds_write2_b32 v239, v242, v243 offset1:4\n\t" \
    "ds_read_b128 v[200:203], v254 offset:896\n\t" \
    "v_and_b32 v253, 0x3ff, v254\n\t" \
    "s_mov_b32 exec_hi, 0xff\n\t" \
    "s_waitcnt lgkmcnt(0)\n\t" \
    "buffer_store_dwordx4 v[200:203], v254, s[60:63], s50 offen sc1\n\t" \
    "buffer_store_dwordx4 v[200:203], v254, s[60:63], s51 offen sc1\n\t" \
    "buffer_store_dwordx4 v[200:203], v254, s[60:63], s52 offen sc1\n\t" \
    "buffer_store_dwordx4 v[200:203], v254, s[60:63], s53 offen sc1\n\t" \
    "s_mov_b32 exec_hi, -1\n\t" \
    "s_setprio 0\n\t" \
    LST5 \
    LJIT \
    "ds_read_b128 v[224:227], v253 offset:0\n\t" \
    "ds_read_b128 v[228:231], v253 offset:1024\n\t" \
    "ds_read_b128 v[232:235], v253 offset:20480\n\t" \
    "ds_read_b128 v[236:239], v253 offset:21504\n\t" \
    "s_add_u32 s40, s40, 1\n\t" \
    "s_cmp_lg_u32 s40, 64\n\t" \
    "s_cbranch_scc1 .Ls03_%=\n\t" \
    "s_branch .Lend%=\n\t" \
    ".Lend%=:\n\t" \
    "s_waitcnt vmcnt(0) lgkmcnt(0)\n\t" \
    "s_mov_b32 %[bud], s41\n\t"
#define ASM_V_LOOP \
    "s_mov_b64 s[60:61], %[ws]\n\t" \
    "s_mov_b32 s62, %[sz]\n\t" \
    "s_mov_b32 s63, 0x20000\n\t" \
    "s_mov_b32 s40, 0\n\t" \
    "s_mov_b32 s41, 0x20000\n\t" \
    LSTINIT \
    "s_cmp_eq_u32 %[wave], 0\n\t" \
    "s_cbranch_scc1 .Lv0_%=\n\t" \
    "s_cmp_eq_u32 %[wave], 1\n\t" \
    "s_cbranch_scc1 .Lv1_%=\n\t" \
    "s_cmp_eq_u32 %[wave], 2\n\t" \
    "s_cbranch_scc1 .Lv2_%=\n\t" \
    "s_cmp_eq_u32 %[wave], 3\n\t" \
    "s_cbranch_scc1 .Lv3_%=\n\t" \
    ".Lv4_%=:\n\t" \
    "s_barrier \n\t" \
    "s_waitcnt vmcnt(15)\n\t" \
    "ds_read_b64_tr_b16 v[200:201], v245 offset:0\n\t" \
    "ds_read_b64_tr_b16 v[16:17], v245 offset:32\n\t" \
    "ds_read_b64_tr_b16 v[208:209], v245 offset:64\n\t" \
    "ds_read_b64_tr_b16 v[56:57], v245 offset:96\n\t" \
    "ds_read_b64_tr_b16 v[216:217], v245 offset:128\n\t" \
    "ds_read_b64_tr_b16 v[96:97], v245 offset:160\n\t" \
    "ds_read_b64_tr_b16 v[224:225], v245 offset:192\n\t" \
    "ds_read_b64_tr_b16 v[136:137], v245 offset:224\n\t" \
    "ds_read_b64_tr_b16 v[232:233], v245 offset:256\n\t" \
    "ds_read_b64_tr_b16 v[176:177], v245 offset:288\n\t" \
    "s_waitcnt lgkmcnt(0)\n\t" \
    "s_add_u32 m0, %[wm], 0x0\n\t" \
    "s_mov_b32 s44, 0x14000\n\t" \
    "buffer_load_dwordx4 v240, %[wr], s44 offen offset:0 nt lds\n\t" \
    "buffer_load_dwordx4 v241, %[wr], s44 offen offset:1024 nt lds\n\t" \
    "buffer_load_dwordx4 v242, %[wr], s44 offen offset:2048 nt lds\n\t" \
    "buffer_load_dwordx4 v243, %[wr], s44 offen offset:3072 nt lds\n\t" \
    "s_add_u32 m0, %[wm], 0x1000\n\t" \
    "s_nop 0\n\t" \
    "buffer_load_dwordx4 v244, %[wr], s44 offen nt lds\n\t" \
    "s_barrier \n\t" \
    "s_barrier \n\t" \
    "s_waitcnt vmcnt(15)\n\t" \
    "ds_read_b64_tr_b16 v[204:205], v245 offset:5120\n\t" \
    "ds_read_b64_tr_b16 v[20:21], v245 offset:5152\n\t" \
    "ds_read_b64_tr_b16 v[212:213], v245 offset:5184\n\t" \
    "ds_read_b64_tr_b16 v[60:61], v245 offset:5216\n\t" \
    "ds_read_b64_tr_b16 v[220:221], v245 offset:5248\n\t" \
    "ds_read_b64_tr_b16 v[100:101], v245 offset:5280\n\t" \
    "ds_read_b64_tr_b16 v[228:229], v245 offset:5312\n\t" \
    "ds_read_b64_tr_b16 v[140:141], v245 offset:5344\n\t" \
    "ds_read_b64_tr_b16 v[236:237], v245 offset:5376\n\t" \
    "ds_read_b64_tr_b16 v[180:181], v245 offset:5408\n\t" \
    "s_waitcnt lgkmcnt(0)\n\t" \
    "s_add_u32 m0, %[wm], 0x1400\n\t" \
    "s_mov_b32 s44, 0x19000\n\t" \
    "buffer_load_dwordx4 v240, %[wr], s44 offen offset:0 nt lds\n\t" \
    "buffer_load_dwordx4 v241, %[wr], s44 offen offset:1024 nt lds\n\t" \
    "buffer_load_dwordx4 v242, %[wr], s44 offen offset:2048 nt lds\n\t" \
    "buffer_load_dwordx4 v243, %[wr], s44 offen offset:3072 nt lds\n\t" \
    "s_add_u32 m0, %[wm], 0x2400\n\t" \
    "s_nop 0\n\t" \
    "buffer_load_dwordx4 v244, %[wr], s44 offen nt lds\n\t" \
    "s_barrier \n\t" \
    "s_barrier \n\t" \
    "s_waitcnt vmcnt(15)\n\t" \
    "ds_read_b64_tr_b16 v[0:1], v245 offset:10240\n\t" \
    "ds_read_b64_tr_b16 v[24:25], v245 offset:10272\n\t" \
    "ds_read_b64_tr_b16 v[40:41], v245 offset:10304\n\t" \
    "ds_read_b64_tr_b16 v[64:65], v245 offset:10336\n\t" \
    "ds_read_b64_tr_b16 v[80:81], v245 offset:10368\n\t" \
    "ds_read_b64_tr_b16 v[104:105], v245 offset:10400\n\t" \
    "ds_read_b64_tr_b16 v[120:121], v245 offset:10432\n\t" \
    "ds_read_b64_tr_b16 v[144:145], v245 offset:10464\n\t" \
    "ds_read_b64_tr_b16 v[160:161], v245 offset:10496\n\t" \
    "ds_read_b64_tr_b16 v[184:185], v245 offset:10528\n\t" \
    "s_waitcnt lgkmcnt(0)\n\t" \
    "s_add_u32 m0, %[wm], 0x2800\n\t" \
    "s_mov_b32 s44, 0x1e000\n\t" \
    "buffer_load_dwordx4 v240, %[wr], s44 offen offset:0 nt lds\n\t" \
    "buffer_load_dwordx4 v241, %[wr], s44 offen offset:1024 nt lds\n\t" \
    "buffer_load_dwordx4 v242, %[wr], s44 offen offset:2048 nt lds\n\t" \
    "buffer_load_dwordx4 v243, %[wr], s44 offen offset:3072 nt lds\n\t" \
    "s_add_u32 m0, %[wm], 0x3800\n\t" \
    "s_nop 0\n\t" \
    "buffer_load_dwordx4 v244, %[wr], s44 offen nt lds\n\t" \
    "s_barrier \n\t" \
    "s_waitcnt vmcnt(15)\n\t" \
    "ds_read_b64_tr_b16 v[4:5], v245 offset:15360\n\t" \
    "ds_read_b64_tr_b16 v[28:29], v245 offset:15392\n\t" \
    "ds_read_b64_tr_b16 v[44:45], v245 offset:15424\n\t" \
    "ds_read_b64_tr_b16 v[68:69], v245 offset:15456\n\t" \
    "ds_read_b64_tr_b16 v[84:85], v245 offset:15488\n\t" \
    "ds_read_b64_tr_b16 v[108:109], v245 offset:15520\n\t" \
    "ds_read_b64_tr_b16 v[124:125], v245 offset:15552\n\t" \
    "ds_read_b64_tr_b16 v[148:149], v245 offset:15584\n\t" \
    "ds_read_b64_tr_b16 v[164:165], v245 offset:15616\n\t" \
    "ds_read_b64_tr_b16 v[188:189], v245 offset:15648\n\t" \
    "s_waitcnt lgkmcnt(0)\n\t" \
    "s_add_u32 m0, %[wm], 0x3c00\n\t" \
    "s_mov_b32 s44, 0x23000\n\t" \
    "buffer_load_dwordx4 v240, %[wr], s44 offen offset:0 nt lds\n\t" \
    "buffer_load_dwordx4 v241, %[wr], s44 offen offset:1024 nt lds\n\t" \
    "buffer_load_dwordx4 v242, %[wr], s44 offen offset:2048 nt lds\n\t" \
    "buffer_load_dwordx4 v243, %[wr], s44 offen offset:3072 nt lds\n\t" \
    "s_add_u32 m0, %[wm], 0x4c00\n\t" \
    "s_nop 0\n\t" \
    "buffer_load_dwordx4 v244, %[wr], s44 offen nt lds\n\t" \
    "s_barrier \n\t" \
    "s_barrier \n\t" \
    "s_waitcnt vmcnt(15)\n\t" \
    "ds_read_b64_tr_b16 v[8:9], v245 offset:0\n\t" \
    "ds_read_b64_tr_b16 v[32:33], v245 offset:32\n\t" \
    "ds_read_b64_tr_b16 v[48:49], v245 offset:64\n\t" \
    "ds_read_b64_tr_b16 v[72:73], v245 offset:96\n\t" \
    "ds_read_b64_tr_b16 v[88:89], v245 offset:128\n\t" \
    "ds_read_b64_tr_b16 v[112:113], v245 offset:160\n\t" \
    "ds_read_b64_tr_b16 v[128:129], v245 offset:192\n\t" \
    "ds_read_b64_tr_b16 v[152:153], v245 offset:224\n\t" \
    "ds_read_b64_tr_b16 v[168:169], v245 offset:256\n\t" \
    "ds_read_b64_tr_b16 v[192:193], v245 offset:288\n\t" \
    "s_waitcnt lgkmcnt(0)\n\t" \
    "s_add_u32 m0, %[wm], 0x0\n\t" \
    "s_mov_b32 s44, 0x28000\n\t" \
    "buffer_load_dwordx4 v240, %[wr], s44 offen offset:0 nt lds\n\t" \
    "buffer_load_dwordx4 v241, %[wr], s44 offen offset:1024 nt lds\n\t" \
    "buffer_load_dwordx4 v242, %[wr], s44 offen offset:2048 nt lds\n\t" \
    "buffer_load_dwordx4 v243, %[wr], s44 offen offset:3072 nt lds\n\t" \
    "s_add_u32 m0, %[wm], 0x1000\n\t" \
    "s_nop 0\n\t" \
    "buffer_load_dwordx4 v244, %[wr], s44 offen nt lds\n\t" \
    "s_barrier \n\t" \
    "s_barrier \n\t" \
    "s_waitcnt vmcnt(15)\n\t" \
    "ds_read_b64_tr_b16 v[12:13], v245 offset:5120\n\t" \
    "ds_read_b64_tr_b16 v[36:37], v245 offset:5152\n\t" \
    "ds_read_b64_tr_b16 v[52:53], v245 offset:5184\n\t" \
    "ds_read_b64_tr_b16 v[76:77], v245 offset:5216\n\t" \
    "ds_read_b64_tr_b16 v[92:93], v245 offset:5248\n\t" \
    "ds_read_b64_tr_b16 v[116:117], v245 offset:5280\n\t" \
    "ds_read_b64_tr_b16 v[132:133], v245 offset:5312\n\t" \
    "ds_read_b64_tr_b16 v[156:157], v245 offset:5344\n\t" \
    "ds_read_b64_tr_b16 v[172:173], v245 offset:5376\n\t" \
    "ds_read_b64_tr_b16 v[196:197], v245 offset:5408\n\t" \
    "s_waitcnt lgkmcnt(0)\n\t" \
    "s_add_u32 m0, %[wm], 0x1400\n\t" \
    "s_mov_b32 s44, 0x2d000\n\t" \
    "buffer_load_dwordx4 v240, %[wr], s44 offen offset:0 nt lds\n\t" \
    "buffer_load_dwordx4 v241, %[wr], s44 offen offset:1024 nt lds\n\t" \
    "buffer_load_dwordx4 v242, %[wr], s44 offen offset:2048 nt lds\n\t" \
    "buffer_load_dwordx4 v243, %[wr], s44 offen offset:3072 nt lds\n\t" \
    "s_add_u32 m0, %[wm], 0x2400\n\t" \
    "s_nop 0\n\t" \
    "buffer_load_dwordx4 v244, %[wr], s44 offen nt lds\n\t" \
    "s_barrier \n\t" \
    "s_waitcnt vmcnt(15)\n\t" \
    "ds_read_b64_tr_b16 v[202:203], v245 offset:10240\n\t" \
    "ds_read_b64_tr_b16 v[18:19], v245 offset:10272\n\t" \
    "ds_read_b64_tr_b16 v[210:211], v245 offset:10304\n\t" \
    "ds_read_b64_tr_b16 v[58:59], v245 offset:10336\n\t" \
    "ds_read_b64_tr_b16 v[218:219], v245 offset:10368\n\t" \
    "ds_read_b64_tr_b16 v[98:99], v245 offset:10400\n\t" \
    "ds_read_b64_tr_b16 v[226:227], v245 offset:10432\n\t" \
    "ds_read_b64_tr_b16 v[138:139], v245 offset:10464\n\t" \
    "ds_read_b64_tr_b16 v[234:235], v245 offset:10496\n\t" \
    "ds_read_b64_tr_b16 v[178:179], v245 offset:10528\n\t" \
    "v_mov_b32 v246, 0\n\t" \
    "buffer_load_dword v246, v246, s[60:63], %[mo] offen sc1\n\t" \
    "s_waitcnt lgkmcnt(0)\n\t" \
    "s_add_u32 m0, %[wm], 0x2800\n\t" \
    "s_mov_b32 s44, 0x32000\n\t" \
    "buffer_load_dwordx4 v240, %[wr], s44 offen offset:0 nt lds\n\t" \
    "buffer_load_dwordx4 v241, %[wr], s44 offen offset:1024 nt lds\n\t" \
    "buffer_load_dwordx4 v242, %[wr], s44 offen offset:2048 nt lds\n\t" \
    "buffer_load_dwordx4 v243, %[wr], s44 offen offset:3072 nt lds\n\t" \
    "s_add_u32 m0, %[wm], 0x3800\n\t" \
    "s_nop 0\n\t" \
    "buffer_load_dwordx4 v244, %[wr], s44 offen nt lds\n\t" \
    "s_barrier \n\t" \
    "s_barrier \n\t" \
    "s_waitcnt vmcnt(15)\n\t" \
    "ds_read_b64_tr_b16 v[206:207], v245 offset:15360\n\t" \
    "ds_read_b64_tr_b16 v[22:23], v245 offset:15392\n\t" \
    "ds_read_b64_tr_b16 v[214:215], v245 offset:15424\n\t" \
    "ds_read_b64_tr_b16 v[62:63], v245 offset:15456\n\t" \
    "ds_read_b64_tr_b16 v[222:223], v245 offset:15488\n\t" \
    "ds_read_b64_tr_b16 v[102:103], v245 offset:15520\n\t" \
    "ds_read_b64_tr_b16 v[230:231], v245 offset:15552\n\t" \
    "ds_read_b64_tr_b16 v[142:143], v245 offset:15584\n\t" \
    "ds_read_b64_tr_b16 v[238:239], v245 offset:15616\n\t" \
    "ds_read_b64_tr_b16 v[182:183], v245 offset:15648\n\t" \
    "s_waitcnt lgkmcnt(0)\n\t" \
    "s_add_u32 m0, %[wm], 0x3c00\n\t" \
    "s_mov_b32 s44, 0x37000\n\t" \
    "buffer_load_dwordx4 v240, %[wr], s44 offen offset:0 nt lds\n\t" \
    "buffer_load_dwordx4 v241, %[wr], s44 offen offset:1024 nt lds\n\t" \
    "buffer_load_dwordx4 v242, %[wr], s44 offen offset:2048 nt lds\n\t" \
    "buffer_load_dwordx4 v243, %[wr], s44 offen offset:3072 nt lds\n\t" \
    "s_add_u32 m0, %[wm], 0x4c00\n\t" \
    "s_nop 0\n\t" \
    "buffer_load_dwordx4 v244, %[wr], s44 offen nt lds\n\t" \
    "s_barrier \n\t" \
    "s_barrier \n\t" \
    "s_waitcnt vmcnt(15)\n\t" \
    "ds_read_b64_tr_b16 v[2:3], v245 offset:0\n\t" \
    "ds_read_b64_tr_b16 v[26:27], v245 offset:32\n\t" \
    "ds_read_b64_tr_b16 v[42:43], v245 offset:64\n\t" \
    "ds_read_b64_tr_b16 v[66:67], v245 offset:96\n\t" \
    "ds_read_b64_tr_b16 v[82:83], v245 offset:128\n\t" \
    "ds_read_b64_tr_b16 v[106:107], v245 offset:160\n\t" \
    "ds_read_b64_tr_b16 v[122:123], v245 offset:192\n\t" \
    "ds_read_b64_tr_b16 v[146:147], v245 offset:224\n\t" \
    "ds_read_b64_tr_b16 v[162:163], v245 offset:256\n\t" \
    "ds_read_b64_tr_b16 v[186:187], v245 offset:288\n\t" \
    "s_barrier \n\t" \
    "s_waitcnt vmcnt(10)\n\t" \
    "ds_read_b64_tr_b16 v[6:7], v245 offset:5120\n\t" \
    "ds_read_b64_tr_b16 v[30:31], v245 offset:5152\n\t" \
    "ds_read_b64_tr_b16 v[46:47], v245 offset:5184\n\t" \
    "ds_read_b64_tr_b16 v[70:71], v245 offset:5216\n\t" \
    "ds_read_b64_tr_b16 v[86:87], v245 offset:5248\n\t" \
    "ds_read_b64_tr_b16 v[110:111], v245 offset:5280\n\t" \
    "ds_read_b64_tr_b16 v[126:127], v245 offset:5312\n\t" \
    "ds_read_b64_tr_b16 v[150:151], v245 offset:5344\n\t" \
    "ds_read_b64_tr_b16 v[166:167], v245 offset:5376\n\t" \
    "ds_read_b64_tr_b16 v[190:191], v245 offset:5408\n\t" \
    "s_barrier \n\t" \
    "s_barrier \n\t" \
    "s_waitcnt vmcnt(5)\n\t" \
    "ds_read_b64_tr_b16 v[10:11], v245 offset:10240\n\t" \
    "ds_read_b64_tr_b16 v[34:35], v245 offset:10272\n\t" \
    "ds_read_b64_tr_b16 v[50:51], v245 offset:10304\n\t" \
    "ds_read_b64_tr_b16 v[74:75], v245 offset:10336\n\t" \
    "ds_read_b64_tr_b16 v[90:91], v245 offset:10368\n\t" \
    "ds_read_b64_tr_b16 v[114:115], v245 offset:10400\n\t" \
    "ds_read_b64_tr_b16 v[130:131], v245 offset:10432\n\t" \
    "ds_read_b64_tr_b16 v[154:155], v245 offset:10464\n\t" \
    "ds_read_b64_tr_b16 v[170:171], v245 offset:10496\n\t" \
    "ds_read_b64_tr_b16 v[194:195], v245 offset:10528\n\t" \
    ".Lv41_%=:\n\t" \
    "v_readfirstlane_b32 s43, v246\n\t" \
    "s_nop 0\n\t" \
    "s_cmp_lg_u32 s43, -1\n\t" \
    "s_cbranch_scc1 .Lv42_%=\n\t" \
    "s_cmp_eq_u32 s41, 0\n\t" \
    "s_cbranch_scc1 .Lv42_%=\n\t" \
    "s_sub_u32 s41, s41, 1\n\t" \
    "s_sleep 4\n\t" \
    "v_mov_b32 v246, 0\n\t" \
    "buffer_load_dword v246, v246, s[60:63], %[mo] offen sc1\n\t" \
    "s_waitcnt vmcnt(0)\n\t" \
    "s_branch .Lv41_%=\n\t" \
    ".Lv42_%=:\n\t" \
    "v_mov_b32 v246, 0x8000\n\t" \
    "s_mov_b64 exec, 1\n\t" \
    "buffer_store_dword v246, v246, s[60:63], %[mo] offen sc1\n\t" \
    "s_mov_b64 exec, -1\n\t" \
    "s_and_b32 s43, s43, 0x2000\n\t" \
    "s_xor_b32 s43, s43, %[lx]\n\t" \
    "s_add_u32 s60, s60, s43\n\t" \
    "s_addc_u32 s61, s61, 0\n\t" \
    "s_barrier \n\t" \
    "s_barrier \n\t" \
    "s_waitcnt vmcnt(0)\n\t" \
    "ds_read_b64_tr_b16 v[14:15], v245 offset:15360\n\t" \
    "ds_read_b64_tr_b16 v[38:39], v245 offset:15392\n\t" \
    "ds_read_b64_tr_b16 v[54:55], v245 offset:15424\n\t" \
    "ds_read_b64_tr_b16 v[78:79], v245 offset:15456\n\t" \
    "ds_read_b64_tr_b16 v[94:95], v245 offset:15488\n\t" \
    "ds_read_b64_tr_b16 v[118:119], v245 offset:15520\n\t" \
    "ds_read_b64_tr_b16 v[134:135], v245 offset:15552\n\t" \
    "ds_read_b64_tr_b16 v[158:159], v245 offset:15584\n\t" \
    "ds_read_b64_tr_b16 v[174:175], v245 offset:15616\n\t" \
    "ds_read_b64_tr_b16 v[198:199], v245 offset:15648\n\t" \
    "s_waitcnt lgkmcnt(0)\n\t" \
    "s_barrier \n\t" \
    LST0 \
    LJIT \
    "ds_write_b128 v254, v[200:203] offset:0\n\t" \
    "ds_write_b128 v254, v[204:207] offset:1024\n\t" \
    "ds_write_b128 v254, v[208:211] offset:2048\n\t" \
    "ds_write_b128 v254, v[212:215] offset:3072\n\t" \
    "ds_write_b128 v254, v[216:219] offset:4096\n\t" \
    "ds_write_b128 v254, v[220:223] offset:5120\n\t" \
    "ds_write_b128 v254, v[224:227] offset:6144\n\t" \
    "ds_write_b128 v254, v[228:231] offset:7168\n\t" \
    "ds_write_b128 v254, v[232:235] offset:8192\n\t" \
    "ds_write_b128 v254, v[236:239] offset:9216\n\t" \
    "s_waitcnt vmcnt(0) lgkmcnt(0)\n\t" \
    "s_barrier \n\t" \
    "v_mov_b64 v[200:201], 0\n\t" \
    "v_mov_b64 v[202:203], 0\n\t" \
    "ds_write_b128 v253, v[200:203] offset:3072\n\t" \
    "ds_write_b128 v253, v[200:203] offset:4096\n\t" \
    "v_bfe_i32 v252, v253, 5, 1\n\t" \
    "v_and_b32 v252, 0xaaaa, v252\n\t" \
    "v_or_b32 v252, 0x4444, v252\n\t" \
    ".Lv43_%=:\n\t" \
    "ds_read_b128 v[248:251], v254 offset:0\n\t" \
    "s_and_b32 s44, s40, %[vm]\n\t" \
    "s_lshl_b32 s44, s44, 20\n\t" \
    "s_and_b32 s45, s40, 2\n\t" \
    "s_lshl_b32 s45, s45, 12\n\t" \
    "s_add_u32 s44, s44, s45\n\t" \
    "s_add_u32 s42, s44, %[vx]\n\t" \
    "s_waitcnt lgkmcnt(0)\n\t" \
    "s_barrier \n\t" \
    "ds_read_b128 v[224:227], v253 offset:6144\n\t" \
    "ds_read_b128 v[232:235], v253 offset:8192\n\t" \
    "ds_read_b128 v[236:239], v253 offset:9216\n\t" \
    "ds_read_b128 v[240:243], v253 offset:10240\n\t" \
    "ds_read_b128 v[244:247], v253 offset:11264\n\t" \
    "ds_read_b128 v[228:231], v253 offset:7168\n\t" \
    "s_waitcnt lgkmcnt(5)\n\t" \
    "v_mfma_f32_16x16x32_bf16 v[200:203], v[224:227], v[16:19], 0\n\t" \
    "s_waitcnt lgkmcnt(4)\n\t" \
    "v_mfma_f32_16x16x32_bf16 v[200:203], v[232:235], v[24:27], v[200:203]\n\t" \
    "s_waitcnt lgkmcnt(3)\n\t" \
    "v_mfma_f32_16x16x32_bf16 v[200:203], v[236:239], v[28:31], v[200:203]\n\t" \
    "s_waitcnt lgkmcnt(2)\n\t" \
    "v_mfma_f32_16x16x32_bf16 v[200:203], v[240:243], v[32:35], v[200:203]\n\t" \
    "s_waitcnt lgkmcnt(1)\n\t" \
    "v_mfma_f32_16x16x32_bf16 v[200:203], v[244:247], v[36:39], v[200:203]\n\t" \
    "s_waitcnt lgkmcnt(0)\n\t" \
    "v_mfma_f32_16x16x32_bf16 v[200:203], v[228:231], v[20:23], v[200:203]\n\t" \
    "v_mfma_f32_16x16x32_bf16 v[204:207], v[224:227], v[248:251], 0\n\t" \
    "ds_read_b128 v[248:251], v254 offset:1024\n\t" \
    "v_mfma_f32_16x16x32_bf16 v[204:207], v[232:235], v[0:3], v[204:207]\n\t" \
    "v_mfma_f32_16x16x32_bf16 v[204:207], v[236:239], v[4:7], v[204:207]\n\t" \
    "v_mfma_f32_16x16x32_bf16 v[204:207], v[240:243], v[8:11], v[204:207]\n\t" \
    "v_mfma_f32_16x16x32_bf16 v[204:207], v[244:247], v[12:15], v[204:207]\n\t" \
    "s_waitcnt lgkmcnt(0)\n\t" \
    "v_mfma_f32_16x16x32_bf16 v[204:207], v[228:231], v[248:251], v[204:207]\n\t" \
    "ds_read_b128 v[248:251], v254 offset:2048\n\t" \
    "s_barrier \n\t" \
    LST7 \
    LJIT \
    "v_mfma_f32_16x16x32_bf16 v[208:211], v[224:227], v[56:59], 0\n\t" \
    "v_mfma_f32_16x16x32_bf16 v[208:211], v[232:235], v[64:67], v[208:211]\n\t" \
    "v_mfma_f32_16x16x32_bf16 v[208:211], v[236:239], v[68:71], v[208:211]\n\t" \
    "v_mfma_f32_16x16x32_bf16 v[208:211], v[240:243], v[72:75], v[208:211]\n\t" \
    "v_mfma_f32_16x16x32_bf16 v[208:211], v[244:247], v[76:79], v[208:211]\n\t" \
    "v_mfma_f32_16x16x32_bf16 v[208:211], v[228:231], v[60:63], v[208:211]\n\t" \
    "s_waitcnt lgkmcnt(0)\n\t" \
    "v_mfma_f32_16x16x32_bf16 v[212:215], v[224:227], v[248:251], 0\n\t" \
    "ds_read_b128 v[248:251], v254 offset:3072\n\t" \
    "ds_read_b128 v[224:227], v253 offset:12288\n\t" \
    "v_mfma_f32_16x16x32_bf16 v[212:215], v[232:235], v[40:43], v[212:215]\n\t" \
    "ds_read_b128 v[232:235], v253 offset:14336\n\t" \
    "v_mfma_f32_16x16x32_bf16 v[212:215], v[236:239], v[44:47], v[212:215]\n\t" \
    "ds_read_b128 v[236:239], v253 offset:15360\n\t" \
    "v_mfma_f32_16x16x32_bf16 v[212:215], v[240:243], v[48:51], v[212:215]\n\t" \
    "ds_read_b128 v[240:243], v253 offset:16384\n\t" \
    "v_mfma_f32_16x16x32_bf16 v[212:215], v[244:247], v[52:55], v[212:215]\n\t" \
    "ds_read_b128 v[244:247], v253 offset:17408\n\t" \
    "s_waitcnt lgkmcnt(5)\n\t" \
    "v_mfma_f32_16x16x32_bf16 v[212:215], v[228:231], v[248:251], v[212:215]\n\t" \
    "ds_read_b128 v[248:251], v254 offset:4096\n\t" \
    "ds_read_b128 v[228:231], v253 offset:13312\n\t" \
    "s_waitcnt lgkmcnt(6)\n\t" \
    "v_mfma_f32_16x16x32_bf16 v[216:219], v[224:227], v[16:19], 0\n\t" \
    "s_waitcnt lgkmcnt(5)\n\t" \
    "v_mfma_f32_16x16x32_bf16 v[216:219], v[232:235], v[24:27], v[216:219]\n\t" \
    "s_waitcnt lgkmcnt(4)\n\t" \
    "v_mfma_f32_16x16x32_bf16 v[216:219], v[236:239], v[28:31], v[216:219]\n\t" \
    "s_waitcnt lgkmcnt(3)\n\t" \
    "v_mfma_f32_16x16x32_bf16 v[216:219], v[240:243], v[32:35], v[216:219]\n\t" \
    "s_waitcnt lgkmcnt(2)\n\t" \
    "v_mfma_f32_16x16x32_bf16 v[216:219], v[244:247], v[36:39], v[216:219]\n\t" \
    "s_waitcnt lgkmcnt(0)\n\t" \
    "v_mfma_f32_16x16x32_bf16 v[216:219], v[228:231], v[20:23], v[216:219]\n\t" \
    "v_mfma_f32_16x16x32_bf16 v[220:223], v[224:227], v[248:251], 0\n\t" \
    "ds_read_b128 v[248:251], v254 offset:5120\n\t" \
    "v_mfma_f32_16x16x32_bf16 v[220:223], v[232:235], v[80:83], v[220:223]\n\t" \
    "v_mfma_f32_16x16x32_bf16 v[220:223], v[236:239], v[84:87], v[220:223]\n\t" \
    "v_cvt_pk_bf16_f32 v200, v200, v216\n\t" \
    "v_cvt_pk_bf16_f32 v201, v201, v217\n\t" \
    "v_mfma_f32_16x16x32_bf16 v[220:223], v[240:243], v[88:91], v[220:223]\n\t" \
    "v_cvt_pk_bf16_f32 v202, v202, v218\n\t" \
    "v_cvt_pk_bf16_f32 v203, v203, v219\n\t" \
    "v_mfma_f32_16x16x32_bf16 v[220:223], v[244:247], v[92:95], v[220:223]\n\t" \
    "s_add_u32 s44, s42, 0x10000\n\t" \
    "s_waitcnt lgkmcnt(0)\n\t" \
    "v_mfma_f32_16x16x32_bf16 v[220:223], v[228:231], v[248:251], v[220:223]\n\t" \
    "ds_read_b128 v[248:251], v254 offset:0\n\t" \
    "buffer_store_dwordx4 v[200:203], v253, s[60:63], s44 offen sc1\n\t" \
    "v_mfma_f32_16x16x32_bf16 v[216:219], v[224:227], v[56:59], 0\n\t" \
    "v_mfma_f32_16x16x32_bf16 v[216:219], v[232:235], v[64:67], v[216:219]\n\t" \
    "v_mfma_f32_16x16x32_bf16 v[216:219], v[236:239], v[68:71], v[216:219]\n\t" \
    "v_mfma_f32_16x16x32_bf16 v[216:219], v[240:243], v[72:75], v[216:219]\n\t" \
    "v_mfma_f32_16x16x32_bf16 v[216:219], v[244:247], v[76:79], v[216:219]\n\t" \
    "v_mfma_f32_16x16x32_bf16 v[216:219], v[228:231], v[60:63], v[216:219]\n\t" \
    "s_waitcnt lgkmcnt(0)\n\t" \
    "v_mfma_f32_16x16x32_bf16 v[200:203], v[224:227], v[248:251], 0\n\t" \
    "ds_read_b128 v[248:251], v254 offset:1024\n\t" \
    "v_mfma_f32_16x16x32_bf16 v[200:203], v[232:235], v[0:3], v[200:203]\n\t" \
    "v_mfma_f32_16x16x32_bf16 v[200:203], v[236:239], v[4:7], v[200:203]\n\t" \
    "v_cvt_pk_bf16_f32 v208, v208, v216\n\t" \
    "v_cvt_pk_bf16_f32 v209, v209, v217\n\t" \
    "v_mfma_f32_16x16x32_bf16 v[200:203], v[240:243], v[8:11], v[200:203]\n\t" \
    "v_cvt_pk_bf16_f32 v210, v210, v218\n\t" \
    "v_cvt_pk_bf16_f32 v211, v211, v219\n\t" \
    "v_mfma_f32_16x16x32_bf16 v[200:203], v[244:247], v[12:15], v[200:203]\n\t" \
    "s_add_u32 s44, s42, 0x30000\n\t" \
    "s_waitcnt lgkmcnt(0)\n\t" \
    "v_mfma_f32_16x16x32_bf16 v[200:203], v[228:231], v[248:251], v[200:203]\n\t" \
    "ds_read_b128 v[248:251], v254 offset:2048\n\t" \
    "buffer_store_dwordx4 v[208:211], v253, s[60:63], s44 offen sc1\n\t" \
    "v_mfma_f32_16x16x32_bf16 v[216:219], v[224:227], v[96:99], 0\n\t" \
    "v_mfma_f32_16x16x32_bf16 v[216:219], v[232:235], v[104:107], v[216:219]\n\t" \
    "v_mfma_f32_16x16x32_bf16 v[216:219], v[236:239], v[108:111], v[216:219]\n\t" \
    "v_cvt_pk_bf16_f32 v204, v204, v200\n\t" \
    "v_mfma_f32_16x16x32_bf16 v[216:219], v[240:243], v[112:115], v[216:219]\n\t" \
    "v_cvt_pk_bf16_f32 v205, v205, v201\n\t" \
    "v_cvt_pk_bf16_f32 v206, v206, v202\n\t" \
    "v_mfma_f32_16x16x32_bf16 v[216:219], v[244:247], v[116:119], v[216:219]\n\t" \
    "v_cvt_pk_bf16_f32 v207, v207, v203\n\t" \
    "v_mfma_f32_16x16x32_bf16 v[216:219], v[228:231], v[100:103], v[216:219]\n\t" \
    "s_add_u32 s44, s42, 0x0\n\t" \
    "buffer_store_dwordx4 v[204:207], v253, s[60:63], s44 offen sc1\n\t" \
    "s_waitcnt lgkmcnt(0)\n\t" \
    "v_mfma_f32_16x16x32_bf16 v[208:211], v[224:227], v[248:251], 0\n\t" \
    "ds_read_b128 v[248:251], v254 offset:3072\n\t" \
    "v_mfma_f32_16x16x32_bf16 v[208:211], v[232:235], v[40:43], v[208:211]\n\t" \
    "v_mfma_f32_16x16x32_bf16 v[208:211], v[236:239], v[44:47], v[208:211]\n\t" \
    "v_mfma_f32_16x16x32_bf16 v[208:211], v[240:243], v[48:51], v[208:211]\n\t" \
    "v_mfma_f32_16x16x32_bf16 v[208:211], v[244:247], v[52:55], v[208:211]\n\t" \
    "s_waitcnt lgkmcnt(0)\n\t" \
    "v_mfma_f32_16x16x32_bf16 v[208:211], v[228:231], v[248:251], v[208:211]\n\t" \
    "ds_read_b128 v[248:251], v254 offset:6144\n\t" \
    "v_mfma_f32_16x16x32_bf16 v[200:203], v[224:227], v[136:139], 0\n\t" \
    "v_mfma_f32_16x16x32_bf16 v[200:203], v[232:235], v[144:147], v[200:203]\n\t" \
    "v_mfma_f32_16x16x32_bf16 v[200:203], v[236:239], v[148:151], v[200:203]\n\t" \
    "v_cvt_pk_bf16_f32 v212, v212, v208\n\t" \
    "v_cvt_pk_bf16_f32 v213, v213, v209\n\t" \
    "v_mfma_f32_16x16x32_bf16 v[200:203], v[240:243], v[152:155], v[200:203]\n\t" \
    "v_cvt_pk_bf16_f32 v214, v214, v210\n\t" \
    "v_cvt_pk_bf16_f32 v215, v215, v211\n\t" \
    "v_mfma_f32_16x16x32_bf16 v[200:203], v[244:247], v[156:159], v[200:203]\n\t" \
    "s_add_u32 s44, s42, 0x20000\n\t" \
    "v_mfma_f32_16x16x32_bf16 v[200:203], v[228:231], v[140:143], v[200:203]\n\t" \
    "buffer_store_dwordx4 v[212:215], v253, s[60:63], s44 offen sc1\n\t" \
    "s_waitcnt lgkmcnt(0)\n\t" \
    "v_mfma_f32_16x16x32_bf16 v[204:207], v[224:227], v[248:251], 0\n\t" \
    "ds_read_b128 v[248:251], v254 offset:7168\n\t" \
    "ds_read_b128 v[224:227], v253 offset:6144\n\t" \
    "v_mfma_f32_16x16x32_bf16 v[204:207], v[232:235], v[120:123], v[204:207]\n\t" \
    "ds_read_b128 v[232:235], v253 offset:8192\n\t" \
    "v_mfma_f32_16x16x32_bf16 v[204:207], v[236:239], v[124:127], v[204:207]\n\t" \
    "ds_read_b128 v[236:239], v253 offset:9216\n\t" \
    "v_mfma_f32_16x16x32_bf16 v[204:207], v[240:243], v[128:131], v[204:207]\n\t" \
    "ds_read_b128 v[240:243], v253 offset:10240\n\t" \
    "v_mfma_f32_16x16x32_bf16 v[204:207], v[244:247], v[132:135], v[204:207]\n\t" \
    "ds_read_b128 v[244:247], v253 offset:11264\n\t" \
    "s_waitcnt lgkmcnt(5)\n\t" \
    "v_mfma_f32_16x16x32_bf16 v[204:207], v[228:231], v[248:251], v[204:207]\n\t" \
    "ds_read_b128 v[248:251], v254 offset:4096\n\t" \
    "ds_read_b128 v[228:231], v253 offset:7168\n\t" \
    "s_waitcnt lgkmcnt(6)\n\t" \
    "v_mfma_f32_16x16x32_bf16 v[208:211], v[224:227], v[96:99], 0\n\t" \
    "s_waitcnt lgkmcnt(5)\n\t" \
    "v_mfma_f32_16x16x32_bf16 v[208:211], v[232:235], v[104:107], v[208:211]\n\t" \
    "s_waitcnt lgkmcnt(4)\n\t" \
    "v_mfma_f32_16x16x32_bf16 v[208:211], v[236:239], v[108:111], v[208:211]\n\t" \
    "s_waitcnt lgkmcnt(3)\n\t" \
    "v_mfma_f32_16x16x32_bf16 v[208:211], v[240:243], v[112:115], v[208:211]\n\t" \
    "s_waitcnt lgkmcnt(2)\n\t" \
    "v_mfma_f32_16x16x32_bf16 v[208:211], v[244:247], v[116:119], v[208:211]\n\t" \
    "s_waitcnt lgkmcnt(0)\n\t" \
    "v_mfma_f32_16x16x32_bf16 v[208:211], v[228:231], v[100:103], v[208:211]\n\t" \
    "v_mfma_f32_16x16x32_bf16 v[212:215], v[224:227], v[248:251], 0\n\t" \
    "ds_read_b128 v[248:251], v254 offset:5120\n\t" \
    "v_mfma_f32_16x16x32_bf16 v[212:215], v[232:235], v[80:83], v[212:215]\n\t" \
    "v_mfma_f32_16x16x32_bf16 v[212:215], v[236:239], v[84:87], v[212:215]\n\t" \
    "v_cvt_pk_bf16_f32 v216, v208, v216\n\t" \
    "v_cvt_pk_bf16_f32 v217, v209, v217\n\t" \
    "v_mfma_f32_16x16x32_bf16 v[212:215], v[240:243], v[88:91], v[212:215]\n\t" \
    "v_cvt_pk_bf16_f32 v218, v210, v218\n\t" \
    "v_cvt_pk_bf16_f32 v219, v211, v219\n\t" \
    "v_mfma_f32_16x16x32_bf16 v[212:215], v[244:247], v[92:95], v[212:215]\n\t" \
    "s_add_u32 s44, s42, 0x50000\n\t" \
    "s_waitcnt lgkmcnt(0)\n\t" \
    "v_mfma_f32_16x16x32_bf16 v[212:215], v[228:231], v[248:251], v[212:215]\n\t" \
    "ds_read_b128 v[248:251], v254 offset:6144\n\t" \
    "buffer_store_dwordx4 v[216:219], v253, s[60:63], s44 offen sc1\n\t" \
    "v_mfma_f32_16x16x32_bf16 v[208:211], v[224:227], v[176:179], 0\n\t" \
    "v_mfma_f32_16x16x32_bf16 v[208:211], v[232:235], v[184:187], v[208:211]\n\t" \
    "v_mfma_f32_16x16x32_bf16 v[208:211], v[236:239], v[188:191], v[208:211]\n\t" \
    "v_cvt_pk_bf16_f32 v220, v212, v220\n\t" \
    "v_mfma_f32_16x16x32_bf16 v[208:211], v[240:243], v[192:195], v[208:211]\n\t" \
    "v_cvt_pk_bf16_f32 v221, v213, v221\n\t" \
    "v_cvt_pk_bf16_f32 v222, v214, v222\n\t" \
    "v_mfma_f32_16x16x32_bf16 v[208:211], v[244:247], v[196:199], v[208:211]\n\t" \
    "v_cvt_pk_bf16_f32 v223, v215, v223\n\t" \
    "v_mfma_f32_16x16x32_bf16 v[208:211], v[228:231], v[180:183], v[208:211]\n\t" \
    "s_add_u32 s44, s42, 0x40000\n\t" \
    "buffer_store_dwordx4 v[220:223], v253, s[60:63], s44 offen sc1\n\t" \
    "s_waitcnt lgkmcnt(0)\n\t" \
    "v_mfma_f32_16x16x32_bf16 v[216:219], v[224:227], v[248:251], 0\n\t" \
    "ds_read_b128 v[248:251], v254 offset:7168\n\t" \
    "v_mfma_f32_16x16x32_bf16 v[216:219], v[232:235], v[120:123], v[216:219]\n\t" \
    "v_mfma_f32_16x16x32_bf16 v[216:219], v[236:239], v[124:127], v[216:219]\n\t" \
    "v_mfma_f32_16x16x32_bf16 v[216:219], v[240:243], v[128:131], v[216:219]\n\t" \
    "v_mfma_f32_16x16x32_bf16 v[216:219], v[244:247], v[132:135], v[216:219]\n\t" \
    "s_waitcnt lgkmcnt(0)\n\t" \
    "v_mfma_f32_16x16x32_bf16 v[216:219], v[228:231], v[248:251], v[216:219]\n\t" \
    "ds_read_b128 v[248:251], v254 offset:8192\n\t" \
    "v_mfma_f32_16x16x32_bf16 v[212:215], v[224:227], v[136:139], 0\n\t" \
    "v_mfma_f32_16x16x32_bf16 v[212:215], v[232:235], v[144:147], v[212:215]\n\t" \
    "v_mfma_f32_16x16x32_bf16 v[212:215], v[236:239], v[148:151], v[212:215]\n\t" \
    "v_cvt_pk_bf16_f32 v204, v216, v204\n\t" \
    "v_cvt_pk_bf16_f32 v205, v217, v205\n\t" \
    "v_mfma_f32_16x16x32_bf16 v[212:215], v[240:243], v[152:155], v[212:215]\n\t" \
    "v_cvt_pk_bf16_f32 v206, v218, v206\n\t" \
    "v_cvt_pk_bf16_f32 v207, v219, v207\n\t" \
    "v_mfma_f32_16x16x32_bf16 v[212:215], v[244:247], v[156:159], v[212:215]\n\t" \
    "s_add_u32 s44, s42, 0x60000\n\t" \
    "v_mfma_f32_16x16x32_bf16 v[212:215], v[228:231], v[140:143], v[212:215]\n\t" \
    "buffer_store_dwordx4 v[204:207], v253, s[60:63], s44 offen sc1\n\t" \
    "s_waitcnt lgkmcnt(0)\n\t" \
    "v_mfma_f32_16x16x32_bf16 v[220:223], v[224:227], v[248:251], 0\n\t" \
    "ds_read_b128 v[248:251], v254 offset:9216\n\t" \
    "ds_read_b128 v[224:227], v253 offset:12288\n\t" \
    "v_mfma_f32_16x16x32_bf16 v[220:223], v[232:235], v[160:163], v[220:223]\n\t" \
    "ds_read_b128 v[232:235], v253 offset:14336\n\t" \
    "v_mfma_f32_16x16x32_bf16 v[220:223], v[236:239], v[164:167], v[220:223]\n\t" \
    "ds_read_b128 v[236:239], v253 offset:15360\n\t" \
    "v_cvt_pk_bf16_f32 v200, v212, v200\n\t" \
    "v_mfma_f32_16x16x32_bf16 v[220:223], v[240:243], v[168:171], v[220:223]\n\t" \
    "ds_read_b128 v[240:243], v253 offset:16384\n\t" \
    "v_cvt_pk_bf16_f32 v201, v213, v201\n\t" \
    "v_cvt_pk_bf16_f32 v202, v214, v202\n\t" \
    "v_mfma_f32_16x16x32_bf16 v[220:223], v[244:247], v[172:175], v[220:223]\n\t" \
    "ds_read_b128 v[244:247], v253 offset:17408\n\t" \
    "v_cvt_pk_bf16_f32 v203, v215, v203\n\t" \
    "s_waitcnt lgkmcnt(5)\n\t" \
    "v_mfma_f32_16x16x32_bf16 v[220:223], v[228:231], v[248:251], v[220:223]\n\t" \
    "ds_read_b128 v[248:251], v254 offset:8192\n\t" \
    "ds_read_b128 v[228:231], v253 offset:13312\n\t" \
    "s_add_u32 s44, s42, 0x70000\n\t" \
    "buffer_store_dwordx4 v[200:203], v253, s[60:63], s44 offen sc1\n\t" \
    "s_waitcnt lgkmcnt(6)\n\t" \
    "v_mfma_f32_16x16x32_bf16 v[216:219], v[224:227], v[176:179], 0\n\t" \
    "s_waitcnt lgkmcnt(5)\n\t" \
    "v_mfma_f32_16x16x32_bf16 v[216:219], v[232:235], v[184:187], v[216:219]\n\t" \
    "s_waitcnt lgkmcnt(4)\n\t" \
    "v_mfma_f32_16x16x32_bf16 v[216:219], v[236:239], v[188:191], v[216:219]\n\t" \
    "s_waitcnt lgkmcnt(3)\n\t" \
    "v_mfma_f32_16x16x32_bf16 v[216:219], v[240:243], v[192:195], v[216:219]\n\t" \
    "s_waitcnt lgkmcnt(2)\n\t" \
    "v_mfma_f32_16x16x32_bf16 v[216:219], v[244:247], v[196:199], v[216:219]\n\t" \
    "s_waitcnt lgkmcnt(0)\n\t" \
    "v_mfma_f32_16x16x32_bf16 v[216:219], v[228:231], v[180:183], v[216:219]\n\t" \
    "v_mfma_f32_16x16x32_bf16 v[204:207], v[224:227], v[248:251], 0\n\t" \
    "ds_read_b128 v[248:251], v254 offset:9216\n\t" \
    "v_mfma_f32_16x16x32_bf16 v[204:207], v[232:235], v[160:163], v[204:207]\n\t" \
    "v_mfma_f32_16x16x32_bf16 v[204:207], v[236:239], v[164:167], v[204:207]\n\t" \
    "v_cvt_pk_bf16_f32 v208, v208, v216\n\t" \
    "v_cvt_pk_bf16_f32 v209, v209, v217\n\t" \
    "v_mfma_f32_16x16x32_bf16 v[204:207], v[240:243], v[168:171], v[204:207]\n\t" \
    "v_cvt_pk_bf16_f32 v210, v210, v218\n\t" \
    "v_cvt_pk_bf16_f32 v211, v211, v219\n\t" \
    "v_mfma_f32_16x16x32_bf16 v[204:207], v[244:247], v[172:175], v[204:207]\n\t" \
    "s_add_u32 s44, s42, 0x90000\n\t" \
    "s_waitcnt lgkmcnt(0)\n\t" \
    "v_mfma_f32_16x16x32_bf16 v[204:207], v[228:231], v[248:251], v[204:207]\n\t" \
    "buffer_store_dwordx4 v[208:211], v253, s[60:63], s44 offen sc1\n\t" \
    "s_nop 6\n\t" \
    "v_cvt_pk_bf16_f32 v220, v220, v204\n\t" \
    "v_cvt_pk_bf16_f32 v221, v221, v205\n\t" \
    "v_cvt_pk_bf16_f32 v222, v222, v206\n\t" \
    "v_cvt_pk_bf16_f32 v223, v223, v207\n\t" \
    "s_add_u32 s44, s42, 0x80000\n\t" \
    "buffer_store_dwordx4 v[220:223], v253, s[60:63], s44 offen sc1\n\t" \
    LST0 \
    LJIT \
    "s_add_u32 s44, s40, 1\n\t" \
    "s_mov_b32 s45, s44\n\t" \
    "v_mov_b64 v[200:201], s[44:45]\n\t" \
    "v_mov_b64 v[202:203], s[44:45]\n\t" \
    "s_waitcnt lgkmcnt(0)\n\t" \
    "s_barrier \n\t" \
    LST3 \
    LJIT \
    "ds_read_b128 v[220:223], v253 offset:3072\n\t" \
    "ds_read_b128 v[224:227], v253 offset:3072\n\t" \
    "ds_read_b128 v[228:231], v253 offset:3072\n\t" \
    "ds_read_b128 v[232:235], v253 offset:3072\n\t" \
    "ds_read_b128 v[236:239], v253 offset:3072\n\t" \
    "ds_read_b128 v[240:243], v253 offset:3072\n\t" \
    "ds_read_b128 v[244:247], v253 offset:3072\n\t" \
    "ds_read_b128 v[248:251], v253 offset:3072\n\t" \
    "ds_read_b128 v[212:215], v254 offset:0\n\t" \
    "ds_read_b128 v[216:219], v254 offset:1024\n\t" \
    "s_and_b32 s44, s40, 3\n\t" \
    "s_mul_i32 s44, s44, 0x202000\n\t" \
    "s_add_u32 s42, s44, %[vo]\n\t" \
    "s_mov_b64 exec, 0xff\n\t" \
    "s_waitcnt vmcnt(0)\n\t" \
    "buffer_store_dwordx4 v[200:203], v253, s[60:63], %[fo] offen sc1\n\t" \
    "s_mov_b64 exec, -1\n\t" \
    LST1 \
    LJIT \
    "s_setprio 3\n\t" \
    "s_waitcnt lgkmcnt(0)\n\t" \
    "s_barrier \n\t" \
    LST4 \
    LJIT \
    "ds_read_b128 v[204:207], v253 offset:1024\n\t" \
    "ds_read_b128 v[208:211], v253 offset:2048\n\t" \
    "ds_read_b128 v[200:203], v253 offset:0\n\t" \
    "s_waitcnt lgkmcnt(2)\n\t" \
    "v_smfmac_f32_16x16x64_bf16 v[224:227], v[204:207], v[24:31], v252\n\t" \
    "v_smfmac_f32_16x16x64_bf16 v[220:223], v[204:207], v[0:7], v252\n\t" \
    "s_waitcnt lgkmcnt(1)\n\t" \
    "v_smfmac_f32_16x16x64_bf16 v[224:227], v[208:211], v[32:39], v252\n\t" \
    "v_smfmac_f32_16x16x64_bf16 v[220:223], v[208:211], v[8:15], v252\n\t" \
    "s_waitcnt lgkmcnt(0)\n\t" \
    "v_smfmac_f32_16x16x64_bf16 v[224:227], v[200:203], v[16:23], v252\n\t" \
    "v_smfmac_f32_16x16x64_bf16 v[220:223], v[200:203], v[212:219], v252\n\t" \
    "ds_read_b128 v[212:215], v254 offset:2048\n\t" \
    "ds_read_b128 v[216:219], v254 offset:3072\n\t" \
    "v_smfmac_f32_16x16x64_bf16 v[232:235], v[204:207], v[64:71], v252\n\t" \
    "v_smfmac_f32_16x16x64_bf16 v[228:231], v[204:207], v[40:47], v252\n\t" \
    "v_smfmac_f32_16x16x64_bf16 v[232:235], v[208:211], v[72:79], v252\n\t" \
    "v_add_f32 v220, v220, v222\n\t" \
    "v_add_f32 v221, v221, v223\n\t" \
    "v_cvt_pk_bf16_f32 v220, v220, v221\n\t" \
    "v_smfmac_f32_16x16x64_bf16 v[228:231], v[208:211], v[48:55], v252\n\t" \
    "v_min_u32 v220, -2, v220\n\t" \
    "v_add_f32 v224, v224, v226\n\t" \
    "v_add_f32 v225, v225, v227\n\t" \
    "v_smfmac_f32_16x16x64_bf16 v[232:235], v[200:203], v[56:63], v252\n\t" \
    "v_cvt_pk_bf16_f32 v221, v224, v225\n\t" \
    "v_min_u32 v221, -2, v221\n\t" \
    "s_waitcnt lgkmcnt(0)\n\t" \
    "v_smfmac_f32_16x16x64_bf16 v[228:231], v[200:203], v[212:219], v252\n\t" \
    "ds_read_b128 v[212:215], v254 offset:4096\n\t" \
    "ds_read_b128 v[216:219], v254 offset:5120\n\t" \
    "v_smfmac_f32_16x16x64_bf16 v[240:243], v[204:207], v[104:111], v252\n\t" \
    "v_smfmac_f32_16x16x64_bf16 v[236:239], v[204:207], v[80:87], v252\n\t" \
    "v_smfmac_f32_16x16x64_bf16 v[240:243], v[208:211], v[112:119], v252\n\t" \
    "v_add_f32 v228, v228, v230\n\t" \
    "v_add_f32 v229, v229, v231\n\t" \
    "v_cvt_pk_bf16_f32 v222, v228, v229\n\t" \
    "v_smfmac_f32_16x16x64_bf16 v[236:239], v[208:211], v[88:95], v252\n\t" \
    "v_min_u32 v222, -2, v222\n\t" \
    "v_add_f32 v232, v232, v234\n\t" \
    "v_add_f32 v233, v233, v235\n\t" \
    "v_smfmac_f32_16x16x64_bf16 v[240:243], v[200:203], v[96:103], v252\n\t" \
    "v_cvt_pk_bf16_f32 v223, v232, v233\n\t" \
    "v_min_u32 v223, -2, v223\n\t" \
    "v_permlane32_swap_b32 v220, v222\n\t" \
    "s_waitcnt lgkmcnt(0)\n\t" \
    "v_smfmac_f32_16x16x64_bf16 v[236:239], v[200:203], v[212:219], v252\n\t" \
    "ds_read_b128 v[212:215], v254 offset:6144\n\t" \
    "ds_read_b128 v[216:219], v254 offset:7168\n\t" \
    "s_nop 0\n\t" \
    "v_permlane32_swap_b32 v221, v223\n\t" \
    "s_nop 1\n\t" \
    "v_permlane16_swap_b32 v220, v221\n\t" \
    "v_permlane16_swap_b32 v222, v223\n\t" \
    "buffer_store_dwordx4 v[220:223], v255, s[60:63], s42 offen sc1\n\t" \
    "s_add_u32 s42, s42, 0x10000\n\t" \
    "ds_read_b128 v[224:227], v253 offset:3072\n\t" \
    "ds_read_b128 v[228:231], v253 offset:3072\n\t" \
    "v_smfmac_f32_16x16x64_bf16 v[248:251], v[204:207], v[144:151], v252\n\t" \
    "v_smfmac_f32_16x16x64_bf16 v[244:247], v[204:207], v[120:127], v252\n\t" \
    "v_smfmac_f32_16x16x64_bf16 v[248:251], v[208:211], v[152:159], v252\n\t" \
    "v_add_f32 v236, v236, v238\n\t" \
    "v_add_f32 v237, v237, v239\n\t" \
    "v_cvt_pk_bf16_f32 v236, v236, v237\n\t" \
    "v_smfmac_f32_16x16x64_bf16 v[244:247], v[208:211], v[128:135], v252\n\t" \
    "v_min_u32 v236, -2, v236\n\t" \
    "v_add_f32 v240, v240, v242\n\t" \
    "v_add_f32 v241, v241, v243\n\t" \
    "v_smfmac_f32_16x16x64_bf16 v[248:251], v[200:203], v[136:143], v252\n\t" \
    "v_cvt_pk_bf16_f32 v237, v240, v241\n\t" \
    "v_min_u32 v237, -2, v237\n\t" \
    "s_waitcnt lgkmcnt(2)\n\t" \
    "v_smfmac_f32_16x16x64_bf16 v[244:247], v[200:203], v[212:219], v252\n\t" \
    "ds_read_b128 v[212:215], v254 offset:8192\n\t" \
    "ds_read_b128 v[216:219], v254 offset:9216\n\t" \
    "s_waitcnt lgkmcnt(2)\n\t" \
    "v_smfmac_f32_16x16x64_bf16 v[228:231], v[204:207], v[184:191], v252\n\t" \
    "v_smfmac_f32_16x16x64_bf16 v[224:227], v[204:207], v[160:167], v252\n\t" \
    "v_smfmac_f32_16x16x64_bf16 v[228:231], v[208:211], v[192:199], v252\n\t" \
    "v_and_b32 v232, 0x7fff, v255\n\t" \
    "v_lshrrev_b32 v233, 15, v255\n\t" \
    "v_lshl_add_u32 v232, v233, 3, v232\n\t" \
    "v_smfmac_f32_16x16x64_bf16 v[224:227], v[208:211], v[168:175], v252\n\t" \
    "v_add_f32 v244, v244, v246\n\t" \
    "v_add_f32 v245, v245, v247\n\t" \
    "v_cvt_pk_bf16_f32 v238, v244, v245\n\t" \
    "v_smfmac_f32_16x16x64_bf16 v[228:231], v[200:203], v[176:183], v252\n\t" \
    "v_min_u32 v238, -2, v238\n\t" \
    "v_add_f32 v248, v248, v250\n\t" \
    "v_add_f32 v249, v249, v251\n\t" \
    "s_waitcnt lgkmcnt(0)\n\t" \
    "v_smfmac_f32_16x16x64_bf16 v[224:227], v[200:203], v[212:219], v252\n\t" \
    "v_cvt_pk_bf16_f32 v239, v248, v249\n\t" \
    "v_min_u32 v239, -2, v239\n\t" \
    "v_permlane32_swap_b32 v236, v238\n\t" \
    "s_nop 0\n\t" \
    "v_permlane32_swap_b32 v237, v239\n\t" \
    "s_nop 1\n\t" \
    "v_permlane16_swap_b32 v236, v237\n\t" \
    "v_permlane16_swap_b32 v238, v239\n\t" \
    "buffer_store_dwordx4 v[236:239], v255, s[60:63], s42 offen sc1\n\t" \
    "s_add_u32 s42, s42, 0x10000\n\t" \
    "v_add_f32 v228, v228, v230\n\t" \
    "v_add_f32 v229, v229, v231\n\t" \
    "v_add_f32 v224, v224, v226\n\t" \
    "v_add_f32 v225, v225, v227\n\t" \
    "v_cvt_pk_bf16_f32 v229, v228, v229\n\t" \
    "v_cvt_pk_bf16_f32 v228, v224, v225\n\t" \
    "v_min_u32 v229, -2, v229\n\t" \
    "v_min_u32 v228, -2, v228\n\t" \
    "s_nop 1\n\t" \
    "v_permlane16_swap_b32 v228, v229\n\t" \
    "buffer_store_dwordx2 v[228:229], v232, s[60:63], s42 offen sc1\n\t" \
    "s_setprio 0\n\t" \
    LST5 \
    LJIT \
    LFLUSH200 \
    "s_add_u32 s40, s40, 1\n\t" \
    "s_cmp_lg_u32 s40, 64\n\t" \
    "s_cbranch_scc1 .Lv43_%=\n\t" \
    "s_branch .Lend%=\n\t" \
    ".Lv3_%=:\n\t" \
    "s_mov_b32 s42, %[xs]\n\t" \
    "v_add_u32 v244, 0xc000, v243\n\t" \
    "s_waitcnt vmcnt(12) lgkmcnt(0)\n\t" \
    "s_barrier \n\t" \
    "s_add_u32 m0, %[wm], 0x9000\n\t" \
    "s_mov_b32 s44, 0x1e0000\n\t" \
    "buffer_load_dwordx4 v240, %[wr], s44 offen offset:0 nt lds\n\t" \
    "buffer_load_dwordx4 v241, %[wr], s44 offen offset:1024 nt lds\n\t" \
    "buffer_load_dwordx4 v242, %[wr], s44 offen offset:2048 nt lds\n\t" \
    "ds_read_b64_tr_b16 v[208:209], v244 offset:0\n\t" \
    "ds_read_b64_tr_b16 v[210:211], v244 offset:1536\n\t" \
    "ds_read_b64_tr_b16 v[40:41], v244 offset:32\n\t" \
    "ds_read_b64_tr_b16 v[42:43], v244 offset:1568\n\t" \
    "ds_read_b64_tr_b16 v[48:49], v244 offset:64\n\t" \
    "ds_read_b64_tr_b16 v[50:51], v244 offset:1600\n\t" \
    "v_add_u32 v244, 0xf000, v243\n\t" \
    "s_waitcnt vmcnt(12) lgkmcnt(0)\n\t" \
    "s_barrier \n\t" \
    "s_add_u32 m0, %[wm], 0xc000\n\t" \
    "s_mov_b32 s44, 0x240000\n\t" \
    "buffer_load_dwordx4 v240, %[wr], s44 offen offset:0 nt lds\n\t" \
    "buffer_load_dwordx4 v241, %[wr], s44 offen offset:1024 nt lds\n\t" \
    "buffer_load_dwordx4 v242, %[wr], s44 offen offset:2048 nt lds\n\t" \
    "ds_read_b64_tr_b16 v[212:213], v244 offset:0\n\t" \
    "ds_read_b64_tr_b16 v[214:215], v244 offset:1536\n\t" \
    "ds_read_b64_tr_b16 v[44:45], v244 offset:32\n\t" \
    "ds_read_b64_tr_b16 v[46:47], v244 offset:1568\n\t" \
    "ds_read_b64_tr_b16 v[52:53], v244 offset:64\n\t" \
    "ds_read_b64_tr_b16 v[54:55], v244 offset:1600\n\t" \
    "v_add_u32 v244, 0x0, v243\n\t" \
    "s_waitcnt vmcnt(12) lgkmcnt(0)\n\t" \
    "s_barrier \n\t" \
    "s_add_u32 m0, %[wm], 0xf000\n\t" \
    "s_mov_b32 s44, 0x2a0000\n\t" \
    "buffer_load_dwordx4 v240, %[wr], s44 offen offset:0 nt lds\n\t" \
    "buffer_load_dwordx4 v241, %[wr], s44 offen offset:1024 nt lds\n\t" \
    "buffer_load_dwordx4 v242, %[wr], s44 offen offset:2048 nt lds\n\t" \
    "ds_read_b64_tr_b16 v[56:57], v244 offset:0\n\t" \
    "ds_read_b64_tr_b16 v[58:59], v244 offset:1536\n\t" \
    "ds_read_b64_tr_b16 v[64:65], v244 offset:32\n\t" \
    "ds_read_b64_tr_b16 v[66:67], v244 offset:1568\n\t" \
    "ds_read_b64_tr_b16 v[72:73], v244 offset:64\n\t" \
    "ds_read_b64_tr_b16 v[74:75], v244 offset:1600\n\t" \
    "v_add_u32 v244, 0x3000, v243\n\t" \
    "s_waitcnt vmcnt(12) lgkmcnt(0)\n\t" \
    "s_barrier \n\t" \
    "s_add_u32 m0, %[wm], 0x0\n\t" \
    "s_mov_b32 s44, 0x300000\n\t" \
    "buffer_load_dwordx4 v240, %[wr], s44 offen offset:0 nt lds\n\t" \
    "buffer_load_dwordx4 v241, %[wr], s44 offen offset:1024 nt lds\n\t" \
    "buffer_load_dwordx4 v242, %[wr], s44 offen offset:2048 nt lds\n\t" \
    "ds_read_b64_tr_b16 v[60:61], v244 offset:0\n\t" \
    "ds_read_b64_tr_b16 v[62:63], v244 offset:1536\n\t" \
    "ds_read_b64_tr_b16 v[68:69], v244 offset:32\n\t" \
    "ds_read_b64_tr_b16 v[70:71], v244 offset:1568\n\t" \
    "ds_read_b64_tr_b16 v[76:77], v244 offset:64\n\t" \
    "ds_read_b64_tr_b16 v[78:79], v244 offset:1600\n\t" \
    "v_add_u32 v244, 0x6000, v243\n\t" \
    "s_waitcnt vmcnt(12) lgkmcnt(0)\n\t" \
    "s_barrier \n\t" \
    "s_add_u32 m0, %[wm], 0x3000\n\t" \
    "s_mov_b32 s44, 0x360000\n\t" \
    "buffer_load_dwordx4 v240, %[wr], s44 offen offset:0 nt lds\n\t" \
    "buffer_load_dwordx4 v241, %[wr], s44 offen offset:1024 nt lds\n\t" \
    "buffer_load_dwordx4 v242, %[wr], s44 offen offset:2048 nt lds\n\t" \
    "ds_read_b64_tr_b16 v[216:217], v244 offset:0\n\t" \
    "ds_read_b64_tr_b16 v[218:219], v244 offset:1536\n\t" \
    "ds_read_b64_tr_b16 v[80:81], v244 offset:32\n\t" \
    "ds_read_b64_tr_b16 v[82:83], v244 offset:1568\n\t" \
    "ds_read_b64_tr_b16 v[88:89], v244 offset:64\n\t" \
    "ds_read_b64_tr_b16 v[90:91], v244 offset:1600\n\t" \
    "v_add_u32 v244, 0x9000, v243\n\t" \
    "s_waitcnt vmcnt(12) lgkmcnt(0)\n\t" \
    "s_barrier \n\t" \
    "s_add_u32 m0, %[wm], 0x6000\n\t" \
    "s_mov_b32 s44, 0x3c0000\n\t" \
    "buffer_load_dwordx4 v240, %[wr], s44 offen offset:0 nt lds\n\t" \
    "buffer_load_dwordx4 v241, %[wr], s44 offen offset:1024 nt lds\n\t" \
    "buffer_load_dwordx4 v242, %[wr], s44 offen offset:2048 nt lds\n\t" \
    "ds_read_b64_tr_b16 v[220:221], v244 offset:0\n\t" \
    "ds_read_b64_tr_b16 v[222:223], v244 offset:1536\n\t" \
    "ds_read_b64_tr_b16 v[84:85], v244 offset:32\n\t" \
    "ds_read_b64_tr_b16 v[86:87], v244 offset:1568\n\t" \
    "ds_read_b64_tr_b16 v[92:93], v244 offset:64\n\t" \
    "ds_read_b64_tr_b16 v[94:95], v244 offset:1600\n\t" \
    "v_add_u32 v244, 0xc000, v243\n\t" \
    "s_waitcnt vmcnt(12) lgkmcnt(0)\n\t" \
    "s_barrier \n\t" \
    "s_add_u32 m0, %[wm], 0x9000\n\t" \
    "s_mov_b32 s44, 0x420000\n\t" \
    "buffer_load_dwordx4 v240, %[wr], s44 offen offset:0 nt lds\n\t" \
    "buffer_load_dwordx4 v241, %[wr], s44 offen offset:1024 nt lds\n\t" \
    "buffer_load_dwordx4 v242, %[wr], s44 offen offset:2048 nt lds\n\t" \
    "ds_read_b64_tr_b16 v[96:97], v244 offset:0\n\t" \
    "ds_read_b64_tr_b16 v[98:99], v244 offset:1536\n\t" \
    "ds_read_b64_tr_b16 v[104:105], v244 offset:32\n\t" \
    "ds_read_b64_tr_b16 v[106:107], v244 offset:1568\n\t" \
    "ds_read_b64_tr_b16 v[112:113], v244 offset:64\n\t" \
    "ds_read_b64_tr_b16 v[114:115], v244 offset:1600\n\t" \
    "v_add_u32 v244, 0xf000, v243\n\t" \
    "s_waitcnt vmcnt(12) lgkmcnt(0)\n\t" \
    "s_barrier \n\t" \
    "s_add_u32 m0, %[wm], 0xc000\n\t" \
    "s_mov_b32 s44, 0x480000\n\t" \
    "buffer_load_dwordx4 v240, %[wr], s44 offen offset:0 nt lds\n\t" \
    "buffer_load_dwordx4 v241, %[wr], s44 offen offset:1024 nt lds\n\t" \
    "buffer_load_dwordx4 v242, %[wr], s44 offen offset:2048 nt lds\n\t" \
    "ds_read_b64_tr_b16 v[100:101], v244 offset:0\n\t" \
    "ds_read_b64_tr_b16 v[102:103], v244 offset:1536\n\t" \
    "ds_read_b64_tr_b16 v[108:109], v244 offset:32\n\t" \
    "ds_read_b64_tr_b16 v[110:111], v244 offset:1568\n\t" \
    "ds_read_b64_tr_b16 v[116:117], v244 offset:64\n\t" \
    "ds_read_b64_tr_b16 v[118:119], v244 offset:1600\n\t" \
    "v_add_u32 v244, 0x0, v243\n\t" \
    "s_waitcnt vmcnt(12) lgkmcnt(0)\n\t" \
    "s_barrier \n\t" \
    "v_mov_b32 v246, 0\n\t" \
    "buffer_load_dword v246, v246, s[60:63], %[mo] offen sc1\n\t" \
    "s_add_u32 m0, %[wm], 0xf000\n\t" \
    "s_mov_b32 s44, 0x4e0000\n\t" \
    "buffer_load_dwordx4 v240, %[wr], s44 offen offset:0 nt lds\n\t" \
    "buffer_load_dwordx4 v241, %[wr], s44 offen offset:1024 nt lds\n\t" \
    "buffer_load_dwordx4 v242, %[wr], s44 offen offset:2048 nt lds\n\t" \
    "ds_read_b64_tr_b16 v[224:225], v244 offset:0\n\t" \
    "ds_read_b64_tr_b16 v[226:227], v244 offset:1536\n\t" \
    "ds_read_b64_tr_b16 v[120:121], v244 offset:32\n\t" \
    "ds_read_b64_tr_b16 v[122:123], v244 offset:1568\n\t" \
    "ds_read_b64_tr_b16 v[128:129], v244 offset:64\n\t" \
    "ds_read_b64_tr_b16 v[130:131], v244 offset:1600\n\t" \
    "v_add_u32 v244, 0x3000, v243\n\t" \
    "s_waitcnt vmcnt(12) lgkmcnt(0)\n\t" \
    "s_barrier \n\t" \
    "s_add_u32 m0, %[wm], 0x0\n\t" \
    "s_mov_b32 s44, 0x540000\n\t" \
    "buffer_load_dwordx4 v240, %[wr], s44 offen offset:0 nt lds\n\t" \
    "buffer_load_dwordx4 v241, %[wr], s44 offen offset:1024 nt lds\n\t" \
    "buffer_load_dwordx4 v242, %[wr], s44 offen offset:2048 nt lds\n\t" \
    "ds_read_b64_tr_b16 v[228:229], v244 offset:0\n\t" \
    "ds_read_b64_tr_b16 v[230:231], v244 offset:1536\n\t" \
    "ds_read_b64_tr_b16 v[124:125], v244 offset:32\n\t" \
    "ds_read_b64_tr_b16 v[126:127], v244 offset:1568\n\t" \
    "ds_read_b64_tr_b16 v[132:133], v244 offset:64\n\t" \
    "ds_read_b64_tr_b16 v[134:135], v244 offset:1600\n\t" \
    "v_add_u32 v244, 0x6000, v243\n\t" \
    "s_waitcnt vmcnt(12) lgkmcnt(0)\n\t" \
    "s_barrier \n\t" \
    "s_add_u32 m0, %[wm], 0x3000\n\t" \
    "s_mov_b32 s44, 0x5a0000\n\t" \
    "buffer_load_dwordx4 v240, %[wr], s44 offen offset:0 nt lds\n\t" \
    "buffer_load_dwordx4 v241, %[wr], s44 offen offset:1024 nt lds\n\t" \
    "buffer_load_dwordx4 v242, %[wr], s44 offen offset:2048 nt lds\n\t" \
    "ds_read_b64_tr_b16 v[136:137], v244 offset:0\n\t" \
    "ds_read_b64_tr_b16 v[138:139], v244 offset:1536\n\t" \
    "ds_read_b64_tr_b16 v[144:145], v244 offset:32\n\t" \
    "ds_read_b64_tr_b16 v[146:147], v244 offset:1568\n\t" \
    "ds_read_b64_tr_b16 v[152:153], v244 offset:64\n\t" \
    "ds_read_b64_tr_b16 v[154:155], v244 offset:1600\n\t" \
    "v_add_u32 v244, 0x9000, v243\n\t" \
    "s_waitcnt vmcnt(12) lgkmcnt(0)\n\t" \
    "s_barrier \n\t" \
    "s_add_u32 m0, %[wm], 0x6000\n\t" \
    "s_mov_b32 s44, 0x600000\n\t" \
    "buffer_load_dwordx4 v240, %[wr], s44 offen offset:0 nt lds\n\t" \
    "buffer_load_dwordx4 v241, %[wr], s44 offen offset:1024 nt lds\n\t" \
    "buffer_load_dwordx4 v242, %[wr], s44 offen offset:2048 nt lds\n\t" \
    "ds_read_b64_tr_b16 v[140:141], v244 offset:0\n\t" \
    "ds_read_b64_tr_b16 v[142:143], v244 offset:1536\n\t" \
    "ds_read_b64_tr_b16 v[148:149], v244 offset:32\n\t" \
    "ds_read_b64_tr_b16 v[150:151], v244 offset:1568\n\t" \
    "ds_read_b64_tr_b16 v[156:157], v244 offset:64\n\t" \
    "ds_read_b64_tr_b16 v[158:159], v244 offset:1600\n\t" \
    "v_add_u32 v244, 0xc000, v243\n\t" \
    "s_waitcnt vmcnt(12) lgkmcnt(0)\n\t" \
    "s_barrier \n\t" \
    "s_add_u32 m0, %[wm], 0x9000\n\t" \
    "s_mov_b32 s44, 0x660000\n\t" \
    "buffer_load_dwordx4 v240, %[wr], s44 offen offset:0 nt lds\n\t" \
    "buffer_load_dwordx4 v241, %[wr], s44 offen offset:1024 nt lds\n\t" \
    "buffer_load_dwordx4 v242, %[wr], s44 offen offset:2048 nt lds\n\t" \
    "ds_read_b64_tr_b16 v[232:233], v244 offset:0\n\t" \
    "ds_read_b64_tr_b16 v[234:235], v244 offset:1536\n\t" \
    "ds_read_b64_tr_b16 v[160:161], v244 offset:32\n\t" \
    "ds_read_b64_tr_b16 v[162:163], v244 offset:1568\n\t" \
    "ds_read_b64_tr_b16 v[168:169], v244 offset:64\n\t" \
    "ds_read_b64_tr_b16 v[170:171], v244 offset:1600\n\t" \
    "v_add_u32 v244, 0xf000, v243\n\t" \
    "s_waitcnt vmcnt(12) lgkmcnt(0)\n\t" \
    "s_barrier \n\t" \
    ".Lv31_%=:\n\t" \
    "v_readfirstlane_b32 s43, v246\n\t" \
    "s_nop 0\n\t" \
    "s_cmp_lg_u32 s43, -1\n\t" \
    "s_cbranch_scc1 .Lv32_%=\n\t" \
    "s_cmp_eq_u32 s41, 0\n\t" \
    "s_cbranch_scc1 .Lv32_%=\n\t" \
    "s_sub_u32 s41, s41, 1\n\t" \
    "s_sleep 4\n\t" \
    "v_mov_b32 v246, 0\n\t" \
    "buffer_load_dword v246, v246, s[60:63], %[mo] offen sc1\n\t" \
    "s_waitcnt vmcnt(0)\n\t" \
    "s_branch .Lv31_%=\n\t" \
    ".Lv32_%=:\n\t" \
    "v_mov_b32 v246, 0x8000\n\t" \
    "s_mov_b64 exec, 1\n\t" \
    "buffer_store_dword v246, v246, s[60:63], %[mo] offen sc1\n\t" \
    "s_mov_b64 exec, -1\n\t" \
    "s_and_b32 s43, s43, 0x2000\n\t" \
    "s_xor_b32 s43, s43, %[lx]\n\t" \
    "s_add_u32 s60, s60, s43\n\t" \
    "s_addc_u32 s61, s61, 0\n\t" \
    "v_and_b32 v252, 0xf0, v253\n\t" \
    "v_lshlrev_b32 v252, 3, v252\n\t" \
    "buffer_load_dword v252, v252, s[60:63], %[fl] offen sc1\n\t" \
    "s_add_u32 m0, %[wm], 0xc000\n\t" \
    "s_mov_b32 s44, 0x6c0000\n\t" \
    "buffer_load_dwordx4 v240, %[wr], s44 offen offset:0 nt lds\n\t" \
    "buffer_load_dwordx4 v241, %[wr], s44 offen offset:1024 nt lds\n\t" \
    "buffer_load_dwordx4 v242, %[wr], s44 offen offset:2048 nt lds\n\t" \
    "ds_read_b64_tr_b16 v[236:237], v244 offset:0\n\t" \
    "ds_read_b64_tr_b16 v[238:239], v244 offset:1536\n\t" \
    "ds_read_b64_tr_b16 v[164:165], v244 offset:32\n\t" \
    "ds_read_b64_tr_b16 v[166:167], v244 offset:1568\n\t" \
    "ds_read_b64_tr_b16 v[172:173], v244 offset:64\n\t" \
    "ds_read_b64_tr_b16 v[174:175], v244 offset:1600\n\t" \
    "v_add_u32 v244, 0x0, v243\n\t" \
    "s_waitcnt vmcnt(12) lgkmcnt(0)\n\t" \
    "s_barrier \n\t" \
    "s_add_u32 m0, %[wm], 0xf000\n\t" \
    "s_mov_b32 s44, 0x720000\n\t" \
    "buffer_load_dwordx4 v240, %[wr], s44 offen offset:0 nt lds\n\t" \
    "buffer_load_dwordx4 v241, %[wr], s44 offen offset:1024 nt lds\n\t" \
    "buffer_load_dwordx4 v242, %[wr], s44 offen offset:2048 nt lds\n\t" \
    "ds_read_b64_tr_b16 v[176:177], v244 offset:0\n\t" \
    "ds_read_b64_tr_b16 v[178:179], v244 offset:1536\n\t" \
    "ds_read_b64_tr_b16 v[184:185], v244 offset:32\n\t" \
    "ds_read_b64_tr_b16 v[186:187], v244 offset:1568\n\t" \
    "ds_read_b64_tr_b16 v[192:193], v244 offset:64\n\t" \
    "ds_read_b64_tr_b16 v[194:195], v244 offset:1600\n\t" \
    "v_add_u32 v244, 0x3000, v243\n\t" \
    "s_waitcnt vmcnt(12) lgkmcnt(0)\n\t" \
    "s_barrier \n\t" \
    "ds_read_b64_tr_b16 v[180:181], v244 offset:0\n\t" \
    "ds_read_b64_tr_b16 v[182:183], v244 offset:1536\n\t" \
    "ds_read_b64_tr_b16 v[188:189], v244 offset:32\n\t" \
    "ds_read_b64_tr_b16 v[190:191], v244 offset:1568\n\t" \
    "ds_read_b64_tr_b16 v[196:197], v244 offset:64\n\t" \
    "ds_read_b64_tr_b16 v[198:199], v244 offset:1600\n\t" \
    "v_add_u32 v244, 0x6000, v243\n\t" \
    "s_waitcnt vmcnt(9) lgkmcnt(0)\n\t" \
    "s_barrier \n\t" \
    "ds_read_b64_tr_b16 v[200:201], v244 offset:0\n\t" \
    "ds_read_b64_tr_b16 v[202:203], v244 offset:1536\n\t" \
    "ds_read_b64_tr_b16 v[0:1], v244 offset:32\n\t" \
    "ds_read_b64_tr_b16 v[2:3], v244 offset:1568\n\t" \
    "ds_read_b64_tr_b16 v[8:9], v244 offset:64\n\t" \
    "ds_read_b64_tr_b16 v[10:11], v244 offset:1600\n\t" \
    "v_add_u32 v244, 0x9000, v243\n\t" \
    "s_waitcnt vmcnt(6) lgkmcnt(0)\n\t" \
    "s_barrier \n\t" \
    "ds_read_b64_tr_b16 v[204:205], v244 offset:0\n\t" \
    "ds_read_b64_tr_b16 v[206:207], v244 offset:1536\n\t" \
    "ds_read_b64_tr_b16 v[4:5], v244 offset:32\n\t" \
    "ds_read_b64_tr_b16 v[6:7], v244 offset:1568\n\t" \
    "ds_read_b64_tr_b16 v[12:13], v244 offset:64\n\t" \
    "ds_read_b64_tr_b16 v[14:15], v244 offset:1600\n\t" \
    "v_add_u32 v244, 0xc000, v243\n\t" \
    "s_waitcnt vmcnt(3) lgkmcnt(0)\n\t" \
    "s_barrier \n\t" \
    "ds_read_b64_tr_b16 v[16:17], v244 offset:0\n\t" \
    "ds_read_b64_tr_b16 v[18:19], v244 offset:1536\n\t" \
    "ds_read_b64_tr_b16 v[24:25], v244 offset:32\n\t" \
    "ds_read_b64_tr_b16 v[26:27], v244 offset:1568\n\t" \
    "ds_read_b64_tr_b16 v[32:33], v244 offset:64\n\t" \
    "ds_read_b64_tr_b16 v[34:35], v244 offset:1600\n\t" \
    "s_mov_b32 s43, 0\n\t" \
    "v_cmp_eq_u32 vcc, 0, v252\n\t" \
    "s_cbranch_vccnz .Lv33_%=\n\t" \
    "s_mov_b32 m0, 0x3000\n\t" \
    "s_add_u32 s49, s42, 0x0\n\t" \
    "buffer_load_dwordx4 v253, s[60:63], s49 offen offset:0 sc1 lds\n\t" \
    "buffer_load_dwordx4 v253, s[60:63], s49 offen offset:1024 sc1 lds\n\t" \
    "buffer_load_dwordx4 v253, s[60:63], s49 offen offset:2048 sc1 lds\n\t" \
    "buffer_load_dwordx4 v253, s[60:63], s49 offen offset:3072 sc1 lds\n\t" \
    "s_mov_b32 m0, 0x4c00\n\t" \
    "s_add_u32 s49, s42, 0x4c00\n\t" \
    "buffer_load_dwordx4 v253, s[60:63], s49 offen offset:0 sc1 lds\n\t" \
    "s_mov_b32 m0, 0x8000\n\t" \
    "s_add_u32 s49, s42, 0x1000\n\t" \
    "buffer_load_dwordx4 v253, s[60:63], s49 offen offset:0 sc1 lds\n\t" \
    "buffer_load_dwordx4 v253, s[60:63], s49 offen offset:1024 sc1 lds\n\t" \
    "buffer_load_dwordx4 v253, s[60:63], s49 offen offset:2048 sc1 lds\n\t" \
    "buffer_load_dwordx4 v253, s[60:63], s49 offen offset:3072 sc1 lds\n\t" \
    "s_mov_b32 m0, 0x9c00\n\t" \
    "s_add_u32 s49, s42, 0x5c00\n\t" \
    "buffer_load_dwordx4 v253, s[60:63], s49 offen offset:0 sc1 lds\n\t" \
    "s_mov_b32 s43, 1\n\t" \
    ".Lv33_%=:\n\t" \
    "v_add_u32 v244, 0xf000, v243\n\t" \
    "s_waitcnt vmcnt(0) lgkmcnt(0)\n\t" \
    "s_barrier \n\t" \
    "ds_read_b64_tr_b16 v[20:21], v244 offset:0\n\t" \
    "ds_read_b64_tr_b16 v[22:23], v244 offset:1536\n\t" \
    "ds_read_b64_tr_b16 v[28:29], v244 offset:32\n\t" \
    "ds_read_b64_tr_b16 v[30:31], v244 offset:1568\n\t" \
    "ds_read_b64_tr_b16 v[36:37], v244 offset:64\n\t" \
    "ds_read_b64_tr_b16 v[38:39], v244 offset:1600\n\t" \
    "s_waitcnt lgkmcnt(0)\n\t" \
    "s_barrier \n\t" \
    LST0 \
    LJIT \
    "ds_write_b128 v254, v[200:203] offset:55296\n\t" \
    "ds_write_b128 v254, v[204:207] offset:56320\n\t" \
    "ds_write_b128 v254, v[208:211] offset:57344\n\t" \
    "ds_write_b128 v254, v[212:215] offset:58368\n\t" \
    "ds_write_b128 v254, v[216:219] offset:59392\n\t" \
    "ds_write_b128 v254, v[220:223] offset:60416\n\t" \
    "ds_write_b128 v254, v[224:227] offset:61440\n\t" \
    "ds_write_b128 v254, v[228:231] offset:62464\n\t" \
    "ds_write_b128 v254, v[232:235] offset:63488\n\t" \
    "ds_write_b128 v254, v[236:239] offset:64512\n\t" \
    "s_cmp_lg_u32 s43, 0\n\t" \
    "s_cbranch_scc1 .Lv34_%=\n\t" \
    "s_mov_b32 s44, 0\n\t" \
    ".Lv35_%=:\n\t" \
    "v_cmp_ge_u32 vcc, s44, v252\n\t" \
    "s_cbranch_vccz .Lv36_%=\n\t" \
    "s_cmp_eq_u32 s41, 0\n\t" \
    "s_cbranch_scc1 .Lv36_%=\n\t" \
    "s_sub_u32 s41, s41, 1\n\t" \
    "s_sleep 8\n\t" \
    "v_and_b32 v252, 0xf0, v253\n\t" \
    "v_lshlrev_b32 v252, 3, v252\n\t" \
    "buffer_load_dword v252, v252, s[60:63], %[fl] offen sc1\n\t" \
    "s_waitcnt vmcnt(0)\n\t" \
    "s_branch .Lv35_%=\n\t" \
    ".Lv36_%=:\n\t" \
    "s_mov_b32 m0, 0x3000\n\t" \
    "s_add_u32 s49, s42, 0x0\n\t" \
    "buffer_load_dwordx4 v253, s[60:63], s49 offen offset:0 sc1 lds\n\t" \
    "buffer_load_dwordx4 v253, s[60:63], s49 offen offset:1024 sc1 lds\n\t" \
    "buffer_load_dwordx4 v253, s[60:63], s49 offen offset:2048 sc1 lds\n\t" \
    "buffer_load_dwordx4 v253, s[60:63], s49 offen offset:3072 sc1 lds\n\t" \
    "s_mov_b32 m0, 0x4c00\n\t" \
    "s_add_u32 s49, s42, 0x4c00\n\t" \
    "buffer_load_dwordx4 v253, s[60:63], s49 offen offset:0 sc1 lds\n\t" \
    "s_mov_b32 m0, 0x8000\n\t" \
    "s_add_u32 s49, s42, 0x1000\n\t" \
    "buffer_load_dwordx4 v253, s[60:63], s49 offen offset:0 sc1 lds\n\t" \
    "buffer_load_dwordx4 v253, s[60:63], s49 offen offset:1024 sc1 lds\n\t" \
    "buffer_load_dwordx4 v253, s[60:63], s49 offen offset:2048 sc1 lds\n\t" \
    "buffer_load_dwordx4 v253, s[60:63], s49 offen offset:3072 sc1 lds\n\t" \
    "s_mov_b32 m0, 0x9c00\n\t" \
    "s_add_u32 s49, s42, 0x5c00\n\t" \
    "buffer_load_dwordx4 v253, s[60:63], s49 offen offset:0 sc1 lds\n\t" \
    ".Lv34_%=:\n\t" \
    "s_waitcnt vmcnt(0) lgkmcnt(0)\n\t" \
    "s_barrier \n\t" \
    "ds_read_b128 v[224:227], v253 offset:16384\n\t" \
    "ds_read_b128 v[244:247], v254 offset:55296\n\t" \
    "ds_read_b128 v[228:231], v253 offset:17408\n\t" \
    "ds_read_b128 v[232:235], v253 offset:36864\n\t" \
    "ds_read_b128 v[236:239], v253 offset:37888\n\t" \
    "ds_read_b128 v[248:251], v254 offset:56320\n\t" \
    ".Lv37_%=:\n\t" \
    LST7 \
    LJIT \
    "s_waitcnt lgkmcnt(5)\n\t" \
    "v_mfma_f32_16x16x32_bf16 v[204:207], v[0:3], v[224:227], 0\n\t" \
    "s_waitcnt lgkmcnt(3)\n\t" \
    "v_mfma_f32_16x16x32_bf16 v[204:207], v[4:7], v[228:231], v[204:207]\n\t" \
    "ds_read_b128 v[240:243], v253 offset:18432\n\t" \
    "v_mfma_f32_16x16x32_bf16 v[208:211], v[8:11], v[224:227], 0\n\t" \
    "v_mfma_f32_16x16x32_bf16 v[208:211], v[12:15], v[228:231], v[208:211]\n\t" \
    "v_mfma_f32_16x16x32_bf16 v[200:203], v[244:247], v[224:227], 0\n\t" \
    "s_waitcnt lgkmcnt(1)\n\t" \
    "v_mfma_f32_16x16x32_bf16 v[200:203], v[248:251], v[228:231], v[200:203]\n\t" \
    "v_mfma_f32_16x16x32_bf16 v[216:219], v[0:3], v[232:235], 0\n\t" \
    "ds_read_b128 v[224:227], v253 offset:19456\n\t" \
    "v_mfma_f32_16x16x32_bf16 v[216:219], v[4:7], v[236:239], v[216:219]\n\t" \
    "ds_read_b128 v[228:231], v253 offset:0\n\t" \
    "v_mfma_f32_16x16x32_bf16 v[220:223], v[8:11], v[232:235], 0\n\t" \
    "v_mfma_f32_16x16x32_bf16 v[220:223], v[12:15], v[236:239], v[220:223]\n\t" \
    "v_mfma_f32_16x16x32_bf16 v[212:215], v[244:247], v[232:235], 0\n\t" \
    "ds_read_b128 v[244:247], v254 offset:57344\n\t" \
    "v_mfma_f32_16x16x32_bf16 v[212:215], v[248:251], v[236:239], v[212:215]\n\t" \
    "ds_read_b128 v[248:251], v254 offset:58368\n\t" \
    "s_waitcnt lgkmcnt(4)\n\t" \
    "v_mfma_f32_16x16x32_bf16 v[204:207], v[24:27], v[240:243], v[204:207]\n\t" \
    "ds_read_b128 v[232:235], v253 offset:1024\n\t" \
    "s_waitcnt lgkmcnt(4)\n\t" \
    "v_mfma_f32_16x16x32_bf16 v[204:207], v[28:31], v[224:227], v[204:207]\n\t" \
    "ds_read_b128 v[236:239], v253 offset:20480\n\t" \
    "v_mfma_f32_16x16x32_bf16 v[208:211], v[32:35], v[240:243], v[208:211]\n\t" \
    "v_mfma_f32_16x16x32_bf16 v[208:211], v[36:39], v[224:227], v[208:211]\n\t" \
    "v_mfma_f32_16x16x32_bf16 v[200:203], v[16:19], v[240:243], v[200:203]\n\t" \
    "v_mfma_f32_16x16x32_bf16 v[200:203], v[20:23], v[224:227], v[200:203]\n\t" \
    "s_waitcnt lgkmcnt(4)\n\t" \
    "v_mfma_f32_16x16x32_bf16 v[204:207], v[40:43], v[228:231], v[204:207]\n\t" \
    "ds_read_b128 v[240:243], v253 offset:21504\n\t" \
    "s_waitcnt lgkmcnt(2)\n\t" \
    "v_mfma_f32_16x16x32_bf16 v[204:207], v[44:47], v[232:235], v[204:207]\n\t" \
    "ds_read_b128 v[224:227], v253 offset:2048\n\t" \
    "v_mfma_f32_16x16x32_bf16 v[208:211], v[48:51], v[228:231], v[208:211]\n\t" \
    "v_mfma_f32_16x16x32_bf16 v[208:211], v[52:55], v[232:235], v[208:211]\n\t" \
    "v_mfma_f32_16x16x32_bf16 v[200:203], v[244:247], v[228:231], v[200:203]\n\t" \
    "v_mfma_f32_16x16x32_bf16 v[200:203], v[248:251], v[232:235], v[200:203]\n\t" \
    "s_waitcnt lgkmcnt(2)\n\t" \
    "v_mfma_f32_16x16x32_bf16 v[216:219], v[40:43], v[236:239], v[216:219]\n\t" \
    "ds_read_b128 v[228:231], v253 offset:3072\n\t" \
    "s_add_u32 s44, s40, 1\n\t" \
    "s_waitcnt lgkmcnt(2)\n\t" \
    "v_mfma_f32_16x16x32_bf16 v[216:219], v[44:47], v[240:243], v[216:219]\n\t" \
    "ds_read_b128 v[232:235], v253 offset:4096\n\t" \
    "s_and_b32 s45, s44, 15\n\t" \
    "v_mfma_f32_16x16x32_bf16 v[220:223], v[48:51], v[236:239], v[220:223]\n\t" \
    "s_cmp_eq_u32 s45, 0\n\t" \
    "v_mfma_f32_16x16x32_bf16 v[220:223], v[52:55], v[240:243], v[220:223]\n\t" \
    "s_cselect_b32 s45, %[cj], %[cs]\n\t" \
    "v_mfma_f32_16x16x32_bf16 v[212:215], v[244:247], v[236:239], v[212:215]\n\t" \
    "ds_read_b128 v[244:247], v254 offset:59392\n\t" \
    "s_cmp_eq_u32 s44, 64\n\t" \
    "v_mfma_f32_16x16x32_bf16 v[212:215], v[248:251], v[240:243], v[212:215]\n\t" \
    "ds_read_b128 v[248:251], v254 offset:60416\n\t" \
    "s_cselect_b32 s45, 0, s45\n\t" \
    "s_waitcnt lgkmcnt(4)\n\t" \
    "v_mfma_f32_16x16x32_bf16 v[204:207], v[64:67], v[224:227], v[204:207]\n\t" \
    "ds_read_b128 v[236:239], v253 offset:5120\n\t" \
    "s_add_u32 s42, s42, s45\n\t" \
    "s_waitcnt lgkmcnt(4)\n\t" \
    "v_mfma_f32_16x16x32_bf16 v[204:207], v[68:71], v[228:231], v[204:207]\n\t" \
    "ds_read_b128 v[240:243], v253 offset:24576\n\t" \
    "v_mfma_f32_16x16x32_bf16 v[208:211], v[72:75], v[224:227], v[208:211]\n\t" \
    "v_mfma_f32_16x16x32_bf16 v[208:211], v[76:79], v[228:231], v[208:211]\n\t" \
    "v_mfma_f32_16x16x32_bf16 v[200:203], v[56:59], v[224:227], v[200:203]\n\t" \
    "v_mfma_f32_16x16x32_bf16 v[200:203], v[60:63], v[228:231], v[200:203]\n\t" \
    "s_waitcnt lgkmcnt(4)\n\t" \
    "v_mfma_f32_16x16x32_bf16 v[204:207], v[80:83], v[232:235], v[204:207]\n\t" \
    "ds_read_b128 v[224:227], v253 offset:25600\n\t" \
    "s_waitcnt lgkmcnt(2)\n\t" \
    "v_mfma_f32_16x16x32_bf16 v[204:207], v[84:87], v[236:239], v[204:207]\n\t" \
    "ds_read_b128 v[228:231], v253 offset:6144\n\t" \
    "v_mfma_f32_16x16x32_bf16 v[208:211], v[88:91], v[232:235], v[208:211]\n\t" \
    "v_mfma_f32_16x16x32_bf16 v[208:211], v[92:95], v[236:239], v[208:211]\n\t" \
    "v_mfma_f32_16x16x32_bf16 v[200:203], v[244:247], v[232:235], v[200:203]\n\t" \
    "v_mfma_f32_16x16x32_bf16 v[200:203], v[248:251], v[236:239], v[200:203]\n\t" \
    "s_waitcnt lgkmcnt(2)\n\t" \
    "v_mfma_f32_16x16x32_bf16 v[216:219], v[80:83], v[240:243], v[216:219]\n\t" \
    "ds_read_b128 v[232:235], v253 offset:7168\n\t" \
    "s_and_b32 s44, s40, 3\n\t" \
    "s_waitcnt lgkmcnt(2)\n\t" \
    "v_mfma_f32_16x16x32_bf16 v[216:219], v[84:87], v[224:227], v[216:219]\n\t" \
    "ds_read_b128 v[236:239], v253 offset:8192\n\t" \
    "s_lshl_b32 s44, s44, 17\n\t" \
    "v_mfma_f32_16x16x32_bf16 v[220:223], v[88:91], v[240:243], v[220:223]\n\t" \
    "s_add_u32 s44, s44, %[mb]\n\t" \
    "v_mfma_f32_16x16x32_bf16 v[220:223], v[92:95], v[224:227], v[220:223]\n\t" \
    "s_add_u32 s46, s44, 0x4000\n\t" \
    "v_mfma_f32_16x16x32_bf16 v[212:215], v[244:247], v[240:243], v[212:215]\n\t" \
    "ds_read_b128 v[244:247], v254 offset:61440\n\t" \
    "s_add_u32 s47, s44, 0x4400\n\t" \
    "v_mfma_f32_16x16x32_bf16 v[212:215], v[248:251], v[224:227], v[212:215]\n\t" \
    "ds_read_b128 v[248:251], v254 offset:62464\n\t" \
    "s_waitcnt lgkmcnt(4)\n\t" \
    "v_mfma_f32_16x16x32_bf16 v[204:207], v[104:107], v[228:231], v[204:207]\n\t" \
    "ds_read_b128 v[240:243], v253 offset:9216\n\t" \
    "s_waitcnt lgkmcnt(4)\n\t" \
    "v_mfma_f32_16x16x32_bf16 v[204:207], v[108:111], v[232:235], v[204:207]\n\t" \
    "ds_read_b128 v[224:227], v253 offset:28672\n\t" \
    "v_mfma_f32_16x16x32_bf16 v[208:211], v[112:115], v[228:231], v[208:211]\n\t" \
    "v_mfma_f32_16x16x32_bf16 v[208:211], v[116:119], v[232:235], v[208:211]\n\t" \
    "v_mfma_f32_16x16x32_bf16 v[200:203], v[96:99], v[228:231], v[200:203]\n\t" \
    "v_mfma_f32_16x16x32_bf16 v[200:203], v[100:103], v[232:235], v[200:203]\n\t" \
    "s_waitcnt lgkmcnt(4)\n\t" \
    "v_mfma_f32_16x16x32_bf16 v[204:207], v[120:123], v[236:239], v[204:207]\n\t" \
    "ds_read_b128 v[228:231], v253 offset:29696\n\t" \
    "s_waitcnt lgkmcnt(2)\n\t" \
    "v_mfma_f32_16x16x32_bf16 v[204:207], v[124:127], v[240:243], v[204:207]\n\t" \
    "ds_read_b128 v[232:235], v253 offset:10240\n\t" \
    "v_mfma_f32_16x16x32_bf16 v[208:211], v[128:131], v[236:239], v[208:211]\n\t" \
    "v_mfma_f32_16x16x32_bf16 v[208:211], v[132:135], v[240:243], v[208:211]\n\t" \
    "v_mfma_f32_16x16x32_bf16 v[200:203], v[244:247], v[236:239], v[200:203]\n\t" \
    "v_mfma_f32_16x16x32_bf16 v[200:203], v[248:251], v[240:243], v[200:203]\n\t" \
    "s_waitcnt lgkmcnt(2)\n\t" \
    "v_mfma_f32_16x16x32_bf16 v[216:219], v[120:123], v[224:227], v[216:219]\n\t" \
    "ds_read_b128 v[236:239], v253 offset:11264\n\t" \
    "s_waitcnt lgkmcnt(2)\n\t" \
    "v_mfma_f32_16x16x32_bf16 v[216:219], v[124:127], v[228:231], v[216:219]\n\t" \
    "ds_read_b128 v[240:243], v253 offset:12288\n\t" \
    "v_mfma_f32_16x16x32_bf16 v[220:223], v[128:131], v[224:227], v[220:223]\n\t" \
    "v_mfma_f32_16x16x32_bf16 v[220:223], v[132:135], v[228:231], v[220:223]\n\t" \
    "v_mfma_f32_16x16x32_bf16 v[212:215], v[244:247], v[224:227], v[212:215]\n\t" \
    "ds_read_b128 v[244:247], v254 offset:63488\n\t" \
    "v_mfma_f32_16x16x32_bf16 v[212:215], v[248:251], v[228:231], v[212:215]\n\t" \
    "ds_read_b128 v[248:251], v254 offset:64512\n\t" \
    "v_and_b32 v252, 0xf0, v253\n\t" \
    "v_lshlrev_b32 v252, 3, v252\n\t" \
    "buffer_load_dword v252, v252, s[60:63], %[fl] offen sc1\n\t" \
    "s_waitcnt lgkmcnt(4)\n\t" \
    "v_mfma_f32_16x16x32_bf16 v[204:207], v[144:147], v[232:235], v[204:207]\n\t" \
    "ds_read_b128 v[224:227], v253 offset:13312\n\t" \
    "s_waitcnt lgkmcnt(4)\n\t" \
    "v_mfma_f32_16x16x32_bf16 v[204:207], v[148:151], v[236:239], v[204:207]\n\t" \
    "ds_read_b128 v[228:231], v253 offset:32768\n\t" \
    "v_mfma_f32_16x16x32_bf16 v[208:211], v[152:155], v[232:235], v[208:211]\n\t" \
    "v_mfma_f32_16x16x32_bf16 v[208:211], v[156:159], v[236:239], v[208:211]\n\t" \
    "v_mfma_f32_16x16x32_bf16 v[200:203], v[136:139], v[232:235], v[200:203]\n\t" \
    "v_mfma_f32_16x16x32_bf16 v[200:203], v[140:143], v[236:239], v[200:203]\n\t" \
    "s_waitcnt lgkmcnt(4)\n\t" \
    "v_mfma_f32_16x16x32_bf16 v[204:207], v[160:163], v[240:243], v[204:207]\n\t" \
    "ds_read_b128 v[232:235], v253 offset:33792\n\t" \
    "s_waitcnt lgkmcnt(2)\n\t" \
    "v_mfma_f32_16x16x32_bf16 v[204:207], v[164:167], v[224:227], v[204:207]\n\t" \
    "ds_read_b128 v[236:239], v253 offset:14336\n\t" \
    "v_mfma_f32_16x16x32_bf16 v[208:211], v[168:171], v[240:243], v[208:211]\n\t" \
    "v_mfma_f32_16x16x32_bf16 v[208:211], v[172:175], v[224:227], v[208:211]\n\t" \
    "v_mfma_f32_16x16x32_bf16 v[200:203], v[244:247], v[240:243], v[200:203]\n\t" \
    "v_mfma_f32_16x16x32_bf16 v[200:203], v[248:251], v[224:227], v[200:203]\n\t" \
    "s_waitcnt lgkmcnt(2)\n\t" \
    "v_mfma_f32_16x16x32_bf16 v[216:219], v[160:163], v[228:231], v[216:219]\n\t" \
    "ds_read_b128 v[240:243], v253 offset:15360\n\t" \
    "s_waitcnt lgkmcnt(2)\n\t" \
    "v_mfma_f32_16x16x32_bf16 v[216:219], v[164:167], v[232:235], v[216:219]\n\t" \
    "ds_read_b128 v[224:227], v253 offset:38912\n\t" \
    "v_mfma_f32_16x16x32_bf16 v[220:223], v[168:171], v[228:231], v[220:223]\n\t" \
    "v_mfma_f32_16x16x32_bf16 v[220:223], v[172:175], v[232:235], v[220:223]\n\t" \
    "v_mfma_f32_16x16x32_bf16 v[212:215], v[244:247], v[228:231], v[212:215]\n\t" \
    "v_mfma_f32_16x16x32_bf16 v[212:215], v[248:251], v[232:235], v[212:215]\n\t" \
    "s_waitcnt lgkmcnt(2)\n\t" \
    "v_mfma_f32_16x16x32_bf16 v[204:207], v[184:187], v[236:239], v[204:207]\n\t" \
    "ds_read_b128 v[228:231], v253 offset:39936\n\t" \
    "s_waitcnt lgkmcnt(2)\n\t" \
    "v_mfma_f32_16x16x32_bf16 v[204:207], v[188:191], v[240:243], v[204:207]\n\t" \
    "ds_read_b128 v[232:235], v253 offset:22528\n\t" \
    "v_mfma_f32_16x16x32_bf16 v[208:211], v[192:195], v[236:239], v[208:211]\n\t" \
    "v_mfma_f32_16x16x32_bf16 v[208:211], v[196:199], v[240:243], v[208:211]\n\t" \
    "v_mfma_f32_16x16x32_bf16 v[200:203], v[176:179], v[236:239], v[200:203]\n\t" \
    "v_mfma_f32_16x16x32_bf16 v[200:203], v[180:183], v[240:243], v[200:203]\n\t" \
    "s_waitcnt lgkmcnt(2)\n\t" \
    "v_mfma_f32_16x16x32_bf16 v[216:219], v[24:27], v[224:227], v[216:219]\n\t" \
    "ds_read_b128 v[236:239], v253 offset:23552\n\t" \
    "s_waitcnt lgkmcnt(2)\n\t" \
    "v_mfma_f32_16x16x32_bf16 v[216:219], v[28:31], v[228:231], v[216:219]\n\t" \
    "ds_read_b128 v[240:243], v253 offset:26624\n\t" \
    "v_mfma_f32_16x16x32_bf16 v[220:223], v[32:35], v[224:227], v[220:223]\n\t" \
    "v_cvt_pk_bf16_f32 v204, v204, v205\n\t" \
    "v_mfma_f32_16x16x32_bf16 v[220:223], v[36:39], v[228:231], v[220:223]\n\t" \
    "v_cvt_pk_bf16_f32 v205, v206, v207\n\t" \
    "v_mfma_f32_16x16x32_bf16 v[212:215], v[16:19], v[224:227], v[212:215]\n\t" \
    "ds_write_b64 v254, v[204:205] offset:4104\n\t" \
    "v_mfma_f32_16x16x32_bf16 v[212:215], v[20:23], v[228:231], v[212:215]\n\t" \
    "v_cvt_pk_bf16_f32 v208, v208, v209\n\t" \
    "s_waitcnt lgkmcnt(3)\n\t" \
    "v_mfma_f32_16x16x32_bf16 v[216:219], v[64:67], v[232:235], v[216:219]\n\t" \
    "ds_read_b128 v[224:227], v253 offset:27648\n\t" \
    "v_cvt_pk_bf16_f32 v209, v210, v211\n\t" \
    "s_waitcnt lgkmcnt(3)\n\t" \
    "v_mfma_f32_16x16x32_bf16 v[216:219], v[68:71], v[236:239], v[216:219]\n\t" \
    "ds_read_b128 v[228:231], v253 offset:30720\n\t" \
    "ds_write_b64 v254, v[208:209] offset:5128\n\t" \
    "v_mfma_f32_16x16x32_bf16 v[220:223], v[72:75], v[232:235], v[220:223]\n\t" \
    "v_cvt_pk_bf16_f32 v200, v200, v201\n\t" \
    "v_mfma_f32_16x16x32_bf16 v[220:223], v[76:79], v[236:239], v[220:223]\n\t" \
    "v_cvt_pk_bf16_f32 v201, v202, v203\n\t" \
    "v_mfma_f32_16x16x32_bf16 v[212:215], v[56:59], v[232:235], v[212:215]\n\t" \
    "ds_write_b64 v254, v[200:201] offset:3080\n\t" \
    "v_mfma_f32_16x16x32_bf16 v[212:215], v[60:63], v[236:239], v[212:215]\n\t" \
    "s_waitcnt lgkmcnt(5)\n\t" \
    "v_mfma_f32_16x16x32_bf16 v[216:219], v[104:107], v[240:243], v[216:219]\n\t" \
    "ds_read_b128 v[232:235], v253 offset:31744\n\t" \
    "s_waitcnt lgkmcnt(4)\n\t" \
    "v_mfma_f32_16x16x32_bf16 v[216:219], v[108:111], v[224:227], v[216:219]\n\t" \
    "ds_read_b128 v[236:239], v253 offset:34816\n\t" \
    "v_mfma_f32_16x16x32_bf16 v[220:223], v[112:115], v[240:243], v[220:223]\n\t" \
    "v_mfma_f32_16x16x32_bf16 v[220:223], v[116:119], v[224:227], v[220:223]\n\t" \
    "v_mfma_f32_16x16x32_bf16 v[212:215], v[96:99], v[240:243], v[212:215]\n\t" \
    "v_mfma_f32_16x16x32_bf16 v[212:215], v[100:103], v[224:227], v[212:215]\n\t" \
    "ds_read_b128 v[240:243], v253 offset:35840\n\t" \
    "s_waitcnt lgkmcnt(3)\n\t" \
    "s_barrier \n\t" \
    "v_mfma_f32_16x16x32_bf16 v[216:219], v[144:147], v[228:231], v[216:219]\n\t" \
    "s_waitcnt lgkmcnt(2)\n\t" \
    "v_mfma_f32_16x16x32_bf16 v[216:219], v[148:151], v[232:235], v[216:219]\n\t" \
    "s_waitcnt lgkmcnt(1)\n\t" \
    "v_mfma_f32_16x16x32_bf16 v[216:219], v[184:187], v[236:239], v[216:219]\n\t" \
    "s_waitcnt lgkmcnt(0)\n\t" \
    "v_mfma_f32_16x16x32_bf16 v[216:219], v[188:191], v[240:243], v[216:219]\n\t" \
    "v_mfma_f32_16x16x32_bf16 v[220:223], v[152:155], v[228:231], v[220:223]\n\t" \
    "v_mfma_f32_16x16x32_bf16 v[220:223], v[156:159], v[232:235], v[220:223]\n\t" \
    "v_mfma_f32_16x16x32_bf16 v[220:223], v[192:195], v[236:239], v[220:223]\n\t" \
    "v_cvt_pk_bf16_f32 v216, v216, v217\n\t" \
    "v_cvt_pk_bf16_f32 v217, v218, v219\n\t" \
    "v_mfma_f32_16x16x32_bf16 v[220:223], v[196:199], v[240:243], v[220:223]\n\t" \
    "ds_write_b64 v254, v[216:217] offset:10248\n\t" \
    "v_mfma_f32_16x16x32_bf16 v[212:215], v[136:139], v[228:231], v[212:215]\n\t" \
    "v_mfma_f32_16x16x32_bf16 v[212:215], v[140:143], v[232:235], v[212:215]\n\t" \
    "v_mfma_f32_16x16x32_bf16 v[212:215], v[176:179], v[236:239], v[212:215]\n\t" \
    "v_cvt_pk_bf16_f32 v220, v220, v221\n\t" \
    "v_cvt_pk_bf16_f32 v221, v222, v223\n\t" \
    "v_mfma_f32_16x16x32_bf16 v[212:215], v[180:183], v[240:243], v[212:215]\n\t" \
    "ds_write_b64 v254, v[220:221] offset:11272\n\t" \
    "s_nop 6\n\t" \
    "v_cvt_pk_bf16_f32 v212, v212, v213\n\t" \
    "v_cvt_pk_bf16_f32 v213, v214, v215\n\t" \
    "ds_write_b64 v254, v[212:213] offset:9224\n\t" \
    LST6 \
    LJIT \
    "s_waitcnt vmcnt(0) lgkmcnt(0)\n\t" \
    "s_barrier \n\t" \
    "s_setprio 2\n\t" \
    "s_add_u32 s44, s40, 1\n\t" \
    "s_min_u32 s44, s44, 63\n\t" \
    ".Lv38_%=:\n\t" \
    "v_cmp_ge_u32 vcc, s44, v252\n\t" \
    "s_cbranch_vccz .Lv39_%=\n\t" \
    "s_cmp_eq_u32 s41, 0\n\t" \
    "s_cbranch_scc1 .Lv39_%=\n\t" \
    "s_sub_u32 s41, s41, 1\n\t" \
    "s_sleep 2\n\t" \
    "v_and_b32 v252, 0xf0, v253\n\t" \
    "v_lshlrev_b32 v252, 3, v252\n\t" \
    "buffer_load_dword v252, v252, s[60:63], %[fl] offen sc1\n\t" \
    "s_waitcnt vmcnt(0)\n\t" \
    "s_branch .Lv38_%=\n\t" \
    ".Lv39_%=:\n\t" \
    "s_mov_b32 m0, 0x3000\n\t" \
    "s_add_u32 s49, s42, 0x0\n\t" \
    "buffer_load_dwordx4 v253, s[60:63], s49 offen offset:0 sc1 lds\n\t" \
    "s_sleep 1\n\t" \
    "buffer_load_dwordx4 v253, s[60:63], s49 offen offset:1024 sc1 lds\n\t" \
    "s_sleep 1\n\t" \
    "buffer_load_dwordx4 v253, s[60:63], s49 offen offset:2048 sc1 lds\n\t" \
    "s_sleep 1\n\t" \
    "buffer_load_dwordx4 v253, s[60:63], s49 offen offset:3072 sc1 lds\n\t" \
    "s_sleep 1\n\t" \
    "s_mov_b32 m0, 0x4c00\n\t" \
    "s_add_u32 s49, s42, 0x4c00\n\t" \
    "buffer_load_dwordx4 v253, s[60:63], s49 offen offset:0 sc1 lds\n\t" \
    "s_sleep 1\n\t" \
    "s_mov_b32 m0, 0x8000\n\t" \
    "s_add_u32 s49, s42, 0x1000\n\t" \
    "buffer_load_dwordx4 v253, s[60:63], s49 offen offset:0 sc1 lds\n\t" \
    "s_sleep 1\n\t" \
    "buffer_load_dwordx4 v253, s[60:63], s49 offen offset:1024 sc1 lds\n\t" \
    "s_sleep 1\n\t" \
    "buffer_load_dwordx4 v253, s[60:63], s49 offen offset:2048 sc1 lds\n\t" \
    "s_sleep 1\n\t" \
    "buffer_load_dwordx4 v253, s[60:63], s49 offen offset:3072 sc1 lds\n\t" \
    "s_sleep 1\n\t" \
    "s_mov_b32 m0, 0x9c00\n\t" \
    "s_add_u32 s49, s42, 0x5c00\n\t" \
    "buffer_load_dwordx4 v253, s[60:63], s49 offen offset:0 sc1 lds\n\t" \
    "s_sleep 1\n\t" \
    "ds_read_b128 v[200:203], v254 offset:55296\n\t" \
    "ds_read_b128 v[204:207], v254 offset:56320\n\t" \
    "ds_read_b128 v[216:219], v254 offset:57344\n\t" \
    "ds_read_b128 v[220:223], v254 offset:58368\n\t" \
    "v_mov_b64 v[208:209], 0\n\t" \
    "v_mov_b64 v[210:211], 0\n\t" \
    "v_mov_b64 v[244:245], 0\n\t" \
    "v_mov_b64 v[246:247], 0\n\t" \
    "v_mov_b64 v[248:249], 0\n\t" \
    "v_mov_b64 v[250:251], 0\n\t" \
    LST1 \
    LJIT \
    "s_sleep 5\n\t" \
    "s_mov_b32 m0, 0xc000\n\t" \
    "s_nop 0\n\t" \
    "buffer_load_dwordx4 v253, s[60:63], s46 offen sc1 lds\n\t" \
    "s_mov_b32 m0, 0xc400\n\t" \
    "s_nop 0\n\t" \
    "buffer_load_dwordx4 v253, s[60:63], s47 offen sc1 lds\n\t" \
    "s_waitcnt lgkmcnt(0)\n\t" \
    LFLUSH212 \
    "v_bfe_i32 v212, v253, 5, 1\n\t" \
    "v_and_b32 v212, 0xaaaa, v212\n\t" \
    "v_or_b32 v212, 0x4444, v212\n\t" \
    "v_and_b32 v213, 0xffff, v255\n\t" \
    "v_lshrrev_b32 v214, 16, v255\n\t" \
    ".Lv310_%=:\n\t" \
    "s_waitcnt vmcnt(0)\n\t" \
    "ds_read_b128 v[224:227], v253 offset:49152\n\t" \
    "ds_read_b128 v[228:231], v253 offset:50176\n\t" \
    "s_waitcnt lgkmcnt(1)\n\t" \
    "v_max_u32 v215, v224, v225\n\t" \
    "v_max3_u32 v215, v226, v227, v215\n\t" \
    "s_waitcnt lgkmcnt(0)\n\t" \
    "v_max3_u32 v215, v228, v229, v215\n\t" \
    "v_max3_u32 v215, v230, v231, v215\n\t" \
    "v_cmp_eq_u32 vcc, -1, v215\n\t" \
    "s_cbranch_vccz .Lv311_%=\n\t" \
    "v_max_u32 v215, v224, v225\n\t" \
    "v_max3_u32 v215, v226, v227, v215\n\t" \
    "v_cmp_eq_u32 vcc, -1, v215\n\t" \
    "s_cbranch_vccz .Lv312_%=\n\t" \
    "s_mov_b32 m0, 0xc000\n\t" \
    "s_nop 0\n\t" \
    "buffer_load_dwordx4 v253, s[60:63], s46 offen sc1 lds\n\t" \
    ".Lv312_%=:\n\t" \
    "v_max_u32 v215, v228, v229\n\t" \
    "v_max3_u32 v215, v230, v231, v215\n\t" \
    "v_cmp_eq_u32 vcc, -1, v215\n\t" \
    "s_cbranch_vccz .Lv313_%=\n\t" \
    "s_mov_b32 m0, 0xc400\n\t" \
    "s_nop 0\n\t" \
    "buffer_load_dwordx4 v253, s[60:63], s47 offen sc1 lds\n\t" \
    ".Lv313_%=:\n\t" \
    "s_cmp_eq_u32 s41, 0\n\t" \
    "s_cbranch_scc1 .Lv311_%=\n\t" \
    "s_sub_u32 s41, s41, 1\n\t" \
    "s_branch .Lv310_%=\n\t" \
    ".Lv311_%=:\n\t" \
    LST2 \
    LJIT \
    "s_waitcnt lgkmcnt(0)\n\t" \
    "s_barrier \n\t" \
    LST3 \
    LJIT \
    "ds_read_b128 v[232:235], v253 offset:40960\n\t" \
    "ds_read_b128 v[236:239], v253 offset:41984\n\t" \
    "ds_read_b128 v[240:243], v253 offset:43008\n\t" \
    "v_smfmac_f32_16x16x64_bf16 v[244:247], v[224:227], v[0:7], v212\n\t" \
    "v_smfmac_f32_16x16x64_bf16 v[248:251], v[224:227], v[8:15], v212\n\t" \
    "v_smfmac_f32_16x16x64_bf16 v[208:211], v[224:227], v[200:207], v212\n\t" \
    "ds_read_b128 v[200:203], v254 offset:59392\n\t" \
    "ds_read_b128 v[204:207], v254 offset:60416\n\t" \
    "ds_read_b128 v[224:227], v253 offset:44032\n\t" \
    "v_smfmac_f32_16x16x64_bf16 v[208:211], v[228:231], v[16:23], v212\n\t" \
    "v_smfmac_f32_16x16x64_bf16 v[244:247], v[228:231], v[24:31], v212\n\t" \
    "v_smfmac_f32_16x16x64_bf16 v[248:251], v[228:231], v[32:39], v212\n\t" \
    "ds_read_b128 v[228:231], v253 offset:45056\n\t" \
    "s_waitcnt lgkmcnt(6)\n\t" \
    "v_smfmac_f32_16x16x64_bf16 v[208:211], v[232:235], v[216:223], v212\n\t" \
    "ds_read_b128 v[216:219], v254 offset:61440\n\t" \
    "ds_read_b128 v[220:223], v254 offset:62464\n\t" \
    "v_smfmac_f32_16x16x64_bf16 v[244:247], v[232:235], v[40:47], v212\n\t" \
    "v_smfmac_f32_16x16x64_bf16 v[248:251], v[232:235], v[48:55], v212\n\t" \
    "ds_read_b128 v[232:235], v253 offset:46080\n\t" \
    "s_waitcnt lgkmcnt(8)\n\t" \
    "v_smfmac_f32_16x16x64_bf16 v[208:211], v[236:239], v[56:63], v212\n\t" \
    "v_smfmac_f32_16x16x64_bf16 v[244:247], v[236:239], v[64:71], v212\n\t" \
    "v_smfmac_f32_16x16x64_bf16 v[248:251], v[236:239], v[72:79], v212\n\t" \
    "ds_read_b128 v[236:239], v253 offset:47104\n\t" \
    "s_waitcnt lgkmcnt(6)\n\t" \
    "v_smfmac_f32_16x16x64_bf16 v[208:211], v[240:243], v[200:207], v212\n\t" \
    "ds_read_b128 v[200:203], v254 offset:63488\n\t" \
    "ds_read_b128 v[204:207], v254 offset:64512\n\t" \
    "v_smfmac_f32_16x16x64_bf16 v[244:247], v[240:243], v[80:87], v212\n\t" \
    "v_smfmac_f32_16x16x64_bf16 v[248:251], v[240:243], v[88:95], v212\n\t" \
    "ds_read_b128 v[240:243], v253 offset:48128\n\t" \
    "s_waitcnt lgkmcnt(8)\n\t" \
    "v_smfmac_f32_16x16x64_bf16 v[208:211], v[224:227], v[96:103], v212\n\t" \
    "v_smfmac_f32_16x16x64_bf16 v[244:247], v[224:227], v[104:111], v212\n\t" \
    "v_smfmac_f32_16x16x64_bf16 v[248:251], v[224:227], v[112:119], v212\n\t" \
    "s_waitcnt lgkmcnt(7)\n\t" \
    "v_smfmac_f32_16x16x64_bf16 v[244:247], v[228:231], v[120:127], v212\n\t" \
    "s_waitcnt lgkmcnt(4)\n\t" \
    "v_smfmac_f32_16x16x64_bf16 v[244:247], v[232:235], v[144:151], v212\n\t" \
    "s_waitcnt lgkmcnt(3)\n\t" \
    "v_smfmac_f32_16x16x64_bf16 v[244:247], v[236:239], v[160:167], v212\n\t" \
    "s_waitcnt lgkmcnt(0)\n\t" \
    "v_smfmac_f32_16x16x64_bf16 v[244:247], v[240:243], v[184:191], v212\n\t" \
    "v_smfmac_f32_16x16x64_bf16 v[248:251], v[228:231], v[128:135], v212\n\t" \
    "v_smfmac_f32_16x16x64_bf16 v[248:251], v[232:235], v[152:159], v212\n\t" \
    "v_smfmac_f32_16x16x64_bf16 v[248:251], v[236:239], v[168:175], v212\n\t" \
    "v_add_f32 v244, v244, v246\n\t" \
    "v_add_f32 v245, v245, v247\n\t" \
    "v_cvt_pk_bf16_f32 v244, v244, v245\n\t" \
    "v_smfmac_f32_16x16x64_bf16 v[248:251], v[240:243], v[192:199], v212\n\t" \
    "ds_write_b16 v213, v244 offset:512\n\t" \
    "ds_write_b16_d16_hi v213, v244 offset:528\n\t" \
    "v_smfmac_f32_16x16x64_bf16 v[208:211], v[228:231], v[216:223], v212\n\t" \
    "v_smfmac_f32_16x16x64_bf16 v[208:211], v[232:235], v[136:143], v212\n\t" \
    "v_smfmac_f32_16x16x64_bf16 v[208:211], v[236:239], v[200:207], v212\n\t" \
    "v_add_f32 v248, v248, v250\n\t" \
    "v_add_f32 v249, v249, v251\n\t" \
    "v_cvt_pk_bf16_f32 v248, v248, v249\n\t" \
    "v_smfmac_f32_16x16x64_bf16 v[208:211], v[240:243], v[176:183], v212\n\t" \
    "ds_write_b16 v213, v248 offset:1024\n\t" \
    "ds_write_b16_d16_hi v213, v248 offset:1040\n\t" \
    "s_nop 5\n\t" \
    "v_add_f32 v208, v208, v210\n\t" \
    "v_add_f32 v209, v209, v211\n\t" \
    "v_cvt_pk_bf16_f32 v208, v208, v209\n\t" \
    "ds_write_b16 v213, v208 offset:0\n\t" \
    "ds_write_b16_d16_hi v213, v208 offset:16\n\t" \
    "s_setprio 0\n\t" \
    "s_waitcnt lgkmcnt(0)\n\t" \
    "s_barrier \n\t" \
    LST4 \
    LJIT \
    "ds_read_b128 v[224:227], v253 offset:16384\n\t" \
    "ds_read_b128 v[244:247], v254 offset:55296\n\t" \
    "ds_read_b128 v[228:231], v253 offset:17408\n\t" \
    "ds_read_b128 v[232:235], v253 offset:36864\n\t" \
    "ds_read_b128 v[236:239], v253 offset:37888\n\t" \
    "ds_read_b128 v[248:251], v254 offset:56320\n\t" \
    "s_add_u32 s40, s40, 1\n\t" \
    "s_cmp_lg_u32 s40, 64\n\t" \
    "s_cbranch_scc1 .Lv37_%=\n\t" \
    "s_branch .Lend%=\n\t" \
    ".Lv2_%=:\n\t" \
    "s_mov_b32 s42, %[xs]\n\t" \
    "v_add_u32 v244, 0xc000, v243\n\t" \
    "s_waitcnt vmcnt(12) lgkmcnt(0)\n\t" \
    "s_barrier \n\t" \
    "s_add_u32 m0, %[wm], 0x9000\n\t" \
    "s_mov_b32 s44, 0x1e0000\n\t" \
    "buffer_load_dwordx4 v240, %[wr], s44 offen offset:0 nt lds\n\t" \
    "buffer_load_dwordx4 v241, %[wr], s44 offen offset:1024 nt lds\n\t" \
    "buffer_load_dwordx4 v242, %[wr], s44 offen offset:2048 nt lds\n\t" \
    "ds_read_b64_tr_b16 v[208:209], v244 offset:0\n\t" \
    "ds_read_b64_tr_b16 v[210:211], v244 offset:1536\n\t" \
    "ds_read_b64_tr_b16 v[40:41], v244 offset:32\n\t" \
    "ds_read_b64_tr_b16 v[42:43], v244 offset:1568\n\t" \
    "ds_read_b64_tr_b16 v[48:49], v244 offset:64\n\t" \
    "ds_read_b64_tr_b16 v[50:51], v244 offset:1600\n\t" \
    "v_add_u32 v244, 0xf000, v243\n\t" \
    "s_waitcnt vmcnt(12) lgkmcnt(0)\n\t" \
    "s_barrier \n\t" \
    "s_add_u32 m0, %[wm], 0xc000\n\t" \
    "s_mov_b32 s44, 0x240000\n\t" \
    "buffer_load_dwordx4 v240, %[wr], s44 offen offset:0 nt lds\n\t" \
    "buffer_load_dwordx4 v241, %[wr], s44 offen offset:1024 nt lds\n\t" \
    "buffer_load_dwordx4 v242, %[wr], s44 offen offset:2048 nt lds\n\t" \
    "ds_read_b64_tr_b16 v[212:213], v244 offset:0\n\t" \
    "ds_read_b64_tr_b16 v[214:215], v244 offset:1536\n\t" \
    "ds_read_b64_tr_b16 v[44:45], v244 offset:32\n\t" \
    "ds_read_b64_tr_b16 v[46:47], v244 offset:1568\n\t" \
    "ds_read_b64_tr_b16 v[52:53], v244 offset:64\n\t" \
    "ds_read_b64_tr_b16 v[54:55], v244 offset:1600\n\t" \
    "v_add_u32 v244, 0x0, v243\n\t" \
    "s_waitcnt vmcnt(12) lgkmcnt(0)\n\t" \
    "s_barrier \n\t" \
    "s_add_u32 m0, %[wm], 0xf000\n\t" \
    "s_mov_b32 s44, 0x2a0000\n\t" \
    "buffer_load_dwordx4 v240, %[wr], s44 offen offset:0 nt lds\n\t" \
    "buffer_load_dwordx4 v241, %[wr], s44 offen offset:1024 nt lds\n\t" \
    "buffer_load_dwordx4 v242, %[wr], s44 offen offset:2048 nt lds\n\t" \
    "ds_read_b64_tr_b16 v[56:57], v244 offset:0\n\t" \
    "ds_read_b64_tr_b16 v[58:59], v244 offset:1536\n\t" \
    "ds_read_b64_tr_b16 v[64:65], v244 offset:32\n\t" \
    "ds_read_b64_tr_b16 v[66:67], v244 offset:1568\n\t" \
    "ds_read_b64_tr_b16 v[72:73], v244 offset:64\n\t" \
    "ds_read_b64_tr_b16 v[74:75], v244 offset:1600\n\t" \
    "v_add_u32 v244, 0x3000, v243\n\t" \
    "s_waitcnt vmcnt(12) lgkmcnt(0)\n\t" \
    "s_barrier \n\t" \
    "s_add_u32 m0, %[wm], 0x0\n\t" \
    "s_mov_b32 s44, 0x300000\n\t" \
    "buffer_load_dwordx4 v240, %[wr], s44 offen offset:0 nt lds\n\t" \
    "buffer_load_dwordx4 v241, %[wr], s44 offen offset:1024 nt lds\n\t" \
    "buffer_load_dwordx4 v242, %[wr], s44 offen offset:2048 nt lds\n\t" \
    "ds_read_b64_tr_b16 v[60:61], v244 offset:0\n\t" \
    "ds_read_b64_tr_b16 v[62:63], v244 offset:1536\n\t" \
    "ds_read_b64_tr_b16 v[68:69], v244 offset:32\n\t" \
    "ds_read_b64_tr_b16 v[70:71], v244 offset:1568\n\t" \
    "ds_read_b64_tr_b16 v[76:77], v244 offset:64\n\t" \
    "ds_read_b64_tr_b16 v[78:79], v244 offset:1600\n\t" \
    "v_add_u32 v244, 0x6000, v243\n\t" \
    "s_waitcnt vmcnt(12) lgkmcnt(0)\n\t" \
    "s_barrier \n\t" \
    "s_add_u32 m0, %[wm], 0x3000\n\t" \
    "s_mov_b32 s44, 0x360000\n\t" \
    "buffer_load_dwordx4 v240, %[wr], s44 offen offset:0 nt lds\n\t" \
    "buffer_load_dwordx4 v241, %[wr], s44 offen offset:1024 nt lds\n\t" \
    "buffer_load_dwordx4 v242, %[wr], s44 offen offset:2048 nt lds\n\t" \
    "ds_read_b64_tr_b16 v[216:217], v244 offset:0\n\t" \
    "ds_read_b64_tr_b16 v[218:219], v244 offset:1536\n\t" \
    "ds_read_b64_tr_b16 v[80:81], v244 offset:32\n\t" \
    "ds_read_b64_tr_b16 v[82:83], v244 offset:1568\n\t" \
    "ds_read_b64_tr_b16 v[88:89], v244 offset:64\n\t" \
    "ds_read_b64_tr_b16 v[90:91], v244 offset:1600\n\t" \
    "v_add_u32 v244, 0x9000, v243\n\t" \
    "s_waitcnt vmcnt(12) lgkmcnt(0)\n\t" \
    "s_barrier \n\t" \
    "s_add_u32 m0, %[wm], 0x6000\n\t" \
    "s_mov_b32 s44, 0x3c0000\n\t" \
    "buffer_load_dwordx4 v240, %[wr], s44 offen offset:0 nt lds\n\t" \
    "buffer_load_dwordx4 v241, %[wr], s44 offen offset:1024 nt lds\n\t" \
    "buffer_load_dwordx4 v242, %[wr], s44 offen offset:2048 nt lds\n\t" \
    "ds_read_b64_tr_b16 v[220:221], v244 offset:0\n\t" \
    "ds_read_b64_tr_b16 v[222:223], v244 offset:1536\n\t" \
    "ds_read_b64_tr_b16 v[84:85], v244 offset:32\n\t" \
    "ds_read_b64_tr_b16 v[86:87], v244 offset:1568\n\t" \
    "ds_read_b64_tr_b16 v[92:93], v244 offset:64\n\t" \
    "ds_read_b64_tr_b16 v[94:95], v244 offset:1600\n\t" \
    "v_add_u32 v244, 0xc000, v243\n\t" \
    "s_waitcnt vmcnt(12) lgkmcnt(0)\n\t" \
    "s_barrier \n\t" \
    "s_add_u32 m0, %[wm], 0x9000\n\t" \
    "s_mov_b32 s44, 0x420000\n\t" \
    "buffer_load_dwordx4 v240, %[wr], s44 offen offset:0 nt lds\n\t" \
    "buffer_load_dwordx4 v241, %[wr], s44 offen offset:1024 nt lds\n\t" \
    "buffer_load_dwordx4 v242, %[wr], s44 offen offset:2048 nt lds\n\t" \
    "ds_read_b64_tr_b16 v[96:97], v244 offset:0\n\t" \
    "ds_read_b64_tr_b16 v[98:99], v244 offset:1536\n\t" \
    "ds_read_b64_tr_b16 v[104:105], v244 offset:32\n\t" \
    "ds_read_b64_tr_b16 v[106:107], v244 offset:1568\n\t" \
    "ds_read_b64_tr_b16 v[112:113], v244 offset:64\n\t" \
    "ds_read_b64_tr_b16 v[114:115], v244 offset:1600\n\t" \
    "v_add_u32 v244, 0xf000, v243\n\t" \
    "s_waitcnt vmcnt(12) lgkmcnt(0)\n\t" \
    "s_barrier \n\t" \
    "s_add_u32 m0, %[wm], 0xc000\n\t" \
    "s_mov_b32 s44, 0x480000\n\t" \
    "buffer_load_dwordx4 v240, %[wr], s44 offen offset:0 nt lds\n\t" \
    "buffer_load_dwordx4 v241, %[wr], s44 offen offset:1024 nt lds\n\t" \
    "buffer_load_dwordx4 v242, %[wr], s44 offen offset:2048 nt lds\n\t" \
    "ds_read_b64_tr_b16 v[100:101], v244 offset:0\n\t" \
    "ds_read_b64_tr_b16 v[102:103], v244 offset:1536\n\t" \
    "ds_read_b64_tr_b16 v[108:109], v244 offset:32\n\t" \
    "ds_read_b64_tr_b16 v[110:111], v244 offset:1568\n\t" \
    "ds_read_b64_tr_b16 v[116:117], v244 offset:64\n\t" \
    "ds_read_b64_tr_b16 v[118:119], v244 offset:1600\n\t" \
    "v_add_u32 v244, 0x0, v243\n\t" \
    "s_waitcnt vmcnt(12) lgkmcnt(0)\n\t" \
    "s_barrier \n\t" \
    "v_mov_b32 v246, 0\n\t" \
    "buffer_load_dword v246, v246, s[60:63], %[mo] offen sc1\n\t" \
    "s_add_u32 m0, %[wm], 0xf000\n\t" \
    "s_mov_b32 s44, 0x4e0000\n\t" \
    "buffer_load_dwordx4 v240, %[wr], s44 offen offset:0 nt lds\n\t" \
    "buffer_load_dwordx4 v241, %[wr], s44 offen offset:1024 nt lds\n\t" \
    "buffer_load_dwordx4 v242, %[wr], s44 offen offset:2048 nt lds\n\t" \
    "ds_read_b64_tr_b16 v[200:201], v244 offset:0\n\t" \
    "ds_read_b64_tr_b16 v[202:203], v244 offset:1536\n\t" \
    "ds_read_b64_tr_b16 v[0:1], v244 offset:32\n\t" \
    "ds_read_b64_tr_b16 v[2:3], v244 offset:1568\n\t" \
    "ds_read_b64_tr_b16 v[8:9], v244 offset:64\n\t" \
    "ds_read_b64_tr_b16 v[10:11], v244 offset:1600\n\t" \
    "v_add_u32 v244, 0x3000, v243\n\t" \
    "s_waitcnt vmcnt(12) lgkmcnt(0)\n\t" \
    "s_barrier \n\t" \
    "s_add_u32 m0, %[wm], 0x0\n\t" \
    "s_mov_b32 s44, 0x540000\n\t" \
    "buffer_load_dwordx4 v240, %[wr], s44 offen offset:0 nt lds\n\t" \
    "buffer_load_dwordx4 v241, %[wr], s44 offen offset:1024 nt lds\n\t" \
    "buffer_load_dwordx4 v242, %[wr], s44 offen offset:2048 nt lds\n\t" \
    "ds_read_b64_tr_b16 v[204:205], v244 offset:0\n\t" \
    "ds_read_b64_tr_b16 v[206:207], v244 offset:1536\n\t" \
    "ds_read_b64_tr_b16 v[4:5], v244 offset:32\n\t" \
    "ds_read_b64_tr_b16 v[6:7], v244 offset:1568\n\t" \
    "ds_read_b64_tr_b16 v[12:13], v244 offset:64\n\t" \
    "ds_read_b64_tr_b16 v[14:15], v244 offset:1600\n\t" \
    "v_add_u32 v244, 0x6000, v243\n\t" \
    "s_waitcnt vmcnt(12) lgkmcnt(0)\n\t" \
    "s_barrier \n\t" \
    "s_add_u32 m0, %[wm], 0x3000\n\t" \
    "s_mov_b32 s44, 0x5a0000\n\t" \
    "buffer_load_dwordx4 v240, %[wr], s44 offen offset:0 nt lds\n\t" \
    "buffer_load_dwordx4 v241, %[wr], s44 offen offset:1024 nt lds\n\t" \
    "buffer_load_dwordx4 v242, %[wr], s44 offen offset:2048 nt lds\n\t" \
    "ds_read_b64_tr_b16 v[224:225], v244 offset:0\n\t" \
    "ds_read_b64_tr_b16 v[226:227], v244 offset:1536\n\t" \
    "ds_read_b64_tr_b16 v[120:121], v244 offset:32\n\t" \
    "ds_read_b64_tr_b16 v[122:123], v244 offset:1568\n\t" \
    "ds_read_b64_tr_b16 v[128:129], v244 offset:64\n\t" \
    "ds_read_b64_tr_b16 v[130:131], v244 offset:1600\n\t" \
    "v_add_u32 v244, 0x9000, v243\n\t" \
    "s_waitcnt vmcnt(12) lgkmcnt(0)\n\t" \
    "s_barrier \n\t" \
    "s_add_u32 m0, %[wm], 0x6000\n\t" \
    "s_mov_b32 s44, 0x600000\n\t" \
    "buffer_load_dwordx4 v240, %[wr], s44 offen offset:0 nt lds\n\t" \
    "buffer_load_dwordx4 v241, %[wr], s44 offen offset:1024 nt lds\n\t" \
    "buffer_load_dwordx4 v242, %[wr], s44 offen offset:2048 nt lds\n\t" \
    "ds_read_b64_tr_b16 v[228:229], v244 offset:0\n\t" \
    "ds_read_b64_tr_b16 v[230:231], v244 offset:1536\n\t" \
    "ds_read_b64_tr_b16 v[124:125], v244 offset:32\n\t" \
    "ds_read_b64_tr_b16 v[126:127], v244 offset:1568\n\t" \
    "ds_read_b64_tr_b16 v[132:133], v244 offset:64\n\t" \
    "ds_read_b64_tr_b16 v[134:135], v244 offset:1600\n\t" \
    "v_add_u32 v244, 0xc000, v243\n\t" \
    "s_waitcnt vmcnt(12) lgkmcnt(0)\n\t" \
    "s_barrier \n\t" \
    "s_add_u32 m0, %[wm], 0x9000\n\t" \
    "s_mov_b32 s44, 0x660000\n\t" \
    "buffer_load_dwordx4 v240, %[wr], s44 offen offset:0 nt lds\n\t" \
    "buffer_load_dwordx4 v241, %[wr], s44 offen offset:1024 nt lds\n\t" \
    "buffer_load_dwordx4 v242, %[wr], s44 offen offset:2048 nt lds\n\t" \
    "ds_read_b64_tr_b16 v[136:137], v244 offset:0\n\t" \
    "ds_read_b64_tr_b16 v[138:139], v244 offset:1536\n\t" \
    "ds_read_b64_tr_b16 v[144:145], v244 offset:32\n\t" \
    "ds_read_b64_tr_b16 v[146:147], v244 offset:1568\n\t" \
    "ds_read_b64_tr_b16 v[152:153], v244 offset:64\n\t" \
    "ds_read_b64_tr_b16 v[154:155], v244 offset:1600\n\t" \
    "v_add_u32 v244, 0xf000, v243\n\t" \
    "s_waitcnt vmcnt(12) lgkmcnt(0)\n\t" \
    "s_barrier \n\t" \
    ".Lv21_%=:\n\t" \
    "v_readfirstlane_b32 s43, v246\n\t" \
    "s_nop 0\n\t" \
    "s_cmp_lg_u32 s43, -1\n\t" \
    "s_cbranch_scc1 .Lv22_%=\n\t" \
    "s_cmp_eq_u32 s41, 0\n\t" \
    "s_cbranch_scc1 .Lv22_%=\n\t" \
    "s_sub_u32 s41, s41, 1\n\t" \
    "s_sleep 4\n\t" \
    "v_mov_b32 v246, 0\n\t" \
    "buffer_load_dword v246, v246, s[60:63], %[mo] offen sc1\n\t" \
    "s_waitcnt vmcnt(0)\n\t" \
    "s_branch .Lv21_%=\n\t" \
    ".Lv22_%=:\n\t" \
    "v_mov_b32 v246, 0x8000\n\t" \
    "s_mov_b64 exec, 1\n\t" \
    "buffer_store_dword v246, v246, s[60:63], %[mo] offen sc1\n\t" \
    "s_mov_b64 exec, -1\n\t" \
    "s_and_b32 s43, s43, 0x2000\n\t" \
    "s_xor_b32 s43, s43, %[lx]\n\t" \
    "s_add_u32 s60, s60, s43\n\t" \
    "s_addc_u32 s61, s61, 0\n\t" \
    "v_and_b32 v252, 0xf0, v253\n\t" \
    "v_lshlrev_b32 v252, 3, v252\n\t" \
    "buffer_load_dword v252, v252, s[60:63], %[fl] offen sc1\n\t" \
    "s_add_u32 m0, %[wm], 0xc000\n\t" \
    "s_mov_b32 s44, 0x6c0000\n\t" \
    "buffer_load_dwordx4 v240, %[wr], s44 offen offset:0 nt lds\n\t" \
    "buffer_load_dwordx4 v241, %[wr], s44 offen offset:1024 nt lds\n\t" \
    "buffer_load_dwordx4 v242, %[wr], s44 offen offset:2048 nt lds\n\t" \
    "ds_read_b64_tr_b16 v[140:141], v244 offset:0\n\t" \
    "ds_read_b64_tr_b16 v[142:143], v244 offset:1536\n\t" \
    "ds_read_b64_tr_b16 v[148:149], v244 offset:32\n\t" \
    "ds_read_b64_tr_b16 v[150:151], v244 offset:1568\n\t" \
    "ds_read_b64_tr_b16 v[156:157], v244 offset:64\n\t" \
    "ds_read_b64_tr_b16 v[158:159], v244 offset:1600\n\t" \
    "v_add_u32 v244, 0x0, v243\n\t" \
    "s_waitcnt vmcnt(12) lgkmcnt(0)\n\t" \
    "s_barrier \n\t" \
    "s_add_u32 m0, %[wm], 0xf000\n\t" \
    "s_mov_b32 s44, 0x720000\n\t" \
    "buffer_load_dwordx4 v240, %[wr], s44 offen offset:0 nt lds\n\t" \
    "buffer_load_dwordx4 v241, %[wr], s44 offen offset:1024 nt lds\n\t" \
    "buffer_load_dwordx4 v242, %[wr], s44 offen offset:2048 nt lds\n\t" \
    "ds_read_b64_tr_b16 v[16:17], v244 offset:0\n\t" \
    "ds_read_b64_tr_b16 v[18:19], v244 offset:1536\n\t" \
    "ds_read_b64_tr_b16 v[24:25], v244 offset:32\n\t" \
    "ds_read_b64_tr_b16 v[26:27], v244 offset:1568\n\t" \
    "ds_read_b64_tr_b16 v[32:33], v244 offset:64\n\t" \
    "ds_read_b64_tr_b16 v[34:35], v244 offset:1600\n\t" \
    "v_add_u32 v244, 0x3000, v243\n\t" \
    "s_waitcnt vmcnt(12) lgkmcnt(0)\n\t" \
    "s_barrier \n\t" \
    "ds_read_b64_tr_b16 v[20:21], v244 offset:0\n\t" \
    "ds_read_b64_tr_b16 v[22:23], v244 offset:1536\n\t" \
    "ds_read_b64_tr_b16 v[28:29], v244 offset:32\n\t" \
    "ds_read_b64_tr_b16 v[30:31], v244 offset:1568\n\t" \
    "ds_read_b64_tr_b16 v[36:37], v244 offset:64\n\t" \
    "ds_read_b64_tr_b16 v[38:39], v244 offset:1600\n\t" \
    "v_add_u32 v244, 0x6000, v243\n\t" \
    "s_waitcnt vmcnt(9) lgkmcnt(0)\n\t" \
    "s_barrier \n\t" \
    "ds_read_b64_tr_b16 v[232:233], v244 offset:0\n\t" \
    "ds_read_b64_tr_b16 v[234:235], v244 offset:1536\n\t" \
    "ds_read_b64_tr_b16 v[160:161], v244 offset:32\n\t" \
    "ds_read_b64_tr_b16 v[162:163], v244 offset:1568\n\t" \
    "ds_read_b64_tr_b16 v[168:169], v244 offset:64\n\t" \
    "ds_read_b64_tr_b16 v[170:171], v244 offset:1600\n\t" \
    "v_add_u32 v244, 0x9000, v243\n\t" \
    "s_waitcnt vmcnt(6) lgkmcnt(0)\n\t" \
    "s_barrier \n\t" \
    "ds_read_b64_tr_b16 v[236:237], v244 offset:0\n\t" \
    "ds_read_b64_tr_b16 v[238:239], v244 offset:1536\n\t" \
    "ds_read_b64_tr_b16 v[164:165], v244 offset:32\n\t" \
    "ds_read_b64_tr_b16 v[166:167], v244 offset:1568\n\t" \
    "ds_read_b64_tr_b16 v[172:173], v244 offset:64\n\t" \
    "ds_read_b64_tr_b16 v[174:175], v244 offset:1600\n\t" \
    "v_add_u32 v244, 0xc000, v243\n\t" \
    "s_waitcnt vmcnt(3) lgkmcnt(0)\n\t" \
    "s_barrier \n\t" \
    "ds_read_b64_tr_b16 v[176:177], v244 offset:0\n\t" \
    "ds_read_b64_tr_b16 v[178:179], v244 offset:1536\n\t" \
    "ds_read_b64_tr_b16 v[184:185], v244 offset:32\n\t" \
    "ds_read_b64_tr_b16 v[186:187], v244 offset:1568\n\t" \
    "ds_read_b64_tr_b16 v[192:193], v244 offset:64\n\t" \
    "ds_read_b64_tr_b16 v[194:195], v244 offset:1600\n\t" \
    "s_mov_b32 s43, 0\n\t" \
    "v_cmp_eq_u32 vcc, 0, v252\n\t" \
    "s_cbranch_vccnz .Lv23_%=\n\t" \
    "s_mov_b32 m0, 0x2000\n\t" \
    "s_add_u32 s49, s42, 0x0\n\t" \
    "buffer_load_dwordx4 v253, s[60:63], s49 offen offset:0 sc1 lds\n\t" \
    "buffer_load_dwordx4 v253, s[60:63], s49 offen offset:1024 sc1 lds\n\t" \
    "buffer_load_dwordx4 v253, s[60:63], s49 offen offset:2048 sc1 lds\n\t" \
    "buffer_load_dwordx4 v253, s[60:63], s49 offen offset:3072 sc1 lds\n\t" \
    "s_mov_b32 m0, 0x4800\n\t" \
    "s_add_u32 s49, s42, 0x8800\n\t" \
    "buffer_load_dwordx4 v253, s[60:63], s49 offen offset:0 sc1 lds\n\t" \
    "s_mov_b32 m0, 0x7000\n\t" \
    "s_add_u32 s49, s42, 0x1000\n\t" \
    "buffer_load_dwordx4 v253, s[60:63], s49 offen offset:0 sc1 lds\n\t" \
    "buffer_load_dwordx4 v253, s[60:63], s49 offen offset:1024 sc1 lds\n\t" \
    "buffer_load_dwordx4 v253, s[60:63], s49 offen offset:2048 sc1 lds\n\t" \
    "buffer_load_dwordx4 v253, s[60:63], s49 offen offset:3072 sc1 lds\n\t" \
    "s_mov_b32 m0, 0x9800\n\t" \
    "s_add_u32 s49, s42, 0x9800\n\t" \
    "buffer_load_dwordx4 v253, s[60:63], s49 offen offset:0 sc1 lds\n\t" \
    "s_mov_b32 s43, 1\n\t" \
    ".Lv23_%=:\n\t" \
    "v_add_u32 v244, 0xf000, v243\n\t" \
    "s_waitcnt vmcnt(0) lgkmcnt(0)\n\t" \
    "s_barrier \n\t" \
    "ds_read_b64_tr_b16 v[180:181], v244 offset:0\n\t" \
    "ds_read_b64_tr_b16 v[182:183], v244 offset:1536\n\t" \
    "ds_read_b64_tr_b16 v[188:189], v244 offset:32\n\t" \
    "ds_read_b64_tr_b16 v[190:191], v244 offset:1568\n\t" \
    "ds_read_b64_tr_b16 v[196:197], v244 offset:64\n\t" \
    "ds_read_b64_tr_b16 v[198:199], v244 offset:1600\n\t" \
    "s_waitcnt lgkmcnt(0)\n\t" \
    "s_barrier \n\t" \
    LST0 \
    LJIT \
    "ds_write_b128 v254, v[200:203] offset:51200\n\t" \
    "ds_write_b128 v254, v[204:207] offset:52224\n\t" \
    "ds_write_b128 v254, v[208:211] offset:53248\n\t" \
    "ds_write_b128 v254, v[212:215] offset:54272\n\t" \
    "ds_write_b128 v254, v[216:219] offset:55296\n\t" \
    "ds_write_b128 v254, v[220:223] offset:56320\n\t" \
    "ds_write_b128 v254, v[224:227] offset:57344\n\t" \
    "ds_write_b128 v254, v[228:231] offset:58368\n\t" \
    "ds_write_b128 v254, v[232:235] offset:59392\n\t" \
    "ds_write_b128 v254, v[236:239] offset:60416\n\t" \
    "s_cmp_lg_u32 s43, 0\n\t" \
    "s_cbranch_scc1 .Lv24_%=\n\t" \
    "s_mov_b32 s44, 0\n\t" \
    ".Lv25_%=:\n\t" \
    "v_cmp_ge_u32 vcc, s44, v252\n\t" \
    "s_cbranch_vccz .Lv26_%=\n\t" \
    "s_cmp_eq_u32 s41, 0\n\t" \
    "s_cbranch_scc1 .Lv26_%=\n\t" \
    "s_sub_u32 s41, s41, 1\n\t" \
    "s_sleep 8\n\t" \
    "v_and_b32 v252, 0xf0, v253\n\t" \
    "v_lshlrev_b32 v252, 3, v252\n\t" \
    "buffer_load_dword v252, v252, s[60:63], %[fl] offen sc1\n\t" \
    "s_waitcnt vmcnt(0)\n\t" \
    "s_branch .Lv25_%=\n\t" \
    ".Lv26_%=:\n\t" \
    "s_mov_b32 m0, 0x2000\n\t" \
    "s_add_u32 s49, s42, 0x0\n\t" \
    "buffer_load_dwordx4 v253, s[60:63], s49 offen offset:0 sc1 lds\n\t" \
    "buffer_load_dwordx4 v253, s[60:63], s49 offen offset:1024 sc1 lds\n\t" \
    "buffer_load_dwordx4 v253, s[60:63], s49 offen offset:2048 sc1 lds\n\t" \
    "buffer_load_dwordx4 v253, s[60:63], s49 offen offset:3072 sc1 lds\n\t" \
    "s_mov_b32 m0, 0x4800\n\t" \
    "s_add_u32 s49, s42, 0x8800\n\t" \
    "buffer_load_dwordx4 v253, s[60:63], s49 offen offset:0 sc1 lds\n\t" \
    "s_mov_b32 m0, 0x7000\n\t" \
    "s_add_u32 s49, s42, 0x1000\n\t" \
    "buffer_load_dwordx4 v253, s[60:63], s49 offen offset:0 sc1 lds\n\t" \
    "buffer_load_dwordx4 v253, s[60:63], s49 offen offset:1024 sc1 lds\n\t" \
    "buffer_load_dwordx4 v253, s[60:63], s49 offen offset:2048 sc1 lds\n\t" \
    "buffer_load_dwordx4 v253, s[60:63], s49 offen offset:3072 sc1 lds\n\t" \
    "s_mov_b32 m0, 0x9800\n\t" \
    "s_add_u32 s49, s42, 0x9800\n\t" \
    "buffer_load_dwordx4 v253, s[60:63], s49 offen offset:0 sc1 lds\n\t" \
    ".Lv24_%=:\n\t" \
    "s_waitcnt vmcnt(0) lgkmcnt(0)\n\t" \
    "s_barrier \n\t" \
    "ds_read_b128 v[224:227], v253 offset:8192\n\t" \
    "ds_read_b128 v[244:247], v254 offset:51200\n\t" \
    "ds_read_b128 v[228:231], v253 offset:9216\n\t" \
    "ds_read_b128 v[232:235], v253 offset:28672\n\t" \
    "ds_read_b128 v[236:239], v253 offset:29696\n\t" \
    "ds_read_b128 v[248:251], v254 offset:52224\n\t" \
    ".Lv27_%=:\n\t" \
    LST7 \
    LJIT \
    "s_waitcnt lgkmcnt(5)\n\t" \
    "v_mfma_f32_16x16x32_bf16 v[204:207], v[0:3], v[224:227], 0\n\t" \
    "s_waitcnt lgkmcnt(3)\n\t" \
    "v_mfma_f32_16x16x32_bf16 v[204:207], v[4:7], v[228:231], v[204:207]\n\t" \
    "ds_read_b128 v[240:243], v253 offset:14336\n\t" \
    "v_mfma_f32_16x16x32_bf16 v[208:211], v[8:11], v[224:227], 0\n\t" \
    "v_mfma_f32_16x16x32_bf16 v[208:211], v[12:15], v[228:231], v[208:211]\n\t" \
    "v_mfma_f32_16x16x32_bf16 v[200:203], v[244:247], v[224:227], 0\n\t" \
    "s_waitcnt lgkmcnt(1)\n\t" \
    "v_mfma_f32_16x16x32_bf16 v[200:203], v[248:251], v[228:231], v[200:203]\n\t" \
    "v_mfma_f32_16x16x32_bf16 v[216:219], v[0:3], v[232:235], 0\n\t" \
    "ds_read_b128 v[224:227], v253 offset:15360\n\t" \
    "v_mfma_f32_16x16x32_bf16 v[216:219], v[4:7], v[236:239], v[216:219]\n\t" \
    "ds_read_b128 v[228:231], v253 offset:0\n\t" \
    "v_mfma_f32_16x16x32_bf16 v[220:223], v[8:11], v[232:235], 0\n\t" \
    "v_mfma_f32_16x16x32_bf16 v[220:223], v[12:15], v[236:239], v[220:223]\n\t" \
    "v_mfma_f32_16x16x32_bf16 v[212:215], v[244:247], v[232:235], 0\n\t" \
    "ds_read_b128 v[244:247], v254 offset:53248\n\t" \
    "v_mfma_f32_16x16x32_bf16 v[212:215], v[248:251], v[236:239], v[212:215]\n\t" \
    "ds_read_b128 v[248:251], v254 offset:54272\n\t" \
    "s_waitcnt lgkmcnt(4)\n\t" \
    "v_mfma_f32_16x16x32_bf16 v[204:207], v[24:27], v[240:243], v[204:207]\n\t" \
    "ds_read_b128 v[232:235], v253 offset:1024\n\t" \
    "s_waitcnt lgkmcnt(4)\n\t" \
    "v_mfma_f32_16x16x32_bf16 v[204:207], v[28:31], v[224:227], v[204:207]\n\t" \
    "ds_read_b128 v[236:239], v253 offset:20480\n\t" \
    "v_mfma_f32_16x16x32_bf16 v[208:211], v[32:35], v[240:243], v[208:211]\n\t" \
    "v_mfma_f32_16x16x32_bf16 v[208:211], v[36:39], v[224:227], v[208:211]\n\t" \
    "v_mfma_f32_16x16x32_bf16 v[200:203], v[16:19], v[240:243], v[200:203]\n\t" \
    "v_mfma_f32_16x16x32_bf16 v[200:203], v[20:23], v[224:227], v[200:203]\n\t" \
    "s_waitcnt lgkmcnt(4)\n\t" \
    "v_mfma_f32_16x16x32_bf16 v[204:207], v[40:43], v[228:231], v[204:207]\n\t" \
    "ds_read_b128 v[240:243], v253 offset:21504\n\t" \
    "s_waitcnt lgkmcnt(2)\n\t" \
    "v_mfma_f32_16x16x32_bf16 v[204:207], v[44:47], v[232:235], v[204:207]\n\t" \
    "ds_read_b128 v[224:227], v253 offset:2048\n\t" \
    "v_mfma_f32_16x16x32_bf16 v[208:211], v[48:51], v[228:231], v[208:211]\n\t" \
    "v_mfma_f32_16x16x32_bf16 v[208:211], v[52:55], v[232:235], v[208:211]\n\t" \
    "v_mfma_f32_16x16x32_bf16 v[200:203], v[244:247], v[228:231], v[200:203]\n\t" \
    "v_mfma_f32_16x16x32_bf16 v[200:203], v[248:251], v[232:235], v[200:203]\n\t" \
    "s_waitcnt lgkmcnt(2)\n\t" \
    "v_mfma_f32_16x16x32_bf16 v[216:219], v[40:43], v[236:239], v[216:219]\n\t" \
    "ds_read_b128 v[228:231], v253 offset:3072\n\t" \
    "s_add_u32 s44, s40, 1\n\t" \
    "s_waitcnt lgkmcnt(2)\n\t" \
    "v_mfma_f32_16x16x32_bf16 v[216:219], v[44:47], v[240:243], v[216:219]\n\t" \
    "ds_read_b128 v[232:235], v253 offset:4096\n\t" \
    "s_and_b32 s45, s44, 15\n\t" \
    "v_mfma_f32_16x16x32_bf16 v[220:223], v[48:51], v[236:239], v[220:223]\n\t" \
    "s_cmp_eq_u32 s45, 0\n\t" \
    "v_mfma_f32_16x16x32_bf16 v[220:223], v[52:55], v[240:243], v[220:223]\n\t" \
    "s_cselect_b32 s45, %[cj], %[cs]\n\t" \
    "v_mfma_f32_16x16x32_bf16 v[212:215], v[244:247], v[236:239], v[212:215]\n\t" \
    "ds_read_b128 v[244:247], v254 offset:55296\n\t" \
    "s_cmp_eq_u32 s44, 64\n\t" \
    "v_mfma_f32_16x16x32_bf16 v[212:215], v[248:251], v[240:243], v[212:215]\n\t" \
    "ds_read_b128 v[248:251], v254 offset:56320\n\t" \
    "s_cselect_b32 s45, 0, s45\n\t" \
    "s_waitcnt lgkmcnt(4)\n\t" \
    "v_mfma_f32_16x16x32_bf16 v[204:207], v[64:67], v[224:227], v[204:207]\n\t" \
    "ds_read_b128 v[236:239], v253 offset:5120\n\t" \
    "s_add_u32 s42, s42, s45\n\t" \
    "s_waitcnt lgkmcnt(4)\n\t" \
    "v_mfma_f32_16x16x32_bf16 v[204:207], v[68:71], v[228:231], v[204:207]\n\t" \
    "ds_read_b128 v[240:243], v253 offset:24576\n\t" \
    "v_mfma_f32_16x16x32_bf16 v[208:211], v[72:75], v[224:227], v[208:211]\n\t" \
    "v_mfma_f32_16x16x32_bf16 v[208:211], v[76:79], v[228:231], v[208:211]\n\t" \
    "v_mfma_f32_16x16x32_bf16 v[200:203], v[56:59], v[224:227], v[200:203]\n\t" \
    "v_mfma_f32_16x16x32_bf16 v[200:203], v[60:63], v[228:231], v[200:203]\n\t" \
    "s_waitcnt lgkmcnt(4)\n\t" \
    "v_mfma_f32_16x16x32_bf16 v[204:207], v[80:83], v[232:235], v[204:207]\n\t" \
    "ds_read_b128 v[224:227], v253 offset:25600\n\t" \
    "s_waitcnt lgkmcnt(2)\n\t" \
    "v_mfma_f32_16x16x32_bf16 v[204:207], v[84:87], v[236:239], v[204:207]\n\t" \
    "ds_read_b128 v[228:231], v253 offset:6144\n\t" \
    "v_mfma_f32_16x16x32_bf16 v[208:211], v[88:91], v[232:235], v[208:211]\n\t" \
    "v_mfma_f32_16x16x32_bf16 v[208:211], v[92:95], v[236:239], v[208:211]\n\t" \
    "v_mfma_f32_16x16x32_bf16 v[200:203], v[244:247], v[232:235], v[200:203]\n\t" \
    "v_mfma_f32_16x16x32_bf16 v[200:203], v[248:251], v[236:239], v[200:203]\n\t" \
    "s_waitcnt lgkmcnt(2)\n\t" \
    "v_mfma_f32_16x16x32_bf16 v[216:219], v[80:83], v[240:243], v[216:219]\n\t" \
    "ds_read_b128 v[232:235], v253 offset:7168\n\t" \
    "s_and_b32 s44, s40, 3\n\t" \
    "s_waitcnt lgkmcnt(2)\n\t" \
    "v_mfma_f32_16x16x32_bf16 v[216:219], v[84:87], v[224:227], v[216:219]\n\t" \
    "ds_read_b128 v[236:239], v253 offset:10240\n\t" \
    "s_lshl_b32 s44, s44, 17\n\t" \
    "v_mfma_f32_16x16x32_bf16 v[220:223], v[88:91], v[240:243], v[220:223]\n\t" \
    "s_add_u32 s44, s44, %[mb]\n\t" \
    "v_mfma_f32_16x16x32_bf16 v[220:223], v[92:95], v[224:227], v[220:223]\n\t" \
    "s_add_u32 s46, s44, 0x1000\n\t" \
    "v_mfma_f32_16x16x32_bf16 v[212:215], v[244:247], v[240:243], v[212:215]\n\t" \
    "ds_read_b128 v[244:247], v254 offset:57344\n\t" \
    "s_add_u32 s47, s44, 0x1c00\n\t" \
    "v_mfma_f32_16x16x32_bf16 v[212:215], v[248:251], v[224:227], v[212:215]\n\t" \
    "ds_read_b128 v[248:251], v254 offset:58368\n\t" \
    "s_waitcnt lgkmcnt(4)\n\t" \
    "v_mfma_f32_16x16x32_bf16 v[204:207], v[104:107], v[228:231], v[204:207]\n\t" \
    "ds_read_b128 v[240:243], v253 offset:11264\n\t" \
    "s_waitcnt lgkmcnt(4)\n\t" \
    "v_mfma_f32_16x16x32_bf16 v[204:207], v[108:111], v[232:235], v[204:207]\n\t" \
    "ds_read_b128 v[224:227], v253 offset:30720\n\t" \
    "v_mfma_f32_16x16x32_bf16 v[208:211], v[112:115], v[228:231], v[208:211]\n\t" \
    "v_mfma_f32_16x16x32_bf16 v[208:211], v[116:119], v[232:235], v[208:211]\n\t" \
    "v_mfma_f32_16x16x32_bf16 v[200:203], v[96:99], v[228:231], v[200:203]\n\t" \
    "v_mfma_f32_16x16x32_bf16 v[200:203], v[100:103], v[232:235], v[200:203]\n\t" \
    "s_waitcnt lgkmcnt(4)\n\t" \
    "v_mfma_f32_16x16x32_bf16 v[204:207], v[120:123], v[236:239], v[204:207]\n\t" \
    "ds_read_b128 v[228:231], v253 offset:31744\n\t" \
    "s_waitcnt lgkmcnt(2)\n\t" \
    "v_mfma_f32_16x16x32_bf16 v[204:207], v[124:127], v[240:243], v[204:207]\n\t" \
    "ds_read_b128 v[232:235], v253 offset:12288\n\t" \
    "v_mfma_f32_16x16x32_bf16 v[208:211], v[128:131], v[236:239], v[208:211]\n\t" \
    "v_mfma_f32_16x16x32_bf16 v[208:211], v[132:135], v[240:243], v[208:211]\n\t" \
    "v_mfma_f32_16x16x32_bf16 v[200:203], v[244:247], v[236:239], v[200:203]\n\t" \
    "v_mfma_f32_16x16x32_bf16 v[200:203], v[248:251], v[240:243], v[200:203]\n\t" \
    "s_waitcnt lgkmcnt(2)\n\t" \
    "v_mfma_f32_16x16x32_bf16 v[216:219], v[120:123], v[224:227], v[216:219]\n\t" \
    "ds_read_b128 v[236:239], v253 offset:13312\n\t" \
    "s_waitcnt lgkmcnt(2)\n\t" \
    "v_mfma_f32_16x16x32_bf16 v[216:219], v[124:127], v[228:231], v[216:219]\n\t" \
    "ds_read_b128 v[240:243], v253 offset:16384\n\t" \
    "v_mfma_f32_16x16x32_bf16 v[220:223], v[128:131], v[224:227], v[220:223]\n\t" \
    "v_mfma_f32_16x16x32_bf16 v[220:223], v[132:135], v[228:231], v[220:223]\n\t" \
    "v_mfma_f32_16x16x32_bf16 v[212:215], v[244:247], v[224:227], v[212:215]\n\t" \
    "ds_read_b128 v[244:247], v254 offset:59392\n\t" \
    "v_mfma_f32_16x16x32_bf16 v[212:215], v[248:251], v[228:231], v[212:215]\n\t" \
    "ds_read_b128 v[248:251], v254 offset:60416\n\t" \
    "v_and_b32 v252, 0xf0, v253\n\t" \
    "v_lshlrev_b32 v252, 3, v252\n\t" \
    "buffer_load_dword v252, v252, s[60:63], %[fl] offen sc1\n\t" \
    "s_waitcnt lgkmcnt(4)\n\t" \
    "v_mfma_f32_16x16x32_bf16 v[204:207], v[144:147], v[232:235], v[204:207]\n\t" \
    "ds_read_b128 v[224:227], v253 offset:17408\n\t" \
    "s_waitcnt lgkmcnt(4)\n\t" \
    "v_mfma_f32_16x16x32_bf16 v[204:207], v[148:151], v[236:239], v[204:207]\n\t" \
    "ds_read_b128 v[228:231], v253 offset:36864\n\t" \
    "v_mfma_f32_16x16x32_bf16 v[208:211], v[152:155], v[232:235], v[208:211]\n\t" \
    "v_mfma_f32_16x16x32_bf16 v[208:211], v[156:159], v[236:239], v[208:211]\n\t" \
    "v_mfma_f32_16x16x32_bf16 v[200:203], v[136:139], v[232:235], v[200:203]\n\t" \
    "v_mfma_f32_16x16x32_bf16 v[200:203], v[140:143], v[236:239], v[200:203]\n\t" \
    "s_waitcnt lgkmcnt(4)\n\t" \
    "v_mfma_f32_16x16x32_bf16 v[204:207], v[160:163], v[240:243], v[204:207]\n\t" \
    "ds_read_b128 v[232:235], v253 offset:37888\n\t" \
    "s_waitcnt lgkmcnt(2)\n\t" \
    "v_mfma_f32_16x16x32_bf16 v[204:207], v[164:167], v[224:227], v[204:207]\n\t" \
    "ds_read_b128 v[236:239], v253 offset:18432\n\t" \
    "v_mfma_f32_16x16x32_bf16 v[208:211], v[168:171], v[240:243], v[208:211]\n\t" \
    "v_mfma_f32_16x16x32_bf16 v[208:211], v[172:175], v[224:227], v[208:211]\n\t" \
    "v_mfma_f32_16x16x32_bf16 v[200:203], v[244:247], v[240:243], v[200:203]\n\t" \
    "v_mfma_f32_16x16x32_bf16 v[200:203], v[248:251], v[224:227], v[200:203]\n\t" \
    "s_waitcnt lgkmcnt(2)\n\t" \
    "v_mfma_f32_16x16x32_bf16 v[216:219], v[160:163], v[228:231], v[216:219]\n\t" \
    "ds_read_b128 v[240:243], v253 offset:19456\n\t" \
    "s_waitcnt lgkmcnt(2)\n\t" \
    "v_mfma_f32_16x16x32_bf16 v[216:219], v[164:167], v[232:235], v[216:219]\n\t" \
    "ds_read_b128 v[224:227], v253 offset:34816\n\t" \
    "v_mfma_f32_16x16x32_bf16 v[220:223], v[168:171], v[228:231], v[220:223]\n\t" \
    "v_mfma_f32_16x16x32_bf16 v[220:223], v[172:175], v[232:235], v[220:223]\n\t" \
    "v_mfma_f32_16x16x32_bf16 v[212:215], v[244:247], v[228:231], v[212:215]\n\t" \
    "v_mfma_f32_16x16x32_bf16 v[212:215], v[248:251], v[232:235], v[212:215]\n\t" \
    "s_waitcnt lgkmcnt(2)\n\t" \
    "v_mfma_f32_16x16x32_bf16 v[204:207], v[184:187], v[236:239], v[204:207]\n\t" \
    "ds_read_b128 v[228:231], v253 offset:35840\n\t" \
    "s_waitcnt lgkmcnt(2)\n\t" \
    "v_mfma_f32_16x16x32_bf16 v[204:207], v[188:191], v[240:243], v[204:207]\n\t" \
    "ds_read_b128 v[232:235], v253 offset:22528\n\t" \
    "v_mfma_f32_16x16x32_bf16 v[208:211], v[192:195], v[236:239], v[208:211]\n\t" \
    "v_mfma_f32_16x16x32_bf16 v[208:211], v[196:199], v[240:243], v[208:211]\n\t" \
    "v_mfma_f32_16x16x32_bf16 v[200:203], v[176:179], v[236:239], v[200:203]\n\t" \
    "v_mfma_f32_16x16x32_bf16 v[200:203], v[180:183], v[240:243], v[200:203]\n\t" \
    "s_waitcnt lgkmcnt(2)\n\t" \
    "v_mfma_f32_16x16x32_bf16 v[216:219], v[24:27], v[224:227], v[216:219]\n\t" \
    "ds_read_b128 v[236:239], v253 offset:23552\n\t" \
    "s_waitcnt lgkmcnt(2)\n\t" \
    "v_mfma_f32_16x16x32_bf16 v[216:219], v[28:31], v[228:231], v[216:219]\n\t" \
    "ds_read_b128 v[240:243], v253 offset:26624\n\t" \
    "v_mfma_f32_16x16x32_bf16 v[220:223], v[32:35], v[224:227], v[220:223]\n\t" \
    "v_cvt_pk_bf16_f32 v204, v204, v205\n\t" \
    "v_mfma_f32_16x16x32_bf16 v[220:223], v[36:39], v[228:231], v[220:223]\n\t" \
    "v_cvt_pk_bf16_f32 v205, v206, v207\n\t" \
    "v_mfma_f32_16x16x32_bf16 v[212:215], v[16:19], v[224:227], v[212:215]\n\t" \
    "ds_write_b64 v254, v[204:205] offset:7176\n\t" \
    "v_mfma_f32_16x16x32_bf16 v[212:215], v[20:23], v[228:231], v[212:215]\n\t" \
    "v_cvt_pk_bf16_f32 v208, v208, v209\n\t" \
    "s_waitcnt lgkmcnt(3)\n\t" \
    "v_mfma_f32_16x16x32_bf16 v[216:219], v[64:67], v[232:235], v[216:219]\n\t" \
    "ds_read_b128 v[224:227], v253 offset:27648\n\t" \
    "v_cvt_pk_bf16_f32 v209, v210, v211\n\t" \
    "s_waitcnt lgkmcnt(3)\n\t" \
    "v_mfma_f32_16x16x32_bf16 v[216:219], v[68:71], v[236:239], v[216:219]\n\t" \
    "ds_read_b128 v[228:231], v253 offset:32768\n\t" \
    "ds_write_b64 v254, v[208:209] offset:8200\n\t" \
    "v_mfma_f32_16x16x32_bf16 v[220:223], v[72:75], v[232:235], v[220:223]\n\t" \
    "v_cvt_pk_bf16_f32 v200, v200, v201\n\t" \
    "v_mfma_f32_16x16x32_bf16 v[220:223], v[76:79], v[236:239], v[220:223]\n\t" \
    "v_cvt_pk_bf16_f32 v201, v202, v203\n\t" \
    "v_mfma_f32_16x16x32_bf16 v[212:215], v[56:59], v[232:235], v[212:215]\n\t" \
    "ds_write_b64 v254, v[200:201] offset:6152\n\t" \
    "v_mfma_f32_16x16x32_bf16 v[212:215], v[60:63], v[236:239], v[212:215]\n\t" \
    "s_waitcnt lgkmcnt(5)\n\t" \
    "v_mfma_f32_16x16x32_bf16 v[216:219], v[104:107], v[240:243], v[216:219]\n\t" \
    "ds_read_b128 v[232:235], v253 offset:33792\n\t" \
    "s_waitcnt lgkmcnt(4)\n\t" \
    "v_mfma_f32_16x16x32_bf16 v[216:219], v[108:111], v[224:227], v[216:219]\n\t" \
    "ds_read_b128 v[236:239], v253 offset:38912\n\t" \
    "v_mfma_f32_16x16x32_bf16 v[220:223], v[112:115], v[240:243], v[220:223]\n\t" \
    "v_mfma_f32_16x16x32_bf16 v[220:223], v[116:119], v[224:227], v[220:223]\n\t" \
    "v_mfma_f32_16x16x32_bf16 v[212:215], v[96:99], v[240:243], v[212:215]\n\t" \
    "v_mfma_f32_16x16x32_bf16 v[212:215], v[100:103], v[224:227], v[212:215]\n\t" \
    "ds_read_b128 v[240:243], v253 offset:39936\n\t" \
    "s_waitcnt lgkmcnt(3)\n\t" \
    "s_barrier \n\t" \
    "v_mfma_f32_16x16x32_bf16 v[216:219], v[144:147], v[228:231], v[216:219]\n\t" \
    "s_waitcnt lgkmcnt(2)\n\t" \
    "v_mfma_f32_16x16x32_bf16 v[216:219], v[148:151], v[232:235], v[216:219]\n\t" \
    "s_waitcnt lgkmcnt(1)\n\t" \
    "v_mfma_f32_16x16x32_bf16 v[216:219], v[184:187], v[236:239], v[216:219]\n\t" \
    "s_waitcnt lgkmcnt(0)\n\t" \
    "v_mfma_f32_16x16x32_bf16 v[216:219], v[188:191], v[240:243], v[216:219]\n\t" \
    "v_mfma_f32_16x16x32_bf16 v[220:223], v[152:155], v[228:231], v[220:223]\n\t" \
    "v_mfma_f32_16x16x32_bf16 v[220:223], v[156:159], v[232:235], v[220:223]\n\t" \
    "v_mfma_f32_16x16x32_bf16 v[220:223], v[192:195], v[236:239], v[220:223]\n\t" \
    "v_cvt_pk_bf16_f32 v216, v216, v217\n\t" \
    "v_cvt_pk_bf16_f32 v217, v218, v219\n\t" \
    "v_mfma_f32_16x16x32_bf16 v[220:223], v[196:199], v[240:243], v[220:223]\n\t" \
    "ds_write_b64 v254, v[216:217] offset:13320\n\t" \
    "v_mfma_f32_16x16x32_bf16 v[212:215], v[136:139], v[228:231], v[212:215]\n\t" \
    "v_mfma_f32_16x16x32_bf16 v[212:215], v[140:143], v[232:235], v[212:215]\n\t" \
    "v_mfma_f32_16x16x32_bf16 v[212:215], v[176:179], v[236:239], v[212:215]\n\t" \
    "v_cvt_pk_bf16_f32 v220, v220, v221\n\t" \
    "v_cvt_pk_bf16_f32 v221, v222, v223\n\t" \
    "v_mfma_f32_16x16x32_bf16 v[212:215], v[180:183], v[240:243], v[212:215]\n\t" \
    "ds_write_b64 v254, v[220:221] offset:14344\n\t" \
    "s_nop 6\n\t" \
    "v_cvt_pk_bf16_f32 v212, v212, v213\n\t" \
    "v_cvt_pk_bf16_f32 v213, v214, v215\n\t" \
    "ds_write_b64 v254, v[212:213] offset:12296\n\t" \
    LST6 \
    LJIT \
    "s_waitcnt vmcnt(0) lgkmcnt(0)\n\t" \
    "s_barrier \n\t" \
    "s_setprio 2\n\t" \
    "s_add_u32 s44, s40, 1\n\t" \
    "s_min_u32 s44, s44, 63\n\t" \
    ".Lv28_%=:\n\t" \
    "v_cmp_ge_u32 vcc, s44, v252\n\t" \
    "s_cbranch_vccz .Lv29_%=\n\t" \
    "s_cmp_eq_u32 s41, 0\n\t" \
    "s_cbranch_scc1 .Lv29_%=\n\t" \
    "s_sub_u32 s41, s41, 1\n\t" \
    "s_sleep 2\n\t" \
    "v_and_b32 v252, 0xf0, v253\n\t" \
    "v_lshlrev_b32 v252, 3, v252\n\t" \
    "buffer_load_dword v252, v252, s[60:63], %[fl] offen sc1\n\t" \
    "s_waitcnt vmcnt(0)\n\t" \
    "s_branch .Lv28_%=\n\t" \
    ".Lv29_%=:\n\t" \
    "s_mov_b32 m0, 0x2000\n\t" \
    "s_add_u32 s49, s42, 0x0\n\t" \
    "buffer_load_dwordx4 v253, s[60:63], s49 offen offset:0 sc1 lds\n\t" \
    "s_sleep 1\n\t" \
    "buffer_load_dwordx4 v253, s[60:63], s49 offen offset:1024 sc1 lds\n\t" \
    "s_sleep 1\n\t" \
    "buffer_load_dwordx4 v253, s[60:63], s49 offen offset:2048 sc1 lds\n\t" \
    "s_sleep 1\n\t" \
    "buffer_load_dwordx4 v253, s[60:63], s49 offen offset:3072 sc1 lds\n\t" \
    "s_sleep 1\n\t" \
    "s_mov_b32 m0, 0x4800\n\t" \
    "s_add_u32 s49, s42, 0x8800\n\t" \
    "buffer_load_dwordx4 v253, s[60:63], s49 offen offset:0 sc1 lds\n\t" \
    "s_sleep 1\n\t" \
    "s_mov_b32 m0, 0x7000\n\t" \
    "s_add_u32 s49, s42, 0x1000\n\t" \
    "buffer_load_dwordx4 v253, s[60:63], s49 offen offset:0 sc1 lds\n\t" \
    "s_sleep 1\n\t" \
    "buffer_load_dwordx4 v253, s[60:63], s49 offen offset:1024 sc1 lds\n\t" \
    "s_sleep 1\n\t" \
    "buffer_load_dwordx4 v253, s[60:63], s49 offen offset:2048 sc1 lds\n\t" \
    "s_sleep 1\n\t" \
    "buffer_load_dwordx4 v253, s[60:63], s49 offen offset:3072 sc1 lds\n\t" \
    "s_sleep 1\n\t" \
    "s_mov_b32 m0, 0x9800\n\t" \
    "s_add_u32 s49, s42, 0x9800\n\t" \
    "buffer_load_dwordx4 v253, s[60:63], s49 offen offset:0 sc1 lds\n\t" \
    "s_sleep 1\n\t" \
    "ds_read_b128 v[200:203], v254 offset:51200\n\t" \
    "ds_read_b128 v[204:207], v254 offset:52224\n\t" \
    "ds_read_b128 v[216:219], v254 offset:53248\n\t" \
    "ds_read_b128 v[220:223], v254 offset:54272\n\t" \
    "v_mov_b64 v[208:209], 0\n\t" \
    "v_mov_b64 v[210:211], 0\n\t" \
    "v_mov_b64 v[244:245], 0\n\t" \
    "v_mov_b64 v[246:247], 0\n\t" \
    "v_mov_b64 v[248:249], 0\n\t" \
    "v_mov_b64 v[250:251], 0\n\t" \
    LST1 \
    LJIT \
    "s_sleep 5\n\t" \
    "s_mov_b32 m0, 0xb000\n\t" \
    "s_nop 0\n\t" \
    "buffer_load_dwordx4 v253, s[60:63], s46 offen sc1 lds\n\t" \
    "s_mov_b32 m0, 0xbc00\n\t" \
    "s_nop 0\n\t" \
    "buffer_load_dwordx4 v253, s[60:63], s47 offen sc1 lds\n\t" \
    "s_waitcnt lgkmcnt(0)\n\t" \
    LFLUSH212 \
    "v_bfe_i32 v212, v253, 5, 1\n\t" \
    "v_and_b32 v212, 0xaaaa, v212\n\t" \
    "v_or_b32 v212, 0x4444, v212\n\t" \
    "v_and_b32 v213, 0xffff, v255\n\t" \
    "v_lshrrev_b32 v214, 16, v255\n\t" \
    ".Lv210_%=:\n\t" \
    "s_waitcnt vmcnt(0)\n\t" \
    "ds_read_b128 v[224:227], v253 offset:45056\n\t" \
    "ds_read_b128 v[228:231], v253 offset:48128\n\t" \
    "s_waitcnt lgkmcnt(1)\n\t" \
    "v_max_u32 v215, v224, v225\n\t" \
    "v_max3_u32 v215, v226, v227, v215\n\t" \
    "s_waitcnt lgkmcnt(0)\n\t" \
    "v_max3_u32 v215, v228, v229, v215\n\t" \
    "v_max3_u32 v215, v230, v231, v215\n\t" \
    "v_cmp_eq_u32 vcc, -1, v215\n\t" \
    "s_cbranch_vccz .Lv211_%=\n\t" \
    "v_max_u32 v215, v224, v225\n\t" \
    "v_max3_u32 v215, v226, v227, v215\n\t" \
    "v_cmp_eq_u32 vcc, -1, v215\n\t" \
    "s_cbranch_vccz .Lv212_%=\n\t" \
    "s_mov_b32 m0, 0xb000\n\t" \
    "s_nop 0\n\t" \
    "buffer_load_dwordx4 v253, s[60:63], s46 offen sc1 lds\n\t" \
    ".Lv212_%=:\n\t" \
    "v_max_u32 v215, v228, v229\n\t" \
    "v_max3_u32 v215, v230, v231, v215\n\t" \
    "v_cmp_eq_u32 vcc, -1, v215\n\t" \
    "s_cbranch_vccz .Lv213_%=\n\t" \
    "s_mov_b32 m0, 0xbc00\n\t" \
    "s_nop 0\n\t" \
    "buffer_load_dwordx4 v253, s[60:63], s47 offen sc1 lds\n\t" \
    ".Lv213_%=:\n\t" \
    "s_cmp_eq_u32 s41, 0\n\t" \
    "s_cbranch_scc1 .Lv211_%=\n\t" \
    "s_sub_u32 s41, s41, 1\n\t" \
    "s_branch .Lv210_%=\n\t" \
    ".Lv211_%=:\n\t" \
    LST2 \
    LJIT \
    "s_waitcnt lgkmcnt(0)\n\t" \
    "s_barrier \n\t" \
    LST3 \
    LJIT \
    "ds_read_b128 v[232:235], v253 offset:40960\n\t" \
    "ds_read_b128 v[236:239], v253 offset:41984\n\t" \
    "ds_read_b128 v[240:243], v253 offset:43008\n\t" \
    "v_smfmac_f32_16x16x64_bf16 v[244:247], v[224:227], v[0:7], v212\n\t" \
    "v_smfmac_f32_16x16x64_bf16 v[248:251], v[224:227], v[8:15], v212\n\t" \
    "v_smfmac_f32_16x16x64_bf16 v[208:211], v[224:227], v[200:207], v212\n\t" \
    "ds_read_b128 v[200:203], v254 offset:55296\n\t" \
    "ds_read_b128 v[204:207], v254 offset:56320\n\t" \
    "ds_read_b128 v[224:227], v253 offset:44032\n\t" \
    "v_smfmac_f32_16x16x64_bf16 v[208:211], v[228:231], v[16:23], v212\n\t" \
    "v_smfmac_f32_16x16x64_bf16 v[244:247], v[228:231], v[24:31], v212\n\t" \
    "v_smfmac_f32_16x16x64_bf16 v[248:251], v[228:231], v[32:39], v212\n\t" \
    "ds_read_b128 v[228:231], v253 offset:46080\n\t" \
    "s_waitcnt lgkmcnt(6)\n\t" \
    "v_smfmac_f32_16x16x64_bf16 v[208:211], v[232:235], v[216:223], v212\n\t" \
    "ds_read_b128 v[216:219], v254 offset:57344\n\t" \
    "ds_read_b128 v[220:223], v254 offset:58368\n\t" \
    "v_smfmac_f32_16x16x64_bf16 v[244:247], v[232:235], v[40:47], v212\n\t" \
    "v_smfmac_f32_16x16x64_bf16 v[248:251], v[232:235], v[48:55], v212\n\t" \
    "ds_read_b128 v[232:235], v253 offset:47104\n\t" \
    "s_waitcnt lgkmcnt(8)\n\t" \
    "v_smfmac_f32_16x16x64_bf16 v[208:211], v[236:239], v[56:63], v212\n\t" \
    "v_smfmac_f32_16x16x64_bf16 v[244:247], v[236:239], v[64:71], v212\n\t" \
    "v_smfmac_f32_16x16x64_bf16 v[248:251], v[236:239], v[72:79], v212\n\t" \
    "ds_read_b128 v[236:239], v253 offset:49152\n\t" \
    "s_waitcnt lgkmcnt(6)\n\t" \
    "v_smfmac_f32_16x16x64_bf16 v[208:211], v[240:243], v[200:207], v212\n\t" \
    "ds_read_b128 v[200:203], v254 offset:59392\n\t" \
    "ds_read_b128 v[204:207], v254 offset:60416\n\t" \
    "v_smfmac_f32_16x16x64_bf16 v[244:247], v[240:243], v[80:87], v212\n\t" \
    "v_smfmac_f32_16x16x64_bf16 v[248:251], v[240:243], v[88:95], v212\n\t" \
    "ds_read_b128 v[240:243], v253 offset:50176\n\t" \
    "s_waitcnt lgkmcnt(8)\n\t" \
    "v_smfmac_f32_16x16x64_bf16 v[208:211], v[224:227], v[96:103], v212\n\t" \
    "v_smfmac_f32_16x16x64_bf16 v[244:247], v[224:227], v[104:111], v212\n\t" \
    "v_smfmac_f32_16x16x64_bf16 v[248:251], v[224:227], v[112:119], v212\n\t" \
    "s_waitcnt lgkmcnt(7)\n\t" \
    "v_smfmac_f32_16x16x64_bf16 v[244:247], v[228:231], v[120:127], v212\n\t" \
    "s_waitcnt lgkmcnt(4)\n\t" \
    "v_smfmac_f32_16x16x64_bf16 v[244:247], v[232:235], v[144:151], v212\n\t" \
    "s_waitcnt lgkmcnt(3)\n\t" \
    "v_smfmac_f32_16x16x64_bf16 v[244:247], v[236:239], v[160:167], v212\n\t" \
    "s_waitcnt lgkmcnt(0)\n\t" \
    "v_smfmac_f32_16x16x64_bf16 v[244:247], v[240:243], v[184:191], v212\n\t" \
    "v_smfmac_f32_16x16x64_bf16 v[248:251], v[228:231], v[128:135], v212\n\t" \
    "v_smfmac_f32_16x16x64_bf16 v[248:251], v[232:235], v[152:159], v212\n\t" \
    "v_smfmac_f32_16x16x64_bf16 v[248:251], v[236:239], v[168:175], v212\n\t" \
    "v_add_f32 v244, v244, v246\n\t" \
    "v_add_f32 v245, v245, v247\n\t" \
    "v_cvt_pk_bf16_f32 v244, v244, v245\n\t" \
    "v_smfmac_f32_16x16x64_bf16 v[248:251], v[240:243], v[192:199], v212\n\t" \
    "ds_write_b16 v213, v244 offset:512\n\t" \
    "ds_write_b16_d16_hi v213, v244 offset:528\n\t" \
    "v_smfmac_f32_16x16x64_bf16 v[208:211], v[228:231], v[216:223], v212\n\t" \
    "v_smfmac_f32_16x16x64_bf16 v[208:211], v[232:235], v[136:143], v212\n\t" \
    "v_smfmac_f32_16x16x64_bf16 v[208:211], v[236:239], v[200:207], v212\n\t" \
    "v_add_f32 v248, v248, v250\n\t" \
    "v_add_f32 v249, v249, v251\n\t" \
    "v_cvt_pk_bf16_f32 v248, v248, v249\n\t" \
    "v_smfmac_f32_16x16x64_bf16 v[208:211], v[240:243], v[176:183], v212\n\t" \
    "ds_write_b16 v213, v248 offset:1024\n\t" \
    "ds_write_b16_d16_hi v213, v248 offset:1040\n\t" \
    "s_nop 5\n\t" \
    "v_add_f32 v208, v208, v210\n\t" \
    "v_add_f32 v209, v209, v211\n\t" \
    "v_cvt_pk_bf16_f32 v208, v208, v209\n\t" \
    "ds_write_b16 v213, v208 offset:0\n\t" \
    "ds_write_b16_d16_hi v213, v208 offset:16\n\t" \
    "s_setprio 0\n\t" \
    "s_waitcnt lgkmcnt(0)\n\t" \
    "s_barrier \n\t" \
    LST4 \
    LJIT \
    "ds_read_b128 v[224:227], v253 offset:8192\n\t" \
    "ds_read_b128 v[244:247], v254 offset:51200\n\t" \
    "ds_read_b128 v[228:231], v253 offset:9216\n\t" \
    "ds_read_b128 v[232:235], v253 offset:28672\n\t" \
    "ds_read_b128 v[236:239], v253 offset:29696\n\t" \
    "ds_read_b128 v[248:251], v254 offset:52224\n\t" \
    "s_add_u32 s40, s40, 1\n\t" \
    "s_cmp_lg_u32 s40, 64\n\t" \
    "s_cbranch_scc1 .Lv27_%=\n\t" \
    "s_branch .Lend%=\n\t" \
    ".Lv1_%=:\n\t" \
    "s_mov_b32 s42, %[xs]\n\t" \
    "v_add_u32 v244, 0xc000, v243\n\t" \
    "s_waitcnt vmcnt(12) lgkmcnt(0)\n\t" \
    "s_barrier \n\t" \
    "s_add_u32 m0, %[wm], 0x9000\n\t" \
    "s_mov_b32 s44, 0x1e0000\n\t" \
    "buffer_load_dwordx4 v240, %[wr], s44 offen offset:0 nt lds\n\t" \
    "buffer_load_dwordx4 v241, %[wr], s44 offen offset:1024 nt lds\n\t" \
    "buffer_load_dwordx4 v242, %[wr], s44 offen offset:2048 nt lds\n\t" \
    "ds_read_b64_tr_b16 v[56:57], v244 offset:0\n\t" \
    "ds_read_b64_tr_b16 v[58:59], v244 offset:1536\n\t" \
    "ds_read_b64_tr_b16 v[64:65], v244 offset:32\n\t" \
    "ds_read_b64_tr_b16 v[66:67], v244 offset:1568\n\t" \
    "ds_read_b64_tr_b16 v[72:73], v244 offset:64\n\t" \
    "ds_read_b64_tr_b16 v[74:75], v244 offset:1600\n\t" \
    "v_add_u32 v244, 0xf000, v243\n\t" \
    "s_waitcnt vmcnt(12) lgkmcnt(0)\n\t" \
    "s_barrier \n\t" \
    "s_add_u32 m0, %[wm], 0xc000\n\t" \
    "s_mov_b32 s44, 0x240000\n\t" \
    "buffer_load_dwordx4 v240, %[wr], s44 offen offset:0 nt lds\n\t" \
    "buffer_load_dwordx4 v241, %[wr], s44 offen offset:1024 nt lds\n\t" \
    "buffer_load_dwordx4 v242, %[wr], s44 offen offset:2048 nt lds\n\t" \
    "ds_read_b64_tr_b16 v[60:61], v244 offset:0\n\t" \
    "ds_read_b64_tr_b16 v[62:63], v244 offset:1536\n\t" \
    "ds_read_b64_tr_b16 v[68:69], v244 offset:32\n\t" \
    "ds_read_b64_tr_b16 v[70:71], v244 offset:1568\n\t" \
    "ds_read_b64_tr_b16 v[76:77], v244 offset:64\n\t" \
    "ds_read_b64_tr_b16 v[78:79], v244 offset:1600\n\t" \
    "v_add_u32 v244, 0x0, v243\n\t" \
    "s_waitcnt vmcnt(12) lgkmcnt(0)\n\t" \
    "s_barrier \n\t" \
    "s_add_u32 m0, %[wm], 0xf000\n\t" \
    "s_mov_b32 s44, 0x2a0000\n\t" \
    "buffer_load_dwordx4 v240, %[wr], s44 offen offset:0 nt lds\n\t" \
    "buffer_load_dwordx4 v241, %[wr], s44 offen offset:1024 nt lds\n\t" \
    "buffer_load_dwordx4 v242, %[wr], s44 offen offset:2048 nt lds\n\t" \
    "ds_read_b64_tr_b16 v[96:97], v244 offset:0\n\t" \
    "ds_read_b64_tr_b16 v[98:99], v244 offset:1536\n\t" \
    "ds_read_b64_tr_b16 v[104:105], v244 offset:32\n\t" \
    "ds_read_b64_tr_b16 v[106:107], v244 offset:1568\n\t" \
    "ds_read_b64_tr_b16 v[112:113], v244 offset:64\n\t" \
    "ds_read_b64_tr_b16 v[114:115], v244 offset:1600\n\t" \
    "v_add_u32 v244, 0x3000, v243\n\t" \
    "s_waitcnt vmcnt(12) lgkmcnt(0)\n\t" \
    "s_barrier \n\t" \
    "s_add_u32 m0, %[wm], 0x0\n\t" \
    "s_mov_b32 s44, 0x300000\n\t" \
    "buffer_load_dwordx4 v240, %[wr], s44 offen offset:0 nt lds\n\t" \
    "buffer_load_dwordx4 v241, %[wr], s44 offen offset:1024 nt lds\n\t" \
    "buffer_load_dwordx4 v242, %[wr], s44 offen offset:2048 nt lds\n\t" \
    "ds_read_b64_tr_b16 v[100:101], v244 offset:0\n\t" \
    "ds_read_b64_tr_b16 v[102:103], v244 offset:1536\n\t" \
    "ds_read_b64_tr_b16 v[108:109], v244 offset:32\n\t" \
    "ds_read_b64_tr_b16 v[110:111], v244 offset:1568\n\t" \
    "ds_read_b64_tr_b16 v[116:117], v244 offset:64\n\t" \
    "ds_read_b64_tr_b16 v[118:119], v244 offset:1600\n\t" \
    "v_add_u32 v244, 0x6000, v243\n\t" \
    "s_waitcnt vmcnt(12) lgkmcnt(0)\n\t" \
    "s_barrier \n\t" \
    "s_add_u32 m0, %[wm], 0x3000\n\t" \
    "s_mov_b32 s44, 0x360000\n\t" \
    "buffer_load_dwordx4 v240, %[wr], s44 offen offset:0 nt lds\n\t" \
    "buffer_load_dwordx4 v241, %[wr], s44 offen offset:1024 nt lds\n\t" \
    "buffer_load_dwordx4 v242, %[wr], s44 offen offset:2048 nt lds\n\t" \
    "ds_read_b64_tr_b16 v[200:201], v244 offset:0\n\t" \
    "ds_read_b64_tr_b16 v[202:203], v244 offset:1536\n\t" \
    "ds_read_b64_tr_b16 v[0:1], v244 offset:32\n\t" \
    "ds_read_b64_tr_b16 v[2:3], v244 offset:1568\n\t" \
    "ds_read_b64_tr_b16 v[8:9], v244 offset:64\n\t" \
    "ds_read_b64_tr_b16 v[10:11], v244 offset:1600\n\t" \
    "v_add_u32 v244, 0x9000, v243\n\t" \
    "s_waitcnt vmcnt(12) lgkmcnt(0)\n\t" \
    "s_barrier \n\t" \
    "s_add_u32 m0, %[wm], 0x6000\n\t" \
    "s_mov_b32 s44, 0x3c0000\n\t" \
    "buffer_load_dwordx4 v240, %[wr], s44 offen offset:0 nt lds\n\t" \
    "buffer_load_dwordx4 v241, %[wr], s44 offen offset:1024 nt lds\n\t" \
    "buffer_load_dwordx4 v242, %[wr], s44 offen offset:2048 nt lds\n\t" \
    "ds_read_b64_tr_b16 v[204:205], v244 offset:0\n\t" \
    "ds_read_b64_tr_b16 v[206:207], v244 offset:1536\n\t" \
    "ds_read_b64_tr_b16 v[4:5], v244 offset:32\n\t" \
    "ds_read_b64_tr_b16 v[6:7], v244 offset:1568\n\t" \
    "ds_read_b64_tr_b16 v[12:13], v244 offset:64\n\t" \
    "ds_read_b64_tr_b16 v[14:15], v244 offset:1600\n\t" \
    "v_add_u32 v244, 0xc000, v243\n\t" \
    "s_waitcnt vmcnt(12) lgkmcnt(0)\n\t" \
    "s_barrier \n\t" \
    "s_add_u32 m0, %[wm], 0x9000\n\t" \
    "s_mov_b32 s44, 0x420000\n\t" \
    "buffer_load_dwordx4 v240, %[wr], s44 offen offset:0 nt lds\n\t" \
    "buffer_load_dwordx4 v241, %[wr], s44 offen offset:1024 nt lds\n\t" \
    "buffer_load_dwordx4 v242, %[wr], s44 offen offset:2048 nt lds\n\t" \
    "ds_read_b64_tr_b16 v[208:209], v244 offset:0\n\t" \
    "ds_read_b64_tr_b16 v[210:211], v244 offset:1536\n\t" \
    "ds_read_b64_tr_b16 v[40:41], v244 offset:32\n\t" \
    "ds_read_b64_tr_b16 v[42:43], v244 offset:1568\n\t" \
    "ds_read_b64_tr_b16 v[48:49], v244 offset:64\n\t" \
    "ds_read_b64_tr_b16 v[50:51], v244 offset:1600\n\t" \
    "v_add_u32 v244, 0xf000, v243\n\t" \
    "s_waitcnt vmcnt(12) lgkmcnt(0)\n\t" \
    "s_barrier \n\t" \
    "s_add_u32 m0, %[wm], 0xc000\n\t" \
    "s_mov_b32 s44, 0x480000\n\t" \
    "buffer_load_dwordx4 v240, %[wr], s44 offen offset:0 nt lds\n\t" \
    "buffer_load_dwordx4 v241, %[wr], s44 offen offset:1024 nt lds\n\t" \
    "buffer_load_dwordx4 v242, %[wr], s44 offen offset:2048 nt lds\n\t" \
    "ds_read_b64_tr_b16 v[212:213], v244 offset:0\n\t" \
    "ds_read_b64_tr_b16 v[214:215], v244 offset:1536\n\t" \
    "ds_read_b64_tr_b16 v[44:45], v244 offset:32\n\t" \
    "ds_read_b64_tr_b16 v[46:47], v244 offset:1568\n\t" \
    "ds_read_b64_tr_b16 v[52:53], v244 offset:64\n\t" \
    "ds_read_b64_tr_b16 v[54:55], v244 offset:1600\n\t" \
    "v_add_u32 v244, 0x0, v243\n\t" \
    "s_waitcnt vmcnt(12) lgkmcnt(0)\n\t" \
    "s_barrier \n\t" \
    "v_mov_b32 v246, 0\n\t" \
    "buffer_load_dword v246, v246, s[60:63], %[mo] offen sc1\n\t" \
    "s_add_u32 m0, %[wm], 0xf000\n\t" \
    "s_mov_b32 s44, 0x4e0000\n\t" \
    "buffer_load_dwordx4 v240, %[wr], s44 offen offset:0 nt lds\n\t" \
    "buffer_load_dwordx4 v241, %[wr], s44 offen offset:1024 nt lds\n\t" \
    "buffer_load_dwordx4 v242, %[wr], s44 offen offset:2048 nt lds\n\t" \
    "ds_read_b64_tr_b16 v[136:137], v244 offset:0\n\t" \
    "ds_read_b64_tr_b16 v[138:139], v244 offset:1536\n\t" \
    "ds_read_b64_tr_b16 v[144:145], v244 offset:32\n\t" \
    "ds_read_b64_tr_b16 v[146:147], v244 offset:1568\n\t" \
    "ds_read_b64_tr_b16 v[152:153], v244 offset:64\n\t" \
    "ds_read_b64_tr_b16 v[154:155], v244 offset:1600\n\t" \
    "v_add_u32 v244, 0x3000, v243\n\t" \
    "s_waitcnt vmcnt(12) lgkmcnt(0)\n\t" \
    "s_barrier \n\t" \
    "s_add_u32 m0, %[wm], 0x0\n\t" \
    "s_mov_b32 s44, 0x540000\n\t" \
    "buffer_load_dwordx4 v240, %[wr], s44 offen offset:0 nt lds\n\t" \
    "buffer_load_dwordx4 v241, %[wr], s44 offen offset:1024 nt lds\n\t" \
    "buffer_load_dwordx4 v242, %[wr], s44 offen offset:2048 nt lds\n\t" \
    "ds_read_b64_tr_b16 v[140:141], v244 offset:0\n\t" \
    "ds_read_b64_tr_b16 v[142:143], v244 offset:1536\n\t" \
    "ds_read_b64_tr_b16 v[148:149], v244 offset:32\n\t" \
    "ds_read_b64_tr_b16 v[150:151], v244 offset:1568\n\t" \
    "ds_read_b64_tr_b16 v[156:157], v244 offset:64\n\t" \
    "ds_read_b64_tr_b16 v[158:159], v244 offset:1600\n\t" \
    "v_add_u32 v244, 0x6000, v243\n\t" \
    "s_waitcnt vmcnt(12) lgkmcnt(0)\n\t" \
    "s_barrier \n\t" \
    "s_add_u32 m0, %[wm], 0x3000\n\t" \
    "s_mov_b32 s44, 0x5a0000\n\t" \
    "buffer_load_dwordx4 v240, %[wr], s44 offen offset:0 nt lds\n\t" \
    "buffer_load_dwordx4 v241, %[wr], s44 offen offset:1024 nt lds\n\t" \
    "buffer_load_dwordx4 v242, %[wr], s44 offen offset:2048 nt lds\n\t" \
    "ds_read_b64_tr_b16 v[216:217], v244 offset:0\n\t" \
    "ds_read_b64_tr_b16 v[218:219], v244 offset:1536\n\t" \
    "ds_read_b64_tr_b16 v[80:81], v244 offset:32\n\t" \
    "ds_read_b64_tr_b16 v[82:83], v244 offset:1568\n\t" \
    "ds_read_b64_tr_b16 v[88:89], v244 offset:64\n\t" \
    "ds_read_b64_tr_b16 v[90:91], v244 offset:1600\n\t" \
    "v_add_u32 v244, 0x9000, v243\n\t" \
    "s_waitcnt vmcnt(12) lgkmcnt(0)\n\t" \
    "s_barrier \n\t" \
    "s_add_u32 m0, %[wm], 0x6000\n\t" \
    "s_mov_b32 s44, 0x600000\n\t" \
    "buffer_load_dwordx4 v240, %[wr], s44 offen offset:0 nt lds\n\t" \
    "buffer_load_dwordx4 v241, %[wr], s44 offen offset:1024 nt lds\n\t" \
    "buffer_load_dwordx4 v242, %[wr], s44 offen offset:2048 nt lds\n\t" \
    "ds_read_b64_tr_b16 v[220:221], v244 offset:0\n\t" \
    "ds_read_b64_tr_b16 v[222:223], v244 offset:1536\n\t" \
    "ds_read_b64_tr_b16 v[84:85], v244 offset:32\n\t" \
    "ds_read_b64_tr_b16 v[86:87], v244 offset:1568\n\t" \
    "ds_read_b64_tr_b16 v[92:93], v244 offset:64\n\t" \
    "ds_read_b64_tr_b16 v[94:95], v244 offset:1600\n\t" \
    "v_add_u32 v244, 0xc000, v243\n\t" \
    "s_waitcnt vmcnt(12) lgkmcnt(0)\n\t" \
    "s_barrier \n\t" \
    "s_add_u32 m0, %[wm], 0x9000\n\t" \
    "s_mov_b32 s44, 0x660000\n\t" \
    "buffer_load_dwordx4 v240, %[wr], s44 offen offset:0 nt lds\n\t" \
    "buffer_load_dwordx4 v241, %[wr], s44 offen offset:1024 nt lds\n\t" \
    "buffer_load_dwordx4 v242, %[wr], s44 offen offset:2048 nt lds\n\t" \
    "ds_read_b64_tr_b16 v[16:17], v244 offset:0\n\t" \
    "ds_read_b64_tr_b16 v[18:19], v244 offset:1536\n\t" \
    "ds_read_b64_tr_b16 v[24:25], v244 offset:32\n\t" \
    "ds_read_b64_tr_b16 v[26:27], v244 offset:1568\n\t" \
    "ds_read_b64_tr_b16 v[32:33], v244 offset:64\n\t" \
    "ds_read_b64_tr_b16 v[34:35], v244 offset:1600\n\t" \
    "v_add_u32 v244, 0xf000, v243\n\t" \
    "s_waitcnt vmcnt(12) lgkmcnt(0)\n\t" \
    "s_barrier \n\t" \
    ".Lv11_%=:\n\t" \
    "v_readfirstlane_b32 s43, v246\n\t" \
    "s_nop 0\n\t" \
    "s_cmp_lg_u32 s43, -1\n\t" \
    "s_cbranch_scc1 .Lv12_%=\n\t" \
    "s_cmp_eq_u32 s41, 0\n\t" \
    "s_cbranch_scc1 .Lv12_%=\n\t" \
    "s_sub_u32 s41, s41, 1\n\t" \
    "s_sleep 4\n\t" \
    "v_mov_b32 v246, 0\n\t" \
    "buffer_load_dword v246, v246, s[60:63], %[mo] offen sc1\n\t" \
    "s_waitcnt vmcnt(0)\n\t" \
    "s_branch .Lv11_%=\n\t" \
    ".Lv12_%=:\n\t" \
    "v_mov_b32 v246, 0x8000\n\t" \
    "s_mov_b64 exec, 1\n\t" \
    "buffer_store_dword v246, v246, s[60:63], %[mo] offen sc1\n\t" \
    "s_mov_b64 exec, -1\n\t" \
    "s_and_b32 s43, s43, 0x2000\n\t" \
    "s_xor_b32 s43, s43, %[lx]\n\t" \
    "s_add_u32 s60, s60, s43\n\t" \
    "s_addc_u32 s61, s61, 0\n\t" \
    "v_and_b32 v252, 0xf0, v253\n\t" \
    "v_lshlrev_b32 v252, 3, v252\n\t" \
    "buffer_load_dword v252, v252, s[60:63], %[fl] offen sc1\n\t" \
    "s_add_u32 m0, %[wm], 0xc000\n\t" \
    "s_mov_b32 s44, 0x6c0000\n\t" \
    "buffer_load_dwordx4 v240, %[wr], s44 offen offset:0 nt lds\n\t" \
    "buffer_load_dwordx4 v241, %[wr], s44 offen offset:1024 nt lds\n\t" \
    "buffer_load_dwordx4 v242, %[wr], s44 offen offset:2048 nt lds\n\t" \
    "ds_read_b64_tr_b16 v[20:21], v244 offset:0\n\t" \
    "ds_read_b64_tr_b16 v[22:23], v244 offset:1536\n\t" \
    "ds_read_b64_tr_b16 v[28:29], v244 offset:32\n\t" \
    "ds_read_b64_tr_b16 v[30:31], v244 offset:1568\n\t" \
    "ds_read_b64_tr_b16 v[36:37], v244 offset:64\n\t" \
    "ds_read_b64_tr_b16 v[38:39], v244 offset:1600\n\t" \
    "v_add_u32 v244, 0x0, v243\n\t" \
    "s_waitcnt vmcnt(12) lgkmcnt(0)\n\t" \
    "s_barrier \n\t" \
    "s_add_u32 m0, %[wm], 0xf000\n\t" \
    "s_mov_b32 s44, 0x720000\n\t" \
    "buffer_load_dwordx4 v240, %[wr], s44 offen offset:0 nt lds\n\t" \
    "buffer_load_dwordx4 v241, %[wr], s44 offen offset:1024 nt lds\n\t" \
    "buffer_load_dwordx4 v242, %[wr], s44 offen offset:2048 nt lds\n\t" \
    "ds_read_b64_tr_b16 v[224:225], v244 offset:0\n\t" \
    "ds_read_b64_tr_b16 v[226:227], v244 offset:1536\n\t" \
    "ds_read_b64_tr_b16 v[120:121], v244 offset:32\n\t" \
    "ds_read_b64_tr_b16 v[122:123], v244 offset:1568\n\t" \
    "ds_read_b64_tr_b16 v[128:129], v244 offset:64\n\t" \
    "ds_read_b64_tr_b16 v[130:131], v244 offset:1600\n\t" \
    "v_add_u32 v244, 0x3000, v243\n\t" \
    "s_waitcnt vmcnt(12) lgkmcnt(0)\n\t" \
    "s_barrier \n\t" \
    "ds_read_b64_tr_b16 v[228:229], v244 offset:0\n\t" \
    "ds_read_b64_tr_b16 v[230:231], v244 offset:1536\n\t" \
    "ds_read_b64_tr_b16 v[124:125], v244 offset:32\n\t" \
    "ds_read_b64_tr_b16 v[126:127], v244 offset:1568\n\t" \
    "ds_read_b64_tr_b16 v[132:133], v244 offset:64\n\t" \
    "ds_read_b64_tr_b16 v[134:135], v244 offset:1600\n\t" \
    "v_add_u32 v244, 0x6000, v243\n\t" \
    "s_waitcnt vmcnt(9) lgkmcnt(0)\n\t" \
    "s_barrier \n\t" \
    "ds_read_b64_tr_b16 v[232:233], v244 offset:0\n\t" \
    "ds_read_b64_tr_b16 v[234:235], v244 offset:1536\n\t" \
    "ds_read_b64_tr_b16 v[160:161], v244 offset:32\n\t" \
    "ds_read_b64_tr_b16 v[162:163], v244 offset:1568\n\t" \
    "ds_read_b64_tr_b16 v[168:169], v244 offset:64\n\t" \
    "ds_read_b64_tr_b16 v[170:171], v244 offset:1600\n\t" \
    "v_add_u32 v244, 0x9000, v243\n\t" \
    "s_waitcnt vmcnt(6) lgkmcnt(0)\n\t" \
    "s_barrier \n\t" \
    "ds_read_b64_tr_b16 v[236:237], v244 offset:0\n\t" \
    "ds_read_b64_tr_b16 v[238:239], v244 offset:1536\n\t" \
    "ds_read_b64_tr_b16 v[164:165], v244 offset:32\n\t" \
    "ds_read_b64_tr_b16 v[166:167], v244 offset:1568\n\t" \
    "ds_read_b64_tr_b16 v[172:173], v244 offset:64\n\t" \
    "ds_read_b64_tr_b16 v[174:175], v244 offset:1600\n\t" \
    "v_add_u32 v244, 0xc000, v243\n\t" \
    "s_waitcnt vmcnt(3) lgkmcnt(0)\n\t" \
    "s_barrier \n\t" \
    "ds_read_b64_tr_b16 v[176:177], v244 offset:0\n\t" \
    "ds_read_b64_tr_b16 v[178:179], v244 offset:1536\n\t" \
    "ds_read_b64_tr_b16 v[184:185], v244 offset:32\n\t" \
    "ds_read_b64_tr_b16 v[186:187], v244 offset:1568\n\t" \
    "ds_read_b64_tr_b16 v[192:193], v244 offset:64\n\t" \
    "ds_read_b64_tr_b16 v[194:195], v244 offset:1600\n\t" \
    "s_mov_b32 s43, 0\n\t" \
    "v_cmp_eq_u32 vcc, 0, v252\n\t" \
    "s_cbranch_vccnz .Lv13_%=\n\t" \
    "s_mov_b32 m0, 0x1000\n\t" \
    "s_add_u32 s49, s42, 0x0\n\t" \
    "buffer_load_dwordx4 v253, s[60:63], s49 offen offset:0 sc1 lds\n\t" \
    "buffer_load_dwordx4 v253, s[60:63], s49 offen offset:1024 sc1 lds\n\t" \
    "buffer_load_dwordx4 v253, s[60:63], s49 offen offset:2048 sc1 lds\n\t" \
    "buffer_load_dwordx4 v253, s[60:63], s49 offen offset:3072 sc1 lds\n\t" \
    "s_mov_b32 m0, 0x4400\n\t" \
    "s_add_u32 s49, s42, 0xc400\n\t" \
    "buffer_load_dwordx4 v253, s[60:63], s49 offen offset:0 sc1 lds\n\t" \
    "s_mov_b32 m0, 0x6000\n\t" \
    "s_add_u32 s49, s42, 0x1000\n\t" \
    "buffer_load_dwordx4 v253, s[60:63], s49 offen offset:0 sc1 lds\n\t" \
    "buffer_load_dwordx4 v253, s[60:63], s49 offen offset:1024 sc1 lds\n\t" \
    "buffer_load_dwordx4 v253, s[60:63], s49 offen offset:2048 sc1 lds\n\t" \
    "buffer_load_dwordx4 v253, s[60:63], s49 offen offset:3072 sc1 lds\n\t" \
    "s_mov_b32 m0, 0x9400\n\t" \
    "s_add_u32 s49, s42, 0xd400\n\t" \
    "buffer_load_dwordx4 v253, s[60:63], s49 offen offset:0 sc1 lds\n\t" \
    "s_mov_b32 s43, 1\n\t" \
    ".Lv13_%=:\n\t" \
    "v_add_u32 v244, 0xf000, v243\n\t" \
    "s_waitcnt vmcnt(0) lgkmcnt(0)\n\t" \
    "s_barrier \n\t" \
    "ds_read_b64_tr_b16 v[180:181], v244 offset:0\n\t" \
    "ds_read_b64_tr_b16 v[182:183], v244 offset:1536\n\t" \
    "ds_read_b64_tr_b16 v[188:189], v244 offset:32\n\t" \
    "ds_read_b64_tr_b16 v[190:191], v244 offset:1568\n\t" \
    "ds_read_b64_tr_b16 v[196:197], v244 offset:64\n\t" \
    "ds_read_b64_tr_b16 v[198:199], v244 offset:1600\n\t" \
    "s_waitcnt lgkmcnt(0)\n\t" \
    "s_barrier \n\t" \
    LST0 \
    LJIT \
    "ds_write_b128 v254, v[200:203] offset:40960\n\t" \
    "ds_write_b128 v254, v[204:207] offset:41984\n\t" \
    "ds_write_b128 v254, v[208:211] offset:43008\n\t" \
    "ds_write_b128 v254, v[212:215] offset:44032\n\t" \
    "ds_write_b128 v254, v[216:219] offset:45056\n\t" \
    "ds_write_b128 v254, v[220:223] offset:46080\n\t" \
    "ds_write_b128 v254, v[224:227] offset:47104\n\t" \
    "ds_write_b128 v254, v[228:231] offset:48128\n\t" \
    "ds_write_b128 v254, v[232:235] offset:49152\n\t" \
    "ds_write_b128 v254, v[236:239] offset:50176\n\t" \
    "s_cmp_lg_u32 s43, 0\n\t" \
    "s_cbranch_scc1 .Lv14_%=\n\t" \
    "s_mov_b32 s44, 0\n\t" \
    ".Lv15_%=:\n\t" \
    "v_cmp_ge_u32 vcc, s44, v252\n\t" \
    "s_cbranch_vccz .Lv16_%=\n\t" \
    "s_cmp_eq_u32 s41, 0\n\t" \
    "s_cbranch_scc1 .Lv16_%=\n\t" \
    "s_sub_u32 s41, s41, 1\n\t" \
    "s_sleep 8\n\t" \
    "v_and_b32 v252, 0xf0, v253\n\t" \
    "v_lshlrev_b32 v252, 3, v252\n\t" \
    "buffer_load_dword v252, v252, s[60:63], %[fl] offen sc1\n\t" \
    "s_waitcnt vmcnt(0)\n\t" \
    "s_branch .Lv15_%=\n\t" \
    ".Lv16_%=:\n\t" \
    "s_mov_b32 m0, 0x1000\n\t" \
    "s_add_u32 s49, s42, 0x0\n\t" \
    "buffer_load_dwordx4 v253, s[60:63], s49 offen offset:0 sc1 lds\n\t" \
    "buffer_load_dwordx4 v253, s[60:63], s49 offen offset:1024 sc1 lds\n\t" \
    "buffer_load_dwordx4 v253, s[60:63], s49 offen offset:2048 sc1 lds\n\t" \
    "buffer_load_dwordx4 v253, s[60:63], s49 offen offset:3072 sc1 lds\n\t" \
    "s_mov_b32 m0, 0x4400\n\t" \
    "s_add_u32 s49, s42, 0xc400\n\t" \
    "buffer_load_dwordx4 v253, s[60:63], s49 offen offset:0 sc1 lds\n\t" \
    "s_mov_b32 m0, 0x6000\n\t" \
    "s_add_u32 s49, s42, 0x1000\n\t" \
    "buffer_load_dwordx4 v253, s[60:63], s49 offen offset:0 sc1 lds\n\t" \
    "buffer_load_dwordx4 v253, s[60:63], s49 offen offset:1024 sc1 lds\n\t" \
    "buffer_load_dwordx4 v253, s[60:63], s49 offen offset:2048 sc1 lds\n\t" \
    "buffer_load_dwordx4 v253, s[60:63], s49 offen offset:3072 sc1 lds\n\t" \
    "s_mov_b32 m0, 0x9400\n\t" \
    "s_add_u32 s49, s42, 0xd400\n\t" \
    "buffer_load_dwordx4 v253, s[60:63], s49 offen offset:0 sc1 lds\n\t" \
    ".Lv14_%=:\n\t" \
    "s_waitcnt vmcnt(0) lgkmcnt(0)\n\t" \
    "s_barrier \n\t" \
    "ds_read_b128 v[224:227], v253 offset:4096\n\t" \
    "ds_read_b128 v[244:247], v254 offset:40960\n\t" \
    "ds_read_b128 v[228:231], v253 offset:5120\n\t" \
    "ds_read_b128 v[232:235], v253 offset:24576\n\t" \
    "ds_read_b128 v[236:239], v253 offset:25600\n\t" \
    "ds_read_b128 v[248:251], v254 offset:41984\n\t" \
    ".Lv17_%=:\n\t" \
    LST7 \
    LJIT \
    "s_waitcnt lgkmcnt(5)\n\t" \
    "v_mfma_f32_16x16x32_bf16 v[204:207], v[0:3], v[224:227], 0\n\t" \
    "s_waitcnt lgkmcnt(3)\n\t" \
    "v_mfma_f32_16x16x32_bf16 v[204:207], v[4:7], v[228:231], v[204:207]\n\t" \
    "ds_read_b128 v[240:243], v253 offset:12288\n\t" \
    "v_mfma_f32_16x16x32_bf16 v[208:211], v[8:11], v[224:227], 0\n\t" \
    "v_mfma_f32_16x16x32_bf16 v[208:211], v[12:15], v[228:231], v[208:211]\n\t" \
    "v_mfma_f32_16x16x32_bf16 v[200:203], v[244:247], v[224:227], 0\n\t" \
    "s_waitcnt lgkmcnt(1)\n\t" \
    "v_mfma_f32_16x16x32_bf16 v[200:203], v[248:251], v[228:231], v[200:203]\n\t" \
    "v_mfma_f32_16x16x32_bf16 v[216:219], v[0:3], v[232:235], 0\n\t" \
    "ds_read_b128 v[224:227], v253 offset:13312\n\t" \
    "v_mfma_f32_16x16x32_bf16 v[216:219], v[4:7], v[236:239], v[216:219]\n\t" \
    "ds_read_b128 v[228:231], v253 offset:6144\n\t" \
    "v_mfma_f32_16x16x32_bf16 v[220:223], v[8:11], v[232:235], 0\n\t" \
    "v_mfma_f32_16x16x32_bf16 v[220:223], v[12:15], v[236:239], v[220:223]\n\t" \
    "v_mfma_f32_16x16x32_bf16 v[212:215], v[244:247], v[232:235], 0\n\t" \
    "ds_read_b128 v[244:247], v254 offset:43008\n\t" \
    "v_mfma_f32_16x16x32_bf16 v[212:215], v[248:251], v[236:239], v[212:215]\n\t" \
    "ds_read_b128 v[248:251], v254 offset:44032\n\t" \
    "s_waitcnt lgkmcnt(4)\n\t" \
    "v_mfma_f32_16x16x32_bf16 v[204:207], v[24:27], v[240:243], v[204:207]\n\t" \
    "ds_read_b128 v[232:235], v253 offset:7168\n\t" \
    "s_waitcnt lgkmcnt(4)\n\t" \
    "v_mfma_f32_16x16x32_bf16 v[204:207], v[28:31], v[224:227], v[204:207]\n\t" \
    "ds_read_b128 v[236:239], v253 offset:26624\n\t" \
    "v_mfma_f32_16x16x32_bf16 v[208:211], v[32:35], v[240:243], v[208:211]\n\t" \
    "v_mfma_f32_16x16x32_bf16 v[208:211], v[36:39], v[224:227], v[208:211]\n\t" \
    "v_mfma_f32_16x16x32_bf16 v[200:203], v[16:19], v[240:243], v[200:203]\n\t" \
    "v_mfma_f32_16x16x32_bf16 v[200:203], v[20:23], v[224:227], v[200:203]\n\t" \
    "s_waitcnt lgkmcnt(4)\n\t" \
    "v_mfma_f32_16x16x32_bf16 v[204:207], v[40:43], v[228:231], v[204:207]\n\t" \
    "ds_read_b128 v[240:243], v253 offset:27648\n\t" \
    "s_waitcnt lgkmcnt(2)\n\t" \
    "v_mfma_f32_16x16x32_bf16 v[204:207], v[44:47], v[232:235], v[204:207]\n\t" \
    "ds_read_b128 v[224:227], v253 offset:0\n\t" \
    "v_mfma_f32_16x16x32_bf16 v[208:211], v[48:51], v[228:231], v[208:211]\n\t" \
    "v_mfma_f32_16x16x32_bf16 v[208:211], v[52:55], v[232:235], v[208:211]\n\t" \
    "v_mfma_f32_16x16x32_bf16 v[200:203], v[244:247], v[228:231], v[200:203]\n\t" \
    "v_mfma_f32_16x16x32_bf16 v[200:203], v[248:251], v[232:235], v[200:203]\n\t" \
    "s_waitcnt lgkmcnt(2)\n\t" \
    "v_mfma_f32_16x16x32_bf16 v[216:219], v[40:43], v[236:239], v[216:219]\n\t" \
    "ds_read_b128 v[228:231], v253 offset:1024\n\t" \
    "s_add_u32 s44, s40, 1\n\t" \
    "s_waitcnt lgkmcnt(2)\n\t" \
    "v_mfma_f32_16x16x32_bf16 v[216:219], v[44:47], v[240:243], v[216:219]\n\t" \
    "ds_read_b128 v[232:235], v253 offset:10240\n\t" \
    "s_and_b32 s45, s44, 15\n\t" \
    "v_mfma_f32_16x16x32_bf16 v[220:223], v[48:51], v[236:239], v[220:223]\n\t" \
    "s_cmp_eq_u32 s45, 0\n\t" \
    "v_mfma_f32_16x16x32_bf16 v[220:223], v[52:55], v[240:243], v[220:223]\n\t" \
    "s_cselect_b32 s45, %[cj], %[cs]\n\t" \
    "v_mfma_f32_16x16x32_bf16 v[212:215], v[244:247], v[236:239], v[212:215]\n\t" \
    "ds_read_b128 v[244:247], v254 offset:45056\n\t" \
    "s_cmp_eq_u32 s44, 64\n\t" \
    "v_mfma_f32_16x16x32_bf16 v[212:215], v[248:251], v[240:243], v[212:215]\n\t" \
    "ds_read_b128 v[248:251], v254 offset:46080\n\t" \
    "s_cselect_b32 s45, 0, s45\n\t" \
    "s_waitcnt lgkmcnt(4)\n\t" \
    "v_mfma_f32_16x16x32_bf16 v[204:207], v[64:67], v[224:227], v[204:207]\n\t" \
    "ds_read_b128 v[236:239], v253 offset:11264\n\t" \
    "s_add_u32 s42, s42, s45\n\t" \
    "s_waitcnt lgkmcnt(4)\n\t" \
    "v_mfma_f32_16x16x32_bf16 v[204:207], v[68:71], v[228:231], v[204:207]\n\t" \
    "ds_read_b128 v[240:243], v253 offset:30720\n\t" \
    "v_mfma_f32_16x16x32_bf16 v[208:211], v[72:75], v[224:227], v[208:211]\n\t" \
    "v_mfma_f32_16x16x32_bf16 v[208:211], v[76:79], v[228:231], v[208:211]\n\t" \
    "v_mfma_f32_16x16x32_bf16 v[200:203], v[56:59], v[224:227], v[200:203]\n\t" \
    "v_mfma_f32_16x16x32_bf16 v[200:203], v[60:63], v[228:231], v[200:203]\n\t" \
    "s_waitcnt lgkmcnt(4)\n\t" \
    "v_mfma_f32_16x16x32_bf16 v[204:207], v[80:83], v[232:235], v[204:207]\n\t" \
    "ds_read_b128 v[224:227], v253 offset:31744\n\t" \
    "s_waitcnt lgkmcnt(2)\n\t" \
    "v_mfma_f32_16x16x32_bf16 v[204:207], v[84:87], v[236:239], v[204:207]\n\t" \
    "ds_read_b128 v[228:231], v253 offset:2048\n\t" \
    "v_mfma_f32_16x16x32_bf16 v[208:211], v[88:91], v[232:235], v[208:211]\n\t" \
    "v_mfma_f32_16x16x32_bf16 v[208:211], v[92:95], v[236:239], v[208:211]\n\t" \
    "v_mfma_f32_16x16x32_bf16 v[200:203], v[244:247], v[232:235], v[200:203]\n\t" \
    "v_mfma_f32_16x16x32_bf16 v[200:203], v[248:251], v[236:239], v[200:203]\n\t" \
    "s_waitcnt lgkmcnt(2)\n\t" \
    "v_mfma_f32_16x16x32_bf16 v[216:219], v[80:83], v[240:243], v[216:219]\n\t" \
    "ds_read_b128 v[232:235], v253 offset:3072\n\t" \
    "s_and_b32 s44, s40, 3\n\t" \
    "s_waitcnt lgkmcnt(2)\n\t" \
    "v_mfma_f32_16x16x32_bf16 v[216:219], v[84:87], v[224:227], v[216:219]\n\t" \
    "ds_read_b128 v[236:239], v253 offset:14336\n\t" \
    "s_lshl_b32 s44, s44, 17\n\t" \
    "v_mfma_f32_16x16x32_bf16 v[220:223], v[88:91], v[240:243], v[220:223]\n\t" \
    "s_add_u32 s44, s44, %[mb]\n\t" \
    "v_mfma_f32_16x16x32_bf16 v[220:223], v[92:95], v[224:227], v[220:223]\n\t" \
    "s_add_u32 s46, s44, 0x800\n\t" \
    "v_mfma_f32_16x16x32_bf16 v[212:215], v[244:247], v[240:243], v[212:215]\n\t" \
    "ds_read_b128 v[244:247], v254 offset:47104\n\t" \
    "s_add_u32 s47, s44, 0x1800\n\t" \
    "v_mfma_f32_16x16x32_bf16 v[212:215], v[248:251], v[224:227], v[212:215]\n\t" \
    "ds_read_b128 v[248:251], v254 offset:48128\n\t" \
    "s_add_u32 s48, s44, 0xc00\n\t" \
    "s_waitcnt lgkmcnt(4)\n\t" \
    "v_mfma_f32_16x16x32_bf16 v[204:207], v[104:107], v[228:231], v[204:207]\n\t" \
    "ds_read_b128 v[240:243], v253 offset:15360\n\t" \
    "s_waitcnt lgkmcnt(4)\n\t" \
    "v_mfma_f32_16x16x32_bf16 v[204:207], v[108:111], v[232:235], v[204:207]\n\t" \
    "ds_read_b128 v[224:227], v253 offset:34816\n\t" \
    "v_mfma_f32_16x16x32_bf16 v[208:211], v[112:115], v[228:231], v[208:211]\n\t" \
    "v_mfma_f32_16x16x32_bf16 v[208:211], v[116:119], v[232:235], v[208:211]\n\t" \
    "v_mfma_f32_16x16x32_bf16 v[200:203], v[96:99], v[228:231], v[200:203]\n\t" \
    "v_mfma_f32_16x16x32_bf16 v[200:203], v[100:103], v[232:235], v[200:203]\n\t" \
    "s_waitcnt lgkmcnt(4)\n\t" \
    "v_mfma_f32_16x16x32_bf16 v[204:207], v[120:123], v[236:239], v[204:207]\n\t" \
    "ds_read_b128 v[228:231], v253 offset:35840\n\t" \
    "s_waitcnt lgkmcnt(2)\n\t" \
    "v_mfma_f32_16x16x32_bf16 v[204:207], v[124:127], v[240:243], v[204:207]\n\t" \
    "ds_read_b128 v[232:235], v253 offset:8192\n\t" \
    "v_mfma_f32_16x16x32_bf16 v[208:211], v[128:131], v[236:239], v[208:211]\n\t" \
    "v_mfma_f32_16x16x32_bf16 v[208:211], v[132:135], v[240:243], v[208:211]\n\t" \
    "v_mfma_f32_16x16x32_bf16 v[200:203], v[244:247], v[236:239], v[200:203]\n\t" \
    "v_mfma_f32_16x16x32_bf16 v[200:203], v[248:251], v[240:243], v[200:203]\n\t" \
    "s_waitcnt lgkmcnt(2)\n\t" \
    "v_mfma_f32_16x16x32_bf16 v[216:219], v[120:123], v[224:227], v[216:219]\n\t" \
    "ds_read_b128 v[236:239], v253 offset:9216\n\t" \
    "s_waitcnt lgkmcnt(2)\n\t" \
    "v_mfma_f32_16x16x32_bf16 v[216:219], v[124:127], v[228:231], v[216:219]\n\t" \
    "ds_read_b128 v[240:243], v253 offset:16384\n\t" \
    "v_mfma_f32_16x16x32_bf16 v[220:223], v[128:131], v[224:227], v[220:223]\n\t" \
    "v_mfma_f32_16x16x32_bf16 v[220:223], v[132:135], v[228:231], v[220:223]\n\t" \
    "v_mfma_f32_16x16x32_bf16 v[212:215], v[244:247], v[224:227], v[212:215]\n\t" \
    "ds_read_b128 v[244:247], v254 offset:49152\n\t" \
    "v_mfma_f32_16x16x32_bf16 v[212:215], v[248:251], v[228:231], v[212:215]\n\t" \
    "ds_read_b128 v[248:251], v254 offset:50176\n\t" \
    "v_and_b32 v252, 0xf0, v253\n\t" \
    "v_lshlrev_b32 v252, 3, v252\n\t" \
    "buffer_load_dword v252, v252, s[60:63], %[fl] offen sc1\n\t" \
    "s_waitcnt lgkmcnt(4)\n\t" \
    "v_mfma_f32_16x16x32_bf16 v[204:207], v[144:147], v[232:235], v[204:207]\n\t" \
    "ds_read_b128 v[224:227], v253 offset:17408\n\t" \
    "s_waitcnt lgkmcnt(4)\n\t" \
    "v_mfma_f32_16x16x32_bf16 v[204:207], v[148:151], v[236:239], v[204:207]\n\t" \
    "ds_read_b128 v[228:231], v253 offset:36864\n\t" \
    "v_mfma_f32_16x16x32_bf16 v[208:211], v[152:155], v[232:235], v[208:211]\n\t" \
    "v_mfma_f32_16x16x32_bf16 v[208:211], v[156:159], v[236:239], v[208:211]\n\t" \
    "v_mfma_f32_16x16x32_bf16 v[200:203], v[136:139], v[232:235], v[200:203]\n\t" \
    "v_mfma_f32_16x16x32_bf16 v[200:203], v[140:143], v[236:239], v[200:203]\n\t" \
    "s_waitcnt lgkmcnt(4)\n\t" \
    "v_mfma_f32_16x16x32_bf16 v[204:207], v[160:163], v[240:243], v[204:207]\n\t" \
    "ds_read_b128 v[232:235], v253 offset:37888\n\t" \
    "s_waitcnt lgkmcnt(2)\n\t" \
    "v_mfma_f32_16x16x32_bf16 v[204:207], v[164:167], v[224:227], v[204:207]\n\t" \
    "ds_read_b128 v[236:239], v253 offset:18432\n\t" \
    "v_mfma_f32_16x16x32_bf16 v[208:211], v[168:171], v[240:243], v[208:211]\n\t" \
    "v_mfma_f32_16x16x32_bf16 v[208:211], v[172:175], v[224:227], v[208:211]\n\t" \
    "v_mfma_f32_16x16x32_bf16 v[200:203], v[244:247], v[240:243], v[200:203]\n\t" \
    "v_mfma_f32_16x16x32_bf16 v[200:203], v[248:251], v[224:227], v[200:203]\n\t" \
    "s_waitcnt lgkmcnt(2)\n\t" \
    "v_mfma_f32_16x16x32_bf16 v[216:219], v[160:163], v[228:231], v[216:219]\n\t" \
    "ds_read_b128 v[240:243], v253 offset:19456\n\t" \
    "s_waitcnt lgkmcnt(2)\n\t" \
    "v_mfma_f32_16x16x32_bf16 v[216:219], v[164:167], v[232:235], v[216:219]\n\t" \
    "ds_read_b128 v[224:227], v253 offset:32768\n\t" \
    "v_mfma_f32_16x16x32_bf16 v[220:223], v[168:171], v[228:231], v[220:223]\n\t" \
    "v_mfma_f32_16x16x32_bf16 v[220:223], v[172:175], v[232:235], v[220:223]\n\t" \
    "v_mfma_f32_16x16x32_bf16 v[212:215], v[244:247], v[228:231], v[212:215]\n\t" \
    "v_mfma_f32_16x16x32_bf16 v[212:215], v[248:251], v[232:235], v[212:215]\n\t" \
    "s_waitcnt lgkmcnt(2)\n\t" \
    "v_mfma_f32_16x16x32_bf16 v[204:207], v[184:187], v[236:239], v[204:207]\n\t" \
    "ds_read_b128 v[228:231], v253 offset:33792\n\t" \
    "s_waitcnt lgkmcnt(2)\n\t" \
    "v_mfma_f32_16x16x32_bf16 v[204:207], v[188:191], v[240:243], v[204:207]\n\t" \
    "ds_read_b128 v[232:235], v253 offset:20480\n\t" \
    "v_mfma_f32_16x16x32_bf16 v[208:211], v[192:195], v[236:239], v[208:211]\n\t" \
    "v_mfma_f32_16x16x32_bf16 v[208:211], v[196:199], v[240:243], v[208:211]\n\t" \
    "v_mfma_f32_16x16x32_bf16 v[200:203], v[176:179], v[236:239], v[200:203]\n\t" \
    "v_mfma_f32_16x16x32_bf16 v[200:203], v[180:183], v[240:243], v[200:203]\n\t" \
    "s_waitcnt lgkmcnt(2)\n\t" \
    "v_mfma_f32_16x16x32_bf16 v[216:219], v[24:27], v[224:227], v[216:219]\n\t" \
    "ds_read_b128 v[236:239], v253 offset:21504\n\t" \
    "s_waitcnt lgkmcnt(2)\n\t" \
    "v_mfma_f32_16x16x32_bf16 v[216:219], v[28:31], v[228:231], v[216:219]\n\t" \
    "ds_read_b128 v[240:243], v253 offset:22528\n\t" \
    "v_mfma_f32_16x16x32_bf16 v[220:223], v[32:35], v[224:227], v[220:223]\n\t" \
    "v_cvt_pk_bf16_f32 v204, v204, v205\n\t" \
    "v_mfma_f32_16x16x32_bf16 v[220:223], v[36:39], v[228:231], v[220:223]\n\t" \
    "v_cvt_pk_bf16_f32 v205, v206, v207\n\t" \
    "v_mfma_f32_16x16x32_bf16 v[212:215], v[16:19], v[224:227], v[212:215]\n\t" \
    "ds_write_b64 v254, v[204:205] offset:10240\n\t" \
    "v_mfma_f32_16x16x32_bf16 v[212:215], v[20:23], v[228:231], v[212:215]\n\t" \
    "v_cvt_pk_bf16_f32 v208, v208, v209\n\t" \
    "s_waitcnt lgkmcnt(3)\n\t" \
    "v_mfma_f32_16x16x32_bf16 v[216:219], v[64:67], v[232:235], v[216:219]\n\t" \
    "ds_read_b128 v[224:227], v253 offset:23552\n\t" \
    "v_cvt_pk_bf16_f32 v209, v210, v211\n\t" \
    "s_waitcnt lgkmcnt(3)\n\t" \
    "v_mfma_f32_16x16x32_bf16 v[216:219], v[68:71], v[236:239], v[216:219]\n\t" \
    "ds_read_b128 v[228:231], v253 offset:28672\n\t" \
    "ds_write_b64 v254, v[208:209] offset:11264\n\t" \
    "v_mfma_f32_16x16x32_bf16 v[220:223], v[72:75], v[232:235], v[220:223]\n\t" \
    "v_cvt_pk_bf16_f32 v200, v200, v201\n\t" \
    "v_mfma_f32_16x16x32_bf16 v[220:223], v[76:79], v[236:239], v[220:223]\n\t" \
    "v_cvt_pk_bf16_f32 v201, v202, v203\n\t" \
    "v_mfma_f32_16x16x32_bf16 v[212:215], v[56:59], v[232:235], v[212:215]\n\t" \
    "ds_write_b64 v254, v[200:201] offset:9216\n\t" \
    "v_mfma_f32_16x16x32_bf16 v[212:215], v[60:63], v[236:239], v[212:215]\n\t" \
    "s_waitcnt lgkmcnt(5)\n\t" \
    "v_mfma_f32_16x16x32_bf16 v[216:219], v[104:107], v[240:243], v[216:219]\n\t" \
    "ds_read_b128 v[232:235], v253 offset:29696\n\t" \
    "s_waitcnt lgkmcnt(4)\n\t" \
    "v_mfma_f32_16x16x32_bf16 v[216:219], v[108:111], v[224:227], v[216:219]\n\t" \
    "ds_read_b128 v[236:239], v253 offset:38912\n\t" \
    "v_mfma_f32_16x16x32_bf16 v[220:223], v[112:115], v[240:243], v[220:223]\n\t" \
    "v_mfma_f32_16x16x32_bf16 v[220:223], v[116:119], v[224:227], v[220:223]\n\t" \
    "v_mfma_f32_16x16x32_bf16 v[212:215], v[96:99], v[240:243], v[212:215]\n\t" \
    "v_mfma_f32_16x16x32_bf16 v[212:215], v[100:103], v[224:227], v[212:215]\n\t" \
    "ds_read_b128 v[240:243], v253 offset:39936\n\t" \
    "s_waitcnt lgkmcnt(3)\n\t" \
    "s_barrier \n\t" \
    "v_mfma_f32_16x16x32_bf16 v[216:219], v[144:147], v[228:231], v[216:219]\n\t" \
    "s_waitcnt lgkmcnt(2)\n\t" \
    "v_mfma_f32_16x16x32_bf16 v[216:219], v[148:151], v[232:235], v[216:219]\n\t" \
    "s_waitcnt lgkmcnt(1)\n\t" \
    "v_mfma_f32_16x16x32_bf16 v[216:219], v[184:187], v[236:239], v[216:219]\n\t" \
    "s_waitcnt lgkmcnt(0)\n\t" \
    "v_mfma_f32_16x16x32_bf16 v[216:219], v[188:191], v[240:243], v[216:219]\n\t" \
    "v_mfma_f32_16x16x32_bf16 v[220:223], v[152:155], v[228:231], v[220:223]\n\t" \
    "v_mfma_f32_16x16x32_bf16 v[220:223], v[156:159], v[232:235], v[220:223]\n\t" \
    "v_mfma_f32_16x16x32_bf16 v[220:223], v[192:195], v[236:239], v[220:223]\n\t" \
    "v_cvt_pk_bf16_f32 v216, v216, v217\n\t" \
    "v_cvt_pk_bf16_f32 v217, v218, v219\n\t" \
    "v_mfma_f32_16x16x32_bf16 v[220:223], v[196:199], v[240:243], v[220:223]\n\t" \
    "ds_write_b64 v254, v[216:217] offset:16384\n\t" \
    "v_mfma_f32_16x16x32_bf16 v[212:215], v[136:139], v[228:231], v[212:215]\n\t" \
    "v_mfma_f32_16x16x32_bf16 v[212:215], v[140:143], v[232:235], v[212:215]\n\t" \
    "v_mfma_f32_16x16x32_bf16 v[212:215], v[176:179], v[236:239], v[212:215]\n\t" \
    "v_cvt_pk_bf16_f32 v220, v220, v221\n\t" \
    "v_cvt_pk_bf16_f32 v221, v222, v223\n\t" \
    "v_mfma_f32_16x16x32_bf16 v[212:215], v[180:183], v[240:243], v[212:215]\n\t" \
    "ds_write_b64 v254, v[220:221] offset:17408\n\t" \
    "s_nop 6\n\t" \
    "v_cvt_pk_bf16_f32 v212, v212, v213\n\t" \
    "v_cvt_pk_bf16_f32 v213, v214, v215\n\t" \
    "ds_write_b64 v254, v[212:213] offset:15360\n\t" \
    LST6 \
    LJIT \
    "s_waitcnt vmcnt(0) lgkmcnt(0)\n\t" \
    "s_barrier \n\t" \
    "s_setprio 2\n\t" \
    "s_add_u32 s44, s40, 1\n\t" \
    "s_min_u32 s44, s44, 63\n\t" \
    ".Lv18_%=:\n\t" \
    "v_cmp_ge_u32 vcc, s44, v252\n\t" \
    "s_cbranch_vccz .Lv19_%=\n\t" \
    "s_cmp_eq_u32 s41, 0\n\t" \
    "s_cbranch_scc1 .Lv19_%=\n\t" \
    "s_sub_u32 s41, s41, 1\n\t" \
    "s_sleep 2\n\t" \
    "v_and_b32 v252, 0xf0, v253\n\t" \
    "v_lshlrev_b32 v252, 3, v252\n\t" \
    "buffer_load_dword v252, v252, s[60:63], %[fl] offen sc1\n\t" \
    "s_waitcnt vmcnt(0)\n\t" \
    "s_branch .Lv18_%=\n\t" \
    ".Lv19_%=:\n\t" \
    "s_mov_b32 m0, 0x1000\n\t" \
    "s_add_u32 s49, s42, 0x0\n\t" \
    "buffer_load_dwordx4 v253, s[60:63], s49 offen offset:0 sc1 lds\n\t" \
    "s_sleep 1\n\t" \
    "buffer_load_dwordx4 v253, s[60:63], s49 offen offset:1024 sc1 lds\n\t" \
    "s_sleep 1\n\t" \
    "buffer_load_dwordx4 v253, s[60:63], s49 offen offset:2048 sc1 lds\n\t" \
    "s_sleep 1\n\t" \
    "buffer_load_dwordx4 v253, s[60:63], s49 offen offset:3072 sc1 lds\n\t" \
    "s_sleep 1\n\t" \
    "s_mov_b32 m0, 0x4400\n\t" \
    "s_add_u32 s49, s42, 0xc400\n\t" \
    "buffer_load_dwordx4 v253, s[60:63], s49 offen offset:0 sc1 lds\n\t" \
    "s_sleep 1\n\t" \
    "s_mov_b32 m0, 0x6000\n\t" \
    "s_add_u32 s49, s42, 0x1000\n\t" \
    "buffer_load_dwordx4 v253, s[60:63], s49 offen offset:0 sc1 lds\n\t" \
    "s_sleep 1\n\t" \
    "buffer_load_dwordx4 v253, s[60:63], s49 offen offset:1024 sc1 lds\n\t" \
    "s_sleep 1\n\t" \
    "buffer_load_dwordx4 v253, s[60:63], s49 offen offset:2048 sc1 lds\n\t" \
    "s_sleep 1\n\t" \
    "buffer_load_dwordx4 v253, s[60:63], s49 offen offset:3072 sc1 lds\n\t" \
    "s_sleep 1\n\t" \
    "s_mov_b32 m0, 0x9400\n\t" \
    "s_add_u32 s49, s42, 0xd400\n\t" \
    "buffer_load_dwordx4 v253, s[60:63], s49 offen offset:0 sc1 lds\n\t" \
    "s_sleep 1\n\t" \
    "ds_read_b128 v[200:203], v254 offset:40960\n\t" \
    "ds_read_b128 v[204:207], v254 offset:41984\n\t" \
    "ds_read_b128 v[216:219], v254 offset:43008\n\t" \
    "ds_read_b128 v[220:223], v254 offset:44032\n\t" \
    "v_mov_b64 v[208:209], 0\n\t" \
    "v_mov_b64 v[210:211], 0\n\t" \
    "v_mov_b64 v[244:245], 0\n\t" \
    "v_mov_b64 v[246:247], 0\n\t" \
    "v_mov_b64 v[248:249], 0\n\t" \
    "v_mov_b64 v[250:251], 0\n\t" \
    LST1 \
    LJIT \
    "s_sleep 5\n\t" \
    "s_mov_b32 m0, 0xa800\n\t" \
    "s_nop 0\n\t" \
    "buffer_load_dwordx4 v253, s[60:63], s46 offen sc1 lds\n\t" \
    "s_mov_b32 m0, 0xb800\n\t" \
    "s_nop 0\n\t" \
    "buffer_load_dwordx4 v253, s[60:63], s47 offen sc1 lds\n\t" \
    "s_mov_b32 m0, 0xac00\n\t" \
    "s_nop 0\n\t" \
    "buffer_load_dwordx4 v253, s[60:63], s48 offen sc1 lds\n\t" \
    "s_waitcnt lgkmcnt(0)\n\t" \
    LFLUSH212 \
    "v_bfe_i32 v212, v253, 5, 1\n\t" \
    "v_and_b32 v212, 0xaaaa, v212\n\t" \
    "v_or_b32 v212, 0x4444, v212\n\t" \
    "v_and_b32 v213, 0xffff, v255\n\t" \
    "v_lshrrev_b32 v214, 16, v255\n\t" \
    ".Lv110_%=:\n\t" \
    "s_waitcnt vmcnt(0)\n\t" \
    "ds_read_b128 v[224:227], v253 offset:43008\n\t" \
    "ds_read_b128 v[228:231], v253 offset:47104\n\t" \
    "ds_read_b128 v[232:235], v253 offset:44032\n\t" \
    "s_waitcnt lgkmcnt(2)\n\t" \
    "v_max_u32 v215, v224, v225\n\t" \
    "v_max3_u32 v215, v226, v227, v215\n\t" \
    "s_waitcnt lgkmcnt(1)\n\t" \
    "v_max3_u32 v215, v228, v229, v215\n\t" \
    "v_max3_u32 v215, v230, v231, v215\n\t" \
    "s_waitcnt lgkmcnt(0)\n\t" \
    "v_max3_u32 v215, v232, v233, v215\n\t" \
    "v_max3_u32 v215, v234, v235, v215\n\t" \
    "v_cmp_eq_u32 vcc, -1, v215\n\t" \
    "s_cbranch_vccz .Lv111_%=\n\t" \
    "v_max_u32 v215, v224, v225\n\t" \
    "v_max3_u32 v215, v226, v227, v215\n\t" \
    "v_cmp_eq_u32 vcc, -1, v215\n\t" \
    "s_cbranch_vccz .Lv112_%=\n\t" \
    "s_mov_b32 m0, 0xa800\n\t" \
    "s_nop 0\n\t" \
    "buffer_load_dwordx4 v253, s[60:63], s46 offen sc1 lds\n\t" \
    ".Lv112_%=:\n\t" \
    "v_max_u32 v215, v228, v229\n\t" \
    "v_max3_u32 v215, v230, v231, v215\n\t" \
    "v_cmp_eq_u32 vcc, -1, v215\n\t" \
    "s_cbranch_vccz .Lv113_%=\n\t" \
    "s_mov_b32 m0, 0xb800\n\t" \
    "s_nop 0\n\t" \
    "buffer_load_dwordx4 v253, s[60:63], s47 offen sc1 lds\n\t" \
    ".Lv113_%=:\n\t" \
    "v_max_u32 v215, v232, v233\n\t" \
    "v_max3_u32 v215, v234, v235, v215\n\t" \
    "v_cmp_eq_u32 vcc, -1, v215\n\t" \
    "s_cbranch_vccz .Lv114_%=\n\t" \
    "s_mov_b32 m0, 0xac00\n\t" \
    "s_nop 0\n\t" \
    "buffer_load_dwordx4 v253, s[60:63], s48 offen sc1 lds\n\t" \
    ".Lv114_%=:\n\t" \
    "s_cmp_eq_u32 s41, 0\n\t" \
    "s_cbranch_scc1 .Lv111_%=\n\t" \
    "s_sub_u32 s41, s41, 1\n\t" \
    "s_branch .Lv110_%=\n\t" \
    ".Lv111_%=:\n\t" \
    LST2 \
    LJIT \
    "s_waitcnt lgkmcnt(0)\n\t" \
    "s_barrier \n\t" \
    LST3 \
    LJIT \
    "ds_read_b128 v[236:239], v253 offset:40960\n\t" \
    "ds_read_b128 v[240:243], v253 offset:46080\n\t" \
    "v_smfmac_f32_16x16x64_bf16 v[244:247], v[224:227], v[0:7], v212\n\t" \
    "v_smfmac_f32_16x16x64_bf16 v[248:251], v[224:227], v[8:15], v212\n\t" \
    "v_smfmac_f32_16x16x64_bf16 v[208:211], v[224:227], v[200:207], v212\n\t" \
    "ds_read_b128 v[200:203], v254 offset:45056\n\t" \
    "ds_read_b128 v[204:207], v254 offset:46080\n\t" \
    "ds_read_b128 v[224:227], v253 offset:41984\n\t" \
    "v_smfmac_f32_16x16x64_bf16 v[208:211], v[228:231], v[16:23], v212\n\t" \
    "v_smfmac_f32_16x16x64_bf16 v[244:247], v[228:231], v[24:31], v212\n\t" \
    "v_smfmac_f32_16x16x64_bf16 v[248:251], v[228:231], v[32:39], v212\n\t" \
    "ds_read_b128 v[228:231], v253 offset:48128\n\t" \
    "v_smfmac_f32_16x16x64_bf16 v[208:211], v[232:235], v[216:223], v212\n\t" \
    "ds_read_b128 v[216:219], v254 offset:47104\n\t" \
    "ds_read_b128 v[220:223], v254 offset:48128\n\t" \
    "v_smfmac_f32_16x16x64_bf16 v[244:247], v[232:235], v[40:47], v212\n\t" \
    "v_smfmac_f32_16x16x64_bf16 v[248:251], v[232:235], v[48:55], v212\n\t" \
    "ds_read_b128 v[232:235], v253 offset:45056\n\t" \
    "s_waitcnt lgkmcnt(8)\n\t" \
    "v_smfmac_f32_16x16x64_bf16 v[208:211], v[236:239], v[56:63], v212\n\t" \
    "v_smfmac_f32_16x16x64_bf16 v[244:247], v[236:239], v[64:71], v212\n\t" \
    "v_smfmac_f32_16x16x64_bf16 v[248:251], v[236:239], v[72:79], v212\n\t" \
    "ds_read_b128 v[236:239], v253 offset:49152\n\t" \
    "s_waitcnt lgkmcnt(6)\n\t" \
    "v_smfmac_f32_16x16x64_bf16 v[208:211], v[240:243], v[200:207], v212\n\t" \
    "ds_read_b128 v[200:203], v254 offset:49152\n\t" \
    "ds_read_b128 v[204:207], v254 offset:50176\n\t" \
    "v_smfmac_f32_16x16x64_bf16 v[244:247], v[240:243], v[80:87], v212\n\t" \
    "v_smfmac_f32_16x16x64_bf16 v[248:251], v[240:243], v[88:95], v212\n\t" \
    "ds_read_b128 v[240:243], v253 offset:50176\n\t" \
    "s_waitcnt lgkmcnt(8)\n\t" \
    "v_smfmac_f32_16x16x64_bf16 v[208:211], v[224:227], v[96:103], v212\n\t" \
    "v_smfmac_f32_16x16x64_bf16 v[244:247], v[224:227], v[104:111], v212\n\t" \
    "v_smfmac_f32_16x16x64_bf16 v[248:251], v[224:227], v[112:119], v212\n\t" \
    "s_waitcnt lgkmcnt(7)\n\t" \
    "v_smfmac_f32_16x16x64_bf16 v[244:247], v[228:231], v[120:127], v212\n\t" \
    "s_waitcnt lgkmcnt(4)\n\t" \
    "v_smfmac_f32_16x16x64_bf16 v[244:247], v[232:235], v[144:151], v212\n\t" \
    "s_waitcnt lgkmcnt(3)\n\t" \
    "v_smfmac_f32_16x16x64_bf16 v[244:247], v[236:239], v[160:167], v212\n\t" \
    "s_waitcnt lgkmcnt(0)\n\t" \
    "v_smfmac_f32_16x16x64_bf16 v[244:247], v[240:243], v[184:191], v212\n\t" \
    "v_smfmac_f32_16x16x64_bf16 v[248:251], v[228:231], v[128:135], v212\n\t" \
    "v_smfmac_f32_16x16x64_bf16 v[248:251], v[232:235], v[152:159], v212\n\t" \
    "v_smfmac_f32_16x16x64_bf16 v[248:251], v[236:239], v[168:175], v212\n\t" \
    "v_add_f32 v244, v244, v246\n\t" \
    "v_add_f32 v245, v245, v247\n\t" \
    "v_cvt_pk_bf16_f32 v244, v244, v245\n\t" \
    "v_smfmac_f32_16x16x64_bf16 v[248:251], v[240:243], v[192:199], v212\n\t" \
    "ds_write_b16 v213, v244 offset:512\n\t" \
    "ds_write_b16_d16_hi v213, v244 offset:528\n\t" \
    "v_smfmac_f32_16x16x64_bf16 v[208:211], v[228:231], v[216:223], v212\n\t" \
    "v_smfmac_f32_16x16x64_bf16 v[208:211], v[232:235], v[136:143], v212\n\t" \
    "v_smfmac_f32_16x16x64_bf16 v[208:211], v[236:239], v[200:207], v212\n\t" \
    "v_add_f32 v248, v248, v250\n\t" \
    "v_add_f32 v249, v249, v251\n\t" \
    "v_cvt_pk_bf16_f32 v248, v248, v249\n\t" \
    "v_smfmac_f32_16x16x64_bf16 v[208:211], v[240:243], v[176:183], v212\n\t" \
    "ds_write_b16 v213, v248 offset:1024\n\t" \
    "ds_write_b16_d16_hi v213, v248 offset:1040\n\t" \
    "s_nop 5\n\t" \
    "v_add_f32 v208, v208, v210\n\t" \
    "v_add_f32 v209, v209, v211\n\t" \
    "v_cvt_pk_bf16_f32 v208, v208, v209\n\t" \
    "ds_write_b16 v213, v208 offset:0\n\t" \
    "ds_write_b16_d16_hi v213, v208 offset:16\n\t" \
    "s_setprio 0\n\t" \
    "s_waitcnt lgkmcnt(0)\n\t" \
    "s_barrier \n\t" \
    LST4 \
    LJIT \
    "ds_read_b128 v[224:227], v253 offset:4096\n\t" \
    "ds_read_b128 v[244:247], v254 offset:40960\n\t" \
    "ds_read_b128 v[228:231], v253 offset:5120\n\t" \
    "ds_read_b128 v[232:235], v253 offset:24576\n\t" \
    "ds_read_b128 v[236:239], v253 offset:25600\n\t" \
    "ds_read_b128 v[248:251], v254 offset:41984\n\t" \
    "s_add_u32 s40, s40, 1\n\t" \
    "s_cmp_lg_u32 s40, 64\n\t" \
    "s_cbranch_scc1 .Lv17_%=\n\t" \
    "s_branch .Lend%=\n\t" \
    ".Lv0_%=:\n\t" \
    "s_mov_b32 s42, %[xs]\n\t" \
    "v_add_u32 v244, 0xc000, v243\n\t" \
    "s_waitcnt vmcnt(12) lgkmcnt(0)\n\t" \
    "s_barrier \n\t" \
    "s_add_u32 m0, %[wm], 0x9000\n\t" \
    "s_mov_b32 s44, 0x1e0000\n\t" \
    "buffer_load_dwordx4 v240, %[wr], s44 offen offset:0 nt lds\n\t" \
    "buffer_load_dwordx4 v241, %[wr], s44 offen offset:1024 nt lds\n\t" \
    "buffer_load_dwordx4 v242, %[wr], s44 offen offset:2048 nt lds\n\t" \
    "ds_read_b64_tr_b16 v[200:201], v244 offset:0\n\t" \
    "ds_read_b64_tr_b16 v[202:203], v244 offset:1536\n\t" \
    "ds_read_b64_tr_b16 v[0:1], v244 offset:32\n\t" \
    "ds_read_b64_tr_b16 v[2:3], v244 offset:1568\n\t" \
    "ds_read_b64_tr_b16 v[8:9], v244 offset:64\n\t" \
    "ds_read_b64_tr_b16 v[10:11], v244 offset:1600\n\t" \
    "v_add_u32 v244, 0xf000, v243\n\t" \
    "s_waitcnt vmcnt(12) lgkmcnt(0)\n\t" \
    "s_barrier \n\t" \
    "s_add_u32 m0, %[wm], 0xc000\n\t" \
    "s_mov_b32 s44, 0x240000\n\t" \
    "buffer_load_dwordx4 v240, %[wr], s44 offen offset:0 nt lds\n\t" \
    "buffer_load_dwordx4 v241, %[wr], s44 offen offset:1024 nt lds\n\t" \
    "buffer_load_dwordx4 v242, %[wr], s44 offen offset:2048 nt lds\n\t" \
    "ds_read_b64_tr_b16 v[204:205], v244 offset:0\n\t" \
    "ds_read_b64_tr_b16 v[206:207], v244 offset:1536\n\t" \
    "ds_read_b64_tr_b16 v[4:5], v244 offset:32\n\t" \
    "ds_read_b64_tr_b16 v[6:7], v244 offset:1568\n\t" \
    "ds_read_b64_tr_b16 v[12:13], v244 offset:64\n\t" \
    "ds_read_b64_tr_b16 v[14:15], v244 offset:1600\n\t" \
    "v_add_u32 v244, 0x0, v243\n\t" \
    "s_waitcnt vmcnt(12) lgkmcnt(0)\n\t" \
    "s_barrier \n\t" \
    "s_add_u32 m0, %[wm], 0xf000\n\t" \
    "s_mov_b32 s44, 0x2a0000\n\t" \
    "buffer_load_dwordx4 v240, %[wr], s44 offen offset:0 nt lds\n\t" \
    "buffer_load_dwordx4 v241, %[wr], s44 offen offset:1024 nt lds\n\t" \
    "buffer_load_dwordx4 v242, %[wr], s44 offen offset:2048 nt lds\n\t" \
    "ds_read_b64_tr_b16 v[208:209], v244 offset:0\n\t" \
    "ds_read_b64_tr_b16 v[210:211], v244 offset:1536\n\t" \
    "ds_read_b64_tr_b16 v[40:41], v244 offset:32\n\t" \
    "ds_read_b64_tr_b16 v[42:43], v244 offset:1568\n\t" \
    "ds_read_b64_tr_b16 v[48:49], v244 offset:64\n\t" \
    "ds_read_b64_tr_b16 v[50:51], v244 offset:1600\n\t" \
    "v_add_u32 v244, 0x3000, v243\n\t" \
    "s_waitcnt vmcnt(12) lgkmcnt(0)\n\t" \
    "s_barrier \n\t" \
    "s_add_u32 m0, %[wm], 0x0\n\t" \
    "s_mov_b32 s44, 0x300000\n\t" \
    "buffer_load_dwordx4 v240, %[wr], s44 offen offset:0 nt lds\n\t" \
    "buffer_load_dwordx4 v241, %[wr], s44 offen offset:1024 nt lds\n\t" \
    "buffer_load_dwordx4 v242, %[wr], s44 offen offset:2048 nt lds\n\t" \
    "ds_read_b64_tr_b16 v[212:213], v244 offset:0\n\t" \
    "ds_read_b64_tr_b16 v[214:215], v244 offset:1536\n\t" \
    "ds_read_b64_tr_b16 v[44:45], v244 offset:32\n\t" \
    "ds_read_b64_tr_b16 v[46:47], v244 offset:1568\n\t" \
    "ds_read_b64_tr_b16 v[52:53], v244 offset:64\n\t" \
    "ds_read_b64_tr_b16 v[54:55], v244 offset:1600\n\t" \
    "v_add_u32 v244, 0x6000, v243\n\t" \
    "s_waitcnt vmcnt(12) lgkmcnt(0)\n\t" \
    "s_barrier \n\t" \
    "s_add_u32 m0, %[wm], 0x3000\n\t" \
    "s_mov_b32 s44, 0x360000\n\t" \
    "buffer_load_dwordx4 v240, %[wr], s44 offen offset:0 nt lds\n\t" \
    "buffer_load_dwordx4 v241, %[wr], s44 offen offset:1024 nt lds\n\t" \
    "buffer_load_dwordx4 v242, %[wr], s44 offen offset:2048 nt lds\n\t" \
    "ds_read_b64_tr_b16 v[216:217], v244 offset:0\n\t" \
    "ds_read_b64_tr_b16 v[218:219], v244 offset:1536\n\t" \
    "ds_read_b64_tr_b16 v[80:81], v244 offset:32\n\t" \
    "ds_read_b64_tr_b16 v[82:83], v244 offset:1568\n\t" \
    "ds_read_b64_tr_b16 v[88:89], v244 offset:64\n\t" \
    "ds_read_b64_tr_b16 v[90:91], v244 offset:1600\n\t" \
    "v_add_u32 v244, 0x9000, v243\n\t" \
    "s_waitcnt vmcnt(12) lgkmcnt(0)\n\t" \
    "s_barrier \n\t" \
    "s_add_u32 m0, %[wm], 0x6000\n\t" \
    "s_mov_b32 s44, 0x3c0000\n\t" \
    "buffer_load_dwordx4 v240, %[wr], s44 offen offset:0 nt lds\n\t" \
    "buffer_load_dwordx4 v241, %[wr], s44 offen offset:1024 nt lds\n\t" \
    "buffer_load_dwordx4 v242, %[wr], s44 offen offset:2048 nt lds\n\t" \
    "ds_read_b64_tr_b16 v[220:221], v244 offset:0\n\t" \
    "ds_read_b64_tr_b16 v[222:223], v244 offset:1536\n\t" \
    "ds_read_b64_tr_b16 v[84:85], v244 offset:32\n\t" \
    "ds_read_b64_tr_b16 v[86:87], v244 offset:1568\n\t" \
    "ds_read_b64_tr_b16 v[92:93], v244 offset:64\n\t" \
    "ds_read_b64_tr_b16 v[94:95], v244 offset:1600\n\t" \
    "v_add_u32 v244, 0xc000, v243\n\t" \
    "s_waitcnt vmcnt(12) lgkmcnt(0)\n\t" \
    "s_barrier \n\t" \
    "s_add_u32 m0, %[wm], 0x9000\n\t" \
    "s_mov_b32 s44, 0x420000\n\t" \
    "buffer_load_dwordx4 v240, %[wr], s44 offen offset:0 nt lds\n\t" \
    "buffer_load_dwordx4 v241, %[wr], s44 offen offset:1024 nt lds\n\t" \
    "buffer_load_dwordx4 v242, %[wr], s44 offen offset:2048 nt lds\n\t" \
    "ds_read_b64_tr_b16 v[224:225], v244 offset:0\n\t" \
    "ds_read_b64_tr_b16 v[226:227], v244 offset:1536\n\t" \
    "ds_read_b64_tr_b16 v[120:121], v244 offset:32\n\t" \
    "ds_read_b64_tr_b16 v[122:123], v244 offset:1568\n\t" \
    "ds_read_b64_tr_b16 v[128:129], v244 offset:64\n\t" \
    "ds_read_b64_tr_b16 v[130:131], v244 offset:1600\n\t" \
    "v_add_u32 v244, 0xf000, v243\n\t" \
    "s_waitcnt vmcnt(12) lgkmcnt(0)\n\t" \
    "s_barrier \n\t" \
    "s_add_u32 m0, %[wm], 0xc000\n\t" \
    "s_mov_b32 s44, 0x480000\n\t" \
    "buffer_load_dwordx4 v240, %[wr], s44 offen offset:0 nt lds\n\t" \
    "buffer_load_dwordx4 v241, %[wr], s44 offen offset:1024 nt lds\n\t" \
    "buffer_load_dwordx4 v242, %[wr], s44 offen offset:2048 nt lds\n\t" \
    "ds_read_b64_tr_b16 v[228:229], v244 offset:0\n\t" \
    "ds_read_b64_tr_b16 v[230:231], v244 offset:1536\n\t" \
    "ds_read_b64_tr_b16 v[124:125], v244 offset:32\n\t" \
    "ds_read_b64_tr_b16 v[126:127], v244 offset:1568\n\t" \
    "ds_read_b64_tr_b16 v[132:133], v244 offset:64\n\t" \
    "ds_read_b64_tr_b16 v[134:135], v244 offset:1600\n\t" \
    "v_add_u32 v244, 0x0, v243\n\t" \
    "s_waitcnt vmcnt(12) lgkmcnt(0)\n\t" \
    "s_barrier \n\t" \
    "v_mov_b32 v246, 0\n\t" \
    "buffer_load_dword v246, v246, s[60:63], %[mo] offen sc1\n\t" \
    "s_add_u32 m0, %[wm], 0xf000\n\t" \
    "s_mov_b32 s44, 0x4e0000\n\t" \
    "buffer_load_dwordx4 v240, %[wr], s44 offen offset:0 nt lds\n\t" \
    "buffer_load_dwordx4 v241, %[wr], s44 offen offset:1024 nt lds\n\t" \
    "buffer_load_dwordx4 v242, %[wr], s44 offen offset:2048 nt lds\n\t" \
    "ds_read_b64_tr_b16 v[232:233], v244 offset:0\n\t" \
    "ds_read_b64_tr_b16 v[234:235], v244 offset:1536\n\t" \
    "ds_read_b64_tr_b16 v[160:161], v244 offset:32\n\t" \
    "ds_read_b64_tr_b16 v[162:163], v244 offset:1568\n\t" \
    "ds_read_b64_tr_b16 v[168:169], v244 offset:64\n\t" \
    "ds_read_b64_tr_b16 v[170:171], v244 offset:1600\n\t" \
    "v_add_u32 v244, 0x3000, v243\n\t" \
    "s_waitcnt vmcnt(12) lgkmcnt(0)\n\t" \
    "s_barrier \n\t" \
    "s_add_u32 m0, %[wm], 0x0\n\t" \
    "s_mov_b32 s44, 0x540000\n\t" \
    "buffer_load_dwordx4 v240, %[wr], s44 offen offset:0 nt lds\n\t" \
    "buffer_load_dwordx4 v241, %[wr], s44 offen offset:1024 nt lds\n\t" \
    "buffer_load_dwordx4 v242, %[wr], s44 offen offset:2048 nt lds\n\t" \
    "ds_read_b64_tr_b16 v[236:237], v244 offset:0\n\t" \
    "ds_read_b64_tr_b16 v[238:239], v244 offset:1536\n\t" \
    "ds_read_b64_tr_b16 v[164:165], v244 offset:32\n\t" \
    "ds_read_b64_tr_b16 v[166:167], v244 offset:1568\n\t" \
    "ds_read_b64_tr_b16 v[172:173], v244 offset:64\n\t" \
    "ds_read_b64_tr_b16 v[174:175], v244 offset:1600\n\t" \
    "v_add_u32 v244, 0x6000, v243\n\t" \
    "s_waitcnt vmcnt(12) lgkmcnt(0)\n\t" \
    "s_barrier \n\t" \
    "s_add_u32 m0, %[wm], 0x3000\n\t" \
    "s_mov_b32 s44, 0x5a0000\n\t" \
    "buffer_load_dwordx4 v240, %[wr], s44 offen offset:0 nt lds\n\t" \
    "buffer_load_dwordx4 v241, %[wr], s44 offen offset:1024 nt lds\n\t" \
    "buffer_load_dwordx4 v242, %[wr], s44 offen offset:2048 nt lds\n\t" \
    "ds_read_b64_tr_b16 v[16:17], v244 offset:0\n\t" \
    "ds_read_b64_tr_b16 v[18:19], v244 offset:1536\n\t" \
    "ds_read_b64_tr_b16 v[24:25], v244 offset:32\n\t" \
    "ds_read_b64_tr_b16 v[26:27], v244 offset:1568\n\t" \
    "ds_read_b64_tr_b16 v[32:33], v244 offset:64\n\t" \
    "ds_read_b64_tr_b16 v[34:35], v244 offset:1600\n\t" \
    "v_add_u32 v244, 0x9000, v243\n\t" \
    "s_waitcnt vmcnt(12) lgkmcnt(0)\n\t" \
    "s_barrier \n\t" \
    "s_add_u32 m0, %[wm], 0x6000\n\t" \
    "s_mov_b32 s44, 0x600000\n\t" \
    "buffer_load_dwordx4 v240, %[wr], s44 offen offset:0 nt lds\n\t" \
    "buffer_load_dwordx4 v241, %[wr], s44 offen offset:1024 nt lds\n\t" \
    "buffer_load_dwordx4 v242, %[wr], s44 offen offset:2048 nt lds\n\t" \
    "ds_read_b64_tr_b16 v[20:21], v244 offset:0\n\t" \
    "ds_read_b64_tr_b16 v[22:23], v244 offset:1536\n\t" \
    "ds_read_b64_tr_b16 v[28:29], v244 offset:32\n\t" \
    "ds_read_b64_tr_b16 v[30:31], v244 offset:1568\n\t" \
    "ds_read_b64_tr_b16 v[36:37], v244 offset:64\n\t" \
    "ds_read_b64_tr_b16 v[38:39], v244 offset:1600\n\t" \
    "v_add_u32 v244, 0xc000, v243\n\t" \
    "s_waitcnt vmcnt(12) lgkmcnt(0)\n\t" \
    "s_barrier \n\t" \
    "s_add_u32 m0, %[wm], 0x9000\n\t" \
    "s_mov_b32 s44, 0x660000\n\t" \
    "buffer_load_dwordx4 v240, %[wr], s44 offen offset:0 nt lds\n\t" \
    "buffer_load_dwordx4 v241, %[wr], s44 offen offset:1024 nt lds\n\t" \
    "buffer_load_dwordx4 v242, %[wr], s44 offen offset:2048 nt lds\n\t" \
    "ds_read_b64_tr_b16 v[56:57], v244 offset:0\n\t" \
    "ds_read_b64_tr_b16 v[58:59], v244 offset:1536\n\t" \
    "ds_read_b64_tr_b16 v[64:65], v244 offset:32\n\t" \
    "ds_read_b64_tr_b16 v[66:67], v244 offset:1568\n\t" \
    "ds_read_b64_tr_b16 v[72:73], v244 offset:64\n\t" \
    "ds_read_b64_tr_b16 v[74:75], v244 offset:1600\n\t" \
    "v_add_u32 v244, 0xf000, v243\n\t" \
    "s_waitcnt vmcnt(12) lgkmcnt(0)\n\t" \
    "s_barrier \n\t" \
    ".Lv01_%=:\n\t" \
    "v_readfirstlane_b32 s43, v246\n\t" \
    "s_nop 0\n\t" \
    "s_cmp_lg_u32 s43, -1\n\t" \
    "s_cbranch_scc1 .Lv02_%=\n\t" \
    "s_cmp_eq_u32 s41, 0\n\t" \
    "s_cbranch_scc1 .Lv02_%=\n\t" \
    "s_sub_u32 s41, s41, 1\n\t" \
    "s_sleep 4\n\t" \
    "v_mov_b32 v246, 0\n\t" \
    "buffer_load_dword v246, v246, s[60:63], %[mo] offen sc1\n\t" \
    "s_waitcnt vmcnt(0)\n\t" \
    "s_branch .Lv01_%=\n\t" \
    ".Lv02_%=:\n\t" \
    "v_mov_b32 v246, 0x8000\n\t" \
    "s_mov_b64 exec, 1\n\t" \
    "buffer_store_dword v246, v246, s[60:63], %[mo] offen sc1\n\t" \
    "s_mov_b64 exec, -1\n\t" \
    "s_and_b32 s43, s43, 0x2000\n\t" \
    "s_xor_b32 s43, s43, %[lx]\n\t" \
    "s_add_u32 s60, s60, s43\n\t" \
    "s_addc_u32 s61, s61, 0\n\t" \
    "v_and_b32 v252, 0xf0, v253\n\t" \
    "v_lshlrev_b32 v252, 3, v252\n\t" \
    "buffer_load_dword v252, v252, s[60:63], %[fl] offen sc1\n\t" \
    "s_add_u32 m0, %[wm], 0xc000\n\t" \
    "s_mov_b32 s44, 0x6c0000\n\t" \
    "buffer_load_dwordx4 v240, %[wr], s44 offen offset:0 nt lds\n\t" \
    "buffer_load_dwordx4 v241, %[wr], s44 offen offset:1024 nt lds\n\t" \
    "buffer_load_dwordx4 v242, %[wr], s44 offen offset:2048 nt lds\n\t" \
    "ds_read_b64_tr_b16 v[60:61], v244 offset:0\n\t" \
    "ds_read_b64_tr_b16 v[62:63], v244 offset:1536\n\t" \
    "ds_read_b64_tr_b16 v[68:69], v244 offset:32\n\t" \
    "ds_read_b64_tr_b16 v[70:71], v244 offset:1568\n\t" \
    "ds_read_b64_tr_b16 v[76:77], v244 offset:64\n\t" \
    "ds_read_b64_tr_b16 v[78:79], v244 offset:1600\n\t" \
    "v_add_u32 v244, 0x0, v243\n\t" \
    "s_waitcnt vmcnt(12) lgkmcnt(0)\n\t" \
    "s_barrier \n\t" \
    "s_add_u32 m0, %[wm], 0xf000\n\t" \
    "s_mov_b32 s44, 0x720000\n\t" \
    "buffer_load_dwordx4 v240, %[wr], s44 offen offset:0 nt lds\n\t" \
    "buffer_load_dwordx4 v241, %[wr], s44 offen offset:1024 nt lds\n\t" \
    "buffer_load_dwordx4 v242, %[wr], s44 offen offset:2048 nt lds\n\t" \
    "ds_read_b64_tr_b16 v[96:97], v244 offset:0\n\t" \
    "ds_read_b64_tr_b16 v[98:99], v244 offset:1536\n\t" \
    "ds_read_b64_tr_b16 v[104:105], v244 offset:32\n\t" \
    "ds_read_b64_tr_b16 v[106:107], v244 offset:1568\n\t" \
    "ds_read_b64_tr_b16 v[112:113], v244 offset:64\n\t" \
    "ds_read_b64_tr_b16 v[114:115], v244 offset:1600\n\t" \
    "v_add_u32 v244, 0x3000, v243\n\t" \
    "s_waitcnt vmcnt(12) lgkmcnt(0)\n\t" \
    "s_barrier \n\t" \
    "ds_read_b64_tr_b16 v[100:101], v244 offset:0\n\t" \
    "ds_read_b64_tr_b16 v[102:103], v244 offset:1536\n\t" \
    "ds_read_b64_tr_b16 v[108:109], v244 offset:32\n\t" \
    "ds_read_b64_tr_b16 v[110:111], v244 offset:1568\n\t" \
    "ds_read_b64_tr_b16 v[116:117], v244 offset:64\n\t" \
    "ds_read_b64_tr_b16 v[118:119], v244 offset:1600\n\t" \
    "v_add_u32 v244, 0x6000, v243\n\t" \
    "s_waitcnt vmcnt(9) lgkmcnt(0)\n\t" \
    "s_barrier \n\t" \
    "ds_read_b64_tr_b16 v[136:137], v244 offset:0\n\t" \
    "ds_read_b64_tr_b16 v[138:139], v244 offset:1536\n\t" \
    "ds_read_b64_tr_b16 v[144:145], v244 offset:32\n\t" \
    "ds_read_b64_tr_b16 v[146:147], v244 offset:1568\n\t" \
    "ds_read_b64_tr_b16 v[152:153], v244 offset:64\n\t" \
    "ds_read_b64_tr_b16 v[154:155], v244 offset:1600\n\t" \
    "v_add_u32 v244, 0x9000, v243\n\t" \
    "s_waitcnt vmcnt(6) lgkmcnt(0)\n\t" \
    "s_barrier \n\t" \
    "ds_read_b64_tr_b16 v[140:141], v244 offset:0\n\t" \
    "ds_read_b64_tr_b16 v[142:143], v244 offset:1536\n\t" \
    "ds_read_b64_tr_b16 v[148:149], v244 offset:32\n\t" \
    "ds_read_b64_tr_b16 v[150:151], v244 offset:1568\n\t" \
    "ds_read_b64_tr_b16 v[156:157], v244 offset:64\n\t" \
    "ds_read_b64_tr_b16 v[158:159], v244 offset:1600\n\t" \
    "v_add_u32 v244, 0xc000, v243\n\t" \
    "s_waitcnt vmcnt(3) lgkmcnt(0)\n\t" \
    "s_barrier \n\t" \
    "ds_read_b64_tr_b16 v[176:177], v244 offset:0\n\t" \
    "ds_read_b64_tr_b16 v[178:179], v244 offset:1536\n\t" \
    "ds_read_b64_tr_b16 v[184:185], v244 offset:32\n\t" \
    "ds_read_b64_tr_b16 v[186:187], v244 offset:1568\n\t" \
    "ds_read_b64_tr_b16 v[192:193], v244 offset:64\n\t" \
    "ds_read_b64_tr_b16 v[194:195], v244 offset:1600\n\t" \
    "s_mov_b32 s43, 0\n\t" \
    "v_cmp_eq_u32 vcc, 0, v252\n\t" \
    "s_cbranch_vccnz .Lv03_%=\n\t" \
    "s_mov_b32 m0, 0x0\n\t" \
    "s_add_u32 s49, s42, 0x0\n\t" \
    "buffer_load_dwordx4 v253, s[60:63], s49 offen offset:0 sc1 lds\n\t" \
    "buffer_load_dwordx4 v253, s[60:63], s49 offen offset:1024 sc1 lds\n\t" \
    "buffer_load_dwordx4 v253, s[60:63], s49 offen offset:2048 sc1 lds\n\t" \
    "buffer_load_dwordx4 v253, s[60:63], s49 offen offset:3072 sc1 lds\n\t" \
    "s_mov_b32 m0, 0x4000\n\t" \
    "s_add_u32 s49, s42, 0x10000\n\t" \
    "buffer_load_dwordx4 v253, s[60:63], s49 offen offset:0 sc1 lds\n\t" \
    "s_mov_b32 m0, 0x5000\n\t" \
    "s_add_u32 s49, s42, 0x1000\n\t" \
    "buffer_load_dwordx4 v253, s[60:63], s49 offen offset:0 sc1 lds\n\t" \
    "buffer_load_dwordx4 v253, s[60:63], s49 offen offset:1024 sc1 lds\n\t" \
    "buffer_load_dwordx4 v253, s[60:63], s49 offen offset:2048 sc1 lds\n\t" \
    "buffer_load_dwordx4 v253, s[60:63], s49 offen offset:3072 sc1 lds\n\t" \
    "s_mov_b32 m0, 0x9000\n\t" \
    "s_add_u32 s49, s42, 0x11000\n\t" \
    "buffer_load_dwordx4 v253, s[60:63], s49 offen offset:0 sc1 lds\n\t" \
    "s_mov_b32 s43, 1\n\t" \
    ".Lv03_%=:\n\t" \
    "v_add_u32 v244, 0xf000, v243\n\t" \
    "s_waitcnt vmcnt(0) lgkmcnt(0)\n\t" \
    "s_barrier \n\t" \
    "ds_read_b64_tr_b16 v[180:181], v244 offset:0\n\t" \
    "ds_read_b64_tr_b16 v[182:183], v244 offset:1536\n\t" \
    "ds_read_b64_tr_b16 v[188:189], v244 offset:32\n\t" \
    "ds_read_b64_tr_b16 v[190:191], v244 offset:1568\n\t" \
    "ds_read_b64_tr_b16 v[196:197], v244 offset:64\n\t" \
    "ds_read_b64_tr_b16 v[198:199], v244 offset:1600\n\t" \
    "s_waitcnt lgkmcnt(0)\n\t" \
    "s_barrier \n\t" \
    LST0 \
    LJIT \
    "ds_write_b128 v254, v[200:203] offset:30720\n\t" \
    "ds_write_b128 v254, v[204:207] offset:31744\n\t" \
    "ds_write_b128 v254, v[208:211] offset:32768\n\t" \
    "ds_write_b128 v254, v[212:215] offset:33792\n\t" \
    "ds_write_b128 v254, v[216:219] offset:34816\n\t" \
    "ds_write_b128 v254, v[220:223] offset:35840\n\t" \
    "ds_write_b128 v254, v[224:227] offset:36864\n\t" \
    "ds_write_b128 v254, v[228:231] offset:37888\n\t" \
    "ds_write_b128 v254, v[232:235] offset:38912\n\t" \
    "ds_write_b128 v254, v[236:239] offset:39936\n\t" \
    "s_cmp_lg_u32 s43, 0\n\t" \
    "s_cbranch_scc1 .Lv04_%=\n\t" \
    "s_mov_b32 s44, 0\n\t" \
    ".Lv05_%=:\n\t" \
    "v_cmp_ge_u32 vcc, s44, v252\n\t" \
    "s_cbranch_vccz .Lv06_%=\n\t" \
    "s_cmp_eq_u32 s41, 0\n\t" \
    "s_cbranch_scc1 .Lv06_%=\n\t" \
    "s_sub_u32 s41, s41, 1\n\t" \
    "s_sleep 8\n\t" \
    "v_and_b32 v252, 0xf0, v253\n\t" \
    "v_lshlrev_b32 v252, 3, v252\n\t" \
    "buffer_load_dword v252, v252, s[60:63], %[fl] offen sc1\n\t" \
    "s_waitcnt vmcnt(0)\n\t" \
    "s_branch .Lv05_%=\n\t" \
    ".Lv06_%=:\n\t" \
    "s_mov_b32 m0, 0x0\n\t" \
    "s_add_u32 s49, s42, 0x0\n\t" \
    "buffer_load_dwordx4 v253, s[60:63], s49 offen offset:0 sc1 lds\n\t" \
    "buffer_load_dwordx4 v253, s[60:63], s49 offen offset:1024 sc1 lds\n\t" \
    "buffer_load_dwordx4 v253, s[60:63], s49 offen offset:2048 sc1 lds\n\t" \
    "buffer_load_dwordx4 v253, s[60:63], s49 offen offset:3072 sc1 lds\n\t" \
    "s_mov_b32 m0, 0x4000\n\t" \
    "s_add_u32 s49, s42, 0x10000\n\t" \
    "buffer_load_dwordx4 v253, s[60:63], s49 offen offset:0 sc1 lds\n\t" \
    "s_mov_b32 m0, 0x5000\n\t" \
    "s_add_u32 s49, s42, 0x1000\n\t" \
    "buffer_load_dwordx4 v253, s[60:63], s49 offen offset:0 sc1 lds\n\t" \
    "buffer_load_dwordx4 v253, s[60:63], s49 offen offset:1024 sc1 lds\n\t" \
    "buffer_load_dwordx4 v253, s[60:63], s49 offen offset:2048 sc1 lds\n\t" \
    "buffer_load_dwordx4 v253, s[60:63], s49 offen offset:3072 sc1 lds\n\t" \
    "s_mov_b32 m0, 0x9000\n\t" \
    "s_add_u32 s49, s42, 0x11000\n\t" \
    "buffer_load_dwordx4 v253, s[60:63], s49 offen offset:0 sc1 lds\n\t" \
    ".Lv04_%=:\n\t" \
    "s_waitcnt vmcnt(0) lgkmcnt(0)\n\t" \
    "s_barrier \n\t" \
    "ds_read_b128 v[224:227], v253 offset:0\n\t" \
    "ds_read_b128 v[244:247], v254 offset:30720\n\t" \
    "ds_read_b128 v[228:231], v253 offset:1024\n\t" \
    "ds_read_b128 v[232:235], v253 offset:20480\n\t" \
    "ds_read_b128 v[236:239], v253 offset:21504\n\t" \
    "ds_read_b128 v[248:251], v254 offset:31744\n\t" \
    ".Lv07_%=:\n\t" \
    LST7 \
    LJIT \
    "s_waitcnt lgkmcnt(5)\n\t" \
    "v_mfma_f32_16x16x32_bf16 v[204:207], v[0:3], v[224:227], 0\n\t" \
    "s_waitcnt lgkmcnt(3)\n\t" \
    "v_mfma_f32_16x16x32_bf16 v[204:207], v[4:7], v[228:231], v[204:207]\n\t" \
    "ds_read_b128 v[240:243], v253 offset:10240\n\t" \
    "v_mfma_f32_16x16x32_bf16 v[208:211], v[8:11], v[224:227], 0\n\t" \
    "v_mfma_f32_16x16x32_bf16 v[208:211], v[12:15], v[228:231], v[208:211]\n\t" \
    "v_mfma_f32_16x16x32_bf16 v[200:203], v[244:247], v[224:227], 0\n\t" \
    "s_waitcnt lgkmcnt(1)\n\t" \
    "v_mfma_f32_16x16x32_bf16 v[200:203], v[248:251], v[228:231], v[200:203]\n\t" \
    "v_mfma_f32_16x16x32_bf16 v[216:219], v[0:3], v[232:235], 0\n\t" \
    "ds_read_b128 v[224:227], v253 offset:11264\n\t" \
    "v_mfma_f32_16x16x32_bf16 v[216:219], v[4:7], v[236:239], v[216:219]\n\t" \
    "ds_read_b128 v[228:231], v253 offset:2048\n\t" \
    "v_mfma_f32_16x16x32_bf16 v[220:223], v[8:11], v[232:235], 0\n\t" \
    "v_mfma_f32_16x16x32_bf16 v[220:223], v[12:15], v[236:239], v[220:223]\n\t" \
    "v_mfma_f32_16x16x32_bf16 v[212:215], v[244:247], v[232:235], 0\n\t" \
    "ds_read_b128 v[244:247], v254 offset:32768\n\t" \
    "v_mfma_f32_16x16x32_bf16 v[212:215], v[248:251], v[236:239], v[212:215]\n\t" \
    "ds_read_b128 v[248:251], v254 offset:33792\n\t" \
    "s_waitcnt lgkmcnt(4)\n\t" \
    "v_mfma_f32_16x16x32_bf16 v[204:207], v[24:27], v[240:243], v[204:207]\n\t" \
    "ds_read_b128 v[232:235], v253 offset:3072\n\t" \
    "s_waitcnt lgkmcnt(4)\n\t" \
    "v_mfma_f32_16x16x32_bf16 v[204:207], v[28:31], v[224:227], v[204:207]\n\t" \
    "ds_read_b128 v[236:239], v253 offset:22528\n\t" \
    "v_mfma_f32_16x16x32_bf16 v[208:211], v[32:35], v[240:243], v[208:211]\n\t" \
    "v_mfma_f32_16x16x32_bf16 v[208:211], v[36:39], v[224:227], v[208:211]\n\t" \
    "v_mfma_f32_16x16x32_bf16 v[200:203], v[16:19], v[240:243], v[200:203]\n\t" \
    "v_mfma_f32_16x16x32_bf16 v[200:203], v[20:23], v[224:227], v[200:203]\n\t" \
    "s_waitcnt lgkmcnt(4)\n\t" \
    "v_mfma_f32_16x16x32_bf16 v[204:207], v[40:43], v[228:231], v[204:207]\n\t" \
    "ds_read_b128 v[240:243], v253 offset:23552\n\t" \
    "s_waitcnt lgkmcnt(2)\n\t" \
    "v_mfma_f32_16x16x32_bf16 v[204:207], v[44:47], v[232:235], v[204:207]\n\t" \
    "ds_read_b128 v[224:227], v253 offset:12288\n\t" \
    "v_mfma_f32_16x16x32_bf16 v[208:211], v[48:51], v[228:231], v[208:211]\n\t" \
    "v_mfma_f32_16x16x32_bf16 v[208:211], v[52:55], v[232:235], v[208:211]\n\t" \
    "v_mfma_f32_16x16x32_bf16 v[200:203], v[244:247], v[228:231], v[200:203]\n\t" \
    "v_mfma_f32_16x16x32_bf16 v[200:203], v[248:251], v[232:235], v[200:203]\n\t" \
    "s_waitcnt lgkmcnt(2)\n\t" \
    "v_mfma_f32_16x16x32_bf16 v[216:219], v[40:43], v[236:239], v[216:219]\n\t" \
    "ds_read_b128 v[228:231], v253 offset:13312\n\t" \
    "s_add_u32 s44, s40, 1\n\t" \
    "s_waitcnt lgkmcnt(2)\n\t" \
    "v_mfma_f32_16x16x32_bf16 v[216:219], v[44:47], v[240:243], v[216:219]\n\t" \
    "ds_read_b128 v[232:235], v253 offset:4096\n\t" \
    "s_and_b32 s45, s44, 15\n\t" \
    "v_mfma_f32_16x16x32_bf16 v[220:223], v[48:51], v[236:239], v[220:223]\n\t" \
    "s_cmp_eq_u32 s45, 0\n\t" \
    "v_mfma_f32_16x16x32_bf16 v[220:223], v[52:55], v[240:243], v[220:223]\n\t" \
    "s_cselect_b32 s45, %[cj], %[cs]\n\t" \
    "v_mfma_f32_16x16x32_bf16 v[212:215], v[244:247], v[236:239], v[212:215]\n\t" \
    "ds_read_b128 v[244:247], v254 offset:34816\n\t" \
    "s_cmp_eq_u32 s44, 64\n\t" \
    "v_mfma_f32_16x16x32_bf16 v[212:215], v[248:251], v[240:243], v[212:215]\n\t" \
    "ds_read_b128 v[248:251], v254 offset:35840\n\t" \
    "s_cselect_b32 s45, 0, s45\n\t" \
    "s_waitcnt lgkmcnt(4)\n\t" \
    "v_mfma_f32_16x16x32_bf16 v[204:207], v[64:67], v[224:227], v[204:207]\n\t" \
    "ds_read_b128 v[236:239], v253 offset:5120\n\t" \
    "s_add_u32 s42, s42, s45\n\t" \
    "s_waitcnt lgkmcnt(4)\n\t" \
    "v_mfma_f32_16x16x32_bf16 v[204:207], v[68:71], v[228:231], v[204:207]\n\t" \
    "ds_read_b128 v[240:243], v253 offset:24576\n\t" \
    "v_mfma_f32_16x16x32_bf16 v[208:211], v[72:75], v[224:227], v[208:211]\n\t" \
    "v_mfma_f32_16x16x32_bf16 v[208:211], v[76:79], v[228:231], v[208:211]\n\t" \
    "v_mfma_f32_16x16x32_bf16 v[200:203], v[56:59], v[224:227], v[200:203]\n\t" \
    "v_mfma_f32_16x16x32_bf16 v[200:203], v[60:63], v[228:231], v[200:203]\n\t" \
    "s_waitcnt lgkmcnt(4)\n\t" \
    "v_mfma_f32_16x16x32_bf16 v[204:207], v[80:83], v[232:235], v[204:207]\n\t" \
    "ds_read_b128 v[224:227], v253 offset:25600\n\t" \
    "s_waitcnt lgkmcnt(2)\n\t" \
    "v_mfma_f32_16x16x32_bf16 v[204:207], v[84:87], v[236:239], v[204:207]\n\t" \
    "ds_read_b128 v[228:231], v253 offset:14336\n\t" \
    "v_mfma_f32_16x16x32_bf16 v[208:211], v[88:91], v[232:235], v[208:211]\n\t" \
    "v_mfma_f32_16x16x32_bf16 v[208:211], v[92:95], v[236:239], v[208:211]\n\t" \
    "v_mfma_f32_16x16x32_bf16 v[200:203], v[244:247], v[232:235], v[200:203]\n\t" \
    "v_mfma_f32_16x16x32_bf16 v[200:203], v[248:251], v[236:239], v[200:203]\n\t" \
    "s_waitcnt lgkmcnt(2)\n\t" \
    "v_mfma_f32_16x16x32_bf16 v[216:219], v[80:83], v[240:243], v[216:219]\n\t" \
    "ds_read_b128 v[232:235], v253 offset:15360\n\t" \
    "s_and_b32 s44, s40, 3\n\t" \
    "s_waitcnt lgkmcnt(2)\n\t" \
    "v_mfma_f32_16x16x32_bf16 v[216:219], v[84:87], v[224:227], v[216:219]\n\t" \
    "ds_read_b128 v[236:239], v253 offset:6144\n\t" \
    "s_lshl_b32 s44, s44, 17\n\t" \
    "v_mfma_f32_16x16x32_bf16 v[220:223], v[88:91], v[240:243], v[220:223]\n\t" \
    "s_add_u32 s44, s44, %[mb]\n\t" \
    "v_mfma_f32_16x16x32_bf16 v[220:223], v[92:95], v[224:227], v[220:223]\n\t" \
    "s_add_u32 s46, s44, 0x0\n\t" \
    "v_mfma_f32_16x16x32_bf16 v[212:215], v[244:247], v[240:243], v[212:215]\n\t" \
    "ds_read_b128 v[244:247], v254 offset:36864\n\t" \
    "s_add_u32 s47, s44, 0x1400\n\t" \
    "v_mfma_f32_16x16x32_bf16 v[212:215], v[248:251], v[224:227], v[212:215]\n\t" \
    "ds_read_b128 v[248:251], v254 offset:37888\n\t" \
    "s_add_u32 s48, s44, 0x400\n\t" \
    "s_waitcnt lgkmcnt(4)\n\t" \
    "v_mfma_f32_16x16x32_bf16 v[204:207], v[104:107], v[228:231], v[204:207]\n\t" \
    "ds_read_b128 v[240:243], v253 offset:7168\n\t" \
    "s_waitcnt lgkmcnt(4)\n\t" \
    "v_mfma_f32_16x16x32_bf16 v[204:207], v[108:111], v[232:235], v[204:207]\n\t" \
    "ds_read_b128 v[224:227], v253 offset:26624\n\t" \
    "v_mfma_f32_16x16x32_bf16 v[208:211], v[112:115], v[228:231], v[208:211]\n\t" \
    "v_mfma_f32_16x16x32_bf16 v[208:211], v[116:119], v[232:235], v[208:211]\n\t" \
    "v_mfma_f32_16x16x32_bf16 v[200:203], v[96:99], v[228:231], v[200:203]\n\t" \
    "v_mfma_f32_16x16x32_bf16 v[200:203], v[100:103], v[232:235], v[200:203]\n\t" \
    "s_waitcnt lgkmcnt(4)\n\t" \
    "v_mfma_f32_16x16x32_bf16 v[204:207], v[120:123], v[236:239], v[204:207]\n\t" \
    "ds_read_b128 v[228:231], v253 offset:27648\n\t" \
    "s_waitcnt lgkmcnt(2)\n\t" \
    "v_mfma_f32_16x16x32_bf16 v[204:207], v[124:127], v[240:243], v[204:207]\n\t" \
    "ds_read_b128 v[232:235], v253 offset:16384\n\t" \
    "v_mfma_f32_16x16x32_bf16 v[208:211], v[128:131], v[236:239], v[208:211]\n\t" \
    "v_mfma_f32_16x16x32_bf16 v[208:211], v[132:135], v[240:243], v[208:211]\n\t" \
    "v_mfma_f32_16x16x32_bf16 v[200:203], v[244:247], v[236:239], v[200:203]\n\t" \
    "v_mfma_f32_16x16x32_bf16 v[200:203], v[248:251], v[240:243], v[200:203]\n\t" \
    "s_waitcnt lgkmcnt(2)\n\t" \
    "v_mfma_f32_16x16x32_bf16 v[216:219], v[120:123], v[224:227], v[216:219]\n\t" \
    "ds_read_b128 v[236:239], v253 offset:17408\n\t" \
    "s_waitcnt lgkmcnt(2)\n\t" \
    "v_mfma_f32_16x16x32_bf16 v[216:219], v[124:127], v[228:231], v[216:219]\n\t" \
    "ds_read_b128 v[240:243], v253 offset:8192\n\t" \
    "v_mfma_f32_16x16x32_bf16 v[220:223], v[128:131], v[224:227], v[220:223]\n\t" \
    "v_mfma_f32_16x16x32_bf16 v[220:223], v[132:135], v[228:231], v[220:223]\n\t" \
    "v_mfma_f32_16x16x32_bf16 v[212:215], v[244:247], v[224:227], v[212:215]\n\t" \
    "ds_read_b128 v[244:247], v254 offset:38912\n\t" \
    "v_mfma_f32_16x16x32_bf16 v[212:215], v[248:251], v[228:231], v[212:215]\n\t" \
    "ds_read_b128 v[248:251], v254 offset:39936\n\t" \
    "v_and_b32 v252, 0xf0, v253\n\t" \
    "v_lshlrev_b32 v252, 3, v252\n\t" \
    "buffer_load_dword v252, v252, s[60:63], %[fl] offen sc1\n\t" \
    "s_waitcnt lgkmcnt(4)\n\t" \
    "v_mfma_f32_16x16x32_bf16 v[204:207], v[144:147], v[232:235], v[204:207]\n\t" \
    "ds_read_b128 v[224:227], v253 offset:9216\n\t" \
    "s_waitcnt lgkmcnt(4)\n\t" \
    "v_mfma_f32_16x16x32_bf16 v[204:207], v[148:151], v[236:239], v[204:207]\n\t" \
    "ds_read_b128 v[228:231], v253 offset:28672\n\t" \
    "v_mfma_f32_16x16x32_bf16 v[208:211], v[152:155], v[232:235], v[208:211]\n\t" \
    "v_mfma_f32_16x16x32_bf16 v[208:211], v[156:159], v[236:239], v[208:211]\n\t" \
    "v_mfma_f32_16x16x32_bf16 v[200:203], v[136:139], v[232:235], v[200:203]\n\t" \
    "v_mfma_f32_16x16x32_bf16 v[200:203], v[140:143], v[236:239], v[200:203]\n\t" \
    "s_waitcnt lgkmcnt(4)\n\t" \
    "v_mfma_f32_16x16x32_bf16 v[204:207], v[160:163], v[240:243], v[204:207]\n\t" \
    "ds_read_b128 v[232:235], v253 offset:29696\n\t" \
    "s_waitcnt lgkmcnt(2)\n\t" \
    "v_mfma_f32_16x16x32_bf16 v[204:207], v[164:167], v[224:227], v[204:207]\n\t" \
    "ds_read_b128 v[236:239], v253 offset:18432\n\t" \
    "v_mfma_f32_16x16x32_bf16 v[208:211], v[168:171], v[240:243], v[208:211]\n\t" \
    "v_mfma_f32_16x16x32_bf16 v[208:211], v[172:175], v[224:227], v[208:211]\n\t" \
    "v_mfma_f32_16x16x32_bf16 v[200:203], v[244:247], v[240:243], v[200:203]\n\t" \
    "v_mfma_f32_16x16x32_bf16 v[200:203], v[248:251], v[224:227], v[200:203]\n\t" \
    "s_waitcnt lgkmcnt(2)\n\t" \
    "v_mfma_f32_16x16x32_bf16 v[216:219], v[160:163], v[228:231], v[216:219]\n\t" \
    "ds_read_b128 v[240:243], v253 offset:19456\n\t" \
    "s_waitcnt lgkmcnt(2)\n\t" \
    "v_mfma_f32_16x16x32_bf16 v[216:219], v[164:167], v[232:235], v[216:219]\n\t" \
    "ds_read_b128 v[224:227], v253 offset:30720\n\t" \
    "v_mfma_f32_16x16x32_bf16 v[220:223], v[168:171], v[228:231], v[220:223]\n\t" \
    "v_mfma_f32_16x16x32_bf16 v[220:223], v[172:175], v[232:235], v[220:223]\n\t" \
    "v_mfma_f32_16x16x32_bf16 v[212:215], v[244:247], v[228:231], v[212:215]\n\t" \
    "v_mfma_f32_16x16x32_bf16 v[212:215], v[248:251], v[232:235], v[212:215]\n\t" \
    "s_waitcnt lgkmcnt(2)\n\t" \
    "v_mfma_f32_16x16x32_bf16 v[204:207], v[184:187], v[236:239], v[204:207]\n\t" \
    "ds_read_b128 v[228:231], v253 offset:31744\n\t" \
    "s_waitcnt lgkmcnt(2)\n\t" \
    "v_mfma_f32_16x16x32_bf16 v[204:207], v[188:191], v[240:243], v[204:207]\n\t" \
    "ds_read_b128 v[232:235], v253 offset:32768\n\t" \
    "v_mfma_f32_16x16x32_bf16 v[208:211], v[192:195], v[236:239], v[208:211]\n\t" \
    "v_mfma_f32_16x16x32_bf16 v[208:211], v[196:199], v[240:243], v[208:211]\n\t" \
    "v_mfma_f32_16x16x32_bf16 v[200:203], v[176:179], v[236:239], v[200:203]\n\t" \
    "v_mfma_f32_16x16x32_bf16 v[200:203], v[180:183], v[240:243], v[200:203]\n\t" \
    "s_waitcnt lgkmcnt(2)\n\t" \
    "v_mfma_f32_16x16x32_bf16 v[216:219], v[24:27], v[224:227], v[216:219]\n\t" \
    "ds_read_b128 v[236:239], v253 offset:33792\n\t" \
    "s_waitcnt lgkmcnt(2)\n\t" \
    "v_mfma_f32_16x16x32_bf16 v[216:219], v[28:31], v[228:231], v[216:219]\n\t" \
    "ds_read_b128 v[240:243], v253 offset:34816\n\t" \
    "v_mfma_f32_16x16x32_bf16 v[220:223], v[32:35], v[224:227], v[220:223]\n\t" \
    "v_cvt_pk_bf16_f32 v204, v204, v205\n\t" \
    "v_mfma_f32_16x16x32_bf16 v[220:223], v[36:39], v[228:231], v[220:223]\n\t" \
    "v_cvt_pk_bf16_f32 v205, v206, v207\n\t" \
    "v_mfma_f32_16x16x32_bf16 v[212:215], v[16:19], v[224:227], v[212:215]\n\t" \
    "ds_write_b64 v254, v[204:205] offset:7168\n\t" \
    "v_mfma_f32_16x16x32_bf16 v[212:215], v[20:23], v[228:231], v[212:215]\n\t" \
    "v_cvt_pk_bf16_f32 v208, v208, v209\n\t" \
    "s_waitcnt lgkmcnt(3)\n\t" \
    "v_mfma_f32_16x16x32_bf16 v[216:219], v[64:67], v[232:235], v[216:219]\n\t" \
    "ds_read_b128 v[224:227], v253 offset:35840\n\t" \
    "v_cvt_pk_bf16_f32 v209, v210, v211\n\t" \
    "s_waitcnt lgkmcnt(3)\n\t" \
    "v_mfma_f32_16x16x32_bf16 v[216:219], v[68:71], v[236:239], v[216:219]\n\t" \
    "ds_read_b128 v[228:231], v253 offset:36864\n\t" \
    "ds_write_b64 v254, v[208:209] offset:8192\n\t" \
    "v_mfma_f32_16x16x32_bf16 v[220:223], v[72:75], v[232:235], v[220:223]\n\t" \
    "v_cvt_pk_bf16_f32 v200, v200, v201\n\t" \
    "v_mfma_f32_16x16x32_bf16 v[220:223], v[76:79], v[236:239], v[220:223]\n\t" \
    "v_cvt_pk_bf16_f32 v201, v202, v203\n\t" \
    "v_mfma_f32_16x16x32_bf16 v[212:215], v[56:59], v[232:235], v[212:215]\n\t" \
    "ds_write_b64 v254, v[200:201] offset:6144\n\t" \
    "v_mfma_f32_16x16x32_bf16 v[212:215], v[60:63], v[236:239], v[212:215]\n\t" \
    "s_waitcnt lgkmcnt(5)\n\t" \
    "v_mfma_f32_16x16x32_bf16 v[216:219], v[104:107], v[240:243], v[216:219]\n\t" \
    "ds_read_b128 v[232:235], v253 offset:37888\n\t" \
    "s_waitcnt lgkmcnt(4)\n\t" \
    "v_mfma_f32_16x16x32_bf16 v[216:219], v[108:111], v[224:227], v[216:219]\n\t" \
    "ds_read_b128 v[236:239], v253 offset:38912\n\t" \
    "v_mfma_f32_16x16x32_bf16 v[220:223], v[112:115], v[240:243], v[220:223]\n\t" \
    "v_mfma_f32_16x16x32_bf16 v[220:223], v[116:119], v[224:227], v[220:223]\n\t" \
    "v_mfma_f32_16x16x32_bf16 v[212:215], v[96:99], v[240:243], v[212:215]\n\t" \
    "v_mfma_f32_16x16x32_bf16 v[212:215], v[100:103], v[224:227], v[212:215]\n\t" \
    "ds_read_b128 v[240:243], v253 offset:39936\n\t" \
    "s_waitcnt lgkmcnt(3)\n\t" \
    "s_barrier \n\t" \
    "v_mfma_f32_16x16x32_bf16 v[216:219], v[144:147], v[228:231], v[216:219]\n\t" \
    "s_waitcnt lgkmcnt(2)\n\t" \
    "v_mfma_f32_16x16x32_bf16 v[216:219], v[148:151], v[232:235], v[216:219]\n\t" \
    "s_waitcnt lgkmcnt(1)\n\t" \
    "v_mfma_f32_16x16x32_bf16 v[216:219], v[184:187], v[236:239], v[216:219]\n\t" \
    "s_waitcnt lgkmcnt(0)\n\t" \
    "v_mfma_f32_16x16x32_bf16 v[216:219], v[188:191], v[240:243], v[216:219]\n\t" \
    "v_mfma_f32_16x16x32_bf16 v[220:223], v[152:155], v[228:231], v[220:223]\n\t" \
    "v_mfma_f32_16x16x32_bf16 v[220:223], v[156:159], v[232:235], v[220:223]\n\t" \
    "v_mfma_f32_16x16x32_bf16 v[220:223], v[192:195], v[236:239], v[220:223]\n\t" \
    "v_cvt_pk_bf16_f32 v216, v216, v217\n\t" \
    "v_cvt_pk_bf16_f32 v217, v218, v219\n\t" \
    "v_mfma_f32_16x16x32_bf16 v[220:223], v[196:199], v[240:243], v[220:223]\n\t" \
    "ds_write_b64 v254, v[216:217] offset:13312\n\t" \
    "v_mfma_f32_16x16x32_bf16 v[212:215], v[136:139], v[228:231], v[212:215]\n\t" \
    "v_mfma_f32_16x16x32_bf16 v[212:215], v[140:143], v[232:235], v[212:215]\n\t" \
    "v_mfma_f32_16x16x32_bf16 v[212:215], v[176:179], v[236:239], v[212:215]\n\t" \
    "v_cvt_pk_bf16_f32 v220, v220, v221\n\t" \
    "v_cvt_pk_bf16_f32 v221, v222, v223\n\t" \
    "v_mfma_f32_16x16x32_bf16 v[212:215], v[180:183], v[240:243], v[212:215]\n\t" \
    "ds_write_b64 v254, v[220:221] offset:14336\n\t" \
    "s_nop 6\n\t" \
    "v_cvt_pk_bf16_f32 v212, v212, v213\n\t" \
    "v_cvt_pk_bf16_f32 v213, v214, v215\n\t" \
    "ds_write_b64 v254, v[212:213] offset:12288\n\t" \
    LST6 \
    LJIT \
    "s_waitcnt vmcnt(0) lgkmcnt(0)\n\t" \
    "s_barrier \n\t" \
    "s_setprio 2\n\t" \
    "s_add_u32 s44, s40, 1\n\t" \
    "s_min_u32 s44, s44, 63\n\t" \
    ".Lv08_%=:\n\t" \
    "v_cmp_ge_u32 vcc, s44, v252\n\t" \
    "s_cbranch_vccz .Lv09_%=\n\t" \
    "s_cmp_eq_u32 s41, 0\n\t" \
    "s_cbranch_scc1 .Lv09_%=\n\t" \
    "s_sub_u32 s41, s41, 1\n\t" \
    "s_sleep 2\n\t" \
    "v_and_b32 v252, 0xf0, v253\n\t" \
    "v_lshlrev_b32 v252, 3, v252\n\t" \
    "buffer_load_dword v252, v252, s[60:63], %[fl] offen sc1\n\t" \
    "s_waitcnt vmcnt(0)\n\t" \
    "s_branch .Lv08_%=\n\t" \
    ".Lv09_%=:\n\t" \
    "s_mov_b32 m0, 0x0\n\t" \
    "s_add_u32 s49, s42, 0x0\n\t" \
    "buffer_load_dwordx4 v253, s[60:63], s49 offen offset:0 sc1 lds\n\t" \
    "s_sleep 1\n\t" \
    "buffer_load_dwordx4 v253, s[60:63], s49 offen offset:1024 sc1 lds\n\t" \
    "s_sleep 1\n\t" \
    "buffer_load_dwordx4 v253, s[60:63], s49 offen offset:2048 sc1 lds\n\t" \
    "s_sleep 1\n\t" \
    "buffer_load_dwordx4 v253, s[60:63], s49 offen offset:3072 sc1 lds\n\t" \
    "s_sleep 1\n\t" \
    "s_mov_b32 m0, 0x4000\n\t" \
    "s_add_u32 s49, s42, 0x10000\n\t" \
    "buffer_load_dwordx4 v253, s[60:63], s49 offen offset:0 sc1 lds\n\t" \
    "s_sleep 1\n\t" \
    "s_mov_b32 m0, 0x5000\n\t" \
    "s_add_u32 s49, s42, 0x1000\n\t" \
    "buffer_load_dwordx4 v253, s[60:63], s49 offen offset:0 sc1 lds\n\t" \
    "s_sleep 1\n\t" \
    "buffer_load_dwordx4 v253, s[60:63], s49 offen offset:1024 sc1 lds\n\t" \
    "s_sleep 1\n\t" \
    "buffer_load_dwordx4 v253, s[60:63], s49 offen offset:2048 sc1 lds\n\t" \
    "s_sleep 1\n\t" \
    "buffer_load_dwordx4 v253, s[60:63], s49 offen offset:3072 sc1 lds\n\t" \
    "s_sleep 1\n\t" \
    "s_mov_b32 m0, 0x9000\n\t" \
    "s_add_u32 s49, s42, 0x11000\n\t" \
    "buffer_load_dwordx4 v253, s[60:63], s49 offen offset:0 sc1 lds\n\t" \
    "s_sleep 1\n\t" \
    "ds_read_b128 v[200:203], v254 offset:30720\n\t" \
    "ds_read_b128 v[204:207], v254 offset:31744\n\t" \
    "ds_read_b128 v[216:219], v254 offset:32768\n\t" \
    "ds_read_b128 v[220:223], v254 offset:33792\n\t" \
    "v_mov_b64 v[208:209], 0\n\t" \
    "v_mov_b64 v[210:211], 0\n\t" \
    "v_mov_b64 v[244:245], 0\n\t" \
    "v_mov_b64 v[246:247], 0\n\t" \
    "v_mov_b64 v[248:249], 0\n\t" \
    "v_mov_b64 v[250:251], 0\n\t" \
    LST1 \
    LJIT \
    "s_sleep 5\n\t" \
    "s_mov_b32 m0, 0xa000\n\t" \
    "s_nop 0\n\t" \
    "buffer_load_dwordx4 v253, s[60:63], s46 offen sc1 lds\n\t" \
    "s_mov_b32 m0, 0xb400\n\t" \
    "s_nop 0\n\t" \
    "buffer_load_dwordx4 v253, s[60:63], s47 offen sc1 lds\n\t" \
    "s_mov_b32 m0, 0xa400\n\t" \
    "s_nop 0\n\t" \
    "buffer_load_dwordx4 v253, s[60:63], s48 offen sc1 lds\n\t" \
    "s_waitcnt lgkmcnt(0)\n\t" \
    LFLUSH212 \
    "v_bfe_i32 v212, v253, 5, 1\n\t" \
    "v_and_b32 v212, 0xaaaa, v212\n\t" \
    "v_or_b32 v212, 0x4444, v212\n\t" \
    "v_and_b32 v213, 0xffff, v255\n\t" \
    "v_lshrrev_b32 v214, 16, v255\n\t" \
    ".Lv010_%=:\n\t" \
    "s_waitcnt vmcnt(0)\n\t" \
    "ds_read_b128 v[224:227], v253 offset:40960\n\t" \
    "ds_read_b128 v[228:231], v253 offset:46080\n\t" \
    "ds_read_b128 v[232:235], v253 offset:41984\n\t" \
    "s_waitcnt lgkmcnt(2)\n\t" \
    "v_max_u32 v215, v224, v225\n\t" \
    "v_max3_u32 v215, v226, v227, v215\n\t" \
    "s_waitcnt lgkmcnt(1)\n\t" \
    "v_max3_u32 v215, v228, v229, v215\n\t" \
    "v_max3_u32 v215, v230, v231, v215\n\t" \
    "s_waitcnt lgkmcnt(0)\n\t" \
    "v_max3_u32 v215, v232, v233, v215\n\t" \
    "v_max3_u32 v215, v234, v235, v215\n\t" \
    "v_cmp_eq_u32 vcc, -1, v215\n\t" \
    "s_cbranch_vccz .Lv011_%=\n\t" \
    "v_max_u32 v215, v224, v225\n\t" \
    "v_max3_u32 v215, v226, v227, v215\n\t" \
    "v_cmp_eq_u32 vcc, -1, v215\n\t" \
    "s_cbranch_vccz .Lv012_%=\n\t" \
    "s_mov_b32 m0, 0xa000\n\t" \
    "s_nop 0\n\t" \
    "buffer_load_dwordx4 v253, s[60:63], s46 offen sc1 lds\n\t" \
    ".Lv012_%=:\n\t" \
    "v_max_u32 v215, v228, v229\n\t" \
    "v_max3_u32 v215, v230, v231, v215\n\t" \
    "v_cmp_eq_u32 vcc, -1, v215\n\t" \
    "s_cbranch_vccz .Lv013_%=\n\t" \
    "s_mov_b32 m0, 0xb400\n\t" \
    "s_nop 0\n\t" \
    "buffer_load_dwordx4 v253, s[60:63], s47 offen sc1 lds\n\t" \
    ".Lv013_%=:\n\t" \
    "v_max_u32 v215, v232, v233\n\t" \
    "v_max3_u32 v215, v234, v235, v215\n\t" \
    "v_cmp_eq_u32 vcc, -1, v215\n\t" \
    "s_cbranch_vccz .Lv014_%=\n\t" \
    "s_mov_b32 m0, 0xa400\n\t" \
    "s_nop 0\n\t" \
    "buffer_load_dwordx4 v253, s[60:63], s48 offen sc1 lds\n\t" \
    ".Lv014_%=:\n\t" \
    "s_cmp_eq_u32 s41, 0\n\t" \
    "s_cbranch_scc1 .Lv011_%=\n\t" \
    "s_sub_u32 s41, s41, 1\n\t" \
    "s_branch .Lv010_%=\n\t" \
    ".Lv011_%=:\n\t" \
    LST2 \
    LJIT \
    "s_waitcnt lgkmcnt(0)\n\t" \
    "s_barrier \n\t" \
    LST3 \
    LJIT \
    "ds_read_b128 v[236:239], v253 offset:47104\n\t" \
    "ds_read_b128 v[240:243], v253 offset:43008\n\t" \
    "v_smfmac_f32_16x16x64_bf16 v[244:247], v[224:227], v[0:7], v212\n\t" \
    "v_smfmac_f32_16x16x64_bf16 v[248:251], v[224:227], v[8:15], v212\n\t" \
    "v_smfmac_f32_16x16x64_bf16 v[208:211], v[224:227], v[200:207], v212\n\t" \
    "ds_read_b128 v[200:203], v254 offset:34816\n\t" \
    "ds_read_b128 v[204:207], v254 offset:35840\n\t" \
    "ds_read_b128 v[224:227], v253 offset:48128\n\t" \
    "v_smfmac_f32_16x16x64_bf16 v[208:211], v[228:231], v[16:23], v212\n\t" \
    "v_smfmac_f32_16x16x64_bf16 v[244:247], v[228:231], v[24:31], v212\n\t" \
    "v_smfmac_f32_16x16x64_bf16 v[248:251], v[228:231], v[32:39], v212\n\t" \
    "ds_read_b128 v[228:231], v253 offset:44032\n\t" \
    "v_smfmac_f32_16x16x64_bf16 v[208:211], v[232:235], v[216:223], v212\n\t" \
    "ds_read_b128 v[216:219], v254 offset:36864\n\t" \
    "ds_read_b128 v[220:223], v254 offset:37888\n\t" \
    "v_smfmac_f32_16x16x64_bf16 v[244:247], v[232:235], v[40:47], v212\n\t" \
    "v_smfmac_f32_16x16x64_bf16 v[248:251], v[232:235], v[48:55], v212\n\t" \
    "ds_read_b128 v[232:235], v253 offset:49152\n\t" \
    "s_waitcnt lgkmcnt(8)\n\t" \
    "v_smfmac_f32_16x16x64_bf16 v[208:211], v[236:239], v[56:63], v212\n\t" \
    "v_smfmac_f32_16x16x64_bf16 v[244:247], v[236:239], v[64:71], v212\n\t" \
    "v_smfmac_f32_16x16x64_bf16 v[248:251], v[236:239], v[72:79], v212\n\t" \
    "ds_read_b128 v[236:239], v253 offset:45056\n\t" \
    "s_waitcnt lgkmcnt(6)\n\t" \
    "v_smfmac_f32_16x16x64_bf16 v[208:211], v[240:243], v[200:207], v212\n\t" \
    "ds_read_b128 v[200:203], v254 offset:38912\n\t" \
    "ds_read_b128 v[204:207], v254 offset:39936\n\t" \
    "v_smfmac_f32_16x16x64_bf16 v[244:247], v[240:243], v[80:87], v212\n\t" \
    "v_smfmac_f32_16x16x64_bf16 v[248:251], v[240:243], v[88:95], v212\n\t" \
    "ds_read_b128 v[240:243], v253 offset:50176\n\t" \
    "s_waitcnt lgkmcnt(8)\n\t" \
    "v_smfmac_f32_16x16x64_bf16 v[208:211], v[224:227], v[96:103], v212\n\t" \
    "v_smfmac_f32_16x16x64_bf16 v[244:247], v[224:227], v[104:111], v212\n\t" \
    "v_smfmac_f32_16x16x64_bf16 v[248:251], v[224:227], v[112:119], v212\n\t" \
    "s_waitcnt lgkmcnt(7)\n\t" \
    "v_smfmac_f32_16x16x64_bf16 v[244:247], v[228:231], v[120:127], v212\n\t" \
    "s_waitcnt lgkmcnt(4)\n\t" \
    "v_smfmac_f32_16x16x64_bf16 v[244:247], v[232:235], v[144:151], v212\n\t" \
    "s_waitcnt lgkmcnt(3)\n\t" \
    "v_smfmac_f32_16x16x64_bf16 v[244:247], v[236:239], v[160:167], v212\n\t" \
    "s_waitcnt lgkmcnt(0)\n\t" \
    "v_smfmac_f32_16x16x64_bf16 v[244:247], v[240:243], v[184:191], v212\n\t" \
    "v_smfmac_f32_16x16x64_bf16 v[248:251], v[228:231], v[128:135], v212\n\t" \
    "v_smfmac_f32_16x16x64_bf16 v[248:251], v[232:235], v[152:159], v212\n\t" \
    "v_smfmac_f32_16x16x64_bf16 v[248:251], v[236:239], v[168:175], v212\n\t" \
    "v_add_f32 v244, v244, v246\n\t" \
    "v_add_f32 v245, v245, v247\n\t" \
    "v_cvt_pk_bf16_f32 v244, v244, v245\n\t" \
    "v_smfmac_f32_16x16x64_bf16 v[248:251], v[240:243], v[192:199], v212\n\t" \
    "ds_write_b16 v213, v244 offset:512\n\t" \
    "ds_write_b16_d16_hi v213, v244 offset:528\n\t" \
    "v_smfmac_f32_16x16x64_bf16 v[208:211], v[228:231], v[216:223], v212\n\t" \
    "v_smfmac_f32_16x16x64_bf16 v[208:211], v[232:235], v[136:143], v212\n\t" \
    "v_smfmac_f32_16x16x64_bf16 v[208:211], v[236:239], v[200:207], v212\n\t" \
    "v_add_f32 v248, v248, v250\n\t" \
    "v_add_f32 v249, v249, v251\n\t" \
    "v_cvt_pk_bf16_f32 v248, v248, v249\n\t" \
    "v_smfmac_f32_16x16x64_bf16 v[208:211], v[240:243], v[176:183], v212\n\t" \
    "ds_write_b16 v213, v248 offset:1024\n\t" \
    "ds_write_b16_d16_hi v213, v248 offset:1040\n\t" \
    "s_nop 5\n\t" \
    "v_add_f32 v208, v208, v210\n\t" \
    "v_add_f32 v209, v209, v211\n\t" \
    "v_cvt_pk_bf16_f32 v208, v208, v209\n\t" \
    "ds_write_b16 v213, v208 offset:0\n\t" \
    "ds_write_b16_d16_hi v213, v208 offset:16\n\t" \
    "s_setprio 0\n\t" \
    "s_waitcnt lgkmcnt(0)\n\t" \
    "s_barrier \n\t" \
    LST4 \
    LJIT \
    "ds_read_b128 v[224:227], v253 offset:0\n\t" \
    "ds_read_b128 v[244:247], v254 offset:30720\n\t" \
    "ds_read_b128 v[228:231], v253 offset:1024\n\t" \
    "ds_read_b128 v[232:235], v253 offset:20480\n\t" \
    "ds_read_b128 v[236:239], v253 offset:21504\n\t" \
    "ds_read_b128 v[248:251], v254 offset:31744\n\t" \
    "s_add_u32 s40, s40, 1\n\t" \
    "s_cmp_lg_u32 s40, 64\n\t" \
    "s_cbranch_scc1 .Lv07_%=\n\t" \
    "s_branch .Lend%=\n\t" \
    ".Lend%=:\n\t" \
    "s_waitcnt vmcnt(0) lgkmcnt(0)\n\t" \
    "s_mov_b32 %[bud], s41\n\t"
#define ASM_R_LOOP \
    "s_mov_b32 s40, 0\n\t" \
    "s_mov_b32 s41, 0x40000\n\t" \
    LSTINIT \
    "v_mov_b64 v[252:253], -1\n\t" \
    "v_mov_b64 v[254:255], -1\n\t" \
    "v_mov_b32 v248, 0\n\t" \
    "s_mov_b32 s53, 0x05040100\n\t" \
    ".Lr1_%=:\n\t" \
    "s_mov_b32 s43, s40\n\t" \
    "s_and_b32 s44, s40, 3\n\t" \
    "s_lshl_b32 s42, s44, 20\n\t" \
    "s_and_b32 s45, s40, 2\n\t" \
    "s_lshl_b32 s45, s45, 12\n\t" \
    "s_add_u32 s42, s42, s45\n\t" \
    "s_add_u32 s42, s42, %[vx]\n\t" \
    "s_mul_i32 s46, s44, 0x202000\n\t" \
    "s_mul_i32 s47, s44, 0x30000\n\t" \
    "s_lshl_b32 s49, s44, 17\n\t" \
    "s_add_u32 s49, s49, %[px]\n\t" \
    "s_add_u32 s50, s49, 0x1000\n\t" \
    "s_add_u32 s44, s40, 1\n\t" \
    "s_and_b32 s44, s44, 3\n\t" \
    "s_lshl_b32 s48, s44, 17\n\t" \
    "s_mul_i32 s52, s40, 0x14000\n\t" \
    "s_lshr_b32 s44, s40, 4\n\t" \
    "s_mul_i32 s44, s44, 0xc2000\n\t" \
    "s_add_u32 s52, s52, s44\n\t" \
    "s_add_u32 s52, s52, %[xr]\n\t" \
    "s_add_i32 s44, s40, %[ad]\n\t" \
    "s_and_b32 s45, s44, 3\n\t" \
    "s_mul_i32 s51, s45, %[as]\n\t" \
    "s_cmp_lt_i32 s44, 0\n\t" \
    "s_cselect_b32 s55, 0x40000000, 0\n\t" \
    "s_add_u32 s44, s40, 3\n\t" \
    "s_mul_i32 s54, s44, 0x14000\n\t" \
    "s_lshr_b32 s45, s44, 4\n\t" \
    "s_mul_i32 s45, s45, 0xc2000\n\t" \
    "s_add_u32 s54, s54, s45\n\t" \
    "s_cmp_eq_u32 %[wave], 7\n\t" \
    "s_cselect_b32 s51, s54, s51\n\t" \
    "s_cselect_b32 s55, 0, s55\n\t" \
    "s_cselect_b32 s44, 60, 62\n\t" \
    "s_cmp_gt_u32 s40, s44\n\t" \
    "s_cselect_b32 s55, 0x40000000, s55\n\t" \
    "v_or_b32 v197, s55, %[ae]\n\t" \
    "v_mov_b64 v[180:181], -1\n\t" \
    "v_mov_b64 v[182:183], -1\n\t" \
    "v_mov_b64 v[184:185], -1\n\t" \
    "v_mov_b64 v[186:187], -1\n\t" \
    "v_mov_b64 v[188:189], -1\n\t" \
    "v_mov_b64 v[190:191], -1\n\t" \
    "v_mov_b64 v[192:193], -1\n\t" \
    "v_mov_b64 v[194:195], -1\n\t" \
    "s_waitcnt vmcnt(0)\n\t" \
    ".Lr2_%=:\n\t" \
    "buffer_load_dword v248, %[fo], %[rs], %[fs] offen sc1\n\t" \
    "s_waitcnt vmcnt(0)\n\t" \
    "v_cmp_ge_u32 vcc, s43, v248\n\t" \
    "s_cbranch_vccz .Lr3_%=\n\t" \
    "s_sleep 2\n\t" \
    "s_cmp_eq_u32 s41, 0\n\t" \
    "s_cbranch_scc1 .Lr3_%=\n\t" \
    "s_sub_u32 s41, s41, 1\n\t" \
    "s_branch .Lr2_%=\n\t" \
    ".Lr3_%=:\n\t" \
    "buffer_load_dwordx4 v[160:163], %[l16], %[rs], s42 offen offset:0 sc1\n\t" \
    "buffer_load_dwordx4 v[164:167], %[l16], %[rs], s42 offen offset:1024 sc1\n\t" \
    "buffer_load_dwordx4 v[168:171], %[l16], %[rs], s42 offen offset:2048 sc1\n\t" \
    "buffer_load_dwordx4 v[172:175], %[l16], %[rs], s42 offen offset:3072 sc1\n\t" \
    LST0 \
    LJIT \
    "s_sleep 8\n\t" \
    "s_cmp_lg_u32 s40, 63\n\t" \
    "s_cbranch_scc1 .Lr5_%=\n\t" \
    "buffer_load_dwordx4 v[180:183], %[ov], %[rs], s46 offen sc1\n\t" \
    ".Lr6_%=:\n\t" \
    "s_waitcnt vmcnt(12)\n\t" \
    "v_max_u32 v196, v180, v181\n\t" \
    "v_max3_u32 v196, v182, v183, v196\n\t" \
    "v_cmp_eq_u32 vcc, -1, v196\n\t" \
    "s_cbranch_vccz .Lr7_%=\n\t" \
    "s_nop 7\n\t" \
    "v_max_u32 v196, v180, v181\n\t" \
    "v_max3_u32 v196, v182, v183, v196\n\t" \
    "v_cmp_eq_u32 vcc, -1, v196\n\t" \
    "s_cbranch_vccz .Lr7_%=\n\t" \
    "s_nop 7\n\t" \
    "v_max_u32 v196, v180, v181\n\t" \
    "v_max3_u32 v196, v182, v183, v196\n\t" \
    "v_cmp_eq_u32 vcc, -1, v196\n\t" \
    "s_cbranch_vccz .Lr7_%=\n\t" \
    "s_nop 7\n\t" \
    "v_max_u32 v196, v180, v181\n\t" \
    "v_max3_u32 v196, v182, v183, v196\n\t" \
    "v_cmp_eq_u32 vcc, -1, v196\n\t" \
    "s_cbranch_vccz .Lr7_%=\n\t" \
    "s_nop 7\n\t" \
    "s_cmp_eq_u32 s41, 0\n\t" \
    "s_cbranch_scc1 .Lr7_%=\n\t" \
    "s_sub_u32 s41, s41, 1\n\t" \
    "v_max_u32 v196, v180, v181\n\t" \
    "v_max3_u32 v196, v182, v183, v196\n\t" \
    "v_cmp_eq_u32 vcc, -1, v196\n\t" \
    "s_cbranch_vccz .Lr8_%=\n\t" \
    "buffer_load_dwordx4 v[180:183], %[ov], %[rs], s46 offen sc1\n\t" \
    ".Lr8_%=:\n\t" \
    "s_branch .Lr6_%=\n\t" \
    ".Lr7_%=:\n\t" \
    "s_branch .Lr4_%=\n\t" \
    ".Lr5_%=:\n\t" \
    LST7 \
    LJIT \
    "buffer_load_dwordx4 v[184:187], %[o0], %[rs], s47 offen sc1\n\t" \
    "buffer_load_dwordx4 v[188:191], %[o1], %[rs], s47 offen sc1\n\t" \
    "buffer_load_dwordx4 v[192:195], %[o2], %[rs], s47 offen sc1\n\t" \
    "buffer_load_dwordx4 v[180:183], %[ov], %[rs], s46 offen sc1\n\t" \
    ".Lr9_%=:\n\t" \
    "s_waitcnt vmcnt(12)\n\t" \
    "v_max_u32 v196, v184, v185\n\t" \
    "v_max3_u32 v196, v186, v187, v196\n\t" \
    "v_max3_u32 v196, v188, v189, v196\n\t" \
    "v_max3_u32 v196, v190, v191, v196\n\t" \
    "v_max3_u32 v196, v192, v193, v196\n\t" \
    "v_max3_u32 v196, v194, v195, v196\n\t" \
    "v_max3_u32 v196, v180, v181, v196\n\t" \
    "v_max3_u32 v196, v182, v183, v196\n\t" \
    "v_cmp_eq_u32 vcc, -1, v196\n\t" \
    "s_cbranch_vccz .Lr10_%=\n\t" \
    "s_nop 7\n\t" \
    "v_max_u32 v196, v184, v185\n\t" \
    "v_max3_u32 v196, v186, v187, v196\n\t" \
    "v_max3_u32 v196, v188, v189, v196\n\t" \
    "v_max3_u32 v196, v190, v191, v196\n\t" \
    "v_max3_u32 v196, v192, v193, v196\n\t" \
    "v_max3_u32 v196, v194, v195, v196\n\t" \
    "v_max3_u32 v196, v180, v181, v196\n\t" \
    "v_max3_u32 v196, v182, v183, v196\n\t" \
    "v_cmp_eq_u32 vcc, -1, v196\n\t" \
    "s_cbranch_vccz .Lr10_%=\n\t" \
    "s_nop 7\n\t" \
    "v_max_u32 v196, v184, v185\n\t" \
    "v_max3_u32 v196, v186, v187, v196\n\t" \
    "v_max3_u32 v196, v188, v189, v196\n\t" \
    "v_max3_u32 v196, v190, v191, v196\n\t" \
    "v_max3_u32 v196, v192, v193, v196\n\t" \
    "v_max3_u32 v196, v194, v195, v196\n\t" \
    "v_max3_u32 v196, v180, v181, v196\n\t" \
    "v_max3_u32 v196, v182, v183, v196\n\t" \
    "v_cmp_eq_u32 vcc, -1, v196\n\t" \
    "s_cbranch_vccz .Lr10_%=\n\t" \
    "s_nop 7\n\t" \
    "v_max_u32 v196, v184, v185\n\t" \
    "v_max3_u32 v196, v186, v187, v196\n\t" \
    "v_max3_u32 v196, v188, v189, v196\n\t" \
    "v_max3_u32 v196, v190, v191, v196\n\t" \
    "v_max3_u32 v196, v192, v193, v196\n\t" \
    "v_max3_u32 v196, v194, v195, v196\n\t" \
    "v_max3_u32 v196, v180, v181, v196\n\t" \
    "v_max3_u32 v196, v182, v183, v196\n\t" \
    "v_cmp_eq_u32 vcc, -1, v196\n\t" \
    "s_cbranch_vccz .Lr10_%=\n\t" \
    "s_nop 7\n\t" \
    "v_max_u32 v196, v184, v185\n\t" \
    "v_max3_u32 v196, v186, v187, v196\n\t" \
    "v_max3_u32 v196, v188, v189, v196\n\t" \
    "v_max3_u32 v196, v190, v191, v196\n\t" \
    "v_max3_u32 v196, v192, v193, v196\n\t" \
    "v_max3_u32 v196, v194, v195, v196\n\t" \
    "v_max3_u32 v196, v180, v181, v196\n\t" \
    "v_max3_u32 v196, v182, v183, v196\n\t" \
    "v_cmp_eq_u32 vcc, -1, v196\n\t" \
    "s_cbranch_vccz .Lr10_%=\n\t" \
    "s_nop 7\n\t" \
    "v_max_u32 v196, v184, v185\n\t" \
    "v_max3_u32 v196, v186, v187, v196\n\t" \
    "v_max3_u32 v196, v188, v189, v196\n\t" \
    "v_max3_u32 v196, v190, v191, v196\n\t" \
    "v_max3_u32 v196, v192, v193, v196\n\t" \
    "v_max3_u32 v196, v194, v195, v196\n\t" \
    "v_max3_u32 v196, v180, v181, v196\n\t" \
    "v_max3_u32 v196, v182, v183, v196\n\t" \
    "v_cmp_eq_u32 vcc, -1, v196\n\t" \
    "s_cbranch_vccz .Lr10_%=\n\t" \
    "s_nop 7\n\t" \
    "v_max_u32 v196, v184, v185\n\t" \
    "v_max3_u32 v196, v186, v187, v196\n\t" \
    "v_max3_u32 v196, v188, v189, v196\n\t" \
    "v_max3_u32 v196, v190, v191, v196\n\t" \
    "v_max3_u32 v196, v192, v193, v196\n\t" \
    "v_max3_u32 v196, v194, v195, v196\n\t" \
    "v_max3_u32 v196, v180, v181, v196\n\t" \
    "v_max3_u32 v196, v182, v183, v196\n\t" \
    "v_cmp_eq_u32 vcc, -1, v196\n\t" \
    "s_cbranch_vccz .Lr10_%=\n\t" \
    "s_nop 7\n\t" \
    "v_max_u32 v196, v184, v185\n\t" \
    "v_max3_u32 v196, v186, v187, v196\n\t" \
    "v_max3_u32 v196, v188, v189, v196\n\t" \
    "v_max3_u32 v196, v190, v191, v196\n\t" \
    "v_max3_u32 v196, v192, v193, v196\n\t" \
    "v_max3_u32 v196, v194, v195, v196\n\t" \
    "v_max3_u32 v196, v180, v181, v196\n\t" \
    "v_max3_u32 v196, v182, v183, v196\n\t" \
    "v_cmp_eq_u32 vcc, -1, v196\n\t" \
    "s_cbranch_vccz .Lr10_%=\n\t" \
    "s_nop 7\n\t" \
    "s_cmp_eq_u32 s41, 0\n\t" \
    "s_cbranch_scc1 .Lr10_%=\n\t" \
    "s_sub_u32 s41, s41, 1\n\t" \
    "v_max_u32 v196, v184, v185\n\t" \
    "v_max3_u32 v196, v186, v187, v196\n\t" \
    "v_cmp_eq_u32 vcc, -1, v196\n\t" \
    "s_cbranch_vccz .Lr11_%=\n\t" \
    "buffer_load_dwordx4 v[184:187], %[o0], %[rs], s47 offen sc1\n\t" \
    ".Lr11_%=:\n\t" \
    "v_max_u32 v196, v188, v189\n\t" \
    "v_max3_u32 v196, v190, v191, v196\n\t" \
    "v_cmp_eq_u32 vcc, -1, v196\n\t" \
    "s_cbranch_vccz .Lr12_%=\n\t" \
    "buffer_load_dwordx4 v[188:191], %[o1], %[rs], s47 offen sc1\n\t" \
    ".Lr12_%=:\n\t" \
    "v_max_u32 v196, v192, v193\n\t" \
    "v_max3_u32 v196, v194, v195, v196\n\t" \
    "v_cmp_eq_u32 vcc, -1, v196\n\t" \
    "s_cbranch_vccz .Lr13_%=\n\t" \
    "buffer_load_dwordx4 v[192:195], %[o2], %[rs], s47 offen sc1\n\t" \
    ".Lr13_%=:\n\t" \
    "v_max_u32 v196, v180, v181\n\t" \
    "v_max3_u32 v196, v182, v183, v196\n\t" \
    "v_cmp_eq_u32 vcc, -1, v196\n\t" \
    "s_cbranch_vccz .Lr14_%=\n\t" \
    "buffer_load_dwordx4 v[180:183], %[ov], %[rs], s46 offen sc1\n\t" \
    ".Lr14_%=:\n\t" \
    "s_branch .Lr9_%=\n\t" \
    ".Lr10_%=:\n\t" \
    LST1 \
    LJIT \
    "v_mfma_f32_16x16x32_bf16 v[176:179], v[192:195], v[180:183], 0\n\t" \
    "v_smfmac_f32_16x16x64_bf16 v[176:179], v[184:187], v[160:167], %[sidx]\n\t" \
    "v_smfmac_f32_16x16x64_bf16 v[176:179], v[188:191], v[168:175], %[sidx]\n\t" \
    "s_cmp_gt_u32 %[wave], 3\n\t" \
    "s_nop 7\n\t" \
    "v_add_f32 v176, v176, v178\n\t" \
    "v_add_f32 v177, v177, v179\n\t" \
    "ds_write2_b32 %[rw], v176, v177 offset1:80\n\t" \
    "s_waitcnt lgkmcnt(0)\n\t" \
    "s_barrier \n\t" \
    LST2 \
    LJIT \
    "s_cbranch_scc1 .Lr4_%=\n\t" \
    "ds_read_b128 v[128:131], %[rd] offset:0\n\t" \
    "ds_read_b128 v[132:135], %[rd] offset:16\n\t" \
    "ds_read_b128 v[136:139], %[rd] offset:32\n\t" \
    "ds_read_b128 v[140:143], %[rd] offset:48\n\t" \
    "s_waitcnt lgkmcnt(2)\n\t" \
    "v_pk_add_f32 v[128:129], v[128:129], v[130:131]\n\t" \
    "v_pk_add_f32 v[132:133], v[132:133], v[134:135]\n\t" \
    "s_waitcnt lgkmcnt(0)\n\t" \
    "v_pk_add_f32 v[136:137], v[136:137], v[138:139]\n\t" \
    "v_pk_add_f32 v[140:141], v[140:141], v[142:143]\n\t" \
    "v_pk_add_f32 v[128:129], v[128:129], v[132:133]\n\t" \
    "v_pk_add_f32 v[136:137], v[136:137], v[140:141]\n\t" \
    "v_pk_add_f32 v[128:129], v[128:129], v[136:137]\n\t" \
    "v_cvt_pk_bf16_f32 v128, v128, v129\n\t" \
    "v_min_u32 v128, -2, v128\n\t" \
    "buffer_store_dword v128, %[oo], %[rs], s48 offen sc1\n\t" \
    ".Lr4_%=:\n\t" \
    LST3 \
    LJIT \
    "s_cmp_lt_u32 %[wave], 4\n\t" \
    "s_cbranch_scc1 .Lr16_%=\n\t" \
    "s_sleep 4\n\t" \
    ".Lr16_%=:\n\t" \
    "s_sleep 2\n\t" \
    "s_cmp_lt_u32 %[wave], 6\n\t" \
    "s_cbranch_scc1 .Lr15_%=\n\t" \
    "buffer_load_dwordx2 v[250:251], %[xo], %[rs], s52 offen sc1\n\t" \
    ".Lr15_%=:\n\t" \
    "buffer_load_dwordx4 v[220:223], %[l16], %[rs], s50 offen offset:1024 sc1\n\t" \
    "buffer_load_dwordx4 v[224:227], %[l16], %[rs], s50 offen offset:2048 sc1\n\t" \
    "buffer_load_dwordx4 v[228:231], %[l16], %[rs], s50 offen offset:3072 sc1\n\t" \
    "v_perm_b32 v232, v161, v160, s53\n\t" \
    "v_perm_b32 v233, v163, v162, s53\n\t" \
    "v_perm_b32 v234, v165, v164, s53\n\t" \
    "v_perm_b32 v235, v167, v166, s53\n\t" \
    "v_perm_b32 v236, v169, v168, s53\n\t" \
    "v_perm_b32 v237, v171, v170, s53\n\t" \
    "v_perm_b32 v238, v173, v172, s53\n\t" \
    "v_perm_b32 v239, v175, v174, s53\n\t" \
    "s_nop 1\n\t" \
    "v_permlane16_swap_b32 v232, v234\n\t" \
    "v_permlane16_swap_b32 v233, v235\n\t" \
    "v_permlane16_swap_b32 v236, v238\n\t" \
    "v_permlane16_swap_b32 v237, v239\n\t" \
    "s_waitcnt vmcnt(0)\n\t" \
    ".Lr17_%=:\n\t" \
    "v_max_u32 v196, v220, v221\n\t" \
    "v_max3_u32 v196, v222, v223, v196\n\t" \
    "v_max3_u32 v196, v224, v225, v196\n\t" \
    "v_max3_u32 v196, v226, v227, v196\n\t" \
    "v_max3_u32 v196, v228, v229, v196\n\t" \
    "v_max3_u32 v196, v230, v231, v196\n\t" \
    "v_cmp_eq_u32 vcc, -1, v196\n\t" \
    "s_cbranch_vccz .Lr18_%=\n\t" \
    "s_cmp_eq_u32 s41, 0\n\t" \
    "s_cbranch_scc1 .Lr18_%=\n\t" \
    "s_sub_u32 s41, s41, 1\n\t" \
    "v_max_u32 v196, v220, v221\n\t" \
    "v_max3_u32 v196, v222, v223, v196\n\t" \
    "v_cmp_eq_u32 vcc, -1, v196\n\t" \
    "s_cbranch_vccz .Lr19_%=\n\t" \
    "buffer_load_dwordx4 v[220:223], %[l16], %[rs], s50 offen offset:1024 sc1\n\t" \
    ".Lr19_%=:\n\t" \
    "v_max_u32 v196, v224, v225\n\t" \
    "v_max3_u32 v196, v226, v227, v196\n\t" \
    "v_cmp_eq_u32 vcc, -1, v196\n\t" \
    "s_cbranch_vccz .Lr20_%=\n\t" \
    "buffer_load_dwordx4 v[224:227], %[l16], %[rs], s50 offen offset:2048 sc1\n\t" \
    ".Lr20_%=:\n\t" \
    "v_max_u32 v196, v228, v229\n\t" \
    "v_max3_u32 v196, v230, v231, v196\n\t" \
    "v_cmp_eq_u32 vcc, -1, v196\n\t" \
    "s_cbranch_vccz .Lr21_%=\n\t" \
    "buffer_load_dwordx4 v[228:231], %[l16], %[rs], s50 offen offset:3072 sc1\n\t" \
    ".Lr21_%=:\n\t" \
    "s_waitcnt vmcnt(0)\n\t" \
    "s_branch .Lr17_%=\n\t" \
    ".Lr18_%=:\n\t" \
    "v_mfma_f32_16x16x32_bf16 v[240:243], v[232:235], v[220:223], 0\n\t" \
    "v_mfma_f32_16x16x32_bf16 v[240:243], v[236:239], v[224:227], v[240:243]\n\t" \
    "v_mfma_f32_16x16x32_bf16 v[240:243], v[180:183], v[228:231], v[240:243]\n\t" \
    "s_nop 7\n\t" \
    "ds_write_b128 %[pw], v[240:243]\n\t" \
    "s_waitcnt lgkmcnt(0)\n\t" \
    "s_barrier \n\t" \
    LST5 \
    LJIT \
    "buffer_load_dwordx4 v[200:203], %[l16], %[rs], s49 offen offset:0 sc1\n\t" \
    "buffer_load_dwordx4 v[204:207], %[l16], %[rs], s49 offen offset:1024 sc1\n\t" \
    "buffer_load_dwordx4 v[208:211], %[l16], %[rs], s49 offen offset:2048 sc1\n\t" \
    "buffer_load_dwordx4 v[212:215], %[l16], %[rs], s49 offen offset:3072 sc1\n\t" \
    "buffer_load_dwordx4 v[216:219], %[l16], %[rs], s50 offen offset:0 sc1\n\t" \
    "s_cmp_lg_u32 %[wave], 6\n\t" \
    "s_cbranch_scc1 .Lr22_%=\n\t" \
    "ds_read_b128 v[128:131], %[pr] offset:0\n\t" \
    "ds_read_b128 v[132:135], %[pr] offset:1024\n\t" \
    "ds_read_b128 v[136:139], %[pr] offset:2048\n\t" \
    "ds_read_b128 v[140:143], %[pr] offset:3072\n\t" \
    "ds_read_b128 v[144:147], %[pr] offset:4096\n\t" \
    "ds_read_b128 v[148:151], %[pr] offset:5120\n\t" \
    "ds_read_b128 v[152:155], %[pr] offset:6144\n\t" \
    "ds_read_b128 v[156:159], %[pr] offset:7168\n\t" \
    "v_lshlrev_b32 v176, 16, v250\n\t" \
    "v_and_b32 v177, 0xffff0000, v250\n\t" \
    "v_lshlrev_b32 v178, 16, v251\n\t" \
    "v_and_b32 v179, 0xffff0000, v251\n\t" \
    "s_waitcnt lgkmcnt(6)\n\t" \
    "v_pk_add_f32 v[128:129], v[128:129], v[132:133]\n\t" \
    "v_pk_add_f32 v[130:131], v[130:131], v[134:135]\n\t" \
    "s_waitcnt lgkmcnt(4)\n\t" \
    "v_pk_add_f32 v[136:137], v[136:137], v[140:141]\n\t" \
    "v_pk_add_f32 v[138:139], v[138:139], v[142:143]\n\t" \
    "v_pk_add_f32 v[128:129], v[128:129], v[136:137]\n\t" \
    "v_pk_add_f32 v[130:131], v[130:131], v[138:139]\n\t" \
    "s_waitcnt lgkmcnt(2)\n\t" \
    "v_pk_add_f32 v[144:145], v[144:145], v[148:149]\n\t" \
    "v_pk_add_f32 v[146:147], v[146:147], v[150:151]\n\t" \
    "s_waitcnt lgkmcnt(0)\n\t" \
    "v_pk_add_f32 v[152:153], v[152:153], v[156:157]\n\t" \
    "v_pk_add_f32 v[154:155], v[154:155], v[158:159]\n\t" \
    "v_pk_add_f32 v[144:145], v[144:145], v[152:153]\n\t" \
    "v_pk_add_f32 v[146:147], v[146:147], v[154:155]\n\t" \
    "v_pk_add_f32 v[128:129], v[128:129], v[144:145]\n\t" \
    "v_pk_add_f32 v[130:131], v[130:131], v[146:147]\n\t" \
    "v_pk_add_f32 v[128:129], v[128:129], v[176:177]\n\t" \
    "v_pk_add_f32 v[130:131], v[130:131], v[178:179]\n\t" \
    "v_cvt_pk_bf16_f32 v128, v128, v129\n\t" \
    "v_cvt_pk_bf16_f32 v129, v130, v131\n\t" \
    "v_min_u32 v128, -2, v128\n\t" \
    "v_min_u32 v129, -2, v129\n\t" \
    "s_add_u32 s44, s52, 0x1000000\n\t" \
    "buffer_store_dwordx2 v[128:129], %[xo], %[rs], s44 offen sc1\n\t" \
    "s_waitcnt vmcnt(1)\n\t" \
    "s_branch .Lr23_%=\n\t" \
    ".Lr22_%=:\n\t" \
    "s_waitcnt vmcnt(0)\n\t" \
    ".Lr23_%=:\n\t" \
    ".Lr24_%=:\n\t" \
    "v_max_u32 v196, v200, v201\n\t" \
    "v_max3_u32 v196, v202, v203, v196\n\t" \
    "v_max3_u32 v196, v204, v205, v196\n\t" \
    "v_max3_u32 v196, v206, v207, v196\n\t" \
    "v_max3_u32 v196, v208, v209, v196\n\t" \
    "v_max3_u32 v196, v210, v211, v196\n\t" \
    "v_max3_u32 v196, v212, v213, v196\n\t" \
    "v_max3_u32 v196, v214, v215, v196\n\t" \
    "v_max3_u32 v196, v216, v217, v196\n\t" \
    "v_max3_u32 v196, v218, v219, v196\n\t" \
    "v_cmp_eq_u32 vcc, -1, v196\n\t" \
    "s_cbranch_vccz .Lr25_%=\n\t" \
    "s_cmp_eq_u32 s41, 0\n\t" \
    "s_cbranch_scc1 .Lr25_%=\n\t" \
    "s_sub_u32 s41, s41, 1\n\t" \
    "v_max_u32 v196, v200, v201\n\t" \
    "v_max3_u32 v196, v202, v203, v196\n\t" \
    "v_cmp_eq_u32 vcc, -1, v196\n\t" \
    "s_cbranch_vccz .Lr26_%=\n\t" \
    "buffer_load_dwordx4 v[200:203], %[l16], %[rs], s49 offen offset:0 sc1\n\t" \
    ".Lr26_%=:\n\t" \
    "v_max_u32 v196, v204, v205\n\t" \
    "v_max3_u32 v196, v206, v207, v196\n\t" \
    "v_cmp_eq_u32 vcc, -1, v196\n\t" \
    "s_cbranch_vccz .Lr27_%=\n\t" \
    "buffer_load_dwordx4 v[204:207], %[l16], %[rs], s49 offen offset:1024 sc1\n\t" \
    ".Lr27_%=:\n\t" \
    "v_max_u32 v196, v208, v209\n\t" \
    "v_max3_u32 v196, v210, v211, v196\n\t" \
    "v_cmp_eq_u32 vcc, -1, v196\n\t" \
    "s_cbranch_vccz .Lr28_%=\n\t" \
    "buffer_load_dwordx4 v[208:211], %[l16], %[rs], s49 offen offset:2048 sc1\n\t" \
    ".Lr28_%=:\n\t" \
    "v_max_u32 v196, v212, v213\n\t" \
    "v_max3_u32 v196, v214, v215, v196\n\t" \
    "v_cmp_eq_u32 vcc, -1, v196\n\t" \
    "s_cbranch_vccz .Lr29_%=\n\t" \
    "buffer_load_dwordx4 v[212:215], %[l16], %[rs], s49 offen offset:3072 sc1\n\t" \
    ".Lr29_%=:\n\t" \
    "v_max_u32 v196, v216, v217\n\t" \
    "v_max3_u32 v196, v218, v219, v196\n\t" \
    "v_cmp_eq_u32 vcc, -1, v196\n\t" \
    "s_cbranch_vccz .Lr30_%=\n\t" \
    "buffer_load_dwordx4 v[216:219], %[l16], %[rs], s50 offen offset:0 sc1\n\t" \
    ".Lr30_%=:\n\t" \
    "s_waitcnt vmcnt(0)\n\t" \
    "s_branch .Lr24_%=\n\t" \
    ".Lr25_%=:\n\t" \
    "v_mfma_f32_16x16x32_bf16 v[244:247], v[160:163], v[200:203], 0\n\t" \
    "v_mfma_f32_16x16x32_bf16 v[244:247], v[164:167], v[204:207], v[244:247]\n\t" \
    "v_mfma_f32_16x16x32_bf16 v[244:247], v[168:171], v[208:211], v[244:247]\n\t" \
    "v_mfma_f32_16x16x32_bf16 v[244:247], v[172:175], v[212:215], v[244:247]\n\t" \
    "v_mfma_f32_16x16x32_bf16 v[244:247], v[180:183], v[216:219], v[244:247]\n\t" \
    "s_nop 7\n\t" \
    "ds_write_b128 %[pw], v[244:247] offset:8192\n\t" \
    "s_waitcnt lgkmcnt(0)\n\t" \
    "s_barrier \n\t" \
    LST4 \
    LJIT \
    "s_cmp_lg_u32 %[wave], 7\n\t" \
    "s_cbranch_scc1 .Lr31_%=\n\t" \
    "ds_read_b128 v[128:131], %[pr] offset:0\n\t" \
    "ds_read_b128 v[132:135], %[pr] offset:1024\n\t" \
    "ds_read_b128 v[136:139], %[pr] offset:2048\n\t" \
    "ds_read_b128 v[140:143], %[pr] offset:3072\n\t" \
    "ds_read_b128 v[144:147], %[pr] offset:4096\n\t" \
    "ds_read_b128 v[148:151], %[pr] offset:5120\n\t" \
    "ds_read_b128 v[152:155], %[pr] offset:6144\n\t" \
    "ds_read_b128 v[156:159], %[pr] offset:7168\n\t" \
    "v_lshlrev_b32 v176, 16, v250\n\t" \
    "v_and_b32 v177, 0xffff0000, v250\n\t" \
    "v_lshlrev_b32 v178, 16, v251\n\t" \
    "v_and_b32 v179, 0xffff0000, v251\n\t" \
    "s_waitcnt lgkmcnt(6)\n\t" \
    "v_pk_add_f32 v[128:129], v[128:129], v[132:133]\n\t" \
    "v_pk_add_f32 v[130:131], v[130:131], v[134:135]\n\t" \
    "s_waitcnt lgkmcnt(4)\n\t" \
    "v_pk_add_f32 v[136:137], v[136:137], v[140:141]\n\t" \
    "v_pk_add_f32 v[138:139], v[138:139], v[142:143]\n\t" \
    "v_pk_add_f32 v[128:129], v[128:129], v[136:137]\n\t" \
    "v_pk_add_f32 v[130:131], v[130:131], v[138:139]\n\t" \
    "s_waitcnt lgkmcnt(2)\n\t" \
    "v_pk_add_f32 v[144:145], v[144:145], v[148:149]\n\t" \
    "v_pk_add_f32 v[146:147], v[146:147], v[150:151]\n\t" \
    "s_waitcnt lgkmcnt(0)\n\t" \
    "v_pk_add_f32 v[152:153], v[152:153], v[156:157]\n\t" \
    "v_pk_add_f32 v[154:155], v[154:155], v[158:159]\n\t" \
    "v_pk_add_f32 v[144:145], v[144:145], v[152:153]\n\t" \
    "v_pk_add_f32 v[146:147], v[146:147], v[154:155]\n\t" \
    "v_pk_add_f32 v[128:129], v[128:129], v[144:145]\n\t" \
    "v_pk_add_f32 v[130:131], v[130:131], v[146:147]\n\t" \
    "v_pk_add_f32 v[128:129], v[128:129], v[176:177]\n\t" \
    "v_pk_add_f32 v[130:131], v[130:131], v[178:179]\n\t" \
    "v_cvt_pk_bf16_f32 v128, v128, v129\n\t" \
    "v_cvt_pk_bf16_f32 v129, v130, v131\n\t" \
    "v_min_u32 v128, -2, v128\n\t" \
    "v_min_u32 v129, -2, v129\n\t" \
    "s_add_u32 s44, s52, 0x1000000\n\t" \
    "buffer_store_dwordx2 v[128:129], %[xo], %[rs], s44 offen sc1\n\t" \
    ".Lr31_%=:\n\t" \
    "buffer_store_dwordx4 v[252:255], %[ov], %[rs], s46 offen sc1\n\t" \
    "buffer_store_dwordx4 v[252:255], v197, %[rs], s51 offen sc1\n\t" \
    LFLUSHR \
    "s_add_u32 s40, s40, 1\n\t" \
    "s_cmp_lg_u32 s40, 64\n\t" \
    "s_cbranch_scc1 .Lr1_%=\n\t" \
    "s_branch .Lend%=\n\t" \
    ".Lend%=:\n\t" \
    "s_waitcnt vmcnt(0) lgkmcnt(0)\n\t" \
    "s_mov_b32 %[bud], s41\n\t"
#define ASM_W4_MUL \
    "s_mov_b32 s40, 0\n\t" \
    "s_mov_b32 s41, 0x40000\n\t" \
    LSTINIT \
    "s_mov_b32 s43, 0\n\t" \
    "s_mov_b32 s46, %[hb]\n\t" \
    "s_add_u32 s47, s46, 0x4000\n\t" \
    "v_mov_b64 v[188:189], -1\n\t" \
    "v_mov_b32 v246, 0x1e000\n\t" \
    ".Lm1_%=:\n\t" \
    LST0 \
    LJIT \
    ".Lm2_%=:\n\t" \
    "ds_read_b32 v245, v246 offset:24\n\t" \
    "s_sub_i32 s44, s40, 3\n\t" \
    "s_waitcnt lgkmcnt(0)\n\t" \
    "v_cmp_gt_i32 vcc, s44, v245\n\t" \
    "s_cbranch_vccnz .Lm2_%=\n\t" \
    "s_add_u32 s44, s40, 1\n\t" \
    "s_lshr_b32 s45, s44, 1\n\t" \
    "s_and_b32 s44, s44, 1\n\t" \
    "s_lshl_b32 s44, s44, 12\n\t" \
    "s_mul_i32 s48, s45, 0x30000\n\t" \
    "s_add_u32 s48, s48, s44\n\t" \
    "s_lshr_b32 s45, s45, 3\n\t" \
    "s_mul_i32 s45, s45, 0x82000\n\t" \
    "s_add_u32 s48, s48, s45\n\t" \
    "s_add_u32 s48, s48, %[hb]\n\t" \
    "s_add_u32 s49, s48, 0x4000\n\t" \
    "s_cmp_lg_u32 s43, 0\n\t" \
    "s_cbranch_scc1 .Lm5_%=\n\t" \
    ".Lm3_%=:\n\t" \
    "v_max_u32 v244, v188, v189\n\t" \
    "v_max3_u32 v244, v190, v191, v244\n\t" \
    "v_cmp_eq_u32 vcc, -1, v244\n\t" \
    "s_cbranch_vccz .Lm4_%=\n\t" \
    "s_cmp_eq_u32 s41, 0\n\t" \
    "s_cbranch_scc1 .Lm4_%=\n\t" \
    "s_sub_u32 s41, s41, 1\n\t" \
    "s_sleep 3\n\t" \
    "buffer_load_dwordx4 v[188:191], %[l16], %[rs], s47 offen offset:3072 sc1\n\t" \
    "s_waitcnt vmcnt(0)\n\t" \
    "s_branch .Lm3_%=\n\t" \
    ".Lm4_%=:\n\t" \
    "buffer_load_dwordx4 v[184:187], %[l16], %[rs], s47 offen offset:2048 sc1\n\t" \
    "buffer_load_dwordx4 v[180:183], %[l16], %[rs], s47 offen offset:1024 sc1\n\t" \
    "buffer_load_dwordx4 v[176:179], %[l16], %[rs], s47 offen offset:0 sc1\n\t" \
    "buffer_load_dwordx4 v[172:175], %[l16], %[rs], s46 offen offset:3072 sc1\n\t" \
    "buffer_load_dwordx4 v[168:171], %[l16], %[rs], s46 offen offset:2048 sc1\n\t" \
    "buffer_load_dwordx4 v[164:167], %[l16], %[rs], s46 offen offset:1024 sc1\n\t" \
    "buffer_load_dwordx4 v[160:163], %[l16], %[rs], s46 offen offset:0 sc1\n\t" \
    ".Lm5_%=:\n\t" \
    LST1 \
    LJIT \
    "v_mfma_f32_16x16x32_bf16 v[224:227], v[28:31], v[188:191], 0\n\t" \
    "v_mfma_f32_16x16x32_bf16 v[228:231], v[60:63], v[188:191], 0\n\t" \
    "v_mfma_f32_16x16x32_bf16 v[232:235], v[92:95], v[188:191], 0\n\t" \
    "v_mfma_f32_16x16x32_bf16 v[236:239], v[124:127], v[188:191], 0\n\t" \
    "v_mfma_f32_16x16x32_bf16 v[240:243], v[156:159], v[188:191], 0\n\t" \
    "s_waitcnt vmcnt(6)\n\t" \
    ".Lm6_%=:\n\t" \
    "v_max_u32 v244, v184, v185\n\t" \
    "v_max3_u32 v244, v186, v187, v244\n\t" \
    "v_cmp_eq_u32 vcc, -1, v244\n\t" \
    "s_cbranch_vccnz .Lm7_%=\n\t" \
    ".Lm8_%=:\n\t" \
    "v_mfma_f32_16x16x32_bf16 v[224:227], v[24:27], v[184:187], v[224:227]\n\t" \
    "v_mfma_f32_16x16x32_bf16 v[228:231], v[56:59], v[184:187], v[228:231]\n\t" \
    "v_mfma_f32_16x16x32_bf16 v[232:235], v[88:91], v[184:187], v[232:235]\n\t" \
    "v_mfma_f32_16x16x32_bf16 v[236:239], v[120:123], v[184:187], v[236:239]\n\t" \
    "v_mfma_f32_16x16x32_bf16 v[240:243], v[152:155], v[184:187], v[240:243]\n\t" \
    "s_waitcnt vmcnt(5)\n\t" \
    ".Lm9_%=:\n\t" \
    "v_max_u32 v244, v180, v181\n\t" \
    "v_max3_u32 v244, v182, v183, v244\n\t" \
    "v_cmp_eq_u32 vcc, -1, v244\n\t" \
    "s_cbranch_vccnz .Lm10_%=\n\t" \
    ".Lm11_%=:\n\t" \
    "v_mfma_f32_16x16x32_bf16 v[224:227], v[20:23], v[180:183], v[224:227]\n\t" \
    "v_mfma_f32_16x16x32_bf16 v[228:231], v[52:55], v[180:183], v[228:231]\n\t" \
    "v_mfma_f32_16x16x32_bf16 v[232:235], v[84:87], v[180:183], v[232:235]\n\t" \
    "v_mfma_f32_16x16x32_bf16 v[236:239], v[116:119], v[180:183], v[236:239]\n\t" \
    "v_mfma_f32_16x16x32_bf16 v[240:243], v[148:151], v[180:183], v[240:243]\n\t" \
    "s_waitcnt vmcnt(4)\n\t" \
    ".Lm12_%=:\n\t" \
    "v_max_u32 v244, v176, v177\n\t" \
    "v_max3_u32 v244, v178, v179, v244\n\t" \
    "v_cmp_eq_u32 vcc, -1, v244\n\t" \
    "s_cbranch_vccnz .Lm13_%=\n\t" \
    ".Lm14_%=:\n\t" \
    "v_mfma_f32_16x16x32_bf16 v[224:227], v[16:19], v[176:179], v[224:227]\n\t" \
    "v_mfma_f32_16x16x32_bf16 v[228:231], v[48:51], v[176:179], v[228:231]\n\t" \
    "v_mfma_f32_16x16x32_bf16 v[232:235], v[80:83], v[176:179], v[232:235]\n\t" \
    "v_mfma_f32_16x16x32_bf16 v[236:239], v[112:115], v[176:179], v[236:239]\n\t" \
    "v_mfma_f32_16x16x32_bf16 v[240:243], v[144:147], v[176:179], v[240:243]\n\t" \
    "s_waitcnt vmcnt(3)\n\t" \
    ".Lm15_%=:\n\t" \
    "v_max_u32 v244, v172, v173\n\t" \
    "v_max3_u32 v244, v174, v175, v244\n\t" \
    "v_cmp_eq_u32 vcc, -1, v244\n\t" \
    "s_cbranch_vccnz .Lm16_%=\n\t" \
    ".Lm17_%=:\n\t" \
    "v_mfma_f32_16x16x32_bf16 v[224:227], v[12:15], v[172:175], v[224:227]\n\t" \
    "v_mfma_f32_16x16x32_bf16 v[228:231], v[44:47], v[172:175], v[228:231]\n\t" \
    "v_mfma_f32_16x16x32_bf16 v[232:235], v[76:79], v[172:175], v[232:235]\n\t" \
    "v_mfma_f32_16x16x32_bf16 v[236:239], v[108:111], v[172:175], v[236:239]\n\t" \
    "v_mfma_f32_16x16x32_bf16 v[240:243], v[140:143], v[172:175], v[240:243]\n\t" \
    "s_waitcnt vmcnt(2)\n\t" \
    ".Lm18_%=:\n\t" \
    "v_max_u32 v244, v168, v169\n\t" \
    "v_max3_u32 v244, v170, v171, v244\n\t" \
    "v_cmp_eq_u32 vcc, -1, v244\n\t" \
    "s_cbranch_vccnz .Lm19_%=\n\t" \
    ".Lm20_%=:\n\t" \
    "buffer_load_dwordx4 v[220:223], %[l16], %[rs], s49 offen offset:3072 sc1\n\t" \
    "v_mfma_f32_16x16x32_bf16 v[224:227], v[8:11], v[168:171], v[224:227]\n\t" \
    "v_mfma_f32_16x16x32_bf16 v[228:231], v[40:43], v[168:171], v[228:231]\n\t" \
    "v_mfma_f32_16x16x32_bf16 v[232:235], v[72:75], v[168:171], v[232:235]\n\t" \
    "v_mfma_f32_16x16x32_bf16 v[236:239], v[104:107], v[168:171], v[236:239]\n\t" \
    "v_mfma_f32_16x16x32_bf16 v[240:243], v[136:139], v[168:171], v[240:243]\n\t" \
    "s_waitcnt vmcnt(2)\n\t" \
    ".Lm21_%=:\n\t" \
    "v_max_u32 v244, v164, v165\n\t" \
    "v_max3_u32 v244, v166, v167, v244\n\t" \
    "v_cmp_eq_u32 vcc, -1, v244\n\t" \
    "s_cbranch_vccnz .Lm22_%=\n\t" \
    ".Lm23_%=:\n\t" \
    "v_mfma_f32_16x16x32_bf16 v[224:227], v[4:7], v[164:167], v[224:227]\n\t" \
    "v_mfma_f32_16x16x32_bf16 v[228:231], v[36:39], v[164:167], v[228:231]\n\t" \
    "v_mfma_f32_16x16x32_bf16 v[232:235], v[68:71], v[164:167], v[232:235]\n\t" \
    "v_mfma_f32_16x16x32_bf16 v[236:239], v[100:103], v[164:167], v[236:239]\n\t" \
    "v_mfma_f32_16x16x32_bf16 v[240:243], v[132:135], v[164:167], v[240:243]\n\t" \
    "s_waitcnt vmcnt(1)\n\t" \
    ".Lm24_%=:\n\t" \
    "v_max_u32 v244, v160, v161\n\t" \
    "v_max3_u32 v244, v162, v163, v244\n\t" \
    "v_cmp_eq_u32 vcc, -1, v244\n\t" \
    "s_cbranch_vccnz .Lm25_%=\n\t" \
    ".Lm26_%=:\n\t" \
    "v_mfma_f32_16x16x32_bf16 v[224:227], v[0:3], v[160:163], v[224:227]\n\t" \
    "v_mfma_f32_16x16x32_bf16 v[228:231], v[32:35], v[160:163], v[228:231]\n\t" \
    "v_mfma_f32_16x16x32_bf16 v[232:235], v[64:67], v[160:163], v[232:235]\n\t" \
    "v_mfma_f32_16x16x32_bf16 v[236:239], v[96:99], v[160:163], v[236:239]\n\t" \
    "v_mfma_f32_16x16x32_bf16 v[240:243], v[128:131], v[160:163], v[240:243]\n\t" \
    "s_add_u32 s40, s40, 1\n\t" \
    "v_mov_b32 v245, s40\n\t" \
    "ds_write_b128 %[ld], v[224:227] offset:0\n\t" \
    "ds_write_b128 %[ld], v[228:231] offset:6144\n\t" \
    "ds_write_b128 %[ld], v[232:235] offset:12288\n\t" \
    "s_nop 0\n\t" \
    "ds_write_b128 %[ld], v[236:239] offset:18432\n\t" \
    "s_nop 1\n\t" \
    "ds_write_b128 %[ld], v[240:243] offset:24576\n\t" \
    "ds_write_b32 %[fa], v245\n\t" \
    LST2 \
    LJIT \
    "s_mov_b32 s43, 0\n\t" \
    "s_waitcnt vmcnt(0)\n\t" \
    "v_max_u32 v244, v220, v221\n\t" \
    "v_max3_u32 v244, v222, v223, v244\n\t" \
    "v_cmp_eq_u32 vcc, -1, v244\n\t" \
    "s_cbranch_vccnz .Lm27_%=\n\t" \
    "buffer_load_dwordx4 v[216:219], %[l16], %[rs], s49 offen offset:2048 sc1\n\t" \
    "buffer_load_dwordx4 v[212:215], %[l16], %[rs], s49 offen offset:1024 sc1\n\t" \
    "buffer_load_dwordx4 v[208:211], %[l16], %[rs], s49 offen offset:0 sc1\n\t" \
    "buffer_load_dwordx4 v[204:207], %[l16], %[rs], s48 offen offset:3072 sc1\n\t" \
    "buffer_load_dwordx4 v[200:203], %[l16], %[rs], s48 offen offset:2048 sc1\n\t" \
    "buffer_load_dwordx4 v[196:199], %[l16], %[rs], s48 offen offset:1024 sc1\n\t" \
    "buffer_load_dwordx4 v[192:195], %[l16], %[rs], s48 offen offset:0 sc1\n\t" \
    "s_mov_b32 s43, 1\n\t" \
    ".Lm27_%=:\n\t" \
    LST0 \
    LJIT \
    ".Lm28_%=:\n\t" \
    "ds_read_b32 v245, v246 offset:28\n\t" \
    "s_sub_i32 s44, s40, 3\n\t" \
    "s_waitcnt lgkmcnt(0)\n\t" \
    "v_cmp_gt_i32 vcc, s44, v245\n\t" \
    "s_cbranch_vccnz .Lm28_%=\n\t" \
    "s_add_u32 s44, s40, 1\n\t" \
    "s_lshr_b32 s45, s44, 1\n\t" \
    "s_and_b32 s44, s44, 1\n\t" \
    "s_lshl_b32 s44, s44, 12\n\t" \
    "s_mul_i32 s46, s45, 0x30000\n\t" \
    "s_add_u32 s46, s46, s44\n\t" \
    "s_lshr_b32 s45, s45, 3\n\t" \
    "s_mul_i32 s45, s45, 0x82000\n\t" \
    "s_add_u32 s46, s46, s45\n\t" \
    "s_add_u32 s46, s46, %[hb]\n\t" \
    "s_add_u32 s47, s46, 0x4000\n\t" \
    "s_cmp_lg_u32 s43, 0\n\t" \
    "s_cbranch_scc1 .Lm31_%=\n\t" \
    ".Lm29_%=:\n\t" \
    "v_max_u32 v244, v220, v221\n\t" \
    "v_max3_u32 v244, v222, v223, v244\n\t" \
    "v_cmp_eq_u32 vcc, -1, v244\n\t" \
    "s_cbranch_vccz .Lm30_%=\n\t" \
    "s_cmp_eq_u32 s41, 0\n\t" \
    "s_cbranch_scc1 .Lm30_%=\n\t" \
    "s_sub_u32 s41, s41, 1\n\t" \
    "s_sleep 3\n\t" \
    "buffer_load_dwordx4 v[220:223], %[l16], %[rs], s49 offen offset:3072 sc1\n\t" \
    "s_waitcnt vmcnt(0)\n\t" \
    "s_branch .Lm29_%=\n\t" \
    ".Lm30_%=:\n\t" \
    "buffer_load_dwordx4 v[216:219], %[l16], %[rs], s49 offen offset:2048 sc1\n\t" \
    "buffer_load_dwordx4 v[212:215], %[l16], %[rs], s49 offen offset:1024 sc1\n\t" \
    "buffer_load_dwordx4 v[208:211], %[l16], %[rs], s49 offen offset:0 sc1\n\t" \
    "buffer_load_dwordx4 v[204:207], %[l16], %[rs], s48 offen offset:3072 sc1\n\t" \
    "buffer_load_dwordx4 v[200:203], %[l16], %[rs], s48 offen offset:2048 sc1\n\t" \
    "buffer_load_dwordx4 v[196:199], %[l16], %[rs], s48 offen offset:1024 sc1\n\t" \
    "buffer_load_dwordx4 v[192:195], %[l16], %[rs], s48 offen offset:0 sc1\n\t" \
    ".Lm31_%=:\n\t" \
    LST1 \
    LJIT \
    "v_mfma_f32_16x16x32_bf16 v[224:227], v[28:31], v[220:223], 0\n\t" \
    "v_mfma_f32_16x16x32_bf16 v[228:231], v[60:63], v[220:223], 0\n\t" \
    "v_mfma_f32_16x16x32_bf16 v[232:235], v[92:95], v[220:223], 0\n\t" \
    "v_mfma_f32_16x16x32_bf16 v[236:239], v[124:127], v[220:223], 0\n\t" \
    "v_mfma_f32_16x16x32_bf16 v[240:243], v[156:159], v[220:223], 0\n\t" \
    "s_waitcnt vmcnt(6)\n\t" \
    ".Lm32_%=:\n\t" \
    "v_max_u32 v244, v216, v217\n\t" \
    "v_max3_u32 v244, v218, v219, v244\n\t" \
    "v_cmp_eq_u32 vcc, -1, v244\n\t" \
    "s_cbranch_vccnz .Lm33_%=\n\t" \
    ".Lm34_%=:\n\t" \
    "v_mfma_f32_16x16x32_bf16 v[224:227], v[24:27], v[216:219], v[224:227]\n\t" \
    "v_mfma_f32_16x16x32_bf16 v[228:231], v[56:59], v[216:219], v[228:231]\n\t" \
    "v_mfma_f32_16x16x32_bf16 v[232:235], v[88:91], v[216:219], v[232:235]\n\t" \
    "v_mfma_f32_16x16x32_bf16 v[236:239], v[120:123], v[216:219], v[236:239]\n\t" \
    "v_mfma_f32_16x16x32_bf16 v[240:243], v[152:155], v[216:219], v[240:243]\n\t" \
    "s_waitcnt vmcnt(5)\n\t" \
    ".Lm35_%=:\n\t" \
    "v_max_u32 v244, v212, v213\n\t" \
    "v_max3_u32 v244, v214, v215, v244\n\t" \
    "v_cmp_eq_u32 vcc, -1, v244\n\t" \
    "s_cbranch_vccnz .Lm36_%=\n\t" \
    ".Lm37_%=:\n\t" \
    "v_mfma_f32_16x16x32_bf16 v[224:227], v[20:23], v[212:215], v[224:227]\n\t" \
    "v_mfma_f32_16x16x32_bf16 v[228:231], v[52:55], v[212:215], v[228:231]\n\t" \
    "v_mfma_f32_16x16x32_bf16 v[232:235], v[84:87], v[212:215], v[232:235]\n\t" \
    "v_mfma_f32_16x16x32_bf16 v[236:239], v[116:119], v[212:215], v[236:239]\n\t" \
    "v_mfma_f32_16x16x32_bf16 v[240:243], v[148:151], v[212:215], v[240:243]\n\t" \
    "s_waitcnt vmcnt(4)\n\t" \
    ".Lm38_%=:\n\t" \
    "v_max_u32 v244, v208, v209\n\t" \
    "v_max3_u32 v244, v210, v211, v244\n\t" \
    "v_cmp_eq_u32 vcc, -1, v244\n\t" \
    "s_cbranch_vccnz .Lm39_%=\n\t" \
    ".Lm40_%=:\n\t" \
    "v_mfma_f32_16x16x32_bf16 v[224:227], v[16:19], v[208:211], v[224:227]\n\t" \
    "v_mfma_f32_16x16x32_bf16 v[228:231], v[48:51], v[208:211], v[228:231]\n\t" \
    "v_mfma_f32_16x16x32_bf16 v[232:235], v[80:83], v[208:211], v[232:235]\n\t" \
    "v_mfma_f32_16x16x32_bf16 v[236:239], v[112:115], v[208:211], v[236:239]\n\t" \
    "v_mfma_f32_16x16x32_bf16 v[240:243], v[144:147], v[208:211], v[240:243]\n\t" \
    "s_waitcnt vmcnt(3)\n\t" \
    ".Lm41_%=:\n\t" \
    "v_max_u32 v244, v204, v205\n\t" \
    "v_max3_u32 v244, v206, v207, v244\n\t" \
    "v_cmp_eq_u32 vcc, -1, v244\n\t" \
    "s_cbranch_vccnz .Lm42_%=\n\t" \
    ".Lm43_%=:\n\t" \
    "v_mfma_f32_16x16x32_bf16 v[224:227], v[12:15], v[204:207], v[224:227]\n\t" \
    "v_mfma_f32_16x16x32_bf16 v[228:231], v[44:47], v[204:207], v[228:231]\n\t" \
    "v_mfma_f32_16x16x32_bf16 v[232:235], v[76:79], v[204:207], v[232:235]\n\t" \
    "v_mfma_f32_16x16x32_bf16 v[236:239], v[108:111], v[204:207], v[236:239]\n\t" \
    "v_mfma_f32_16x16x32_bf16 v[240:243], v[140:143], v[204:207], v[240:243]\n\t" \
    "s_waitcnt vmcnt(2)\n\t" \
    ".Lm44_%=:\n\t" \
    "v_max_u32 v244, v200, v201\n\t" \
    "v_max3_u32 v244, v202, v203, v244\n\t" \
    "v_cmp_eq_u32 vcc, -1, v244\n\t" \
    "s_cbranch_vccnz .Lm45_%=\n\t" \
    ".Lm46_%=:\n\t" \
    "buffer_load_dwordx4 v[188:191], %[l16], %[rs], s47 offen offset:3072 sc1\n\t" \
    "v_mfma_f32_16x16x32_bf16 v[224:227], v[8:11], v[200:203], v[224:227]\n\t" \
    "v_mfma_f32_16x16x32_bf16 v[228:231], v[40:43], v[200:203], v[228:231]\n\t" \
    "v_mfma_f32_16x16x32_bf16 v[232:235], v[72:75], v[200:203], v[232:235]\n\t" \
    "v_mfma_f32_16x16x32_bf16 v[236:239], v[104:107], v[200:203], v[236:239]\n\t" \
    "v_mfma_f32_16x16x32_bf16 v[240:243], v[136:139], v[200:203], v[240:243]\n\t" \
    "s_waitcnt vmcnt(2)\n\t" \
    ".Lm47_%=:\n\t" \
    "v_max_u32 v244, v196, v197\n\t" \
    "v_max3_u32 v244, v198, v199, v244\n\t" \
    "v_cmp_eq_u32 vcc, -1, v244\n\t" \
    "s_cbranch_vccnz .Lm48_%=\n\t" \
    ".Lm49_%=:\n\t" \
    "v_mfma_f32_16x16x32_bf16 v[224:227], v[4:7], v[196:199], v[224:227]\n\t" \
    "v_mfma_f32_16x16x32_bf16 v[228:231], v[36:39], v[196:199], v[228:231]\n\t" \
    "v_mfma_f32_16x16x32_bf16 v[232:235], v[68:71], v[196:199], v[232:235]\n\t" \
    "v_mfma_f32_16x16x32_bf16 v[236:239], v[100:103], v[196:199], v[236:239]\n\t" \
    "v_mfma_f32_16x16x32_bf16 v[240:243], v[132:135], v[196:199], v[240:243]\n\t" \
    "s_waitcnt vmcnt(1)\n\t" \
    ".Lm50_%=:\n\t" \
    "v_max_u32 v244, v192, v193\n\t" \
    "v_max3_u32 v244, v194, v195, v244\n\t" \
    "v_cmp_eq_u32 vcc, -1, v244\n\t" \
    "s_cbranch_vccnz .Lm51_%=\n\t" \
    ".Lm52_%=:\n\t" \
    "v_mfma_f32_16x16x32_bf16 v[224:227], v[0:3], v[192:195], v[224:227]\n\t" \
    "v_mfma_f32_16x16x32_bf16 v[228:231], v[32:35], v[192:195], v[228:231]\n\t" \
    "v_mfma_f32_16x16x32_bf16 v[232:235], v[64:67], v[192:195], v[232:235]\n\t" \
    "v_mfma_f32_16x16x32_bf16 v[236:239], v[96:99], v[192:195], v[236:239]\n\t" \
    "v_mfma_f32_16x16x32_bf16 v[240:243], v[128:131], v[192:195], v[240:243]\n\t" \
    "s_add_u32 s40, s40, 1\n\t" \
    "v_mov_b32 v245, s40\n\t" \
    "ds_write_b128 %[ld], v[224:227] offset:30720\n\t" \
    "ds_write_b128 %[ld], v[228:231] offset:36864\n\t" \
    "ds_write_b128 %[ld], v[232:235] offset:43008\n\t" \
    "s_nop 0\n\t" \
    "ds_write_b128 %[ld], v[236:239] offset:49152\n\t" \
    "s_nop 1\n\t" \
    "ds_write_b128 %[ld], v[240:243] offset:55296\n\t" \
    "ds_write_b32 %[fa], v245\n\t" \
    LST2 \
    LJIT \
    "s_mov_b32 s43, 0\n\t" \
    "s_waitcnt vmcnt(0)\n\t" \
    "v_max_u32 v244, v188, v189\n\t" \
    "v_max3_u32 v244, v190, v191, v244\n\t" \
    "v_cmp_eq_u32 vcc, -1, v244\n\t" \
    "s_cbranch_vccnz .Lm53_%=\n\t" \
    "buffer_load_dwordx4 v[184:187], %[l16], %[rs], s47 offen offset:2048 sc1\n\t" \
    "buffer_load_dwordx4 v[180:183], %[l16], %[rs], s47 offen offset:1024 sc1\n\t" \
    "buffer_load_dwordx4 v[176:179], %[l16], %[rs], s47 offen offset:0 sc1\n\t" \
    "buffer_load_dwordx4 v[172:175], %[l16], %[rs], s46 offen offset:3072 sc1\n\t" \
    "buffer_load_dwordx4 v[168:171], %[l16], %[rs], s46 offen offset:2048 sc1\n\t" \
    "buffer_load_dwordx4 v[164:167], %[l16], %[rs], s46 offen offset:1024 sc1\n\t" \
    "buffer_load_dwordx4 v[160:163], %[l16], %[rs], s46 offen offset:0 sc1\n\t" \
    "s_mov_b32 s43, 1\n\t" \
    ".Lm53_%=:\n\t" \
    "v_sub_u32 %[ld], %[l2], %[ld]\n\t" \
    LFLUSHM \
    "s_nop 0\n\t" \
    "s_cmp_lg_u32 s40, 128\n\t" \
    "s_cbranch_scc1 .Lm1_%=\n\t" \
    "s_branch .Lend%=\n\t" \
    ".Lm7_%=:\n\t" \
    "s_cmp_eq_u32 s41, 0\n\t" \
    "s_cbranch_scc1 .Lm8_%=\n\t" \
    "s_sub_u32 s41, s41, 1\n\t" \
    "buffer_load_dwordx4 v[184:187], %[l16], %[rs], s47 offen offset:2048 sc1\n\t" \
    "s_waitcnt vmcnt(0)\n\t" \
    "s_branch .Lm6_%=\n\t" \
    ".Lm10_%=:\n\t" \
    "s_cmp_eq_u32 s41, 0\n\t" \
    "s_cbranch_scc1 .Lm11_%=\n\t" \
    "s_sub_u32 s41, s41, 1\n\t" \
    "buffer_load_dwordx4 v[180:183], %[l16], %[rs], s47 offen offset:1024 sc1\n\t" \
    "s_waitcnt vmcnt(0)\n\t" \
    "s_branch .Lm9_%=\n\t" \
    ".Lm13_%=:\n\t" \
    "s_cmp_eq_u32 s41, 0\n\t" \
    "s_cbranch_scc1 .Lm14_%=\n\t" \
    "s_sub_u32 s41, s41, 1\n\t" \
    "buffer_load_dwordx4 v[176:179], %[l16], %[rs], s47 offen offset:0 sc1\n\t" \
    "s_waitcnt vmcnt(0)\n\t" \
    "s_branch .Lm12_%=\n\t" \
    ".Lm16_%=:\n\t" \
    "s_cmp_eq_u32 s41, 0\n\t" \
    "s_cbranch_scc1 .Lm17_%=\n\t" \
    "s_sub_u32 s41, s41, 1\n\t" \
    "buffer_load_dwordx4 v[172:175], %[l16], %[rs], s46 offen offset:3072 sc1\n\t" \
    "s_waitcnt vmcnt(0)\n\t" \
    "s_branch .Lm15_%=\n\t" \
    ".Lm19_%=:\n\t" \
    "s_cmp_eq_u32 s41, 0\n\t" \
    "s_cbranch_scc1 .Lm20_%=\n\t" \
    "s_sub_u32 s41, s41, 1\n\t" \
    "buffer_load_dwordx4 v[168:171], %[l16], %[rs], s46 offen offset:2048 sc1\n\t" \
    "s_waitcnt vmcnt(0)\n\t" \
    "s_branch .Lm18_%=\n\t" \
    ".Lm22_%=:\n\t" \
    "s_cmp_eq_u32 s41, 0\n\t" \
    "s_cbranch_scc1 .Lm23_%=\n\t" \
    "s_sub_u32 s41, s41, 1\n\t" \
    "buffer_load_dwordx4 v[164:167], %[l16], %[rs], s46 offen offset:1024 sc1\n\t" \
    "s_waitcnt vmcnt(0)\n\t" \
    "s_branch .Lm21_%=\n\t" \
    ".Lm25_%=:\n\t" \
    "s_cmp_eq_u32 s41, 0\n\t" \
    "s_cbranch_scc1 .Lm26_%=\n\t" \
    "s_sub_u32 s41, s41, 1\n\t" \
    "buffer_load_dwordx4 v[160:163], %[l16], %[rs], s46 offen offset:0 sc1\n\t" \
    "s_waitcnt vmcnt(0)\n\t" \
    "s_branch .Lm24_%=\n\t" \
    ".Lm33_%=:\n\t" \
    "s_cmp_eq_u32 s41, 0\n\t" \
    "s_cbranch_scc1 .Lm34_%=\n\t" \
    "s_sub_u32 s41, s41, 1\n\t" \
    "buffer_load_dwordx4 v[216:219], %[l16], %[rs], s49 offen offset:2048 sc1\n\t" \
    "s_waitcnt vmcnt(0)\n\t" \
    "s_branch .Lm32_%=\n\t" \
    ".Lm36_%=:\n\t" \
    "s_cmp_eq_u32 s41, 0\n\t" \
    "s_cbranch_scc1 .Lm37_%=\n\t" \
    "s_sub_u32 s41, s41, 1\n\t" \
    "buffer_load_dwordx4 v[212:215], %[l16], %[rs], s49 offen offset:1024 sc1\n\t" \
    "s_waitcnt vmcnt(0)\n\t" \
    "s_branch .Lm35_%=\n\t" \
    ".Lm39_%=:\n\t" \
    "s_cmp_eq_u32 s41, 0\n\t" \
    "s_cbranch_scc1 .Lm40_%=\n\t" \
    "s_sub_u32 s41, s41, 1\n\t" \
    "buffer_load_dwordx4 v[208:211], %[l16], %[rs], s49 offen offset:0 sc1\n\t" \
    "s_waitcnt vmcnt(0)\n\t" \
    "s_branch .Lm38_%=\n\t" \
    ".Lm42_%=:\n\t" \
    "s_cmp_eq_u32 s41, 0\n\t" \
    "s_cbranch_scc1 .Lm43_%=\n\t" \
    "s_sub_u32 s41, s41, 1\n\t" \
    "buffer_load_dwordx4 v[204:207], %[l16], %[rs], s48 offen offset:3072 sc1\n\t" \
    "s_waitcnt vmcnt(0)\n\t" \
    "s_branch .Lm41_%=\n\t" \
    ".Lm45_%=:\n\t" \
    "s_cmp_eq_u32 s41, 0\n\t" \
    "s_cbranch_scc1 .Lm46_%=\n\t" \
    "s_sub_u32 s41, s41, 1\n\t" \
    "buffer_load_dwordx4 v[200:203], %[l16], %[rs], s48 offen offset:2048 sc1\n\t" \
    "s_waitcnt vmcnt(0)\n\t" \
    "s_branch .Lm44_%=\n\t" \
    ".Lm48_%=:\n\t" \
    "s_cmp_eq_u32 s41, 0\n\t" \
    "s_cbranch_scc1 .Lm49_%=\n\t" \
    "s_sub_u32 s41, s41, 1\n\t" \
    "buffer_load_dwordx4 v[196:199], %[l16], %[rs], s48 offen offset:1024 sc1\n\t" \
    "s_waitcnt vmcnt(0)\n\t" \
    "s_branch .Lm47_%=\n\t" \
    ".Lm51_%=:\n\t" \
    "s_cmp_eq_u32 s41, 0\n\t" \
    "s_cbranch_scc1 .Lm52_%=\n\t" \
    "s_sub_u32 s41, s41, 1\n\t" \
    "buffer_load_dwordx4 v[192:195], %[l16], %[rs], s48 offen offset:0 sc1\n\t" \
    "s_waitcnt vmcnt(0)\n\t" \
    "s_branch .Lm50_%=\n\t" \
    ".Lend%=:\n\t" \
    "s_waitcnt vmcnt(0) lgkmcnt(0)\n\t" \
    "s_mov_b32 %[bud], s41\n\t"
#define ASM_W4_FIN0 \
    "s_mov_b32 s40, %[mt]\n\t" \
    "s_mov_b32 s41, 0x40000\n\t" \
    LSTINIT \
    "s_mov_b32 s43, 0\n\t" \
    "v_mov_b32 v99, 0x3f803f80\n\t" \
    "s_setprio 3\n\t" \
    "s_add_u32 s46, s43, 0x2c00000\n\t" \
    "s_add_u32 s47, s43, 0x1c00000\n\t" \
    "buffer_load_dwordx2 v[120:121], %[t0], %[rs], s46 offen sc1\n\t" \
    "buffer_load_dwordx2 v[122:123], %[t0], %[rs], s47 offen sc1\n\t" \
    "buffer_load_dwordx2 v[124:125], %[t1], %[rs], s46 offen sc1\n\t" \
    "buffer_load_dwordx2 v[126:127], %[t1], %[rs], s47 offen sc1\n\t" \
    "buffer_load_dwordx2 v[128:129], %[t2], %[rs], s46 offen sc1\n\t" \
    "buffer_load_dwordx2 v[130:131], %[t2], %[rs], s47 offen sc1\n\t" \
    "buffer_load_dwordx2 v[132:133], %[t3], %[rs], s46 offen sc1\n\t" \
    "buffer_load_dwordx2 v[134:135], %[t3], %[rs], s47 offen sc1\n\t" \
    "buffer_load_dwordx2 v[136:137], %[t4], %[rs], s46 offen sc1\n\t" \
    "buffer_load_dwordx2 v[138:139], %[t4], %[rs], s47 offen sc1\n\t" \
    "s_waitcnt vmcnt(0)\n\t" \
    ".Lf01_%=:\n\t" \
    LST0 \
    LJIT \
    ".Lf02_%=:\n\t" \
    "s_waitcnt vmcnt(1)\n\t" \
    "v_max_u32 v80, v120, v121\n\t" \
    "v_max3_u32 v80, v122, v123, v80\n\t" \
    "v_max3_u32 v80, v124, v125, v80\n\t" \
    "v_max3_u32 v80, v126, v127, v80\n\t" \
    "v_max3_u32 v80, v128, v129, v80\n\t" \
    "v_max3_u32 v80, v130, v131, v80\n\t" \
    "v_max3_u32 v80, v132, v133, v80\n\t" \
    "v_max3_u32 v80, v134, v135, v80\n\t" \
    "v_max3_u32 v80, v136, v137, v80\n\t" \
    "v_max3_u32 v80, v138, v139, v80\n\t" \
    "v_cmp_eq_u32 vcc, -1, v80\n\t" \
    "s_cbranch_vccz .Lf03_%=\n\t" \
    "s_cmp_eq_u32 s41, 0\n\t" \
    "s_cbranch_scc1 .Lf03_%=\n\t" \
    "s_sub_u32 s41, s41, 1\n\t" \
    "s_sleep 8\n\t" \
    "s_add_u32 s46, s43, 0x2c00000\n\t" \
    "s_add_u32 s47, s43, 0x1c00000\n\t" \
    "buffer_load_dwordx2 v[120:121], %[t0], %[rs], s46 offen sc1\n\t" \
    "buffer_load_dwordx2 v[122:123], %[t0], %[rs], s47 offen sc1\n\t" \
    "buffer_load_dwordx2 v[124:125], %[t1], %[rs], s46 offen sc1\n\t" \
    "buffer_load_dwordx2 v[126:127], %[t1], %[rs], s47 offen sc1\n\t" \
    "buffer_load_dwordx2 v[128:129], %[t2], %[rs], s46 offen sc1\n\t" \
    "buffer_load_dwordx2 v[130:131], %[t2], %[rs], s47 offen sc1\n\t" \
    "buffer_load_dwordx2 v[132:133], %[t3], %[rs], s46 offen sc1\n\t" \
    "buffer_load_dwordx2 v[134:135], %[t3], %[rs], s47 offen sc1\n\t" \
    "buffer_load_dwordx2 v[136:137], %[t4], %[rs], s46 offen sc1\n\t" \
    "buffer_load_dwordx2 v[138:139], %[t4], %[rs], s47 offen sc1\n\t" \
    "s_waitcnt vmcnt(0)\n\t" \
    "s_branch .Lf02_%=\n\t" \
    ".Lf03_%=:\n\t" \
    "v_lshlrev_b32 v140, 16, v120\n\t" \
    "v_and_b32 v141, 0xffff0000, v120\n\t" \
    "v_lshlrev_b32 v142, 16, v121\n\t" \
    "v_and_b32 v143, 0xffff0000, v121\n\t" \
    "v_lshlrev_b32 v80, 16, v122\n\t" \
    "v_and_b32 v81, 0xffff0000, v122\n\t" \
    "v_lshlrev_b32 v82, 16, v123\n\t" \
    "v_and_b32 v83, 0xffff0000, v123\n\t" \
    "v_add_f32 v140, v140, v80\n\t" \
    "v_add_f32 v141, v141, v81\n\t" \
    "v_add_f32 v142, v142, v82\n\t" \
    "v_add_f32 v143, v143, v83\n\t" \
    "v_lshlrev_b32 v144, 16, v124\n\t" \
    "v_and_b32 v145, 0xffff0000, v124\n\t" \
    "v_lshlrev_b32 v146, 16, v125\n\t" \
    "v_and_b32 v147, 0xffff0000, v125\n\t" \
    "v_lshlrev_b32 v80, 16, v126\n\t" \
    "v_and_b32 v81, 0xffff0000, v126\n\t" \
    "v_lshlrev_b32 v82, 16, v127\n\t" \
    "v_and_b32 v83, 0xffff0000, v127\n\t" \
    "v_add_f32 v144, v144, v80\n\t" \
    "v_add_f32 v145, v145, v81\n\t" \
    "v_add_f32 v146, v146, v82\n\t" \
    "v_add_f32 v147, v147, v83\n\t" \
    "v_lshlrev_b32 v148, 16, v128\n\t" \
    "v_and_b32 v149, 0xffff0000, v128\n\t" \
    "v_lshlrev_b32 v150, 16, v129\n\t" \
    "v_and_b32 v151, 0xffff0000, v129\n\t" \
    "v_lshlrev_b32 v80, 16, v130\n\t" \
    "v_and_b32 v81, 0xffff0000, v130\n\t" \
    "v_lshlrev_b32 v82, 16, v131\n\t" \
    "v_and_b32 v83, 0xffff0000, v131\n\t" \
    "v_add_f32 v148, v148, v80\n\t" \
    "v_add_f32 v149, v149, v81\n\t" \
    "v_add_f32 v150, v150, v82\n\t" \
    "v_add_f32 v151, v151, v83\n\t" \
    "v_lshlrev_b32 v152, 16, v132\n\t" \
    "v_and_b32 v153, 0xffff0000, v132\n\t" \
    "v_lshlrev_b32 v154, 16, v133\n\t" \
    "v_and_b32 v155, 0xffff0000, v133\n\t" \
    "v_lshlrev_b32 v80, 16, v134\n\t" \
    "v_and_b32 v81, 0xffff0000, v134\n\t" \
    "v_lshlrev_b32 v82, 16, v135\n\t" \
    "v_and_b32 v83, 0xffff0000, v135\n\t" \
    "v_add_f32 v152, v152, v80\n\t" \
    "v_add_f32 v153, v153, v81\n\t" \
    "v_add_f32 v154, v154, v82\n\t" \
    "v_add_f32 v155, v155, v83\n\t" \
    "v_lshlrev_b32 v156, 16, v136\n\t" \
    "v_and_b32 v157, 0xffff0000, v136\n\t" \
    "v_lshlrev_b32 v158, 16, v137\n\t" \
    "v_and_b32 v159, 0xffff0000, v137\n\t" \
    "v_lshlrev_b32 v80, 16, v138\n\t" \
    "v_and_b32 v81, 0xffff0000, v138\n\t" \
    "v_lshlrev_b32 v82, 16, v139\n\t" \
    "v_and_b32 v83, 0xffff0000, v139\n\t" \
    "v_add_f32 v156, v156, v80\n\t" \
    "v_add_f32 v157, v157, v81\n\t" \
    "v_add_f32 v158, v158, v82\n\t" \
    "v_add_f32 v159, v159, v83\n\t" \
    "s_mov_b32 s42, s43\n\t" \
    "s_lshr_b32 s48, s40, 1\n\t" \
    "s_add_u32 s48, s48, 1\n\t" \
    "s_mul_i32 s43, s48, 0x14000\n\t" \
    "s_lshr_b32 s44, s48, 4\n\t" \
    "s_mul_i32 s44, s44, 0xc2000\n\t" \
    "s_add_u32 s43, s43, s44\n\t" \
    "s_lshr_b32 s44, s40, 3\n\t" \
    "s_lshl_b32 s44, s44, 14\n\t" \
    "s_and_b32 s45, s40, 7\n\t" \
    "s_lshl_b32 s45, s45, 10\n\t" \
    "s_add_u32 s49, s44, s45\n\t" \
    "s_add_u32 s49, s49, 0x1c0000\n\t" \
    "s_add_u32 s50, s49, %[co]\n\t" \
    "s_add_u32 s51, s42, 0x1400000\n\t" \
    "s_add_u32 s52, s42, 0xc00000\n\t" \
    "s_mov_b32 s44, s48\n\t" \
    "s_mov_b32 s45, s48\n\t" \
    "v_mov_b64 v[76:77], s[44:45]\n\t" \
    "v_mov_b64 v[78:79], s[44:45]\n\t" \
    "s_add_u32 s44, s40, 1\n\t" \
    "v_mov_b32 v98, s44\n\t" \
    LST1 \
    LJIT \
    ".Lf04_%=:\n\t" \
    "s_nop 7\n\t" \
    "ds_read_b32 v80, %[fa]\n\t" \
    "s_waitcnt lgkmcnt(0)\n\t" \
    "v_cmp_ge_u32 vcc, s40, v80\n\t" \
    "s_cbranch_vccnz .Lf04_%=\n\t" \
    LST2 \
    LJIT \
    "ds_read_b128 v[160:163], %[la] offset:0\n\t" \
    "ds_read_b128 v[164:167], %[la] offset:6144\n\t" \
    "ds_read_b128 v[168:171], %[la] offset:12288\n\t" \
    "ds_read_b128 v[172:175], %[la] offset:18432\n\t" \
    "ds_read_b128 v[176:179], %[la] offset:24576\n\t" \
    "ds_read_b128 v[180:183], %[la] offset:1024\n\t" \
    "ds_read_b128 v[184:187], %[la] offset:7168\n\t" \
    "ds_read_b128 v[188:191], %[la] offset:13312\n\t" \
    "ds_read_b128 v[192:195], %[la] offset:19456\n\t" \
    "ds_read_b128 v[196:199], %[la] offset:25600\n\t" \
    "ds_read_b128 v[200:203], %[la] offset:2048\n\t" \
    "ds_read_b128 v[204:207], %[la] offset:8192\n\t" \
    "ds_read_b128 v[208:211], %[la] offset:14336\n\t" \
    "ds_read_b128 v[212:215], %[la] offset:20480\n\t" \
    "ds_read_b128 v[216:219], %[la] offset:26624\n\t" \
    "s_waitcnt lgkmcnt(14)\n\t" \
    "v_pk_add_f32 v[100:101], v[140:141], v[160:161]\n\t" \
    "v_pk_add_f32 v[102:103], v[142:143], v[162:163]\n\t" \
    "ds_read_b128 v[220:223], %[la] offset:3072\n\t" \
    "s_waitcnt lgkmcnt(14)\n\t" \
    "v_pk_add_f32 v[104:105], v[144:145], v[164:165]\n\t" \
    "v_pk_add_f32 v[106:107], v[146:147], v[166:167]\n\t" \
    "ds_read_b128 v[224:227], %[la] offset:9216\n\t" \
    "s_waitcnt lgkmcnt(14)\n\t" \
    "v_pk_add_f32 v[108:109], v[148:149], v[168:169]\n\t" \
    "v_pk_add_f32 v[110:111], v[150:151], v[170:171]\n\t" \
    "ds_read_b128 v[228:231], %[la] offset:15360\n\t" \
    "s_waitcnt lgkmcnt(14)\n\t" \
    "v_pk_add_f32 v[112:113], v[152:153], v[172:173]\n\t" \
    "v_pk_add_f32 v[114:115], v[154:155], v[174:175]\n\t" \
    "ds_read_b128 v[232:235], %[la] offset:21504\n\t" \
    "s_waitcnt lgkmcnt(14)\n\t" \
    "v_pk_add_f32 v[116:117], v[156:157], v[176:177]\n\t" \
    "v_pk_add_f32 v[118:119], v[158:159], v[178:179]\n\t" \
    "ds_read_b128 v[236:239], %[la] offset:27648\n\t" \
    "s_waitcnt lgkmcnt(14)\n\t" \
    "v_pk_add_f32 v[100:101], v[100:101], v[180:181]\n\t" \
    "v_pk_add_f32 v[102:103], v[102:103], v[182:183]\n\t" \
    "ds_read_b128 v[160:163], %[la] offset:4096\n\t" \
    "s_waitcnt lgkmcnt(14)\n\t" \
    "v_pk_add_f32 v[104:105], v[104:105], v[184:185]\n\t" \
    "v_pk_add_f32 v[106:107], v[106:107], v[186:187]\n\t" \
    "ds_read_b128 v[164:167], %[la] offset:10240\n\t" \
    "s_waitcnt lgkmcnt(14)\n\t" \
    "v_pk_add_f32 v[108:109], v[108:109], v[188:189]\n\t" \
    "v_pk_add_f32 v[110:111], v[110:111], v[190:191]\n\t" \
    "ds_read_b128 v[168:171], %[la] offset:16384\n\t" \
    "s_waitcnt lgkmcnt(14)\n\t" \
    "v_pk_add_f32 v[112:113], v[112:113], v[192:193]\n\t" \
    "v_pk_add_f32 v[114:115], v[114:115], v[194:195]\n\t" \
    "ds_read_b128 v[172:175], %[la] offset:22528\n\t" \
    "s_waitcnt lgkmcnt(14)\n\t" \
    "v_pk_add_f32 v[116:117], v[116:117], v[196:197]\n\t" \
    "v_pk_add_f32 v[118:119], v[118:119], v[198:199]\n\t" \
    "ds_read_b128 v[176:179], %[la] offset:28672\n\t" \
    "s_waitcnt lgkmcnt(14)\n\t" \
    "v_pk_add_f32 v[100:101], v[100:101], v[200:201]\n\t" \
    "v_pk_add_f32 v[102:103], v[102:103], v[202:203]\n\t" \
    "ds_read_b128 v[180:183], %[la] offset:5120\n\t" \
    "s_waitcnt lgkmcnt(14)\n\t" \
    "v_pk_add_f32 v[104:105], v[104:105], v[204:205]\n\t" \
    "v_pk_add_f32 v[106:107], v[106:107], v[206:207]\n\t" \
    "ds_read_b128 v[184:187], %[la] offset:11264\n\t" \
    "s_waitcnt lgkmcnt(14)\n\t" \
    "v_pk_add_f32 v[108:109], v[108:109], v[208:209]\n\t" \
    "v_pk_add_f32 v[110:111], v[110:111], v[210:211]\n\t" \
    "ds_read_b128 v[188:191], %[la] offset:17408\n\t" \
    "s_waitcnt lgkmcnt(14)\n\t" \
    "v_pk_add_f32 v[112:113], v[112:113], v[212:213]\n\t" \
    "v_pk_add_f32 v[114:115], v[114:115], v[214:215]\n\t" \
    "ds_read_b128 v[192:195], %[la] offset:23552\n\t" \
    "s_waitcnt lgkmcnt(14)\n\t" \
    "v_pk_add_f32 v[116:117], v[116:117], v[216:217]\n\t" \
    "v_pk_add_f32 v[118:119], v[118:119], v[218:219]\n\t" \
    "ds_read_b128 v[196:199], %[la] offset:29696\n\t" \
    "s_waitcnt lgkmcnt(14)\n\t" \
    "v_pk_add_f32 v[100:101], v[100:101], v[220:221]\n\t" \
    "v_pk_add_f32 v[102:103], v[102:103], v[222:223]\n\t" \
    "s_waitcnt lgkmcnt(13)\n\t" \
    "v_pk_add_f32 v[104:105], v[104:105], v[224:225]\n\t" \
    "v_pk_add_f32 v[106:107], v[106:107], v[226:227]\n\t" \
    "s_waitcnt lgkmcnt(12)\n\t" \
    "v_pk_add_f32 v[108:109], v[108:109], v[228:229]\n\t" \
    "v_pk_add_f32 v[110:111], v[110:111], v[230:231]\n\t" \
    "s_waitcnt lgkmcnt(11)\n\t" \
    "v_pk_add_f32 v[112:113], v[112:113], v[232:233]\n\t" \
    "v_pk_add_f32 v[114:115], v[114:115], v[234:235]\n\t" \
    "s_waitcnt lgkmcnt(10)\n\t" \
    "v_pk_add_f32 v[116:117], v[116:117], v[236:237]\n\t" \
    "v_pk_add_f32 v[118:119], v[118:119], v[238:239]\n\t" \
    "s_waitcnt lgkmcnt(9)\n\t" \
    "v_pk_add_f32 v[100:101], v[100:101], v[160:161]\n\t" \
    "v_pk_add_f32 v[102:103], v[102:103], v[162:163]\n\t" \
    "s_waitcnt lgkmcnt(8)\n\t" \
    "v_pk_add_f32 v[104:105], v[104:105], v[164:165]\n\t" \
    "v_pk_add_f32 v[106:107], v[106:107], v[166:167]\n\t" \
    "s_waitcnt lgkmcnt(7)\n\t" \
    "v_pk_add_f32 v[108:109], v[108:109], v[168:169]\n\t" \
    "v_pk_add_f32 v[110:111], v[110:111], v[170:171]\n\t" \
    "s_waitcnt lgkmcnt(6)\n\t" \
    "v_pk_add_f32 v[112:113], v[112:113], v[172:173]\n\t" \
    "v_pk_add_f32 v[114:115], v[114:115], v[174:175]\n\t" \
    "s_waitcnt lgkmcnt(5)\n\t" \
    "v_pk_add_f32 v[116:117], v[116:117], v[176:177]\n\t" \
    "v_pk_add_f32 v[118:119], v[118:119], v[178:179]\n\t" \
    "s_waitcnt lgkmcnt(4)\n\t" \
    "v_pk_add_f32 v[100:101], v[100:101], v[180:181]\n\t" \
    "v_pk_add_f32 v[102:103], v[102:103], v[182:183]\n\t" \
    "s_waitcnt lgkmcnt(3)\n\t" \
    "v_pk_add_f32 v[104:105], v[104:105], v[184:185]\n\t" \
    "v_pk_add_f32 v[106:107], v[106:107], v[186:187]\n\t" \
    "s_waitcnt lgkmcnt(2)\n\t" \
    "v_pk_add_f32 v[108:109], v[108:109], v[188:189]\n\t" \
    "v_pk_add_f32 v[110:111], v[110:111], v[190:191]\n\t" \
    "s_waitcnt lgkmcnt(1)\n\t" \
    "v_pk_add_f32 v[112:113], v[112:113], v[192:193]\n\t" \
    "v_pk_add_f32 v[114:115], v[114:115], v[194:195]\n\t" \
    "s_waitcnt lgkmcnt(0)\n\t" \
    "v_pk_add_f32 v[116:117], v[116:117], v[196:197]\n\t" \
    "v_pk_add_f32 v[118:119], v[118:119], v[198:199]\n\t" \
    "ds_write_b32 %[fc], v98\n\t" \
    "v_sub_u32 %[la], %[l2], %[la]\n\t" \
    "v_cvt_pk_bf16_f32 v84, v100, v101\n\t" \
    "v_cvt_pk_bf16_f32 v85, v102, v103\n\t" \
    "v_cvt_pk_bf16_f32 v86, v104, v105\n\t" \
    "v_cvt_pk_bf16_f32 v87, v106, v107\n\t" \
    "v_cvt_pk_bf16_f32 v88, v108, v109\n\t" \
    "v_cvt_pk_bf16_f32 v89, v110, v111\n\t" \
    "v_cvt_pk_bf16_f32 v90, v112, v113\n\t" \
    "v_cvt_pk_bf16_f32 v91, v114, v115\n\t" \
    "v_cvt_pk_bf16_f32 v92, v116, v117\n\t" \
    "v_cvt_pk_bf16_f32 v93, v118, v119\n\t" \
    "v_mov_b32 v80, 0\n\t" \
    "v_mov_b32 v82, 0\n\t" \
    "v_dot2c_f32_bf16 v80, v84, v99\n\t" \
    "v_dot2c_f32_bf16 v82, v84, v84\n\t" \
    "v_dot2c_f32_bf16 v80, v85, v99\n\t" \
    "v_dot2c_f32_bf16 v82, v85, v85\n\t" \
    "v_dot2c_f32_bf16 v80, v86, v99\n\t" \
    "v_dot2c_f32_bf16 v82, v86, v86\n\t" \
    "v_dot2c_f32_bf16 v80, v87, v99\n\t" \
    "v_dot2c_f32_bf16 v82, v87, v87\n\t" \
    "v_dot2c_f32_bf16 v80, v88, v99\n\t" \
    "v_dot2c_f32_bf16 v82, v88, v88\n\t" \
    "v_dot2c_f32_bf16 v80, v89, v99\n\t" \
    "v_dot2c_f32_bf16 v82, v89, v89\n\t" \
    "v_dot2c_f32_bf16 v80, v90, v99\n\t" \
    "v_dot2c_f32_bf16 v82, v90, v90\n\t" \
    "v_dot2c_f32_bf16 v80, v91, v99\n\t" \
    "v_dot2c_f32_bf16 v82, v91, v91\n\t" \
    "v_dot2c_f32_bf16 v80, v92, v99\n\t" \
    "v_dot2c_f32_bf16 v82, v92, v92\n\t" \
    "v_dot2c_f32_bf16 v80, v93, v99\n\t" \
    "v_dot2c_f32_bf16 v82, v93, v93\n\t" \
    "s_nop 2\n\t" \
    "v_mov_b32 v81, v80\n\t" \
    "v_mov_b32 v83, v82\n\t" \
    "s_nop 1\n\t" \
    "v_permlane16_swap_b32 v80, v81\n\t" \
    "v_permlane16_swap_b32 v82, v83\n\t" \
    "v_add_f32 v80, v80, v81\n\t" \
    "v_add_f32 v82, v82, v83\n\t" \
    "v_mov_b32 v81, v80\n\t" \
    "v_mov_b32 v83, v82\n\t" \
    "s_nop 1\n\t" \
    "v_permlane32_swap_b32 v80, v81\n\t" \
    "v_permlane32_swap_b32 v82, v83\n\t" \
    "v_add_f32 v80, v80, v81\n\t" \
    "v_add_f32 v82, v82, v83\n\t" \
    "v_min_u32 v80, -2, v80\n\t" \
    "v_min_u32 v81, -2, v82\n\t" \
    "s_mov_b64 exec, 0xffff\n\t" \
    "buffer_store_dwordx2 v[80:81], %[l8], %[rs], s50 offen sc1\n\t" \
    "s_mov_b64 exec, -1\n\t" \
    LST3 \
    LJIT \
    "s_add_u32 s46, s43, 0x2c00000\n\t" \
    "s_add_u32 s47, s43, 0x1c00000\n\t" \
    "buffer_load_dwordx2 v[120:121], %[t0], %[rs], s46 offen sc1\n\t" \
    "buffer_load_dwordx2 v[122:123], %[t0], %[rs], s47 offen sc1\n\t" \
    "buffer_load_dwordx2 v[124:125], %[t1], %[rs], s46 offen sc1\n\t" \
    "buffer_load_dwordx2 v[126:127], %[t1], %[rs], s47 offen sc1\n\t" \
    "buffer_load_dwordx2 v[128:129], %[t2], %[rs], s46 offen sc1\n\t" \
    "buffer_load_dwordx2 v[130:131], %[t2], %[rs], s47 offen sc1\n\t" \
    "buffer_load_dwordx2 v[132:133], %[t3], %[rs], s46 offen sc1\n\t" \
    "buffer_load_dwordx2 v[134:135], %[t3], %[rs], s47 offen sc1\n\t" \
    "buffer_load_dwordx2 v[136:137], %[t4], %[rs], s46 offen sc1\n\t" \
    "buffer_load_dwordx2 v[138:139], %[t4], %[rs], s47 offen sc1\n\t" \
    "s_nop 0\n\t" \
    "v_permlane16_swap_b32 v84, v86\n\t" \
    "v_permlane16_swap_b32 v85, v87\n\t" \
    "v_permlane16_swap_b32 v88, v90\n\t" \
    "v_permlane16_swap_b32 v89, v91\n\t" \
    "s_sleep 4\n\t" \
    ".Lf05_%=:\n\t" \
    "buffer_load_dwordx2 v[94:95], %[l8], %[rs], s49 offen sc1\n\t" \
    "buffer_load_dwordx2 v[96:97], %[l8], %[rs], s49 offen offset:512 sc1\n\t" \
    "s_waitcnt vmcnt(0)\n\t" \
    "v_max_u32 v80, v94, v95\n\t" \
    "v_max3_u32 v80, v96, v97, v80\n\t" \
    "v_cmp_eq_u32 vcc, -1, v80\n\t" \
    "s_cbranch_vccz .Lf06_%=\n\t" \
    "s_cmp_eq_u32 s41, 0\n\t" \
    "s_cbranch_scc1 .Lf06_%=\n\t" \
    "s_sub_u32 s41, s41, 1\n\t" \
    "s_branch .Lf05_%=\n\t" \
    ".Lf06_%=:\n\t" \
    LST4 \
    LJIT \
    "v_add_f32 v80, v94, v96\n\t" \
    "v_add_f32 v82, v95, v97\n\t" \
    "v_mov_b32 v81, v80\n\t" \
    "v_mov_b32 v83, v82\n\t" \
    "s_nop 1\n\t" \
    "v_permlane16_swap_b32 v80, v81\n\t" \
    "v_permlane16_swap_b32 v82, v83\n\t" \
    "v_add_f32 v80, v80, v81\n\t" \
    "v_add_f32 v82, v82, v83\n\t" \
    "v_mov_b32 v81, v80\n\t" \
    "v_mov_b32 v83, v82\n\t" \
    "s_nop 1\n\t" \
    "v_permlane32_swap_b32 v80, v81\n\t" \
    "v_permlane32_swap_b32 v82, v83\n\t" \
    "v_add_f32 v80, v80, v81\n\t" \
    "v_add_f32 v82, v82, v83\n\t" \
    "v_mul_f32 v80, 0x3acccccd, v80\n\t" \
    "v_mul_f32 v82, 0x3acccccd, v82\n\t" \
    "v_fma_f32 v82, -v80, v80, v82\n\t" \
    "v_cvt_pk_bf16_f32 v80, v80, v80\n\t" \
    "v_and_b32 v80, 0xffff0000, v80\n\t" \
    "v_max_f32 v82, 0, v82\n\t" \
    "v_cvt_pk_bf16_f32 v82, v82, v82\n\t" \
    "v_and_b32 v82, 0xffff0000, v82\n\t" \
    "v_add_f32 v82, 0x3727c5ac, v82\n\t" \
    "v_cvt_pk_bf16_f32 v82, v82, v82\n\t" \
    "v_and_b32 v82, 0xffff0000, v82\n\t" \
    "v_sqrt_f32 v82, v82\n\t" \
    "s_nop 0\n\t" \
    "v_cvt_pk_bf16_f32 v82, v82, v82\n\t" \
    "v_and_b32 v82, 0xffff0000, v82\n\t" \
    "v_rcp_f32 v82, v82\n\t" \
    "s_nop 0\n\t" \
    "v_mul_f32 v80, -v80, v82\n\t" \
    "v_fma_f32 v100, v100, v82, v80\n\t" \
    "v_fma_f32 v101, v101, v82, v80\n\t" \
    "v_fma_f32 v102, v102, v82, v80\n\t" \
    "v_fma_f32 v103, v103, v82, v80\n\t" \
    "v_fma_f32 v104, v104, v82, v80\n\t" \
    "v_fma_f32 v105, v105, v82, v80\n\t" \
    "v_fma_f32 v106, v106, v82, v80\n\t" \
    "v_fma_f32 v107, v107, v82, v80\n\t" \
    "v_fma_f32 v108, v108, v82, v80\n\t" \
    "v_fma_f32 v109, v109, v82, v80\n\t" \
    "v_fma_f32 v110, v110, v82, v80\n\t" \
    "v_fma_f32 v111, v111, v82, v80\n\t" \
    "v_fma_f32 v112, v112, v82, v80\n\t" \
    "v_fma_f32 v113, v113, v82, v80\n\t" \
    "v_fma_f32 v114, v114, v82, v80\n\t" \
    "v_fma_f32 v115, v115, v82, v80\n\t" \
    "v_fma_f32 v116, v116, v82, v80\n\t" \
    "v_fma_f32 v117, v117, v82, v80\n\t" \
    "v_fma_f32 v118, v118, v82, v80\n\t" \
    "v_fma_f32 v119, v119, v82, v80\n\t" \
    "v_cvt_pk_bf16_f32 v64, v100, v101\n\t" \
    "v_cvt_pk_bf16_f32 v65, v102, v103\n\t" \
    "v_cvt_pk_bf16_f32 v66, v104, v105\n\t" \
    "v_cvt_pk_bf16_f32 v67, v106, v107\n\t" \
    "v_cvt_pk_bf16_f32 v68, v108, v109\n\t" \
    "v_cvt_pk_bf16_f32 v69, v110, v111\n\t" \
    "v_cvt_pk_bf16_f32 v70, v112, v113\n\t" \
    "v_cvt_pk_bf16_f32 v71, v114, v115\n\t" \
    "v_cvt_pk_bf16_f32 v72, v116, v117\n\t" \
    "v_cvt_pk_bf16_f32 v73, v118, v119\n\t" \
    "s_nop 0\n\t" \
    "v_permlane16_swap_b32 v64, v66\n\t" \
    "v_permlane16_swap_b32 v65, v67\n\t" \
    "v_permlane16_swap_b32 v68, v70\n\t" \
    "v_permlane16_swap_b32 v69, v71\n\t" \
    "buffer_store_dwordx4 v[64:67], %[o16], %[rn], s51 offen sc1\n\t" \
    "buffer_store_dwordx4 v[68:71], %[o17], %[rn], s51 offen sc1\n\t" \
    "buffer_store_dwordx2 v[72:73], %[t4], %[rn], s51 offen sc1\n\t" \
    "buffer_store_dwordx4 v[84:87], %[o16], %[rn], s52 offen sc1\n\t" \
    "buffer_store_dwordx4 v[88:91], %[o17], %[rn], s52 offen sc1\n\t" \
    "buffer_store_dwordx2 v[92:93], %[t4], %[rn], s52 offen sc1\n\t" \
    "s_waitcnt vmcnt(0)\n\t" \
    "buffer_store_dwordx4 v[76:79], %[fo], %[rn], %[fs] offen sc1\n\t" \
    LST5 \
    LJIT \
    LFLUSHF \
    "s_add_u32 s40, s40, 2\n\t" \
    "s_cmp_lt_u32 s40, 128\n\t" \
    "s_cbranch_scc1 .Lf01_%=\n\t" \
    "s_branch .Lend%=\n\t" \
    ".Lend%=:\n\t" \
    "s_waitcnt vmcnt(0) lgkmcnt(0)\n\t" \
    "s_mov_b32 %[bud], s41\n\t"
#define ASM_W4_FIN1 \
    "s_mov_b32 s40, %[mt]\n\t" \
    "s_mov_b32 s41, 0x40000\n\t" \
    LSTINIT \
    "s_mov_b32 s43, 0\n\t" \
    "v_mov_b32 v99, 0x3f803f80\n\t" \
    "s_setprio 3\n\t" \
    "s_add_u32 s46, s43, 0x2c00000\n\t" \
    "s_add_u32 s47, s43, 0x1c00000\n\t" \
    "buffer_load_dwordx2 v[120:121], %[t0], %[rs], s46 offen sc1\n\t" \
    "buffer_load_dwordx2 v[122:123], %[t0], %[rs], s47 offen sc1\n\t" \
    "buffer_load_dwordx2 v[124:125], %[t1], %[rs], s46 offen sc1\n\t" \
    "buffer_load_dwordx2 v[126:127], %[t1], %[rs], s47 offen sc1\n\t" \
    "buffer_load_dwordx2 v[128:129], %[t2], %[rs], s46 offen sc1\n\t" \
    "buffer_load_dwordx2 v[130:131], %[t2], %[rs], s47 offen sc1\n\t" \
    "buffer_load_dwordx2 v[132:133], %[t3], %[rs], s46 offen sc1\n\t" \
    "buffer_load_dwordx2 v[134:135], %[t3], %[rs], s47 offen sc1\n\t" \
    "buffer_load_dwordx2 v[136:137], %[t4], %[rs], s46 offen sc1\n\t" \
    "buffer_load_dwordx2 v[138:139], %[t4], %[rs], s47 offen sc1\n\t" \
    ".Lf11_%=:\n\t" \
    LST0 \
    LJIT \
    ".Lf12_%=:\n\t" \
    "s_waitcnt vmcnt(0)\n\t" \
    "v_max_u32 v80, v120, v121\n\t" \
    "v_max3_u32 v80, v122, v123, v80\n\t" \
    "v_max3_u32 v80, v124, v125, v80\n\t" \
    "v_max3_u32 v80, v126, v127, v80\n\t" \
    "v_max3_u32 v80, v128, v129, v80\n\t" \
    "v_max3_u32 v80, v130, v131, v80\n\t" \
    "v_max3_u32 v80, v132, v133, v80\n\t" \
    "v_max3_u32 v80, v134, v135, v80\n\t" \
    "v_max3_u32 v80, v136, v137, v80\n\t" \
    "v_max3_u32 v80, v138, v139, v80\n\t" \
    "v_cmp_eq_u32 vcc, -1, v80\n\t" \
    "s_cbranch_vccz .Lf13_%=\n\t" \
    "s_cmp_eq_u32 s41, 0\n\t" \
    "s_cbranch_scc1 .Lf13_%=\n\t" \
    "s_sub_u32 s41, s41, 1\n\t" \
    "s_sleep 8\n\t" \
    "s_add_u32 s46, s43, 0x2c00000\n\t" \
    "s_add_u32 s47, s43, 0x1c00000\n\t" \
    "buffer_load_dwordx2 v[120:121], %[t0], %[rs], s46 offen sc1\n\t" \
    "buffer_load_dwordx2 v[122:123], %[t0], %[rs], s47 offen sc1\n\t" \
    "buffer_load_dwordx2 v[124:125], %[t1], %[rs], s46 offen sc1\n\t" \
    "buffer_load_dwordx2 v[126:127], %[t1], %[rs], s47 offen sc1\n\t" \
    "buffer_load_dwordx2 v[128:129], %[t2], %[rs], s46 offen sc1\n\t" \
    "buffer_load_dwordx2 v[130:131], %[t2], %[rs], s47 offen sc1\n\t" \
    "buffer_load_dwordx2 v[132:133], %[t3], %[rs], s46 offen sc1\n\t" \
    "buffer_load_dwordx2 v[134:135], %[t3], %[rs], s47 offen sc1\n\t" \
    "buffer_load_dwordx2 v[136:137], %[t4], %[rs], s46 offen sc1\n\t" \
    "buffer_load_dwordx2 v[138:139], %[t4], %[rs], s47 offen sc1\n\t" \
    "s_waitcnt vmcnt(0)\n\t" \
    "s_branch .Lf12_%=\n\t" \
    ".Lf13_%=:\n\t" \
    "v_lshlrev_b32 v140, 16, v120\n\t" \
    "v_and_b32 v141, 0xffff0000, v120\n\t" \
    "v_lshlrev_b32 v142, 16, v121\n\t" \
    "v_and_b32 v143, 0xffff0000, v121\n\t" \
    "v_lshlrev_b32 v80, 16, v122\n\t" \
    "v_and_b32 v81, 0xffff0000, v122\n\t" \
    "v_lshlrev_b32 v82, 16, v123\n\t" \
    "v_and_b32 v83, 0xffff0000, v123\n\t" \
    "v_add_f32 v140, v140, v80\n\t" \
    "v_add_f32 v141, v141, v81\n\t" \
    "v_add_f32 v142, v142, v82\n\t" \
    "v_add_f32 v143, v143, v83\n\t" \
    "v_lshlrev_b32 v144, 16, v124\n\t" \
    "v_and_b32 v145, 0xffff0000, v124\n\t" \
    "v_lshlrev_b32 v146, 16, v125\n\t" \
    "v_and_b32 v147, 0xffff0000, v125\n\t" \
    "v_lshlrev_b32 v80, 16, v126\n\t" \
    "v_and_b32 v81, 0xffff0000, v126\n\t" \
    "v_lshlrev_b32 v82, 16, v127\n\t" \
    "v_and_b32 v83, 0xffff0000, v127\n\t" \
    "v_add_f32 v144, v144, v80\n\t" \
    "v_add_f32 v145, v145, v81\n\t" \
    "v_add_f32 v146, v146, v82\n\t" \
    "v_add_f32 v147, v147, v83\n\t" \
    "v_lshlrev_b32 v148, 16, v128\n\t" \
    "v_and_b32 v149, 0xffff0000, v128\n\t" \
    "v_lshlrev_b32 v150, 16, v129\n\t" \
    "v_and_b32 v151, 0xffff0000, v129\n\t" \
    "v_lshlrev_b32 v80, 16, v130\n\t" \
    "v_and_b32 v81, 0xffff0000, v130\n\t" \
    "v_lshlrev_b32 v82, 16, v131\n\t" \
    "v_and_b32 v83, 0xffff0000, v131\n\t" \
    "v_add_f32 v148, v148, v80\n\t" \
    "v_add_f32 v149, v149, v81\n\t" \
    "v_add_f32 v150, v150, v82\n\t" \
    "v_add_f32 v151, v151, v83\n\t" \
    "v_lshlrev_b32 v152, 16, v132\n\t" \
    "v_and_b32 v153, 0xffff0000, v132\n\t" \
    "v_lshlrev_b32 v154, 16, v133\n\t" \
    "v_and_b32 v155, 0xffff0000, v133\n\t" \
    "v_lshlrev_b32 v80, 16, v134\n\t" \
    "v_and_b32 v81, 0xffff0000, v134\n\t" \
    "v_lshlrev_b32 v82, 16, v135\n\t" \
    "v_and_b32 v83, 0xffff0000, v135\n\t" \
    "v_add_f32 v152, v152, v80\n\t" \
    "v_add_f32 v153, v153, v81\n\t" \
    "v_add_f32 v154, v154, v82\n\t" \
    "v_add_f32 v155, v155, v83\n\t" \
    "v_lshlrev_b32 v156, 16, v136\n\t" \
    "v_and_b32 v157, 0xffff0000, v136\n\t" \
    "v_lshlrev_b32 v158, 16, v137\n\t" \
    "v_and_b32 v159, 0xffff0000, v137\n\t" \
    "v_lshlrev_b32 v80, 16, v138\n\t" \
    "v_and_b32 v81, 0xffff0000, v138\n\t" \
    "v_lshlrev_b32 v82, 16, v139\n\t" \
    "v_and_b32 v83, 0xffff0000, v139\n\t" \
    "v_add_f32 v156, v156, v80\n\t" \
    "v_add_f32 v157, v157, v81\n\t" \
    "v_add_f32 v158, v158, v82\n\t" \
    "v_add_f32 v159, v159, v83\n\t" \
    "s_mov_b32 s42, s43\n\t" \
    "s_lshr_b32 s48, s40, 1\n\t" \
    "s_add_u32 s48, s48, 1\n\t" \
    "s_mul_i32 s43, s48, 0x14000\n\t" \
    "s_lshr_b32 s44, s48, 4\n\t" \
    "s_mul_i32 s44, s44, 0xc2000\n\t" \
    "s_add_u32 s43, s43, s44\n\t" \
    "s_mul_i32 s49, s40, 20480\n\t" \
    "s_add_u32 s49, s49, %[co]\n\t" \
    "s_add_u32 s44, s40, 1\n\t" \
    "v_mov_b32 v98, s44\n\t" \
    LST1 \
    LJIT \
    ".Lf14_%=:\n\t" \
    "s_nop 7\n\t" \
    "ds_read_b32 v80, %[fa]\n\t" \
    "s_waitcnt lgkmcnt(0)\n\t" \
    "v_cmp_ge_u32 vcc, s40, v80\n\t" \
    "s_cbranch_vccnz .Lf14_%=\n\t" \
    LST2 \
    LJIT \
    "ds_read_b128 v[160:163], %[la] offset:0\n\t" \
    "ds_read_b128 v[164:167], %[la] offset:6144\n\t" \
    "ds_read_b128 v[168:171], %[la] offset:12288\n\t" \
    "ds_read_b128 v[172:175], %[la] offset:18432\n\t" \
    "ds_read_b128 v[176:179], %[la] offset:24576\n\t" \
    "ds_read_b128 v[180:183], %[la] offset:1024\n\t" \
    "ds_read_b128 v[184:187], %[la] offset:7168\n\t" \
    "ds_read_b128 v[188:191], %[la] offset:13312\n\t" \
    "ds_read_b128 v[192:195], %[la] offset:19456\n\t" \
    "ds_read_b128 v[196:199], %[la] offset:25600\n\t" \
    "ds_read_b128 v[200:203], %[la] offset:2048\n\t" \
    "ds_read_b128 v[204:207], %[la] offset:8192\n\t" \
    "ds_read_b128 v[208:211], %[la] offset:14336\n\t" \
    "ds_read_b128 v[212:215], %[la] offset:20480\n\t" \
    "ds_read_b128 v[216:219], %[la] offset:26624\n\t" \
    "s_waitcnt lgkmcnt(14)\n\t" \
    "v_pk_add_f32 v[100:101], v[140:141], v[160:161]\n\t" \
    "v_pk_add_f32 v[102:103], v[142:143], v[162:163]\n\t" \
    "ds_read_b128 v[220:223], %[la] offset:3072\n\t" \
    "s_waitcnt lgkmcnt(14)\n\t" \
    "v_pk_add_f32 v[104:105], v[144:145], v[164:165]\n\t" \
    "v_pk_add_f32 v[106:107], v[146:147], v[166:167]\n\t" \
    "ds_read_b128 v[224:227], %[la] offset:9216\n\t" \
    "s_waitcnt lgkmcnt(14)\n\t" \
    "v_pk_add_f32 v[108:109], v[148:149], v[168:169]\n\t" \
    "v_pk_add_f32 v[110:111], v[150:151], v[170:171]\n\t" \
    "ds_read_b128 v[228:231], %[la] offset:15360\n\t" \
    "s_waitcnt lgkmcnt(14)\n\t" \
    "v_pk_add_f32 v[112:113], v[152:153], v[172:173]\n\t" \
    "v_pk_add_f32 v[114:115], v[154:155], v[174:175]\n\t" \
    "ds_read_b128 v[232:235], %[la] offset:21504\n\t" \
    "s_waitcnt lgkmcnt(14)\n\t" \
    "v_pk_add_f32 v[116:117], v[156:157], v[176:177]\n\t" \
    "v_pk_add_f32 v[118:119], v[158:159], v[178:179]\n\t" \
    "ds_read_b128 v[236:239], %[la] offset:27648\n\t" \
    "s_waitcnt lgkmcnt(14)\n\t" \
    "v_pk_add_f32 v[100:101], v[100:101], v[180:181]\n\t" \
    "v_pk_add_f32 v[102:103], v[102:103], v[182:183]\n\t" \
    "ds_read_b128 v[160:163], %[la] offset:4096\n\t" \
    "s_waitcnt lgkmcnt(14)\n\t" \
    "v_pk_add_f32 v[104:105], v[104:105], v[184:185]\n\t" \
    "v_pk_add_f32 v[106:107], v[106:107], v[186:187]\n\t" \
    "ds_read_b128 v[164:167], %[la] offset:10240\n\t" \
    "s_waitcnt lgkmcnt(14)\n\t" \
    "v_pk_add_f32 v[108:109], v[108:109], v[188:189]\n\t" \
    "v_pk_add_f32 v[110:111], v[110:111], v[190:191]\n\t" \
    "ds_read_b128 v[168:171], %[la] offset:16384\n\t" \
    "s_waitcnt lgkmcnt(14)\n\t" \
    "v_pk_add_f32 v[112:113], v[112:113], v[192:193]\n\t" \
    "v_pk_add_f32 v[114:115], v[114:115], v[194:195]\n\t" \
    "ds_read_b128 v[172:175], %[la] offset:22528\n\t" \
    "s_waitcnt lgkmcnt(14)\n\t" \
    "v_pk_add_f32 v[116:117], v[116:117], v[196:197]\n\t" \
    "v_pk_add_f32 v[118:119], v[118:119], v[198:199]\n\t" \
    "ds_read_b128 v[176:179], %[la] offset:28672\n\t" \
    "s_waitcnt lgkmcnt(14)\n\t" \
    "v_pk_add_f32 v[100:101], v[100:101], v[200:201]\n\t" \
    "v_pk_add_f32 v[102:103], v[102:103], v[202:203]\n\t" \
    "ds_read_b128 v[180:183], %[la] offset:5120\n\t" \
    "s_waitcnt lgkmcnt(14)\n\t" \
    "v_pk_add_f32 v[104:105], v[104:105], v[204:205]\n\t" \
    "v_pk_add_f32 v[106:107], v[106:107], v[206:207]\n\t" \
    "ds_read_b128 v[184:187], %[la] offset:11264\n\t" \
    "s_waitcnt lgkmcnt(14)\n\t" \
    "v_pk_add_f32 v[108:109], v[108:109], v[208:209]\n\t" \
    "v_pk_add_f32 v[110:111], v[110:111], v[210:211]\n\t" \
    "ds_read_b128 v[188:191], %[la] offset:17408\n\t" \
    "s_waitcnt lgkmcnt(14)\n\t" \
    "v_pk_add_f32 v[112:113], v[112:113], v[212:213]\n\t" \
    "v_pk_add_f32 v[114:115], v[114:115], v[214:215]\n\t" \
    "ds_read_b128 v[192:195], %[la] offset:23552\n\t" \
    "s_waitcnt lgkmcnt(14)\n\t" \
    "v_pk_add_f32 v[116:117], v[116:117], v[216:217]\n\t" \
    "v_pk_add_f32 v[118:119], v[118:119], v[218:219]\n\t" \
    "ds_read_b128 v[196:199], %[la] offset:29696\n\t" \
    "s_waitcnt lgkmcnt(14)\n\t" \
    "v_pk_add_f32 v[100:101], v[100:101], v[220:221]\n\t" \
    "v_pk_add_f32 v[102:103], v[102:103], v[222:223]\n\t" \
    "s_waitcnt lgkmcnt(13)\n\t" \
    "v_pk_add_f32 v[104:105], v[104:105], v[224:225]\n\t" \
    "v_pk_add_f32 v[106:107], v[106:107], v[226:227]\n\t" \
    "s_waitcnt lgkmcnt(12)\n\t" \
    "v_pk_add_f32 v[108:109], v[108:109], v[228:229]\n\t" \
    "v_pk_add_f32 v[110:111], v[110:111], v[230:231]\n\t" \
    "s_waitcnt lgkmcnt(11)\n\t" \
    "v_pk_add_f32 v[112:113], v[112:113], v[232:233]\n\t" \
    "v_pk_add_f32 v[114:115], v[114:115], v[234:235]\n\t" \
    "s_waitcnt lgkmcnt(10)\n\t" \
    "v_pk_add_f32 v[116:117], v[116:117], v[236:237]\n\t" \
    "v_pk_add_f32 v[118:119], v[118:119], v[238:239]\n\t" \
    "s_waitcnt lgkmcnt(9)\n\t" \
    "v_pk_add_f32 v[100:101], v[100:101], v[160:161]\n\t" \
    "v_pk_add_f32 v[102:103], v[102:103], v[162:163]\n\t" \
    "s_waitcnt lgkmcnt(8)\n\t" \
    "v_pk_add_f32 v[104:105], v[104:105], v[164:165]\n\t" \
    "v_pk_add_f32 v[106:107], v[106:107], v[166:167]\n\t" \
    "s_waitcnt lgkmcnt(7)\n\t" \
    "v_pk_add_f32 v[108:109], v[108:109], v[168:169]\n\t" \
    "v_pk_add_f32 v[110:111], v[110:111], v[170:171]\n\t" \
    "s_waitcnt lgkmcnt(6)\n\t" \
    "v_pk_add_f32 v[112:113], v[112:113], v[172:173]\n\t" \
    "v_pk_add_f32 v[114:115], v[114:115], v[174:175]\n\t" \
    "s_waitcnt lgkmcnt(5)\n\t" \
    "v_pk_add_f32 v[116:117], v[116:117], v[176:177]\n\t" \
    "v_pk_add_f32 v[118:119], v[118:119], v[178:179]\n\t" \
    "s_waitcnt lgkmcnt(4)\n\t" \
    "v_pk_add_f32 v[100:101], v[100:101], v[180:181]\n\t" \
    "v_pk_add_f32 v[102:103], v[102:103], v[182:183]\n\t" \
    "s_waitcnt lgkmcnt(3)\n\t" \
    "v_pk_add_f32 v[104:105], v[104:105], v[184:185]\n\t" \
    "v_pk_add_f32 v[106:107], v[106:107], v[186:187]\n\t" \
    "s_waitcnt lgkmcnt(2)\n\t" \
    "v_pk_add_f32 v[108:109], v[108:109], v[188:189]\n\t" \
    "v_pk_add_f32 v[110:111], v[110:111], v[190:191]\n\t" \
    "s_waitcnt lgkmcnt(1)\n\t" \
    "v_pk_add_f32 v[112:113], v[112:113], v[192:193]\n\t" \
    "v_pk_add_f32 v[114:115], v[114:115], v[194:195]\n\t" \
    "s_waitcnt lgkmcnt(0)\n\t" \
    "v_pk_add_f32 v[116:117], v[116:117], v[196:197]\n\t" \
    "v_pk_add_f32 v[118:119], v[118:119], v[198:199]\n\t" \
    "ds_write_b32 %[fc], v98\n\t" \
    "v_sub_u32 %[la], %[l2], %[la]\n\t" \
    "v_cvt_pk_bf16_f32 v84, v100, v101\n\t" \
    "v_cvt_pk_bf16_f32 v85, v102, v103\n\t" \
    "v_cvt_pk_bf16_f32 v86, v104, v105\n\t" \
    "v_cvt_pk_bf16_f32 v87, v106, v107\n\t" \
    "v_cvt_pk_bf16_f32 v88, v108, v109\n\t" \
    "v_cvt_pk_bf16_f32 v89, v110, v111\n\t" \
    "v_cvt_pk_bf16_f32 v90, v112, v113\n\t" \
    "v_cvt_pk_bf16_f32 v91, v114, v115\n\t" \
    "v_cvt_pk_bf16_f32 v92, v116, v117\n\t" \
    "v_cvt_pk_bf16_f32 v93, v118, v119\n\t" \
    "s_nop 0\n\t" \
    "v_permlane16_swap_b32 v84, v86\n\t" \
    "v_permlane16_swap_b32 v85, v87\n\t" \
    "v_permlane16_swap_b32 v88, v90\n\t" \
    "v_permlane16_swap_b32 v89, v91\n\t" \
    "buffer_store_dwordx4 v[84:87], %[q16], %[ro], s49 offen\n\t" \
    "buffer_store_dwordx4 v[88:91], %[q16], %[ro], s49 offen offset:64\n\t" \
    "buffer_store_dwordx2 v[92:93], %[q8], %[ro], s49 offen offset:128\n\t" \
    "s_add_u32 s46, s43, 0x2c00000\n\t" \
    "s_add_u32 s47, s43, 0x1c00000\n\t" \
    "buffer_load_dwordx2 v[120:121], %[t0], %[rs], s46 offen sc1\n\t" \
    "buffer_load_dwordx2 v[122:123], %[t0], %[rs], s47 offen sc1\n\t" \
    "buffer_load_dwordx2 v[124:125], %[t1], %[rs], s46 offen sc1\n\t" \
    "buffer_load_dwordx2 v[126:127], %[t1], %[rs], s47 offen sc1\n\t" \
    "buffer_load_dwordx2 v[128:129], %[t2], %[rs], s46 offen sc1\n\t" \
    "buffer_load_dwordx2 v[130:131], %[t2], %[rs], s47 offen sc1\n\t" \
    "buffer_load_dwordx2 v[132:133], %[t3], %[rs], s46 offen sc1\n\t" \
    "buffer_load_dwordx2 v[134:135], %[t3], %[rs], s47 offen sc1\n\t" \
    "buffer_load_dwordx2 v[136:137], %[t4], %[rs], s46 offen sc1\n\t" \
    "buffer_load_dwordx2 v[138:139], %[t4], %[rs], s47 offen sc1\n\t" \
    LST5 \
    LJIT \
    LFLUSHF \
    "s_add_u32 s40, s40, 2\n\t" \
    "s_cmp_lt_u32 s40, 128\n\t" \
    "s_cbranch_scc1 .Lf11_%=\n\t" \
    "s_branch .Lend%=\n\t" \
    ".Lend%=:\n\t" \
    "s_waitcnt vmcnt(0) lgkmcnt(0)\n\t" \
    "s_mov_b32 %[bud], s41\n\t"
#define ASM_W1_LOOP \
    "s_mov_b32 s40, 0\n\t" \
    "s_mov_b32 s41, 0x20000\n\t" \
    LSTINIT \
    "s_add_u32 m0, %[wm], 0x0\n\t" \
    "s_add_u32 s44, %[wo], 0x0\n\t" \
    "buffer_load_dwordx4 v176, %[wr], s44 offen nt lds\n\t" \
    "s_add_u32 m0, %[wm], 0x2000\n\t" \
    "s_add_u32 s44, %[wo], 0xa000\n\t" \
    "buffer_load_dwordx4 v176, %[wr], s44 offen nt lds\n\t" \
    "s_add_u32 m0, %[wm], 0x4000\n\t" \
    "s_add_u32 s44, %[wo], 0x14000\n\t" \
    "buffer_load_dwordx4 v176, %[wr], s44 offen nt lds\n\t" \
    "s_add_u32 m0, %[wm], 0x6000\n\t" \
    "s_add_u32 s44, %[wo], 0x1e000\n\t" \
    "buffer_load_dwordx4 v176, %[wr], s44 offen nt lds\n\t" \
    "s_add_u32 m0, %[wm], 0x8000\n\t" \
    "s_add_u32 s44, %[wo], 0x28000\n\t" \
    "buffer_load_dwordx4 v176, %[wr], s44 offen nt lds\n\t" \
    "s_add_u32 m0, %[wm], 0xa000\n\t" \
    "s_add_u32 s44, %[wo], 0x32000\n\t" \
    "buffer_load_dwordx4 v176, %[wr], s44 offen nt lds\n\t" \
    "s_add_u32 m0, %[wm], 0xc000\n\t" \
    "s_add_u32 s44, %[wo], 0x3c000\n\t" \
    "buffer_load_dwordx4 v176, %[wr], s44 offen nt lds\n\t" \
    "s_add_u32 m0, %[wm], 0xe000\n\t" \
    "s_add_u32 s44, %[wo], 0x46000\n\t" \
    "buffer_load_dwordx4 v176, %[wr], s44 offen nt lds\n\t" \
    "s_add_u32 m0, %[wm], 0x10000\n\t" \
    "s_add_u32 s44, %[wo], 0x50000\n\t" \
    "buffer_load_dwordx4 v176, %[wr], s44 offen nt lds\n\t" \
    "s_add_u32 m0, %[wm], 0x12000\n\t" \
    "s_add_u32 s44, %[wo], 0x5a000\n\t" \
    "buffer_load_dwordx4 v176, %[wr], s44 offen nt lds\n\t" \
    "s_add_u32 m0, %[wm], 0x14000\n\t" \
    "s_add_u32 s44, %[wo], 0x64000\n\t" \
    "buffer_load_dwordx4 v176, %[wr], s44 offen nt lds\n\t" \
    "s_add_u32 m0, %[wm], 0x16000\n\t" \
    "s_add_u32 s44, %[wo], 0x6e000\n\t" \
    "buffer_load_dwordx4 v176, %[wr], s44 offen nt lds\n\t" \
    "s_add_u32 m0, %[wm], 0x18000\n\t" \
    "s_add_u32 s44, %[wo], 0x78000\n\t" \
    "buffer_load_dwordx4 v176, %[wr], s44 offen nt lds\n\t" \
    "s_add_u32 m0, %[wm], 0x1a000\n\t" \
    "s_add_u32 s44, %[wo], 0x82000\n\t" \
    "buffer_load_dwordx4 v176, %[wr], s44 offen nt lds\n\t" \
    "s_add_u32 m0, %[wm], 0x1c000\n\t" \
    "s_add_u32 s44, %[wo], 0x8c000\n\t" \
    "buffer_load_dwordx4 v176, %[wr], s44 offen nt lds\n\t" \
    "s_add_u32 m0, %[wm], 0x1e000\n\t" \
    "s_add_u32 s44, %[wo], 0x96000\n\t" \
    "buffer_load_dwordx4 v176, %[wr], s44 offen nt lds\n\t" \
    "s_add_u32 m0, %[wm], 0x20000\n\t" \
    "s_add_u32 s44, %[wo], 0xa0000\n\t" \
    "buffer_load_dwordx4 v176, %[wr], s44 offen nt lds\n\t" \
    "s_add_u32 m0, %[wm], 0x22000\n\t" \
    "s_add_u32 s44, %[wo], 0xaa000\n\t" \
    "buffer_load_dwordx4 v176, %[wr], s44 offen nt lds\n\t" \
    "s_add_u32 m0, %[wm], 0x24000\n\t" \
    "s_add_u32 s44, %[wo], 0xb4000\n\t" \
    "buffer_load_dwordx4 v176, %[wr], s44 offen nt lds\n\t" \
    "s_add_u32 m0, %[wm], 0x26000\n\t" \
    "s_add_u32 s44, %[wo], 0xbe000\n\t" \
    "buffer_load_dwordx4 v176, %[wr], s44 offen nt lds\n\t" \
    "s_waitcnt vmcnt(0) lgkmcnt(0)\n\t" \
    "s_barrier \n\t" \
    "v_add_u32 v178, 0x0, v177\n\t" \
    "ds_read_b64_tr_b16 v[0:1], v178 offset:0\n\t" \
    "ds_read_b64_tr_b16 v[2:3], v178 offset:1024\n\t" \
    "ds_read_b64_tr_b16 v[4:5], v178 offset:8192\n\t" \
    "ds_read_b64_tr_b16 v[6:7], v178 offset:9216\n\t" \
    "ds_read_b64_tr_b16 v[8:9], v178 offset:16384\n\t" \
    "ds_read_b64_tr_b16 v[10:11], v178 offset:17408\n\t" \
    "ds_read_b64_tr_b16 v[12:13], v178 offset:24576\n\t" \
    "ds_read_b64_tr_b16 v[14:15], v178 offset:25600\n\t" \
    "v_add_u32 v178, 0x8000, v177\n\t" \
    "ds_read_b64_tr_b16 v[16:17], v178 offset:0\n\t" \
    "ds_read_b64_tr_b16 v[18:19], v178 offset:1024\n\t" \
    "ds_read_b64_tr_b16 v[20:21], v178 offset:8192\n\t" \
    "ds_read_b64_tr_b16 v[22:23], v178 offset:9216\n\t" \
    "ds_read_b64_tr_b16 v[24:25], v178 offset:16384\n\t" \
    "ds_read_b64_tr_b16 v[26:27], v178 offset:17408\n\t" \
    "ds_read_b64_tr_b16 v[28:29], v178 offset:24576\n\t" \
    "ds_read_b64_tr_b16 v[30:31], v178 offset:25600\n\t" \
    "v_add_u32 v178, 0x10000, v177\n\t" \
    "ds_read_b64_tr_b16 v[32:33], v178 offset:0\n\t" \
    "ds_read_b64_tr_b16 v[34:35], v178 offset:1024\n\t" \
    "ds_read_b64_tr_b16 v[36:37], v178 offset:8192\n\t" \
    "ds_read_b64_tr_b16 v[38:39], v178 offset:9216\n\t" \
    "ds_read_b64_tr_b16 v[40:41], v178 offset:16384\n\t" \
    "ds_read_b64_tr_b16 v[42:43], v178 offset:17408\n\t" \
    "ds_read_b64_tr_b16 v[44:45], v178 offset:24576\n\t" \
    "ds_read_b64_tr_b16 v[46:47], v178 offset:25600\n\t" \
    "v_add_u32 v178, 0x18000, v177\n\t" \
    "ds_read_b64_tr_b16 v[48:49], v178 offset:0\n\t" \
    "ds_read_b64_tr_b16 v[50:51], v178 offset:1024\n\t" \
    "ds_read_b64_tr_b16 v[52:53], v178 offset:8192\n\t" \
    "ds_read_b64_tr_b16 v[54:55], v178 offset:9216\n\t" \
    "ds_read_b64_tr_b16 v[56:57], v178 offset:16384\n\t" \
    "ds_read_b64_tr_b16 v[58:59], v178 offset:17408\n\t" \
    "ds_read_b64_tr_b16 v[60:61], v178 offset:24576\n\t" \
    "ds_read_b64_tr_b16 v[62:63], v178 offset:25600\n\t" \
    "v_add_u32 v178, 0x20000, v177\n\t" \
    "ds_read_b64_tr_b16 v[64:65], v178 offset:0\n\t" \
    "ds_read_b64_tr_b16 v[66:67], v178 offset:1024\n\t" \
    "ds_read_b64_tr_b16 v[68:69], v178 offset:8192\n\t" \
    "ds_read_b64_tr_b16 v[70:71], v178 offset:9216\n\t" \
    "ds_read_b64_tr_b16 v[72:73], v178 offset:16384\n\t" \
    "ds_read_b64_tr_b16 v[74:75], v178 offset:17408\n\t" \
    "ds_read_b64_tr_b16 v[76:77], v178 offset:24576\n\t" \
    "ds_read_b64_tr_b16 v[78:79], v178 offset:25600\n\t" \
    "v_mov_b32 v228, 0x3f803f80\n\t" \
    "v_mov_b32 v229, 0x3f803f80\n\t" \
    "v_mov_b32 v230, 0x3f803f80\n\t" \
    "v_mov_b32 v231, 0x3f803f80\n\t" \
    "v_mov_b64 v[216:217], 0\n\t" \
    "v_mov_b32 v219, 0\n\t" \
    "v_bfe_u32 v83, v253, 2, 5\n\t" \
    "v_add_u32 v83, 0x7000, v83\n\t" \
    "v_mov_b32 v218, 0\n\t" \
    "s_waitcnt lgkmcnt(0)\n\t" \
    "s_barrier \n\t" \
    "ds_write_b32 v251, v228 offset:72\n\t" \
    "ds_write_b32 v251, v228 offset:2120\n\t" \
    "v_mfma_f32_16x16x32_bf16 v[160:163], v[0:3], v[228:231], 0\n\t" \
    "v_mfma_f32_16x16x32_bf16 v[160:163], v[4:7], v[228:231], v[160:163]\n\t" \
    "v_mfma_f32_16x16x32_bf16 v[160:163], v[8:11], v[228:231], v[160:163]\n\t" \
    "v_mfma_f32_16x16x32_bf16 v[160:163], v[12:15], v[228:231], v[160:163]\n\t" \
    "v_mfma_f32_16x16x32_bf16 v[160:163], v[16:19], v[228:231], v[160:163]\n\t" \
    "v_mfma_f32_16x16x32_bf16 v[160:163], v[20:23], v[228:231], v[160:163]\n\t" \
    "v_mfma_f32_16x16x32_bf16 v[160:163], v[24:27], v[228:231], v[160:163]\n\t" \
    "v_mfma_f32_16x16x32_bf16 v[160:163], v[28:31], v[228:231], v[160:163]\n\t" \
    "v_mfma_f32_16x16x32_bf16 v[160:163], v[32:35], v[228:231], v[160:163]\n\t" \
    "v_mfma_f32_16x16x32_bf16 v[160:163], v[36:39], v[228:231], v[160:163]\n\t" \
    "v_mfma_f32_16x16x32_bf16 v[160:163], v[40:43], v[228:231], v[160:163]\n\t" \
    "v_mfma_f32_16x16x32_bf16 v[160:163], v[44:47], v[228:231], v[160:163]\n\t" \
    "v_mfma_f32_16x16x32_bf16 v[160:163], v[48:51], v[228:231], v[160:163]\n\t" \
    "v_mfma_f32_16x16x32_bf16 v[160:163], v[52:55], v[228:231], v[160:163]\n\t" \
    "v_mfma_f32_16x16x32_bf16 v[160:163], v[56:59], v[228:231], v[160:163]\n\t" \
    "v_mfma_f32_16x16x32_bf16 v[160:163], v[60:63], v[228:231], v[160:163]\n\t" \
    "v_mfma_f32_16x16x32_bf16 v[160:163], v[64:67], v[228:231], v[160:163]\n\t" \
    "v_mfma_f32_16x16x32_bf16 v[160:163], v[68:71], v[228:231], v[160:163]\n\t" \
    "v_mfma_f32_16x16x32_bf16 v[160:163], v[72:75], v[228:231], v[160:163]\n\t" \
    "v_mfma_f32_16x16x32_bf16 v[160:163], v[76:79], v[228:231], v[160:163]\n\t" \
    "s_mov_b32 s52, 0x22222222\n\t" \
    "s_mov_b32 s53, 0x22222222\n\t" \
    "s_mov_b32 s54, 0x44444444\n\t" \
    "s_mov_b32 s55, 0x44444444\n\t" \
    "s_mov_b32 s56, 0x88888888\n\t" \
    "s_mov_b32 s57, 0x88888888\n\t" \
    "ds_write_b32 v83, v218\n\t" \
    "s_waitcnt lgkmcnt(0)\n\t" \
    "s_barrier \n\t" \
    ".Lw11_%=:\n\t" \
    "v_mov_b32 v248, 0\n\t" \
    "buffer_load_dword v248, v248, %[rs], %[go] offen sc1\n\t" \
    "s_waitcnt vmcnt(0)\n\t" \
    "v_cmp_ne_u32 vcc, 0, v248\n\t" \
    "s_cbranch_vccnz .Lw12_%=\n\t" \
    "s_cmp_eq_u32 s41, 0\n\t" \
    "s_cbranch_scc1 .Lw12_%=\n\t" \
    "s_sub_u32 s41, s41, 1\n\t" \
    "s_sleep 16\n\t" \
    "s_branch .Lw11_%=\n\t" \
    ".Lw12_%=:\n\t" \
    "s_sub_i32 s60, 0, %[ng]\n\t" \
    "s_sub_i32 s61, 1, %[ng]\n\t" \
    "s_add_i32 s62, s60, 6\n\t" \
    "s_add_i32 s63, s61, 6\n\t" \
    "s_mul_i32 s42, s61, 127\n\t" \
    "s_mul_i32 s44, %[ng], 0x733000\n\t" \
    "s_add_u32 s44, s44, 0x2400000\n\t" \
    "v_cmp_ne_u32 s[58:59], %[ng], v255\n\t" \
    "s_nop 1\n\t" \
    "s_mov_b64 exec, s[58:59]\n\t" \
    "buffer_store_dwordx2 v[216:217], v254, %[rs], s44 offen sc1\n\t" \
    "s_mov_b64 exec, -1\n\t" \
    "s_lshr_b32 s48, %[wave], 2\n\t" \
    "s_mul_i32 s48, s48, 0x3000\n\t" \
    "s_add_u32 s48, s48, %[pi]\n\t" \
    "s_mov_b32 s43, 0\n\t" \
    "s_add_u32 s46, s43, s48\n\t" \
    "s_add_u32 s47, s46, 0x0\n\t" \
    "buffer_load_dwordx4 v[208:211], v253, %[rs], s47 offen sc1\n\t" \
    "s_add_u32 s47, s46, 0x8000\n\t" \
    "buffer_load_dwordx4 v[212:215], v253, %[rs], s47 offen sc1\n\t" \
    "s_cmp_gt_u32 %[wave], 3\n\t" \
    "s_cbranch_scc1 .Lw13_%=\n\t" \
    "s_add_u32 s47, s46, 0x10000\n\t" \
    "buffer_load_dwordx4 v[216:219], v253, %[rs], s47 offen sc1\n\t" \
    ".Lw13_%=:\n\t" \
    "s_waitcnt vmcnt(0)\n\t" \
    "v_lshrrev_b32 v82, 1, v253\n\t" \
    ".Lw14_%=:\n\t" \
    "s_add_i32 s44, s40, s60\n\t" \
    "s_max_i32 s44, s44, 0\n\t" \
    "s_lshr_b32 s45, s44, 1\n\t" \
    "s_and_b32 s44, s44, 1\n\t" \
    "s_lshl_b32 s44, s44, 12\n\t" \
    "s_mul_i32 s49, s45, 0x14000\n\t" \
    "s_add_u32 s49, s49, s44\n\t" \
    "s_lshr_b32 s45, s45, 4\n\t" \
    "s_mul_i32 s45, s45, 0xc2000\n\t" \
    "s_add_u32 s49, s49, s45\n\t" \
    "s_add_i32 s44, s40, s61\n\t" \
    "s_max_i32 s44, s44, 0\n\t" \
    "s_lshr_b32 s45, s44, 1\n\t" \
    "s_and_b32 s44, s44, 1\n\t" \
    "s_lshl_b32 s44, s44, 12\n\t" \
    "s_mul_i32 s50, s45, 0x14000\n\t" \
    "s_add_u32 s50, s50, s44\n\t" \
    "s_lshr_b32 s45, s45, 4\n\t" \
    "s_mul_i32 s45, s45, 0xc2000\n\t" \
    "s_add_u32 s50, s50, s45\n\t" \
    "s_sub_u32 s50, s50, s49\n\t" \
    "s_add_u32 s49, s49, 0x2400000\n\t" \
    "v_mad_u32_u24 v80, v255, s50, v254\n\t" \
    "s_lshr_b32 s44, s40, 6\n\t" \
    "s_lshl_b32 s44, s44, 14\n\t" \
    "s_and_b32 s45, s40, 63\n\t" \
    "s_lshl_b32 s45, s45, 7\n\t" \
    "s_add_u32 s44, s44, s45\n\t" \
    "s_add_u32 s50, s44, 0x4d0000\n\t" \
    "s_add_i32 s44, s40, 1\n\t" \
    "s_max_i32 s44, s44, 0\n\t" \
    "s_lshr_b32 s45, s44, 1\n\t" \
    "s_and_b32 s44, s44, 1\n\t" \
    "s_lshl_b32 s44, s44, 12\n\t" \
    "s_mul_i32 s43, s45, 0x14000\n\t" \
    "s_add_u32 s43, s43, s44\n\t" \
    "s_lshr_b32 s45, s45, 4\n\t" \
    "s_mul_i32 s45, s45, 0xc2000\n\t" \
    "s_add_u32 s43, s43, s45\n\t" \
    LST0 \
    LJIT \
    "s_setprio 2\n\t" \
    "v_max_u32 v248, v208, v209\n\t" \
    "v_max3_u32 v248, v210, v211, v248\n\t" \
    "v_max3_u32 v248, v212, v213, v248\n\t" \
    "v_max3_u32 v248, v214, v215, v248\n\t" \
    "v_max3_u32 v248, v216, v217, v248\n\t" \
    "v_max3_u32 v248, v218, v219, v248\n\t" \
    "v_cmp_eq_u32 vcc, -1, v248\n\t" \
    "s_cbranch_vccz .Lw16_%=\n\t" \
    "s_and_b32 s44, s40, 1\n\t" \
    "s_cmp_eq_u32 s44, 1\n\t" \
    "s_cbranch_scc1 .Lw15_%=\n\t" \
    "s_sleep 10\n\t" \
    ".Lw15_%=:\n\t" \
    "v_max_u32 v248, v208, v209\n\t" \
    "v_max3_u32 v248, v210, v211, v248\n\t" \
    "v_cmp_eq_u32 vcc, -1, v248\n\t" \
    "s_cbranch_vccz .Lw18_%=\n\t" \
    "s_add_u32 s47, s46, 0x0\n\t" \
    "buffer_load_dwordx4 v[208:211], v253, %[rs], s47 offen sc1\n\t" \
    ".Lw18_%=:\n\t" \
    "v_max_u32 v248, v212, v213\n\t" \
    "v_max3_u32 v248, v214, v215, v248\n\t" \
    "v_cmp_eq_u32 vcc, -1, v248\n\t" \
    "s_cbranch_vccz .Lw19_%=\n\t" \
    "s_add_u32 s47, s46, 0x8000\n\t" \
    "buffer_load_dwordx4 v[212:215], v253, %[rs], s47 offen sc1\n\t" \
    ".Lw19_%=:\n\t" \
    "v_max_u32 v248, v216, v217\n\t" \
    "v_max3_u32 v248, v218, v219, v248\n\t" \
    "v_cmp_eq_u32 vcc, -1, v248\n\t" \
    "s_cbranch_vccz .Lw110_%=\n\t" \
    "s_add_u32 s47, s46, 0x10000\n\t" \
    "buffer_load_dwordx4 v[216:219], v253, %[rs], s47 offen sc1\n\t" \
    ".Lw110_%=:\n\t" \
    "s_waitcnt vmcnt(0)\n\t" \
    "v_max_u32 v248, v208, v209\n\t" \
    "v_max3_u32 v248, v210, v211, v248\n\t" \
    "v_max3_u32 v248, v212, v213, v248\n\t" \
    "v_max3_u32 v248, v214, v215, v248\n\t" \
    "v_max3_u32 v248, v216, v217, v248\n\t" \
    "v_max3_u32 v248, v218, v219, v248\n\t" \
    "v_cmp_eq_u32 vcc, -1, v248\n\t" \
    "s_cbranch_vccz .Lw16_%=\n\t" \
    "s_cmp_eq_u32 s41, 0\n\t" \
    "s_cbranch_scc1 .Lw16_%=\n\t" \
    "s_sub_u32 s41, s41, 1\n\t" \
    "s_branch .Lw15_%=\n\t" \
    ".Lw16_%=:\n\t" \
    LST1 \
    LJIT \
    "ds_write_b128 v250, v[208:211] offset:0\n\t" \
    "v_mfma_f32_16x16x32_bf16 v[236:239], v[208:211], v[208:211], 0\n\t" \
    "v_mfma_f32_16x16x32_bf16 v[232:235], v[228:231], v[208:211], 0\n\t" \
    "ds_write_b128 v250, v[212:215] offset:8192\n\t" \
    "v_mfma_f32_16x16x32_bf16 v[236:239], v[212:215], v[212:215], v[236:239]\n\t" \
    "v_mfma_f32_16x16x32_bf16 v[232:235], v[228:231], v[212:215], v[232:235]\n\t" \
    "s_cmp_gt_u32 %[wave], 3\n\t" \
    "s_cbranch_scc1 .Lw111_%=\n\t" \
    "ds_write_b128 v250, v[216:219] offset:16384\n\t" \
    ".Lw111_%=:\n\t" \
    "v_mfma_f32_16x16x32_bf16 v[236:239], v[216:219], v[216:219], v[236:239]\n\t" \
    "v_mfma_f32_16x16x32_bf16 v[232:235], v[228:231], v[216:219], v[232:235]\n\t" \
    "s_add_u32 s46, s43, s48\n\t" \
    "s_nop 4\n\t" \
    "v_cndmask_b32 v236, v236, v237, s[52:53]\n\t" \
    "v_cndmask_b32 v236, v236, v238, s[54:55]\n\t" \
    "v_cndmask_b32 v236, v236, v239, s[56:57]\n\t" \
    "s_mov_b32 exec_lo, 0xf0000f\n\t" \
    "s_mov_b32 exec_hi, 0xf0000f00\n\t" \
    "ds_write2_b32 v252, v232, v236 offset1:1\n\t" \
    "s_mov_b64 exec, -1\n\t" \
    "s_waitcnt lgkmcnt(0)\n\t" \
    "s_barrier \n\t" \
    "s_setprio 0\n\t" \
    LST2 \
    LJIT \
    "s_cmp_lg_u32 %[wave], 4\n\t" \
    "s_cbranch_scc1 .Lw112_%=\n\t" \
    "s_setprio 1\n\t" \
    "ds_read_b128 v[232:235], v251 offset:0\n\t" \
    "ds_read_b128 v[236:239], v251 offset:16\n\t" \
    "ds_read_b128 v[240:243], v251 offset:32\n\t" \
    "ds_read_b128 v[244:247], v251 offset:48\n\t" \
    "s_waitcnt lgkmcnt(0)\n\t" \
    "v_add_f32 v232, v232, v240\n\t" \
    "v_add_f32 v233, v233, v241\n\t" \
    "v_add_f32 v234, v234, v242\n\t" \
    "v_add_f32 v235, v235, v243\n\t" \
    "v_add_f32 v236, v236, v244\n\t" \
    "v_add_f32 v237, v237, v245\n\t" \
    "v_add_f32 v238, v238, v246\n\t" \
    "v_add_f32 v239, v239, v247\n\t" \
    "v_add_f32 v232, v232, v236\n\t" \
    "v_add_f32 v233, v233, v237\n\t" \
    "v_add_f32 v234, v234, v238\n\t" \
    "v_add_f32 v235, v235, v239\n\t" \
    "v_add_f32 v232, v232, v234\n\t" \
    "v_add_f32 v233, v233, v235\n\t" \
    "v_mul_f32 v232, 0x3acccccd, v232\n\t" \
    "v_mul_f32 v233, 0x3acccccd, v233\n\t" \
    "v_fma_f32 v233, -v232, v232, v233\n\t" \
    "v_mov_b32 v234, v232\n\t" \
    "v_max_f32 v233, 0, v233\n\t" \
    "v_cvt_pk_bf16_f32 v234, v234, v234\n\t" \
    "v_and_b32 v234, 0xffff0000, v234\n\t" \
    "v_mov_b32 v236, v233\n\t" \
    "v_cvt_pk_bf16_f32 v236, v236, v236\n\t" \
    "v_and_b32 v236, 0xffff0000, v236\n\t" \
    "v_add_f32 v236, 0x3727c5ac, v236\n\t" \
    "v_cvt_pk_bf16_f32 v236, v236, v236\n\t" \
    "v_and_b32 v236, 0xffff0000, v236\n\t" \
    "v_sqrt_f32 v236, v236\n\t" \
    "s_nop 0\n\t" \
    "v_cvt_pk_bf16_f32 v236, v236, v236\n\t" \
    "v_and_b32 v236, 0xffff0000, v236\n\t" \
    "v_rcp_f32 v235, v236\n\t" \
    "s_nop 0\n\t" \
    "v_sub_f32 v232, v232, v234\n\t" \
    "v_mul_f32 v233, v233, v235\n\t" \
    "v_mul_f32 v232, v232, v235\n\t" \
    "v_mul_f32 v233, v233, v235\n\t" \
    "v_cvt_pk_bf16_f32 v232, v232, v232\n\t" \
    "v_and_b32 v232, 0xffff0000, v232\n\t" \
    "v_cvt_pk_bf16_f32 v233, v233, v233\n\t" \
    "v_and_b32 v233, 0xffff0000, v233\n\t" \
    "v_add_f32 v233, 0x3727c5ac, v233\n\t" \
    "v_cvt_pk_bf16_f32 v233, v233, v233\n\t" \
    "v_and_b32 v233, 0xffff0000, v233\n\t" \
    "v_sqrt_f32 v233, v233\n\t" \
    "s_nop 0\n\t" \
    "v_cvt_pk_bf16_f32 v233, v233, v233\n\t" \
    "v_and_b32 v233, 0xffff0000, v233\n\t" \
    "v_rcp_f32 v233, v233\n\t" \
    "v_fma_f32 v232, v232, v236, v234\n\t" \
    "v_mul_f32 v237, v235, v233\n\t" \
    "v_mul_f32 v238, -v232, v237\n\t" \
    "v_min_u32 v234, -2, v234\n\t" \
    "v_min_u32 v235, -2, v235\n\t" \
    "v_mov_b32 v239, s40\n\t" \
    "ds_write2_b32 v251, v237, v238 offset0:16 offset1:17\n\t" \
    "ds_write_b32 v251, v239 offset:72\n\t" \
    "s_setprio 0\n\t" \
    "s_cmp_eq_u32 %[ss], 0\n\t" \
    "s_cbranch_scc1 .Lw113_%=\n\t" \
    "s_mov_b64 exec, 0xffff\n\t" \
    "buffer_store_dwordx2 v[234:235], v82, %[rs], s50 offen sc1\n\t" \
    "s_mov_b64 exec, -1\n\t" \
    ".Lw113_%=:\n\t" \
    ".Lw112_%=:\n\t" \
    "ds_read_b128 v[176:179], v249 offset:0\n\t" \
    "ds_read_b128 v[180:183], v249 offset:1024\n\t" \
    "ds_read_b128 v[184:187], v249 offset:2048\n\t" \
    "ds_read_b128 v[188:191], v249 offset:3072\n\t" \
    "ds_read_b128 v[192:195], v249 offset:4096\n\t" \
    "ds_read_b128 v[196:199], v249 offset:5120\n\t" \
    "ds_read_b128 v[200:203], v249 offset:6144\n\t" \
    "s_waitcnt lgkmcnt(6)\n\t" \
    "v_mfma_f32_16x16x32_bf16 v[168:171], v[0:3], v[176:179], 0\n\t" \
    "ds_read_b128 v[204:207], v249 offset:7168\n\t" \
    "s_waitcnt lgkmcnt(6)\n\t" \
    "v_mfma_f32_16x16x32_bf16 v[168:171], v[4:7], v[180:183], v[168:171]\n\t" \
    "ds_read_b128 v[176:179], v249 offset:8192\n\t" \
    "s_waitcnt lgkmcnt(6)\n\t" \
    "v_mfma_f32_16x16x32_bf16 v[168:171], v[8:11], v[184:187], v[168:171]\n\t" \
    "ds_read_b128 v[180:183], v249 offset:9216\n\t" \
    "s_waitcnt lgkmcnt(6)\n\t" \
    "v_mfma_f32_16x16x32_bf16 v[168:171], v[12:15], v[188:191], v[168:171]\n\t" \
    "ds_read_b128 v[184:187], v249 offset:10240\n\t" \
    "s_add_u32 s47, s46, 0x0\n\t" \
    "buffer_load_dwordx4 v[208:211], v253, %[rs], s47 offen sc1\n\t" \
    "s_add_u32 s47, s46, 0x8000\n\t" \
    "buffer_load_dwordx4 v[212:215], v253, %[rs], s47 offen sc1\n\t" \
    "s_cmp_gt_u32 %[wave], 3\n\t" \
    "s_cbranch_scc1 .Lw114_%=\n\t" \
    "s_add_u32 s47, s46, 0x10000\n\t" \
    "buffer_load_dwordx4 v[216:219], v253, %[rs], s47 offen sc1\n\t" \
    ".Lw114_%=:\n\t" \
    "s_waitcnt lgkmcnt(6)\n\t" \
    "v_mfma_f32_16x16x32_bf16 v[168:171], v[16:19], v[192:195], v[168:171]\n\t" \
    "ds_read_b128 v[188:191], v249 offset:11264\n\t" \
    "s_waitcnt lgkmcnt(6)\n\t" \
    "v_mfma_f32_16x16x32_bf16 v[168:171], v[20:23], v[196:199], v[168:171]\n\t" \
    "ds_read_b128 v[192:195], v249 offset:12288\n\t" \
    "s_waitcnt lgkmcnt(6)\n\t" \
    "v_mfma_f32_16x16x32_bf16 v[168:171], v[24:27], v[200:203], v[168:171]\n\t" \
    "ds_read_b128 v[196:199], v249 offset:13312\n\t" \
    "s_waitcnt lgkmcnt(6)\n\t" \
    "v_mfma_f32_16x16x32_bf16 v[168:171], v[28:31], v[204:207], v[168:171]\n\t" \
    "ds_read_b128 v[200:203], v249 offset:14336\n\t" \
    "s_waitcnt lgkmcnt(6)\n\t" \
    "v_mfma_f32_16x16x32_bf16 v[168:171], v[32:35], v[176:179], v[168:171]\n\t" \
    "ds_read_b128 v[204:207], v249 offset:15360\n\t" \
    "s_waitcnt lgkmcnt(6)\n\t" \
    "v_mfma_f32_16x16x32_bf16 v[168:171], v[36:39], v[180:183], v[168:171]\n\t" \
    "ds_read_b128 v[176:179], v249 offset:16384\n\t" \
    "s_waitcnt lgkmcnt(6)\n\t" \
    "v_mfma_f32_16x16x32_bf16 v[168:171], v[40:43], v[184:187], v[168:171]\n\t" \
    "ds_read_b128 v[180:183], v249 offset:17408\n\t" \
    "s_waitcnt lgkmcnt(6)\n\t" \
    "v_mfma_f32_16x16x32_bf16 v[168:171], v[44:47], v[188:191], v[168:171]\n\t" \
    "ds_read_b128 v[184:187], v249 offset:18432\n\t" \
    "s_waitcnt lgkmcnt(6)\n\t" \
    "v_mfma_f32_16x16x32_bf16 v[168:171], v[48:51], v[192:195], v[168:171]\n\t" \
    "ds_read_b128 v[188:191], v249 offset:19456\n\t" \
    "s_waitcnt lgkmcnt(6)\n\t" \
    "v_mfma_f32_16x16x32_bf16 v[168:171], v[52:55], v[196:199], v[168:171]\n\t" \
    "ds_read_b32 v236, v251 offset:72\n\t" \
    "ds_read_b64 v[238:239], v251 offset:64\n\t" \
    "s_waitcnt lgkmcnt(7)\n\t" \
    "v_mfma_f32_16x16x32_bf16 v[168:171], v[56:59], v[200:203], v[168:171]\n\t" \
    "s_waitcnt lgkmcnt(6)\n\t" \
    "v_mfma_f32_16x16x32_bf16 v[168:171], v[60:63], v[204:207], v[168:171]\n\t" \
    "s_waitcnt lgkmcnt(5)\n\t" \
    "v_mfma_f32_16x16x32_bf16 v[168:171], v[64:67], v[176:179], v[168:171]\n\t" \
    "s_waitcnt lgkmcnt(4)\n\t" \
    "v_mfma_f32_16x16x32_bf16 v[168:171], v[68:71], v[180:183], v[168:171]\n\t" \
    ".Lw115_%=:\n\t" \
    "s_waitcnt lgkmcnt(0)\n\t" \
    "v_cmp_ne_u32 vcc, s40, v236\n\t" \
    "s_cbranch_vccz .Lw116_%=\n\t" \
    "ds_read_b32 v236, v251 offset:72\n\t" \
    "ds_read_b64 v[238:239], v251 offset:64\n\t" \
    "s_branch .Lw115_%=\n\t" \
    ".Lw116_%=:\n\t" \
    "s_waitcnt lgkmcnt(0)\n\t" \
    "v_mul_f32 v240, v160, v239\n\t" \
    "v_mul_f32 v241, v161, v239\n\t" \
    "v_mfma_f32_16x16x32_bf16 v[168:171], v[72:75], v[184:187], v[168:171]\n\t" \
    "v_mul_f32 v242, v162, v239\n\t" \
    "v_mul_f32 v243, v163, v239\n\t" \
    "v_mfma_f32_16x16x32_bf16 v[168:171], v[76:79], v[188:191], v[168:171]\n\t" \
    "v_mov_b32 v237, 0xc0135761\n\t" \
    LST3 \
    LJIT \
    "s_nop 6\n\t" \
    "v_fma_f32 v168, v168, v238, v240\n\t" \
    "v_fma_f32 v169, v169, v238, v241\n\t" \
    "v_fma_f32 v170, v170, v238, v242\n\t" \
    "v_fma_f32 v171, v171, v238, v243\n\t" \
    "v_mul_f32 v176, v168, v168\n\t" \
    "v_mul_f32 v177, v169, v169\n\t" \
    "v_mul_f32 v178, v170, v170\n\t" \
    "v_mul_f32 v179, v171, v171\n\t" \
    "v_fmamk_f32 v176, v176, 0xbdd2d3e7, v237\n\t" \
    "v_fmamk_f32 v177, v177, 0xbdd2d3e7, v237\n\t" \
    "v_fmamk_f32 v178, v178, 0xbdd2d3e7, v237\n\t" \
    "v_fmamk_f32 v179, v179, 0xbdd2d3e7, v237\n\t" \
    "v_mul_f32 v176, v168, v176\n\t" \
    "v_mul_f32 v177, v169, v177\n\t" \
    "v_mul_f32 v178, v170, v178\n\t" \
    "v_mul_f32 v179, v171, v179\n\t" \
    "v_exp_f32 v176, v176\n\t" \
    "v_exp_f32 v177, v177\n\t" \
    "v_exp_f32 v178, v178\n\t" \
    "v_exp_f32 v179, v179\n\t" \
    "v_add_f32 v176, 1.0, v176\n\t" \
    "v_add_f32 v177, 1.0, v177\n\t" \
    "v_add_f32 v178, 1.0, v178\n\t" \
    "v_add_f32 v179, 1.0, v179\n\t" \
    "v_rcp_f32 v176, v176\n\t" \
    "v_rcp_f32 v177, v177\n\t" \
    "v_rcp_f32 v178, v178\n\t" \
    "v_rcp_f32 v179, v179\n\t" \
    "v_mul_f32 v168, v168, v176\n\t" \
    "v_mul_f32 v169, v169, v177\n\t" \
    "v_mul_f32 v170, v170, v178\n\t" \
    "v_mul_f32 v171, v171, v179\n\t" \
    "s_waitcnt vmcnt(0)\n\t" \
    "v_cvt_pk_bf16_f32 v184, v168, v169\n\t" \
    "v_cvt_pk_bf16_f32 v185, v170, v171\n\t" \
    "v_min_u32 v184, -2, v184\n\t" \
    "v_min_u32 v185, -2, v185\n\t" \
    "s_cmp_eq_u32 s40, s42\n\t" \
    "s_cselect_b64 s[44:45], s[58:59], 0\n\t" \
    "s_andn2_b64 exec, -1, s[44:45]\n\t" \
    "buffer_store_dwordx2 v[184:185], v80, %[rs], s49 offen sc1\n\t" \
    "s_mov_b64 exec, -1\n\t" \
    LST4 \
    LJIT \
    "s_cmp_ge_u32 s40, 122\n\t" \
    "s_cbranch_scc1 .Lw117_%=\n\t" \
    "s_add_i32 s44, s40, s62\n\t" \
    "s_max_i32 s44, s44, 0\n\t" \
    "s_lshr_b32 s45, s44, 1\n\t" \
    "s_and_b32 s44, s44, 1\n\t" \
    "s_lshl_b32 s44, s44, 12\n\t" \
    "s_mul_i32 s51, s45, 0x14000\n\t" \
    "s_add_u32 s51, s51, s44\n\t" \
    "s_lshr_b32 s45, s45, 4\n\t" \
    "s_mul_i32 s45, s45, 0xc2000\n\t" \
    "s_add_u32 s51, s51, s45\n\t" \
    "s_add_i32 s44, s40, s63\n\t" \
    "s_max_i32 s44, s44, 0\n\t" \
    "s_lshr_b32 s45, s44, 1\n\t" \
    "s_and_b32 s44, s44, 1\n\t" \
    "s_lshl_b32 s44, s44, 12\n\t" \
    "s_mul_i32 s50, s45, 0x14000\n\t" \
    "s_add_u32 s50, s50, s44\n\t" \
    "s_lshr_b32 s45, s45, 4\n\t" \
    "s_mul_i32 s45, s45, 0xc2000\n\t" \
    "s_add_u32 s50, s50, s45\n\t" \
    "s_sub_u32 s50, s50, s51\n\t" \
    "s_add_u32 s51, s51, 0x2400000\n\t" \
    "v_mad_u32_u24 v81, v255, s50, v254\n\t" \
    "v_mov_b64 v[188:189], -1\n\t" \
    "s_add_u32 s44, s40, 6\n\t" \
    "s_cmp_eq_u32 s44, s42\n\t" \
    "s_cselect_b64 s[44:45], s[58:59], 0\n\t" \
    "s_andn2_b64 exec, -1, s[44:45]\n\t" \
    "buffer_store_dwordx2 v[188:189], v81, %[rs], s51 offen sc1\n\t" \
    "s_mov_b64 exec, -1\n\t" \
    ".Lw117_%=:\n\t" \
    "v_xor_b32 v249, 0x8000, v249\n\t" \
    "v_xor_b32 v250, 0x8000, v250\n\t" \
    "v_xor_b32 v251, 0x800, v251\n\t" \
    "v_xor_b32 v252, 0x800, v252\n\t" \
    LFLUSHW \
    "s_add_u32 s40, s40, 1\n\t" \
    "s_cmp_lg_u32 s40, 128\n\t" \
    "s_cbranch_scc1 .Lw14_%=\n\t" \
    "s_branch .Lend%=\n\t" \
    ".Lend%=:\n\t" \
    "s_waitcnt vmcnt(0) lgkmcnt(0)\n\t" \
    "s_mov_b32 %[bud], s41\n\t"
#define ASM_W2_LOOP \
    "s_mov_b32 s40, 0\n\t" \
    "s_mov_b32 s41, 0x20000\n\t" \
    LSTINIT \
    "s_add_u32 m0, %[wm], 0x0\n\t" \
    "s_add_u32 s44, %[wo], 0x0\n\t" \
    "buffer_load_dwordx4 v176, %[wr], s44 offen nt lds\n\t" \
    "s_add_u32 m0, %[wm], 0x2000\n\t" \
    "s_add_u32 s44, %[wo], 0xa000\n\t" \
    "buffer_load_dwordx4 v176, %[wr], s44 offen nt lds\n\t" \
    "s_add_u32 m0, %[wm], 0x4000\n\t" \
    "s_add_u32 s44, %[wo], 0x14000\n\t" \
    "buffer_load_dwordx4 v176, %[wr], s44 offen nt lds\n\t" \
    "s_add_u32 m0, %[wm], 0x6000\n\t" \
    "s_add_u32 s44, %[wo], 0x1e000\n\t" \
    "buffer_load_dwordx4 v176, %[wr], s44 offen nt lds\n\t" \
    "s_add_u32 m0, %[wm], 0x8000\n\t" \
    "s_add_u32 s44, %[wo], 0x28000\n\t" \
    "buffer_load_dwordx4 v176, %[wr], s44 offen nt lds\n\t" \
    "s_add_u32 m0, %[wm], 0xa000\n\t" \
    "s_add_u32 s44, %[wo], 0x32000\n\t" \
    "buffer_load_dwordx4 v176, %[wr], s44 offen nt lds\n\t" \
    "s_add_u32 m0, %[wm], 0xc000\n\t" \
    "s_add_u32 s44, %[wo], 0x3c000\n\t" \
    "buffer_load_dwordx4 v176, %[wr], s44 offen nt lds\n\t" \
    "s_add_u32 m0, %[wm], 0xe000\n\t" \
    "s_add_u32 s44, %[wo], 0x46000\n\t" \
    "buffer_load_dwordx4 v176, %[wr], s44 offen nt lds\n\t" \
    "s_add_u32 m0, %[wm], 0x10000\n\t" \
    "s_add_u32 s44, %[wo], 0x50000\n\t" \
    "buffer_load_dwordx4 v176, %[wr], s44 offen nt lds\n\t" \
    "s_add_u32 m0, %[wm], 0x12000\n\t" \
    "s_add_u32 s44, %[wo], 0x5a000\n\t" \
    "buffer_load_dwordx4 v176, %[wr], s44 offen nt lds\n\t" \
    "s_add_u32 m0, %[wm], 0x14000\n\t" \
    "s_add_u32 s44, %[wo], 0x64000\n\t" \
    "buffer_load_dwordx4 v176, %[wr], s44 offen nt lds\n\t" \
    "s_add_u32 m0, %[wm], 0x16000\n\t" \
    "s_add_u32 s44, %[wo], 0x6e000\n\t" \
    "buffer_load_dwordx4 v176, %[wr], s44 offen nt lds\n\t" \
    "s_add_u32 m0, %[wm], 0x18000\n\t" \
    "s_add_u32 s44, %[wo], 0x78000\n\t" \
    "buffer_load_dwordx4 v176, %[wr], s44 offen nt lds\n\t" \
    "s_add_u32 m0, %[wm], 0x1a000\n\t" \
    "s_add_u32 s44, %[wo], 0x82000\n\t" \
    "buffer_load_dwordx4 v176, %[wr], s44 offen nt lds\n\t" \
    "s_add_u32 m0, %[wm], 0x1c000\n\t" \
    "s_add_u32 s44, %[wo], 0x8c000\n\t" \
    "buffer_load_dwordx4 v176, %[wr], s44 offen nt lds\n\t" \
    "s_add_u32 m0, %[wm], 0x1e000\n\t" \
    "s_add_u32 s44, %[wo], 0x96000\n\t" \
    "buffer_load_dwordx4 v176, %[wr], s44 offen nt lds\n\t" \
    "s_add_u32 m0, %[wm], 0x20000\n\t" \
    "s_add_u32 s44, %[wo], 0xa0000\n\t" \
    "buffer_load_dwordx4 v176, %[wr], s44 offen nt lds\n\t" \
    "s_add_u32 m0, %[wm], 0x22000\n\t" \
    "s_add_u32 s44, %[wo], 0xaa000\n\t" \
    "buffer_load_dwordx4 v176, %[wr], s44 offen nt lds\n\t" \
    "s_add_u32 m0, %[wm], 0x24000\n\t" \
    "s_add_u32 s44, %[wo], 0xb4000\n\t" \
    "buffer_load_dwordx4 v176, %[wr], s44 offen nt lds\n\t" \
    "s_add_u32 m0, %[wm], 0x26000\n\t" \
    "s_add_u32 s44, %[wo], 0xbe000\n\t" \
    "buffer_load_dwordx4 v176, %[wr], s44 offen nt lds\n\t" \
    "s_waitcnt vmcnt(0) lgkmcnt(0)\n\t" \
    "s_barrier \n\t" \
    "v_add_u32 v178, 0x0, v177\n\t" \
    "ds_read_b64_tr_b16 v[0:1], v178 offset:0\n\t" \
    "ds_read_b64_tr_b16 v[2:3], v178 offset:1024\n\t" \
    "ds_read_b64_tr_b16 v[4:5], v178 offset:8192\n\t" \
    "ds_read_b64_tr_b16 v[6:7], v178 offset:9216\n\t" \
    "ds_read_b64_tr_b16 v[8:9], v178 offset:16384\n\t" \
    "ds_read_b64_tr_b16 v[10:11], v178 offset:17408\n\t" \
    "ds_read_b64_tr_b16 v[12:13], v178 offset:24576\n\t" \
    "ds_read_b64_tr_b16 v[14:15], v178 offset:25600\n\t" \
    "v_add_u32 v178, 0x8000, v177\n\t" \
    "ds_read_b64_tr_b16 v[16:17], v178 offset:0\n\t" \
    "ds_read_b64_tr_b16 v[18:19], v178 offset:1024\n\t" \
    "ds_read_b64_tr_b16 v[20:21], v178 offset:8192\n\t" \
    "ds_read_b64_tr_b16 v[22:23], v178 offset:9216\n\t" \
    "ds_read_b64_tr_b16 v[24:25], v178 offset:16384\n\t" \
    "ds_read_b64_tr_b16 v[26:27], v178 offset:17408\n\t" \
    "ds_read_b64_tr_b16 v[28:29], v178 offset:24576\n\t" \
    "ds_read_b64_tr_b16 v[30:31], v178 offset:25600\n\t" \
    "v_add_u32 v178, 0x10000, v177\n\t" \
    "ds_read_b64_tr_b16 v[32:33], v178 offset:0\n\t" \
    "ds_read_b64_tr_b16 v[34:35], v178 offset:1024\n\t" \
    "ds_read_b64_tr_b16 v[36:37], v178 offset:8192\n\t" \
    "ds_read_b64_tr_b16 v[38:39], v178 offset:9216\n\t" \
    "ds_read_b64_tr_b16 v[40:41], v178 offset:16384\n\t" \
    "ds_read_b64_tr_b16 v[42:43], v178 offset:17408\n\t" \
    "ds_read_b64_tr_b16 v[44:45], v178 offset:24576\n\t" \
    "ds_read_b64_tr_b16 v[46:47], v178 offset:25600\n\t" \
    "v_add_u32 v178, 0x18000, v177\n\t" \
    "ds_read_b64_tr_b16 v[48:49], v178 offset:0\n\t" \
    "ds_read_b64_tr_b16 v[50:51], v178 offset:1024\n\t" \
    "ds_read_b64_tr_b16 v[52:53], v178 offset:8192\n\t" \
    "ds_read_b64_tr_b16 v[54:55], v178 offset:9216\n\t" \
    "ds_read_b64_tr_b16 v[56:57], v178 offset:16384\n\t" \
    "ds_read_b64_tr_b16 v[58:59], v178 offset:17408\n\t" \
    "ds_read_b64_tr_b16 v[60:61], v178 offset:24576\n\t" \
    "ds_read_b64_tr_b16 v[62:63], v178 offset:25600\n\t" \
    "v_add_u32 v178, 0x20000, v177\n\t" \
    "ds_read_b64_tr_b16 v[64:65], v178 offset:0\n\t" \
    "ds_read_b64_tr_b16 v[66:67], v178 offset:1024\n\t" \
    "ds_read_b64_tr_b16 v[68:69], v178 offset:8192\n\t" \
    "ds_read_b64_tr_b16 v[70:71], v178 offset:9216\n\t" \
    "ds_read_b64_tr_b16 v[72:73], v178 offset:16384\n\t" \
    "ds_read_b64_tr_b16 v[74:75], v178 offset:17408\n\t" \
    "ds_read_b64_tr_b16 v[76:77], v178 offset:24576\n\t" \
    "ds_read_b64_tr_b16 v[78:79], v178 offset:25600\n\t" \
    "v_mov_b32 v228, 0x3f803f80\n\t" \
    "v_mov_b32 v229, 0x3f803f80\n\t" \
    "v_mov_b32 v230, 0x3f803f80\n\t" \
    "v_mov_b32 v231, 0x3f803f80\n\t" \
    "v_mov_b64 v[216:217], 0\n\t" \
    "v_mov_b32 v219, 0\n\t" \
    "v_bfe_u32 v255, v253, 2, 5\n\t" \
    "v_add_u32 v255, 0x7000, v255\n\t" \
    "v_mov_b32 v218, 0\n\t" \
    "s_waitcnt lgkmcnt(0)\n\t" \
    "s_barrier \n\t" \
    "ds_write_b32 v255, v218\n\t" \
    "s_waitcnt lgkmcnt(0)\n\t" \
    "s_barrier \n\t" \
    ".Lw21_%=:\n\t" \
    "v_mov_b32 v248, 0\n\t" \
    "buffer_load_dword v248, v248, %[rs], %[go] offen sc1\n\t" \
    "s_waitcnt vmcnt(0)\n\t" \
    "v_cmp_ne_u32 vcc, 0, v248\n\t" \
    "s_cbranch_vccnz .Lw22_%=\n\t" \
    "s_cmp_eq_u32 s41, 0\n\t" \
    "s_cbranch_scc1 .Lw22_%=\n\t" \
    "s_sub_u32 s41, s41, 1\n\t" \
    "s_sleep 16\n\t" \
    "s_branch .Lw21_%=\n\t" \
    ".Lw22_%=:\n\t" \
    "s_lshr_b32 s48, %[wave], 2\n\t" \
    "s_mul_i32 s48, s48, 0x3000\n\t" \
    "s_add_u32 s48, s48, %[pi]\n\t" \
    "s_mov_b32 s43, 0\n\t" \
    "s_add_u32 s46, s43, s48\n\t" \
    "s_add_u32 s47, s46, 0x0\n\t" \
    "buffer_load_dwordx4 v[208:211], v253, %[rs], s47 offen sc1\n\t" \
    "s_add_u32 s47, s46, 0x8000\n\t" \
    "buffer_load_dwordx4 v[212:215], v253, %[rs], s47 offen sc1\n\t" \
    "s_cmp_gt_u32 %[wave], 3\n\t" \
    "s_cbranch_scc1 .Lw23_%=\n\t" \
    "s_add_u32 s47, s46, 0x10000\n\t" \
    "buffer_load_dwordx4 v[216:219], v253, %[rs], s47 offen sc1\n\t" \
    ".Lw23_%=:\n\t" \
    "s_waitcnt vmcnt(0)\n\t" \
    ".Lw24_%=:\n\t" \
    "s_add_u32 s49, s43, 0x2c00000\n\t" \
    "s_add_u32 s50, s43, 0x1c00000\n\t" \
    "s_lshr_b32 s44, s40, 6\n\t" \
    "s_lshl_b32 s44, s44, 14\n\t" \
    "s_and_b32 s45, s40, 63\n\t" \
    "s_lshl_b32 s45, s45, 7\n\t" \
    "s_add_u32 s44, s44, s45\n\t" \
    "s_add_u32 s51, s44, 0x4d0000\n\t" \
    "s_add_i32 s44, s40, 1\n\t" \
    "s_max_i32 s44, s44, 0\n\t" \
    "s_lshr_b32 s45, s44, 1\n\t" \
    "s_and_b32 s44, s44, 1\n\t" \
    "s_lshl_b32 s44, s44, 12\n\t" \
    "s_mul_i32 s43, s45, 0x14000\n\t" \
    "s_add_u32 s43, s43, s44\n\t" \
    "s_lshr_b32 s45, s45, 4\n\t" \
    "s_mul_i32 s45, s45, 0xc2000\n\t" \
    "s_add_u32 s43, s43, s45\n\t" \
    LST0 \
    LJIT \
    "s_setprio 2\n\t" \
    "v_max_u32 v248, v208, v209\n\t" \
    "v_max3_u32 v248, v210, v211, v248\n\t" \
    "v_max3_u32 v248, v212, v213, v248\n\t" \
    "v_max3_u32 v248, v214, v215, v248\n\t" \
    "v_max3_u32 v248, v216, v217, v248\n\t" \
    "v_max3_u32 v248, v218, v219, v248\n\t" \
    "v_cmp_eq_u32 vcc, -1, v248\n\t" \
    "s_cbranch_vccz .Lw26_%=\n\t" \
    "s_and_b32 s44, s40, 1\n\t" \
    "s_cmp_eq_u32 s44, 0\n\t" \
    "s_cbranch_scc1 .Lw25_%=\n\t" \
    "s_sleep 6\n\t" \
    ".Lw25_%=:\n\t" \
    "v_max_u32 v248, v208, v209\n\t" \
    "v_max3_u32 v248, v210, v211, v248\n\t" \
    "v_cmp_eq_u32 vcc, -1, v248\n\t" \
    "s_cbranch_vccz .Lw28_%=\n\t" \
    "s_add_u32 s47, s46, 0x0\n\t" \
    "buffer_load_dwordx4 v[208:211], v253, %[rs], s47 offen sc1\n\t" \
    ".Lw28_%=:\n\t" \
    "v_max_u32 v248, v212, v213\n\t" \
    "v_max3_u32 v248, v214, v215, v248\n\t" \
    "v_cmp_eq_u32 vcc, -1, v248\n\t" \
    "s_cbranch_vccz .Lw29_%=\n\t" \
    "s_add_u32 s47, s46, 0x8000\n\t" \
    "buffer_load_dwordx4 v[212:215], v253, %[rs], s47 offen sc1\n\t" \
    ".Lw29_%=:\n\t" \
    "v_max_u32 v248, v216, v217\n\t" \
    "v_max3_u32 v248, v218, v219, v248\n\t" \
    "v_cmp_eq_u32 vcc, -1, v248\n\t" \
    "s_cbranch_vccz .Lw210_%=\n\t" \
    "s_add_u32 s47, s46, 0x10000\n\t" \
    "buffer_load_dwordx4 v[216:219], v253, %[rs], s47 offen sc1\n\t" \
    ".Lw210_%=:\n\t" \
    "s_waitcnt vmcnt(0)\n\t" \
    "v_max_u32 v248, v208, v209\n\t" \
    "v_max3_u32 v248, v210, v211, v248\n\t" \
    "v_max3_u32 v248, v212, v213, v248\n\t" \
    "v_max3_u32 v248, v214, v215, v248\n\t" \
    "v_max3_u32 v248, v216, v217, v248\n\t" \
    "v_max3_u32 v248, v218, v219, v248\n\t" \
    "v_cmp_eq_u32 vcc, -1, v248\n\t" \
    "s_cbranch_vccz .Lw26_%=\n\t" \
    "s_cmp_eq_u32 s41, 0\n\t" \
    "s_cbranch_scc1 .Lw26_%=\n\t" \
    "s_sub_u32 s41, s41, 1\n\t" \
    "s_branch .Lw25_%=\n\t" \
    ".Lw26_%=:\n\t" \
    LST1 \
    LJIT \
    "ds_write_b128 v250, v[208:211] offset:0\n\t" \
    "ds_write_b128 v250, v[212:215] offset:8192\n\t" \
    "s_cmp_gt_u32 %[wave], 3\n\t" \
    "s_cbranch_scc1 .Lw211_%=\n\t" \
    "ds_write_b128 v250, v[216:219] offset:16384\n\t" \
    ".Lw211_%=:\n\t" \
    "s_add_u32 s46, s43, s48\n\t" \
    "s_waitcnt lgkmcnt(0)\n\t" \
    "s_barrier \n\t" \
    "s_setprio 0\n\t" \
    LST2 \
    LJIT \
    "ds_read_b128 v[176:179], v249 offset:0\n\t" \
    "buffer_load_dwordx2 v[232:233], v254, %[rs], s50 offen sc1\n\t" \
    "buffer_load_dwordx2 v[234:235], v251, %[rs], s51 offen sc1\n\t" \
    "ds_read_b128 v[180:183], v249 offset:1024\n\t" \
    "ds_read_b128 v[184:187], v249 offset:2048\n\t" \
    "ds_read_b128 v[188:191], v249 offset:3072\n\t" \
    "ds_read_b128 v[192:195], v249 offset:4096\n\t" \
    "ds_read_b128 v[196:199], v249 offset:5120\n\t" \
    "ds_read_b128 v[200:203], v249 offset:6144\n\t" \
    "s_waitcnt lgkmcnt(6)\n\t" \
    "v_mfma_f32_16x16x32_bf16 v[168:171], v[0:3], v[176:179], 0\n\t" \
    "ds_read_b128 v[204:207], v249 offset:7168\n\t" \
    "s_waitcnt lgkmcnt(6)\n\t" \
    "v_mfma_f32_16x16x32_bf16 v[168:171], v[4:7], v[180:183], v[168:171]\n\t" \
    "ds_read_b128 v[176:179], v249 offset:8192\n\t" \
    "s_waitcnt lgkmcnt(6)\n\t" \
    "v_mfma_f32_16x16x32_bf16 v[168:171], v[8:11], v[184:187], v[168:171]\n\t" \
    "ds_read_b128 v[180:183], v249 offset:9216\n\t" \
    "s_waitcnt lgkmcnt(6)\n\t" \
    "v_mfma_f32_16x16x32_bf16 v[168:171], v[12:15], v[188:191], v[168:171]\n\t" \
    "ds_read_b128 v[184:187], v249 offset:10240\n\t" \
    "s_add_u32 s47, s46, 0x0\n\t" \
    "buffer_load_dwordx4 v[208:211], v253, %[rs], s47 offen sc1\n\t" \
    "s_add_u32 s47, s46, 0x8000\n\t" \
    "buffer_load_dwordx4 v[212:215], v253, %[rs], s47 offen sc1\n\t" \
    "s_cmp_gt_u32 %[wave], 3\n\t" \
    "s_cbranch_scc1 .Lw212_%=\n\t" \
    "s_add_u32 s47, s46, 0x10000\n\t" \
    "buffer_load_dwordx4 v[216:219], v253, %[rs], s47 offen sc1\n\t" \
    ".Lw212_%=:\n\t" \
    "s_waitcnt lgkmcnt(6)\n\t" \
    "v_mfma_f32_16x16x32_bf16 v[168:171], v[16:19], v[192:195], v[168:171]\n\t" \
    "ds_read_b128 v[188:191], v249 offset:11264\n\t" \
    "s_waitcnt lgkmcnt(6)\n\t" \
    "v_mfma_f32_16x16x32_bf16 v[168:171], v[20:23], v[196:199], v[168:171]\n\t" \
    "ds_read_b128 v[192:195], v249 offset:12288\n\t" \
    "s_waitcnt lgkmcnt(6)\n\t" \
    "v_mfma_f32_16x16x32_bf16 v[168:171], v[24:27], v[200:203], v[168:171]\n\t" \
    "ds_read_b128 v[196:199], v249 offset:13312\n\t" \
    "s_waitcnt lgkmcnt(6)\n\t" \
    "v_mfma_f32_16x16x32_bf16 v[168:171], v[28:31], v[204:207], v[168:171]\n\t" \
    "ds_read_b128 v[200:203], v249 offset:14336\n\t" \
    "s_waitcnt lgkmcnt(6)\n\t" \
    "v_mfma_f32_16x16x32_bf16 v[168:171], v[32:35], v[176:179], v[168:171]\n\t" \
    "ds_read_b128 v[204:207], v249 offset:15360\n\t" \
    "s_waitcnt lgkmcnt(6)\n\t" \
    "v_mfma_f32_16x16x32_bf16 v[168:171], v[36:39], v[180:183], v[168:171]\n\t" \
    "ds_read_b128 v[176:179], v249 offset:16384\n\t" \
    "s_waitcnt lgkmcnt(6)\n\t" \
    "v_mfma_f32_16x16x32_bf16 v[168:171], v[40:43], v[184:187], v[168:171]\n\t" \
    "ds_read_b128 v[180:183], v249 offset:17408\n\t" \
    "s_waitcnt lgkmcnt(6)\n\t" \
    "v_mfma_f32_16x16x32_bf16 v[168:171], v[44:47], v[188:191], v[168:171]\n\t" \
    "ds_read_b128 v[184:187], v249 offset:18432\n\t" \
    "s_waitcnt lgkmcnt(6)\n\t" \
    "v_mfma_f32_16x16x32_bf16 v[168:171], v[48:51], v[192:195], v[168:171]\n\t" \
    "ds_read_b128 v[188:191], v249 offset:19456\n\t" \
    "s_waitcnt lgkmcnt(6)\n\t" \
    "v_mfma_f32_16x16x32_bf16 v[168:171], v[52:55], v[196:199], v[168:171]\n\t" \
    "s_waitcnt lgkmcnt(5)\n\t" \
    "v_mfma_f32_16x16x32_bf16 v[168:171], v[56:59], v[200:203], v[168:171]\n\t" \
    "s_waitcnt lgkmcnt(4)\n\t" \
    "v_mfma_f32_16x16x32_bf16 v[168:171], v[60:63], v[204:207], v[168:171]\n\t" \
    "s_waitcnt lgkmcnt(3)\n\t" \
    "v_mfma_f32_16x16x32_bf16 v[168:171], v[64:67], v[176:179], v[168:171]\n\t" \
    "s_waitcnt lgkmcnt(2)\n\t" \
    "v_mfma_f32_16x16x32_bf16 v[168:171], v[68:71], v[180:183], v[168:171]\n\t" \
    "s_waitcnt lgkmcnt(1)\n\t" \
    "v_mfma_f32_16x16x32_bf16 v[168:171], v[72:75], v[184:187], v[168:171]\n\t" \
    "s_waitcnt lgkmcnt(0)\n\t" \
    "v_mfma_f32_16x16x32_bf16 v[168:171], v[76:79], v[188:191], v[168:171]\n\t" \
    LST3 \
    LJIT \
    "s_waitcnt vmcnt(0)\n\t" \
    ".Lw213_%=:\n\t" \
    "v_max_u32 v248, v234, v235\n\t" \
    "v_cmp_eq_u32 vcc, -1, v248\n\t" \
    "s_cbranch_vccz .Lw214_%=\n\t" \
    "s_cmp_eq_u32 s41, 0\n\t" \
    "s_cbranch_scc1 .Lw214_%=\n\t" \
    "s_sub_u32 s41, s41, 1\n\t" \
    "buffer_load_dwordx2 v[234:235], v251, %[rs], s51 offen sc1\n\t" \
    "s_waitcnt vmcnt(0)\n\t" \
    "s_branch .Lw213_%=\n\t" \
    ".Lw214_%=:\n\t" \
    "v_lshlrev_b32 v176, 16, v232\n\t" \
    "v_and_b32 v177, 0xffff0000, v232\n\t" \
    "v_lshlrev_b32 v178, 16, v233\n\t" \
    "v_and_b32 v179, 0xffff0000, v233\n\t" \
    "v_sub_f32 v176, v176, v234\n\t" \
    "v_sub_f32 v177, v177, v234\n\t" \
    "v_sub_f32 v178, v178, v234\n\t" \
    "v_sub_f32 v179, v179, v234\n\t" \
    "v_mul_f32 v176, v176, v235\n\t" \
    "v_mul_f32 v177, v177, v235\n\t" \
    "v_mul_f32 v178, v178, v235\n\t" \
    "v_mul_f32 v179, v179, v235\n\t" \
    "v_cvt_pk_bf16_f32 v180, v176, v177\n\t" \
    "v_cvt_pk_bf16_f32 v181, v178, v179\n\t" \
    "v_lshlrev_b32 v176, 16, v180\n\t" \
    "v_and_b32 v177, 0xffff0000, v180\n\t" \
    "v_lshlrev_b32 v178, 16, v181\n\t" \
    "v_and_b32 v179, 0xffff0000, v181\n\t" \
    "v_add_f32 v168, v176, v168\n\t" \
    "v_add_f32 v169, v177, v169\n\t" \
    "v_add_f32 v170, v178, v170\n\t" \
    "v_add_f32 v171, v179, v171\n\t" \
    "v_cvt_pk_bf16_f32 v184, v168, v169\n\t" \
    "v_cvt_pk_bf16_f32 v185, v170, v171\n\t" \
    "v_min_u32 v184, -2, v184\n\t" \
    "v_min_u32 v185, -2, v185\n\t" \
    "buffer_store_dwordx2 v[184:185], v254, %[rs], s49 offen sc1\n\t" \
    LST4 \
    LJIT \
    "s_cmp_ge_u32 s40, 122\n\t" \
    "s_cbranch_scc1 .Lw215_%=\n\t" \
    "s_add_i32 s44, s40, 6\n\t" \
    "s_max_i32 s44, s44, 0\n\t" \
    "s_lshr_b32 s45, s44, 1\n\t" \
    "s_and_b32 s44, s44, 1\n\t" \
    "s_lshl_b32 s44, s44, 12\n\t" \
    "s_mul_i32 s50, s45, 0x14000\n\t" \
    "s_add_u32 s50, s50, s44\n\t" \
    "s_lshr_b32 s45, s45, 4\n\t" \
    "s_mul_i32 s45, s45, 0xc2000\n\t" \
    "s_add_u32 s50, s50, s45\n\t" \
    "s_add_u32 s50, s50, 0x2c00000\n\t" \
    "v_mov_b64 v[188:189], -1\n\t" \
    "buffer_store_dwordx2 v[188:189], v254, %[rs], s50 offen sc1\n\t" \
    ".Lw215_%=:\n\t" \
    "v_xor_b32 v249, 0x8000, v249\n\t" \
    "v_xor_b32 v250, 0x8000, v250\n\t" \
    LFLUSHW \
    "s_add_u32 s40, s40, 1\n\t" \
    "s_cmp_lg_u32 s40, 128\n\t" \
    "s_cbranch_scc1 .Lw24_%=\n\t" \
    "s_branch .Lend%=\n\t" \
    ".Lend%=:\n\t" \
    "s_waitcnt vmcnt(0) lgkmcnt(0)\n\t" \
    "s_mov_b32 %[bud], s41\n\t"
#define ASM_W3_LOOP \
    "s_mov_b32 s40, 0\n\t" \
    "s_mov_b32 s41, 0x20000\n\t" \
    LSTINIT \
    "s_add_u32 m0, %[wm], 0x0\n\t" \
    "s_add_u32 s44, %[wo], 0x0\n\t" \
    "buffer_load_dwordx4 v176, %[wr], s44 offen nt lds\n\t" \
    "s_add_u32 m0, %[wm], 0x2000\n\t" \
    "s_add_u32 s44, %[wo], 0xc000\n\t" \
    "buffer_load_dwordx4 v176, %[wr], s44 offen nt lds\n\t" \
    "s_add_u32 m0, %[wm], 0x4000\n\t" \
    "s_add_u32 s44, %[wo], 0x18000\n\t" \
    "buffer_load_dwordx4 v176, %[wr], s44 offen nt lds\n\t" \
    "s_add_u32 m0, %[wm], 0x6000\n\t" \
    "s_add_u32 s44, %[wo], 0x24000\n\t" \
    "buffer_load_dwordx4 v176, %[wr], s44 offen nt lds\n\t" \
    "s_add_u32 m0, %[wm], 0x8000\n\t" \
    "s_add_u32 s44, %[wo], 0x30000\n\t" \
    "buffer_load_dwordx4 v176, %[wr], s44 offen nt lds\n\t" \
    "s_add_u32 m0, %[wm], 0xa000\n\t" \
    "s_add_u32 s44, %[wo], 0x3c000\n\t" \
    "buffer_load_dwordx4 v176, %[wr], s44 offen nt lds\n\t" \
    "s_add_u32 m0, %[wm], 0xc000\n\t" \
    "s_add_u32 s44, %[wo], 0x48000\n\t" \
    "buffer_load_dwordx4 v176, %[wr], s44 offen nt lds\n\t" \
    "s_add_u32 m0, %[wm], 0xe000\n\t" \
    "s_add_u32 s44, %[wo], 0x54000\n\t" \
    "buffer_load_dwordx4 v176, %[wr], s44 offen nt lds\n\t" \
    "s_add_u32 m0, %[wm], 0x10000\n\t" \
    "s_add_u32 s44, %[wo], 0x60000\n\t" \
    "buffer_load_dwordx4 v176, %[wr], s44 offen nt lds\n\t" \
    "s_add_u32 m0, %[wm], 0x12000\n\t" \
    "s_add_u32 s44, %[wo], 0x6c000\n\t" \
    "buffer_load_dwordx4 v176, %[wr], s44 offen nt lds\n\t" \
    "s_add_u32 m0, %[wm], 0x14000\n\t" \
    "s_add_u32 s44, %[wo], 0x78000\n\t" \
    "buffer_load_dwordx4 v176, %[wr], s44 offen nt lds\n\t" \
    "s_add_u32 m0, %[wm], 0x16000\n\t" \
    "s_add_u32 s44, %[wo], 0x84000\n\t" \
    "buffer_load_dwordx4 v176, %[wr], s44 offen nt lds\n\t" \
    "s_add_u32 m0, %[wm], 0x18000\n\t" \
    "s_add_u32 s44, %[wo], 0x90000\n\t" \
    "buffer_load_dwordx4 v176, %[wr], s44 offen nt lds\n\t" \
    "s_add_u32 m0, %[wm], 0x1a000\n\t" \
    "s_add_u32 s44, %[wo], 0x9c000\n\t" \
    "buffer_load_dwordx4 v176, %[wr], s44 offen nt lds\n\t" \
    "s_add_u32 m0, %[wm], 0x1c000\n\t" \
    "s_add_u32 s44, %[wo], 0xa8000\n\t" \
    "buffer_load_dwordx4 v176, %[wr], s44 offen nt lds\n\t" \
    "s_add_u32 m0, %[wm], 0x1e000\n\t" \
    "s_add_u32 s44, %[wo], 0xb4000\n\t" \
    "buffer_load_dwordx4 v176, %[wr], s44 offen nt lds\n\t" \
    "s_add_u32 m0, %[wm], 0x20000\n\t" \
    "s_add_u32 s44, %[wo], 0xc0000\n\t" \
    "buffer_load_dwordx4 v176, %[wr], s44 offen nt lds\n\t" \
    "s_add_u32 m0, %[wm], 0x22000\n\t" \
    "s_add_u32 s44, %[wo], 0xcc000\n\t" \
    "buffer_load_dwordx4 v176, %[wr], s44 offen nt lds\n\t" \
    "s_add_u32 m0, %[wm], 0x24000\n\t" \
    "s_add_u32 s44, %[wo], 0xd8000\n\t" \
    "buffer_load_dwordx4 v176, %[wr], s44 offen nt lds\n\t" \
    "s_add_u32 m0, %[wm], 0x26000\n\t" \
    "s_add_u32 s44, %[wo], 0xe4000\n\t" \
    "buffer_load_dwordx4 v176, %[wr], s44 offen nt lds\n\t" \
    "s_waitcnt vmcnt(0) lgkmcnt(0)\n\t" \
    "s_barrier \n\t" \
    "v_add_u32 v178, 0x0, v177\n\t" \
    "v_xor_b32 v179, 32, v178\n\t" \
    "ds_read_b64_tr_b16 v[0:1], v178 offset:0\n\t" \
    "ds_read_b64_tr_b16 v[2:3], v178 offset:2048\n\t" \
    "ds_read_b64_tr_b16 v[80:81], v179 offset:0\n\t" \
    "ds_read_b64_tr_b16 v[82:83], v179 offset:2048\n\t" \
    "ds_read_b64_tr_b16 v[4:5], v178 offset:16384\n\t" \
    "ds_read_b64_tr_b16 v[6:7], v178 offset:18432\n\t" \
    "ds_read_b64_tr_b16 v[84:85], v179 offset:16384\n\t" \
    "ds_read_b64_tr_b16 v[86:87], v179 offset:18432\n\t" \
    "v_add_u32 v178, 0x8000, v177\n\t" \
    "v_xor_b32 v179, 32, v178\n\t" \
    "ds_read_b64_tr_b16 v[8:9], v178 offset:0\n\t" \
    "ds_read_b64_tr_b16 v[10:11], v178 offset:2048\n\t" \
    "ds_read_b64_tr_b16 v[88:89], v179 offset:0\n\t" \
    "ds_read_b64_tr_b16 v[90:91], v179 offset:2048\n\t" \
    "ds_read_b64_tr_b16 v[12:13], v178 offset:16384\n\t" \
    "ds_read_b64_tr_b16 v[14:15], v178 offset:18432\n\t" \
    "ds_read_b64_tr_b16 v[92:93], v179 offset:16384\n\t" \
    "ds_read_b64_tr_b16 v[94:95], v179 offset:18432\n\t" \
    "v_add_u32 v178, 0x10000, v177\n\t" \
    "v_xor_b32 v179, 32, v178\n\t" \
    "ds_read_b64_tr_b16 v[16:17], v178 offset:0\n\t" \
    "ds_read_b64_tr_b16 v[18:19], v178 offset:2048\n\t" \
    "ds_read_b64_tr_b16 v[96:97], v179 offset:0\n\t" \
    "ds_read_b64_tr_b16 v[98:99], v179 offset:2048\n\t" \
    "ds_read_b64_tr_b16 v[20:21], v178 offset:16384\n\t" \
    "ds_read_b64_tr_b16 v[22:23], v178 offset:18432\n\t" \
    "ds_read_b64_tr_b16 v[100:101], v179 offset:16384\n\t" \
    "ds_read_b64_tr_b16 v[102:103], v179 offset:18432\n\t" \
    "v_add_u32 v178, 0x18000, v177\n\t" \
    "v_xor_b32 v179, 32, v178\n\t" \
    "ds_read_b64_tr_b16 v[24:25], v178 offset:0\n\t" \
    "ds_read_b64_tr_b16 v[26:27], v178 offset:2048\n\t" \
    "ds_read_b64_tr_b16 v[104:105], v179 offset:0\n\t" \
    "ds_read_b64_tr_b16 v[106:107], v179 offset:2048\n\t" \
    "ds_read_b64_tr_b16 v[28:29], v178 offset:16384\n\t" \
    "ds_read_b64_tr_b16 v[30:31], v178 offset:18432\n\t" \
    "ds_read_b64_tr_b16 v[108:109], v179 offset:16384\n\t" \
    "ds_read_b64_tr_b16 v[110:111], v179 offset:18432\n\t" \
    "v_add_u32 v178, 0x20000, v177\n\t" \
    "v_xor_b32 v179, 32, v178\n\t" \
    "ds_read_b64_tr_b16 v[32:33], v178 offset:0\n\t" \
    "ds_read_b64_tr_b16 v[34:35], v178 offset:2048\n\t" \
    "ds_read_b64_tr_b16 v[112:113], v179 offset:0\n\t" \
    "ds_read_b64_tr_b16 v[114:115], v179 offset:2048\n\t" \
    "ds_read_b64_tr_b16 v[36:37], v178 offset:16384\n\t" \
    "ds_read_b64_tr_b16 v[38:39], v178 offset:18432\n\t" \
    "ds_read_b64_tr_b16 v[116:117], v179 offset:16384\n\t" \
    "ds_read_b64_tr_b16 v[118:119], v179 offset:18432\n\t" \
    "s_waitcnt lgkmcnt(0)\n\t" \
    "s_barrier \n\t" \
    "s_add_u32 m0, %[wm], 0x0\n\t" \
    "s_add_u32 s44, %[wo], 0xf0000\n\t" \
    "buffer_load_dwordx4 v176, %[wr], s44 offen nt lds\n\t" \
    "s_add_u32 m0, %[wm], 0x2000\n\t" \
    "s_add_u32 s44, %[wo], 0xfc000\n\t" \
    "buffer_load_dwordx4 v176, %[wr], s44 offen nt lds\n\t" \
    "s_add_u32 m0, %[wm], 0x4000\n\t" \
    "s_add_u32 s44, %[wo], 0x108000\n\t" \
    "buffer_load_dwordx4 v176, %[wr], s44 offen nt lds\n\t" \
    "s_add_u32 m0, %[wm], 0x6000\n\t" \
    "s_add_u32 s44, %[wo], 0x114000\n\t" \
    "buffer_load_dwordx4 v176, %[wr], s44 offen nt lds\n\t" \
    "s_add_u32 m0, %[wm], 0x8000\n\t" \
    "s_add_u32 s44, %[wo], 0x120000\n\t" \
    "buffer_load_dwordx4 v176, %[wr], s44 offen nt lds\n\t" \
    "s_add_u32 m0, %[wm], 0xa000\n\t" \
    "s_add_u32 s44, %[wo], 0x12c000\n\t" \
    "buffer_load_dwordx4 v176, %[wr], s44 offen nt lds\n\t" \
    "s_add_u32 m0, %[wm], 0xc000\n\t" \
    "s_add_u32 s44, %[wo], 0x138000\n\t" \
    "buffer_load_dwordx4 v176, %[wr], s44 offen nt lds\n\t" \
    "s_add_u32 m0, %[wm], 0xe000\n\t" \
    "s_add_u32 s44, %[wo], 0x144000\n\t" \
    "buffer_load_dwordx4 v176, %[wr], s44 offen nt lds\n\t" \
    "s_add_u32 m0, %[wm], 0x10000\n\t" \
    "s_add_u32 s44, %[wo], 0x150000\n\t" \
    "buffer_load_dwordx4 v176, %[wr], s44 offen nt lds\n\t" \
    "s_add_u32 m0, %[wm], 0x12000\n\t" \
    "s_add_u32 s44, %[wo], 0x15c000\n\t" \
    "buffer_load_dwordx4 v176, %[wr], s44 offen nt lds\n\t" \
    "s_add_u32 m0, %[wm], 0x14000\n\t" \
    "s_add_u32 s44, %[wo], 0x168000\n\t" \
    "buffer_load_dwordx4 v176, %[wr], s44 offen nt lds\n\t" \
    "s_add_u32 m0, %[wm], 0x16000\n\t" \
    "s_add_u32 s44, %[wo], 0x174000\n\t" \
    "buffer_load_dwordx4 v176, %[wr], s44 offen nt lds\n\t" \
    "s_add_u32 m0, %[wm], 0x18000\n\t" \
    "s_add_u32 s44, %[wo], 0x180000\n\t" \
    "buffer_load_dwordx4 v176, %[wr], s44 offen nt lds\n\t" \
    "s_add_u32 m0, %[wm], 0x1a000\n\t" \
    "s_add_u32 s44, %[wo], 0x18c000\n\t" \
    "buffer_load_dwordx4 v176, %[wr], s44 offen nt lds\n\t" \
    "s_add_u32 m0, %[wm], 0x1c000\n\t" \
    "s_add_u32 s44, %[wo], 0x198000\n\t" \
    "buffer_load_dwordx4 v176, %[wr], s44 offen nt lds\n\t" \
    "s_add_u32 m0, %[wm], 0x1e000\n\t" \
    "s_add_u32 s44, %[wo], 0x1a4000\n\t" \
    "buffer_load_dwordx4 v176, %[wr], s44 offen nt lds\n\t" \
    "s_add_u32 m0, %[wm], 0x20000\n\t" \
    "s_add_u32 s44, %[wo], 0x1b0000\n\t" \
    "buffer_load_dwordx4 v176, %[wr], s44 offen nt lds\n\t" \
    "s_add_u32 m0, %[wm], 0x22000\n\t" \
    "s_add_u32 s44, %[wo], 0x1bc000\n\t" \
    "buffer_load_dwordx4 v176, %[wr], s44 offen nt lds\n\t" \
    "s_add_u32 m0, %[wm], 0x24000\n\t" \
    "s_add_u32 s44, %[wo], 0x1c8000\n\t" \
    "buffer_load_dwordx4 v176, %[wr], s44 offen nt lds\n\t" \
    "s_add_u32 m0, %[wm], 0x26000\n\t" \
    "s_add_u32 s44, %[wo], 0x1d4000\n\t" \
    "buffer_load_dwordx4 v176, %[wr], s44 offen nt lds\n\t" \
    "s_waitcnt vmcnt(0) lgkmcnt(0)\n\t" \
    "s_barrier \n\t" \
    "v_add_u32 v178, 0x0, v177\n\t" \
    "v_xor_b32 v179, 32, v178\n\t" \
    "ds_read_b64_tr_b16 v[40:41], v178 offset:0\n\t" \
    "ds_read_b64_tr_b16 v[42:43], v178 offset:2048\n\t" \
    "ds_read_b64_tr_b16 v[120:121], v179 offset:0\n\t" \
    "ds_read_b64_tr_b16 v[122:123], v179 offset:2048\n\t" \
    "ds_read_b64_tr_b16 v[44:45], v178 offset:16384\n\t" \
    "ds_read_b64_tr_b16 v[46:47], v178 offset:18432\n\t" \
    "ds_read_b64_tr_b16 v[124:125], v179 offset:16384\n\t" \
    "ds_read_b64_tr_b16 v[126:127], v179 offset:18432\n\t" \
    "v_add_u32 v178, 0x8000, v177\n\t" \
    "v_xor_b32 v179, 32, v178\n\t" \
    "ds_read_b64_tr_b16 v[48:49], v178 offset:0\n\t" \
    "ds_read_b64_tr_b16 v[50:51], v178 offset:2048\n\t" \
    "ds_read_b64_tr_b16 v[128:129], v179 offset:0\n\t" \
    "ds_read_b64_tr_b16 v[130:131], v179 offset:2048\n\t" \
    "ds_read_b64_tr_b16 v[52:53], v178 offset:16384\n\t" \
    "ds_read_b64_tr_b16 v[54:55], v178 offset:18432\n\t" \
    "ds_read_b64_tr_b16 v[132:133], v179 offset:16384\n\t" \
    "ds_read_b64_tr_b16 v[134:135], v179 offset:18432\n\t" \
    "v_add_u32 v178, 0x10000, v177\n\t" \
    "v_xor_b32 v179, 32, v178\n\t" \
    "ds_read_b64_tr_b16 v[56:57], v178 offset:0\n\t" \
    "ds_read_b64_tr_b16 v[58:59], v178 offset:2048\n\t" \
    "ds_read_b64_tr_b16 v[136:137], v179 offset:0\n\t" \
    "ds_read_b64_tr_b16 v[138:139], v179 offset:2048\n\t" \
    "ds_read_b64_tr_b16 v[60:61], v178 offset:16384\n\t" \
    "ds_read_b64_tr_b16 v[62:63], v178 offset:18432\n\t" \
    "ds_read_b64_tr_b16 v[140:141], v179 offset:16384\n\t" \
    "ds_read_b64_tr_b16 v[142:143], v179 offset:18432\n\t" \
    "v_add_u32 v178, 0x18000, v177\n\t" \
    "v_xor_b32 v179, 32, v178\n\t" \
    "ds_read_b64_tr_b16 v[64:65], v178 offset:0\n\t" \
    "ds_read_b64_tr_b16 v[66:67], v178 offset:2048\n\t" \
    "ds_read_b64_tr_b16 v[144:145], v179 offset:0\n\t" \
    "ds_read_b64_tr_b16 v[146:147], v179 offset:2048\n\t" \
    "ds_read_b64_tr_b16 v[68:69], v178 offset:16384\n\t" \
    "ds_read_b64_tr_b16 v[70:71], v178 offset:18432\n\t" \
    "ds_read_b64_tr_b16 v[148:149], v179 offset:16384\n\t" \
    "ds_read_b64_tr_b16 v[150:151], v179 offset:18432\n\t" \
    "v_add_u32 v178, 0x20000, v177\n\t" \
    "v_xor_b32 v179, 32, v178\n\t" \
    "ds_read_b64_tr_b16 v[72:73], v178 offset:0\n\t" \
    "ds_read_b64_tr_b16 v[74:75], v178 offset:2048\n\t" \
    "ds_read_b64_tr_b16 v[152:153], v179 offset:0\n\t" \
    "ds_read_b64_tr_b16 v[154:155], v179 offset:2048\n\t" \
    "ds_read_b64_tr_b16 v[76:77], v178 offset:16384\n\t" \
    "ds_read_b64_tr_b16 v[78:79], v178 offset:18432\n\t" \
    "ds_read_b64_tr_b16 v[156:157], v179 offset:16384\n\t" \
    "ds_read_b64_tr_b16 v[158:159], v179 offset:18432\n\t" \
    "v_mov_b32 v228, 0x3f803f80\n\t" \
    "v_mov_b32 v229, 0x3f803f80\n\t" \
    "v_mov_b32 v230, 0x3f803f80\n\t" \
    "v_mov_b32 v231, 0x3f803f80\n\t" \
    "v_mov_b64 v[216:217], 0\n\t" \
    "v_mov_b32 v219, 0\n\t" \
    "v_bfe_u32 v255, v253, 2, 5\n\t" \
    "v_add_u32 v255, 0x7000, v255\n\t" \
    "v_bfe_u32 v218, v253, 6, 1\n\t" \
    "s_waitcnt lgkmcnt(0)\n\t" \
    "s_barrier \n\t" \
    "ds_write_b32 v251, v228 offset:72\n\t" \
    "ds_write_b32 v251, v228 offset:2120\n\t" \
    "v_mfma_f32_16x16x32_bf16 v[160:163], v[0:3], v[228:231], 0\n\t" \
    "v_mfma_f32_16x16x32_bf16 v[164:167], v[80:83], v[228:231], 0\n\t" \
    "v_mfma_f32_16x16x32_bf16 v[160:163], v[4:7], v[228:231], v[160:163]\n\t" \
    "v_mfma_f32_16x16x32_bf16 v[164:167], v[84:87], v[228:231], v[164:167]\n\t" \
    "v_mfma_f32_16x16x32_bf16 v[160:163], v[8:11], v[228:231], v[160:163]\n\t" \
    "v_mfma_f32_16x16x32_bf16 v[164:167], v[88:91], v[228:231], v[164:167]\n\t" \
    "v_mfma_f32_16x16x32_bf16 v[160:163], v[12:15], v[228:231], v[160:163]\n\t" \
    "v_mfma_f32_16x16x32_bf16 v[164:167], v[92:95], v[228:231], v[164:167]\n\t" \
    "v_mfma_f32_16x16x32_bf16 v[160:163], v[16:19], v[228:231], v[160:163]\n\t" \
    "v_mfma_f32_16x16x32_bf16 v[164:167], v[96:99], v[228:231], v[164:167]\n\t" \
    "v_mfma_f32_16x16x32_bf16 v[160:163], v[20:23], v[228:231], v[160:163]\n\t" \
    "v_mfma_f32_16x16x32_bf16 v[164:167], v[100:103], v[228:231], v[164:167]\n\t" \
    "v_mfma_f32_16x16x32_bf16 v[160:163], v[24:27], v[228:231], v[160:163]\n\t" \
    "v_mfma_f32_16x16x32_bf16 v[164:167], v[104:107], v[228:231], v[164:167]\n\t" \
    "v_mfma_f32_16x16x32_bf16 v[160:163], v[28:31], v[228:231], v[160:163]\n\t" \
    "v_mfma_f32_16x16x32_bf16 v[164:167], v[108:111], v[228:231], v[164:167]\n\t" \
    "v_mfma_f32_16x16x32_bf16 v[160:163], v[32:35], v[228:231], v[160:163]\n\t" \
    "v_mfma_f32_16x16x32_bf16 v[164:167], v[112:115], v[228:231], v[164:167]\n\t" \
    "v_mfma_f32_16x16x32_bf16 v[160:163], v[36:39], v[228:231], v[160:163]\n\t" \
    "v_mfma_f32_16x16x32_bf16 v[164:167], v[116:119], v[228:231], v[164:167]\n\t" \
    "v_mfma_f32_16x16x32_bf16 v[160:163], v[40:43], v[228:231], v[160:163]\n\t" \
    "v_mfma_f32_16x16x32_bf16 v[164:167], v[120:123], v[228:231], v[164:167]\n\t" \
    "v_mfma_f32_16x16x32_bf16 v[160:163], v[44:47], v[228:231], v[160:163]\n\t" \
    "v_mfma_f32_16x16x32_bf16 v[164:167], v[124:127], v[228:231], v[164:167]\n\t" \
    "v_mfma_f32_16x16x32_bf16 v[160:163], v[48:51], v[228:231], v[160:163]\n\t" \
    "v_mfma_f32_16x16x32_bf16 v[164:167], v[128:131], v[228:231], v[164:167]\n\t" \
    "v_mfma_f32_16x16x32_bf16 v[160:163], v[52:55], v[228:231], v[160:163]\n\t" \
    "v_mfma_f32_16x16x32_bf16 v[164:167], v[132:135], v[228:231], v[164:167]\n\t" \
    "v_mfma_f32_16x16x32_bf16 v[160:163], v[56:59], v[228:231], v[160:163]\n\t" \
    "v_mfma_f32_16x16x32_bf16 v[164:167], v[136:139], v[228:231], v[164:167]\n\t" \
    "v_mfma_f32_16x16x32_bf16 v[160:163], v[60:63], v[228:231], v[160:163]\n\t" \
    "v_mfma_f32_16x16x32_bf16 v[164:167], v[140:143], v[228:231], v[164:167]\n\t" \
    "v_mfma_f32_16x16x32_bf16 v[160:163], v[64:67], v[228:231], v[160:163]\n\t" \
    "v_mfma_f32_16x16x32_bf16 v[164:167], v[144:147], v[228:231], v[164:167]\n\t" \
    "v_mfma_f32_16x16x32_bf16 v[160:163], v[68:71], v[228:231], v[160:163]\n\t" \
    "v_mfma_f32_16x16x32_bf16 v[164:167], v[148:151], v[228:231], v[164:167]\n\t" \
    "v_mfma_f32_16x16x32_bf16 v[160:163], v[72:75], v[228:231], v[160:163]\n\t" \
    "v_mfma_f32_16x16x32_bf16 v[164:167], v[152:155], v[228:231], v[164:167]\n\t" \
    "v_mfma_f32_16x16x32_bf16 v[160:163], v[76:79], v[228:231], v[160:163]\n\t" \
    "v_mfma_f32_16x16x32_bf16 v[164:167], v[156:159], v[228:231], v[164:167]\n\t" \
    "s_mov_b32 s52, 0x22222222\n\t" \
    "s_mov_b32 s53, 0x22222222\n\t" \
    "s_mov_b32 s54, 0x44444444\n\t" \
    "s_mov_b32 s55, 0x44444444\n\t" \
    "s_mov_b32 s56, 0x88888888\n\t" \
    "s_mov_b32 s57, 0x88888888\n\t" \
    "ds_write_b32 v255, v218\n\t" \
    "s_waitcnt lgkmcnt(0)\n\t" \
    "s_barrier \n\t" \
    ".Lw31_%=:\n\t" \
    "v_mov_b32 v248, 0\n\t" \
    "buffer_load_dword v248, v248, %[rs], %[go] offen sc1\n\t" \
    "s_waitcnt vmcnt(0)\n\t" \
    "v_cmp_ne_u32 vcc, 0, v248\n\t" \
    "s_cbranch_vccnz .Lw32_%=\n\t" \
    "s_cmp_eq_u32 s41, 0\n\t" \
    "s_cbranch_scc1 .Lw32_%=\n\t" \
    "s_sub_u32 s41, s41, 1\n\t" \
    "s_sleep 16\n\t" \
    "s_branch .Lw31_%=\n\t" \
    ".Lw32_%=:\n\t" \
    "s_mov_b32 s43, 0\n\t" \
    "s_add_u32 s46, s43, %[pi]\n\t" \
    "s_cmp_gt_u32 %[wave], 3\n\t" \
    "s_cbranch_scc1 .Lw33_%=\n\t" \
    "s_add_u32 s47, s46, 0x0\n\t" \
    "buffer_load_dwordx4 v[208:211], v253, %[rs], s47 offen sc1\n\t" \
    "s_add_u32 s47, s46, 0x4000\n\t" \
    "buffer_load_dwordx4 v[212:215], v253, %[rs], s47 offen sc1\n\t" \
    "s_add_u32 s47, s46, 0x8000\n\t" \
    "buffer_load_dwordx4 v[216:219], v253, %[rs], s47 offen sc1\n\t" \
    "s_add_u32 s47, s46, 0xc000\n\t" \
    "buffer_load_dwordx4 v[220:223], v253, %[rs], s47 offen sc1\n\t" \
    "s_add_u32 s47, s46, 0x10000\n\t" \
    "buffer_load_dwordx4 v[224:227], v253, %[rs], s47 offen sc1\n\t" \
    ".Lw33_%=:\n\t" \
    "s_waitcnt vmcnt(0)\n\t" \
    ".Lw34_%=:\n\t" \
    "s_add_i32 s44, s40, 0\n\t" \
    "s_max_i32 s44, s44, 0\n\t" \
    "s_lshr_b32 s45, s44, 1\n\t" \
    "s_and_b32 s44, s44, 1\n\t" \
    "s_lshl_b32 s44, s44, 12\n\t" \
    "s_mul_i32 s49, s45, 0x30000\n\t" \
    "s_add_u32 s49, s49, s44\n\t" \
    "s_lshr_b32 s45, s45, 3\n\t" \
    "s_mul_i32 s45, s45, 0x82000\n\t" \
    "s_add_u32 s49, s49, s45\n\t" \
    "s_add_u32 s49, s49, %[ng]\n\t" \
    "s_add_i32 s44, s40, 1\n\t" \
    "s_max_i32 s44, s44, 0\n\t" \
    "s_lshr_b32 s45, s44, 1\n\t" \
    "s_and_b32 s44, s44, 1\n\t" \
    "s_lshl_b32 s44, s44, 12\n\t" \
    "s_mul_i32 s43, s45, 0x14000\n\t" \
    "s_add_u32 s43, s43, s44\n\t" \
    "s_lshr_b32 s45, s45, 4\n\t" \
    "s_mul_i32 s45, s45, 0xc2000\n\t" \
    "s_add_u32 s43, s43, s45\n\t" \
    LST0 \
    LJIT \
    "s_cmp_gt_u32 %[wave], 3\n\t" \
    "s_cbranch_scc1 .Lw37_%=\n\t" \
    "s_setprio 2\n\t" \
    "v_max_u32 v248, v208, v209\n\t" \
    "v_max3_u32 v248, v210, v211, v248\n\t" \
    "v_max3_u32 v248, v212, v213, v248\n\t" \
    "v_max3_u32 v248, v214, v215, v248\n\t" \
    "v_max3_u32 v248, v216, v217, v248\n\t" \
    "v_max3_u32 v248, v218, v219, v248\n\t" \
    "v_max3_u32 v248, v220, v221, v248\n\t" \
    "v_max3_u32 v248, v222, v223, v248\n\t" \
    "v_max3_u32 v248, v224, v225, v248\n\t" \
    "v_max3_u32 v248, v226, v227, v248\n\t" \
    "v_cmp_eq_u32 vcc, -1, v248\n\t" \
    "s_cbranch_vccz .Lw36_%=\n\t" \
    ".Lw35_%=:\n\t" \
    "v_max_u32 v248, v208, v209\n\t" \
    "v_max3_u32 v248, v210, v211, v248\n\t" \
    "v_cmp_eq_u32 vcc, -1, v248\n\t" \
    "s_cbranch_vccz .Lw38_%=\n\t" \
    "s_add_u32 s47, s46, 0x0\n\t" \
    "buffer_load_dwordx4 v[208:211], v253, %[rs], s47 offen sc1\n\t" \
    ".Lw38_%=:\n\t" \
    "v_max_u32 v248, v212, v213\n\t" \
    "v_max3_u32 v248, v214, v215, v248\n\t" \
    "v_cmp_eq_u32 vcc, -1, v248\n\t" \
    "s_cbranch_vccz .Lw39_%=\n\t" \
    "s_add_u32 s47, s46, 0x4000\n\t" \
    "buffer_load_dwordx4 v[212:215], v253, %[rs], s47 offen sc1\n\t" \
    ".Lw39_%=:\n\t" \
    "v_max_u32 v248, v216, v217\n\t" \
    "v_max3_u32 v248, v218, v219, v248\n\t" \
    "v_cmp_eq_u32 vcc, -1, v248\n\t" \
    "s_cbranch_vccz .Lw310_%=\n\t" \
    "s_add_u32 s47, s46, 0x8000\n\t" \
    "buffer_load_dwordx4 v[216:219], v253, %[rs], s47 offen sc1\n\t" \
    ".Lw310_%=:\n\t" \
    "v_max_u32 v248, v220, v221\n\t" \
    "v_max3_u32 v248, v222, v223, v248\n\t" \
    "v_cmp_eq_u32 vcc, -1, v248\n\t" \
    "s_cbranch_vccz .Lw311_%=\n\t" \
    "s_add_u32 s47, s46, 0xc000\n\t" \
    "buffer_load_dwordx4 v[220:223], v253, %[rs], s47 offen sc1\n\t" \
    ".Lw311_%=:\n\t" \
    "v_max_u32 v248, v224, v225\n\t" \
    "v_max3_u32 v248, v226, v227, v248\n\t" \
    "v_cmp_eq_u32 vcc, -1, v248\n\t" \
    "s_cbranch_vccz .Lw312_%=\n\t" \
    "s_add_u32 s47, s46, 0x10000\n\t" \
    "buffer_load_dwordx4 v[224:227], v253, %[rs], s47 offen sc1\n\t" \
    ".Lw312_%=:\n\t" \
    "s_waitcnt vmcnt(0)\n\t" \
    "v_max_u32 v248, v208, v209\n\t" \
    "v_max3_u32 v248, v210, v211, v248\n\t" \
    "v_max3_u32 v248, v212, v213, v248\n\t" \
    "v_max3_u32 v248, v214, v215, v248\n\t" \
    "v_max3_u32 v248, v216, v217, v248\n\t" \
    "v_max3_u32 v248, v218, v219, v248\n\t" \
    "v_max3_u32 v248, v220, v221, v248\n\t" \
    "v_max3_u32 v248, v222, v223, v248\n\t" \
    "v_max3_u32 v248, v224, v225, v248\n\t" \
    "v_max3_u32 v248, v226, v227, v248\n\t" \
    "v_cmp_eq_u32 vcc, -1, v248\n\t" \
    "s_cbranch_vccz .Lw36_%=\n\t" \
    "s_cmp_eq_u32 s41, 0\n\t" \
    "s_cbranch_scc1 .Lw36_%=\n\t" \
    "s_sub_u32 s41, s41, 1\n\t" \
    "s_branch .Lw35_%=\n\t" \
    ".Lw36_%=:\n\t" \
    LST1 \
    LJIT \
    "ds_write_b128 v250, v[208:211] offset:0\n\t" \
    "v_mfma_f32_16x16x32_bf16 v[236:239], v[208:211], v[208:211], 0\n\t" \
    "v_mfma_f32_16x16x32_bf16 v[232:235], v[228:231], v[208:211], 0\n\t" \
    "ds_write_b128 v250, v[212:215] offset:4096\n\t" \
    "v_mfma_f32_16x16x32_bf16 v[236:239], v[212:215], v[212:215], v[236:239]\n\t" \
    "v_mfma_f32_16x16x32_bf16 v[232:235], v[228:231], v[212:215], v[232:235]\n\t" \
    "ds_write_b128 v250, v[216:219] offset:8192\n\t" \
    "v_mfma_f32_16x16x32_bf16 v[236:239], v[216:219], v[216:219], v[236:239]\n\t" \
    "v_mfma_f32_16x16x32_bf16 v[232:235], v[228:231], v[216:219], v[232:235]\n\t" \
    "ds_write_b128 v250, v[220:223] offset:12288\n\t" \
    "v_mfma_f32_16x16x32_bf16 v[236:239], v[220:223], v[220:223], v[236:239]\n\t" \
    "v_mfma_f32_16x16x32_bf16 v[232:235], v[228:231], v[220:223], v[232:235]\n\t" \
    "ds_write_b128 v250, v[224:227] offset:16384\n\t" \
    "v_mfma_f32_16x16x32_bf16 v[236:239], v[224:227], v[224:227], v[236:239]\n\t" \
    "v_mfma_f32_16x16x32_bf16 v[232:235], v[228:231], v[224:227], v[232:235]\n\t" \
    "s_add_u32 s46, s43, %[pi]\n\t" \
    "s_nop 4\n\t" \
    "v_cndmask_b32 v236, v236, v237, s[52:53]\n\t" \
    "v_cndmask_b32 v236, v236, v238, s[54:55]\n\t" \
    "v_cndmask_b32 v236, v236, v239, s[56:57]\n\t" \
    "s_mov_b32 exec_lo, 0xf0000f\n\t" \
    "s_mov_b32 exec_hi, 0xf0000f00\n\t" \
    "ds_write2_b32 v252, v232, v236 offset1:1\n\t" \
    "s_mov_b64 exec, -1\n\t" \
    "s_add_u32 s44, s40, 1\n\t" \
    "v_mov_b32 v248, s44\n\t" \
    "s_lshl_b64 exec, 1, %[wave]\n\t" \
    "ds_write_b32 v255, v248\n\t" \
    "s_mov_b64 exec, -1\n\t" \
    ".Lw37_%=:\n\t" \
    "ds_read_b32 v248, v255\n\t" \
    "s_waitcnt lgkmcnt(0)\n\t" \
    "v_cmp_ge_u32 vcc, s40, v248\n\t" \
    "s_cbranch_vccz .Lw313_%=\n\t" \
    "s_cmp_lt_u32 %[wave], 4\n\t" \
    "s_cbranch_scc1 .Lw37_%=\n\t" \
    "s_sleep 1\n\t" \
    "s_branch .Lw37_%=\n\t" \
    ".Lw313_%=:\n\t" \
    "s_setprio 0\n\t" \
    LST2 \
    LJIT \
    "s_cmp_lg_u32 %[wave], 4\n\t" \
    "s_cbranch_scc1 .Lw314_%=\n\t" \
    "s_setprio 1\n\t" \
    "ds_read_b128 v[232:235], v251 offset:0\n\t" \
    "ds_read_b128 v[236:239], v251 offset:16\n\t" \
    "s_waitcnt lgkmcnt(0)\n\t" \
    "v_add_f32 v232, v232, v236\n\t" \
    "v_add_f32 v233, v233, v237\n\t" \
    "v_add_f32 v234, v234, v238\n\t" \
    "v_add_f32 v235, v235, v239\n\t" \
    "v_add_f32 v232, v232, v234\n\t" \
    "v_add_f32 v233, v233, v235\n\t" \
    "v_mul_f32 v232, 0x3acccccd, v232\n\t" \
    "v_mul_f32 v233, 0x3acccccd, v233\n\t" \
    "v_fma_f32 v233, -v232, v232, v233\n\t" \
    "v_mov_b32 v234, v232\n\t" \
    "v_max_f32 v233, 0, v233\n\t" \
    "v_cvt_pk_bf16_f32 v234, v234, v234\n\t" \
    "v_and_b32 v234, 0xffff0000, v234\n\t" \
    "v_mov_b32 v236, v233\n\t" \
    "v_cvt_pk_bf16_f32 v236, v236, v236\n\t" \
    "v_and_b32 v236, 0xffff0000, v236\n\t" \
    "v_add_f32 v236, 0x3727c5ac, v236\n\t" \
    "v_cvt_pk_bf16_f32 v236, v236, v236\n\t" \
    "v_and_b32 v236, 0xffff0000, v236\n\t" \
    "v_sqrt_f32 v236, v236\n\t" \
    "s_nop 0\n\t" \
    "v_cvt_pk_bf16_f32 v236, v236, v236\n\t" \
    "v_and_b32 v236, 0xffff0000, v236\n\t" \
    "v_rcp_f32 v235, v236\n\t" \
    "s_nop 0\n\t" \
    "v_mov_b32 v237, v235\n\t" \
    "v_mul_f32 v238, -v234, v235\n\t" \
    "v_mov_b32 v239, s40\n\t" \
    "ds_write2_b32 v251, v237, v238 offset0:16 offset1:17\n\t" \
    "ds_write_b32 v251, v239 offset:72\n\t" \
    "s_setprio 0\n\t" \
    ".Lw314_%=:\n\t" \
    "ds_read_b128 v[176:179], v249 offset:0\n\t" \
    "ds_read_b128 v[180:183], v249 offset:1024\n\t" \
    "ds_read_b128 v[184:187], v249 offset:2048\n\t" \
    "ds_read_b128 v[188:191], v249 offset:3072\n\t" \
    "ds_read_b128 v[192:195], v249 offset:4096\n\t" \
    "ds_read_b128 v[196:199], v249 offset:5120\n\t" \
    "ds_read_b128 v[200:203], v249 offset:6144\n\t" \
    "s_waitcnt lgkmcnt(6)\n\t" \
    "v_mfma_f32_16x16x32_bf16 v[168:171], v[0:3], v[176:179], 0\n\t" \
    "ds_read_b128 v[204:207], v249 offset:7168\n\t" \
    "v_mfma_f32_16x16x32_bf16 v[172:175], v[80:83], v[176:179], 0\n\t" \
    "s_waitcnt lgkmcnt(6)\n\t" \
    "v_mfma_f32_16x16x32_bf16 v[168:171], v[4:7], v[180:183], v[168:171]\n\t" \
    "ds_read_b128 v[176:179], v249 offset:8192\n\t" \
    "v_mfma_f32_16x16x32_bf16 v[172:175], v[84:87], v[180:183], v[172:175]\n\t" \
    "s_waitcnt lgkmcnt(6)\n\t" \
    "v_mfma_f32_16x16x32_bf16 v[168:171], v[8:11], v[184:187], v[168:171]\n\t" \
    "ds_read_b128 v[180:183], v249 offset:9216\n\t" \
    "v_mfma_f32_16x16x32_bf16 v[172:175], v[88:91], v[184:187], v[172:175]\n\t" \
    "s_waitcnt lgkmcnt(6)\n\t" \
    "v_mfma_f32_16x16x32_bf16 v[168:171], v[12:15], v[188:191], v[168:171]\n\t" \
    "ds_read_b128 v[184:187], v249 offset:10240\n\t" \
    "v_mfma_f32_16x16x32_bf16 v[172:175], v[92:95], v[188:191], v[172:175]\n\t" \
    "s_waitcnt lgkmcnt(6)\n\t" \
    "v_mfma_f32_16x16x32_bf16 v[168:171], v[16:19], v[192:195], v[168:171]\n\t" \
    "ds_read_b128 v[188:191], v249 offset:11264\n\t" \
    "v_mfma_f32_16x16x32_bf16 v[172:175], v[96:99], v[192:195], v[172:175]\n\t" \
    "s_waitcnt lgkmcnt(6)\n\t" \
    "v_mfma_f32_16x16x32_bf16 v[168:171], v[20:23], v[196:199], v[168:171]\n\t" \
    "ds_read_b128 v[192:195], v249 offset:12288\n\t" \
    "v_mfma_f32_16x16x32_bf16 v[172:175], v[100:103], v[196:199], v[172:175]\n\t" \
    "s_waitcnt lgkmcnt(6)\n\t" \
    "v_mfma_f32_16x16x32_bf16 v[168:171], v[24:27], v[200:203], v[168:171]\n\t" \
    "ds_read_b128 v[196:199], v249 offset:13312\n\t" \
    "v_mfma_f32_16x16x32_bf16 v[172:175], v[104:107], v[200:203], v[172:175]\n\t" \
    "s_waitcnt lgkmcnt(6)\n\t" \
    "v_mfma_f32_16x16x32_bf16 v[168:171], v[28:31], v[204:207], v[168:171]\n\t" \
    "ds_read_b128 v[200:203], v249 offset:14336\n\t" \
    "v_mfma_f32_16x16x32_bf16 v[172:175], v[108:111], v[204:207], v[172:175]\n\t" \
    "s_waitcnt lgkmcnt(6)\n\t" \
    "v_mfma_f32_16x16x32_bf16 v[168:171], v[32:35], v[176:179], v[168:171]\n\t" \
    "ds_read_b128 v[204:207], v249 offset:15360\n\t" \
    "v_mfma_f32_16x16x32_bf16 v[172:175], v[112:115], v[176:179], v[172:175]\n\t" \
    "s_waitcnt lgkmcnt(6)\n\t" \
    "v_mfma_f32_16x16x32_bf16 v[168:171], v[36:39], v[180:183], v[168:171]\n\t" \
    "ds_read_b128 v[176:179], v249 offset:16384\n\t" \
    "v_mfma_f32_16x16x32_bf16 v[172:175], v[116:119], v[180:183], v[172:175]\n\t" \
    "s_waitcnt lgkmcnt(6)\n\t" \
    "v_mfma_f32_16x16x32_bf16 v[168:171], v[40:43], v[184:187], v[168:171]\n\t" \
    "ds_read_b128 v[180:183], v249 offset:17408\n\t" \
    "v_mfma_f32_16x16x32_bf16 v[172:175], v[120:123], v[184:187], v[172:175]\n\t" \
    "s_waitcnt lgkmcnt(6)\n\t" \
    "v_mfma_f32_16x16x32_bf16 v[168:171], v[44:47], v[188:191], v[168:171]\n\t" \
    "ds_read_b128 v[184:187], v249 offset:18432\n\t" \
    "v_mfma_f32_16x16x32_bf16 v[172:175], v[124:127], v[188:191], v[172:175]\n\t" \
    "s_waitcnt lgkmcnt(6)\n\t" \
    "v_mfma_f32_16x16x32_bf16 v[168:171], v[48:51], v[192:195], v[168:171]\n\t" \
    "ds_read_b128 v[188:191], v249 offset:19456\n\t" \
    "v_mfma_f32_16x16x32_bf16 v[172:175], v[128:131], v[192:195], v[172:175]\n\t" \
    "s_waitcnt lgkmcnt(6)\n\t" \
    "v_mfma_f32_16x16x32_bf16 v[168:171], v[52:55], v[196:199], v[168:171]\n\t" \
    "v_mfma_f32_16x16x32_bf16 v[172:175], v[132:135], v[196:199], v[172:175]\n\t" \
    "s_waitcnt lgkmcnt(5)\n\t" \
    "v_mfma_f32_16x16x32_bf16 v[168:171], v[56:59], v[200:203], v[168:171]\n\t" \
    "v_mfma_f32_16x16x32_bf16 v[172:175], v[136:139], v[200:203], v[172:175]\n\t" \
    "s_cmp_gt_u32 %[wave], 3\n\t" \
    "s_cbranch_scc1 .Lw316_%=\n\t" \
    "s_add_u32 s47, s46, 0x0\n\t" \
    "buffer_load_dwordx4 v[208:211], v253, %[rs], s47 offen sc1\n\t" \
    "s_add_u32 s47, s46, 0x4000\n\t" \
    "buffer_load_dwordx4 v[212:215], v253, %[rs], s47 offen sc1\n\t" \
    "s_add_u32 s47, s46, 0x8000\n\t" \
    "buffer_load_dwordx4 v[216:219], v253, %[rs], s47 offen sc1\n\t" \
    "s_add_u32 s47, s46, 0xc000\n\t" \
    "buffer_load_dwordx4 v[220:223], v253, %[rs], s47 offen sc1\n\t" \
    "s_add_u32 s47, s46, 0x10000\n\t" \
    "buffer_load_dwordx4 v[224:227], v253, %[rs], s47 offen sc1\n\t" \
    ".Lw316_%=:\n\t" \
    "s_waitcnt lgkmcnt(4)\n\t" \
    "v_mfma_f32_16x16x32_bf16 v[168:171], v[60:63], v[204:207], v[168:171]\n\t" \
    "v_mfma_f32_16x16x32_bf16 v[172:175], v[140:143], v[204:207], v[172:175]\n\t" \
    "ds_read_b32 v236, v251 offset:72\n\t" \
    "ds_read_b64 v[238:239], v251 offset:64\n\t" \
    "s_waitcnt lgkmcnt(5)\n\t" \
    "v_mfma_f32_16x16x32_bf16 v[168:171], v[64:67], v[176:179], v[168:171]\n\t" \
    "v_mfma_f32_16x16x32_bf16 v[172:175], v[144:147], v[176:179], v[172:175]\n\t" \
    "s_waitcnt lgkmcnt(4)\n\t" \
    "v_mfma_f32_16x16x32_bf16 v[168:171], v[68:71], v[180:183], v[168:171]\n\t" \
    "v_mfma_f32_16x16x32_bf16 v[172:175], v[148:151], v[180:183], v[172:175]\n\t" \
    ".Lw317_%=:\n\t" \
    "s_waitcnt lgkmcnt(0)\n\t" \
    "v_cmp_ne_u32 vcc, s40, v236\n\t" \
    "s_cbranch_vccz .Lw318_%=\n\t" \
    "ds_read_b32 v236, v251 offset:72\n\t" \
    "ds_read_b64 v[238:239], v251 offset:64\n\t" \
    "s_branch .Lw317_%=\n\t" \
    ".Lw318_%=:\n\t" \
    "s_waitcnt lgkmcnt(0)\n\t" \
    "v_mul_f32 v240, v160, v239\n\t" \
    "v_mul_f32 v241, v161, v239\n\t" \
    "v_mfma_f32_16x16x32_bf16 v[168:171], v[72:75], v[184:187], v[168:171]\n\t" \
    "v_mul_f32 v242, v162, v239\n\t" \
    "v_mul_f32 v243, v163, v239\n\t" \
    "v_mfma_f32_16x16x32_bf16 v[172:175], v[152:155], v[184:187], v[172:175]\n\t" \
    "v_mul_f32 v244, v164, v239\n\t" \
    "v_mul_f32 v245, v165, v239\n\t" \
    "v_mfma_f32_16x16x32_bf16 v[168:171], v[76:79], v[188:191], v[168:171]\n\t" \
    "v_mul_f32 v246, v166, v239\n\t" \
    "v_mul_f32 v247, v167, v239\n\t" \
    "v_mfma_f32_16x16x32_bf16 v[172:175], v[156:159], v[188:191], v[172:175]\n\t" \
    "v_mov_b32 v237, 0xc0135761\n\t" \
    "s_cmp_lt_u32 %[wave], 4\n\t" \
    "s_cbranch_scc1 .Lw319_%=\n\t" \
    "s_add_u32 s44, s40, 2\n\t" \
    "v_mov_b32 v248, s44\n\t" \
    "s_lshl_b64 exec, 1, %[wave]\n\t" \
    "ds_write_b32 v255, v248\n\t" \
    "s_mov_b64 exec, -1\n\t" \
    ".Lw319_%=:\n\t" \
    "s_nop 1\n\t" \
    LST3 \
    LJIT \
    "s_nop 2\n\t" \
    "v_fma_f32 v168, v168, v238, v240\n\t" \
    "v_fma_f32 v169, v169, v238, v241\n\t" \
    "v_fma_f32 v170, v170, v238, v242\n\t" \
    "v_fma_f32 v171, v171, v238, v243\n\t" \
    "v_fma_f32 v172, v172, v238, v244\n\t" \
    "v_fma_f32 v173, v173, v238, v245\n\t" \
    "v_fma_f32 v174, v174, v238, v246\n\t" \
    "v_fma_f32 v175, v175, v238, v247\n\t" \
    "v_mul_f32 v176, v168, v168\n\t" \
    "v_mul_f32 v177, v169, v169\n\t" \
    "v_mul_f32 v178, v170, v170\n\t" \
    "v_mul_f32 v179, v171, v171\n\t" \
    "v_mul_f32 v180, v172, v172\n\t" \
    "v_mul_f32 v181, v173, v173\n\t" \
    "v_mul_f32 v182, v174, v174\n\t" \
    "v_mul_f32 v183, v175, v175\n\t" \
    "v_fmamk_f32 v176, v176, 0xbdd2d3e7, v237\n\t" \
    "v_fmamk_f32 v177, v177, 0xbdd2d3e7, v237\n\t" \
    "v_fmamk_f32 v178, v178, 0xbdd2d3e7, v237\n\t" \
    "v_fmamk_f32 v179, v179, 0xbdd2d3e7, v237\n\t" \
    "v_fmamk_f32 v180, v180, 0xbdd2d3e7, v237\n\t" \
    "v_fmamk_f32 v181, v181, 0xbdd2d3e7, v237\n\t" \
    "v_fmamk_f32 v182, v182, 0xbdd2d3e7, v237\n\t" \
    "v_fmamk_f32 v183, v183, 0xbdd2d3e7, v237\n\t" \
    "v_mul_f32 v176, v168, v176\n\t" \
    "v_mul_f32 v177, v169, v177\n\t" \
    "v_mul_f32 v178, v170, v178\n\t" \
    "v_mul_f32 v179, v171, v179\n\t" \
    "v_mul_f32 v180, v172, v180\n\t" \
    "v_mul_f32 v181, v173, v181\n\t" \
    "v_mul_f32 v182, v174, v182\n\t" \
    "v_mul_f32 v183, v175, v183\n\t" \
    "v_exp_f32 v176, v176\n\t" \
    "v_exp_f32 v177, v177\n\t" \
    "v_exp_f32 v178, v178\n\t" \
    "v_exp_f32 v179, v179\n\t" \
    "v_exp_f32 v180, v180\n\t" \
    "v_exp_f32 v181, v181\n\t" \
    "v_exp_f32 v182, v182\n\t" \
    "v_exp_f32 v183, v183\n\t" \
    "v_add_f32 v176, 1.0, v176\n\t" \
    "v_add_f32 v177, 1.0, v177\n\t" \
    "v_add_f32 v178, 1.0, v178\n\t" \
    "v_add_f32 v179, 1.0, v179\n\t" \
    "v_add_f32 v180, 1.0, v180\n\t" \
    "v_add_f32 v181, 1.0, v181\n\t" \
    "v_add_f32 v182, 1.0, v182\n\t" \
    "v_add_f32 v183, 1.0, v183\n\t" \
    "v_rcp_f32 v176, v176\n\t" \
    "v_rcp_f32 v177, v177\n\t" \
    "v_rcp_f32 v178, v178\n\t" \
    "v_rcp_f32 v179, v179\n\t" \
    "v_rcp_f32 v180, v180\n\t" \
    "v_rcp_f32 v181, v181\n\t" \
    "v_rcp_f32 v182, v182\n\t" \
    "v_rcp_f32 v183, v183\n\t" \
    "v_mul_f32 v168, v168, v176\n\t" \
    "v_mul_f32 v169, v169, v177\n\t" \
    "v_mul_f32 v170, v170, v178\n\t" \
    "v_mul_f32 v171, v171, v179\n\t" \
    "v_mul_f32 v172, v172, v180\n\t" \
    "v_mul_f32 v173, v173, v181\n\t" \
    "v_mul_f32 v174, v174, v182\n\t" \
    "v_mul_f32 v175, v175, v183\n\t" \
    "s_waitcnt vmcnt(0)\n\t" \
    "v_cvt_pk_bf16_f32 v184, v168, v169\n\t" \
    "v_cvt_pk_bf16_f32 v185, v170, v171\n\t" \
    "v_cvt_pk_bf16_f32 v186, v172, v173\n\t" \
    "v_cvt_pk_bf16_f32 v187, v174, v175\n\t" \
    "v_min_u32 v184, -2, v184\n\t" \
    "v_min_u32 v185, -2, v185\n\t" \
    "v_min_u32 v186, -2, v186\n\t" \
    "v_min_u32 v187, -2, v187\n\t" \
    "s_nop 0\n\t" \
    "v_permlane16_swap_b32 v184, v186\n\t" \
    "v_permlane16_swap_b32 v185, v187\n\t" \
    "buffer_store_dwordx4 v[184:187], v254, %[rs], s49 offen sc1\n\t" \
    LST4 \
    LJIT \
    "s_cmp_ge_u32 s40, 122\n\t" \
    "s_cbranch_scc1 .Lw320_%=\n\t" \
    "s_add_i32 s44, s40, 6\n\t" \
    "s_max_i32 s44, s44, 0\n\t" \
    "s_lshr_b32 s45, s44, 1\n\t" \
    "s_and_b32 s44, s44, 1\n\t" \
    "s_lshl_b32 s44, s44, 12\n\t" \
    "s_mul_i32 s50, s45, 0x30000\n\t" \
    "s_add_u32 s50, s50, s44\n\t" \
    "s_lshr_b32 s45, s45, 3\n\t" \
    "s_mul_i32 s45, s45, 0x82000\n\t" \
    "s_add_u32 s50, s50, s45\n\t" \
    "s_add_u32 s50, s50, %[ng]\n\t" \
    "v_mov_b32 v188, -1\n\t" \
    "v_mov_b32 v189, -1\n\t" \
    "v_mov_b32 v190, -1\n\t" \
    "v_mov_b32 v191, -1\n\t" \
    "buffer_store_dwordx4 v[188:191], v253, %[rs], s50 offen sc1\n\t" \
    ".Lw320_%=:\n\t" \
    "v_xor_b32 v249, 0x8000, v249\n\t" \
    "v_xor_b32 v250, 0x8000, v250\n\t" \
    "v_xor_b32 v251, 0x800, v251\n\t" \
    "v_xor_b32 v252, 0x800, v252\n\t" \
    LFLUSHW \
    "s_add_u32 s40, s40, 1\n\t" \
    "s_cmp_lg_u32 s40, 128\n\t" \
    "s_cbranch_scc1 .Lw34_%=\n\t" \
    "s_branch .Lend%=\n\t" \
    ".Lend%=:\n\t" \
    "s_waitcnt vmcnt(0) lgkmcnt(0)\n\t" \
    "s_mov_b32 %[bud], s41\n\t"
using u32x8 = __attribute__((ext_vector_type(8))) unsigned;
using u32x16 = __attribute__((ext_vector_type(16))) unsigned;
using u32x32 = __attribute__((ext_vector_type(32))) unsigned;
__device__ __forceinline__ u32x32 cat32(u32x8 a, u32x8 b, u32x8 c, u32x8 d) {
    const u32x16 lo = __builtin_shufflevector(a, b, 0, 1, 2, 3, 4, 5, 6, 7, 8, 9, 10, 11, 12, 13, 14, 15);
    const u32x16 hi = __builtin_shufflevector(c, d, 0, 1, 2, 3, 4, 5, 6, 7, 8, 9, 10, 11, 12, 13, 14, 15);
    return __builtin_shufflevector(lo, hi, 0, 1, 2, 3, 4, 5, 6, 7, 8, 9, 10, 11, 12, 13, 14, 15, 16, 17, 18, 19, 20, 21, 22, 23, 24, 25, 26, 27, 28, 29, 30, 31);
}
__device__ __forceinline__ unsigned c1_dst(int wave, int kind) {
    const int ln = lane_id(), c = ln & 15, g = ln >> 4, hf = (wave >> 1) & 1;
    if (kind == 2) {
        const unsigned b = L_KM + (c >> 2) * 256 + (wave & 1) * 128 + g * 32;
        return (b + hf * 8 + (c & 3) * 2) | (b + 1024 + hf * 1024 + (c & 3) * 4) << 16;
    }
    const unsigned b = L_Q + (c >> 3) * 256 + g * 64 + ((c >> 2) & 1) * 8;
    return (b + (wave & 1) * (kind ? 512 : 1536) + ((c >> 1) & 1) * 32 + hf * 4 + (c & 1) * 2) | (b + 1024 + (wave & 3) * 512 + (c & 1) * 32 + ((c >> 1) & 1) * 4) << 16;
}
__device__ __forceinline__ void dma1(rsrc_t rs, unsigned vo, unsigned so, unsigned dst) {
    asm volatile("s_mov_b32 m0, %3\n\ts_nop 0\n\tbuffer_load_dwordx4 %0, %1, %2 offen nt lds" :: "v"(vo), "s"(rs), "s"(so), "s"(dst) : "memory");
}
using s16x4 = __attribute__((ext_vector_type(4))) short;
__device__ __forceinline__ u32x4 tr_frag(unsigned a, unsigned step) {
    const u32x2 x = __builtin_bit_cast(u32x2, __builtin_amdgcn_ds_read_tr16_b64_v4i16((LAS s16x4*)(size_t)a)), y = __builtin_bit_cast(u32x2, __builtin_amdgcn_ds_read_tr16_b64_v4i16((LAS s16x4*)(size_t)(a + step)));
    return (u32x4){x[0], x[1], y[0], y[1]};
}
#define LOOP_CLOB "memory", "vcc", "scc", "v0", "v1", "v2", "v3", "v4", "v5", "v6", "v7", "v8", "v9", "v10", "v11", "v12", "v13", "v14", "v15", "v16", "v17", "v18", "v19", "v20", "v21", "v22", "v23", "v24", "v25", "v26", "v27", "v28", "v29", "v30", "v31", "v32", "v33", "v34", "v35", "v36", "v37", "v38", "v39", "v40", "v41", "v42", "v43", "v44", "v45", "v46", "v47", "v48", "v49", "v50", "v51", "v52", "v53", "v54", "v55", "v56", "v57", "v58", "v59", "v60", "v61", "v62", "v63", "v64", "v65", "v66", "v67", "v68", "v69", "v70", "v71", "v72", "v73", "v74", "v75", "v76", "v77", "v78", "v79", "v80", "v81", "v82", "v83", "v84", "v85", "v86", "v87", "v88", "v89", "v90", "v91", "v92", "v93", "v94", "v95", "v96", "v97", "v98", "v99", "v100", "v101", "v102", "v103", "v104", "v105", "v106", "v107", "v108", "v109", "v110", "v111", "v112", "v113", "v114", "v115", "v116", "v117", "v118", "v119", "v120", "v121", "v122", "v123", "v124", "v125", "v126", "v127", "v128", "v129", "v130", "v131", "v132", "v133", "v134", "v135", "v136", "v137", "v138", "v139", "v140", "v141", "v142", "v143", "v144", "v145", "v146", "v147", "v148", "v149", "v150", "v151", "v152", "v153", "v154", "v155", "v156", "v157", "v158", "v159", "v160", "v161", "v162", "v163", "v164", "v165", "v166", "v167", "v168", "v169", "v170", "v171", "v172", "v173", "v174", "v175", "v176", "v177", "v178", "v179", "v180", "v181", "v182", "v183", "v184", "v185", "v186", "v187", "v188", "v189", "v190", "v191", "v192", "v193", "v194", "v195", "v196", "v197", "v198", "v199", "v200", "v201", "v202", "v203", "v204", "v205", "v206", "v207", "v208", "v209", "v210", "v211", "v212", "v213", "v214", "v215", "v216", "v217", "v218", "v219", "v220", "v221", "v222", "v223", "v224", "v225", "v226", "v227", "v228", "v229", "v230", "v231", "v232", "v233", "v234", "v235", "v236", "v237", "v238", "v239", "v246", "v247", "v248", "v249", "v250", "v251", "v252", "s40", "s41", "s42", "s43", "s44", "s45", "s46", "s47", "s48", "s49", "s50", "s51", "s52", "s53", "s54", "s55", "s56", "s57", "s58", "s59", "s60", "s61", "s62", "s63", "s64", "s65", "s66", "s67", "s68", "s69", "s70", "s71", "s72", "s73", "s74", "s75", "s76", "s77", "s78", "s79"
__device__ __forceinline__ unsigned UNI(unsigned x) {
    unsigned r;
    asm volatile("s_nop 0\n\tv_readfirstlane_b32 %0, %1\n\ts_nop 4" : "=s"(r) : "v"(x));
    return r;
}
__device__ __forceinline__ void note_timeout(const Params& P, unsigned bud) {
    if (bud == 0) __builtin_amdgcn_raw_buffer_store_b32(1u, mkrsrc(P.ws + FL0, 512), 0x100, 0, 16);
}

#define LOOP_IO [bud] "=s"(bud), "+{v253}"(l16), "+{v240}"(W.a[0]), "+{v241}"(W.a[1]), "+{v242}"(W.a[2]), "+{v243}"(W.a[3]), "+{v244}"(W.a[4]), "+{v245}"(W.a[5])
#define LOOP_IN "{v254}"(b2), "{v255}"(dsto), [wave] "s"(wave), [ws] "s"(P.ws), [mo] "s"(FL_MB + blockIdx.x * 128 + wave * 4), [sz] "s"(RG_L), [lx] "s"(L << 13), [wr] "s"(wdesc(P, wave, isv, h)), [wm] "s"(UNI(wstage(wave, isv))), [fl] "s"(FL_XN + cpy * 2048), \
                [cj] "s"(REGS - 75 * WIN), [cs] "s"(5 * WIN), [mb] "s"(RG_XM + cpy * XM_COPY)
struct WLoad { unsigned a[6]; };
__device__ __forceinline__ rsrc_t wdesc(const Params& P, int wave, bool isv, int h) {
    const unsigned wo = (isv && wave >= 4) ? h * DH * DM + (wave & 3) * 160 : h * DH;
    typedef const __attribute__((address_space(4))) unsigned long long* kptr;
    static_assert(__builtin_offsetof(Params, wq) == 24 && __builtin_offsetof(Params, wo) == 48, "argument order");
    const unsigned long long w = ((kptr)__builtin_amdgcn_kernarg_segment_ptr())[3 + (isv ? 2 : 0) + (wave >> 2)];
    return mkrsrc((const bf16_t*)w + wo, (DM * HD - wo) * 2);
}
__device__ __forceinline__ unsigned wstage(int wave, bool isv) { return (isv && wave >= 4) ? L_W + (wave & 3) * 20480 : wave * 3072; }
template <bool START>
__device__ __forceinline__ void wload_start(WLoad& W, const Params& P, int wave, bool isv, int h) {
    const int ln = lane_id(), w4 = wave & 3;
    const rsrc_t wr = wdesc(P, wave, isv, h);
    const unsigned wm = UNI(wstage(wave, isv));
    if (isv && wave >= 4) {
#pragma unroll
        for (int k = 0; k < 5; ++k) { const unsigned u = k * 64 + ln; W.a[k] = (u / 20) * (DM * 2) + (u % 20) * 16 - (k & 3) * 1024; }
#pragma unroll 1
        for (int k = 0; k < (START ? 4 : 0); ++k)
            asm volatile("s_mov_b32 m0, %7\n\ts_nop 0\n\tbuffer_load_dwordx4 %0, %5, %6 offen nt lds\n\tbuffer_load_dwordx4 %1, %5, %6 offen offset:1024 nt lds\n\tbuffer_load_dwordx4 %2, %5, %6 offen offset:2048 nt lds\n\t"
                         "buffer_load_dwordx4 %3, %5, %6 offen offset:3072 nt lds\n\ts_mov_b32 m0, %8\n\ts_nop 0\n\tbuffer_load_dwordx4 %4, %5, %6 offen nt lds"
                         :: "v"(W.a[0]), "v"(W.a[1]), "v"(W.a[2]), "v"(W.a[3]), "v"(W.a[4]), "s"(wr), "s"(k * 20480), "s"(wm + k * 5120), "s"(wm + k * 5120 + 4096) : "memory");
        W.a[5] = L_W + w4 * 20480 + (ln >> 2) * 320 + (ln & 3) * 8;
    } else {
        const unsigned sl = isv ? 12288 : 24576;
#pragma unroll
        for (int k = 0; k < 3; ++k) { const unsigned u = (w4 * 3 + k) * 64 + ln; W.a[k] = (u / 24) * (HD * 2) + (u % 24) * 16 - k * 1024; }
#pragma unroll 1
        for (int k = 0; k < (START ? 5 : 0); ++k)
            asm volatile("s_mov_b32 m0, %5\n\ts_nop 0\n\tbuffer_load_dwordx4 %0, %3, %4 offen nt lds\n\tbuffer_load_dwordx4 %1, %3, %4 offen offset:1024 nt lds\n\tbuffer_load_dwordx4 %2, %3, %4 offen offset:2048 nt lds"
                         :: "v"(W.a[0]), "v"(W.a[1]), "v"(W.a[2]), "s"(wr), "s"(k * 32 * HD * 2), "s"(wm + (k < 2 ? k + 4 : k - 2) * sl) : "memory");
        W.a[3] = (wave >> 2) * 12288 + ((ln >> 4) * 8 + ((ln >> 2) & 3)) * 384 + (ln & 3) * 8 + w4 * 96;
        W.a[4] = 0;
        W.a[5] = 0;
    }
}
__device__ __forceinline__ void s_role(const Params& P, int wave, int L, int h, int cpy) {
    const int ln = lane_id();
    unsigned l16 = ln * 16;
    constexpr bool isv = false;
    WLoad W;
    wload_start<false>(W, P, wave, isv, h);
    const int mt = (wave >> 1) & 1, hf = wave & 1;
    const unsigned xs = D_XN + mt * 4096 + hf * WIN, x1 = (2 - hf) * WIN + hf * 2048, m0 = L_X + mt * 20480 + hf * 4096, m1 = L_X + mt * 20480 + 8192 + hf * 2048;
    const float SCL = 0.07216878364870322f * 1.4426950408889634f;
    const unsigned px = RG_PX + (h >> 2) * WIN;
    const unsigned pb = wave == 0 ? RG_XP + (h / 12) * WIN + (h % 12) * 640 - L_Q : px + (wave == 2 ? (h & 3) * 1024 : 5120 + ((h >> 1) & 1) * 1024 + (h & 1) * 256);
    unsigned b2 = l16 + (wave < 3 ? L_Q : wave == 3 ? L_QX : L_W + wave * 10240), dsto = c1_dst(wave, 1 + (wave >> 2)), bud;
    asm volatile(ASM_S_LOOP : LOOP_IO
                 : LOOP_IN, [xs] "s"(xs), [x1] "s"(x1), [m0] "s"(m0), [m1] "s"(m1), [qs] "s"(UNI(L_KX + hf * 1024 + mt * 8)), [q2] "s"(UNI(L_KX + 2048 + (wave & 3) * 1024)), [pb] "s"(UNI(pb)),
                   [pm] "s"(UNI(px + (wave == 2 ? 4096 : 7168) + (h & 3) * 256)), [scl] "s"(__float_as_uint(SCL)) LST_IN
                 : LOOP_CLOB);
    note_timeout(P, bud);
}

__device__ __forceinline__ void v_role(const Params& P, int wave, int L, int h, int cpy) {
    const int ln = lane_id(), w4 = wave & 3;
    unsigned l16 = ln * 16;
    constexpr bool isv = true;
    WLoad W;
    wload_start<false>(W, P, wave, isv, h);
    unsigned b2 = l16 + (wave < 3 ? L_Q : wave == 3 ? L_QX : L_W + wave * 10240), dsto = wave < 4 ? c1_dst(wave, 0) : (l16 >> 8) * WIN + (l16 & 255), bud;
    if (wave >= 4) l16 += L_Q;
    asm volatile(ASM_V_LOOP : LOOP_IO
                 : LOOP_IN, [xs] "s"(D_XN + w4 * WIN), [vm] "s"(VXD - 1), [vx] "s"(vxb(w4, 0) + (h >> 3) * WIN + (h & 7) * 1024 - L_Q),
                   [fo] "s"(FL_VO + (w4 >> 1) * WIN + ((w4 & 1) * 32 + h) * 128 - L_Q), [vo] "s"(RG_VM + w4 * 10 * WIN + (h >> 2) * 1024 + (h & 3) * 256) LST_IN
                 : LOOP_CLOB);
    note_timeout(P, bud);
}

#define D_W0 4
#define D_A1 8
#define D_R1 9
#define D_W1 12
__device__ __forceinline__ void delay(int n) { for (int i = 0; i < n; ++i) __builtin_amdgcn_s_sleep(64); }
__device__ __forceinline__ unsigned handshake(rsrc_t rw, unsigned mo) {
    wait_vm<0>();
    unsigned v = SENT;
    for (int spins = 0; spins < SPIN_CAP && v == SENT; ++spins) {
        asm volatile("s_sleep 4" ::: "memory");
        v = __builtin_amdgcn_readfirstlane(__builtin_amdgcn_raw_buffer_load_b32(rw, 0, mo, 16));
    }
    __builtin_amdgcn_raw_buffer_store_b32(1u, rw, lane_id() ? OOB : 32768u, mo, 16);
    return (v >> 13) & 1;
}
template <class F>
__device__ __forceinline__ void x_tile(const Params& P, int tile, F target) {
    const int ln = lane_id(), l15 = ln & 15, l4 = ln >> 4;
    u32x4 v[20];
    float s = 0.f, q = 0.f;
#pragma unroll
    for (int c = 0; c < 20; ++c) v[c] = *(const u32x4*)(P.x + (size_t)(tile * 16 + l15) * DM + c * 32 + l4 * 8);
#pragma unroll
    for (int c = 0; c < 20; ++c)
#pragma unroll
        for (int e = 0; e < 4; ++e) s += lo2f(v[c][e]) + hi2f(v[c][e]);
    sum2_rows(s, q);
#pragma unroll
    for (int c = 0; c < 20; ++c) asm volatile("" : "+v"(v[c]));
    const float mean = s * (1.0f / DM);
#pragma unroll
    for (int c = 0; c < 20; ++c)
#pragma unroll
        for (int e = 0; e < 4; ++e) { const float a = lo2f(v[c][e]) - mean, b = hi2f(v[c][e]) - mean; q += a * a + b * b; }
    sum2_rows(s, q);
#pragma unroll
    for (int c = 0; c < 20; ++c) asm volatile("" : "+v"(v[c]));
    const float m1 = rbf(mean), r = 1.0f / ln_den(q * (1.0f / DM));
    const unsigned sb = segb(tile >> 1) + (tile & 1) * 4096;
    u32x4 n[20];
#pragma unroll
    for (int c = 0; c < 20; ++c) {
#pragma unroll
        for (int e = 0; e < 4; ++e) { const unsigned d = pack2(lo2f(v[c][e]) - m1, hi2f(v[c][e]) - m1); n[c][e] = pack2(lo2f(d) * r, hi2f(d) * r); }
        asm volatile("" : "+v"(n[c]));
    }
    const rsrc_t rr = target();
#pragma unroll
    for (int c = 0; c < 20; ++c) {
        st128(n[c], rr, ln * 16 + (c & 3) * 1024, D_XN + sb + (c >> 2) * WIN);
        st128(v[c], rr, ln * 16 + (c & 3) * 1024, D_XR + sb + (c >> 2) * WIN);
    }
    wait_vm<0>();
}
__device__ __forceinline__ void wait_go(const Params& P, rsrc_t rd, int k) {
    for (int spins = 0;; ++spins) {
        if (__builtin_amdgcn_readfirstlane(__builtin_amdgcn_raw_buffer_load_b32(rd, 0, FL_VO + k * 128, 16))) break;
        asm volatile("s_sleep 16" ::: "memory");
        if (spin_fail(spins * 4 + 3, P)) break;
    }
    asm volatile("" ::: "memory");
}

constexpr unsigned R_RED = 0;
constexpr unsigned R_PART = 8192;
__device__ __forceinline__ void r_role(const Params& P, int wave, int L, int j, int cpy) {
    const int lane = lane_id(), l15 = lane & 15, l4 = lane >> 4;
    const unsigned l16 = lane * 16;
    const rsrc_t rr = mkrsrc(lbase(P, L), RG_L);
    const int sidx = (lane & 2) ? 0xEEEE : 0x4444;
    unsigned offp[3];
    auto hq = [&](int q) { return RG_XP + cpy * XP_COPY + (q / 3) * WIN + (q % 3) * 2560; };
#define ROT 1
    const int hg = (wave + ROT * j) & 7;
    offp[0] = hq(hg) + (lane >> 5) * 640 + 128 + (lane & 31) * 16;
    offp[1] = offp[0] + 1280;
    offp[2] = (lane & 2) ? OOB : hq(hg) + l4 * 640 + ((l15 >> 2) * 2 + (l15 & 1)) * 16;
    const int jq = j / 10, j10 = j % 10;
    const unsigned ae = wave < 3 ? ((j10 < 8 && wave * 1024 + l16 < 2560) ? hq(j10) + (jq - cpy) * XP_COPY + wave * 1024 + l16 : OOB)
                      : wave == 3 ? RG_XM + jq * XM_COPY + (j10 >> 3) * WIN + (j10 & 7) * 1024 + l16
                      : wave < 6 ? (j < 32 ? RG_PX + (j >> 2) * WIN + (j & 3) * 2048 + (wave & 1) * 1024 + l16 : OOB)
                      : wave == 7 ? D_XA + (j >> 3) * WIN + (j & 7) * 512 + (lane >> 5) * 4096 + (lane & 31) * 16 : OOB;
    const unsigned as = wave < 3 ? XP_SLOT : PX_SLOT;
    static_assert(PX_SLOT == XM_SLOT && AH == 3 && VXD == 4, "generated loop");
    const unsigned offv = RG_VM + j * WIN + hg * 1024 + l16;
    const unsigned offo = (wave < (int)NCP) ? RG_XM + wave * XM_COPY + (j >> 5) * WIN + (j & 31) * 256 + lane * 4 : OOB;
    {
        const u32x4 s4 = sent4();
#pragma unroll
        for (int k = 0; k < (int)NSLOT; ++k) {
            st128(s4, rr, offv, k * VM_SLOT);
            if (wave < 6 && (k || wave != 3)) st128(s4, rr, ae, k * as);
        }
        for (int u = j; u < 126; u += 40) st128(s4, rr, wave * 1024 + l16, u < 108 ? D_XA + (u < 60 ? u / 20 : 3) * 4 * REG + (u < 60 ? u % 20 : u - 60) * WIN : u < 124 ? RG_ST + (u - 108) * WIN : D_SS + (u - 124) * WIN);
        wait_vm<0>();
        bar();
        const int row = l4 * 2 + ((lane >> 2) & 1), col = 16 * j + 4 * (lane & 3) + 2 * ((lane >> 3) & 1);
        __builtin_amdgcn_raw_buffer_store_b32(nosent(*(const unsigned*)(P.mem0 + row * DM + col)), rr, offo, 0, 16);
    }
    auto red_addr = [](int fl) { return R_RED + fl * 80 + (fl >> 4) * 16; };
    const unsigned rw0 = red_addr(l4 * 16 + ((l15 >> 1) & 1) * 8 + (l15 >> 2)) + (l15 & 1) * 4 + (hg ^ (l15 >> 3)) * 8;
    const unsigned rrd = red_addr(lane);
    unsigned bud;
    asm volatile(ASM_R_LOOP : [bud] "=s"(bud)
                 : [wave] "s"(wave), [rs] "s"(rr), [l16] "v"(l16), [ov] "v"(offv), [o0] "v"(offp[0]), [o1] "v"(offp[1]), [o2] "v"(offp[2]), [oo] "v"(offo), [sidx] "v"(sidx), [rw] "v"(rw0), [rd] "v"(rrd),
                   [xo] "v"((l4 >> 1) * 256 + l15 * 16 + (l4 & 1) * 8), [pw] "v"(R_PART + hg * 1024 + l16), [pr] "v"(R_PART + (wave & 1) * 8192 + l16), [fo] "v"((lane & 3) * 128), [ae] "v"(ae),
                   [fs] "s"(UNI(FL_VO + (jq >> 1) * WIN + ((jq & 1) * 32 + hg * 4) * 128)), [vx] "s"(UNI(vxb(jq, 0) + (j10 * 4 + (hg >> 1)) * WIN + (hg & 1) * 4096)), [px] "s"(UNI(RG_PX + hg * WIN)),
                   [xr] "s"(UNI(D_XR + (j >> 3) * WIN + (wave & 1) * 4096 + (j & 7) * 512)), [ad] "s"(UNI(wave < 3 ? -1 : wave == 3 ? 0 : -2)), [as] "s"(UNI(as)) LST_IN
                 : "memory", "vcc", "scc", "v128", "v129", "v130", "v131", "v132", "v133", "v134", "v135", "v136", "v137", "v138", "v139", "v140", "v141", "v142", "v143", "v144", "v145", "v146", "v147", "v148", "v149", "v150", "v151", "v152", "v153", "v154", "v155", "v156", "v157", "v158", "v159", "v160", "v161", "v162", "v163", "v164", "v165", "v166", "v167", "v168", "v169", "v170", "v171", "v172", "v173", "v174", "v175", "v176", "v177", "v178", "v179", "v180", "v181", "v182", "v183", "v184", "v185", "v186", "v187", "v188", "v189", "v190", "v191", "v192", "v193", "v194", "v195", "v196", "v197", "v198", "v199", "v200", "v201", "v202", "v203", "v204", "v205", "v206", "v207", "v208", "v209", "v210", "v211", "v212", "v213", "v214", "v215", "v216", "v217", "v218", "v219", "v220", "v221", "v222", "v223", "v224", "v225", "v226", "v227", "v228", "v229", "v230", "v231", "v232", "v233", "v234", "v235", "v236", "v237", "v238", "v239", "v240", "v241", "v242", "v243", "v244", "v245", "v246", "v247", "v248", "v249", "v250", "v251", "v252", "v253", "v254", "v255", "s40", "s41", "s42", "s43", "s44", "s45", "s46", "s47", "s48", "s49", "s50", "s51", "s52", "s53", "s54", "s55", "s64", "s65", "s66", "s67", "s68", "s69", "s70", "s71", "s72", "s73", "s74", "s75", "s76", "s77", "s78", "s79");
    note_timeout(P, bud);
}

template <int WHICH>
__device__ __forceinline__ void w_role(const Params& P, int wave, int L, int cu) {
    constexpr int NT = (WHICH == 3) ? 2 : 1, N = (WHICH == 3) ? FF : DM, RB = NT * 256, RPI = 1024 / RB;
    const int ln = lane_id(), l15 = ln & 15, l4 = ln >> 4;
    const rsrc_t rd = mkrsrc(lbase(P, L), RG_L);
    const int nt0 = (cu * 8 + wave) * NT;
    const rsrc_t wr = mkrsrc((WHICH == 1 ? P.w1 : WHICH == 2 ? P.w2 : P.w3) + cu * (RB / 2), DM * N * 2 - cu * RB);
    auto swz = [](int row) { return ((row & 3) | ((row >> 1) & 4)) * 32; };
    const int dr = ln * 16 / RB, tr = l4 * 8 + ((ln >> 2) & 3);
    unsigned vo = dr * (N * 2) + ((ln * 16 % RB) ^ swz(wave * RPI + dr)), ta = tr * RB + ((wave * NT * 32 + (ln & 3) * 8) ^ swz(tr));
    const int grp = nt0 / 10, neg = grp & 1, sh = (WHICH == 1) ? (neg ? -1 : 1) * ((grp >> 1) + 1) : 0;
    unsigned ra = ln * 16, wa = ln * 16 + wave * 1024, sr = WHICH == 2 ? l15 * 8 : 24576 + l15 * 80, sw = sr + wave * 8, bud;
    const unsigned oo = WHICH == 3 ? (l4 & 1) * 512 + (l4 >> 1) * 256 + l15 * 16 : (nt0 >> 3) * WIN + (nt0 & 7) * 512 + (l4 >> 1) * 256 + ((l15 + sh) & 15) * 16 + (l4 & 1) * 8;
    unsigned up = ((unsigned)(l15 + sh) > 15u) ^ neg;
#define W_ARGS : [bud] "=s"(bud), "+{v176}"(vo), "+{v177}"(ta), "+{v249}"(ra), "+{v250}"(wa), "+{v251}"(sr), "+{v252}"(sw) \
               , "+{v255}"(up) : "{v253}"(ln * 16), "{v254}"(oo), [wave] "s"(wave), [rs] "s"(rd), [wr] "s"(wr), [wm] "s"(UNI(wave * 1024)), [wo] "s"(UNI(wave * RPI * N * 2)), [go] "s"(FL_VO + (WHICH * 8 + cu) * 128), \
                 [pi] "s"(UNI((WHICH == 1 ? D_XA : WHICH == 2 ? D_HS : D_X1) + wave * 1024)), \
                 [ng] "s"(UNI(WHICH == 3 ? D_H2 + (nt0 >> 3) * WIN + ((nt0 >> 1) & 3) * 1024 : neg)), [ss] "s"(UNI(cu == 0)) LST_IN LST_INW \
               : "memory", "vcc", "scc", "v0", "v1", "v2", "v3", "v4", "v5", "v6", "v7", "v8", "v9", "v10", "v11", "v12", "v13", "v14", "v15", "v16", "v17", "v18", "v19", "v20", "v21", "v22", "v23", "v24", "v25", "v26", "v27", "v28", "v29", "v30", "v31", "v32", "v33", "v34", "v35", "v36", "v37", "v38", "v39", "v40", "v41", "v42", "v43", "v44", "v45", "v46", "v47", "v48", "v49", "v50", "v51", "v52", "v53", "v54", "v55", "v56", "v57", "v58", "v59", "v60", "v61", "v62", "v63", "v64", "v65", "v66", "v67", "v68", "v69", "v70", "v71", "v72", "v73", "v74", "v75", "v76", "v77", "v78", "v79", "v80", "v81", "v82", "v83", "v84", "v85", "v86", "v87", "v88", "v89", "v90", "v91", "v92", "v93", "v94", "v95", "v96", "v97", "v98", "v99", "v100", "v101", "v102", "v103", "v104", "v105", "v106", "v107", "v108", "v109", "v110", "v111", "v112", "v113", "v114", "v115", "v116", "v117", "v118", "v119", "v120", "v121", "v122", "v123", "v124", "v125", "v126", "v127", "v128", "v129", "v130", "v131", "v132", "v133", "v134", "v135", "v136", "v137", "v138", "v139", "v140", "v141", "v142", "v143", "v144", "v145", "v146", "v147", "v148", "v149", "v150", "v151", "v152", "v153", "v154", "v155", "v156", "v157", "v158", "v159", "v160", "v161", "v162", "v163", "v164", "v165", "v166", "v167", "v168", "v169", "v170", "v171", "v172", "v173", "v174", "v175", "v178", "v179", "v180", "v181", "v182", "v183", "v184", "v185", "v186", "v187", "v188", "v189", "v190", "v191", "v192", "v193", "v194", "v195", "v196", "v197", "v198", "v199", "v200", "v201", "v202", "v203", "v204", "v205", "v206", "v207", "v208", "v209", "v210", "v211", "v212", "v213", "v214", "v215", "v216", "v217", "v218", "v219", "v220", "v221", "v222", "v223", "v224", "v225", "v226", "v227", "v228", "v229", "v230", "v231", "v232", "v233", "v234", "v235", "v236", "v237", "v238", "v239", "v240", "v241", "v242", "v243", "v244", "v245", "v246", "v247", "v248", "s40", "s41", "s42", "s43", "s44", "s45", "s46", "s47", "s48", "s49", "s50", "s51", "s52", "s53", "s54", "s55", "s56", "s57", "s58", "s59", "s60", "s61", "s62", "s63", "s64", "s65", "s66", "s67", "s68", "s69", "s70", "s71", "s72", "s73", "s74", "s75", "s76", "s77", "s78", "s79"
    if (WHICH == 1) asm volatile(ASM_W1_LOOP W_ARGS);
    if (WHICH == 2) asm volatile(ASM_W2_LOOP W_ARGS);
    if (WHICH == 3) asm volatile(ASM_W3_LOOP W_ARGS);
    note_timeout(P, bud);
}

constexpr unsigned W4_FL = 122880;
__device__ __forceinline__ unsigned lds_vld(unsigned a) { asm volatile("" ::: "memory"); const unsigned v = *(volatile LAS unsigned*)(size_t)a; asm volatile("" ::: "memory"); return v; }
__device__ __forceinline__ void lds_vst(unsigned a, unsigned v) { asm volatile("" ::: "memory"); *(volatile LAS unsigned*)(size_t)a = v; asm volatile("" ::: "memory"); }
__device__ __forceinline__ void w4_role(const Params& P, int wave, int L, int cu) {
    const int ln = lane_id(), l15 = ln & 15, l4 = ln >> 4;
    const unsigned l16 = ln * 16;
    const rsrc_t rd = mkrsrc(lbase(P, L), RG_L);
    unsigned bud;
    if (wave >= 6) wait_go(P, rd, 32 + cu);
    if (wave < 6) {
        u32x32 w[5];
        {
            const rsrc_t wr = mkrsrc(P.w4 + wave * 256 * DM + cu * 80, (FF * DM - wave * 256 * DM - cu * 80) * 2);
            const unsigned ta = wave * 20480 + (l4 * 8 + ((ln >> 2) & 3)) * 160 + (ln & 3) * 8;
            u32x4 f[5][8];
#pragma unroll
            for (int r = 0; r < 2; ++r) {
#pragma unroll
                for (int i = 0; i < 5; ++i) {
                    const unsigned u = i * 64 + ln, vo = (u / 10) * (DM * 2) + (u % 10) * 16;
#pragma unroll
                    for (int k = 0; k < 4; ++k) dma1(wr, vo, (r * 128 + k * 32) * (DM * 2), wave * 20480 + k * 5120 + i * 1024);
                }
                wait_vm<0>();
#pragma unroll
                for (int k = 0; k < 4; ++k)
#pragma unroll
                    for (int n = 0; n < 5; ++n) f[n][r * 4 + k] = tr_frag(ta + k * 5120 + n * 32, 640);
                asm volatile("s_waitcnt lgkmcnt(0)" ::: "memory");
                SB();
            }
#pragma unroll
            for (int n = 0; n < 5; ++n) {
                u32x8 g[4];
#pragma unroll
                for (int i = 0; i < 4; ++i) g[i] = __builtin_shufflevector(f[n][2 * i], f[n][2 * i + 1], 0, 1, 2, 3, 4, 5, 6, 7);
                w[n] = cat32(g[0], g[1], g[2], g[3]);
            }
            asm volatile("" : "+{v[0:31]}"(w[0]), "+{v[32:63]}"(w[1]), "+{v[64:95]}"(w[2]), "+{v[96:127]}"(w[3]), "+{v[128:159]}"(w[4]));
        }
        wait_go(P, rd, 32 + cu);
        bar();
        unsigned ld = l16 + wave * 1024;
        asm volatile(ASM_W4_MUL : [bud] "=s"(bud), [ld] "+v"(ld)
                     : "{v[0:31]}"(w[0]), "{v[32:63]}"(w[1]), "{v[64:95]}"(w[2]), "{v[96:127]}"(w[3]), "{v[128:159]}"(w[4]), [l2] "v"(2 * ld + 61440), [l16] "v"(l16), [fa] "v"(W4_FL + wave * 4), [rs] "s"(rd),
                       [hb] "s"(UNI(D_H2 + wave * 2 * WIN)), [wave] "s"(wave), [ro] "s"(UNI(wave * 5120)), [fp] "v"(W4_FL + (ln < 6 ? ln : 0) * 4) LST_IN
                     : "memory", "vcc", "scc", "v160", "v161", "v162", "v163", "v164", "v165", "v166", "v167", "v168", "v169", "v170", "v171", "v172", "v173", "v174", "v175", "v176", "v177", "v178", "v179", "v180", "v181", "v182", "v183", "v184", "v185", "v186", "v187", "v188", "v189", "v190", "v191", "v192", "v193", "v194", "v195", "v196", "v197", "v198", "v199", "v200", "v201", "v202", "v203", "v204", "v205", "v206", "v207", "v208", "v209", "v210", "v211", "v212", "v213", "v214", "v215", "v216", "v217", "v218", "v219", "v220", "v221", "v222", "v223", "v224", "v225", "v226", "v227", "v228", "v229", "v230", "v231", "v232", "v233", "v234", "v235", "v236", "v237", "v238", "v239", "v240", "v241", "v242", "v243", "v244", "v245", "v246", "v247", "s40", "s41", "s42", "s43", "s44", "s45", "s46", "s47", "s48", "s49", "s50", "s51", "s52", "s53", "s54", "s55", "s64", "s65", "s66", "s67", "s68", "s69", "s70", "s71", "s72", "s73", "s74", "s75", "s76", "s77", "s78", "s79");
    } else {
        const int mt = wave - 6;
        if (ln < 16) lds_vst(W4_FL + ln * 4, 0u);
        bar();
        auto tl = [&](int n) { return (unsigned)((cu * 5 + n) >> 3) * WIN + ((cu * 5 + n) & 7) * 512 + mt * 4096; };
        const unsigned o8 = (l4 >> 1) * 256 + l15 * 16 + (l4 & 1) * 8;
        unsigned la = mt * 30720 + l16;
        static_assert(RG_ST == 112 * WIN && D_XN == 10 * REG && D_XR == 6 * REG, "generated loop");
#define F_IN [l2] "v"(2 * la + 61440), [fa] "v"(W4_FL + (ln < 6 ? ln : 0) * 4), [fb] "v"(W4_FL + 32 + (ln < 5 ? ln : 0) * 4), [fc] "v"(W4_FL + 24 + mt * 4), [rs] "s"(rd), [mt] "s"(mt), \
             [t0] "v"(o8 + tl(0)), [t1] "v"(o8 + tl(1)), [t2] "v"(o8 + tl(2)), [t3] "v"(o8 + tl(3)), [t4] "v"(o8 + tl(4))
#define F_CLOB "memory", "vcc", "scc", "v64", "v65", "v66", "v67", "v68", "v69", "v70", "v71", "v72", "v73", "v74", "v75", "v76", "v77", "v78", "v79", "v80", "v81", "v82", "v83", "v84", "v85", "v86", "v87", "v88", "v89", "v90", "v91", "v92", "v93", "v94", "v95", "v96", "v97", "v98", "v99", "v100", "v101", "v102", "v103", "v104", "v105", "v106", "v107", "v108", "v109", "v110", "v111", "v112", "v113", "v114", "v115", "v116", "v117", "v118", "v119", "v120", "v121", "v122", "v123", "v124", "v125", "v126", "v127", "v128", "v129", "v130", "v131", "v132", "v133", "v134", "v135", "v136", "v137", "v138", "v139", "v140", "v141", "v142", "v143", "v144", "v145", "v146", "v147", "v148", "v149", "v150", "v151", "v152", "v153", "v154", "v155", "v156", "v157", "v158", "v159", "v160", "v161", "v162", "v163", "v164", "v165", "v166", "v167", "v168", "v169", "v170", "v171", "v172", "v173", "v174", "v175", "v176", "v177", "v178", "v179", "v180", "v181", "v182", "v183", "v184", "v185", "v186", "v187", "v188", "v189", "v190", "v191", "v192", "v193", "v194", "v195", "v196", "v197", "v198", "v199", "v200", "v201", "v202", "v203", "v204", "v205", "v206", "v207", "v208", "v209", "v210", "v211", "v212", "v213", "v214", "v215", "v216", "v217", "v218", "v219", "v220", "v221", "v222", "v223", "v224", "v225", "v226", "v227", "v228", "v229", "v230", "v231", "v232", "v233", "v234", "v235", "v236", "v237", "v238", "v239", "s40", "s41", "s42", "s43", "s44", "s45", "s46", "s47", "s48", "s49", "s50", "s51", "s52", "s53", "s54", "s55", "s64", "s65", "s66", "s67", "s68", "s69", "s70", "s71", "s72", "s73", "s74", "s75", "s76", "s77", "s78", "s79"
        if (L) asm volatile(ASM_W4_FIN1 : [bud] "=s"(bud), [la] "+v"(la)
                            : F_IN, [ro] "s"(mkrsrc(P.out, ACT)), [co] "s"(cu * 160), [q8] "v"(l15 * (DM * 2) + l4 * 8), [q16] "v"(l15 * (DM * 2) + (l4 & 1) * 32 + (l4 >> 1) * 16) LST_IN : F_CLOB);
        else asm volatile(ASM_W4_FIN0 : [bud] "=s"(bud), [la] "+v"(la)
                          : F_IN, [rn] "s"(mkrsrc(lbase(P, 1), RG_L)), [co] "s"(cu * 128), [l8] "v"(ln * 8), [o16] "v"(tl(l4 & 1) + (l4 >> 1) * 256 + l15 * 16), [o17] "v"(tl(2 + (l4 & 1)) + (l4 >> 1) * 256 + l15 * 16),
                            [fo] "v"(ln < 32 ? (ln >> 3) * 2048 + (ln & 7) * 16 : OOB), [fs] "s"(FL_XN + (cu * 2 + mt) * 128) LST_IN : F_CLOB);
    }
    note_timeout(P, bud);
}

constexpr int LEADER = 252;
__device__ __forceinline__ unsigned leader(const Params& P, rsrc_t rw, int wave) {
    const int ln = lane_id();
    if (wave == 0) {
        const unsigned o = ln ? OOB : 0u;
        __builtin_amdgcn_raw_buffer_store_b32(0u, rw, o, CAL0, 16);
        __builtin_amdgcn_raw_buffer_store_b32(0u, rw, o, CAL0 + 8192, 16);
        unsigned m[2] = {~0u, ~0u};
#pragma unroll
        for (int r = 0; r < CALN; ++r) {
            unsigned long long a, c;
            asm volatile("s_waitcnt vmcnt(0) lgkmcnt(0)\n\ts_memtime %0\n\ts_waitcnt lgkmcnt(0)\n\tbuffer_store_dword %2, %2, %3, %4 offen sc1\n\ts_waitcnt vmcnt(0)\n\ts_memtime %1\n\ts_waitcnt lgkmcnt(0)"
                         : "=&s"(a), "=&s"(c) : "v"(o), "s"(rw), "s"(CAL0 + (r & 1) * 8192) : "memory");
            const unsigned d = (unsigned)(c - a);
            m[r & 1] = d < m[r & 1] ? d : m[r & 1];
        }
        lds_vst(0, m[0] < m[1] ? 1u : 0u);
    }
    bar();
    const unsigned par = __builtin_amdgcn_readfirstlane(lds_vld(0));
    const u32x4 s4 = sent4(), z4 = s4 ^ s4;
    for (int k = 0; k < 6; ++k) st128(z4, rw, ln * 16, FL_VO + (k * 8 + wave) * 1024);
    for (int k = 0; k < 10; ++k) st128(s4, rw, ln * 16, RG_XM + (wave >> 1) * XM_COPY + (wave & 1) * 8192 + (k >> 3) * WIN + (k & 7) * 1024);
    const unsigned mo = (wave * 32 + (ln >> 1)) * 128 + (ln & 1) * 16;
    st128(z4, rw, mo, FL_MB + 32768);
    if (wave == 0) st128(z4, rw, ln ? OOB : 256u, FL0);
    wait_vm<0>();
    bar();
    bool open = wave * 32 + (ln >> 1) != LEADER;
    for (int spins = 0; spins < SPIN_CAP; ++spins) {
        st128(s4 & (par << 13), rw, open ? mo : OOB, FL_MB);
        __builtin_amdgcn_s_sleep(20);
        const u32x4 d = LD128(rw, mo, FL_MB + 32768);
        open = open && !(d[0] && d[1] && d[2] && d[3]);
        if (!__any(open)) break;
    }
    bar();
    return par;
}
__global__ __launch_bounds__(512, 2) void mega_kernel(Params P) {
    const int wg = blockIdx.x;
    const int wave = __builtin_amdgcn_readfirstlane(threadIdx.x >> 6);
    const int L = (wg >> 2) & 1, x = wg & 3, r = wg >> 3, cpy = x;
    if (__builtin_expect(r < 16, 1)) {
        WLoad W;
        if (L) delay(D_A1);
        wload_start<true>(W, P, wave, r >= 8, x * 8 + (r & 7));
    }
    const int nr = (x & 1) ? 10 + RK : 10 - RK, j = (x >> 1) * 20 + (x & 1) * (10 - RK) + r - 16, i0 = (x >> 1) * 12 + (x & 1) * (6 + RK) + r - 16 - nr;
    const int i = (i0 * 5) % 24;
    const rsrc_t rw = mkrsrc(P.ws, RG_L);
    const bool lead = wg == LEADER;
    CJIT(30);
    if (!lead) __builtin_amdgcn_raw_buffer_store_b32(SENT, rw, lane_id() ? OOB : 0u, FL_MB + wg * 128 + wave * 4, 16);
#define FIN() __builtin_amdgcn_endpgm()
    if (r < 8) { s_role(P, wave, L, x * 8 + r, cpy); FIN(); }
    if (r < 16) { v_role(P, wave, L, x * 8 + r - 8, cpy); FIN(); }
    if (wave == 6 && !lead) {
        const unsigned o = lane_id() * (REG / PRET);
        unsigned a = 0;
        for (int k = 0; k < PRET; ++k) a |= __builtin_amdgcn_raw_buffer_load_b32(rw, o, k * 64 * (REG / PRET), 16);
        unsigned b = __builtin_amdgcn_raw_buffer_load_b32(mkrsrc(P.out, ACT), lane_id() * 65536, 0, 16);
        asm volatile("" :: "v"(a), "v"(b));
    }
    if (lead) P.par = leader(P, rw, wave);
    else {
        const unsigned mo = FL_MB + wg * 128 + wave * 4;
        const bool ln = r - 16 < nr && j < 16, first = ln && !L && wave == 7;
        if (!first) { P.par = handshake(rw, mo); if (L || r - 16 >= nr) delay(!L ? D_W0 : r - 16 < nr ? D_R1 : D_W1); }
        if (first || (ln && L && wave)) x_tile(P, (L ? wave * 16 : 0) + j, [&]() { if (first) P.par = handshake(rw, mo); return mkrsrc(lbase(P, 0), RG_L); });
        if (ln && L) bar();
        if (first || (ln && L && !wave)) {
            const rsrc_t r0 = mkrsrc(lbase(P, 0), RG_L);
            const int lane = lane_id();
            const unsigned fo = lane < 32 ? (lane >> 3) * 2048 + (lane & 7) * 16 : OOB;
            if (L) for (int spins = 0; spins < SPIN_CAP && __any(lane < 32 && LD128(r0, fo, FL_XN + j * 128)[0] == 0u); ++spins) asm volatile("s_sleep 8" ::: "memory");
            st128(sent4() & (L ? 64u : 8u), r0, fo, FL_XN + j * 128);
        }
    }
    CJIT(30);
    if (r - 16 < nr) { r_role(P, wave, L, j, cpy); FIN(); }
    if (i < 5) { w_role<1>(P, wave, L, i); FIN(); }
    if (i < 10) { w_role<2>(P, wave, L, i - 5); FIN(); }
    if (i < 16) { w_role<3>(P, wave, L, i - 10); FIN(); }
    w4_role(P, wave, L, i - 16);
    FIN();
}

extern "C" void kernel_launch(void* const* d_in, const int* in_sizes, int n_in, void* d_out, int out_size, void* d_ws,
                              size_t ws_size, hipStream_t stream) {
    char* ws = (char*)d_ws + (-(size_t)d_ws & (REG - 1));
    Params P;
    P.par = 0; P.pad = 0; P.ws = ws; P.out = (bf16_t*)d_out;
    P.x = (const bf16_t*)d_in[0]; P.mem0 = (const bf16_t*)d_in[1]; P.wq = (const bf16_t*)d_in[2]; P.wk = (const bf16_t*)d_in[3]; P.wv = (const bf16_t*)d_in[4]; P.wo = (const bf16_t*)d_in[5];
    P.w1 = (const bf16_t*)d_in[8]; P.w2 = (const bf16_t*)d_in[9]; P.w3 = (const bf16_t*)d_in[10]; P.w4 = (const bf16_t*)d_in[11];
    (void)hipFuncSetAttribute((const void*)mega_kernel, hipFuncAttributeMaxDynamicSharedMemorySize, MEGA_LDS);

    mega_kernel<<<256, 512, MEGA_LDS, stream>>>(P);
}
```

```cpp
#include <hip/hip_runtime.h>
#include <stdint.h>

typedef unsigned short bf16_t;
using bf16x2 = __attribute__((ext_vector_type(2))) __bf16;
using f32x2 = __attribute__((ext_vector_type(2))) float;
using u32x2 = __attribute__((ext_vector_type(2))) unsigned;
using u32x4 = __attribute__((ext_vector_type(4))) unsigned;
typedef __amdgpu_buffer_rsrc_t rsrc_t;

__device__ __forceinline__ unsigned pack2(float a, float b) {
    bf16x2 r = __builtin_convertvector((f32x2){a, b}, bf16x2);
    return __builtin_bit_cast(unsigned, r);
}
__device__ __forceinline__ float rbf(float f) { return __uint_as_float(pack2(f, 0.f) << 16); }
__device__ __forceinline__ float lo2f(unsigned u) { return __uint_as_float(u << 16); }
__device__ __forceinline__ float hi2f(unsigned u) { return __uint_as_float(u & 0xFFFF0000u); }
__device__ __forceinline__ rsrc_t mkrsrc(const void* p, unsigned bytes) {
    return __builtin_amdgcn_make_buffer_rsrc((void*)p, 0, bytes, 0x00020000);
}

constexpr int S_ = 2048, DM = 640, NH = 32, DH = 192, HD = NH * DH  , SEG = 32, NSEG = 64;
constexpr int FF = 1536;

constexpr unsigned NSLOT = 4, NCP = 4, VXD = 4;
constexpr unsigned WIN = 16384, REG = 2u << 20, REGS = REG + 8192;
constexpr unsigned GRP = 32768, CH0 = 48 * REG, CH_SLOT = 2 * REG;
__host__ __device__ constexpr unsigned blk(unsigned grp, unsigned x) { return CH0 + grp * GRP + (((x ^ (grp >> 4)) & 1) << 14) + ((((x >> 1) ^ (grp >> 5)) & 1) << 12); }
constexpr unsigned PX_SLOT = 8 * WIN;
constexpr unsigned RG_PX = 80 * WIN, RG_ST = RG_PX + NSLOT * PX_SLOT;
static_assert(RG_ST + 16 * WIN <= REG, "ring region");
constexpr unsigned FL0 = REG, FL_PAR = REG + 4096, FL_MB = REG + 65536;
constexpr unsigned FL_VO = blk(14, 0), FL_XN = blk(15, 0), D_SS = 2 * REG + 52 * WIN, CAL0 = blk(13, 0);
constexpr unsigned ACT = S_ * DM * 2;
constexpr unsigned D_XR = 6 * REG, D_XN = 10 * REG, D_XA = 14 * REG, D_HS = 18 * REG, D_X1 = 22 * REG, D_H2 = 26 * REG, D_VX = 34 * REG;
__host__ __device__ constexpr unsigned segb(unsigned seg) { return seg * (5 * WIN) + (seg >> 4) * (REGS - 80 * WIN); }
__host__ __device__ constexpr unsigned segh(unsigned seg) { return seg * (12 * WIN) + (seg >> 3) * (REGS - 96 * WIN); }
__host__ __device__ constexpr unsigned vxb(unsigned q, unsigned t) { return D_VX + q * (VXD / 2 * REG) + (t & (VXD - 1)) * (REG / 2) + (t & 2) * 4096; }
constexpr int AH = 3;
constexpr unsigned RG_L = CH0 + NSLOT * CH_SLOT;
static_assert(D_VX + 2 * VXD * REG <= CH0, "layout");

#define SB() __builtin_amdgcn_sched_barrier(0)
#define LAS __attribute__((address_space(3)))
constexpr unsigned OOB = 0x7FFFFF00u;
template <int N> __device__ __forceinline__ void wait_vm() { asm volatile("s_waitcnt vmcnt(%0)" :: "i"(N) : "memory"); }
__device__ __forceinline__ void bar() {
    asm volatile("s_waitcnt lgkmcnt(0)" ::: "memory");
    __builtin_amdgcn_s_barrier();
    asm volatile("" ::: "memory");
}
__device__ __forceinline__ int lane_id() {
    int ln;
    asm volatile("v_mbcnt_lo_u32_b32 %0, -1, 0\n\tv_mbcnt_hi_u32_b32 %0, -1, %0" : "=v"(ln));
    return ln;
}
__device__ __forceinline__ void sum2_rows(float& s, float& q) {
    unsigned a = __float_as_uint(s), b = a, c = __float_as_uint(q), d = c;
    asm volatile("s_nop 2\n\tv_permlane16_swap_b32 %0, %1\n\tv_permlane16_swap_b32 %2, %3\n\tv_add_f32 %0, %0, %1\n\tv_add_f32 %2, %2, %3\n\tv_mov_b32 %1, %0\n\tv_mov_b32 %3, %2\n\t"
                 "s_nop 1\n\tv_permlane32_swap_b32 %0, %1\n\tv_permlane32_swap_b32 %2, %3\n\tv_add_f32 %0, %0, %1\n\tv_add_f32 %2, %2, %3" : "+v"(a), "+v"(b), "+v"(c), "+v"(d));
    s = __uint_as_float(a); q = __uint_as_float(c);
}
template <int AUXV>
__device__ __forceinline__ void st128_(u32x4 v, rsrc_t r, unsigned voff, unsigned soff) {
    __builtin_amdgcn_raw_buffer_store_b128(v, r, voff, soff, AUXV);
    asm volatile("s_nop 1" :: "v"(v) : "memory");
}
#define st128(V, R, VO_, SO_) st128_<16>(V, R, VO_, SO_)
#define LD128(R, VO_, SO_) __builtin_amdgcn_raw_buffer_load_b128(R, VO_, SO_, 16)
__device__ __forceinline__ float ln_den(float var) { return rbf(__builtin_amdgcn_sqrtf(rbf(rbf(var) + 1e-5f))); }

struct Params {
    char* ws;
    const bf16_t *x, *mem0, *wq, *wk, *wv, *wo, *w1, *w2, *w3, *w4;
    bf16_t* out;
    unsigned par, pad;
};

#define SENT 0xFFFFFFFFu
constexpr int SPIN_CAP = 400000;
__device__ __forceinline__ bool spin_fail(int spins, const Params& P) {
    if ((spins & 1023) == 1023) {
        const rsrc_t rt = mkrsrc(P.ws + FL0, 512);
        unsigned o;
        asm volatile("v_mov_b32 %0, 0x100" : "=v"(o));
        if (__builtin_amdgcn_readfirstlane(__builtin_amdgcn_raw_buffer_load_b32(rt, o, 0, 16)) != 0u) return true;
        if (spins > SPIN_CAP) { __builtin_amdgcn_raw_buffer_store_b32(o, rt, o, 0, 16); return true; }
    }
    return false;
}
__device__ __forceinline__ unsigned nosent(unsigned d) { return d < 0xFFFFFFFEu ? d : 0xFFFFFFFEu; }
__device__ __forceinline__ u32x4 sent4() { unsigned sv; asm volatile("v_mov_b32 %0, -1" : "=v"(sv)); return (u32x4){sv, sv, sv, sv}; }
__device__ __forceinline__ char* lbase(const Params& P, int L) { return P.ws + ((P.par ^ (L << 13)) & 0x2000); }

constexpr unsigned L_X = 0;
constexpr unsigned L_MEM = 40960;
constexpr unsigned L_Q = 51200;
constexpr unsigned L_KM = 54272;
constexpr unsigned L_QX = 57344;
constexpr unsigned L_KX = 69632;
constexpr unsigned L_W = 81920;
constexpr int MEGA_LDS = 163840;

#define ASM_S_LOOP \
    "s_mov_b64 s[60:61], %[ws]\n\t" \
    "s_mov_b32 s62, %[sz]\n\t" \
    "s_mov_b32 s63, 0x20000\n\t" \
    "s_mov_b32 s40, 0\n\t" \
    "s_mov_b32 s41, 0x20000\n\t" \
    "s_cmp_eq_u32 %[wave], 0\n\t" \
    "s_cbranch_scc1 .Ls0_%=\n\t" \
    "s_cmp_eq_u32 %[wave], 1\n\t" \
    "s_cbranch_scc1 .Ls1_%=\n\t" \
    "s_cmp_eq_u32 %[wave], 2\n\t" \
    "s_cbranch_scc1 .Ls2_%=\n\t" \
    "s_cmp_eq_u32 %[wave], 3\n\t" \
    "s_cbranch_scc1 .Ls3_%=\n\t" \
    ".Ls4_%=:\n\t" \
    "s_mov_b32 s42, %[xs]\n\t" \
    "v_add_u32 v244, 0x18000, v243\n\t" \
    "s_waitcnt vmcnt(12) lgkmcnt(0)\n\t" \
    "s_barrier \n\t" \
    "s_add_u32 m0, %[wm], 0x12000\n\t" \
    "s_mov_b32 s44, 0x1e0000\n\t" \
    "buffer_load_dwordx4 v240, %[wr], s44 offen offset:0 nt lds\n\t" \
    "buffer_load_dwordx4 v241, %[wr], s44 offen offset:1024 nt lds\n\t" \
    "buffer_load_dwordx4 v242, %[wr], s44 offen offset:2048 nt lds\n\t" \
    "ds_read_b64_tr_b16 v[200:201], v244 offset:0\n\t" \
    "ds_read_b64_tr_b16 v[202:203], v244 offset:1536\n\t" \
    "ds_read_b64_tr_b16 v[0:1], v244 offset:32\n\t" \
    "ds_read_b64_tr_b16 v[2:3], v244 offset:1568\n\t" \
    "ds_read_b64_tr_b16 v[8:9], v244 offset:64\n\t" \
    "ds_read_b64_tr_b16 v[10:11], v244 offset:1600\n\t" \
    "v_add_u32 v244, 0x1e000, v243\n\t" \
    "s_waitcnt vmcnt(12) lgkmcnt(0)\n\t" \
    "s_barrier \n\t" \
    "s_add_u32 m0, %[wm], 0x18000\n\t" \
    "s_mov_b32 s44, 0x240000\n\t" \
    "buffer_load_dwordx4 v240, %[wr], s44 offen offset:0 nt lds\n\t" \
    "buffer_load_dwordx4 v241, %[wr], s44 offen offset:1024 nt lds\n\t" \
    "buffer_load_dwordx4 v242, %[wr], s44 offen offset:2048 nt lds\n\t" \
    "ds_read_b64_tr_b16 v[204:205], v244 offset:0\n\t" \
    "ds_read_b64_tr_b16 v[206:207], v244 offset:1536\n\t" \
    "ds_read_b64_tr_b16 v[4:5], v244 offset:32\n\t" \
    "ds_read_b64_tr_b16 v[6:7], v244 offset:1568\n\t" \
    "ds_read_b64_tr_b16 v[12:13], v244 offset:64\n\t" \
    "ds_read_b64_tr_b16 v[14:15], v244 offset:1600\n\t" \
    "v_add_u32 v244, 0x0, v243\n\t" \
    "s_waitcnt vmcnt(12) lgkmcnt(0)\n\t" \
    "s_barrier \n\t" \
    "s_add_u32 m0, %[wm], 0x1e000\n\t" \
    "s_mov_b32 s44, 0x2a0000\n\t" \
    "buffer_load_dwordx4 v240, %[wr], s44 offen offset:0 nt lds\n\t" \
    "buffer_load_dwordx4 v241, %[wr], s44 offen offset:1024 nt lds\n\t" \
    "buffer_load_dwordx4 v242, %[wr], s44 offen offset:2048 nt lds\n\t" \
    "ds_read_b64_tr_b16 v[16:17], v244 offset:0\n\t" \
    "ds_read_b64_tr_b16 v[18:19], v244 offset:1536\n\t" \
    "ds_read_b64_tr_b16 v[24:25], v244 offset:32\n\t" \
    "ds_read_b64_tr_b16 v[26:27], v244 offset:1568\n\t" \
    "ds_read_b64_tr_b16 v[32:33], v244 offset:64\n\t" \
    "ds_read_b64_tr_b16 v[34:35], v244 offset:1600\n\t" \
    "v_add_u32 v244, 0x6000, v243\n\t" \
    "s_waitcnt vmcnt(12) lgkmcnt(0)\n\t" \
    "s_barrier \n\t" \
    "s_add_u32 m0, %[wm], 0x0\n\t" \
    "s_mov_b32 s44, 0x300000\n\t" \
    "buffer_load_dwordx4 v240, %[wr], s44 offen offset:0 nt lds\n\t" \
    "buffer_load_dwordx4 v241, %[wr], s44 offen offset:1024 nt lds\n\t" \
    "buffer_load_dwordx4 v242, %[wr], s44 offen offset:2048 nt lds\n\t" \
    "ds_read_b64_tr_b16 v[20:21], v244 offset:0\n\t" \
    "ds_read_b64_tr_b16 v[22:23], v244 offset:1536\n\t" \
    "ds_read_b64_tr_b16 v[28:29], v244 offset:32\n\t" \
    "ds_read_b64_tr_b16 v[30:31], v244 offset:1568\n\t" \
    "ds_read_b64_tr_b16 v[36:37], v244 offset:64\n\t" \
    "ds_read_b64_tr_b16 v[38:39], v244 offset:1600\n\t" \
    "v_add_u32 v244, 0xc000, v243\n\t" \
    "s_waitcnt vmcnt(12) lgkmcnt(0)\n\t" \
    "s_barrier \n\t" \
    "s_add_u32 m0, %[wm], 0x6000\n\t" \
    "s_mov_b32 s44, 0x360000\n\t" \
    "buffer_load_dwordx4 v240, %[wr], s44 offen offset:0 nt lds\n\t" \
    "buffer_load_dwordx4 v241, %[wr], s44 offen offset:1024 nt lds\n\t" \
    "buffer_load_dwordx4 v242, %[wr], s44 offen offset:2048 nt lds\n\t" \
    "ds_read_b64_tr_b16 v[208:209], v244 offset:0\n\t" \
    "ds_read_b64_tr_b16 v[210:211], v244 offset:1536\n\t" \
    "ds_read_b64_tr_b16 v[40:41], v244 offset:32\n\t" \
    "ds_read_b64_tr_b16 v[42:43], v244 offset:1568\n\t" \
    "ds_read_b64_tr_b16 v[48:49], v244 offset:64\n\t" \
    "ds_read_b64_tr_b16 v[50:51], v244 offset:1600\n\t" \
    "v_add_u32 v244, 0x12000, v243\n\t" \
    "s_waitcnt vmcnt(12) lgkmcnt(0)\n\t" \
    "s_barrier \n\t" \
    "s_add_u32 m0, %[wm], 0xc000\n\t" \
    "s_mov_b32 s44, 0x3c0000\n\t" \
    "buffer_load_dwordx4 v240, %[wr], s44 offen offset:0 nt lds\n\t" \
    "buffer_load_dwordx4 v241, %[wr], s44 offen offset:1024 nt lds\n\t" \
    "buffer_load_dwordx4 v242, %[wr], s44 offen offset:2048 nt lds\n\t" \
    "ds_read_b64_tr_b16 v[212:213], v244 offset:0\n\t" \
    "ds_read_b64_tr_b16 v[214:215], v244 offset:1536\n\t" \
    "ds_read_b64_tr_b16 v[44:45], v244 offset:32\n\t" \
    "ds_read_b64_tr_b16 v[46:47], v244 offset:1568\n\t" \
    "ds_read_b64_tr_b16 v[52:53], v244 offset:64\n\t" \
    "ds_read_b64_tr_b16 v[54:55], v244 offset:1600\n\t" \
    "v_add_u32 v244, 0x18000, v243\n\t" \
    "s_waitcnt vmcnt(12) lgkmcnt(0)\n\t" \
    "s_barrier \n\t" \
    "s_add_u32 m0, %[wm], 0x12000\n\t" \
    "s_mov_b32 s44, 0x420000\n\t" \
    "buffer_load_dwordx4 v240, %[wr], s44 offen offset:0 nt lds\n\t" \
    "buffer_load_dwordx4 v241, %[wr], s44 offen offset:1024 nt lds\n\t" \
    "buffer_load_dwordx4 v242, %[wr], s44 offen offset:2048 nt lds\n\t" \
    "ds_read_b64_tr_b16 v[56:57], v244 offset:0\n\t" \
    "ds_read_b64_tr_b16 v[58:59], v244 offset:1536\n\t" \
    "ds_read_b64_tr_b16 v[64:65], v244 offset:32\n\t" \
    "ds_read_b64_tr_b16 v[66:67], v244 offset:1568\n\t" \
    "ds_read_b64_tr_b16 v[72:73], v244 offset:64\n\t" \
    "ds_read_b64_tr_b16 v[74:75], v244 offset:1600\n\t" \
    "v_add_u32 v244, 0x1e000, v243\n\t" \
    "s_waitcnt vmcnt(12) lgkmcnt(0)\n\t" \
    "s_barrier \n\t" \
    "s_add_u32 m0, %[wm], 0x18000\n\t" \
    "s_mov_b32 s44, 0x480000\n\t" \
    "buffer_load_dwordx4 v240, %[wr], s44 offen offset:0 nt lds\n\t" \
    "buffer_load_dwordx4 v241, %[wr], s44 offen offset:1024 nt lds\n\t" \
    "buffer_load_dwordx4 v242, %[wr], s44 offen offset:2048 nt lds\n\t" \
    "ds_read_b64_tr_b16 v[60:61], v244 offset:0\n\t" \
    "ds_read_b64_tr_b16 v[62:63], v244 offset:1536\n\t" \
    "ds_read_b64_tr_b16 v[68:69], v244 offset:32\n\t" \
    "ds_read_b64_tr_b16 v[70:71], v244 offset:1568\n\t" \
    "ds_read_b64_tr_b16 v[76:77], v244 offset:64\n\t" \
    "ds_read_b64_tr_b16 v[78:79], v244 offset:1600\n\t" \
    "v_add_u32 v244, 0x0, v243\n\t" \
    "s_waitcnt vmcnt(12) lgkmcnt(0)\n\t" \
    "s_barrier \n\t" \
    "v_mov_b32 v246, 0\n\t" \
    "buffer_load_dword v246, v246, s[60:63], %[mo] offen sc1\n\t" \
    "s_add_u32 m0, %[wm], 0x1e000\n\t" \
    "s_mov_b32 s44, 0x4e0000\n\t" \
    "buffer_load_dwordx4 v240, %[wr], s44 offen offset:0 nt lds\n\t" \
    "buffer_load_dwordx4 v241, %[wr], s44 offen offset:1024 nt lds\n\t" \
    "buffer_load_dwordx4 v242, %[wr], s44 offen offset:2048 nt lds\n\t" \
    "ds_read_b64_tr_b16 v[216:217], v244 offset:0\n\t" \
    "ds_read_b64_tr_b16 v[218:219], v244 offset:1536\n\t" \
    "ds_read_b64_tr_b16 v[80:81], v244 offset:32\n\t" \
    "ds_read_b64_tr_b16 v[82:83], v244 offset:1568\n\t" \
    "ds_read_b64_tr_b16 v[88:89], v244 offset:64\n\t" \
    "ds_read_b64_tr_b16 v[90:91], v244 offset:1600\n\t" \
    "v_add_u32 v244, 0x6000, v243\n\t" \
    "s_waitcnt vmcnt(12) lgkmcnt(0)\n\t" \
    "s_barrier \n\t" \
    "s_add_u32 m0, %[wm], 0x0\n\t" \
    "s_mov_b32 s44, 0x540000\n\t" \
    "buffer_load_dwordx4 v240, %[wr], s44 offen offset:0 nt lds\n\t" \
    "buffer_load_dwordx4 v241, %[wr], s44 offen offset:1024 nt lds\n\t" \
    "buffer_load_dwordx4 v242, %[wr], s44 offen offset:2048 nt lds\n\t" \
    "ds_read_b64_tr_b16 v[220:221], v244 offset:0\n\t" \
    "ds_read_b64_tr_b16 v[222:223], v244 offset:1536\n\t" \
    "ds_read_b64_tr_b16 v[84:85], v244 offset:32\n\t" \
    "ds_read_b64_tr_b16 v[86:87], v244 offset:1568\n\t" \
    "ds_read_b64_tr_b16 v[92:93], v244 offset:64\n\t" \
    "ds_read_b64_tr_b16 v[94:95], v244 offset:1600\n\t" \
    "v_add_u32 v244, 0xc000, v243\n\t" \
    "s_waitcnt vmcnt(12) lgkmcnt(0)\n\t" \
    "s_barrier \n\t" \
    "s_add_u32 m0, %[wm], 0x6000\n\t" \
    "s_mov_b32 s44, 0x5a0000\n\t" \
    "buffer_load_dwordx4 v240, %[wr], s44 offen offset:0 nt lds\n\t" \
    "buffer_load_dwordx4 v241, %[wr], s44 offen offset:1024 nt lds\n\t" \
    "buffer_load_dwordx4 v242, %[wr], s44 offen offset:2048 nt lds\n\t" \
    "ds_read_b64_tr_b16 v[96:97], v244 offset:0\n\t" \
    "ds_read_b64_tr_b16 v[98:99], v244 offset:1536\n\t" \
    "ds_read_b64_tr_b16 v[104:105], v244 offset:32\n\t" \
    "ds_read_b64_tr_b16 v[106:107], v244 offset:1568\n\t" \
    "ds_read_b64_tr_b16 v[112:113], v244 offset:64\n\t" \
    "ds_read_b64_tr_b16 v[114:115], v244 offset:1600\n\t" \
    "v_add_u32 v244, 0x12000, v243\n\t" \
    "s_waitcnt vmcnt(12) lgkmcnt(0)\n\t" \
    "s_barrier \n\t" \
    "s_add_u32 m0, %[wm], 0xc000\n\t" \
    "s_mov_b32 s44, 0x600000\n\t" \
    "buffer_load_dwordx4 v240, %[wr], s44 offen offset:0 nt lds\n\t" \
    "buffer_load_dwordx4 v241, %[wr], s44 offen offset:1024 nt lds\n\t" \
    "buffer_load_dwordx4 v242, %[wr], s44 offen offset:2048 nt lds\n\t" \
    "ds_read_b64_tr_b16 v[100:101], v244 offset:0\n\t" \
    "ds_read_b64_tr_b16 v[102:103], v244 offset:1536\n\t" \
    "ds_read_b64_tr_b16 v[108:109], v244 offset:32\n\t" \
    "ds_read_b64_tr_b16 v[110:111], v244 offset:1568\n\t" \
    "ds_read_b64_tr_b16 v[116:117], v244 offset:64\n\t" \
    "ds_read_b64_tr_b16 v[118:119], v244 offset:1600\n\t" \
    "v_add_u32 v244, 0x18000, v243\n\t" \
    "s_waitcnt vmcnt(12) lgkmcnt(0)\n\t" \
    "s_barrier \n\t" \
    "s_add_u32 m0, %[wm], 0x12000\n\t" \
    "s_mov_b32 s44, 0x660000\n\t" \
    "buffer_load_dwordx4 v240, %[wr], s44 offen offset:0 nt lds\n\t" \
    "buffer_load_dwordx4 v241, %[wr], s44 offen offset:1024 nt lds\n\t" \
    "buffer_load_dwordx4 v242, %[wr], s44 offen offset:2048 nt lds\n\t" \
    "ds_read_b64_tr_b16 v[224:225], v244 offset:0\n\t" \
    "ds_read_b64_tr_b16 v[226:227], v244 offset:1536\n\t" \
    "ds_read_b64_tr_b16 v[120:121], v244 offset:32\n\t" \
    "ds_read_b64_tr_b16 v[122:123], v244 offset:1568\n\t" \
    "ds_read_b64_tr_b16 v[128:129], v244 offset:64\n\t" \
    "ds_read_b64_tr_b16 v[130:131], v244 offset:1600\n\t" \
    "v_add_u32 v244, 0x1e000, v243\n\t" \
    "s_waitcnt vmcnt(12) lgkmcnt(0)\n\t" \
    "s_barrier \n\t" \
    ".Ls41_%=:\n\t" \
    "v_readfirstlane_b32 s43, v246\n\t" \
    "s_nop 0\n\t" \
    "s_cmp_lg_u32 s43, -1\n\t" \
    "s_cbranch_scc1 .Ls42_%=\n\t" \
    "s_cmp_eq_u32 s41, 0\n\t" \
    "s_cbranch_scc1 .Ls42_%=\n\t" \
    "s_sub_u32 s41, s41, 1\n\t" \
    "s_sleep 4\n\t" \
    "v_mov_b32 v246, 0\n\t" \
    "buffer_load_dword v246, v246, s[60:63], %[mo] offen sc1\n\t" \
    "s_waitcnt vmcnt(0)\n\t" \
    "s_branch .Ls41_%=\n\t" \
    ".Ls42_%=:\n\t" \
    "v_mov_b32 v246, 0x8000\n\t" \
    "s_mov_b64 exec, 1\n\t" \
    "buffer_store_dword v246, v246, s[60:63], %[mo] offen sc1\n\t" \
    "s_mov_b64 exec, -1\n\t" \
    "s_and_b32 s44, s43, 0x5000\n\t" \
    "s_xor_b32 %[mb], %[mb], s44\n\t" \
    "s_xor_b32 %[fl], %[fl], s44\n\t" \
    "s_and_b32 s43, s43, 0x2000\n\t" \
    "s_xor_b32 s43, s43, %[lx]\n\t" \
    "s_add_u32 s60, s60, s43\n\t" \
    "s_addc_u32 s61, s61, 0\n\t" \
    "v_and_b32 v252, 0xf0, v253\n\t" \
    "v_lshlrev_b32 v252, 3, v252\n\t" \
    "buffer_load_dword v252, v252, s[60:63], %[fl] offen sc1\n\t" \
    "s_add_u32 m0, %[wm], 0x18000\n\t" \
    "s_mov_b32 s44, 0x6c0000\n\t" \
    "buffer_load_dwordx4 v240, %[wr], s44 offen offset:0 nt lds\n\t" \
    "buffer_load_dwordx4 v241, %[wr], s44 offen offset:1024 nt lds\n\t" \
    "buffer_load_dwordx4 v242, %[wr], s44 offen offset:2048 nt lds\n\t" \
    "ds_read_b64_tr_b16 v[228:229], v244 offset:0\n\t" \
    "ds_read_b64_tr_b16 v[230:231], v244 offset:1536\n\t" \
    "ds_read_b64_tr_b16 v[124:125], v244 offset:32\n\t" \
    "ds_read_b64_tr_b16 v[126:127], v244 offset:1568\n\t" \
    "ds_read_b64_tr_b16 v[132:133], v244 offset:64\n\t" \
    "ds_read_b64_tr_b16 v[134:135], v244 offset:1600\n\t" \
    "v_add_u32 v244, 0x0, v243\n\t" \
    "s_waitcnt vmcnt(12) lgkmcnt(0)\n\t" \
    "s_barrier \n\t" \
    "s_add_u32 m0, %[wm], 0x1e000\n\t" \
    "s_mov_b32 s44, 0x720000\n\t" \
    "buffer_load_dwordx4 v240, %[wr], s44 offen offset:0 nt lds\n\t" \
    "buffer_load_dwordx4 v241, %[wr], s44 offen offset:1024 nt lds\n\t" \
    "buffer_load_dwordx4 v242, %[wr], s44 offen offset:2048 nt lds\n\t" \
    "ds_read_b64_tr_b16 v[136:137], v244 offset:0\n\t" \
    "ds_read_b64_tr_b16 v[138:139], v244 offset:1536\n\t" \
    "ds_read_b64_tr_b16 v[144:145], v244 offset:32\n\t" \
    "ds_read_b64_tr_b16 v[146:147], v244 offset:1568\n\t" \
    "ds_read_b64_tr_b16 v[152:153], v244 offset:64\n\t" \
    "ds_read_b64_tr_b16 v[154:155], v244 offset:1600\n\t" \
    "v_add_u32 v244, 0x6000, v243\n\t" \
    "s_waitcnt vmcnt(12) lgkmcnt(0)\n\t" \
    "s_barrier \n\t" \
    "ds_read_b64_tr_b16 v[140:141], v244 offset:0\n\t" \
    "ds_read_b64_tr_b16 v[142:143], v244 offset:1536\n\t" \
    "ds_read_b64_tr_b16 v[148:149], v244 offset:32\n\t" \
    "ds_read_b64_tr_b16 v[150:151], v244 offset:1568\n\t" \
    "ds_read_b64_tr_b16 v[156:157], v244 offset:64\n\t" \
    "ds_read_b64_tr_b16 v[158:159], v244 offset:1600\n\t" \
    "v_add_u32 v244, 0xc000, v243\n\t" \
    "s_waitcnt vmcnt(9) lgkmcnt(0)\n\t" \
    "s_barrier \n\t" \
    "ds_read_b64_tr_b16 v[232:233], v244 offset:0\n\t" \
    "ds_read_b64_tr_b16 v[234:235], v244 offset:1536\n\t" \
    "ds_read_b64_tr_b16 v[160:161], v244 offset:32\n\t" \
    "ds_read_b64_tr_b16 v[162:163], v244 offset:1568\n\t" \
    "ds_read_b64_tr_b16 v[168:169], v244 offset:64\n\t" \
    "ds_read_b64_tr_b16 v[170:171], v244 offset:1600\n\t" \
    "v_add_u32 v244, 0x12000, v243\n\t" \
    "s_waitcnt vmcnt(6) lgkmcnt(0)\n\t" \
    "s_barrier \n\t" \
    "ds_read_b64_tr_b16 v[236:237], v244 offset:0\n\t" \
    "ds_read_b64_tr_b16 v[238:239], v244 offset:1536\n\t" \
    "ds_read_b64_tr_b16 v[164:165], v244 offset:32\n\t" \
    "ds_read_b64_tr_b16 v[166:167], v244 offset:1568\n\t" \
    "ds_read_b64_tr_b16 v[172:173], v244 offset:64\n\t" \
    "ds_read_b64_tr_b16 v[174:175], v244 offset:1600\n\t" \
    "v_add_u32 v244, 0x18000, v243\n\t" \
    "s_waitcnt vmcnt(3) lgkmcnt(0)\n\t" \
    "s_barrier \n\t" \
    "ds_read_b64_tr_b16 v[176:177], v244 offset:0\n\t" \
    "ds_read_b64_tr_b16 v[178:179], v244 offset:1536\n\t" \
    "ds_read_b64_tr_b16 v[184:185], v244 offset:32\n\t" \
    "ds_read_b64_tr_b16 v[186:187], v244 offset:1568\n\t" \
    "ds_read_b64_tr_b16 v[192:193], v244 offset:64\n\t" \
    "ds_read_b64_tr_b16 v[194:195], v244 offset:1600\n\t" \
    "s_mov_b32 s43, 0\n\t" \
    "v_cmp_eq_u32 vcc, 0, v252\n\t" \
    "s_cbranch_vccnz .Ls43_%=\n\t" \
    "s_mov_b32 m0, %[m0]\n\t" \
    "s_add_u32 s45, s42, %[x1]\n\t" \
    "buffer_load_dwordx4 v253, s[60:63], s42 offen offset:0 sc1 lds\n\t" \
    "buffer_load_dwordx4 v253, s[60:63], s42 offen offset:1024 sc1 lds\n\t" \
    "buffer_load_dwordx4 v253, s[60:63], s42 offen offset:2048 sc1 lds\n\t" \
    "buffer_load_dwordx4 v253, s[60:63], s42 offen offset:3072 sc1 lds\n\t" \
    "s_mov_b32 m0, %[m1]\n\t" \
    "s_nop 0\n\t" \
    "buffer_load_dwordx4 v253, s[60:63], s45 offen offset:0 sc1 lds\n\t" \
    "buffer_load_dwordx4 v253, s[60:63], s45 offen offset:1024 sc1 lds\n\t" \
    "s_mov_b32 s43, 1\n\t" \
    ".Ls43_%=:\n\t" \
    "v_add_u32 v244, 0x1e000, v243\n\t" \
    "s_waitcnt vmcnt(0) lgkmcnt(0)\n\t" \
    "s_barrier \n\t" \
    "ds_read_b64_tr_b16 v[180:181], v244 offset:0\n\t" \
    "ds_read_b64_tr_b16 v[182:183], v244 offset:1536\n\t" \
    "ds_read_b64_tr_b16 v[188:189], v244 offset:32\n\t" \
    "ds_read_b64_tr_b16 v[190:191], v244 offset:1568\n\t" \
    "ds_read_b64_tr_b16 v[196:197], v244 offset:64\n\t" \
    "ds_read_b64_tr_b16 v[198:199], v244 offset:1600\n\t" \
    "s_waitcnt lgkmcnt(0)\n\t" \
    "s_barrier \n\t" \
    "ds_write_b128 v254, v[200:203] offset:0\n\t" \
    "ds_write_b128 v254, v[204:207] offset:1024\n\t" \
    "ds_write_b128 v254, v[208:211] offset:2048\n\t" \
    "ds_write_b128 v254, v[212:215] offset:3072\n\t" \
    "ds_write_b128 v254, v[216:219] offset:4096\n\t" \
    "ds_write_b128 v254, v[220:223] offset:5120\n\t" \
    "ds_write_b128 v254, v[224:227] offset:6144\n\t" \
    "ds_write_b128 v254, v[228:231] offset:7168\n\t" \
    "ds_write_b128 v254, v[232:235] offset:8192\n\t" \
    "ds_write_b128 v254, v[236:239] offset:9216\n\t" \
    "s_cmp_lg_u32 s43, 0\n\t" \
    "s_cbranch_scc1 .Ls44_%=\n\t" \
    "s_mov_b32 s44, 0\n\t" \
    ".Ls45_%=:\n\t" \
    "v_cmp_ge_u32 vcc, s44, v252\n\t" \
    "s_cbranch_vccz .Ls46_%=\n\t" \
    "s_cmp_eq_u32 s41, 0\n\t" \
    "s_cbranch_scc1 .Ls46_%=\n\t" \
    "s_sub_u32 s41, s41, 1\n\t" \
    "s_sleep 8\n\t" \
    "v_and_b32 v252, 0xf0, v253\n\t" \
    "v_lshlrev_b32 v252, 3, v252\n\t" \
    "buffer_load_dword v252, v252, s[60:63], %[fl] offen sc1\n\t" \
    "s_waitcnt vmcnt(0)\n\t" \
    "s_branch .Ls45_%=\n\t" \
    ".Ls46_%=:\n\t" \
    "s_mov_b32 m0, %[m0]\n\t" \
    "s_add_u32 s45, s42, %[x1]\n\t" \
    "buffer_load_dwordx4 v253, s[60:63], s42 offen offset:0 sc1 lds\n\t" \
    "buffer_load_dwordx4 v253, s[60:63], s42 offen offset:1024 sc1 lds\n\t" \
    "buffer_load_dwordx4 v253, s[60:63], s42 offen offset:2048 sc1 lds\n\t" \
    "buffer_load_dwordx4 v253, s[60:63], s42 offen offset:3072 sc1 lds\n\t" \
    "s_mov_b32 m0, %[m1]\n\t" \
    "s_nop 0\n\t" \
    "buffer_load_dwordx4 v253, s[60:63], s45 offen offset:0 sc1 lds\n\t" \
    "buffer_load_dwordx4 v253, s[60:63], s45 offen offset:1024 sc1 lds\n\t" \
    ".Ls44_%=:\n\t" \
    "s_waitcnt vmcnt(0) lgkmcnt(0)\n\t" \
    "s_barrier \n\t" \
    "ds_read_b128 v[224:227], v253 offset:0\n\t" \
    "ds_read_b128 v[244:247], v254 offset:0\n\t" \
    "ds_read_b128 v[228:231], v253 offset:1024\n\t" \
    "ds_read_b128 v[232:235], v253 offset:20480\n\t" \
    "ds_read_b128 v[236:239], v253 offset:21504\n\t" \
    "ds_read_b128 v[248:251], v254 offset:1024\n\t" \
    ".Ls47_%=:\n\t" \
    "s_cmp_lg_u32 %[wave], 4\n\t" \
    "s_cbranch_scc1 .Ls48_%=\n\t" \
    "s_sleep 8\n\t" \
    ".Ls48_%=:\n\t" \
    "s_waitcnt lgkmcnt(5)\n\t" \
    "v_mfma_f32_16x16x32_bf16 v[204:207], v[0:3], v[224:227], 0\n\t" \
    "s_add_u32 m0, %[m0], 0x3000\n\t" \
    "s_waitcnt lgkmcnt(3)\n\t" \
    "v_mfma_f32_16x16x32_bf16 v[204:207], v[4:7], v[228:231], v[204:207]\n\t" \
    "ds_read_b128 v[240:243], v253 offset:2048\n\t" \
    "s_add_u32 s45, s42, 0xc000\n\t" \
    "v_mfma_f32_16x16x32_bf16 v[208:211], v[8:11], v[224:227], 0\n\t" \
    "buffer_load_dwordx4 v253, s[60:63], s45 offen offset:0 sc1 lds\n\t" \
    "v_mfma_f32_16x16x32_bf16 v[208:211], v[12:15], v[228:231], v[208:211]\n\t" \
    "v_mfma_f32_16x16x32_bf16 v[200:203], v[244:247], v[224:227], 0\n\t" \
    "s_waitcnt lgkmcnt(1)\n\t" \
    "v_mfma_f32_16x16x32_bf16 v[200:203], v[248:251], v[228:231], v[200:203]\n\t" \
    "buffer_load_dwordx4 v253, s[60:63], s45 offen offset:1024 sc1 lds\n\t" \
    "v_mfma_f32_16x16x32_bf16 v[216:219], v[0:3], v[232:235], 0\n\t" \
    "ds_read_b128 v[224:227], v253 offset:3072\n\t" \
    "v_mfma_f32_16x16x32_bf16 v[216:219], v[4:7], v[236:239], v[216:219]\n\t" \
    "ds_read_b128 v[228:231], v253 offset:22528\n\t" \
    "v_mfma_f32_16x16x32_bf16 v[220:223], v[8:11], v[232:235], 0\n\t" \
    "buffer_load_dwordx4 v253, s[60:63], s45 offen offset:2048 sc1 lds\n\t" \
    "v_mfma_f32_16x16x32_bf16 v[220:223], v[12:15], v[236:239], v[220:223]\n\t" \
    "v_mfma_f32_16x16x32_bf16 v[212:215], v[244:247], v[232:235], 0\n\t" \
    "ds_read_b128 v[244:247], v254 offset:2048\n\t" \
    "v_mfma_f32_16x16x32_bf16 v[212:215], v[248:251], v[236:239], v[212:215]\n\t" \
    "ds_read_b128 v[248:251], v254 offset:3072\n\t" \
    "buffer_load_dwordx4 v253, s[60:63], s45 offen offset:3072 sc1 lds\n\t" \
    "s_waitcnt lgkmcnt(4)\n\t" \
    "v_mfma_f32_16x16x32_bf16 v[204:207], v[24:27], v[240:243], v[204:207]\n\t" \
    "ds_read_b128 v[232:235], v253 offset:23552\n\t" \
    "s_waitcnt lgkmcnt(4)\n\t" \
    "v_mfma_f32_16x16x32_bf16 v[204:207], v[28:31], v[224:227], v[204:207]\n\t" \
    "ds_read_b128 v[236:239], v253 offset:4096\n\t" \
    "v_mfma_f32_16x16x32_bf16 v[208:211], v[32:35], v[240:243], v[208:211]\n\t" \
    "v_mfma_f32_16x16x32_bf16 v[208:211], v[36:39], v[224:227], v[208:211]\n\t" \
    "v_mfma_f32_16x16x32_bf16 v[200:203], v[16:19], v[240:243], v[200:203]\n\t" \
    "v_mfma_f32_16x16x32_bf16 v[200:203], v[20:23], v[224:227], v[200:203]\n\t" \
    "s_waitcnt lgkmcnt(4)\n\t" \
    "v_mfma_f32_16x16x32_bf16 v[216:219], v[24:27], v[228:231], v[216:219]\n\t" \
    "ds_read_b128 v[240:243], v253 offset:5120\n\t" \
    "s_waitcnt lgkmcnt(2)\n\t" \
    "v_mfma_f32_16x16x32_bf16 v[216:219], v[28:31], v[232:235], v[216:219]\n\t" \
    "ds_read_b128 v[224:227], v253 offset:24576\n\t" \
    "v_mfma_f32_16x16x32_bf16 v[220:223], v[32:35], v[228:231], v[220:223]\n\t" \
    "v_mfma_f32_16x16x32_bf16 v[220:223], v[36:39], v[232:235], v[220:223]\n\t" \
    "v_mfma_f32_16x16x32_bf16 v[212:215], v[16:19], v[228:231], v[212:215]\n\t" \
    "v_mfma_f32_16x16x32_bf16 v[212:215], v[20:23], v[232:235], v[212:215]\n\t" \
    "s_waitcnt lgkmcnt(2)\n\t" \
    "v_mfma_f32_16x16x32_bf16 v[204:207], v[40:43], v[236:239], v[204:207]\n\t" \
    "ds_read_b128 v[228:231], v253 offset:25600\n\t" \
    "s_waitcnt lgkmcnt(2)\n\t" \
    "v_mfma_f32_16x16x32_bf16 v[204:207], v[44:47], v[240:243], v[204:207]\n\t" \
    "ds_read_b128 v[232:235], v253 offset:6144\n\t" \
    "v_mfma_f32_16x16x32_bf16 v[208:211], v[48:51], v[236:239], v[208:211]\n\t" \
    "v_mfma_f32_16x16x32_bf16 v[208:211], v[52:55], v[240:243], v[208:211]\n\t" \
    "v_mfma_f32_16x16x32_bf16 v[200:203], v[244:247], v[236:239], v[200:203]\n\t" \
    "v_mfma_f32_16x16x32_bf16 v[200:203], v[248:251], v[240:243], v[200:203]\n\t" \
    "s_waitcnt lgkmcnt(2)\n\t" \
    "v_mfma_f32_16x16x32_bf16 v[216:219], v[40:43], v[224:227], v[216:219]\n\t" \
    "ds_read_b128 v[236:239], v253 offset:7168\n\t" \
    "s_waitcnt lgkmcnt(2)\n\t" \
    "v_mfma_f32_16x16x32_bf16 v[216:219], v[44:47], v[228:231], v[216:219]\n\t" \
    "ds_read_b128 v[240:243], v253 offset:26624\n\t" \
    "v_mfma_f32_16x16x32_bf16 v[220:223], v[48:51], v[224:227], v[220:223]\n\t" \
    "v_mfma_f32_16x16x32_bf16 v[220:223], v[52:55], v[228:231], v[220:223]\n\t" \
    "v_mfma_f32_16x16x32_bf16 v[212:215], v[244:247], v[224:227], v[212:215]\n\t" \
    "ds_read_b128 v[244:247], v254 offset:4096\n\t" \
    "v_mfma_f32_16x16x32_bf16 v[212:215], v[248:251], v[228:231], v[212:215]\n\t" \
    "ds_read_b128 v[248:251], v254 offset:5120\n\t" \
    "s_waitcnt lgkmcnt(4)\n\t" \
    "v_mfma_f32_16x16x32_bf16 v[204:207], v[64:67], v[232:235], v[204:207]\n\t" \
    "ds_read_b128 v[224:227], v253 offset:27648\n\t" \
    "s_waitcnt lgkmcnt(4)\n\t" \
    "v_mfma_f32_16x16x32_bf16 v[204:207], v[68:71], v[236:239], v[204:207]\n\t" \
    "ds_read_b128 v[228:231], v253 offset:8192\n\t" \
    "v_mfma_f32_16x16x32_bf16 v[208:211], v[72:75], v[232:235], v[208:211]\n\t" \
    "v_mfma_f32_16x16x32_bf16 v[208:211], v[76:79], v[236:239], v[208:211]\n\t" \
    "v_mfma_f32_16x16x32_bf16 v[200:203], v[56:59], v[232:235], v[200:203]\n\t" \
    "v_mfma_f32_16x16x32_bf16 v[200:203], v[60:63], v[236:239], v[200:203]\n\t" \
    "v_and_b32 v252, 0xf0, v253\n\t" \
    "v_lshlrev_b32 v252, 3, v252\n\t" \
    "buffer_load_dword v252, v252, s[60:63], %[fl] offen sc1\n\t" \
    "s_waitcnt lgkmcnt(4)\n\t" \
    "v_mfma_f32_16x16x32_bf16 v[216:219], v[64:67], v[240:243], v[216:219]\n\t" \
    "ds_read_b128 v[232:235], v253 offset:9216\n\t" \
    "s_waitcnt lgkmcnt(2)\n\t" \
    "v_mfma_f32_16x16x32_bf16 v[216:219], v[68:71], v[224:227], v[216:219]\n\t" \
    "ds_read_b128 v[236:239], v253 offset:28672\n\t" \
    "v_mfma_f32_16x16x32_bf16 v[220:223], v[72:75], v[240:243], v[220:223]\n\t" \
    "v_mfma_f32_16x16x32_bf16 v[220:223], v[76:79], v[224:227], v[220:223]\n\t" \
    "v_mfma_f32_16x16x32_bf16 v[212:215], v[56:59], v[240:243], v[212:215]\n\t" \
    "v_mfma_f32_16x16x32_bf16 v[212:215], v[60:63], v[224:227], v[212:215]\n\t" \
    "s_waitcnt lgkmcnt(2)\n\t" \
    "v_mfma_f32_16x16x32_bf16 v[204:207], v[80:83], v[228:231], v[204:207]\n\t" \
    "ds_read_b128 v[240:243], v253 offset:29696\n\t" \
    "s_waitcnt lgkmcnt(2)\n\t" \
    "v_mfma_f32_16x16x32_bf16 v[204:207], v[84:87], v[232:235], v[204:207]\n\t" \
    "ds_read_b128 v[224:227], v253 offset:10240\n\t" \
    "v_mfma_f32_16x16x32_bf16 v[208:211], v[88:91], v[228:231], v[208:211]\n\t" \
    "v_mfma_f32_16x16x32_bf16 v[208:211], v[92:95], v[232:235], v[208:211]\n\t" \
    "v_mfma_f32_16x16x32_bf16 v[200:203], v[244:247], v[228:231], v[200:203]\n\t" \
    "v_mfma_f32_16x16x32_bf16 v[200:203], v[248:251], v[232:235], v[200:203]\n\t" \
    "s_waitcnt lgkmcnt(2)\n\t" \
    "v_mfma_f32_16x16x32_bf16 v[216:219], v[80:83], v[236:239], v[216:219]\n\t" \
    "ds_read_b128 v[228:231], v253 offset:11264\n\t" \
    "s_add_u32 s44, s40, 1\n\t" \
    "s_waitcnt lgkmcnt(2)\n\t" \
    "v_mfma_f32_16x16x32_bf16 v[216:219], v[84:87], v[240:243], v[216:219]\n\t" \
    "ds_read_b128 v[232:235], v253 offset:30720\n\t" \
    "s_and_b32 s45, s44, 15\n\t" \
    "v_mfma_f32_16x16x32_bf16 v[220:223], v[88:91], v[236:239], v[220:223]\n\t" \
    "s_cmp_eq_u32 s45, 0\n\t" \
    "v_mfma_f32_16x16x32_bf16 v[220:223], v[92:95], v[240:243], v[220:223]\n\t" \
    "s_cselect_b32 s45, %[cj], %[cs]\n\t" \
    "v_mfma_f32_16x16x32_bf16 v[212:215], v[244:247], v[236:239], v[212:215]\n\t" \
    "ds_read_b128 v[244:247], v254 offset:6144\n\t" \
    "s_cmp_eq_u32 s44, 64\n\t" \
    "v_mfma_f32_16x16x32_bf16 v[212:215], v[248:251], v[240:243], v[212:215]\n\t" \
    "ds_read_b128 v[248:251], v254 offset:7168\n\t" \
    "s_cselect_b32 s45, 0, s45\n\t" \
    "ds_read_b128 v[236:239], v253 offset:31744\n\t" \
    "s_setprio 1\n\t" \
    "s_waitcnt vmcnt(0) lgkmcnt(0)\n\t" \
    "s_barrier \n\t" \
    "s_add_u32 s42, s42, s45\n\t" \
    "s_add_u32 s44, s40, 1\n\t" \
    "s_min_u32 s44, s44, 63\n\t" \
    ".Ls49_%=:\n\t" \
    "v_cmp_ge_u32 vcc, s44, v252\n\t" \
    "s_cbranch_vccz .Ls410_%=\n\t" \
    "s_cmp_eq_u32 s41, 0\n\t" \
    "s_cbranch_scc1 .Ls410_%=\n\t" \
    "s_sub_u32 s41, s41, 1\n\t" \
    "s_sleep 2\n\t" \
    "v_and_b32 v252, 0xf0, v253\n\t" \
    "v_lshlrev_b32 v252, 3, v252\n\t" \
    "buffer_load_dword v252, v252, s[60:63], %[fl] offen sc1\n\t" \
    "s_waitcnt vmcnt(0)\n\t" \
    "s_branch .Ls49_%=\n\t" \
    ".Ls410_%=:\n\t" \
    "s_mov_b32 m0, %[m0]\n\t" \
    "s_add_u32 s45, s42, %[x1]\n\t" \
    "buffer_load_dwordx4 v253, s[60:63], s42 offen offset:0 sc1 lds\n\t" \
    "buffer_load_dwordx4 v253, s[60:63], s42 offen offset:1024 sc1 lds\n\t" \
    "buffer_load_dwordx4 v253, s[60:63], s42 offen offset:2048 sc1 lds\n\t" \
    "buffer_load_dwordx4 v253, s[60:63], s42 offen offset:3072 sc1 lds\n\t" \
    "s_mov_b32 m0, %[m1]\n\t" \
    "s_nop 0\n\t" \
    "buffer_load_dwordx4 v253, s[60:63], s45 offen offset:0 sc1 lds\n\t" \
    "buffer_load_dwordx4 v253, s[60:63], s45 offen offset:1024 sc1 lds\n\t" \
    "v_mfma_f32_16x16x32_bf16 v[204:207], v[104:107], v[224:227], v[204:207]\n\t" \
    "v_mfma_f32_16x16x32_bf16 v[204:207], v[108:111], v[228:231], v[204:207]\n\t" \
    "ds_read_b128 v[240:243], v253 offset:12288\n\t" \
    "v_mfma_f32_16x16x32_bf16 v[208:211], v[112:115], v[224:227], v[208:211]\n\t" \
    "v_mfma_f32_16x16x32_bf16 v[208:211], v[116:119], v[228:231], v[208:211]\n\t" \
    "v_mfma_f32_16x16x32_bf16 v[200:203], v[96:99], v[224:227], v[200:203]\n\t" \
    "v_mfma_f32_16x16x32_bf16 v[200:203], v[100:103], v[228:231], v[200:203]\n\t" \
    "v_mfma_f32_16x16x32_bf16 v[216:219], v[104:107], v[232:235], v[216:219]\n\t" \
    "ds_read_b128 v[224:227], v253 offset:13312\n\t" \
    "v_mfma_f32_16x16x32_bf16 v[216:219], v[108:111], v[236:239], v[216:219]\n\t" \
    "ds_read_b128 v[228:231], v253 offset:32768\n\t" \
    "v_mfma_f32_16x16x32_bf16 v[220:223], v[112:115], v[232:235], v[220:223]\n\t" \
    "v_mfma_f32_16x16x32_bf16 v[220:223], v[116:119], v[236:239], v[220:223]\n\t" \
    "v_mfma_f32_16x16x32_bf16 v[212:215], v[96:99], v[232:235], v[212:215]\n\t" \
    "v_mfma_f32_16x16x32_bf16 v[212:215], v[100:103], v[236:239], v[212:215]\n\t" \
    "s_waitcnt lgkmcnt(2)\n\t" \
    "v_mfma_f32_16x16x32_bf16 v[204:207], v[120:123], v[240:243], v[204:207]\n\t" \
    "ds_read_b128 v[232:235], v253 offset:33792\n\t" \
    "s_waitcnt lgkmcnt(2)\n\t" \
    "v_mfma_f32_16x16x32_bf16 v[204:207], v[124:127], v[224:227], v[204:207]\n\t" \
    "ds_read_b128 v[236:239], v253 offset:16384\n\t" \
    "v_mfma_f32_16x16x32_bf16 v[208:211], v[128:131], v[240:243], v[208:211]\n\t" \
    "v_mfma_f32_16x16x32_bf16 v[208:211], v[132:135], v[224:227], v[208:211]\n\t" \
    "v_mfma_f32_16x16x32_bf16 v[200:203], v[244:247], v[240:243], v[200:203]\n\t" \
    "v_mfma_f32_16x16x32_bf16 v[200:203], v[248:251], v[224:227], v[200:203]\n\t" \
    "s_waitcnt lgkmcnt(2)\n\t" \
    "v_mfma_f32_16x16x32_bf16 v[216:219], v[120:123], v[228:231], v[216:219]\n\t" \
    "ds_read_b128 v[240:243], v253 offset:17408\n\t" \
    "s_waitcnt lgkmcnt(2)\n\t" \
    "v_mfma_f32_16x16x32_bf16 v[216:219], v[124:127], v[232:235], v[216:219]\n\t" \
    "ds_read_b128 v[224:227], v253 offset:36864\n\t" \
    "v_mfma_f32_16x16x32_bf16 v[220:223], v[128:131], v[228:231], v[220:223]\n\t" \
    "v_mfma_f32_16x16x32_bf16 v[220:223], v[132:135], v[232:235], v[220:223]\n\t" \
    "v_mfma_f32_16x16x32_bf16 v[212:215], v[244:247], v[228:231], v[212:215]\n\t" \
    "ds_read_b128 v[244:247], v254 offset:8192\n\t" \
    "v_mfma_f32_16x16x32_bf16 v[212:215], v[248:251], v[232:235], v[212:215]\n\t" \
    "ds_read_b128 v[248:251], v254 offset:9216\n\t" \
    "s_waitcnt lgkmcnt(4)\n\t" \
    "v_mfma_f32_16x16x32_bf16 v[204:207], v[160:163], v[236:239], v[204:207]\n\t" \
    "ds_read_b128 v[228:231], v253 offset:37888\n\t" \
    "s_waitcnt lgkmcnt(4)\n\t" \
    "v_mfma_f32_16x16x32_bf16 v[204:207], v[164:167], v[240:243], v[204:207]\n\t" \
    "ds_read_b128 v[232:235], v253 offset:14336\n\t" \
    "v_mfma_f32_16x16x32_bf16 v[208:211], v[168:171], v[236:239], v[208:211]\n\t" \
    "v_mfma_f32_16x16x32_bf16 v[208:211], v[172:175], v[240:243], v[208:211]\n\t" \
    "s_waitcnt lgkmcnt(3)\n\t" \
    "v_mfma_f32_16x16x32_bf16 v[200:203], v[244:247], v[236:239], v[200:203]\n\t" \
    "s_waitcnt lgkmcnt(2)\n\t" \
    "v_mfma_f32_16x16x32_bf16 v[200:203], v[248:251], v[240:243], v[200:203]\n\t" \
    "v_mfma_f32_16x16x32_bf16 v[216:219], v[160:163], v[224:227], v[216:219]\n\t" \
    "ds_read_b128 v[236:239], v253 offset:15360\n\t" \
    "v_add_u32 v252, %[q2], v253\n\t" \
    "s_waitcnt lgkmcnt(2)\n\t" \
    "v_mfma_f32_16x16x32_bf16 v[216:219], v[164:167], v[228:231], v[216:219]\n\t" \
    "ds_read_b128 v[240:243], v253 offset:18432\n\t" \
    "v_mfma_f32_16x16x32_bf16 v[220:223], v[168:171], v[224:227], v[220:223]\n\t" \
    "v_mfma_f32_16x16x32_bf16 v[220:223], v[172:175], v[228:231], v[220:223]\n\t" \
    "v_mfma_f32_16x16x32_bf16 v[212:215], v[244:247], v[224:227], v[212:215]\n\t" \
    "v_mfma_f32_16x16x32_bf16 v[212:215], v[248:251], v[228:231], v[212:215]\n\t" \
    "s_waitcnt lgkmcnt(2)\n\t" \
    "v_mfma_f32_16x16x32_bf16 v[204:207], v[144:147], v[232:235], v[204:207]\n\t" \
    "ds_read_b128 v[224:227], v253 offset:19456\n\t" \
    "v_mov_b64 v[244:245], 0\n\t" \
    "s_waitcnt lgkmcnt(2)\n\t" \
    "v_mfma_f32_16x16x32_bf16 v[204:207], v[148:151], v[236:239], v[204:207]\n\t" \
    "ds_read_b128 v[228:231], v253 offset:34816\n\t" \
    "v_mov_b64 v[246:247], 0\n\t" \
    "v_mfma_f32_16x16x32_bf16 v[208:211], v[152:155], v[232:235], v[208:211]\n\t" \
    "v_mov_b64 v[248:249], 0\n\t" \
    "v_mfma_f32_16x16x32_bf16 v[208:211], v[156:159], v[236:239], v[208:211]\n\t" \
    "v_mov_b64 v[250:251], 0\n\t" \
    "v_mfma_f32_16x16x32_bf16 v[200:203], v[136:139], v[232:235], v[200:203]\n\t" \
    "v_mfma_f32_16x16x32_bf16 v[200:203], v[140:143], v[236:239], v[200:203]\n\t" \
    "s_waitcnt lgkmcnt(2)\n\t" \
    "v_mfma_f32_16x16x32_bf16 v[204:207], v[184:187], v[240:243], v[204:207]\n\t" \
    "ds_read_b128 v[232:235], v253 offset:35840\n\t" \
    "s_waitcnt lgkmcnt(2)\n\t" \
    "v_mfma_f32_16x16x32_bf16 v[204:207], v[188:191], v[224:227], v[204:207]\n\t" \
    "ds_read_b128 v[236:239], v253 offset:38912\n\t" \
    "v_mfma_f32_16x16x32_bf16 v[208:211], v[192:195], v[240:243], v[208:211]\n\t" \
    "v_mfma_f32_16x16x32_bf16 v[208:211], v[196:199], v[224:227], v[208:211]\n\t" \
    "v_mfma_f32_16x16x32_bf16 v[200:203], v[176:179], v[240:243], v[200:203]\n\t" \
    "v_mfma_f32_16x16x32_bf16 v[200:203], v[180:183], v[224:227], v[200:203]\n\t" \
    "ds_read_b128 v[240:243], v253 offset:39936\n\t" \
    "s_waitcnt lgkmcnt(3)\n\t" \
    "v_mfma_f32_16x16x32_bf16 v[216:219], v[144:147], v[228:231], v[216:219]\n\t" \
    "s_waitcnt lgkmcnt(2)\n\t" \
    "v_mfma_f32_16x16x32_bf16 v[216:219], v[148:151], v[232:235], v[216:219]\n\t" \
    "v_cvt_pk_bf16_f32 v204, v204, v208\n\t" \
    "v_cvt_pk_bf16_f32 v205, v205, v209\n\t" \
    "s_waitcnt lgkmcnt(1)\n\t" \
    "v_mfma_f32_16x16x32_bf16 v[216:219], v[184:187], v[236:239], v[216:219]\n\t" \
    "v_cvt_pk_bf16_f32 v206, v206, v210\n\t" \
    "v_cvt_pk_bf16_f32 v207, v207, v211\n\t" \
    "s_waitcnt lgkmcnt(0)\n\t" \
    "v_mfma_f32_16x16x32_bf16 v[216:219], v[188:191], v[240:243], v[216:219]\n\t" \
    "ds_write_b128 v252, v[204:207] offset:0\n\t" \
    "v_mfma_f32_16x16x32_bf16 v[220:223], v[152:155], v[228:231], v[220:223]\n\t" \
    "v_mfma_f32_16x16x32_bf16 v[220:223], v[156:159], v[232:235], v[220:223]\n\t" \
    "v_add_u32 v252, %[qs], v253\n\t" \
    "v_cvt_pk_bf16_f32 v200, v200, v201\n\t" \
    "v_mfma_f32_16x16x32_bf16 v[220:223], v[192:195], v[236:239], v[220:223]\n\t" \
    "v_cvt_pk_bf16_f32 v201, v202, v203\n\t" \
    "ds_write_b64 v252, v[200:201] offset:0\n\t" \
    "v_mfma_f32_16x16x32_bf16 v[220:223], v[196:199], v[240:243], v[220:223]\n\t" \
    "v_mfma_f32_16x16x32_bf16 v[212:215], v[136:139], v[228:231], v[212:215]\n\t" \
    "v_mov_b64 v[208:209], 0\n\t" \
    "v_mfma_f32_16x16x32_bf16 v[212:215], v[140:143], v[232:235], v[212:215]\n\t" \
    "v_mov_b64 v[210:211], 0\n\t" \
    "v_mfma_f32_16x16x32_bf16 v[212:215], v[176:179], v[236:239], v[212:215]\n\t" \
    "v_add_u32 v252, %[q2], v253\n\t" \
    "v_cvt_pk_bf16_f32 v216, v216, v220\n\t" \
    "v_mfma_f32_16x16x32_bf16 v[212:215], v[180:183], v[240:243], v[212:215]\n\t" \
    "v_cvt_pk_bf16_f32 v217, v217, v221\n\t" \
    "v_cvt_pk_bf16_f32 v218, v218, v222\n\t" \
    "v_cvt_pk_bf16_f32 v219, v219, v223\n\t" \
    "ds_write_b128 v252, v[216:219] offset:6144\n\t" \
    "v_add_u32 v252, %[qs], v253\n\t" \
    "s_nop 2\n\t" \
    "v_cvt_pk_bf16_f32 v212, v212, v213\n\t" \
    "v_cvt_pk_bf16_f32 v213, v214, v215\n\t" \
    "ds_write_b64 v252, v[212:213] offset:6144\n\t" \
    "s_setprio 2\n\t" \
    "s_waitcnt vmcnt(0) lgkmcnt(0)\n\t" \
    "s_barrier \n\t" \
    "ds_read_b128 v[224:227], v253 offset:40960\n\t" \
    "ds_read_b128 v[228:231], v253 offset:41984\n\t" \
    "ds_read_b128 v[232:235], v253 offset:43008\n\t" \
    "ds_read_b128 v[236:239], v253 offset:44032\n\t" \
    "ds_read_b128 v[240:243], v253 offset:45056\n\t" \
    "ds_read_b128 v[200:203], v254 offset:0\n\t" \
    "ds_read_b128 v[204:207], v254 offset:1024\n\t" \
    "ds_read_b128 v[216:219], v254 offset:2048\n\t" \
    "ds_read_b128 v[220:223], v254 offset:3072\n\t" \
    "v_bfe_i32 v212, v253, 5, 1\n\t" \
    "v_and_b32 v212, 0xaaaa, v212\n\t" \
    "v_or_b32 v212, 0x4444, v212\n\t" \
    "v_and_b32 v213, 0xffff, v255\n\t" \
    "v_lshrrev_b32 v214, 16, v255\n\t" \
    "s_waitcnt lgkmcnt(8)\n\t" \
    "v_smfmac_f32_16x16x64_bf16 v[244:247], v[224:227], v[0:7], v212\n\t" \
    "v_smfmac_f32_16x16x64_bf16 v[248:251], v[224:227], v[8:15], v212\n\t" \
    "s_waitcnt lgkmcnt(2)\n\t" \
    "v_smfmac_f32_16x16x64_bf16 v[208:211], v[224:227], v[200:207], v212\n\t" \
    "ds_read_b128 v[200:203], v254 offset:4096\n\t" \
    "ds_read_b128 v[204:207], v254 offset:5120\n\t" \
    "ds_read_b128 v[224:227], v253 offset:46080\n\t" \
    "v_smfmac_f32_16x16x64_bf16 v[208:211], v[228:231], v[16:23], v212\n\t" \
    "v_smfmac_f32_16x16x64_bf16 v[244:247], v[228:231], v[24:31], v212\n\t" \
    "v_smfmac_f32_16x16x64_bf16 v[248:251], v[228:231], v[32:39], v212\n\t" \
    "ds_read_b128 v[228:231], v253 offset:47104\n\t" \
    "s_waitcnt lgkmcnt(4)\n\t" \
    "v_smfmac_f32_16x16x64_bf16 v[208:211], v[232:235], v[216:223], v212\n\t" \
    "ds_read_b128 v[216:219], v254 offset:6144\n\t" \
    "ds_read_b128 v[220:223], v254 offset:7168\n\t" \
    "v_smfmac_f32_16x16x64_bf16 v[244:247], v[232:235], v[40:47], v212\n\t" \
    "v_smfmac_f32_16x16x64_bf16 v[248:251], v[232:235], v[48:55], v212\n\t" \
    "ds_read_b128 v[232:235], v253 offset:48128\n\t" \
    "v_smfmac_f32_16x16x64_bf16 v[208:211], v[236:239], v[56:63], v212\n\t" \
    "v_smfmac_f32_16x16x64_bf16 v[244:247], v[236:239], v[64:71], v212\n\t" \
    "v_smfmac_f32_16x16x64_bf16 v[248:251], v[236:239], v[72:79], v212\n\t" \
    "ds_read_b128 v[236:239], v253 offset:49152\n\t" \
    "s_waitcnt lgkmcnt(6)\n\t" \
    "v_smfmac_f32_16x16x64_bf16 v[208:211], v[240:243], v[200:207], v212\n\t" \
    "ds_read_b128 v[200:203], v254 offset:8192\n\t" \
    "ds_read_b128 v[204:207], v254 offset:9216\n\t" \
    "v_smfmac_f32_16x16x64_bf16 v[244:247], v[240:243], v[80:87], v212\n\t" \
    "v_smfmac_f32_16x16x64_bf16 v[248:251], v[240:243], v[88:95], v212\n\t" \
    "ds_read_b128 v[240:243], v253 offset:50176\n\t" \
    "s_waitcnt lgkmcnt(8)\n\t" \
    "v_smfmac_f32_16x16x64_bf16 v[208:211], v[224:227], v[96:103], v212\n\t" \
    "v_smfmac_f32_16x16x64_bf16 v[244:247], v[224:227], v[104:111], v212\n\t" \
    "v_smfmac_f32_16x16x64_bf16 v[248:251], v[224:227], v[112:119], v212\n\t" \
    "s_waitcnt lgkmcnt(5)\n\t" \
    "v_smfmac_f32_16x16x64_bf16 v[208:211], v[228:231], v[216:223], v212\n\t" \
    "s_waitcnt lgkmcnt(4)\n\t" \
    "v_smfmac_f32_16x16x64_bf16 v[208:211], v[232:235], v[136:143], v212\n\t" \
    "s_waitcnt lgkmcnt(1)\n\t" \
    "v_smfmac_f32_16x16x64_bf16 v[208:211], v[236:239], v[200:207], v212\n\t" \
    "s_waitcnt lgkmcnt(0)\n\t" \
    "v_smfmac_f32_16x16x64_bf16 v[208:211], v[240:243], v[176:183], v212\n\t" \
    "v_smfmac_f32_16x16x64_bf16 v[244:247], v[228:231], v[120:127], v212\n\t" \
    "v_smfmac_f32_16x16x64_bf16 v[244:247], v[232:235], v[144:151], v212\n\t" \
    "v_smfmac_f32_16x16x64_bf16 v[244:247], v[236:239], v[160:167], v212\n\t" \
    "v_add_f32 v208, v208, v210\n\t" \
    "v_add_f32 v209, v209, v211\n\t" \
    "v_cvt_pk_bf16_f32 v208, v208, v209\n\t" \
    "v_smfmac_f32_16x16x64_bf16 v[244:247], v[240:243], v[184:191], v212\n\t" \
    "ds_write_b16 v213, v208 offset:0\n\t" \
    "ds_write_b16_d16_hi v213, v208 offset:16\n\t" \
    "v_smfmac_f32_16x16x64_bf16 v[248:251], v[228:231], v[128:135], v212\n\t" \
    "v_smfmac_f32_16x16x64_bf16 v[248:251], v[232:235], v[152:159], v212\n\t" \
    "v_smfmac_f32_16x16x64_bf16 v[248:251], v[236:239], v[168:175], v212\n\t" \
    "v_add_f32 v244, v244, v246\n\t" \
    "v_add_f32 v245, v245, v247\n\t" \
    "v_smfmac_f32_16x16x64_bf16 v[248:251], v[240:243], v[192:199], v212\n\t" \
    "s_nop 7\n\t" \
    "v_add_f32 v248, v248, v250\n\t" \
    "v_add_f32 v249, v249, v251\n\t" \
    "v_cvt_pk_bf16_f32 v244, v244, v248\n\t" \
    "v_cvt_pk_bf16_f32 v245, v245, v249\n\t" \
    "ds_write2_b32 v214, v244, v245 offset1:4\n\t" \
    "s_setprio 0\n\t" \
    "s_waitcnt lgkmcnt(0)\n\t" \
    "s_barrier \n\t" \
    "ds_read_b128 v[224:227], v253 offset:0\n\t" \
    "ds_read_b128 v[244:247], v254 offset:0\n\t" \
    "ds_read_b128 v[228:231], v253 offset:1024\n\t" \
    "ds_read_b128 v[232:235], v253 offset:20480\n\t" \
    "ds_read_b128 v[236:239], v253 offset:21504\n\t" \
    "ds_read_b128 v[248:251], v254 offset:1024\n\t" \
    "s_setprio 1\n\t" \
    "s_add_u32 s40, s40, 1\n\t" \
    "s_cmp_lg_u32 s40, 64\n\t" \
    "s_cbranch_scc1 .Ls47_%=\n\t" \
    "s_branch .Lend%=\n\t" \
    ".Ls3_%=:\n\t" \
    "v_add_u32 v244, 0x18000, v243\n\t" \
    "s_waitcnt vmcnt(12) lgkmcnt(0)\n\t" \
    "s_barrier \n\t" \
    "s_add_u32 m0, %[wm], 0x12000\n\t" \
    "s_mov_b32 s44, 0x1e0000\n\t" \
    "buffer_load_dwordx4 v240, %[wr], s44 offen offset:0 nt lds\n\t" \
    "buffer_load_dwordx4 v241, %[wr], s44 offen offset:1024 nt lds\n\t" \
    "buffer_load_dwordx4 v242, %[wr], s44 offen offset:2048 nt lds\n\t" \
    "ds_read_b64_tr_b16 v[208:209], v244 offset:0\n\t" \
    "ds_read_b64_tr_b16 v[210:211], v244 offset:1536\n\t" \
    "ds_read_b64_tr_b16 v[40:41], v244 offset:32\n\t" \
    "ds_read_b64_tr_b16 v[42:43], v244 offset:1568\n\t" \
    "ds_read_b64_tr_b16 v[48:49], v244 offset:64\n\t" \
    "ds_read_b64_tr_b16 v[50:51], v244 offset:1600\n\t" \
    "v_add_u32 v244, 0x1e000, v243\n\t" \
    "s_waitcnt vmcnt(12) lgkmcnt(0)\n\t" \
    "s_barrier \n\t" \
    "s_add_u32 m0, %[wm], 0x18000\n\t" \
    "s_mov_b32 s44, 0x240000\n\t" \
    "buffer_load_dwordx4 v240, %[wr], s44 offen offset:0 nt lds\n\t" \
    "buffer_load_dwordx4 v241, %[wr], s44 offen offset:1024 nt lds\n\t" \
    "buffer_load_dwordx4 v242, %[wr], s44 offen offset:2048 nt lds\n\t" \
    "ds_read_b64_tr_b16 v[212:213], v244 offset:0\n\t" \
    "ds_read_b64_tr_b16 v[214:215], v244 offset:1536\n\t" \
    "ds_read_b64_tr_b16 v[44:45], v244 offset:32\n\t" \
    "ds_read_b64_tr_b16 v[46:47], v244 offset:1568\n\t" \
    "ds_read_b64_tr_b16 v[52:53], v244 offset:64\n\t" \
    "ds_read_b64_tr_b16 v[54:55], v244 offset:1600\n\t" \
    "v_add_u32 v244, 0x0, v243\n\t" \
    "s_waitcnt vmcnt(12) lgkmcnt(0)\n\t" \
    "s_barrier \n\t" \
    "s_add_u32 m0, %[wm], 0x1e000\n\t" \
    "s_mov_b32 s44, 0x2a0000\n\t" \
    "buffer_load_dwordx4 v240, %[wr], s44 offen offset:0 nt lds\n\t" \
    "buffer_load_dwordx4 v241, %[wr], s44 offen offset:1024 nt lds\n\t" \
    "buffer_load_dwordx4 v242, %[wr], s44 offen offset:2048 nt lds\n\t" \
    "ds_read_b64_tr_b16 v[56:57], v244 offset:0\n\t" \
    "ds_read_b64_tr_b16 v[58:59], v244 offset:1536\n\t" \
    "ds_read_b64_tr_b16 v[64:65], v244 offset:32\n\t" \
    "ds_read_b64_tr_b16 v[66:67], v244 offset:1568\n\t" \
    "ds_read_b64_tr_b16 v[72:73], v244 offset:64\n\t" \
    "ds_read_b64_tr_b16 v[74:75], v244 offset:1600\n\t" \
    "v_add_u32 v244, 0x6000, v243\n\t" \
    "s_waitcnt vmcnt(12) lgkmcnt(0)\n\t" \
    "s_barrier \n\t" \
    "s_add_u32 m0, %[wm], 0x0\n\t" \
    "s_mov_b32 s44, 0x300000\n\t" \
    "buffer_load_dwordx4 v240, %[wr], s44 offen offset:0 nt lds\n\t" \
    "buffer_load_dwordx4 v241, %[wr], s44 offen offset:1024 nt lds\n\t" \
    "buffer_load_dwordx4 v242, %[wr], s44 offen offset:2048 nt lds\n\t" \
    "ds_read_b64_tr_b16 v[60:61], v244 offset:0\n\t" \
    "ds_read_b64_tr_b16 v[62:63], v244 offset:1536\n\t" \
    "ds_read_b64_tr_b16 v[68:69], v244 offset:32\n\t" \
    "ds_read_b64_tr_b16 v[70:71], v244 offset:1568\n\t" \
    "ds_read_b64_tr_b16 v[76:77], v244 offset:64\n\t" \
    "ds_read_b64_tr_b16 v[78:79], v244 offset:1600\n\t" \
    "v_add_u32 v244, 0xc000, v243\n\t" \
    "s_waitcnt vmcnt(12) lgkmcnt(0)\n\t" \
    "s_barrier \n\t" \
    "s_add_u32 m0, %[wm], 0x6000\n\t" \
    "s_mov_b32 s44, 0x360000\n\t" \
    "buffer_load_dwordx4 v240, %[wr], s44 offen offset:0 nt lds\n\t" \
    "buffer_load_dwordx4 v241, %[wr], s44 offen offset:1024 nt lds\n\t" \
    "buffer_load_dwordx4 v242, %[wr], s44 offen offset:2048 nt lds\n\t" \
    "ds_read_b64_tr_b16 v[216:217], v244 offset:0\n\t" \
    "ds_read_b64_tr_b16 v[218:219], v244 offset:1536\n\t" \
    "ds_read_b64_tr_b16 v[80:81], v244 offset:32\n\t" \
    "ds_read_b64_tr_b16 v[82:83], v244 offset:1568\n\t" \
    "ds_read_b64_tr_b16 v[88:89], v244 offset:64\n\t" \
    "ds_read_b64_tr_b16 v[90:91], v244 offset:1600\n\t" \
    "v_add_u32 v244, 0x12000, v243\n\t" \
    "s_waitcnt vmcnt(12) lgkmcnt(0)\n\t" \
    "s_barrier \n\t" \
    "s_add_u32 m0, %[wm], 0xc000\n\t" \
    "s_mov_b32 s44, 0x3c0000\n\t" \
    "buffer_load_dwordx4 v240, %[wr], s44 offen offset:0 nt lds\n\t" \
    "buffer_load_dwordx4 v241, %[wr], s44 offen offset:1024 nt lds\n\t" \
    "buffer_load_dwordx4 v242, %[wr], s44 offen offset:2048 nt lds\n\t" \
    "ds_read_b64_tr_b16 v[220:221], v244 offset:0\n\t" \
    "ds_read_b64_tr_b16 v[222:223], v244 offset:1536\n\t" \
    "ds_read_b64_tr_b16 v[84:85], v244 offset:32\n\t" \
    "ds_read_b64_tr_b16 v[86:87], v244 offset:1568\n\t" \
    "ds_read_b64_tr_b16 v[92:93], v244 offset:64\n\t" \
    "ds_read_b64_tr_b16 v[94:95], v244 offset:1600\n\t" \
    "v_add_u32 v244, 0x18000, v243\n\t" \
    "s_waitcnt vmcnt(12) lgkmcnt(0)\n\t" \
    "s_barrier \n\t" \
    "s_add_u32 m0, %[wm], 0x12000\n\t" \
    "s_mov_b32 s44, 0x420000\n\t" \
    "buffer_load_dwordx4 v240, %[wr], s44 offen offset:0 nt lds\n\t" \
    "buffer_load_dwordx4 v241, %[wr], s44 offen offset:1024 nt lds\n\t" \
    "buffer_load_dwordx4 v242, %[wr], s44 offen offset:2048 nt lds\n\t" \
    "ds_read_b64_tr_b16 v[96:97], v244 offset:0\n\t" \
    "ds_read_b64_tr_b16 v[98:99], v244 offset:1536\n\t" \
    "ds_read_b64_tr_b16 v[104:105], v244 offset:32\n\t" \
    "ds_read_b64_tr_b16 v[106:107], v244 offset:1568\n\t" \
    "ds_read_b64_tr_b16 v[112:113], v244 offset:64\n\t" \
    "ds_read_b64_tr_b16 v[114:115], v244 offset:1600\n\t" \
    "v_add_u32 v244, 0x1e000, v243\n\t" \
    "s_waitcnt vmcnt(12) lgkmcnt(0)\n\t" \
    "s_barrier \n\t" \
    "s_add_u32 m0, %[wm], 0x18000\n\t" \
    "s_mov_b32 s44, 0x480000\n\t" \
    "buffer_load_dwordx4 v240, %[wr], s44 offen offset:0 nt lds\n\t" \
    "buffer_load_dwordx4 v241, %[wr], s44 offen offset:1024 nt lds\n\t" \
    "buffer_load_dwordx4 v242, %[wr], s44 offen offset:2048 nt lds\n\t" \
    "ds_read_b64_tr_b16 v[100:101], v244 offset:0\n\t" \
    "ds_read_b64_tr_b16 v[102:103], v244 offset:1536\n\t" \
    "ds_read_b64_tr_b16 v[108:109], v244 offset:32\n\t" \
    "ds_read_b64_tr_b16 v[110:111], v244 offset:1568\n\t" \
    "ds_read_b64_tr_b16 v[116:117], v244 offset:64\n\t" \
    "ds_read_b64_tr_b16 v[118:119], v244 offset:1600\n\t" \
    "v_add_u32 v244, 0x0, v243\n\t" \
    "s_waitcnt vmcnt(12) lgkmcnt(0)\n\t" \
    "s_barrier \n\t" \
    "v_mov_b32 v246, 0\n\t" \
    "buffer_load_dword v246, v246, s[60:63], %[mo] offen sc1\n\t" \
    "s_add_u32 m0, %[wm], 0x1e000\n\t" \
    "s_mov_b32 s44, 0x4e0000\n\t" \
    "buffer_load_dwordx4 v240, %[wr], s44 offen offset:0 nt lds\n\t" \
    "buffer_load_dwordx4 v241, %[wr], s44 offen offset:1024 nt lds\n\t" \
    "buffer_load_dwordx4 v242, %[wr], s44 offen offset:2048 nt lds\n\t" \
    "ds_read_b64_tr_b16 v[224:225], v244 offset:0\n\t" \
    "ds_read_b64_tr_b16 v[226:227], v244 offset:1536\n\t" \
    "ds_read_b64_tr_b16 v[120:121], v244 offset:32\n\t" \
    "ds_read_b64_tr_b16 v[122:123], v244 offset:1568\n\t" \
    "ds_read_b64_tr_b16 v[128:129], v244 offset:64\n\t" \
    "ds_read_b64_tr_b16 v[130:131], v244 offset:1600\n\t" \
    "v_add_u32 v244, 0x6000, v243\n\t" \
    "s_waitcnt vmcnt(12) lgkmcnt(0)\n\t" \
    "s_barrier \n\t" \
    "s_add_u32 m0, %[wm], 0x0\n\t" \
    "s_mov_b32 s44, 0x540000\n\t" \
    "buffer_load_dwordx4 v240, %[wr], s44 offen offset:0 nt lds\n\t" \
    "buffer_load_dwordx4 v241, %[wr], s44 offen offset:1024 nt lds\n\t" \
    "buffer_load_dwordx4 v242, %[wr], s44 offen offset:2048 nt lds\n\t" \
    "ds_read_b64_tr_b16 v[228:229], v244 offset:0\n\t" \
    "ds_read_b64_tr_b16 v[230:231], v244 offset:1536\n\t" \
    "ds_read_b64_tr_b16 v[124:125], v244 offset:32\n\t" \
    "ds_read_b64_tr_b16 v[126:127], v244 offset:1568\n\t" \
    "ds_read_b64_tr_b16 v[132:133], v244 offset:64\n\t" \
    "ds_read_b64_tr_b16 v[134:135], v244 offset:1600\n\t" \
    "v_add_u32 v244, 0xc000, v243\n\t" \
    "s_waitcnt vmcnt(12) lgkmcnt(0)\n\t" \
    "s_barrier \n\t" \
    "s_add_u32 m0, %[wm], 0x6000\n\t" \
    "s_mov_b32 s44, 0x5a0000\n\t" \
    "buffer_load_dwordx4 v240, %[wr], s44 offen offset:0 nt lds\n\t" \
    "buffer_load_dwordx4 v241, %[wr], s44 offen offset:1024 nt lds\n\t" \
    "buffer_load_dwordx4 v242, %[wr], s44 offen offset:2048 nt lds\n\t" \
    "ds_read_b64_tr_b16 v[136:137], v244 offset:0\n\t" \
    "ds_read_b64_tr_b16 v[138:139], v244 offset:1536\n\t" \
    "ds_read_b64_tr_b16 v[144:145], v244 offset:32\n\t" \
    "ds_read_b64_tr_b16 v[146:147], v244 offset:1568\n\t" \
    "ds_read_b64_tr_b16 v[152:153], v244 offset:64\n\t" \
    "ds_read_b64_tr_b16 v[154:155], v244 offset:1600\n\t" \
    "v_add_u32 v244, 0x12000, v243\n\t" \
    "s_waitcnt vmcnt(12) lgkmcnt(0)\n\t" \
    "s_barrier \n\t" \
    "s_add_u32 m0, %[wm], 0xc000\n\t" \
    "s_mov_b32 s44, 0x600000\n\t" \
    "buffer_load_dwordx4 v240, %[wr], s44 offen offset:0 nt lds\n\t" \
    "buffer_load_dwordx4 v241, %[wr], s44 offen offset:1024 nt lds\n\t" \
    "buffer_load_dwordx4 v242, %[wr], s44 offen offset:2048 nt lds\n\t" \
    "ds_read_b64_tr_b16 v[140:141], v244 offset:0\n\t" \
    "ds_read_b64_tr_b16 v[142:143], v244 offset:1536\n\t" \
    "ds_read_b64_tr_b16 v[148:149], v244 offset:32\n\t" \
    "ds_read_b64_tr_b16 v[150:151], v244 offset:1568\n\t" \
    "ds_read_b64_tr_b16 v[156:157], v244 offset:64\n\t" \
    "ds_read_b64_tr_b16 v[158:159], v244 offset:1600\n\t" \
    "v_add_u32 v244, 0x18000, v243\n\t" \
    "s_waitcnt vmcnt(12) lgkmcnt(0)\n\t" \
    "s_barrier \n\t" \
    "s_add_u32 m0, %[wm], 0x12000\n\t" \
    "s_mov_b32 s44, 0x660000\n\t" \
    "buffer_load_dwordx4 v240, %[wr], s44 offen offset:0 nt lds\n\t" \
    "buffer_load_dwordx4 v241, %[wr], s44 offen offset:1024 nt lds\n\t" \
    "buffer_load_dwordx4 v242, %[wr], s44 offen offset:2048 nt lds\n\t" \
    "ds_read_b64_tr_b16 v[232:233], v244 offset:0\n\t" \
    "ds_read_b64_tr_b16 v[234:235], v244 offset:1536\n\t" \
    "ds_read_b64_tr_b16 v[160:161], v244 offset:32\n\t" \
    "ds_read_b64_tr_b16 v[162:163], v244 offset:1568\n\t" \
    "ds_read_b64_tr_b16 v[168:169], v244 offset:64\n\t" \
    "ds_read_b64_tr_b16 v[170:171], v244 offset:1600\n\t" \
    "v_add_u32 v244, 0x1e000, v243\n\t" \
    "s_waitcnt vmcnt(12) lgkmcnt(0)\n\t" \
    "s_barrier \n\t" \
    ".Ls31_%=:\n\t" \
    "v_readfirstlane_b32 s43, v246\n\t" \
    "s_nop 0\n\t" \
    "s_cmp_lg_u32 s43, -1\n\t" \
    "s_cbranch_scc1 .Ls32_%=\n\t" \
    "s_cmp_eq_u32 s41, 0\n\t" \
    "s_cbranch_scc1 .Ls32_%=\n\t" \
    "s_sub_u32 s41, s41, 1\n\t" \
    "s_sleep 4\n\t" \
    "v_mov_b32 v246, 0\n\t" \
    "buffer_load_dword v246, v246, s[60:63], %[mo] offen sc1\n\t" \
    "s_waitcnt vmcnt(0)\n\t" \
    "s_branch .Ls31_%=\n\t" \
    ".Ls32_%=:\n\t" \
    "v_mov_b32 v246, 0x8000\n\t" \
    "s_mov_b64 exec, 1\n\t" \
    "buffer_store_dword v246, v246, s[60:63], %[mo] offen sc1\n\t" \
    "s_mov_b64 exec, -1\n\t" \
    "s_and_b32 s44, s43, 0x5000\n\t" \
    "s_xor_b32 %[mb], %[mb], s44\n\t" \
    "s_xor_b32 %[fl], %[fl], s44\n\t" \
    "s_and_b32 s43, s43, 0x2000\n\t" \
    "s_xor_b32 s43, s43, %[lx]\n\t" \
    "s_add_u32 s60, s60, s43\n\t" \
    "s_addc_u32 s61, s61, 0\n\t" \
    "s_add_u32 m0, %[wm], 0x18000\n\t" \
    "s_mov_b32 s44, 0x6c0000\n\t" \
    "buffer_load_dwordx4 v240, %[wr], s44 offen offset:0 nt lds\n\t" \
    "buffer_load_dwordx4 v241, %[wr], s44 offen offset:1024 nt lds\n\t" \
    "buffer_load_dwordx4 v242, %[wr], s44 offen offset:2048 nt lds\n\t" \
    "ds_read_b64_tr_b16 v[236:237], v244 offset:0\n\t" \
    "ds_read_b64_tr_b16 v[238:239], v244 offset:1536\n\t" \
    "ds_read_b64_tr_b16 v[164:165], v244 offset:32\n\t" \
    "ds_read_b64_tr_b16 v[166:167], v244 offset:1568\n\t" \
    "ds_read_b64_tr_b16 v[172:173], v244 offset:64\n\t" \
    "ds_read_b64_tr_b16 v[174:175], v244 offset:1600\n\t" \
    "v_add_u32 v244, 0x0, v243\n\t" \
    "s_waitcnt vmcnt(12) lgkmcnt(0)\n\t" \
    "s_barrier \n\t" \
    "s_add_u32 m0, %[wm], 0x1e000\n\t" \
    "s_mov_b32 s44, 0x720000\n\t" \
    "buffer_load_dwordx4 v240, %[wr], s44 offen offset:0 nt lds\n\t" \
    "buffer_load_dwordx4 v241, %[wr], s44 offen offset:1024 nt lds\n\t" \
    "buffer_load_dwordx4 v242, %[wr], s44 offen offset:2048 nt lds\n\t" \
    "ds_read_b64_tr_b16 v[176:177], v244 offset:0\n\t" \
    "ds_read_b64_tr_b16 v[178:179], v244 offset:1536\n\t" \
    "ds_read_b64_tr_b16 v[184:185], v244 offset:32\n\t" \
    "ds_read_b64_tr_b16 v[186:187], v244 offset:1568\n\t" \
    "ds_read_b64_tr_b16 v[192:193], v244 offset:64\n\t" \
    "ds_read_b64_tr_b16 v[194:195], v244 offset:1600\n\t" \
    "v_add_u32 v244, 0x6000, v243\n\t" \
    "s_waitcnt vmcnt(12) lgkmcnt(0)\n\t" \
    "s_barrier \n\t" \
    "ds_read_b64_tr_b16 v[180:181], v244 offset:0\n\t" \
    "ds_read_b64_tr_b16 v[182:183], v244 offset:1536\n\t" \
    "ds_read_b64_tr_b16 v[188:189], v244 offset:32\n\t" \
    "ds_read_b64_tr_b16 v[190:191], v244 offset:1568\n\t" \
    "ds_read_b64_tr_b16 v[196:197], v244 offset:64\n\t" \
    "ds_read_b64_tr_b16 v[198:199], v244 offset:1600\n\t" \
    "v_add_u32 v244, 0xc000, v243\n\t" \
    "s_waitcnt vmcnt(9) lgkmcnt(0)\n\t" \
    "s_barrier \n\t" \
    "ds_read_b64_tr_b16 v[200:201], v244 offset:0\n\t" \
    "ds_read_b64_tr_b16 v[202:203], v244 offset:1536\n\t" \
    "ds_read_b64_tr_b16 v[0:1], v244 offset:32\n\t" \
    "ds_read_b64_tr_b16 v[2:3], v244 offset:1568\n\t" \
    "ds_read_b64_tr_b16 v[8:9], v244 offset:64\n\t" \
    "ds_read_b64_tr_b16 v[10:11], v244 offset:1600\n\t" \
    "v_add_u32 v244, 0x12000, v243\n\t" \
    "s_waitcnt vmcnt(6) lgkmcnt(0)\n\t" \
    "s_barrier \n\t" \
    "ds_read_b64_tr_b16 v[204:205], v244 offset:0\n\t" \
    "ds_read_b64_tr_b16 v[206:207], v244 offset:1536\n\t" \
    "ds_read_b64_tr_b16 v[4:5], v244 offset:32\n\t" \
    "ds_read_b64_tr_b16 v[6:7], v244 offset:1568\n\t" \
    "ds_read_b64_tr_b16 v[12:13], v244 offset:64\n\t" \
    "ds_read_b64_tr_b16 v[14:15], v244 offset:1600\n\t" \
    "v_add_u32 v244, 0x18000, v243\n\t" \
    "s_waitcnt vmcnt(3) lgkmcnt(0)\n\t" \
    "s_barrier \n\t" \
    "ds_read_b64_tr_b16 v[16:17], v244 offset:0\n\t" \
    "ds_read_b64_tr_b16 v[18:19], v244 offset:1536\n\t" \
    "ds_read_b64_tr_b16 v[24:25], v244 offset:32\n\t" \
    "ds_read_b64_tr_b16 v[26:27], v244 offset:1568\n\t" \
    "ds_read_b64_tr_b16 v[32:33], v244 offset:64\n\t" \
    "ds_read_b64_tr_b16 v[34:35], v244 offset:1600\n\t" \
    "v_add_u32 v244, 0x1e000, v243\n\t" \
    "s_waitcnt vmcnt(0) lgkmcnt(0)\n\t" \
    "s_barrier \n\t" \
    "ds_read_b64_tr_b16 v[20:21], v244 offset:0\n\t" \
    "ds_read_b64_tr_b16 v[22:23], v244 offset:1536\n\t" \
    "ds_read_b64_tr_b16 v[28:29], v244 offset:32\n\t" \
    "ds_read_b64_tr_b16 v[30:31], v244 offset:1568\n\t" \
    "ds_read_b64_tr_b16 v[36:37], v244 offset:64\n\t" \
    "ds_read_b64_tr_b16 v[38:39], v244 offset:1600\n\t" \
    "s_waitcnt lgkmcnt(0)\n\t" \
    "s_barrier \n\t" \
    "ds_write_b128 v254, v[200:203] offset:55296\n\t" \
    "ds_write_b128 v254, v[204:207] offset:56320\n\t" \
    "ds_write_b128 v254, v[208:211] offset:57344\n\t" \
    "ds_write_b128 v254, v[212:215] offset:58368\n\t" \
    "ds_write_b128 v254, v[216:219] offset:59392\n\t" \
    "ds_write_b128 v254, v[220:223] offset:60416\n\t" \
    "ds_write_b128 v254, v[224:227] offset:61440\n\t" \
    "ds_write_b128 v254, v[228:231] offset:62464\n\t" \
    "ds_write_b128 v254, v[232:235] offset:63488\n\t" \
    "ds_write_b128 v254, v[236:239] offset:64512\n\t" \
    "s_waitcnt vmcnt(0) lgkmcnt(0)\n\t" \
    "s_barrier \n\t" \
    "ds_read_b128 v[208:211], v254 offset:24576\n\t" \
    "ds_read_b128 v[224:227], v253 offset:0\n\t" \
    "ds_read_b128 v[228:231], v253 offset:20480\n\t" \
    "ds_read_b128 v[212:215], v254 offset:25600\n\t" \
    "ds_read_b128 v[232:235], v253 offset:1024\n\t" \
    "ds_read_b128 v[236:239], v253 offset:21504\n\t" \
    "ds_read_b128 v[216:219], v254 offset:26624\n\t" \
    "ds_read_b128 v[240:243], v253 offset:2048\n\t" \
    "ds_read_b128 v[244:247], v253 offset:22528\n\t" \
    "ds_read_b128 v[220:223], v254 offset:27648\n\t" \
    "ds_read_b128 v[248:251], v253 offset:3072\n\t" \
    "s_waitcnt lgkmcnt(9)\n\t" \
    "v_mfma_f32_16x16x32_bf16 v[200:203], v[208:211], v[224:227], 0\n\t" \
    "ds_read_b128 v[224:227], v253 offset:23552\n\t" \
    "s_waitcnt lgkmcnt(9)\n\t" \
    "v_mfma_f32_16x16x32_bf16 v[204:207], v[208:211], v[228:231], 0\n\t" \
    "ds_read_b128 v[228:231], v253 offset:4096\n\t" \
    "ds_read_b128 v[208:211], v254 offset:28672\n\t" \
    "s_waitcnt lgkmcnt(9)\n\t" \
    "v_mfma_f32_16x16x32_bf16 v[200:203], v[212:215], v[232:235], v[200:203]\n\t" \
    "ds_read_b128 v[232:235], v253 offset:24576\n\t" \
    "s_waitcnt lgkmcnt(9)\n\t" \
    "v_mfma_f32_16x16x32_bf16 v[204:207], v[212:215], v[236:239], v[204:207]\n\t" \
    "ds_read_b128 v[236:239], v253 offset:5120\n\t" \
    "ds_read_b128 v[212:215], v254 offset:29696\n\t" \
    ".Ls33_%=:\n\t" \
    "s_waitcnt lgkmcnt(9)\n\t" \
    "v_mfma_f32_16x16x32_bf16 v[200:203], v[216:219], v[240:243], v[200:203]\n\t" \
    "ds_read_b128 v[240:243], v253 offset:25600\n\t" \
    "s_waitcnt lgkmcnt(9)\n\t" \
    "v_mfma_f32_16x16x32_bf16 v[204:207], v[216:219], v[244:247], v[204:207]\n\t" \
    "ds_read_b128 v[244:247], v253 offset:6144\n\t" \
    "ds_read_b128 v[216:219], v254 offset:30720\n\t" \
    "s_waitcnt lgkmcnt(9)\n\t" \
    "v_mfma_f32_16x16x32_bf16 v[200:203], v[220:223], v[248:251], v[200:203]\n\t" \
    "ds_read_b128 v[248:251], v253 offset:26624\n\t" \
    "s_waitcnt lgkmcnt(9)\n\t" \
    "v_mfma_f32_16x16x32_bf16 v[204:207], v[220:223], v[224:227], v[204:207]\n\t" \
    "ds_read_b128 v[224:227], v253 offset:7168\n\t" \
    "ds_read_b128 v[220:223], v254 offset:31744\n\t" \
    "s_waitcnt lgkmcnt(9)\n\t" \
    "v_mfma_f32_16x16x32_bf16 v[200:203], v[208:211], v[228:231], v[200:203]\n\t" \
    "ds_read_b128 v[228:231], v253 offset:27648\n\t" \
    "s_waitcnt lgkmcnt(9)\n\t" \
    "v_mfma_f32_16x16x32_bf16 v[204:207], v[208:211], v[232:235], v[204:207]\n\t" \
    "ds_read_b128 v[232:235], v253 offset:8192\n\t" \
    "ds_read_b128 v[208:211], v254 offset:32768\n\t" \
    "s_waitcnt lgkmcnt(9)\n\t" \
    "v_mfma_f32_16x16x32_bf16 v[200:203], v[212:215], v[236:239], v[200:203]\n\t" \
    "ds_read_b128 v[236:239], v253 offset:28672\n\t" \
    "s_waitcnt lgkmcnt(9)\n\t" \
    "v_mfma_f32_16x16x32_bf16 v[204:207], v[212:215], v[240:243], v[204:207]\n\t" \
    "ds_read_b128 v[240:243], v253 offset:9216\n\t" \
    "ds_read_b128 v[212:215], v254 offset:33792\n\t" \
    "s_waitcnt lgkmcnt(9)\n\t" \
    "v_mfma_f32_16x16x32_bf16 v[200:203], v[216:219], v[244:247], v[200:203]\n\t" \
    "ds_read_b128 v[244:247], v253 offset:29696\n\t" \
    "s_waitcnt lgkmcnt(9)\n\t" \
    "v_mfma_f32_16x16x32_bf16 v[204:207], v[216:219], v[248:251], v[204:207]\n\t" \
    "s_waitcnt lgkmcnt(7)\n\t" \
    "v_mfma_f32_16x16x32_bf16 v[200:203], v[220:223], v[224:227], v[200:203]\n\t" \
    "s_waitcnt lgkmcnt(6)\n\t" \
    "v_mfma_f32_16x16x32_bf16 v[204:207], v[220:223], v[228:231], v[204:207]\n\t" \
    "s_waitcnt lgkmcnt(4)\n\t" \
    "v_mfma_f32_16x16x32_bf16 v[200:203], v[208:211], v[232:235], v[200:203]\n\t" \
    "s_waitcnt lgkmcnt(3)\n\t" \
    "v_mfma_f32_16x16x32_bf16 v[204:207], v[208:211], v[236:239], v[204:207]\n\t" \
    "s_waitcnt lgkmcnt(1)\n\t" \
    "v_mfma_f32_16x16x32_bf16 v[200:203], v[212:215], v[240:243], v[200:203]\n\t" \
    "s_waitcnt lgkmcnt(0)\n\t" \
    "v_mfma_f32_16x16x32_bf16 v[204:207], v[212:215], v[244:247], v[204:207]\n\t" \
    "ds_read_b128 v[224:227], v253 offset:0\n\t" \
    "ds_read_b128 v[244:247], v254 offset:57344\n\t" \
    "ds_read_b128 v[228:231], v253 offset:1024\n\t" \
    "ds_read_b128 v[232:235], v253 offset:20480\n\t" \
    "ds_read_b128 v[236:239], v253 offset:21504\n\t" \
    "ds_read_b128 v[248:251], v254 offset:58368\n\t" \
    "s_nop 1\n\t" \
    "ds_write_b128 v253, v[200:203] offset:40960\n\t" \
    "ds_write_b128 v253, v[204:207] offset:41984\n\t" \
    "s_nop 1\n\t" \
    "s_waitcnt lgkmcnt(7)\n\t" \
    "v_mfma_f32_16x16x32_bf16 v[204:207], v[40:43], v[224:227], 0\n\t" \
    "s_waitcnt lgkmcnt(5)\n\t" \
    "v_mfma_f32_16x16x32_bf16 v[204:207], v[44:47], v[228:231], v[204:207]\n\t" \
    "ds_read_b128 v[240:243], v253 offset:2048\n\t" \
    "v_mfma_f32_16x16x32_bf16 v[208:211], v[48:51], v[224:227], 0\n\t" \
    "v_mfma_f32_16x16x32_bf16 v[208:211], v[52:55], v[228:231], v[208:211]\n\t" \
    "v_mfma_f32_16x16x32_bf16 v[200:203], v[244:247], v[224:227], 0\n\t" \
    "s_waitcnt lgkmcnt(3)\n\t" \
    "v_mfma_f32_16x16x32_bf16 v[200:203], v[248:251], v[228:231], v[200:203]\n\t" \
    "v_mfma_f32_16x16x32_bf16 v[216:219], v[40:43], v[232:235], 0\n\t" \
    "ds_read_b128 v[224:227], v253 offset:3072\n\t" \
    "v_mfma_f32_16x16x32_bf16 v[216:219], v[44:47], v[236:239], v[216:219]\n\t" \
    "ds_read_b128 v[228:231], v253 offset:22528\n\t" \
    "v_mfma_f32_16x16x32_bf16 v[220:223], v[48:51], v[232:235], 0\n\t" \
    "v_mfma_f32_16x16x32_bf16 v[220:223], v[52:55], v[236:239], v[220:223]\n\t" \
    "v_mfma_f32_16x16x32_bf16 v[212:215], v[244:247], v[232:235], 0\n\t" \
    "ds_read_b128 v[244:247], v254 offset:59392\n\t" \
    "v_mfma_f32_16x16x32_bf16 v[212:215], v[248:251], v[236:239], v[212:215]\n\t" \
    "ds_read_b128 v[248:251], v254 offset:60416\n\t" \
    "s_waitcnt lgkmcnt(4)\n\t" \
    "v_mfma_f32_16x16x32_bf16 v[204:207], v[64:67], v[240:243], v[204:207]\n\t" \
    "ds_read_b128 v[232:235], v253 offset:23552\n\t" \
    "s_waitcnt lgkmcnt(4)\n\t" \
    "v_mfma_f32_16x16x32_bf16 v[204:207], v[68:71], v[224:227], v[204:207]\n\t" \
    "ds_read_b128 v[236:239], v253 offset:4096\n\t" \
    "v_mfma_f32_16x16x32_bf16 v[208:211], v[72:75], v[240:243], v[208:211]\n\t" \
    "v_mfma_f32_16x16x32_bf16 v[208:211], v[76:79], v[224:227], v[208:211]\n\t" \
    "v_mfma_f32_16x16x32_bf16 v[200:203], v[56:59], v[240:243], v[200:203]\n\t" \
    "v_mfma_f32_16x16x32_bf16 v[200:203], v[60:63], v[224:227], v[200:203]\n\t" \
    "s_waitcnt lgkmcnt(4)\n\t" \
    "v_mfma_f32_16x16x32_bf16 v[216:219], v[64:67], v[228:231], v[216:219]\n\t" \
    "ds_read_b128 v[240:243], v253 offset:5120\n\t" \
    "s_waitcnt lgkmcnt(2)\n\t" \
    "v_mfma_f32_16x16x32_bf16 v[216:219], v[68:71], v[232:235], v[216:219]\n\t" \
    "ds_read_b128 v[224:227], v253 offset:24576\n\t" \
    "v_mfma_f32_16x16x32_bf16 v[220:223], v[72:75], v[228:231], v[220:223]\n\t" \
    "v_mfma_f32_16x16x32_bf16 v[220:223], v[76:79], v[232:235], v[220:223]\n\t" \
    "v_mfma_f32_16x16x32_bf16 v[212:215], v[56:59], v[228:231], v[212:215]\n\t" \
    "v_mfma_f32_16x16x32_bf16 v[212:215], v[60:63], v[232:235], v[212:215]\n\t" \
    "s_waitcnt lgkmcnt(2)\n\t" \
    "v_mfma_f32_16x16x32_bf16 v[204:207], v[80:83], v[236:239], v[204:207]\n\t" \
    "ds_read_b128 v[228:231], v253 offset:25600\n\t" \
    "s_waitcnt lgkmcnt(2)\n\t" \
    "v_mfma_f32_16x16x32_bf16 v[204:207], v[84:87], v[240:243], v[204:207]\n\t" \
    "ds_read_b128 v[232:235], v253 offset:6144\n\t" \
    "v_mfma_f32_16x16x32_bf16 v[208:211], v[88:91], v[236:239], v[208:211]\n\t" \
    "v_mfma_f32_16x16x32_bf16 v[208:211], v[92:95], v[240:243], v[208:211]\n\t" \
    "v_mfma_f32_16x16x32_bf16 v[200:203], v[244:247], v[236:239], v[200:203]\n\t" \
    "v_mfma_f32_16x16x32_bf16 v[200:203], v[248:251], v[240:243], v[200:203]\n\t" \
    "s_waitcnt lgkmcnt(2)\n\t" \
    "v_mfma_f32_16x16x32_bf16 v[216:219], v[80:83], v[224:227], v[216:219]\n\t" \
    "ds_read_b128 v[236:239], v253 offset:7168\n\t" \
    "s_waitcnt lgkmcnt(2)\n\t" \
    "v_mfma_f32_16x16x32_bf16 v[216:219], v[84:87], v[228:231], v[216:219]\n\t" \
    "ds_read_b128 v[240:243], v253 offset:26624\n\t" \
    "v_mfma_f32_16x16x32_bf16 v[220:223], v[88:91], v[224:227], v[220:223]\n\t" \
    "v_mfma_f32_16x16x32_bf16 v[220:223], v[92:95], v[228:231], v[220:223]\n\t" \
    "v_mfma_f32_16x16x32_bf16 v[212:215], v[244:247], v[224:227], v[212:215]\n\t" \
    "ds_read_b128 v[244:247], v254 offset:61440\n\t" \
    "v_mfma_f32_16x16x32_bf16 v[212:215], v[248:251], v[228:231], v[212:215]\n\t" \
    "ds_read_b128 v[248:251], v254 offset:62464\n\t" \
    "s_waitcnt lgkmcnt(4)\n\t" \
    "v_mfma_f32_16x16x32_bf16 v[204:207], v[104:107], v[232:235], v[204:207]\n\t" \
    "ds_read_b128 v[224:227], v253 offset:27648\n\t" \
    "s_waitcnt lgkmcnt(4)\n\t" \
    "v_mfma_f32_16x16x32_bf16 v[204:207], v[108:111], v[236:239], v[204:207]\n\t" \
    "ds_read_b128 v[228:231], v253 offset:8192\n\t" \
    "v_mfma_f32_16x16x32_bf16 v[208:211], v[112:115], v[232:235], v[208:211]\n\t" \
    "v_mfma_f32_16x16x32_bf16 v[208:211], v[116:119], v[236:239], v[208:211]\n\t" \
    "v_mfma_f32_16x16x32_bf16 v[200:203], v[96:99], v[232:235], v[200:203]\n\t" \
    "v_mfma_f32_16x16x32_bf16 v[200:203], v[100:103], v[236:239], v[200:203]\n\t" \
    "s_waitcnt lgkmcnt(4)\n\t" \
    "v_mfma_f32_16x16x32_bf16 v[216:219], v[104:107], v[240:243], v[216:219]\n\t" \
    "ds_read_b128 v[232:235], v253 offset:9216\n\t" \
    "s_waitcnt lgkmcnt(2)\n\t" \
    "v_mfma_f32_16x16x32_bf16 v[216:219], v[108:111], v[224:227], v[216:219]\n\t" \
    "ds_read_b128 v[236:239], v253 offset:28672\n\t" \
    "v_mfma_f32_16x16x32_bf16 v[220:223], v[112:115], v[240:243], v[220:223]\n\t" \
    "v_mfma_f32_16x16x32_bf16 v[220:223], v[116:119], v[224:227], v[220:223]\n\t" \
    "v_mfma_f32_16x16x32_bf16 v[212:215], v[96:99], v[240:243], v[212:215]\n\t" \
    "v_mfma_f32_16x16x32_bf16 v[212:215], v[100:103], v[224:227], v[212:215]\n\t" \
    "s_waitcnt lgkmcnt(2)\n\t" \
    "v_mfma_f32_16x16x32_bf16 v[204:207], v[120:123], v[228:231], v[204:207]\n\t" \
    "ds_read_b128 v[240:243], v253 offset:29696\n\t" \
    "s_waitcnt lgkmcnt(2)\n\t" \
    "v_mfma_f32_16x16x32_bf16 v[204:207], v[124:127], v[232:235], v[204:207]\n\t" \
    "ds_read_b128 v[224:227], v253 offset:10240\n\t" \
    "v_mfma_f32_16x16x32_bf16 v[208:211], v[128:131], v[228:231], v[208:211]\n\t" \
    "v_mfma_f32_16x16x32_bf16 v[208:211], v[132:135], v[232:235], v[208:211]\n\t" \
    "v_mfma_f32_16x16x32_bf16 v[200:203], v[244:247], v[228:231], v[200:203]\n\t" \
    "v_mfma_f32_16x16x32_bf16 v[200:203], v[248:251], v[232:235], v[200:203]\n\t" \
    "s_waitcnt lgkmcnt(2)\n\t" \
    "v_mfma_f32_16x16x32_bf16 v[216:219], v[120:123], v[236:239], v[216:219]\n\t" \
    "ds_read_b128 v[228:231], v253 offset:11264\n\t" \
    "s_waitcnt lgkmcnt(2)\n\t" \
    "v_mfma_f32_16x16x32_bf16 v[216:219], v[124:127], v[240:243], v[216:219]\n\t" \
    "ds_read_b128 v[232:235], v253 offset:30720\n\t" \
    "v_mfma_f32_16x16x32_bf16 v[220:223], v[128:131], v[236:239], v[220:223]\n\t" \
    "v_mfma_f32_16x16x32_bf16 v[220:223], v[132:135], v[240:243], v[220:223]\n\t" \
    "v_mfma_f32_16x16x32_bf16 v[212:215], v[244:247], v[236:239], v[212:215]\n\t" \
    "ds_read_b128 v[244:247], v254 offset:63488\n\t" \
    "v_mfma_f32_16x16x32_bf16 v[212:215], v[248:251], v[240:243], v[212:215]\n\t" \
    "ds_read_b128 v[248:251], v254 offset:64512\n\t" \
    "ds_read_b128 v[236:239], v253 offset:31744\n\t" \
    "s_setprio 2\n\t" \
    "s_waitcnt lgkmcnt(0)\n\t" \
    "s_barrier \n\t" \
    "v_mfma_f32_16x16x32_bf16 v[204:207], v[144:147], v[224:227], v[204:207]\n\t" \
    "v_mfma_f32_16x16x32_bf16 v[204:207], v[148:151], v[228:231], v[204:207]\n\t" \
    "ds_read_b128 v[240:243], v253 offset:12288\n\t" \
    "v_mfma_f32_16x16x32_bf16 v[208:211], v[152:155], v[224:227], v[208:211]\n\t" \
    "v_mfma_f32_16x16x32_bf16 v[208:211], v[156:159], v[228:231], v[208:211]\n\t" \
    "v_mfma_f32_16x16x32_bf16 v[200:203], v[136:139], v[224:227], v[200:203]\n\t" \
    "v_mfma_f32_16x16x32_bf16 v[200:203], v[140:143], v[228:231], v[200:203]\n\t" \
    "v_mfma_f32_16x16x32_bf16 v[216:219], v[144:147], v[232:235], v[216:219]\n\t" \
    "ds_read_b128 v[224:227], v253 offset:13312\n\t" \
    "s_and_b32 s44, s40, 3\n\t" \
    "v_mfma_f32_16x16x32_bf16 v[216:219], v[148:151], v[236:239], v[216:219]\n\t" \
    "ds_read_b128 v[228:231], v253 offset:32768\n\t" \
    "s_lshl_b32 s44, s44, 22\n\t" \
    "v_mfma_f32_16x16x32_bf16 v[220:223], v[152:155], v[232:235], v[220:223]\n\t" \
    "s_add_u32 s44, s44, %[mb]\n\t" \
    "v_mfma_f32_16x16x32_bf16 v[220:223], v[156:159], v[236:239], v[220:223]\n\t" \
    "s_add_u32 s46, s44, 0x10000\n\t" \
    "v_mfma_f32_16x16x32_bf16 v[212:215], v[136:139], v[232:235], v[212:215]\n\t" \
    "s_add_u32 s47, s44, 0x10400\n\t" \
    "v_mfma_f32_16x16x32_bf16 v[212:215], v[140:143], v[236:239], v[212:215]\n\t" \
    "s_waitcnt lgkmcnt(2)\n\t" \
    "v_mfma_f32_16x16x32_bf16 v[204:207], v[160:163], v[240:243], v[204:207]\n\t" \
    "ds_read_b128 v[232:235], v253 offset:33792\n\t" \
    "s_waitcnt lgkmcnt(2)\n\t" \
    "v_mfma_f32_16x16x32_bf16 v[204:207], v[164:167], v[224:227], v[204:207]\n\t" \
    "ds_read_b128 v[236:239], v253 offset:16384\n\t" \
    "v_mfma_f32_16x16x32_bf16 v[208:211], v[168:171], v[240:243], v[208:211]\n\t" \
    "v_mfma_f32_16x16x32_bf16 v[208:211], v[172:175], v[224:227], v[208:211]\n\t" \
    "v_mfma_f32_16x16x32_bf16 v[200:203], v[244:247], v[240:243], v[200:203]\n\t" \
    "v_mfma_f32_16x16x32_bf16 v[200:203], v[248:251], v[224:227], v[200:203]\n\t" \
    "s_waitcnt lgkmcnt(2)\n\t" \
    "v_mfma_f32_16x16x32_bf16 v[216:219], v[160:163], v[228:231], v[216:219]\n\t" \
    "ds_read_b128 v[240:243], v253 offset:17408\n\t" \
    "s_waitcnt lgkmcnt(2)\n\t" \
    "v_mfma_f32_16x16x32_bf16 v[216:219], v[164:167], v[232:235], v[216:219]\n\t" \
    "ds_read_b128 v[224:227], v253 offset:36864\n\t" \
    "v_mfma_f32_16x16x32_bf16 v[220:223], v[168:171], v[228:231], v[220:223]\n\t" \
    "v_mfma_f32_16x16x32_bf16 v[220:223], v[172:175], v[232:235], v[220:223]\n\t" \
    "v_mfma_f32_16x16x32_bf16 v[212:215], v[244:247], v[228:231], v[212:215]\n\t" \
    "ds_read_b128 v[244:247], v254 offset:55296\n\t" \
    "v_mfma_f32_16x16x32_bf16 v[212:215], v[248:251], v[232:235], v[212:215]\n\t" \
    "ds_read_b128 v[248:251], v254 offset:56320\n\t" \
    "s_waitcnt lgkmcnt(4)\n\t" \
    "v_mfma_f32_16x16x32_bf16 v[204:207], v[0:3], v[236:239], v[204:207]\n\t" \
    "ds_read_b128 v[228:231], v253 offset:37888\n\t" \
    "s_waitcnt lgkmcnt(4)\n\t" \
    "v_mfma_f32_16x16x32_bf16 v[204:207], v[4:7], v[240:243], v[204:207]\n\t" \
    "ds_read_b128 v[232:235], v253 offset:14336\n\t" \
    "v_mfma_f32_16x16x32_bf16 v[208:211], v[8:11], v[236:239], v[208:211]\n\t" \
    "v_mfma_f32_16x16x32_bf16 v[208:211], v[12:15], v[240:243], v[208:211]\n\t" \
    "s_waitcnt lgkmcnt(3)\n\t" \
    "v_mfma_f32_16x16x32_bf16 v[200:203], v[244:247], v[236:239], v[200:203]\n\t" \
    "s_waitcnt lgkmcnt(2)\n\t" \
    "v_mfma_f32_16x16x32_bf16 v[200:203], v[248:251], v[240:243], v[200:203]\n\t" \
    "v_mfma_f32_16x16x32_bf16 v[216:219], v[0:3], v[224:227], v[216:219]\n\t" \
    "ds_read_b128 v[236:239], v253 offset:15360\n\t" \
    "s_waitcnt lgkmcnt(2)\n\t" \
    "v_mfma_f32_16x16x32_bf16 v[216:219], v[4:7], v[228:231], v[216:219]\n\t" \
    "ds_read_b128 v[240:243], v253 offset:18432\n\t" \
    "v_mfma_f32_16x16x32_bf16 v[220:223], v[8:11], v[224:227], v[220:223]\n\t" \
    "v_mfma_f32_16x16x32_bf16 v[220:223], v[12:15], v[228:231], v[220:223]\n\t" \
    "v_mfma_f32_16x16x32_bf16 v[212:215], v[244:247], v[224:227], v[212:215]\n\t" \
    "v_mfma_f32_16x16x32_bf16 v[212:215], v[248:251], v[228:231], v[212:215]\n\t" \
    "s_waitcnt lgkmcnt(2)\n\t" \
    "v_mfma_f32_16x16x32_bf16 v[204:207], v[184:187], v[232:235], v[204:207]\n\t" \
    "ds_read_b128 v[224:227], v253 offset:19456\n\t" \
    "v_mov_b64 v[244:245], 0\n\t" \
    "s_waitcnt lgkmcnt(2)\n\t" \
    "v_mfma_f32_16x16x32_bf16 v[204:207], v[188:191], v[236:239], v[204:207]\n\t" \
    "ds_read_b128 v[228:231], v253 offset:34816\n\t" \
    "v_mov_b64 v[246:247], 0\n\t" \
    "v_mfma_f32_16x16x32_bf16 v[208:211], v[192:195], v[232:235], v[208:211]\n\t" \
    "v_mov_b64 v[248:249], 0\n\t" \
    "v_mfma_f32_16x16x32_bf16 v[208:211], v[196:199], v[236:239], v[208:211]\n\t" \
    "v_mov_b64 v[250:251], 0\n\t" \
    "v_mfma_f32_16x16x32_bf16 v[200:203], v[176:179], v[232:235], v[200:203]\n\t" \
    "v_mfma_f32_16x16x32_bf16 v[200:203], v[180:183], v[236:239], v[200:203]\n\t" \
    "s_waitcnt lgkmcnt(2)\n\t" \
    "v_mfma_f32_16x16x32_bf16 v[204:207], v[24:27], v[240:243], v[204:207]\n\t" \
    "ds_read_b128 v[232:235], v253 offset:35840\n\t" \
    "s_waitcnt lgkmcnt(2)\n\t" \
    "v_mfma_f32_16x16x32_bf16 v[204:207], v[28:31], v[224:227], v[204:207]\n\t" \
    "ds_read_b128 v[236:239], v253 offset:38912\n\t" \
    "v_mfma_f32_16x16x32_bf16 v[208:211], v[32:35], v[240:243], v[208:211]\n\t" \
    "v_mfma_f32_16x16x32_bf16 v[208:211], v[36:39], v[224:227], v[208:211]\n\t" \
    "v_mfma_f32_16x16x32_bf16 v[200:203], v[16:19], v[240:243], v[200:203]\n\t" \
    "v_mfma_f32_16x16x32_bf16 v[200:203], v[20:23], v[224:227], v[200:203]\n\t" \
    "ds_read_b128 v[240:243], v253 offset:39936\n\t" \
    "s_waitcnt lgkmcnt(3)\n\t" \
    "v_mfma_f32_16x16x32_bf16 v[216:219], v[184:187], v[228:231], v[216:219]\n\t" \
    "s_waitcnt lgkmcnt(2)\n\t" \
    "v_mfma_f32_16x16x32_bf16 v[216:219], v[188:191], v[232:235], v[216:219]\n\t" \
    "v_cvt_pk_bf16_f32 v204, v204, v208\n\t" \
    "v_cvt_pk_bf16_f32 v205, v205, v209\n\t" \
    "s_waitcnt lgkmcnt(1)\n\t" \
    "v_mfma_f32_16x16x32_bf16 v[216:219], v[24:27], v[236:239], v[216:219]\n\t" \
    "v_cvt_pk_bf16_f32 v206, v206, v210\n\t" \
    "v_cvt_pk_bf16_f32 v207, v207, v211\n\t" \
    "s_waitcnt lgkmcnt(0)\n\t" \
    "v_mfma_f32_16x16x32_bf16 v[216:219], v[28:31], v[240:243], v[216:219]\n\t" \
    "ds_write_b128 v254, v[204:207] offset:5120\n\t" \
    "v_mfma_f32_16x16x32_bf16 v[220:223], v[192:195], v[228:231], v[220:223]\n\t" \
    "v_mfma_f32_16x16x32_bf16 v[220:223], v[196:199], v[232:235], v[220:223]\n\t" \
    "v_cvt_pk_bf16_f32 v200, v200, v201\n\t" \
    "v_cvt_pk_bf16_f32 v201, v202, v203\n\t" \
    "v_mfma_f32_16x16x32_bf16 v[220:223], v[32:35], v[236:239], v[220:223]\n\t" \
    "ds_write_b64 v254, v[200:201] offset:1032\n\t" \
    "v_mfma_f32_16x16x32_bf16 v[220:223], v[36:39], v[240:243], v[220:223]\n\t" \
    "v_mfma_f32_16x16x32_bf16 v[212:215], v[176:179], v[228:231], v[212:215]\n\t" \
    "ds_read_b128 v[200:203], v254 offset:55296\n\t" \
    "ds_read_b128 v[204:207], v254 offset:56320\n\t" \
    "v_mfma_f32_16x16x32_bf16 v[212:215], v[180:183], v[232:235], v[212:215]\n\t" \
    "v_mov_b64 v[208:209], 0\n\t" \
    "v_mov_b64 v[210:211], 0\n\t" \
    "v_mfma_f32_16x16x32_bf16 v[212:215], v[16:19], v[236:239], v[212:215]\n\t" \
    "v_cvt_pk_bf16_f32 v216, v216, v220\n\t" \
    "v_cvt_pk_bf16_f32 v217, v217, v221\n\t" \
    "v_mfma_f32_16x16x32_bf16 v[212:215], v[20:23], v[240:243], v[212:215]\n\t" \
    "v_cvt_pk_bf16_f32 v218, v218, v222\n\t" \
    "v_cvt_pk_bf16_f32 v219, v219, v223\n\t" \
    "s_setprio 0\n\t" \
    "ds_write_b128 v254, v[216:219] offset:11264\n\t" \
    "ds_read_b128 v[216:219], v254 offset:57344\n\t" \
    "ds_read_b128 v[220:223], v254 offset:58368\n\t" \
    "s_nop 2\n\t" \
    "v_cvt_pk_bf16_f32 v212, v212, v213\n\t" \
    "v_cvt_pk_bf16_f32 v213, v214, v215\n\t" \
    "ds_write_b64 v254, v[212:213] offset:7176\n\t" \
    "s_sleep 3\n\t" \
    "s_mov_b32 m0, 0xc000\n\t" \
    "s_nop 0\n\t" \
    "buffer_load_dwordx4 v253, s[60:63], s46 offen sc1 lds\n\t" \
    "s_mov_b32 m0, 0xc400\n\t" \
    "s_nop 0\n\t" \
    "buffer_load_dwordx4 v253, s[60:63], s47 offen sc1 lds\n\t" \
    "s_waitcnt lgkmcnt(0)\n\t" \
    "v_bfe_i32 v212, v253, 5, 1\n\t" \
    "v_and_b32 v212, 0xaaaa, v212\n\t" \
    "v_or_b32 v212, 0x4444, v212\n\t" \
    "v_and_b32 v213, 0xffff, v255\n\t" \
    "v_lshrrev_b32 v214, 16, v255\n\t" \
    ".Ls34_%=:\n\t" \
    "s_waitcnt vmcnt(0)\n\t" \
    "ds_read_b128 v[224:227], v253 offset:49152\n\t" \
    "ds_read_b128 v[228:231], v253 offset:50176\n\t" \
    "s_waitcnt lgkmcnt(1)\n\t" \
    "v_max_u32 v215, v224, v225\n\t" \
    "v_max3_u32 v215, v226, v227, v215\n\t" \
    "s_waitcnt lgkmcnt(0)\n\t" \
    "v_max3_u32 v215, v228, v229, v215\n\t" \
    "v_max3_u32 v215, v230, v231, v215\n\t" \
    "v_cmp_eq_u32 vcc, -1, v215\n\t" \
    "s_cbranch_vccz .Ls35_%=\n\t" \
    "v_max_u32 v215, v224, v225\n\t" \
    "v_max3_u32 v215, v226, v227, v215\n\t" \
    "v_cmp_eq_u32 vcc, -1, v215\n\t" \
    "s_cbranch_vccz .Ls36_%=\n\t" \
    "s_mov_b32 m0, 0xc000\n\t" \
    "s_nop 0\n\t" \
    "buffer_load_dwordx4 v253, s[60:63], s46 offen sc1 lds\n\t" \
    ".Ls36_%=:\n\t" \
    "v_max_u32 v215, v228, v229\n\t" \
    "v_max3_u32 v215, v230, v231, v215\n\t" \
    "v_cmp_eq_u32 vcc, -1, v215\n\t" \
    "s_cbranch_vccz .Ls37_%=\n\t" \
    "s_mov_b32 m0, 0xc400\n\t" \
    "s_nop 0\n\t" \
    "buffer_load_dwordx4 v253, s[60:63], s47 offen sc1 lds\n\t" \
    ".Ls37_%=:\n\t" \
    "s_cmp_eq_u32 s41, 0\n\t" \
    "s_cbranch_scc1 .Ls35_%=\n\t" \
    "s_sub_u32 s41, s41, 1\n\t" \
    "s_branch .Ls34_%=\n\t" \
    ".Ls35_%=:\n\t" \
    "s_setprio 3\n\t" \
    "s_waitcnt lgkmcnt(0)\n\t" \
    "s_barrier \n\t" \
    "ds_read_b128 v[232:235], v253 offset:40960\n\t" \
    "ds_read_b128 v[236:239], v253 offset:41984\n\t" \
    "ds_read_b128 v[240:243], v253 offset:43008\n\t" \
    "v_smfmac_f32_16x16x64_bf16 v[244:247], v[224:227], v[0:7], v212\n\t" \
    "v_smfmac_f32_16x16x64_bf16 v[248:251], v[224:227], v[8:15], v212\n\t" \
    "v_smfmac_f32_16x16x64_bf16 v[208:211], v[224:227], v[200:207], v212\n\t" \
    "ds_read_b128 v[200:203], v254 offset:59392\n\t" \
    "ds_read_b128 v[204:207], v254 offset:60416\n\t" \
    "ds_read_b128 v[224:227], v253 offset:44032\n\t" \
    "v_smfmac_f32_16x16x64_bf16 v[208:211], v[228:231], v[16:23], v212\n\t" \
    "v_smfmac_f32_16x16x64_bf16 v[244:247], v[228:231], v[24:31], v212\n\t" \
    "v_smfmac_f32_16x16x64_bf16 v[248:251], v[228:231], v[32:39], v212\n\t" \
    "ds_read_b128 v[228:231], v253 offset:45056\n\t" \
    "s_waitcnt lgkmcnt(6)\n\t" \
    "v_smfmac_f32_16x16x64_bf16 v[208:211], v[232:235], v[216:223], v212\n\t" \
    "ds_read_b128 v[216:219], v254 offset:61440\n\t" \
    "ds_read_b128 v[220:223], v254 offset:62464\n\t" \
    "v_smfmac_f32_16x16x64_bf16 v[244:247], v[232:235], v[40:47], v212\n\t" \
    "v_smfmac_f32_16x16x64_bf16 v[248:251], v[232:235], v[48:55], v212\n\t" \
    "ds_read_b128 v[232:235], v253 offset:46080\n\t" \
    "s_waitcnt lgkmcnt(8)\n\t" \
    "v_smfmac_f32_16x16x64_bf16 v[208:211], v[236:239], v[56:63], v212\n\t" \
    "v_smfmac_f32_16x16x64_bf16 v[244:247], v[236:239], v[64:71], v212\n\t" \
    "v_smfmac_f32_16x16x64_bf16 v[248:251], v[236:239], v[72:79], v212\n\t" \
    "ds_read_b128 v[236:239], v253 offset:47104\n\t" \
    "s_waitcnt lgkmcnt(6)\n\t" \
    "v_smfmac_f32_16x16x64_bf16 v[208:211], v[240:243], v[200:207], v212\n\t" \
    "ds_read_b128 v[200:203], v254 offset:63488\n\t" \
    "ds_read_b128 v[204:207], v254 offset:64512\n\t" \
    "v_smfmac_f32_16x16x64_bf16 v[244:247], v[240:243], v[80:87], v212\n\t" \
    "v_smfmac_f32_16x16x64_bf16 v[248:251], v[240:243], v[88:95], v212\n\t" \
    "ds_read_b128 v[240:243], v253 offset:48128\n\t" \
    "s_waitcnt lgkmcnt(8)\n\t" \
    "v_smfmac_f32_16x16x64_bf16 v[208:211], v[224:227], v[96:103], v212\n\t" \
    "v_smfmac_f32_16x16x64_bf16 v[244:247], v[224:227], v[104:111], v212\n\t" \
    "v_smfmac_f32_16x16x64_bf16 v[248:251], v[224:227], v[112:119], v212\n\t" \
    "s_waitcnt lgkmcnt(5)\n\t" \
    "v_smfmac_f32_16x16x64_bf16 v[208:211], v[228:231], v[216:223], v212\n\t" \
    "s_waitcnt lgkmcnt(4)\n\t" \
    "v_smfmac_f32_16x16x64_bf16 v[208:211], v[232:235], v[136:143], v212\n\t" \
    "s_waitcnt lgkmcnt(1)\n\t" \
    "v_smfmac_f32_16x16x64_bf16 v[208:211], v[236:239], v[200:207], v212\n\t" \
    "s_waitcnt lgkmcnt(0)\n\t" \
    "v_smfmac_f32_16x16x64_bf16 v[208:211], v[240:243], v[176:183], v212\n\t" \
    "v_smfmac_f32_16x16x64_bf16 v[244:247], v[228:231], v[120:127], v212\n\t" \
    "v_smfmac_f32_16x16x64_bf16 v[244:247], v[232:235], v[144:151], v212\n\t" \
    "v_smfmac_f32_16x16x64_bf16 v[244:247], v[236:239], v[160:167], v212\n\t" \
    "v_add_f32 v208, v208, v210\n\t" \
    "v_add_f32 v209, v209, v211\n\t" \
    "v_cvt_pk_bf16_f32 v208, v208, v209\n\t" \
    "v_smfmac_f32_16x16x64_bf16 v[244:247], v[240:243], v[184:191], v212\n\t" \
    "ds_write_b16 v213, v208 offset:0\n\t" \
    "ds_write_b16_d16_hi v213, v208 offset:16\n\t" \
    "v_smfmac_f32_16x16x64_bf16 v[248:251], v[228:231], v[128:135], v212\n\t" \
    "v_smfmac_f32_16x16x64_bf16 v[248:251], v[232:235], v[152:159], v212\n\t" \
    "v_smfmac_f32_16x16x64_bf16 v[248:251], v[236:239], v[168:175], v212\n\t" \
    "v_add_f32 v244, v244, v246\n\t" \
    "v_add_f32 v245, v245, v247\n\t" \
    "v_smfmac_f32_16x16x64_bf16 v[248:251], v[240:243], v[192:199], v212\n\t" \
    "s_nop 7\n\t" \
    "v_add_f32 v248, v248, v250\n\t" \
    "v_add_f32 v249, v249, v251\n\t" \
    "v_cvt_pk_bf16_f32 v244, v244, v248\n\t" \
    "v_cvt_pk_bf16_f32 v245, v245, v249\n\t" \
    "ds_write2_b32 v214, v244, v245 offset1:4\n\t" \
    "s_setprio 0\n\t" \
    "s_sleep 1\n\t" \
    "ds_read_b128 v[208:211], v254 offset:24576\n\t" \
    "ds_read_b128 v[224:227], v253 offset:0\n\t" \
    "ds_read_b128 v[228:231], v253 offset:20480\n\t" \
    "ds_read_b128 v[212:215], v254 offset:25600\n\t" \
    "ds_read_b128 v[232:235], v253 offset:1024\n\t" \
    "ds_read_b128 v[236:239], v253 offset:21504\n\t" \
    "ds_read_b128 v[216:219], v254 offset:26624\n\t" \
    "ds_read_b128 v[240:243], v253 offset:2048\n\t" \
    "ds_read_b128 v[244:247], v253 offset:22528\n\t" \
    "ds_read_b128 v[220:223], v254 offset:27648\n\t" \
    "ds_read_b128 v[248:251], v253 offset:3072\n\t" \
    "s_waitcnt lgkmcnt(9)\n\t" \
    "v_mfma_f32_16x16x32_bf16 v[200:203], v[208:211], v[224:227], 0\n\t" \
    "ds_read_b128 v[224:227], v253 offset:23552\n\t" \
    "s_waitcnt lgkmcnt(9)\n\t" \
    "v_mfma_f32_16x16x32_bf16 v[204:207], v[208:211], v[228:231], 0\n\t" \
    "ds_read_b128 v[228:231], v253 offset:4096\n\t" \
    "ds_read_b128 v[208:211], v254 offset:28672\n\t" \
    "s_waitcnt lgkmcnt(9)\n\t" \
    "v_mfma_f32_16x16x32_bf16 v[200:203], v[212:215], v[232:235], v[200:203]\n\t" \
    "ds_read_b128 v[232:235], v253 offset:24576\n\t" \
    "s_waitcnt lgkmcnt(9)\n\t" \
    "v_mfma_f32_16x16x32_bf16 v[204:207], v[212:215], v[236:239], v[204:207]\n\t" \
    "ds_read_b128 v[236:239], v253 offset:5120\n\t" \
    "ds_read_b128 v[212:215], v254 offset:29696\n\t" \
    "s_waitcnt lgkmcnt(0)\n\t" \
    "s_barrier \n\t" \
    "s_add_u32 s40, s40, 1\n\t" \
    "s_cmp_lg_u32 s40, 64\n\t" \
    "s_cbranch_scc1 .Ls33_%=\n\t" \
    "s_branch .Lend%=\n\t" \
    ".Ls2_%=:\n\t" \
    "v_add_u32 v244, 0x18000, v243\n\t" \
    "s_waitcnt vmcnt(12) lgkmcnt(0)\n\t" \
    "s_barrier \n\t" \
    "s_add_u32 m0, %[wm], 0x12000\n\t" \
    "s_mov_b32 s44, 0x1e0000\n\t" \
    "buffer_load_dwordx4 v240, %[wr], s44 offen offset:0 nt lds\n\t" \
    "buffer_load_dwordx4 v241, %[wr], s44 offen offset:1024 nt lds\n\t" \
    "buffer_load_dwordx4 v242, %[wr], s44 offen offset:2048 nt lds\n\t" \
    "ds_read_b64_tr_b16 v[208:209], v244 offset:0\n\t" \
    "ds_read_b64_tr_b16 v[210:211], v244 offset:1536\n\t" \
    "ds_read_b64_tr_b16 v[40:41], v244 offset:32\n\t" \
    "ds_read_b64_tr_b16 v[42:43], v244 offset:1568\n\t" \
    "ds_read_b64_tr_b16 v[48:49], v244 offset:64\n\t" \
    "ds_read_b64_tr_b16 v[50:51], v244 offset:1600\n\t" \
    "v_add_u32 v244, 0x1e000, v243\n\t" \
    "s_waitcnt vmcnt(12) lgkmcnt(0)\n\t" \
    "s_barrier \n\t" \
    "s_add_u32 m0, %[wm], 0x18000\n\t" \
    "s_mov_b32 s44, 0x240000\n\t" \
    "buffer_load_dwordx4 v240, %[wr], s44 offen offset:0 nt lds\n\t" \
    "buffer_load_dwordx4 v241, %[wr], s44 offen offset:1024 nt lds\n\t" \
    "buffer_load_dwordx4 v242, %[wr], s44 offen offset:2048 nt lds\n\t" \
    "ds_read_b64_tr_b16 v[212:213], v244 offset:0\n\t" \
    "ds_read_b64_tr_b16 v[214:215], v244 offset:1536\n\t" \
    "ds_read_b64_tr_b16 v[44:45], v244 offset:32\n\t" \
    "ds_read_b64_tr_b16 v[46:47], v244 offset:1568\n\t" \
    "ds_read_b64_tr_b16 v[52:53], v244 offset:64\n\t" \
    "ds_read_b64_tr_b16 v[54:55], v244 offset:1600\n\t" \
    "v_add_u32 v244, 0x0, v243\n\t" \
    "s_waitcnt vmcnt(12) lgkmcnt(0)\n\t" \
    "s_barrier \n\t" \
    "s_add_u32 m0, %[wm], 0x1e000\n\t" \
    "s_mov_b32 s44, 0x2a0000\n\t" \
    "buffer_load_dwordx4 v240, %[wr], s44 offen offset:0 nt lds\n\t" \
    "buffer_load_dwordx4 v241, %[wr], s44 offen offset:1024 nt lds\n\t" \
    "buffer_load_dwordx4 v242, %[wr], s44 offen offset:2048 nt lds\n\t" \
    "ds_read_b64_tr_b16 v[56:57], v244 offset:0\n\t" \
    "ds_read_b64_tr_b16 v[58:59], v244 offset:1536\n\t" \
    "ds_read_b64_tr_b16 v[64:65], v244 offset:32\n\t" \
    "ds_read_b64_tr_b16 v[66:67], v244 offset:1568\n\t" \
    "ds_read_b64_tr_b16 v[72:73], v244 offset:64\n\t" \
    "ds_read_b64_tr_b16 v[74:75], v244 offset:1600\n\t" \
    "v_add_u32 v244, 0x6000, v243\n\t" \
    "s_waitcnt vmcnt(12) lgkmcnt(0)\n\t" \
    "s_barrier \n\t" \
    "s_add_u32 m0, %[wm], 0x0\n\t" \
    "s_mov_b32 s44, 0x300000\n\t" \
    "buffer_load_dwordx4 v240, %[wr], s44 offen offset:0 nt lds\n\t" \
    "buffer_load_dwordx4 v241, %[wr], s44 offen offset:1024 nt lds\n\t" \
    "buffer_load_dwordx4 v242, %[wr], s44 offen offset:2048 nt lds\n\t" \
    "ds_read_b64_tr_b16 v[60:61], v244 offset:0\n\t" \
    "ds_read_b64_tr_b16 v[62:63], v244 offset:1536\n\t" \
    "ds_read_b64_tr_b16 v[68:69], v244 offset:32\n\t" \
    "ds_read_b64_tr_b16 v[70:71], v244 offset:1568\n\t" \
    "ds_read_b64_tr_b16 v[76:77], v244 offset:64\n\t" \
    "ds_read_b64_tr_b16 v[78:79], v244 offset:1600\n\t" \
    "v_add_u32 v244, 0xc000, v243\n\t" \
    "s_waitcnt vmcnt(12) lgkmcnt(0)\n\t" \
    "s_barrier \n\t" \
    "s_add_u32 m0, %[wm], 0x6000\n\t" \
    "s_mov_b32 s44, 0x360000\n\t" \
    "buffer_load_dwordx4 v240, %[wr], s44 offen offset:0 nt lds\n\t" \
    "buffer_load_dwordx4 v241, %[wr], s44 offen offset:1024 nt lds\n\t" \
    "buffer_load_dwordx4 v242, %[wr], s44 offen offset:2048 nt lds\n\t" \
    "ds_read_b64_tr_b16 v[216:217], v244 offset:0\n\t" \
    "ds_read_b64_tr_b16 v[218:219], v244 offset:1536\n\t" \
    "ds_read_b64_tr_b16 v[80:81], v244 offset:32\n\t" \
    "ds_read_b64_tr_b16 v[82:83], v244 offset:1568\n\t" \
    "ds_read_b64_tr_b16 v[88:89], v244 offset:64\n\t" \
    "ds_read_b64_tr_b16 v[90:91], v244 offset:1600\n\t" \
    "v_add_u32 v244, 0x12000, v243\n\t" \
    "s_waitcnt vmcnt(12) lgkmcnt(0)\n\t" \
    "s_barrier \n\t" \
    "s_add_u32 m0, %[wm], 0xc000\n\t" \
    "s_mov_b32 s44, 0x3c0000\n\t" \
    "buffer_load_dwordx4 v240, %[wr], s44 offen offset:0 nt lds\n\t" \
    "buffer_load_dwordx4 v241, %[wr], s44 offen offset:1024 nt lds\n\t" \
    "buffer_load_dwordx4 v242, %[wr], s44 offen offset:2048 nt lds\n\t" \
    "ds_read_b64_tr_b16 v[220:221], v244 offset:0\n\t" \
    "ds_read_b64_tr_b16 v[222:223], v244 offset:1536\n\t" \
    "ds_read_b64_tr_b16 v[84:85], v244 offset:32\n\t" \
    "ds_read_b64_tr_b16 v[86:87], v244 offset:1568\n\t" \
    "ds_read_b64_tr_b16 v[92:93], v244 offset:64\n\t" \
    "ds_read_b64_tr_b16 v[94:95], v244 offset:1600\n\t" \
    "v_add_u32 v244, 0x18000, v243\n\t" \
    "s_waitcnt vmcnt(12) lgkmcnt(0)\n\t" \
    "s_barrier \n\t" \
    "s_add_u32 m0, %[wm], 0x12000\n\t" \
    "s_mov_b32 s44, 0x420000\n\t" \
    "buffer_load_dwordx4 v240, %[wr], s44 offen offset:0 nt lds\n\t" \
    "buffer_load_dwordx4 v241, %[wr], s44 offen offset:1024 nt lds\n\t" \
    "buffer_load_dwordx4 v242, %[wr], s44 offen offset:2048 nt lds\n\t" \
    "ds_read_b64_tr_b16 v[96:97], v244 offset:0\n\t" \
    "ds_read_b64_tr_b16 v[98:99], v244 offset:1536\n\t" \
    "ds_read_b64_tr_b16 v[104:105], v244 offset:32\n\t" \
    "ds_read_b64_tr_b16 v[106:107], v244 offset:1568\n\t" \
    "ds_read_b64_tr_b16 v[112:113], v244 offset:64\n\t" \
    "ds_read_b64_tr_b16 v[114:115], v244 offset:1600\n\t" \
    "v_add_u32 v244, 0x1e000, v243\n\t" \
    "s_waitcnt vmcnt(12) lgkmcnt(0)\n\t" \
    "s_barrier \n\t" \
    "s_add_u32 m0, %[wm], 0x18000\n\t" \
    "s_mov_b32 s44, 0x480000\n\t" \
    "buffer_load_dwordx4 v240, %[wr], s44 offen offset:0 nt lds\n\t" \
    "buffer_load_dwordx4 v241, %[wr], s44 offen offset:1024 nt lds\n\t" \
    "buffer_load_dwordx4 v242, %[wr], s44 offen offset:2048 nt lds\n\t" \
    "ds_read_b64_tr_b16 v[100:101], v244 offset:0\n\t" \
    "ds_read_b64_tr_b16 v[102:103], v244 offset:1536\n\t" \
    "ds_read_b64_tr_b16 v[108:109], v244 offset:32\n\t" \
    "ds_read_b64_tr_b16 v[110:111], v244 offset:1568\n\t" \
    "ds_read_b64_tr_b16 v[116:117], v244 offset:64\n\t" \
    "ds_read_b64_tr_b16 v[118:119], v244 offset:1600\n\t" \
    "v_add_u32 v244, 0x0, v243\n\t" \
    "s_waitcnt vmcnt(12) lgkmcnt(0)\n\t" \
    "s_barrier \n\t" \
    "v_mov_b32 v246, 0\n\t" \
    "buffer_load_dword v246, v246, s[60:63], %[mo] offen sc1\n\t" \
    "s_add_u32 m0, %[wm], 0x1e000\n\t" \
    "s_mov_b32 s44, 0x4e0000\n\t" \
    "buffer_load_dwordx4 v240, %[wr], s44 offen offset:0 nt lds\n\t" \
    "buffer_load_dwordx4 v241, %[wr], s44 offen offset:1024 nt lds\n\t" \
    "buffer_load_dwordx4 v242, %[wr], s44 offen offset:2048 nt lds\n\t" \
    "ds_read_b64_tr_b16 v[200:201], v244 offset:0\n\t" \
    "ds_read_b64_tr_b16 v[202:203], v244 offset:1536\n\t" \
    "ds_read_b64_tr_b16 v[0:1], v244 offset:32\n\t" \
    "ds_read_b64_tr_b16 v[2:3], v244 offset:1568\n\t" \
    "ds_read_b64_tr_b16 v[8:9], v244 offset:64\n\t" \
    "ds_read_b64_tr_b16 v[10:11], v244 offset:1600\n\t" \
    "v_add_u32 v244, 0x6000, v243\n\t" \
    "s_waitcnt vmcnt(12) lgkmcnt(0)\n\t" \
    "s_barrier \n\t" \
    "s_add_u32 m0, %[wm], 0x0\n\t" \
    "s_mov_b32 s44, 0x540000\n\t" \
    "buffer_load_dwordx4 v240, %[wr], s44 offen offset:0 nt lds\n\t" \
    "buffer_load_dwordx4 v241, %[wr], s44 offen offset:1024 nt lds\n\t" \
    "buffer_load_dwordx4 v242, %[wr], s44 offen offset:2048 nt lds\n\t" \
    "ds_read_b64_tr_b16 v[204:205], v244 offset:0\n\t" \
    "ds_read_b64_tr_b16 v[206:207], v244 offset:1536\n\t" \
    "ds_read_b64_tr_b16 v[4:5], v244 offset:32\n\t" \
    "ds_read_b64_tr_b16 v[6:7], v244 offset:1568\n\t" \
    "ds_read_b64_tr_b16 v[12:13], v244 offset:64\n\t" \
    "ds_read_b64_tr_b16 v[14:15], v244 offset:1600\n\t" \
    "v_add_u32 v244, 0xc000, v243\n\t" \
    "s_waitcnt vmcnt(12) lgkmcnt(0)\n\t" \
    "s_barrier \n\t" \
    "s_add_u32 m0, %[wm], 0x6000\n\t" \
    "s_mov_b32 s44, 0x5a0000\n\t" \
    "buffer_load_dwordx4 v240, %[wr], s44 offen offset:0 nt lds\n\t" \
    "buffer_load_dwordx4 v241, %[wr], s44 offen offset:1024 nt lds\n\t" \
    "buffer_load_dwordx4 v242, %[wr], s44 offen offset:2048 nt lds\n\t" \
    "ds_read_b64_tr_b16 v[224:225], v244 offset:0\n\t" \
    "ds_read_b64_tr_b16 v[226:227], v244 offset:1536\n\t" \
    "ds_read_b64_tr_b16 v[120:121], v244 offset:32\n\t" \
    "ds_read_b64_tr_b16 v[122:123], v244 offset:1568\n\t" \
    "ds_read_b64_tr_b16 v[128:129], v244 offset:64\n\t" \
    "ds_read_b64_tr_b16 v[130:131], v244 offset:1600\n\t" \
    "v_add_u32 v244, 0x12000, v243\n\t" \
    "s_waitcnt vmcnt(12) lgkmcnt(0)\n\t" \
    "s_barrier \n\t" \
    "s_add_u32 m0, %[wm], 0xc000\n\t" \
    "s_mov_b32 s44, 0x600000\n\t" \
    "buffer_load_dwordx4 v240, %[wr], s44 offen offset:0 nt lds\n\t" \
    "buffer_load_dwordx4 v241, %[wr], s44 offen offset:1024 nt lds\n\t" \
    "buffer_load_dwordx4 v242, %[wr], s44 offen offset:2048 nt lds\n\t" \
    "ds_read_b64_tr_b16 v[228:229], v244 offset:0\n\t" \
    "ds_read_b64_tr_b16 v[230:231], v244 offset:1536\n\t" \
    "ds_read_b64_tr_b16 v[124:125], v244 offset:32\n\t" \
    "ds_read_b64_tr_b16 v[126:127], v244 offset:1568\n\t" \
    "ds_read_b64_tr_b16 v[132:133], v244 offset:64\n\t" \
    "ds_read_b64_tr_b16 v[134:135], v244 offset:1600\n\t" \
    "v_add_u32 v244, 0x18000, v243\n\t" \
    "s_waitcnt vmcnt(12) lgkmcnt(0)\n\t" \
    "s_barrier \n\t" \
    "s_add_u32 m0, %[wm], 0x12000\n\t" \
    "s_mov_b32 s44, 0x660000\n\t" \
    "buffer_load_dwordx4 v240, %[wr], s44 offen offset:0 nt lds\n\t" \
    "buffer_load_dwordx4 v241, %[wr], s44 offen offset:1024 nt lds\n\t" \
    "buffer_load_dwordx4 v242, %[wr], s44 offen offset:2048 nt lds\n\t" \
    "ds_read_b64_tr_b16 v[136:137], v244 offset:0\n\t" \
    "ds_read_b64_tr_b16 v[138:139], v244 offset:1536\n\t" \
    "ds_read_b64_tr_b16 v[144:145], v244 offset:32\n\t" \
    "ds_read_b64_tr_b16 v[146:147], v244 offset:1568\n\t" \
    "ds_read_b64_tr_b16 v[152:153], v244 offset:64\n\t" \
    "ds_read_b64_tr_b16 v[154:155], v244 offset:1600\n\t" \
    "v_add_u32 v244, 0x1e000, v243\n\t" \
    "s_waitcnt vmcnt(12) lgkmcnt(0)\n\t" \
    "s_barrier \n\t" \
    ".Ls21_%=:\n\t" \
    "v_readfirstlane_b32 s43, v246\n\t" \
    "s_nop 0\n\t" \
    "s_cmp_lg_u32 s43, -1\n\t" \
    "s_cbranch_scc1 .Ls22_%=\n\t" \
    "s_cmp_eq_u32 s41, 0\n\t" \
    "s_cbranch_scc1 .Ls22_%=\n\t" \
    "s_sub_u32 s41, s41, 1\n\t" \
    "s_sleep 4\n\t" \
    "v_mov_b32 v246, 0\n\t" \
    "buffer_load_dword v246, v246, s[60:63], %[mo] offen sc1\n\t" \
    "s_waitcnt vmcnt(0)\n\t" \
    "s_branch .Ls21_%=\n\t" \
    ".Ls22_%=:\n\t" \
    "v_mov_b32 v246, 0x8000\n\t" \
    "s_mov_b64 exec, 1\n\t" \
    "buffer_store_dword v246, v246, s[60:63], %[mo] offen sc1\n\t" \
    "s_mov_b64 exec, -1\n\t" \
    "s_and_b32 s44, s43, 0x5000\n\t" \
    "s_xor_b32 %[mb], %[mb], s44\n\t" \
    "s_xor_b32 %[fl], %[fl], s44\n\t" \
    "s_and_b32 s43, s43, 0x2000\n\t" \
    "s_xor_b32 s43, s43, %[lx]\n\t" \
    "s_add_u32 s60, s60, s43\n\t" \
    "s_addc_u32 s61, s61, 0\n\t" \
    "s_add_u32 m0, %[wm], 0x18000\n\t" \
    "s_mov_b32 s44, 0x6c0000\n\t" \
    "buffer_load_dwordx4 v240, %[wr], s44 offen offset:0 nt lds\n\t" \
    "buffer_load_dwordx4 v241, %[wr], s44 offen offset:1024 nt lds\n\t" \
    "buffer_load_dwordx4 v242, %[wr], s44 offen offset:2048 nt lds\n\t" \
    "ds_read_b64_tr_b16 v[140:141], v244 offset:0\n\t" \
    "ds_read_b64_tr_b16 v[142:143], v244 offset:1536\n\t" \
    "ds_read_b64_tr_b16 v[148:149], v244 offset:32\n\t" \
    "ds_read_b64_tr_b16 v[150:151], v244 offset:1568\n\t" \
    "ds_read_b64_tr_b16 v[156:157], v244 offset:64\n\t" \
    "ds_read_b64_tr_b16 v[158:159], v244 offset:1600\n\t" \
    "v_add_u32 v244, 0x0, v243\n\t" \
    "s_waitcnt vmcnt(12) lgkmcnt(0)\n\t" \
    "s_barrier \n\t" \
    "s_add_u32 m0, %[wm], 0x1e000\n\t" \
    "s_mov_b32 s44, 0x720000\n\t" \
    "buffer_load_dwordx4 v240, %[wr], s44 offen offset:0 nt lds\n\t" \
    "buffer_load_dwordx4 v241, %[wr], s44 offen offset:1024 nt lds\n\t" \
    "buffer_load_dwordx4 v242, %[wr], s44 offen offset:2048 nt lds\n\t" \
    "ds_read_b64_tr_b16 v[16:17], v244 offset:0\n\t" \
    "ds_read_b64_tr_b16 v[18:19], v244 offset:1536\n\t" \
    "ds_read_b64_tr_b16 v[24:25], v244 offset:32\n\t" \
    "ds_read_b64_tr_b16 v[26:27], v244 offset:1568\n\t" \
    "ds_read_b64_tr_b16 v[32:33], v244 offset:64\n\t" \
    "ds_read_b64_tr_b16 v[34:35], v244 offset:1600\n\t" \
    "v_add_u32 v244, 0x6000, v243\n\t" \
    "s_waitcnt vmcnt(12) lgkmcnt(0)\n\t" \
    "s_barrier \n\t" \
    "ds_read_b64_tr_b16 v[20:21], v244 offset:0\n\t" \
    "ds_read_b64_tr_b16 v[22:23], v244 offset:1536\n\t" \
    "ds_read_b64_tr_b16 v[28:29], v244 offset:32\n\t" \
    "ds_read_b64_tr_b16 v[30:31], v244 offset:1568\n\t" \
    "ds_read_b64_tr_b16 v[36:37], v244 offset:64\n\t" \
    "ds_read_b64_tr_b16 v[38:39], v244 offset:1600\n\t" \
    "v_add_u32 v244, 0xc000, v243\n\t" \
    "s_waitcnt vmcnt(9) lgkmcnt(0)\n\t" \
    "s_barrier \n\t" \
    "ds_read_b64_tr_b16 v[232:233], v244 offset:0\n\t" \
    "ds_read_b64_tr_b16 v[234:235], v244 offset:1536\n\t" \
    "ds_read_b64_tr_b16 v[160:161], v244 offset:32\n\t" \
    "ds_read_b64_tr_b16 v[162:163], v244 offset:1568\n\t" \
    "ds_read_b64_tr_b16 v[168:169], v244 offset:64\n\t" \
    "ds_read_b64_tr_b16 v[170:171], v244 offset:1600\n\t" \
    "v_add_u32 v244, 0x12000, v243\n\t" \
    "s_waitcnt vmcnt(6) lgkmcnt(0)\n\t" \
    "s_barrier \n\t" \
    "ds_read_b64_tr_b16 v[236:237], v244 offset:0\n\t" \
    "ds_read_b64_tr_b16 v[238:239], v244 offset:1536\n\t" \
    "ds_read_b64_tr_b16 v[164:165], v244 offset:32\n\t" \
    "ds_read_b64_tr_b16 v[166:167], v244 offset:1568\n\t" \
    "ds_read_b64_tr_b16 v[172:173], v244 offset:64\n\t" \
    "ds_read_b64_tr_b16 v[174:175], v244 offset:1600\n\t" \
    "v_add_u32 v244, 0x18000, v243\n\t" \
    "s_waitcnt vmcnt(3) lgkmcnt(0)\n\t" \
    "s_barrier \n\t" \
    "ds_read_b64_tr_b16 v[176:177], v244 offset:0\n\t" \
    "ds_read_b64_tr_b16 v[178:179], v244 offset:1536\n\t" \
    "ds_read_b64_tr_b16 v[184:185], v244 offset:32\n\t" \
    "ds_read_b64_tr_b16 v[186:187], v244 offset:1568\n\t" \
    "ds_read_b64_tr_b16 v[192:193], v244 offset:64\n\t" \
    "ds_read_b64_tr_b16 v[194:195], v244 offset:1600\n\t" \
    "v_add_u32 v244, 0x1e000, v243\n\t" \
    "s_waitcnt vmcnt(0) lgkmcnt(0)\n\t" \
    "s_barrier \n\t" \
    "ds_read_b64_tr_b16 v[180:181], v244 offset:0\n\t" \
    "ds_read_b64_tr_b16 v[182:183], v244 offset:1536\n\t" \
    "ds_read_b64_tr_b16 v[188:189], v244 offset:32\n\t" \
    "ds_read_b64_tr_b16 v[190:191], v244 offset:1568\n\t" \
    "ds_read_b64_tr_b16 v[196:197], v244 offset:64\n\t" \
    "ds_read_b64_tr_b16 v[198:199], v244 offset:1600\n\t" \
    "s_waitcnt lgkmcnt(0)\n\t" \
    "s_barrier \n\t" \
    "ds_write_b128 v254, v[200:203] offset:51200\n\t" \
    "ds_write_b128 v254, v[204:207] offset:52224\n\t" \
    "ds_write_b128 v254, v[208:211] offset:53248\n\t" \
    "ds_write_b128 v254, v[212:215] offset:54272\n\t" \
    "ds_write_b128 v254, v[216:219] offset:55296\n\t" \
    "ds_write_b128 v254, v[220:223] offset:56320\n\t" \
    "ds_write_b128 v254, v[224:227] offset:57344\n\t" \
    "ds_write_b128 v254, v[228:231] offset:58368\n\t" \
    "ds_write_b128 v254, v[232:235] offset:59392\n\t" \
    "ds_write_b128 v254, v[236:239] offset:60416\n\t" \
    "s_waitcnt vmcnt(0) lgkmcnt(0)\n\t" \
    "s_barrier \n\t" \
    "ds_read_b128 v[224:227], v253 offset:0\n\t" \
    "ds_read_b128 v[244:247], v254 offset:53248\n\t" \
    "ds_read_b128 v[228:231], v253 offset:1024\n\t" \
    "ds_read_b128 v[232:235], v253 offset:20480\n\t" \
    "ds_read_b128 v[236:239], v253 offset:21504\n\t" \
    "ds_read_b128 v[248:251], v254 offset:54272\n\t" \
    ".Ls23_%=:\n\t" \
    "s_waitcnt lgkmcnt(5)\n\t" \
    "v_mfma_f32_16x16x32_bf16 v[204:207], v[40:43], v[224:227], 0\n\t" \
    "s_waitcnt lgkmcnt(3)\n\t" \
    "v_mfma_f32_16x16x32_bf16 v[204:207], v[44:47], v[228:231], v[204:207]\n\t" \
    "ds_read_b128 v[240:243], v253 offset:2048\n\t" \
    "v_mfma_f32_16x16x32_bf16 v[208:211], v[48:51], v[224:227], 0\n\t" \
    "v_mfma_f32_16x16x32_bf16 v[208:211], v[52:55], v[228:231], v[208:211]\n\t" \
    "v_mfma_f32_16x16x32_bf16 v[200:203], v[244:247], v[224:227], 0\n\t" \
    "s_waitcnt lgkmcnt(1)\n\t" \
    "v_mfma_f32_16x16x32_bf16 v[200:203], v[248:251], v[228:231], v[200:203]\n\t" \
    "v_mfma_f32_16x16x32_bf16 v[216:219], v[40:43], v[232:235], 0\n\t" \
    "ds_read_b128 v[224:227], v253 offset:3072\n\t" \
    "v_mfma_f32_16x16x32_bf16 v[216:219], v[44:47], v[236:239], v[216:219]\n\t" \
    "ds_read_b128 v[228:231], v253 offset:22528\n\t" \
    "v_mfma_f32_16x16x32_bf16 v[220:223], v[48:51], v[232:235], 0\n\t" \
    "v_mfma_f32_16x16x32_bf16 v[220:223], v[52:55], v[236:239], v[220:223]\n\t" \
    "v_mfma_f32_16x16x32_bf16 v[212:215], v[244:247], v[232:235], 0\n\t" \
    "ds_read_b128 v[244:247], v254 offset:55296\n\t" \
    "v_mfma_f32_16x16x32_bf16 v[212:215], v[248:251], v[236:239], v[212:215]\n\t" \
    "ds_read_b128 v[248:251], v254 offset:56320\n\t" \
    "s_waitcnt lgkmcnt(4)\n\t" \
    "v_mfma_f32_16x16x32_bf16 v[204:207], v[64:67], v[240:243], v[204:207]\n\t" \
    "ds_read_b128 v[232:235], v253 offset:23552\n\t" \
    "s_waitcnt lgkmcnt(4)\n\t" \
    "v_mfma_f32_16x16x32_bf16 v[204:207], v[68:71], v[224:227], v[204:207]\n\t" \
    "ds_read_b128 v[236:239], v253 offset:4096\n\t" \
    "v_mfma_f32_16x16x32_bf16 v[208:211], v[72:75], v[240:243], v[208:211]\n\t" \
    "v_mfma_f32_16x16x32_bf16 v[208:211], v[76:79], v[224:227], v[208:211]\n\t" \
    "v_mfma_f32_16x16x32_bf16 v[200:203], v[56:59], v[240:243], v[200:203]\n\t" \
    "v_mfma_f32_16x16x32_bf16 v[200:203], v[60:63], v[224:227], v[200:203]\n\t" \
    "s_waitcnt lgkmcnt(4)\n\t" \
    "v_mfma_f32_16x16x32_bf16 v[216:219], v[64:67], v[228:231], v[216:219]\n\t" \
    "ds_read_b128 v[240:243], v253 offset:5120\n\t" \
    "s_waitcnt lgkmcnt(2)\n\t" \
    "v_mfma_f32_16x16x32_bf16 v[216:219], v[68:71], v[232:235], v[216:219]\n\t" \
    "ds_read_b128 v[224:227], v253 offset:24576\n\t" \
    "v_mfma_f32_16x16x32_bf16 v[220:223], v[72:75], v[228:231], v[220:223]\n\t" \
    "v_mfma_f32_16x16x32_bf16 v[220:223], v[76:79], v[232:235], v[220:223]\n\t" \
    "v_mfma_f32_16x16x32_bf16 v[212:215], v[56:59], v[228:231], v[212:215]\n\t" \
    "v_mfma_f32_16x16x32_bf16 v[212:215], v[60:63], v[232:235], v[212:215]\n\t" \
    "s_waitcnt lgkmcnt(2)\n\t" \
    "v_mfma_f32_16x16x32_bf16 v[204:207], v[80:83], v[236:239], v[204:207]\n\t" \
    "ds_read_b128 v[228:231], v253 offset:25600\n\t" \
    "s_waitcnt lgkmcnt(2)\n\t" \
    "v_mfma_f32_16x16x32_bf16 v[204:207], v[84:87], v[240:243], v[204:207]\n\t" \
    "ds_read_b128 v[232:235], v253 offset:6144\n\t" \
    "v_mfma_f32_16x16x32_bf16 v[208:211], v[88:91], v[236:239], v[208:211]\n\t" \
    "v_mfma_f32_16x16x32_bf16 v[208:211], v[92:95], v[240:243], v[208:211]\n\t" \
    "v_mfma_f32_16x16x32_bf16 v[200:203], v[244:247], v[236:239], v[200:203]\n\t" \
    "v_mfma_f32_16x16x32_bf16 v[200:203], v[248:251], v[240:243], v[200:203]\n\t" \
    "s_waitcnt lgkmcnt(2)\n\t" \
    "v_mfma_f32_16x16x32_bf16 v[216:219], v[80:83], v[224:227], v[216:219]\n\t" \
    "ds_read_b128 v[236:239], v253 offset:7168\n\t" \
    "s_waitcnt lgkmcnt(2)\n\t" \
    "v_mfma_f32_16x16x32_bf16 v[216:219], v[84:87], v[228:231], v[216:219]\n\t" \
    "ds_read_b128 v[240:243], v253 offset:26624\n\t" \
    "v_mfma_f32_16x16x32_bf16 v[220:223], v[88:91], v[224:227], v[220:223]\n\t" \
    "v_mfma_f32_16x16x32_bf16 v[220:223], v[92:95], v[228:231], v[220:223]\n\t" \
    "v_mfma_f32_16x16x32_bf16 v[212:215], v[244:247], v[224:227], v[212:215]\n\t" \
    "ds_read_b128 v[244:247], v254 offset:51200\n\t" \
    "v_mfma_f32_16x16x32_bf16 v[212:215], v[248:251], v[228:231], v[212:215]\n\t" \
    "ds_read_b128 v[248:251], v254 offset:52224\n\t" \
    "s_waitcnt lgkmcnt(4)\n\t" \
    "v_mfma_f32_16x16x32_bf16 v[204:207], v[104:107], v[232:235], v[204:207]\n\t" \
    "ds_read_b128 v[224:227], v253 offset:27648\n\t" \
    "s_waitcnt lgkmcnt(4)\n\t" \
    "v_mfma_f32_16x16x32_bf16 v[204:207], v[108:111], v[236:239], v[204:207]\n\t" \
    "ds_read_b128 v[228:231], v253 offset:8192\n\t" \
    "v_mfma_f32_16x16x32_bf16 v[208:211], v[112:115], v[232:235], v[208:211]\n\t" \
    "v_mfma_f32_16x16x32_bf16 v[208:211], v[116:119], v[236:239], v[208:211]\n\t" \
    "v_mfma_f32_16x16x32_bf16 v[200:203], v[96:99], v[232:235], v[200:203]\n\t" \
    "v_mfma_f32_16x16x32_bf16 v[200:203], v[100:103], v[236:239], v[200:203]\n\t" \
    "s_waitcnt lgkmcnt(4)\n\t" \
    "v_mfma_f32_16x16x32_bf16 v[216:219], v[104:107], v[240:243], v[216:219]\n\t" \
    "ds_read_b128 v[232:235], v253 offset:9216\n\t" \
    "s_waitcnt lgkmcnt(2)\n\t" \
    "v_mfma_f32_16x16x32_bf16 v[216:219], v[108:111], v[224:227], v[216:219]\n\t" \
    "ds_read_b128 v[236:239], v253 offset:28672\n\t" \
    "v_mfma_f32_16x16x32_bf16 v[220:223], v[112:115], v[240:243], v[220:223]\n\t" \
    "v_mfma_f32_16x16x32_bf16 v[220:223], v[116:119], v[224:227], v[220:223]\n\t" \
    "v_mfma_f32_16x16x32_bf16 v[212:215], v[96:99], v[240:243], v[212:215]\n\t" \
    "v_mfma_f32_16x16x32_bf16 v[212:215], v[100:103], v[224:227], v[212:215]\n\t" \
    "s_waitcnt lgkmcnt(2)\n\t" \
    "v_mfma_f32_16x16x32_bf16 v[204:207], v[0:3], v[228:231], v[204:207]\n\t" \
    "ds_read_b128 v[240:243], v253 offset:29696\n\t" \
    "s_waitcnt lgkmcnt(2)\n\t" \
    "v_mfma_f32_16x16x32_bf16 v[204:207], v[4:7], v[232:235], v[204:207]\n\t" \
    "ds_read_b128 v[224:227], v253 offset:10240\n\t" \
    "v_mfma_f32_16x16x32_bf16 v[208:211], v[8:11], v[228:231], v[208:211]\n\t" \
    "v_mfma_f32_16x16x32_bf16 v[208:211], v[12:15], v[232:235], v[208:211]\n\t" \
    "v_mfma_f32_16x16x32_bf16 v[200:203], v[244:247], v[228:231], v[200:203]\n\t" \
    "v_mfma_f32_16x16x32_bf16 v[200:203], v[248:251], v[232:235], v[200:203]\n\t" \
    "s_waitcnt lgkmcnt(2)\n\t" \
    "v_mfma_f32_16x16x32_bf16 v[216:219], v[0:3], v[236:239], v[216:219]\n\t" \
    "ds_read_b128 v[228:231], v253 offset:11264\n\t" \
    "s_waitcnt lgkmcnt(2)\n\t" \
    "v_mfma_f32_16x16x32_bf16 v[216:219], v[4:7], v[240:243], v[216:219]\n\t" \
    "ds_read_b128 v[232:235], v253 offset:30720\n\t" \
    "v_mfma_f32_16x16x32_bf16 v[220:223], v[8:11], v[236:239], v[220:223]\n\t" \
    "v_mfma_f32_16x16x32_bf16 v[220:223], v[12:15], v[240:243], v[220:223]\n\t" \
    "v_mfma_f32_16x16x32_bf16 v[212:215], v[244:247], v[236:239], v[212:215]\n\t" \
    "ds_read_b128 v[244:247], v254 offset:57344\n\t" \
    "v_mfma_f32_16x16x32_bf16 v[212:215], v[248:251], v[240:243], v[212:215]\n\t" \
    "ds_read_b128 v[248:251], v254 offset:58368\n\t" \
    "ds_read_b128 v[236:239], v253 offset:31744\n\t" \
    "s_setprio 2\n\t" \
    "s_waitcnt lgkmcnt(0)\n\t" \
    "s_barrier \n\t" \
    "v_mfma_f32_16x16x32_bf16 v[204:207], v[120:123], v[224:227], v[204:207]\n\t" \
    "v_mfma_f32_16x16x32_bf16 v[204:207], v[124:127], v[228:231], v[204:207]\n\t" \
    "ds_read_b128 v[240:243], v253 offset:12288\n\t" \
    "v_mfma_f32_16x16x32_bf16 v[208:211], v[128:131], v[224:227], v[208:211]\n\t" \
    "v_mfma_f32_16x16x32_bf16 v[208:211], v[132:135], v[228:231], v[208:211]\n\t" \
    "v_mfma_f32_16x16x32_bf16 v[200:203], v[244:247], v[224:227], v[200:203]\n\t" \
    "v_mfma_f32_16x16x32_bf16 v[200:203], v[248:251], v[228:231], v[200:203]\n\t" \
    "v_mfma_f32_16x16x32_bf16 v[216:219], v[120:123], v[232:235], v[216:219]\n\t" \
    "ds_read_b128 v[224:227], v253 offset:13312\n\t" \
    "s_and_b32 s44, s40, 3\n\t" \
    "v_mfma_f32_16x16x32_bf16 v[216:219], v[124:127], v[236:239], v[216:219]\n\t" \
    "ds_read_b128 v[228:231], v253 offset:32768\n\t" \
    "s_lshl_b32 s44, s44, 22\n\t" \
    "v_mfma_f32_16x16x32_bf16 v[220:223], v[128:131], v[232:235], v[220:223]\n\t" \
    "s_add_u32 s44, s44, %[mb]\n\t" \
    "v_mfma_f32_16x16x32_bf16 v[220:223], v[132:135], v[236:239], v[220:223]\n\t" \
    "s_add_u32 s46, s44, 0x8000\n\t" \
    "v_mfma_f32_16x16x32_bf16 v[212:215], v[244:247], v[232:235], v[212:215]\n\t" \
    "ds_read_b128 v[244:247], v254 offset:59392\n\t" \
    "s_add_u32 s47, s44, 0x8c00\n\t" \
    "v_mfma_f32_16x16x32_bf16 v[212:215], v[248:251], v[236:239], v[212:215]\n\t" \
    "ds_read_b128 v[248:251], v254 offset:60416\n\t" \
    "s_waitcnt lgkmcnt(4)\n\t" \
    "v_mfma_f32_16x16x32_bf16 v[204:207], v[144:147], v[240:243], v[204:207]\n\t" \
    "ds_read_b128 v[232:235], v253 offset:33792\n\t" \
    "s_waitcnt lgkmcnt(4)\n\t" \
    "v_mfma_f32_16x16x32_bf16 v[204:207], v[148:151], v[224:227], v[204:207]\n\t" \
    "ds_read_b128 v[236:239], v253 offset:16384\n\t" \
    "v_mfma_f32_16x16x32_bf16 v[208:211], v[152:155], v[240:243], v[208:211]\n\t" \
    "v_mfma_f32_16x16x32_bf16 v[208:211], v[156:159], v[224:227], v[208:211]\n\t" \
    "v_mfma_f32_16x16x32_bf16 v[200:203], v[136:139], v[240:243], v[200:203]\n\t" \
    "v_mfma_f32_16x16x32_bf16 v[200:203], v[140:143], v[224:227], v[200:203]\n\t" \
    "s_waitcnt lgkmcnt(4)\n\t" \
    "v_mfma_f32_16x16x32_bf16 v[216:219], v[144:147], v[228:231], v[216:219]\n\t" \
    "ds_read_b128 v[240:243], v253 offset:17408\n\t" \
    "s_waitcnt lgkmcnt(2)\n\t" \
    "v_mfma_f32_16x16x32_bf16 v[216:219], v[148:151], v[232:235], v[216:219]\n\t" \
    "ds_read_b128 v[224:227], v253 offset:36864\n\t" \
    "v_mfma_f32_16x16x32_bf16 v[220:223], v[152:155], v[228:231], v[220:223]\n\t" \
    "v_mfma_f32_16x16x32_bf16 v[220:223], v[156:159], v[232:235], v[220:223]\n\t" \
    "v_mfma_f32_16x16x32_bf16 v[212:215], v[136:139], v[228:231], v[212:215]\n\t" \
    "v_mfma_f32_16x16x32_bf16 v[212:215], v[140:143], v[232:235], v[212:215]\n\t" \
    "s_waitcnt lgkmcnt(2)\n\t" \
    "v_mfma_f32_16x16x32_bf16 v[204:207], v[160:163], v[236:239], v[204:207]\n\t" \
    "ds_read_b128 v[228:231], v253 offset:37888\n\t" \
    "s_waitcnt lgkmcnt(2)\n\t" \
    "v_mfma_f32_16x16x32_bf16 v[204:207], v[164:167], v[240:243], v[204:207]\n\t" \
    "ds_read_b128 v[232:235], v253 offset:14336\n\t" \
    "v_mfma_f32_16x16x32_bf16 v[208:211], v[168:171], v[236:239], v[208:211]\n\t" \
    "v_mfma_f32_16x16x32_bf16 v[208:211], v[172:175], v[240:243], v[208:211]\n\t" \
    "v_mfma_f32_16x16x32_bf16 v[200:203], v[244:247], v[236:239], v[200:203]\n\t" \
    "v_mfma_f32_16x16x32_bf16 v[200:203], v[248:251], v[240:243], v[200:203]\n\t" \
    "s_waitcnt lgkmcnt(2)\n\t" \
    "v_mfma_f32_16x16x32_bf16 v[216:219], v[160:163], v[224:227], v[216:219]\n\t" \
    "ds_read_b128 v[236:239], v253 offset:15360\n\t" \
    "s_waitcnt lgkmcnt(2)\n\t" \
    "v_mfma_f32_16x16x32_bf16 v[216:219], v[164:167], v[228:231], v[216:219]\n\t" \
    "ds_read_b128 v[240:243], v253 offset:18432\n\t" \
    "v_mfma_f32_16x16x32_bf16 v[220:223], v[168:171], v[224:227], v[220:223]\n\t" \
    "v_mfma_f32_16x16x32_bf16 v[220:223], v[172:175], v[228:231], v[220:223]\n\t" \
    "v_mfma_f32_16x16x32_bf16 v[212:215], v[244:247], v[224:227], v[212:215]\n\t" \
    "v_mfma_f32_16x16x32_bf16 v[212:215], v[248:251], v[228:231], v[212:215]\n\t" \
    "s_waitcnt lgkmcnt(2)\n\t" \
    "v_mfma_f32_16x16x32_bf16 v[204:207], v[24:27], v[232:235], v[204:207]\n\t" \
    "ds_read_b128 v[224:227], v253 offset:19456\n\t" \
    "v_mov_b64 v[244:245], 0\n\t" \
    "s_waitcnt lgkmcnt(2)\n\t" \
    "v_mfma_f32_16x16x32_bf16 v[204:207], v[28:31], v[236:239], v[204:207]\n\t" \
    "ds_read_b128 v[228:231], v253 offset:34816\n\t" \
    "v_mov_b64 v[246:247], 0\n\t" \
    "v_mfma_f32_16x16x32_bf16 v[208:211], v[32:35], v[232:235], v[208:211]\n\t" \
    "v_mov_b64 v[248:249], 0\n\t" \
    "v_mfma_f32_16x16x32_bf16 v[208:211], v[36:39], v[236:239], v[208:211]\n\t" \
    "v_mov_b64 v[250:251], 0\n\t" \
    "v_mfma_f32_16x16x32_bf16 v[200:203], v[16:19], v[232:235], v[200:203]\n\t" \
    "v_mfma_f32_16x16x32_bf16 v[200:203], v[20:23], v[236:239], v[200:203]\n\t" \
    "s_waitcnt lgkmcnt(2)\n\t" \
    "v_mfma_f32_16x16x32_bf16 v[204:207], v[184:187], v[240:243], v[204:207]\n\t" \
    "ds_read_b128 v[232:235], v253 offset:35840\n\t" \
    "s_waitcnt lgkmcnt(2)\n\t" \
    "v_mfma_f32_16x16x32_bf16 v[204:207], v[188:191], v[224:227], v[204:207]\n\t" \
    "ds_read_b128 v[236:239], v253 offset:38912\n\t" \
    "v_mfma_f32_16x16x32_bf16 v[208:211], v[192:195], v[240:243], v[208:211]\n\t" \
    "v_mfma_f32_16x16x32_bf16 v[208:211], v[196:199], v[224:227], v[208:211]\n\t" \
    "v_mfma_f32_16x16x32_bf16 v[200:203], v[176:179], v[240:243], v[200:203]\n\t" \
    "v_mfma_f32_16x16x32_bf16 v[200:203], v[180:183], v[224:227], v[200:203]\n\t" \
    "ds_read_b128 v[240:243], v253 offset:39936\n\t" \
    "s_waitcnt lgkmcnt(3)\n\t" \
    "v_mfma_f32_16x16x32_bf16 v[216:219], v[24:27], v[228:231], v[216:219]\n\t" \
    "s_waitcnt lgkmcnt(2)\n\t" \
    "v_mfma_f32_16x16x32_bf16 v[216:219], v[28:31], v[232:235], v[216:219]\n\t" \
    "v_cvt_pk_bf16_f32 v204, v204, v208\n\t" \
    "v_cvt_pk_bf16_f32 v205, v205, v209\n\t" \
    "s_waitcnt lgkmcnt(1)\n\t" \
    "v_mfma_f32_16x16x32_bf16 v[216:219], v[184:187], v[236:239], v[216:219]\n\t" \
    "v_cvt_pk_bf16_f32 v206, v206, v210\n\t" \
    "v_cvt_pk_bf16_f32 v207, v207, v211\n\t" \
    "s_waitcnt lgkmcnt(0)\n\t" \
    "v_mfma_f32_16x16x32_bf16 v[216:219], v[188:191], v[240:243], v[216:219]\n\t" \
    "ds_write_b128 v254, v[204:207] offset:10240\n\t" \
    "v_mfma_f32_16x16x32_bf16 v[220:223], v[32:35], v[228:231], v[220:223]\n\t" \
    "v_mfma_f32_16x16x32_bf16 v[220:223], v[36:39], v[232:235], v[220:223]\n\t" \
    "v_cvt_pk_bf16_f32 v200, v200, v201\n\t" \
    "v_cvt_pk_bf16_f32 v201, v202, v203\n\t" \
    "v_mfma_f32_16x16x32_bf16 v[220:223], v[192:195], v[236:239], v[220:223]\n\t" \
    "ds_write_b64 v254, v[200:201] offset:6152\n\t" \
    "v_mfma_f32_16x16x32_bf16 v[220:223], v[196:199], v[240:243], v[220:223]\n\t" \
    "v_mfma_f32_16x16x32_bf16 v[212:215], v[16:19], v[228:231], v[212:215]\n\t" \
    "ds_read_b128 v[200:203], v254 offset:51200\n\t" \
    "ds_read_b128 v[204:207], v254 offset:52224\n\t" \
    "v_mfma_f32_16x16x32_bf16 v[212:215], v[20:23], v[232:235], v[212:215]\n\t" \
    "v_mov_b64 v[208:209], 0\n\t" \
    "v_mov_b64 v[210:211], 0\n\t" \
    "v_mfma_f32_16x16x32_bf16 v[212:215], v[176:179], v[236:239], v[212:215]\n\t" \
    "v_cvt_pk_bf16_f32 v216, v216, v220\n\t" \
    "v_cvt_pk_bf16_f32 v217, v217, v221\n\t" \
    "v_mfma_f32_16x16x32_bf16 v[212:215], v[180:183], v[240:243], v[212:215]\n\t" \
    "v_cvt_pk_bf16_f32 v218, v218, v222\n\t" \
    "v_cvt_pk_bf16_f32 v219, v219, v223\n\t" \
    "s_setprio 0\n\t" \
    "ds_write_b128 v254, v[216:219] offset:16384\n\t" \
    "ds_read_b128 v[216:219], v254 offset:53248\n\t" \
    "ds_read_b128 v[220:223], v254 offset:54272\n\t" \
    "s_nop 2\n\t" \
    "v_cvt_pk_bf16_f32 v212, v212, v213\n\t" \
    "v_cvt_pk_bf16_f32 v213, v214, v215\n\t" \
    "ds_write_b64 v254, v[212:213] offset:12296\n\t" \
    "s_sleep 3\n\t" \
    "s_mov_b32 m0, 0xb000\n\t" \
    "s_nop 0\n\t" \
    "buffer_load_dwordx4 v253, s[60:63], s46 offen sc1 lds\n\t" \
    "s_mov_b32 m0, 0xbc00\n\t" \
    "s_nop 0\n\t" \
    "buffer_load_dwordx4 v253, s[60:63], s47 offen sc1 lds\n\t" \
    "s_waitcnt lgkmcnt(0)\n\t" \
    "v_bfe_i32 v212, v253, 5, 1\n\t" \
    "v_and_b32 v212, 0xaaaa, v212\n\t" \
    "v_or_b32 v212, 0x4444, v212\n\t" \
    "v_and_b32 v213, 0xffff, v255\n\t" \
    "v_lshrrev_b32 v214, 16, v255\n\t" \
    ".Ls24_%=:\n\t" \
    "s_waitcnt vmcnt(0)\n\t" \
    "ds_read_b128 v[224:227], v253 offset:45056\n\t" \
    "ds_read_b128 v[228:231], v253 offset:48128\n\t" \
    "s_waitcnt lgkmcnt(1)\n\t" \
    "v_max_u32 v215, v224, v225\n\t" \
    "v_max3_u32 v215, v226, v227, v215\n\t" \
    "s_waitcnt lgkmcnt(0)\n\t" \
    "v_max3_u32 v215, v228, v229, v215\n\t" \
    "v_max3_u32 v215, v230, v231, v215\n\t" \
    "v_cmp_eq_u32 vcc, -1, v215\n\t" \
    "s_cbranch_vccz .Ls25_%=\n\t" \
    "v_max_u32 v215, v224, v225\n\t" \
    "v_max3_u32 v215, v226, v227, v215\n\t" \
    "v_cmp_eq_u32 vcc, -1, v215\n\t" \
    "s_cbranch_vccz .Ls26_%=\n\t" \
    "s_mov_b32 m0, 0xb000\n\t" \
    "s_nop 0\n\t" \
    "buffer_load_dwordx4 v253, s[60:63], s46 offen sc1 lds\n\t" \
    ".Ls26_%=:\n\t" \
    "v_max_u32 v215, v228, v229\n\t" \
    "v_max3_u32 v215, v230, v231, v215\n\t" \
    "v_cmp_eq_u32 vcc, -1, v215\n\t" \
    "s_cbranch_vccz .Ls27_%=\n\t" \
    "s_mov_b32 m0, 0xbc00\n\t" \
    "s_nop 0\n\t" \
    "buffer_load_dwordx4 v253, s[60:63], s47 offen sc1 lds\n\t" \
    ".Ls27_%=:\n\t" \
    "s_cmp_eq_u32 s41, 0\n\t" \
    "s_cbranch_scc1 .Ls25_%=\n\t" \
    "s_sub_u32 s41, s41, 1\n\t" \
    "s_branch .Ls24_%=\n\t" \
    ".Ls25_%=:\n\t" \
    "s_setprio 3\n\t" \
    "s_waitcnt lgkmcnt(0)\n\t" \
    "s_barrier \n\t" \
    "ds_read_b128 v[232:235], v253 offset:40960\n\t" \
    "ds_read_b128 v[236:239], v253 offset:41984\n\t" \
    "ds_read_b128 v[240:243], v253 offset:43008\n\t" \
    "v_smfmac_f32_16x16x64_bf16 v[244:247], v[224:227], v[0:7], v212\n\t" \
    "v_smfmac_f32_16x16x64_bf16 v[248:251], v[224:227], v[8:15], v212\n\t" \
    "v_smfmac_f32_16x16x64_bf16 v[208:211], v[224:227], v[200:207], v212\n\t" \
    "ds_read_b128 v[200:203], v254 offset:55296\n\t" \
    "ds_read_b128 v[204:207], v254 offset:56320\n\t" \
    "ds_read_b128 v[224:227], v253 offset:44032\n\t" \
    "v_smfmac_f32_16x16x64_bf16 v[208:211], v[228:231], v[16:23], v212\n\t" \
    "v_smfmac_f32_16x16x64_bf16 v[244:247], v[228:231], v[24:31], v212\n\t" \
    "v_smfmac_f32_16x16x64_bf16 v[248:251], v[228:231], v[32:39], v212\n\t" \
    "ds_read_b128 v[228:231], v253 offset:46080\n\t" \
    "s_waitcnt lgkmcnt(6)\n\t" \
    "v_smfmac_f32_16x16x64_bf16 v[208:211], v[232:235], v[216:223], v212\n\t" \
    "ds_read_b128 v[216:219], v254 offset:57344\n\t" \
    "ds_read_b128 v[220:223], v254 offset:58368\n\t" \
    "v_smfmac_f32_16x16x64_bf16 v[244:247], v[232:235], v[40:47], v212\n\t" \
    "v_smfmac_f32_16x16x64_bf16 v[248:251], v[232:235], v[48:55], v212\n\t" \
    "ds_read_b128 v[232:235], v253 offset:47104\n\t" \
    "s_waitcnt lgkmcnt(8)\n\t" \
    "v_smfmac_f32_16x16x64_bf16 v[208:211], v[236:239], v[56:63], v212\n\t" \
    "v_smfmac_f32_16x16x64_bf16 v[244:247], v[236:239], v[64:71], v212\n\t" \
    "v_smfmac_f32_16x16x64_bf16 v[248:251], v[236:239], v[72:79], v212\n\t" \
    "ds_read_b128 v[236:239], v253 offset:49152\n\t" \
    "s_waitcnt lgkmcnt(6)\n\t" \
    "v_smfmac_f32_16x16x64_bf16 v[208:211], v[240:243], v[200:207], v212\n\t" \
    "ds_read_b128 v[200:203], v254 offset:59392\n\t" \
    "ds_read_b128 v[204:207], v254 offset:60416\n\t" \
    "v_smfmac_f32_16x16x64_bf16 v[244:247], v[240:243], v[80:87], v212\n\t" \
    "v_smfmac_f32_16x16x64_bf16 v[248:251], v[240:243], v[88:95], v212\n\t" \
    "ds_read_b128 v[240:243], v253 offset:50176\n\t" \
    "s_waitcnt lgkmcnt(8)\n\t" \
    "v_smfmac_f32_16x16x64_bf16 v[208:211], v[224:227], v[96:103], v212\n\t" \
    "v_smfmac_f32_16x16x64_bf16 v[244:247], v[224:227], v[104:111], v212\n\t" \
    "v_smfmac_f32_16x16x64_bf16 v[248:251], v[224:227], v[112:119], v212\n\t" \
    "s_waitcnt lgkmcnt(5)\n\t" \
    "v_smfmac_f32_16x16x64_bf16 v[208:211], v[228:231], v[216:223], v212\n\t" \
    "s_waitcnt lgkmcnt(4)\n\t" \
    "v_smfmac_f32_16x16x64_bf16 v[208:211], v[232:235], v[136:143], v212\n\t" \
    "s_waitcnt lgkmcnt(1)\n\t" \
    "v_smfmac_f32_16x16x64_bf16 v[208:211], v[236:239], v[200:207], v212\n\t" \
    "s_waitcnt lgkmcnt(0)\n\t" \
    "v_smfmac_f32_16x16x64_bf16 v[208:211], v[240:243], v[176:183], v212\n\t" \
    "v_smfmac_f32_16x16x64_bf16 v[244:247], v[228:231], v[120:127], v212\n\t" \
    "v_smfmac_f32_16x16x64_bf16 v[244:247], v[232:235], v[144:151], v212\n\t" \
    "v_smfmac_f32_16x16x64_bf16 v[244:247], v[236:239], v[160:167], v212\n\t" \
    "v_add_f32 v208, v208, v210\n\t" \
    "v_add_f32 v209, v209, v211\n\t" \
    "v_cvt_pk_bf16_f32 v208, v208, v209\n\t" \
    "v_smfmac_f32_16x16x64_bf16 v[244:247], v[240:243], v[184:191], v212\n\t" \
    "ds_write_b16 v213, v208 offset:0\n\t" \
    "ds_write_b16_d16_hi v213, v208 offset:16\n\t" \
    "v_smfmac_f32_16x16x64_bf16 v[248:251], v[228:231], v[128:135], v212\n\t" \
    "v_smfmac_f32_16x16x64_bf16 v[248:251], v[232:235], v[152:159], v212\n\t" \
    "v_smfmac_f32_16x16x64_bf16 v[248:251], v[236:239], v[168:175], v212\n\t" \
    "v_add_f32 v244, v244, v246\n\t" \
    "v_add_f32 v245, v245, v247\n\t" \
    "v_smfmac_f32_16x16x64_bf16 v[248:251], v[240:243], v[192:199], v212\n\t" \
    "s_nop 7\n\t" \
    "v_add_f32 v248, v248, v250\n\t" \
    "v_add_f32 v249, v249, v251\n\t" \
    "v_cvt_pk_bf16_f32 v244, v244, v248\n\t" \
    "v_cvt_pk_bf16_f32 v245, v245, v249\n\t" \
    "ds_write2_b32 v214, v244, v245 offset1:4\n\t" \
    "s_setprio 0\n\t" \
    "s_sleep 1\n\t" \
    "ds_read_b128 v[200:203], v254 offset:12288\n\t" \
    "ds_read_b128 v[224:227], v254 offset:24576\n\t" \
    "ds_read_b128 v[204:207], v254 offset:13312\n\t" \
    "ds_read_b128 v[228:231], v254 offset:25600\n\t" \
    "ds_read_b128 v[208:211], v254 offset:14336\n\t" \
    "ds_read_b128 v[232:235], v254 offset:26624\n\t" \
    "ds_read_b128 v[212:215], v254 offset:15360\n\t" \
    "ds_read_b128 v[236:239], v254 offset:27648\n\t" \
    "ds_read_b128 v[216:219], v254 offset:16384\n\t" \
    "ds_read_b128 v[220:223], v254 offset:17408\n\t" \
    "s_and_b32 s44, s40, 3\n\t" \
    "s_lshl_b32 s44, s44, 17\n\t" \
    "s_add_u32 s50, s44, %[pb]\n\t" \
    "s_add_u32 s51, s44, %[pm]\n\t" \
    "s_mov_b32 s52, 0xf0000\n\t" \
    "s_mov_b32 s53, 0xfff00ff\n\t" \
    "s_mov_b32 s54, 0x1f0001\n\t" \
    "s_mov_b32 s55, 0x1fff01ff\n\t" \
    "s_mov_b32 s56, 0x3f0003\n\t" \
    "s_mov_b32 s57, 0x3fff03ff\n\t" \
    "s_mov_b32 s58, 0x7f0007\n\t" \
    "s_mov_b32 s59, 0x7fff07ff\n\t" \
    "s_waitcnt lgkmcnt(8)\n\t" \
    "v_mfma_f32_16x16x32_bf16 v[240:243], v[224:227], v[200:203], 0\n\t" \
    "ds_read_b128 v[224:227], v254 offset:28672\n\t" \
    "s_waitcnt lgkmcnt(7)\n\t" \
    "v_mfma_f32_16x16x32_bf16 v[240:243], v[228:231], v[204:207], v[240:243]\n\t" \
    "ds_read_b128 v[228:231], v254 offset:29696\n\t" \
    "s_waitcnt lgkmcnt(6)\n\t" \
    "v_mfma_f32_16x16x32_bf16 v[240:243], v[232:235], v[208:211], v[240:243]\n\t" \
    "ds_read_b128 v[232:235], v254 offset:18432\n\t" \
    "s_waitcnt lgkmcnt(5)\n\t" \
    "v_mfma_f32_16x16x32_bf16 v[240:243], v[236:239], v[212:215], v[240:243]\n\t" \
    "ds_read_b128 v[236:239], v254 offset:19456\n\t" \
    "s_waitcnt lgkmcnt(3)\n\t" \
    "v_mfma_f32_16x16x32_bf16 v[240:243], v[224:227], v[216:219], v[240:243]\n\t" \
    "ds_read_b128 v[224:227], v254 offset:20480\n\t" \
    "s_waitcnt lgkmcnt(3)\n\t" \
    "v_mfma_f32_16x16x32_bf16 v[240:243], v[228:231], v[220:223], v[240:243]\n\t" \
    "ds_read_b128 v[228:231], v254 offset:21504\n\t" \
    "v_and_b32 v252, 0xffffff7f, v254\n\t" \
    "s_waitcnt lgkmcnt(0)\n\t" \
    "s_barrier \n\t" \
    "v_mfma_f32_16x16x32_bf16 v[244:247], v[232:235], v[200:203], 0\n\t" \
    "ds_read_b128 v[232:235], v254 offset:22528\n\t" \
    "v_mfma_f32_16x16x32_bf16 v[244:247], v[236:239], v[204:207], v[244:247]\n\t" \
    "ds_read_b128 v[236:239], v254 offset:23552\n\t" \
    "v_mfma_f32_16x16x32_bf16 v[244:247], v[224:227], v[208:211], v[244:247]\n\t" \
    "ds_read_b128 v[224:227], v252 offset:3072\n\t" \
    "v_mfma_f32_16x16x32_bf16 v[244:247], v[228:231], v[212:215], v[244:247]\n\t" \
    "ds_read_b128 v[228:231], v252 offset:3200\n\t" \
    "s_waitcnt lgkmcnt(3)\n\t" \
    "v_mfma_f32_16x16x32_bf16 v[244:247], v[232:235], v[216:219], v[244:247]\n\t" \
    "ds_read_b128 v[232:235], v252 offset:4096\n\t" \
    "s_waitcnt lgkmcnt(3)\n\t" \
    "v_mfma_f32_16x16x32_bf16 v[244:247], v[236:239], v[220:223], v[244:247]\n\t" \
    "ds_read_b128 v[236:239], v252 offset:4224\n\t" \
    "s_waitcnt lgkmcnt(3)\n\t" \
    "v_mfma_f32_16x16x32_bf16 v[248:251], v[224:227], v[200:203], 0\n\t" \
    "ds_read_b128 v[224:227], v252 offset:5120\n\t" \
    "s_waitcnt lgkmcnt(3)\n\t" \
    "v_mfma_f32_16x16x32_bf16 v[248:251], v[228:231], v[204:207], v[248:251]\n\t" \
    "ds_read_b128 v[228:231], v252 offset:5248\n\t" \
    "s_waitcnt lgkmcnt(3)\n\t" \
    "v_mfma_f32_16x16x32_bf16 v[248:251], v[232:235], v[208:211], v[248:251]\n\t" \
    "s_waitcnt lgkmcnt(2)\n\t" \
    "v_mfma_f32_16x16x32_bf16 v[248:251], v[236:239], v[212:215], v[248:251]\n\t" \
    "s_waitcnt lgkmcnt(1)\n\t" \
    "v_mfma_f32_16x16x32_bf16 v[248:251], v[224:227], v[216:219], v[248:251]\n\t" \
    "s_waitcnt lgkmcnt(0)\n\t" \
    "v_mfma_f32_16x16x32_bf16 v[248:251], v[228:231], v[220:223], v[248:251]\n\t" \
    "v_mov_b32 v226, 0xff7fffff\n\t" \
    "v_cndmask_b32 v240, v240, v226, s[52:53]\n\t" \
    "v_cndmask_b32 v241, v241, v226, s[54:55]\n\t" \
    "v_cndmask_b32 v242, v242, v226, s[56:57]\n\t" \
    "v_cndmask_b32 v243, v243, v226, s[58:59]\n\t" \
    "v_max3_f32 v224, v240, v241, v242\n\t" \
    "v_max3_f32 v225, v244, v245, v246\n\t" \
    "v_max3_f32 v224, v224, v243, v247\n\t" \
    "v_lshrrev_b32 v200, 5, v253\n\t" \
    "v_and_b32 v201, 0xf0, v253\n\t" \
    "v_and_or_b32 v200, v200, 8, v201\n\t" \
    "v_max3_f32 v224, v224, v248, v249\n\t" \
    "v_max3_f32 v225, v225, v250, v251\n\t" \
    "v_max_f32 v224, v224, v225\n\t" \
    "v_mov_b32 v225, v224\n\t" \
    "s_nop 1\n\t" \
    "v_permlane16_swap_b32 v224, v225\n\t" \
    "v_max_f32 v224, v224, v225\n\t" \
    "v_mov_b32 v225, v224\n\t" \
    "s_nop 1\n\t" \
    "v_permlane32_swap_b32 v224, v225\n\t" \
    "v_max_f32 v224, v224, v225\n\t" \
    "v_mul_f32 v224, %[scl], v224\n\t" \
    "v_fma_f32 v240, v240, %[scl], -v224\n\t" \
    "v_fma_f32 v241, v241, %[scl], -v224\n\t" \
    "v_exp_f32 v240, v240\n\t" \
    "v_fma_f32 v242, v242, %[scl], -v224\n\t" \
    "v_exp_f32 v241, v241\n\t" \
    "v_fma_f32 v243, v243, %[scl], -v224\n\t" \
    "v_exp_f32 v242, v242\n\t" \
    "v_fma_f32 v244, v244, %[scl], -v224\n\t" \
    "v_exp_f32 v243, v243\n\t" \
    "v_fma_f32 v245, v245, %[scl], -v224\n\t" \
    "v_exp_f32 v244, v244\n\t" \
    "v_fma_f32 v246, v246, %[scl], -v224\n\t" \
    "v_exp_f32 v245, v245\n\t" \
    "v_fma_f32 v247, v247, %[scl], -v224\n\t" \
    "v_exp_f32 v246, v246\n\t" \
    "v_fma_f32 v248, v248, %[scl], -v224\n\t" \
    "v_exp_f32 v247, v247\n\t" \
    "v_fma_f32 v249, v249, %[scl], -v224\n\t" \
    "v_exp_f32 v248, v248\n\t" \
    "v_fma_f32 v250, v250, %[scl], -v224\n\t" \
    "v_exp_f32 v249, v249\n\t" \
    "v_fma_f32 v251, v251, %[scl], -v224\n\t" \
    "v_exp_f32 v250, v250\n\t" \
    "v_exp_f32 v251, v251\n\t" \
    "v_add_f32 v224, v240, v241\n\t" \
    "v_add_f32 v225, v242, v243\n\t" \
    "v_add_f32 v226, v244, v245\n\t" \
    "v_add_f32 v227, v246, v247\n\t" \
    "v_add_f32 v232, v248, v249\n\t" \
    "v_add_f32 v233, v250, v251\n\t" \
    "v_add_f32 v224, v224, v225\n\t" \
    "v_add_f32 v226, v226, v227\n\t" \
    "v_add_f32 v232, v232, v233\n\t" \
    "v_add_f32 v224, v224, v226\n\t" \
    "v_fma_f32 v224, v232, 0.5, v224\n\t" \
    "v_mov_b32 v225, v224\n\t" \
    "s_nop 1\n\t" \
    "v_permlane16_swap_b32 v224, v225\n\t" \
    "v_add_f32 v224, v224, v225\n\t" \
    "v_mov_b32 v225, v224\n\t" \
    "s_nop 1\n\t" \
    "v_permlane32_swap_b32 v224, v225\n\t" \
    "v_add_f32 v224, v224, v225\n\t" \
    "v_rcp_f32 v224, v224\n\t" \
    "s_nop 0\n\t" \
    "v_mul_f32 v240, v240, v224\n\t" \
    "v_mul_f32 v241, v241, v224\n\t" \
    "v_mul_f32 v242, v242, v224\n\t" \
    "v_mul_f32 v243, v243, v224\n\t" \
    "v_mul_f32 v244, v244, v224\n\t" \
    "v_mul_f32 v245, v245, v224\n\t" \
    "v_mul_f32 v246, v246, v224\n\t" \
    "v_mul_f32 v247, v247, v224\n\t" \
    "v_mul_f32 v248, v248, v224\n\t" \
    "v_mul_f32 v249, v249, v224\n\t" \
    "v_mul_f32 v250, v250, v224\n\t" \
    "v_mul_f32 v251, v251, v224\n\t" \
    "v_cvt_pk_bf16_f32 v204, |v244|, |v240|\n\t" \
    "v_cvt_pk_bf16_f32 v205, |v245|, |v241|\n\t" \
    "v_cvt_pk_bf16_f32 v206, |v246|, |v242|\n\t" \
    "v_cvt_pk_bf16_f32 v207, |v247|, |v243|\n\t" \
    "v_cvt_pk_bf16_f32 v208, |v248|, |v249|\n\t" \
    "v_cvt_pk_bf16_f32 v209, |v250|, |v251|\n\t" \
    "buffer_store_dwordx4 v[204:207], v253, s[60:63], s50 offen sc1\n\t" \
    "s_mov_b32 exec_lo, -1\n\t" \
    "s_mov_b32 exec_hi, 0\n\t" \
    "buffer_store_dwordx2 v[208:209], v200, s[60:63], s51 offen sc1\n\t" \
    "s_mov_b32 exec_hi, -1\n\t" \
    "ds_read_b128 v[224:227], v253 offset:0\n\t" \
    "ds_read_b128 v[244:247], v254 offset:53248\n\t" \
    "ds_read_b128 v[228:231], v253 offset:1024\n\t" \
    "ds_read_b128 v[232:235], v253 offset:20480\n\t" \
    "ds_read_b128 v[236:239], v253 offset:21504\n\t" \
    "ds_read_b128 v[248:251], v254 offset:54272\n\t" \
    "s_add_u32 s40, s40, 1\n\t" \
    "s_cmp_lg_u32 s40, 64\n\t" \
    "s_cbranch_scc1 .Ls23_%=\n\t" \
    "s_branch .Lend%=\n\t" \
    ".Ls1_%=:\n\t" \
    "v_add_u32 v244, 0x18000, v243\n\t" \
    "s_waitcnt vmcnt(12) lgkmcnt(0)\n\t" \
    "s_barrier \n\t" \
    "s_add_u32 m0, %[wm], 0x12000\n\t" \
    "s_mov_b32 s44, 0x1e0000\n\t" \
    "buffer_load_dwordx4 v240, %[wr], s44 offen offset:0 nt lds\n\t" \
    "buffer_load_dwordx4 v241, %[wr], s44 offen offset:1024 nt lds\n\t" \
    "buffer_load_dwordx4 v242, %[wr], s44 offen offset:2048 nt lds\n\t" \
    "ds_read_b64_tr_b16 v[56:57], v244 offset:0\n\t" \
    "ds_read_b64_tr_b16 v[58:59], v244 offset:1536\n\t" \
    "ds_read_b64_tr_b16 v[64:65], v244 offset:32\n\t" \
    "ds_read_b64_tr_b16 v[66:67], v244 offset:1568\n\t" \
    "ds_read_b64_tr_b16 v[72:73], v244 offset:64\n\t" \
    "ds_read_b64_tr_b16 v[74:75], v244 offset:1600\n\t" \
    "v_add_u32 v244, 0x1e000, v243\n\t" \
    "s_waitcnt vmcnt(12) lgkmcnt(0)\n\t" \
    "s_barrier \n\t" \
    "s_add_u32 m0, %[wm], 0x18000\n\t" \
    "s_mov_b32 s44, 0x240000\n\t" \
    "buffer_load_dwordx4 v240, %[wr], s44 offen offset:0 nt lds\n\t" \
    "buffer_load_dwordx4 v241, %[wr], s44 offen offset:1024 nt lds\n\t" \
    "buffer_load_dwordx4 v242, %[wr], s44 offen offset:2048 nt lds\n\t" \
    "ds_read_b64_tr_b16 v[60:61], v244 offset:0\n\t" \
    "ds_read_b64_tr_b16 v[62:63], v244 offset:1536\n\t" \
    "ds_read_b64_tr_b16 v[68:69], v244 offset:32\n\t" \
    "ds_read_b64_tr_b16 v[70:71], v244 offset:1568\n\t" \
    "ds_read_b64_tr_b16 v[76:77], v244 offset:64\n\t" \
    "ds_read_b64_tr_b16 v[78:79], v244 offset:1600\n\t" \
    "v_add_u32 v244, 0x0, v243\n\t" \
    "s_waitcnt vmcnt(12) lgkmcnt(0)\n\t" \
    "s_barrier \n\t" \
    "s_add_u32 m0, %[wm], 0x1e000\n\t" \
    "s_mov_b32 s44, 0x2a0000\n\t" \
    "buffer_load_dwordx4 v240, %[wr], s44 offen offset:0 nt lds\n\t" \
    "buffer_load_dwordx4 v241, %[wr], s44 offen offset:1024 nt lds\n\t" \
    "buffer_load_dwordx4 v242, %[wr], s44 offen offset:2048 nt lds\n\t" \
    "ds_read_b64_tr_b16 v[96:97], v244 offset:0\n\t" \
    "ds_read_b64_tr_b16 v[98:99], v244 offset:1536\n\t" \
    "ds_read_b64_tr_b16 v[104:105], v244 offset:32\n\t" \
    "ds_read_b64_tr_b16 v[106:107], v244 offset:1568\n\t" \
    "ds_read_b64_tr_b16 v[112:113], v244 offset:64\n\t" \
    "ds_read_b64_tr_b16 v[114:115], v244 offset:1600\n\t" \
    "v_add_u32 v244, 0x6000, v243\n\t" \
    "s_waitcnt vmcnt(12) lgkmcnt(0)\n\t" \
    "s_barrier \n\t" \
    "s_add_u32 m0, %[wm], 0x0\n\t" \
    "s_mov_b32 s44, 0x300000\n\t" \
    "buffer_load_dwordx4 v240, %[wr], s44 offen offset:0 nt lds\n\t" \
    "buffer_load_dwordx4 v241, %[wr], s44 offen offset:1024 nt lds\n\t" \
    "buffer_load_dwordx4 v242, %[wr], s44 offen offset:2048 nt lds\n\t" \
    "ds_read_b64_tr_b16 v[100:101], v244 offset:0\n\t" \
    "ds_read_b64_tr_b16 v[102:103], v244 offset:1536\n\t" \
    "ds_read_b64_tr_b16 v[108:109], v244 offset:32\n\t" \
    "ds_read_b64_tr_b16 v[110:111], v244 offset:1568\n\t" \
    "ds_read_b64_tr_b16 v[116:117], v244 offset:64\n\t" \
    "ds_read_b64_tr_b16 v[118:119], v244 offset:1600\n\t" \
    "v_add_u32 v244, 0xc000, v243\n\t" \
    "s_waitcnt vmcnt(12) lgkmcnt(0)\n\t" \
    "s_barrier \n\t" \
    "s_add_u32 m0, %[wm], 0x6000\n\t" \
    "s_mov_b32 s44, 0x360000\n\t" \
    "buffer_load_dwordx4 v240, %[wr], s44 offen offset:0 nt lds\n\t" \
    "buffer_load_dwordx4 v241, %[wr], s44 offen offset:1024 nt lds\n\t" \
    "buffer_load_dwordx4 v242, %[wr], s44 offen offset:2048 nt lds\n\t" \
    "ds_read_b64_tr_b16 v[200:201], v244 offset:0\n\t" \
    "ds_read_b64_tr_b16 v[202:203], v244 offset:1536\n\t" \
    "ds_read_b64_tr_b16 v[0:1], v244 offset:32\n\t" \
    "ds_read_b64_tr_b16 v[2:3], v244 offset:1568\n\t" \
    "ds_read_b64_tr_b16 v[8:9], v244 offset:64\n\t" \
    "ds_read_b64_tr_b16 v[10:11], v244 offset:1600\n\t" \
    "v_add_u32 v244, 0x12000, v243\n\t" \
    "s_waitcnt vmcnt(12) lgkmcnt(0)\n\t" \
    "s_barrier \n\t" \
    "s_add_u32 m0, %[wm], 0xc000\n\t" \
    "s_mov_b32 s44, 0x3c0000\n\t" \
    "buffer_load_dwordx4 v240, %[wr], s44 offen offset:0 nt lds\n\t" \
    "buffer_load_dwordx4 v241, %[wr], s44 offen offset:1024 nt lds\n\t" \
    "buffer_load_dwordx4 v242, %[wr], s44 offen offset:2048 nt lds\n\t" \
    "ds_read_b64_tr_b16 v[204:205], v244 offset:0\n\t" \
    "ds_read_b64_tr_b16 v[206:207], v244 offset:1536\n\t" \
    "ds_read_b64_tr_b16 v[4:5], v244 offset:32\n\t" \
    "ds_read_b64_tr_b16 v[6:7], v244 offset:1568\n\t" \
    "ds_read_b64_tr_b16 v[12:13], v244 offset:64\n\t" \
    "ds_read_b64_tr_b16 v[14:15], v244 offset:1600\n\t" \
    "v_add_u32 v244, 0x18000, v243\n\t" \
    "s_waitcnt vmcnt(12) lgkmcnt(0)\n\t" \
    "s_barrier \n\t" \
    "s_add_u32 m0, %[wm], 0x12000\n\t" \
    "s_mov_b32 s44, 0x420000\n\t" \
    "buffer_load_dwordx4 v240, %[wr], s44 offen offset:0 nt lds\n\t" \
    "buffer_load_dwordx4 v241, %[wr], s44 offen offset:1024 nt lds\n\t" \
    "buffer_load_dwordx4 v242, %[wr], s44 offen offset:2048 nt lds\n\t" \
    "ds_read_b64_tr_b16 v[208:209], v244 offset:0\n\t" \
    "ds_read_b64_tr_b16 v[210:211], v244 offset:1536\n\t" \
    "ds_read_b64_tr_b16 v[40:41], v244 offset:32\n\t" \
    "ds_read_b64_tr_b16 v[42:43], v244 offset:1568\n\t" \
    "ds_read_b64_tr_b16 v[48:49], v244 offset:64\n\t" \
    "ds_read_b64_tr_b16 v[50:51], v244 offset:1600\n\t" \
    "v_add_u32 v244, 0x1e000, v243\n\t" \
    "s_waitcnt vmcnt(12) lgkmcnt(0)\n\t" \
    "s_barrier \n\t" \
    "s_add_u32 m0, %[wm], 0x18000\n\t" \
    "s_mov_b32 s44, 0x480000\n\t" \
    "buffer_load_dwordx4 v240, %[wr], s44 offen offset:0 nt lds\n\t" \
    "buffer_load_dwordx4 v241, %[wr], s44 offen offset:1024 nt lds\n\t" \
    "buffer_load_dwordx4 v242, %[wr], s44 offen offset:2048 nt lds\n\t" \
    "ds_read_b64_tr_b16 v[212:213], v244 offset:0\n\t" \
    "ds_read_b64_tr_b16 v[214:215], v244 offset:1536\n\t" \
    "ds_read_b64_tr_b16 v[44:45], v244 offset:32\n\t" \
    "ds_read_b64_tr_b16 v[46:47], v244 offset:1568\n\t" \
    "ds_read_b64_tr_b16 v[52:53], v244 offset:64\n\t" \
    "ds_read_b64_tr_b16 v[54:55], v244 offset:1600\n\t" \
    "v_add_u32 v244, 0x0, v243\n\t" \
    "s_waitcnt vmcnt(12) lgkmcnt(0)\n\t" \
    "s_barrier \n\t" \
    "v_mov_b32 v246, 0\n\t" \
    "buffer_load_dword v246, v246, s[60:63], %[mo] offen sc1\n\t" \
    "s_add_u32 m0, %[wm], 0x1e000\n\t" \
    "s_mov_b32 s44, 0x4e0000\n\t" \
    "buffer_load_dwordx4 v240, %[wr], s44 offen offset:0 nt lds\n\t" \
    "buffer_load_dwordx4 v241, %[wr], s44 offen offset:1024 nt lds\n\t" \
    "buffer_load_dwordx4 v242, %[wr], s44 offen offset:2048 nt lds\n\t" \
    "ds_read_b64_tr_b16 v[136:137], v244 offset:0\n\t" \
    "ds_read_b64_tr_b16 v[138:139], v244 offset:1536\n\t" \
    "ds_read_b64_tr_b16 v[144:145], v244 offset:32\n\t" \
    "ds_read_b64_tr_b16 v[146:147], v244 offset:1568\n\t" \
    "ds_read_b64_tr_b16 v[152:153], v244 offset:64\n\t" \
    "ds_read_b64_tr_b16 v[154:155], v244 offset:1600\n\t" \
    "v_add_u32 v244, 0x6000, v243\n\t" \
    "s_waitcnt vmcnt(12) lgkmcnt(0)\n\t" \
    "s_barrier \n\t" \
    "s_add_u32 m0, %[wm], 0x0\n\t" \
    "s_mov_b32 s44, 0x540000\n\t" \
    "buffer_load_dwordx4 v240, %[wr], s44 offen offset:0 nt lds\n\t" \
    "buffer_load_dwordx4 v241, %[wr], s44 offen offset:1024 nt lds\n\t" \
    "buffer_load_dwordx4 v242, %[wr], s44 offen offset:2048 nt lds\n\t" \
    "ds_read_b64_tr_b16 v[140:141], v244 offset:0\n\t" \
    "ds_read_b64_tr_b16 v[142:143], v244 offset:1536\n\t" \
    "ds_read_b64_tr_b16 v[148:149], v244 offset:32\n\t" \
    "ds_read_b64_tr_b16 v[150:151], v244 offset:1568\n\t" \
    "ds_read_b64_tr_b16 v[156:157], v244 offset:64\n\t" \
    "ds_read_b64_tr_b16 v[158:159], v244 offset:1600\n\t" \
    "v_add_u32 v244, 0xc000, v243\n\t" \
    "s_waitcnt vmcnt(12) lgkmcnt(0)\n\t" \
    "s_barrier \n\t" \
    "s_add_u32 m0, %[wm], 0x6000\n\t" \
    "s_mov_b32 s44, 0x5a0000\n\t" \
    "buffer_load_dwordx4 v240, %[wr], s44 offen offset:0 nt lds\n\t" \
    "buffer_load_dwordx4 v241, %[wr], s44 offen offset:1024 nt lds\n\t" \
    "buffer_load_dwordx4 v242, %[wr], s44 offen offset:2048 nt lds\n\t" \
    "ds_read_b64_tr_b16 v[216:217], v244 offset:0\n\t" \
    "ds_read_b64_tr_b16 v[218:219], v244 offset:1536\n\t" \
    "ds_read_b64_tr_b16 v[80:81], v244 offset:32\n\t" \
    "ds_read_b64_tr_b16 v[82:83], v244 offset:1568\n\t" \
    "ds_read_b64_tr_b16 v[88:89], v244 offset:64\n\t" \
    "ds_read_b64_tr_b16 v[90:91], v244 offset:1600\n\t" \
    "v_add_u32 v244, 0x12000, v243\n\t" \
    "s_waitcnt vmcnt(12) lgkmcnt(0)\n\t" \
    "s_barrier \n\t" \
    "s_add_u32 m0, %[wm], 0xc000\n\t" \
    "s_mov_b32 s44, 0x600000\n\t" \
    "buffer_load_dwordx4 v240, %[wr], s44 offen offset:0 nt lds\n\t" \
    "buffer_load_dwordx4 v241, %[wr], s44 offen offset:1024 nt lds\n\t" \
    "buffer_load_dwordx4 v242, %[wr], s44 offen offset:2048 nt lds\n\t" \
    "ds_read_b64_tr_b16 v[220:221], v244 offset:0\n\t" \
    "ds_read_b64_tr_b16 v[222:223], v244 offset:1536\n\t" \
    "ds_read_b64_tr_b16 v[84:85], v244 offset:32\n\t" \
    "ds_read_b64_tr_b16 v[86:87], v244 offset:1568\n\t" \
    "ds_read_b64_tr_b16 v[92:93], v244 offset:64\n\t" \
    "ds_read_b64_tr_b16 v[94:95], v244 offset:1600\n\t" \
    "v_add_u32 v244, 0x18000, v243\n\t" \
    "s_waitcnt vmcnt(12) lgkmcnt(0)\n\t" \
    "s_barrier \n\t" \
    "s_add_u32 m0, %[wm], 0x12000\n\t" \
    "s_mov_b32 s44, 0x660000\n\t" \
    "buffer_load_dwordx4 v240, %[wr], s44 offen offset:0 nt lds\n\t" \
    "buffer_load_dwordx4 v241, %[wr], s44 offen offset:1024 nt lds\n\t" \
    "buffer_load_dwordx4 v242, %[wr], s44 offen offset:2048 nt lds\n\t" \
    "ds_read_b64_tr_b16 v[16:17], v244 offset:0\n\t" \
    "ds_read_b64_tr_b16 v[18:19], v244 offset:1536\n\t" \
    "ds_read_b64_tr_b16 v[24:25], v244 offset:32\n\t" \
    "ds_read_b64_tr_b16 v[26:27], v244 offset:1568\n\t" \
    "ds_read_b64_tr_b16 v[32:33], v244 offset:64\n\t" \
    "ds_read_b64_tr_b16 v[34:35], v244 offset:1600\n\t" \
    "v_add_u32 v244, 0x1e000, v243\n\t" \
    "s_waitcnt vmcnt(12) lgkmcnt(0)\n\t" \
    "s_barrier \n\t" \
    ".Ls11_%=:\n\t" \
    "v_readfirstlane_b32 s43, v246\n\t" \
    "s_nop 0\n\t" \
    "s_cmp_lg_u32 s43, -1\n\t" \
    "s_cbranch_scc1 .Ls12_%=\n\t" \
    "s_cmp_eq_u32 s41, 0\n\t" \
    "s_cbranch_scc1 .Ls12_%=\n\t" \
    "s_sub_u32 s41, s41, 1\n\t" \
    "s_sleep 4\n\t" \
    "v_mov_b32 v246, 0\n\t" \
    "buffer_load_dword v246, v246, s[60:63], %[mo] offen sc1\n\t" \
    "s_waitcnt vmcnt(0)\n\t" \
    "s_branch .Ls11_%=\n\t" \
    ".Ls12_%=:\n\t" \
    "v_mov_b32 v246, 0x8000\n\t" \
    "s_mov_b64 exec, 1\n\t" \
    "buffer_store_dword v246, v246, s[60:63], %[mo] offen sc1\n\t" \
    "s_mov_b64 exec, -1\n\t" \
    "s_and_b32 s44, s43, 0x5000\n\t" \
    "s_xor_b32 %[mb], %[mb], s44\n\t" \
    "s_xor_b32 %[fl], %[fl], s44\n\t" \
    "s_and_b32 s43, s43, 0x2000\n\t" \
    "s_xor_b32 s43, s43, %[lx]\n\t" \
    "s_add_u32 s60, s60, s43\n\t" \
    "s_addc_u32 s61, s61, 0\n\t" \
    "s_add_u32 m0, %[wm], 0x18000\n\t" \
    "s_mov_b32 s44, 0x6c0000\n\t" \
    "buffer_load_dwordx4 v240, %[wr], s44 offen offset:0 nt lds\n\t" \
    "buffer_load_dwordx4 v241, %[wr], s44 offen offset:1024 nt lds\n\t" \
    "buffer_load_dwordx4 v242, %[wr], s44 offen offset:2048 nt lds\n\t" \
    "ds_read_b64_tr_b16 v[20:21], v244 offset:0\n\t" \
    "ds_read_b64_tr_b16 v[22:23], v244 offset:1536\n\t" \
    "ds_read_b64_tr_b16 v[28:29], v244 offset:32\n\t" \
    "ds_read_b64_tr_b16 v[30:31], v244 offset:1568\n\t" \
    "ds_read_b64_tr_b16 v[36:37], v244 offset:64\n\t" \
    "ds_read_b64_tr_b16 v[38:39], v244 offset:1600\n\t" \
    "v_add_u32 v244, 0x0, v243\n\t" \
    "s_waitcnt vmcnt(12) lgkmcnt(0)\n\t" \
    "s_barrier \n\t" \
    "s_add_u32 m0, %[wm], 0x1e000\n\t" \
    "s_mov_b32 s44, 0x720000\n\t" \
    "buffer_load_dwordx4 v240, %[wr], s44 offen offset:0 nt lds\n\t" \
    "buffer_load_dwordx4 v241, %[wr], s44 offen offset:1024 nt lds\n\t" \
    "buffer_load_dwordx4 v242, %[wr], s44 offen offset:2048 nt lds\n\t" \
    "ds_read_b64_tr_b16 v[224:225], v244 offset:0\n\t" \
    "ds_read_b64_tr_b16 v[226:227], v244 offset:1536\n\t" \
    "ds_read_b64_tr_b16 v[120:121], v244 offset:32\n\t" \
    "ds_read_b64_tr_b16 v[122:123], v244 offset:1568\n\t" \
    "ds_read_b64_tr_b16 v[128:129], v244 offset:64\n\t" \
    "ds_read_b64_tr_b16 v[130:131], v244 offset:1600\n\t" \
    "v_add_u32 v244, 0x6000, v243\n\t" \
    "s_waitcnt vmcnt(12) lgkmcnt(0)\n\t" \
    "s_barrier \n\t" \
    "ds_read_b64_tr_b16 v[228:229], v244 offset:0\n\t" \
    "ds_read_b64_tr_b16 v[230:231], v244 offset:1536\n\t" \
    "ds_read_b64_tr_b16 v[124:125], v244 offset:32\n\t" \
    "ds_read_b64_tr_b16 v[126:127], v244 offset:1568\n\t" \
    "ds_read_b64_tr_b16 v[132:133], v244 offset:64\n\t" \
    "ds_read_b64_tr_b16 v[134:135], v244 offset:1600\n\t" \
    "v_add_u32 v244, 0xc000, v243\n\t" \
    "s_waitcnt vmcnt(9) lgkmcnt(0)\n\t" \
    "s_barrier \n\t" \
    "ds_read_b64_tr_b16 v[232:233], v244 offset:0\n\t" \
    "ds_read_b64_tr_b16 v[234:235], v244 offset:1536\n\t" \
    "ds_read_b64_tr_b16 v[160:161], v244 offset:32\n\t" \
    "ds_read_b64_tr_b16 v[162:163], v244 offset:1568\n\t" \
    "ds_read_b64_tr_b16 v[168:169], v244 offset:64\n\t" \
    "ds_read_b64_tr_b16 v[170:171], v244 offset:1600\n\t" \
    "v_add_u32 v244, 0x12000, v243\n\t" \
    "s_waitcnt vmcnt(6) lgkmcnt(0)\n\t" \
    "s_barrier \n\t" \
    "ds_read_b64_tr_b16 v[236:237], v244 offset:0\n\t" \
    "ds_read_b64_tr_b16 v[238:239], v244 offset:1536\n\t" \
    "ds_read_b64_tr_b16 v[164:165], v244 offset:32\n\t" \
    "ds_read_b64_tr_b16 v[166:167], v244 offset:1568\n\t" \
    "ds_read_b64_tr_b16 v[172:173], v244 offset:64\n\t" \
    "ds_read_b64_tr_b16 v[174:175], v244 offset:1600\n\t" \
    "v_add_u32 v244, 0x18000, v243\n\t" \
    "s_waitcnt vmcnt(3) lgkmcnt(0)\n\t" \
    "s_barrier \n\t" \
    "ds_read_b64_tr_b16 v[176:177], v244 offset:0\n\t" \
    "ds_read_b64_tr_b16 v[178:179], v244 offset:1536\n\t" \
    "ds_read_b64_tr_b16 v[184:185], v244 offset:32\n\t" \
    "ds_read_b64_tr_b16 v[186:187], v244 offset:1568\n\t" \
    "ds_read_b64_tr_b16 v[192:193], v244 offset:64\n\t" \
    "ds_read_b64_tr_b16 v[194:195], v244 offset:1600\n\t" \
    "v_add_u32 v244, 0x1e000, v243\n\t" \
    "s_waitcnt vmcnt(0) lgkmcnt(0)\n\t" \
    "s_barrier \n\t" \
    "ds_read_b64_tr_b16 v[180:181], v244 offset:0\n\t" \
    "ds_read_b64_tr_b16 v[182:183], v244 offset:1536\n\t" \
    "ds_read_b64_tr_b16 v[188:189], v244 offset:32\n\t" \
    "ds_read_b64_tr_b16 v[190:191], v244 offset:1568\n\t" \
    "ds_read_b64_tr_b16 v[196:197], v244 offset:64\n\t" \
    "ds_read_b64_tr_b16 v[198:199], v244 offset:1600\n\t" \
    "s_waitcnt lgkmcnt(0)\n\t" \
    "s_barrier \n\t" \
    "ds_write_b128 v254, v[200:203] offset:40960\n\t" \
    "ds_write_b128 v254, v[204:207] offset:41984\n\t" \
    "ds_write_b128 v254, v[208:211] offset:43008\n\t" \
    "ds_write_b128 v254, v[212:215] offset:44032\n\t" \
    "ds_write_b128 v254, v[216:219] offset:45056\n\t" \
    "ds_write_b128 v254, v[220:223] offset:46080\n\t" \
    "ds_write_b128 v254, v[224:227] offset:47104\n\t" \
    "ds_write_b128 v254, v[228:231] offset:48128\n\t" \
    "ds_write_b128 v254, v[232:235] offset:49152\n\t" \
    "ds_write_b128 v254, v[236:239] offset:50176\n\t" \
    "s_waitcnt vmcnt(0) lgkmcnt(0)\n\t" \
    "s_barrier \n\t" \
    "ds_read_b128 v[224:227], v253 offset:0\n\t" \
    "ds_read_b128 v[244:247], v254 offset:40960\n\t" \
    "ds_read_b128 v[228:231], v253 offset:1024\n\t" \
    "ds_read_b128 v[232:235], v253 offset:20480\n\t" \
    "ds_read_b128 v[236:239], v253 offset:21504\n\t" \
    "ds_read_b128 v[248:251], v254 offset:41984\n\t" \
    ".Ls13_%=:\n\t" \
    "s_waitcnt lgkmcnt(5)\n\t" \
    "v_mfma_f32_16x16x32_bf16 v[204:207], v[64:67], v[224:227], 0\n\t" \
    "s_waitcnt lgkmcnt(3)\n\t" \
    "v_mfma_f32_16x16x32_bf16 v[204:207], v[68:71], v[228:231], v[204:207]\n\t" \
    "ds_read_b128 v[240:243], v253 offset:2048\n\t" \
    "v_mfma_f32_16x16x32_bf16 v[208:211], v[72:75], v[224:227], 0\n\t" \
    "v_mfma_f32_16x16x32_bf16 v[208:211], v[76:79], v[228:231], v[208:211]\n\t" \
    "v_mfma_f32_16x16x32_bf16 v[200:203], v[56:59], v[224:227], 0\n\t" \
    "v_mfma_f32_16x16x32_bf16 v[200:203], v[60:63], v[228:231], v[200:203]\n\t" \
    "s_waitcnt lgkmcnt(3)\n\t" \
    "v_mfma_f32_16x16x32_bf16 v[216:219], v[64:67], v[232:235], 0\n\t" \
    "ds_read_b128 v[224:227], v253 offset:3072\n\t" \
    "s_waitcnt lgkmcnt(3)\n\t" \
    "v_mfma_f32_16x16x32_bf16 v[216:219], v[68:71], v[236:239], v[216:219]\n\t" \
    "ds_read_b128 v[228:231], v253 offset:22528\n\t" \
    "v_mfma_f32_16x16x32_bf16 v[220:223], v[72:75], v[232:235], 0\n\t" \
    "v_mfma_f32_16x16x32_bf16 v[220:223], v[76:79], v[236:239], v[220:223]\n\t" \
    "v_mfma_f32_16x16x32_bf16 v[212:215], v[56:59], v[232:235], 0\n\t" \
    "v_mfma_f32_16x16x32_bf16 v[212:215], v[60:63], v[236:239], v[212:215]\n\t" \
    "s_waitcnt lgkmcnt(2)\n\t" \
    "v_mfma_f32_16x16x32_bf16 v[204:207], v[104:107], v[240:243], v[204:207]\n\t" \
    "ds_read_b128 v[232:235], v253 offset:23552\n\t" \
    "s_waitcnt lgkmcnt(2)\n\t" \
    "v_mfma_f32_16x16x32_bf16 v[204:207], v[108:111], v[224:227], v[204:207]\n\t" \
    "ds_read_b128 v[236:239], v253 offset:4096\n\t" \
    "v_mfma_f32_16x16x32_bf16 v[208:211], v[112:115], v[240:243], v[208:211]\n\t" \
    "v_mfma_f32_16x16x32_bf16 v[208:211], v[116:119], v[224:227], v[208:211]\n\t" \
    "v_mfma_f32_16x16x32_bf16 v[200:203], v[96:99], v[240:243], v[200:203]\n\t" \
    "v_mfma_f32_16x16x32_bf16 v[200:203], v[100:103], v[224:227], v[200:203]\n\t" \
    "s_waitcnt lgkmcnt(2)\n\t" \
    "v_mfma_f32_16x16x32_bf16 v[216:219], v[104:107], v[228:231], v[216:219]\n\t" \
    "ds_read_b128 v[240:243], v253 offset:5120\n\t" \
    "s_waitcnt lgkmcnt(2)\n\t" \
    "v_mfma_f32_16x16x32_bf16 v[216:219], v[108:111], v[232:235], v[216:219]\n\t" \
    "ds_read_b128 v[224:227], v253 offset:24576\n\t" \
    "v_mfma_f32_16x16x32_bf16 v[220:223], v[112:115], v[228:231], v[220:223]\n\t" \
    "v_mfma_f32_16x16x32_bf16 v[220:223], v[116:119], v[232:235], v[220:223]\n\t" \
    "v_mfma_f32_16x16x32_bf16 v[212:215], v[96:99], v[228:231], v[212:215]\n\t" \
    "v_mfma_f32_16x16x32_bf16 v[212:215], v[100:103], v[232:235], v[212:215]\n\t" \
    "s_waitcnt lgkmcnt(2)\n\t" \
    "v_mfma_f32_16x16x32_bf16 v[204:207], v[0:3], v[236:239], v[204:207]\n\t" \
    "ds_read_b128 v[228:231], v253 offset:25600\n\t" \
    "s_waitcnt lgkmcnt(2)\n\t" \
    "v_mfma_f32_16x16x32_bf16 v[204:207], v[4:7], v[240:243], v[204:207]\n\t" \
    "ds_read_b128 v[232:235], v253 offset:6144\n\t" \
    "v_mfma_f32_16x16x32_bf16 v[208:211], v[8:11], v[236:239], v[208:211]\n\t" \
    "v_mfma_f32_16x16x32_bf16 v[208:211], v[12:15], v[240:243], v[208:211]\n\t" \
    "v_mfma_f32_16x16x32_bf16 v[200:203], v[244:247], v[236:239], v[200:203]\n\t" \
    "v_mfma_f32_16x16x32_bf16 v[200:203], v[248:251], v[240:243], v[200:203]\n\t" \
    "s_waitcnt lgkmcnt(2)\n\t" \
    "v_mfma_f32_16x16x32_bf16 v[216:219], v[0:3], v[224:227], v[216:219]\n\t" \
    "ds_read_b128 v[236:239], v253 offset:7168\n\t" \
    "s_waitcnt lgkmcnt(2)\n\t" \
    "v_mfma_f32_16x16x32_bf16 v[216:219], v[4:7], v[228:231], v[216:219]\n\t" \
    "ds_read_b128 v[240:243], v253 offset:26624\n\t" \
    "v_mfma_f32_16x16x32_bf16 v[220:223], v[8:11], v[224:227], v[220:223]\n\t" \
    "v_mfma_f32_16x16x32_bf16 v[220:223], v[12:15], v[228:231], v[220:223]\n\t" \
    "v_mfma_f32_16x16x32_bf16 v[212:215], v[244:247], v[224:227], v[212:215]\n\t" \
    "ds_read_b128 v[244:247], v254 offset:43008\n\t" \
    "v_mfma_f32_16x16x32_bf16 v[212:215], v[248:251], v[228:231], v[212:215]\n\t" \
    "ds_read_b128 v[248:251], v254 offset:44032\n\t" \
    "s_waitcnt lgkmcnt(4)\n\t" \
    "v_mfma_f32_16x16x32_bf16 v[204:207], v[40:43], v[232:235], v[204:207]\n\t" \
    "ds_read_b128 v[224:227], v253 offset:27648\n\t" \
    "s_waitcnt lgkmcnt(4)\n\t" \
    "v_mfma_f32_16x16x32_bf16 v[204:207], v[44:47], v[236:239], v[204:207]\n\t" \
    "ds_read_b128 v[228:231], v253 offset:8192\n\t" \
    "v_mfma_f32_16x16x32_bf16 v[208:211], v[48:51], v[232:235], v[208:211]\n\t" \
    "v_mfma_f32_16x16x32_bf16 v[208:211], v[52:55], v[236:239], v[208:211]\n\t" \
    "s_waitcnt lgkmcnt(3)\n\t" \
    "v_mfma_f32_16x16x32_bf16 v[200:203], v[244:247], v[232:235], v[200:203]\n\t" \
    "s_waitcnt lgkmcnt(2)\n\t" \
    "v_mfma_f32_16x16x32_bf16 v[200:203], v[248:251], v[236:239], v[200:203]\n\t" \
    "v_mfma_f32_16x16x32_bf16 v[216:219], v[40:43], v[240:243], v[216:219]\n\t" \
    "ds_read_b128 v[232:235], v253 offset:9216\n\t" \
    "s_waitcnt lgkmcnt(2)\n\t" \
    "v_mfma_f32_16x16x32_bf16 v[216:219], v[44:47], v[224:227], v[216:219]\n\t" \
    "ds_read_b128 v[236:239], v253 offset:28672\n\t" \
    "v_mfma_f32_16x16x32_bf16 v[220:223], v[48:51], v[240:243], v[220:223]\n\t" \
    "v_mfma_f32_16x16x32_bf16 v[220:223], v[52:55], v[224:227], v[220:223]\n\t" \
    "v_mfma_f32_16x16x32_bf16 v[212:215], v[244:247], v[240:243], v[212:215]\n\t" \
    "ds_read_b128 v[244:247], v254 offset:45056\n\t" \
    "v_mfma_f32_16x16x32_bf16 v[212:215], v[248:251], v[224:227], v[212:215]\n\t" \
    "ds_read_b128 v[248:251], v254 offset:46080\n\t" \
    "s_waitcnt lgkmcnt(4)\n\t" \
    "v_mfma_f32_16x16x32_bf16 v[204:207], v[144:147], v[228:231], v[204:207]\n\t" \
    "ds_read_b128 v[240:243], v253 offset:29696\n\t" \
    "s_waitcnt lgkmcnt(4)\n\t" \
    "v_mfma_f32_16x16x32_bf16 v[204:207], v[148:151], v[232:235], v[204:207]\n\t" \
    "ds_read_b128 v[224:227], v253 offset:10240\n\t" \
    "v_mfma_f32_16x16x32_bf16 v[208:211], v[152:155], v[228:231], v[208:211]\n\t" \
    "v_mfma_f32_16x16x32_bf16 v[208:211], v[156:159], v[232:235], v[208:211]\n\t" \
    "v_mfma_f32_16x16x32_bf16 v[200:203], v[136:139], v[228:231], v[200:203]\n\t" \
    "v_mfma_f32_16x16x32_bf16 v[200:203], v[140:143], v[232:235], v[200:203]\n\t" \
    "s_waitcnt lgkmcnt(4)\n\t" \
    "v_mfma_f32_16x16x32_bf16 v[216:219], v[144:147], v[236:239], v[216:219]\n\t" \
    "ds_read_b128 v[228:231], v253 offset:11264\n\t" \
    "s_waitcnt lgkmcnt(2)\n\t" \
    "v_mfma_f32_16x16x32_bf16 v[216:219], v[148:151], v[240:243], v[216:219]\n\t" \
    "ds_read_b128 v[232:235], v253 offset:30720\n\t" \
    "v_mfma_f32_16x16x32_bf16 v[220:223], v[152:155], v[236:239], v[220:223]\n\t" \
    "v_mfma_f32_16x16x32_bf16 v[220:223], v[156:159], v[240:243], v[220:223]\n\t" \
    "v_mfma_f32_16x16x32_bf16 v[212:215], v[136:139], v[236:239], v[212:215]\n\t" \
    "v_mfma_f32_16x16x32_bf16 v[212:215], v[140:143], v[240:243], v[212:215]\n\t" \
    "ds_read_b128 v[236:239], v253 offset:31744\n\t" \
    "s_setprio 2\n\t" \
    "s_waitcnt lgkmcnt(0)\n\t" \
    "s_barrier \n\t" \
    "v_mfma_f32_16x16x32_bf16 v[204:207], v[80:83], v[224:227], v[204:207]\n\t" \
    "v_mfma_f32_16x16x32_bf16 v[204:207], v[84:87], v[228:231], v[204:207]\n\t" \
    "ds_read_b128 v[240:243], v253 offset:14336\n\t" \
    "v_mfma_f32_16x16x32_bf16 v[208:211], v[88:91], v[224:227], v[208:211]\n\t" \
    "v_mfma_f32_16x16x32_bf16 v[208:211], v[92:95], v[228:231], v[208:211]\n\t" \
    "v_mfma_f32_16x16x32_bf16 v[200:203], v[244:247], v[224:227], v[200:203]\n\t" \
    "v_mfma_f32_16x16x32_bf16 v[200:203], v[248:251], v[228:231], v[200:203]\n\t" \
    "v_mfma_f32_16x16x32_bf16 v[216:219], v[80:83], v[232:235], v[216:219]\n\t" \
    "ds_read_b128 v[224:227], v253 offset:15360\n\t" \
    "s_and_b32 s44, s40, 3\n\t" \
    "v_mfma_f32_16x16x32_bf16 v[216:219], v[84:87], v[236:239], v[216:219]\n\t" \
    "ds_read_b128 v[228:231], v253 offset:34816\n\t" \
    "s_lshl_b32 s44, s44, 22\n\t" \
    "v_mfma_f32_16x16x32_bf16 v[220:223], v[88:91], v[232:235], v[220:223]\n\t" \
    "s_add_u32 s44, s44, %[mb]\n\t" \
    "v_mfma_f32_16x16x32_bf16 v[220:223], v[92:95], v[236:239], v[220:223]\n\t" \
    "s_add_u32 s46, s44, 0x800\n\t" \
    "v_mfma_f32_16x16x32_bf16 v[212:215], v[244:247], v[232:235], v[212:215]\n\t" \
    "ds_read_b128 v[244:247], v254 offset:47104\n\t" \
    "s_add_u32 s47, s44, 0x8800\n\t" \
    "v_mfma_f32_16x16x32_bf16 v[212:215], v[248:251], v[236:239], v[212:215]\n\t" \
    "ds_read_b128 v[248:251], v254 offset:48128\n\t" \
    "s_add_u32 s48, s44, 0xc00\n\t" \
    "s_waitcnt lgkmcnt(4)\n\t" \
    "v_mfma_f32_16x16x32_bf16 v[204:207], v[120:123], v[240:243], v[204:207]\n\t" \
    "ds_read_b128 v[232:235], v253 offset:35840\n\t" \
    "s_waitcnt lgkmcnt(4)\n\t" \
    "v_mfma_f32_16x16x32_bf16 v[204:207], v[124:127], v[224:227], v[204:207]\n\t" \
    "ds_read_b128 v[236:239], v253 offset:16384\n\t" \
    "v_mfma_f32_16x16x32_bf16 v[208:211], v[128:131], v[240:243], v[208:211]\n\t" \
    "v_mfma_f32_16x16x32_bf16 v[208:211], v[132:135], v[224:227], v[208:211]\n\t" \
    "s_waitcnt lgkmcnt(3)\n\t" \
    "v_mfma_f32_16x16x32_bf16 v[200:203], v[244:247], v[240:243], v[200:203]\n\t" \
    "s_waitcnt lgkmcnt(2)\n\t" \
    "v_mfma_f32_16x16x32_bf16 v[200:203], v[248:251], v[224:227], v[200:203]\n\t" \
    "v_mfma_f32_16x16x32_bf16 v[216:219], v[120:123], v[228:231], v[216:219]\n\t" \
    "ds_read_b128 v[240:243], v253 offset:17408\n\t" \
    "s_waitcnt lgkmcnt(2)\n\t" \
    "v_mfma_f32_16x16x32_bf16 v[216:219], v[124:127], v[232:235], v[216:219]\n\t" \
    "ds_read_b128 v[224:227], v253 offset:36864\n\t" \
    "v_mfma_f32_16x16x32_bf16 v[220:223], v[128:131], v[228:231], v[220:223]\n\t" \
    "v_mfma_f32_16x16x32_bf16 v[220:223], v[132:135], v[232:235], v[220:223]\n\t" \
    "v_mfma_f32_16x16x32_bf16 v[212:215], v[244:247], v[228:231], v[212:215]\n\t" \
    "ds_read_b128 v[244:247], v254 offset:49152\n\t" \
    "v_mfma_f32_16x16x32_bf16 v[212:215], v[248:251], v[232:235], v[212:215]\n\t" \
    "ds_read_b128 v[248:251], v254 offset:50176\n\t" \
    "s_waitcnt lgkmcnt(4)\n\t" \
    "v_mfma_f32_16x16x32_bf16 v[204:207], v[160:163], v[236:239], v[204:207]\n\t" \
    "ds_read_b128 v[228:231], v253 offset:37888\n\t" \
    "s_waitcnt lgkmcnt(4)\n\t" \
    "v_mfma_f32_16x16x32_bf16 v[204:207], v[164:167], v[240:243], v[204:207]\n\t" \
    "ds_read_b128 v[232:235], v253 offset:12288\n\t" \
    "v_mfma_f32_16x16x32_bf16 v[208:211], v[168:171], v[236:239], v[208:211]\n\t" \
    "v_mfma_f32_16x16x32_bf16 v[208:211], v[172:175], v[240:243], v[208:211]\n\t" \
    "s_waitcnt lgkmcnt(3)\n\t" \
    "v_mfma_f32_16x16x32_bf16 v[200:203], v[244:247], v[236:239], v[200:203]\n\t" \
    "s_waitcnt lgkmcnt(2)\n\t" \
    "v_mfma_f32_16x16x32_bf16 v[200:203], v[248:251], v[240:243], v[200:203]\n\t" \
    "v_mfma_f32_16x16x32_bf16 v[216:219], v[160:163], v[224:227], v[216:219]\n\t" \
    "ds_read_b128 v[236:239], v253 offset:13312\n\t" \
    "s_waitcnt lgkmcnt(2)\n\t" \
    "v_mfma_f32_16x16x32_bf16 v[216:219], v[164:167], v[228:231], v[216:219]\n\t" \
    "ds_read_b128 v[240:243], v253 offset:18432\n\t" \
    "v_mfma_f32_16x16x32_bf16 v[220:223], v[168:171], v[224:227], v[220:223]\n\t" \
    "v_mfma_f32_16x16x32_bf16 v[220:223], v[172:175], v[228:231], v[220:223]\n\t" \
    "v_mfma_f32_16x16x32_bf16 v[212:215], v[244:247], v[224:227], v[212:215]\n\t" \
    "v_mfma_f32_16x16x32_bf16 v[212:215], v[248:251], v[228:231], v[212:215]\n\t" \
    "s_waitcnt lgkmcnt(2)\n\t" \
    "v_mfma_f32_16x16x32_bf16 v[204:207], v[24:27], v[232:235], v[204:207]\n\t" \
    "ds_read_b128 v[224:227], v253 offset:19456\n\t" \
    "v_mov_b64 v[244:245], 0\n\t" \
    "s_waitcnt lgkmcnt(2)\n\t" \
    "v_mfma_f32_16x16x32_bf16 v[204:207], v[28:31], v[236:239], v[204:207]\n\t" \
    "ds_read_b128 v[228:231], v253 offset:32768\n\t" \
    "v_mov_b64 v[246:247], 0\n\t" \
    "v_mfma_f32_16x16x32_bf16 v[208:211], v[32:35], v[232:235], v[208:211]\n\t" \
    "v_mov_b64 v[248:249], 0\n\t" \
    "v_mfma_f32_16x16x32_bf16 v[208:211], v[36:39], v[236:239], v[208:211]\n\t" \
    "v_mov_b64 v[250:251], 0\n\t" \
    "v_mfma_f32_16x16x32_bf16 v[200:203], v[16:19], v[232:235], v[200:203]\n\t" \
    "v_mfma_f32_16x16x32_bf16 v[200:203], v[20:23], v[236:239], v[200:203]\n\t" \
    "s_waitcnt lgkmcnt(2)\n\t" \
    "v_mfma_f32_16x16x32_bf16 v[204:207], v[184:187], v[240:243], v[204:207]\n\t" \
    "ds_read_b128 v[232:235], v253 offset:33792\n\t" \
    "s_waitcnt lgkmcnt(2)\n\t" \
    "v_mfma_f32_16x16x32_bf16 v[204:207], v[188:191], v[224:227], v[204:207]\n\t" \
    "ds_read_b128 v[236:239], v253 offset:38912\n\t" \
    "v_mfma_f32_16x16x32_bf16 v[208:211], v[192:195], v[240:243], v[208:211]\n\t" \
    "v_mfma_f32_16x16x32_bf16 v[208:211], v[196:199], v[224:227], v[208:211]\n\t" \
    "v_mfma_f32_16x16x32_bf16 v[200:203], v[176:179], v[240:243], v[200:203]\n\t" \
    "v_mfma_f32_16x16x32_bf16 v[200:203], v[180:183], v[224:227], v[200:203]\n\t" \
    "ds_read_b128 v[240:243], v253 offset:39936\n\t" \
    "s_waitcnt lgkmcnt(3)\n\t" \
    "v_mfma_f32_16x16x32_bf16 v[216:219], v[24:27], v[228:231], v[216:219]\n\t" \
    "s_waitcnt lgkmcnt(2)\n\t" \
    "v_mfma_f32_16x16x32_bf16 v[216:219], v[28:31], v[232:235], v[216:219]\n\t" \
    "v_cvt_pk_bf16_f32 v204, v204, v208\n\t" \
    "v_cvt_pk_bf16_f32 v205, v205, v209\n\t" \
    "s_waitcnt lgkmcnt(1)\n\t" \
    "v_mfma_f32_16x16x32_bf16 v[216:219], v[184:187], v[236:239], v[216:219]\n\t" \
    "v_cvt_pk_bf16_f32 v206, v206, v210\n\t" \
    "v_cvt_pk_bf16_f32 v207, v207, v211\n\t" \
    "s_waitcnt lgkmcnt(0)\n\t" \
    "v_mfma_f32_16x16x32_bf16 v[216:219], v[188:191], v[240:243], v[216:219]\n\t" \
    "ds_write_b128 v254, v[204:207] offset:9216\n\t" \
    "v_mfma_f32_16x16x32_bf16 v[220:223], v[32:35], v[228:231], v[220:223]\n\t" \
    "v_mfma_f32_16x16x32_bf16 v[220:223], v[36:39], v[232:235], v[220:223]\n\t" \
    "v_cvt_pk_bf16_f32 v200, v200, v201\n\t" \
    "v_cvt_pk_bf16_f32 v201, v202, v203\n\t" \
    "v_mfma_f32_16x16x32_bf16 v[220:223], v[192:195], v[236:239], v[220:223]\n\t" \
    "ds_write_b64 v254, v[200:201] offset:7168\n\t" \
    "v_mfma_f32_16x16x32_bf16 v[220:223], v[196:199], v[240:243], v[220:223]\n\t" \
    "v_mfma_f32_16x16x32_bf16 v[212:215], v[16:19], v[228:231], v[212:215]\n\t" \
    "ds_read_b128 v[200:203], v254 offset:40960\n\t" \
    "ds_read_b128 v[204:207], v254 offset:41984\n\t" \
    "v_mfma_f32_16x16x32_bf16 v[212:215], v[20:23], v[232:235], v[212:215]\n\t" \
    "v_mov_b64 v[208:209], 0\n\t" \
    "v_mov_b64 v[210:211], 0\n\t" \
    "v_mfma_f32_16x16x32_bf16 v[212:215], v[176:179], v[236:239], v[212:215]\n\t" \
    "v_cvt_pk_bf16_f32 v216, v216, v220\n\t" \
    "v_cvt_pk_bf16_f32 v217, v217, v221\n\t" \
    "v_mfma_f32_16x16x32_bf16 v[212:215], v[180:183], v[240:243], v[212:215]\n\t" \
    "v_cvt_pk_bf16_f32 v218, v218, v222\n\t" \
    "v_cvt_pk_bf16_f32 v219, v219, v223\n\t" \
    "s_setprio 0\n\t" \
    "ds_write_b128 v254, v[216:219] offset:15360\n\t" \
    "ds_read_b128 v[216:219], v254 offset:43008\n\t" \
    "ds_read_b128 v[220:223], v254 offset:44032\n\t" \
    "s_nop 2\n\t" \
    "v_cvt_pk_bf16_f32 v212, v212, v213\n\t" \
    "v_cvt_pk_bf16_f32 v213, v214, v215\n\t" \
    "ds_write_b64 v254, v[212:213] offset:13312\n\t" \
    "s_sleep 3\n\t" \
    "s_mov_b32 m0, 0xa800\n\t" \
    "s_nop 0\n\t" \
    "buffer_load_dwordx4 v253, s[60:63], s46 offen sc1 lds\n\t" \
    "s_mov_b32 m0, 0xb800\n\t" \
    "s_nop 0\n\t" \
    "buffer_load_dwordx4 v253, s[60:63], s47 offen sc1 lds\n\t" \
    "s_mov_b32 m0, 0xac00\n\t" \
    "s_nop 0\n\t" \
    "buffer_load_dwordx4 v253, s[60:63], s48 offen sc1 lds\n\t" \
    "s_waitcnt lgkmcnt(0)\n\t" \
    "v_bfe_i32 v212, v253, 5, 1\n\t" \
    "v_and_b32 v212, 0xaaaa, v212\n\t" \
    "v_or_b32 v212, 0x4444, v212\n\t" \
    "v_and_b32 v213, 0xffff, v255\n\t" \
    "v_lshrrev_b32 v214, 16, v255\n\t" \
    ".Ls14_%=:\n\t" \
    "s_waitcnt vmcnt(0)\n\t" \
    "ds_read_b128 v[224:227], v253 offset:43008\n\t" \
    "ds_read_b128 v[228:231], v253 offset:47104\n\t" \
    "ds_read_b128 v[232:235], v253 offset:44032\n\t" \
    "s_waitcnt lgkmcnt(2)\n\t" \
    "v_max_u32 v215, v224, v225\n\t" \
    "v_max3_u32 v215, v226, v227, v215\n\t" \
    "s_waitcnt lgkmcnt(1)\n\t" \
    "v_max3_u32 v215, v228, v229, v215\n\t" \
    "v_max3_u32 v215, v230, v231, v215\n\t" \
    "s_waitcnt lgkmcnt(0)\n\t" \
    "v_max3_u32 v215, v232, v233, v215\n\t" \
    "v_max3_u32 v215, v234, v235, v215\n\t" \
    "v_cmp_eq_u32 vcc, -1, v215\n\t" \
    "s_cbranch_vccz .Ls15_%=\n\t" \
    "v_max_u32 v215, v224, v225\n\t" \
    "v_max3_u32 v215, v226, v227, v215\n\t" \
    "v_cmp_eq_u32 vcc, -1, v215\n\t" \
    "s_cbranch_vccz .Ls16_%=\n\t" \
    "s_mov_b32 m0, 0xa800\n\t" \
    "s_nop 0\n\t" \
    "buffer_load_dwordx4 v253, s[60:63], s46 offen sc1 lds\n\t" \
    ".Ls16_%=:\n\t" \
    "v_max_u32 v215, v228, v229\n\t" \
    "v_max3_u32 v215, v230, v231, v215\n\t" \
    "v_cmp_eq_u32 vcc, -1, v215\n\t" \
    "s_cbranch_vccz .Ls17_%=\n\t" \
    "s_mov_b32 m0, 0xb800\n\t" \
    "s_nop 0\n\t" \
    "buffer_load_dwordx4 v253, s[60:63], s47 offen sc1 lds\n\t" \
    ".Ls17_%=:\n\t" \
    "v_max_u32 v215, v232, v233\n\t" \
    "v_max3_u32 v215, v234, v235, v215\n\t" \
    "v_cmp_eq_u32 vcc, -1, v215\n\t" \
    "s_cbranch_vccz .Ls18_%=\n\t" \
    "s_mov_b32 m0, 0xac00\n\t" \
    "s_nop 0\n\t" \
    "buffer_load_dwordx4 v253, s[60:63], s48 offen sc1 lds\n\t" \
    ".Ls18_%=:\n\t" \
    "s_cmp_eq_u32 s41, 0\n\t" \
    "s_cbranch_scc1 .Ls15_%=\n\t" \
    "s_sub_u32 s41, s41, 1\n\t" \
    "s_branch .Ls14_%=\n\t" \
    ".Ls15_%=:\n\t" \
    "s_setprio 3\n\t" \
    "s_waitcnt lgkmcnt(0)\n\t" \
    "s_barrier \n\t" \
    "ds_read_b128 v[236:239], v253 offset:40960\n\t" \
    "ds_read_b128 v[240:243], v253 offset:46080\n\t" \
    "v_smfmac_f32_16x16x64_bf16 v[244:247], v[224:227], v[0:7], v212\n\t" \
    "v_smfmac_f32_16x16x64_bf16 v[248:251], v[224:227], v[8:15], v212\n\t" \
    "v_smfmac_f32_16x16x64_bf16 v[208:211], v[224:227], v[200:207], v212\n\t" \
    "ds_read_b128 v[200:203], v254 offset:45056\n\t" \
    "ds_read_b128 v[204:207], v254 offset:46080\n\t" \
    "ds_read_b128 v[224:227], v253 offset:41984\n\t" \
    "v_smfmac_f32_16x16x64_bf16 v[208:211], v[228:231], v[16:23], v212\n\t" \
    "v_smfmac_f32_16x16x64_bf16 v[244:247], v[228:231], v[24:31], v212\n\t" \
    "v_smfmac_f32_16x16x64_bf16 v[248:251], v[228:231], v[32:39], v212\n\t" \
    "ds_read_b128 v[228:231], v253 offset:48128\n\t" \
    "v_smfmac_f32_16x16x64_bf16 v[208:211], v[232:235], v[216:223], v212\n\t" \
    "ds_read_b128 v[216:219], v254 offset:47104\n\t" \
    "ds_read_b128 v[220:223], v254 offset:48128\n\t" \
    "v_smfmac_f32_16x16x64_bf16 v[244:247], v[232:235], v[40:47], v212\n\t" \
    "v_smfmac_f32_16x16x64_bf16 v[248:251], v[232:235], v[48:55], v212\n\t" \
    "ds_read_b128 v[232:235], v253 offset:45056\n\t" \
    "s_waitcnt lgkmcnt(8)\n\t" \
    "v_smfmac_f32_16x16x64_bf16 v[208:211], v[236:239], v[56:63], v212\n\t" \
    "v_smfmac_f32_16x16x64_bf16 v[244:247], v[236:239], v[64:71], v212\n\t" \
    "v_smfmac_f32_16x16x64_bf16 v[248:251], v[236:239], v[72:79], v212\n\t" \
    "ds_read_b128 v[236:239], v253 offset:49152\n\t" \
    "s_waitcnt lgkmcnt(6)\n\t" \
    "v_smfmac_f32_16x16x64_bf16 v[208:211], v[240:243], v[200:207], v212\n\t" \
    "ds_read_b128 v[200:203], v254 offset:49152\n\t" \
    "ds_read_b128 v[204:207], v254 offset:50176\n\t" \
    "v_smfmac_f32_16x16x64_bf16 v[244:247], v[240:243], v[80:87], v212\n\t" \
    "v_smfmac_f32_16x16x64_bf16 v[248:251], v[240:243], v[88:95], v212\n\t" \
    "ds_read_b128 v[240:243], v253 offset:50176\n\t" \
    "s_waitcnt lgkmcnt(8)\n\t" \
    "v_smfmac_f32_16x16x64_bf16 v[208:211], v[224:227], v[96:103], v212\n\t" \
    "v_smfmac_f32_16x16x64_bf16 v[244:247], v[224:227], v[104:111], v212\n\t" \
    "v_smfmac_f32_16x16x64_bf16 v[248:251], v[224:227], v[112:119], v212\n\t" \
    "s_waitcnt lgkmcnt(5)\n\t" \
    "v_smfmac_f32_16x16x64_bf16 v[208:211], v[228:231], v[216:223], v212\n\t" \
    "s_waitcnt lgkmcnt(4)\n\t" \
    "v_smfmac_f32_16x16x64_bf16 v[208:211], v[232:235], v[136:143], v212\n\t" \
    "s_waitcnt lgkmcnt(1)\n\t" \
    "v_smfmac_f32_16x16x64_bf16 v[208:211], v[236:239], v[200:207], v212\n\t" \
    "s_waitcnt lgkmcnt(0)\n\t" \
    "v_smfmac_f32_16x16x64_bf16 v[208:211], v[240:243], v[176:183], v212\n\t" \
    "v_smfmac_f32_16x16x64_bf16 v[244:247], v[228:231], v[120:127], v212\n\t" \
    "v_smfmac_f32_16x16x64_bf16 v[244:247], v[232:235], v[144:151], v212\n\t" \
    "v_smfmac_f32_16x16x64_bf16 v[244:247], v[236:239], v[160:167], v212\n\t" \
    "v_add_f32 v208, v208, v210\n\t" \
    "v_add_f32 v209, v209, v211\n\t" \
    "v_cvt_pk_bf16_f32 v208, v208, v209\n\t" \
    "v_smfmac_f32_16x16x64_bf16 v[244:247], v[240:243], v[184:191], v212\n\t" \
    "ds_write_b16 v213, v208 offset:0\n\t" \
    "ds_write_b16_d16_hi v213, v208 offset:16\n\t" \
    "v_smfmac_f32_16x16x64_bf16 v[248:251], v[228:231], v[128:135], v212\n\t" \
    "v_smfmac_f32_16x16x64_bf16 v[248:251], v[232:235], v[152:159], v212\n\t" \
    "v_smfmac_f32_16x16x64_bf16 v[248:251], v[236:239], v[168:175], v212\n\t" \
    "v_add_f32 v244, v244, v246\n\t" \
    "v_add_f32 v245, v245, v247\n\t" \
    "v_smfmac_f32_16x16x64_bf16 v[248:251], v[240:243], v[192:199], v212\n\t" \
    "s_nop 7\n\t" \
    "v_add_f32 v248, v248, v250\n\t" \
    "v_add_f32 v249, v249, v251\n\t" \
    "v_cvt_pk_bf16_f32 v244, v244, v248\n\t" \
    "v_cvt_pk_bf16_f32 v245, v245, v249\n\t" \
    "ds_write2_b32 v214, v244, v245 offset1:4\n\t" \
    "s_setprio 0\n\t" \
    "s_sleep 1\n\t" \
    "ds_read_b128 v[200:203], v254 offset:6144\n\t" \
    "ds_read_b128 v[224:227], v254 offset:18432\n\t" \
    "ds_read_b128 v[204:207], v254 offset:7168\n\t" \
    "ds_read_b128 v[228:231], v254 offset:19456\n\t" \
    "ds_read_b128 v[208:211], v254 offset:8192\n\t" \
    "ds_read_b128 v[232:235], v254 offset:20480\n\t" \
    "ds_read_b128 v[212:215], v254 offset:9216\n\t" \
    "ds_read_b128 v[236:239], v254 offset:21504\n\t" \
    "ds_read_b128 v[216:219], v254 offset:10240\n\t" \
    "ds_read_b128 v[220:223], v254 offset:11264\n\t" \
    "s_and_b32 s44, s40, 3\n\t" \
    "s_lshl_b32 s44, s44, 17\n\t" \
    "s_add_u32 s50, s44, %[pb]\n\t" \
    "s_add_u32 s51, s44, %[pm]\n\t" \
    "s_mov_b32 s52, 0xf0000\n\t" \
    "s_mov_b32 s53, 0xfff00ff\n\t" \
    "s_mov_b32 s54, 0x1f0001\n\t" \
    "s_mov_b32 s55, 0x1fff01ff\n\t" \
    "s_mov_b32 s56, 0x3f0003\n\t" \
    "s_mov_b32 s57, 0x3fff03ff\n\t" \
    "s_mov_b32 s58, 0x7f0007\n\t" \
    "s_mov_b32 s59, 0x7fff07ff\n\t" \
    "s_waitcnt lgkmcnt(8)\n\t" \
    "v_mfma_f32_16x16x32_bf16 v[240:243], v[224:227], v[200:203], 0\n\t" \
    "ds_read_b128 v[224:227], v254 offset:22528\n\t" \
    "s_waitcnt lgkmcnt(7)\n\t" \
    "v_mfma_f32_16x16x32_bf16 v[240:243], v[228:231], v[204:207], v[240:243]\n\t" \
    "ds_read_b128 v[228:231], v254 offset:23552\n\t" \
    "s_waitcnt lgkmcnt(6)\n\t" \
    "v_mfma_f32_16x16x32_bf16 v[240:243], v[232:235], v[208:211], v[240:243]\n\t" \
    "s_waitcnt lgkmcnt(4)\n\t" \
    "v_mfma_f32_16x16x32_bf16 v[240:243], v[236:239], v[212:215], v[240:243]\n\t" \
    "v_and_b32 v252, 0xffffff7f, v254\n\t" \
    "s_waitcnt lgkmcnt(0)\n\t" \
    "s_barrier \n\t" \
    "ds_read_b128 v[232:235], v252 offset:3072\n\t" \
    "ds_read_b128 v[236:239], v252 offset:3200\n\t" \
    "v_mfma_f32_16x16x32_bf16 v[240:243], v[224:227], v[216:219], v[240:243]\n\t" \
    "ds_read_b128 v[224:227], v252 offset:4096\n\t" \
    "v_mfma_f32_16x16x32_bf16 v[240:243], v[228:231], v[220:223], v[240:243]\n\t" \
    "ds_read_b128 v[228:231], v252 offset:4224\n\t" \
    "s_waitcnt lgkmcnt(3)\n\t" \
    "v_mfma_f32_16x16x32_bf16 v[248:251], v[232:235], v[200:203], 0\n\t" \
    "ds_read_b128 v[232:235], v252 offset:5120\n\t" \
    "s_waitcnt lgkmcnt(3)\n\t" \
    "v_mfma_f32_16x16x32_bf16 v[248:251], v[236:239], v[204:207], v[248:251]\n\t" \
    "ds_read_b128 v[236:239], v252 offset:5248\n\t" \
    "s_waitcnt lgkmcnt(3)\n\t" \
    "v_mfma_f32_16x16x32_bf16 v[248:251], v[224:227], v[208:211], v[248:251]\n\t" \
    "s_waitcnt lgkmcnt(2)\n\t" \
    "v_mfma_f32_16x16x32_bf16 v[248:251], v[228:231], v[212:215], v[248:251]\n\t" \
    "s_waitcnt lgkmcnt(1)\n\t" \
    "v_mfma_f32_16x16x32_bf16 v[248:251], v[232:235], v[216:219], v[248:251]\n\t" \
    "s_waitcnt lgkmcnt(0)\n\t" \
    "v_mfma_f32_16x16x32_bf16 v[248:251], v[236:239], v[220:223], v[248:251]\n\t" \
    "v_mov_b32 v226, 0xff7fffff\n\t" \
    "v_cndmask_b32 v240, v240, v226, s[52:53]\n\t" \
    "v_cndmask_b32 v241, v241, v226, s[54:55]\n\t" \
    "v_cndmask_b32 v242, v242, v226, s[56:57]\n\t" \
    "v_cndmask_b32 v243, v243, v226, s[58:59]\n\t" \
    "v_max3_f32 v224, v240, v241, v242\n\t" \
    "v_mov_b32 v225, v243\n\t" \
    "v_lshrrev_b32 v200, 5, v253\n\t" \
    "v_and_b32 v201, 0xf0, v253\n\t" \
    "v_and_or_b32 v200, v200, 8, v201\n\t" \
    "v_max3_f32 v224, v224, v248, v249\n\t" \
    "v_max3_f32 v225, v225, v250, v251\n\t" \
    "v_max_f32 v224, v224, v225\n\t" \
    "v_mov_b32 v225, v224\n\t" \
    "s_nop 1\n\t" \
    "v_permlane16_swap_b32 v224, v225\n\t" \
    "v_max_f32 v224, v224, v225\n\t" \
    "v_mov_b32 v225, v224\n\t" \
    "s_nop 1\n\t" \
    "v_permlane32_swap_b32 v224, v225\n\t" \
    "v_max_f32 v224, v224, v225\n\t" \
    "v_mul_f32 v224, %[scl], v224\n\t" \
    "v_fma_f32 v240, v240, %[scl], -v224\n\t" \
    "v_fma_f32 v241, v241, %[scl], -v224\n\t" \
    "v_exp_f32 v240, v240\n\t" \
    "v_fma_f32 v242, v242, %[scl], -v224\n\t" \
    "v_exp_f32 v241, v241\n\t" \
    "v_fma_f32 v243, v243, %[scl], -v224\n\t" \
    "v_exp_f32 v242, v242\n\t" \
    "v_fma_f32 v248, v248, %[scl], -v224\n\t" \
    "v_exp_f32 v243, v243\n\t" \
    "v_fma_f32 v249, v249, %[scl], -v224\n\t" \
    "v_exp_f32 v248, v248\n\t" \
    "v_fma_f32 v250, v250, %[scl], -v224\n\t" \
    "v_exp_f32 v249, v249\n\t" \
    "v_fma_f32 v251, v251, %[scl], -v224\n\t" \
    "v_exp_f32 v250, v250\n\t" \
    "v_exp_f32 v251, v251\n\t" \
    "v_add_f32 v224, v240, v241\n\t" \
    "v_add_f32 v225, v242, v243\n\t" \
    "v_add_f32 v232, v248, v249\n\t" \
    "v_add_f32 v233, v250, v251\n\t" \
    "v_add_f32 v224, v224, v225\n\t" \
    "v_add_f32 v232, v232, v233\n\t" \
    "v_fma_f32 v224, v232, 0.5, v224\n\t" \
    "v_mov_b32 v225, v224\n\t" \
    "s_nop 1\n\t" \
    "v_permlane16_swap_b32 v224, v225\n\t" \
    "v_add_f32 v224, v224, v225\n\t" \
    "v_mov_b32 v225, v224\n\t" \
    "s_nop 1\n\t" \
    "v_permlane32_swap_b32 v224, v225\n\t" \
    "v_add_f32 v224, v224, v225\n\t" \
    "v_rcp_f32 v224, v224\n\t" \
    "s_nop 0\n\t" \
    "v_mul_f32 v240, v240, v224\n\t" \
    "v_mul_f32 v241, v241, v224\n\t" \
    "v_mul_f32 v242, v242, v224\n\t" \
    "v_mul_f32 v243, v243, v224\n\t" \
    "v_mul_f32 v248, v248, v224\n\t" \
    "v_mul_f32 v249, v249, v224\n\t" \
    "v_mul_f32 v250, v250, v224\n\t" \
    "v_mul_f32 v251, v251, v224\n\t" \
    "v_cvt_pk_bf16_f32 v204, |v240|, |v241|\n\t" \
    "v_cvt_pk_bf16_f32 v205, |v242|, |v243|\n\t" \
    "v_cvt_pk_bf16_f32 v208, |v248|, |v249|\n\t" \
    "v_cvt_pk_bf16_f32 v209, |v250|, |v251|\n\t" \
    "v_permlane16_swap_b32 v204, v206\n\t" \
    "v_permlane16_swap_b32 v205, v207\n\t" \
    "s_mov_b32 exec_lo, 0xffff\n\t" \
    "s_mov_b32 exec_hi, 0xffff\n\t" \
    "buffer_store_dwordx4 v[204:207], v253, s[60:63], s50 offen sc1\n\t" \
    "s_mov_b32 exec_lo, -1\n\t" \
    "s_mov_b32 exec_hi, 0\n\t" \
    "buffer_store_dwordx2 v[208:209], v200, s[60:63], s51 offen sc1\n\t" \
    "s_mov_b32 exec_hi, -1\n\t" \
    "ds_read_b128 v[224:227], v253 offset:0\n\t" \
    "ds_read_b128 v[244:247], v254 offset:40960\n\t" \
    "ds_read_b128 v[228:231], v253 offset:1024\n\t" \
    "ds_read_b128 v[232:235], v253 offset:20480\n\t" \
    "ds_read_b128 v[236:239], v253 offset:21504\n\t" \
    "ds_read_b128 v[248:251], v254 offset:41984\n\t" \
    "s_add_u32 s40, s40, 1\n\t" \
    "s_cmp_lg_u32 s40, 64\n\t" \
    "s_cbranch_scc1 .Ls13_%=\n\t" \
    "s_branch .Lend%=\n\t" \
    ".Ls0_%=:\n\t" \
    "v_add_u32 v244, 0x18000, v243\n\t" \
    "s_waitcnt vmcnt(12) lgkmcnt(0)\n\t" \
    "s_barrier \n\t" \
    "s_add_u32 m0, %[wm], 0x12000\n\t" \
    "s_mov_b32 s44, 0x1e0000\n\t" \
    "buffer_load_dwordx4 v240, %[wr], s44 offen offset:0 nt lds\n\t" \
    "buffer_load_dwordx4 v241, %[wr], s44 offen offset:1024 nt lds\n\t" \
    "buffer_load_dwordx4 v242, %[wr], s44 offen offset:2048 nt lds\n\t" \
    "ds_read_b64_tr_b16 v[200:201], v244 offset:0\n\t" \
    "ds_read_b64_tr_b16 v[202:203], v244 offset:1536\n\t" \
    "ds_read_b64_tr_b16 v[0:1], v244 offset:32\n\t" \
    "ds_read_b64_tr_b16 v[2:3], v244 offset:1568\n\t" \
    "ds_read_b64_tr_b16 v[8:9], v244 offset:64\n\t" \
    "ds_read_b64_tr_b16 v[10:11], v244 offset:1600\n\t" \
    "v_add_u32 v244, 0x1e000, v243\n\t" \
    "s_waitcnt vmcnt(12) lgkmcnt(0)\n\t" \
    "s_barrier \n\t" \
    "s_add_u32 m0, %[wm], 0x18000\n\t" \
    "s_mov_b32 s44, 0x240000\n\t" \
    "buffer_load_dwordx4 v240, %[wr], s44 offen offset:0 nt lds\n\t" \
    "buffer_load_dwordx4 v241, %[wr], s44 offen offset:1024 nt lds\n\t" \
    "buffer_load_dwordx4 v242, %[wr], s44 offen offset:2048 nt lds\n\t" \
    "ds_read_b64_tr_b16 v[204:205], v244 offset:0\n\t" \
    "ds_read_b64_tr_b16 v[206:207], v244 offset:1536\n\t" \
    "ds_read_b64_tr_b16 v[4:5], v244 offset:32\n\t" \
    "ds_read_b64_tr_b16 v[6:7], v244 offset:1568\n\t" \
    "ds_read_b64_tr_b16 v[12:13], v244 offset:64\n\t" \
    "ds_read_b64_tr_b16 v[14:15], v244 offset:1600\n\t" \
    "v_add_u32 v244, 0x0, v243\n\t" \
    "s_waitcnt vmcnt(12) lgkmcnt(0)\n\t" \
    "s_barrier \n\t" \
    "s_add_u32 m0, %[wm], 0x1e000\n\t" \
    "s_mov_b32 s44, 0x2a0000\n\t" \
    "buffer_load_dwordx4 v240, %[wr], s44 offen offset:0 nt lds\n\t" \
    "buffer_load_dwordx4 v241, %[wr], s44 offen offset:1024 nt lds\n\t" \
    "buffer_load_dwordx4 v242, %[wr], s44 offen offset:2048 nt lds\n\t" \
    "ds_read_b64_tr_b16 v[208:209], v244 offset:0\n\t" \
    "ds_read_b64_tr_b16 v[210:211], v244 offset:1536\n\t" \
    "ds_read_b64_tr_b16 v[40:41], v244 offset:32\n\t" \
    "ds_read_b64_tr_b16 v[42:43], v244 offset:1568\n\t" \
    "ds_read_b64_tr_b16 v[48:49], v244 offset:64\n\t" \
    "ds_read_b64_tr_b16 v[50:51], v244 offset:1600\n\t" \
    "v_add_u32 v244, 0x6000, v243\n\t" \
    "s_waitcnt vmcnt(12) lgkmcnt(0)\n\t" \
    "s_barrier \n\t" \
    "s_add_u32 m0, %[wm], 0x0\n\t" \
    "s_mov_b32 s44, 0x300000\n\t" \
    "buffer_load_dwordx4 v240, %[wr], s44 offen offset:0 nt lds\n\t" \
    "buffer_load_dwordx4 v241, %[wr], s44 offen offset:1024 nt lds\n\t" \
    "buffer_load_dwordx4 v242, %[wr], s44 offen offset:2048 nt lds\n\t" \
    "ds_read_b64_tr_b16 v[212:213], v244 offset:0\n\t" \
    "ds_read_b64_tr_b16 v[214:215], v244 offset:1536\n\t" \
    "ds_read_b64_tr_b16 v[44:45], v244 offset:32\n\t" \
    "ds_read_b64_tr_b16 v[46:47], v244 offset:1568\n\t" \
    "ds_read_b64_tr_b16 v[52:53], v244 offset:64\n\t" \
    "ds_read_b64_tr_b16 v[54:55], v244 offset:1600\n\t" \
    "v_add_u32 v244, 0xc000, v243\n\t" \
    "s_waitcnt vmcnt(12) lgkmcnt(0)\n\t" \
    "s_barrier \n\t" \
    "s_add_u32 m0, %[wm], 0x6000\n\t" \
    "s_mov_b32 s44, 0x360000\n\t" \
    "buffer_load_dwordx4 v240, %[wr], s44 offen offset:0 nt lds\n\t" \
    "buffer_load_dwordx4 v241, %[wr], s44 offen offset:1024 nt lds\n\t" \
    "buffer_load_dwordx4 v242, %[wr], s44 offen offset:2048 nt lds\n\t" \
    "ds_read_b64_tr_b16 v[216:217], v244 offset:0\n\t" \
    "ds_read_b64_tr_b16 v[218:219], v244 offset:1536\n\t" \
    "ds_read_b64_tr_b16 v[80:81], v244 offset:32\n\t" \
    "ds_read_b64_tr_b16 v[82:83], v244 offset:1568\n\t" \
    "ds_read_b64_tr_b16 v[88:89], v244 offset:64\n\t" \
    "ds_read_b64_tr_b16 v[90:91], v244 offset:1600\n\t" \
    "v_add_u32 v244, 0x12000, v243\n\t" \
    "s_waitcnt vmcnt(12) lgkmcnt(0)\n\t" \
    "s_barrier \n\t" \
    "s_add_u32 m0, %[wm], 0xc000\n\t" \
    "s_mov_b32 s44, 0x3c0000\n\t" \
    "buffer_load_dwordx4 v240, %[wr], s44 offen offset:0 nt lds\n\t" \
    "buffer_load_dwordx4 v241, %[wr], s44 offen offset:1024 nt lds\n\t" \
    "buffer_load_dwordx4 v242, %[wr], s44 offen offset:2048 nt lds\n\t" \
    "ds_read_b64_tr_b16 v[220:221], v244 offset:0\n\t" \
    "ds_read_b64_tr_b16 v[222:223], v244 offset:1536\n\t" \
    "ds_read_b64_tr_b16 v[84:85], v244 offset:32\n\t" \
    "ds_read_b64_tr_b16 v[86:87], v244 offset:1568\n\t" \
    "ds_read_b64_tr_b16 v[92:93], v244 offset:64\n\t" \
    "ds_read_b64_tr_b16 v[94:95], v244 offset:1600\n\t" \
    "v_add_u32 v244, 0x18000, v243\n\t" \
    "s_waitcnt vmcnt(12) lgkmcnt(0)\n\t" \
    "s_barrier \n\t" \
    "s_add_u32 m0, %[wm], 0x12000\n\t" \
    "s_mov_b32 s44, 0x420000\n\t" \
    "buffer_load_dwordx4 v240, %[wr], s44 offen offset:0 nt lds\n\t" \
    "buffer_load_dwordx4 v241, %[wr], s44 offen offset:1024 nt lds\n\t" \
    "buffer_load_dwordx4 v242, %[wr], s44 offen offset:2048 nt lds\n\t" \
    "ds_read_b64_tr_b16 v[224:225], v244 offset:0\n\t" \
    "ds_read_b64_tr_b16 v[226:227], v244 offset:1536\n\t" \
    "ds_read_b64_tr_b16 v[120:121], v244 offset:32\n\t" \
    "ds_read_b64_tr_b16 v[122:123], v244 offset:1568\n\t" \
    "ds_read_b64_tr_b16 v[128:129], v244 offset:64\n\t" \
    "ds_read_b64_tr_b16 v[130:131], v244 offset:1600\n\t" \
    "v_add_u32 v244, 0x1e000, v243\n\t" \
    "s_waitcnt vmcnt(12) lgkmcnt(0)\n\t" \
    "s_barrier \n\t" \
    "s_add_u32 m0, %[wm], 0x18000\n\t" \
    "s_mov_b32 s44, 0x480000\n\t" \
    "buffer_load_dwordx4 v240, %[wr], s44 offen offset:0 nt lds\n\t" \
    "buffer_load_dwordx4 v241, %[wr], s44 offen offset:1024 nt lds\n\t" \
    "buffer_load_dwordx4 v242, %[wr], s44 offen offset:2048 nt lds\n\t" \
    "ds_read_b64_tr_b16 v[228:229], v244 offset:0\n\t" \
    "ds_read_b64_tr_b16 v[230:231], v244 offset:1536\n\t" \
    "ds_read_b64_tr_b16 v[124:125], v244 offset:32\n\t" \
    "ds_read_b64_tr_b16 v[126:127], v244 offset:1568\n\t" \
    "ds_read_b64_tr_b16 v[132:133], v244 offset:64\n\t" \
    "ds_read_b64_tr_b16 v[134:135], v244 offset:1600\n\t" \
    "v_add_u32 v244, 0x0, v243\n\t" \
    "s_waitcnt vmcnt(12) lgkmcnt(0)\n\t" \
    "s_barrier \n\t" \
    "v_mov_b32 v246, 0\n\t" \
    "buffer_load_dword v246, v246, s[60:63], %[mo] offen sc1\n\t" \
    "s_add_u32 m0, %[wm], 0x1e000\n\t" \
    "s_mov_b32 s44, 0x4e0000\n\t" \
    "buffer_load_dwordx4 v240, %[wr], s44 offen offset:0 nt lds\n\t" \
    "buffer_load_dwordx4 v241, %[wr], s44 offen offset:1024 nt lds\n\t" \
    "buffer_load_dwordx4 v242, %[wr], s44 offen offset:2048 nt lds\n\t" \
    "ds_read_b64_tr_b16 v[232:233], v244 offset:0\n\t" \
    "ds_read_b64_tr_b16 v[234:235], v244 offset:1536\n\t" \
    "ds_read_b64_tr_b16 v[160:161], v244 offset:32\n\t" \
    "ds_read_b64_tr_b16 v[162:163], v244 offset:1568\n\t" \
    "ds_read_b64_tr_b16 v[168:169], v244 offset:64\n\t" \
    "ds_read_b64_tr_b16 v[170:171], v244 offset:1600\n\t" \
    "v_add_u32 v244, 0x6000, v243\n\t" \
    "s_waitcnt vmcnt(12) lgkmcnt(0)\n\t" \
    "s_barrier \n\t" \
    "s_add_u32 m0, %[wm], 0x0\n\t" \
    "s_mov_b32 s44, 0x540000\n\t" \
    "buffer_load_dwordx4 v240, %[wr], s44 offen offset:0 nt lds\n\t" \
    "buffer_load_dwordx4 v241, %[wr], s44 offen offset:1024 nt lds\n\t" \
    "buffer_load_dwordx4 v242, %[wr], s44 offen offset:2048 nt lds\n\t" \
    "ds_read_b64_tr_b16 v[236:237], v244 offset:0\n\t" \
    "ds_read_b64_tr_b16 v[238:239], v244 offset:1536\n\t" \
    "ds_read_b64_tr_b16 v[164:165], v244 offset:32\n\t" \
    "ds_read_b64_tr_b16 v[166:167], v244 offset:1568\n\t" \
    "ds_read_b64_tr_b16 v[172:173], v244 offset:64\n\t" \
    "ds_read_b64_tr_b16 v[174:175], v244 offset:1600\n\t" \
    "v_add_u32 v244, 0xc000, v243\n\t" \
    "s_waitcnt vmcnt(12) lgkmcnt(0)\n\t" \
    "s_barrier \n\t" \
    "s_add_u32 m0, %[wm], 0x6000\n\t" \
    "s_mov_b32 s44, 0x5a0000\n\t" \
    "buffer_load_dwordx4 v240, %[wr], s44 offen offset:0 nt lds\n\t" \
    "buffer_load_dwordx4 v241, %[wr], s44 offen offset:1024 nt lds\n\t" \
    "buffer_load_dwordx4 v242, %[wr], s44 offen offset:2048 nt lds\n\t" \
    "ds_read_b64_tr_b16 v[16:17], v244 offset:0\n\t" \
    "ds_read_b64_tr_b16 v[18:19], v244 offset:1536\n\t" \
    "ds_read_b64_tr_b16 v[24:25], v244 offset:32\n\t" \
    "ds_read_b64_tr_b16 v[26:27], v244 offset:1568\n\t" \
    "ds_read_b64_tr_b16 v[32:33], v244 offset:64\n\t" \
    "ds_read_b64_tr_b16 v[34:35], v244 offset:1600\n\t" \
    "v_add_u32 v244, 0x12000, v243\n\t" \
    "s_waitcnt vmcnt(12) lgkmcnt(0)\n\t" \
    "s_barrier \n\t" \
    "s_add_u32 m0, %[wm], 0xc000\n\t" \
    "s_mov_b32 s44, 0x600000\n\t" \
    "buffer_load_dwordx4 v240, %[wr], s44 offen offset:0 nt lds\n\t" \
    "buffer_load_dwordx4 v241, %[wr], s44 offen offset:1024 nt lds\n\t" \
    "buffer_load_dwordx4 v242, %[wr], s44 offen offset:2048 nt lds\n\t" \
    "ds_read_b64_tr_b16 v[20:21], v244 offset:0\n\t" \
    "ds_read_b64_tr_b16 v[22:23], v244 offset:1536\n\t" \
    "ds_read_b64_tr_b16 v[28:29], v244 offset:32\n\t" \
    "ds_read_b64_tr_b16 v[30:31], v244 offset:1568\n\t" \
    "ds_read_b64_tr_b16 v[36:37], v244 offset:64\n\t" \
    "ds_read_b64_tr_b16 v[38:39], v244 offset:1600\n\t" \
    "v_add_u32 v244, 0x18000, v243\n\t" \
    "s_waitcnt vmcnt(12) lgkmcnt(0)\n\t" \
    "s_barrier \n\t" \
    "s_add_u32 m0, %[wm], 0x12000\n\t" \
    "s_mov_b32 s44, 0x660000\n\t" \
    "buffer_load_dwordx4 v240, %[wr], s44 offen offset:0 nt lds\n\t" \
    "buffer_load_dwordx4 v241, %[wr], s44 offen offset:1024 nt lds\n\t" \
    "buffer_load_dwordx4 v242, %[wr], s44 offen offset:2048 nt lds\n\t" \
    "ds_read_b64_tr_b16 v[56:57], v244 offset:0\n\t" \
    "ds_read_b64_tr_b16 v[58:59], v244 offset:1536\n\t" \
    "ds_read_b64_tr_b16 v[64:65], v244 offset:32\n\t" \
    "ds_read_b64_tr_b16 v[66:67], v244 offset:1568\n\t" \
    "ds_read_b64_tr_b16 v[72:73], v244 offset:64\n\t" \
    "ds_read_b64_tr_b16 v[74:75], v244 offset:1600\n\t" \
    "v_add_u32 v244, 0x1e000, v243\n\t" \
    "s_waitcnt vmcnt(12) lgkmcnt(0)\n\t" \
    "s_barrier \n\t" \
    ".Ls01_%=:\n\t" \
    "v_readfirstlane_b32 s43, v246\n\t" \
    "s_nop 0\n\t" \
    "s_cmp_lg_u32 s43, -1\n\t" \
    "s_cbranch_scc1 .Ls02_%=\n\t" \
    "s_cmp_eq_u32 s41, 0\n\t" \
    "s_cbranch_scc1 .Ls02_%=\n\t" \
    "s_sub_u32 s41, s41, 1\n\t" \
    "s_sleep 4\n\t" \
    "v_mov_b32 v246, 0\n\t" \
    "buffer_load_dword v246, v246, s[60:63], %[mo] offen sc1\n\t" \
    "s_waitcnt vmcnt(0)\n\t" \
    "s_branch .Ls01_%=\n\t" \
    ".Ls02_%=:\n\t" \
    "v_mov_b32 v246, 0x8000\n\t" \
    "s_mov_b64 exec, 1\n\t" \
    "buffer_store_dword v246, v246, s[60:63], %[mo] offen sc1\n\t" \
    "s_mov_b64 exec, -1\n\t" \
    "s_and_b32 s44, s43, 0x5000\n\t" \
    "s_xor_b32 %[mb], %[mb], s44\n\t" \
    "s_xor_b32 %[fl], %[fl], s44\n\t" \
    "s_xor_b32 %[pb], %[pb], s44\n\t" \
    "s_and_b32 s43, s43, 0x2000\n\t" \
    "s_xor_b32 s43, s43, %[lx]\n\t" \
    "s_add_u32 s60, s60, s43\n\t" \
    "s_addc_u32 s61, s61, 0\n\t" \
    "s_add_u32 m0, %[wm], 0x18000\n\t" \
    "s_mov_b32 s44, 0x6c0000\n\t" \
    "buffer_load_dwordx4 v240, %[wr], s44 offen offset:0 nt lds\n\t" \
    "buffer_load_dwordx4 v241, %[wr], s44 offen offset:1024 nt lds\n\t" \
    "buffer_load_dwordx4 v242, %[wr], s44 offen offset:2048 nt lds\n\t" \
    "ds_read_b64_tr_b16 v[60:61], v244 offset:0\n\t" \
    "ds_read_b64_tr_b16 v[62:63], v244 offset:1536\n\t" \
    "ds_read_b64_tr_b16 v[68:69], v244 offset:32\n\t" \
    "ds_read_b64_tr_b16 v[70:71], v244 offset:1568\n\t" \
    "ds_read_b64_tr_b16 v[76:77], v244 offset:64\n\t" \
    "ds_read_b64_tr_b16 v[78:79], v244 offset:1600\n\t" \
    "v_add_u32 v244, 0x0, v243\n\t" \
    "s_waitcnt vmcnt(12) lgkmcnt(0)\n\t" \
    "s_barrier \n\t" \
    "s_add_u32 m0, %[wm], 0x1e000\n\t" \
    "s_mov_b32 s44, 0x720000\n\t" \
    "buffer_load_dwordx4 v240, %[wr], s44 offen offset:0 nt lds\n\t" \
    "buffer_load_dwordx4 v241, %[wr], s44 offen offset:1024 nt lds\n\t" \
    "buffer_load_dwordx4 v242, %[wr], s44 offen offset:2048 nt lds\n\t" \
    "ds_read_b64_tr_b16 v[96:97], v244 offset:0\n\t" \
    "ds_read_b64_tr_b16 v[98:99], v244 offset:1536\n\t" \
    "ds_read_b64_tr_b16 v[104:105], v244 offset:32\n\t" \
    "ds_read_b64_tr_b16 v[106:107], v244 offset:1568\n\t" \
    "ds_read_b64_tr_b16 v[112:113], v244 offset:64\n\t" \
    "ds_read_b64_tr_b16 v[114:115], v244 offset:1600\n\t" \
    "v_add_u32 v244, 0x6000, v243\n\t" \
    "s_waitcnt vmcnt(12) lgkmcnt(0)\n\t" \
    "s_barrier \n\t" \
    "ds_read_b64_tr_b16 v[100:101], v244 offset:0\n\t" \
    "ds_read_b64_tr_b16 v[102:103], v244 offset:1536\n\t" \
    "ds_read_b64_tr_b16 v[108:109], v244 offset:32\n\t" \
    "ds_read_b64_tr_b16 v[110:111], v244 offset:1568\n\t" \
    "ds_read_b64_tr_b16 v[116:117], v244 offset:64\n\t" \
    "ds_read_b64_tr_b16 v[118:119], v244 offset:1600\n\t" \
    "v_add_u32 v244, 0xc000, v243\n\t" \
    "s_waitcnt vmcnt(9) lgkmcnt(0)\n\t" \
    "s_barrier \n\t" \
    "ds_read_b64_tr_b16 v[136:137], v244 offset:0\n\t" \
    "ds_read_b64_tr_b16 v[138:139], v244 offset:1536\n\t" \
    "ds_read_b64_tr_b16 v[144:145], v244 offset:32\n\t" \
    "ds_read_b64_tr_b16 v[146:147], v244 offset:1568\n\t" \
    "ds_read_b64_tr_b16 v[152:153], v244 offset:64\n\t" \
    "ds_read_b64_tr_b16 v[154:155], v244 offset:1600\n\t" \
    "v_add_u32 v244, 0x12000, v243\n\t" \
    "s_waitcnt vmcnt(6) lgkmcnt(0)\n\t" \
    "s_barrier \n\t" \
    "ds_read_b64_tr_b16 v[140:141], v244 offset:0\n\t" \
    "ds_read_b64_tr_b16 v[142:143], v244 offset:1536\n\t" \
    "ds_read_b64_tr_b16 v[148:149], v244 offset:32\n\t" \
    "ds_read_b64_tr_b16 v[150:151], v244 offset:1568\n\t" \
    "ds_read_b64_tr_b16 v[156:157], v244 offset:64\n\t" \
    "ds_read_b64_tr_b16 v[158:159], v244 offset:1600\n\t" \
    "v_add_u32 v244, 0x18000, v243\n\t" \
    "s_waitcnt vmcnt(3) lgkmcnt(0)\n\t" \
    "s_barrier \n\t" \
    "ds_read_b64_tr_b16 v[176:177], v244 offset:0\n\t" \
    "ds_read_b64_tr_b16 v[178:179], v244 offset:1536\n\t" \
    "ds_read_b64_tr_b16 v[184:185], v244 offset:32\n\t" \
    "ds_read_b64_tr_b16 v[186:187], v244 offset:1568\n\t" \
    "ds_read_b64_tr_b16 v[192:193], v244 offset:64\n\t" \
    "ds_read_b64_tr_b16 v[194:195], v244 offset:1600\n\t" \
    "v_add_u32 v244, 0x1e000, v243\n\t" \
    "s_waitcnt vmcnt(0) lgkmcnt(0)\n\t" \
    "s_barrier \n\t" \
    "ds_read_b64_tr_b16 v[180:181], v244 offset:0\n\t" \
    "ds_read_b64_tr_b16 v[182:183], v244 offset:1536\n\t" \
    "ds_read_b64_tr_b16 v[188:189], v244 offset:32\n\t" \
    "ds_read_b64_tr_b16 v[190:191], v244 offset:1568\n\t" \
    "ds_read_b64_tr_b16 v[196:197], v244 offset:64\n\t" \
    "ds_read_b64_tr_b16 v[198:199], v244 offset:1600\n\t" \
    "s_waitcnt lgkmcnt(0)\n\t" \
    "s_barrier \n\t" \
    "ds_write_b128 v254, v[200:203] offset:30720\n\t" \
    "ds_write_b128 v254, v[204:207] offset:31744\n\t" \
    "ds_write_b128 v254, v[208:211] offset:32768\n\t" \
    "ds_write_b128 v254, v[212:215] offset:33792\n\t" \
    "ds_write_b128 v254, v[216:219] offset:34816\n\t" \
    "ds_write_b128 v254, v[220:223] offset:35840\n\t" \
    "ds_write_b128 v254, v[224:227] offset:36864\n\t" \
    "ds_write_b128 v254, v[228:231] offset:37888\n\t" \
    "ds_write_b128 v254, v[232:235] offset:38912\n\t" \
    "ds_write_b128 v254, v[236:239] offset:39936\n\t" \
    "s_waitcnt vmcnt(0) lgkmcnt(0)\n\t" \
    "s_barrier \n\t" \
    "ds_read_b128 v[224:227], v253 offset:0\n\t" \
    "ds_read_b128 v[228:231], v253 offset:1024\n\t" \
    "ds_read_b128 v[232:235], v253 offset:20480\n\t" \
    "ds_read_b128 v[236:239], v253 offset:21504\n\t" \
    ".Ls03_%=:\n\t" \
    "s_waitcnt lgkmcnt(3)\n\t" \
    "v_mfma_f32_16x16x32_bf16 v[204:207], v[0:3], v[224:227], 0\n\t" \
    "s_waitcnt lgkmcnt(2)\n\t" \
    "v_mfma_f32_16x16x32_bf16 v[204:207], v[4:7], v[228:231], v[204:207]\n\t" \
    "ds_read_b128 v[240:243], v253 offset:2048\n\t" \
    "v_mfma_f32_16x16x32_bf16 v[208:211], v[8:11], v[224:227], 0\n\t" \
    "v_mfma_f32_16x16x32_bf16 v[208:211], v[12:15], v[228:231], v[208:211]\n\t" \
    "s_waitcnt lgkmcnt(2)\n\t" \
    "v_mfma_f32_16x16x32_bf16 v[216:219], v[0:3], v[232:235], 0\n\t" \
    "ds_read_b128 v[224:227], v253 offset:3072\n\t" \
    "s_waitcnt lgkmcnt(2)\n\t" \
    "v_mfma_f32_16x16x32_bf16 v[216:219], v[4:7], v[236:239], v[216:219]\n\t" \
    "ds_read_b128 v[228:231], v253 offset:22528\n\t" \
    "v_mfma_f32_16x16x32_bf16 v[220:223], v[8:11], v[232:235], 0\n\t" \
    "v_mfma_f32_16x16x32_bf16 v[220:223], v[12:15], v[236:239], v[220:223]\n\t" \
    "s_waitcnt lgkmcnt(2)\n\t" \
    "v_mfma_f32_16x16x32_bf16 v[204:207], v[40:43], v[240:243], v[204:207]\n\t" \
    "ds_read_b128 v[232:235], v253 offset:23552\n\t" \
    "s_waitcnt lgkmcnt(2)\n\t" \
    "v_mfma_f32_16x16x32_bf16 v[204:207], v[44:47], v[224:227], v[204:207]\n\t" \
    "ds_read_b128 v[236:239], v253 offset:4096\n\t" \
    "v_mfma_f32_16x16x32_bf16 v[208:211], v[48:51], v[240:243], v[208:211]\n\t" \
    "v_mfma_f32_16x16x32_bf16 v[208:211], v[52:55], v[224:227], v[208:211]\n\t" \
    "s_waitcnt lgkmcnt(2)\n\t" \
    "v_mfma_f32_16x16x32_bf16 v[216:219], v[40:43], v[228:231], v[216:219]\n\t" \
    "ds_read_b128 v[240:243], v253 offset:5120\n\t" \
    "s_waitcnt lgkmcnt(2)\n\t" \
    "v_mfma_f32_16x16x32_bf16 v[216:219], v[44:47], v[232:235], v[216:219]\n\t" \
    "ds_read_b128 v[224:227], v253 offset:24576\n\t" \
    "v_mfma_f32_16x16x32_bf16 v[220:223], v[48:51], v[228:231], v[220:223]\n\t" \
    "v_mfma_f32_16x16x32_bf16 v[220:223], v[52:55], v[232:235], v[220:223]\n\t" \
    "s_waitcnt lgkmcnt(2)\n\t" \
    "v_mfma_f32_16x16x32_bf16 v[204:207], v[80:83], v[236:239], v[204:207]\n\t" \
    "ds_read_b128 v[228:231], v253 offset:25600\n\t" \
    "s_waitcnt lgkmcnt(2)\n\t" \
    "v_mfma_f32_16x16x32_bf16 v[204:207], v[84:87], v[240:243], v[204:207]\n\t" \
    "ds_read_b128 v[232:235], v253 offset:6144\n\t" \
    "v_mfma_f32_16x16x32_bf16 v[208:211], v[88:91], v[236:239], v[208:211]\n\t" \
    "v_mfma_f32_16x16x32_bf16 v[208:211], v[92:95], v[240:243], v[208:211]\n\t" \
    "s_waitcnt lgkmcnt(2)\n\t" \
    "v_mfma_f32_16x16x32_bf16 v[216:219], v[80:83], v[224:227], v[216:219]\n\t" \
    "ds_read_b128 v[236:239], v253 offset:7168\n\t" \
    "s_waitcnt lgkmcnt(2)\n\t" \
    "v_mfma_f32_16x16x32_bf16 v[216:219], v[84:87], v[228:231], v[216:219]\n\t" \
    "ds_read_b128 v[240:243], v253 offset:26624\n\t" \
    "v_mfma_f32_16x16x32_bf16 v[220:223], v[88:91], v[224:227], v[220:223]\n\t" \
    "v_mfma_f32_16x16x32_bf16 v[220:223], v[92:95], v[228:231], v[220:223]\n\t" \
    "s_waitcnt lgkmcnt(2)\n\t" \
    "v_mfma_f32_16x16x32_bf16 v[204:207], v[120:123], v[232:235], v[204:207]\n\t" \
    "ds_read_b128 v[224:227], v253 offset:27648\n\t" \
    "s_waitcnt lgkmcnt(2)\n\t" \
    "v_mfma_f32_16x16x32_bf16 v[204:207], v[124:127], v[236:239], v[204:207]\n\t" \
    "ds_read_b128 v[228:231], v253 offset:8192\n\t" \
    "v_mfma_f32_16x16x32_bf16 v[208:211], v[128:131], v[232:235], v[208:211]\n\t" \
    "v_mfma_f32_16x16x32_bf16 v[208:211], v[132:135], v[236:239], v[208:211]\n\t" \
    "s_waitcnt lgkmcnt(2)\n\t" \
    "v_mfma_f32_16x16x32_bf16 v[216:219], v[120:123], v[240:243], v[216:219]\n\t" \
    "ds_read_b128 v[232:235], v253 offset:9216\n\t" \
    "s_waitcnt lgkmcnt(2)\n\t" \
    "v_mfma_f32_16x16x32_bf16 v[216:219], v[124:127], v[224:227], v[216:219]\n\t" \
    "ds_read_b128 v[236:239], v253 offset:28672\n\t" \
    "v_mfma_f32_16x16x32_bf16 v[220:223], v[128:131], v[240:243], v[220:223]\n\t" \
    "v_mfma_f32_16x16x32_bf16 v[220:223], v[132:135], v[224:227], v[220:223]\n\t" \
    "s_waitcnt lgkmcnt(2)\n\t" \
    "v_mfma_f32_16x16x32_bf16 v[204:207], v[160:163], v[228:231], v[204:207]\n\t" \
    "ds_read_b128 v[240:243], v253 offset:29696\n\t" \
    "s_waitcnt lgkmcnt(2)\n\t" \
    "v_mfma_f32_16x16x32_bf16 v[204:207], v[164:167], v[232:235], v[204:207]\n\t" \
    "ds_read_b128 v[224:227], v253 offset:10240\n\t" \
    "v_mfma_f32_16x16x32_bf16 v[208:211], v[168:171], v[228:231], v[208:211]\n\t" \
    "v_mfma_f32_16x16x32_bf16 v[208:211], v[172:175], v[232:235], v[208:211]\n\t" \
    "s_waitcnt lgkmcnt(2)\n\t" \
    "v_mfma_f32_16x16x32_bf16 v[216:219], v[160:163], v[236:239], v[216:219]\n\t" \
    "ds_read_b128 v[228:231], v253 offset:11264\n\t" \
    "s_waitcnt lgkmcnt(2)\n\t" \
    "v_mfma_f32_16x16x32_bf16 v[216:219], v[164:167], v[240:243], v[216:219]\n\t" \
    "ds_read_b128 v[232:235], v253 offset:30720\n\t" \
    "v_mfma_f32_16x16x32_bf16 v[220:223], v[168:171], v[236:239], v[220:223]\n\t" \
    "v_mfma_f32_16x16x32_bf16 v[220:223], v[172:175], v[240:243], v[220:223]\n\t" \
    "ds_read_b128 v[236:239], v253 offset:31744\n\t" \
    "s_setprio 2\n\t" \
    "s_waitcnt lgkmcnt(0)\n\t" \
    "s_barrier \n\t" \
    "ds_read_b128 v[200:203], v253 offset:40960\n\t" \
    "ds_read_b128 v[212:215], v253 offset:41984\n\t" \
    "v_mfma_f32_16x16x32_bf16 v[204:207], v[24:27], v[224:227], v[204:207]\n\t" \
    "v_mfma_f32_16x16x32_bf16 v[204:207], v[28:31], v[228:231], v[204:207]\n\t" \
    "ds_read_b128 v[240:243], v253 offset:12288\n\t" \
    "v_mfma_f32_16x16x32_bf16 v[208:211], v[32:35], v[224:227], v[208:211]\n\t" \
    "v_mfma_f32_16x16x32_bf16 v[208:211], v[36:39], v[228:231], v[208:211]\n\t" \
    "s_waitcnt lgkmcnt(2)\n\t" \
    "v_mfma_f32_16x16x32_bf16 v[200:203], v[16:19], v[224:227], v[200:203]\n\t" \
    "v_mfma_f32_16x16x32_bf16 v[200:203], v[20:23], v[228:231], v[200:203]\n\t" \
    "v_mfma_f32_16x16x32_bf16 v[216:219], v[24:27], v[232:235], v[216:219]\n\t" \
    "ds_read_b128 v[224:227], v253 offset:13312\n\t" \
    "s_and_b32 s44, s40, 3\n\t" \
    "v_mfma_f32_16x16x32_bf16 v[216:219], v[28:31], v[236:239], v[216:219]\n\t" \
    "ds_read_b128 v[228:231], v253 offset:32768\n\t" \
    "s_lshl_b32 s44, s44, 22\n\t" \
    "v_mfma_f32_16x16x32_bf16 v[220:223], v[32:35], v[232:235], v[220:223]\n\t" \
    "s_add_u32 s44, s44, %[mb]\n\t" \
    "v_mfma_f32_16x16x32_bf16 v[220:223], v[36:39], v[236:239], v[220:223]\n\t" \
    "s_add_u32 s46, s44, 0x0\n\t" \
    "s_waitcnt lgkmcnt(3)\n\t" \
    "v_mfma_f32_16x16x32_bf16 v[212:215], v[16:19], v[232:235], v[212:215]\n\t" \
    "s_add_u32 s47, s44, 0x8400\n\t" \
    "v_mfma_f32_16x16x32_bf16 v[212:215], v[20:23], v[236:239], v[212:215]\n\t" \
    "s_add_u32 s48, s44, 0x400\n\t" \
    "s_waitcnt lgkmcnt(2)\n\t" \
    "v_mfma_f32_16x16x32_bf16 v[204:207], v[64:67], v[240:243], v[204:207]\n\t" \
    "ds_read_b128 v[232:235], v253 offset:33792\n\t" \
    "s_waitcnt lgkmcnt(2)\n\t" \
    "v_mfma_f32_16x16x32_bf16 v[204:207], v[68:71], v[224:227], v[204:207]\n\t" \
    "ds_read_b128 v[236:239], v253 offset:14336\n\t" \
    "v_mfma_f32_16x16x32_bf16 v[208:211], v[72:75], v[240:243], v[208:211]\n\t" \
    "v_mfma_f32_16x16x32_bf16 v[208:211], v[76:79], v[224:227], v[208:211]\n\t" \
    "v_mfma_f32_16x16x32_bf16 v[200:203], v[56:59], v[240:243], v[200:203]\n\t" \
    "v_mfma_f32_16x16x32_bf16 v[200:203], v[60:63], v[224:227], v[200:203]\n\t" \
    "s_waitcnt lgkmcnt(2)\n\t" \
    "v_mfma_f32_16x16x32_bf16 v[216:219], v[64:67], v[228:231], v[216:219]\n\t" \
    "ds_read_b128 v[240:243], v253 offset:15360\n\t" \
    "s_waitcnt lgkmcnt(2)\n\t" \
    "v_mfma_f32_16x16x32_bf16 v[216:219], v[68:71], v[232:235], v[216:219]\n\t" \
    "ds_read_b128 v[224:227], v253 offset:34816\n\t" \
    "v_mfma_f32_16x16x32_bf16 v[220:223], v[72:75], v[228:231], v[220:223]\n\t" \
    "v_mfma_f32_16x16x32_bf16 v[220:223], v[76:79], v[232:235], v[220:223]\n\t" \
    "v_mfma_f32_16x16x32_bf16 v[212:215], v[56:59], v[228:231], v[212:215]\n\t" \
    "v_mfma_f32_16x16x32_bf16 v[212:215], v[60:63], v[232:235], v[212:215]\n\t" \
    "s_waitcnt lgkmcnt(2)\n\t" \
    "v_mfma_f32_16x16x32_bf16 v[204:207], v[104:107], v[236:239], v[204:207]\n\t" \
    "ds_read_b128 v[228:231], v253 offset:35840\n\t" \
    "s_waitcnt lgkmcnt(2)\n\t" \
    "v_mfma_f32_16x16x32_bf16 v[204:207], v[108:111], v[240:243], v[204:207]\n\t" \
    "ds_read_b128 v[232:235], v253 offset:16384\n\t" \
    "v_mfma_f32_16x16x32_bf16 v[208:211], v[112:115], v[236:239], v[208:211]\n\t" \
    "v_mfma_f32_16x16x32_bf16 v[208:211], v[116:119], v[240:243], v[208:211]\n\t" \
    "v_mfma_f32_16x16x32_bf16 v[200:203], v[96:99], v[236:239], v[200:203]\n\t" \
    "v_mfma_f32_16x16x32_bf16 v[200:203], v[100:103], v[240:243], v[200:203]\n\t" \
    "s_waitcnt lgkmcnt(2)\n\t" \
    "v_mfma_f32_16x16x32_bf16 v[216:219], v[104:107], v[224:227], v[216:219]\n\t" \
    "ds_read_b128 v[236:239], v253 offset:17408\n\t" \
    "s_waitcnt lgkmcnt(2)\n\t" \
    "v_mfma_f32_16x16x32_bf16 v[216:219], v[108:111], v[228:231], v[216:219]\n\t" \
    "ds_read_b128 v[240:243], v253 offset:18432\n\t" \
    "v_mfma_f32_16x16x32_bf16 v[220:223], v[112:115], v[224:227], v[220:223]\n\t" \
    "v_mfma_f32_16x16x32_bf16 v[220:223], v[116:119], v[228:231], v[220:223]\n\t" \
    "v_mfma_f32_16x16x32_bf16 v[212:215], v[96:99], v[224:227], v[212:215]\n\t" \
    "v_mfma_f32_16x16x32_bf16 v[212:215], v[100:103], v[228:231], v[212:215]\n\t" \
    "s_waitcnt lgkmcnt(2)\n\t" \
    "v_mfma_f32_16x16x32_bf16 v[204:207], v[144:147], v[232:235], v[204:207]\n\t" \
    "ds_read_b128 v[224:227], v253 offset:19456\n\t" \
    "v_mov_b64 v[244:245], 0\n\t" \
    "s_waitcnt lgkmcnt(2)\n\t" \
    "v_mfma_f32_16x16x32_bf16 v[204:207], v[148:151], v[236:239], v[204:207]\n\t" \
    "ds_read_b128 v[228:231], v253 offset:36864\n\t" \
    "v_mov_b64 v[246:247], 0\n\t" \
    "v_mfma_f32_16x16x32_bf16 v[208:211], v[152:155], v[232:235], v[208:211]\n\t" \
    "v_mov_b64 v[248:249], 0\n\t" \
    "v_mfma_f32_16x16x32_bf16 v[208:211], v[156:159], v[236:239], v[208:211]\n\t" \
    "v_mov_b64 v[250:251], 0\n\t" \
    "v_mfma_f32_16x16x32_bf16 v[200:203], v[136:139], v[232:235], v[200:203]\n\t" \
    "v_mfma_f32_16x16x32_bf16 v[200:203], v[140:143], v[236:239], v[200:203]\n\t" \
    "s_waitcnt lgkmcnt(2)\n\t" \
    "v_mfma_f32_16x16x32_bf16 v[204:207], v[184:187], v[240:243], v[204:207]\n\t" \
    "ds_read_b128 v[232:235], v253 offset:37888\n\t" \
    "s_waitcnt lgkmcnt(2)\n\t" \
    "v_mfma_f32_16x16x32_bf16 v[204:207], v[188:191], v[224:227], v[204:207]\n\t" \
    "ds_read_b128 v[236:239], v253 offset:38912\n\t" \
    "v_mfma_f32_16x16x32_bf16 v[208:211], v[192:195], v[240:243], v[208:211]\n\t" \
    "v_mfma_f32_16x16x32_bf16 v[208:211], v[196:199], v[224:227], v[208:211]\n\t" \
    "v_mfma_f32_16x16x32_bf16 v[200:203], v[176:179], v[240:243], v[200:203]\n\t" \
    "v_mfma_f32_16x16x32_bf16 v[200:203], v[180:183], v[224:227], v[200:203]\n\t" \
    "ds_read_b128 v[240:243], v253 offset:39936\n\t" \
    "s_waitcnt lgkmcnt(3)\n\t" \
    "v_mfma_f32_16x16x32_bf16 v[216:219], v[144:147], v[228:231], v[216:219]\n\t" \
    "s_waitcnt lgkmcnt(2)\n\t" \
    "v_mfma_f32_16x16x32_bf16 v[216:219], v[148:151], v[232:235], v[216:219]\n\t" \
    "v_cvt_pk_bf16_f32 v204, v204, v208\n\t" \
    "v_cvt_pk_bf16_f32 v205, v205, v209\n\t" \
    "s_waitcnt lgkmcnt(1)\n\t" \
    "v_mfma_f32_16x16x32_bf16 v[216:219], v[184:187], v[236:239], v[216:219]\n\t" \
    "v_cvt_pk_bf16_f32 v206, v206, v210\n\t" \
    "v_cvt_pk_bf16_f32 v207, v207, v211\n\t" \
    "s_waitcnt lgkmcnt(0)\n\t" \
    "v_mfma_f32_16x16x32_bf16 v[216:219], v[188:191], v[240:243], v[216:219]\n\t" \
    "ds_write_b128 v254, v[204:207] offset:8192\n\t" \
    "v_mfma_f32_16x16x32_bf16 v[220:223], v[152:155], v[228:231], v[220:223]\n\t" \
    "v_mfma_f32_16x16x32_bf16 v[220:223], v[156:159], v[232:235], v[220:223]\n\t" \
    "v_cvt_pk_bf16_f32 v200, v200, v201\n\t" \
    "v_cvt_pk_bf16_f32 v201, v202, v203\n\t" \
    "v_mfma_f32_16x16x32_bf16 v[220:223], v[192:195], v[236:239], v[220:223]\n\t" \
    "ds_write_b64 v254, v[200:201] offset:6144\n\t" \
    "v_mfma_f32_16x16x32_bf16 v[220:223], v[196:199], v[240:243], v[220:223]\n\t" \
    "v_mfma_f32_16x16x32_bf16 v[212:215], v[136:139], v[228:231], v[212:215]\n\t" \
    "ds_read_b128 v[200:203], v254 offset:30720\n\t" \
    "ds_read_b128 v[204:207], v254 offset:31744\n\t" \
    "v_mfma_f32_16x16x32_bf16 v[212:215], v[140:143], v[232:235], v[212:215]\n\t" \
    "v_mov_b64 v[208:209], 0\n\t" \
    "v_mov_b64 v[210:211], 0\n\t" \
    "v_mfma_f32_16x16x32_bf16 v[212:215], v[176:179], v[236:239], v[212:215]\n\t" \
    "v_cvt_pk_bf16_f32 v216, v216, v220\n\t" \
    "v_cvt_pk_bf16_f32 v217, v217, v221\n\t" \
    "v_mfma_f32_16x16x32_bf16 v[212:215], v[180:183], v[240:243], v[212:215]\n\t" \
    "v_cvt_pk_bf16_f32 v218, v218, v222\n\t" \
    "v_cvt_pk_bf16_f32 v219, v219, v223\n\t" \
    "s_setprio 0\n\t" \
    "ds_write_b128 v254, v[216:219] offset:14336\n\t" \
    "ds_read_b128 v[216:219], v254 offset:32768\n\t" \
    "ds_read_b128 v[220:223], v254 offset:33792\n\t" \
    "s_nop 2\n\t" \
    "v_cvt_pk_bf16_f32 v212, v212, v213\n\t" \
    "v_cvt_pk_bf16_f32 v213, v214, v215\n\t" \
    "ds_write_b64 v254, v[212:213] offset:12288\n\t" \
    "s_sleep 3\n\t" \
    "s_mov_b32 m0, 0xa000\n\t" \
    "s_nop 0\n\t" \
    "buffer_load_dwordx4 v253, s[60:63], s46 offen sc1 lds\n\t" \
    "s_mov_b32 m0, 0xb400\n\t" \
    "s_nop 0\n\t" \
    "buffer_load_dwordx4 v253, s[60:63], s47 offen sc1 lds\n\t" \
    "s_mov_b32 m0, 0xa400\n\t" \
    "s_nop 0\n\t" \
    "buffer_load_dwordx4 v253, s[60:63], s48 offen sc1 lds\n\t" \
    "s_waitcnt lgkmcnt(0)\n\t" \
    "v_bfe_i32 v212, v253, 5, 1\n\t" \
    "v_and_b32 v212, 0xaaaa, v212\n\t" \
    "v_or_b32 v212, 0x4444, v212\n\t" \
    "v_and_b32 v213, 0xffff, v255\n\t" \
    "v_lshrrev_b32 v214, 16, v255\n\t" \
    ".Ls04_%=:\n\t" \
    "s_waitcnt vmcnt(0)\n\t" \
    "ds_read_b128 v[224:227], v253 offset:40960\n\t" \
    "ds_read_b128 v[228:231], v253 offset:46080\n\t" \
    "ds_read_b128 v[232:235], v253 offset:41984\n\t" \
    "s_waitcnt lgkmcnt(2)\n\t" \
    "v_max_u32 v215, v224, v225\n\t" \
    "v_max3_u32 v215, v226, v227, v215\n\t" \
    "s_waitcnt lgkmcnt(1)\n\t" \
    "v_max3_u32 v215, v228, v229, v215\n\t" \
    "v_max3_u32 v215, v230, v231, v215\n\t" \
    "s_waitcnt lgkmcnt(0)\n\t" \
    "v_max3_u32 v215, v232, v233, v215\n\t" \
    "v_max3_u32 v215, v234, v235, v215\n\t" \
    "v_cmp_eq_u32 vcc, -1, v215\n\t" \
    "s_cbranch_vccz .Ls05_%=\n\t" \
    "v_max_u32 v215, v224, v225\n\t" \
    "v_max3_u32 v215, v226, v227, v215\n\t" \
    "v_cmp_eq_u32 vcc, -1, v215\n\t" \
    "s_cbranch_vccz .Ls06_%=\n\t" \
    "s_mov_b32 m0, 0xa000\n\t" \
    "s_nop 0\n\t" \
    "buffer_load_dwordx4 v253, s[60:63], s46 offen sc1 lds\n\t" \
    ".Ls06_%=:\n\t" \
    "v_max_u32 v215, v228, v229\n\t" \
    "v_max3_u32 v215, v230, v231, v215\n\t" \
    "v_cmp_eq_u32 vcc, -1, v215\n\t" \
    "s_cbranch_vccz .Ls07_%=\n\t" \
    "s_mov_b32 m0, 0xb400\n\t" \
    "s_nop 0\n\t" \
    "buffer_load_dwordx4 v253, s[60:63], s47 offen sc1 lds\n\t" \
    ".Ls07_%=:\n\t" \
    "v_max_u32 v215, v232, v233\n\t" \
    "v_max3_u32 v215, v234, v235, v215\n\t" \
    "v_cmp_eq_u32 vcc, -1, v215\n\t" \
    "s_cbranch_vccz .Ls08_%=\n\t" \
    "s_mov_b32 m0, 0xa400\n\t" \
    "s_nop 0\n\t" \
    "buffer_load_dwordx4 v253, s[60:63], s48 offen sc1 lds\n\t" \
    ".Ls08_%=:\n\t" \
    "s_cmp_eq_u32 s41, 0\n\t" \
    "s_cbranch_scc1 .Ls05_%=\n\t" \
    "s_sub_u32 s41, s41, 1\n\t" \
    "s_branch .Ls04_%=\n\t" \
    ".Ls05_%=:\n\t" \
    "s_setprio 3\n\t" \
    "s_waitcnt lgkmcnt(0)\n\t" \
    "s_barrier \n\t" \
    "ds_read_b128 v[236:239], v253 offset:47104\n\t" \
    "ds_read_b128 v[240:243], v253 offset:43008\n\t" \
    "v_smfmac_f32_16x16x64_bf16 v[244:247], v[224:227], v[0:7], v212\n\t" \
    "v_smfmac_f32_16x16x64_bf16 v[248:251], v[224:227], v[8:15], v212\n\t" \
    "v_smfmac_f32_16x16x64_bf16 v[208:211], v[224:227], v[200:207], v212\n\t" \
    "ds_read_b128 v[200:203], v254 offset:34816\n\t" \
    "ds_read_b128 v[204:207], v254 offset:35840\n\t" \
    "ds_read_b128 v[224:227], v253 offset:48128\n\t" \
    "v_smfmac_f32_16x16x64_bf16 v[208:211], v[228:231], v[16:23], v212\n\t" \
    "v_smfmac_f32_16x16x64_bf16 v[244:247], v[228:231], v[24:31], v212\n\t" \
    "v_smfmac_f32_16x16x64_bf16 v[248:251], v[228:231], v[32:39], v212\n\t" \
    "ds_read_b128 v[228:231], v253 offset:44032\n\t" \
    "v_smfmac_f32_16x16x64_bf16 v[208:211], v[232:235], v[216:223], v212\n\t" \
    "ds_read_b128 v[216:219], v254 offset:36864\n\t" \
    "ds_read_b128 v[220:223], v254 offset:37888\n\t" \
    "v_smfmac_f32_16x16x64_bf16 v[244:247], v[232:235], v[40:47], v212\n\t" \
    "v_smfmac_f32_16x16x64_bf16 v[248:251], v[232:235], v[48:55], v212\n\t" \
    "ds_read_b128 v[232:235], v253 offset:49152\n\t" \
    "s_waitcnt lgkmcnt(8)\n\t" \
    "v_smfmac_f32_16x16x64_bf16 v[208:211], v[236:239], v[56:63], v212\n\t" \
    "v_smfmac_f32_16x16x64_bf16 v[244:247], v[236:239], v[64:71], v212\n\t" \
    "v_smfmac_f32_16x16x64_bf16 v[248:251], v[236:239], v[72:79], v212\n\t" \
    "ds_read_b128 v[236:239], v253 offset:45056\n\t" \
    "s_waitcnt lgkmcnt(6)\n\t" \
    "v_smfmac_f32_16x16x64_bf16 v[208:211], v[240:243], v[200:207], v212\n\t" \
    "ds_read_b128 v[200:203], v254 offset:38912\n\t" \
    "ds_read_b128 v[204:207], v254 offset:39936\n\t" \
    "v_smfmac_f32_16x16x64_bf16 v[244:247], v[240:243], v[80:87], v212\n\t" \
    "v_smfmac_f32_16x16x64_bf16 v[248:251], v[240:243], v[88:95], v212\n\t" \
    "ds_read_b128 v[240:243], v253 offset:50176\n\t" \
    "s_waitcnt lgkmcnt(8)\n\t" \
    "v_smfmac_f32_16x16x64_bf16 v[208:211], v[224:227], v[96:103], v212\n\t" \
    "v_smfmac_f32_16x16x64_bf16 v[244:247], v[224:227], v[104:111], v212\n\t" \
    "v_smfmac_f32_16x16x64_bf16 v[248:251], v[224:227], v[112:119], v212\n\t" \
    "s_waitcnt lgkmcnt(5)\n\t" \
    "v_smfmac_f32_16x16x64_bf16 v[208:211], v[228:231], v[216:223], v212\n\t" \
    "s_waitcnt lgkmcnt(4)\n\t" \
    "v_smfmac_f32_16x16x64_bf16 v[208:211], v[232:235], v[136:143], v212\n\t" \
    "s_waitcnt lgkmcnt(1)\n\t" \
    "v_smfmac_f32_16x16x64_bf16 v[208:211], v[236:239], v[200:207], v212\n\t" \
    "s_waitcnt lgkmcnt(0)\n\t" \
    "v_smfmac_f32_16x16x64_bf16 v[208:211], v[240:243], v[176:183], v212\n\t" \
    "v_smfmac_f32_16x16x64_bf16 v[244:247], v[228:231], v[120:127], v212\n\t" \
    "v_smfmac_f32_16x16x64_bf16 v[244:247], v[232:235], v[144:151], v212\n\t" \
    "v_smfmac_f32_16x16x64_bf16 v[244:247], v[236:239], v[160:167], v212\n\t" \
    "v_add_f32 v208, v208, v210\n\t" \
    "v_add_f32 v209, v209, v211\n\t" \
    "v_cvt_pk_bf16_f32 v208, v208, v209\n\t" \
    "v_smfmac_f32_16x16x64_bf16 v[244:247], v[240:243], v[184:191], v212\n\t" \
    "ds_write_b16 v213, v208 offset:0\n\t" \
    "ds_write_b16_d16_hi v213, v208 offset:16\n\t" \
    "v_smfmac_f32_16x16x64_bf16 v[248:251], v[228:231], v[128:135], v212\n\t" \
    "v_smfmac_f32_16x16x64_bf16 v[248:251], v[232:235], v[152:159], v212\n\t" \
    "v_smfmac_f32_16x16x64_bf16 v[248:251], v[236:239], v[168:175], v212\n\t" \
    "v_add_f32 v244, v244, v246\n\t" \
    "v_add_f32 v245, v245, v247\n\t" \
    "v_smfmac_f32_16x16x64_bf16 v[248:251], v[240:243], v[192:199], v212\n\t" \
    "s_nop 7\n\t" \
    "v_add_f32 v248, v248, v250\n\t" \
    "v_add_f32 v249, v249, v251\n\t" \
    "v_cvt_pk_bf16_f32 v244, v244, v248\n\t" \
    "v_cvt_pk_bf16_f32 v245, v245, v249\n\t" \
    "ds_write2_b32 v214, v244, v245 offset1:4\n\t" \
    "s_setprio 0\n\t" \
    "s_sleep 3\n\t" \
    "ds_read_b128 v[200:203], v254 offset:18432\n\t" \
    "ds_read_b128 v[204:207], v254 offset:19456\n\t" \
    "ds_read_b128 v[214:217], v254 offset:24576\n\t" \
    "ds_read_b128 v[218:221], v254 offset:25600\n\t" \
    "ds_read_b128 v[222:225], v254 offset:20480\n\t" \
    "ds_read_b128 v[226:229], v254 offset:21504\n\t" \
    "ds_read_b128 v[230:233], v254 offset:26624\n\t" \
    "ds_read_b128 v[234:237], v254 offset:27648\n\t" \
    "v_and_b32 v213, 0xffffff7f, v254\n\t" \
    "v_mov_b64 v[242:243], 0\n\t" \
    "v_mov_b64 v[244:245], 0\n\t" \
    "v_mov_b64 v[246:247], 0\n\t" \
    "v_mov_b64 v[248:249], 0\n\t" \
    "v_mov_b64 v[250:251], 0\n\t" \
    "v_mov_b64 v[252:253], 0\n\t" \
    "s_and_b32 s44, s40, 3\n\t" \
    "s_lshl_b32 s44, s44, 22\n\t" \
    "s_add_u32 s50, s44, %[pb]\n\t" \
    "s_xor_b32 s51, s50, 0x4000\n\t" \
    "s_xor_b32 s52, s50, 0x1000\n\t" \
    "s_xor_b32 s53, s50, 0x5000\n\t" \
    "s_setprio 3\n\t" \
    "s_waitcnt lgkmcnt(0)\n\t" \
    "s_barrier \n\t" \
    "ds_read_b128 v[208:211], v254 offset:0\n\t" \
    "ds_read_b128 v[238:241], v254 offset:1024\n\t" \
    "s_waitcnt lgkmcnt(1)\n\t" \
    "v_smfmac_f32_16x16x64_bf16 v[242:245], v[208:211], v[200:207], v212\n\t" \
    "ds_read_b128 v[200:203], v213 offset:3072\n\t" \
    "ds_read_b128 v[204:207], v213 offset:3200\n\t" \
    "v_smfmac_f32_16x16x64_bf16 v[246:249], v[208:211], v[214:221], v212\n\t" \
    "ds_read_b128 v[214:217], v213 offset:4096\n\t" \
    "ds_read_b128 v[218:221], v213 offset:4224\n\t" \
    "s_waitcnt lgkmcnt(4)\n\t" \
    "v_smfmac_f32_16x16x64_bf16 v[242:245], v[238:241], v[222:229], v212\n\t" \
    "ds_read_b128 v[222:225], v254 offset:22528\n\t" \
    "ds_read_b128 v[226:229], v254 offset:23552\n\t" \
    "v_smfmac_f32_16x16x64_bf16 v[246:249], v[238:241], v[230:237], v212\n\t" \
    "ds_read_b128 v[230:233], v254 offset:28672\n\t" \
    "ds_read_b128 v[234:237], v254 offset:29696\n\t" \
    "s_waitcnt lgkmcnt(6)\n\t" \
    "v_smfmac_f32_16x16x64_bf16 v[250:253], v[208:211], v[200:207], v212\n\t" \
    "ds_read_b128 v[208:211], v254 offset:2048\n\t" \
    "ds_read_b128 v[200:203], v213 offset:5120\n\t" \
    "ds_read_b128 v[204:207], v213 offset:5248\n\t" \
    "s_waitcnt lgkmcnt(7)\n\t" \
    "v_smfmac_f32_16x16x64_bf16 v[250:253], v[238:241], v[214:221], v212\n\t" \
    "v_lshrrev_b32 v238, 3, v254\n\t" \
    "v_and_b32 v238, 0x6e, v238\n\t" \
    "v_lshrrev_b32 v239, 16, v255\n\t" \
    "s_waitcnt lgkmcnt(2)\n\t" \
    "v_smfmac_f32_16x16x64_bf16 v[242:245], v[208:211], v[222:229], v212\n\t" \
    "v_smfmac_f32_16x16x64_bf16 v[246:249], v[208:211], v[230:237], v212\n\t" \
    "s_waitcnt lgkmcnt(0)\n\t" \
    "v_smfmac_f32_16x16x64_bf16 v[250:253], v[208:211], v[200:207], v212\n\t" \
    "s_nop 1\n\t" \
    "v_add_f32 v242, v242, v244\n\t" \
    "v_add_f32 v243, v243, v245\n\t" \
    "s_nop 0\n\t" \
    "v_add_f32 v246, v246, v248\n\t" \
    "v_add_f32 v247, v247, v249\n\t" \
    "s_nop 0\n\t" \
    "v_add_f32 v250, v250, v252\n\t" \
    "v_add_f32 v251, v251, v253\n\t" \
    "v_max3_f32 v244, v242, v246, v250\n\t" \
    "v_max3_f32 v245, v243, v247, v251\n\t" \
    "s_nop 0\n\t" \
    "v_max_f32_dpp v244, v244, v244 quad_perm:[1,0,3,2] row_mask:0xf bank_mask:0xf\n\t" \
    "v_max_f32_dpp v245, v245, v245 quad_perm:[1,0,3,2] row_mask:0xf bank_mask:0xf\n\t" \
    "s_nop 0\n\t" \
    "v_max_f32_dpp v244, v244, v244 quad_perm:[2,3,0,1] row_mask:0xf bank_mask:0xf\n\t" \
    "v_max_f32_dpp v245, v245, v245 quad_perm:[2,3,0,1] row_mask:0xf bank_mask:0xf\n\t" \
    "s_nop 0\n\t" \
    "v_max_f32_dpp v244, v244, v244 row_half_mirror row_mask:0xf bank_mask:0xf\n\t" \
    "v_max_f32_dpp v245, v245, v245 row_half_mirror row_mask:0xf bank_mask:0xf\n\t" \
    "s_nop 0\n\t" \
    "v_max_f32_dpp v244, v244, v244 row_mirror row_mask:0xf bank_mask:0xf\n\t" \
    "v_max_f32_dpp v245, v245, v245 row_mirror row_mask:0xf bank_mask:0xf\n\t" \
    "v_mul_f32 v244, %[scl], v244\n\t" \
    "v_mul_f32 v245, %[scl], v245\n\t" \
    "v_fma_f32 v242, v242, %[scl], -v244\n\t" \
    "v_fma_f32 v243, v243, %[scl], -v245\n\t" \
    "v_fma_f32 v246, v246, %[scl], -v244\n\t" \
    "v_fma_f32 v247, v247, %[scl], -v245\n\t" \
    "v_exp_f32 v242, v242\n\t" \
    "v_exp_f32 v243, v243\n\t" \
    "v_fma_f32 v250, v250, %[scl], -v244\n\t" \
    "v_fma_f32 v251, v251, %[scl], -v245\n\t" \
    "v_exp_f32 v246, v246\n\t" \
    "v_exp_f32 v247, v247\n\t" \
    "v_exp_f32 v250, v250\n\t" \
    "v_exp_f32 v251, v251\n\t" \
    "v_add_f32 v244, v242, v246\n\t" \
    "v_add_f32 v245, v243, v247\n\t" \
    "v_fma_f32 v244, v250, 0.5, v244\n\t" \
    "v_fma_f32 v245, v251, 0.5, v245\n\t" \
    "s_nop 0\n\t" \
    "v_add_f32_dpp v244, v244, v244 quad_perm:[1,0,3,2] row_mask:0xf bank_mask:0xf\n\t" \
    "v_add_f32_dpp v245, v245, v245 quad_perm:[1,0,3,2] row_mask:0xf bank_mask:0xf\n\t" \
    "s_nop 0\n\t" \
    "v_add_f32_dpp v244, v244, v244 quad_perm:[2,3,0,1] row_mask:0xf bank_mask:0xf\n\t" \
    "v_add_f32_dpp v245, v245, v245 quad_perm:[2,3,0,1] row_mask:0xf bank_mask:0xf\n\t" \
    "s_nop 0\n\t" \
    "v_add_f32_dpp v244, v244, v244 row_half_mirror row_mask:0xf bank_mask:0xf\n\t" \
    "v_add_f32_dpp v245, v245, v245 row_half_mirror row_mask:0xf bank_mask:0xf\n\t" \
    "s_nop 0\n\t" \
    "v_add_f32_dpp v244, v244, v244 row_mirror row_mask:0xf bank_mask:0xf\n\t" \
    "v_add_f32_dpp v245, v245, v245 row_mirror row_mask:0xf bank_mask:0xf\n\t" \
    "v_rcp_f32 v244, v244\n\t" \
    "v_rcp_f32 v245, v245\n\t" \
    "s_wakeup\n\t" \
    "v_mul_f32 v250, v250, v244\n\t" \
    "v_mul_f32 v251, v251, v245\n\t" \
    "v_mul_f32 v242, v242, v244\n\t" \
    "v_mul_f32 v243, v243, v245\n\t" \
    "v_mul_f32 v246, v246, v244\n\t" \
    "v_mul_f32 v247, v247, v245\n\t" \
    "v_cvt_pk_bf16_f32 v250, |v250|, |v251|\n\t" \
    "ds_write_b16 v238, v250 offset:52096\n\t" \
    "ds_write_b16_d16_hi v238, v250 offset:52112\n\t" \
    "v_cvt_pk_bf16_f32 v242, |v242|, |v246|\n\t" \
    "v_cvt_pk_bf16_f32 v243, |v243|, |v247|\n\t" \
    "ds_write2_b32 v239, v242, v243 offset1:4\n\t" \
    "ds_read_b128 v[200:203], v254 offset:896\n\t" \
    "v_and_b32 v253, 0x3ff, v254\n\t" \
    "s_mov_b32 exec_hi, 0xff\n\t" \
    "s_waitcnt lgkmcnt(0)\n\t" \
    "buffer_store_dwordx4 v[200:203], v253, s[60:63], s50 offen sc1\n\t" \
    "buffer_store_dwordx4 v[200:203], v253, s[60:63], s51 offen sc1\n\t" \
    "buffer_store_dwordx4 v[200:203], v253, s[60:63], s52 offen sc1\n\t" \
    "buffer_store_dwordx4 v[200:203], v253, s[60:63], s53 offen sc1\n\t" \
    "s_mov_b32 exec_hi, -1\n\t" \
    "s_setprio 0\n\t" \
    "ds_read_b128 v[224:227], v253 offset:0\n\t" \
    "ds_read_b128 v[228:231], v253 offset:1024\n\t" \
    "ds_read_b128 v[232:235], v253 offset:20480\n\t" \
    "ds_read_b128 v[236:239], v253 offset:21504\n\t" \
    "s_add_u32 s40, s40, 1\n\t" \
    "s_cmp_lg_u32 s40, 64\n\t" \
    "s_cbranch_scc1 .Ls03_%=\n\t" \
    "s_branch .Lend%=\n\t" \
    ".Lend%=:\n\t" \
    "s_waitcnt vmcnt(0) lgkmcnt(0)\n\t" \
    "s_mov_b32 %[bud], s41\n\t"
#define ASM_V_LOOP \
    "s_mov_b64 s[60:61], %[ws]\n\t" \
    "s_mov_b32 s62, %[sz]\n\t" \
    "s_mov_b32 s63, 0x20000\n\t" \
    "s_mov_b32 s40, 0\n\t" \
    "s_mov_b32 s41, 0x20000\n\t" \
    "s_cmp_eq_u32 %[wave], 0\n\t" \
    "s_cbranch_scc1 .Lv0_%=\n\t" \
    "s_cmp_eq_u32 %[wave], 1\n\t" \
    "s_cbranch_scc1 .Lv1_%=\n\t" \
    "s_cmp_eq_u32 %[wave], 2\n\t" \
    "s_cbranch_scc1 .Lv2_%=\n\t" \
    "s_cmp_eq_u32 %[wave], 3\n\t" \
    "s_cbranch_scc1 .Lv3_%=\n\t" \
    ".Lv4_%=:\n\t" \
    "s_barrier \n\t" \
    "s_waitcnt vmcnt(15)\n\t" \
    "ds_read_b64_tr_b16 v[200:201], v245 offset:0\n\t" \
    "ds_read_b64_tr_b16 v[16:17], v245 offset:32\n\t" \
    "ds_read_b64_tr_b16 v[208:209], v245 offset:64\n\t" \
    "ds_read_b64_tr_b16 v[56:57], v245 offset:96\n\t" \
    "ds_read_b64_tr_b16 v[216:217], v245 offset:128\n\t" \
    "ds_read_b64_tr_b16 v[96:97], v245 offset:160\n\t" \
    "ds_read_b64_tr_b16 v[224:225], v245 offset:192\n\t" \
    "ds_read_b64_tr_b16 v[136:137], v245 offset:224\n\t" \
    "ds_read_b64_tr_b16 v[232:233], v245 offset:256\n\t" \
    "ds_read_b64_tr_b16 v[176:177], v245 offset:288\n\t" \
    "s_waitcnt lgkmcnt(0)\n\t" \
    "s_add_u32 m0, %[wm], 0x0\n\t" \
    "s_mov_b32 s44, 0x14000\n\t" \
    "buffer_load_dwordx4 v240, %[wr], s44 offen offset:0 nt lds\n\t" \
    "buffer_load_dwordx4 v241, %[wr], s44 offen offset:1024 nt lds\n\t" \
    "buffer_load_dwordx4 v242, %[wr], s44 offen offset:2048 nt lds\n\t" \
    "buffer_load_dwordx4 v243, %[wr], s44 offen offset:3072 nt lds\n\t" \
    "s_add_u32 m0, %[wm], 0x1000\n\t" \
    "s_nop 0\n\t" \
    "buffer_load_dwordx4 v244, %[wr], s44 offen nt lds\n\t" \
    "s_barrier \n\t" \
    "s_barrier \n\t" \
    "s_waitcnt vmcnt(15)\n\t" \
    "ds_read_b64_tr_b16 v[204:205], v245 offset:5120\n\t" \
    "ds_read_b64_tr_b16 v[20:21], v245 offset:5152\n\t" \
    "ds_read_b64_tr_b16 v[212:213], v245 offset:5184\n\t" \
    "ds_read_b64_tr_b16 v[60:61], v245 offset:5216\n\t" \
    "ds_read_b64_tr_b16 v[220:221], v245 offset:5248\n\t" \
    "ds_read_b64_tr_b16 v[100:101], v245 offset:5280\n\t" \
    "ds_read_b64_tr_b16 v[228:229], v245 offset:5312\n\t" \
    "ds_read_b64_tr_b16 v[140:141], v245 offset:5344\n\t" \
    "ds_read_b64_tr_b16 v[236:237], v245 offset:5376\n\t" \
    "ds_read_b64_tr_b16 v[180:181], v245 offset:5408\n\t" \
    "s_waitcnt lgkmcnt(0)\n\t" \
    "s_add_u32 m0, %[wm], 0x1400\n\t" \
    "s_mov_b32 s44, 0x19000\n\t" \
    "buffer_load_dwordx4 v240, %[wr], s44 offen offset:0 nt lds\n\t" \
    "buffer_load_dwordx4 v241, %[wr], s44 offen offset:1024 nt lds\n\t" \
    "buffer_load_dwordx4 v242, %[wr], s44 offen offset:2048 nt lds\n\t" \
    "buffer_load_dwordx4 v243, %[wr], s44 offen offset:3072 nt lds\n\t" \
    "s_add_u32 m0, %[wm], 0x2400\n\t" \
    "s_nop 0\n\t" \
    "buffer_load_dwordx4 v244, %[wr], s44 offen nt lds\n\t" \
    "s_barrier \n\t" \
    "s_barrier \n\t" \
    "s_waitcnt vmcnt(15)\n\t" \
    "ds_read_b64_tr_b16 v[0:1], v245 offset:10240\n\t" \
    "ds_read_b64_tr_b16 v[24:25], v245 offset:10272\n\t" \
    "ds_read_b64_tr_b16 v[40:41], v245 offset:10304\n\t" \
    "ds_read_b64_tr_b16 v[64:65], v245 offset:10336\n\t" \
    "ds_read_b64_tr_b16 v[80:81], v245 offset:10368\n\t" \
    "ds_read_b64_tr_b16 v[104:105], v245 offset:10400\n\t" \
    "ds_read_b64_tr_b16 v[120:121], v245 offset:10432\n\t" \
    "ds_read_b64_tr_b16 v[144:145], v245 offset:10464\n\t" \
    "ds_read_b64_tr_b16 v[160:161], v245 offset:10496\n\t" \
    "ds_read_b64_tr_b16 v[184:185], v245 offset:10528\n\t" \
    "s_waitcnt lgkmcnt(0)\n\t" \
    "s_add_u32 m0, %[wm], 0x2800\n\t" \
    "s_mov_b32 s44, 0x1e000\n\t" \
    "buffer_load_dwordx4 v240, %[wr], s44 offen offset:0 nt lds\n\t" \
    "buffer_load_dwordx4 v241, %[wr], s44 offen offset:1024 nt lds\n\t" \
    "buffer_load_dwordx4 v242, %[wr], s44 offen offset:2048 nt lds\n\t" \
    "buffer_load_dwordx4 v243, %[wr], s44 offen offset:3072 nt lds\n\t" \
    "s_add_u32 m0, %[wm], 0x3800\n\t" \
    "s_nop 0\n\t" \
    "buffer_load_dwordx4 v244, %[wr], s44 offen nt lds\n\t" \
    "s_barrier \n\t" \
    "s_waitcnt vmcnt(15)\n\t" \
    "ds_read_b64_tr_b16 v[4:5], v245 offset:15360\n\t" \
    "ds_read_b64_tr_b16 v[28:29], v245 offset:15392\n\t" \
    "ds_read_b64_tr_b16 v[44:45], v245 offset:15424\n\t" \
    "ds_read_b64_tr_b16 v[68:69], v245 offset:15456\n\t" \
    "ds_read_b64_tr_b16 v[84:85], v245 offset:15488\n\t" \
    "ds_read_b64_tr_b16 v[108:109], v245 offset:15520\n\t" \
    "ds_read_b64_tr_b16 v[124:125], v245 offset:15552\n\t" \
    "ds_read_b64_tr_b16 v[148:149], v245 offset:15584\n\t" \
    "ds_read_b64_tr_b16 v[164:165], v245 offset:15616\n\t" \
    "ds_read_b64_tr_b16 v[188:189], v245 offset:15648\n\t" \
    "s_waitcnt lgkmcnt(0)\n\t" \
    "s_add_u32 m0, %[wm], 0x3c00\n\t" \
    "s_mov_b32 s44, 0x23000\n\t" \
    "buffer_load_dwordx4 v240, %[wr], s44 offen offset:0 nt lds\n\t" \
    "buffer_load_dwordx4 v241, %[wr], s44 offen offset:1024 nt lds\n\t" \
    "buffer_load_dwordx4 v242, %[wr], s44 offen offset:2048 nt lds\n\t" \
    "buffer_load_dwordx4 v243, %[wr], s44 offen offset:3072 nt lds\n\t" \
    "s_add_u32 m0, %[wm], 0x4c00\n\t" \
    "s_nop 0\n\t" \
    "buffer_load_dwordx4 v244, %[wr], s44 offen nt lds\n\t" \
    "s_barrier \n\t" \
    "s_barrier \n\t" \
    "s_waitcnt vmcnt(15)\n\t" \
    "ds_read_b64_tr_b16 v[8:9], v245 offset:0\n\t" \
    "ds_read_b64_tr_b16 v[32:33], v245 offset:32\n\t" \
    "ds_read_b64_tr_b16 v[48:49], v245 offset:64\n\t" \
    "ds_read_b64_tr_b16 v[72:73], v245 offset:96\n\t" \
    "ds_read_b64_tr_b16 v[88:89], v245 offset:128\n\t" \
    "ds_read_b64_tr_b16 v[112:113], v245 offset:160\n\t" \
    "ds_read_b64_tr_b16 v[128:129], v245 offset:192\n\t" \
    "ds_read_b64_tr_b16 v[152:153], v245 offset:224\n\t" \
    "ds_read_b64_tr_b16 v[168:169], v245 offset:256\n\t" \
    "ds_read_b64_tr_b16 v[192:193], v245 offset:288\n\t" \
    "s_waitcnt lgkmcnt(0)\n\t" \
    "s_add_u32 m0, %[wm], 0x0\n\t" \
    "s_mov_b32 s44, 0x28000\n\t" \
    "buffer_load_dwordx4 v240, %[wr], s44 offen offset:0 nt lds\n\t" \
    "buffer_load_dwordx4 v241, %[wr], s44 offen offset:1024 nt lds\n\t" \
    "buffer_load_dwordx4 v242, %[wr], s44 offen offset:2048 nt lds\n\t" \
    "buffer_load_dwordx4 v243, %[wr], s44 offen offset:3072 nt lds\n\t" \
    "s_add_u32 m0, %[wm], 0x1000\n\t" \
    "s_nop 0\n\t" \
    "buffer_load_dwordx4 v244, %[wr], s44 offen nt lds\n\t" \
    "s_barrier \n\t" \
    "s_barrier \n\t" \
    "s_waitcnt vmcnt(15)\n\t" \
    "ds_read_b64_tr_b16 v[12:13], v245 offset:5120\n\t" \
    "ds_read_b64_tr_b16 v[36:37], v245 offset:5152\n\t" \
    "ds_read_b64_tr_b16 v[52:53], v245 offset:5184\n\t" \
    "ds_read_b64_tr_b16 v[76:77], v245 offset:5216\n\t" \
    "ds_read_b64_tr_b16 v[92:93], v245 offset:5248\n\t" \
    "ds_read_b64_tr_b16 v[116:117], v245 offset:5280\n\t" \
    "ds_read_b64_tr_b16 v[132:133], v245 offset:5312\n\t" \
    "ds_read_b64_tr_b16 v[156:157], v245 offset:5344\n\t" \
    "ds_read_b64_tr_b16 v[172:173], v245 offset:5376\n\t" \
    "ds_read_b64_tr_b16 v[196:197], v245 offset:5408\n\t" \
    "s_waitcnt lgkmcnt(0)\n\t" \
    "s_add_u32 m0, %[wm], 0x1400\n\t" \
    "s_mov_b32 s44, 0x2d000\n\t" \
    "buffer_load_dwordx4 v240, %[wr], s44 offen offset:0 nt lds\n\t" \
    "buffer_load_dwordx4 v241, %[wr], s44 offen offset:1024 nt lds\n\t" \
    "buffer_load_dwordx4 v242, %[wr], s44 offen offset:2048 nt lds\n\t" \
    "buffer_load_dwordx4 v243, %[wr], s44 offen offset:3072 nt lds\n\t" \
    "s_add_u32 m0, %[wm], 0x2400\n\t" \
    "s_nop 0\n\t" \
    "buffer_load_dwordx4 v244, %[wr], s44 offen nt lds\n\t" \
    "s_barrier \n\t" \
    "s_waitcnt vmcnt(15)\n\t" \
    "ds_read_b64_tr_b16 v[202:203], v245 offset:10240\n\t" \
    "ds_read_b64_tr_b16 v[18:19], v245 offset:10272\n\t" \
    "ds_read_b64_tr_b16 v[210:211], v245 offset:10304\n\t" \
    "ds_read_b64_tr_b16 v[58:59], v245 offset:10336\n\t" \
    "ds_read_b64_tr_b16 v[218:219], v245 offset:10368\n\t" \
    "ds_read_b64_tr_b16 v[98:99], v245 offset:10400\n\t" \
    "ds_read_b64_tr_b16 v[226:227], v245 offset:10432\n\t" \
    "ds_read_b64_tr_b16 v[138:139], v245 offset:10464\n\t" \
    "ds_read_b64_tr_b16 v[234:235], v245 offset:10496\n\t" \
    "ds_read_b64_tr_b16 v[178:179], v245 offset:10528\n\t" \
    "v_mov_b32 v246, 0\n\t" \
    "buffer_load_dword v246, v246, s[60:63], %[mo] offen sc1\n\t" \
    "s_waitcnt lgkmcnt(0)\n\t" \
    "s_add_u32 m0, %[wm], 0x2800\n\t" \
    "s_mov_b32 s44, 0x32000\n\t" \
    "buffer_load_dwordx4 v240, %[wr], s44 offen offset:0 nt lds\n\t" \
    "buffer_load_dwordx4 v241, %[wr], s44 offen offset:1024 nt lds\n\t" \
    "buffer_load_dwordx4 v242, %[wr], s44 offen offset:2048 nt lds\n\t" \
    "buffer_load_dwordx4 v243, %[wr], s44 offen offset:3072 nt lds\n\t" \
    "s_add_u32 m0, %[wm], 0x3800\n\t" \
    "s_nop 0\n\t" \
    "buffer_load_dwordx4 v244, %[wr], s44 offen nt lds\n\t" \
    "s_barrier \n\t" \
    "s_barrier \n\t" \
    "s_waitcnt vmcnt(15)\n\t" \
    "ds_read_b64_tr_b16 v[206:207], v245 offset:15360\n\t" \
    "ds_read_b64_tr_b16 v[22:23], v245 offset:15392\n\t" \
    "ds_read_b64_tr_b16 v[214:215], v245 offset:15424\n\t" \
    "ds_read_b64_tr_b16 v[62:63], v245 offset:15456\n\t" \
    "ds_read_b64_tr_b16 v[222:223], v245 offset:15488\n\t" \
    "ds_read_b64_tr_b16 v[102:103], v245 offset:15520\n\t" \
    "ds_read_b64_tr_b16 v[230:231], v245 offset:15552\n\t" \
    "ds_read_b64_tr_b16 v[142:143], v245 offset:15584\n\t" \
    "ds_read_b64_tr_b16 v[238:239], v245 offset:15616\n\t" \
    "ds_read_b64_tr_b16 v[182:183], v245 offset:15648\n\t" \
    "s_waitcnt lgkmcnt(0)\n\t" \
    "s_add_u32 m0, %[wm], 0x3c00\n\t" \
    "s_mov_b32 s44, 0x37000\n\t" \
    "buffer_load_dwordx4 v240, %[wr], s44 offen offset:0 nt lds\n\t" \
    "buffer_load_dwordx4 v241, %[wr], s44 offen offset:1024 nt lds\n\t" \
    "buffer_load_dwordx4 v242, %[wr], s44 offen offset:2048 nt lds\n\t" \
    "buffer_load_dwordx4 v243, %[wr], s44 offen offset:3072 nt lds\n\t" \
    "s_add_u32 m0, %[wm], 0x4c00\n\t" \
    "s_nop 0\n\t" \
    "buffer_load_dwordx4 v244, %[wr], s44 offen nt lds\n\t" \
    "s_barrier \n\t" \
    "s_barrier \n\t" \
    "s_waitcnt vmcnt(15)\n\t" \
    "ds_read_b64_tr_b16 v[2:3], v245 offset:0\n\t" \
    "ds_read_b64_tr_b16 v[26:27], v245 offset:32\n\t" \
    "ds_read_b64_tr_b16 v[42:43], v245 offset:64\n\t" \
    "ds_read_b64_tr_b16 v[66:67], v245 offset:96\n\t" \
    "ds_read_b64_tr_b16 v[82:83], v245 offset:128\n\t" \
    "ds_read_b64_tr_b16 v[106:107], v245 offset:160\n\t" \
    "ds_read_b64_tr_b16 v[122:123], v245 offset:192\n\t" \
    "ds_read_b64_tr_b16 v[146:147], v245 offset:224\n\t" \
    "ds_read_b64_tr_b16 v[162:163], v245 offset:256\n\t" \
    "ds_read_b64_tr_b16 v[186:187], v245 offset:288\n\t" \
    "s_barrier \n\t" \
    "s_waitcnt vmcnt(10)\n\t" \
    "ds_read_b64_tr_b16 v[6:7], v245 offset:5120\n\t" \
    "ds_read_b64_tr_b16 v[30:31], v245 offset:5152\n\t" \
    "ds_read_b64_tr_b16 v[46:47], v245 offset:5184\n\t" \
    "ds_read_b64_tr_b16 v[70:71], v245 offset:5216\n\t" \
    "ds_read_b64_tr_b16 v[86:87], v245 offset:5248\n\t" \
    "ds_read_b64_tr_b16 v[110:111], v245 offset:5280\n\t" \
    "ds_read_b64_tr_b16 v[126:127], v245 offset:5312\n\t" \
    "ds_read_b64_tr_b16 v[150:151], v245 offset:5344\n\t" \
    "ds_read_b64_tr_b16 v[166:167], v245 offset:5376\n\t" \
    "ds_read_b64_tr_b16 v[190:191], v245 offset:5408\n\t" \
    "s_barrier \n\t" \
    "s_barrier \n\t" \
    "s_waitcnt vmcnt(5)\n\t" \
    "ds_read_b64_tr_b16 v[10:11], v245 offset:10240\n\t" \
    "ds_read_b64_tr_b16 v[34:35], v245 offset:10272\n\t" \
    "ds_read_b64_tr_b16 v[50:51], v245 offset:10304\n\t" \
    "ds_read_b64_tr_b16 v[74:75], v245 offset:10336\n\t" \
    "ds_read_b64_tr_b16 v[90:91], v245 offset:10368\n\t" \
    "ds_read_b64_tr_b16 v[114:115], v245 offset:10400\n\t" \
    "ds_read_b64_tr_b16 v[130:131], v245 offset:10432\n\t" \
    "ds_read_b64_tr_b16 v[154:155], v245 offset:10464\n\t" \
    "ds_read_b64_tr_b16 v[170:171], v245 offset:10496\n\t" \
    "ds_read_b64_tr_b16 v[194:195], v245 offset:10528\n\t" \
    ".Lv41_%=:\n\t" \
    "v_readfirstlane_b32 s43, v246\n\t" \
    "s_nop 0\n\t" \
    "s_cmp_lg_u32 s43, -1\n\t" \
    "s_cbranch_scc1 .Lv42_%=\n\t" \
    "s_cmp_eq_u32 s41, 0\n\t" \
    "s_cbranch_scc1 .Lv42_%=\n\t" \
    "s_sub_u32 s41, s41, 1\n\t" \
    "s_sleep 4\n\t" \
    "v_mov_b32 v246, 0\n\t" \
    "buffer_load_dword v246, v246, s[60:63], %[mo] offen sc1\n\t" \
    "s_waitcnt vmcnt(0)\n\t" \
    "s_branch .Lv41_%=\n\t" \
    ".Lv42_%=:\n\t" \
    "v_mov_b32 v246, 0x8000\n\t" \
    "s_mov_b64 exec, 1\n\t" \
    "buffer_store_dword v246, v246, s[60:63], %[mo] offen sc1\n\t" \
    "s_mov_b64 exec, -1\n\t" \
    "s_and_b32 s44, s43, 0x5000\n\t" \
    "s_xor_b32 %[mb], %[mb], s44\n\t" \
    "s_xor_b32 %[fl], %[fl], s44\n\t" \
    "s_sub_u32 %[fl], %[fl], 0xc800\n\t" \
    "s_xor_b32 %[pb], %[pb], s44\n\t" \
    "s_and_b32 s43, s43, 0x2000\n\t" \
    "s_xor_b32 s43, s43, %[lx]\n\t" \
    "s_add_u32 s60, s60, s43\n\t" \
    "s_addc_u32 s61, s61, 0\n\t" \
    "s_barrier \n\t" \
    "s_barrier \n\t" \
    "s_waitcnt vmcnt(0)\n\t" \
    "ds_read_b64_tr_b16 v[14:15], v245 offset:15360\n\t" \
    "ds_read_b64_tr_b16 v[38:39], v245 offset:15392\n\t" \
    "ds_read_b64_tr_b16 v[54:55], v245 offset:15424\n\t" \
    "ds_read_b64_tr_b16 v[78:79], v245 offset:15456\n\t" \
    "ds_read_b64_tr_b16 v[94:95], v245 offset:15488\n\t" \
    "ds_read_b64_tr_b16 v[118:119], v245 offset:15520\n\t" \
    "ds_read_b64_tr_b16 v[134:135], v245 offset:15552\n\t" \
    "ds_read_b64_tr_b16 v[158:159], v245 offset:15584\n\t" \
    "ds_read_b64_tr_b16 v[174:175], v245 offset:15616\n\t" \
    "ds_read_b64_tr_b16 v[198:199], v245 offset:15648\n\t" \
    "s_waitcnt lgkmcnt(0)\n\t" \
    "s_barrier \n\t" \
    "ds_write_b128 v254, v[200:203] offset:0\n\t" \
    "ds_write_b128 v254, v[204:207] offset:1024\n\t" \
    "ds_write_b128 v254, v[208:211] offset:2048\n\t" \
    "ds_write_b128 v254, v[212:215] offset:3072\n\t" \
    "ds_write_b128 v254, v[216:219] offset:4096\n\t" \
    "ds_write_b128 v254, v[220:223] offset:5120\n\t" \
    "ds_write_b128 v254, v[224:227] offset:6144\n\t" \
    "ds_write_b128 v254, v[228:231] offset:7168\n\t" \
    "ds_write_b128 v254, v[232:235] offset:8192\n\t" \
    "ds_write_b128 v254, v[236:239] offset:9216\n\t" \
    "s_waitcnt vmcnt(0) lgkmcnt(0)\n\t" \
    "s_barrier \n\t" \
    "v_mov_b64 v[200:201], 0\n\t" \
    "v_mov_b64 v[202:203], 0\n\t" \
    "ds_write_b128 v253, v[200:203] offset:3072\n\t" \
    "ds_write_b128 v253, v[200:203] offset:4096\n\t" \
    "v_bfe_i32 v252, v253, 5, 1\n\t" \
    "v_and_b32 v252, 0xaaaa, v252\n\t" \
    "v_or_b32 v252, 0x4444, v252\n\t" \
    ".Lv43_%=:\n\t" \
    "ds_read_b128 v[248:251], v254 offset:0\n\t" \
    "s_and_b32 s44, s40, %[vm]\n\t" \
    "s_lshl_b32 s44, s44, 20\n\t" \
    "s_and_b32 s45, s40, 2\n\t" \
    "s_lshl_b32 s45, s45, 12\n\t" \
    "s_add_u32 s44, s44, s45\n\t" \
    "s_add_u32 s42, s44, %[vx]\n\t" \
    "s_waitcnt lgkmcnt(0)\n\t" \
    "s_barrier \n\t" \
    "ds_read_b128 v[224:227], v253 offset:6144\n\t" \
    "ds_read_b128 v[232:235], v253 offset:8192\n\t" \
    "ds_read_b128 v[236:239], v253 offset:9216\n\t" \
    "ds_read_b128 v[240:243], v253 offset:10240\n\t" \
    "ds_read_b128 v[244:247], v253 offset:11264\n\t" \
    "ds_read_b128 v[228:231], v253 offset:7168\n\t" \
    "s_waitcnt lgkmcnt(5)\n\t" \
    "v_mfma_f32_16x16x32_bf16 v[200:203], v[224:227], v[16:19], 0\n\t" \
    "s_waitcnt lgkmcnt(4)\n\t" \
    "v_mfma_f32_16x16x32_bf16 v[200:203], v[232:235], v[24:27], v[200:203]\n\t" \
    "s_waitcnt lgkmcnt(3)\n\t" \
    "v_mfma_f32_16x16x32_bf16 v[200:203], v[236:239], v[28:31], v[200:203]\n\t" \
    "s_waitcnt lgkmcnt(2)\n\t" \
    "v_mfma_f32_16x16x32_bf16 v[200:203], v[240:243], v[32:35], v[200:203]\n\t" \
    "s_waitcnt lgkmcnt(1)\n\t" \
    "v_mfma_f32_16x16x32_bf16 v[200:203], v[244:247], v[36:39], v[200:203]\n\t" \
    "s_waitcnt lgkmcnt(0)\n\t" \
    "v_mfma_f32_16x16x32_bf16 v[200:203], v[228:231], v[20:23], v[200:203]\n\t" \
    "v_mfma_f32_16x16x32_bf16 v[204:207], v[224:227], v[248:251], 0\n\t" \
    "ds_read_b128 v[248:251], v254 offset:1024\n\t" \
    "v_mfma_f32_16x16x32_bf16 v[204:207], v[232:235], v[0:3], v[204:207]\n\t" \
    "v_mfma_f32_16x16x32_bf16 v[204:207], v[236:239], v[4:7], v[204:207]\n\t" \
    "v_mfma_f32_16x16x32_bf16 v[204:207], v[240:243], v[8:11], v[204:207]\n\t" \
    "v_mfma_f32_16x16x32_bf16 v[204:207], v[244:247], v[12:15], v[204:207]\n\t" \
    "s_waitcnt lgkmcnt(0)\n\t" \
    "v_mfma_f32_16x16x32_bf16 v[204:207], v[228:231], v[248:251], v[204:207]\n\t" \
    "ds_read_b128 v[248:251], v254 offset:2048\n\t" \
    "s_barrier \n\t" \
    "v_mfma_f32_16x16x32_bf16 v[208:211], v[224:227], v[56:59], 0\n\t" \
    "v_mfma_f32_16x16x32_bf16 v[208:211], v[232:235], v[64:67], v[208:211]\n\t" \
    "v_mfma_f32_16x16x32_bf16 v[208:211], v[236:239], v[68:71], v[208:211]\n\t" \
    "v_mfma_f32_16x16x32_bf16 v[208:211], v[240:243], v[72:75], v[208:211]\n\t" \
    "v_mfma_f32_16x16x32_bf16 v[208:211], v[244:247], v[76:79], v[208:211]\n\t" \
    "v_mfma_f32_16x16x32_bf16 v[208:211], v[228:231], v[60:63], v[208:211]\n\t" \
    "s_waitcnt lgkmcnt(0)\n\t" \
    "v_mfma_f32_16x16x32_bf16 v[212:215], v[224:227], v[248:251], 0\n\t" \
    "ds_read_b128 v[248:251], v254 offset:3072\n\t" \
    "ds_read_b128 v[224:227], v253 offset:12288\n\t" \
    "v_mfma_f32_16x16x32_bf16 v[212:215], v[232:235], v[40:43], v[212:215]\n\t" \
    "ds_read_b128 v[232:235], v253 offset:14336\n\t" \
    "v_mfma_f32_16x16x32_bf16 v[212:215], v[236:239], v[44:47], v[212:215]\n\t" \
    "ds_read_b128 v[236:239], v253 offset:15360\n\t" \
    "v_mfma_f32_16x16x32_bf16 v[212:215], v[240:243], v[48:51], v[212:215]\n\t" \
    "ds_read_b128 v[240:243], v253 offset:16384\n\t" \
    "v_mfma_f32_16x16x32_bf16 v[212:215], v[244:247], v[52:55], v[212:215]\n\t" \
    "ds_read_b128 v[244:247], v253 offset:17408\n\t" \
    "s_waitcnt lgkmcnt(5)\n\t" \
    "v_mfma_f32_16x16x32_bf16 v[212:215], v[228:231], v[248:251], v[212:215]\n\t" \
    "ds_read_b128 v[248:251], v254 offset:4096\n\t" \
    "ds_read_b128 v[228:231], v253 offset:13312\n\t" \
    "s_waitcnt lgkmcnt(6)\n\t" \
    "v_mfma_f32_16x16x32_bf16 v[216:219], v[224:227], v[16:19], 0\n\t" \
    "s_waitcnt lgkmcnt(5)\n\t" \
    "v_mfma_f32_16x16x32_bf16 v[216:219], v[232:235], v[24:27], v[216:219]\n\t" \
    "s_waitcnt lgkmcnt(4)\n\t" \
    "v_mfma_f32_16x16x32_bf16 v[216:219], v[236:239], v[28:31], v[216:219]\n\t" \
    "s_waitcnt lgkmcnt(3)\n\t" \
    "v_mfma_f32_16x16x32_bf16 v[216:219], v[240:243], v[32:35], v[216:219]\n\t" \
    "s_waitcnt lgkmcnt(2)\n\t" \
    "v_mfma_f32_16x16x32_bf16 v[216:219], v[244:247], v[36:39], v[216:219]\n\t" \
    "s_waitcnt lgkmcnt(0)\n\t" \
    "v_mfma_f32_16x16x32_bf16 v[216:219], v[228:231], v[20:23], v[216:219]\n\t" \
    "v_mfma_f32_16x16x32_bf16 v[220:223], v[224:227], v[248:251], 0\n\t" \
    "ds_read_b128 v[248:251], v254 offset:5120\n\t" \
    "v_mfma_f32_16x16x32_bf16 v[220:223], v[232:235], v[80:83], v[220:223]\n\t" \
    "v_mfma_f32_16x16x32_bf16 v[220:223], v[236:239], v[84:87], v[220:223]\n\t" \
    "v_cvt_pk_bf16_f32 v200, v200, v216\n\t" \
    "v_cvt_pk_bf16_f32 v201, v201, v217\n\t" \
    "v_mfma_f32_16x16x32_bf16 v[220:223], v[240:243], v[88:91], v[220:223]\n\t" \
    "v_cvt_pk_bf16_f32 v202, v202, v218\n\t" \
    "v_cvt_pk_bf16_f32 v203, v203, v219\n\t" \
    "v_mfma_f32_16x16x32_bf16 v[220:223], v[244:247], v[92:95], v[220:223]\n\t" \
    "s_add_u32 s44, s42, 0x10000\n\t" \
    "s_waitcnt lgkmcnt(0)\n\t" \
    "v_mfma_f32_16x16x32_bf16 v[220:223], v[228:231], v[248:251], v[220:223]\n\t" \
    "ds_read_b128 v[248:251], v254 offset:0\n\t" \
    "buffer_store_dwordx4 v[200:203], v253, s[60:63], s44 offen sc1\n\t" \
    "v_mfma_f32_16x16x32_bf16 v[216:219], v[224:227], v[56:59], 0\n\t" \
    "v_mfma_f32_16x16x32_bf16 v[216:219], v[232:235], v[64:67], v[216:219]\n\t" \
    "v_mfma_f32_16x16x32_bf16 v[216:219], v[236:239], v[68:71], v[216:219]\n\t" \
    "v_mfma_f32_16x16x32_bf16 v[216:219], v[240:243], v[72:75], v[216:219]\n\t" \
    "v_mfma_f32_16x16x32_bf16 v[216:219], v[244:247], v[76:79], v[216:219]\n\t" \
    "v_mfma_f32_16x16x32_bf16 v[216:219], v[228:231], v[60:63], v[216:219]\n\t" \
    "s_waitcnt lgkmcnt(0)\n\t" \
    "v_mfma_f32_16x16x32_bf16 v[200:203], v[224:227], v[248:251], 0\n\t" \
    "ds_read_b128 v[248:251], v254 offset:1024\n\t" \
    "v_mfma_f32_16x16x32_bf16 v[200:203], v[232:235], v[0:3], v[200:203]\n\t" \
    "v_mfma_f32_16x16x32_bf16 v[200:203], v[236:239], v[4:7], v[200:203]\n\t" \
    "v_cvt_pk_bf16_f32 v208, v208, v216\n\t" \
    "v_cvt_pk_bf16_f32 v209, v209, v217\n\t" \
    "v_mfma_f32_16x16x32_bf16 v[200:203], v[240:243], v[8:11], v[200:203]\n\t" \
    "v_cvt_pk_bf16_f32 v210, v210, v218\n\t" \
    "v_cvt_pk_bf16_f32 v211, v211, v219\n\t" \
    "v_mfma_f32_16x16x32_bf16 v[200:203], v[244:247], v[12:15], v[200:203]\n\t" \
    "s_add_u32 s44, s42, 0x30000\n\t" \
    "s_waitcnt lgkmcnt(0)\n\t" \
    "v_mfma_f32_16x16x32_bf16 v[200:203], v[228:231], v[248:251], v[200:203]\n\t" \
    "ds_read_b128 v[248:251], v254 offset:2048\n\t" \
    "buffer_store_dwordx4 v[208:211], v253, s[60:63], s44 offen sc1\n\t" \
    "v_mfma_f32_16x16x32_bf16 v[216:219], v[224:227], v[96:99], 0\n\t" \
    "v_mfma_f32_16x16x32_bf16 v[216:219], v[232:235], v[104:107], v[216:219]\n\t" \
    "v_mfma_f32_16x16x32_bf16 v[216:219], v[236:239], v[108:111], v[216:219]\n\t" \
    "v_cvt_pk_bf16_f32 v204, v204, v200\n\t" \
    "v_mfma_f32_16x16x32_bf16 v[216:219], v[240:243], v[112:115], v[216:219]\n\t" \
    "v_cvt_pk_bf16_f32 v205, v205, v201\n\t" \
    "v_cvt_pk_bf16_f32 v206, v206, v202\n\t" \
    "v_mfma_f32_16x16x32_bf16 v[216:219], v[244:247], v[116:119], v[216:219]\n\t" \
    "v_cvt_pk_bf16_f32 v207, v207, v203\n\t" \
    "v_mfma_f32_16x16x32_bf16 v[216:219], v[228:231], v[100:103], v[216:219]\n\t" \
    "s_add_u32 s44, s42, 0x0\n\t" \
    "buffer_store_dwordx4 v[204:207], v253, s[60:63], s44 offen sc1\n\t" \
    "s_waitcnt lgkmcnt(0)\n\t" \
    "v_mfma_f32_16x16x32_bf16 v[208:211], v[224:227], v[248:251], 0\n\t" \
    "ds_read_b128 v[248:251], v254 offset:3072\n\t" \
    "v_mfma_f32_16x16x32_bf16 v[208:211], v[232:235], v[40:43], v[208:211]\n\t" \
    "v_mfma_f32_16x16x32_bf16 v[208:211], v[236:239], v[44:47], v[208:211]\n\t" \
    "v_mfma_f32_16x16x32_bf16 v[208:211], v[240:243], v[48:51], v[208:211]\n\t" \
    "v_mfma_f32_16x16x32_bf16 v[208:211], v[244:247], v[52:55], v[208:211]\n\t" \
    "s_waitcnt lgkmcnt(0)\n\t" \
    "v_mfma_f32_16x16x32_bf16 v[208:211], v[228:231], v[248:251], v[208:211]\n\t" \
    "ds_read_b128 v[248:251], v254 offset:6144\n\t" \
    "v_mfma_f32_16x16x32_bf16 v[200:203], v[224:227], v[136:139], 0\n\t" \
    "v_mfma_f32_16x16x32_bf16 v[200:203], v[232:235], v[144:147], v[200:203]\n\t" \
    "v_mfma_f32_16x16x32_bf16 v[200:203], v[236:239], v[148:151], v[200:203]\n\t" \
    "v_cvt_pk_bf16_f32 v212, v212, v208\n\t" \
    "v_cvt_pk_bf16_f32 v213, v213, v209\n\t" \
    "v_mfma_f32_16x16x32_bf16 v[200:203], v[240:243], v[152:155], v[200:203]\n\t" \
    "v_cvt_pk_bf16_f32 v214, v214, v210\n\t" \
    "v_cvt_pk_bf16_f32 v215, v215, v211\n\t" \
    "v_mfma_f32_16x16x32_bf16 v[200:203], v[244:247], v[156:159], v[200:203]\n\t" \
    "s_add_u32 s44, s42, 0x20000\n\t" \
    "v_mfma_f32_16x16x32_bf16 v[200:203], v[228:231], v[140:143], v[200:203]\n\t" \
    "buffer_store_dwordx4 v[212:215], v253, s[60:63], s44 offen sc1\n\t" \
    "s_waitcnt lgkmcnt(0)\n\t" \
    "v_mfma_f32_16x16x32_bf16 v[204:207], v[224:227], v[248:251], 0\n\t" \
    "ds_read_b128 v[248:251], v254 offset:7168\n\t" \
    "ds_read_b128 v[224:227], v253 offset:6144\n\t" \
    "v_mfma_f32_16x16x32_bf16 v[204:207], v[232:235], v[120:123], v[204:207]\n\t" \
    "ds_read_b128 v[232:235], v253 offset:8192\n\t" \
    "v_mfma_f32_16x16x32_bf16 v[204:207], v[236:239], v[124:127], v[204:207]\n\t" \
    "ds_read_b128 v[236:239], v253 offset:9216\n\t" \
    "v_mfma_f32_16x16x32_bf16 v[204:207], v[240:243], v[128:131], v[204:207]\n\t" \
    "ds_read_b128 v[240:243], v253 offset:10240\n\t" \
    "v_mfma_f32_16x16x32_bf16 v[204:207], v[244:247], v[132:135], v[204:207]\n\t" \
    "ds_read_b128 v[244:247], v253 offset:11264\n\t" \
    "s_waitcnt lgkmcnt(5)\n\t" \
    "v_mfma_f32_16x16x32_bf16 v[204:207], v[228:231], v[248:251], v[204:207]\n\t" \
    "ds_read_b128 v[248:251], v254 offset:4096\n\t" \
    "ds_read_b128 v[228:231], v253 offset:7168\n\t" \
    "s_waitcnt lgkmcnt(6)\n\t" \
    "v_mfma_f32_16x16x32_bf16 v[208:211], v[224:227], v[96:99], 0\n\t" \
    "s_waitcnt lgkmcnt(5)\n\t" \
    "v_mfma_f32_16x16x32_bf16 v[208:211], v[232:235], v[104:107], v[208:211]\n\t" \
    "s_waitcnt lgkmcnt(4)\n\t" \
    "v_mfma_f32_16x16x32_bf16 v[208:211], v[236:239], v[108:111], v[208:211]\n\t" \
    "s_waitcnt lgkmcnt(3)\n\t" \
    "v_mfma_f32_16x16x32_bf16 v[208:211], v[240:243], v[112:115], v[208:211]\n\t" \
    "s_waitcnt lgkmcnt(2)\n\t" \
    "v_mfma_f32_16x16x32_bf16 v[208:211], v[244:247], v[116:119], v[208:211]\n\t" \
    "s_waitcnt lgkmcnt(0)\n\t" \
    "v_mfma_f32_16x16x32_bf16 v[208:211], v[228:231], v[100:103], v[208:211]\n\t" \
    "v_mfma_f32_16x16x32_bf16 v[212:215], v[224:227], v[248:251], 0\n\t" \
    "ds_read_b128 v[248:251], v254 offset:5120\n\t" \
    "v_mfma_f32_16x16x32_bf16 v[212:215], v[232:235], v[80:83], v[212:215]\n\t" \
    "v_mfma_f32_16x16x32_bf16 v[212:215], v[236:239], v[84:87], v[212:215]\n\t" \
    "v_cvt_pk_bf16_f32 v216, v208, v216\n\t" \
    "v_cvt_pk_bf16_f32 v217, v209, v217\n\t" \
    "v_mfma_f32_16x16x32_bf16 v[212:215], v[240:243], v[88:91], v[212:215]\n\t" \
    "v_cvt_pk_bf16_f32 v218, v210, v218\n\t" \
    "v_cvt_pk_bf16_f32 v219, v211, v219\n\t" \
    "v_mfma_f32_16x16x32_bf16 v[212:215], v[244:247], v[92:95], v[212:215]\n\t" \
    "s_add_u32 s44, s42, 0x50000\n\t" \
    "s_waitcnt lgkmcnt(0)\n\t" \
    "v_mfma_f32_16x16x32_bf16 v[212:215], v[228:231], v[248:251], v[212:215]\n\t" \
    "ds_read_b128 v[248:251], v254 offset:6144\n\t" \
    "buffer_store_dwordx4 v[216:219], v253, s[60:63], s44 offen sc1\n\t" \
    "v_mfma_f32_16x16x32_bf16 v[208:211], v[224:227], v[176:179], 0\n\t" \
    "v_mfma_f32_16x16x32_bf16 v[208:211], v[232:235], v[184:187], v[208:211]\n\t" \
    "v_mfma_f32_16x16x32_bf16 v[208:211], v[236:239], v[188:191], v[208:211]\n\t" \
    "v_cvt_pk_bf16_f32 v220, v212, v220\n\t" \
    "v_mfma_f32_16x16x32_bf16 v[208:211], v[240:243], v[192:195], v[208:211]\n\t" \
    "v_cvt_pk_bf16_f32 v221, v213, v221\n\t" \
    "v_cvt_pk_bf16_f32 v222, v214, v222\n\t" \
    "v_mfma_f32_16x16x32_bf16 v[208:211], v[244:247], v[196:199], v[208:211]\n\t" \
    "v_cvt_pk_bf16_f32 v223, v215, v223\n\t" \
    "v_mfma_f32_16x16x32_bf16 v[208:211], v[228:231], v[180:183], v[208:211]\n\t" \
    "s_add_u32 s44, s42, 0x40000\n\t" \
    "buffer_store_dwordx4 v[220:223], v253, s[60:63], s44 offen sc1\n\t" \
    "s_waitcnt lgkmcnt(0)\n\t" \
    "v_mfma_f32_16x16x32_bf16 v[216:219], v[224:227], v[248:251], 0\n\t" \
    "ds_read_b128 v[248:251], v254 offset:7168\n\t" \
    "v_mfma_f32_16x16x32_bf16 v[216:219], v[232:235], v[120:123], v[216:219]\n\t" \
    "v_mfma_f32_16x16x32_bf16 v[216:219], v[236:239], v[124:127], v[216:219]\n\t" \
    "v_mfma_f32_16x16x32_bf16 v[216:219], v[240:243], v[128:131], v[216:219]\n\t" \
    "v_mfma_f32_16x16x32_bf16 v[216:219], v[244:247], v[132:135], v[216:219]\n\t" \
    "s_waitcnt lgkmcnt(0)\n\t" \
    "v_mfma_f32_16x16x32_bf16 v[216:219], v[228:231], v[248:251], v[216:219]\n\t" \
    "ds_read_b128 v[248:251], v254 offset:8192\n\t" \
    "v_mfma_f32_16x16x32_bf16 v[212:215], v[224:227], v[136:139], 0\n\t" \
    "v_mfma_f32_16x16x32_bf16 v[212:215], v[232:235], v[144:147], v[212:215]\n\t" \
    "v_mfma_f32_16x16x32_bf16 v[212:215], v[236:239], v[148:151], v[212:215]\n\t" \
    "v_cvt_pk_bf16_f32 v204, v216, v204\n\t" \
    "v_cvt_pk_bf16_f32 v205, v217, v205\n\t" \
    "v_mfma_f32_16x16x32_bf16 v[212:215], v[240:243], v[152:155], v[212:215]\n\t" \
    "v_cvt_pk_bf16_f32 v206, v218, v206\n\t" \
    "v_cvt_pk_bf16_f32 v207, v219, v207\n\t" \
    "v_mfma_f32_16x16x32_bf16 v[212:215], v[244:247], v[156:159], v[212:215]\n\t" \
    "s_add_u32 s44, s42, 0x60000\n\t" \
    "v_mfma_f32_16x16x32_bf16 v[212:215], v[228:231], v[140:143], v[212:215]\n\t" \
    "buffer_store_dwordx4 v[204:207], v253, s[60:63], s44 offen sc1\n\t" \
    "s_waitcnt lgkmcnt(0)\n\t" \
    "v_mfma_f32_16x16x32_bf16 v[220:223], v[224:227], v[248:251], 0\n\t" \
    "ds_read_b128 v[248:251], v254 offset:9216\n\t" \
    "ds_read_b128 v[224:227], v253 offset:12288\n\t" \
    "v_mfma_f32_16x16x32_bf16 v[220:223], v[232:235], v[160:163], v[220:223]\n\t" \
    "ds_read_b128 v[232:235], v253 offset:14336\n\t" \
    "v_mfma_f32_16x16x32_bf16 v[220:223], v[236:239], v[164:167], v[220:223]\n\t" \
    "ds_read_b128 v[236:239], v253 offset:15360\n\t" \
    "v_cvt_pk_bf16_f32 v200, v212, v200\n\t" \
    "v_mfma_f32_16x16x32_bf16 v[220:223], v[240:243], v[168:171], v[220:223]\n\t" \
    "ds_read_b128 v[240:243], v253 offset:16384\n\t" \
    "v_cvt_pk_bf16_f32 v201, v213, v201\n\t" \
    "v_cvt_pk_bf16_f32 v202, v214, v202\n\t" \
    "v_mfma_f32_16x16x32_bf16 v[220:223], v[244:247], v[172:175], v[220:223]\n\t" \
    "ds_read_b128 v[244:247], v253 offset:17408\n\t" \
    "v_cvt_pk_bf16_f32 v203, v215, v203\n\t" \
    "s_waitcnt lgkmcnt(5)\n\t" \
    "v_mfma_f32_16x16x32_bf16 v[220:223], v[228:231], v[248:251], v[220:223]\n\t" \
    "ds_read_b128 v[248:251], v254 offset:8192\n\t" \
    "ds_read_b128 v[228:231], v253 offset:13312\n\t" \
    "s_add_u32 s44, s42, 0x70000\n\t" \
    "buffer_store_dwordx4 v[200:203], v253, s[60:63], s44 offen sc1\n\t" \
    "s_waitcnt lgkmcnt(6)\n\t" \
    "v_mfma_f32_16x16x32_bf16 v[216:219], v[224:227], v[176:179], 0\n\t" \
    "s_waitcnt lgkmcnt(5)\n\t" \
    "v_mfma_f32_16x16x32_bf16 v[216:219], v[232:235], v[184:187], v[216:219]\n\t" \
    "s_waitcnt lgkmcnt(4)\n\t" \
    "v_mfma_f32_16x16x32_bf16 v[216:219], v[236:239], v[188:191], v[216:219]\n\t" \
    "s_waitcnt lgkmcnt(3)\n\t" \
    "v_mfma_f32_16x16x32_bf16 v[216:219], v[240:243], v[192:195], v[216:219]\n\t" \
    "s_waitcnt lgkmcnt(2)\n\t" \
    "v_mfma_f32_16x16x32_bf16 v[216:219], v[244:247], v[196:199], v[216:219]\n\t" \
    "s_waitcnt lgkmcnt(0)\n\t" \
    "v_mfma_f32_16x16x32_bf16 v[216:219], v[228:231], v[180:183], v[216:219]\n\t" \
    "v_mfma_f32_16x16x32_bf16 v[204:207], v[224:227], v[248:251], 0\n\t" \
    "ds_read_b128 v[248:251], v254 offset:9216\n\t" \
    "v_mfma_f32_16x16x32_bf16 v[204:207], v[232:235], v[160:163], v[204:207]\n\t" \
    "v_mfma_f32_16x16x32_bf16 v[204:207], v[236:239], v[164:167], v[204:207]\n\t" \
    "v_cvt_pk_bf16_f32 v208, v208, v216\n\t" \
    "v_cvt_pk_bf16_f32 v209, v209, v217\n\t" \
    "v_mfma_f32_16x16x32_bf16 v[204:207], v[240:243], v[168:171], v[204:207]\n\t" \
    "v_cvt_pk_bf16_f32 v210, v210, v218\n\t" \
    "v_cvt_pk_bf16_f32 v211, v211, v219\n\t" \
    "v_mfma_f32_16x16x32_bf16 v[204:207], v[244:247], v[172:175], v[204:207]\n\t" \
    "s_add_u32 s44, s42, 0x90000\n\t" \
    "s_waitcnt lgkmcnt(0)\n\t" \
    "v_mfma_f32_16x16x32_bf16 v[204:207], v[228:231], v[248:251], v[204:207]\n\t" \
    "buffer_store_dwordx4 v[208:211], v253, s[60:63], s44 offen sc1\n\t" \
    "s_nop 6\n\t" \
    "v_cvt_pk_bf16_f32 v220, v220, v204\n\t" \
    "v_cvt_pk_bf16_f32 v221, v221, v205\n\t" \
    "v_cvt_pk_bf16_f32 v222, v222, v206\n\t" \
    "v_cvt_pk_bf16_f32 v223, v223, v207\n\t" \
    "s_add_u32 s44, s42, 0x80000\n\t" \
    "buffer_store_dwordx4 v[220:223], v253, s[60:63], s44 offen sc1\n\t" \
    "s_add_u32 s44, s40, 1\n\t" \
    "s_mov_b32 s45, s44\n\t" \
    "v_mov_b64 v[200:201], s[44:45]\n\t" \
    "v_mov_b64 v[202:203], s[44:45]\n\t" \
    "s_waitcnt lgkmcnt(0)\n\t" \
    "s_barrier \n\t" \
    "ds_read_b128 v[220:223], v253 offset:3072\n\t" \
    "ds_read_b128 v[224:227], v253 offset:3072\n\t" \
    "ds_read_b128 v[228:231], v253 offset:3072\n\t" \
    "ds_read_b128 v[232:235], v253 offset:3072\n\t" \
    "ds_read_b128 v[236:239], v253 offset:3072\n\t" \
    "ds_read_b128 v[240:243], v253 offset:3072\n\t" \
    "ds_read_b128 v[244:247], v253 offset:3072\n\t" \
    "ds_read_b128 v[248:251], v253 offset:3072\n\t" \
    "ds_read_b128 v[212:215], v254 offset:0\n\t" \
    "ds_read_b128 v[216:219], v254 offset:1024\n\t" \
    "s_and_b32 s44, s40, 3\n\t" \
    "s_lshl_b32 s44, s44, 22\n\t" \
    "s_add_u32 s42, s44, %[pb]\n\t" \
    "s_mov_b64 exec, 0xff\n\t" \
    "s_waitcnt vmcnt(0)\n\t" \
    "buffer_store_dwordx4 v[200:203], v253, s[60:63], %[fl] offen sc1\n\t" \
    "s_mov_b64 exec, -1\n\t" \
    "s_setprio 3\n\t" \
    "s_waitcnt lgkmcnt(0)\n\t" \
    "s_barrier \n\t" \
    "ds_read_b128 v[204:207], v253 offset:1024\n\t" \
    "ds_read_b128 v[208:211], v253 offset:2048\n\t" \
    "ds_read_b128 v[200:203], v253 offset:0\n\t" \
    "s_waitcnt lgkmcnt(2)\n\t" \
    "v_smfmac_f32_16x16x64_bf16 v[224:227], v[204:207], v[24:31], v252\n\t" \
    "v_smfmac_f32_16x16x64_bf16 v[220:223], v[204:207], v[0:7], v252\n\t" \
    "s_waitcnt lgkmcnt(1)\n\t" \
    "v_smfmac_f32_16x16x64_bf16 v[224:227], v[208:211], v[32:39], v252\n\t" \
    "v_smfmac_f32_16x16x64_bf16 v[220:223], v[208:211], v[8:15], v252\n\t" \
    "s_waitcnt lgkmcnt(0)\n\t" \
    "v_smfmac_f32_16x16x64_bf16 v[224:227], v[200:203], v[16:23], v252\n\t" \
    "v_smfmac_f32_16x16x64_bf16 v[220:223], v[200:203], v[212:219], v252\n\t" \
    "ds_read_b128 v[212:215], v254 offset:2048\n\t" \
    "ds_read_b128 v[216:219], v254 offset:3072\n\t" \
    "v_smfmac_f32_16x16x64_bf16 v[232:235], v[204:207], v[64:71], v252\n\t" \
    "v_smfmac_f32_16x16x64_bf16 v[228:231], v[204:207], v[40:47], v252\n\t" \
    "v_smfmac_f32_16x16x64_bf16 v[232:235], v[208:211], v[72:79], v252\n\t" \
    "v_add_f32 v220, v220, v222\n\t" \
    "v_add_f32 v221, v221, v223\n\t" \
    "v_cvt_pk_bf16_f32 v220, v220, v221\n\t" \
    "v_smfmac_f32_16x16x64_bf16 v[228:231], v[208:211], v[48:55], v252\n\t" \
    "v_min_u32 v220, -2, v220\n\t" \
    "v_add_f32 v224, v224, v226\n\t" \
    "v_add_f32 v225, v225, v227\n\t" \
    "v_smfmac_f32_16x16x64_bf16 v[232:235], v[200:203], v[56:63], v252\n\t" \
    "v_cvt_pk_bf16_f32 v221, v224, v225\n\t" \
    "v_min_u32 v221, -2, v221\n\t" \
    "s_waitcnt lgkmcnt(0)\n\t" \
    "v_smfmac_f32_16x16x64_bf16 v[228:231], v[200:203], v[212:219], v252\n\t" \
    "ds_read_b128 v[212:215], v254 offset:4096\n\t" \
    "ds_read_b128 v[216:219], v254 offset:5120\n\t" \
    "v_smfmac_f32_16x16x64_bf16 v[240:243], v[204:207], v[104:111], v252\n\t" \
    "v_smfmac_f32_16x16x64_bf16 v[236:239], v[204:207], v[80:87], v252\n\t" \
    "v_smfmac_f32_16x16x64_bf16 v[240:243], v[208:211], v[112:119], v252\n\t" \
    "v_add_f32 v228, v228, v230\n\t" \
    "v_add_f32 v229, v229, v231\n\t" \
    "v_cvt_pk_bf16_f32 v222, v228, v229\n\t" \
    "v_smfmac_f32_16x16x64_bf16 v[236:239], v[208:211], v[88:95], v252\n\t" \
    "v_min_u32 v222, -2, v222\n\t" \
    "v_add_f32 v232, v232, v234\n\t" \
    "v_add_f32 v233, v233, v235\n\t" \
    "v_smfmac_f32_16x16x64_bf16 v[240:243], v[200:203], v[96:103], v252\n\t" \
    "v_cvt_pk_bf16_f32 v223, v232, v233\n\t" \
    "v_min_u32 v223, -2, v223\n\t" \
    "v_permlane32_swap_b32 v220, v222\n\t" \
    "s_waitcnt lgkmcnt(0)\n\t" \
    "v_smfmac_f32_16x16x64_bf16 v[236:239], v[200:203], v[212:219], v252\n\t" \
    "ds_read_b128 v[212:215], v254 offset:6144\n\t" \
    "ds_read_b128 v[216:219], v254 offset:7168\n\t" \
    "s_nop 0\n\t" \
    "v_permlane32_swap_b32 v221, v223\n\t" \
    "s_nop 1\n\t" \
    "v_permlane16_swap_b32 v220, v221\n\t" \
    "v_permlane16_swap_b32 v222, v223\n\t" \
    "buffer_store_dwordx4 v[220:223], v255, s[60:63], s42 offen sc1\n\t" \
    "s_add_u32 s42, s42, 0x20000\n\t" \
    "ds_read_b128 v[224:227], v253 offset:3072\n\t" \
    "ds_read_b128 v[228:231], v253 offset:3072\n\t" \
    "v_smfmac_f32_16x16x64_bf16 v[248:251], v[204:207], v[144:151], v252\n\t" \
    "v_smfmac_f32_16x16x64_bf16 v[244:247], v[204:207], v[120:127], v252\n\t" \
    "v_smfmac_f32_16x16x64_bf16 v[248:251], v[208:211], v[152:159], v252\n\t" \
    "v_add_f32 v236, v236, v238\n\t" \
    "v_add_f32 v237, v237, v239\n\t" \
    "v_cvt_pk_bf16_f32 v236, v236, v237\n\t" \
    "v_smfmac_f32_16x16x64_bf16 v[244:247], v[208:211], v[128:135], v252\n\t" \
    "v_min_u32 v236, -2, v236\n\t" \
    "v_add_f32 v240, v240, v242\n\t" \
    "v_add_f32 v241, v241, v243\n\t" \
    "v_smfmac_f32_16x16x64_bf16 v[248:251], v[200:203], v[136:143], v252\n\t" \
    "v_cvt_pk_bf16_f32 v237, v240, v241\n\t" \
    "v_min_u32 v237, -2, v237\n\t" \
    "s_waitcnt lgkmcnt(2)\n\t" \
    "v_smfmac_f32_16x16x64_bf16 v[244:247], v[200:203], v[212:219], v252\n\t" \
    "ds_read_b128 v[212:215], v254 offset:8192\n\t" \
    "ds_read_b128 v[216:219], v254 offset:9216\n\t" \
    "s_waitcnt lgkmcnt(2)\n\t" \
    "v_smfmac_f32_16x16x64_bf16 v[228:231], v[204:207], v[184:191], v252\n\t" \
    "v_smfmac_f32_16x16x64_bf16 v[224:227], v[204:207], v[160:167], v252\n\t" \
    "v_smfmac_f32_16x16x64_bf16 v[228:231], v[208:211], v[192:199], v252\n\t" \
    "v_and_b32 v232, 0xffff, v255\n\t" \
    "v_lshrrev_b32 v233, 16, v255\n\t" \
    "v_lshl_add_u32 v232, v233, 3, v232\n\t" \
    "v_smfmac_f32_16x16x64_bf16 v[224:227], v[208:211], v[168:175], v252\n\t" \
    "v_add_f32 v244, v244, v246\n\t" \
    "v_add_f32 v245, v245, v247\n\t" \
    "v_cvt_pk_bf16_f32 v238, v244, v245\n\t" \
    "v_smfmac_f32_16x16x64_bf16 v[228:231], v[200:203], v[176:183], v252\n\t" \
    "v_min_u32 v238, -2, v238\n\t" \
    "v_add_f32 v248, v248, v250\n\t" \
    "v_add_f32 v249, v249, v251\n\t" \
    "s_waitcnt lgkmcnt(0)\n\t" \
    "v_smfmac_f32_16x16x64_bf16 v[224:227], v[200:203], v[212:219], v252\n\t" \
    "v_cvt_pk_bf16_f32 v239, v248, v249\n\t" \
    "v_min_u32 v239, -2, v239\n\t" \
    "v_permlane32_swap_b32 v236, v238\n\t" \
    "s_nop 0\n\t" \
    "v_permlane32_swap_b32 v237, v239\n\t" \
    "s_nop 1\n\t" \
    "v_permlane16_swap_b32 v236, v237\n\t" \
    "v_permlane16_swap_b32 v238, v239\n\t" \
    "buffer_store_dwordx4 v[236:239], v255, s[60:63], s42 offen sc1\n\t" \
    "s_add_u32 s42, s42, 0x20000\n\t" \
    "v_add_f32 v228, v228, v230\n\t" \
    "v_add_f32 v229, v229, v231\n\t" \
    "v_add_f32 v224, v224, v226\n\t" \
    "v_add_f32 v225, v225, v227\n\t" \
    "v_cvt_pk_bf16_f32 v229, v228, v229\n\t" \
    "v_cvt_pk_bf16_f32 v228, v224, v225\n\t" \
    "v_min_u32 v229, -2, v229\n\t" \
    "v_min_u32 v228, -2, v228\n\t" \
    "s_nop 1\n\t" \
    "v_permlane16_swap_b32 v228, v229\n\t" \
    "buffer_store_dwordx2 v[228:229], v232, s[60:63], s42 offen sc1\n\t" \
    "s_setprio 0\n\t" \
    "s_add_u32 s40, s40, 1\n\t" \
    "s_cmp_lg_u32 s40, 64\n\t" \
    "s_cbranch_scc1 .Lv43_%=\n\t" \
    "s_branch .Lend%=\n\t" \
    ".Lv3_%=:\n\t" \
    "s_mov_b32 s42, %[xs]\n\t" \
    "v_add_u32 v244, 0xc000, v243\n\t" \
    "s_waitcnt vmcnt(12) lgkmcnt(0)\n\t" \
    "s_barrier \n\t" \
    "s_add_u32 m0, %[wm], 0x9000\n\t" \
    "s_mov_b32 s44, 0x1e0000\n\t" \
    "buffer_load_dwordx4 v240, %[wr], s44 offen offset:0 nt lds\n\t" \
    "buffer_load_dwordx4 v241, %[wr], s44 offen offset:1024 nt lds\n\t" \
    "buffer_load_dwordx4 v242, %[wr], s44 offen offset:2048 nt lds\n\t" \
    "ds_read_b64_tr_b16 v[208:209], v244 offset:0\n\t" \
    "ds_read_b64_tr_b16 v[210:211], v244 offset:1536\n\t" \
    "ds_read_b64_tr_b16 v[40:41], v244 offset:32\n\t" \
    "ds_read_b64_tr_b16 v[42:43], v244 offset:1568\n\t" \
    "ds_read_b64_tr_b16 v[48:49], v244 offset:64\n\t" \
    "ds_read_b64_tr_b16 v[50:51], v244 offset:1600\n\t" \
    "v_add_u32 v244, 0xf000, v243\n\t" \
    "s_waitcnt vmcnt(12) lgkmcnt(0)\n\t" \
    "s_barrier \n\t" \
    "s_add_u32 m0, %[wm], 0xc000\n\t" \
    "s_mov_b32 s44, 0x240000\n\t" \
    "buffer_load_dwordx4 v240, %[wr], s44 offen offset:0 nt lds\n\t" \
    "buffer_load_dwordx4 v241, %[wr], s44 offen offset:1024 nt lds\n\t" \
    "buffer_load_dwordx4 v242, %[wr], s44 offen offset:2048 nt lds\n\t" \
    "ds_read_b64_tr_b16 v[212:213], v244 offset:0\n\t" \
    "ds_read_b64_tr_b16 v[214:215], v244 offset:1536\n\t" \
    "ds_read_b64_tr_b16 v[44:45], v244 offset:32\n\t" \
    "ds_read_b64_tr_b16 v[46:47], v244 offset:1568\n\t" \
    "ds_read_b64_tr_b16 v[52:53], v244 offset:64\n\t" \
    "ds_read_b64_tr_b16 v[54:55], v244 offset:1600\n\t" \
    "v_add_u32 v244, 0x0, v243\n\t" \
    "s_waitcnt vmcnt(12) lgkmcnt(0)\n\t" \
    "s_barrier \n\t" \
    "s_add_u32 m0, %[wm], 0xf000\n\t" \
    "s_mov_b32 s44, 0x2a0000\n\t" \
    "buffer_load_dwordx4 v240, %[wr], s44 offen offset:0 nt lds\n\t" \
    "buffer_load_dwordx4 v241, %[wr], s44 offen offset:1024 nt lds\n\t" \
    "buffer_load_dwordx4 v242, %[wr], s44 offen offset:2048 nt lds\n\t" \
    "ds_read_b64_tr_b16 v[56:57], v244 offset:0\n\t" \
    "ds_read_b64_tr_b16 v[58:59], v244 offset:1536\n\t" \
    "ds_read_b64_tr_b16 v[64:65], v244 offset:32\n\t" \
    "ds_read_b64_tr_b16 v[66:67], v244 offset:1568\n\t" \
    "ds_read_b64_tr_b16 v[72:73], v244 offset:64\n\t" \
    "ds_read_b64_tr_b16 v[74:75], v244 offset:1600\n\t" \
    "v_add_u32 v244, 0x3000, v243\n\t" \
    "s_waitcnt vmcnt(12) lgkmcnt(0)\n\t" \
    "s_barrier \n\t" \
    "s_add_u32 m0, %[wm], 0x0\n\t" \
    "s_mov_b32 s44, 0x300000\n\t" \
    "buffer_load_dwordx4 v240, %[wr], s44 offen offset:0 nt lds\n\t" \
    "buffer_load_dwordx4 v241, %[wr], s44 offen offset:1024 nt lds\n\t" \
    "buffer_load_dwordx4 v242, %[wr], s44 offen offset:2048 nt lds\n\t" \
    "ds_read_b64_tr_b16 v[60:61], v244 offset:0\n\t" \
    "ds_read_b64_tr_b16 v[62:63], v244 offset:1536\n\t" \
    "ds_read_b64_tr_b16 v[68:69], v244 offset:32\n\t" \
    "ds_read_b64_tr_b16 v[70:71], v244 offset:1568\n\t" \
    "ds_read_b64_tr_b16 v[76:77], v244 offset:64\n\t" \
    "ds_read_b64_tr_b16 v[78:79], v244 offset:1600\n\t" \
    "v_add_u32 v244, 0x6000, v243\n\t" \
    "s_waitcnt vmcnt(12) lgkmcnt(0)\n\t" \
    "s_barrier \n\t" \
    "s_add_u32 m0, %[wm], 0x3000\n\t" \
    "s_mov_b32 s44, 0x360000\n\t" \
    "buffer_load_dwordx4 v240, %[wr], s44 offen offset:0 nt lds\n\t" \
    "buffer_load_dwordx4 v241, %[wr], s44 offen offset:1024 nt lds\n\t" \
    "buffer_load_dwordx4 v242, %[wr], s44 offen offset:2048 nt lds\n\t" \
    "ds_read_b64_tr_b16 v[216:217], v244 offset:0\n\t" \
    "ds_read_b64_tr_b16 v[218:219], v244 offset:1536\n\t" \
    "ds_read_b64_tr_b16 v[80:81], v244 offset:32\n\t" \
    "ds_read_b64_tr_b16 v[82:83], v244 offset:1568\n\t" \
    "ds_read_b64_tr_b16 v[88:89], v244 offset:64\n\t" \
    "ds_read_b64_tr_b16 v[90:91], v244 offset:1600\n\t" \
    "v_add_u32 v244, 0x9000, v243\n\t" \
    "s_waitcnt vmcnt(12) lgkmcnt(0)\n\t" \
    "s_barrier \n\t" \
    "s_add_u32 m0, %[wm], 0x6000\n\t" \
    "s_mov_b32 s44, 0x3c0000\n\t" \
    "buffer_load_dwordx4 v240, %[wr], s44 offen offset:0 nt lds\n\t" \
    "buffer_load_dwordx4 v241, %[wr], s44 offen offset:1024 nt lds\n\t" \
    "buffer_load_dwordx4 v242, %[wr], s44 offen offset:2048 nt lds\n\t" \
    "ds_read_b64_tr_b16 v[220:221], v244 offset:0\n\t" \
    "ds_read_b64_tr_b16 v[222:223], v244 offset:1536\n\t" \
    "ds_read_b64_tr_b16 v[84:85], v244 offset:32\n\t" \
    "ds_read_b64_tr_b16 v[86:87], v244 offset:1568\n\t" \
    "ds_read_b64_tr_b16 v[92:93], v244 offset:64\n\t" \
    "ds_read_b64_tr_b16 v[94:95], v244 offset:1600\n\t" \
    "v_add_u32 v244, 0xc000, v243\n\t" \
    "s_waitcnt vmcnt(12) lgkmcnt(0)\n\t" \
    "s_barrier \n\t" \
    "s_add_u32 m0, %[wm], 0x9000\n\t" \
    "s_mov_b32 s44, 0x420000\n\t" \
    "buffer_load_dwordx4 v240, %[wr], s44 offen offset:0 nt lds\n\t" \
    "buffer_load_dwordx4 v241, %[wr], s44 offen offset:1024 nt lds\n\t" \
    "buffer_load_dwordx4 v242, %[wr], s44 offen offset:2048 nt lds\n\t" \
    "ds_read_b64_tr_b16 v[96:97], v244 offset:0\n\t" \
    "ds_read_b64_tr_b16 v[98:99], v244 offset:1536\n\t" \
    "ds_read_b64_tr_b16 v[104:105], v244 offset:32\n\t" \
    "ds_read_b64_tr_b16 v[106:107], v244 offset:1568\n\t" \
    "ds_read_b64_tr_b16 v[112:113], v244 offset:64\n\t" \
    "ds_read_b64_tr_b16 v[114:115], v244 offset:1600\n\t" \
    "v_add_u32 v244, 0xf000, v243\n\t" \
    "s_waitcnt vmcnt(12) lgkmcnt(0)\n\t" \
    "s_barrier \n\t" \
    "s_add_u32 m0, %[wm], 0xc000\n\t" \
    "s_mov_b32 s44, 0x480000\n\t" \
    "buffer_load_dwordx4 v240, %[wr], s44 offen offset:0 nt lds\n\t" \
    "buffer_load_dwordx4 v241, %[wr], s44 offen offset:1024 nt lds\n\t" \
    "buffer_load_dwordx4 v242, %[wr], s44 offen offset:2048 nt lds\n\t" \
    "ds_read_b64_tr_b16 v[100:101], v244 offset:0\n\t" \
    "ds_read_b64_tr_b16 v[102:103], v244 offset:1536\n\t" \
    "ds_read_b64_tr_b16 v[108:109], v244 offset:32\n\t" \
    "ds_read_b64_tr_b16 v[110:111], v244 offset:1568\n\t" \
    "ds_read_b64_tr_b16 v[116:117], v244 offset:64\n\t" \
    "ds_read_b64_tr_b16 v[118:119], v244 offset:1600\n\t" \
    "v_add_u32 v244, 0x0, v243\n\t" \
    "s_waitcnt vmcnt(12) lgkmcnt(0)\n\t" \
    "s_barrier \n\t" \
    "v_mov_b32 v246, 0\n\t" \
    "buffer_load_dword v246, v246, s[60:63], %[mo] offen sc1\n\t" \
    "s_add_u32 m0, %[wm], 0xf000\n\t" \
    "s_mov_b32 s44, 0x4e0000\n\t" \
    "buffer_load_dwordx4 v240, %[wr], s44 offen offset:0 nt lds\n\t" \
    "buffer_load_dwordx4 v241, %[wr], s44 offen offset:1024 nt lds\n\t" \
    "buffer_load_dwordx4 v242, %[wr], s44 offen offset:2048 nt lds\n\t" \
    "ds_read_b64_tr_b16 v[224:225], v244 offset:0\n\t" \
    "ds_read_b64_tr_b16 v[226:227], v244 offset:1536\n\t" \
    "ds_read_b64_tr_b16 v[120:121], v244 offset:32\n\t" \
    "ds_read_b64_tr_b16 v[122:123], v244 offset:1568\n\t" \
    "ds_read_b64_tr_b16 v[128:129], v244 offset:64\n\t" \
    "ds_read_b64_tr_b16 v[130:131], v244 offset:1600\n\t" \
    "v_add_u32 v244, 0x3000, v243\n\t" \
    "s_waitcnt vmcnt(12) lgkmcnt(0)\n\t" \
    "s_barrier \n\t" \
    "s_add_u32 m0, %[wm], 0x0\n\t" \
    "s_mov_b32 s44, 0x540000\n\t" \
    "buffer_load_dwordx4 v240, %[wr], s44 offen offset:0 nt lds\n\t" \
    "buffer_load_dwordx4 v241, %[wr], s44 offen offset:1024 nt lds\n\t" \
    "buffer_load_dwordx4 v242, %[wr], s44 offen offset:2048 nt lds\n\t" \
    "ds_read_b64_tr_b16 v[228:229], v244 offset:0\n\t" \
    "ds_read_b64_tr_b16 v[230:231], v244 offset:1536\n\t" \
    "ds_read_b64_tr_b16 v[124:125], v244 offset:32\n\t" \
    "ds_read_b64_tr_b16 v[126:127], v244 offset:1568\n\t" \
    "ds_read_b64_tr_b16 v[132:133], v244 offset:64\n\t" \
    "ds_read_b64_tr_b16 v[134:135], v244 offset:1600\n\t" \
    "v_add_u32 v244, 0x6000, v243\n\t" \
    "s_waitcnt vmcnt(12) lgkmcnt(0)\n\t" \
    "s_barrier \n\t" \
    "s_add_u32 m0, %[wm], 0x3000\n\t" \
    "s_mov_b32 s44, 0x5a0000\n\t" \
    "buffer_load_dwordx4 v240, %[wr], s44 offen offset:0 nt lds\n\t" \
    "buffer_load_dwordx4 v241, %[wr], s44 offen offset:1024 nt lds\n\t" \
    "buffer_load_dwordx4 v242, %[wr], s44 offen offset:2048 nt lds\n\t" \
    "ds_read_b64_tr_b16 v[136:137], v244 offset:0\n\t" \
    "ds_read_b64_tr_b16 v[138:139], v244 offset:1536\n\t" \
    "ds_read_b64_tr_b16 v[144:145], v244 offset:32\n\t" \
    "ds_read_b64_tr_b16 v[146:147], v244 offset:1568\n\t" \
    "ds_read_b64_tr_b16 v[152:153], v244 offset:64\n\t" \
    "ds_read_b64_tr_b16 v[154:155], v244 offset:1600\n\t" \
    "v_add_u32 v244, 0x9000, v243\n\t" \
    "s_waitcnt vmcnt(12) lgkmcnt(0)\n\t" \
    "s_barrier \n\t" \
    "s_add_u32 m0, %[wm], 0x6000\n\t" \
    "s_mov_b32 s44, 0x600000\n\t" \
    "buffer_load_dwordx4 v240, %[wr], s44 offen offset:0 nt lds\n\t" \
    "buffer_load_dwordx4 v241, %[wr], s44 offen offset:1024 nt lds\n\t" \
    "buffer_load_dwordx4 v242, %[wr], s44 offen offset:2048 nt lds\n\t" \
    "ds_read_b64_tr_b16 v[140:141], v244 offset:0\n\t" \
    "ds_read_b64_tr_b16 v[142:143], v244 offset:1536\n\t" \
    "ds_read_b64_tr_b16 v[148:149], v244 offset:32\n\t" \
    "ds_read_b64_tr_b16 v[150:151], v244 offset:1568\n\t" \
    "ds_read_b64_tr_b16 v[156:157], v244 offset:64\n\t" \
    "ds_read_b64_tr_b16 v[158:159], v244 offset:1600\n\t" \
    "v_add_u32 v244, 0xc000, v243\n\t" \
    "s_waitcnt vmcnt(12) lgkmcnt(0)\n\t" \
    "s_barrier \n\t" \
    "s_add_u32 m0, %[wm], 0x9000\n\t" \
    "s_mov_b32 s44, 0x660000\n\t" \
    "buffer_load_dwordx4 v240, %[wr], s44 offen offset:0 nt lds\n\t" \
    "buffer_load_dwordx4 v241, %[wr], s44 offen offset:1024 nt lds\n\t" \
    "buffer_load_dwordx4 v242, %[wr], s44 offen offset:2048 nt lds\n\t" \
    "ds_read_b64_tr_b16 v[232:233], v244 offset:0\n\t" \
    "ds_read_b64_tr_b16 v[234:235], v244 offset:1536\n\t" \
    "ds_read_b64_tr_b16 v[160:161], v244 offset:32\n\t" \
    "ds_read_b64_tr_b16 v[162:163], v244 offset:1568\n\t" \
    "ds_read_b64_tr_b16 v[168:169], v244 offset:64\n\t" \
    "ds_read_b64_tr_b16 v[170:171], v244 offset:1600\n\t" \
    "v_add_u32 v244, 0xf000, v243\n\t" \
    "s_waitcnt vmcnt(12) lgkmcnt(0)\n\t" \
    "s_barrier \n\t" \
    ".Lv31_%=:\n\t" \
    "v_readfirstlane_b32 s43, v246\n\t" \
    "s_nop 0\n\t" \
    "s_cmp_lg_u32 s43, -1\n\t" \
    "s_cbranch_scc1 .Lv32_%=\n\t" \
    "s_cmp_eq_u32 s41, 0\n\t" \
    "s_cbranch_scc1 .Lv32_%=\n\t" \
    "s_sub_u32 s41, s41, 1\n\t" \
    "s_sleep 4\n\t" \
    "v_mov_b32 v246, 0\n\t" \
    "buffer_load_dword v246, v246, s[60:63], %[mo] offen sc1\n\t" \
    "s_waitcnt vmcnt(0)\n\t" \
    "s_branch .Lv31_%=\n\t" \
    ".Lv32_%=:\n\t" \
    "v_mov_b32 v246, 0x8000\n\t" \
    "s_mov_b64 exec, 1\n\t" \
    "buffer_store_dword v246, v246, s[60:63], %[mo] offen sc1\n\t" \
    "s_mov_b64 exec, -1\n\t" \
    "s_and_b32 s44, s43, 0x5000\n\t" \
    "s_xor_b32 %[mb], %[mb], s44\n\t" \
    "s_xor_b32 %[fl], %[fl], s44\n\t" \
    "s_and_b32 s43, s43, 0x2000\n\t" \
    "s_xor_b32 s43, s43, %[lx]\n\t" \
    "s_add_u32 s60, s60, s43\n\t" \
    "s_addc_u32 s61, s61, 0\n\t" \
    "v_and_b32 v252, 0xf0, v253\n\t" \
    "v_lshlrev_b32 v252, 3, v252\n\t" \
    "buffer_load_dword v252, v252, s[60:63], %[fl] offen sc1\n\t" \
    "s_add_u32 m0, %[wm], 0xc000\n\t" \
    "s_mov_b32 s44, 0x6c0000\n\t" \
    "buffer_load_dwordx4 v240, %[wr], s44 offen offset:0 nt lds\n\t" \
    "buffer_load_dwordx4 v241, %[wr], s44 offen offset:1024 nt lds\n\t" \
    "buffer_load_dwordx4 v242, %[wr], s44 offen offset:2048 nt lds\n\t" \
    "ds_read_b64_tr_b16 v[236:237], v244 offset:0\n\t" \
    "ds_read_b64_tr_b16 v[238:239], v244 offset:1536\n\t" \
    "ds_read_b64_tr_b16 v[164:165], v244 offset:32\n\t" \
    "ds_read_b64_tr_b16 v[166:167], v244 offset:1568\n\t" \
    "ds_read_b64_tr_b16 v[172:173], v244 offset:64\n\t" \
    "ds_read_b64_tr_b16 v[174:175], v244 offset:1600\n\t" \
    "v_add_u32 v244, 0x0, v243\n\t" \
    "s_waitcnt vmcnt(12) lgkmcnt(0)\n\t" \
    "s_barrier \n\t" \
    "s_add_u32 m0, %[wm], 0xf000\n\t" \
    "s_mov_b32 s44, 0x720000\n\t" \
    "buffer_load_dwordx4 v240, %[wr], s44 offen offset:0 nt lds\n\t" \
    "buffer_load_dwordx4 v241, %[wr], s44 offen offset:1024 nt lds\n\t" \
    "buffer_load_dwordx4 v242, %[wr], s44 offen offset:2048 nt lds\n\t" \
    "ds_read_b64_tr_b16 v[176:177], v244 offset:0\n\t" \
    "ds_read_b64_tr_b16 v[178:179], v244 offset:1536\n\t" \
    "ds_read_b64_tr_b16 v[184:185], v244 offset:32\n\t" \
    "ds_read_b64_tr_b16 v[186:187], v244 offset:1568\n\t" \
    "ds_read_b64_tr_b16 v[192:193], v244 offset:64\n\t" \
    "ds_read_b64_tr_b16 v[194:195], v244 offset:1600\n\t" \
    "v_add_u32 v244, 0x3000, v243\n\t" \
    "s_waitcnt vmcnt(12) lgkmcnt(0)\n\t" \
    "s_barrier \n\t" \
    "ds_read_b64_tr_b16 v[180:181], v244 offset:0\n\t" \
    "ds_read_b64_tr_b16 v[182:183], v244 offset:1536\n\t" \
    "ds_read_b64_tr_b16 v[188:189], v244 offset:32\n\t" \
    "ds_read_b64_tr_b16 v[190:191], v244 offset:1568\n\t" \
    "ds_read_b64_tr_b16 v[196:197], v244 offset:64\n\t" \
    "ds_read_b64_tr_b16 v[198:199], v244 offset:1600\n\t" \
    "v_add_u32 v244, 0x6000, v243\n\t" \
    "s_waitcnt vmcnt(9) lgkmcnt(0)\n\t" \
    "s_barrier \n\t" \
    "ds_read_b64_tr_b16 v[200:201], v244 offset:0\n\t" \
    "ds_read_b64_tr_b16 v[202:203], v244 offset:1536\n\t" \
    "ds_read_b64_tr_b16 v[0:1], v244 offset:32\n\t" \
    "ds_read_b64_tr_b16 v[2:3], v244 offset:1568\n\t" \
    "ds_read_b64_tr_b16 v[8:9], v244 offset:64\n\t" \
    "ds_read_b64_tr_b16 v[10:11], v244 offset:1600\n\t" \
    "v_add_u32 v244, 0x9000, v243\n\t" \
    "s_waitcnt vmcnt(6) lgkmcnt(0)\n\t" \
    "s_barrier \n\t" \
    "ds_read_b64_tr_b16 v[204:205], v244 offset:0\n\t" \
    "ds_read_b64_tr_b16 v[206:207], v244 offset:1536\n\t" \
    "ds_read_b64_tr_b16 v[4:5], v244 offset:32\n\t" \
    "ds_read_b64_tr_b16 v[6:7], v244 offset:1568\n\t" \
    "ds_read_b64_tr_b16 v[12:13], v244 offset:64\n\t" \
    "ds_read_b64_tr_b16 v[14:15], v244 offset:1600\n\t" \
    "v_add_u32 v244, 0xc000, v243\n\t" \
    "s_waitcnt vmcnt(3) lgkmcnt(0)\n\t" \
    "s_barrier \n\t" \
    "ds_read_b64_tr_b16 v[16:17], v244 offset:0\n\t" \
    "ds_read_b64_tr_b16 v[18:19], v244 offset:1536\n\t" \
    "ds_read_b64_tr_b16 v[24:25], v244 offset:32\n\t" \
    "ds_read_b64_tr_b16 v[26:27], v244 offset:1568\n\t" \
    "ds_read_b64_tr_b16 v[32:33], v244 offset:64\n\t" \
    "ds_read_b64_tr_b16 v[34:35], v244 offset:1600\n\t" \
    "s_mov_b32 s43, 0\n\t" \
    "v_cmp_eq_u32 vcc, 0, v252\n\t" \
    "s_cbranch_vccnz .Lv33_%=\n\t" \
    "s_mov_b32 m0, 0x3000\n\t" \
    "s_add_u32 s49, s42, 0x0\n\t" \
    "buffer_load_dwordx4 v253, s[60:63], s49 offen offset:0 sc1 lds\n\t" \
    "buffer_load_dwordx4 v253, s[60:63], s49 offen offset:1024 sc1 lds\n\t" \
    "buffer_load_dwordx4 v253, s[60:63], s49 offen offset:2048 sc1 lds\n\t" \
    "buffer_load_dwordx4 v253, s[60:63], s49 offen offset:3072 sc1 lds\n\t" \
    "s_mov_b32 m0, 0x4c00\n\t" \
    "s_add_u32 s49, s42, 0x4c00\n\t" \
    "buffer_load_dwordx4 v253, s[60:63], s49 offen offset:0 sc1 lds\n\t" \
    "s_mov_b32 m0, 0x8000\n\t" \
    "s_add_u32 s49, s42, 0x1000\n\t" \
    "buffer_load_dwordx4 v253, s[60:63], s49 offen offset:0 sc1 lds\n\t" \
    "buffer_load_dwordx4 v253, s[60:63], s49 offen offset:1024 sc1 lds\n\t" \
    "buffer_load_dwordx4 v253, s[60:63], s49 offen offset:2048 sc1 lds\n\t" \
    "buffer_load_dwordx4 v253, s[60:63], s49 offen offset:3072 sc1 lds\n\t" \
    "s_mov_b32 m0, 0x9c00\n\t" \
    "s_add_u32 s49, s42, 0x5c00\n\t" \
    "buffer_load_dwordx4 v253, s[60:63], s49 offen offset:0 sc1 lds\n\t" \
    "s_mov_b32 s43, 1\n\t" \
    ".Lv33_%=:\n\t" \
    "v_add_u32 v244, 0xf000, v243\n\t" \
    "s_waitcnt vmcnt(0) lgkmcnt(0)\n\t" \
    "s_barrier \n\t" \
    "ds_read_b64_tr_b16 v[20:21], v244 offset:0\n\t" \
    "ds_read_b64_tr_b16 v[22:23], v244 offset:1536\n\t" \
    "ds_read_b64_tr_b16 v[28:29], v244 offset:32\n\t" \
    "ds_read_b64_tr_b16 v[30:31], v244 offset:1568\n\t" \
    "ds_read_b64_tr_b16 v[36:37], v244 offset:64\n\t" \
    "ds_read_b64_tr_b16 v[38:39], v244 offset:1600\n\t" \
    "s_waitcnt lgkmcnt(0)\n\t" \
    "s_barrier \n\t" \
    "ds_write_b128 v254, v[200:203] offset:55296\n\t" \
    "ds_write_b128 v254, v[204:207] offset:56320\n\t" \
    "ds_write_b128 v254, v[208:211] offset:57344\n\t" \
    "ds_write_b128 v254, v[212:215] offset:58368\n\t" \
    "ds_write_b128 v254, v[216:219] offset:59392\n\t" \
    "ds_write_b128 v254, v[220:223] offset:60416\n\t" \
    "ds_write_b128 v254, v[224:227] offset:61440\n\t" \
    "ds_write_b128 v254, v[228:231] offset:62464\n\t" \
    "ds_write_b128 v254, v[232:235] offset:63488\n\t" \
    "ds_write_b128 v254, v[236:239] offset:64512\n\t" \
    "s_cmp_lg_u32 s43, 0\n\t" \
    "s_cbranch_scc1 .Lv34_%=\n\t" \
    "s_mov_b32 s44, 0\n\t" \
    ".Lv35_%=:\n\t" \
    "v_cmp_ge_u32 vcc, s44, v252\n\t" \
    "s_cbranch_vccz .Lv36_%=\n\t" \
    "s_cmp_eq_u32 s41, 0\n\t" \
    "s_cbranch_scc1 .Lv36_%=\n\t" \
    "s_sub_u32 s41, s41, 1\n\t" \
    "s_sleep 8\n\t" \
    "v_and_b32 v252, 0xf0, v253\n\t" \
    "v_lshlrev_b32 v252, 3, v252\n\t" \
    "buffer_load_dword v252, v252, s[60:63], %[fl] offen sc1\n\t" \
    "s_waitcnt vmcnt(0)\n\t" \
    "s_branch .Lv35_%=\n\t" \
    ".Lv36_%=:\n\t" \
    "s_mov_b32 m0, 0x3000\n\t" \
    "s_add_u32 s49, s42, 0x0\n\t" \
    "buffer_load_dwordx4 v253, s[60:63], s49 offen offset:0 sc1 lds\n\t" \
    "buffer_load_dwordx4 v253, s[60:63], s49 offen offset:1024 sc1 lds\n\t" \
    "buffer_load_dwordx4 v253, s[60:63], s49 offen offset:2048 sc1 lds\n\t" \
    "buffer_load_dwordx4 v253, s[60:63], s49 offen offset:3072 sc1 lds\n\t" \
    "s_mov_b32 m0, 0x4c00\n\t" \
    "s_add_u32 s49, s42, 0x4c00\n\t" \
    "buffer_load_dwordx4 v253, s[60:63], s49 offen offset:0 sc1 lds\n\t" \
    "s_mov_b32 m0, 0x8000\n\t" \
    "s_add_u32 s49, s42, 0x1000\n\t" \
    "buffer_load_dwordx4 v253, s[60:63], s49 offen offset:0 sc1 lds\n\t" \
    "buffer_load_dwordx4 v253, s[60:63], s49 offen offset:1024 sc1 lds\n\t" \
    "buffer_load_dwordx4 v253, s[60:63], s49 offen offset:2048 sc1 lds\n\t" \
    "buffer_load_dwordx4 v253, s[60:63], s49 offen offset:3072 sc1 lds\n\t" \
    "s_mov_b32 m0, 0x9c00\n\t" \
    "s_add_u32 s49, s42, 0x5c00\n\t" \
    "buffer_load_dwordx4 v253, s[60:63], s49 offen offset:0 sc1 lds\n\t" \
    ".Lv34_%=:\n\t" \
    "s_waitcnt vmcnt(0) lgkmcnt(0)\n\t" \
    "s_barrier \n\t" \
    "ds_read_b128 v[224:227], v253 offset:16384\n\t" \
    "ds_read_b128 v[244:247], v254 offset:55296\n\t" \
    "ds_read_b128 v[228:231], v253 offset:17408\n\t" \
    "ds_read_b128 v[232:235], v253 offset:36864\n\t" \
    "ds_read_b128 v[236:239], v253 offset:37888\n\t" \
    "ds_read_b128 v[248:251], v254 offset:56320\n\t" \
    ".Lv37_%=:\n\t" \
    "s_waitcnt lgkmcnt(5)\n\t" \
    "v_mfma_f32_16x16x32_bf16 v[204:207], v[0:3], v[224:227], 0\n\t" \
    "s_waitcnt lgkmcnt(3)\n\t" \
    "v_mfma_f32_16x16x32_bf16 v[204:207], v[4:7], v[228:231], v[204:207]\n\t" \
    "ds_read_b128 v[240:243], v253 offset:18432\n\t" \
    "v_mfma_f32_16x16x32_bf16 v[208:211], v[8:11], v[224:227], 0\n\t" \
    "v_mfma_f32_16x16x32_bf16 v[208:211], v[12:15], v[228:231], v[208:211]\n\t" \
    "v_mfma_f32_16x16x32_bf16 v[200:203], v[244:247], v[224:227], 0\n\t" \
    "s_waitcnt lgkmcnt(1)\n\t" \
    "v_mfma_f32_16x16x32_bf16 v[200:203], v[248:251], v[228:231], v[200:203]\n\t" \
    "v_mfma_f32_16x16x32_bf16 v[216:219], v[0:3], v[232:235], 0\n\t" \
    "ds_read_b128 v[224:227], v253 offset:19456\n\t" \
    "v_mfma_f32_16x16x32_bf16 v[216:219], v[4:7], v[236:239], v[216:219]\n\t" \
    "ds_read_b128 v[228:231], v253 offset:0\n\t" \
    "v_mfma_f32_16x16x32_bf16 v[220:223], v[8:11], v[232:235], 0\n\t" \
    "v_mfma_f32_16x16x32_bf16 v[220:223], v[12:15], v[236:239], v[220:223]\n\t" \
    "v_mfma_f32_16x16x32_bf16 v[212:215], v[244:247], v[232:235], 0\n\t" \
    "ds_read_b128 v[244:247], v254 offset:57344\n\t" \
    "v_mfma_f32_16x16x32_bf16 v[212:215], v[248:251], v[236:239], v[212:215]\n\t" \
    "ds_read_b128 v[248:251], v254 offset:58368\n\t" \
    "s_waitcnt lgkmcnt(4)\n\t" \
    "v_mfma_f32_16x16x32_bf16 v[204:207], v[24:27], v[240:243], v[204:207]\n\t" \
    "ds_read_b128 v[232:235], v253 offset:1024\n\t" \
    "s_waitcnt lgkmcnt(4)\n\t" \
    "v_mfma_f32_16x16x32_bf16 v[204:207], v[28:31], v[224:227], v[204:207]\n\t" \
    "ds_read_b128 v[236:239], v253 offset:20480\n\t" \
    "v_mfma_f32_16x16x32_bf16 v[208:211], v[32:35], v[240:243], v[208:211]\n\t" \
    "v_mfma_f32_16x16x32_bf16 v[208:211], v[36:39], v[224:227], v[208:211]\n\t" \
    "v_mfma_f32_16x16x32_bf16 v[200:203], v[16:19], v[240:243], v[200:203]\n\t" \
    "v_mfma_f32_16x16x32_bf16 v[200:203], v[20:23], v[224:227], v[200:203]\n\t" \
    "s_waitcnt lgkmcnt(4)\n\t" \
    "v_mfma_f32_16x16x32_bf16 v[204:207], v[40:43], v[228:231], v[204:207]\n\t" \
    "ds_read_b128 v[240:243], v253 offset:21504\n\t" \
    "s_waitcnt lgkmcnt(2)\n\t" \
    "v_mfma_f32_16x16x32_bf16 v[204:207], v[44:47], v[232:235], v[204:207]\n\t" \
    "ds_read_b128 v[224:227], v253 offset:2048\n\t" \
    "v_mfma_f32_16x16x32_bf16 v[208:211], v[48:51], v[228:231], v[208:211]\n\t" \
    "v_mfma_f32_16x16x32_bf16 v[208:211], v[52:55], v[232:235], v[208:211]\n\t" \
    "v_mfma_f32_16x16x32_bf16 v[200:203], v[244:247], v[228:231], v[200:203]\n\t" \
    "v_mfma_f32_16x16x32_bf16 v[200:203], v[248:251], v[232:235], v[200:203]\n\t" \
    "s_waitcnt lgkmcnt(2)\n\t" \
    "v_mfma_f32_16x16x32_bf16 v[216:219], v[40:43], v[236:239], v[216:219]\n\t" \
    "ds_read_b128 v[228:231], v253 offset:3072\n\t" \
    "s_add_u32 s44, s40, 1\n\t" \
    "s_waitcnt lgkmcnt(2)\n\t" \
    "v_mfma_f32_16x16x32_bf16 v[216:219], v[44:47], v[240:243], v[216:219]\n\t" \
    "ds_read_b128 v[232:235], v253 offset:4096\n\t" \
    "s_and_b32 s45, s44, 15\n\t" \
    "v_mfma_f32_16x16x32_bf16 v[220:223], v[48:51], v[236:239], v[220:223]\n\t" \
    "s_cmp_eq_u32 s45, 0\n\t" \
    "v_mfma_f32_16x16x32_bf16 v[220:223], v[52:55], v[240:243], v[220:223]\n\t" \
    "s_cselect_b32 s45, %[cj], %[cs]\n\t" \
    "v_mfma_f32_16x16x32_bf16 v[212:215], v[244:247], v[236:239], v[212:215]\n\t" \
    "ds_read_b128 v[244:247], v254 offset:59392\n\t" \
    "s_cmp_eq_u32 s44, 64\n\t" \
    "v_mfma_f32_16x16x32_bf16 v[212:215], v[248:251], v[240:243], v[212:215]\n\t" \
    "ds_read_b128 v[248:251], v254 offset:60416\n\t" \
    "s_cselect_b32 s45, 0, s45\n\t" \
    "s_waitcnt lgkmcnt(4)\n\t" \
    "v_mfma_f32_16x16x32_bf16 v[204:207], v[64:67], v[224:227], v[204:207]\n\t" \
    "ds_read_b128 v[236:239], v253 offset:5120\n\t" \
    "s_add_u32 s42, s42, s45\n\t" \
    "s_waitcnt lgkmcnt(4)\n\t" \
    "v_mfma_f32_16x16x32_bf16 v[204:207], v[68:71], v[228:231], v[204:207]\n\t" \
    "ds_read_b128 v[240:243], v253 offset:24576\n\t" \
    "v_mfma_f32_16x16x32_bf16 v[208:211], v[72:75], v[224:227], v[208:211]\n\t" \
    "v_mfma_f32_16x16x32_bf16 v[208:211], v[76:79], v[228:231], v[208:211]\n\t" \
    "v_mfma_f32_16x16x32_bf16 v[200:203], v[56:59], v[224:227], v[200:203]\n\t" \
    "v_mfma_f32_16x16x32_bf16 v[200:203], v[60:63], v[228:231], v[200:203]\n\t" \
    "s_waitcnt lgkmcnt(4)\n\t" \
    "v_mfma_f32_16x16x32_bf16 v[204:207], v[80:83], v[232:235], v[204:207]\n\t" \
    "ds_read_b128 v[224:227], v253 offset:25600\n\t" \
    "s_waitcnt lgkmcnt(2)\n\t" \
    "v_mfma_f32_16x16x32_bf16 v[204:207], v[84:87], v[236:239], v[204:207]\n\t" \
    "ds_read_b128 v[228:231], v253 offset:6144\n\t" \
    "v_mfma_f32_16x16x32_bf16 v[208:211], v[88:91], v[232:235], v[208:211]\n\t" \
    "v_mfma_f32_16x16x32_bf16 v[208:211], v[92:95], v[236:239], v[208:211]\n\t" \
    "v_mfma_f32_16x16x32_bf16 v[200:203], v[244:247], v[232:235], v[200:203]\n\t" \
    "v_mfma_f32_16x16x32_bf16 v[200:203], v[248:251], v[236:239], v[200:203]\n\t" \
    "s_waitcnt lgkmcnt(2)\n\t" \
    "v_mfma_f32_16x16x32_bf16 v[216:219], v[80:83], v[240:243], v[216:219]\n\t" \
    "ds_read_b128 v[232:235], v253 offset:7168\n\t" \
    "s_and_b32 s44, s40, 3\n\t" \
    "s_waitcnt lgkmcnt(2)\n\t" \
    "v_mfma_f32_16x16x32_bf16 v[216:219], v[84:87], v[224:227], v[216:219]\n\t" \
    "ds_read_b128 v[236:239], v253 offset:8192\n\t" \
    "s_lshl_b32 s44, s44, 22\n\t" \
    "v_mfma_f32_16x16x32_bf16 v[220:223], v[88:91], v[240:243], v[220:223]\n\t" \
    "s_add_u32 s44, s44, %[mb]\n\t" \
    "v_mfma_f32_16x16x32_bf16 v[220:223], v[92:95], v[224:227], v[220:223]\n\t" \
    "s_add_u32 s46, s44, 0x10000\n\t" \
    "v_mfma_f32_16x16x32_bf16 v[212:215], v[244:247], v[240:243], v[212:215]\n\t" \
    "ds_read_b128 v[244:247], v254 offset:61440\n\t" \
    "s_add_u32 s47, s44, 0x10400\n\t" \
    "v_mfma_f32_16x16x32_bf16 v[212:215], v[248:251], v[224:227], v[212:215]\n\t" \
    "ds_read_b128 v[248:251], v254 offset:62464\n\t" \
    "s_waitcnt lgkmcnt(4)\n\t" \
    "v_mfma_f32_16x16x32_bf16 v[204:207], v[104:107], v[228:231], v[204:207]\n\t" \
    "ds_read_b128 v[240:243], v253 offset:9216\n\t" \
    "s_waitcnt lgkmcnt(4)\n\t" \
    "v_mfma_f32_16x16x32_bf16 v[204:207], v[108:111], v[232:235], v[204:207]\n\t" \
    "ds_read_b128 v[224:227], v253 offset:28672\n\t" \
    "v_mfma_f32_16x16x32_bf16 v[208:211], v[112:115], v[228:231], v[208:211]\n\t" \
    "v_mfma_f32_16x16x32_bf16 v[208:211], v[116:119], v[232:235], v[208:211]\n\t" \
    "v_mfma_f32_16x16x32_bf16 v[200:203], v[96:99], v[228:231], v[200:203]\n\t" \
    "v_mfma_f32_16x16x32_bf16 v[200:203], v[100:103], v[232:235], v[200:203]\n\t" \
    "s_waitcnt lgkmcnt(4)\n\t" \
    "v_mfma_f32_16x16x32_bf16 v[204:207], v[120:123], v[236:239], v[204:207]\n\t" \
    "ds_read_b128 v[228:231], v253 offset:29696\n\t" \
    "s_waitcnt lgkmcnt(2)\n\t" \
    "v_mfma_f32_16x16x32_bf16 v[204:207], v[124:127], v[240:243], v[204:207]\n\t" \
    "ds_read_b128 v[232:235], v253 offset:10240\n\t" \
    "v_mfma_f32_16x16x32_bf16 v[208:211], v[128:131], v[236:239], v[208:211]\n\t" \
    "v_mfma_f32_16x16x32_bf16 v[208:211], v[132:135], v[240:243], v[208:211]\n\t" \
    "v_mfma_f32_16x16x32_bf16 v[200:203], v[244:247], v[236:239], v[200:203]\n\t" \
    "v_mfma_f32_16x16x32_bf16 v[200:203], v[248:251], v[240:243], v[200:203]\n\t" \
    "s_waitcnt lgkmcnt(2)\n\t" \
    "v_mfma_f32_16x16x32_bf16 v[216:219], v[120:123], v[224:227], v[216:219]\n\t" \
    "ds_read_b128 v[236:239], v253 offset:11264\n\t" \
    "s_waitcnt lgkmcnt(2)\n\t" \
    "v_mfma_f32_16x16x32_bf16 v[216:219], v[124:127], v[228:231], v[216:219]\n\t" \
    "ds_read_b128 v[240:243], v253 offset:12288\n\t" \
    "v_mfma_f32_16x16x32_bf16 v[220:223], v[128:131], v[224:227], v[220:223]\n\t" \
    "v_mfma_f32_16x16x32_bf16 v[220:223], v[132:135], v[228:231], v[220:223]\n\t" \
    "v_mfma_f32_16x16x32_bf16 v[212:215], v[244:247], v[224:227], v[212:215]\n\t" \
    "ds_read_b128 v[244:247], v254 offset:63488\n\t" \
    "v_mfma_f32_16x16x32_bf16 v[212:215], v[248:251], v[228:231], v[212:215]\n\t" \
    "ds_read_b128 v[248:251], v254 offset:64512\n\t" \
    "v_and_b32 v252, 0xf0, v253\n\t" \
    "v_lshlrev_b32 v252, 3, v252\n\t" \
    "buffer_load_dword v252, v252, s[60:63], %[fl] offen sc1\n\t" \
    "s_waitcnt lgkmcnt(4)\n\t" \
    "v_mfma_f32_16x16x32_bf16 v[204:207], v[144:147], v[232:235], v[204:207]\n\t" \
    "ds_read_b128 v[224:227], v253 offset:13312\n\t" \
    "s_waitcnt lgkmcnt(4)\n\t" \
    "v_mfma_f32_16x16x32_bf16 v[204:207], v[148:151], v[236:239], v[204:207]\n\t" \
    "ds_read_b128 v[228:231], v253 offset:32768\n\t" \
    "v_mfma_f32_16x16x32_bf16 v[208:211], v[152:155], v[232:235], v[208:211]\n\t" \
    "v_mfma_f32_16x16x32_bf16 v[208:211], v[156:159], v[236:239], v[208:211]\n\t" \
    "v_mfma_f32_16x16x32_bf16 v[200:203], v[136:139], v[232:235], v[200:203]\n\t" \
    "v_mfma_f32_16x16x32_bf16 v[200:203], v[140:143], v[236:239], v[200:203]\n\t" \
    "s_waitcnt lgkmcnt(4)\n\t" \
    "v_mfma_f32_16x16x32_bf16 v[204:207], v[160:163], v[240:243], v[204:207]\n\t" \
    "ds_read_b128 v[232:235], v253 offset:33792\n\t" \
    "s_waitcnt lgkmcnt(2)\n\t" \
    "v_mfma_f32_16x16x32_bf16 v[204:207], v[164:167], v[224:227], v[204:207]\n\t" \
    "ds_read_b128 v[236:239], v253 offset:14336\n\t" \
    "v_mfma_f32_16x16x32_bf16 v[208:211], v[168:171], v[240:243], v[208:211]\n\t" \
    "v_mfma_f32_16x16x32_bf16 v[208:211], v[172:175], v[224:227], v[208:211]\n\t" \
    "v_mfma_f32_16x16x32_bf16 v[200:203], v[244:247], v[240:243], v[200:203]\n\t" \
    "v_mfma_f32_16x16x32_bf16 v[200:203], v[248:251], v[224:227], v[200:203]\n\t" \
    "s_waitcnt lgkmcnt(2)\n\t" \
    "v_mfma_f32_16x16x32_bf16 v[216:219], v[160:163], v[228:231], v[216:219]\n\t" \
    "ds_read_b128 v[240:243], v253 offset:15360\n\t" \
    "s_waitcnt lgkmcnt(2)\n\t" \
    "v_mfma_f32_16x16x32_bf16 v[216:219], v[164:167], v[232:235], v[216:219]\n\t" \
    "ds_read_b128 v[224:227], v253 offset:38912\n\t" \
    "v_mfma_f32_16x16x32_bf16 v[220:223], v[168:171], v[228:231], v[220:223]\n\t" \
    "v_mfma_f32_16x16x32_bf16 v[220:223], v[172:175], v[232:235], v[220:223]\n\t" \
    "v_mfma_f32_16x16x32_bf16 v[212:215], v[244:247], v[228:231], v[212:215]\n\t" \
    "v_mfma_f32_16x16x32_bf16 v[212:215], v[248:251], v[232:235], v[212:215]\n\t" \
    "s_waitcnt lgkmcnt(2)\n\t" \
    "v_mfma_f32_16x16x32_bf16 v[204:207], v[184:187], v[236:239], v[204:207]\n\t" \
    "ds_read_b128 v[228:231], v253 offset:39936\n\t" \
    "s_waitcnt lgkmcnt(2)\n\t" \
    "v_mfma_f32_16x16x32_bf16 v[204:207], v[188:191], v[240:243], v[204:207]\n\t" \
    "ds_read_b128 v[232:235], v253 offset:22528\n\t" \
    "v_mfma_f32_16x16x32_bf16 v[208:211], v[192:195], v[236:239], v[208:211]\n\t" \
    "v_mfma_f32_16x16x32_bf16 v[208:211], v[196:199], v[240:243], v[208:211]\n\t" \
    "v_mfma_f32_16x16x32_bf16 v[200:203], v[176:179], v[236:239], v[200:203]\n\t" \
    "v_mfma_f32_16x16x32_bf16 v[200:203], v[180:183], v[240:243], v[200:203]\n\t" \
    "s_waitcnt lgkmcnt(2)\n\t" \
    "v_mfma_f32_16x16x32_bf16 v[216:219], v[24:27], v[224:227], v[216:219]\n\t" \
    "ds_read_b128 v[236:239], v253 offset:23552\n\t" \
    "s_waitcnt lgkmcnt(2)\n\t" \
    "v_mfma_f32_16x16x32_bf16 v[216:219], v[28:31], v[228:231], v[216:219]\n\t" \
    "ds_read_b128 v[240:243], v253 offset:26624\n\t" \
    "v_mfma_f32_16x16x32_bf16 v[220:223], v[32:35], v[224:227], v[220:223]\n\t" \
    "v_cvt_pk_bf16_f32 v204, v204, v205\n\t" \
    "v_mfma_f32_16x16x32_bf16 v[220:223], v[36:39], v[228:231], v[220:223]\n\t" \
    "v_cvt_pk_bf16_f32 v205, v206, v207\n\t" \
    "v_mfma_f32_16x16x32_bf16 v[212:215], v[16:19], v[224:227], v[212:215]\n\t" \
    "ds_write_b64 v254, v[204:205] offset:4104\n\t" \
    "v_mfma_f32_16x16x32_bf16 v[212:215], v[20:23], v[228:231], v[212:215]\n\t" \
    "v_cvt_pk_bf16_f32 v208, v208, v209\n\t" \
    "s_waitcnt lgkmcnt(3)\n\t" \
    "v_mfma_f32_16x16x32_bf16 v[216:219], v[64:67], v[232:235], v[216:219]\n\t" \
    "ds_read_b128 v[224:227], v253 offset:27648\n\t" \
    "v_cvt_pk_bf16_f32 v209, v210, v211\n\t" \
    "s_waitcnt lgkmcnt(3)\n\t" \
    "v_mfma_f32_16x16x32_bf16 v[216:219], v[68:71], v[236:239], v[216:219]\n\t" \
    "ds_read_b128 v[228:231], v253 offset:30720\n\t" \
    "ds_write_b64 v254, v[208:209] offset:5128\n\t" \
    "v_mfma_f32_16x16x32_bf16 v[220:223], v[72:75], v[232:235], v[220:223]\n\t" \
    "v_cvt_pk_bf16_f32 v200, v200, v201\n\t" \
    "v_mfma_f32_16x16x32_bf16 v[220:223], v[76:79], v[236:239], v[220:223]\n\t" \
    "v_cvt_pk_bf16_f32 v201, v202, v203\n\t" \
    "v_mfma_f32_16x16x32_bf16 v[212:215], v[56:59], v[232:235], v[212:215]\n\t" \
    "ds_write_b64 v254, v[200:201] offset:3080\n\t" \
    "v_mfma_f32_16x16x32_bf16 v[212:215], v[60:63], v[236:239], v[212:215]\n\t" \
    "s_waitcnt lgkmcnt(5)\n\t" \
    "v_mfma_f32_16x16x32_bf16 v[216:219], v[104:107], v[240:243], v[216:219]\n\t" \
    "ds_read_b128 v[232:235], v253 offset:31744\n\t" \
    "s_waitcnt lgkmcnt(4)\n\t" \
    "v_mfma_f32_16x16x32_bf16 v[216:219], v[108:111], v[224:227], v[216:219]\n\t" \
    "ds_read_b128 v[236:239], v253 offset:34816\n\t" \
    "v_mfma_f32_16x16x32_bf16 v[220:223], v[112:115], v[240:243], v[220:223]\n\t" \
    "v_mfma_f32_16x16x32_bf16 v[220:223], v[116:119], v[224:227], v[220:223]\n\t" \
    "v_mfma_f32_16x16x32_bf16 v[212:215], v[96:99], v[240:243], v[212:215]\n\t" \
    "v_mfma_f32_16x16x32_bf16 v[212:215], v[100:103], v[224:227], v[212:215]\n\t" \
    "ds_read_b128 v[240:243], v253 offset:35840\n\t" \
    "s_waitcnt lgkmcnt(3)\n\t" \
    "s_barrier \n\t" \
    "v_mfma_f32_16x16x32_bf16 v[216:219], v[144:147], v[228:231], v[216:219]\n\t" \
    "s_waitcnt lgkmcnt(2)\n\t" \
    "v_mfma_f32_16x16x32_bf16 v[216:219], v[148:151], v[232:235], v[216:219]\n\t" \
    "s_waitcnt lgkmcnt(1)\n\t" \
    "v_mfma_f32_16x16x32_bf16 v[216:219], v[184:187], v[236:239], v[216:219]\n\t" \
    "s_waitcnt lgkmcnt(0)\n\t" \
    "v_mfma_f32_16x16x32_bf16 v[216:219], v[188:191], v[240:243], v[216:219]\n\t" \
    "v_mfma_f32_16x16x32_bf16 v[220:223], v[152:155], v[228:231], v[220:223]\n\t" \
    "v_mfma_f32_16x16x32_bf16 v[220:223], v[156:159], v[232:235], v[220:223]\n\t" \
    "v_mfma_f32_16x16x32_bf16 v[220:223], v[192:195], v[236:239], v[220:223]\n\t" \
    "v_cvt_pk_bf16_f32 v216, v216, v217\n\t" \
    "v_cvt_pk_bf16_f32 v217, v218, v219\n\t" \
    "v_mfma_f32_16x16x32_bf16 v[220:223], v[196:199], v[240:243], v[220:223]\n\t" \
    "ds_write_b64 v254, v[216:217] offset:10248\n\t" \
    "v_mfma_f32_16x16x32_bf16 v[212:215], v[136:139], v[228:231], v[212:215]\n\t" \
    "v_mfma_f32_16x16x32_bf16 v[212:215], v[140:143], v[232:235], v[212:215]\n\t" \
    "v_mfma_f32_16x16x32_bf16 v[212:215], v[176:179], v[236:239], v[212:215]\n\t" \
    "v_cvt_pk_bf16_f32 v220, v220, v221\n\t" \
    "v_cvt_pk_bf16_f32 v221, v222, v223\n\t" \
    "v_mfma_f32_16x16x32_bf16 v[212:215], v[180:183], v[240:243], v[212:215]\n\t" \
    "ds_write_b64 v254, v[220:221] offset:11272\n\t" \
    "s_nop 6\n\t" \
    "v_cvt_pk_bf16_f32 v212, v212, v213\n\t" \
    "v_cvt_pk_bf16_f32 v213, v214, v215\n\t" \
    "ds_write_b64 v254, v[212:213] offset:9224\n\t" \
    "s_waitcnt vmcnt(0) lgkmcnt(0)\n\t" \
    "s_barrier \n\t" \
    "s_setprio 2\n\t" \
    "s_add_u32 s44, s40, 1\n\t" \
    "s_min_u32 s44, s44, 63\n\t" \
    ".Lv38_%=:\n\t" \
    "v_cmp_ge_u32 vcc, s44, v252\n\t" \
    "s_cbranch_vccz .Lv39_%=\n\t" \
    "s_cmp_eq_u32 s41, 0\n\t" \
    "s_cbranch_scc1 .Lv39_%=\n\t" \
    "s_sub_u32 s41, s41, 1\n\t" \
    "s_sleep 2\n\t" \
    "v_and_b32 v252, 0xf0, v253\n\t" \
    "v_lshlrev_b32 v252, 3, v252\n\t" \
    "buffer_load_dword v252, v252, s[60:63], %[fl] offen sc1\n\t" \
    "s_waitcnt vmcnt(0)\n\t" \
    "s_branch .Lv38_%=\n\t" \
    ".Lv39_%=:\n\t" \
    "s_mov_b32 m0, 0x3000\n\t" \
    "s_add_u32 s49, s42, 0x0\n\t" \
    "buffer_load_dwordx4 v253, s[60:63], s49 offen offset:0 sc1 lds\n\t" \
    "s_sleep 1\n\t" \
    "buffer_load_dwordx4 v253, s[60:63], s49 offen offset:1024 sc1 lds\n\t" \
    "s_sleep 1\n\t" \
    "buffer_load_dwordx4 v253, s[60:63], s49 offen offset:2048 sc1 lds\n\t" \
    "s_sleep 1\n\t" \
    "buffer_load_dwordx4 v253, s[60:63], s49 offen offset:3072 sc1 lds\n\t" \
    "s_sleep 1\n\t" \
    "s_mov_b32 m0, 0x4c00\n\t" \
    "s_add_u32 s49, s42, 0x4c00\n\t" \
    "buffer_load_dwordx4 v253, s[60:63], s49 offen offset:0 sc1 lds\n\t" \
    "s_sleep 1\n\t" \
    "s_mov_b32 m0, 0x8000\n\t" \
    "s_add_u32 s49, s42, 0x1000\n\t" \
    "buffer_load_dwordx4 v253, s[60:63], s49 offen offset:0 sc1 lds\n\t" \
    "s_sleep 1\n\t" \
    "buffer_load_dwordx4 v253, s[60:63], s49 offen offset:1024 sc1 lds\n\t" \
    "s_sleep 1\n\t" \
    "buffer_load_dwordx4 v253, s[60:63], s49 offen offset:2048 sc1 lds\n\t" \
    "s_sleep 1\n\t" \
    "buffer_load_dwordx4 v253, s[60:63], s49 offen offset:3072 sc1 lds\n\t" \
    "s_sleep 1\n\t" \
    "s_mov_b32 m0, 0x9c00\n\t" \
    "s_add_u32 s49, s42, 0x5c00\n\t" \
    "buffer_load_dwordx4 v253, s[60:63], s49 offen offset:0 sc1 lds\n\t" \
    "s_sleep 1\n\t" \
    "ds_read_b128 v[200:203], v254 offset:55296\n\t" \
    "ds_read_b128 v[204:207], v254 offset:56320\n\t" \
    "ds_read_b128 v[216:219], v254 offset:57344\n\t" \
    "ds_read_b128 v[220:223], v254 offset:58368\n\t" \
    "v_mov_b64 v[208:209], 0\n\t" \
    "v_mov_b64 v[210:211], 0\n\t" \
    "v_mov_b64 v[244:245], 0\n\t" \
    "v_mov_b64 v[246:247], 0\n\t" \
    "v_mov_b64 v[248:249], 0\n\t" \
    "v_mov_b64 v[250:251], 0\n\t" \
    "s_sleep 5\n\t" \
    "s_mov_b32 m0, 0xc000\n\t" \
    "s_nop 0\n\t" \
    "buffer_load_dwordx4 v253, s[60:63], s46 offen sc1 lds\n\t" \
    "s_mov_b32 m0, 0xc400\n\t" \
    "s_nop 0\n\t" \
    "buffer_load_dwordx4 v253, s[60:63], s47 offen sc1 lds\n\t" \
    "s_waitcnt lgkmcnt(0)\n\t" \
    "v_bfe_i32 v212, v253, 5, 1\n\t" \
    "v_and_b32 v212, 0xaaaa, v212\n\t" \
    "v_or_b32 v212, 0x4444, v212\n\t" \
    "v_and_b32 v213, 0xffff, v255\n\t" \
    "v_lshrrev_b32 v214, 16, v255\n\t" \
    ".Lv310_%=:\n\t" \
    "s_waitcnt vmcnt(0)\n\t" \
    "ds_read_b128 v[224:227], v253 offset:49152\n\t" \
    "ds_read_b128 v[228:231], v253 offset:50176\n\t" \
    "s_waitcnt lgkmcnt(1)\n\t" \
    "v_max_u32 v215, v224, v225\n\t" \
    "v_max3_u32 v215, v226, v227, v215\n\t" \
    "s_waitcnt lgkmcnt(0)\n\t" \
    "v_max3_u32 v215, v228, v229, v215\n\t" \
    "v_max3_u32 v215, v230, v231, v215\n\t" \
    "v_cmp_eq_u32 vcc, -1, v215\n\t" \
    "s_cbranch_vccz .Lv311_%=\n\t" \
    "v_max_u32 v215, v224, v225\n\t" \
    "v_max3_u32 v215, v226, v227, v215\n\t" \
    "v_cmp_eq_u32 vcc, -1, v215\n\t" \
    "s_cbranch_vccz .Lv312_%=\n\t" \
    "s_mov_b32 m0, 0xc000\n\t" \
    "s_nop 0\n\t" \
    "buffer_load_dwordx4 v253, s[60:63], s46 offen sc1 lds\n\t" \
    ".Lv312_%=:\n\t" \
    "v_max_u32 v215, v228, v229\n\t" \
    "v_max3_u32 v215, v230, v231, v215\n\t" \
    "v_cmp_eq_u32 vcc, -1, v215\n\t" \
    "s_cbranch_vccz .Lv313_%=\n\t" \
    "s_mov_b32 m0, 0xc400\n\t" \
    "s_nop 0\n\t" \
    "buffer_load_dwordx4 v253, s[60:63], s47 offen sc1 lds\n\t" \
    ".Lv313_%=:\n\t" \
    "s_cmp_eq_u32 s41, 0\n\t" \
    "s_cbranch_scc1 .Lv311_%=\n\t" \
    "s_sub_u32 s41, s41, 1\n\t" \
    "s_branch .Lv310_%=\n\t" \
    ".Lv311_%=:\n\t" \
    "s_waitcnt lgkmcnt(0)\n\t" \
    "s_barrier \n\t" \
    "ds_read_b128 v[232:235], v253 offset:40960\n\t" \
    "ds_read_b128 v[236:239], v253 offset:41984\n\t" \
    "ds_read_b128 v[240:243], v253 offset:43008\n\t" \
    "v_smfmac_f32_16x16x64_bf16 v[244:247], v[224:227], v[0:7], v212\n\t" \
    "v_smfmac_f32_16x16x64_bf16 v[248:251], v[224:227], v[8:15], v212\n\t" \
    "v_smfmac_f32_16x16x64_bf16 v[208:211], v[224:227], v[200:207], v212\n\t" \
    "ds_read_b128 v[200:203], v254 offset:59392\n\t" \
    "ds_read_b128 v[204:207], v254 offset:60416\n\t" \
    "ds_read_b128 v[224:227], v253 offset:44032\n\t" \
    "v_smfmac_f32_16x16x64_bf16 v[208:211], v[228:231], v[16:23], v212\n\t" \
    "v_smfmac_f32_16x16x64_bf16 v[244:247], v[228:231], v[24:31], v212\n\t" \
    "v_smfmac_f32_16x16x64_bf16 v[248:251], v[228:231], v[32:39], v212\n\t" \
    "ds_read_b128 v[228:231], v253 offset:45056\n\t" \
    "s_waitcnt lgkmcnt(6)\n\t" \
    "v_smfmac_f32_16x16x64_bf16 v[208:211], v[232:235], v[216:223], v212\n\t" \
    "ds_read_b128 v[216:219], v254 offset:61440\n\t" \
    "ds_read_b128 v[220:223], v254 offset:62464\n\t" \
    "v_smfmac_f32_16x16x64_bf16 v[244:247], v[232:235], v[40:47], v212\n\t" \
    "v_smfmac_f32_16x16x64_bf16 v[248:251], v[232:235], v[48:55], v212\n\t" \
    "ds_read_b128 v[232:235], v253 offset:46080\n\t" \
    "s_waitcnt lgkmcnt(8)\n\t" \
    "v_smfmac_f32_16x16x64_bf16 v[208:211], v[236:239], v[56:63], v212\n\t" \
    "v_smfmac_f32_16x16x64_bf16 v[244:247], v[236:239], v[64:71], v212\n\t" \
    "v_smfmac_f32_16x16x64_bf16 v[248:251], v[236:239], v[72:79], v212\n\t" \
    "ds_read_b128 v[236:239], v253 offset:47104\n\t" \
    "s_waitcnt lgkmcnt(6)\n\t" \
    "v_smfmac_f32_16x16x64_bf16 v[208:211], v[240:243], v[200:207], v212\n\t" \
    "ds_read_b128 v[200:203], v254 offset:63488\n\t" \
    "ds_read_b128 v[204:207], v254 offset:64512\n\t" \
    "v_smfmac_f32_16x16x64_bf16 v[244:247], v[240:243], v[80:87], v212\n\t" \
    "v_smfmac_f32_16x16x64_bf16 v[248:251], v[240:243], v[88:95], v212\n\t" \
    "ds_read_b128 v[240:243], v253 offset:48128\n\t" \
    "s_waitcnt lgkmcnt(8)\n\t" \
    "v_smfmac_f32_16x16x64_bf16 v[208:211], v[224:227], v[96:103], v212\n\t" \
    "v_smfmac_f32_16x16x64_bf16 v[244:247], v[224:227], v[104:111], v212\n\t" \
    "v_smfmac_f32_16x16x64_bf16 v[248:251], v[224:227], v[112:119], v212\n\t" \
    "s_waitcnt lgkmcnt(7)\n\t" \
    "v_smfmac_f32_16x16x64_bf16 v[244:247], v[228:231], v[120:127], v212\n\t" \
    "s_waitcnt lgkmcnt(4)\n\t" \
    "v_smfmac_f32_16x16x64_bf16 v[244:247], v[232:235], v[144:151], v212\n\t" \
    "s_waitcnt lgkmcnt(3)\n\t" \
    "v_smfmac_f32_16x16x64_bf16 v[244:247], v[236:239], v[160:167], v212\n\t" \
    "s_waitcnt lgkmcnt(0)\n\t" \
    "v_smfmac_f32_16x16x64_bf16 v[244:247], v[240:243], v[184:191], v212\n\t" \
    "v_smfmac_f32_16x16x64_bf16 v[248:251], v[228:231], v[128:135], v212\n\t" \
    "v_smfmac_f32_16x16x64_bf16 v[248:251], v[232:235], v[152:159], v212\n\t" \
    "v_smfmac_f32_16x16x64_bf16 v[248:251], v[236:239], v[168:175], v212\n\t" \
    "v_add_f32 v244, v244, v246\n\t" \
    "v_add_f32 v245, v245, v247\n\t" \
    "v_cvt_pk_bf16_f32 v244, v244, v245\n\t" \
    "v_smfmac_f32_16x16x64_bf16 v[248:251], v[240:243], v[192:199], v212\n\t" \
    "ds_write_b16 v213, v244 offset:512\n\t" \
    "ds_write_b16_d16_hi v213, v244 offset:528\n\t" \
    "v_smfmac_f32_16x16x64_bf16 v[208:211], v[228:231], v[216:223], v212\n\t" \
    "v_smfmac_f32_16x16x64_bf16 v[208:211], v[232:235], v[136:143], v212\n\t" \
    "v_smfmac_f32_16x16x64_bf16 v[208:211], v[236:239], v[200:207], v212\n\t" \
    "v_add_f32 v248, v248, v250\n\t" \
    "v_add_f32 v249, v249, v251\n\t" \
    "v_cvt_pk_bf16_f32 v248, v248, v249\n\t" \
    "v_smfmac_f32_16x16x64_bf16 v[208:211], v[240:243], v[176:183], v212\n\t" \
    "ds_write_b16 v213, v248 offset:1024\n\t" \
    "ds_write_b16_d16_hi v213, v248 offset:1040\n\t" \
    "s_nop 5\n\t" \
    "v_add_f32 v208, v208, v210\n\t" \
    "v_add_f32 v209, v209, v211\n\t" \
    "v_cvt_pk_bf16_f32 v208, v208, v209\n\t" \
    "ds_write_b16 v213, v208 offset:0\n\t" \
    "ds_write_b16_d16_hi v213, v208 offset:16\n\t" \
    "s_setprio 0\n\t" \
    "s_waitcnt lgkmcnt(0)\n\t" \
    "s_barrier \n\t" \
    "ds_read_b128 v[224:227], v253 offset:16384\n\t" \
    "ds_read_b128 v[244:247], v254 offset:55296\n\t" \
    "ds_read_b128 v[228:231], v253 offset:17408\n\t" \
    "ds_read_b128 v[232:235], v253 offset:36864\n\t" \
    "ds_read_b128 v[236:239], v253 offset:37888\n\t" \
    "ds_read_b128 v[248:251], v254 offset:56320\n\t" \
    "s_add_u32 s40, s40, 1\n\t" \
    "s_cmp_lg_u32 s40, 64\n\t" \
    "s_cbranch_scc1 .Lv37_%=\n\t" \
    "s_branch .Lend%=\n\t" \
    ".Lv2_%=:\n\t" \
    "s_mov_b32 s42, %[xs]\n\t" \
    "v_add_u32 v244, 0xc000, v243\n\t" \
    "s_waitcnt vmcnt(12) lgkmcnt(0)\n\t" \
    "s_barrier \n\t" \
    "s_add_u32 m0, %[wm], 0x9000\n\t" \
    "s_mov_b32 s44, 0x1e0000\n\t" \
    "buffer_load_dwordx4 v240, %[wr], s44 offen offset:0 nt lds\n\t" \
    "buffer_load_dwordx4 v241, %[wr], s44 offen offset:1024 nt lds\n\t" \
    "buffer_load_dwordx4 v242, %[wr], s44 offen offset:2048 nt lds\n\t" \
    "ds_read_b64_tr_b16 v[208:209], v244 offset:0\n\t" \
    "ds_read_b64_tr_b16 v[210:211], v244 offset:1536\n\t" \
    "ds_read_b64_tr_b16 v[40:41], v244 offset:32\n\t" \
    "ds_read_b64_tr_b16 v[42:43], v244 offset:1568\n\t" \
    "ds_read_b64_tr_b16 v[48:49], v244 offset:64\n\t" \
    "ds_read_b64_tr_b16 v[50:51], v244 offset:1600\n\t" \
    "v_add_u32 v244, 0xf000, v243\n\t" \
    "s_waitcnt vmcnt(12) lgkmcnt(0)\n\t" \
    "s_barrier \n\t" \
    "s_add_u32 m0, %[wm], 0xc000\n\t" \
    "s_mov_b32 s44, 0x240000\n\t" \
    "buffer_load_dwordx4 v240, %[wr], s44 offen offset:0 nt lds\n\t" \
    "buffer_load_dwordx4 v241, %[wr], s44 offen offset:1024 nt lds\n\t" \
    "buffer_load_dwordx4 v242, %[wr], s44 offen offset:2048 nt lds\n\t" \
    "ds_read_b64_tr_b16 v[212:213], v244 offset:0\n\t" \
    "ds_read_b64_tr_b16 v[214:215], v244 offset:1536\n\t" \
    "ds_read_b64_tr_b16 v[44:45], v244 offset:32\n\t" \
    "ds_read_b64_tr_b16 v[46:47], v244 offset:1568\n\t" \
    "ds_read_b64_tr_b16 v[52:53], v244 offset:64\n\t" \
    "ds_read_b64_tr_b16 v[54:55], v244 offset:1600\n\t" \
    "v_add_u32 v244, 0x0, v243\n\t" \
    "s_waitcnt vmcnt(12) lgkmcnt(0)\n\t" \
    "s_barrier \n\t" \
    "s_add_u32 m0, %[wm], 0xf000\n\t" \
    "s_mov_b32 s44, 0x2a0000\n\t" \
    "buffer_load_dwordx4 v240, %[wr], s44 offen offset:0 nt lds\n\t" \
    "buffer_load_dwordx4 v241, %[wr], s44 offen offset:1024 nt lds\n\t" \
    "buffer_load_dwordx4 v242, %[wr], s44 offen offset:2048 nt lds\n\t" \
    "ds_read_b64_tr_b16 v[56:57], v244 offset:0\n\t" \
    "ds_read_b64_tr_b16 v[58:59], v244 offset:1536\n\t" \
    "ds_read_b64_tr_b16 v[64:65], v244 offset:32\n\t" \
    "ds_read_b64_tr_b16 v[66:67], v244 offset:1568\n\t" \
    "ds_read_b64_tr_b16 v[72:73], v244 offset:64\n\t" \
    "ds_read_b64_tr_b16 v[74:75], v244 offset:1600\n\t" \
    "v_add_u32 v244, 0x3000, v243\n\t" \
    "s_waitcnt vmcnt(12) lgkmcnt(0)\n\t" \
    "s_barrier \n\t" \
    "s_add_u32 m0, %[wm], 0x0\n\t" \
    "s_mov_b32 s44, 0x300000\n\t" \
    "buffer_load_dwordx4 v240, %[wr], s44 offen offset:0 nt lds\n\t" \
    "buffer_load_dwordx4 v241, %[wr], s44 offen offset:1024 nt lds\n\t" \
    "buffer_load_dwordx4 v242, %[wr], s44 offen offset:2048 nt lds\n\t" \
    "ds_read_b64_tr_b16 v[60:61], v244 offset:0\n\t" \
    "ds_read_b64_tr_b16 v[62:63], v244 offset:1536\n\t" \
    "ds_read_b64_tr_b16 v[68:69], v244 offset:32\n\t" \
    "ds_read_b64_tr_b16 v[70:71], v244 offset:1568\n\t" \
    "ds_read_b64_tr_b16 v[76:77], v244 offset:64\n\t" \
    "ds_read_b64_tr_b16 v[78:79], v244 offset:1600\n\t" \
    "v_add_u32 v244, 0x6000, v243\n\t" \
    "s_waitcnt vmcnt(12) lgkmcnt(0)\n\t" \
    "s_barrier \n\t" \
    "s_add_u32 m0, %[wm], 0x3000\n\t" \
    "s_mov_b32 s44, 0x360000\n\t" \
    "buffer_load_dwordx4 v240, %[wr], s44 offen offset:0 nt lds\n\t" \
    "buffer_load_dwordx4 v241, %[wr], s44 offen offset:1024 nt lds\n\t" \
    "buffer_load_dwordx4 v242, %[wr], s44 offen offset:2048 nt lds\n\t" \
    "ds_read_b64_tr_b16 v[216:217], v244 offset:0\n\t" \
    "ds_read_b64_tr_b16 v[218:219], v244 offset:1536\n\t" \
    "ds_read_b64_tr_b16 v[80:81], v244 offset:32\n\t" \
    "ds_read_b64_tr_b16 v[82:83], v244 offset:1568\n\t" \
    "ds_read_b64_tr_b16 v[88:89], v244 offset:64\n\t" \
    "ds_read_b64_tr_b16 v[90:91], v244 offset:1600\n\t" \
    "v_add_u32 v244, 0x9000, v243\n\t" \
    "s_waitcnt vmcnt(12) lgkmcnt(0)\n\t" \
    "s_barrier \n\t" \
    "s_add_u32 m0, %[wm], 0x6000\n\t" \
    "s_mov_b32 s44, 0x3c0000\n\t" \
    "buffer_load_dwordx4 v240, %[wr], s44 offen offset:0 nt lds\n\t" \
    "buffer_load_dwordx4 v241, %[wr], s44 offen offset:1024 nt lds\n\t" \
    "buffer_load_dwordx4 v242, %[wr], s44 offen offset:2048 nt lds\n\t" \
    "ds_read_b64_tr_b16 v[220:221], v244 offset:0\n\t" \
    "ds_read_b64_tr_b16 v[222:223], v244 offset:1536\n\t" \
    "ds_read_b64_tr_b16 v[84:85], v244 offset:32\n\t" \
    "ds_read_b64_tr_b16 v[86:87], v244 offset:1568\n\t" \
    "ds_read_b64_tr_b16 v[92:93], v244 offset:64\n\t" \
    "ds_read_b64_tr_b16 v[94:95], v244 offset:1600\n\t" \
    "v_add_u32 v244, 0xc000, v243\n\t" \
    "s_waitcnt vmcnt(12) lgkmcnt(0)\n\t" \
    "s_barrier \n\t" \
    "s_add_u32 m0, %[wm], 0x9000\n\t" \
    "s_mov_b32 s44, 0x420000\n\t" \
    "buffer_load_dwordx4 v240, %[wr], s44 offen offset:0 nt lds\n\t" \
    "buffer_load_dwordx4 v241, %[wr], s44 offen offset:1024 nt lds\n\t" \
    "buffer_load_dwordx4 v242, %[wr], s44 offen offset:2048 nt lds\n\t" \
    "ds_read_b64_tr_b16 v[96:97], v244 offset:0\n\t" \
    "ds_read_b64_tr_b16 v[98:99], v244 offset:1536\n\t" \
    "ds_read_b64_tr_b16 v[104:105], v244 offset:32\n\t" \
    "ds_read_b64_tr_b16 v[106:107], v244 offset:1568\n\t" \
    "ds_read_b64_tr_b16 v[112:113], v244 offset:64\n\t" \
    "ds_read_b64_tr_b16 v[114:115], v244 offset:1600\n\t" \
    "v_add_u32 v244, 0xf000, v243\n\t" \
    "s_waitcnt vmcnt(12) lgkmcnt(0)\n\t" \
    "s_barrier \n\t" \
    "s_add_u32 m0, %[wm], 0xc000\n\t" \
    "s_mov_b32 s44, 0x480000\n\t" \
    "buffer_load_dwordx4 v240, %[wr], s44 offen offset:0 nt lds\n\t" \
    "buffer_load_dwordx4 v241, %[wr], s44 offen offset:1024 nt lds\n\t" \
    "buffer_load_dwordx4 v242, %[wr], s44 offen offset:2048 nt lds\n\t" \
    "ds_read_b64_tr_b16 v[100:101], v244 offset:0\n\t" \
    "ds_read_b64_tr_b16 v[102:103], v244 offset:1536\n\t" \
    "ds_read_b64_tr_b16 v[108:109], v244 offset:32\n\t" \
    "ds_read_b64_tr_b16 v[110:111], v244 offset:1568\n\t" \
    "ds_read_b64_tr_b16 v[116:117], v244 offset:64\n\t" \
    "ds_read_b64_tr_b16 v[118:119], v244 offset:1600\n\t" \
    "v_add_u32 v244, 0x0, v243\n\t" \
    "s_waitcnt vmcnt(12) lgkmcnt(0)\n\t" \
    "s_barrier \n\t" \
    "v_mov_b32 v246, 0\n\t" \
    "buffer_load_dword v246, v246, s[60:63], %[mo] offen sc1\n\t" \
    "s_add_u32 m0, %[wm], 0xf000\n\t" \
    "s_mov_b32 s44, 0x4e0000\n\t" \
    "buffer_load_dwordx4 v240, %[wr], s44 offen offset:0 nt lds\n\t" \
    "buffer_load_dwordx4 v241, %[wr], s44 offen offset:1024 nt lds\n\t" \
    "buffer_load_dwordx4 v242, %[wr], s44 offen offset:2048 nt lds\n\t" \
    "ds_read_b64_tr_b16 v[200:201], v244 offset:0\n\t" \
    "ds_read_b64_tr_b16 v[202:203], v244 offset:1536\n\t" \
    "ds_read_b64_tr_b16 v[0:1], v244 offset:32\n\t" \
    "ds_read_b64_tr_b16 v[2:3], v244 offset:1568\n\t" \
    "ds_read_b64_tr_b16 v[8:9], v244 offset:64\n\t" \
    "ds_read_b64_tr_b16 v[10:11], v244 offset:1600\n\t" \
    "v_add_u32 v244, 0x3000, v243\n\t" \
    "s_waitcnt vmcnt(12) lgkmcnt(0)\n\t" \
    "s_barrier \n\t" \
    "s_add_u32 m0, %[wm], 0x0\n\t" \
    "s_mov_b32 s44, 0x540000\n\t" \
    "buffer_load_dwordx4 v240, %[wr], s44 offen offset:0 nt lds\n\t" \
    "buffer_load_dwordx4 v241, %[wr], s44 offen offset:1024 nt lds\n\t" \
    "buffer_load_dwordx4 v242, %[wr], s44 offen offset:2048 nt lds\n\t" \
    "ds_read_b64_tr_b16 v[204:205], v244 offset:0\n\t" \
    "ds_read_b64_tr_b16 v[206:207], v244 offset:1536\n\t" \
    "ds_read_b64_tr_b16 v[4:5], v244 offset:32\n\t" \
    "ds_read_b64_tr_b16 v[6:7], v244 offset:1568\n\t" \
    "ds_read_b64_tr_b16 v[12:13], v244 offset:64\n\t" \
    "ds_read_b64_tr_b16 v[14:15], v244 offset:1600\n\t" \
    "v_add_u32 v244, 0x6000, v243\n\t" \
    "s_waitcnt vmcnt(12) lgkmcnt(0)\n\t" \
    "s_barrier \n\t" \
    "s_add_u32 m0, %[wm], 0x3000\n\t" \
    "s_mov_b32 s44, 0x5a0000\n\t" \
    "buffer_load_dwordx4 v240, %[wr], s44 offen offset:0 nt lds\n\t" \
    "buffer_load_dwordx4 v241, %[wr], s44 offen offset:1024 nt lds\n\t" \
    "buffer_load_dwordx4 v242, %[wr], s44 offen offset:2048 nt lds\n\t" \
    "ds_read_b64_tr_b16 v[224:225], v244 offset:0\n\t" \
    "ds_read_b64_tr_b16 v[226:227], v244 offset:1536\n\t" \
    "ds_read_b64_tr_b16 v[120:121], v244 offset:32\n\t" \
    "ds_read_b64_tr_b16 v[122:123], v244 offset:1568\n\t" \
    "ds_read_b64_tr_b16 v[128:129], v244 offset:64\n\t" \
    "ds_read_b64_tr_b16 v[130:131], v244 offset:1600\n\t" \
    "v_add_u32 v244, 0x9000, v243\n\t" \
    "s_waitcnt vmcnt(12) lgkmcnt(0)\n\t" \
    "s_barrier \n\t" \
    "s_add_u32 m0, %[wm], 0x6000\n\t" \
    "s_mov_b32 s44, 0x600000\n\t" \
    "buffer_load_dwordx4 v240, %[wr], s44 offen offset:0 nt lds\n\t" \
    "buffer_load_dwordx4 v241, %[wr], s44 offen offset:1024 nt lds\n\t" \
    "buffer_load_dwordx4 v242, %[wr], s44 offen offset:2048 nt lds\n\t" \
    "ds_read_b64_tr_b16 v[228:229], v244 offset:0\n\t" \
    "ds_read_b64_tr_b16 v[230:231], v244 offset:1536\n\t" \
    "ds_read_b64_tr_b16 v[124:125], v244 offset:32\n\t" \
    "ds_read_b64_tr_b16 v[126:127], v244 offset:1568\n\t" \
    "ds_read_b64_tr_b16 v[132:133], v244 offset:64\n\t" \
    "ds_read_b64_tr_b16 v[134:135], v244 offset:1600\n\t" \
    "v_add_u32 v244, 0xc000, v243\n\t" \
    "s_waitcnt vmcnt(12) lgkmcnt(0)\n\t" \
    "s_barrier \n\t" \
    "s_add_u32 m0, %[wm], 0x9000\n\t" \
    "s_mov_b32 s44, 0x660000\n\t" \
    "buffer_load_dwordx4 v240, %[wr], s44 offen offset:0 nt lds\n\t" \
    "buffer_load_dwordx4 v241, %[wr], s44 offen offset:1024 nt lds\n\t" \
    "buffer_load_dwordx4 v242, %[wr], s44 offen offset:2048 nt lds\n\t" \
    "ds_read_b64_tr_b16 v[136:137], v244 offset:0\n\t" \
    "ds_read_b64_tr_b16 v[138:139], v244 offset:1536\n\t" \
    "ds_read_b64_tr_b16 v[144:145], v244 offset:32\n\t" \
    "ds_read_b64_tr_b16 v[146:147], v244 offset:1568\n\t" \
    "ds_read_b64_tr_b16 v[152:153], v244 offset:64\n\t" \
    "ds_read_b64_tr_b16 v[154:155], v244 offset:1600\n\t" \
    "v_add_u32 v244, 0xf000, v243\n\t" \
    "s_waitcnt vmcnt(12) lgkmcnt(0)\n\t" \
    "s_barrier \n\t" \
    ".Lv21_%=:\n\t" \
    "v_readfirstlane_b32 s43, v246\n\t" \
    "s_nop 0\n\t" \
    "s_cmp_lg_u32 s43, -1\n\t" \
    "s_cbranch_scc1 .Lv22_%=\n\t" \
    "s_cmp_eq_u32 s41, 0\n\t" \
    "s_cbranch_scc1 .Lv22_%=\n\t" \
    "s_sub_u32 s41, s41, 1\n\t" \
    "s_sleep 4\n\t" \
    "v_mov_b32 v246, 0\n\t" \
    "buffer_load_dword v246, v246, s[60:63], %[mo] offen sc1\n\t" \
    "s_waitcnt vmcnt(0)\n\t" \
    "s_branch .Lv21_%=\n\t" \
    ".Lv22_%=:\n\t" \
    "v_mov_b32 v246, 0x8000\n\t" \
    "s_mov_b64 exec, 1\n\t" \
    "buffer_store_dword v246, v246, s[60:63], %[mo] offen sc1\n\t" \
    "s_mov_b64 exec, -1\n\t" \
    "s_and_b32 s44, s43, 0x5000\n\t" \
    "s_xor_b32 %[mb], %[mb], s44\n\t" \
    "s_xor_b32 %[fl], %[fl], s44\n\t" \
    "s_and_b32 s43, s43, 0x2000\n\t" \
    "s_xor_b32 s43, s43, %[lx]\n\t" \
    "s_add_u32 s60, s60, s43\n\t" \
    "s_addc_u32 s61, s61, 0\n\t" \
    "v_and_b32 v252, 0xf0, v253\n\t" \
    "v_lshlrev_b32 v252, 3, v252\n\t" \
    "buffer_load_dword v252, v252, s[60:63], %[fl] offen sc1\n\t" \
    "s_add_u32 m0, %[wm], 0xc000\n\t" \
    "s_mov_b32 s44, 0x6c0000\n\t" \
    "buffer_load_dwordx4 v240, %[wr], s44 offen offset:0 nt lds\n\t" \
    "buffer_load_dwordx4 v241, %[wr], s44 offen offset:1024 nt lds\n\t" \
    "buffer_load_dwordx4 v242, %[wr], s44 offen offset:2048 nt lds\n\t" \
    "ds_read_b64_tr_b16 v[140:141], v244 offset:0\n\t" \
    "ds_read_b64_tr_b16 v[142:143], v244 offset:1536\n\t" \
    "ds_read_b64_tr_b16 v[148:149], v244 offset:32\n\t" \
    "ds_read_b64_tr_b16 v[150:151], v244 offset:1568\n\t" \
    "ds_read_b64_tr_b16 v[156:157], v244 offset:64\n\t" \
    "ds_read_b64_tr_b16 v[158:159], v244 offset:1600\n\t" \
    "v_add_u32 v244, 0x0, v243\n\t" \
    "s_waitcnt vmcnt(12) lgkmcnt(0)\n\t" \
    "s_barrier \n\t" \
    "s_add_u32 m0, %[wm], 0xf000\n\t" \
    "s_mov_b32 s44, 0x720000\n\t" \
    "buffer_load_dwordx4 v240, %[wr], s44 offen offset:0 nt lds\n\t" \
    "buffer_load_dwordx4 v241, %[wr], s44 offen offset:1024 nt lds\n\t" \
    "buffer_load_dwordx4 v242, %[wr], s44 offen offset:2048 nt lds\n\t" \
    "ds_read_b64_tr_b16 v[16:17], v244 offset:0\n\t" \
    "ds_read_b64_tr_b16 v[18:19], v244 offset:1536\n\t" \
    "ds_read_b64_tr_b16 v[24:25], v244 offset:32\n\t" \
    "ds_read_b64_tr_b16 v[26:27], v244 offset:1568\n\t" \
    "ds_read_b64_tr_b16 v[32:33], v244 offset:64\n\t" \
    "ds_read_b64_tr_b16 v[34:35], v244 offset:1600\n\t" \
    "v_add_u32 v244, 0x3000, v243\n\t" \
    "s_waitcnt vmcnt(12) lgkmcnt(0)\n\t" \
    "s_barrier \n\t" \
    "ds_read_b64_tr_b16 v[20:21], v244 offset:0\n\t" \
    "ds_read_b64_tr_b16 v[22:23], v244 offset:1536\n\t" \
    "ds_read_b64_tr_b16 v[28:29], v244 offset:32\n\t" \
    "ds_read_b64_tr_b16 v[30:31], v244 offset:1568\n\t" \
    "ds_read_b64_tr_b16 v[36:37], v244 offset:64\n\t" \
    "ds_read_b64_tr_b16 v[38:39], v244 offset:1600\n\t" \
    "v_add_u32 v244, 0x6000, v243\n\t" \
    "s_waitcnt vmcnt(9) lgkmcnt(0)\n\t" \
    "s_barrier \n\t" \
    "ds_read_b64_tr_b16 v[232:233], v244 offset:0\n\t" \
    "ds_read_b64_tr_b16 v[234:235], v244 offset:1536\n\t" \
    "ds_read_b64_tr_b16 v[160:161], v244 offset:32\n\t" \
    "ds_read_b64_tr_b16 v[162:163], v244 offset:1568\n\t" \
    "ds_read_b64_tr_b16 v[168:169], v244 offset:64\n\t" \
    "ds_read_b64_tr_b16 v[170:171], v244 offset:1600\n\t" \
    "v_add_u32 v244, 0x9000, v243\n\t" \
    "s_waitcnt vmcnt(6) lgkmcnt(0)\n\t" \
    "s_barrier \n\t" \
    "ds_read_b64_tr_b16 v[236:237], v244 offset:0\n\t" \
    "ds_read_b64_tr_b16 v[238:239], v244 offset:1536\n\t" \
    "ds_read_b64_tr_b16 v[164:165], v244 offset:32\n\t" \
    "ds_read_b64_tr_b16 v[166:167], v244 offset:1568\n\t" \
    "ds_read_b64_tr_b16 v[172:173], v244 offset:64\n\t" \
    "ds_read_b64_tr_b16 v[174:175], v244 offset:1600\n\t" \
    "v_add_u32 v244, 0xc000, v243\n\t" \
    "s_waitcnt vmcnt(3) lgkmcnt(0)\n\t" \
    "s_barrier \n\t" \
    "ds_read_b64_tr_b16 v[176:177], v244 offset:0\n\t" \
    "ds_read_b64_tr_b16 v[178:179], v244 offset:1536\n\t" \
    "ds_read_b64_tr_b16 v[184:185], v244 offset:32\n\t" \
    "ds_read_b64_tr_b16 v[186:187], v244 offset:1568\n\t" \
    "ds_read_b64_tr_b16 v[192:193], v244 offset:64\n\t" \
    "ds_read_b64_tr_b16 v[194:195], v244 offset:1600\n\t" \
    "s_mov_b32 s43, 0\n\t" \
    "v_cmp_eq_u32 vcc, 0, v252\n\t" \
    "s_cbranch_vccnz .Lv23_%=\n\t" \
    "s_mov_b32 m0, 0x2000\n\t" \
    "s_add_u32 s49, s42, 0x0\n\t" \
    "buffer_load_dwordx4 v253, s[60:63], s49 offen offset:0 sc1 lds\n\t" \
    "buffer_load_dwordx4 v253, s[60:63], s49 offen offset:1024 sc1 lds\n\t" \
    "buffer_load_dwordx4 v253, s[60:63], s49 offen offset:2048 sc1 lds\n\t" \
    "buffer_load_dwordx4 v253, s[60:63], s49 offen offset:3072 sc1 lds\n\t" \
    "s_mov_b32 m0, 0x4800\n\t" \
    "s_add_u32 s49, s42, 0x8800\n\t" \
    "buffer_load_dwordx4 v253, s[60:63], s49 offen offset:0 sc1 lds\n\t" \
    "s_mov_b32 m0, 0x7000\n\t" \
    "s_add_u32 s49, s42, 0x1000\n\t" \
    "buffer_load_dwordx4 v253, s[60:63], s49 offen offset:0 sc1 lds\n\t" \
    "buffer_load_dwordx4 v253, s[60:63], s49 offen offset:1024 sc1 lds\n\t" \
    "buffer_load_dwordx4 v253, s[60:63], s49 offen offset:2048 sc1 lds\n\t" \
    "buffer_load_dwordx4 v253, s[60:63], s49 offen offset:3072 sc1 lds\n\t" \
    "s_mov_b32 m0, 0x9800\n\t" \
    "s_add_u32 s49, s42, 0x9800\n\t" \
    "buffer_load_dwordx4 v253, s[60:63], s49 offen offset:0 sc1 lds\n\t" \
    "s_mov_b32 s43, 1\n\t" \
    ".Lv23_%=:\n\t" \
    "v_add_u32 v244, 0xf000, v243\n\t" \
    "s_waitcnt vmcnt(0) lgkmcnt(0)\n\t" \
    "s_barrier \n\t" \
    "ds_read_b64_tr_b16 v[180:181], v244 offset:0\n\t" \
    "ds_read_b64_tr_b16 v[182:183], v244 offset:1536\n\t" \
    "ds_read_b64_tr_b16 v[188:189], v244 offset:32\n\t" \
    "ds_read_b64_tr_b16 v[190:191], v244 offset:1568\n\t" \
    "ds_read_b64_tr_b16 v[196:197], v244 offset:64\n\t" \
    "ds_read_b64_tr_b16 v[198:199], v244 offset:1600\n\t" \
    "s_waitcnt lgkmcnt(0)\n\t" \
    "s_barrier \n\t" \
    "ds_write_b128 v254, v[200:203] offset:51200\n\t" \
    "ds_write_b128 v254, v[204:207] offset:52224\n\t" \
    "ds_write_b128 v254, v[208:211] offset:53248\n\t" \
    "ds_write_b128 v254, v[212:215] offset:54272\n\t" \
    "ds_write_b128 v254, v[216:219] offset:55296\n\t" \
    "ds_write_b128 v254, v[220:223] offset:56320\n\t" \
    "ds_write_b128 v254, v[224:227] offset:57344\n\t" \
    "ds_write_b128 v254, v[228:231] offset:58368\n\t" \
    "ds_write_b128 v254, v[232:235] offset:59392\n\t" \
    "ds_write_b128 v254, v[236:239] offset:60416\n\t" \
    "s_cmp_lg_u32 s43, 0\n\t" \
    "s_cbranch_scc1 .Lv24_%=\n\t" \
    "s_mov_b32 s44, 0\n\t" \
    ".Lv25_%=:\n\t" \
    "v_cmp_ge_u32 vcc, s44, v252\n\t" \
    "s_cbranch_vccz .Lv26_%=\n\t" \
    "s_cmp_eq_u32 s41, 0\n\t" \
    "s_cbranch_scc1 .Lv26_%=\n\t" \
    "s_sub_u32 s41, s41, 1\n\t" \
    "s_sleep 8\n\t" \
    "v_and_b32 v252, 0xf0, v253\n\t" \
    "v_lshlrev_b32 v252, 3, v252\n\t" \
    "buffer_load_dword v252, v252, s[60:63], %[fl] offen sc1\n\t" \
    "s_waitcnt vmcnt(0)\n\t" \
    "s_branch .Lv25_%=\n\t" \
    ".Lv26_%=:\n\t" \
    "s_mov_b32 m0, 0x2000\n\t" \
    "s_add_u32 s49, s42, 0x0\n\t" \
    "buffer_load_dwordx4 v253, s[60:63], s49 offen offset:0 sc1 lds\n\t" \
    "buffer_load_dwordx4 v253, s[60:63], s49 offen offset:1024 sc1 lds\n\t" \
    "buffer_load_dwordx4 v253, s[60:63], s49 offen offset:2048 sc1 lds\n\t" \
    "buffer_load_dwordx4 v253, s[60:63], s49 offen offset:3072 sc1 lds\n\t" \
    "s_mov_b32 m0, 0x4800\n\t" \
    "s_add_u32 s49, s42, 0x8800\n\t" \
    "buffer_load_dwordx4 v253, s[60:63], s49 offen offset:0 sc1 lds\n\t" \
    "s_mov_b32 m0, 0x7000\n\t" \
    "s_add_u32 s49, s42, 0x1000\n\t" \
    "buffer_load_dwordx4 v253, s[60:63], s49 offen offset:0 sc1 lds\n\t" \
    "buffer_load_dwordx4 v253, s[60:63], s49 offen offset:1024 sc1 lds\n\t" \
    "buffer_load_dwordx4 v253, s[60:63], s49 offen offset:2048 sc1 lds\n\t" \
    "buffer_load_dwordx4 v253, s[60:63], s49 offen offset:3072 sc1 lds\n\t" \
    "s_mov_b32 m0, 0x9800\n\t" \
    "s_add_u32 s49, s42, 0x9800\n\t" \
    "buffer_load_dwordx4 v253, s[60:63], s49 offen offset:0 sc1 lds\n\t" \
    ".Lv24_%=:\n\t" \
    "s_waitcnt vmcnt(0) lgkmcnt(0)\n\t" \
    "s_barrier \n\t" \
    "ds_read_b128 v[224:227], v253 offset:8192\n\t" \
    "ds_read_b128 v[244:247], v254 offset:51200\n\t" \
    "ds_read_b128 v[228:231], v253 offset:9216\n\t" \
    "ds_read_b128 v[232:235], v253 offset:28672\n\t" \
    "ds_read_b128 v[236:239], v253 offset:29696\n\t" \
    "ds_read_b128 v[248:251], v254 offset:52224\n\t" \
    ".Lv27_%=:\n\t" \
    "s_waitcnt lgkmcnt(5)\n\t" \
    "v_mfma_f32_16x16x32_bf16 v[204:207], v[0:3], v[224:227], 0\n\t" \
    "s_waitcnt lgkmcnt(3)\n\t" \
    "v_mfma_f32_16x16x32_bf16 v[204:207], v[4:7], v[228:231], v[204:207]\n\t" \
    "ds_read_b128 v[240:243], v253 offset:14336\n\t" \
    "v_mfma_f32_16x16x32_bf16 v[208:211], v[8:11], v[224:227], 0\n\t" \
    "v_mfma_f32_16x16x32_bf16 v[208:211], v[12:15], v[228:231], v[208:211]\n\t" \
    "v_mfma_f32_16x16x32_bf16 v[200:203], v[244:247], v[224:227], 0\n\t" \
    "s_waitcnt lgkmcnt(1)\n\t" \
    "v_mfma_f32_16x16x32_bf16 v[200:203], v[248:251], v[228:231], v[200:203]\n\t" \
    "v_mfma_f32_16x16x32_bf16 v[216:219], v[0:3], v[232:235], 0\n\t" \
    "ds_read_b128 v[224:227], v253 offset:15360\n\t" \
    "v_mfma_f32_16x16x32_bf16 v[216:219], v[4:7], v[236:239], v[216:219]\n\t" \
    "ds_read_b128 v[228:231], v253 offset:0\n\t" \
    "v_mfma_f32_16x16x32_bf16 v[220:223], v[8:11], v[232:235], 0\n\t" \
    "v_mfma_f32_16x16x32_bf16 v[220:223], v[12:15], v[236:239], v[220:223]\n\t" \
    "v_mfma_f32_16x16x32_bf16 v[212:215], v[244:247], v[232:235], 0\n\t" \
    "ds_read_b128 v[244:247], v254 offset:53248\n\t" \
    "v_mfma_f32_16x16x32_bf16 v[212:215], v[248:251], v[236:239], v[212:215]\n\t" \
    "ds_read_b128 v[248:251], v254 offset:54272\n\t" \
    "s_waitcnt lgkmcnt(4)\n\t" \
    "v_mfma_f32_16x16x32_bf16 v[204:207], v[24:27], v[240:243], v[204:207]\n\t" \
    "ds_read_b128 v[232:235], v253 offset:1024\n\t" \
    "s_waitcnt lgkmcnt(4)\n\t" \
    "v_mfma_f32_16x16x32_bf16 v[204:207], v[28:31], v[224:227], v[204:207]\n\t" \
    "ds_read_b128 v[236:239], v253 offset:20480\n\t" \
    "v_mfma_f32_16x16x32_bf16 v[208:211], v[32:35], v[240:243], v[208:211]\n\t" \
    "v_mfma_f32_16x16x32_bf16 v[208:211], v[36:39], v[224:227], v[208:211]\n\t" \
    "v_mfma_f32_16x16x32_bf16 v[200:203], v[16:19], v[240:243], v[200:203]\n\t" \
    "v_mfma_f32_16x16x32_bf16 v[200:203], v[20:23], v[224:227], v[200:203]\n\t" \
    "s_waitcnt lgkmcnt(4)\n\t" \
    "v_mfma_f32_16x16x32_bf16 v[204:207], v[40:43], v[228:231], v[204:207]\n\t" \
    "ds_read_b128 v[240:243], v253 offset:21504\n\t" \
    "s_waitcnt lgkmcnt(2)\n\t" \
    "v_mfma_f32_16x16x32_bf16 v[204:207], v[44:47], v[232:235], v[204:207]\n\t" \
    "ds_read_b128 v[224:227], v253 offset:2048\n\t" \
    "v_mfma_f32_16x16x32_bf16 v[208:211], v[48:51], v[228:231], v[208:211]\n\t" \
    "v_mfma_f32_16x16x32_bf16 v[208:211], v[52:55], v[232:235], v[208:211]\n\t" \
    "v_mfma_f32_16x16x32_bf16 v[200:203], v[244:247], v[228:231], v[200:203]\n\t" \
    "v_mfma_f32_16x16x32_bf16 v[200:203], v[248:251], v[232:235], v[200:203]\n\t" \
    "s_waitcnt lgkmcnt(2)\n\t" \
    "v_mfma_f32_16x16x32_bf16 v[216:219], v[40:43], v[236:239], v[216:219]\n\t" \
    "ds_read_b128 v[228:231], v253 offset:3072\n\t" \
    "s_add_u32 s44, s40, 1\n\t" \
    "s_waitcnt lgkmcnt(2)\n\t" \
    "v_mfma_f32_16x16x32_bf16 v[216:219], v[44:47], v[240:243], v[216:219]\n\t" \
    "ds_read_b128 v[232:235], v253 offset:4096\n\t" \
    "s_and_b32 s45, s44, 15\n\t" \
    "v_mfma_f32_16x16x32_bf16 v[220:223], v[48:51], v[236:239], v[220:223]\n\t" \
    "s_cmp_eq_u32 s45, 0\n\t" \
    "v_mfma_f32_16x16x32_bf16 v[220:223], v[52:55], v[240:243], v[220:223]\n\t" \
    "s_cselect_b32 s45, %[cj], %[cs]\n\t" \
    "v_mfma_f32_16x16x32_bf16 v[212:215], v[244:247], v[236:239], v[212:215]\n\t" \
    "ds_read_b128 v[244:247], v254 offset:55296\n\t" \
    "s_cmp_eq_u32 s44, 64\n\t" \
    "v_mfma_f32_16x16x32_bf16 v[212:215], v[248:251], v[240:243], v[212:215]\n\t" \
    "ds_read_b128 v[248:251], v254 offset:56320\n\t" \
    "s_cselect_b32 s45, 0, s45\n\t" \
    "s_waitcnt lgkmcnt(4)\n\t" \
    "v_mfma_f32_16x16x32_bf16 v[204:207], v[64:67], v[224:227], v[204:207]\n\t" \
    "ds_read_b128 v[236:239], v253 offset:5120\n\t" \
    "s_add_u32 s42, s42, s45\n\t" \
    "s_waitcnt lgkmcnt(4)\n\t" \
    "v_mfma_f32_16x16x32_bf16 v[204:207], v[68:71], v[228:231], v[204:207]\n\t" \
    "ds_read_b128 v[240:243], v253 offset:24576\n\t" \
    "v_mfma_f32_16x16x32_bf16 v[208:211], v[72:75], v[224:227], v[208:211]\n\t" \
    "v_mfma_f32_16x16x32_bf16 v[208:211], v[76:79], v[228:231], v[208:211]\n\t" \
    "v_mfma_f32_16x16x32_bf16 v[200:203], v[56:59], v[224:227], v[200:203]\n\t" \
    "v_mfma_f32_16x16x32_bf16 v[200:203], v[60:63], v[228:231], v[200:203]\n\t" \
    "s_waitcnt lgkmcnt(4)\n\t" \
    "v_mfma_f32_16x16x32_bf16 v[204:207], v[80:83], v[232:235], v[204:207]\n\t" \
    "ds_read_b128 v[224:227], v253 offset:25600\n\t" \
    "s_waitcnt lgkmcnt(2)\n\t" \
    "v_mfma_f32_16x16x32_bf16 v[204:207], v[84:87], v[236:239], v[204:207]\n\t" \
    "ds_read_b128 v[228:231], v253 offset:6144\n\t" \
    "v_mfma_f32_16x16x32_bf16 v[208:211], v[88:91], v[232:235], v[208:211]\n\t" \
    "v_mfma_f32_16x16x32_bf16 v[208:211], v[92:95], v[236:239], v[208:211]\n\t" \
    "v_mfma_f32_16x16x32_bf16 v[200:203], v[244:247], v[232:235], v[200:203]\n\t" \
    "v_mfma_f32_16x16x32_bf16 v[200:203], v[248:251], v[236:239], v[200:203]\n\t" \
    "s_waitcnt lgkmcnt(2)\n\t" \
    "v_mfma_f32_16x16x32_bf16 v[216:219], v[80:83], v[240:243], v[216:219]\n\t" \
    "ds_read_b128 v[232:235], v253 offset:7168\n\t" \
    "s_and_b32 s44, s40, 3\n\t" \
    "s_waitcnt lgkmcnt(2)\n\t" \
    "v_mfma_f32_16x16x32_bf16 v[216:219], v[84:87], v[224:227], v[216:219]\n\t" \
    "ds_read_b128 v[236:239], v253 offset:10240\n\t" \
    "s_lshl_b32 s44, s44, 22\n\t" \
    "v_mfma_f32_16x16x32_bf16 v[220:223], v[88:91], v[240:243], v[220:223]\n\t" \
    "s_add_u32 s44, s44, %[mb]\n\t" \
    "v_mfma_f32_16x16x32_bf16 v[220:223], v[92:95], v[224:227], v[220:223]\n\t" \
    "s_add_u32 s46, s44, 0x8000\n\t" \
    "v_mfma_f32_16x16x32_bf16 v[212:215], v[244:247], v[240:243], v[212:215]\n\t" \
    "ds_read_b128 v[244:247], v254 offset:57344\n\t" \
    "s_add_u32 s47, s44, 0x8c00\n\t" \
    "v_mfma_f32_16x16x32_bf16 v[212:215], v[248:251], v[224:227], v[212:215]\n\t" \
    "ds_read_b128 v[248:251], v254 offset:58368\n\t" \
    "s_waitcnt lgkmcnt(4)\n\t" \
    "v_mfma_f32_16x16x32_bf16 v[204:207], v[104:107], v[228:231], v[204:207]\n\t" \
    "ds_read_b128 v[240:243], v253 offset:11264\n\t" \
    "s_waitcnt lgkmcnt(4)\n\t" \
    "v_mfma_f32_16x16x32_bf16 v[204:207], v[108:111], v[232:235], v[204:207]\n\t" \
    "ds_read_b128 v[224:227], v253 offset:30720\n\t" \
    "v_mfma_f32_16x16x32_bf16 v[208:211], v[112:115], v[228:231], v[208:211]\n\t" \
    "v_mfma_f32_16x16x32_bf16 v[208:211], v[116:119], v[232:235], v[208:211]\n\t" \
    "v_mfma_f32_16x16x32_bf16 v[200:203], v[96:99], v[228:231], v[200:203]\n\t" \
    "v_mfma_f32_16x16x32_bf16 v[200:203], v[100:103], v[232:235], v[200:203]\n\t" \
    "s_waitcnt lgkmcnt(4)\n\t" \
    "v_mfma_f32_16x16x32_bf16 v[204:207], v[120:123], v[236:239], v[204:207]\n\t" \
    "ds_read_b128 v[228:231], v253 offset:31744\n\t" \
    "s_waitcnt lgkmcnt(2)\n\t" \
    "v_mfma_f32_16x16x32_bf16 v[204:207], v[124:127], v[240:243], v[204:207]\n\t" \
    "ds_read_b128 v[232:235], v253 offset:12288\n\t" \
    "v_mfma_f32_16x16x32_bf16 v[208:211], v[128:131], v[236:239], v[208:211]\n\t" \
    "v_mfma_f32_16x16x32_bf16 v[208:211], v[132:135], v[240:243], v[208:211]\n\t" \
    "v_mfma_f32_16x16x32_bf16 v[200:203], v[244:247], v[236:239], v[200:203]\n\t" \
    "v_mfma_f32_16x16x32_bf16 v[200:203], v[248:251], v[240:243], v[200:203]\n\t" \
    "s_waitcnt lgkmcnt(2)\n\t" \
    "v_mfma_f32_16x16x32_bf16 v[216:219], v[120:123], v[224:227], v[216:219]\n\t" \
    "ds_read_b128 v[236:239], v253 offset:13312\n\t" \
    "s_waitcnt lgkmcnt(2)\n\t" \
    "v_mfma_f32_16x16x32_bf16 v[216:219], v[124:127], v[228:231], v[216:219]\n\t" \
    "ds_read_b128 v[240:243], v253 offset:16384\n\t" \
    "v_mfma_f32_16x16x32_bf16 v[220:223], v[128:131], v[224:227], v[220:223]\n\t" \
    "v_mfma_f32_16x16x32_bf16 v[220:223], v[132:135], v[228:231], v[220:223]\n\t" \
    "v_mfma_f32_16x16x32_bf16 v[212:215], v[244:247], v[224:227], v[212:215]\n\t" \
    "ds_read_b128 v[244:247], v254 offset:59392\n\t" \
    "v_mfma_f32_16x16x32_bf16 v[212:215], v[248:251], v[228:231], v[212:215]\n\t" \
    "ds_read_b128 v[248:251], v254 offset:60416\n\t" \
    "v_and_b32 v252, 0xf0, v253\n\t" \
    "v_lshlrev_b32 v252, 3, v252\n\t" \
    "buffer_load_dword v252, v252, s[60:63], %[fl] offen sc1\n\t" \
    "s_waitcnt lgkmcnt(4)\n\t" \
    "v_mfma_f32_16x16x32_bf16 v[204:207], v[144:147], v[232:235], v[204:207]\n\t" \
    "ds_read_b128 v[224:227], v253 offset:17408\n\t" \
    "s_waitcnt lgkmcnt(4)\n\t" \
    "v_mfma_f32_16x16x32_bf16 v[204:207], v[148:151], v[236:239], v[204:207]\n\t" \
    "ds_read_b128 v[228:231], v253 offset:36864\n\t" \
    "v_mfma_f32_16x16x32_bf16 v[208:211], v[152:155], v[232:235], v[208:211]\n\t" \
    "v_mfma_f32_16x16x32_bf16 v[208:211], v[156:159], v[236:239], v[208:211]\n\t" \
    "v_mfma_f32_16x16x32_bf16 v[200:203], v[136:139], v[232:235], v[200:203]\n\t" \
    "v_mfma_f32_16x16x32_bf16 v[200:203], v[140:143], v[236:239], v[200:203]\n\t" \
    "s_waitcnt lgkmcnt(4)\n\t" \
    "v_mfma_f32_16x16x32_bf16 v[204:207], v[160:163], v[240:243], v[204:207]\n\t" \
    "ds_read_b128 v[232:235], v253 offset:37888\n\t" \
    "s_waitcnt lgkmcnt(2)\n\t" \
    "v_mfma_f32_16x16x32_bf16 v[204:207], v[164:167], v[224:227], v[204:207]\n\t" \
    "ds_read_b128 v[236:239], v253 offset:18432\n\t" \
    "v_mfma_f32_16x16x32_bf16 v[208:211], v[168:171], v[240:243], v[208:211]\n\t" \
    "v_mfma_f32_16x16x32_bf16 v[208:211], v[172:175], v[224:227], v[208:211]\n\t" \
    "v_mfma_f32_16x16x32_bf16 v[200:203], v[244:247], v[240:243], v[200:203]\n\t" \
    "v_mfma_f32_16x16x32_bf16 v[200:203], v[248:251], v[224:227], v[200:203]\n\t" \
    "s_waitcnt lgkmcnt(2)\n\t" \
    "v_mfma_f32_16x16x32_bf16 v[216:219], v[160:163], v[228:231], v[216:219]\n\t" \
    "ds_read_b128 v[240:243], v253 offset:19456\n\t" \
    "s_waitcnt lgkmcnt(2)\n\t" \
    "v_mfma_f32_16x16x32_bf16 v[216:219], v[164:167], v[232:235], v[216:219]\n\t" \
    "ds_read_b128 v[224:227], v253 offset:34816\n\t" \
    "v_mfma_f32_16x16x32_bf16 v[220:223], v[168:171], v[228:231], v[220:223]\n\t" \
    "v_mfma_f32_16x16x32_bf16 v[220:223], v[172:175], v[232:235], v[220:223]\n\t" \
    "v_mfma_f32_16x16x32_bf16 v[212:215], v[244:247], v[228:231], v[212:215]\n\t" \
    "v_mfma_f32_16x16x32_bf16 v[212:215], v[248:251], v[232:235], v[212:215]\n\t" \
    "s_waitcnt lgkmcnt(2)\n\t" \
    "v_mfma_f32_16x16x32_bf16 v[204:207], v[184:187], v[236:239], v[204:207]\n\t" \
    "ds_read_b128 v[228:231], v253 offset:35840\n\t" \
    "s_waitcnt lgkmcnt(2)\n\t" \
    "v_mfma_f32_16x16x32_bf16 v[204:207], v[188:191], v[240:243], v[204:207]\n\t" \
    "ds_read_b128 v[232:235], v253 offset:22528\n\t" \
    "v_mfma_f32_16x16x32_bf16 v[208:211], v[192:195], v[236:239], v[208:211]\n\t" \
    "v_mfma_f32_16x16x32_bf16 v[208:211], v[196:199], v[240:243], v[208:211]\n\t" \
    "v_mfma_f32_16x16x32_bf16 v[200:203], v[176:179], v[236:239], v[200:203]\n\t" \
    "v_mfma_f32_16x16x32_bf16 v[200:203], v[180:183], v[240:243], v[200:203]\n\t" \
    "s_waitcnt lgkmcnt(2)\n\t" \
    "v_mfma_f32_16x16x32_bf16 v[216:219], v[24:27], v[224:227], v[216:219]\n\t" \
    "ds_read_b128 v[236:239], v253 offset:23552\n\t" \
    "s_waitcnt lgkmcnt(2)\n\t" \
    "v_mfma_f32_16x16x32_bf16 v[216:219], v[28:31], v[228:231], v[216:219]\n\t" \
    "ds_read_b128 v[240:243], v253 offset:26624\n\t" \
    "v_mfma_f32_16x16x32_bf16 v[220:223], v[32:35], v[224:227], v[220:223]\n\t" \
    "v_cvt_pk_bf16_f32 v204, v204, v205\n\t" \
    "v_mfma_f32_16x16x32_bf16 v[220:223], v[36:39], v[228:231], v[220:223]\n\t" \
    "v_cvt_pk_bf16_f32 v205, v206, v207\n\t" \
    "v_mfma_f32_16x16x32_bf16 v[212:215], v[16:19], v[224:227], v[212:215]\n\t" \
    "ds_write_b64 v254, v[204:205] offset:7176\n\t" \
    "v_mfma_f32_16x16x32_bf16 v[212:215], v[20:23], v[228:231], v[212:215]\n\t" \
    "v_cvt_pk_bf16_f32 v208, v208, v209\n\t" \
    "s_waitcnt lgkmcnt(3)\n\t" \
    "v_mfma_f32_16x16x32_bf16 v[216:219], v[64:67], v[232:235], v[216:219]\n\t" \
    "ds_read_b128 v[224:227], v253 offset:27648\n\t" \
    "v_cvt_pk_bf16_f32 v209, v210, v211\n\t" \
    "s_waitcnt lgkmcnt(3)\n\t" \
    "v_mfma_f32_16x16x32_bf16 v[216:219], v[68:71], v[236:239], v[216:219]\n\t" \
    "ds_read_b128 v[228:231], v253 offset:32768\n\t" \
    "ds_write_b64 v254, v[208:209] offset:8200\n\t" \
    "v_mfma_f32_16x16x32_bf16 v[220:223], v[72:75], v[232:235], v[220:223]\n\t" \
    "v_cvt_pk_bf16_f32 v200, v200, v201\n\t" \
    "v_mfma_f32_16x16x32_bf16 v[220:223], v[76:79], v[236:239], v[220:223]\n\t" \
    "v_cvt_pk_bf16_f32 v201, v202, v203\n\t" \
    "v_mfma_f32_16x16x32_bf16 v[212:215], v[56:59], v[232:235], v[212:215]\n\t" \
    "ds_write_b64 v254, v[200:201] offset:6152\n\t" \
    "v_mfma_f32_16x16x32_bf16 v[212:215], v[60:63], v[236:239], v[212:215]\n\t" \
    "s_waitcnt lgkmcnt(5)\n\t" \
    "v_mfma_f32_16x16x32_bf16 v[216:219], v[104:107], v[240:243], v[216:219]\n\t" \
    "ds_read_b128 v[232:235], v253 offset:33792\n\t" \
    "s_waitcnt lgkmcnt(4)\n\t" \
    "v_mfma_f32_16x16x32_bf16 v[216:219], v[108:111], v[224:227], v[216:219]\n\t" \
    "ds_read_b128 v[236:239], v253 offset:38912\n\t" \
    "v_mfma_f32_16x16x32_bf16 v[220:223], v[112:115], v[240:243], v[220:223]\n\t" \
    "v_mfma_f32_16x16x32_bf16 v[220:223], v[116:119], v[224:227], v[220:223]\n\t" \
    "v_mfma_f32_16x16x32_bf16 v[212:215], v[96:99], v[240:243], v[212:215]\n\t" \
    "v_mfma_f32_16x16x32_bf16 v[212:215], v[100:103], v[224:227], v[212:215]\n\t" \
    "ds_read_b128 v[240:243], v253 offset:39936\n\t" \
    "s_waitcnt lgkmcnt(3)\n\t" \
    "s_barrier \n\t" \
    "v_mfma_f32_16x16x32_bf16 v[216:219], v[144:147], v[228:231], v[216:219]\n\t" \
    "s_waitcnt lgkmcnt(2)\n\t" \
    "v_mfma_f32_16x16x32_bf16 v[216:219], v[148:151], v[232:235], v[216:219]\n\t" \
    "s_waitcnt lgkmcnt(1)\n\t" \
    "v_mfma_f32_16x16x32_bf16 v[216:219], v[184:187], v[236:239], v[216:219]\n\t" \
    "s_waitcnt lgkmcnt(0)\n\t" \
    "v_mfma_f32_16x16x32_bf16 v[216:219], v[188:191], v[240:243], v[216:219]\n\t" \
    "v_mfma_f32_16x16x32_bf16 v[220:223], v[152:155], v[228:231], v[220:223]\n\t" \
    "v_mfma_f32_16x16x32_bf16 v[220:223], v[156:159], v[232:235], v[220:223]\n\t" \
    "v_mfma_f32_16x16x32_bf16 v[220:223], v[192:195], v[236:239], v[220:223]\n\t" \
    "v_cvt_pk_bf16_f32 v216, v216, v217\n\t" \
    "v_cvt_pk_bf16_f32 v217, v218, v219\n\t" \
    "v_mfma_f32_16x16x32_bf16 v[220:223], v[196:199], v[240:243], v[220:223]\n\t" \
    "ds_write_b64 v254, v[216:217] offset:13320\n\t" \
    "v_mfma_f32_16x16x32_bf16 v[212:215], v[136:139], v[228:231], v[212:215]\n\t" \
    "v_mfma_f32_16x16x32_bf16 v[212:215], v[140:143], v[232:235], v[212:215]\n\t" \
    "v_mfma_f32_16x16x32_bf16 v[212:215], v[176:179], v[236:239], v[212:215]\n\t" \
    "v_cvt_pk_bf16_f32 v220, v220, v221\n\t" \
    "v_cvt_pk_bf16_f32 v221, v222, v223\n\t" \
    "v_mfma_f32_16x16x32_bf16 v[212:215], v[180:183], v[240:243], v[212:215]\n\t" \
    "ds_write_b64 v254, v[220:221] offset:14344\n\t" \
    "s_nop 6\n\t" \
    "v_cvt_pk_bf16_f32 v212, v212, v213\n\t" \
    "v_cvt_pk_bf16_f32 v213, v214, v215\n\t" \
    "ds_write_b64 v254, v[212:213] offset:12296\n\t" \
    "s_waitcnt vmcnt(0) lgkmcnt(0)\n\t" \
    "s_barrier \n\t" \
    "s_setprio 2\n\t" \
    "s_add_u32 s44, s40, 1\n\t" \
    "s_min_u32 s44, s44, 63\n\t" \
    ".Lv28_%=:\n\t" \
    "v_cmp_ge_u32 vcc, s44, v252\n\t" \
    "s_cbranch_vccz .Lv29_%=\n\t" \
    "s_cmp_eq_u32 s41, 0\n\t" \
    "s_cbranch_scc1 .Lv29_%=\n\t" \
    "s_sub_u32 s41, s41, 1\n\t" \
    "s_sleep 2\n\t" \
    "v_and_b32 v252, 0xf0, v253\n\t" \
    "v_lshlrev_b32 v252, 3, v252\n\t" \
    "buffer_load_dword v252, v252, s[60:63], %[fl] offen sc1\n\t" \
    "s_waitcnt vmcnt(0)\n\t" \
    "s_branch .Lv28_%=\n\t" \
    ".Lv29_%=:\n\t" \
    "s_mov_b32 m0, 0x2000\n\t" \
    "s_add_u32 s49, s42, 0x0\n\t" \
    "buffer_load_dwordx4 v253, s[60:63], s49 offen offset:0 sc1 lds\n\t" \
    "s_sleep 1\n\t" \
    "buffer_load_dwordx4 v253, s[60:63], s49 offen offset:1024 sc1 lds\n\t" \
    "s_sleep 1\n\t" \
    "buffer_load_dwordx4 v253, s[60:63], s49 offen offset:2048 sc1 lds\n\t" \
    "s_sleep 1\n\t" \
    "buffer_load_dwordx4 v253, s[60:63], s49 offen offset:3072 sc1 lds\n\t" \
    "s_sleep 1\n\t" \
    "s_mov_b32 m0, 0x4800\n\t" \
    "s_add_u32 s49, s42, 0x8800\n\t" \
    "buffer_load_dwordx4 v253, s[60:63], s49 offen offset:0 sc1 lds\n\t" \
    "s_sleep 1\n\t" \
    "s_mov_b32 m0, 0x7000\n\t" \
    "s_add_u32 s49, s42, 0x1000\n\t" \
    "buffer_load_dwordx4 v253, s[60:63], s49 offen offset:0 sc1 lds\n\t" \
    "s_sleep 1\n\t" \
    "buffer_load_dwordx4 v253, s[60:63], s49 offen offset:1024 sc1 lds\n\t" \
    "s_sleep 1\n\t" \
    "buffer_load_dwordx4 v253, s[60:63], s49 offen offset:2048 sc1 lds\n\t" \
    "s_sleep 1\n\t" \
    "buffer_load_dwordx4 v253, s[60:63], s49 offen offset:3072 sc1 lds\n\t" \
    "s_sleep 1\n\t" \
    "s_mov_b32 m0, 0x9800\n\t" \
    "s_add_u32 s49, s42, 0x9800\n\t" \
    "buffer_load_dwordx4 v253, s[60:63], s49 offen offset:0 sc1 lds\n\t" \
    "s_sleep 1\n\t" \
    "ds_read_b128 v[200:203], v254 offset:51200\n\t" \
    "ds_read_b128 v[204:207], v254 offset:52224\n\t" \
    "ds_read_b128 v[216:219], v254 offset:53248\n\t" \
    "ds_read_b128 v[220:223], v254 offset:54272\n\t" \
    "v_mov_b64 v[208:209], 0\n\t" \
    "v_mov_b64 v[210:211], 0\n\t" \
    "v_mov_b64 v[244:245], 0\n\t" \
    "v_mov_b64 v[246:247], 0\n\t" \
    "v_mov_b64 v[248:249], 0\n\t" \
    "v_mov_b64 v[250:251], 0\n\t" \
    "s_sleep 5\n\t" \
    "s_mov_b32 m0, 0xb000\n\t" \
    "s_nop 0\n\t" \
    "buffer_load_dwordx4 v253, s[60:63], s46 offen sc1 lds\n\t" \
    "s_mov_b32 m0, 0xbc00\n\t" \
    "s_nop 0\n\t" \
    "buffer_load_dwordx4 v253, s[60:63], s47 offen sc1 lds\n\t" \
    "s_waitcnt lgkmcnt(0)\n\t" \
    "v_bfe_i32 v212, v253, 5, 1\n\t" \
    "v_and_b32 v212, 0xaaaa, v212\n\t" \
    "v_or_b32 v212, 0x4444, v212\n\t" \
    "v_and_b32 v213, 0xffff, v255\n\t" \
    "v_lshrrev_b32 v214, 16, v255\n\t" \
    ".Lv210_%=:\n\t" \
    "s_waitcnt vmcnt(0)\n\t" \
    "ds_read_b128 v[224:227], v253 offset:45056\n\t" \
    "ds_read_b128 v[228:231], v253 offset:48128\n\t" \
    "s_waitcnt lgkmcnt(1)\n\t" \
    "v_max_u32 v215, v224, v225\n\t" \
    "v_max3_u32 v215, v226, v227, v215\n\t" \
    "s_waitcnt lgkmcnt(0)\n\t" \
    "v_max3_u32 v215, v228, v229, v215\n\t" \
    "v_max3_u32 v215, v230, v231, v215\n\t" \
    "v_cmp_eq_u32 vcc, -1, v215\n\t" \
    "s_cbranch_vccz .Lv211_%=\n\t" \
    "v_max_u32 v215, v224, v225\n\t" \
    "v_max3_u32 v215, v226, v227, v215\n\t" \
    "v_cmp_eq_u32 vcc, -1, v215\n\t" \
    "s_cbranch_vccz .Lv212_%=\n\t" \
    "s_mov_b32 m0, 0xb000\n\t" \
    "s_nop 0\n\t" \
    "buffer_load_dwordx4 v253, s[60:63], s46 offen sc1 lds\n\t" \
    ".Lv212_%=:\n\t" \
    "v_max_u32 v215, v228, v229\n\t" \
    "v_max3_u32 v215, v230, v231, v215\n\t" \
    "v_cmp_eq_u32 vcc, -1, v215\n\t" \
    "s_cbranch_vccz .Lv213_%=\n\t" \
    "s_mov_b32 m0, 0xbc00\n\t" \
    "s_nop 0\n\t" \
    "buffer_load_dwordx4 v253, s[60:63], s47 offen sc1 lds\n\t" \
    ".Lv213_%=:\n\t" \
    "s_cmp_eq_u32 s41, 0\n\t" \
    "s_cbranch_scc1 .Lv211_%=\n\t" \
    "s_sub_u32 s41, s41, 1\n\t" \
    "s_branch .Lv210_%=\n\t" \
    ".Lv211_%=:\n\t" \
    "s_waitcnt lgkmcnt(0)\n\t" \
    "s_barrier \n\t" \
    "ds_read_b128 v[232:235], v253 offset:40960\n\t" \
    "ds_read_b128 v[236:239], v253 offset:41984\n\t" \
    "ds_read_b128 v[240:243], v253 offset:43008\n\t" \
    "v_smfmac_f32_16x16x64_bf16 v[244:247], v[224:227], v[0:7], v212\n\t" \
    "v_smfmac_f32_16x16x64_bf16 v[248:251], v[224:227], v[8:15], v212\n\t" \
    "v_smfmac_f32_16x16x64_bf16 v[208:211], v[224:227], v[200:207], v212\n\t" \
    "ds_read_b128 v[200:203], v254 offset:55296\n\t" \
    "ds_read_b128 v[204:207], v254 offset:56320\n\t" \
    "ds_read_b128 v[224:227], v253 offset:44032\n\t" \
    "v_smfmac_f32_16x16x64_bf16 v[208:211], v[228:231], v[16:23], v212\n\t" \
    "v_smfmac_f32_16x16x64_bf16 v[244:247], v[228:231], v[24:31], v212\n\t" \
    "v_smfmac_f32_16x16x64_bf16 v[248:251], v[228:231], v[32:39], v212\n\t" \
    "ds_read_b128 v[228:231], v253 offset:46080\n\t" \
    "s_waitcnt lgkmcnt(6)\n\t" \
    "v_smfmac_f32_16x16x64_bf16 v[208:211], v[232:235], v[216:223], v212\n\t" \
    "ds_read_b128 v[216:219], v254 offset:57344\n\t" \
    "ds_read_b128 v[220:223], v254 offset:58368\n\t" \
    "v_smfmac_f32_16x16x64_bf16 v[244:247], v[232:235], v[40:47], v212\n\t" \
    "v_smfmac_f32_16x16x64_bf16 v[248:251], v[232:235], v[48:55], v212\n\t" \
    "ds_read_b128 v[232:235], v253 offset:47104\n\t" \
    "s_waitcnt lgkmcnt(8)\n\t" \
    "v_smfmac_f32_16x16x64_bf16 v[208:211], v[236:239], v[56:63], v212\n\t" \
    "v_smfmac_f32_16x16x64_bf16 v[244:247], v[236:239], v[64:71], v212\n\t" \
    "v_smfmac_f32_16x16x64_bf16 v[248:251], v[236:239], v[72:79], v212\n\t" \
    "ds_read_b128 v[236:239], v253 offset:49152\n\t" \
    "s_waitcnt lgkmcnt(6)\n\t" \
    "v_smfmac_f32_16x16x64_bf16 v[208:211], v[240:243], v[200:207], v212\n\t" \
    "ds_read_b128 v[200:203], v254 offset:59392\n\t" \
    "ds_read_b128 v[204:207], v254 offset:60416\n\t" \
    "v_smfmac_f32_16x16x64_bf16 v[244:247], v[240:243], v[80:87], v212\n\t" \
    "v_smfmac_f32_16x16x64_bf16 v[248:251], v[240:243], v[88:95], v212\n\t" \
    "ds_read_b128 v[240:243], v253 offset:50176\n\t" \
    "s_waitcnt lgkmcnt(8)\n\t" \
    "v_smfmac_f32_16x16x64_bf16 v[208:211], v[224:227], v[96:103], v212\n\t" \
    "v_smfmac_f32_16x16x64_bf16 v[244:247], v[224:227], v[104:111], v212\n\t" \
    "v_smfmac_f32_16x16x64_bf16 v[248:251], v[224:227], v[112:119], v212\n\t" \
    "s_waitcnt lgkmcnt(7)\n\t" \
    "v_smfmac_f32_16x16x64_bf16 v[244:247], v[228:231], v[120:127], v212\n\t" \
    "s_waitcnt lgkmcnt(4)\n\t" \
    "v_smfmac_f32_16x16x64_bf16 v[244:247], v[232:235], v[144:151], v212\n\t" \
    "s_waitcnt lgkmcnt(3)\n\t" \
    "v_smfmac_f32_16x16x64_bf16 v[244:247], v[236:239], v[160:167], v212\n\t" \
    "s_waitcnt lgkmcnt(0)\n\t" \
    "v_smfmac_f32_16x16x64_bf16 v[244:247], v[240:243], v[184:191], v212\n\t" \
    "v_smfmac_f32_16x16x64_bf16 v[248:251], v[228:231], v[128:135], v212\n\t" \
    "v_smfmac_f32_16x16x64_bf16 v[248:251], v[232:235], v[152:159], v212\n\t" \
    "v_smfmac_f32_16x16x64_bf16 v[248:251], v[236:239], v[168:175], v212\n\t" \
    "v_add_f32 v244, v244, v246\n\t" \
    "v_add_f32 v245, v245, v247\n\t" \
    "v_cvt_pk_bf16_f32 v244, v244, v245\n\t" \
    "v_smfmac_f32_16x16x64_bf16 v[248:251], v[240:243], v[192:199], v212\n\t" \
    "ds_write_b16 v213, v244 offset:512\n\t" \
    "ds_write_b16_d16_hi v213, v244 offset:528\n\t" \
    "v_smfmac_f32_16x16x64_bf16 v[208:211], v[228:231], v[216:223], v212\n\t" \
    "v_smfmac_f32_16x16x64_bf16 v[208:211], v[232:235], v[136:143], v212\n\t" \
    "v_smfmac_f32_16x16x64_bf16 v[208:211], v[236:239], v[200:207], v212\n\t" \
    "v_add_f32 v248, v248, v250\n\t" \
    "v_add_f32 v249, v249, v251\n\t" \
    "v_cvt_pk_bf16_f32 v248, v248, v249\n\t" \
    "v_smfmac_f32_16x16x64_bf16 v[208:211], v[240:243], v[176:183], v212\n\t" \
    "ds_write_b16 v213, v248 offset:1024\n\t" \
    "ds_write_b16_d16_hi v213, v248 offset:1040\n\t" \
    "s_nop 5\n\t" \
    "v_add_f32 v208, v208, v210\n\t" \
    "v_add_f32 v209, v209, v211\n\t" \
    "v_cvt_pk_bf16_f32 v208, v208, v209\n\t" \
    "ds_write_b16 v213, v208 offset:0\n\t" \
    "ds_write_b16_d16_hi v213, v208 offset:16\n\t" \
    "s_setprio 0\n\t" \
    "s_waitcnt lgkmcnt(0)\n\t" \
    "s_barrier \n\t" \
    "ds_read_b128 v[224:227], v253 offset:8192\n\t" \
    "ds_read_b128 v[244:247], v254 offset:51200\n\t" \
    "ds_read_b128 v[228:231], v253 offset:9216\n\t" \
    "ds_read_b128 v[232:235], v253 offset:28672\n\t" \
    "ds_read_b128 v[236:239], v253 offset:29696\n\t" \
    "ds_read_b128 v[248:251], v254 offset:52224\n\t" \
    "s_add_u32 s40, s40, 1\n\t" \
    "s_cmp_lg_u32 s40, 64\n\t" \
    "s_cbranch_scc1 .Lv27_%=\n\t" \
    "s_branch .Lend%=\n\t" \
    ".Lv1_%=:\n\t" \
    "s_mov_b32 s42, %[xs]\n\t" \
    "v_add_u32 v244, 0xc000, v243\n\t" \
    "s_waitcnt vmcnt(12) lgkmcnt(0)\n\t" \
    "s_barrier \n\t" \
    "s_add_u32 m0, %[wm], 0x9000\n\t" \
    "s_mov_b32 s44, 0x1e0000\n\t" \
    "buffer_load_dwordx4 v240, %[wr], s44 offen offset:0 nt lds\n\t" \
    "buffer_load_dwordx4 v241, %[wr], s44 offen offset:1024 nt lds\n\t" \
    "buffer_load_dwordx4 v242, %[wr], s44 offen offset:2048 nt lds\n\t" \
    "ds_read_b64_tr_b16 v[56:57], v244 offset:0\n\t" \
    "ds_read_b64_tr_b16 v[58:59], v244 offset:1536\n\t" \
    "ds_read_b64_tr_b16 v[64:65], v244 offset:32\n\t" \
    "ds_read_b64_tr_b16 v[66:67], v244 offset:1568\n\t" \
    "ds_read_b64_tr_b16 v[72:73], v244 offset:64\n\t" \
    "ds_read_b64_tr_b16 v[74:75], v244 offset:1600\n\t" \
    "v_add_u32 v244, 0xf000, v243\n\t" \
    "s_waitcnt vmcnt(12) lgkmcnt(0)\n\t" \
    "s_barrier \n\t" \
    "s_add_u32 m0, %[wm], 0xc000\n\t" \
    "s_mov_b32 s44, 0x240000\n\t" \
    "buffer_load_dwordx4 v240, %[wr], s44 offen offset:0 nt lds\n\t" \
    "buffer_load_dwordx4 v241, %[wr], s44 offen offset:1024 nt lds\n\t" \
    "buffer_load_dwordx4 v242, %[wr], s44 offen offset:2048 nt lds\n\t" \
    "ds_read_b64_tr_b16 v[60:61], v244 offset:0\n\t" \
    "ds_read_b64_tr_b16 v[62:63], v244 offset:1536\n\t" \
    "ds_read_b64_tr_b16 v[68:69], v244 offset:32\n\t" \
    "ds_read_b64_tr_b16 v[70:71], v244 offset:1568\n\t" \
    "ds_read_b64_tr_b16 v[76:77], v244 offset:64\n\t" \
    "ds_read_b64_tr_b16 v[78:79], v244 offset:1600\n\t" \
    "v_add_u32 v244, 0x0, v243\n\t" \
    "s_waitcnt vmcnt(12) lgkmcnt(0)\n\t" \
    "s_barrier \n\t" \
    "s_add_u32 m0, %[wm], 0xf000\n\t" \
    "s_mov_b32 s44, 0x2a0000\n\t" \
    "buffer_load_dwordx4 v240, %[wr], s44 offen offset:0 nt lds\n\t" \
    "buffer_load_dwordx4 v241, %[wr], s44 offen offset:1024 nt lds\n\t" \
    "buffer_load_dwordx4 v242, %[wr], s44 offen offset:2048 nt lds\n\t" \
    "ds_read_b64_tr_b16 v[96:97], v244 offset:0\n\t" \
    "ds_read_b64_tr_b16 v[98:99], v244 offset:1536\n\t" \
    "ds_read_b64_tr_b16 v[104:105], v244 offset:32\n\t" \
    "ds_read_b64_tr_b16 v[106:107], v244 offset:1568\n\t" \
    "ds_read_b64_tr_b16 v[112:113], v244 offset:64\n\t" \
    "ds_read_b64_tr_b16 v[114:115], v244 offset:1600\n\t" \
    "v_add_u32 v244, 0x3000, v243\n\t" \
    "s_waitcnt vmcnt(12) lgkmcnt(0)\n\t" \
    "s_barrier \n\t" \
    "s_add_u32 m0, %[wm], 0x0\n\t" \
    "s_mov_b32 s44, 0x300000\n\t" \
    "buffer_load_dwordx4 v240, %[wr], s44 offen offset:0 nt lds\n\t" \
    "buffer_load_dwordx4 v241, %[wr], s44 offen offset:1024 nt lds\n\t" \
    "buffer_load_dwordx4 v242, %[wr], s44 offen offset:2048 nt lds\n\t" \
    "ds_read_b64_tr_b16 v[100:101], v244 offset:0\n\t" \
    "ds_read_b64_tr_b16 v[102:103], v244 offset:1536\n\t" \
    "ds_read_b64_tr_b16 v[108:109], v244 offset:32\n\t" \
    "ds_read_b64_tr_b16 v[110:111], v244 offset:1568\n\t" \
    "ds_read_b64_tr_b16 v[116:117], v244 offset:64\n\t" \
    "ds_read_b64_tr_b16 v[118:119], v244 offset:1600\n\t" \
    "v_add_u32 v244, 0x6000, v243\n\t" \
    "s_waitcnt vmcnt(12) lgkmcnt(0)\n\t" \
    "s_barrier \n\t" \
    "s_add_u32 m0, %[wm], 0x3000\n\t" \
    "s_mov_b32 s44, 0x360000\n\t" \
    "buffer_load_dwordx4 v240, %[wr], s44 offen offset:0 nt lds\n\t" \
    "buffer_load_dwordx4 v241, %[wr], s44 offen offset:1024 nt lds\n\t" \
    "buffer_load_dwordx4 v242, %[wr], s44 offen offset:2048 nt lds\n\t" \
    "ds_read_b64_tr_b16 v[200:201], v244 offset:0\n\t" \
    "ds_read_b64_tr_b16 v[202:203], v244 offset:1536\n\t" \
    "ds_read_b64_tr_b16 v[0:1], v244 offset:32\n\t" \
    "ds_read_b64_tr_b16 v[2:3], v244 offset:1568\n\t" \
    "ds_read_b64_tr_b16 v[8:9], v244 offset:64\n\t" \
    "ds_read_b64_tr_b16 v[10:11], v244 offset:1600\n\t" \
    "v_add_u32 v244, 0x9000, v243\n\t" \
    "s_waitcnt vmcnt(12) lgkmcnt(0)\n\t" \
    "s_barrier \n\t" \
    "s_add_u32 m0, %[wm], 0x6000\n\t" \
    "s_mov_b32 s44, 0x3c0000\n\t" \
    "buffer_load_dwordx4 v240, %[wr], s44 offen offset:0 nt lds\n\t" \
    "buffer_load_dwordx4 v241, %[wr], s44 offen offset:1024 nt lds\n\t" \
    "buffer_load_dwordx4 v242, %[wr], s44 offen offset:2048 nt lds\n\t" \
    "ds_read_b64_tr_b16 v[204:205], v244 offset:0\n\t" \
    "ds_read_b64_tr_b16 v[206:207], v244 offset:1536\n\t" \
    "ds_read_b64_tr_b16 v[4:5], v244 offset:32\n\t" \
    "ds_read_b64_tr_b16 v[6:7], v244 offset:1568\n\t" \
    "ds_read_b64_tr_b16 v[12:13], v244 offset:64\n\t" \
    "ds_read_b64_tr_b16 v[14:15], v244 offset:1600\n\t" \
    "v_add_u32 v244, 0xc000, v243\n\t" \
    "s_waitcnt vmcnt(12) lgkmcnt(0)\n\t" \
    "s_barrier \n\t" \
    "s_add_u32 m0, %[wm], 0x9000\n\t" \
    "s_mov_b32 s44, 0x420000\n\t" \
    "buffer_load_dwordx4 v240, %[wr], s44 offen offset:0 nt lds\n\t" \
    "buffer_load_dwordx4 v241, %[wr], s44 offen offset:1024 nt lds\n\t" \
    "buffer_load_dwordx4 v242, %[wr], s44 offen offset:2048 nt lds\n\t" \
    "ds_read_b64_tr_b16 v[208:209], v244 offset:0\n\t" \
    "ds_read_b64_tr_b16 v[210:211], v244 offset:1536\n\t" \
    "ds_read_b64_tr_b16 v[40:41], v244 offset:32\n\t" \
    "ds_read_b64_tr_b16 v[42:43], v244 offset:1568\n\t" \
    "ds_read_b64_tr_b16 v[48:49], v244 offset:64\n\t" \
    "ds_read_b64_tr_b16 v[50:51], v244 offset:1600\n\t" \
    "v_add_u32 v244, 0xf000, v243\n\t" \
    "s_waitcnt vmcnt(12) lgkmcnt(0)\n\t" \
    "s_barrier \n\t" \
    "s_add_u32 m0, %[wm], 0xc000\n\t" \
    "s_mov_b32 s44, 0x480000\n\t" \
    "buffer_load_dwordx4 v240, %[wr], s44 offen offset:0 nt lds\n\t" \
    "buffer_load_dwordx4 v241, %[wr], s44 offen offset:1024 nt lds\n\t" \
    "buffer_load_dwordx4 v242, %[wr], s44 offen offset:2048 nt lds\n\t" \
    "ds_read_b64_tr_b16 v[212:213], v244 offset:0\n\t" \
    "ds_read_b64_tr_b16 v[214:215], v244 offset:1536\n\t" \
    "ds_read_b64_tr_b16 v[44:45], v244 offset:32\n\t" \
    "ds_read_b64_tr_b16 v[46:47], v244 offset:1568\n\t" \
    "ds_read_b64_tr_b16 v[52:53], v244 offset:64\n\t" \
    "ds_read_b64_tr_b16 v[54:55], v244 offset:1600\n\t" \
    "v_add_u32 v244, 0x0, v243\n\t" \
    "s_waitcnt vmcnt(12) lgkmcnt(0)\n\t" \
    "s_barrier \n\t" \
    "v_mov_b32 v246, 0\n\t" \
    "buffer_load_dword v246, v246, s[60:63], %[mo] offen sc1\n\t" \
    "s_add_u32 m0, %[wm], 0xf000\n\t" \
    "s_mov_b32 s44, 0x4e0000\n\t" \
    "buffer_load_dwordx4 v240, %[wr], s44 offen offset:0 nt lds\n\t" \
    "buffer_load_dwordx4 v241, %[wr], s44 offen offset:1024 nt lds\n\t" \
    "buffer_load_dwordx4 v242, %[wr], s44 offen offset:2048 nt lds\n\t" \
    "ds_read_b64_tr_b16 v[136:137], v244 offset:0\n\t" \
    "ds_read_b64_tr_b16 v[138:139], v244 offset:1536\n\t" \
    "ds_read_b64_tr_b16 v[144:145], v244 offset:32\n\t" \
    "ds_read_b64_tr_b16 v[146:147], v244 offset:1568\n\t" \
    "ds_read_b64_tr_b16 v[152:153], v244 offset:64\n\t" \
    "ds_read_b64_tr_b16 v[154:155], v244 offset:1600\n\t" \
    "v_add_u32 v244, 0x3000, v243\n\t" \
    "s_waitcnt vmcnt(12) lgkmcnt(0)\n\t" \
    "s_barrier \n\t" \
    "s_add_u32 m0, %[wm], 0x0\n\t" \
    "s_mov_b32 s44, 0x540000\n\t" \
    "buffer_load_dwordx4 v240, %[wr], s44 offen offset:0 nt lds\n\t" \
    "buffer_load_dwordx4 v241, %[wr], s44 offen offset:1024 nt lds\n\t" \
    "buffer_load_dwordx4 v242, %[wr], s44 offen offset:2048 nt lds\n\t" \
    "ds_read_b64_tr_b16 v[140:141], v244 offset:0\n\t" \
    "ds_read_b64_tr_b16 v[142:143], v244 offset:1536\n\t" \
    "ds_read_b64_tr_b16 v[148:149], v244 offset:32\n\t" \
    "ds_read_b64_tr_b16 v[150:151], v244 offset:1568\n\t" \
    "ds_read_b64_tr_b16 v[156:157], v244 offset:64\n\t" \
    "ds_read_b64_tr_b16 v[158:159], v244 offset:1600\n\t" \
    "v_add_u32 v244, 0x6000, v243\n\t" \
    "s_waitcnt vmcnt(12) lgkmcnt(0)\n\t" \
    "s_barrier \n\t" \
    "s_add_u32 m0, %[wm], 0x3000\n\t" \
    "s_mov_b32 s44, 0x5a0000\n\t" \
    "buffer_load_dwordx4 v240, %[wr], s44 offen offset:0 nt lds\n\t" \
    "buffer_load_dwordx4 v241, %[wr], s44 offen offset:1024 nt lds\n\t" \
    "buffer_load_dwordx4 v242, %[wr], s44 offen offset:2048 nt lds\n\t" \
    "ds_read_b64_tr_b16 v[216:217], v244 offset:0\n\t" \
    "ds_read_b64_tr_b16 v[218:219], v244 offset:1536\n\t" \
    "ds_read_b64_tr_b16 v[80:81], v244 offset:32\n\t" \
    "ds_read_b64_tr_b16 v[82:83], v244 offset:1568\n\t" \
    "ds_read_b64_tr_b16 v[88:89], v244 offset:64\n\t" \
    "ds_read_b64_tr_b16 v[90:91], v244 offset:1600\n\t" \
    "v_add_u32 v244, 0x9000, v243\n\t" \
    "s_waitcnt vmcnt(12) lgkmcnt(0)\n\t" \
    "s_barrier \n\t" \
    "s_add_u32 m0, %[wm], 0x6000\n\t" \
    "s_mov_b32 s44, 0x600000\n\t" \
    "buffer_load_dwordx4 v240, %[wr], s44 offen offset:0 nt lds\n\t" \
    "buffer_load_dwordx4 v241, %[wr], s44 offen offset:1024 nt lds\n\t" \
    "buffer_load_dwordx4 v242, %[wr], s44 offen offset:2048 nt lds\n\t" \
    "ds_read_b64_tr_b16 v[220:221], v244 offset:0\n\t" \
    "ds_read_b64_tr_b16 v[222:223], v244 offset:1536\n\t" \
    "ds_read_b64_tr_b16 v[84:85], v244 offset:32\n\t" \
    "ds_read_b64_tr_b16 v[86:87], v244 offset:1568\n\t" \
    "ds_read_b64_tr_b16 v[92:93], v244 offset:64\n\t" \
    "ds_read_b64_tr_b16 v[94:95], v244 offset:1600\n\t" \
    "v_add_u32 v244, 0xc000, v243\n\t" \
    "s_waitcnt vmcnt(12) lgkmcnt(0)\n\t" \
    "s_barrier \n\t" \
    "s_add_u32 m0, %[wm], 0x9000\n\t" \
    "s_mov_b32 s44, 0x660000\n\t" \
    "buffer_load_dwordx4 v240, %[wr], s44 offen offset:0 nt lds\n\t" \
    "buffer_load_dwordx4 v241, %[wr], s44 offen offset:1024 nt lds\n\t" \
    "buffer_load_dwordx4 v242, %[wr], s44 offen offset:2048 nt lds\n\t" \
    "ds_read_b64_tr_b16 v[16:17], v244 offset:0\n\t" \
    "ds_read_b64_tr_b16 v[18:19], v244 offset:1536\n\t" \
    "ds_read_b64_tr_b16 v[24:25], v244 offset:32\n\t" \
    "ds_read_b64_tr_b16 v[26:27], v244 offset:1568\n\t" \
    "ds_read_b64_tr_b16 v[32:33], v244 offset:64\n\t" \
    "ds_read_b64_tr_b16 v[34:35], v244 offset:1600\n\t" \
    "v_add_u32 v244, 0xf000, v243\n\t" \
    "s_waitcnt vmcnt(12) lgkmcnt(0)\n\t" \
    "s_barrier \n\t" \
    ".Lv11_%=:\n\t" \
    "v_readfirstlane_b32 s43, v246\n\t" \
    "s_nop 0\n\t" \
    "s_cmp_lg_u32 s43, -1\n\t" \
    "s_cbranch_scc1 .Lv12_%=\n\t" \
    "s_cmp_eq_u32 s41, 0\n\t" \
    "s_cbranch_scc1 .Lv12_%=\n\t" \
    "s_sub_u32 s41, s41, 1\n\t" \
    "s_sleep 4\n\t" \
    "v_mov_b32 v246, 0\n\t" \
    "buffer_load_dword v246, v246, s[60:63], %[mo] offen sc1\n\t" \
    "s_waitcnt vmcnt(0)\n\t" \
    "s_branch .Lv11_%=\n\t" \
    ".Lv12_%=:\n\t" \
    "v_mov_b32 v246, 0x8000\n\t" \
    "s_mov_b64 exec, 1\n\t" \
    "buffer_store_dword v246, v246, s[60:63], %[mo] offen sc1\n\t" \
    "s_mov_b64 exec, -1\n\t" \
    "s_and_b32 s44, s43, 0x5000\n\t" \
    "s_xor_b32 %[mb], %[mb], s44\n\t" \
    "s_xor_b32 %[fl], %[fl], s44\n\t" \
    "s_and_b32 s43, s43, 0x2000\n\t" \
    "s_xor_b32 s43, s43, %[lx]\n\t" \
    "s_add_u32 s60, s60, s43\n\t" \
    "s_addc_u32 s61, s61, 0\n\t" \
    "v_and_b32 v252, 0xf0, v253\n\t" \
    "v_lshlrev_b32 v252, 3, v252\n\t" \
    "buffer_load_dword v252, v252, s[60:63], %[fl] offen sc1\n\t" \
    "s_add_u32 m0, %[wm], 0xc000\n\t" \
    "s_mov_b32 s44, 0x6c0000\n\t" \
    "buffer_load_dwordx4 v240, %[wr], s44 offen offset:0 nt lds\n\t" \
    "buffer_load_dwordx4 v241, %[wr], s44 offen offset:1024 nt lds\n\t" \
    "buffer_load_dwordx4 v242, %[wr], s44 offen offset:2048 nt lds\n\t" \
    "ds_read_b64_tr_b16 v[20:21], v244 offset:0\n\t" \
    "ds_read_b64_tr_b16 v[22:23], v244 offset:1536\n\t" \
    "ds_read_b64_tr_b16 v[28:29], v244 offset:32\n\t" \
    "ds_read_b64_tr_b16 v[30:31], v244 offset:1568\n\t" \
    "ds_read_b64_tr_b16 v[36:37], v244 offset:64\n\t" \
    "ds_read_b64_tr_b16 v[38:39], v244 offset:1600\n\t" \
    "v_add_u32 v244, 0x0, v243\n\t" \
    "s_waitcnt vmcnt(12) lgkmcnt(0)\n\t" \
    "s_barrier \n\t" \
    "s_add_u32 m0, %[wm], 0xf000\n\t" \
    "s_mov_b32 s44, 0x720000\n\t" \
    "buffer_load_dwordx4 v240, %[wr], s44 offen offset:0 nt lds\n\t" \
    "buffer_load_dwordx4 v241, %[wr], s44 offen offset:1024 nt lds\n\t" \
    "buffer_load_dwordx4 v242, %[wr], s44 offen offset:2048 nt lds\n\t" \
    "ds_read_b64_tr_b16 v[224:225], v244 offset:0\n\t" \
    "ds_read_b64_tr_b16 v[226:227], v244 offset:1536\n\t" \
    "ds_read_b64_tr_b16 v[120:121], v244 offset:32\n\t" \
    "ds_read_b64_tr_b16 v[122:123], v244 offset:1568\n\t" \
    "ds_read_b64_tr_b16 v[128:129], v244 offset:64\n\t" \
    "ds_read_b64_tr_b16 v[130:131], v244 offset:1600\n\t" \
    "v_add_u32 v244, 0x3000, v243\n\t" \
    "s_waitcnt vmcnt(12) lgkmcnt(0)\n\t" \
    "s_barrier \n\t" \
    "ds_read_b64_tr_b16 v[228:229], v244 offset:0\n\t" \
    "ds_read_b64_tr_b16 v[230:231], v244 offset:1536\n\t" \
    "ds_read_b64_tr_b16 v[124:125], v244 offset:32\n\t" \
    "ds_read_b64_tr_b16 v[126:127], v244 offset:1568\n\t" \
    "ds_read_b64_tr_b16 v[132:133], v244 offset:64\n\t" \
    "ds_read_b64_tr_b16 v[134:135], v244 offset:1600\n\t" \
    "v_add_u32 v244, 0x6000, v243\n\t" \
    "s_waitcnt vmcnt(9) lgkmcnt(0)\n\t" \
    "s_barrier \n\t" \
    "ds_read_b64_tr_b16 v[232:233], v244 offset:0\n\t" \
    "ds_read_b64_tr_b16 v[234:235], v244 offset:1536\n\t" \
    "ds_read_b64_tr_b16 v[160:161], v244 offset:32\n\t" \
    "ds_read_b64_tr_b16 v[162:163], v244 offset:1568\n\t" \
    "ds_read_b64_tr_b16 v[168:169], v244 offset:64\n\t" \
    "ds_read_b64_tr_b16 v[170:171], v244 offset:1600\n\t" \
    "v_add_u32 v244, 0x9000, v243\n\t" \
    "s_waitcnt vmcnt(6) lgkmcnt(0)\n\t" \
    "s_barrier \n\t" \
    "ds_read_b64_tr_b16 v[236:237], v244 offset:0\n\t" \
    "ds_read_b64_tr_b16 v[238:239], v244 offset:1536\n\t" \
    "ds_read_b64_tr_b16 v[164:165], v244 offset:32\n\t" \
    "ds_read_b64_tr_b16 v[166:167], v244 offset:1568\n\t" \
    "ds_read_b64_tr_b16 v[172:173], v244 offset:64\n\t" \
    "ds_read_b64_tr_b16 v[174:175], v244 offset:1600\n\t" \
    "v_add_u32 v244, 0xc000, v243\n\t" \
    "s_waitcnt vmcnt(3) lgkmcnt(0)\n\t" \
    "s_barrier \n\t" \
    "ds_read_b64_tr_b16 v[176:177], v244 offset:0\n\t" \
    "ds_read_b64_tr_b16 v[178:179], v244 offset:1536\n\t" \
    "ds_read_b64_tr_b16 v[184:185], v244 offset:32\n\t" \
    "ds_read_b64_tr_b16 v[186:187], v244 offset:1568\n\t" \
    "ds_read_b64_tr_b16 v[192:193], v244 offset:64\n\t" \
    "ds_read_b64_tr_b16 v[194:195], v244 offset:1600\n\t" \
    "s_mov_b32 s43, 0\n\t" \
    "v_cmp_eq_u32 vcc, 0, v252\n\t" \
    "s_cbranch_vccnz .Lv13_%=\n\t" \
    "s_mov_b32 m0, 0x1000\n\t" \
    "s_add_u32 s49, s42, 0x0\n\t" \
    "buffer_load_dwordx4 v253, s[60:63], s49 offen offset:0 sc1 lds\n\t" \
    "buffer_load_dwordx4 v253, s[60:63], s49 offen offset:1024 sc1 lds\n\t" \
    "buffer_load_dwordx4 v253, s[60:63], s49 offen offset:2048 sc1 lds\n\t" \
    "buffer_load_dwordx4 v253, s[60:63], s49 offen offset:3072 sc1 lds\n\t" \
    "s_mov_b32 m0, 0x4400\n\t" \
    "s_add_u32 s49, s42, 0xc400\n\t" \
    "buffer_load_dwordx4 v253, s[60:63], s49 offen offset:0 sc1 lds\n\t" \
    "s_mov_b32 m0, 0x6000\n\t" \
    "s_add_u32 s49, s42, 0x1000\n\t" \
    "buffer_load_dwordx4 v253, s[60:63], s49 offen offset:0 sc1 lds\n\t" \
    "buffer_load_dwordx4 v253, s[60:63], s49 offen offset:1024 sc1 lds\n\t" \
    "buffer_load_dwordx4 v253, s[60:63], s49 offen offset:2048 sc1 lds\n\t" \
    "buffer_load_dwordx4 v253, s[60:63], s49 offen offset:3072 sc1 lds\n\t" \
    "s_mov_b32 m0, 0x9400\n\t" \
    "s_add_u32 s49, s42, 0xd400\n\t" \
    "buffer_load_dwordx4 v253, s[60:63], s49 offen offset:0 sc1 lds\n\t" \
    "s_mov_b32 s43, 1\n\t" \
    ".Lv13_%=:\n\t" \
    "v_add_u32 v244, 0xf000, v243\n\t" \
    "s_waitcnt vmcnt(0) lgkmcnt(0)\n\t" \
    "s_barrier \n\t" \
    "ds_read_b64_tr_b16 v[180:181], v244 offset:0\n\t" \
    "ds_read_b64_tr_b16 v[182:183], v244 offset:1536\n\t" \
    "ds_read_b64_tr_b16 v[188:189], v244 offset:32\n\t" \
    "ds_read_b64_tr_b16 v[190:191], v244 offset:1568\n\t" \
    "ds_read_b64_tr_b16 v[196:197], v244 offset:64\n\t" \
    "ds_read_b64_tr_b16 v[198:199], v244 offset:1600\n\t" \
    "s_waitcnt lgkmcnt(0)\n\t" \
    "s_barrier \n\t" \
    "ds_write_b128 v254, v[200:203] offset:40960\n\t" \
    "ds_write_b128 v254, v[204:207] offset:41984\n\t" \
    "ds_write_b128 v254, v[208:211] offset:43008\n\t" \
    "ds_write_b128 v254, v[212:215] offset:44032\n\t" \
    "ds_write_b128 v254, v[216:219] offset:45056\n\t" \
    "ds_write_b128 v254, v[220:223] offset:46080\n\t" \
    "ds_write_b128 v254, v[224:227] offset:47104\n\t" \
    "ds_write_b128 v254, v[228:231] offset:48128\n\t" \
    "ds_write_b128 v254, v[232:235] offset:49152\n\t" \
    "ds_write_b128 v254, v[236:239] offset:50176\n\t" \
    "s_cmp_lg_u32 s43, 0\n\t" \
    "s_cbranch_scc1 .Lv14_%=\n\t" \
    "s_mov_b32 s44, 0\n\t" \
    ".Lv15_%=:\n\t" \
    "v_cmp_ge_u32 vcc, s44, v252\n\t" \
    "s_cbranch_vccz .Lv16_%=\n\t" \
    "s_cmp_eq_u32 s41, 0\n\t" \
    "s_cbranch_scc1 .Lv16_%=\n\t" \
    "s_sub_u32 s41, s41, 1\n\t" \
    "s_sleep 8\n\t" \
    "v_and_b32 v252, 0xf0, v253\n\t" \
    "v_lshlrev_b32 v252, 3, v252\n\t" \
    "buffer_load_dword v252, v252, s[60:63], %[fl] offen sc1\n\t" \
    "s_waitcnt vmcnt(0)\n\t" \
    "s_branch .Lv15_%=\n\t" \
    ".Lv16_%=:\n\t" \
    "s_mov_b32 m0, 0x1000\n\t" \
    "s_add_u32 s49, s42, 0x0\n\t" \
    "buffer_load_dwordx4 v253, s[60:63], s49 offen offset:0 sc1 lds\n\t" \
    "buffer_load_dwordx4 v253, s[60:63], s49 offen offset:1024 sc1 lds\n\t" \
    "buffer_load_dwordx4 v253, s[60:63], s49 offen offset:2048 sc1 lds\n\t" \
    "buffer_load_dwordx4 v253, s[60:63], s49 offen offset:3072 sc1 lds\n\t" \
    "s_mov_b32 m0, 0x4400\n\t" \
    "s_add_u32 s49, s42, 0xc400\n\t" \
    "buffer_load_dwordx4 v253, s[60:63], s49 offen offset:0 sc1 lds\n\t" \
    "s_mov_b32 m0, 0x6000\n\t" \
    "s_add_u32 s49, s42, 0x1000\n\t" \
    "buffer_load_dwordx4 v253, s[60:63], s49 offen offset:0 sc1 lds\n\t" \
    "buffer_load_dwordx4 v253, s[60:63], s49 offen offset:1024 sc1 lds\n\t" \
    "buffer_load_dwordx4 v253, s[60:63], s49 offen offset:2048 sc1 lds\n\t" \
    "buffer_load_dwordx4 v253, s[60:63], s49 offen offset:3072 sc1 lds\n\t" \
    "s_mov_b32 m0, 0x9400\n\t" \
    "s_add_u32 s49, s42, 0xd400\n\t" \
    "buffer_load_dwordx4 v253, s[60:63], s49 offen offset:0 sc1 lds\n\t" \
    ".Lv14_%=:\n\t" \
    "s_waitcnt vmcnt(0) lgkmcnt(0)\n\t" \
    "s_barrier \n\t" \
    "ds_read_b128 v[224:227], v253 offset:4096\n\t" \
    "ds_read_b128 v[244:247], v254 offset:40960\n\t" \
    "ds_read_b128 v[228:231], v253 offset:5120\n\t" \
    "ds_read_b128 v[232:235], v253 offset:24576\n\t" \
    "ds_read_b128 v[236:239], v253 offset:25600\n\t" \
    "ds_read_b128 v[248:251], v254 offset:41984\n\t" \
    ".Lv17_%=:\n\t" \
    "s_waitcnt lgkmcnt(5)\n\t" \
    "v_mfma_f32_16x16x32_bf16 v[204:207], v[0:3], v[224:227], 0\n\t" \
    "s_waitcnt lgkmcnt(3)\n\t" \
    "v_mfma_f32_16x16x32_bf16 v[204:207], v[4:7], v[228:231], v[204:207]\n\t" \
    "ds_read_b128 v[240:243], v253 offset:12288\n\t" \
    "v_mfma_f32_16x16x32_bf16 v[208:211], v[8:11], v[224:227], 0\n\t" \
    "v_mfma_f32_16x16x32_bf16 v[208:211], v[12:15], v[228:231], v[208:211]\n\t" \
    "v_mfma_f32_16x16x32_bf16 v[200:203], v[244:247], v[224:227], 0\n\t" \
    "s_waitcnt lgkmcnt(1)\n\t" \
    "v_mfma_f32_16x16x32_bf16 v[200:203], v[248:251], v[228:231], v[200:203]\n\t" \
    "v_mfma_f32_16x16x32_bf16 v[216:219], v[0:3], v[232:235], 0\n\t" \
    "ds_read_b128 v[224:227], v253 offset:13312\n\t" \
    "v_mfma_f32_16x16x32_bf16 v[216:219], v[4:7], v[236:239], v[216:219]\n\t" \
    "ds_read_b128 v[228:231], v253 offset:6144\n\t" \
    "v_mfma_f32_16x16x32_bf16 v[220:223], v[8:11], v[232:235], 0\n\t" \
    "v_mfma_f32_16x16x32_bf16 v[220:223], v[12:15], v[236:239], v[220:223]\n\t" \
    "v_mfma_f32_16x16x32_bf16 v[212:215], v[244:247], v[232:235], 0\n\t" \
    "ds_read_b128 v[244:247], v254 offset:43008\n\t" \
    "v_mfma_f32_16x16x32_bf16 v[212:215], v[248:251], v[236:239], v[212:215]\n\t" \
    "ds_read_b128 v[248:251], v254 offset:44032\n\t" \
    "s_waitcnt lgkmcnt(4)\n\t" \
    "v_mfma_f32_16x16x32_bf16 v[204:207], v[24:27], v[240:243], v[204:207]\n\t" \
    "ds_read_b128 v[232:235], v253 offset:7168\n\t" \
    "s_waitcnt lgkmcnt(4)\n\t" \
    "v_mfma_f32_16x16x32_bf16 v[204:207], v[28:31], v[224:227], v[204:207]\n\t" \
    "ds_read_b128 v[236:239], v253 offset:26624\n\t" \
    "v_mfma_f32_16x16x32_bf16 v[208:211], v[32:35], v[240:243], v[208:211]\n\t" \
    "v_mfma_f32_16x16x32_bf16 v[208:211], v[36:39], v[224:227], v[208:211]\n\t" \
    "v_mfma_f32_16x16x32_bf16 v[200:203], v[16:19], v[240:243], v[200:203]\n\t" \
    "v_mfma_f32_16x16x32_bf16 v[200:203], v[20:23], v[224:227], v[200:203]\n\t" \
    "s_waitcnt lgkmcnt(4)\n\t" \
    "v_mfma_f32_16x16x32_bf16 v[204:207], v[40:43], v[228:231], v[204:207]\n\t" \
    "ds_read_b128 v[240:243], v253 offset:27648\n\t" \
    "s_waitcnt lgkmcnt(2)\n\t" \
    "v_mfma_f32_16x16x32_bf16 v[204:207], v[44:47], v[232:235], v[204:207]\n\t" \
    "ds_read_b128 v[224:227], v253 offset:0\n\t" \
    "v_mfma_f32_16x16x32_bf16 v[208:211], v[48:51], v[228:231], v[208:211]\n\t" \
    "v_mfma_f32_16x16x32_bf16 v[208:211], v[52:55], v[232:235], v[208:211]\n\t" \
    "v_mfma_f32_16x16x32_bf16 v[200:203], v[244:247], v[228:231], v[200:203]\n\t" \
    "v_mfma_f32_16x16x32_bf16 v[200:203], v[248:251], v[232:235], v[200:203]\n\t" \
    "s_waitcnt lgkmcnt(2)\n\t" \
    "v_mfma_f32_16x16x32_bf16 v[216:219], v[40:43], v[236:239], v[216:219]\n\t" \
    "ds_read_b128 v[228:231], v253 offset:1024\n\t" \
    "s_add_u32 s44, s40, 1\n\t" \
    "s_waitcnt lgkmcnt(2)\n\t" \
    "v_mfma_f32_16x16x32_bf16 v[216:219], v[44:47], v[240:243], v[216:219]\n\t" \
    "ds_read_b128 v[232:235], v253 offset:10240\n\t" \
    "s_and_b32 s45, s44, 15\n\t" \
    "v_mfma_f32_16x16x32_bf16 v[220:223], v[48:51], v[236:239], v[220:223]\n\t" \
    "s_cmp_eq_u32 s45, 0\n\t" \
    "v_mfma_f32_16x16x32_bf16 v[220:223], v[52:55], v[240:243], v[220:223]\n\t" \
    "s_cselect_b32 s45, %[cj], %[cs]\n\t" \
    "v_mfma_f32_16x16x32_bf16 v[212:215], v[244:247], v[236:239], v[212:215]\n\t" \
    "ds_read_b128 v[244:247], v254 offset:45056\n\t" \
    "s_cmp_eq_u32 s44, 64\n\t" \
    "v_mfma_f32_16x16x32_bf16 v[212:215], v[248:251], v[240:243], v[212:215]\n\t" \
    "ds_read_b128 v[248:251], v254 offset:46080\n\t" \
    "s_cselect_b32 s45, 0, s45\n\t" \
    "s_waitcnt lgkmcnt(4)\n\t" \
    "v_mfma_f32_16x16x32_bf16 v[204:207], v[64:67], v[224:227], v[204:207]\n\t" \
    "ds_read_b128 v[236:239], v253 offset:11264\n\t" \
    "s_add_u32 s42, s42, s45\n\t" \
    "s_waitcnt lgkmcnt(4)\n\t" \
    "v_mfma_f32_16x16x32_bf16 v[204:207], v[68:71], v[228:231], v[204:207]\n\t" \
    "ds_read_b128 v[240:243], v253 offset:30720\n\t" \
    "v_mfma_f32_16x16x32_bf16 v[208:211], v[72:75], v[224:227], v[208:211]\n\t" \
    "v_mfma_f32_16x16x32_bf16 v[208:211], v[76:79], v[228:231], v[208:211]\n\t" \
    "v_mfma_f32_16x16x32_bf16 v[200:203], v[56:59], v[224:227], v[200:203]\n\t" \
    "v_mfma_f32_16x16x32_bf16 v[200:203], v[60:63], v[228:231], v[200:203]\n\t" \
    "s_waitcnt lgkmcnt(4)\n\t" \
    "v_mfma_f32_16x16x32_bf16 v[204:207], v[80:83], v[232:235], v[204:207]\n\t" \
    "ds_read_b128 v[224:227], v253 offset:31744\n\t" \
    "s_waitcnt lgkmcnt(2)\n\t" \
    "v_mfma_f32_16x16x32_bf16 v[204:207], v[84:87], v[236:239], v[204:207]\n\t" \
    "ds_read_b128 v[228:231], v253 offset:2048\n\t" \
    "v_mfma_f32_16x16x32_bf16 v[208:211], v[88:91], v[232:235], v[208:211]\n\t" \
    "v_mfma_f32_16x16x32_bf16 v[208:211], v[92:95], v[236:239], v[208:211]\n\t" \
    "v_mfma_f32_16x16x32_bf16 v[200:203], v[244:247], v[232:235], v[200:203]\n\t" \
    "v_mfma_f32_16x16x32_bf16 v[200:203], v[248:251], v[236:239], v[200:203]\n\t" \
    "s_waitcnt lgkmcnt(2)\n\t" \
    "v_mfma_f32_16x16x32_bf16 v[216:219], v[80:83], v[240:243], v[216:219]\n\t" \
    "ds_read_b128 v[232:235], v253 offset:3072\n\t" \
    "s_and_b32 s44, s40, 3\n\t" \
    "s_waitcnt lgkmcnt(2)\n\t" \
    "v_mfma_f32_16x16x32_bf16 v[216:219], v[84:87], v[224:227], v[216:219]\n\t" \
    "ds_read_b128 v[236:239], v253 offset:14336\n\t" \
    "s_lshl_b32 s44, s44, 22\n\t" \
    "v_mfma_f32_16x16x32_bf16 v[220:223], v[88:91], v[240:243], v[220:223]\n\t" \
    "s_add_u32 s44, s44, %[mb]\n\t" \
    "v_mfma_f32_16x16x32_bf16 v[220:223], v[92:95], v[224:227], v[220:223]\n\t" \
    "s_add_u32 s46, s44, 0x800\n\t" \
    "v_mfma_f32_16x16x32_bf16 v[212:215], v[244:247], v[240:243], v[212:215]\n\t" \
    "ds_read_b128 v[244:247], v254 offset:47104\n\t" \
    "s_add_u32 s47, s44, 0x8800\n\t" \
    "v_mfma_f32_16x16x32_bf16 v[212:215], v[248:251], v[224:227], v[212:215]\n\t" \
    "ds_read_b128 v[248:251], v254 offset:48128\n\t" \
    "s_add_u32 s48, s44, 0xc00\n\t" \
    "s_waitcnt lgkmcnt(4)\n\t" \
    "v_mfma_f32_16x16x32_bf16 v[204:207], v[104:107], v[228:231], v[204:207]\n\t" \
    "ds_read_b128 v[240:243], v253 offset:15360\n\t" \
    "s_waitcnt lgkmcnt(4)\n\t" \
    "v_mfma_f32_16x16x32_bf16 v[204:207], v[108:111], v[232:235], v[204:207]\n\t" \
    "ds_read_b128 v[224:227], v253 offset:34816\n\t" \
    "v_mfma_f32_16x16x32_bf16 v[208:211], v[112:115], v[228:231], v[208:211]\n\t" \
    "v_mfma_f32_16x16x32_bf16 v[208:211], v[116:119], v[232:235], v[208:211]\n\t" \
    "v_mfma_f32_16x16x32_bf16 v[200:203], v[96:99], v[228:231], v[200:203]\n\t" \
    "v_mfma_f32_16x16x32_bf16 v[200:203], v[100:103], v[232:235], v[200:203]\n\t" \
    "s_waitcnt lgkmcnt(4)\n\t" \
    "v_mfma_f32_16x16x32_bf16 v[204:207], v[120:123], v[236:239], v[204:207]\n\t" \
    "ds_read_b128 v[228:231], v253 offset:35840\n\t" \
    "s_waitcnt lgkmcnt(2)\n\t" \
    "v_mfma_f32_16x16x32_bf16 v[204:207], v[124:127], v[240:243], v[204:207]\n\t" \
    "ds_read_b128 v[232:235], v253 offset:8192\n\t" \
    "v_mfma_f32_16x16x32_bf16 v[208:211], v[128:131], v[236:239], v[208:211]\n\t" \
    "v_mfma_f32_16x16x32_bf16 v[208:211], v[132:135], v[240:243], v[208:211]\n\t" \
    "v_mfma_f32_16x16x32_bf16 v[200:203], v[244:247], v[236:239], v[200:203]\n\t" \
    "v_mfma_f32_16x16x32_bf16 v[200:203], v[248:251], v[240:243], v[200:203]\n\t" \
    "s_waitcnt lgkmcnt(2)\n\t" \
    "v_mfma_f32_16x16x32_bf16 v[216:219], v[120:123], v[224:227], v[216:219]\n\t" \
    "ds_read_b128 v[236:239], v253 offset:9216\n\t" \
    "s_waitcnt lgkmcnt(2)\n\t" \
    "v_mfma_f32_16x16x32_bf16 v[216:219], v[124:127], v[228:231], v[216:219]\n\t" \
    "ds_read_b128 v[240:243], v253 offset:16384\n\t" \
    "v_mfma_f32_16x16x32_bf16 v[220:223], v[128:131], v[224:227], v[220:223]\n\t" \
    "v_mfma_f32_16x16x32_bf16 v[220:223], v[132:135], v[228:231], v[220:223]\n\t" \
    "v_mfma_f32_16x16x32_bf16 v[212:215], v[244:247], v[224:227], v[212:215]\n\t" \
    "ds_read_b128 v[244:247], v254 offset:49152\n\t" \
    "v_mfma_f32_16x16x32_bf16 v[212:215], v[248:251], v[228:231], v[212:215]\n\t" \
    "ds_read_b128 v[248:251], v254 offset:50176\n\t" \
    "v_and_b32 v252, 0xf0, v253\n\t" \
    "v_lshlrev_b32 v252, 3, v252\n\t" \
    "buffer_load_dword v252, v252, s[60:63], %[fl] offen sc1\n\t" \
    "s_waitcnt lgkmcnt(4)\n\t" \
    "v_mfma_f32_16x16x32_bf16 v[204:207], v[144:147], v[232:235], v[204:207]\n\t" \
    "ds_read_b128 v[224:227], v253 offset:17408\n\t" \
    "s_waitcnt lgkmcnt(4)\n\t" \
    "v_mfma_f32_16x16x32_bf16 v[204:207], v[148:151], v[236:239], v[204:207]\n\t" \
    "ds_read_b128 v[228:231], v253 offset:36864\n\t" \
    "v_mfma_f32_16x16x32_bf16 v[208:211], v[152:155], v[232:235], v[208:211]\n\t" \
    "v_mfma_f32_16x16x32_bf16 v[208:211], v[156:159], v[236:239], v[208:211]\n\t" \
    "v_mfma_f32_16x16x32_bf16 v[200:203], v[136:139], v[232:235], v[200:203]\n\t" \
    "v_mfma_f32_16x16x32_bf16 v[200:203], v[140:143], v[236:239], v[200:203]\n\t" \
    "s_waitcnt lgkmcnt(4)\n\t" \
    "v_mfma_f32_16x16x32_bf16 v[204:207], v[160:163], v[240:243], v[204:207]\n\t" \
    "ds_read_b128 v[232:235], v253 offset:37888\n\t" \
    "s_waitcnt lgkmcnt(2)\n\t" \
    "v_mfma_f32_16x16x32_bf16 v[204:207], v[164:167], v[224:227], v[204:207]\n\t" \
    "ds_read_b128 v[236:239], v253 offset:18432\n\t" \
    "v_mfma_f32_16x16x32_bf16 v[208:211], v[168:171], v[240:243], v[208:211]\n\t" \
    "v_mfma_f32_16x16x32_bf16 v[208:211], v[172:175], v[224:227], v[208:211]\n\t" \
    "v_mfma_f32_16x16x32_bf16 v[200:203], v[244:247], v[240:243], v[200:203]\n\t" \
    "v_mfma_f32_16x16x32_bf16 v[200:203], v[248:251], v[224:227], v[200:203]\n\t" \
    "s_waitcnt lgkmcnt(2)\n\t" \
    "v_mfma_f32_16x16x32_bf16 v[216:219], v[160:163], v[228:231], v[216:219]\n\t" \
    "ds_read_b128 v[240:243], v253 offset:19456\n\t" \
    "s_waitcnt lgkmcnt(2)\n\t" \
    "v_mfma_f32_16x16x32_bf16 v[216:219], v[164:167], v[232:235], v[216:219]\n\t" \
    "ds_read_b128 v[224:227], v253 offset:32768\n\t" \
    "v_mfma_f32_16x16x32_bf16 v[220:223], v[168:171], v[228:231], v[220:223]\n\t" \
    "v_mfma_f32_16x16x32_bf16 v[220:223], v[172:175], v[232:235], v[220:223]\n\t" \
    "v_mfma_f32_16x16x32_bf16 v[212:215], v[244:247], v[228:231], v[212:215]\n\t" \
    "v_mfma_f32_16x16x32_bf16 v[212:215], v[248:251], v[232:235], v[212:215]\n\t" \
    "s_waitcnt lgkmcnt(2)\n\t" \
    "v_mfma_f32_16x16x32_bf16 v[204:207], v[184:187], v[236:239], v[204:207]\n\t" \
    "ds_read_b128 v[228:231], v253 offset:33792\n\t" \
    "s_waitcnt lgkmcnt(2)\n\t" \
    "v_mfma_f32_16x16x32_bf16 v[204:207], v[188:191], v[240:243], v[204:207]\n\t" \
    "ds_read_b128 v[232:235], v253 offset:20480\n\t" \
    "v_mfma_f32_16x16x32_bf16 v[208:211], v[192:195], v[236:239], v[208:211]\n\t" \
    "v_mfma_f32_16x16x32_bf16 v[208:211], v[196:199], v[240:243], v[208:211]\n\t" \
    "v_mfma_f32_16x16x32_bf16 v[200:203], v[176:179], v[236:239], v[200:203]\n\t" \
    "v_mfma_f32_16x16x32_bf16 v[200:203], v[180:183], v[240:243], v[200:203]\n\t" \
    "s_waitcnt lgkmcnt(2)\n\t" \
    "v_mfma_f32_16x16x32_bf16 v[216:219], v[24:27], v[224:227], v[216:219]\n\t" \
    "ds_read_b128 v[236:239], v253 offset:21504\n\t" \
    "s_waitcnt lgkmcnt(2)\n\t" \
    "v_mfma_f32_16x16x32_bf16 v[216:219], v[28:31], v[228:231], v[216:219]\n\t" \
    "ds_read_b128 v[240:243], v253 offset:22528\n\t" \
    "v_mfma_f32_16x16x32_bf16 v[220:223], v[32:35], v[224:227], v[220:223]\n\t" \
    "v_cvt_pk_bf16_f32 v204, v204, v205\n\t" \
    "v_mfma_f32_16x16x32_bf16 v[220:223], v[36:39], v[228:231], v[220:223]\n\t" \
    "v_cvt_pk_bf16_f32 v205, v206, v207\n\t" \
    "v_mfma_f32_16x16x32_bf16 v[212:215], v[16:19], v[224:227], v[212:215]\n\t" \
    "ds_write_b64 v254, v[204:205] offset:10240\n\t" \
    "v_mfma_f32_16x16x32_bf16 v[212:215], v[20:23], v[228:231], v[212:215]\n\t" \
    "v_cvt_pk_bf16_f32 v208, v208, v209\n\t" \
    "s_waitcnt lgkmcnt(3)\n\t" \
    "v_mfma_f32_16x16x32_bf16 v[216:219], v[64:67], v[232:235], v[216:219]\n\t" \
    "ds_read_b128 v[224:227], v253 offset:23552\n\t" \
    "v_cvt_pk_bf16_f32 v209, v210, v211\n\t" \
    "s_waitcnt lgkmcnt(3)\n\t" \
    "v_mfma_f32_16x16x32_bf16 v[216:219], v[68:71], v[236:239], v[216:219]\n\t" \
    "ds_read_b128 v[228:231], v253 offset:28672\n\t" \
    "ds_write_b64 v254, v[208:209] offset:11264\n\t" \
    "v_mfma_f32_16x16x32_bf16 v[220:223], v[72:75], v[232:235], v[220:223]\n\t" \
    "v_cvt_pk_bf16_f32 v200, v200, v201\n\t" \
    "v_mfma_f32_16x16x32_bf16 v[220:223], v[76:79], v[236:239], v[220:223]\n\t" \
    "v_cvt_pk_bf16_f32 v201, v202, v203\n\t" \
    "v_mfma_f32_16x16x32_bf16 v[212:215], v[56:59], v[232:235], v[212:215]\n\t" \
    "ds_write_b64 v254, v[200:201] offset:9216\n\t" \
    "v_mfma_f32_16x16x32_bf16 v[212:215], v[60:63], v[236:239], v[212:215]\n\t" \
    "s_waitcnt lgkmcnt(5)\n\t" \
    "v_mfma_f32_16x16x32_bf16 v[216:219], v[104:107], v[240:243], v[216:219]\n\t" \
    "ds_read_b128 v[232:235], v253 offset:29696\n\t" \
    "s_waitcnt lgkmcnt(4)\n\t" \
    "v_mfma_f32_16x16x32_bf16 v[216:219], v[108:111], v[224:227], v[216:219]\n\t" \
    "ds_read_b128 v[236:239], v253 offset:38912\n\t" \
    "v_mfma_f32_16x16x32_bf16 v[220:223], v[112:115], v[240:243], v[220:223]\n\t" \
    "v_mfma_f32_16x16x32_bf16 v[220:223], v[116:119], v[224:227], v[220:223]\n\t" \
    "v_mfma_f32_16x16x32_bf16 v[212:215], v[96:99], v[240:243], v[212:215]\n\t" \
    "v_mfma_f32_16x16x32_bf16 v[212:215], v[100:103], v[224:227], v[212:215]\n\t" \
    "ds_read_b128 v[240:243], v253 offset:39936\n\t" \
    "s_waitcnt lgkmcnt(3)\n\t" \
    "s_barrier \n\t" \
    "v_mfma_f32_16x16x32_bf16 v[216:219], v[144:147], v[228:231], v[216:219]\n\t" \
    "s_waitcnt lgkmcnt(2)\n\t" \
    "v_mfma_f32_16x16x32_bf16 v[216:219], v[148:151], v[232:235], v[216:219]\n\t" \
    "s_waitcnt lgkmcnt(1)\n\t" \
    "v_mfma_f32_16x16x32_bf16 v[216:219], v[184:187], v[236:239], v[216:219]\n\t" \
    "s_waitcnt lgkmcnt(0)\n\t" \
    "v_mfma_f32_16x16x32_bf16 v[216:219], v[188:191], v[240:243], v[216:219]\n\t" \
    "v_mfma_f32_16x16x32_bf16 v[220:223], v[152:155], v[228:231], v[220:223]\n\t" \
    "v_mfma_f32_16x16x32_bf16 v[220:223], v[156:159], v[232:235], v[220:223]\n\t" \
    "v_mfma_f32_16x16x32_bf16 v[220:223], v[192:195], v[236:239], v[220:223]\n\t" \
    "v_cvt_pk_bf16_f32 v216, v216, v217\n\t" \
    "v_cvt_pk_bf16_f32 v217, v218, v219\n\t" \
    "v_mfma_f32_16x16x32_bf16 v[220:223], v[196:199], v[240:243], v[220:223]\n\t" \
    "ds_write_b64 v254, v[216:217] offset:16384\n\t" \
    "v_mfma_f32_16x16x32_bf16 v[212:215], v[136:139], v[228:231], v[212:215]\n\t" \
    "v_mfma_f32_16x16x32_bf16 v[212:215], v[140:143], v[232:235], v[212:215]\n\t" \
    "v_mfma_f32_16x16x32_bf16 v[212:215], v[176:179], v[236:239], v[212:215]\n\t" \
    "v_cvt_pk_bf16_f32 v220, v220, v221\n\t" \
    "v_cvt_pk_bf16_f32 v221, v222, v223\n\t" \
    "v_mfma_f32_16x16x32_bf16 v[212:215], v[180:183], v[240:243], v[212:215]\n\t" \
    "ds_write_b64 v254, v[220:221] offset:17408\n\t" \
    "s_nop 6\n\t" \
    "v_cvt_pk_bf16_f32 v212, v212, v213\n\t" \
    "v_cvt_pk_bf16_f32 v213, v214, v215\n\t" \
    "ds_write_b64 v254, v[212:213] offset:15360\n\t" \
    "s_waitcnt vmcnt(0) lgkmcnt(0)\n\t" \
    "s_barrier \n\t" \
    "s_setprio 2\n\t" \
    "s_add_u32 s44, s40, 1\n\t" \
    "s_min_u32 s44, s44, 63\n\t" \
    ".Lv18_%=:\n\t" \
    "v_cmp_ge_u32 vcc, s44, v252\n\t" \
    "s_cbranch_vccz .Lv19_%=\n\t" \
    "s_cmp_eq_u32 s41, 0\n\t" \
    "s_cbranch_scc1 .Lv19_%=\n\t" \
    "s_sub_u32 s41, s41, 1\n\t" \
    "s_sleep 2\n\t" \
    "v_and_b32 v252, 0xf0, v253\n\t" \
    "v_lshlrev_b32 v252, 3, v252\n\t" \
    "buffer_load_dword v252, v252, s[60:63], %[fl] offen sc1\n\t" \
    "s_waitcnt vmcnt(0)\n\t" \
    "s_branch .Lv18_%=\n\t" \
    ".Lv19_%=:\n\t" \
    "s_mov_b32 m0, 0x1000\n\t" \
    "s_add_u32 s49, s42, 0x0\n\t" \
    "buffer_load_dwordx4 v253, s[60:63], s49 offen offset:0 sc1 lds\n\t" \
    "s_sleep 1\n\t" \
    "buffer_load_dwordx4 v253, s[60:63], s49 offen offset:1024 sc1 lds\n\t" \
    "s_sleep 1\n\t" \
    "buffer_load_dwordx4 v253, s[60:63], s49 offen offset:2048 sc1 lds\n\t" \
    "s_sleep 1\n\t" \
    "buffer_load_dwordx4 v253, s[60:63], s49 offen offset:3072 sc1 lds\n\t" \
    "s_sleep 1\n\t" \
    "s_mov_b32 m0, 0x4400\n\t" \
    "s_add_u32 s49, s42, 0xc400\n\t" \
    "buffer_load_dwordx4 v253, s[60:63], s49 offen offset:0 sc1 lds\n\t" \
    "s_sleep 1\n\t" \
    "s_mov_b32 m0, 0x6000\n\t" \
    "s_add_u32 s49, s42, 0x1000\n\t" \
    "buffer_load_dwordx4 v253, s[60:63], s49 offen offset:0 sc1 lds\n\t" \
    "s_sleep 1\n\t" \
    "buffer_load_dwordx4 v253, s[60:63], s49 offen offset:1024 sc1 lds\n\t" \
    "s_sleep 1\n\t" \
    "buffer_load_dwordx4 v253, s[60:63], s49 offen offset:2048 sc1 lds\n\t" \
    "s_sleep 1\n\t" \
    "buffer_load_dwordx4 v253, s[60:63], s49 offen offset:3072 sc1 lds\n\t" \
    "s_sleep 1\n\t" \
    "s_mov_b32 m0, 0x9400\n\t" \
    "s_add_u32 s49, s42, 0xd400\n\t" \
    "buffer_load_dwordx4 v253, s[60:63], s49 offen offset:0 sc1 lds\n\t" \
    "s_sleep 1\n\t" \
    "ds_read_b128 v[200:203], v254 offset:40960\n\t" \
    "ds_read_b128 v[204:207], v254 offset:41984\n\t" \
    "ds_read_b128 v[216:219], v254 offset:43008\n\t" \
    "ds_read_b128 v[220:223], v254 offset:44032\n\t" \
    "v_mov_b64 v[208:209], 0\n\t" \
    "v_mov_b64 v[210:211], 0\n\t" \
    "v_mov_b64 v[244:245], 0\n\t" \
    "v_mov_b64 v[246:247], 0\n\t" \
    "v_mov_b64 v[248:249], 0\n\t" \
    "v_mov_b64 v[250:251], 0\n\t" \
    "s_sleep 5\n\t" \
    "s_mov_b32 m0, 0xa800\n\t" \
    "s_nop 0\n\t" \
    "buffer_load_dwordx4 v253, s[60:63], s46 offen sc1 lds\n\t" \
    "s_mov_b32 m0, 0xb800\n\t" \
    "s_nop 0\n\t" \
    "buffer_load_dwordx4 v253, s[60:63], s47 offen sc1 lds\n\t" \
    "s_mov_b32 m0, 0xac00\n\t" \
    "s_nop 0\n\t" \
    "buffer_load_dwordx4 v253, s[60:63], s48 offen sc1 lds\n\t" \
    "s_waitcnt lgkmcnt(0)\n\t" \
    "v_bfe_i32 v212, v253, 5, 1\n\t" \
    "v_and_b32 v212, 0xaaaa, v212\n\t" \
    "v_or_b32 v212, 0x4444, v212\n\t" \
    "v_and_b32 v213, 0xffff, v255\n\t" \
    "v_lshrrev_b32 v214, 16, v255\n\t" \
    ".Lv110_%=:\n\t" \
    "s_waitcnt vmcnt(0)\n\t" \
    "ds_read_b128 v[224:227], v253 offset:43008\n\t" \
    "ds_read_b128 v[228:231], v253 offset:47104\n\t" \
    "ds_read_b128 v[232:235], v253 offset:44032\n\t" \
    "s_waitcnt lgkmcnt(2)\n\t" \
    "v_max_u32 v215, v224, v225\n\t" \
    "v_max3_u32 v215, v226, v227, v215\n\t" \
    "s_waitcnt lgkmcnt(1)\n\t" \
    "v_max3_u32 v215, v228, v229, v215\n\t" \
    "v_max3_u32 v215, v230, v231, v215\n\t" \
    "s_waitcnt lgkmcnt(0)\n\t" \
    "v_max3_u32 v215, v232, v233, v215\n\t" \
    "v_max3_u32 v215, v234, v235, v215\n\t" \
    "v_cmp_eq_u32 vcc, -1, v215\n\t" \
    "s_cbranch_vccz .Lv111_%=\n\t" \
    "v_max_u32 v215, v224, v225\n\t" \
    "v_max3_u32 v215, v226, v227, v215\n\t" \
    "v_cmp_eq_u32 vcc, -1, v215\n\t" \
    "s_cbranch_vccz .Lv112_%=\n\t" \
    "s_mov_b32 m0, 0xa800\n\t" \
    "s_nop 0\n\t" \
    "buffer_load_dwordx4 v253, s[60:63], s46 offen sc1 lds\n\t" \
    ".Lv112_%=:\n\t" \
    "v_max_u32 v215, v228, v229\n\t" \
    "v_max3_u32 v215, v230, v231, v215\n\t" \
    "v_cmp_eq_u32 vcc, -1, v215\n\t" \
    "s_cbranch_vccz .Lv113_%=\n\t" \
    "s_mov_b32 m0, 0xb800\n\t" \
    "s_nop 0\n\t" \
    "buffer_load_dwordx4 v253, s[60:63], s47 offen sc1 lds\n\t" \
    ".Lv113_%=:\n\t" \
    "v_max_u32 v215, v232, v233\n\t" \
    "v_max3_u32 v215, v234, v235, v215\n\t" \
    "v_cmp_eq_u32 vcc, -1, v215\n\t" \
    "s_cbranch_vccz .Lv114_%=\n\t" \
    "s_mov_b32 m0, 0xac00\n\t" \
    "s_nop 0\n\t" \
    "buffer_load_dwordx4 v253, s[60:63], s48 offen sc1 lds\n\t" \
    ".Lv114_%=:\n\t" \
    "s_cmp_eq_u32 s41, 0\n\t" \
    "s_cbranch_scc1 .Lv111_%=\n\t" \
    "s_sub_u32 s41, s41, 1\n\t" \
    "s_branch .Lv110_%=\n\t" \
    ".Lv111_%=:\n\t" \
    "s_waitcnt lgkmcnt(0)\n\t" \
    "s_barrier \n\t" \
    "ds_read_b128 v[236:239], v253 offset:40960\n\t" \
    "ds_read_b128 v[240:243], v253 offset:46080\n\t" \
    "v_smfmac_f32_16x16x64_bf16 v[244:247], v[224:227], v[0:7], v212\n\t" \
    "v_smfmac_f32_16x16x64_bf16 v[248:251], v[224:227], v[8:15], v212\n\t" \
    "v_smfmac_f32_16x16x64_bf16 v[208:211], v[224:227], v[200:207], v212\n\t" \
    "ds_read_b128 v[200:203], v254 offset:45056\n\t" \
    "ds_read_b128 v[204:207], v254 offset:46080\n\t" \
    "ds_read_b128 v[224:227], v253 offset:41984\n\t" \
    "v_smfmac_f32_16x16x64_bf16 v[208:211], v[228:231], v[16:23], v212\n\t" \
    "v_smfmac_f32_16x16x64_bf16 v[244:247], v[228:231], v[24:31], v212\n\t" \
    "v_smfmac_f32_16x16x64_bf16 v[248:251], v[228:231], v[32:39], v212\n\t" \
    "ds_read_b128 v[228:231], v253 offset:48128\n\t" \
    "v_smfmac_f32_16x16x64_bf16 v[208:211], v[232:235], v[216:223], v212\n\t" \
    "ds_read_b128 v[216:219], v254 offset:47104\n\t" \
    "ds_read_b128 v[220:223], v254 offset:48128\n\t" \
    "v_smfmac_f32_16x16x64_bf16 v[244:247], v[232:235], v[40:47], v212\n\t" \
    "v_smfmac_f32_16x16x64_bf16 v[248:251], v[232:235], v[48:55], v212\n\t" \
    "ds_read_b128 v[232:235], v253 offset:45056\n\t" \
    "s_waitcnt lgkmcnt(8)\n\t" \
    "v_smfmac_f32_16x16x64_bf16 v[208:211], v[236:239], v[56:63], v212\n\t" \
    "v_smfmac_f32_16x16x64_bf16 v[244:247], v[236:239], v[64:71], v212\n\t" \
    "v_smfmac_f32_16x16x64_bf16 v[248:251], v[236:239], v[72:79], v212\n\t" \
    "ds_read_b128 v[236:239], v253 offset:49152\n\t" \
    "s_waitcnt lgkmcnt(6)\n\t" \
    "v_smfmac_f32_16x16x64_bf16 v[208:211], v[240:243], v[200:207], v212\n\t" \
    "ds_read_b128 v[200:203], v254 offset:49152\n\t" \
    "ds_read_b128 v[204:207], v254 offset:50176\n\t" \
    "v_smfmac_f32_16x16x64_bf16 v[244:247], v[240:243], v[80:87], v212\n\t" \
    "v_smfmac_f32_16x16x64_bf16 v[248:251], v[240:243], v[88:95], v212\n\t" \
    "ds_read_b128 v[240:243], v253 offset:50176\n\t" \
    "s_waitcnt lgkmcnt(8)\n\t" \
    "v_smfmac_f32_16x16x64_bf16 v[208:211], v[224:227], v[96:103], v212\n\t" \
    "v_smfmac_f32_16x16x64_bf16 v[244:247], v[224:227], v[104:111], v212\n\t" \
    "v_smfmac_f32_16x16x64_bf16 v[248:251], v[224:227], v[112:119], v212\n\t" \
    "s_waitcnt lgkmcnt(7)\n\t" \
    "v_smfmac_f32_16x16x64_bf16 v[244:247], v[228:231], v[120:127], v212\n\t" \
    "s_waitcnt lgkmcnt(4)\n\t" \
    "v_smfmac_f32_16x16x64_bf16 v[244:247], v[232:235], v[144:151], v212\n\t" \
    "s_waitcnt lgkmcnt(3)\n\t" \
    "v_smfmac_f32_16x16x64_bf16 v[244:247], v[236:239], v[160:167], v212\n\t" \
    "s_waitcnt lgkmcnt(0)\n\t" \
    "v_smfmac_f32_16x16x64_bf16 v[244:247], v[240:243], v[184:191], v212\n\t" \
    "v_smfmac_f32_16x16x64_bf16 v[248:251], v[228:231], v[128:135], v212\n\t" \
    "v_smfmac_f32_16x16x64_bf16 v[248:251], v[232:235], v[152:159], v212\n\t" \
    "v_smfmac_f32_16x16x64_bf16 v[248:251], v[236:239], v[168:175], v212\n\t" \
    "v_add_f32 v244, v244, v246\n\t" \
    "v_add_f32 v245, v245, v247\n\t" \
    "v_cvt_pk_bf16_f32 v244, v244, v245\n\t" \
    "v_smfmac_f32_16x16x64_bf16 v[248:251], v[240:243], v[192:199], v212\n\t" \
    "ds_write_b16 v213, v244 offset:512\n\t" \
    "ds_write_b16_d16_hi v213, v244 offset:528\n\t" \
    "v_smfmac_f32_16x16x64_bf16 v[208:211], v[228:231], v[216:223], v212\n\t" \
    "v_smfmac_f32_16x16x64_bf16 v[208:211], v[232:235], v[136:143], v212\n\t" \
    "v_smfmac_f32_16x16x64_bf16 v[208:211], v[236:239], v[200:207], v212\n\t" \
    "v_add_f32 v248, v248, v250\n\t" \
    "v_add_f32 v249, v249, v251\n\t" \
    "v_cvt_pk_bf16_f32 v248, v248, v249\n\t" \
    "v_smfmac_f32_16x16x64_bf16 v[208:211], v[240:243], v[176:183], v212\n\t" \
    "ds_write_b16 v213, v248 offset:1024\n\t" \
    "ds_write_b16_d16_hi v213, v248 offset:1040\n\t" \
    "s_nop 5\n\t" \
    "v_add_f32 v208, v208, v210\n\t" \
    "v_add_f32 v209, v209, v211\n\t" \
    "v_cvt_pk_bf16_f32 v208, v208, v209\n\t" \
    "ds_write_b16 v213, v208 offset:0\n\t" \
    "ds_write_b16_d16_hi v213, v208 offset:16\n\t" \
    "s_setprio 0\n\t" \
    "s_waitcnt lgkmcnt(0)\n\t" \
    "s_barrier \n\t" \
    "ds_read_b128 v[224:227], v253 offset:4096\n\t" \
    "ds_read_b128 v[244:247], v254 offset:40960\n\t" \
    "ds_read_b128 v[228:231], v253 offset:5120\n\t" \
    "ds_read_b128 v[232:235], v253 offset:24576\n\t" \
    "ds_read_b128 v[236:239], v253 offset:25600\n\t" \
    "ds_read_b128 v[248:251], v254 offset:41984\n\t" \
    "s_add_u32 s40, s40, 1\n\t" \
    "s_cmp_lg_u32 s40, 64\n\t" \
    "s_cbranch_scc1 .Lv17_%=\n\t" \
    "s_branch .Lend%=\n\t" \
    ".Lv0_%=:\n\t" \
    "s_mov_b32 s42, %[xs]\n\t" \
    "v_add_u32 v244, 0xc000, v243\n\t" \
    "s_waitcnt vmcnt(12) lgkmcnt(0)\n\t" \
    "s_barrier \n\t" \
    "s_add_u32 m0, %[wm], 0x9000\n\t" \
    "s_mov_b32 s44, 0x1e0000\n\t" \
    "buffer_load_dwordx4 v240, %[wr], s44 offen offset:0 nt lds\n\t" \
    "buffer_load_dwordx4 v241, %[wr], s44 offen offset:1024 nt lds\n\t" \
    "buffer_load_dwordx4 v242, %[wr], s44 offen offset:2048 nt lds\n\t" \
    "ds_read_b64_tr_b16 v[200:201], v244 offset:0\n\t" \
    "ds_read_b64_tr_b16 v[202:203], v244 offset:1536\n\t" \
    "ds_read_b64_tr_b16 v[0:1], v244 offset:32\n\t" \
    "ds_read_b64_tr_b16 v[2:3], v244 offset:1568\n\t" \
    "ds_read_b64_tr_b16 v[8:9], v244 offset:64\n\t" \
    "ds_read_b64_tr_b16 v[10:11], v244 offset:1600\n\t" \
    "v_add_u32 v244, 0xf000, v243\n\t" \
    "s_waitcnt vmcnt(12) lgkmcnt(0)\n\t" \
    "s_barrier \n\t" \
    "s_add_u32 m0, %[wm], 0xc000\n\t" \
    "s_mov_b32 s44, 0x240000\n\t" \
    "buffer_load_dwordx4 v240, %[wr], s44 offen offset:0 nt lds\n\t" \
    "buffer_load_dwordx4 v241, %[wr], s44 offen offset:1024 nt lds\n\t" \
    "buffer_load_dwordx4 v242, %[wr], s44 offen offset:2048 nt lds\n\t" \
    "ds_read_b64_tr_b16 v[204:205], v244 offset:0\n\t" \
    "ds_read_b64_tr_b16 v[206:207], v244 offset:1536\n\t" \
    "ds_read_b64_tr_b16 v[4:5], v244 offset:32\n\t" \
    "ds_read_b64_tr_b16 v[6:7], v244 offset:1568\n\t" \
    "ds_read_b64_tr_b16 v[12:13], v244 offset:64\n\t" \
    "ds_read_b64_tr_b16 v[14:15], v244 offset:1600\n\t" \
    "v_add_u32 v244, 0x0, v243\n\t" \
    "s_waitcnt vmcnt(12) lgkmcnt(0)\n\t" \
    "s_barrier \n\t" \
    "s_add_u32 m0, %[wm], 0xf000\n\t" \
    "s_mov_b32 s44, 0x2a0000\n\t" \
    "buffer_load_dwordx4 v240, %[wr], s44 offen offset:0 nt lds\n\t" \
    "buffer_load_dwordx4 v241, %[wr], s44 offen offset:1024 nt lds\n\t" \
    "buffer_load_dwordx4 v242, %[wr], s44 offen offset:2048 nt lds\n\t" \
    "ds_read_b64_tr_b16 v[208:209], v244 offset:0\n\t" \
    "ds_read_b64_tr_b16 v[210:211], v244 offset:1536\n\t" \
    "ds_read_b64_tr_b16 v[40:41], v244 offset:32\n\t" \
    "ds_read_b64_tr_b16 v[42:43], v244 offset:1568\n\t" \
    "ds_read_b64_tr_b16 v[48:49], v244 offset:64\n\t" \
    "ds_read_b64_tr_b16 v[50:51], v244 offset:1600\n\t" \
    "v_add_u32 v244, 0x3000, v243\n\t" \
    "s_waitcnt vmcnt(12) lgkmcnt(0)\n\t" \
    "s_barrier \n\t" \
    "s_add_u32 m0, %[wm], 0x0\n\t" \
    "s_mov_b32 s44, 0x300000\n\t" \
    "buffer_load_dwordx4 v240, %[wr], s44 offen offset:0 nt lds\n\t" \
    "buffer_load_dwordx4 v241, %[wr], s44 offen offset:1024 nt lds\n\t" \
    "buffer_load_dwordx4 v242, %[wr], s44 offen offset:2048 nt lds\n\t" \
    "ds_read_b64_tr_b16 v[212:213], v244 offset:0\n\t" \
    "ds_read_b64_tr_b16 v[214:215], v244 offset:1536\n\t" \
    "ds_read_b64_tr_b16 v[44:45], v244 offset:32\n\t" \
    "ds_read_b64_tr_b16 v[46:47], v244 offset:1568\n\t" \
    "ds_read_b64_tr_b16 v[52:53], v244 offset:64\n\t" \
    "ds_read_b64_tr_b16 v[54:55], v244 offset:1600\n\t" \
    "v_add_u32 v244, 0x6000, v243\n\t" \
    "s_waitcnt vmcnt(12) lgkmcnt(0)\n\t" \
    "s_barrier \n\t" \
    "s_add_u32 m0, %[wm], 0x3000\n\t" \
    "s_mov_b32 s44, 0x360000\n\t" \
    "buffer_load_dwordx4 v240, %[wr], s44 offen offset:0 nt lds\n\t" \
    "buffer_load_dwordx4 v241, %[wr], s44 offen offset:1024 nt lds\n\t" \
    "buffer_load_dwordx4 v242, %[wr], s44 offen offset:2048 nt lds\n\t" \
    "ds_read_b64_tr_b16 v[216:217], v244 offset:0\n\t" \
    "ds_read_b64_tr_b16 v[218:219], v244 offset:1536\n\t" \
    "ds_read_b64_tr_b16 v[80:81], v244 offset:32\n\t" \
    "ds_read_b64_tr_b16 v[82:83], v244 offset:1568\n\t" \
    "ds_read_b64_tr_b16 v[88:89], v244 offset:64\n\t" \
    "ds_read_b64_tr_b16 v[90:91], v244 offset:1600\n\t" \
    "v_add_u32 v244, 0x9000, v243\n\t" \
    "s_waitcnt vmcnt(12) lgkmcnt(0)\n\t" \
    "s_barrier \n\t" \
    "s_add_u32 m0, %[wm], 0x6000\n\t" \
    "s_mov_b32 s44, 0x3c0000\n\t" \
    "buffer_load_dwordx4 v240, %[wr], s44 offen offset:0 nt lds\n\t" \
    "buffer_load_dwordx4 v241, %[wr], s44 offen offset:1024 nt lds\n\t" \
    "buffer_load_dwordx4 v242, %[wr], s44 offen offset:2048 nt lds\n\t" \
    "ds_read_b64_tr_b16 v[220:221], v244 offset:0\n\t" \
    "ds_read_b64_tr_b16 v[222:223], v244 offset:1536\n\t" \
    "ds_read_b64_tr_b16 v[84:85], v244 offset:32\n\t" \
    "ds_read_b64_tr_b16 v[86:87], v244 offset:1568\n\t" \
    "ds_read_b64_tr_b16 v[92:93], v244 offset:64\n\t" \
    "ds_read_b64_tr_b16 v[94:95], v244 offset:1600\n\t" \
    "v_add_u32 v244, 0xc000, v243\n\t" \
    "s_waitcnt vmcnt(12) lgkmcnt(0)\n\t" \
    "s_barrier \n\t" \
    "s_add_u32 m0, %[wm], 0x9000\n\t" \
    "s_mov_b32 s44, 0x420000\n\t" \
    "buffer_load_dwordx4 v240, %[wr], s44 offen offset:0 nt lds\n\t" \
    "buffer_load_dwordx4 v241, %[wr], s44 offen offset:1024 nt lds\n\t" \
    "buffer_load_dwordx4 v242, %[wr], s44 offen offset:2048 nt lds\n\t" \
    "ds_read_b64_tr_b16 v[224:225], v244 offset:0\n\t" \
    "ds_read_b64_tr_b16 v[226:227], v244 offset:1536\n\t" \
    "ds_read_b64_tr_b16 v[120:121], v244 offset:32\n\t" \
    "ds_read_b64_tr_b16 v[122:123], v244 offset:1568\n\t" \
    "ds_read_b64_tr_b16 v[128:129], v244 offset:64\n\t" \
    "ds_read_b64_tr_b16 v[130:131], v244 offset:1600\n\t" \
    "v_add_u32 v244, 0xf000, v243\n\t" \
    "s_waitcnt vmcnt(12) lgkmcnt(0)\n\t" \
    "s_barrier \n\t" \
    "s_add_u32 m0, %[wm], 0xc000\n\t" \
    "s_mov_b32 s44, 0x480000\n\t" \
    "buffer_load_dwordx4 v240, %[wr], s44 offen offset:0 nt lds\n\t" \
    "buffer_load_dwordx4 v241, %[wr], s44 offen offset:1024 nt lds\n\t" \
    "buffer_load_dwordx4 v242, %[wr], s44 offen offset:2048 nt lds\n\t" \
    "ds_read_b64_tr_b16 v[228:229], v244 offset:0\n\t" \
    "ds_read_b64_tr_b16 v[230:231], v244 offset:1536\n\t" \
    "ds_read_b64_tr_b16 v[124:125], v244 offset:32\n\t" \
    "ds_read_b64_tr_b16 v[126:127], v244 offset:1568\n\t" \
    "ds_read_b64_tr_b16 v[132:133], v244 offset:64\n\t" \
    "ds_read_b64_tr_b16 v[134:135], v244 offset:1600\n\t" \
    "v_add_u32 v244, 0x0, v243\n\t" \
    "s_waitcnt vmcnt(12) lgkmcnt(0)\n\t" \
    "s_barrier \n\t" \
    "v_mov_b32 v246, 0\n\t" \
    "buffer_load_dword v246, v246, s[60:63], %[mo] offen sc1\n\t" \
    "s_add_u32 m0, %[wm], 0xf000\n\t" \
    "s_mov_b32 s44, 0x4e0000\n\t" \
    "buffer_load_dwordx4 v240, %[wr], s44 offen offset:0 nt lds\n\t" \
    "buffer_load_dwordx4 v241, %[wr], s44 offen offset:1024 nt lds\n\t" \
    "buffer_load_dwordx4 v242, %[wr], s44 offen offset:2048 nt lds\n\t" \
    "ds_read_b64_tr_b16 v[232:233], v244 offset:0\n\t" \
    "ds_read_b64_tr_b16 v[234:235], v244 offset:1536\n\t" \
    "ds_read_b64_tr_b16 v[160:161], v244 offset:32\n\t" \
    "ds_read_b64_tr_b16 v[162:163], v244 offset:1568\n\t" \
    "ds_read_b64_tr_b16 v[168:169], v244 offset:64\n\t" \
    "ds_read_b64_tr_b16 v[170:171], v244 offset:1600\n\t" \
    "v_add_u32 v244, 0x3000, v243\n\t" \
    "s_waitcnt vmcnt(12) lgkmcnt(0)\n\t" \
    "s_barrier \n\t" \
    "s_add_u32 m0, %[wm], 0x0\n\t" \
    "s_mov_b32 s44, 0x540000\n\t" \
    "buffer_load_dwordx4 v240, %[wr], s44 offen offset:0 nt lds\n\t" \
    "buffer_load_dwordx4 v241, %[wr], s44 offen offset:1024 nt lds\n\t" \
    "buffer_load_dwordx4 v242, %[wr], s44 offen offset:2048 nt lds\n\t" \
    "ds_read_b64_tr_b16 v[236:237], v244 offset:0\n\t" \
    "ds_read_b64_tr_b16 v[238:239], v244 offset:1536\n\t" \
    "ds_read_b64_tr_b16 v[164:165], v244 offset:32\n\t" \
    "ds_read_b64_tr_b16 v[166:167], v244 offset:1568\n\t" \
    "ds_read_b64_tr_b16 v[172:173], v244 offset:64\n\t" \
    "ds_read_b64_tr_b16 v[174:175], v244 offset:1600\n\t" \
    "v_add_u32 v244, 0x6000, v243\n\t" \
    "s_waitcnt vmcnt(12) lgkmcnt(0)\n\t" \
    "s_barrier \n\t" \
    "s_add_u32 m0, %[wm], 0x3000\n\t" \
    "s_mov_b32 s44, 0x5a0000\n\t" \
    "buffer_load_dwordx4 v240, %[wr], s44 offen offset:0 nt lds\n\t" \
    "buffer_load_dwordx4 v241, %[wr], s44 offen offset:1024 nt lds\n\t" \
    "buffer_load_dwordx4 v242, %[wr], s44 offen offset:2048 nt lds\n\t" \
    "ds_read_b64_tr_b16 v[16:17], v244 offset:0\n\t" \
    "ds_read_b64_tr_b16 v[18:19], v244 offset:1536\n\t" \
    "ds_read_b64_tr_b16 v[24:25], v244 offset:32\n\t" \
    "ds_read_b64_tr_b16 v[26:27], v244 offset:1568\n\t" \
    "ds_read_b64_tr_b16 v[32:33], v244 offset:64\n\t" \
    "ds_read_b64_tr_b16 v[34:35], v244 offset:1600\n\t" \
    "v_add_u32 v244, 0x9000, v243\n\t" \
    "s_waitcnt vmcnt(12) lgkmcnt(0)\n\t" \
    "s_barrier \n\t" \
    "s_add_u32 m0, %[wm], 0x6000\n\t" \
    "s_mov_b32 s44, 0x600000\n\t" \
    "buffer_load_dwordx4 v240, %[wr], s44 offen offset:0 nt lds\n\t" \
    "buffer_load_dwordx4 v241, %[wr], s44 offen offset:1024 nt lds\n\t" \
    "buffer_load_dwordx4 v242, %[wr], s44 offen offset:2048 nt lds\n\t" \
    "ds_read_b64_tr_b16 v[20:21], v244 offset:0\n\t" \
    "ds_read_b64_tr_b16 v[22:23], v244 offset:1536\n\t" \
    "ds_read_b64_tr_b16 v[28:29], v244 offset:32\n\t" \
    "ds_read_b64_tr_b16 v[30:31], v244 offset:1568\n\t" \
    "ds_read_b64_tr_b16 v[36:37], v244 offset:64\n\t" \
    "ds_read_b64_tr_b16 v[38:39], v244 offset:1600\n\t" \
    "v_add_u32 v244, 0xc000, v243\n\t" \
    "s_waitcnt vmcnt(12) lgkmcnt(0)\n\t" \
    "s_barrier \n\t" \
    "s_add_u32 m0, %[wm], 0x9000\n\t" \
    "s_mov_b32 s44, 0x660000\n\t" \
    "buffer_load_dwordx4 v240, %[wr], s44 offen offset:0 nt lds\n\t" \
    "buffer_load_dwordx4 v241, %[wr], s44 offen offset:1024 nt lds\n\t" \
    "buffer_load_dwordx4 v242, %[wr], s44 offen offset:2048 nt lds\n\t" \
    "ds_read_b64_tr_b16 v[56:57], v244 offset:0\n\t" \
    "ds_read_b64_tr_b16 v[58:59], v244 offset:1536\n\t" \
    "ds_read_b64_tr_b16 v[64:65], v244 offset:32\n\t" \
    "ds_read_b64_tr_b16 v[66:67], v244 offset:1568\n\t" \
    "ds_read_b64_tr_b16 v[72:73], v244 offset:64\n\t" \
    "ds_read_b64_tr_b16 v[74:75], v244 offset:1600\n\t" \
    "v_add_u32 v244, 0xf000, v243\n\t" \
    "s_waitcnt vmcnt(12) lgkmcnt(0)\n\t" \
    "s_barrier \n\t" \
    ".Lv01_%=:\n\t" \
    "v_readfirstlane_b32 s43, v246\n\t" \
    "s_nop 0\n\t" \
    "s_cmp_lg_u32 s43, -1\n\t" \
    "s_cbranch_scc1 .Lv02_%=\n\t" \
    "s_cmp_eq_u32 s41, 0\n\t" \
    "s_cbranch_scc1 .Lv02_%=\n\t" \
    "s_sub_u32 s41, s41, 1\n\t" \
    "s_sleep 4\n\t" \
    "v_mov_b32 v246, 0\n\t" \
    "buffer_load_dword v246, v246, s[60:63], %[mo] offen sc1\n\t" \
    "s_waitcnt vmcnt(0)\n\t" \
    "s_branch .Lv01_%=\n\t" \
    ".Lv02_%=:\n\t" \
    "v_mov_b32 v246, 0x8000\n\t" \
    "s_mov_b64 exec, 1\n\t" \
    "buffer_store_dword v246, v246, s[60:63], %[mo] offen sc1\n\t" \
    "s_mov_b64 exec, -1\n\t" \
    "s_and_b32 s44, s43, 0x5000\n\t" \
    "s_xor_b32 %[mb], %[mb], s44\n\t" \
    "s_xor_b32 %[fl], %[fl], s44\n\t" \
    "s_and_b32 s43, s43, 0x2000\n\t" \
    "s_xor_b32 s43, s43, %[lx]\n\t" \
    "s_add_u32 s60, s60, s43\n\t" \
    "s_addc_u32 s61, s61, 0\n\t" \
    "v_and_b32 v252, 0xf0, v253\n\t" \
    "v_lshlrev_b32 v252, 3, v252\n\t" \
    "buffer_load_dword v252, v252, s[60:63], %[fl] offen sc1\n\t" \
    "s_add_u32 m0, %[wm], 0xc000\n\t" \
    "s_mov_b32 s44, 0x6c0000\n\t" \
    "buffer_load_dwordx4 v240, %[wr], s44 offen offset:0 nt lds\n\t" \
    "buffer_load_dwordx4 v241, %[wr], s44 offen offset:1024 nt lds\n\t" \
    "buffer_load_dwordx4 v242, %[wr], s44 offen offset:2048 nt lds\n\t" \
    "ds_read_b64_tr_b16 v[60:61], v244 offset:0\n\t" \
    "ds_read_b64_tr_b16 v[62:63], v244 offset:1536\n\t" \
    "ds_read_b64_tr_b16 v[68:69], v244 offset:32\n\t" \
    "ds_read_b64_tr_b16 v[70:71], v244 offset:1568\n\t" \
    "ds_read_b64_tr_b16 v[76:77], v244 offset:64\n\t" \
    "ds_read_b64_tr_b16 v[78:79], v244 offset:1600\n\t" \
    "v_add_u32 v244, 0x0, v243\n\t" \
    "s_waitcnt vmcnt(12) lgkmcnt(0)\n\t" \
    "s_barrier \n\t" \
    "s_add_u32 m0, %[wm], 0xf000\n\t" \
    "s_mov_b32 s44, 0x720000\n\t" \
    "buffer_load_dwordx4 v240, %[wr], s44 offen offset:0 nt lds\n\t" \
    "buffer_load_dwordx4 v241, %[wr], s44 offen offset:1024 nt lds\n\t" \
    "buffer_load_dwordx4 v242, %[wr], s44 offen offset:2048 nt lds\n\t" \
    "ds_read_b64_tr_b16 v[96:97], v244 offset:0\n\t" \
    "ds_read_b64_tr_b16 v[98:99], v244 offset:1536\n\t" \
    "ds_read_b64_tr_b16 v[104:105], v244 offset:32\n\t" \
    "ds_read_b64_tr_b16 v[106:107], v244 offset:1568\n\t" \
    "ds_read_b64_tr_b16 v[112:113], v244 offset:64\n\t" \
    "ds_read_b64_tr_b16 v[114:115], v244 offset:1600\n\t" \
    "v_add_u32 v244, 0x3000, v243\n\t" \
    "s_waitcnt vmcnt(12) lgkmcnt(0)\n\t" \
    "s_barrier \n\t" \
    "ds_read_b64_tr_b16 v[100:101], v244 offset:0\n\t" \
    "ds_read_b64_tr_b16 v[102:103], v244 offset:1536\n\t" \
    "ds_read_b64_tr_b16 v[108:109], v244 offset:32\n\t" \
    "ds_read_b64_tr_b16 v[110:111], v244 offset:1568\n\t" \
    "ds_read_b64_tr_b16 v[116:117], v244 offset:64\n\t" \
    "ds_read_b64_tr_b16 v[118:119], v244 offset:1600\n\t" \
    "v_add_u32 v244, 0x6000, v243\n\t" \
    "s_waitcnt vmcnt(9) lgkmcnt(0)\n\t" \
    "s_barrier \n\t" \
    "ds_read_b64_tr_b16 v[136:137], v244 offset:0\n\t" \
    "ds_read_b64_tr_b16 v[138:139], v244 offset:1536\n\t" \
    "ds_read_b64_tr_b16 v[144:145], v244 offset:32\n\t" \
    "ds_read_b64_tr_b16 v[146:147], v244 offset:1568\n\t" \
    "ds_read_b64_tr_b16 v[152:153], v244 offset:64\n\t" \
    "ds_read_b64_tr_b16 v[154:155], v244 offset:1600\n\t" \
    "v_add_u32 v244, 0x9000, v243\n\t" \
    "s_waitcnt vmcnt(6) lgkmcnt(0)\n\t" \
    "s_barrier \n\t" \
    "ds_read_b64_tr_b16 v[140:141], v244 offset:0\n\t" \
    "ds_read_b64_tr_b16 v[142:143], v244 offset:1536\n\t" \
    "ds_read_b64_tr_b16 v[148:149], v244 offset:32\n\t" \
    "ds_read_b64_tr_b16 v[150:151], v244 offset:1568\n\t" \
    "ds_read_b64_tr_b16 v[156:157], v244 offset:64\n\t" \
    "ds_read_b64_tr_b16 v[158:159], v244 offset:1600\n\t" \
    "v_add_u32 v244, 0xc000, v243\n\t" \
    "s_waitcnt vmcnt(3) lgkmcnt(0)\n\t" \
    "s_barrier \n\t" \
    "ds_read_b64_tr_b16 v[176:177], v244 offset:0\n\t" \
    "ds_read_b64_tr_b16 v[178:179], v244 offset:1536\n\t" \
    "ds_read_b64_tr_b16 v[184:185], v244 offset:32\n\t" \
    "ds_read_b64_tr_b16 v[186:187], v244 offset:1568\n\t" \
    "ds_read_b64_tr_b16 v[192:193], v244 offset:64\n\t" \
    "ds_read_b64_tr_b16 v[194:195], v244 offset:1600\n\t" \
    "s_mov_b32 s43, 0\n\t" \
    "v_cmp_eq_u32 vcc, 0, v252\n\t" \
    "s_cbranch_vccnz .Lv03_%=\n\t" \
    "s_mov_b32 m0, 0x0\n\t" \
    "s_add_u32 s49, s42, 0x0\n\t" \
    "buffer_load_dwordx4 v253, s[60:63], s49 offen offset:0 sc1 lds\n\t" \
    "buffer_load_dwordx4 v253, s[60:63], s49 offen offset:1024 sc1 lds\n\t" \
    "buffer_load_dwordx4 v253, s[60:63], s49 offen offset:2048 sc1 lds\n\t" \
    "buffer_load_dwordx4 v253, s[60:63], s49 offen offset:3072 sc1 lds\n\t" \
    "s_mov_b32 m0, 0x4000\n\t" \
    "s_add_u32 s49, s42, 0x10000\n\t" \
    "buffer_load_dwordx4 v253, s[60:63], s49 offen offset:0 sc1 lds\n\t" \
    "s_mov_b32 m0, 0x5000\n\t" \
    "s_add_u32 s49, s42, 0x1000\n\t" \
    "buffer_load_dwordx4 v253, s[60:63], s49 offen offset:0 sc1 lds\n\t" \
    "buffer_load_dwordx4 v253, s[60:63], s49 offen offset:1024 sc1 lds\n\t" \
    "buffer_load_dwordx4 v253, s[60:63], s49 offen offset:2048 sc1 lds\n\t" \
    "buffer_load_dwordx4 v253, s[60:63], s49 offen offset:3072 sc1 lds\n\t" \
    "s_mov_b32 m0, 0x9000\n\t" \
    "s_add_u32 s49, s42, 0x11000\n\t" \
    "buffer_load_dwordx4 v253, s[60:63], s49 offen offset:0 sc1 lds\n\t" \
    "s_mov_b32 s43, 1\n\t" \
    ".Lv03_%=:\n\t" \
    "v_add_u32 v244, 0xf000, v243\n\t" \
    "s_waitcnt vmcnt(0) lgkmcnt(0)\n\t" \
    "s_barrier \n\t" \
    "ds_read_b64_tr_b16 v[180:181], v244 offset:0\n\t" \
    "ds_read_b64_tr_b16 v[182:183], v244 offset:1536\n\t" \
    "ds_read_b64_tr_b16 v[188:189], v244 offset:32\n\t" \
    "ds_read_b64_tr_b16 v[190:191], v244 offset:1568\n\t" \
    "ds_read_b64_tr_b16 v[196:197], v244 offset:64\n\t" \
    "ds_read_b64_tr_b16 v[198:199], v244 offset:1600\n\t" \
    "s_waitcnt lgkmcnt(0)\n\t" \
    "s_barrier \n\t" \
    "ds_write_b128 v254, v[200:203] offset:30720\n\t" \
    "ds_write_b128 v254, v[204:207] offset:31744\n\t" \
    "ds_write_b128 v254, v[208:211] offset:32768\n\t" \
    "ds_write_b128 v254, v[212:215] offset:33792\n\t" \
    "ds_write_b128 v254, v[216:219] offset:34816\n\t" \
    "ds_write_b128 v254, v[220:223] offset:35840\n\t" \
    "ds_write_b128 v254, v[224:227] offset:36864\n\t" \
    "ds_write_b128 v254, v[228:231] offset:37888\n\t" \
    "ds_write_b128 v254, v[232:235] offset:38912\n\t" \
    "ds_write_b128 v254, v[236:239] offset:39936\n\t" \
    "s_cmp_lg_u32 s43, 0\n\t" \
    "s_cbranch_scc1 .Lv04_%=\n\t" \
    "s_mov_b32 s44, 0\n\t" \
    ".Lv05_%=:\n\t" \
    "v_cmp_ge_u32 vcc, s44, v252\n\t" \
    "s_cbranch_vccz .Lv06_%=\n\t" \
    "s_cmp_eq_u32 s41, 0\n\t" \
    "s_cbranch_scc1 .Lv06_%=\n\t" \
    "s_sub_u32 s41, s41, 1\n\t" \
    "s_sleep 8\n\t" \
    "v_and_b32 v252, 0xf0, v253\n\t" \
    "v_lshlrev_b32 v252, 3, v252\n\t" \
    "buffer_load_dword v252, v252, s[60:63], %[fl] offen sc1\n\t" \
    "s_waitcnt vmcnt(0)\n\t" \
    "s_branch .Lv05_%=\n\t" \
    ".Lv06_%=:\n\t" \
    "s_mov_b32 m0, 0x0\n\t" \
    "s_add_u32 s49, s42, 0x0\n\t" \
    "buffer_load_dwordx4 v253, s[60:63], s49 offen offset:0 sc1 lds\n\t" \
    "buffer_load_dwordx4 v253, s[60:63], s49 offen offset:1024 sc1 lds\n\t" \
    "buffer_load_dwordx4 v253, s[60:63], s49 offen offset:2048 sc1 lds\n\t" \
    "buffer_load_dwordx4 v253, s[60:63], s49 offen offset:3072 sc1 lds\n\t" \
    "s_mov_b32 m0, 0x4000\n\t" \
    "s_add_u32 s49, s42, 0x10000\n\t" \
    "buffer_load_dwordx4 v253, s[60:63], s49 offen offset:0 sc1 lds\n\t" \
    "s_mov_b32 m0, 0x5000\n\t" \
    "s_add_u32 s49, s42, 0x1000\n\t" \
    "buffer_load_dwordx4 v253, s[60:63], s49 offen offset:0 sc1 lds\n\t" \
    "buffer_load_dwordx4 v253, s[60:63], s49 offen offset:1024 sc1 lds\n\t" \
    "buffer_load_dwordx4 v253, s[60:63], s49 offen offset:2048 sc1 lds\n\t" \
    "buffer_load_dwordx4 v253, s[60:63], s49 offen offset:3072 sc1 lds\n\t" \
    "s_mov_b32 m0, 0x9000\n\t" \
    "s_add_u32 s49, s42, 0x11000\n\t" \
    "buffer_load_dwordx4 v253, s[60:63], s49 offen offset:0 sc1 lds\n\t" \
    ".Lv04_%=:\n\t" \
    "s_waitcnt vmcnt(0) lgkmcnt(0)\n\t" \
    "s_barrier \n\t" \
    "ds_read_b128 v[224:227], v253 offset:0\n\t" \
    "ds_read_b128 v[244:247], v254 offset:30720\n\t" \
    "ds_read_b128 v[228:231], v253 offset:1024\n\t" \
    "ds_read_b128 v[232:235], v253 offset:20480\n\t" \
    "ds_read_b128 v[236:239], v253 offset:21504\n\t" \
    "ds_read_b128 v[248:251], v254 offset:31744\n\t" \
    ".Lv07_%=:\n\t" \
    "s_waitcnt lgkmcnt(5)\n\t" \
    "v_mfma_f32_16x16x32_bf16 v[204:207], v[0:3], v[224:227], 0\n\t" \
    "s_waitcnt lgkmcnt(3)\n\t" \
    "v_mfma_f32_16x16x32_bf16 v[204:207], v[4:7], v[228:231], v[204:207]\n\t" \
    "ds_read_b128 v[240:243], v253 offset:10240\n\t" \
    "v_mfma_f32_16x16x32_bf16 v[208:211], v[8:11], v[224:227], 0\n\t" \
    "v_mfma_f32_16x16x32_bf16 v[208:211], v[12:15], v[228:231], v[208:211]\n\t" \
    "v_mfma_f32_16x16x32_bf16 v[200:203], v[244:247], v[224:227], 0\n\t" \
    "s_waitcnt lgkmcnt(1)\n\t" \
    "v_mfma_f32_16x16x32_bf16 v[200:203], v[248:251], v[228:231], v[200:203]\n\t" \
    "v_mfma_f32_16x16x32_bf16 v[216:219], v[0:3], v[232:235], 0\n\t" \
    "ds_read_b128 v[224:227], v253 offset:11264\n\t" \
    "v_mfma_f32_16x16x32_bf16 v[216:219], v[4:7], v[236:239], v[216:219]\n\t" \
    "ds_read_b128 v[228:231], v253 offset:2048\n\t" \
    "v_mfma_f32_16x16x32_bf16 v[220:223], v[8:11], v[232:235], 0\n\t" \
    "v_mfma_f32_16x16x32_bf16 v[220:223], v[12:15], v[236:239], v[220:223]\n\t" \
    "v_mfma_f32_16x16x32_bf16 v[212:215], v[244:247], v[232:235], 0\n\t" \
    "ds_read_b128 v[244:247], v254 offset:32768\n\t" \
    "v_mfma_f32_16x16x32_bf16 v[212:215], v[248:251], v[236:239], v[212:215]\n\t" \
    "ds_read_b128 v[248:251], v254 offset:33792\n\t" \
    "s_waitcnt lgkmcnt(4)\n\t" \
    "v_mfma_f32_16x16x32_bf16 v[204:207], v[24:27], v[240:243], v[204:207]\n\t" \
    "ds_read_b128 v[232:235], v253 offset:3072\n\t" \
    "s_waitcnt lgkmcnt(4)\n\t" \
    "v_mfma_f32_16x16x32_bf16 v[204:207], v[28:31], v[224:227], v[204:207]\n\t" \
    "ds_read_b128 v[236:239], v253 offset:22528\n\t" \
    "v_mfma_f32_16x16x32_bf16 v[208:211], v[32:35], v[240:243], v[208:211]\n\t" \
    "v_mfma_f32_16x16x32_bf16 v[208:211], v[36:39], v[224:227], v[208:211]\n\t" \
    "v_mfma_f32_16x16x32_bf16 v[200:203], v[16:19], v[240:243], v[200:203]\n\t" \
    "v_mfma_f32_16x16x32_bf16 v[200:203], v[20:23], v[224:227], v[200:203]\n\t" \
    "s_waitcnt lgkmcnt(4)\n\t" \
    "v_mfma_f32_16x16x32_bf16 v[204:207], v[40:43], v[228:231], v[204:207]\n\t" \
    "ds_read_b128 v[240:243], v253 offset:23552\n\t" \
    "s_waitcnt lgkmcnt(2)\n\t" \
    "v_mfma_f32_16x16x32_bf16 v[204:207], v[44:47], v[232:235], v[204:207]\n\t" \
    "ds_read_b128 v[224:227], v253 offset:12288\n\t" \
    "v_mfma_f32_16x16x32_bf16 v[208:211], v[48:51], v[228:231], v[208:211]\n\t" \
    "v_mfma_f32_16x16x32_bf16 v[208:211], v[52:55], v[232:235], v[208:211]\n\t" \
    "v_mfma_f32_16x16x32_bf16 v[200:203], v[244:247], v[228:231], v[200:203]\n\t" \
    "v_mfma_f32_16x16x32_bf16 v[200:203], v[248:251], v[232:235], v[200:203]\n\t" \
    "s_waitcnt lgkmcnt(2)\n\t" \
    "v_mfma_f32_16x16x32_bf16 v[216:219], v[40:43], v[236:239], v[216:219]\n\t" \
    "ds_read_b128 v[228:231], v253 offset:13312\n\t" \
    "s_add_u32 s44, s40, 1\n\t" \
    "s_waitcnt lgkmcnt(2)\n\t" \
    "v_mfma_f32_16x16x32_bf16 v[216:219], v[44:47], v[240:243], v[216:219]\n\t" \
    "ds_read_b128 v[232:235], v253 offset:4096\n\t" \
    "s_and_b32 s45, s44, 15\n\t" \
    "v_mfma_f32_16x16x32_bf16 v[220:223], v[48:51], v[236:239], v[220:223]\n\t" \
    "s_cmp_eq_u32 s45, 0\n\t" \
    "v_mfma_f32_16x16x32_bf16 v[220:223], v[52:55], v[240:243], v[220:223]\n\t" \
    "s_cselect_b32 s45, %[cj], %[cs]\n\t" \
    "v_mfma_f32_16x16x32_bf16 v[212:215], v[244:247], v[236:239], v[212:215]\n\t" \
    "ds_read_b128 v[244:247], v254 offset:34816\n\t" \
    "s_cmp_eq_u32 s44, 64\n\t" \
    "v_mfma_f32_16x16x32_bf16 v[212:215], v[248:251], v[240:243], v[212:215]\n\t" \
    "ds_read_b128 v[248:251], v254 offset:35840\n\t" \
    "s_cselect_b32 s45, 0, s45\n\t" \
    "s_waitcnt lgkmcnt(4)\n\t" \
    "v_mfma_f32_16x16x32_bf16 v[204:207], v[64:67], v[224:227], v[204:207]\n\t" \
    "ds_read_b128 v[236:239], v253 offset:5120\n\t" \
    "s_add_u32 s42, s42, s45\n\t" \
    "s_waitcnt lgkmcnt(4)\n\t" \
    "v_mfma_f32_16x16x32_bf16 v[204:207], v[68:71], v[228:231], v[204:207]\n\t" \
    "ds_read_b128 v[240:243], v253 offset:24576\n\t" \
    "v_mfma_f32_16x16x32_bf16 v[208:211], v[72:75], v[224:227], v[208:211]\n\t" \
    "v_mfma_f32_16x16x32_bf16 v[208:211], v[76:79], v[228:231], v[208:211]\n\t" \
    "v_mfma_f32_16x16x32_bf16 v[200:203], v[56:59], v[224:227], v[200:203]\n\t" \
    "v_mfma_f32_16x16x32_bf16 v[200:203], v[60:63], v[228:231], v[200:203]\n\t" \
    "s_waitcnt lgkmcnt(4)\n\t" \
    "v_mfma_f32_16x16x32_bf16 v[204:207], v[80:83], v[232:235], v[204:207]\n\t" \
    "ds_read_b128 v[224:227], v253 offset:25600\n\t" \
    "s_waitcnt lgkmcnt(2)\n\t" \
    "v_mfma_f32_16x16x32_bf16 v[204:207], v[84:87], v[236:239], v[204:207]\n\t" \
    "ds_read_b128 v[228:231], v253 offset:14336\n\t" \
    "v_mfma_f32_16x16x32_bf16 v[208:211], v[88:91], v[232:235], v[208:211]\n\t" \
    "v_mfma_f32_16x16x32_bf16 v[208:211], v[92:95], v[236:239], v[208:211]\n\t" \
    "v_mfma_f32_16x16x32_bf16 v[200:203], v[244:247], v[232:235], v[200:203]\n\t" \
    "v_mfma_f32_16x16x32_bf16 v[200:203], v[248:251], v[236:239], v[200:203]\n\t" \
    "s_waitcnt lgkmcnt(2)\n\t" \
    "v_mfma_f32_16x16x32_bf16 v[216:219], v[80:83], v[240:243], v[216:219]\n\t" \
    "ds_read_b128 v[232:235], v253 offset:15360\n\t" \
    "s_and_b32 s44, s40, 3\n\t" \
    "s_waitcnt lgkmcnt(2)\n\t" \
    "v_mfma_f32_16x16x32_bf16 v[216:219], v[84:87], v[224:227], v[216:219]\n\t" \
    "ds_read_b128 v[236:239], v253 offset:6144\n\t" \
    "s_lshl_b32 s44, s44, 22\n\t" \
    "v_mfma_f32_16x16x32_bf16 v[220:223], v[88:91], v[240:243], v[220:223]\n\t" \
    "s_add_u32 s44, s44, %[mb]\n\t" \
    "v_mfma_f32_16x16x32_bf16 v[220:223], v[92:95], v[224:227], v[220:223]\n\t" \
    "s_add_u32 s46, s44, 0x0\n\t" \
    "v_mfma_f32_16x16x32_bf16 v[212:215], v[244:247], v[240:243], v[212:215]\n\t" \
    "ds_read_b128 v[244:247], v254 offset:36864\n\t" \
    "s_add_u32 s47, s44, 0x8400\n\t" \
    "v_mfma_f32_16x16x32_bf16 v[212:215], v[248:251], v[224:227], v[212:215]\n\t" \
    "ds_read_b128 v[248:251], v254 offset:37888\n\t" \
    "s_add_u32 s48, s44, 0x400\n\t" \
    "s_waitcnt lgkmcnt(4)\n\t" \
    "v_mfma_f32_16x16x32_bf16 v[204:207], v[104:107], v[228:231], v[204:207]\n\t" \
    "ds_read_b128 v[240:243], v253 offset:7168\n\t" \
    "s_waitcnt lgkmcnt(4)\n\t" \
    "v_mfma_f32_16x16x32_bf16 v[204:207], v[108:111], v[232:235], v[204:207]\n\t" \
    "ds_read_b128 v[224:227], v253 offset:26624\n\t" \
    "v_mfma_f32_16x16x32_bf16 v[208:211], v[112:115], v[228:231], v[208:211]\n\t" \
    "v_mfma_f32_16x16x32_bf16 v[208:211], v[116:119], v[232:235], v[208:211]\n\t" \
    "v_mfma_f32_16x16x32_bf16 v[200:203], v[96:99], v[228:231], v[200:203]\n\t" \
    "v_mfma_f32_16x16x32_bf16 v[200:203], v[100:103], v[232:235], v[200:203]\n\t" \
    "s_waitcnt lgkmcnt(4)\n\t" \
    "v_mfma_f32_16x16x32_bf16 v[204:207], v[120:123], v[236:239], v[204:207]\n\t" \
    "ds_read_b128 v[228:231], v253 offset:27648\n\t" \
    "s_waitcnt lgkmcnt(2)\n\t" \
    "v_mfma_f32_16x16x32_bf16 v[204:207], v[124:127], v[240:243], v[204:207]\n\t" \
    "ds_read_b128 v[232:235], v253 offset:16384\n\t" \
    "v_mfma_f32_16x16x32_bf16 v[208:211], v[128:131], v[236:239], v[208:211]\n\t" \
    "v_mfma_f32_16x16x32_bf16 v[208:211], v[132:135], v[240:243], v[208:211]\n\t" \
    "v_mfma_f32_16x16x32_bf16 v[200:203], v[244:247], v[236:239], v[200:203]\n\t" \
    "v_mfma_f32_16x16x32_bf16 v[200:203], v[248:251], v[240:243], v[200:203]\n\t" \
    "s_waitcnt lgkmcnt(2)\n\t" \
    "v_mfma_f32_16x16x32_bf16 v[216:219], v[120:123], v[224:227], v[216:219]\n\t" \
    "ds_read_b128 v[236:239], v253 offset:17408\n\t" \
    "s_waitcnt lgkmcnt(2)\n\t" \
    "v_mfma_f32_16x16x32_bf16 v[216:219], v[124:127], v[228:231], v[216:219]\n\t" \
    "ds_read_b128 v[240:243], v253 offset:8192\n\t" \
    "v_mfma_f32_16x16x32_bf16 v[220:223], v[128:131], v[224:227], v[220:223]\n\t" \
    "v_mfma_f32_16x16x32_bf16 v[220:223], v[132:135], v[228:231], v[220:223]\n\t" \
    "v_mfma_f32_16x16x32_bf16 v[212:215], v[244:247], v[224:227], v[212:215]\n\t" \
    "ds_read_b128 v[244:247], v254 offset:38912\n\t" \
    "v_mfma_f32_16x16x32_bf16 v[212:215], v[248:251], v[228:231], v[212:215]\n\t" \
    "ds_read_b128 v[248:251], v254 offset:39936\n\t" \
    "v_and_b32 v252, 0xf0, v253\n\t" \
    "v_lshlrev_b32 v252, 3, v252\n\t" \
    "buffer_load_dword v252, v252, s[60:63], %[fl] offen sc1\n\t" \
    "s_waitcnt lgkmcnt(4)\n\t" \
    "v_mfma_f32_16x16x32_bf16 v[204:207], v[144:147], v[232:235], v[204:207]\n\t" \
    "ds_read_b128 v[224:227], v253 offset:9216\n\t" \
    "s_waitcnt lgkmcnt(4)\n\t" \
    "v_mfma_f32_16x16x32_bf16 v[204:207], v[148:151], v[236:239], v[204:207]\n\t" \
    "ds_read_b128 v[228:231], v253 offset:28672\n\t" \
    "v_mfma_f32_16x16x32_bf16 v[208:211], v[152:155], v[232:235], v[208:211]\n\t" \
    "v_mfma_f32_16x16x32_bf16 v[208:211], v[156:159], v[236:239], v[208:211]\n\t" \
    "v_mfma_f32_16x16x32_bf16 v[200:203], v[136:139], v[232:235], v[200:203]\n\t" \
    "v_mfma_f32_16x16x32_bf16 v[200:203], v[140:143], v[236:239], v[200:203]\n\t" \
    "s_waitcnt lgkmcnt(4)\n\t" \
    "v_mfma_f32_16x16x32_bf16 v[204:207], v[160:163], v[240:243], v[204:207]\n\t" \
    "ds_read_b128 v[232:235], v253 offset:29696\n\t" \
    "s_waitcnt lgkmcnt(2)\n\t" \
    "v_mfma_f32_16x16x32_bf16 v[204:207], v[164:167], v[224:227], v[204:207]\n\t" \
    "ds_read_b128 v[236:239], v253 offset:18432\n\t" \
    "v_mfma_f32_16x16x32_bf16 v[208:211], v[168:171], v[240:243], v[208:211]\n\t" \
    "v_mfma_f32_16x16x32_bf16 v[208:211], v[172:175], v[224:227], v[208:211]\n\t" \
    "v_mfma_f32_16x16x32_bf16 v[200:203], v[244:247], v[240:243], v[200:203]\n\t" \
    "v_mfma_f32_16x16x32_bf16 v[200:203], v[248:251], v[224:227], v[200:203]\n\t" \
    "s_waitcnt lgkmcnt(2)\n\t" \
    "v_mfma_f32_16x16x32_bf16 v[216:219], v[160:163], v[228:231], v[216:219]\n\t" \
    "ds_read_b128 v[240:243], v253 offset:19456\n\t" \
    "s_waitcnt lgkmcnt(2)\n\t" \
    "v_mfma_f32_16x16x32_bf16 v[216:219], v[164:167], v[232:235], v[216:219]\n\t" \
    "ds_read_b128 v[224:227], v253 offset:30720\n\t" \
    "v_mfma_f32_16x16x32_bf16 v[220:223], v[168:171], v[228:231], v[220:223]\n\t" \
    "v_mfma_f32_16x16x32_bf16 v[220:223], v[172:175], v[232:235], v[220:223]\n\t" \
    "v_mfma_f32_16x16x32_bf16 v[212:215], v[244:247], v[228:231], v[212:215]\n\t" \
    "v_mfma_f32_16x16x32_bf16 v[212:215], v[248:251], v[232:235], v[212:215]\n\t" \
    "s_waitcnt lgkmcnt(2)\n\t" \
    "v_mfma_f32_16x16x32_bf16 v[204:207], v[184:187], v[236:239], v[204:207]\n\t" \
    "ds_read_b128 v[228:231], v253 offset:31744\n\t" \
    "s_waitcnt lgkmcnt(2)\n\t" \
    "v_mfma_f32_16x16x32_bf16 v[204:207], v[188:191], v[240:243], v[204:207]\n\t" \
    "ds_read_b128 v[232:235], v253 offset:32768\n\t" \
    "v_mfma_f32_16x16x32_bf16 v[208:211], v[192:195], v[236:239], v[208:211]\n\t" \
    "v_mfma_f32_16x16x32_bf16 v[208:211], v[196:199], v[240:243], v[208:211]\n\t" \
    "v_mfma_f32_16x16x32_bf16 v[200:203], v[176:179], v[236:239], v[200:203]\n\t" \
    "v_mfma_f32_16x16x32_bf16 v[200:203], v[180:183], v[240:243], v[200:203]\n\t" \
    "s_waitcnt lgkmcnt(2)\n\t" \
    "v_mfma_f32_16x16x32_bf16 v[216:219], v[24:27], v[224:227], v[216:219]\n\t" \
    "ds_read_b128 v[236:239], v253 offset:33792\n\t" \
    "s_waitcnt lgkmcnt(2)\n\t" \
    "v_mfma_f32_16x16x32_bf16 v[216:219], v[28:31], v[228:231], v[216:219]\n\t" \
    "ds_read_b128 v[240:243], v253 offset:34816\n\t" \
    "v_mfma_f32_16x16x32_bf16 v[220:223], v[32:35], v[224:227], v[220:223]\n\t" \
    "v_cvt_pk_bf16_f32 v204, v204, v205\n\t" \
    "v_mfma_f32_16x16x32_bf16 v[220:223], v[36:39], v[228:231], v[220:223]\n\t" \
    "v_cvt_pk_bf16_f32 v205, v206, v207\n\t" \
    "v_mfma_f32_16x16x32_bf16 v[212:215], v[16:19], v[224:227], v[212:215]\n\t" \
    "ds_write_b64 v254, v[204:205] offset:7168\n\t" \
    "v_mfma_f32_16x16x32_bf16 v[212:215], v[20:23], v[228:231], v[212:215]\n\t" \
    "v_cvt_pk_bf16_f32 v208, v208, v209\n\t" \
    "s_waitcnt lgkmcnt(3)\n\t" \
    "v_mfma_f32_16x16x32_bf16 v[216:219], v[64:67], v[232:235], v[216:219]\n\t" \
    "ds_read_b128 v[224:227], v253 offset:35840\n\t" \
    "v_cvt_pk_bf16_f32 v209, v210, v211\n\t" \
    "s_waitcnt lgkmcnt(3)\n\t" \
    "v_mfma_f32_16x16x32_bf16 v[216:219], v[68:71], v[236:239], v[216:219]\n\t" \
    "ds_read_b128 v[228:231], v253 offset:36864\n\t" \
    "ds_write_b64 v254, v[208:209] offset:8192\n\t" \
    "v_mfma_f32_16x16x32_bf16 v[220:223], v[72:75], v[232:235], v[220:223]\n\t" \
    "v_cvt_pk_bf16_f32 v200, v200, v201\n\t" \
    "v_mfma_f32_16x16x32_bf16 v[220:223], v[76:79], v[236:239], v[220:223]\n\t" \
    "v_cvt_pk_bf16_f32 v201, v202, v203\n\t" \
    "v_mfma_f32_16x16x32_bf16 v[212:215], v[56:59], v[232:235], v[212:215]\n\t" \
    "ds_write_b64 v254, v[200:201] offset:6144\n\t" \
    "v_mfma_f32_16x16x32_bf16 v[212:215], v[60:63], v[236:239], v[212:215]\n\t" \
    "s_waitcnt lgkmcnt(5)\n\t" \
    "v_mfma_f32_16x16x32_bf16 v[216:219], v[104:107], v[240:243], v[216:219]\n\t" \
    "ds_read_b128 v[232:235], v253 offset:37888\n\t" \
    "s_waitcnt lgkmcnt(4)\n\t" \
    "v_mfma_f32_16x16x32_bf16 v[216:219], v[108:111], v[224:227], v[216:219]\n\t" \
    "ds_read_b128 v[236:239], v253 offset:38912\n\t" \
    "v_mfma_f32_16x16x32_bf16 v[220:223], v[112:115], v[240:243], v[220:223]\n\t" \
    "v_mfma_f32_16x16x32_bf16 v[220:223], v[116:119], v[224:227], v[220:223]\n\t" \
    "v_mfma_f32_16x16x32_bf16 v[212:215], v[96:99], v[240:243], v[212:215]\n\t" \
    "v_mfma_f32_16x16x32_bf16 v[212:215], v[100:103], v[224:227], v[212:215]\n\t" \
    "ds_read_b128 v[240:243], v253 offset:39936\n\t" \
    "s_waitcnt lgkmcnt(3)\n\t" \
    "s_barrier \n\t" \
    "v_mfma_f32_16x16x32_bf16 v[216:219], v[144:147], v[228:231], v[216:219]\n\t" \
    "s_waitcnt lgkmcnt(2)\n\t" \
    "v_mfma_f32_16x16x32_bf16 v[216:219], v[148:151], v[232:235], v[216:219]\n\t" \
    "s_waitcnt lgkmcnt(1)\n\t" \
    "v_mfma_f32_16x16x32_bf16 v[216:219], v[184:187], v[236:239], v[216:219]\n\t" \
    "s_waitcnt lgkmcnt(0)\n\t" \
    "v_mfma_f32_16x16x32_bf16 v[216:219], v[188:191], v[240:243], v[216:219]\n\t" \
    "v_mfma_f32_16x16x32_bf16 v[220:223], v[152:155], v[228:231], v[220:223]\n\t" \
    "v_mfma_f32_16x16x32_bf16 v[220:223], v[156:159], v[232:235], v[220:223]\n\t" \
    "v_mfma_f32_16x16x32_bf16 v[220:223], v[192:195], v[236:239], v[220:223]\n\t" \
    "v_cvt_pk_bf16_f32 v216, v216, v217\n\t" \
    "v_cvt_pk_bf16_f32 v217, v218, v219\n\t" \
    "v_mfma_f32_16x16x32_bf16 v[220:223], v[196:199], v[240:243], v[220:223]\n\t" \
    "ds_write_b64 v254, v[216:217] offset:13312\n\t" \
    "v_mfma_f32_16x16x32_bf16 v[212:215], v[136:139], v[228:231], v[212:215]\n\t" \
    "v_mfma_f32_16x16x32_bf16 v[212:215], v[140:143], v[232:235], v[212:215]\n\t" \
    "v_mfma_f32_16x16x32_bf16 v[212:215], v[176:179], v[236:239], v[212:215]\n\t" \
    "v_cvt_pk_bf16_f32 v220, v220, v221\n\t" \
    "v_cvt_pk_bf16_f32 v221, v222, v223\n\t" \
    "v_mfma_f32_16x16x32_bf16 v[212:215], v[180:183], v[240:243], v[212:215]\n\t" \
    "ds_write_b64 v254, v[220:221] offset:14336\n\t" \
    "s_nop 6\n\t" \
    "v_cvt_pk_bf16_f32 v212, v212, v213\n\t" \
    "v_cvt_pk_bf16_f32 v213, v214, v215\n\t" \
    "ds_write_b64 v254, v[212:213] offset:12288\n\t" \
    "s_waitcnt vmcnt(0) lgkmcnt(0)\n\t" \
    "s_barrier \n\t" \
    "s_setprio 2\n\t" \
    "s_add_u32 s44, s40, 1\n\t" \
    "s_min_u32 s44, s44, 63\n\t" \
    ".Lv08_%=:\n\t" \
    "v_cmp_ge_u32 vcc, s44, v252\n\t" \
    "s_cbranch_vccz .Lv09_%=\n\t" \
    "s_cmp_eq_u32 s41, 0\n\t" \
    "s_cbranch_scc1 .Lv09_%=\n\t" \
    "s_sub_u32 s41, s41, 1\n\t" \
    "s_sleep 2\n\t" \
    "v_and_b32 v252, 0xf0, v253\n\t" \
    "v_lshlrev_b32 v252, 3, v252\n\t" \
    "buffer_load_dword v252, v252, s[60:63], %[fl] offen sc1\n\t" \
    "s_waitcnt vmcnt(0)\n\t" \
    "s_branch .Lv08_%=\n\t" \
    ".Lv09_%=:\n\t" \
    "s_mov_b32 m0, 0x0\n\t" \
    "s_add_u32 s49, s42, 0x0\n\t" \
    "buffer_load_dwordx4 v253, s[60:63], s49 offen offset:0 sc1 lds\n\t" \
    "s_sleep 1\n\t" \
    "buffer_load_dwordx4 v253, s[60:63], s49 offen offset:1024 sc1 lds\n\t" \
    "s_sleep 1\n\t" \
    "buffer_load_dwordx4 v253, s[60:63], s49 offen offset:2048 sc1 lds\n\t" \
    "s_sleep 1\n\t" \
    "buffer_load_dwordx4 v253, s[60:63], s49 offen offset:3072 sc1 lds\n\t" \
    "s_sleep 1\n\t" \
    "s_mov_b32 m0, 0x4000\n\t" \
    "s_add_u32 s49, s42, 0x10000\n\t" \
    "buffer_load_dwordx4 v253, s[60:63], s49 offen offset:0 sc1 lds\n\t" \
    "s_sleep 1\n\t" \
    "s_mov_b32 m0, 0x5000\n\t" \
    "s_add_u32 s49, s42, 0x1000\n\t" \
    "buffer_load_dwordx4 v253, s[60:63], s49 offen offset:0 sc1 lds\n\t" \
    "s_sleep 1\n\t" \
    "buffer_load_dwordx4 v253, s[60:63], s49 offen offset:1024 sc1 lds\n\t" \
    "s_sleep 1\n\t" \
    "buffer_load_dwordx4 v253, s[60:63], s49 offen offset:2048 sc1 lds\n\t" \
    "s_sleep 1\n\t" \
    "buffer_load_dwordx4 v253, s[60:63], s49 offen offset:3072 sc1 lds\n\t" \
    "s_sleep 1\n\t" \
    "s_mov_b32 m0, 0x9000\n\t" \
    "s_add_u32 s49, s42, 0x11000\n\t" \
    "buffer_load_dwordx4 v253, s[60:63], s49 offen offset:0 sc1 lds\n\t" \
    "s_sleep 1\n\t" \
    "ds_read_b128 v[200:203], v254 offset:30720\n\t" \
    "ds_read_b128 v[204:207], v254 offset:31744\n\t" \
    "ds_read_b128 v[216:219], v254 offset:32768\n\t" \
    "ds_read_b128 v[220:223], v254 offset:33792\n\t" \
    "v_mov_b64 v[208:209], 0\n\t" \
    "v_mov_b64 v[210:211], 0\n\t" \
    "v_mov_b64 v[244:245], 0\n\t" \
    "v_mov_b64 v[246:247], 0\n\t" \
    "v_mov_b64 v[248:249], 0\n\t" \
    "v_mov_b64 v[250:251], 0\n\t" \
    "s_sleep 5\n\t" \
    "s_mov_b32 m0, 0xa000\n\t" \
    "s_nop 0\n\t" \
    "buffer_load_dwordx4 v253, s[60:63], s46 offen sc1 lds\n\t" \
    "s_mov_b32 m0, 0xb400\n\t" \
    "s_nop 0\n\t" \
    "buffer_load_dwordx4 v253, s[60:63], s47 offen sc1 lds\n\t" \
    "s_mov_b32 m0, 0xa400\n\t" \
    "s_nop 0\n\t" \
    "buffer_load_dwordx4 v253, s[60:63], s48 offen sc1 lds\n\t" \
    "s_waitcnt lgkmcnt(0)\n\t" \
    "v_bfe_i32 v212, v253, 5, 1\n\t" \
    "v_and_b32 v212, 0xaaaa, v212\n\t" \
    "v_or_b32 v212, 0x4444, v212\n\t" \
    "v_and_b32 v213, 0xffff, v255\n\t" \
    "v_lshrrev_b32 v214, 16, v255\n\t" \
    ".Lv010_%=:\n\t" \
    "s_waitcnt vmcnt(0)\n\t" \
    "ds_read_b128 v[224:227], v253 offset:40960\n\t" \
    "ds_read_b128 v[228:231], v253 offset:46080\n\t" \
    "ds_read_b128 v[232:235], v253 offset:41984\n\t" \
    "s_waitcnt lgkmcnt(2)\n\t" \
    "v_max_u32 v215, v224, v225\n\t" \
    "v_max3_u32 v215, v226, v227, v215\n\t" \
    "s_waitcnt lgkmcnt(1)\n\t" \
    "v_max3_u32 v215, v228, v229, v215\n\t" \
    "v_max3_u32 v215, v230, v231, v215\n\t" \
    "s_waitcnt lgkmcnt(0)\n\t" \
    "v_max3_u32 v215, v232, v233, v215\n\t" \
    "v_max3_u32 v215, v234, v235, v215\n\t" \
    "v_cmp_eq_u32 vcc, -1, v215\n\t" \
    "s_cbranch_vccz .Lv011_%=\n\t" \
    "v_max_u32 v215, v224, v225\n\t" \
    "v_max3_u32 v215, v226, v227, v215\n\t" \
    "v_cmp_eq_u32 vcc, -1, v215\n\t" \
    "s_cbranch_vccz .Lv012_%=\n\t" \
    "s_mov_b32 m0, 0xa000\n\t" \
    "s_nop 0\n\t" \
    "buffer_load_dwordx4 v253, s[60:63], s46 offen sc1 lds\n\t" \
    ".Lv012_%=:\n\t" \
    "v_max_u32 v215, v228, v229\n\t" \
    "v_max3_u32 v215, v230, v231, v215\n\t" \
    "v_cmp_eq_u32 vcc, -1, v215\n\t" \
    "s_cbranch_vccz .Lv013_%=\n\t" \
    "s_mov_b32 m0, 0xb400\n\t" \
    "s_nop 0\n\t" \
    "buffer_load_dwordx4 v253, s[60:63], s47 offen sc1 lds\n\t" \
    ".Lv013_%=:\n\t" \
    "v_max_u32 v215, v232, v233\n\t" \
    "v_max3_u32 v215, v234, v235, v215\n\t" \
    "v_cmp_eq_u32 vcc, -1, v215\n\t" \
    "s_cbranch_vccz .Lv014_%=\n\t" \
    "s_mov_b32 m0, 0xa400\n\t" \
    "s_nop 0\n\t" \
    "buffer_load_dwordx4 v253, s[60:63], s48 offen sc1 lds\n\t" \
    ".Lv014_%=:\n\t" \
    "s_cmp_eq_u32 s41, 0\n\t" \
    "s_cbranch_scc1 .Lv011_%=\n\t" \
    "s_sub_u32 s41, s41, 1\n\t" \
    "s_branch .Lv010_%=\n\t" \
    ".Lv011_%=:\n\t" \
    "s_waitcnt lgkmcnt(0)\n\t" \
    "s_barrier \n\t" \
    "ds_read_b128 v[236:239], v253 offset:47104\n\t" \
    "ds_read_b128 v[240:243], v253 offset:43008\n\t" \
    "v_smfmac_f32_16x16x64_bf16 v[244:247], v[224:227], v[0:7], v212\n\t" \
    "v_smfmac_f32_16x16x64_bf16 v[248:251], v[224:227], v[8:15], v212\n\t" \
    "v_smfmac_f32_16x16x64_bf16 v[208:211], v[224:227], v[200:207], v212\n\t" \
    "ds_read_b128 v[200:203], v254 offset:34816\n\t" \
    "ds_read_b128 v[204:207], v254 offset:35840\n\t" \
    "ds_read_b128 v[224:227], v253 offset:48128\n\t" \
    "v_smfmac_f32_16x16x64_bf16 v[208:211], v[228:231], v[16:23], v212\n\t" \
    "v_smfmac_f32_16x16x64_bf16 v[244:247], v[228:231], v[24:31], v212\n\t" \
    "v_smfmac_f32_16x16x64_bf16 v[248:251], v[228:231], v[32:39], v212\n\t" \
    "ds_read_b128 v[228:231], v253 offset:44032\n\t" \
    "v_smfmac_f32_16x16x64_bf16 v[208:211], v[232:235], v[216:223], v212\n\t" \
    "ds_read_b128 v[216:219], v254 offset:36864\n\t" \
    "ds_read_b128 v[220:223], v254 offset:37888\n\t" \
    "v_smfmac_f32_16x16x64_bf16 v[244:247], v[232:235], v[40:47], v212\n\t" \
    "v_smfmac_f32_16x16x64_bf16 v[248:251], v[232:235], v[48:55], v212\n\t" \
    "ds_read_b128 v[232:235], v253 offset:49152\n\t" \
    "s_waitcnt lgkmcnt(8)\n\t" \
    "v_smfmac_f32_16x16x64_bf16 v[208:211], v[236:239], v[56:63], v212\n\t" \
    "v_smfmac_f32_16x16x64_bf16 v[244:247], v[236:239], v[64:71], v212\n\t" \
    "v_smfmac_f32_16x16x64_bf16 v[248:251], v[236:239], v[72:79], v212\n\t" \
    "ds_read_b128 v[236:239], v253 offset:45056\n\t" \
    "s_waitcnt lgkmcnt(6)\n\t" \
    "v_smfmac_f32_16x16x64_bf16 v[208:211], v[240:243], v[200:207], v212\n\t" \
    "ds_read_b128 v[200:203], v254 offset:38912\n\t" \
    "ds_read_b128 v[204:207], v254 offset:39936\n\t" \
    "v_smfmac_f32_16x16x64_bf16 v[244:247], v[240:243], v[80:87], v212\n\t" \
    "v_smfmac_f32_16x16x64_bf16 v[248:251], v[240:243], v[88:95], v212\n\t" \
    "ds_read_b128 v[240:243], v253 offset:50176\n\t" \
    "s_waitcnt lgkmcnt(8)\n\t" \
    "v_smfmac_f32_16x16x64_bf16 v[208:211], v[224:227], v[96:103], v212\n\t" \
    "v_smfmac_f32_16x16x64_bf16 v[244:247], v[224:227], v[104:111], v212\n\t" \
    "v_smfmac_f32_16x16x64_bf16 v[248:251], v[224:227], v[112:119], v212\n\t" \
    "s_waitcnt lgkmcnt(7)\n\t" \
    "v_smfmac_f32_16x16x64_bf16 v[244:247], v[228:231], v[120:127], v212\n\t" \
    "s_waitcnt lgkmcnt(4)\n\t" \
    "v_smfmac_f32_16x16x64_bf16 v[244:247], v[232:235], v[144:151], v212\n\t" \
    "s_waitcnt lgkmcnt(3)\n\t" \
    "v_smfmac_f32_16x16x64_bf16 v[244:247], v[236:239], v[160:167], v212\n\t" \
    "s_waitcnt lgkmcnt(0)\n\t" \
    "v_smfmac_f32_16x16x64_bf16 v[244:247], v[240:243], v[184:191], v212\n\t" \
    "v_smfmac_f32_16x16x64_bf16 v[248:251], v[228:231], v[128:135], v212\n\t" \
    "v_smfmac_f32_16x16x64_bf16 v[248:251], v[232:235], v[152:159], v212\n\t" \
    "v_smfmac_f32_16x16x64_bf16 v[248:251], v[236:239], v[168:175], v212\n\t" \
    "v_add_f32 v244, v244, v246\n\t" \
    "v_add_f32 v245, v245, v247\n\t" \
    "v_cvt_pk_bf16_f32 v244, v244, v245\n\t" \
    "v_smfmac_f32_16x16x64_bf16 v[248:251], v[240:243], v[192:199], v212\n\t" \
    "ds_write_b16 v213, v244 offset:512\n\t" \
    "ds_write_b16_d16_hi v213, v244 offset:528\n\t" \
    "v_smfmac_f32_16x16x64_bf16 v[208:211], v[228:231], v[216:223], v212\n\t" \
    "v_smfmac_f32_16x16x64_bf16 v[208:211], v[232:235], v[136:143], v212\n\t" \
    "v_smfmac_f32_16x16x64_bf16 v[208:211], v[236:239], v[200:207], v212\n\t" \
    "v_add_f32 v248, v248, v250\n\t" \
    "v_add_f32 v249, v249, v251\n\t" \
    "v_cvt_pk_bf16_f32 v248, v248, v249\n\t" \
    "v_smfmac_f32_16x16x64_bf16 v[208:211], v[240:243], v[176:183], v212\n\t" \
    "ds_write_b16 v213, v248 offset:1024\n\t" \
    "ds_write_b16_d16_hi v213, v248 offset:1040\n\t" \
    "s_nop 5\n\t" \
    "v_add_f32 v208, v208, v210\n\t" \
    "v_add_f32 v209, v209, v211\n\t" \
    "v_cvt_pk_bf16_f32 v208, v208, v209\n\t" \
    "ds_write_b16 v213, v208 offset:0\n\t" \
    "ds_write_b16_d16_hi v213, v208 offset:16\n\t" \
    "s_setprio 0\n\t" \
    "s_waitcnt lgkmcnt(0)\n\t" \
    "s_barrier \n\t" \
    "ds_read_b128 v[224:227], v253 offset:0\n\t" \
    "ds_read_b128 v[244:247], v254 offset:30720\n\t" \
    "ds_read_b128 v[228:231], v253 offset:1024\n\t" \
    "ds_read_b128 v[232:235], v253 offset:20480\n\t" \
    "ds_read_b128 v[236:239], v253 offset:21504\n\t" \
    "ds_read_b128 v[248:251], v254 offset:31744\n\t" \
    "s_add_u32 s40, s40, 1\n\t" \
    "s_cmp_lg_u32 s40, 64\n\t" \
    "s_cbranch_scc1 .Lv07_%=\n\t" \
    "s_branch .Lend%=\n\t" \
    ".Lend%=:\n\t" \
    "s_waitcnt vmcnt(0) lgkmcnt(0)\n\t" \
    "s_mov_b32 %[bud], s41\n\t"
#define ASM_R_LOOP \
    "s_mov_b32 s40, 0\n\t" \
    "s_mov_b32 s41, 0x40000\n\t" \
    "v_mov_b64 v[252:253], -1\n\t" \
    "v_mov_b64 v[254:255], -1\n\t" \
    "v_mov_b32 v248, 0\n\t" \
    "s_mov_b32 s53, 0x05040100\n\t" \
    ".Lr1_%=:\n\t" \
    "s_mov_b32 s43, s40\n\t" \
    "s_and_b32 s44, s40, 3\n\t" \
    "s_lshl_b32 s42, s44, 20\n\t" \
    "s_and_b32 s45, s40, 2\n\t" \
    "s_lshl_b32 s45, s45, 12\n\t" \
    "s_add_u32 s42, s42, s45\n\t" \
    "s_add_u32 s42, s42, %[vx]\n\t" \
    "s_lshl_b32 s46, s44, 22\n\t" \
    "s_mov_b32 s47, s46\n\t" \
    "s_lshl_b32 s49, s44, 17\n\t" \
    "s_add_u32 s49, s49, %[px]\n\t" \
    "s_add_u32 s50, s49, 0x1000\n\t" \
    "s_add_u32 s44, s40, 1\n\t" \
    "s_and_b32 s44, s44, 3\n\t" \
    "s_lshl_b32 s48, s44, 22\n\t" \
    "s_mul_i32 s52, s40, 0x14000\n\t" \
    "s_lshr_b32 s44, s40, 4\n\t" \
    "s_mul_i32 s44, s44, 0xc2000\n\t" \
    "s_add_u32 s52, s52, s44\n\t" \
    "s_add_u32 s52, s52, %[xr]\n\t" \
    "s_add_i32 s44, s40, %[ad]\n\t" \
    "s_and_b32 s45, s44, 3\n\t" \
    "s_mul_i32 s51, s45, %[as]\n\t" \
    "s_cmp_lt_i32 s44, 0\n\t" \
    "s_cselect_b32 s55, 0x40000000, 0\n\t" \
    "s_add_u32 s44, s40, 3\n\t" \
    "s_mul_i32 s54, s44, 0x14000\n\t" \
    "s_lshr_b32 s45, s44, 4\n\t" \
    "s_mul_i32 s45, s45, 0xc2000\n\t" \
    "s_add_u32 s54, s54, s45\n\t" \
    "s_cmp_eq_u32 %[wave], 7\n\t" \
    "s_cselect_b32 s51, s54, s51\n\t" \
    "s_cselect_b32 s55, 0, s55\n\t" \
    "s_cselect_b32 s44, 60, 62\n\t" \
    "s_cmp_gt_u32 s40, s44\n\t" \
    "s_cselect_b32 s55, 0x40000000, s55\n\t" \
    "v_or_b32 v197, s55, %[ae]\n\t" \
    "v_mov_b64 v[180:181], -1\n\t" \
    "v_mov_b64 v[182:183], -1\n\t" \
    "v_mov_b64 v[184:185], -1\n\t" \
    "v_mov_b64 v[186:187], -1\n\t" \
    "v_mov_b64 v[188:189], -1\n\t" \
    "v_mov_b64 v[190:191], -1\n\t" \
    "v_mov_b64 v[192:193], -1\n\t" \
    "v_mov_b64 v[194:195], -1\n\t" \
    "s_waitcnt vmcnt(0)\n\t" \
    ".Lr2_%=:\n\t" \
    "buffer_load_dword v248, %[fo], %[rs], %[fs] offen sc1\n\t" \
    "s_waitcnt vmcnt(0)\n\t" \
    "v_cmp_ge_u32 vcc, s43, v248\n\t" \
    "s_cbranch_vccz .Lr3_%=\n\t" \
    "s_sleep 2\n\t" \
    "s_cmp_eq_u32 s41, 0\n\t" \
    "s_cbranch_scc1 .Lr3_%=\n\t" \
    "s_sub_u32 s41, s41, 1\n\t" \
    "s_branch .Lr2_%=\n\t" \
    ".Lr3_%=:\n\t" \
    "buffer_load_dwordx4 v[160:163], %[l16], %[rs], s42 offen offset:0 sc1\n\t" \
    "buffer_load_dwordx4 v[164:167], %[l16], %[rs], s42 offen offset:1024 sc1\n\t" \
    "buffer_load_dwordx4 v[168:171], %[l16], %[rs], s42 offen offset:2048 sc1\n\t" \
    "buffer_load_dwordx4 v[172:175], %[l16], %[rs], s42 offen offset:3072 sc1\n\t" \
    "s_sleep 8\n\t" \
    "s_cmp_lg_u32 s40, 63\n\t" \
    "s_cbranch_scc1 .Lr5_%=\n\t" \
    "buffer_load_dwordx4 v[180:183], %[ov], %[rs], s46 offen sc1\n\t" \
    ".Lr6_%=:\n\t" \
    "s_waitcnt vmcnt(12)\n\t" \
    "v_max_u32 v196, v180, v181\n\t" \
    "v_max3_u32 v196, v182, v183, v196\n\t" \
    "v_cmp_eq_u32 vcc, -1, v196\n\t" \
    "s_cbranch_vccz .Lr7_%=\n\t" \
    "s_nop 7\n\t" \
    "v_max_u32 v196, v180, v181\n\t" \
    "v_max3_u32 v196, v182, v183, v196\n\t" \
    "v_cmp_eq_u32 vcc, -1, v196\n\t" \
    "s_cbranch_vccz .Lr7_%=\n\t" \
    "s_nop 7\n\t" \
    "v_max_u32 v196, v180, v181\n\t" \
    "v_max3_u32 v196, v182, v183, v196\n\t" \
    "v_cmp_eq_u32 vcc, -1, v196\n\t" \
    "s_cbranch_vccz .Lr7_%=\n\t" \
    "s_nop 7\n\t" \
    "v_max_u32 v196, v180, v181\n\t" \
    "v_max3_u32 v196, v182, v183, v196\n\t" \
    "v_cmp_eq_u32 vcc, -1, v196\n\t" \
    "s_cbranch_vccz .Lr7_%=\n\t" \
    "s_nop 7\n\t" \
    "s_cmp_eq_u32 s41, 0\n\t" \
    "s_cbranch_scc1 .Lr7_%=\n\t" \
    "s_sub_u32 s41, s41, 1\n\t" \
    "v_max_u32 v196, v180, v181\n\t" \
    "v_max3_u32 v196, v182, v183, v196\n\t" \
    "v_cmp_eq_u32 vcc, -1, v196\n\t" \
    "s_cbranch_vccz .Lr8_%=\n\t" \
    "buffer_load_dwordx4 v[180:183], %[ov], %[rs], s46 offen sc1\n\t" \
    ".Lr8_%=:\n\t" \
    "s_branch .Lr6_%=\n\t" \
    ".Lr7_%=:\n\t" \
    "s_branch .Lr4_%=\n\t" \
    ".Lr5_%=:\n\t" \
    "buffer_load_dwordx4 v[184:187], %[o0], %[rs], s47 offen sc1\n\t" \
    "buffer_load_dwordx4 v[188:191], %[o1], %[rs], s47 offen sc1\n\t" \
    "buffer_load_dwordx4 v[192:195], %[o2], %[rs], s47 offen sc1\n\t" \
    "buffer_load_dwordx4 v[180:183], %[ov], %[rs], s46 offen sc1\n\t" \
    ".Lr9_%=:\n\t" \
    "s_waitcnt vmcnt(12)\n\t" \
    "v_max_u32 v196, v184, v185\n\t" \
    "v_max3_u32 v196, v186, v187, v196\n\t" \
    "v_max3_u32 v196, v188, v189, v196\n\t" \
    "v_max3_u32 v196, v190, v191, v196\n\t" \
    "v_max3_u32 v196, v192, v193, v196\n\t" \
    "v_max3_u32 v196, v194, v195, v196\n\t" \
    "v_max3_u32 v196, v180, v181, v196\n\t" \
    "v_max3_u32 v196, v182, v183, v196\n\t" \
    "v_cmp_eq_u32 vcc, -1, v196\n\t" \
    "s_cbranch_vccz .Lr10_%=\n\t" \
    "s_nop 7\n\t" \
    "v_max_u32 v196, v184, v185\n\t" \
    "v_max3_u32 v196, v186, v187, v196\n\t" \
    "v_max3_u32 v196, v188, v189, v196\n\t" \
    "v_max3_u32 v196, v190, v191, v196\n\t" \
    "v_max3_u32 v196, v192, v193, v196\n\t" \
    "v_max3_u32 v196, v194, v195, v196\n\t" \
    "v_max3_u32 v196, v180, v181, v196\n\t" \
    "v_max3_u32 v196, v182, v183, v196\n\t" \
    "v_cmp_eq_u32 vcc, -1, v196\n\t" \
    "s_cbranch_vccz .Lr10_%=\n\t" \
    "s_nop 7\n\t" \
    "v_max_u32 v196, v184, v185\n\t" \
    "v_max3_u32 v196, v186, v187, v196\n\t" \
    "v_max3_u32 v196, v188, v189, v196\n\t" \
    "v_max3_u32 v196, v190, v191, v196\n\t" \
    "v_max3_u32 v196, v192, v193, v196\n\t" \
    "v_max3_u32 v196, v194, v195, v196\n\t" \
    "v_max3_u32 v196, v180, v181, v196\n\t" \
    "v_max3_u32 v196, v182, v183, v196\n\t" \
    "v_cmp_eq_u32 vcc, -1, v196\n\t" \
    "s_cbranch_vccz .Lr10_%=\n\t" \
    "s_nop 7\n\t" \
    "v_max_u32 v196, v184, v185\n\t" \
    "v_max3_u32 v196, v186, v187, v196\n\t" \
    "v_max3_u32 v196, v188, v189, v196\n\t" \
    "v_max3_u32 v196, v190, v191, v196\n\t" \
    "v_max3_u32 v196, v192, v193, v196\n\t" \
    "v_max3_u32 v196, v194, v195, v196\n\t" \
    "v_max3_u32 v196, v180, v181, v196\n\t" \
    "v_max3_u32 v196, v182, v183, v196\n\t" \
    "v_cmp_eq_u32 vcc, -1, v196\n\t" \
    "s_cbranch_vccz .Lr10_%=\n\t" \
    "s_nop 7\n\t" \
    "v_max_u32 v196, v184, v185\n\t" \
    "v_max3_u32 v196, v186, v187, v196\n\t" \
    "v_max3_u32 v196, v188, v189, v196\n\t" \
    "v_max3_u32 v196, v190, v191, v196\n\t" \
    "v_max3_u32 v196, v192, v193, v196\n\t" \
    "v_max3_u32 v196, v194, v195, v196\n\t" \
    "v_max3_u32 v196, v180, v181, v196\n\t" \
    "v_max3_u32 v196, v182, v183, v196\n\t" \
    "v_cmp_eq_u32 vcc, -1, v196\n\t" \
    "s_cbranch_vccz .Lr10_%=\n\t" \
    "s_nop 7\n\t" \
    "v_max_u32 v196, v184, v185\n\t" \
    "v_max3_u32 v196, v186, v187, v196\n\t" \
    "v_max3_u32 v196, v188, v189, v196\n\t" \
    "v_max3_u32 v196, v190, v191, v196\n\t" \
    "v_max3_u32 v196, v192, v193, v196\n\t" \
    "v_max3_u32 v196, v194, v195, v196\n\t" \
    "v_max3_u32 v196, v180, v181, v196\n\t" \
    "v_max3_u32 v196, v182, v183, v196\n\t" \
    "v_cmp_eq_u32 vcc, -1, v196\n\t" \
    "s_cbranch_vccz .Lr10_%=\n\t" \
    "s_nop 7\n\t" \
    "v_max_u32 v196, v184, v185\n\t" \
    "v_max3_u32 v196, v186, v187, v196\n\t" \
    "v_max3_u32 v196, v188, v189, v196\n\t" \
    "v_max3_u32 v196, v190, v191, v196\n\t" \
    "v_max3_u32 v196, v192, v193, v196\n\t" \
    "v_max3_u32 v196, v194, v195, v196\n\t" \
    "v_max3_u32 v196, v180, v181, v196\n\t" \
    "v_max3_u32 v196, v182, v183, v196\n\t" \
    "v_cmp_eq_u32 vcc, -1, v196\n\t" \
    "s_cbranch_vccz .Lr10_%=\n\t" \
    "s_nop 7\n\t" \
    "v_max_u32 v196, v184, v185\n\t" \
    "v_max3_u32 v196, v186, v187, v196\n\t" \
    "v_max3_u32 v196, v188, v189, v196\n\t" \
    "v_max3_u32 v196, v190, v191, v196\n\t" \
    "v_max3_u32 v196, v192, v193, v196\n\t" \
    "v_max3_u32 v196, v194, v195, v196\n\t" \
    "v_max3_u32 v196, v180, v181, v196\n\t" \
    "v_max3_u32 v196, v182, v183, v196\n\t" \
    "v_cmp_eq_u32 vcc, -1, v196\n\t" \
    "s_cbranch_vccz .Lr10_%=\n\t" \
    "s_nop 7\n\t" \
    "s_cmp_eq_u32 s41, 0\n\t" \
    "s_cbranch_scc1 .Lr10_%=\n\t" \
    "s_sub_u32 s41, s41, 1\n\t" \
    "v_max_u32 v196, v184, v185\n\t" \
    "v_max3_u32 v196, v186, v187, v196\n\t" \
    "v_cmp_eq_u32 vcc, -1, v196\n\t" \
    "s_cbranch_vccz .Lr11_%=\n\t" \
    "buffer_load_dwordx4 v[184:187], %[o0], %[rs], s47 offen sc1\n\t" \
    ".Lr11_%=:\n\t" \
    "v_max_u32 v196, v188, v189\n\t" \
    "v_max3_u32 v196, v190, v191, v196\n\t" \
    "v_cmp_eq_u32 vcc, -1, v196\n\t" \
    "s_cbranch_vccz .Lr12_%=\n\t" \
    "buffer_load_dwordx4 v[188:191], %[o1], %[rs], s47 offen sc1\n\t" \
    ".Lr12_%=:\n\t" \
    "v_max_u32 v196, v192, v193\n\t" \
    "v_max3_u32 v196, v194, v195, v196\n\t" \
    "v_cmp_eq_u32 vcc, -1, v196\n\t" \
    "s_cbranch_vccz .Lr13_%=\n\t" \
    "buffer_load_dwordx4 v[192:195], %[o2], %[rs], s47 offen sc1\n\t" \
    ".Lr13_%=:\n\t" \
    "v_max_u32 v196, v180, v181\n\t" \
    "v_max3_u32 v196, v182, v183, v196\n\t" \
    "v_cmp_eq_u32 vcc, -1, v196\n\t" \
    "s_cbranch_vccz .Lr14_%=\n\t" \
    "buffer_load_dwordx4 v[180:183], %[ov], %[rs], s46 offen sc1\n\t" \
    ".Lr14_%=:\n\t" \
    "s_branch .Lr9_%=\n\t" \
    ".Lr10_%=:\n\t" \
    "v_mfma_f32_16x16x32_bf16 v[176:179], v[192:195], v[180:183], 0\n\t" \
    "v_smfmac_f32_16x16x64_bf16 v[176:179], v[184:187], v[160:167], %[sidx]\n\t" \
    "v_smfmac_f32_16x16x64_bf16 v[176:179], v[188:191], v[168:175], %[sidx]\n\t" \
    "s_cmp_gt_u32 %[wave], 3\n\t" \
    "s_nop 7\n\t" \
    "v_add_f32 v176, v176, v178\n\t" \
    "v_add_f32 v177, v177, v179\n\t" \
    "ds_write2_b32 %[rw], v176, v177 offset1:80\n\t" \
    "s_waitcnt lgkmcnt(0)\n\t" \
    "s_barrier \n\t" \
    "s_cbranch_scc1 .Lr4_%=\n\t" \
    "ds_read_b128 v[128:131], %[rd] offset:0\n\t" \
    "ds_read_b128 v[132:135], %[rd] offset:16\n\t" \
    "ds_read_b128 v[136:139], %[rd] offset:32\n\t" \
    "ds_read_b128 v[140:143], %[rd] offset:48\n\t" \
    "s_waitcnt lgkmcnt(2)\n\t" \
    "v_pk_add_f32 v[128:129], v[128:129], v[130:131]\n\t" \
    "v_pk_add_f32 v[132:133], v[132:133], v[134:135]\n\t" \
    "s_waitcnt lgkmcnt(0)\n\t" \
    "v_pk_add_f32 v[136:137], v[136:137], v[138:139]\n\t" \
    "v_pk_add_f32 v[140:141], v[140:141], v[142:143]\n\t" \
    "v_pk_add_f32 v[128:129], v[128:129], v[132:133]\n\t" \
    "v_pk_add_f32 v[136:137], v[136:137], v[140:141]\n\t" \
    "v_pk_add_f32 v[128:129], v[128:129], v[136:137]\n\t" \
    "v_cvt_pk_bf16_f32 v128, v128, v129\n\t" \
    "v_min_u32 v128, -2, v128\n\t" \
    "buffer_store_dword v128, %[oo], %[rs], s48 offen sc1\n\t" \
    ".Lr4_%=:\n\t" \
    "s_cmp_lt_u32 %[wave], 4\n\t" \
    "s_cbranch_scc1 .Lr16_%=\n\t" \
    "s_sleep 4\n\t" \
    ".Lr16_%=:\n\t" \
    "s_sleep 2\n\t" \
    "s_cmp_lt_u32 %[wave], 6\n\t" \
    "s_cbranch_scc1 .Lr15_%=\n\t" \
    "buffer_load_dwordx2 v[250:251], %[xo], %[rs], s52 offen sc1\n\t" \
    ".Lr15_%=:\n\t" \
    "buffer_load_dwordx4 v[220:223], %[l16], %[rs], s50 offen offset:1024 sc1\n\t" \
    "buffer_load_dwordx4 v[224:227], %[l16], %[rs], s50 offen offset:2048 sc1\n\t" \
    "buffer_load_dwordx4 v[228:231], %[l16], %[rs], s50 offen offset:3072 sc1\n\t" \
    "v_perm_b32 v232, v161, v160, s53\n\t" \
    "v_perm_b32 v233, v163, v162, s53\n\t" \
    "v_perm_b32 v234, v165, v164, s53\n\t" \
    "v_perm_b32 v235, v167, v166, s53\n\t" \
    "v_perm_b32 v236, v169, v168, s53\n\t" \
    "v_perm_b32 v237, v171, v170, s53\n\t" \
    "v_perm_b32 v238, v173, v172, s53\n\t" \
    "v_perm_b32 v239, v175, v174, s53\n\t" \
    "s_nop 1\n\t" \
    "v_permlane16_swap_b32 v232, v234\n\t" \
    "v_permlane16_swap_b32 v233, v235\n\t" \
    "v_permlane16_swap_b32 v236, v238\n\t" \
    "v_permlane16_swap_b32 v237, v239\n\t" \
    "s_waitcnt vmcnt(0)\n\t" \
    ".Lr17_%=:\n\t" \
    "v_max_u32 v196, v220, v221\n\t" \
    "v_max3_u32 v196, v222, v223, v196\n\t" \
    "v_max3_u32 v196, v224, v225, v196\n\t" \
    "v_max3_u32 v196, v226, v227, v196\n\t" \
    "v_max3_u32 v196, v228, v229, v196\n\t" \
    "v_max3_u32 v196, v230, v231, v196\n\t" \
    "v_cmp_eq_u32 vcc, -1, v196\n\t" \
    "s_cbranch_vccz .Lr18_%=\n\t" \
    "s_cmp_eq_u32 s41, 0\n\t" \
    "s_cbranch_scc1 .Lr18_%=\n\t" \
    "s_sub_u32 s41, s41, 1\n\t" \
    "v_max_u32 v196, v220, v221\n\t" \
    "v_max3_u32 v196, v222, v223, v196\n\t" \
    "v_cmp_eq_u32 vcc, -1, v196\n\t" \
    "s_cbranch_vccz .Lr19_%=\n\t" \
    "buffer_load_dwordx4 v[220:223], %[l16], %[rs], s50 offen offset:1024 sc1\n\t" \
    ".Lr19_%=:\n\t" \
    "v_max_u32 v196, v224, v225\n\t" \
    "v_max3_u32 v196, v226, v227, v196\n\t" \
    "v_cmp_eq_u32 vcc, -1, v196\n\t" \
    "s_cbranch_vccz .Lr20_%=\n\t" \
    "buffer_load_dwordx4 v[224:227], %[l16], %[rs], s50 offen offset:2048 sc1\n\t" \
    ".Lr20_%=:\n\t" \
    "v_max_u32 v196, v228, v229\n\t" \
    "v_max3_u32 v196, v230, v231, v196\n\t" \
    "v_cmp_eq_u32 vcc, -1, v196\n\t" \
    "s_cbranch_vccz .Lr21_%=\n\t" \
    "buffer_load_dwordx4 v[228:231], %[l16], %[rs], s50 offen offset:3072 sc1\n\t" \
    ".Lr21_%=:\n\t" \
    "s_waitcnt vmcnt(0)\n\t" \
    "s_branch .Lr17_%=\n\t" \
    ".Lr18_%=:\n\t" \
    "v_mfma_f32_16x16x32_bf16 v[240:243], v[232:235], v[220:223], 0\n\t" \
    "v_mfma_f32_16x16x32_bf16 v[240:243], v[236:239], v[224:227], v[240:243]\n\t" \
    "v_mfma_f32_16x16x32_bf16 v[240:243], v[180:183], v[228:231], v[240:243]\n\t" \
    "s_nop 7\n\t" \
    "ds_write_b128 %[pw], v[240:243]\n\t" \
    "s_waitcnt lgkmcnt(0)\n\t" \
    "s_barrier \n\t" \
    "buffer_load_dwordx4 v[200:203], %[l16], %[rs], s49 offen offset:0 sc1\n\t" \
    "buffer_load_dwordx4 v[204:207], %[l16], %[rs], s49 offen offset:1024 sc1\n\t" \
    "buffer_load_dwordx4 v[208:211], %[l16], %[rs], s49 offen offset:2048 sc1\n\t" \
    "buffer_load_dwordx4 v[212:215], %[l16], %[rs], s49 offen offset:3072 sc1\n\t" \
    "buffer_load_dwordx4 v[216:219], %[l16], %[rs], s50 offen offset:0 sc1\n\t" \
    "s_cmp_lg_u32 %[wave], 6\n\t" \
    "s_cbranch_scc1 .Lr22_%=\n\t" \
    "ds_read_b128 v[128:131], %[pr] offset:0\n\t" \
    "ds_read_b128 v[132:135], %[pr] offset:1024\n\t" \
    "ds_read_b128 v[136:139], %[pr] offset:2048\n\t" \
    "ds_read_b128 v[140:143], %[pr] offset:3072\n\t" \
    "ds_read_b128 v[144:147], %[pr] offset:4096\n\t" \
    "ds_read_b128 v[148:151], %[pr] offset:5120\n\t" \
    "ds_read_b128 v[152:155], %[pr] offset:6144\n\t" \
    "ds_read_b128 v[156:159], %[pr] offset:7168\n\t" \
    "v_lshlrev_b32 v176, 16, v250\n\t" \
    "v_and_b32 v177, 0xffff0000, v250\n\t" \
    "v_lshlrev_b32 v178, 16, v251\n\t" \
    "v_and_b32 v179, 0xffff0000, v251\n\t" \
    "s_waitcnt lgkmcnt(6)\n\t" \
    "v_pk_add_f32 v[128:129], v[128:129], v[132:133]\n\t" \
    "v_pk_add_f32 v[130:131], v[130:131], v[134:135]\n\t" \
    "s_waitcnt lgkmcnt(4)\n\t" \
    "v_pk_add_f32 v[136:137], v[136:137], v[140:141]\n\t" \
    "v_pk_add_f32 v[138:139], v[138:139], v[142:143]\n\t" \
    "v_pk_add_f32 v[128:129], v[128:129], v[136:137]\n\t" \
    "v_pk_add_f32 v[130:131], v[130:131], v[138:139]\n\t" \
    "s_waitcnt lgkmcnt(2)\n\t" \
    "v_pk_add_f32 v[144:145], v[144:145], v[148:149]\n\t" \
    "v_pk_add_f32 v[146:147], v[146:147], v[150:151]\n\t" \
    "s_waitcnt lgkmcnt(0)\n\t" \
    "v_pk_add_f32 v[152:153], v[152:153], v[156:157]\n\t" \
    "v_pk_add_f32 v[154:155], v[154:155], v[158:159]\n\t" \
    "v_pk_add_f32 v[144:145], v[144:145], v[152:153]\n\t" \
    "v_pk_add_f32 v[146:147], v[146:147], v[154:155]\n\t" \
    "v_pk_add_f32 v[128:129], v[128:129], v[144:145]\n\t" \
    "v_pk_add_f32 v[130:131], v[130:131], v[146:147]\n\t" \
    "v_pk_add_f32 v[128:129], v[128:129], v[176:177]\n\t" \
    "v_pk_add_f32 v[130:131], v[130:131], v[178:179]\n\t" \
    "v_cvt_pk_bf16_f32 v128, v128, v129\n\t" \
    "v_cvt_pk_bf16_f32 v129, v130, v131\n\t" \
    "v_min_u32 v128, -2, v128\n\t" \
    "v_min_u32 v129, -2, v129\n\t" \
    "s_add_u32 s44, s52, 0x1000000\n\t" \
    "buffer_store_dwordx2 v[128:129], %[xo], %[rs], s44 offen sc1\n\t" \
    "s_waitcnt vmcnt(1)\n\t" \
    "s_branch .Lr23_%=\n\t" \
    ".Lr22_%=:\n\t" \
    "s_waitcnt vmcnt(0)\n\t" \
    ".Lr23_%=:\n\t" \
    ".Lr24_%=:\n\t" \
    "v_max_u32 v196, v200, v201\n\t" \
    "v_max3_u32 v196, v202, v203, v196\n\t" \
    "v_max3_u32 v196, v204, v205, v196\n\t" \
    "v_max3_u32 v196, v206, v207, v196\n\t" \
    "v_max3_u32 v196, v208, v209, v196\n\t" \
    "v_max3_u32 v196, v210, v211, v196\n\t" \
    "v_max3_u32 v196, v212, v213, v196\n\t" \
    "v_max3_u32 v196, v214, v215, v196\n\t" \
    "v_max3_u32 v196, v216, v217, v196\n\t" \
    "v_max3_u32 v196, v218, v219, v196\n\t" \
    "v_cmp_eq_u32 vcc, -1, v196\n\t" \
    "s_cbranch_vccz .Lr25_%=\n\t" \
    "s_cmp_eq_u32 s41, 0\n\t" \
    "s_cbranch_scc1 .Lr25_%=\n\t" \
    "s_sub_u32 s41, s41, 1\n\t" \
    "v_max_u32 v196, v200, v201\n\t" \
    "v_max3_u32 v196, v202, v203, v196\n\t" \
    "v_cmp_eq_u32 vcc, -1, v196\n\t" \
    "s_cbranch_vccz .Lr26_%=\n\t" \
    "buffer_load_dwordx4 v[200:203], %[l16], %[rs], s49 offen offset:0 sc1\n\t" \
    ".Lr26_%=:\n\t" \
    "v_max_u32 v196, v204, v205\n\t" \
    "v_max3_u32 v196, v206, v207, v196\n\t" \
    "v_cmp_eq_u32 vcc, -1, v196\n\t" \
    "s_cbranch_vccz .Lr27_%=\n\t" \
    "buffer_load_dwordx4 v[204:207], %[l16], %[rs], s49 offen offset:1024 sc1\n\t" \
    ".Lr27_%=:\n\t" \
    "v_max_u32 v196, v208, v209\n\t" \
    "v_max3_u32 v196, v210, v211, v196\n\t" \
    "v_cmp_eq_u32 vcc, -1, v196\n\t" \
    "s_cbranch_vccz .Lr28_%=\n\t" \
    "buffer_load_dwordx4 v[208:211], %[l16], %[rs], s49 offen offset:2048 sc1\n\t" \
    ".Lr28_%=:\n\t" \
    "v_max_u32 v196, v212, v213\n\t" \
    "v_max3_u32 v196, v214, v215, v196\n\t" \
    "v_cmp_eq_u32 vcc, -1, v196\n\t" \
    "s_cbranch_vccz .Lr29_%=\n\t" \
    "buffer_load_dwordx4 v[212:215], %[l16], %[rs], s49 offen offset:3072 sc1\n\t" \
    ".Lr29_%=:\n\t" \
    "v_max_u32 v196, v216, v217\n\t" \
    "v_max3_u32 v196, v218, v219, v196\n\t" \
    "v_cmp_eq_u32 vcc, -1, v196\n\t" \
    "s_cbranch_vccz .Lr30_%=\n\t" \
    "buffer_load_dwordx4 v[216:219], %[l16], %[rs], s50 offen offset:0 sc1\n\t" \
    ".Lr30_%=:\n\t" \
    "s_waitcnt vmcnt(0)\n\t" \
    "s_branch .Lr24_%=\n\t" \
    ".Lr25_%=:\n\t" \
    "v_mfma_f32_16x16x32_bf16 v[244:247], v[160:163], v[200:203], 0\n\t" \
    "v_mfma_f32_16x16x32_bf16 v[244:247], v[164:167], v[204:207], v[244:247]\n\t" \
    "v_mfma_f32_16x16x32_bf16 v[244:247], v[168:171], v[208:211], v[244:247]\n\t" \
    "v_mfma_f32_16x16x32_bf16 v[244:247], v[172:175], v[212:215], v[244:247]\n\t" \
    "v_mfma_f32_16x16x32_bf16 v[244:247], v[180:183], v[216:219], v[244:247]\n\t" \
    "s_nop 7\n\t" \
    "ds_write_b128 %[pw], v[244:247] offset:8192\n\t" \
    "s_waitcnt lgkmcnt(0)\n\t" \
    "s_barrier \n\t" \
    "s_cmp_lg_u32 %[wave], 7\n\t" \
    "s_cbranch_scc1 .Lr31_%=\n\t" \
    "ds_read_b128 v[128:131], %[pr] offset:0\n\t" \
    "ds_read_b128 v[132:135], %[pr] offset:1024\n\t" \
    "ds_read_b128 v[136:139], %[pr] offset:2048\n\t" \
    "ds_read_b128 v[140:143], %[pr] offset:3072\n\t" \
    "ds_read_b128 v[144:147], %[pr] offset:4096\n\t" \
    "ds_read_b128 v[148:151], %[pr] offset:5120\n\t" \
    "ds_read_b128 v[152:155], %[pr] offset:6144\n\t" \
    "ds_read_b128 v[156:159], %[pr] offset:7168\n\t" \
    "v_lshlrev_b32 v176, 16, v250\n\t" \
    "v_and_b32 v177, 0xffff0000, v250\n\t" \
    "v_lshlrev_b32 v178, 16, v251\n\t" \
    "v_and_b32 v179, 0xffff0000, v251\n\t" \
    "s_waitcnt lgkmcnt(6)\n\t" \
    "v_pk_add_f32 v[128:129], v[128:129], v[132:133]\n\t" \
    "v_pk_add_f32 v[130:131], v[130:131], v[134:135]\n\t" \
    "s_waitcnt lgkmcnt(4)\n\t" \
    "v_pk_add_f32 v[136:137], v[136:137], v[140:141]\n\t" \
    "v_pk_add_f32 v[138:139], v[138:139], v[142:143]\n\t" \
    "v_pk_add_f32 v[128:129], v[128:129], v[136:137]\n\t" \
    "v_pk_add_f32 v[130:131], v[130:131], v[138:139]\n\t" \
    "s_waitcnt lgkmcnt(2)\n\t" \
    "v_pk_add_f32 v[144:145], v[144:145], v[148:149]\n\t" \
    "v_pk_add_f32 v[146:147], v[146:147], v[150:151]\n\t" \
    "s_waitcnt lgkmcnt(0)\n\t" \
    "v_pk_add_f32 v[152:153], v[152:153], v[156:157]\n\t" \
    "v_pk_add_f32 v[154:155], v[154:155], v[158:159]\n\t" \
    "v_pk_add_f32 v[144:145], v[144:145], v[152:153]\n\t" \
    "v_pk_add_f32 v[146:147], v[146:147], v[154:155]\n\t" \
    "v_pk_add_f32 v[128:129], v[128:129], v[144:145]\n\t" \
    "v_pk_add_f32 v[130:131], v[130:131], v[146:147]\n\t" \
    "v_pk_add_f32 v[128:129], v[128:129], v[176:177]\n\t" \
    "v_pk_add_f32 v[130:131], v[130:131], v[178:179]\n\t" \
    "v_cvt_pk_bf16_f32 v128, v128, v129\n\t" \
    "v_cvt_pk_bf16_f32 v129, v130, v131\n\t" \
    "v_min_u32 v128, -2, v128\n\t" \
    "v_min_u32 v129, -2, v129\n\t" \
    "s_add_u32 s44, s52, 0x1000000\n\t" \
    "buffer_store_dwordx2 v[128:129], %[xo], %[rs], s44 offen sc1\n\t" \
    ".Lr31_%=:\n\t" \
    "buffer_store_dwordx4 v[252:255], %[ov], %[rs], s46 offen sc1\n\t" \
    "buffer_store_dwordx4 v[252:255], v197, %[rs], s51 offen sc1\n\t" \
    "s_add_u32 s40, s40, 1\n\t" \
    "s_cmp_lg_u32 s40, 64\n\t" \
    "s_cbranch_scc1 .Lr1_%=\n\t" \
    "s_branch .Lend%=\n\t" \
    ".Lend%=:\n\t" \
    "s_waitcnt vmcnt(0) lgkmcnt(0)\n\t" \
    "s_mov_b32 %[bud], s41\n\t"
#define ASM_W4_MUL \
    "s_mov_b32 s40, 0\n\t" \
    "s_mov_b32 s41, 0x40000\n\t" \
    "s_mov_b32 s43, 0\n\t" \
    "s_mov_b32 s46, %[hb]\n\t" \
    "s_add_u32 s47, s46, 0x4000\n\t" \
    "v_mov_b64 v[188:189], -1\n\t" \
    "v_mov_b32 v246, 0x1e000\n\t" \
    ".Lm1_%=:\n\t" \
    ".Lm2_%=:\n\t" \
    "ds_read_b32 v245, v246 offset:24\n\t" \
    "s_sub_i32 s44, s40, 3\n\t" \
    "s_waitcnt lgkmcnt(0)\n\t" \
    "v_cmp_gt_i32 vcc, s44, v245\n\t" \
    "s_cbranch_vccnz .Lm2_%=\n\t" \
    "s_add_u32 s44, s40, 1\n\t" \
    "s_lshr_b32 s45, s44, 1\n\t" \
    "s_and_b32 s44, s44, 1\n\t" \
    "s_lshl_b32 s44, s44, 12\n\t" \
    "s_mul_i32 s48, s45, 0x30000\n\t" \
    "s_add_u32 s48, s48, s44\n\t" \
    "s_lshr_b32 s45, s45, 3\n\t" \
    "s_mul_i32 s45, s45, 0x82000\n\t" \
    "s_add_u32 s48, s48, s45\n\t" \
    "s_add_u32 s48, s48, %[hb]\n\t" \
    "s_add_u32 s49, s48, 0x4000\n\t" \
    "s_cmp_lg_u32 s43, 0\n\t" \
    "s_cbranch_scc1 .Lm5_%=\n\t" \
    ".Lm3_%=:\n\t" \
    "v_max_u32 v244, v188, v189\n\t" \
    "v_max3_u32 v244, v190, v191, v244\n\t" \
    "v_cmp_eq_u32 vcc, -1, v244\n\t" \
    "s_cbranch_vccz .Lm4_%=\n\t" \
    "s_cmp_eq_u32 s41, 0\n\t" \
    "s_cbranch_scc1 .Lm4_%=\n\t" \
    "s_sub_u32 s41, s41, 1\n\t" \
    "s_sleep 3\n\t" \
    "buffer_load_dwordx4 v[188:191], %[l16], %[rs], s47 offen offset:3072 sc1\n\t" \
    "s_waitcnt vmcnt(0)\n\t" \
    "s_branch .Lm3_%=\n\t" \
    ".Lm4_%=:\n\t" \
    "buffer_load_dwordx4 v[184:187], %[l16], %[rs], s47 offen offset:2048 sc1\n\t" \
    "buffer_load_dwordx4 v[180:183], %[l16], %[rs], s47 offen offset:1024 sc1\n\t" \
    "buffer_load_dwordx4 v[176:179], %[l16], %[rs], s47 offen offset:0 sc1\n\t" \
    "buffer_load_dwordx4 v[172:175], %[l16], %[rs], s46 offen offset:3072 sc1\n\t" \
    "buffer_load_dwordx4 v[168:171], %[l16], %[rs], s46 offen offset:2048 sc1\n\t" \
    "buffer_load_dwordx4 v[164:167], %[l16], %[rs], s46 offen offset:1024 sc1\n\t" \
    "buffer_load_dwordx4 v[160:163], %[l16], %[rs], s46 offen offset:0 sc1\n\t" \
    ".Lm5_%=:\n\t" \
    "v_mfma_f32_16x16x32_bf16 v[224:227], v[28:31], v[188:191], 0\n\t" \
    "v_mfma_f32_16x16x32_bf16 v[228:231], v[60:63], v[188:191], 0\n\t" \
    "v_mfma_f32_16x16x32_bf16 v[232:235], v[92:95], v[188:191], 0\n\t" \
    "v_mfma_f32_16x16x32_bf16 v[236:239], v[124:127], v[188:191], 0\n\t" \
    "v_mfma_f32_16x16x32_bf16 v[240:243], v[156:159], v[188:191], 0\n\t" \
    "s_waitcnt vmcnt(6)\n\t" \
    ".Lm6_%=:\n\t" \
    "v_max_u32 v244, v184, v185\n\t" \
    "v_max3_u32 v244, v186, v187, v244\n\t" \
    "v_cmp_eq_u32 vcc, -1, v244\n\t" \
    "s_cbranch_vccnz .Lm7_%=\n\t" \
    ".Lm8_%=:\n\t" \
    "v_mfma_f32_16x16x32_bf16 v[224:227], v[24:27], v[184:187], v[224:227]\n\t" \
    "v_mfma_f32_16x16x32_bf16 v[228:231], v[56:59], v[184:187], v[228:231]\n\t" \
    "v_mfma_f32_16x16x32_bf16 v[232:235], v[88:91], v[184:187], v[232:235]\n\t" \
    "v_mfma_f32_16x16x32_bf16 v[236:239], v[120:123], v[184:187], v[236:239]\n\t" \
    "v_mfma_f32_16x16x32_bf16 v[240:243], v[152:155], v[184:187], v[240:243]\n\t" \
    "s_waitcnt vmcnt(5)\n\t" \
    ".Lm9_%=:\n\t" \
    "v_max_u32 v244, v180, v181\n\t" \
    "v_max3_u32 v244, v182, v183, v244\n\t" \
    "v_cmp_eq_u32 vcc, -1, v244\n\t" \
    "s_cbranch_vccnz .Lm10_%=\n\t" \
    ".Lm11_%=:\n\t" \
    "v_mfma_f32_16x16x32_bf16 v[224:227], v[20:23], v[180:183], v[224:227]\n\t" \
    "v_mfma_f32_16x16x32_bf16 v[228:231], v[52:55], v[180:183], v[228:231]\n\t" \
    "v_mfma_f32_16x16x32_bf16 v[232:235], v[84:87], v[180:183], v[232:235]\n\t" \
    "v_mfma_f32_16x16x32_bf16 v[236:239], v[116:119], v[180:183], v[236:239]\n\t" \
    "v_mfma_f32_16x16x32_bf16 v[240:243], v[148:151], v[180:183], v[240:243]\n\t" \
    "s_waitcnt vmcnt(4)\n\t" \
    ".Lm12_%=:\n\t" \
    "v_max_u32 v244, v176, v177\n\t" \
    "v_max3_u32 v244, v178, v179, v244\n\t" \
    "v_cmp_eq_u32 vcc, -1, v244\n\t" \
    "s_cbranch_vccnz .Lm13_%=\n\t" \
    ".Lm14_%=:\n\t" \
    "v_mfma_f32_16x16x32_bf16 v[224:227], v[16:19], v[176:179], v[224:227]\n\t" \
    "v_mfma_f32_16x16x32_bf16 v[228:231], v[48:51], v[176:179], v[228:231]\n\t" \
    "v_mfma_f32_16x16x32_bf16 v[232:235], v[80:83], v[176:179], v[232:235]\n\t" \
    "v_mfma_f32_16x16x32_bf16 v[236:239], v[112:115], v[176:179], v[236:239]\n\t" \
    "v_mfma_f32_16x16x32_bf16 v[240:243], v[144:147], v[176:179], v[240:243]\n\t" \
    "s_waitcnt vmcnt(3)\n\t" \
    ".Lm15_%=:\n\t" \
    "v_max_u32 v244, v172, v173\n\t" \
    "v_max3_u32 v244, v174, v175, v244\n\t" \
    "v_cmp_eq_u32 vcc, -1, v244\n\t" \
    "s_cbranch_vccnz .Lm16_%=\n\t" \
    ".Lm17_%=:\n\t" \
    "v_mfma_f32_16x16x32_bf16 v[224:227], v[12:15], v[172:175], v[224:227]\n\t" \
    "v_mfma_f32_16x16x32_bf16 v[228:231], v[44:47], v[172:175], v[228:231]\n\t" \
    "v_mfma_f32_16x16x32_bf16 v[232:235], v[76:79], v[172:175], v[232:235]\n\t" \
    "v_mfma_f32_16x16x32_bf16 v[236:239], v[108:111], v[172:175], v[236:239]\n\t" \
    "v_mfma_f32_16x16x32_bf16 v[240:243], v[140:143], v[172:175], v[240:243]\n\t" \
    "s_waitcnt vmcnt(2)\n\t" \
    ".Lm18_%=:\n\t" \
    "v_max_u32 v244, v168, v169\n\t" \
    "v_max3_u32 v244, v170, v171, v244\n\t" \
    "v_cmp_eq_u32 vcc, -1, v244\n\t" \
    "s_cbranch_vccnz .Lm19_%=\n\t" \
    ".Lm20_%=:\n\t" \
    "buffer_load_dwordx4 v[220:223], %[l16], %[rs], s49 offen offset:3072 sc1\n\t" \
    "v_mfma_f32_16x16x32_bf16 v[224:227], v[8:11], v[168:171], v[224:227]\n\t" \
    "v_mfma_f32_16x16x32_bf16 v[228:231], v[40:43], v[168:171], v[228:231]\n\t" \
    "v_mfma_f32_16x16x32_bf16 v[232:235], v[72:75], v[168:171], v[232:235]\n\t" \
    "v_mfma_f32_16x16x32_bf16 v[236:239], v[104:107], v[168:171], v[236:239]\n\t" \
    "v_mfma_f32_16x16x32_bf16 v[240:243], v[136:139], v[168:171], v[240:243]\n\t" \
    "s_waitcnt vmcnt(2)\n\t" \
    ".Lm21_%=:\n\t" \
    "v_max_u32 v244, v164, v165\n\t" \
    "v_max3_u32 v244, v166, v167, v244\n\t" \
    "v_cmp_eq_u32 vcc, -1, v244\n\t" \
    "s_cbranch_vccnz .Lm22_%=\n\t" \
    ".Lm23_%=:\n\t" \
    "v_mfma_f32_16x16x32_bf16 v[224:227], v[4:7], v[164:167], v[224:227]\n\t" \
    "v_mfma_f32_16x16x32_bf16 v[228:231], v[36:39], v[164:167], v[228:231]\n\t" \
    "v_mfma_f32_16x16x32_bf16 v[232:235], v[68:71], v[164:167], v[232:235]\n\t" \
    "v_mfma_f32_16x16x32_bf16 v[236:239], v[100:103], v[164:167], v[236:239]\n\t" \
    "v_mfma_f32_16x16x32_bf16 v[240:243], v[132:135], v[164:167], v[240:243]\n\t" \
    "s_waitcnt vmcnt(1)\n\t" \
    ".Lm24_%=:\n\t" \
    "v_max_u32 v244, v160, v161\n\t" \
    "v_max3_u32 v244, v162, v163, v244\n\t" \
    "v_cmp_eq_u32 vcc, -1, v244\n\t" \
    "s_cbranch_vccnz .Lm25_%=\n\t" \
    ".Lm26_%=:\n\t" \
    "v_mfma_f32_16x16x32_bf16 v[224:227], v[0:3], v[160:163], v[224:227]\n\t" \
    "v_mfma_f32_16x16x32_bf16 v[228:231], v[32:35], v[160:163], v[228:231]\n\t" \
    "v_mfma_f32_16x16x32_bf16 v[232:235], v[64:67], v[160:163], v[232:235]\n\t" \
    "v_mfma_f32_16x16x32_bf16 v[236:239], v[96:99], v[160:163], v[236:239]\n\t" \
    "v_mfma_f32_16x16x32_bf16 v[240:243], v[128:131], v[160:163], v[240:243]\n\t" \
    "s_add_u32 s40, s40, 1\n\t" \
    "v_mov_b32 v245, s40\n\t" \
    "ds_write_b128 %[ld], v[224:227] offset:0\n\t" \
    "ds_write_b128 %[ld], v[228:231] offset:6144\n\t" \
    "ds_write_b128 %[ld], v[232:235] offset:12288\n\t" \
    "s_nop 0\n\t" \
    "ds_write_b128 %[ld], v[236:239] offset:18432\n\t" \
    "s_nop 1\n\t" \
    "ds_write_b128 %[ld], v[240:243] offset:24576\n\t" \
    "ds_write_b32 %[fa], v245\n\t" \
    "s_mov_b32 s43, 0\n\t" \
    "s_waitcnt vmcnt(0)\n\t" \
    "v_max_u32 v244, v220, v221\n\t" \
    "v_max3_u32 v244, v222, v223, v244\n\t" \
    "v_cmp_eq_u32 vcc, -1, v244\n\t" \
    "s_cbranch_vccnz .Lm27_%=\n\t" \
    "buffer_load_dwordx4 v[216:219], %[l16], %[rs], s49 offen offset:2048 sc1\n\t" \
    "buffer_load_dwordx4 v[212:215], %[l16], %[rs], s49 offen offset:1024 sc1\n\t" \
    "buffer_load_dwordx4 v[208:211], %[l16], %[rs], s49 offen offset:0 sc1\n\t" \
    "buffer_load_dwordx4 v[204:207], %[l16], %[rs], s48 offen offset:3072 sc1\n\t" \
    "buffer_load_dwordx4 v[200:203], %[l16], %[rs], s48 offen offset:2048 sc1\n\t" \
    "buffer_load_dwordx4 v[196:199], %[l16], %[rs], s48 offen offset:1024 sc1\n\t" \
    "buffer_load_dwordx4 v[192:195], %[l16], %[rs], s48 offen offset:0 sc1\n\t" \
    "s_mov_b32 s43, 1\n\t" \
    ".Lm27_%=:\n\t" \
    ".Lm28_%=:\n\t" \
    "ds_read_b32 v245, v246 offset:28\n\t" \
    "s_sub_i32 s44, s40, 3\n\t" \
    "s_waitcnt lgkmcnt(0)\n\t" \
    "v_cmp_gt_i32 vcc, s44, v245\n\t" \
    "s_cbranch_vccnz .Lm28_%=\n\t" \
    "s_add_u32 s44, s40, 1\n\t" \
    "s_lshr_b32 s45, s44, 1\n\t" \
    "s_and_b32 s44, s44, 1\n\t" \
    "s_lshl_b32 s44, s44, 12\n\t" \
    "s_mul_i32 s46, s45, 0x30000\n\t" \
    "s_add_u32 s46, s46, s44\n\t" \
    "s_lshr_b32 s45, s45, 3\n\t" \
    "s_mul_i32 s45, s45, 0x82000\n\t" \
    "s_add_u32 s46, s46, s45\n\t" \
    "s_add_u32 s46, s46, %[hb]\n\t" \
    "s_add_u32 s47, s46, 0x4000\n\t" \
    "s_cmp_lg_u32 s43, 0\n\t" \
    "s_cbranch_scc1 .Lm31_%=\n\t" \
    ".Lm29_%=:\n\t" \
    "v_max_u32 v244, v220, v221\n\t" \
    "v_max3_u32 v244, v222, v223, v244\n\t" \
    "v_cmp_eq_u32 vcc, -1, v244\n\t" \
    "s_cbranch_vccz .Lm30_%=\n\t" \
    "s_cmp_eq_u32 s41, 0\n\t" \
    "s_cbranch_scc1 .Lm30_%=\n\t" \
    "s_sub_u32 s41, s41, 1\n\t" \
    "s_sleep 3\n\t" \
    "buffer_load_dwordx4 v[220:223], %[l16], %[rs], s49 offen offset:3072 sc1\n\t" \
    "s_waitcnt vmcnt(0)\n\t" \
    "s_branch .Lm29_%=\n\t" \
    ".Lm30_%=:\n\t" \
    "buffer_load_dwordx4 v[216:219], %[l16], %[rs], s49 offen offset:2048 sc1\n\t" \
    "buffer_load_dwordx4 v[212:215], %[l16], %[rs], s49 offen offset:1024 sc1\n\t" \
    "buffer_load_dwordx4 v[208:211], %[l16], %[rs], s49 offen offset:0 sc1\n\t" \
    "buffer_load_dwordx4 v[204:207], %[l16], %[rs], s48 offen offset:3072 sc1\n\t" \
    "buffer_load_dwordx4 v[200:203], %[l16], %[rs], s48 offen offset:2048 sc1\n\t" \
    "buffer_load_dwordx4 v[196:199], %[l16], %[rs], s48 offen offset:1024 sc1\n\t" \
    "buffer_load_dwordx4 v[192:195], %[l16], %[rs], s48 offen offset:0 sc1\n\t" \
    ".Lm31_%=:\n\t" \
    "v_mfma_f32_16x16x32_bf16 v[224:227], v[28:31], v[220:223], 0\n\t" \
    "v_mfma_f32_16x16x32_bf16 v[228:231], v[60:63], v[220:223], 0\n\t" \
    "v_mfma_f32_16x16x32_bf16 v[232:235], v[92:95], v[220:223], 0\n\t" \
    "v_mfma_f32_16x16x32_bf16 v[236:239], v[124:127], v[220:223], 0\n\t" \
    "v_mfma_f32_16x16x32_bf16 v[240:243], v[156:159], v[220:223], 0\n\t" \
    "s_waitcnt vmcnt(6)\n\t" \
    ".Lm32_%=:\n\t" \
    "v_max_u32 v244, v216, v217\n\t" \
    "v_max3_u32 v244, v218, v219, v244\n\t" \
    "v_cmp_eq_u32 vcc, -1, v244\n\t" \
    "s_cbranch_vccnz .Lm33_%=\n\t" \
    ".Lm34_%=:\n\t" \
    "v_mfma_f32_16x16x32_bf16 v[224:227], v[24:27], v[216:219], v[224:227]\n\t" \
    "v_mfma_f32_16x16x32_bf16 v[228:231], v[56:59], v[216:219], v[228:231]\n\t" \
    "v_mfma_f32_16x16x32_bf16 v[232:235], v[88:91], v[216:219], v[232:235]\n\t" \
    "v_mfma_f32_16x16x32_bf16 v[236:239], v[120:123], v[216:219], v[236:239]\n\t" \
    "v_mfma_f32_16x16x32_bf16 v[240:243], v[152:155], v[216:219], v[240:243]\n\t" \
    "s_waitcnt vmcnt(5)\n\t" \
    ".Lm35_%=:\n\t" \
    "v_max_u32 v244, v212, v213\n\t" \
    "v_max3_u32 v244, v214, v215, v244\n\t" \
    "v_cmp_eq_u32 vcc, -1, v244\n\t" \
    "s_cbranch_vccnz .Lm36_%=\n\t" \
    ".Lm37_%=:\n\t" \
    "v_mfma_f32_16x16x32_bf16 v[224:227], v[20:23], v[212:215], v[224:227]\n\t" \
    "v_mfma_f32_16x16x32_bf16 v[228:231], v[52:55], v[212:215], v[228:231]\n\t" \
    "v_mfma_f32_16x16x32_bf16 v[232:235], v[84:87], v[212:215], v[232:235]\n\t" \
    "v_mfma_f32_16x16x32_bf16 v[236:239], v[116:119], v[212:215], v[236:239]\n\t" \
    "v_mfma_f32_16x16x32_bf16 v[240:243], v[148:151], v[212:215], v[240:243]\n\t" \
    "s_waitcnt vmcnt(4)\n\t" \
    ".Lm38_%=:\n\t" \
    "v_max_u32 v244, v208, v209\n\t" \
    "v_max3_u32 v244, v210, v211, v244\n\t" \
    "v_cmp_eq_u32 vcc, -1, v244\n\t" \
    "s_cbranch_vccnz .Lm39_%=\n\t" \
    ".Lm40_%=:\n\t" \
    "v_mfma_f32_16x16x32_bf16 v[224:227], v[16:19], v[208:211], v[224:227]\n\t" \
    "v_mfma_f32_16x16x32_bf16 v[228:231], v[48:51], v[208:211], v[228:231]\n\t" \
    "v_mfma_f32_16x16x32_bf16 v[232:235], v[80:83], v[208:211], v[232:235]\n\t" \
    "v_mfma_f32_16x16x32_bf16 v[236:239], v[112:115], v[208:211], v[236:239]\n\t" \
    "v_mfma_f32_16x16x32_bf16 v[240:243], v[144:147], v[208:211], v[240:243]\n\t" \
    "s_waitcnt vmcnt(3)\n\t" \
    ".Lm41_%=:\n\t" \
    "v_max_u32 v244, v204, v205\n\t" \
    "v_max3_u32 v244, v206, v207, v244\n\t" \
    "v_cmp_eq_u32 vcc, -1, v244\n\t" \
    "s_cbranch_vccnz .Lm42_%=\n\t" \
    ".Lm43_%=:\n\t" \
    "v_mfma_f32_16x16x32_bf16 v[224:227], v[12:15], v[204:207], v[224:227]\n\t" \
    "v_mfma_f32_16x16x32_bf16 v[228:231], v[44:47], v[204:207], v[228:231]\n\t" \
    "v_mfma_f32_16x16x32_bf16 v[232:235], v[76:79], v[204:207], v[232:235]\n\t" \
    "v_mfma_f32_16x16x32_bf16 v[236:239], v[108:111], v[204:207], v[236:239]\n\t" \
    "v_mfma_f32_16x16x32_bf16 v[240:243], v[140:143], v[204:207], v[240:243]\n\t" \
    "s_waitcnt vmcnt(2)\n\t" \
    ".Lm44_%=:\n\t" \
    "v_max_u32 v244, v200, v201\n\t" \
    "v_max3_u32 v244, v202, v203, v244\n\t" \
    "v_cmp_eq_u32 vcc, -1, v244\n\t" \
    "s_cbranch_vccnz .Lm45_%=\n\t" \
    ".Lm46_%=:\n\t" \
    "buffer_load_dwordx4 v[188:191], %[l16], %[rs], s47 offen offset:3072 sc1\n\t" \
    "v_mfma_f32_16x16x32_bf16 v[224:227], v[8:11], v[200:203], v[224:227]\n\t" \
    "v_mfma_f32_16x16x32_bf16 v[228:231], v[40:43], v[200:203], v[228:231]\n\t" \
    "v_mfma_f32_16x16x32_bf16 v[232:235], v[72:75], v[200:203], v[232:235]\n\t" \
    "v_mfma_f32_16x16x32_bf16 v[236:239], v[104:107], v[200:203], v[236:239]\n\t" \
    "v_mfma_f32_16x16x32_bf16 v[240:243], v[136:139], v[200:203], v[240:243]\n\t" \
    "s_waitcnt vmcnt(2)\n\t" \
    ".Lm47_%=:\n\t" \
    "v_max_u32 v244, v196, v197\n\t" \
    "v_max3_u32 v244, v198, v199, v244\n\t" \
    "v_cmp_eq_u32 vcc, -1, v244\n\t" \
    "s_cbranch_vccnz .Lm48_%=\n\t" \
    ".Lm49_%=:\n\t" \
    "v_mfma_f32_16x16x32_bf16 v[224:227], v[4:7], v[196:199], v[224:227]\n\t" \
    "v_mfma_f32_16x16x32_bf16 v[228:231], v[36:39], v[196:199], v[228:231]\n\t" \
    "v_mfma_f32_16x16x32_bf16 v[232:235], v[68:71], v[196:199], v[232:235]\n\t" \
    "v_mfma_f32_16x16x32_bf16 v[236:239], v[100:103], v[196:199], v[236:239]\n\t" \
    "v_mfma_f32_16x16x32_bf16 v[240:243], v[132:135], v[196:199], v[240:243]\n\t" \
    "s_waitcnt vmcnt(1)\n\t" \
    ".Lm50_%=:\n\t" \
    "v_max_u32 v244, v192, v193\n\t" \
    "v_max3_u32 v244, v194, v195, v244\n\t" \
    "v_cmp_eq_u32 vcc, -1, v244\n\t" \
    "s_cbranch_vccnz .Lm51_%=\n\t" \
    ".Lm52_%=:\n\t" \
    "v_mfma_f32_16x16x32_bf16 v[224:227], v[0:3], v[192:195], v[224:227]\n\t" \
    "v_mfma_f32_16x16x32_bf16 v[228:231], v[32:35], v[192:195], v[228:231]\n\t" \
    "v_mfma_f32_16x16x32_bf16 v[232:235], v[64:67], v[192:195], v[232:235]\n\t" \
    "v_mfma_f32_16x16x32_bf16 v[236:239], v[96:99], v[192:195], v[236:239]\n\t" \
    "v_mfma_f32_16x16x32_bf16 v[240:243], v[128:131], v[192:195], v[240:243]\n\t" \
    "s_add_u32 s40, s40, 1\n\t" \
    "v_mov_b32 v245, s40\n\t" \
    "ds_write_b128 %[ld], v[224:227] offset:30720\n\t" \
    "ds_write_b128 %[ld], v[228:231] offset:36864\n\t" \
    "ds_write_b128 %[ld], v[232:235] offset:43008\n\t" \
    "s_nop 0\n\t" \
    "ds_write_b128 %[ld], v[236:239] offset:49152\n\t" \
    "s_nop 1\n\t" \
    "ds_write_b128 %[ld], v[240:243] offset:55296\n\t" \
    "ds_write_b32 %[fa], v245\n\t" \
    "s_mov_b32 s43, 0\n\t" \
    "s_waitcnt vmcnt(0)\n\t" \
    "v_max_u32 v244, v188, v189\n\t" \
    "v_max3_u32 v244, v190, v191, v244\n\t" \
    "v_cmp_eq_u32 vcc, -1, v244\n\t" \
    "s_cbranch_vccnz .Lm53_%=\n\t" \
    "buffer_load_dwordx4 v[184:187], %[l16], %[rs], s47 offen offset:2048 sc1\n\t" \
    "buffer_load_dwordx4 v[180:183], %[l16], %[rs], s47 offen offset:1024 sc1\n\t" \
    "buffer_load_dwordx4 v[176:179], %[l16], %[rs], s47 offen offset:0 sc1\n\t" \
    "buffer_load_dwordx4 v[172:175], %[l16], %[rs], s46 offen offset:3072 sc1\n\t" \
    "buffer_load_dwordx4 v[168:171], %[l16], %[rs], s46 offen offset:2048 sc1\n\t" \
    "buffer_load_dwordx4 v[164:167], %[l16], %[rs], s46 offen offset:1024 sc1\n\t" \
    "buffer_load_dwordx4 v[160:163], %[l16], %[rs], s46 offen offset:0 sc1\n\t" \
    "s_mov_b32 s43, 1\n\t" \
    ".Lm53_%=:\n\t" \
    "v_sub_u32 %[ld], %[l2], %[ld]\n\t" \
    "s_nop 0\n\t" \
    "s_cmp_lg_u32 s40, 128\n\t" \
    "s_cbranch_scc1 .Lm1_%=\n\t" \
    "s_branch .Lend%=\n\t" \
    ".Lm7_%=:\n\t" \
    "s_cmp_eq_u32 s41, 0\n\t" \
    "s_cbranch_scc1 .Lm8_%=\n\t" \
    "s_sub_u32 s41, s41, 1\n\t" \
    "buffer_load_dwordx4 v[184:187], %[l16], %[rs], s47 offen offset:2048 sc1\n\t" \
    "s_waitcnt vmcnt(0)\n\t" \
    "s_branch .Lm6_%=\n\t" \
    ".Lm10_%=:\n\t" \
    "s_cmp_eq_u32 s41, 0\n\t" \
    "s_cbranch_scc1 .Lm11_%=\n\t" \
    "s_sub_u32 s41, s41, 1\n\t" \
    "buffer_load_dwordx4 v[180:183], %[l16], %[rs], s47 offen offset:1024 sc1\n\t" \
    "s_waitcnt vmcnt(0)\n\t" \
    "s_branch .Lm9_%=\n\t" \
    ".Lm13_%=:\n\t" \
    "s_cmp_eq_u32 s41, 0\n\t" \
    "s_cbranch_scc1 .Lm14_%=\n\t" \
    "s_sub_u32 s41, s41, 1\n\t" \
    "buffer_load_dwordx4 v[176:179], %[l16], %[rs], s47 offen offset:0 sc1\n\t" \
    "s_waitcnt vmcnt(0)\n\t" \
    "s_branch .Lm12_%=\n\t" \
    ".Lm16_%=:\n\t" \
    "s_cmp_eq_u32 s41, 0\n\t" \
    "s_cbranch_scc1 .Lm17_%=\n\t" \
    "s_sub_u32 s41, s41, 1\n\t" \
    "buffer_load_dwordx4 v[172:175], %[l16], %[rs], s46 offen offset:3072 sc1\n\t" \
    "s_waitcnt vmcnt(0)\n\t" \
    "s_branch .Lm15_%=\n\t" \
    ".Lm19_%=:\n\t" \
    "s_cmp_eq_u32 s41, 0\n\t" \
    "s_cbranch_scc1 .Lm20_%=\n\t" \
    "s_sub_u32 s41, s41, 1\n\t" \
    "buffer_load_dwordx4 v[168:171], %[l16], %[rs], s46 offen offset:2048 sc1\n\t" \
    "s_waitcnt vmcnt(0)\n\t" \
    "s_branch .Lm18_%=\n\t" \
    ".Lm22_%=:\n\t" \
    "s_cmp_eq_u32 s41, 0\n\t" \
    "s_cbranch_scc1 .Lm23_%=\n\t" \
    "s_sub_u32 s41, s41, 1\n\t" \
    "buffer_load_dwordx4 v[164:167], %[l16], %[rs], s46 offen offset:1024 sc1\n\t" \
    "s_waitcnt vmcnt(0)\n\t" \
    "s_branch .Lm21_%=\n\t" \
    ".Lm25_%=:\n\t" \
    "s_cmp_eq_u32 s41, 0\n\t" \
    "s_cbranch_scc1 .Lm26_%=\n\t" \
    "s_sub_u32 s41, s41, 1\n\t" \
    "buffer_load_dwordx4 v[160:163], %[l16], %[rs], s46 offen offset:0 sc1\n\t" \
    "s_waitcnt vmcnt(0)\n\t" \
    "s_branch .Lm24_%=\n\t" \
    ".Lm33_%=:\n\t" \
    "s_cmp_eq_u32 s41, 0\n\t" \
    "s_cbranch_scc1 .Lm34_%=\n\t" \
    "s_sub_u32 s41, s41, 1\n\t" \
    "buffer_load_dwordx4 v[216:219], %[l16], %[rs], s49 offen offset:2048 sc1\n\t" \
    "s_waitcnt vmcnt(0)\n\t" \
    "s_branch .Lm32_%=\n\t" \
    ".Lm36_%=:\n\t" \
    "s_cmp_eq_u32 s41, 0\n\t" \
    "s_cbranch_scc1 .Lm37_%=\n\t" \
    "s_sub_u32 s41, s41, 1\n\t" \
    "buffer_load_dwordx4 v[212:215], %[l16], %[rs], s49 offen offset:1024 sc1\n\t" \
    "s_waitcnt vmcnt(0)\n\t" \
    "s_branch .Lm35_%=\n\t" \
    ".Lm39_%=:\n\t" \
    "s_cmp_eq_u32 s41, 0\n\t" \
    "s_cbranch_scc1 .Lm40_%=\n\t" \
    "s_sub_u32 s41, s41, 1\n\t" \
    "buffer_load_dwordx4 v[208:211], %[l16], %[rs], s49 offen offset:0 sc1\n\t" \
    "s_waitcnt vmcnt(0)\n\t" \
    "s_branch .Lm38_%=\n\t" \
    ".Lm42_%=:\n\t" \
    "s_cmp_eq_u32 s41, 0\n\t" \
    "s_cbranch_scc1 .Lm43_%=\n\t" \
    "s_sub_u32 s41, s41, 1\n\t" \
    "buffer_load_dwordx4 v[204:207], %[l16], %[rs], s48 offen offset:3072 sc1\n\t" \
    "s_waitcnt vmcnt(0)\n\t" \
    "s_branch .Lm41_%=\n\t" \
    ".Lm45_%=:\n\t" \
    "s_cmp_eq_u32 s41, 0\n\t" \
    "s_cbranch_scc1 .Lm46_%=\n\t" \
    "s_sub_u32 s41, s41, 1\n\t" \
    "buffer_load_dwordx4 v[200:203], %[l16], %[rs], s48 offen offset:2048 sc1\n\t" \
    "s_waitcnt vmcnt(0)\n\t" \
    "s_branch .Lm44_%=\n\t" \
    ".Lm48_%=:\n\t" \
    "s_cmp_eq_u32 s41, 0\n\t" \
    "s_cbranch_scc1 .Lm49_%=\n\t" \
    "s_sub_u32 s41, s41, 1\n\t" \
    "buffer_load_dwordx4 v[196:199], %[l16], %[rs], s48 offen offset:1024 sc1\n\t" \
    "s_waitcnt vmcnt(0)\n\t" \
    "s_branch .Lm47_%=\n\t" \
    ".Lm51_%=:\n\t" \
    "s_cmp_eq_u32 s41, 0\n\t" \
    "s_cbranch_scc1 .Lm52_%=\n\t" \
    "s_sub_u32 s41, s41, 1\n\t" \
    "buffer_load_dwordx4 v[192:195], %[l16], %[rs], s48 offen offset:0 sc1\n\t" \
    "s_waitcnt vmcnt(0)\n\t" \
    "s_branch .Lm50_%=\n\t" \
    ".Lend%=:\n\t" \
    "s_waitcnt vmcnt(0) lgkmcnt(0)\n\t" \
    "s_mov_b32 %[bud], s41\n\t"
#define ASM_W4_FIN0 \
    "s_mov_b32 s40, %[mt]\n\t" \
    "s_mov_b32 s41, 0x40000\n\t" \
    "s_mov_b32 s43, 0\n\t" \
    "v_mov_b32 v99, 0x3f803f80\n\t" \
    "s_setprio 3\n\t" \
    "s_add_u32 s46, s43, 0x2c00000\n\t" \
    "s_add_u32 s47, s43, 0x1c00000\n\t" \
    "buffer_load_dwordx2 v[120:121], %[t0], %[rs], s46 offen sc1\n\t" \
    "buffer_load_dwordx2 v[122:123], %[t0], %[rs], s47 offen sc1\n\t" \
    "buffer_load_dwordx2 v[124:125], %[t1], %[rs], s46 offen sc1\n\t" \
    "buffer_load_dwordx2 v[126:127], %[t1], %[rs], s47 offen sc1\n\t" \
    "buffer_load_dwordx2 v[128:129], %[t2], %[rs], s46 offen sc1\n\t" \
    "buffer_load_dwordx2 v[130:131], %[t2], %[rs], s47 offen sc1\n\t" \
    "buffer_load_dwordx2 v[132:133], %[t3], %[rs], s46 offen sc1\n\t" \
    "buffer_load_dwordx2 v[134:135], %[t3], %[rs], s47 offen sc1\n\t" \
    "buffer_load_dwordx2 v[136:137], %[t4], %[rs], s46 offen sc1\n\t" \
    "buffer_load_dwordx2 v[138:139], %[t4], %[rs], s47 offen sc1\n\t" \
    "s_waitcnt vmcnt(0)\n\t" \
    ".Lf01_%=:\n\t" \
    ".Lf02_%=:\n\t" \
    "s_waitcnt vmcnt(1)\n\t" \
    "v_max_u32 v80, v120, v121\n\t" \
    "v_max3_u32 v80, v122, v123, v80\n\t" \
    "v_max3_u32 v80, v124, v125, v80\n\t" \
    "v_max3_u32 v80, v126, v127, v80\n\t" \
    "v_max3_u32 v80, v128, v129, v80\n\t" \
    "v_max3_u32 v80, v130, v131, v80\n\t" \
    "v_max3_u32 v80, v132, v133, v80\n\t" \
    "v_max3_u32 v80, v134, v135, v80\n\t" \
    "v_max3_u32 v80, v136, v137, v80\n\t" \
    "v_max3_u32 v80, v138, v139, v80\n\t" \
    "v_cmp_eq_u32 vcc, -1, v80\n\t" \
    "s_cbranch_vccz .Lf03_%=\n\t" \
    "s_cmp_eq_u32 s41, 0\n\t" \
    "s_cbranch_scc1 .Lf03_%=\n\t" \
    "s_sub_u32 s41, s41, 1\n\t" \
    "s_sleep 8\n\t" \
    "s_add_u32 s46, s43, 0x2c00000\n\t" \
    "s_add_u32 s47, s43, 0x1c00000\n\t" \
    "buffer_load_dwordx2 v[120:121], %[t0], %[rs], s46 offen sc1\n\t" \
    "buffer_load_dwordx2 v[122:123], %[t0], %[rs], s47 offen sc1\n\t" \
    "buffer_load_dwordx2 v[124:125], %[t1], %[rs], s46 offen sc1\n\t" \
    "buffer_load_dwordx2 v[126:127], %[t1], %[rs], s47 offen sc1\n\t" \
    "buffer_load_dwordx2 v[128:129], %[t2], %[rs], s46 offen sc1\n\t" \
    "buffer_load_dwordx2 v[130:131], %[t2], %[rs], s47 offen sc1\n\t" \
    "buffer_load_dwordx2 v[132:133], %[t3], %[rs], s46 offen sc1\n\t" \
    "buffer_load_dwordx2 v[134:135], %[t3], %[rs], s47 offen sc1\n\t" \
    "buffer_load_dwordx2 v[136:137], %[t4], %[rs], s46 offen sc1\n\t" \
    "buffer_load_dwordx2 v[138:139], %[t4], %[rs], s47 offen sc1\n\t" \
    "s_waitcnt vmcnt(0)\n\t" \
    "s_branch .Lf02_%=\n\t" \
    ".Lf03_%=:\n\t" \
    "v_lshlrev_b32 v140, 16, v120\n\t" \
    "v_and_b32 v141, 0xffff0000, v120\n\t" \
    "v_lshlrev_b32 v142, 16, v121\n\t" \
    "v_and_b32 v143, 0xffff0000, v121\n\t" \
    "v_lshlrev_b32 v80, 16, v122\n\t" \
    "v_and_b32 v81, 0xffff0000, v122\n\t" \
    "v_lshlrev_b32 v82, 16, v123\n\t" \
    "v_and_b32 v83, 0xffff0000, v123\n\t" \
    "v_add_f32 v140, v140, v80\n\t" \
    "v_add_f32 v141, v141, v81\n\t" \
    "v_add_f32 v142, v142, v82\n\t" \
    "v_add_f32 v143, v143, v83\n\t" \
    "v_lshlrev_b32 v144, 16, v124\n\t" \
    "v_and_b32 v145, 0xffff0000, v124\n\t" \
    "v_lshlrev_b32 v146, 16, v125\n\t" \
    "v_and_b32 v147, 0xffff0000, v125\n\t" \
    "v_lshlrev_b32 v80, 16, v126\n\t" \
    "v_and_b32 v81, 0xffff0000, v126\n\t" \
    "v_lshlrev_b32 v82, 16, v127\n\t" \
    "v_and_b32 v83, 0xffff0000, v127\n\t" \
    "v_add_f32 v144, v144, v80\n\t" \
    "v_add_f32 v145, v145, v81\n\t" \
    "v_add_f32 v146, v146, v82\n\t" \
    "v_add_f32 v147, v147, v83\n\t" \
    "v_lshlrev_b32 v148, 16, v128\n\t" \
    "v_and_b32 v149, 0xffff0000, v128\n\t" \
    "v_lshlrev_b32 v150, 16, v129\n\t" \
    "v_and_b32 v151, 0xffff0000, v129\n\t" \
    "v_lshlrev_b32 v80, 16, v130\n\t" \
    "v_and_b32 v81, 0xffff0000, v130\n\t" \
    "v_lshlrev_b32 v82, 16, v131\n\t" \
    "v_and_b32 v83, 0xffff0000, v131\n\t" \
    "v_add_f32 v148, v148, v80\n\t" \
    "v_add_f32 v149, v149, v81\n\t" \
    "v_add_f32 v150, v150, v82\n\t" \
    "v_add_f32 v151, v151, v83\n\t" \
    "v_lshlrev_b32 v152, 16, v132\n\t" \
    "v_and_b32 v153, 0xffff0000, v132\n\t" \
    "v_lshlrev_b32 v154, 16, v133\n\t" \
    "v_and_b32 v155, 0xffff0000, v133\n\t" \
    "v_lshlrev_b32 v80, 16, v134\n\t" \
    "v_and_b32 v81, 0xffff0000, v134\n\t" \
    "v_lshlrev_b32 v82, 16, v135\n\t" \
    "v_and_b32 v83, 0xffff0000, v135\n\t" \
    "v_add_f32 v152, v152, v80\n\t" \
    "v_add_f32 v153, v153, v81\n\t" \
    "v_add_f32 v154, v154, v82\n\t" \
    "v_add_f32 v155, v155, v83\n\t" \
    "v_lshlrev_b32 v156, 16, v136\n\t" \
    "v_and_b32 v157, 0xffff0000, v136\n\t" \
    "v_lshlrev_b32 v158, 16, v137\n\t" \
    "v_and_b32 v159, 0xffff0000, v137\n\t" \
    "v_lshlrev_b32 v80, 16, v138\n\t" \
    "v_and_b32 v81, 0xffff0000, v138\n\t" \
    "v_lshlrev_b32 v82, 16, v139\n\t" \
    "v_and_b32 v83, 0xffff0000, v139\n\t" \
    "v_add_f32 v156, v156, v80\n\t" \
    "v_add_f32 v157, v157, v81\n\t" \
    "v_add_f32 v158, v158, v82\n\t" \
    "v_add_f32 v159, v159, v83\n\t" \
    "s_mov_b32 s42, s43\n\t" \
    "s_lshr_b32 s48, s40, 1\n\t" \
    "s_add_u32 s48, s48, 1\n\t" \
    "s_mul_i32 s43, s48, 0x14000\n\t" \
    "s_lshr_b32 s44, s48, 4\n\t" \
    "s_mul_i32 s44, s44, 0xc2000\n\t" \
    "s_add_u32 s43, s43, s44\n\t" \
    "s_lshr_b32 s44, s40, 3\n\t" \
    "s_lshl_b32 s44, s44, 14\n\t" \
    "s_and_b32 s45, s40, 7\n\t" \
    "s_lshl_b32 s45, s45, 10\n\t" \
    "s_add_u32 s49, s44, s45\n\t" \
    "s_add_u32 s49, s49, 0x1c0000\n\t" \
    "s_add_u32 s50, s49, %[co]\n\t" \
    "s_add_u32 s51, s42, 0x1400000\n\t" \
    "s_add_u32 s52, s42, 0xc00000\n\t" \
    "s_mov_b32 s44, s48\n\t" \
    "s_mov_b32 s45, s48\n\t" \
    "v_mov_b64 v[76:77], s[44:45]\n\t" \
    "v_mov_b64 v[78:79], s[44:45]\n\t" \
    "s_add_u32 s44, s40, 1\n\t" \
    "v_mov_b32 v98, s44\n\t" \
    ".Lf04_%=:\n\t" \
    "s_nop 7\n\t" \
    "ds_read_b32 v80, %[fa]\n\t" \
    "s_waitcnt lgkmcnt(0)\n\t" \
    "v_cmp_ge_u32 vcc, s40, v80\n\t" \
    "s_cbranch_vccnz .Lf04_%=\n\t" \
    "ds_read_b128 v[160:163], %[la] offset:0\n\t" \
    "ds_read_b128 v[164:167], %[la] offset:6144\n\t" \
    "ds_read_b128 v[168:171], %[la] offset:12288\n\t" \
    "ds_read_b128 v[172:175], %[la] offset:18432\n\t" \
    "ds_read_b128 v[176:179], %[la] offset:24576\n\t" \
    "ds_read_b128 v[180:183], %[la] offset:1024\n\t" \
    "ds_read_b128 v[184:187], %[la] offset:7168\n\t" \
    "ds_read_b128 v[188:191], %[la] offset:13312\n\t" \
    "ds_read_b128 v[192:195], %[la] offset:19456\n\t" \
    "ds_read_b128 v[196:199], %[la] offset:25600\n\t" \
    "ds_read_b128 v[200:203], %[la] offset:2048\n\t" \
    "ds_read_b128 v[204:207], %[la] offset:8192\n\t" \
    "ds_read_b128 v[208:211], %[la] offset:14336\n\t" \
    "ds_read_b128 v[212:215], %[la] offset:20480\n\t" \
    "ds_read_b128 v[216:219], %[la] offset:26624\n\t" \
    "s_waitcnt lgkmcnt(14)\n\t" \
    "v_pk_add_f32 v[100:101], v[140:141], v[160:161]\n\t" \
    "v_pk_add_f32 v[102:103], v[142:143], v[162:163]\n\t" \
    "ds_read_b128 v[220:223], %[la] offset:3072\n\t" \
    "s_waitcnt lgkmcnt(14)\n\t" \
    "v_pk_add_f32 v[104:105], v[144:145], v[164:165]\n\t" \
    "v_pk_add_f32 v[106:107], v[146:147], v[166:167]\n\t" \
    "ds_read_b128 v[224:227], %[la] offset:9216\n\t" \
    "s_waitcnt lgkmcnt(14)\n\t" \
    "v_pk_add_f32 v[108:109], v[148:149], v[168:169]\n\t" \
    "v_pk_add_f32 v[110:111], v[150:151], v[170:171]\n\t" \
    "ds_read_b128 v[228:231], %[la] offset:15360\n\t" \
    "s_waitcnt lgkmcnt(14)\n\t" \
    "v_pk_add_f32 v[112:113], v[152:153], v[172:173]\n\t" \
    "v_pk_add_f32 v[114:115], v[154:155], v[174:175]\n\t" \
    "ds_read_b128 v[232:235], %[la] offset:21504\n\t" \
    "s_waitcnt lgkmcnt(14)\n\t" \
    "v_pk_add_f32 v[116:117], v[156:157], v[176:177]\n\t" \
    "v_pk_add_f32 v[118:119], v[158:159], v[178:179]\n\t" \
    "ds_read_b128 v[236:239], %[la] offset:27648\n\t" \
    "s_waitcnt lgkmcnt(14)\n\t" \
    "v_pk_add_f32 v[100:101], v[100:101], v[180:181]\n\t" \
    "v_pk_add_f32 v[102:103], v[102:103], v[182:183]\n\t" \
    "ds_read_b128 v[160:163], %[la] offset:4096\n\t" \
    "s_waitcnt lgkmcnt(14)\n\t" \
    "v_pk_add_f32 v[104:105], v[104:105], v[184:185]\n\t" \
    "v_pk_add_f32 v[106:107], v[106:107], v[186:187]\n\t" \
    "ds_read_b128 v[164:167], %[la] offset:10240\n\t" \
    "s_waitcnt lgkmcnt(14)\n\t" \
    "v_pk_add_f32 v[108:109], v[108:109], v[188:189]\n\t" \
    "v_pk_add_f32 v[110:111], v[110:111], v[190:191]\n\t" \
    "ds_read_b128 v[168:171], %[la] offset:16384\n\t" \
    "s_waitcnt lgkmcnt(14)\n\t" \
    "v_pk_add_f32 v[112:113], v[112:113], v[192:193]\n\t" \
    "v_pk_add_f32 v[114:115], v[114:115], v[194:195]\n\t" \
    "ds_read_b128 v[172:175], %[la] offset:22528\n\t" \
    "s_waitcnt lgkmcnt(14)\n\t" \
    "v_pk_add_f32 v[116:117], v[116:117], v[196:197]\n\t" \
    "v_pk_add_f32 v[118:119], v[118:119], v[198:199]\n\t" \
    "ds_read_b128 v[176:179], %[la] offset:28672\n\t" \
    "s_waitcnt lgkmcnt(14)\n\t" \
    "v_pk_add_f32 v[100:101], v[100:101], v[200:201]\n\t" \
    "v_pk_add_f32 v[102:103], v[102:103], v[202:203]\n\t" \
    "ds_read_b128 v[180:183], %[la] offset:5120\n\t" \
    "s_waitcnt lgkmcnt(14)\n\t" \
    "v_pk_add_f32 v[104:105], v[104:105], v[204:205]\n\t" \
    "v_pk_add_f32 v[106:107], v[106:107], v[206:207]\n\t" \
    "ds_read_b128 v[184:187], %[la] offset:11264\n\t" \
    "s_waitcnt lgkmcnt(14)\n\t" \
    "v_pk_add_f32 v[108:109], v[108:109], v[208:209]\n\t" \
    "v_pk_add_f32 v[110:111], v[110:111], v[210:211]\n\t" \
    "ds_read_b128 v[188:191], %[la] offset:17408\n\t" \
    "s_waitcnt lgkmcnt(14)\n\t" \
    "v_pk_add_f32 v[112:113], v[112:113], v[212:213]\n\t" \
    "v_pk_add_f32 v[114:115], v[114:115], v[214:215]\n\t" \
    "ds_read_b128 v[192:195], %[la] offset:23552\n\t" \
    "s_waitcnt lgkmcnt(14)\n\t" \
    "v_pk_add_f32 v[116:117], v[116:117], v[216:217]\n\t" \
    "v_pk_add_f32 v[118:119], v[118:119], v[218:219]\n\t" \
    "ds_read_b128 v[196:199], %[la] offset:29696\n\t" \
    "s_waitcnt lgkmcnt(14)\n\t" \
    "v_pk_add_f32 v[100:101], v[100:101], v[220:221]\n\t" \
    "v_pk_add_f32 v[102:103], v[102:103], v[222:223]\n\t" \
    "s_waitcnt lgkmcnt(13)\n\t" \
    "v_pk_add_f32 v[104:105], v[104:105], v[224:225]\n\t" \
    "v_pk_add_f32 v[106:107], v[106:107], v[226:227]\n\t" \
    "s_waitcnt lgkmcnt(12)\n\t" \
    "v_pk_add_f32 v[108:109], v[108:109], v[228:229]\n\t" \
    "v_pk_add_f32 v[110:111], v[110:111], v[230:231]\n\t" \
    "s_waitcnt lgkmcnt(11)\n\t" \
    "v_pk_add_f32 v[112:113], v[112:113], v[232:233]\n\t" \
    "v_pk_add_f32 v[114:115], v[114:115], v[234:235]\n\t" \
    "s_waitcnt lgkmcnt(10)\n\t" \
    "v_pk_add_f32 v[116:117], v[116:117], v[236:237]\n\t" \
    "v_pk_add_f32 v[118:119], v[118:119], v[238:239]\n\t" \
    "s_waitcnt lgkmcnt(9)\n\t" \
    "v_pk_add_f32 v[100:101], v[100:101], v[160:161]\n\t" \
    "v_pk_add_f32 v[102:103], v[102:103], v[162:163]\n\t" \
    "s_waitcnt lgkmcnt(8)\n\t" \
    "v_pk_add_f32 v[104:105], v[104:105], v[164:165]\n\t" \
    "v_pk_add_f32 v[106:107], v[106:107], v[166:167]\n\t" \
    "s_waitcnt lgkmcnt(7)\n\t" \
    "v_pk_add_f32 v[108:109], v[108:109], v[168:169]\n\t" \
    "v_pk_add_f32 v[110:111], v[110:111], v[170:171]\n\t" \
    "s_waitcnt lgkmcnt(6)\n\t" \
    "v_pk_add_f32 v[112:113], v[112:113], v[172:173]\n\t" \
    "v_pk_add_f32 v[114:115], v[114:115], v[174:175]\n\t" \
    "s_waitcnt lgkmcnt(5)\n\t" \
    "v_pk_add_f32 v[116:117], v[116:117], v[176:177]\n\t" \
    "v_pk_add_f32 v[118:119], v[118:119], v[178:179]\n\t" \
    "s_waitcnt lgkmcnt(4)\n\t" \
    "v_pk_add_f32 v[100:101], v[100:101], v[180:181]\n\t" \
    "v_pk_add_f32 v[102:103], v[102:103], v[182:183]\n\t" \
    "s_waitcnt lgkmcnt(3)\n\t" \
    "v_pk_add_f32 v[104:105], v[104:105], v[184:185]\n\t" \
    "v_pk_add_f32 v[106:107], v[106:107], v[186:187]\n\t" \
    "s_waitcnt lgkmcnt(2)\n\t" \
    "v_pk_add_f32 v[108:109], v[108:109], v[188:189]\n\t" \
    "v_pk_add_f32 v[110:111], v[110:111], v[190:191]\n\t" \
    "s_waitcnt lgkmcnt(1)\n\t" \
    "v_pk_add_f32 v[112:113], v[112:113], v[192:193]\n\t" \
    "v_pk_add_f32 v[114:115], v[114:115], v[194:195]\n\t" \
    "s_waitcnt lgkmcnt(0)\n\t" \
    "v_pk_add_f32 v[116:117], v[116:117], v[196:197]\n\t" \
    "v_pk_add_f32 v[118:119], v[118:119], v[198:199]\n\t" \
    "ds_write_b32 %[fc], v98\n\t" \
    "v_sub_u32 %[la], %[l2], %[la]\n\t" \
    "v_cvt_pk_bf16_f32 v84, v100, v101\n\t" \
    "v_cvt_pk_bf16_f32 v85, v102, v103\n\t" \
    "v_cvt_pk_bf16_f32 v86, v104, v105\n\t" \
    "v_cvt_pk_bf16_f32 v87, v106, v107\n\t" \
    "v_cvt_pk_bf16_f32 v88, v108, v109\n\t" \
    "v_cvt_pk_bf16_f32 v89, v110, v111\n\t" \
    "v_cvt_pk_bf16_f32 v90, v112, v113\n\t" \
    "v_cvt_pk_bf16_f32 v91, v114, v115\n\t" \
    "v_cvt_pk_bf16_f32 v92, v116, v117\n\t" \
    "v_cvt_pk_bf16_f32 v93, v118, v119\n\t" \
    "v_mov_b32 v80, 0\n\t" \
    "v_mov_b32 v82, 0\n\t" \
    "v_dot2c_f32_bf16 v80, v84, v99\n\t" \
    "v_dot2c_f32_bf16 v82, v84, v84\n\t" \
    "v_dot2c_f32_bf16 v80, v85, v99\n\t" \
    "v_dot2c_f32_bf16 v82, v85, v85\n\t" \
    "v_dot2c_f32_bf16 v80, v86, v99\n\t" \
    "v_dot2c_f32_bf16 v82, v86, v86\n\t" \
    "v_dot2c_f32_bf16 v80, v87, v99\n\t" \
    "v_dot2c_f32_bf16 v82, v87, v87\n\t" \
    "v_dot2c_f32_bf16 v80, v88, v99\n\t" \
    "v_dot2c_f32_bf16 v82, v88, v88\n\t" \
    "v_dot2c_f32_bf16 v80, v89, v99\n\t" \
    "v_dot2c_f32_bf16 v82, v89, v89\n\t" \
    "v_dot2c_f32_bf16 v80, v90, v99\n\t" \
    "v_dot2c_f32_bf16 v82, v90, v90\n\t" \
    "v_dot2c_f32_bf16 v80, v91, v99\n\t" \
    "v_dot2c_f32_bf16 v82, v91, v91\n\t" \
    "v_dot2c_f32_bf16 v80, v92, v99\n\t" \
    "v_dot2c_f32_bf16 v82, v92, v92\n\t" \
    "v_dot2c_f32_bf16 v80, v93, v99\n\t" \
    "v_dot2c_f32_bf16 v82, v93, v93\n\t" \
    "s_nop 2\n\t" \
    "v_mov_b32 v81, v80\n\t" \
    "v_mov_b32 v83, v82\n\t" \
    "s_nop 1\n\t" \
    "v_permlane16_swap_b32 v80, v81\n\t" \
    "v_permlane16_swap_b32 v82, v83\n\t" \
    "v_add_f32 v80, v80, v81\n\t" \
    "v_add_f32 v82, v82, v83\n\t" \
    "v_mov_b32 v81, v80\n\t" \
    "v_mov_b32 v83, v82\n\t" \
    "s_nop 1\n\t" \
    "v_permlane32_swap_b32 v80, v81\n\t" \
    "v_permlane32_swap_b32 v82, v83\n\t" \
    "v_add_f32 v80, v80, v81\n\t" \
    "v_add_f32 v82, v82, v83\n\t" \
    "v_min_u32 v80, -2, v80\n\t" \
    "v_min_u32 v81, -2, v82\n\t" \
    "s_mov_b64 exec, 0xffff\n\t" \
    "buffer_store_dwordx2 v[80:81], %[l8], %[rs], s50 offen sc1\n\t" \
    "s_mov_b64 exec, -1\n\t" \
    "s_add_u32 s46, s43, 0x2c00000\n\t" \
    "s_add_u32 s47, s43, 0x1c00000\n\t" \
    "buffer_load_dwordx2 v[120:121], %[t0], %[rs], s46 offen sc1\n\t" \
    "buffer_load_dwordx2 v[122:123], %[t0], %[rs], s47 offen sc1\n\t" \
    "buffer_load_dwordx2 v[124:125], %[t1], %[rs], s46 offen sc1\n\t" \
    "buffer_load_dwordx2 v[126:127], %[t1], %[rs], s47 offen sc1\n\t" \
    "buffer_load_dwordx2 v[128:129], %[t2], %[rs], s46 offen sc1\n\t" \
    "buffer_load_dwordx2 v[130:131], %[t2], %[rs], s47 offen sc1\n\t" \
    "buffer_load_dwordx2 v[132:133], %[t3], %[rs], s46 offen sc1\n\t" \
    "buffer_load_dwordx2 v[134:135], %[t3], %[rs], s47 offen sc1\n\t" \
    "buffer_load_dwordx2 v[136:137], %[t4], %[rs], s46 offen sc1\n\t" \
    "buffer_load_dwordx2 v[138:139], %[t4], %[rs], s47 offen sc1\n\t" \
    "s_nop 0\n\t" \
    "v_permlane16_swap_b32 v84, v86\n\t" \
    "v_permlane16_swap_b32 v85, v87\n\t" \
    "v_permlane16_swap_b32 v88, v90\n\t" \
    "v_permlane16_swap_b32 v89, v91\n\t" \
    "s_sleep 4\n\t" \
    ".Lf05_%=:\n\t" \
    "buffer_load_dwordx2 v[94:95], %[l8], %[rs], s49 offen sc1\n\t" \
    "buffer_load_dwordx2 v[96:97], %[l8], %[rs], s49 offen offset:512 sc1\n\t" \
    "s_waitcnt vmcnt(0)\n\t" \
    "v_max_u32 v80, v94, v95\n\t" \
    "v_max3_u32 v80, v96, v97, v80\n\t" \
    "v_cmp_eq_u32 vcc, -1, v80\n\t" \
    "s_cbranch_vccz .Lf06_%=\n\t" \
    "s_cmp_eq_u32 s41, 0\n\t" \
    "s_cbranch_scc1 .Lf06_%=\n\t" \
    "s_sub_u32 s41, s41, 1\n\t" \
    "s_branch .Lf05_%=\n\t" \
    ".Lf06_%=:\n\t" \
    "v_add_f32 v80, v94, v96\n\t" \
    "v_add_f32 v82, v95, v97\n\t" \
    "v_mov_b32 v81, v80\n\t" \
    "v_mov_b32 v83, v82\n\t" \
    "s_nop 1\n\t" \
    "v_permlane16_swap_b32 v80, v81\n\t" \
    "v_permlane16_swap_b32 v82, v83\n\t" \
    "v_add_f32 v80, v80, v81\n\t" \
    "v_add_f32 v82, v82, v83\n\t" \
    "v_mov_b32 v81, v80\n\t" \
    "v_mov_b32 v83, v82\n\t" \
    "s_nop 1\n\t" \
    "v_permlane32_swap_b32 v80, v81\n\t" \
    "v_permlane32_swap_b32 v82, v83\n\t" \
    "v_add_f32 v80, v80, v81\n\t" \
    "v_add_f32 v82, v82, v83\n\t" \
    "v_mul_f32 v80, 0x3acccccd, v80\n\t" \
    "v_mul_f32 v82, 0x3acccccd, v82\n\t" \
    "v_fma_f32 v82, -v80, v80, v82\n\t" \
    "v_cvt_pk_bf16_f32 v80, v80, v80\n\t" \
    "v_and_b32 v80, 0xffff0000, v80\n\t" \
    "v_max_f32 v82, 0, v82\n\t" \
    "v_cvt_pk_bf16_f32 v82, v82, v82\n\t" \
    "v_and_b32 v82, 0xffff0000, v82\n\t" \
    "v_add_f32 v82, 0x3727c5ac, v82\n\t" \
    "v_cvt_pk_bf16_f32 v82, v82, v82\n\t" \
    "v_and_b32 v82, 0xffff0000, v82\n\t" \
    "v_sqrt_f32 v82, v82\n\t" \
    "s_nop 0\n\t" \
    "v_cvt_pk_bf16_f32 v82, v82, v82\n\t" \
    "v_and_b32 v82, 0xffff0000, v82\n\t" \
    "v_rcp_f32 v82, v82\n\t" \
    "s_nop 0\n\t" \
    "v_mul_f32 v80, -v80, v82\n\t" \
    "v_fma_f32 v100, v100, v82, v80\n\t" \
    "v_fma_f32 v101, v101, v82, v80\n\t" \
    "v_fma_f32 v102, v102, v82, v80\n\t" \
    "v_fma_f32 v103, v103, v82, v80\n\t" \
    "v_fma_f32 v104, v104, v82, v80\n\t" \
    "v_fma_f32 v105, v105, v82, v80\n\t" \
    "v_fma_f32 v106, v106, v82, v80\n\t" \
    "v_fma_f32 v107, v107, v82, v80\n\t" \
    "v_fma_f32 v108, v108, v82, v80\n\t" \
    "v_fma_f32 v109, v109, v82, v80\n\t" \
    "v_fma_f32 v110, v110, v82, v80\n\t" \
    "v_fma_f32 v111, v111, v82, v80\n\t" \
    "v_fma_f32 v112, v112, v82, v80\n\t" \
    "v_fma_f32 v113, v113, v82, v80\n\t" \
    "v_fma_f32 v114, v114, v82, v80\n\t" \
    "v_fma_f32 v115, v115, v82, v80\n\t" \
    "v_fma_f32 v116, v116, v82, v80\n\t" \
    "v_fma_f32 v117, v117, v82, v80\n\t" \
    "v_fma_f32 v118, v118, v82, v80\n\t" \
    "v_fma_f32 v119, v119, v82, v80\n\t" \
    "v_cvt_pk_bf16_f32 v64, v100, v101\n\t" \
    "v_cvt_pk_bf16_f32 v65, v102, v103\n\t" \
    "v_cvt_pk_bf16_f32 v66, v104, v105\n\t" \
    "v_cvt_pk_bf16_f32 v67, v106, v107\n\t" \
    "v_cvt_pk_bf16_f32 v68, v108, v109\n\t" \
    "v_cvt_pk_bf16_f32 v69, v110, v111\n\t" \
    "v_cvt_pk_bf16_f32 v70, v112, v113\n\t" \
    "v_cvt_pk_bf16_f32 v71, v114, v115\n\t" \
    "v_cvt_pk_bf16_f32 v72, v116, v117\n\t" \
    "v_cvt_pk_bf16_f32 v73, v118, v119\n\t" \
    "s_nop 0\n\t" \
    "v_permlane16_swap_b32 v64, v66\n\t" \
    "v_permlane16_swap_b32 v65, v67\n\t" \
    "v_permlane16_swap_b32 v68, v70\n\t" \
    "v_permlane16_swap_b32 v69, v71\n\t" \
    "buffer_store_dwordx4 v[64:67], %[o16], %[rn], s51 offen sc1\n\t" \
    "buffer_store_dwordx4 v[68:71], %[o17], %[rn], s51 offen sc1\n\t" \
    "buffer_store_dwordx2 v[72:73], %[t4], %[rn], s51 offen sc1\n\t" \
    "buffer_store_dwordx4 v[84:87], %[o16], %[rn], s52 offen sc1\n\t" \
    "buffer_store_dwordx4 v[88:91], %[o17], %[rn], s52 offen sc1\n\t" \
    "buffer_store_dwordx2 v[92:93], %[t4], %[rn], s52 offen sc1\n\t" \
    "s_waitcnt vmcnt(0)\n\t" \
    "buffer_store_dwordx4 v[76:79], %[fo], %[rn], %[fs] offen sc1\n\t" \
    "s_add_u32 s40, s40, 2\n\t" \
    "s_cmp_lt_u32 s40, 128\n\t" \
    "s_cbranch_scc1 .Lf01_%=\n\t" \
    "s_branch .Lend%=\n\t" \
    ".Lend%=:\n\t" \
    "s_waitcnt vmcnt(0) lgkmcnt(0)\n\t" \
    "s_mov_b32 %[bud], s41\n\t"
#define ASM_W4_FIN1 \
    "s_mov_b32 s40, %[mt]\n\t" \
    "s_mov_b32 s41, 0x40000\n\t" \
    "s_mov_b32 s43, 0\n\t" \
    "v_mov_b32 v99, 0x3f803f80\n\t" \
    "s_setprio 3\n\t" \
    "s_add_u32 s46, s43, 0x2c00000\n\t" \
    "s_add_u32 s47, s43, 0x1c00000\n\t" \
    "buffer_load_dwordx2 v[120:121], %[t0], %[rs], s46 offen sc1\n\t" \
    "buffer_load_dwordx2 v[122:123], %[t0], %[rs], s47 offen sc1\n\t" \
    "buffer_load_dwordx2 v[124:125], %[t1], %[rs], s46 offen sc1\n\t" \
    "buffer_load_dwordx2 v[126:127], %[t1], %[rs], s47 offen sc1\n\t" \
    "buffer_load_dwordx2 v[128:129], %[t2], %[rs], s46 offen sc1\n\t" \
    "buffer_load_dwordx2 v[130:131], %[t2], %[rs], s47 offen sc1\n\t" \
    "buffer_load_dwordx2 v[132:133], %[t3], %[rs], s46 offen sc1\n\t" \
    "buffer_load_dwordx2 v[134:135], %[t3], %[rs], s47 offen sc1\n\t" \
    "buffer_load_dwordx2 v[136:137], %[t4], %[rs], s46 offen sc1\n\t" \
    "buffer_load_dwordx2 v[138:139], %[t4], %[rs], s47 offen sc1\n\t" \
    ".Lf11_%=:\n\t" \
    ".Lf12_%=:\n\t" \
    "s_waitcnt vmcnt(0)\n\t" \
    "v_max_u32 v80, v120, v121\n\t" \
    "v_max3_u32 v80, v122, v123, v80\n\t" \
    "v_max3_u32 v80, v124, v125, v80\n\t" \
    "v_max3_u32 v80, v126, v127, v80\n\t" \
    "v_max3_u32 v80, v128, v129, v80\n\t" \
    "v_max3_u32 v80, v130, v131, v80\n\t" \
    "v_max3_u32 v80, v132, v133, v80\n\t" \
    "v_max3_u32 v80, v134, v135, v80\n\t" \
    "v_max3_u32 v80, v136, v137, v80\n\t" \
    "v_max3_u32 v80, v138, v139, v80\n\t" \
    "v_cmp_eq_u32 vcc, -1, v80\n\t" \
    "s_cbranch_vccz .Lf13_%=\n\t" \
    "s_cmp_eq_u32 s41, 0\n\t" \
    "s_cbranch_scc1 .Lf13_%=\n\t" \
    "s_sub_u32 s41, s41, 1\n\t" \
    "s_sleep 8\n\t" \
    "s_add_u32 s46, s43, 0x2c00000\n\t" \
    "s_add_u32 s47, s43, 0x1c00000\n\t" \
    "buffer_load_dwordx2 v[120:121], %[t0], %[rs], s46 offen sc1\n\t" \
    "buffer_load_dwordx2 v[122:123], %[t0], %[rs], s47 offen sc1\n\t" \
    "buffer_load_dwordx2 v[124:125], %[t1], %[rs], s46 offen sc1\n\t" \
    "buffer_load_dwordx2 v[126:127], %[t1], %[rs], s47 offen sc1\n\t" \
    "buffer_load_dwordx2 v[128:129], %[t2], %[rs], s46 offen sc1\n\t" \
    "buffer_load_dwordx2 v[130:131], %[t2], %[rs], s47 offen sc1\n\t" \
    "buffer_load_dwordx2 v[132:133], %[t3], %[rs], s46 offen sc1\n\t" \
    "buffer_load_dwordx2 v[134:135], %[t3], %[rs], s47 offen sc1\n\t" \
    "buffer_load_dwordx2 v[136:137], %[t4], %[rs], s46 offen sc1\n\t" \
    "buffer_load_dwordx2 v[138:139], %[t4], %[rs], s47 offen sc1\n\t" \
    "s_waitcnt vmcnt(0)\n\t" \
    "s_branch .Lf12_%=\n\t" \
    ".Lf13_%=:\n\t" \
    "v_lshlrev_b32 v140, 16, v120\n\t" \
    "v_and_b32 v141, 0xffff0000, v120\n\t" \
    "v_lshlrev_b32 v142, 16, v121\n\t" \
    "v_and_b32 v143, 0xffff0000, v121\n\t" \
    "v_lshlrev_b32 v80, 16, v122\n\t" \
    "v_and_b32 v81, 0xffff0000, v122\n\t" \
    "v_lshlrev_b32 v82, 16, v123\n\t" \
    "v_and_b32 v83, 0xffff0000, v123\n\t" \
    "v_add_f32 v140, v140, v80\n\t" \
    "v_add_f32 v141, v141, v81\n\t" \
    "v_add_f32 v142, v142, v82\n\t" \
    "v_add_f32 v143, v143, v83\n\t" \
    "v_lshlrev_b32 v144, 16, v124\n\t" \
    "v_and_b32 v145, 0xffff0000, v124\n\t" \
    "v_lshlrev_b32 v146, 16, v125\n\t" \
    "v_and_b32 v147, 0xffff0000, v125\n\t" \
    "v_lshlrev_b32 v80, 16, v126\n\t" \
    "v_and_b32 v81, 0xffff0000, v126\n\t" \
    "v_lshlrev_b32 v82, 16, v127\n\t" \
    "v_and_b32 v83, 0xffff0000, v127\n\t" \
    "v_add_f32 v144, v144, v80\n\t" \
    "v_add_f32 v145, v145, v81\n\t" \
    "v_add_f32 v146, v146, v82\n\t" \
    "v_add_f32 v147, v147, v83\n\t" \
    "v_lshlrev_b32 v148, 16, v128\n\t" \
    "v_and_b32 v149, 0xffff0000, v128\n\t" \
    "v_lshlrev_b32 v150, 16, v129\n\t" \
    "v_and_b32 v151, 0xffff0000, v129\n\t" \
    "v_lshlrev_b32 v80, 16, v130\n\t" \
    "v_and_b32 v81, 0xffff0000, v130\n\t" \
    "v_lshlrev_b32 v82, 16, v131\n\t" \
    "v_and_b32 v83, 0xffff0000, v131\n\t" \
    "v_add_f32 v148, v148, v80\n\t" \
    "v_add_f32 v149, v149, v81\n\t" \
    "v_add_f32 v150, v150, v82\n\t" \
    "v_add_f32 v151, v151, v83\n\t" \
    "v_lshlrev_b32 v152, 16, v132\n\t" \
    "v_and_b32 v153, 0xffff0000, v132\n\t" \
    "v_lshlrev_b32 v154, 16, v133\n\t" \
    "v_and_b32 v155, 0xffff0000, v133\n\t" \
    "v_lshlrev_b32 v80, 16, v134\n\t" \
    "v_and_b32 v81, 0xffff0000, v134\n\t" \
    "v_lshlrev_b32 v82, 16, v135\n\t" \
    "v_and_b32 v83, 0xffff0000, v135\n\t" \
    "v_add_f32 v152, v152, v80\n\t" \
    "v_add_f32 v153, v153, v81\n\t" \
    "v_add_f32 v154, v154, v82\n\t" \
    "v_add_f32 v155, v155, v83\n\t" \
    "v_lshlrev_b32 v156, 16, v136\n\t" \
    "v_and_b32 v157, 0xffff0000, v136\n\t" \
    "v_lshlrev_b32 v158, 16, v137\n\t" \
    "v_and_b32 v159, 0xffff0000, v137\n\t" \
    "v_lshlrev_b32 v80, 16, v138\n\t" \
    "v_and_b32 v81, 0xffff0000, v138\n\t" \
    "v_lshlrev_b32 v82, 16, v139\n\t" \
    "v_and_b32 v83, 0xffff0000, v139\n\t" \
    "v_add_f32 v156, v156, v80\n\t" \
    "v_add_f32 v157, v157, v81\n\t" \
    "v_add_f32 v158, v158, v82\n\t" \
    "v_add_f32 v159, v159, v83\n\t" \
    "s_mov_b32 s42, s43\n\t" \
    "s_lshr_b32 s48, s40, 1\n\t" \
    "s_add_u32 s48, s48, 1\n\t" \
    "s_mul_i32 s43, s48, 0x14000\n\t" \
    "s_lshr_b32 s44, s48, 4\n\t" \
    "s_mul_i32 s44, s44, 0xc2000\n\t" \
    "s_add_u32 s43, s43, s44\n\t" \
    "s_mul_i32 s49, s40, 20480\n\t" \
    "s_add_u32 s49, s49, %[co]\n\t" \
    "s_add_u32 s44, s40, 1\n\t" \
    "v_mov_b32 v98, s44\n\t" \
    ".Lf14_%=:\n\t" \
    "s_nop 7\n\t" \
    "ds_read_b32 v80, %[fa]\n\t" \
    "s_waitcnt lgkmcnt(0)\n\t" \
    "v_cmp_ge_u32 vcc, s40, v80\n\t" \
    "s_cbranch_vccnz .Lf14_%=\n\t" \
    "ds_read_b128 v[160:163], %[la] offset:0\n\t" \
    "ds_read_b128 v[164:167], %[la] offset:6144\n\t" \
    "ds_read_b128 v[168:171], %[la] offset:12288\n\t" \
    "ds_read_b128 v[172:175], %[la] offset:18432\n\t" \
    "ds_read_b128 v[176:179], %[la] offset:24576\n\t" \
    "ds_read_b128 v[180:183], %[la] offset:1024\n\t" \
    "ds_read_b128 v[184:187], %[la] offset:7168\n\t" \
    "ds_read_b128 v[188:191], %[la] offset:13312\n\t" \
    "ds_read_b128 v[192:195], %[la] offset:19456\n\t" \
    "ds_read_b128 v[196:199], %[la] offset:25600\n\t" \
    "ds_read_b128 v[200:203], %[la] offset:2048\n\t" \
    "ds_read_b128 v[204:207], %[la] offset:8192\n\t" \
    "ds_read_b128 v[208:211], %[la] offset:14336\n\t" \
    "ds_read_b128 v[212:215], %[la] offset:20480\n\t" \
    "ds_read_b128 v[216:219], %[la] offset:26624\n\t" \
    "s_waitcnt lgkmcnt(14)\n\t" \
    "v_pk_add_f32 v[100:101], v[140:141], v[160:161]\n\t" \
    "v_pk_add_f32 v[102:103], v[142:143], v[162:163]\n\t" \
    "ds_read_b128 v[220:223], %[la] offset:3072\n\t" \
    "s_waitcnt lgkmcnt(14)\n\t" \
    "v_pk_add_f32 v[104:105], v[144:145], v[164:165]\n\t" \
    "v_pk_add_f32 v[106:107], v[146:147], v[166:167]\n\t" \
    "ds_read_b128 v[224:227], %[la] offset:9216\n\t" \
    "s_waitcnt lgkmcnt(14)\n\t" \
    "v_pk_add_f32 v[108:109], v[148:149], v[168:169]\n\t" \
    "v_pk_add_f32 v[110:111], v[150:151], v[170:171]\n\t" \
    "ds_read_b128 v[228:231], %[la] offset:15360\n\t" \
    "s_waitcnt lgkmcnt(14)\n\t" \
    "v_pk_add_f32 v[112:113], v[152:153], v[172:173]\n\t" \
    "v_pk_add_f32 v[114:115], v[154:155], v[174:175]\n\t" \
    "ds_read_b128 v[232:235], %[la] offset:21504\n\t" \
    "s_waitcnt lgkmcnt(14)\n\t" \
    "v_pk_add_f32 v[116:117], v[156:157], v[176:177]\n\t" \
    "v_pk_add_f32 v[118:119], v[158:159], v[178:179]\n\t" \
    "ds_read_b128 v[236:239], %[la] offset:27648\n\t" \
    "s_waitcnt lgkmcnt(14)\n\t" \
    "v_pk_add_f32 v[100:101], v[100:101], v[180:181]\n\t" \
    "v_pk_add_f32 v[102:103], v[102:103], v[182:183]\n\t" \
    "ds_read_b128 v[160:163], %[la] offset:4096\n\t" \
    "s_waitcnt lgkmcnt(14)\n\t" \
    "v_pk_add_f32 v[104:105], v[104:105], v[184:185]\n\t" \
    "v_pk_add_f32 v[106:107], v[106:107], v[186:187]\n\t" \
    "ds_read_b128 v[164:167], %[la] offset:10240\n\t" \
    "s_waitcnt lgkmcnt(14)\n\t" \
    "v_pk_add_f32 v[108:109], v[108:109], v[188:189]\n\t" \
    "v_pk_add_f32 v[110:111], v[110:111], v[190:191]\n\t" \
    "ds_read_b128 v[168:171], %[la] offset:16384\n\t" \
    "s_waitcnt lgkmcnt(14)\n\t" \
    "v_pk_add_f32 v[112:113], v[112:113], v[192:193]\n\t" \
    "v_pk_add_f32 v[114:115], v[114:115], v[194:195]\n\t" \
    "ds_read_b128 v[172:175], %[la] offset:22528\n\t" \
    "s_waitcnt lgkmcnt(14)\n\t" \
    "v_pk_add_f32 v[116:117], v[116:117], v[196:197]\n\t" \
    "v_pk_add_f32 v[118:119], v[118:119], v[198:199]\n\t" \
    "ds_read_b128 v[176:179], %[la] offset:28672\n\t" \
    "s_waitcnt lgkmcnt(14)\n\t" \
    "v_pk_add_f32 v[100:101], v[100:101], v[200:201]\n\t" \
    "v_pk_add_f32 v[102:103], v[102:103], v[202:203]\n\t" \
    "ds_read_b128 v[180:183], %[la] offset:5120\n\t" \
    "s_waitcnt lgkmcnt(14)\n\t" \
    "v_pk_add_f32 v[104:105], v[104:105], v[204:205]\n\t" \
    "v_pk_add_f32 v[106:107], v[106:107], v[206:207]\n\t" \
    "ds_read_b128 v[184:187], %[la] offset:11264\n\t" \
    "s_waitcnt lgkmcnt(14)\n\t" \
    "v_pk_add_f32 v[108:109], v[108:109], v[208:209]\n\t" \
    "v_pk_add_f32 v[110:111], v[110:111], v[210:211]\n\t" \
    "ds_read_b128 v[188:191], %[la] offset:17408\n\t" \
    "s_waitcnt lgkmcnt(14)\n\t" \
    "v_pk_add_f32 v[112:113], v[112:113], v[212:213]\n\t" \
    "v_pk_add_f32 v[114:115], v[114:115], v[214:215]\n\t" \
    "ds_read_b128 v[192:195], %[la] offset:23552\n\t" \
    "s_waitcnt lgkmcnt(14)\n\t" \
    "v_pk_add_f32 v[116:117], v[116:117], v[216:217]\n\t" \
    "v_pk_add_f32 v[118:119], v[118:119], v[218:219]\n\t" \
    "ds_read_b128 v[196:199], %[la] offset:29696\n\t" \
    "s_waitcnt lgkmcnt(14)\n\t" \
    "v_pk_add_f32 v[100:101], v[100:101], v[220:221]\n\t" \
    "v_pk_add_f32 v[102:103], v[102:103], v[222:223]\n\t" \
    "s_waitcnt lgkmcnt(13)\n\t" \
    "v_pk_add_f32 v[104:105], v[104:105], v[224:225]\n\t" \
    "v_pk_add_f32 v[106:107], v[106:107], v[226:227]\n\t" \
    "s_waitcnt lgkmcnt(12)\n\t" \
    "v_pk_add_f32 v[108:109], v[108:109], v[228:229]\n\t" \
    "v_pk_add_f32 v[110:111], v[110:111], v[230:231]\n\t" \
    "s_waitcnt lgkmcnt(11)\n\t" \
    "v_pk_add_f32 v[112:113], v[112:113], v[232:233]\n\t" \
    "v_pk_add_f32 v[114:115], v[114:115], v[234:235]\n\t" \
    "s_waitcnt lgkmcnt(10)\n\t" \
    "v_pk_add_f32 v[116:117], v[116:117], v[236:237]\n\t" \
    "v_pk_add_f32 v[118:119], v[118:119], v[238:239]\n\t" \
    "s_waitcnt lgkmcnt(9)\n\t" \
    "v_pk_add_f32 v[100:101], v[100:101], v[160:161]\n\t" \
    "v_pk_add_f32 v[102:103], v[102:103], v[162:163]\n\t" \
    "s_waitcnt lgkmcnt(8)\n\t" \
    "v_pk_add_f32 v[104:105], v[104:105], v[164:165]\n\t" \
    "v_pk_add_f32 v[106:107], v[106:107], v[166:167]\n\t" \
    "s_waitcnt lgkmcnt(7)\n\t" \
    "v_pk_add_f32 v[108:109], v[108:109], v[168:169]\n\t" \
    "v_pk_add_f32 v[110:111], v[110:111], v[170:171]\n\t" \
    "s_waitcnt lgkmcnt(6)\n\t" \
    "v_pk_add_f32 v[112:113], v[112:113], v[172:173]\n\t" \
    "v_pk_add_f32 v[114:115], v[114:115], v[174:175]\n\t" \
    "s_waitcnt lgkmcnt(5)\n\t" \
    "v_pk_add_f32 v[116:117], v[116:117], v[176:177]\n\t" \
    "v_pk_add_f32 v[118:119], v[118:119], v[178:179]\n\t" \
    "s_waitcnt lgkmcnt(4)\n\t" \
    "v_pk_add_f32 v[100:101], v[100:101], v[180:181]\n\t" \
    "v_pk_add_f32 v[102:103], v[102:103], v[182:183]\n\t" \
    "s_waitcnt lgkmcnt(3)\n\t" \
    "v_pk_add_f32 v[104:105], v[104:105], v[184:185]\n\t" \
    "v_pk_add_f32 v[106:107], v[106:107], v[186:187]\n\t" \
    "s_waitcnt lgkmcnt(2)\n\t" \
    "v_pk_add_f32 v[108:109], v[108:109], v[188:189]\n\t" \
    "v_pk_add_f32 v[110:111], v[110:111], v[190:191]\n\t" \
    "s_waitcnt lgkmcnt(1)\n\t" \
    "v_pk_add_f32 v[112:113], v[112:113], v[192:193]\n\t" \
    "v_pk_add_f32 v[114:115], v[114:115], v[194:195]\n\t" \
    "s_waitcnt lgkmcnt(0)\n\t" \
    "v_pk_add_f32 v[116:117], v[116:117], v[196:197]\n\t" \
    "v_pk_add_f32 v[118:119], v[118:119], v[198:199]\n\t" \
    "ds_write_b32 %[fc], v98\n\t" \
    "v_sub_u32 %[la], %[l2], %[la]\n\t" \
    "v_cvt_pk_bf16_f32 v84, v100, v101\n\t" \
    "v_cvt_pk_bf16_f32 v85, v102, v103\n\t" \
    "v_cvt_pk_bf16_f32 v86, v104, v105\n\t" \
    "v_cvt_pk_bf16_f32 v87, v106, v107\n\t" \
    "v_cvt_pk_bf16_f32 v88, v108, v109\n\t" \
    "v_cvt_pk_bf16_f32 v89, v110, v111\n\t" \
    "v_cvt_pk_bf16_f32 v90, v112, v113\n\t" \
    "v_cvt_pk_bf16_f32 v91, v114, v115\n\t" \
    "v_cvt_pk_bf16_f32 v92, v116, v117\n\t" \
    "v_cvt_pk_bf16_f32 v93, v118, v119\n\t" \
    "s_nop 0\n\t" \
    "v_permlane16_swap_b32 v84, v86\n\t" \
    "v_permlane16_swap_b32 v85, v87\n\t" \
    "v_permlane16_swap_b32 v88, v90\n\t" \
    "v_permlane16_swap_b32 v89, v91\n\t" \
    "buffer_store_dwordx4 v[84:87], %[q16], %[ro], s49 offen\n\t" \
    "buffer_store_dwordx4 v[88:91], %[q16], %[ro], s49 offen offset:64\n\t" \
    "buffer_store_dwordx2 v[92:93], %[q8], %[ro], s49 offen offset:128\n\t" \
    "s_add_u32 s46, s43, 0x2c00000\n\t" \
    "s_add_u32 s47, s43, 0x1c00000\n\t" \
    "buffer_load_dwordx2 v[120:121], %[t0], %[rs], s46 offen sc1\n\t" \
    "buffer_load_dwordx2 v[122:123], %[t0], %[rs], s47 offen sc1\n\t" \
    "buffer_load_dwordx2 v[124:125], %[t1], %[rs], s46 offen sc1\n\t" \
    "buffer_load_dwordx2 v[126:127], %[t1], %[rs], s47 offen sc1\n\t" \
    "buffer_load_dwordx2 v[128:129], %[t2], %[rs], s46 offen sc1\n\t" \
    "buffer_load_dwordx2 v[130:131], %[t2], %[rs], s47 offen sc1\n\t" \
    "buffer_load_dwordx2 v[132:133], %[t3], %[rs], s46 offen sc1\n\t" \
    "buffer_load_dwordx2 v[134:135], %[t3], %[rs], s47 offen sc1\n\t" \
    "buffer_load_dwordx2 v[136:137], %[t4], %[rs], s46 offen sc1\n\t" \
    "buffer_load_dwordx2 v[138:139], %[t4], %[rs], s47 offen sc1\n\t" \
    "s_add_u32 s40, s40, 2\n\t" \
    "s_cmp_lt_u32 s40, 128\n\t" \
    "s_cbranch_scc1 .Lf11_%=\n\t" \
    "s_branch .Lend%=\n\t" \
    ".Lend%=:\n\t" \
    "s_waitcnt vmcnt(0) lgkmcnt(0)\n\t" \
    "s_mov_b32 %[bud], s41\n\t"
#define ASM_W1_LOOP \
    "s_mov_b32 s40, 0\n\t" \
    "s_mov_b32 s41, 0x20000\n\t" \
    "s_add_u32 m0, %[wm], 0x0\n\t" \
    "s_add_u32 s44, %[wo], 0x0\n\t" \
    "buffer_load_dwordx4 v176, %[wr], s44 offen nt lds\n\t" \
    "s_add_u32 m0, %[wm], 0x2000\n\t" \
    "s_add_u32 s44, %[wo], 0xa000\n\t" \
    "buffer_load_dwordx4 v176, %[wr], s44 offen nt lds\n\t" \
    "s_add_u32 m0, %[wm], 0x4000\n\t" \
    "s_add_u32 s44, %[wo], 0x14000\n\t" \
    "buffer_load_dwordx4 v176, %[wr], s44 offen nt lds\n\t" \
    "s_add_u32 m0, %[wm], 0x6000\n\t" \
    "s_add_u32 s44, %[wo], 0x1e000\n\t" \
    "buffer_load_dwordx4 v176, %[wr], s44 offen nt lds\n\t" \
    "s_add_u32 m0, %[wm], 0x8000\n\t" \
    "s_add_u32 s44, %[wo], 0x28000\n\t" \
    "buffer_load_dwordx4 v176, %[wr], s44 offen nt lds\n\t" \
    "s_add_u32 m0, %[wm], 0xa000\n\t" \
    "s_add_u32 s44, %[wo], 0x32000\n\t" \
    "buffer_load_dwordx4 v176, %[wr], s44 offen nt lds\n\t" \
    "s_add_u32 m0, %[wm], 0xc000\n\t" \
    "s_add_u32 s44, %[wo], 0x3c000\n\t" \
    "buffer_load_dwordx4 v176, %[wr], s44 offen nt lds\n\t" \
    "s_add_u32 m0, %[wm], 0xe000\n\t" \
    "s_add_u32 s44, %[wo], 0x46000\n\t" \
    "buffer_load_dwordx4 v176, %[wr], s44 offen nt lds\n\t" \
    "s_add_u32 m0, %[wm], 0x10000\n\t" \
    "s_add_u32 s44, %[wo], 0x50000\n\t" \
    "buffer_load_dwordx4 v176, %[wr], s44 offen nt lds\n\t" \
    "s_add_u32 m0, %[wm], 0x12000\n\t" \
    "s_add_u32 s44, %[wo], 0x5a000\n\t" \
    "buffer_load_dwordx4 v176, %[wr], s44 offen nt lds\n\t" \
    "s_add_u32 m0, %[wm], 0x14000\n\t" \
    "s_add_u32 s44, %[wo], 0x64000\n\t" \
    "buffer_load_dwordx4 v176, %[wr], s44 offen nt lds\n\t" \
    "s_add_u32 m0, %[wm], 0x16000\n\t" \
    "s_add_u32 s44, %[wo], 0x6e000\n\t" \
    "buffer_load_dwordx4 v176, %[wr], s44 offen nt lds\n\t" \
    "s_add_u32 m0, %[wm], 0x18000\n\t" \
    "s_add_u32 s44, %[wo], 0x78000\n\t" \
    "buffer_load_dwordx4 v176, %[wr], s44 offen nt lds\n\t" \
    "s_add_u32 m0, %[wm], 0x1a000\n\t" \
    "s_add_u32 s44, %[wo], 0x82000\n\t" \
    "buffer_load_dwordx4 v176, %[wr], s44 offen nt lds\n\t" \
    "s_add_u32 m0, %[wm], 0x1c000\n\t" \
    "s_add_u32 s44, %[wo], 0x8c000\n\t" \
    "buffer_load_dwordx4 v176, %[wr], s44 offen nt lds\n\t" \
    "s_add_u32 m0, %[wm], 0x1e000\n\t" \
    "s_add_u32 s44, %[wo], 0x96000\n\t" \
    "buffer_load_dwordx4 v176, %[wr], s44 offen nt lds\n\t" \
    "s_add_u32 m0, %[wm], 0x20000\n\t" \
    "s_add_u32 s44, %[wo], 0xa0000\n\t" \
    "buffer_load_dwordx4 v176, %[wr], s44 offen nt lds\n\t" \
    "s_add_u32 m0, %[wm], 0x22000\n\t" \
    "s_add_u32 s44, %[wo], 0xaa000\n\t" \
    "buffer_load_dwordx4 v176, %[wr], s44 offen nt lds\n\t" \
    "s_add_u32 m0, %[wm], 0x24000\n\t" \
    "s_add_u32 s44, %[wo], 0xb4000\n\t" \
    "buffer_load_dwordx4 v176, %[wr], s44 offen nt lds\n\t" \
    "s_add_u32 m0, %[wm], 0x26000\n\t" \
    "s_add_u32 s44, %[wo], 0xbe000\n\t" \
    "buffer_load_dwordx4 v176, %[wr], s44 offen nt lds\n\t" \
    "s_waitcnt vmcnt(0) lgkmcnt(0)\n\t" \
    "s_barrier \n\t" \
    "v_add_u32 v178, 0x0, v177\n\t" \
    "ds_read_b64_tr_b16 v[0:1], v178 offset:0\n\t" \
    "ds_read_b64_tr_b16 v[2:3], v178 offset:1024\n\t" \
    "ds_read_b64_tr_b16 v[4:5], v178 offset:8192\n\t" \
    "ds_read_b64_tr_b16 v[6:7], v178 offset:9216\n\t" \
    "ds_read_b64_tr_b16 v[8:9], v178 offset:16384\n\t" \
    "ds_read_b64_tr_b16 v[10:11], v178 offset:17408\n\t" \
    "ds_read_b64_tr_b16 v[12:13], v178 offset:24576\n\t" \
    "ds_read_b64_tr_b16 v[14:15], v178 offset:25600\n\t" \
    "v_add_u32 v178, 0x8000, v177\n\t" \
    "ds_read_b64_tr_b16 v[16:17], v178 offset:0\n\t" \
    "ds_read_b64_tr_b16 v[18:19], v178 offset:1024\n\t" \
    "ds_read_b64_tr_b16 v[20:21], v178 offset:8192\n\t" \
    "ds_read_b64_tr_b16 v[22:23], v178 offset:9216\n\t" \
    "ds_read_b64_tr_b16 v[24:25], v178 offset:16384\n\t" \
    "ds_read_b64_tr_b16 v[26:27], v178 offset:17408\n\t" \
    "ds_read_b64_tr_b16 v[28:29], v178 offset:24576\n\t" \
    "ds_read_b64_tr_b16 v[30:31], v178 offset:25600\n\t" \
    "v_add_u32 v178, 0x10000, v177\n\t" \
    "ds_read_b64_tr_b16 v[32:33], v178 offset:0\n\t" \
    "ds_read_b64_tr_b16 v[34:35], v178 offset:1024\n\t" \
    "ds_read_b64_tr_b16 v[36:37], v178 offset:8192\n\t" \
    "ds_read_b64_tr_b16 v[38:39], v178 offset:9216\n\t" \
    "ds_read_b64_tr_b16 v[40:41], v178 offset:16384\n\t" \
    "ds_read_b64_tr_b16 v[42:43], v178 offset:17408\n\t" \
    "ds_read_b64_tr_b16 v[44:45], v178 offset:24576\n\t" \
    "ds_read_b64_tr_b16 v[46:47], v178 offset:25600\n\t" \
    "v_add_u32 v178, 0x18000, v177\n\t" \
    "ds_read_b64_tr_b16 v[48:49], v178 offset:0\n\t" \
    "ds_read_b64_tr_b16 v[50:51], v178 offset:1024\n\t" \
    "ds_read_b64_tr_b16 v[52:53], v178 offset:8192\n\t" \
    "ds_read_b64_tr_b16 v[54:55], v178 offset:9216\n\t" \
    "ds_read_b64_tr_b16 v[56:57], v178 offset:16384\n\t" \
    "ds_read_b64_tr_b16 v[58:59], v178 offset:17408\n\t" \
    "ds_read_b64_tr_b16 v[60:61], v178 offset:24576\n\t" \
    "ds_read_b64_tr_b16 v[62:63], v178 offset:25600\n\t" \
    "v_add_u32 v178, 0x20000, v177\n\t" \
    "ds_read_b64_tr_b16 v[64:65], v178 offset:0\n\t" \
    "ds_read_b64_tr_b16 v[66:67], v178 offset:1024\n\t" \
    "ds_read_b64_tr_b16 v[68:69], v178 offset:8192\n\t" \
    "ds_read_b64_tr_b16 v[70:71], v178 offset:9216\n\t" \
    "ds_read_b64_tr_b16 v[72:73], v178 offset:16384\n\t" \
    "ds_read_b64_tr_b16 v[74:75], v178 offset:17408\n\t" \
    "ds_read_b64_tr_b16 v[76:77], v178 offset:24576\n\t" \
    "ds_read_b64_tr_b16 v[78:79], v178 offset:25600\n\t" \
    "v_mov_b32 v228, 0x3f803f80\n\t" \
    "v_mov_b32 v229, 0x3f803f80\n\t" \
    "v_mov_b32 v230, 0x3f803f80\n\t" \
    "v_mov_b32 v231, 0x3f803f80\n\t" \
    "v_mov_b64 v[216:217], 0\n\t" \
    "v_mov_b32 v219, 0\n\t" \
    "v_bfe_u32 v83, v253, 2, 5\n\t" \
    "v_add_u32 v83, 0x7000, v83\n\t" \
    "v_mov_b32 v218, 0\n\t" \
    "s_waitcnt lgkmcnt(0)\n\t" \
    "s_barrier \n\t" \
    "ds_write_b32 v251, v228 offset:72\n\t" \
    "ds_write_b32 v251, v228 offset:2120\n\t" \
    "v_mfma_f32_16x16x32_bf16 v[160:163], v[0:3], v[228:231], 0\n\t" \
    "v_mfma_f32_16x16x32_bf16 v[160:163], v[4:7], v[228:231], v[160:163]\n\t" \
    "v_mfma_f32_16x16x32_bf16 v[160:163], v[8:11], v[228:231], v[160:163]\n\t" \
    "v_mfma_f32_16x16x32_bf16 v[160:163], v[12:15], v[228:231], v[160:163]\n\t" \
    "v_mfma_f32_16x16x32_bf16 v[160:163], v[16:19], v[228:231], v[160:163]\n\t" \
    "v_mfma_f32_16x16x32_bf16 v[160:163], v[20:23], v[228:231], v[160:163]\n\t" \
    "v_mfma_f32_16x16x32_bf16 v[160:163], v[24:27], v[228:231], v[160:163]\n\t" \
    "v_mfma_f32_16x16x32_bf16 v[160:163], v[28:31], v[228:231], v[160:163]\n\t" \
    "v_mfma_f32_16x16x32_bf16 v[160:163], v[32:35], v[228:231], v[160:163]\n\t" \
    "v_mfma_f32_16x16x32_bf16 v[160:163], v[36:39], v[228:231], v[160:163]\n\t" \
    "v_mfma_f32_16x16x32_bf16 v[160:163], v[40:43], v[228:231], v[160:163]\n\t" \
    "v_mfma_f32_16x16x32_bf16 v[160:163], v[44:47], v[228:231], v[160:163]\n\t" \
    "v_mfma_f32_16x16x32_bf16 v[160:163], v[48:51], v[228:231], v[160:163]\n\t" \
    "v_mfma_f32_16x16x32_bf16 v[160:163], v[52:55], v[228:231], v[160:163]\n\t" \
    "v_mfma_f32_16x16x32_bf16 v[160:163], v[56:59], v[228:231], v[160:163]\n\t" \
    "v_mfma_f32_16x16x32_bf16 v[160:163], v[60:63], v[228:231], v[160:163]\n\t" \
    "v_mfma_f32_16x16x32_bf16 v[160:163], v[64:67], v[228:231], v[160:163]\n\t" \
    "v_mfma_f32_16x16x32_bf16 v[160:163], v[68:71], v[228:231], v[160:163]\n\t" \
    "v_mfma_f32_16x16x32_bf16 v[160:163], v[72:75], v[228:231], v[160:163]\n\t" \
    "v_mfma_f32_16x16x32_bf16 v[160:163], v[76:79], v[228:231], v[160:163]\n\t" \
    "s_mov_b32 s52, 0x22222222\n\t" \
    "s_mov_b32 s53, 0x22222222\n\t" \
    "s_mov_b32 s54, 0x44444444\n\t" \
    "s_mov_b32 s55, 0x44444444\n\t" \
    "s_mov_b32 s56, 0x88888888\n\t" \
    "s_mov_b32 s57, 0x88888888\n\t" \
    "ds_write_b32 v83, v218\n\t" \
    "s_waitcnt lgkmcnt(0)\n\t" \
    "s_barrier \n\t" \
    ".Lw11_%=:\n\t" \
    "v_mov_b32 v248, 0\n\t" \
    "buffer_load_dword v248, v248, %[rs], %[go] offen sc1\n\t" \
    "s_waitcnt vmcnt(0)\n\t" \
    "v_cmp_ne_u32 vcc, 0, v248\n\t" \
    "s_cbranch_vccnz .Lw12_%=\n\t" \
    "s_cmp_eq_u32 s41, 0\n\t" \
    "s_cbranch_scc1 .Lw12_%=\n\t" \
    "s_sub_u32 s41, s41, 1\n\t" \
    "s_sleep 16\n\t" \
    "s_branch .Lw11_%=\n\t" \
    ".Lw12_%=:\n\t" \
    "s_sub_i32 s60, 0, %[ng]\n\t" \
    "s_sub_i32 s61, 1, %[ng]\n\t" \
    "s_add_i32 s62, s60, 6\n\t" \
    "s_add_i32 s63, s61, 6\n\t" \
    "s_mul_i32 s42, s61, 127\n\t" \
    "s_mul_i32 s44, %[ng], 0x733000\n\t" \
    "s_add_u32 s44, s44, 0x2400000\n\t" \
    "v_cmp_ne_u32 s[58:59], %[ng], v255\n\t" \
    "s_nop 1\n\t" \
    "s_mov_b64 exec, s[58:59]\n\t" \
    "buffer_store_dwordx2 v[216:217], v254, %[rs], s44 offen sc1\n\t" \
    "s_mov_b64 exec, -1\n\t" \
    "s_lshr_b32 s48, %[wave], 2\n\t" \
    "s_mul_i32 s48, s48, 0x3000\n\t" \
    "s_add_u32 s48, s48, %[pi]\n\t" \
    "s_mov_b32 s43, 0\n\t" \
    "s_add_u32 s46, s43, s48\n\t" \
    "s_add_u32 s47, s46, 0x0\n\t" \
    "buffer_load_dwordx4 v[208:211], v253, %[rs], s47 offen sc1\n\t" \
    "s_add_u32 s47, s46, 0x8000\n\t" \
    "buffer_load_dwordx4 v[212:215], v253, %[rs], s47 offen sc1\n\t" \
    "s_cmp_gt_u32 %[wave], 3\n\t" \
    "s_cbranch_scc1 .Lw13_%=\n\t" \
    "s_add_u32 s47, s46, 0x10000\n\t" \
    "buffer_load_dwordx4 v[216:219], v253, %[rs], s47 offen sc1\n\t" \
    ".Lw13_%=:\n\t" \
    "s_waitcnt vmcnt(0)\n\t" \
    "v_lshrrev_b32 v82, 1, v253\n\t" \
    ".Lw14_%=:\n\t" \
    "s_add_i32 s44, s40, s60\n\t" \
    "s_max_i32 s44, s44, 0\n\t" \
    "s_lshr_b32 s45, s44, 1\n\t" \
    "s_and_b32 s44, s44, 1\n\t" \
    "s_lshl_b32 s44, s44, 12\n\t" \
    "s_mul_i32 s49, s45, 0x14000\n\t" \
    "s_add_u32 s49, s49, s44\n\t" \
    "s_lshr_b32 s45, s45, 4\n\t" \
    "s_mul_i32 s45, s45, 0xc2000\n\t" \
    "s_add_u32 s49, s49, s45\n\t" \
    "s_add_i32 s44, s40, s61\n\t" \
    "s_max_i32 s44, s44, 0\n\t" \
    "s_lshr_b32 s45, s44, 1\n\t" \
    "s_and_b32 s44, s44, 1\n\t" \
    "s_lshl_b32 s44, s44, 12\n\t" \
    "s_mul_i32 s50, s45, 0x14000\n\t" \
    "s_add_u32 s50, s50, s44\n\t" \
    "s_lshr_b32 s45, s45, 4\n\t" \
    "s_mul_i32 s45, s45, 0xc2000\n\t" \
    "s_add_u32 s50, s50, s45\n\t" \
    "s_sub_u32 s50, s50, s49\n\t" \
    "s_add_u32 s49, s49, 0x2400000\n\t" \
    "v_mad_u32_u24 v80, v255, s50, v254\n\t" \
    "s_lshr_b32 s44, s40, 6\n\t" \
    "s_lshl_b32 s44, s44, 14\n\t" \
    "s_and_b32 s45, s40, 63\n\t" \
    "s_lshl_b32 s45, s45, 7\n\t" \
    "s_add_u32 s44, s44, s45\n\t" \
    "s_add_u32 s50, s44, 0x4d0000\n\t" \
    "s_add_i32 s44, s40, 1\n\t" \
    "s_max_i32 s44, s44, 0\n\t" \
    "s_lshr_b32 s45, s44, 1\n\t" \
    "s_and_b32 s44, s44, 1\n\t" \
    "s_lshl_b32 s44, s44, 12\n\t" \
    "s_mul_i32 s43, s45, 0x14000\n\t" \
    "s_add_u32 s43, s43, s44\n\t" \
    "s_lshr_b32 s45, s45, 4\n\t" \
    "s_mul_i32 s45, s45, 0xc2000\n\t" \
    "s_add_u32 s43, s43, s45\n\t" \
    "s_setprio 2\n\t" \
    "v_max_u32 v248, v208, v209\n\t" \
    "v_max3_u32 v248, v210, v211, v248\n\t" \
    "v_max3_u32 v248, v212, v213, v248\n\t" \
    "v_max3_u32 v248, v214, v215, v248\n\t" \
    "v_max3_u32 v248, v216, v217, v248\n\t" \
    "v_max3_u32 v248, v218, v219, v248\n\t" \
    "v_cmp_eq_u32 vcc, -1, v248\n\t" \
    "s_cbranch_vccz .Lw16_%=\n\t" \
    "s_and_b32 s44, s40, 1\n\t" \
    "s_cmp_eq_u32 s44, 1\n\t" \
    "s_cbranch_scc1 .Lw15_%=\n\t" \
    "s_sleep 10\n\t" \
    ".Lw15_%=:\n\t" \
    "v_max_u32 v248, v208, v209\n\t" \
    "v_max3_u32 v248, v210, v211, v248\n\t" \
    "v_cmp_eq_u32 vcc, -1, v248\n\t" \
    "s_cbranch_vccz .Lw18_%=\n\t" \
    "s_add_u32 s47, s46, 0x0\n\t" \
    "buffer_load_dwordx4 v[208:211], v253, %[rs], s47 offen sc1\n\t" \
    ".Lw18_%=:\n\t" \
    "v_max_u32 v248, v212, v213\n\t" \
    "v_max3_u32 v248, v214, v215, v248\n\t" \
    "v_cmp_eq_u32 vcc, -1, v248\n\t" \
    "s_cbranch_vccz .Lw19_%=\n\t" \
    "s_add_u32 s47, s46, 0x8000\n\t" \
    "buffer_load_dwordx4 v[212:215], v253, %[rs], s47 offen sc1\n\t" \
    ".Lw19_%=:\n\t" \
    "v_max_u32 v248, v216, v217\n\t" \
    "v_max3_u32 v248, v218, v219, v248\n\t" \
    "v_cmp_eq_u32 vcc, -1, v248\n\t" \
    "s_cbranch_vccz .Lw110_%=\n\t" \
    "s_add_u32 s47, s46, 0x10000\n\t" \
    "buffer_load_dwordx4 v[216:219], v253, %[rs], s47 offen sc1\n\t" \
    ".Lw110_%=:\n\t" \
    "s_waitcnt vmcnt(0)\n\t" \
    "v_max_u32 v248, v208, v209\n\t" \
    "v_max3_u32 v248, v210, v211, v248\n\t" \
    "v_max3_u32 v248, v212, v213, v248\n\t" \
    "v_max3_u32 v248, v214, v215, v248\n\t" \
    "v_max3_u32 v248, v216, v217, v248\n\t" \
    "v_max3_u32 v248, v218, v219, v248\n\t" \
    "v_cmp_eq_u32 vcc, -1, v248\n\t" \
    "s_cbranch_vccz .Lw16_%=\n\t" \
    "s_cmp_eq_u32 s41, 0\n\t" \
    "s_cbranch_scc1 .Lw16_%=\n\t" \
    "s_sub_u32 s41, s41, 1\n\t" \
    "s_branch .Lw15_%=\n\t" \
    ".Lw16_%=:\n\t" \
    "ds_write_b128 v250, v[208:211] offset:0\n\t" \
    "v_mfma_f32_16x16x32_bf16 v[236:239], v[208:211], v[208:211], 0\n\t" \
    "v_mfma_f32_16x16x32_bf16 v[232:235], v[228:231], v[208:211], 0\n\t" \
    "ds_write_b128 v250, v[212:215] offset:8192\n\t" \
    "v_mfma_f32_16x16x32_bf16 v[236:239], v[212:215], v[212:215], v[236:239]\n\t" \
    "v_mfma_f32_16x16x32_bf16 v[232:235], v[228:231], v[212:215], v[232:235]\n\t" \
    "s_cmp_gt_u32 %[wave], 3\n\t" \
    "s_cbranch_scc1 .Lw111_%=\n\t" \
    "ds_write_b128 v250, v[216:219] offset:16384\n\t" \
    ".Lw111_%=:\n\t" \
    "v_mfma_f32_16x16x32_bf16 v[236:239], v[216:219], v[216:219], v[236:239]\n\t" \
    "v_mfma_f32_16x16x32_bf16 v[232:235], v[228:231], v[216:219], v[232:235]\n\t" \
    "s_add_u32 s46, s43, s48\n\t" \
    "s_nop 4\n\t" \
    "v_cndmask_b32 v236, v236, v237, s[52:53]\n\t" \
    "v_cndmask_b32 v236, v236, v238, s[54:55]\n\t" \
    "v_cndmask_b32 v236, v236, v239, s[56:57]\n\t" \
    "s_mov_b32 exec_lo, 0xf0000f\n\t" \
    "s_mov_b32 exec_hi, 0xf0000f00\n\t" \
    "ds_write2_b32 v252, v232, v236 offset1:1\n\t" \
    "s_mov_b64 exec, -1\n\t" \
    "s_waitcnt lgkmcnt(0)\n\t" \
    "s_barrier \n\t" \
    "s_setprio 0\n\t" \
    "s_cmp_lg_u32 %[wave], 4\n\t" \
    "s_cbranch_scc1 .Lw112_%=\n\t" \
    "s_setprio 1\n\t" \
    "ds_read_b128 v[232:235], v251 offset:0\n\t" \
    "ds_read_b128 v[236:239], v251 offset:16\n\t" \
    "ds_read_b128 v[240:243], v251 offset:32\n\t" \
    "ds_read_b128 v[244:247], v251 offset:48\n\t" \
    "s_waitcnt lgkmcnt(0)\n\t" \
    "v_add_f32 v232, v232, v240\n\t" \
    "v_add_f32 v233, v233, v241\n\t" \
    "v_add_f32 v234, v234, v242\n\t" \
    "v_add_f32 v235, v235, v243\n\t" \
    "v_add_f32 v236, v236, v244\n\t" \
    "v_add_f32 v237, v237, v245\n\t" \
    "v_add_f32 v238, v238, v246\n\t" \
    "v_add_f32 v239, v239, v247\n\t" \
    "v_add_f32 v232, v232, v236\n\t" \
    "v_add_f32 v233, v233, v237\n\t" \
    "v_add_f32 v234, v234, v238\n\t" \
    "v_add_f32 v235, v235, v239\n\t" \
    "v_add_f32 v232, v232, v234\n\t" \
    "v_add_f32 v233, v233, v235\n\t" \
    "v_mul_f32 v232, 0x3acccccd, v232\n\t" \
    "v_mul_f32 v233, 0x3acccccd, v233\n\t" \
    "v_fma_f32 v233, -v232, v232, v233\n\t" \
    "v_mov_b32 v234, v232\n\t" \
    "v_max_f32 v233, 0, v233\n\t" \
    "v_cvt_pk_bf16_f32 v234, v234, v234\n\t" \
    "v_and_b32 v234, 0xffff0000, v234\n\t" \
    "v_mov_b32 v236, v233\n\t" \
    "v_cvt_pk_bf16_f32 v236, v236, v236\n\t" \
    "v_and_b32 v236, 0xffff0000, v236\n\t" \
    "v_add_f32 v236, 0x3727c5ac, v236\n\t" \
    "v_cvt_pk_bf16_f32 v236, v236, v236\n\t" \
    "v_and_b32 v236, 0xffff0000, v236\n\t" \
    "v_sqrt_f32 v236, v236\n\t" \
    "s_nop 0\n\t" \
    "v_cvt_pk_bf16_f32 v236, v236, v236\n\t" \
    "v_and_b32 v236, 0xffff0000, v236\n\t" \
    "v_rcp_f32 v235, v236\n\t" \
    "s_nop 0\n\t" \
    "v_sub_f32 v232, v232, v234\n\t" \
    "v_mul_f32 v233, v233, v235\n\t" \
    "v_mul_f32 v232, v232, v235\n\t" \
    "v_mul_f32 v233, v233, v235\n\t" \
    "v_cvt_pk_bf16_f32 v232, v232, v232\n\t" \
    "v_and_b32 v232, 0xffff0000, v232\n\t" \
    "v_cvt_pk_bf16_f32 v233, v233, v233\n\t" \
    "v_and_b32 v233, 0xffff0000, v233\n\t" \
    "v_add_f32 v233, 0x3727c5ac, v233\n\t" \
    "v_cvt_pk_bf16_f32 v233, v233, v233\n\t" \
    "v_and_b32 v233, 0xffff0000, v233\n\t" \
    "v_sqrt_f32 v233, v233\n\t" \
    "s_nop 0\n\t" \
    "v_cvt_pk_bf16_f32 v233, v233, v233\n\t" \
    "v_and_b32 v233, 0xffff0000, v233\n\t" \
    "v_rcp_f32 v233, v233\n\t" \
    "v_fma_f32 v232, v232, v236, v234\n\t" \
    "v_mul_f32 v237, v235, v233\n\t" \
    "v_mul_f32 v238, -v232, v237\n\t" \
    "v_min_u32 v234, -2, v234\n\t" \
    "v_min_u32 v235, -2, v235\n\t" \
    "v_mov_b32 v239, s40\n\t" \
    "ds_write2_b32 v251, v237, v238 offset0:16 offset1:17\n\t" \
    "ds_write_b32 v251, v239 offset:72\n\t" \
    "s_setprio 0\n\t" \
    "s_cmp_eq_u32 %[ss], 0\n\t" \
    "s_cbranch_scc1 .Lw113_%=\n\t" \
    "s_mov_b64 exec, 0xffff\n\t" \
    "buffer_store_dwordx2 v[234:235], v82, %[rs], s50 offen sc1\n\t" \
    "s_mov_b64 exec, -1\n\t" \
    ".Lw113_%=:\n\t" \
    ".Lw112_%=:\n\t" \
    "ds_read_b128 v[176:179], v249 offset:0\n\t" \
    "ds_read_b128 v[180:183], v249 offset:1024\n\t" \
    "ds_read_b128 v[184:187], v249 offset:2048\n\t" \
    "ds_read_b128 v[188:191], v249 offset:3072\n\t" \
    "ds_read_b128 v[192:195], v249 offset:4096\n\t" \
    "ds_read_b128 v[196:199], v249 offset:5120\n\t" \
    "ds_read_b128 v[200:203], v249 offset:6144\n\t" \
    "s_waitcnt lgkmcnt(6)\n\t" \
    "v_mfma_f32_16x16x32_bf16 v[168:171], v[0:3], v[176:179], 0\n\t" \
    "ds_read_b128 v[204:207], v249 offset:7168\n\t" \
    "s_waitcnt lgkmcnt(6)\n\t" \
    "v_mfma_f32_16x16x32_bf16 v[168:171], v[4:7], v[180:183], v[168:171]\n\t" \
    "ds_read_b128 v[176:179], v249 offset:8192\n\t" \
    "s_waitcnt lgkmcnt(6)\n\t" \
    "v_mfma_f32_16x16x32_bf16 v[168:171], v[8:11], v[184:187], v[168:171]\n\t" \
    "ds_read_b128 v[180:183], v249 offset:9216\n\t" \
    "s_waitcnt lgkmcnt(6)\n\t" \
    "v_mfma_f32_16x16x32_bf16 v[168:171], v[12:15], v[188:191], v[168:171]\n\t" \
    "ds_read_b128 v[184:187], v249 offset:10240\n\t" \
    "s_add_u32 s47, s46, 0x0\n\t" \
    "buffer_load_dwordx4 v[208:211], v253, %[rs], s47 offen sc1\n\t" \
    "s_add_u32 s47, s46, 0x8000\n\t" \
    "buffer_load_dwordx4 v[212:215], v253, %[rs], s47 offen sc1\n\t" \
    "s_cmp_gt_u32 %[wave], 3\n\t" \
    "s_cbranch_scc1 .Lw114_%=\n\t" \
    "s_add_u32 s47, s46, 0x10000\n\t" \
    "buffer_load_dwordx4 v[216:219], v253, %[rs], s47 offen sc1\n\t" \
    ".Lw114_%=:\n\t" \
    "s_waitcnt lgkmcnt(6)\n\t" \
    "v_mfma_f32_16x16x32_bf16 v[168:171], v[16:19], v[192:195], v[168:171]\n\t" \
    "ds_read_b128 v[188:191], v249 offset:11264\n\t" \
    "s_waitcnt lgkmcnt(6)\n\t" \
    "v_mfma_f32_16x16x32_bf16 v[168:171], v[20:23], v[196:199], v[168:171]\n\t" \
    "ds_read_b128 v[192:195], v249 offset:12288\n\t" \
    "s_waitcnt lgkmcnt(6)\n\t" \
    "v_mfma_f32_16x16x32_bf16 v[168:171], v[24:27], v[200:203], v[168:171]\n\t" \
    "ds_read_b128 v[196:199], v249 offset:13312\n\t" \
    "s_waitcnt lgkmcnt(6)\n\t" \
    "v_mfma_f32_16x16x32_bf16 v[168:171], v[28:31], v[204:207], v[168:171]\n\t" \
    "ds_read_b128 v[200:203], v249 offset:14336\n\t" \
    "s_waitcnt lgkmcnt(6)\n\t" \
    "v_mfma_f32_16x16x32_bf16 v[168:171], v[32:35], v[176:179], v[168:171]\n\t" \
    "ds_read_b128 v[204:207], v249 offset:15360\n\t" \
    "s_waitcnt lgkmcnt(6)\n\t" \
    "v_mfma_f32_16x16x32_bf16 v[168:171], v[36:39], v[180:183], v[168:171]\n\t" \
    "ds_read_b128 v[176:179], v249 offset:16384\n\t" \
    "s_waitcnt lgkmcnt(6)\n\t" \
    "v_mfma_f32_16x16x32_bf16 v[168:171], v[40:43], v[184:187], v[168:171]\n\t" \
    "ds_read_b128 v[180:183], v249 offset:17408\n\t" \
    "s_waitcnt lgkmcnt(6)\n\t" \
    "v_mfma_f32_16x16x32_bf16 v[168:171], v[44:47], v[188:191], v[168:171]\n\t" \
    "ds_read_b128 v[184:187], v249 offset:18432\n\t" \
    "s_waitcnt lgkmcnt(6)\n\t" \
    "v_mfma_f32_16x16x32_bf16 v[168:171], v[48:51], v[192:195], v[168:171]\n\t" \
    "ds_read_b128 v[188:191], v249 offset:19456\n\t" \
    "s_waitcnt lgkmcnt(6)\n\t" \
    "v_mfma_f32_16x16x32_bf16 v[168:171], v[52:55], v[196:199], v[168:171]\n\t" \
    "ds_read_b32 v236, v251 offset:72\n\t" \
    "ds_read_b64 v[238:239], v251 offset:64\n\t" \
    "s_waitcnt lgkmcnt(7)\n\t" \
    "v_mfma_f32_16x16x32_bf16 v[168:171], v[56:59], v[200:203], v[168:171]\n\t" \
    "s_waitcnt lgkmcnt(6)\n\t" \
    "v_mfma_f32_16x16x32_bf16 v[168:171], v[60:63], v[204:207], v[168:171]\n\t" \
    "s_waitcnt lgkmcnt(5)\n\t" \
    "v_mfma_f32_16x16x32_bf16 v[168:171], v[64:67], v[176:179], v[168:171]\n\t" \
    "s_waitcnt lgkmcnt(4)\n\t" \
    "v_mfma_f32_16x16x32_bf16 v[168:171], v[68:71], v[180:183], v[168:171]\n\t" \
    ".Lw115_%=:\n\t" \
    "s_waitcnt lgkmcnt(0)\n\t" \
    "v_cmp_ne_u32 vcc, s40, v236\n\t" \
    "s_cbranch_vccz .Lw116_%=\n\t" \
    "ds_read_b32 v236, v251 offset:72\n\t" \
    "ds_read_b64 v[238:239], v251 offset:64\n\t" \
    "s_branch .Lw115_%=\n\t" \
    ".Lw116_%=:\n\t" \
    "s_waitcnt lgkmcnt(0)\n\t" \
    "v_mul_f32 v240, v160, v239\n\t" \
    "v_mul_f32 v241, v161, v239\n\t" \
    "v_mfma_f32_16x16x32_bf16 v[168:171], v[72:75], v[184:187], v[168:171]\n\t" \
    "v_mul_f32 v242, v162, v239\n\t" \
    "v_mul_f32 v243, v163, v239\n\t" \
    "v_mfma_f32_16x16x32_bf16 v[168:171], v[76:79], v[188:191], v[168:171]\n\t" \
    "v_mov_b32 v237, 0xc0135761\n\t" \
    "s_nop 6\n\t" \
    "v_fma_f32 v168, v168, v238, v240\n\t" \
    "v_fma_f32 v169, v169, v238, v241\n\t" \
    "v_fma_f32 v170, v170, v238, v242\n\t" \
    "v_fma_f32 v171, v171, v238, v243\n\t" \
    "v_mul_f32 v176, v168, v168\n\t" \
    "v_mul_f32 v177, v169, v169\n\t" \
    "v_mul_f32 v178, v170, v170\n\t" \
    "v_mul_f32 v179, v171, v171\n\t" \
    "v_fmamk_f32 v176, v176, 0xbdd2d3e7, v237\n\t" \
    "v_fmamk_f32 v177, v177, 0xbdd2d3e7, v237\n\t" \
    "v_fmamk_f32 v178, v178, 0xbdd2d3e7, v237\n\t" \
    "v_fmamk_f32 v179, v179, 0xbdd2d3e7, v237\n\t" \
    "v_mul_f32 v176, v168, v176\n\t" \
    "v_mul_f32 v177, v169, v177\n\t" \
    "v_mul_f32 v178, v170, v178\n\t" \
    "v_mul_f32 v179, v171, v179\n\t" \
    "v_exp_f32 v176, v176\n\t" \
    "v_exp_f32 v177, v177\n\t" \
    "v_exp_f32 v178, v178\n\t" \
    "v_exp_f32 v179, v179\n\t" \
    "v_add_f32 v176, 1.0, v176\n\t" \
    "v_add_f32 v177, 1.0, v177\n\t" \
    "v_add_f32 v178, 1.0, v178\n\t" \
    "v_add_f32 v179, 1.0, v179\n\t" \
    "v_rcp_f32 v176, v176\n\t" \
    "v_rcp_f32 v177, v177\n\t" \
    "v_rcp_f32 v178, v178\n\t" \
    "v_rcp_f32 v179, v179\n\t" \
    "v_mul_f32 v168, v168, v176\n\t" \
    "v_mul_f32 v169, v169, v177\n\t" \
    "v_mul_f32 v170, v170, v178\n\t" \
    "v_mul_f32 v171, v171, v179\n\t" \
    "s_waitcnt vmcnt(0)\n\t" \
    "v_cvt_pk_bf16_f32 v184, v168, v169\n\t" \
    "v_cvt_pk_bf16_f32 v185, v170, v171\n\t" \
    "v_min_u32 v184, -2, v184\n\t" \
    "v_min_u32 v185, -2, v185\n\t" \
    "s_cmp_eq_u32 s40, s42\n\t" \
    "s_cselect_b64 s[44:45], s[58:59], 0\n\t" \
    "s_andn2_b64 exec, -1, s[44:45]\n\t" \
    "buffer_store_dwordx2 v[184:185], v80, %[rs], s49 offen sc1\n\t" \
    "s_mov_b64 exec, -1\n\t" \
    "s_cmp_ge_u32 s40, 122\n\t" \
    "s_cbranch_scc1 .Lw117_%=\n\t" \
    "s_add_i32 s44, s40, s62\n\t" \
    "s_max_i32 s44, s44, 0\n\t" \
    "s_lshr_b32 s45, s44, 1\n\t" \
    "s_and_b32 s44, s44, 1\n\t" \
    "s_lshl_b32 s44, s44, 12\n\t" \
    "s_mul_i32 s51, s45, 0x14000\n\t" \
    "s_add_u32 s51, s51, s44\n\t" \
    "s_lshr_b32 s45, s45, 4\n\t" \
    "s_mul_i32 s45, s45, 0xc2000\n\t" \
    "s_add_u32 s51, s51, s45\n\t" \
    "s_add_i32 s44, s40, s63\n\t" \
    "s_max_i32 s44, s44, 0\n\t" \
    "s_lshr_b32 s45, s44, 1\n\t" \
    "s_and_b32 s44, s44, 1\n\t" \
    "s_lshl_b32 s44, s44, 12\n\t" \
    "s_mul_i32 s50, s45, 0x14000\n\t" \
    "s_add_u32 s50, s50, s44\n\t" \
    "s_lshr_b32 s45, s45, 4\n\t" \
    "s_mul_i32 s45, s45, 0xc2000\n\t" \
    "s_add_u32 s50, s50, s45\n\t" \
    "s_sub_u32 s50, s50, s51\n\t" \
    "s_add_u32 s51, s51, 0x2400000\n\t" \
    "v_mad_u32_u24 v81, v255, s50, v254\n\t" \
    "v_mov_b64 v[188:189], -1\n\t" \
    "s_add_u32 s44, s40, 6\n\t" \
    "s_cmp_eq_u32 s44, s42\n\t" \
    "s_cselect_b64 s[44:45], s[58:59], 0\n\t" \
    "s_andn2_b64 exec, -1, s[44:45]\n\t" \
    "buffer_store_dwordx2 v[188:189], v81, %[rs], s51 offen sc1\n\t" \
    "s_mov_b64 exec, -1\n\t" \
    ".Lw117_%=:\n\t" \
    "v_xor_b32 v249, 0x8000, v249\n\t" \
    "v_xor_b32 v250, 0x8000, v250\n\t" \
    "v_xor_b32 v251, 0x800, v251\n\t" \
    "v_xor_b32 v252, 0x800, v252\n\t" \
    "s_add_u32 s40, s40, 1\n\t" \
    "s_cmp_lg_u32 s40, 128\n\t" \
    "s_cbranch_scc1 .Lw14_%=\n\t" \
    "s_branch .Lend%=\n\t" \
    ".Lend%=:\n\t" \
    "s_waitcnt vmcnt(0) lgkmcnt(0)\n\t" \
    "s_mov_b32 %[bud], s41\n\t"
#define ASM_W2_LOOP \
    "s_mov_b32 s40, 0\n\t" \
    "s_mov_b32 s41, 0x20000\n\t" \
    "s_add_u32 m0, %[wm], 0x0\n\t" \
    "s_add_u32 s44, %[wo], 0x0\n\t" \
    "buffer_load_dwordx4 v176, %[wr], s44 offen nt lds\n\t" \
    "s_add_u32 m0, %[wm], 0x2000\n\t" \
    "s_add_u32 s44, %[wo], 0xa000\n\t" \
    "buffer_load_dwordx4 v176, %[wr], s44 offen nt lds\n\t" \
    "s_add_u32 m0, %[wm], 0x4000\n\t" \
    "s_add_u32 s44, %[wo], 0x14000\n\t" \
    "buffer_load_dwordx4 v176, %[wr], s44 offen nt lds\n\t" \
    "s_add_u32 m0, %[wm], 0x6000\n\t" \
    "s_add_u32 s44, %[wo], 0x1e000\n\t" \
    "buffer_load_dwordx4 v176, %[wr], s44 offen nt lds\n\t" \
    "s_add_u32 m0, %[wm], 0x8000\n\t" \
    "s_add_u32 s44, %[wo], 0x28000\n\t" \
    "buffer_load_dwordx4 v176, %[wr], s44 offen nt lds\n\t" \
    "s_add_u32 m0, %[wm], 0xa000\n\t" \
    "s_add_u32 s44, %[wo], 0x32000\n\t" \
    "buffer_load_dwordx4 v176, %[wr], s44 offen nt lds\n\t" \
    "s_add_u32 m0, %[wm], 0xc000\n\t" \
    "s_add_u32 s44, %[wo], 0x3c000\n\t" \
    "buffer_load_dwordx4 v176, %[wr], s44 offen nt lds\n\t" \
    "s_add_u32 m0, %[wm], 0xe000\n\t" \
    "s_add_u32 s44, %[wo], 0x46000\n\t" \
    "buffer_load_dwordx4 v176, %[wr], s44 offen nt lds\n\t" \
    "s_add_u32 m0, %[wm], 0x10000\n\t" \
    "s_add_u32 s44, %[wo], 0x50000\n\t" \
    "buffer_load_dwordx4 v176, %[wr], s44 offen nt lds\n\t" \
    "s_add_u32 m0, %[wm], 0x12000\n\t" \
    "s_add_u32 s44, %[wo], 0x5a000\n\t" \
    "buffer_load_dwordx4 v176, %[wr], s44 offen nt lds\n\t" \
    "s_add_u32 m0, %[wm], 0x14000\n\t" \
    "s_add_u32 s44, %[wo], 0x64000\n\t" \
    "buffer_load_dwordx4 v176, %[wr], s44 offen nt lds\n\t" \
    "s_add_u32 m0, %[wm], 0x16000\n\t" \
    "s_add_u32 s44, %[wo], 0x6e000\n\t" \
    "buffer_load_dwordx4 v176, %[wr], s44 offen nt lds\n\t" \
    "s_add_u32 m0, %[wm], 0x18000\n\t" \
    "s_add_u32 s44, %[wo], 0x78000\n\t" \
    "buffer_load_dwordx4 v176, %[wr], s44 offen nt lds\n\t" \
    "s_add_u32 m0, %[wm], 0x1a000\n\t" \
    "s_add_u32 s44, %[wo], 0x82000\n\t" \
    "buffer_load_dwordx4 v176, %[wr], s44 offen nt lds\n\t" \
    "s_add_u32 m0, %[wm], 0x1c000\n\t" \
    "s_add_u32 s44, %[wo], 0x8c000\n\t" \
    "buffer_load_dwordx4 v176, %[wr], s44 offen nt lds\n\t" \
    "s_add_u32 m0, %[wm], 0x1e000\n\t" \
    "s_add_u32 s44, %[wo], 0x96000\n\t" \
    "buffer_load_dwordx4 v176, %[wr], s44 offen nt lds\n\t" \
    "s_add_u32 m0, %[wm], 0x20000\n\t" \
    "s_add_u32 s44, %[wo], 0xa0000\n\t" \
    "buffer_load_dwordx4 v176, %[wr], s44 offen nt lds\n\t" \
    "s_add_u32 m0, %[wm], 0x22000\n\t" \
    "s_add_u32 s44, %[wo], 0xaa000\n\t" \
    "buffer_load_dwordx4 v176, %[wr], s44 offen nt lds\n\t" \
    "s_add_u32 m0, %[wm], 0x24000\n\t" \
    "s_add_u32 s44, %[wo], 0xb4000\n\t" \
    "buffer_load_dwordx4 v176, %[wr], s44 offen nt lds\n\t" \
    "s_add_u32 m0, %[wm], 0x26000\n\t" \
    "s_add_u32 s44, %[wo], 0xbe000\n\t" \
    "buffer_load_dwordx4 v176, %[wr], s44 offen nt lds\n\t" \
    "s_waitcnt vmcnt(0) lgkmcnt(0)\n\t" \
    "s_barrier \n\t" \
    "v_add_u32 v178, 0x0, v177\n\t" \
    "ds_read_b64_tr_b16 v[0:1], v178 offset:0\n\t" \
    "ds_read_b64_tr_b16 v[2:3], v178 offset:1024\n\t" \
    "ds_read_b64_tr_b16 v[4:5], v178 offset:8192\n\t" \
    "ds_read_b64_tr_b16 v[6:7], v178 offset:9216\n\t" \
    "ds_read_b64_tr_b16 v[8:9], v178 offset:16384\n\t" \
    "ds_read_b64_tr_b16 v[10:11], v178 offset:17408\n\t" \
    "ds_read_b64_tr_b16 v[12:13], v178 offset:24576\n\t" \
    "ds_read_b64_tr_b16 v[14:15], v178 offset:25600\n\t" \
    "v_add_u32 v178, 0x8000, v177\n\t" \
    "ds_read_b64_tr_b16 v[16:17], v178 offset:0\n\t" \
    "ds_read_b64_tr_b16 v[18:19], v178 offset:1024\n\t" \
    "ds_read_b64_tr_b16 v[20:21], v178 offset:8192\n\t" \
    "ds_read_b64_tr_b16 v[22:23], v178 offset:9216\n\t" \
    "ds_read_b64_tr_b16 v[24:25], v178 offset:16384\n\t" \
    "ds_read_b64_tr_b16 v[26:27], v178 offset:17408\n\t" \
    "ds_read_b64_tr_b16 v[28:29], v178 offset:24576\n\t" \
    "ds_read_b64_tr_b16 v[30:31], v178 offset:25600\n\t" \
    "v_add_u32 v178, 0x10000, v177\n\t" \
    "ds_read_b64_tr_b16 v[32:33], v178 offset:0\n\t" \
    "ds_read_b64_tr_b16 v[34:35], v178 offset:1024\n\t" \
    "ds_read_b64_tr_b16 v[36:37], v178 offset:8192\n\t" \
    "ds_read_b64_tr_b16 v[38:39], v178 offset:9216\n\t" \
    "ds_read_b64_tr_b16 v[40:41], v178 offset:16384\n\t" \
    "ds_read_b64_tr_b16 v[42:43], v178 offset:17408\n\t" \
    "ds_read_b64_tr_b16 v[44:45], v178 offset:24576\n\t" \
    "ds_read_b64_tr_b16 v[46:47], v178 offset:25600\n\t" \
    "v_add_u32 v178, 0x18000, v177\n\t" \
    "ds_read_b64_tr_b16 v[48:49], v178 offset:0\n\t" \
    "ds_read_b64_tr_b16 v[50:51], v178 offset:1024\n\t" \
    "ds_read_b64_tr_b16 v[52:53], v178 offset:8192\n\t" \
    "ds_read_b64_tr_b16 v[54:55], v178 offset:9216\n\t" \
    "ds_read_b64_tr_b16 v[56:57], v178 offset:16384\n\t" \
    "ds_read_b64_tr_b16 v[58:59], v178 offset:17408\n\t" \
    "ds_read_b64_tr_b16 v[60:61], v178 offset:24576\n\t" \
    "ds_read_b64_tr_b16 v[62:63], v178 offset:25600\n\t" \
    "v_add_u32 v178, 0x20000, v177\n\t" \
    "ds_read_b64_tr_b16 v[64:65], v178 offset:0\n\t" \
    "ds_read_b64_tr_b16 v[66:67], v178 offset:1024\n\t" \
    "ds_read_b64_tr_b16 v[68:69], v178 offset:8192\n\t" \
    "ds_read_b64_tr_b16 v[70:71], v178 offset:9216\n\t" \
    "ds_read_b64_tr_b16 v[72:73], v178 offset:16384\n\t" \
    "ds_read_b64_tr_b16 v[74:75], v178 offset:17408\n\t" \
    "ds_read_b64_tr_b16 v[76:77], v178 offset:24576\n\t" \
    "ds_read_b64_tr_b16 v[78:79], v178 offset:25600\n\t" \
    "v_mov_b32 v228, 0x3f803f80\n\t" \
    "v_mov_b32 v229, 0x3f803f80\n\t" \
    "v_mov_b32 v230, 0x3f803f80\n\t" \
    "v_mov_b32 v231, 0x3f803f80\n\t" \
    "v_mov_b64 v[216:217], 0\n\t" \
    "v_mov_b32 v219, 0\n\t" \
    "v_bfe_u32 v255, v253, 2, 5\n\t" \
    "v_add_u32 v255, 0x7000, v255\n\t" \
    "v_mov_b32 v218, 0\n\t" \
    "s_waitcnt lgkmcnt(0)\n\t" \
    "s_barrier \n\t" \
    "ds_write_b32 v255, v218\n\t" \
    "s_waitcnt lgkmcnt(0)\n\t" \
    "s_barrier \n\t" \
    ".Lw21_%=:\n\t" \
    "v_mov_b32 v248, 0\n\t" \
    "buffer_load_dword v248, v248, %[rs], %[go] offen sc1\n\t" \
    "s_waitcnt vmcnt(0)\n\t" \
    "v_cmp_ne_u32 vcc, 0, v248\n\t" \
    "s_cbranch_vccnz .Lw22_%=\n\t" \
    "s_cmp_eq_u32 s41, 0\n\t" \
    "s_cbranch_scc1 .Lw22_%=\n\t" \
    "s_sub_u32 s41, s41, 1\n\t" \
    "s_sleep 16\n\t" \
    "s_branch .Lw21_%=\n\t" \
    ".Lw22_%=:\n\t" \
    "s_lshr_b32 s48, %[wave], 2\n\t" \
    "s_mul_i32 s48, s48, 0x3000\n\t" \
    "s_add_u32 s48, s48, %[pi]\n\t" \
    "s_mov_b32 s43, 0\n\t" \
    "s_add_u32 s46, s43, s48\n\t" \
    "s_add_u32 s47, s46, 0x0\n\t" \
    "buffer_load_dwordx4 v[208:211], v253, %[rs], s47 offen sc1\n\t" \
    "s_add_u32 s47, s46, 0x8000\n\t" \
    "buffer_load_dwordx4 v[212:215], v253, %[rs], s47 offen sc1\n\t" \
    "s_cmp_gt_u32 %[wave], 3\n\t" \
    "s_cbranch_scc1 .Lw23_%=\n\t" \
    "s_add_u32 s47, s46, 0x10000\n\t" \
    "buffer_load_dwordx4 v[216:219], v253, %[rs], s47 offen sc1\n\t" \
    ".Lw23_%=:\n\t" \
    "s_waitcnt vmcnt(0)\n\t" \
    ".Lw24_%=:\n\t" \
    "s_add_u32 s49, s43, 0x2c00000\n\t" \
    "s_add_u32 s50, s43, 0x1c00000\n\t" \
    "s_lshr_b32 s44, s40, 6\n\t" \
    "s_lshl_b32 s44, s44, 14\n\t" \
    "s_and_b32 s45, s40, 63\n\t" \
    "s_lshl_b32 s45, s45, 7\n\t" \
    "s_add_u32 s44, s44, s45\n\t" \
    "s_add_u32 s51, s44, 0x4d0000\n\t" \
    "s_add_i32 s44, s40, 1\n\t" \
    "s_max_i32 s44, s44, 0\n\t" \
    "s_lshr_b32 s45, s44, 1\n\t" \
    "s_and_b32 s44, s44, 1\n\t" \
    "s_lshl_b32 s44, s44, 12\n\t" \
    "s_mul_i32 s43, s45, 0x14000\n\t" \
    "s_add_u32 s43, s43, s44\n\t" \
    "s_lshr_b32 s45, s45, 4\n\t" \
    "s_mul_i32 s45, s45, 0xc2000\n\t" \
    "s_add_u32 s43, s43, s45\n\t" \
    "s_setprio 2\n\t" \
    "v_max_u32 v248, v208, v209\n\t" \
    "v_max3_u32 v248, v210, v211, v248\n\t" \
    "v_max3_u32 v248, v212, v213, v248\n\t" \
    "v_max3_u32 v248, v214, v215, v248\n\t" \
    "v_max3_u32 v248, v216, v217, v248\n\t" \
    "v_max3_u32 v248, v218, v219, v248\n\t" \
    "v_cmp_eq_u32 vcc, -1, v248\n\t" \
    "s_cbranch_vccz .Lw26_%=\n\t" \
    "s_and_b32 s44, s40, 1\n\t" \
    "s_cmp_eq_u32 s44, 0\n\t" \
    "s_cbranch_scc1 .Lw25_%=\n\t" \
    "s_sleep 6\n\t" \
    ".Lw25_%=:\n\t" \
    "v_max_u32 v248, v208, v209\n\t" \
    "v_max3_u32 v248, v210, v211, v248\n\t" \
    "v_cmp_eq_u32 vcc, -1, v248\n\t" \
    "s_cbranch_vccz .Lw28_%=\n\t" \
    "s_add_u32 s47, s46, 0x0\n\t" \
    "buffer_load_dwordx4 v[208:211], v253, %[rs], s47 offen sc1\n\t" \
    ".Lw28_%=:\n\t" \
    "v_max_u32 v248, v212, v213\n\t" \
    "v_max3_u32 v248, v214, v215, v248\n\t" \
    "v_cmp_eq_u32 vcc, -1, v248\n\t" \
    "s_cbranch_vccz .Lw29_%=\n\t" \
    "s_add_u32 s47, s46, 0x8000\n\t" \
    "buffer_load_dwordx4 v[212:215], v253, %[rs], s47 offen sc1\n\t" \
    ".Lw29_%=:\n\t" \
    "v_max_u32 v248, v216, v217\n\t" \
    "v_max3_u32 v248, v218, v219, v248\n\t" \
    "v_cmp_eq_u32 vcc, -1, v248\n\t" \
    "s_cbranch_vccz .Lw210_%=\n\t" \
    "s_add_u32 s47, s46, 0x10000\n\t" \
    "buffer_load_dwordx4 v[216:219], v253, %[rs], s47 offen sc1\n\t" \
    ".Lw210_%=:\n\t" \
    "s_waitcnt vmcnt(0)\n\t" \
    "v_max_u32 v248, v208, v209\n\t" \
    "v_max3_u32 v248, v210, v211, v248\n\t" \
    "v_max3_u32 v248, v212, v213, v248\n\t" \
    "v_max3_u32 v248, v214, v215, v248\n\t" \
    "v_max3_u32 v248, v216, v217, v248\n\t" \
    "v_max3_u32 v248, v218, v219, v248\n\t" \
    "v_cmp_eq_u32 vcc, -1, v248\n\t" \
    "s_cbranch_vccz .Lw26_%=\n\t" \
    "s_cmp_eq_u32 s41, 0\n\t" \
    "s_cbranch_scc1 .Lw26_%=\n\t" \
    "s_sub_u32 s41, s41, 1\n\t" \
    "s_branch .Lw25_%=\n\t" \
    ".Lw26_%=:\n\t" \
    "ds_write_b128 v250, v[208:211] offset:0\n\t" \
    "ds_write_b128 v250, v[212:215] offset:8192\n\t" \
    "s_cmp_gt_u32 %[wave], 3\n\t" \
    "s_cbranch_scc1 .Lw211_%=\n\t" \
    "ds_write_b128 v250, v[216:219] offset:16384\n\t" \
    ".Lw211_%=:\n\t" \
    "s_add_u32 s46, s43, s48\n\t" \
    "s_waitcnt lgkmcnt(0)\n\t" \
    "s_barrier \n\t" \
    "s_setprio 0\n\t" \
    "ds_read_b128 v[176:179], v249 offset:0\n\t" \
    "buffer_load_dwordx2 v[232:233], v254, %[rs], s50 offen sc1\n\t" \
    "buffer_load_dwordx2 v[234:235], v251, %[rs], s51 offen sc1\n\t" \
    "ds_read_b128 v[180:183], v249 offset:1024\n\t" \
    "ds_read_b128 v[184:187], v249 offset:2048\n\t" \
    "ds_read_b128 v[188:191], v249 offset:3072\n\t" \
    "ds_read_b128 v[192:195], v249 offset:4096\n\t" \
    "ds_read_b128 v[196:199], v249 offset:5120\n\t" \
    "ds_read_b128 v[200:203], v249 offset:6144\n\t" \
    "s_waitcnt lgkmcnt(6)\n\t" \
    "v_mfma_f32_16x16x32_bf16 v[168:171], v[0:3], v[176:179], 0\n\t" \
    "ds_read_b128 v[204:207], v249 offset:7168\n\t" \
    "s_waitcnt lgkmcnt(6)\n\t" \
    "v_mfma_f32_16x16x32_bf16 v[168:171], v[4:7], v[180:183], v[168:171]\n\t" \
    "ds_read_b128 v[176:179], v249 offset:8192\n\t" \
    "s_waitcnt lgkmcnt(6)\n\t" \
    "v_mfma_f32_16x16x32_bf16 v[168:171], v[8:11], v[184:187], v[168:171]\n\t" \
    "ds_read_b128 v[180:183], v249 offset:9216\n\t" \
    "s_waitcnt lgkmcnt(6)\n\t" \
    "v_mfma_f32_16x16x32_bf16 v[168:171], v[12:15], v[188:191], v[168:171]\n\t" \
    "ds_read_b128 v[184:187], v249 offset:10240\n\t" \
    "s_add_u32 s47, s46, 0x0\n\t" \
    "buffer_load_dwordx4 v[208:211], v253, %[rs], s47 offen sc1\n\t" \
    "s_add_u32 s47, s46, 0x8000\n\t" \
    "buffer_load_dwordx4 v[212:215], v253, %[rs], s47 offen sc1\n\t" \
    "s_cmp_gt_u32 %[wave], 3\n\t" \
    "s_cbranch_scc1 .Lw212_%=\n\t" \
    "s_add_u32 s47, s46, 0x10000\n\t" \
    "buffer_load_dwordx4 v[216:219], v253, %[rs], s47 offen sc1\n\t" \
    ".Lw212_%=:\n\t" \
    "s_waitcnt lgkmcnt(6)\n\t" \
    "v_mfma_f32_16x16x32_bf16 v[168:171], v[16:19], v[192:195], v[168:171]\n\t" \
    "ds_read_b128 v[188:191], v249 offset:11264\n\t" \
    "s_waitcnt lgkmcnt(6)\n\t" \
    "v_mfma_f32_16x16x32_bf16 v[168:171], v[20:23], v[196:199], v[168:171]\n\t" \
    "ds_read_b128 v[192:195], v249 offset:12288\n\t" \
    "s_waitcnt lgkmcnt(6)\n\t" \
    "v_mfma_f32_16x16x32_bf16 v[168:171], v[24:27], v[200:203], v[168:171]\n\t" \
    "ds_read_b128 v[196:199], v249 offset:13312\n\t" \
    "s_waitcnt lgkmcnt(6)\n\t" \
    "v_mfma_f32_16x16x32_bf16 v[168:171], v[28:31], v[204:207], v[168:171]\n\t" \
    "ds_read_b128 v[200:203], v249 offset:14336\n\t" \
    "s_waitcnt lgkmcnt(6)\n\t" \
    "v_mfma_f32_16x16x32_bf16 v[168:171], v[32:35], v[176:179], v[168:171]\n\t" \
    "ds_read_b128 v[204:207], v249 offset:15360\n\t" \
    "s_waitcnt lgkmcnt(6)\n\t" \
    "v_mfma_f32_16x16x32_bf16 v[168:171], v[36:39], v[180:183], v[168:171]\n\t" \
    "ds_read_b128 v[176:179], v249 offset:16384\n\t" \
    "s_waitcnt lgkmcnt(6)\n\t" \
    "v_mfma_f32_16x16x32_bf16 v[168:171], v[40:43], v[184:187], v[168:171]\n\t" \
    "ds_read_b128 v[180:183], v249 offset:17408\n\t" \
    "s_waitcnt lgkmcnt(6)\n\t" \
    "v_mfma_f32_16x16x32_bf16 v[168:171], v[44:47], v[188:191], v[168:171]\n\t" \
    "ds_read_b128 v[184:187], v249 offset:18432\n\t" \
    "s_waitcnt lgkmcnt(6)\n\t" \
    "v_mfma_f32_16x16x32_bf16 v[168:171], v[48:51], v[192:195], v[168:171]\n\t" \
    "ds_read_b128 v[188:191], v249 offset:19456\n\t" \
    "s_waitcnt lgkmcnt(6)\n\t" \
    "v_mfma_f32_16x16x32_bf16 v[168:171], v[52:55], v[196:199], v[168:171]\n\t" \
    "s_waitcnt lgkmcnt(5)\n\t" \
    "v_mfma_f32_16x16x32_bf16 v[168:171], v[56:59], v[200:203], v[168:171]\n\t" \
    "s_waitcnt lgkmcnt(4)\n\t" \
    "v_mfma_f32_16x16x32_bf16 v[168:171], v[60:63], v[204:207], v[168:171]\n\t" \
    "s_waitcnt lgkmcnt(3)\n\t" \
    "v_mfma_f32_16x16x32_bf16 v[168:171], v[64:67], v[176:179], v[168:171]\n\t" \
    "s_waitcnt lgkmcnt(2)\n\t" \
    "v_mfma_f32_16x16x32_bf16 v[168:171], v[68:71], v[180:183], v[168:171]\n\t" \
    "s_waitcnt lgkmcnt(1)\n\t" \
    "v_mfma_f32_16x16x32_bf16 v[168:171], v[72:75], v[184:187], v[168:171]\n\t" \
    "s_waitcnt lgkmcnt(0)\n\t" \
    "v_mfma_f32_16x16x32_bf16 v[168:171], v[76:79], v[188:191], v[168:171]\n\t" \
    "s_waitcnt vmcnt(0)\n\t" \
    ".Lw213_%=:\n\t" \
    "v_max_u32 v248, v234, v235\n\t" \
    "v_cmp_eq_u32 vcc, -1, v248\n\t" \
    "s_cbranch_vccz .Lw214_%=\n\t" \
    "s_cmp_eq_u32 s41, 0\n\t" \
    "s_cbranch_scc1 .Lw214_%=\n\t" \
    "s_sub_u32 s41, s41, 1\n\t" \
    "buffer_load_dwordx2 v[234:235], v251, %[rs], s51 offen sc1\n\t" \
    "s_waitcnt vmcnt(0)\n\t" \
    "s_branch .Lw213_%=\n\t" \
    ".Lw214_%=:\n\t" \
    "v_lshlrev_b32 v176, 16, v232\n\t" \
    "v_and_b32 v177, 0xffff0000, v232\n\t" \
    "v_lshlrev_b32 v178, 16, v233\n\t" \
    "v_and_b32 v179, 0xffff0000, v233\n\t" \
    "v_sub_f32 v176, v176, v234\n\t" \
    "v_sub_f32 v177, v177, v234\n\t" \
    "v_sub_f32 v178, v178, v234\n\t" \
    "v_sub_f32 v179, v179, v234\n\t" \
    "v_mul_f32 v176, v176, v235\n\t" \
    "v_mul_f32 v177, v177, v235\n\t" \
    "v_mul_f32 v178, v178, v235\n\t" \
    "v_mul_f32 v179, v179, v235\n\t" \
    "v_cvt_pk_bf16_f32 v180, v176, v177\n\t" \
    "v_cvt_pk_bf16_f32 v181, v178, v179\n\t" \
    "v_lshlrev_b32 v176, 16, v180\n\t" \
    "v_and_b32 v177, 0xffff0000, v180\n\t" \
    "v_lshlrev_b32 v178, 16, v181\n\t" \
    "v_and_b32 v179, 0xffff0000, v181\n\t" \
    "v_add_f32 v168, v176, v168\n\t" \
    "v_add_f32 v169, v177, v169\n\t" \
    "v_add_f32 v170, v178, v170\n\t" \
    "v_add_f32 v171, v179, v171\n\t" \
    "v_cvt_pk_bf16_f32 v184, v168, v169\n\t" \
    "v_cvt_pk_bf16_f32 v185, v170, v171\n\t" \
    "v_min_u32 v184, -2, v184\n\t" \
    "v_min_u32 v185, -2, v185\n\t" \
    "buffer_store_dwordx2 v[184:185], v254, %[rs], s49 offen sc1\n\t" \
    "s_cmp_ge_u32 s40, 122\n\t" \
    "s_cbranch_scc1 .Lw215_%=\n\t" \
    "s_add_i32 s44, s40, 6\n\t" \
    "s_max_i32 s44, s44, 0\n\t" \
    "s_lshr_b32 s45, s44, 1\n\t" \
    "s_and_b32 s44, s44, 1\n\t" \
    "s_lshl_b32 s44, s44, 12\n\t" \
    "s_mul_i32 s50, s45, 0x14000\n\t" \
    "s_add_u32 s50, s50, s44\n\t" \
    "s_lshr_b32 s45, s45, 4\n\t" \
    "s_mul_i32 s45, s45, 0xc2000\n\t" \
    "s_add_u32 s50, s50, s45\n\t" \
    "s_add_u32 s50, s50, 0x2c00000\n\t" \
    "v_mov_b64 v[188:189], -1\n\t" \
    "buffer_store_dwordx2 v[188:189], v254, %[rs], s50 offen sc1\n\t" \
    ".Lw215_%=:\n\t" \
    "v_xor_b32 v249, 0x8000, v249\n\t" \
    "v_xor_b32 v250, 0x8000, v250\n\t" \
    "s_add_u32 s40, s40, 1\n\t" \
    "s_cmp_lg_u32 s40, 128\n\t" \
    "s_cbranch_scc1 .Lw24_%=\n\t" \
    "s_branch .Lend%=\n\t" \
    ".Lend%=:\n\t" \
    "s_waitcnt vmcnt(0) lgkmcnt(0)\n\t" \
    "s_mov_b32 %[bud], s41\n\t"
#define ASM_W3_LOOP \
    "s_mov_b32 s40, 0\n\t" \
    "s_mov_b32 s41, 0x20000\n\t" \
    "s_add_u32 m0, %[wm], 0x0\n\t" \
    "s_add_u32 s44, %[wo], 0x0\n\t" \
    "buffer_load_dwordx4 v176, %[wr], s44 offen nt lds\n\t" \
    "s_add_u32 m0, %[wm], 0x2000\n\t" \
    "s_add_u32 s44, %[wo], 0xc000\n\t" \
    "buffer_load_dwordx4 v176, %[wr], s44 offen nt lds\n\t" \
    "s_add_u32 m0, %[wm], 0x4000\n\t" \
    "s_add_u32 s44, %[wo], 0x18000\n\t" \
    "buffer_load_dwordx4 v176, %[wr], s44 offen nt lds\n\t" \
    "s_add_u32 m0, %[wm], 0x6000\n\t" \
    "s_add_u32 s44, %[wo], 0x24000\n\t" \
    "buffer_load_dwordx4 v176, %[wr], s44 offen nt lds\n\t" \
    "s_add_u32 m0, %[wm], 0x8000\n\t" \
    "s_add_u32 s44, %[wo], 0x30000\n\t" \
    "buffer_load_dwordx4 v176, %[wr], s44 offen nt lds\n\t" \
    "s_add_u32 m0, %[wm], 0xa000\n\t" \
    "s_add_u32 s44, %[wo], 0x3c000\n\t" \
    "buffer_load_dwordx4 v176, %[wr], s44 offen nt lds\n\t" \
    "s_add_u32 m0, %[wm], 0xc000\n\t" \
    "s_add_u32 s44, %[wo], 0x48000\n\t" \
    "buffer_load_dwordx4 v176, %[wr], s44 offen nt lds\n\t" \
    "s_add_u32 m0, %[wm], 0xe000\n\t" \
    "s_add_u32 s44, %[wo], 0x54000\n\t" \
    "buffer_load_dwordx4 v176, %[wr], s44 offen nt lds\n\t" \
    "s_add_u32 m0, %[wm], 0x10000\n\t" \
    "s_add_u32 s44, %[wo], 0x60000\n\t" \
    "buffer_load_dwordx4 v176, %[wr], s44 offen nt lds\n\t" \
    "s_add_u32 m0, %[wm], 0x12000\n\t" \
    "s_add_u32 s44, %[wo], 0x6c000\n\t" \
    "buffer_load_dwordx4 v176, %[wr], s44 offen nt lds\n\t" \
    "s_add_u32 m0, %[wm], 0x14000\n\t" \
    "s_add_u32 s44, %[wo], 0x78000\n\t" \
    "buffer_load_dwordx4 v176, %[wr], s44 offen nt lds\n\t" \
    "s_add_u32 m0, %[wm], 0x16000\n\t" \
    "s_add_u32 s44, %[wo], 0x84000\n\t" \
    "buffer_load_dwordx4 v176, %[wr], s44 offen nt lds\n\t" \
    "s_add_u32 m0, %[wm], 0x18000\n\t" \
    "s_add_u32 s44, %[wo], 0x90000\n\t" \
    "buffer_load_dwordx4 v176, %[wr], s44 offen nt lds\n\t" \
    "s_add_u32 m0, %[wm], 0x1a000\n\t" \
    "s_add_u32 s44, %[wo], 0x9c000\n\t" \
    "buffer_load_dwordx4 v176, %[wr], s44 offen nt lds\n\t" \
    "s_add_u32 m0, %[wm], 0x1c000\n\t" \
    "s_add_u32 s44, %[wo], 0xa8000\n\t" \
    "buffer_load_dwordx4 v176, %[wr], s44 offen nt lds\n\t" \
    "s_add_u32 m0, %[wm], 0x1e000\n\t" \
    "s_add_u32 s44, %[wo], 0xb4000\n\t" \
    "buffer_load_dwordx4 v176, %[wr], s44 offen nt lds\n\t" \
    "s_add_u32 m0, %[wm], 0x20000\n\t" \
    "s_add_u32 s44, %[wo], 0xc0000\n\t" \
    "buffer_load_dwordx4 v176, %[wr], s44 offen nt lds\n\t" \
    "s_add_u32 m0, %[wm], 0x22000\n\t" \
    "s_add_u32 s44, %[wo], 0xcc000\n\t" \
    "buffer_load_dwordx4 v176, %[wr], s44 offen nt lds\n\t" \
    "s_add_u32 m0, %[wm], 0x24000\n\t" \
    "s_add_u32 s44, %[wo], 0xd8000\n\t" \
    "buffer_load_dwordx4 v176, %[wr], s44 offen nt lds\n\t" \
    "s_add_u32 m0, %[wm], 0x26000\n\t" \
    "s_add_u32 s44, %[wo], 0xe4000\n\t" \
    "buffer_load_dwordx4 v176, %[wr], s44 offen nt lds\n\t" \
    "s_waitcnt vmcnt(0) lgkmcnt(0)\n\t" \
    "s_barrier \n\t" \
    "v_add_u32 v178, 0x0, v177\n\t" \
    "v_xor_b32 v179, 32, v178\n\t" \
    "ds_read_b64_tr_b16 v[0:1], v178 offset:0\n\t" \
    "ds_read_b64_tr_b16 v[2:3], v178 offset:2048\n\t" \
    "ds_read_b64_tr_b16 v[80:81], v179 offset:0\n\t" \
    "ds_read_b64_tr_b16 v[82:83], v179 offset:2048\n\t" \
    "ds_read_b64_tr_b16 v[4:5], v178 offset:16384\n\t" \
    "ds_read_b64_tr_b16 v[6:7], v178 offset:18432\n\t" \
    "ds_read_b64_tr_b16 v[84:85], v179 offset:16384\n\t" \
    "ds_read_b64_tr_b16 v[86:87], v179 offset:18432\n\t" \
    "v_add_u32 v178, 0x8000, v177\n\t" \
    "v_xor_b32 v179, 32, v178\n\t" \
    "ds_read_b64_tr_b16 v[8:9], v178 offset:0\n\t" \
    "ds_read_b64_tr_b16 v[10:11], v178 offset:2048\n\t" \
    "ds_read_b64_tr_b16 v[88:89], v179 offset:0\n\t" \
    "ds_read_b64_tr_b16 v[90:91], v179 offset:2048\n\t" \
    "ds_read_b64_tr_b16 v[12:13], v178 offset:16384\n\t" \
    "ds_read_b64_tr_b16 v[14:15], v178 offset:18432\n\t" \
    "ds_read_b64_tr_b16 v[92:93], v179 offset:16384\n\t" \
    "ds_read_b64_tr_b16 v[94:95], v179 offset:18432\n\t" \
    "v_add_u32 v178, 0x10000, v177\n\t" \
    "v_xor_b32 v179, 32, v178\n\t" \
    "ds_read_b64_tr_b16 v[16:17], v178 offset:0\n\t" \
    "ds_read_b64_tr_b16 v[18:19], v178 offset:2048\n\t" \
    "ds_read_b64_tr_b16 v[96:97], v179 offset:0\n\t" \
    "ds_read_b64_tr_b16 v[98:99], v179 offset:2048\n\t" \
    "ds_read_b64_tr_b16 v[20:21], v178 offset:16384\n\t" \
    "ds_read_b64_tr_b16 v[22:23], v178 offset:18432\n\t" \
    "ds_read_b64_tr_b16 v[100:101], v179 offset:16384\n\t" \
    "ds_read_b64_tr_b16 v[102:103], v179 offset:18432\n\t" \
    "v_add_u32 v178, 0x18000, v177\n\t" \
    "v_xor_b32 v179, 32, v178\n\t" \
    "ds_read_b64_tr_b16 v[24:25], v178 offset:0\n\t" \
    "ds_read_b64_tr_b16 v[26:27], v178 offset:2048\n\t" \
    "ds_read_b64_tr_b16 v[104:105], v179 offset:0\n\t" \
    "ds_read_b64_tr_b16 v[106:107], v179 offset:2048\n\t" \
    "ds_read_b64_tr_b16 v[28:29], v178 offset:16384\n\t" \
    "ds_read_b64_tr_b16 v[30:31], v178 offset:18432\n\t" \
    "ds_read_b64_tr_b16 v[108:109], v179 offset:16384\n\t" \
    "ds_read_b64_tr_b16 v[110:111], v179 offset:18432\n\t" \
    "v_add_u32 v178, 0x20000, v177\n\t" \
    "v_xor_b32 v179, 32, v178\n\t" \
    "ds_read_b64_tr_b16 v[32:33], v178 offset:0\n\t" \
    "ds_read_b64_tr_b16 v[34:35], v178 offset:2048\n\t" \
    "ds_read_b64_tr_b16 v[112:113], v179 offset:0\n\t" \
    "ds_read_b64_tr_b16 v[114:115], v179 offset:2048\n\t" \
    "ds_read_b64_tr_b16 v[36:37], v178 offset:16384\n\t" \
    "ds_read_b64_tr_b16 v[38:39], v178 offset:18432\n\t" \
    "ds_read_b64_tr_b16 v[116:117], v179 offset:16384\n\t" \
    "ds_read_b64_tr_b16 v[118:119], v179 offset:18432\n\t" \
    "s_waitcnt lgkmcnt(0)\n\t" \
    "s_barrier \n\t" \
    "s_add_u32 m0, %[wm], 0x0\n\t" \
    "s_add_u32 s44, %[wo], 0xf0000\n\t" \
    "buffer_load_dwordx4 v176, %[wr], s44 offen nt lds\n\t" \
    "s_add_u32 m0, %[wm], 0x2000\n\t" \
    "s_add_u32 s44, %[wo], 0xfc000\n\t" \
    "buffer_load_dwordx4 v176, %[wr], s44 offen nt lds\n\t" \
    "s_add_u32 m0, %[wm], 0x4000\n\t" \
    "s_add_u32 s44, %[wo], 0x108000\n\t" \
    "buffer_load_dwordx4 v176, %[wr], s44 offen nt lds\n\t" \
    "s_add_u32 m0, %[wm], 0x6000\n\t" \
    "s_add_u32 s44, %[wo], 0x114000\n\t" \
    "buffer_load_dwordx4 v176, %[wr], s44 offen nt lds\n\t" \
    "s_add_u32 m0, %[wm], 0x8000\n\t" \
    "s_add_u32 s44, %[wo], 0x120000\n\t" \
    "buffer_load_dwordx4 v176, %[wr], s44 offen nt lds\n\t" \
    "s_add_u32 m0, %[wm], 0xa000\n\t" \
    "s_add_u32 s44, %[wo], 0x12c000\n\t" \
    "buffer_load_dwordx4 v176, %[wr], s44 offen nt lds\n\t" \
    "s_add_u32 m0, %[wm], 0xc000\n\t" \
    "s_add_u32 s44, %[wo], 0x138000\n\t" \
    "buffer_load_dwordx4 v176, %[wr], s44 offen nt lds\n\t" \
    "s_add_u32 m0, %[wm], 0xe000\n\t" \
    "s_add_u32 s44, %[wo], 0x144000\n\t" \
    "buffer_load_dwordx4 v176, %[wr], s44 offen nt lds\n\t" \
    "s_add_u32 m0, %[wm], 0x10000\n\t" \
    "s_add_u32 s44, %[wo], 0x150000\n\t" \
    "buffer_load_dwordx4 v176, %[wr], s44 offen nt lds\n\t" \
    "s_add_u32 m0, %[wm], 0x12000\n\t" \
    "s_add_u32 s44, %[wo], 0x15c000\n\t" \
    "buffer_load_dwordx4 v176, %[wr], s44 offen nt lds\n\t" \
    "s_add_u32 m0, %[wm], 0x14000\n\t" \
    "s_add_u32 s44, %[wo], 0x168000\n\t" \
    "buffer_load_dwordx4 v176, %[wr], s44 offen nt lds\n\t" \
    "s_add_u32 m0, %[wm], 0x16000\n\t" \
    "s_add_u32 s44, %[wo], 0x174000\n\t" \
    "buffer_load_dwordx4 v176, %[wr], s44 offen nt lds\n\t" \
    "s_add_u32 m0, %[wm], 0x18000\n\t" \
    "s_add_u32 s44, %[wo], 0x180000\n\t" \
    "buffer_load_dwordx4 v176, %[wr], s44 offen nt lds\n\t" \
    "s_add_u32 m0, %[wm], 0x1a000\n\t" \
    "s_add_u32 s44, %[wo], 0x18c000\n\t" \
    "buffer_load_dwordx4 v176, %[wr], s44 offen nt lds\n\t" \
    "s_add_u32 m0, %[wm], 0x1c000\n\t" \
    "s_add_u32 s44, %[wo], 0x198000\n\t" \
    "buffer_load_dwordx4 v176, %[wr], s44 offen nt lds\n\t" \
    "s_add_u32 m0, %[wm], 0x1e000\n\t" \
    "s_add_u32 s44, %[wo], 0x1a4000\n\t" \
    "buffer_load_dwordx4 v176, %[wr], s44 offen nt lds\n\t" \
    "s_add_u32 m0, %[wm], 0x20000\n\t" \
    "s_add_u32 s44, %[wo], 0x1b0000\n\t" \
    "buffer_load_dwordx4 v176, %[wr], s44 offen nt lds\n\t" \
    "s_add_u32 m0, %[wm], 0x22000\n\t" \
    "s_add_u32 s44, %[wo], 0x1bc000\n\t" \
    "buffer_load_dwordx4 v176, %[wr], s44 offen nt lds\n\t" \
    "s_add_u32 m0, %[wm], 0x24000\n\t" \
    "s_add_u32 s44, %[wo], 0x1c8000\n\t" \
    "buffer_load_dwordx4 v176, %[wr], s44 offen nt lds\n\t" \
    "s_add_u32 m0, %[wm], 0x26000\n\t" \
    "s_add_u32 s44, %[wo], 0x1d4000\n\t" \
    "buffer_load_dwordx4 v176, %[wr], s44 offen nt lds\n\t" \
    "s_waitcnt vmcnt(0) lgkmcnt(0)\n\t" \
    "s_barrier \n\t" \
    "v_add_u32 v178, 0x0, v177\n\t" \
    "v_xor_b32 v179, 32, v178\n\t" \
    "ds_read_b64_tr_b16 v[40:41], v178 offset:0\n\t" \
    "ds_read_b64_tr_b16 v[42:43], v178 offset:2048\n\t" \
    "ds_read_b64_tr_b16 v[120:121], v179 offset:0\n\t" \
    "ds_read_b64_tr_b16 v[122:123], v179 offset:2048\n\t" \
    "ds_read_b64_tr_b16 v[44:45], v178 offset:16384\n\t" \
    "ds_read_b64_tr_b16 v[46:47], v178 offset:18432\n\t" \
    "ds_read_b64_tr_b16 v[124:125], v179 offset:16384\n\t" \
    "ds_read_b64_tr_b16 v[126:127], v179 offset:18432\n\t" \
    "v_add_u32 v178, 0x8000, v177\n\t" \
    "v_xor_b32 v179, 32, v178\n\t" \
    "ds_read_b64_tr_b16 v[48:49], v178 offset:0\n\t" \
    "ds_read_b64_tr_b16 v[50:51], v178 offset:2048\n\t" \
    "ds_read_b64_tr_b16 v[128:129], v179 offset:0\n\t" \
    "ds_read_b64_tr_b16 v[130:131], v179 offset:2048\n\t" \
    "ds_read_b64_tr_b16 v[52:53], v178 offset:16384\n\t" \
    "ds_read_b64_tr_b16 v[54:55], v178 offset:18432\n\t" \
    "ds_read_b64_tr_b16 v[132:133], v179 offset:16384\n\t" \
    "ds_read_b64_tr_b16 v[134:135], v179 offset:18432\n\t" \
    "v_add_u32 v178, 0x10000, v177\n\t" \
    "v_xor_b32 v179, 32, v178\n\t" \
    "ds_read_b64_tr_b16 v[56:57], v178 offset:0\n\t" \
    "ds_read_b64_tr_b16 v[58:59], v178 offset:2048\n\t" \
    "ds_read_b64_tr_b16 v[136:137], v179 offset:0\n\t" \
    "ds_read_b64_tr_b16 v[138:139], v179 offset:2048\n\t" \
    "ds_read_b64_tr_b16 v[60:61], v178 offset:16384\n\t" \
    "ds_read_b64_tr_b16 v[62:63], v178 offset:18432\n\t" \
    "ds_read_b64_tr_b16 v[140:141], v179 offset:16384\n\t" \
    "ds_read_b64_tr_b16 v[142:143], v179 offset:18432\n\t" \
    "v_add_u32 v178, 0x18000, v177\n\t" \
    "v_xor_b32 v179, 32, v178\n\t" \
    "ds_read_b64_tr_b16 v[64:65], v178 offset:0\n\t" \
    "ds_read_b64_tr_b16 v[66:67], v178 offset:2048\n\t" \
    "ds_read_b64_tr_b16 v[144:145], v179 offset:0\n\t" \
    "ds_read_b64_tr_b16 v[146:147], v179 offset:2048\n\t" \
    "ds_read_b64_tr_b16 v[68:69], v178 offset:16384\n\t" \
    "ds_read_b64_tr_b16 v[70:71], v178 offset:18432\n\t" \
    "ds_read_b64_tr_b16 v[148:149], v179 offset:16384\n\t" \
    "ds_read_b64_tr_b16 v[150:151], v179 offset:18432\n\t" \
    "v_add_u32 v178, 0x20000, v177\n\t" \
    "v_xor_b32 v179, 32, v178\n\t" \
    "ds_read_b64_tr_b16 v[72:73], v178 offset:0\n\t" \
    "ds_read_b64_tr_b16 v[74:75], v178 offset:2048\n\t" \
    "ds_read_b64_tr_b16 v[152:153], v179 offset:0\n\t" \
    "ds_read_b64_tr_b16 v[154:155], v179 offset:2048\n\t" \
    "ds_read_b64_tr_b16 v[76:77], v178 offset:16384\n\t" \
    "ds_read_b64_tr_b16 v[78:79], v178 offset:18432\n\t" \
    "ds_read_b64_tr_b16 v[156:157], v179 offset:16384\n\t" \
    "ds_read_b64_tr_b16 v[158:159], v179 offset:18432\n\t" \
    "v_mov_b32 v228, 0x3f803f80\n\t" \
    "v_mov_b32 v229, 0x3f803f80\n\t" \
    "v_mov_b32 v230, 0x3f803f80\n\t" \
    "v_mov_b32 v231, 0x3f803f80\n\t" \
    "v_mov_b64 v[216:217], 0\n\t" \
    "v_mov_b32 v219, 0\n\t" \
    "v_bfe_u32 v255, v253, 2, 5\n\t" \
    "v_add_u32 v255, 0x7000, v255\n\t" \
    "v_bfe_u32 v218, v253, 6, 1\n\t" \
    "s_waitcnt lgkmcnt(0)\n\t" \
    "s_barrier \n\t" \
    "ds_write_b32 v251, v228 offset:72\n\t" \
    "ds_write_b32 v251, v228 offset:2120\n\t" \
    "v_mfma_f32_16x16x32_bf16 v[160:163], v[0:3], v[228:231], 0\n\t" \
    "v_mfma_f32_16x16x32_bf16 v[164:167], v[80:83], v[228:231], 0\n\t" \
    "v_mfma_f32_16x16x32_bf16 v[160:163], v[4:7], v[228:231], v[160:163]\n\t" \
    "v_mfma_f32_16x16x32_bf16 v[164:167], v[84:87], v[228:231], v[164:167]\n\t" \
    "v_mfma_f32_16x16x32_bf16 v[160:163], v[8:11], v[228:231], v[160:163]\n\t" \
    "v_mfma_f32_16x16x32_bf16 v[164:167], v[88:91], v[228:231], v[164:167]\n\t" \
    "v_mfma_f32_16x16x32_bf16 v[160:163], v[12:15], v[228:231], v[160:163]\n\t" \
    "v_mfma_f32_16x16x32_bf16 v[164:167], v[92:95], v[228:231], v[164:167]\n\t" \
    "v_mfma_f32_16x16x32_bf16 v[160:163], v[16:19], v[228:231], v[160:163]\n\t" \
    "v_mfma_f32_16x16x32_bf16 v[164:167], v[96:99], v[228:231], v[164:167]\n\t" \
    "v_mfma_f32_16x16x32_bf16 v[160:163], v[20:23], v[228:231], v[160:163]\n\t" \
    "v_mfma_f32_16x16x32_bf16 v[164:167], v[100:103], v[228:231], v[164:167]\n\t" \
    "v_mfma_f32_16x16x32_bf16 v[160:163], v[24:27], v[228:231], v[160:163]\n\t" \
    "v_mfma_f32_16x16x32_bf16 v[164:167], v[104:107], v[228:231], v[164:167]\n\t" \
    "v_mfma_f32_16x16x32_bf16 v[160:163], v[28:31], v[228:231], v[160:163]\n\t" \
    "v_mfma_f32_16x16x32_bf16 v[164:167], v[108:111], v[228:231], v[164:167]\n\t" \
    "v_mfma_f32_16x16x32_bf16 v[160:163], v[32:35], v[228:231], v[160:163]\n\t" \
    "v_mfma_f32_16x16x32_bf16 v[164:167], v[112:115], v[228:231], v[164:167]\n\t" \
    "v_mfma_f32_16x16x32_bf16 v[160:163], v[36:39], v[228:231], v[160:163]\n\t" \
    "v_mfma_f32_16x16x32_bf16 v[164:167], v[116:119], v[228:231], v[164:167]\n\t" \
    "v_mfma_f32_16x16x32_bf16 v[160:163], v[40:43], v[228:231], v[160:163]\n\t" \
    "v_mfma_f32_16x16x32_bf16 v[164:167], v[120:123], v[228:231], v[164:167]\n\t" \
    "v_mfma_f32_16x16x32_bf16 v[160:163], v[44:47], v[228:231], v[160:163]\n\t" \
    "v_mfma_f32_16x16x32_bf16 v[164:167], v[124:127], v[228:231], v[164:167]\n\t" \
    "v_mfma_f32_16x16x32_bf16 v[160:163], v[48:51], v[228:231], v[160:163]\n\t" \
    "v_mfma_f32_16x16x32_bf16 v[164:167], v[128:131], v[228:231], v[164:167]\n\t" \
    "v_mfma_f32_16x16x32_bf16 v[160:163], v[52:55], v[228:231], v[160:163]\n\t" \
    "v_mfma_f32_16x16x32_bf16 v[164:167], v[132:135], v[228:231], v[164:167]\n\t" \
    "v_mfma_f32_16x16x32_bf16 v[160:163], v[56:59], v[228:231], v[160:163]\n\t" \
    "v_mfma_f32_16x16x32_bf16 v[164:167], v[136:139], v[228:231], v[164:167]\n\t" \
    "v_mfma_f32_16x16x32_bf16 v[160:163], v[60:63], v[228:231], v[160:163]\n\t" \
    "v_mfma_f32_16x16x32_bf16 v[164:167], v[140:143], v[228:231], v[164:167]\n\t" \
    "v_mfma_f32_16x16x32_bf16 v[160:163], v[64:67], v[228:231], v[160:163]\n\t" \
    "v_mfma_f32_16x16x32_bf16 v[164:167], v[144:147], v[228:231], v[164:167]\n\t" \
    "v_mfma_f32_16x16x32_bf16 v[160:163], v[68:71], v[228:231], v[160:163]\n\t" \
    "v_mfma_f32_16x16x32_bf16 v[164:167], v[148:151], v[228:231], v[164:167]\n\t" \
    "v_mfma_f32_16x16x32_bf16 v[160:163], v[72:75], v[228:231], v[160:163]\n\t" \
    "v_mfma_f32_16x16x32_bf16 v[164:167], v[152:155], v[228:231], v[164:167]\n\t" \
    "v_mfma_f32_16x16x32_bf16 v[160:163], v[76:79], v[228:231], v[160:163]\n\t" \
    "v_mfma_f32_16x16x32_bf16 v[164:167], v[156:159], v[228:231], v[164:167]\n\t" \
    "s_mov_b32 s52, 0x22222222\n\t" \
    "s_mov_b32 s53, 0x22222222\n\t" \
    "s_mov_b32 s54, 0x44444444\n\t" \
    "s_mov_b32 s55, 0x44444444\n\t" \
    "s_mov_b32 s56, 0x88888888\n\t" \
    "s_mov_b32 s57, 0x88888888\n\t" \
    "ds_write_b32 v255, v218\n\t" \
    "s_waitcnt lgkmcnt(0)\n\t" \
    "s_barrier \n\t" \
    ".Lw31_%=:\n\t" \
    "v_mov_b32 v248, 0\n\t" \
    "buffer_load_dword v248, v248, %[rs], %[go] offen sc1\n\t" \
    "s_waitcnt vmcnt(0)\n\t" \
    "v_cmp_ne_u32 vcc, 0, v248\n\t" \
    "s_cbranch_vccnz .Lw32_%=\n\t" \
    "s_cmp_eq_u32 s41, 0\n\t" \
    "s_cbranch_scc1 .Lw32_%=\n\t" \
    "s_sub_u32 s41, s41, 1\n\t" \
    "s_sleep 16\n\t" \
    "s_branch .Lw31_%=\n\t" \
    ".Lw32_%=:\n\t" \
    "s_mov_b32 s43, 0\n\t" \
    "s_add_u32 s46, s43, %[pi]\n\t" \
    "s_cmp_gt_u32 %[wave], 3\n\t" \
    "s_cbranch_scc1 .Lw33_%=\n\t" \
    "s_add_u32 s47, s46, 0x0\n\t" \
    "buffer_load_dwordx4 v[208:211], v253, %[rs], s47 offen sc1\n\t" \
    "s_add_u32 s47, s46, 0x4000\n\t" \
    "buffer_load_dwordx4 v[212:215], v253, %[rs], s47 offen sc1\n\t" \
    "s_add_u32 s47, s46, 0x8000\n\t" \
    "buffer_load_dwordx4 v[216:219], v253, %[rs], s47 offen sc1\n\t" \
    "s_add_u32 s47, s46, 0xc000\n\t" \
    "buffer_load_dwordx4 v[220:223], v253, %[rs], s47 offen sc1\n\t" \
    "s_add_u32 s47, s46, 0x10000\n\t" \
    "buffer_load_dwordx4 v[224:227], v253, %[rs], s47 offen sc1\n\t" \
    ".Lw33_%=:\n\t" \
    "s_waitcnt vmcnt(0)\n\t" \
    ".Lw34_%=:\n\t" \
    "s_add_i32 s44, s40, 0\n\t" \
    "s_max_i32 s44, s44, 0\n\t" \
    "s_lshr_b32 s45, s44, 1\n\t" \
    "s_and_b32 s44, s44, 1\n\t" \
    "s_lshl_b32 s44, s44, 12\n\t" \
    "s_mul_i32 s49, s45, 0x30000\n\t" \
    "s_add_u32 s49, s49, s44\n\t" \
    "s_lshr_b32 s45, s45, 3\n\t" \
    "s_mul_i32 s45, s45, 0x82000\n\t" \
    "s_add_u32 s49, s49, s45\n\t" \
    "s_add_u32 s49, s49, %[ng]\n\t" \
    "s_add_i32 s44, s40, 1\n\t" \
    "s_max_i32 s44, s44, 0\n\t" \
    "s_lshr_b32 s45, s44, 1\n\t" \
    "s_and_b32 s44, s44, 1\n\t" \
    "s_lshl_b32 s44, s44, 12\n\t" \
    "s_mul_i32 s43, s45, 0x14000\n\t" \
    "s_add_u32 s43, s43, s44\n\t" \
    "s_lshr_b32 s45, s45, 4\n\t" \
    "s_mul_i32 s45, s45, 0xc2000\n\t" \
    "s_add_u32 s43, s43, s45\n\t" \
    "s_cmp_gt_u32 %[wave], 3\n\t" \
    "s_cbranch_scc1 .Lw37_%=\n\t" \
    "s_setprio 2\n\t" \
    "v_max_u32 v248, v208, v209\n\t" \
    "v_max3_u32 v248, v210, v211, v248\n\t" \
    "v_max3_u32 v248, v212, v213, v248\n\t" \
    "v_max3_u32 v248, v214, v215, v248\n\t" \
    "v_max3_u32 v248, v216, v217, v248\n\t" \
    "v_max3_u32 v248, v218, v219, v248\n\t" \
    "v_max3_u32 v248, v220, v221, v248\n\t" \
    "v_max3_u32 v248, v222, v223, v248\n\t" \
    "v_max3_u32 v248, v224, v225, v248\n\t" \
    "v_max3_u32 v248, v226, v227, v248\n\t" \
    "v_cmp_eq_u32 vcc, -1, v248\n\t" \
    "s_cbranch_vccz .Lw36_%=\n\t" \
    ".Lw35_%=:\n\t" \
    "v_max_u32 v248, v208, v209\n\t" \
    "v_max3_u32 v248, v210, v211, v248\n\t" \
    "v_cmp_eq_u32 vcc, -1, v248\n\t" \
    "s_cbranch_vccz .Lw38_%=\n\t" \
    "s_add_u32 s47, s46, 0x0\n\t" \
    "buffer_load_dwordx4 v[208:211], v253, %[rs], s47 offen sc1\n\t" \
    ".Lw38_%=:\n\t" \
    "v_max_u32 v248, v212, v213\n\t" \
    "v_max3_u32 v248, v214, v215, v248\n\t" \
    "v_cmp_eq_u32 vcc, -1, v248\n\t" \
    "s_cbranch_vccz .Lw39_%=\n\t" \
    "s_add_u32 s47, s46, 0x4000\n\t" \
    "buffer_load_dwordx4 v[212:215], v253, %[rs], s47 offen sc1\n\t" \
    ".Lw39_%=:\n\t" \
    "v_max_u32 v248, v216, v217\n\t" \
    "v_max3_u32 v248, v218, v219, v248\n\t" \
    "v_cmp_eq_u32 vcc, -1, v248\n\t" \
    "s_cbranch_vccz .Lw310_%=\n\t" \
    "s_add_u32 s47, s46, 0x8000\n\t" \
    "buffer_load_dwordx4 v[216:219], v253, %[rs], s47 offen sc1\n\t" \
    ".Lw310_%=:\n\t" \
    "v_max_u32 v248, v220, v221\n\t" \
    "v_max3_u32 v248, v222, v223, v248\n\t" \
    "v_cmp_eq_u32 vcc, -1, v248\n\t" \
    "s_cbranch_vccz .Lw311_%=\n\t" \
    "s_add_u32 s47, s46, 0xc000\n\t" \
    "buffer_load_dwordx4 v[220:223], v253, %[rs], s47 offen sc1\n\t" \
    ".Lw311_%=:\n\t" \
    "v_max_u32 v248, v224, v225\n\t" \
    "v_max3_u32 v248, v226, v227, v248\n\t" \
    "v_cmp_eq_u32 vcc, -1, v248\n\t" \
    "s_cbranch_vccz .Lw312_%=\n\t" \
    "s_add_u32 s47, s46, 0x10000\n\t" \
    "buffer_load_dwordx4 v[224:227], v253, %[rs], s47 offen sc1\n\t" \
    ".Lw312_%=:\n\t" \
    "s_waitcnt vmcnt(0)\n\t" \
    "v_max_u32 v248, v208, v209\n\t" \
    "v_max3_u32 v248, v210, v211, v248\n\t" \
    "v_max3_u32 v248, v212, v213, v248\n\t" \
    "v_max3_u32 v248, v214, v215, v248\n\t" \
    "v_max3_u32 v248, v216, v217, v248\n\t" \
    "v_max3_u32 v248, v218, v219, v248\n\t" \
    "v_max3_u32 v248, v220, v221, v248\n\t" \
    "v_max3_u32 v248, v222, v223, v248\n\t" \
    "v_max3_u32 v248, v224, v225, v248\n\t" \
    "v_max3_u32 v248, v226, v227, v248\n\t" \
    "v_cmp_eq_u32 vcc, -1, v248\n\t" \
    "s_cbranch_vccz .Lw36_%=\n\t" \
    "s_cmp_eq_u32 s41, 0\n\t" \
    "s_cbranch_scc1 .Lw36_%=\n\t" \
    "s_sub_u32 s41, s41, 1\n\t" \
    "s_branch .Lw35_%=\n\t" \
    ".Lw36_%=:\n\t" \
    "ds_write_b128 v250, v[208:211] offset:0\n\t" \
    "v_mfma_f32_16x16x32_bf16 v[236:239], v[208:211], v[208:211], 0\n\t" \
    "v_mfma_f32_16x16x32_bf16 v[232:235], v[228:231], v[208:211], 0\n\t" \
    "ds_write_b128 v250, v[212:215] offset:4096\n\t" \
    "v_mfma_f32_16x16x32_bf16 v[236:239], v[212:215], v[212:215], v[236:239]\n\t" \
    "v_mfma_f32_16x16x32_bf16 v[232:235], v[228:231], v[212:215], v[232:235]\n\t" \
    "ds_write_b128 v250, v[216:219] offset:8192\n\t" \
    "v_mfma_f32_16x16x32_bf16 v[236:239], v[216:219], v[216:219], v[236:239]\n\t" \
    "v_mfma_f32_16x16x32_bf16 v[232:235], v[228:231], v[216:219], v[232:235]\n\t" \
    "ds_write_b128 v250, v[220:223] offset:12288\n\t" \
    "v_mfma_f32_16x16x32_bf16 v[236:239], v[220:223], v[220:223], v[236:239]\n\t" \
    "v_mfma_f32_16x16x32_bf16 v[232:235], v[228:231], v[220:223], v[232:235]\n\t" \
    "ds_write_b128 v250, v[224:227] offset:16384\n\t" \
    "v_mfma_f32_16x16x32_bf16 v[236:239], v[224:227], v[224:227], v[236:239]\n\t" \
    "v_mfma_f32_16x16x32_bf16 v[232:235], v[228:231], v[224:227], v[232:235]\n\t" \
    "s_add_u32 s46, s43, %[pi]\n\t" \
    "s_nop 4\n\t" \
    "v_cndmask_b32 v236, v236, v237, s[52:53]\n\t" \
    "v_cndmask_b32 v236, v236, v238, s[54:55]\n\t" \
    "v_cndmask_b32 v236, v236, v239, s[56:57]\n\t" \
    "s_mov_b32 exec_lo, 0xf0000f\n\t" \
    "s_mov_b32 exec_hi, 0xf0000f00\n\t" \
    "ds_write2_b32 v252, v232, v236 offset1:1\n\t" \
    "s_mov_b64 exec, -1\n\t" \
    "s_add_u32 s44, s40, 1\n\t" \
    "v_mov_b32 v248, s44\n\t" \
    "s_lshl_b64 exec, 1, %[wave]\n\t" \
    "ds_write_b32 v255, v248\n\t" \
    "s_mov_b64 exec, -1\n\t" \
    ".Lw37_%=:\n\t" \
    "ds_read_b32 v248, v255\n\t" \
    "s_waitcnt lgkmcnt(0)\n\t" \
    "v_cmp_ge_u32 vcc, s40, v248\n\t" \
    "s_cbranch_vccz .Lw313_%=\n\t" \
    "s_cmp_lt_u32 %[wave], 4\n\t" \
    "s_cbranch_scc1 .Lw37_%=\n\t" \
    "s_sleep 1\n\t" \
    "s_branch .Lw37_%=\n\t" \
    ".Lw313_%=:\n\t" \
    "s_setprio 0\n\t" \
    "s_cmp_lg_u32 %[wave], 4\n\t" \
    "s_cbranch_scc1 .Lw314_%=\n\t" \
    "s_setprio 1\n\t" \
    "ds_read_b128 v[232:235], v251 offset:0\n\t" \
    "ds_read_b128 v[236:239], v251 offset:16\n\t" \
    "s_waitcnt lgkmcnt(0)\n\t" \
    "v_add_f32 v232, v232, v236\n\t" \
    "v_add_f32 v233, v233, v237\n\t" \
    "v_add_f32 v234, v234, v238\n\t" \
    "v_add_f32 v235, v235, v239\n\t" \
    "v_add_f32 v232, v232, v234\n\t" \
    "v_add_f32 v233, v233, v235\n\t" \
    "v_mul_f32 v232, 0x3acccccd, v232\n\t" \
    "v_mul_f32 v233, 0x3acccccd, v233\n\t" \
    "v_fma_f32 v233, -v232, v232, v233\n\t" \
    "v_mov_b32 v234, v232\n\t" \
    "v_max_f32 v233, 0, v233\n\t" \
    "v_cvt_pk_bf16_f32 v234, v234, v234\n\t" \
    "v_and_b32 v234, 0xffff0000, v234\n\t" \
    "v_mov_b32 v236, v233\n\t" \
    "v_cvt_pk_bf16_f32 v236, v236, v236\n\t" \
    "v_and_b32 v236, 0xffff0000, v236\n\t" \
    "v_add_f32 v236, 0x3727c5ac, v236\n\t" \
    "v_cvt_pk_bf16_f32 v236, v236, v236\n\t" \
    "v_and_b32 v236, 0xffff0000, v236\n\t" \
    "v_sqrt_f32 v236, v236\n\t" \
    "s_nop 0\n\t" \
    "v_cvt_pk_bf16_f32 v236, v236, v236\n\t" \
    "v_and_b32 v236, 0xffff0000, v236\n\t" \
    "v_rcp_f32 v235, v236\n\t" \
    "s_nop 0\n\t" \
    "v_mov_b32 v237, v235\n\t" \
    "v_mul_f32 v238, -v234, v235\n\t" \
    "v_mov_b32 v239, s40\n\t" \
    "ds_write2_b32 v251, v237, v238 offset0:16 offset1:17\n\t" \
    "ds_write_b32 v251, v239 offset:72\n\t" \
    "s_setprio 0\n\t" \
    ".Lw314_%=:\n\t" \
    "ds_read_b128 v[176:179], v249 offset:0\n\t" \
    "ds_read_b128 v[180:183], v249 offset:1024\n\t" \
    "ds_read_b128 v[184:187], v249 offset:2048\n\t" \
    "ds_read_b128 v[188:191], v249 offset:3072\n\t" \
    "ds_read_b128 v[192:195], v249 offset:4096\n\t" \
    "ds_read_b128 v[196:199], v249 offset:5120\n\t" \
    "ds_read_b128 v[200:203], v249 offset:6144\n\t" \
    "s_waitcnt lgkmcnt(6)\n\t" \
    "v_mfma_f32_16x16x32_bf16 v[168:171], v[0:3], v[176:179], 0\n\t" \
    "ds_read_b128 v[204:207], v249 offset:7168\n\t" \
    "v_mfma_f32_16x16x32_bf16 v[172:175], v[80:83], v[176:179], 0\n\t" \
    "s_waitcnt lgkmcnt(6)\n\t" \
    "v_mfma_f32_16x16x32_bf16 v[168:171], v[4:7], v[180:183], v[168:171]\n\t" \
    "ds_read_b128 v[176:179], v249 offset:8192\n\t" \
    "v_mfma_f32_16x16x32_bf16 v[172:175], v[84:87], v[180:183], v[172:175]\n\t" \
    "s_waitcnt lgkmcnt(6)\n\t" \
    "v_mfma_f32_16x16x32_bf16 v[168:171], v[8:11], v[184:187], v[168:171]\n\t" \
    "ds_read_b128 v[180:183], v249 offset:9216\n\t" \
    "v_mfma_f32_16x16x32_bf16 v[172:175], v[88:91], v[184:187], v[172:175]\n\t" \
    "s_waitcnt lgkmcnt(6)\n\t" \
    "v_mfma_f32_16x16x32_bf16 v[168:171], v[12:15], v[188:191], v[168:171]\n\t" \
    "ds_read_b128 v[184:187], v249 offset:10240\n\t" \
    "v_mfma_f32_16x16x32_bf16 v[172:175], v[92:95], v[188:191], v[172:175]\n\t" \
    "s_waitcnt lgkmcnt(6)\n\t" \
    "v_mfma_f32_16x16x32_bf16 v[168:171], v[16:19], v[192:195], v[168:171]\n\t" \
    "ds_read_b128 v[188:191], v249 offset:11264\n\t" \
    "v_mfma_f32_16x16x32_bf16 v[172:175], v[96:99], v[192:195], v[172:175]\n\t" \
    "s_waitcnt lgkmcnt(6)\n\t" \
    "v_mfma_f32_16x16x32_bf16 v[168:171], v[20:23], v[196:199], v[168:171]\n\t" \
    "ds_read_b128 v[192:195], v249 offset:12288\n\t" \
    "v_mfma_f32_16x16x32_bf16 v[172:175], v[100:103], v[196:199], v[172:175]\n\t" \
    "s_waitcnt lgkmcnt(6)\n\t" \
    "v_mfma_f32_16x16x32_bf16 v[168:171], v[24:27], v[200:203], v[168:171]\n\t" \
    "ds_read_b128 v[196:199], v249 offset:13312\n\t" \
    "v_mfma_f32_16x16x32_bf16 v[172:175], v[104:107], v[200:203], v[172:175]\n\t" \
    "s_waitcnt lgkmcnt(6)\n\t" \
    "v_mfma_f32_16x16x32_bf16 v[168:171], v[28:31], v[204:207], v[168:171]\n\t" \
    "ds_read_b128 v[200:203], v249 offset:14336\n\t" \
    "v_mfma_f32_16x16x32_bf16 v[172:175], v[108:111], v[204:207], v[172:175]\n\t" \
    "s_waitcnt lgkmcnt(6)\n\t" \
    "v_mfma_f32_16x16x32_bf16 v[168:171], v[32:35], v[176:179], v[168:171]\n\t" \
    "ds_read_b128 v[204:207], v249 offset:15360\n\t" \
    "v_mfma_f32_16x16x32_bf16 v[172:175], v[112:115], v[176:179], v[172:175]\n\t" \
    "s_waitcnt lgkmcnt(6)\n\t" \
    "v_mfma_f32_16x16x32_bf16 v[168:171], v[36:39], v[180:183], v[168:171]\n\t" \
    "ds_read_b128 v[176:179], v249 offset:16384\n\t" \
    "v_mfma_f32_16x16x32_bf16 v[172:175], v[116:119], v[180:183], v[172:175]\n\t" \
    "s_waitcnt lgkmcnt(6)\n\t" \
    "v_mfma_f32_16x16x32_bf16 v[168:171], v[40:43], v[184:187], v[168:171]\n\t" \
    "ds_read_b128 v[180:183], v249 offset:17408\n\t" \
    "v_mfma_f32_16x16x32_bf16 v[172:175], v[120:123], v[184:187], v[172:175]\n\t" \
    "s_waitcnt lgkmcnt(6)\n\t" \
    "v_mfma_f32_16x16x32_bf16 v[168:171], v[44:47], v[188:191], v[168:171]\n\t" \
    "ds_read_b128 v[184:187], v249 offset:18432\n\t" \
    "v_mfma_f32_16x16x32_bf16 v[172:175], v[124:127], v[188:191], v[172:175]\n\t" \
    "s_waitcnt lgkmcnt(6)\n\t" \
    "v_mfma_f32_16x16x32_bf16 v[168:171], v[48:51], v[192:195], v[168:171]\n\t" \
    "ds_read_b128 v[188:191], v249 offset:19456\n\t" \
    "v_mfma_f32_16x16x32_bf16 v[172:175], v[128:131], v[192:195], v[172:175]\n\t" \
    "s_waitcnt lgkmcnt(6)\n\t" \
    "v_mfma_f32_16x16x32_bf16 v[168:171], v[52:55], v[196:199], v[168:171]\n\t" \
    "v_mfma_f32_16x16x32_bf16 v[172:175], v[132:135], v[196:199], v[172:175]\n\t" \
    "s_waitcnt lgkmcnt(5)\n\t" \
    "v_mfma_f32_16x16x32_bf16 v[168:171], v[56:59], v[200:203], v[168:171]\n\t" \
    "v_mfma_f32_16x16x32_bf16 v[172:175], v[136:139], v[200:203], v[172:175]\n\t" \
    "s_cmp_gt_u32 %[wave], 3\n\t" \
    "s_cbranch_scc1 .Lw316_%=\n\t" \
    "s_add_u32 s47, s46, 0x0\n\t" \
    "buffer_load_dwordx4 v[208:211], v253, %[rs], s47 offen sc1\n\t" \
    "s_add_u32 s47, s46, 0x4000\n\t" \
    "buffer_load_dwordx4 v[212:215], v253, %[rs], s47 offen sc1\n\t" \
    "s_add_u32 s47, s46, 0x8000\n\t" \
    "buffer_load_dwordx4 v[216:219], v253, %[rs], s47 offen sc1\n\t" \
    "s_add_u32 s47, s46, 0xc000\n\t" \
    "buffer_load_dwordx4 v[220:223], v253, %[rs], s47 offen sc1\n\t" \
    "s_add_u32 s47, s46, 0x10000\n\t" \
    "buffer_load_dwordx4 v[224:227], v253, %[rs], s47 offen sc1\n\t" \
    ".Lw316_%=:\n\t" \
    "s_waitcnt lgkmcnt(4)\n\t" \
    "v_mfma_f32_16x16x32_bf16 v[168:171], v[60:63], v[204:207], v[168:171]\n\t" \
    "v_mfma_f32_16x16x32_bf16 v[172:175], v[140:143], v[204:207], v[172:175]\n\t" \
    "ds_read_b32 v236, v251 offset:72\n\t" \
    "ds_read_b64 v[238:239], v251 offset:64\n\t" \
    "s_waitcnt lgkmcnt(5)\n\t" \
    "v_mfma_f32_16x16x32_bf16 v[168:171], v[64:67], v[176:179], v[168:171]\n\t" \
    "v_mfma_f32_16x16x32_bf16 v[172:175], v[144:147], v[176:179], v[172:175]\n\t" \
    "s_waitcnt lgkmcnt(4)\n\t" \
    "v_mfma_f32_16x16x32_bf16 v[168:171], v[68:71], v[180:183], v[168:171]\n\t" \
    "v_mfma_f32_16x16x32_bf16 v[172:175], v[148:151], v[180:183], v[172:175]\n\t" \
    ".Lw317_%=:\n\t" \
    "s_waitcnt lgkmcnt(0)\n\t" \
    "v_cmp_ne_u32 vcc, s40, v236\n\t" \
    "s_cbranch_vccz .Lw318_%=\n\t" \
    "ds_read_b32 v236, v251 offset:72\n\t" \
    "ds_read_b64 v[238:239], v251 offset:64\n\t" \
    "s_branch .Lw317_%=\n\t" \
    ".Lw318_%=:\n\t" \
    "s_waitcnt lgkmcnt(0)\n\t" \
    "v_mul_f32 v240, v160, v239\n\t" \
    "v_mul_f32 v241, v161, v239\n\t" \
    "v_mfma_f32_16x16x32_bf16 v[168:171], v[72:75], v[184:187], v[168:171]\n\t" \
    "v_mul_f32 v242, v162, v239\n\t" \
    "v_mul_f32 v243, v163, v239\n\t" \
    "v_mfma_f32_16x16x32_bf16 v[172:175], v[152:155], v[184:187], v[172:175]\n\t" \
    "v_mul_f32 v244, v164, v239\n\t" \
    "v_mul_f32 v245, v165, v239\n\t" \
    "v_mfma_f32_16x16x32_bf16 v[168:171], v[76:79], v[188:191], v[168:171]\n\t" \
    "v_mul_f32 v246, v166, v239\n\t" \
    "v_mul_f32 v247, v167, v239\n\t" \
    "v_mfma_f32_16x16x32_bf16 v[172:175], v[156:159], v[188:191], v[172:175]\n\t" \
    "v_mov_b32 v237, 0xc0135761\n\t" \
    "s_cmp_lt_u32 %[wave], 4\n\t" \
    "s_cbranch_scc1 .Lw319_%=\n\t" \
    "s_add_u32 s44, s40, 2\n\t" \
    "v_mov_b32 v248, s44\n\t" \
    "s_lshl_b64 exec, 1, %[wave]\n\t" \
    "ds_write_b32 v255, v248\n\t" \
    "s_mov_b64 exec, -1\n\t" \
    ".Lw319_%=:\n\t" \
    "s_nop 1\n\t" \
    "s_nop 2\n\t" \
    "v_fma_f32 v168, v168, v238, v240\n\t" \
    "v_fma_f32 v169, v169, v238, v241\n\t" \
    "v_fma_f32 v170, v170, v238, v242\n\t" \
    "v_fma_f32 v171, v171, v238, v243\n\t" \
    "v_fma_f32 v172, v172, v238, v244\n\t" \
    "v_fma_f32 v173, v173, v238, v245\n\t" \
    "v_fma_f32 v174, v174, v238, v246\n\t" \
    "v_fma_f32 v175, v175, v238, v247\n\t" \
    "v_mul_f32 v176, v168, v168\n\t" \
    "v_mul_f32 v177, v169, v169\n\t" \
    "v_mul_f32 v178, v170, v170\n\t" \
    "v_mul_f32 v179, v171, v171\n\t" \
    "v_mul_f32 v180, v172, v172\n\t" \
    "v_mul_f32 v181, v173, v173\n\t" \
    "v_mul_f32 v182, v174, v174\n\t" \
    "v_mul_f32 v183, v175, v175\n\t" \
    "v_fmamk_f32 v176, v176, 0xbdd2d3e7, v237\n\t" \
    "v_fmamk_f32 v177, v177, 0xbdd2d3e7, v237\n\t" \
    "v_fmamk_f32 v178, v178, 0xbdd2d3e7, v237\n\t" \
    "v_fmamk_f32 v179, v179, 0xbdd2d3e7, v237\n\t" \
    "v_fmamk_f32 v180, v180, 0xbdd2d3e7, v237\n\t" \
    "v_fmamk_f32 v181, v181, 0xbdd2d3e7, v237\n\t" \
    "v_fmamk_f32 v182, v182, 0xbdd2d3e7, v237\n\t" \
    "v_fmamk_f32 v183, v183, 0xbdd2d3e7, v237\n\t" \
    "v_mul_f32 v176, v168, v176\n\t" \
    "v_mul_f32 v177, v169, v177\n\t" \
    "v_mul_f32 v178, v170, v178\n\t" \
    "v_mul_f32 v179, v171, v179\n\t" \
    "v_mul_f32 v180, v172, v180\n\t" \
    "v_mul_f32 v181, v173, v181\n\t" \
    "v_mul_f32 v182, v174, v182\n\t" \
    "v_mul_f32 v183, v175, v183\n\t" \
    "v_exp_f32 v176, v176\n\t" \
    "v_exp_f32 v177, v177\n\t" \
    "v_exp_f32 v178, v178\n\t" \
    "v_exp_f32 v179, v179\n\t" \
    "v_exp_f32 v180, v180\n\t" \
    "v_exp_f32 v181, v181\n\t" \
    "v_exp_f32 v182, v182\n\t" \
    "v_exp_f32 v183, v183\n\t" \
    "v_add_f32 v176, 1.0, v176\n\t" \
    "v_add_f32 v177, 1.0, v177\n\t" \
    "v_add_f32 v178, 1.0, v178\n\t" \
    "v_add_f32 v179, 1.0, v179\n\t" \
    "v_add_f32 v180, 1.0, v180\n\t" \
    "v_add_f32 v181, 1.0, v181\n\t" \
    "v_add_f32 v182, 1.0, v182\n\t" \
    "v_add_f32 v183, 1.0, v183\n\t" \
    "v_rcp_f32 v176, v176\n\t" \
    "v_rcp_f32 v177, v177\n\t" \
    "v_rcp_f32 v178, v178\n\t" \
    "v_rcp_f32 v179, v179\n\t" \
    "v_rcp_f32 v180, v180\n\t" \
    "v_rcp_f32 v181, v181\n\t" \
    "v_rcp_f32 v182, v182\n\t" \
    "v_rcp_f32 v183, v183\n\t" \
    "v_mul_f32 v168, v168, v176\n\t" \
    "v_mul_f32 v169, v169, v177\n\t" \
    "v_mul_f32 v170, v170, v178\n\t" \
    "v_mul_f32 v171, v171, v179\n\t" \
    "v_mul_f32 v172, v172, v180\n\t" \
    "v_mul_f32 v173, v173, v181\n\t" \
    "v_mul_f32 v174, v174, v182\n\t" \
    "v_mul_f32 v175, v175, v183\n\t" \
    "s_waitcnt vmcnt(0)\n\t" \
    "v_cvt_pk_bf16_f32 v184, v168, v169\n\t" \
    "v_cvt_pk_bf16_f32 v185, v170, v171\n\t" \
    "v_cvt_pk_bf16_f32 v186, v172, v173\n\t" \
    "v_cvt_pk_bf16_f32 v187, v174, v175\n\t" \
    "v_min_u32 v184, -2, v184\n\t" \
    "v_min_u32 v185, -2, v185\n\t" \
    "v_min_u32 v186, -2, v186\n\t" \
    "v_min_u32 v187, -2, v187\n\t" \
    "s_nop 0\n\t" \
    "v_permlane16_swap_b32 v184, v186\n\t" \
    "v_permlane16_swap_b32 v185, v187\n\t" \
    "buffer_store_dwordx4 v[184:187], v254, %[rs], s49 offen sc1\n\t" \
    "s_cmp_ge_u32 s40, 122\n\t" \
    "s_cbranch_scc1 .Lw320_%=\n\t" \
    "s_add_i32 s44, s40, 6\n\t" \
    "s_max_i32 s44, s44, 0\n\t" \
    "s_lshr_b32 s45, s44, 1\n\t" \
    "s_and_b32 s44, s44, 1\n\t" \
    "s_lshl_b32 s44, s44, 12\n\t" \
    "s_mul_i32 s50, s45, 0x30000\n\t" \
    "s_add_u32 s50, s50, s44\n\t" \
    "s_lshr_b32 s45, s45, 3\n\t" \
    "s_mul_i32 s45, s45, 0x82000\n\t" \
    "s_add_u32 s50, s50, s45\n\t" \
    "s_add_u32 s50, s50, %[ng]\n\t" \
    "v_mov_b32 v188, -1\n\t" \
    "v_mov_b32 v189, -1\n\t" \
    "v_mov_b32 v190, -1\n\t" \
    "v_mov_b32 v191, -1\n\t" \
    "buffer_store_dwordx4 v[188:191], v253, %[rs], s50 offen sc1\n\t" \
    ".Lw320_%=:\n\t" \
    "v_xor_b32 v249, 0x8000, v249\n\t" \
    "v_xor_b32 v250, 0x8000, v250\n\t" \
    "v_xor_b32 v251, 0x800, v251\n\t" \
    "v_xor_b32 v252, 0x800, v252\n\t" \
    "s_add_u32 s40, s40, 1\n\t" \
    "s_cmp_lg_u32 s40, 128\n\t" \
    "s_cbranch_scc1 .Lw34_%=\n\t" \
    "s_branch .Lend%=\n\t" \
    ".Lend%=:\n\t" \
    "s_waitcnt vmcnt(0) lgkmcnt(0)\n\t" \
    "s_mov_b32 %[bud], s41\n\t"
using u32x8 = __attribute__((ext_vector_type(8))) unsigned;
using u32x16 = __attribute__((ext_vector_type(16))) unsigned;
using u32x32 = __attribute__((ext_vector_type(32))) unsigned;
__device__ __forceinline__ u32x32 cat32(u32x8 a, u32x8 b, u32x8 c, u32x8 d) {
    const u32x16 lo = __builtin_shufflevector(a, b, 0, 1, 2, 3, 4, 5, 6, 7, 8, 9, 10, 11, 12, 13, 14, 15);
    const u32x16 hi = __builtin_shufflevector(c, d, 0, 1, 2, 3, 4, 5, 6, 7, 8, 9, 10, 11, 12, 13, 14, 15);
    return __builtin_shufflevector(lo, hi, 0, 1, 2, 3, 4, 5, 6, 7, 8, 9, 10, 11, 12, 13, 14, 15, 16, 17, 18, 19, 20, 21, 22, 23, 24, 25, 26, 27, 28, 29, 30, 31);
}
__device__ __forceinline__ unsigned c1_dst(int wave, int kind) {
    const int ln = lane_id(), c = ln & 15, g = ln >> 4, hf = (wave >> 1) & 1;
    if (kind == 2) {
        const unsigned b = L_KM + (c >> 2) * 256 + (wave & 1) * 128 + g * 32;
        return (b + hf * 8 + (c & 3) * 2) | (b + 1024 + hf * 1024 + (c & 3) * 4) << 16;
    }
    const unsigned b = L_Q + (c >> 3) * 256 + g * 64 + ((c >> 2) & 1) * 8;
    return (b + (wave & 1) * (kind ? 512 : 1536) + ((c >> 1) & 1) * 32 + hf * 4 + (c & 1) * 2) | (b + 1024 + (wave & 3) * 512 + (c & 1) * 32 + ((c >> 1) & 1) * 4) << 16;
}
__device__ __forceinline__ void dma1(rsrc_t rs, unsigned vo, unsigned so, unsigned dst) {
    asm volatile("s_mov_b32 m0, %3\n\ts_nop 0\n\tbuffer_load_dwordx4 %0, %1, %2 offen nt lds" :: "v"(vo), "s"(rs), "s"(so), "s"(dst) : "memory");
}
using s16x4 = __attribute__((ext_vector_type(4))) short;
__device__ __forceinline__ u32x4 tr_frag(unsigned a, unsigned step) {
    const u32x2 x = __builtin_bit_cast(u32x2, __builtin_amdgcn_ds_read_tr16_b64_v4i16((LAS s16x4*)(size_t)a)), y = __builtin_bit_cast(u32x2, __builtin_amdgcn_ds_read_tr16_b64_v4i16((LAS s16x4*)(size_t)(a + step)));
    return (u32x4){x[0], x[1], y[0], y[1]};
}
#define LOOP_CLOB "memory", "vcc", "scc", "v0", "v1", "v2", "v3", "v4", "v5", "v6", "v7", "v8", "v9", "v10", "v11", "v12", "v13", "v14", "v15", "v16", "v17", "v18", "v19", "v20", "v21", "v22", "v23", "v24", "v25", "v26", "v27", "v28", "v29", "v30", "v31", "v32", "v33", "v34", "v35", "v36", "v37", "v38", "v39", "v40", "v41", "v42", "v43", "v44", "v45", "v46", "v47", "v48", "v49", "v50", "v51", "v52", "v53", "v54", "v55", "v56", "v57", "v58", "v59", "v60", "v61", "v62", "v63", "v64", "v65", "v66", "v67", "v68", "v69", "v70", "v71", "v72", "v73", "v74", "v75", "v76", "v77", "v78", "v79", "v80", "v81", "v82", "v83", "v84", "v85", "v86", "v87", "v88", "v89", "v90", "v91", "v92", "v93", "v94", "v95", "v96", "v97", "v98", "v99", "v100", "v101", "v102", "v103", "v104", "v105", "v106", "v107", "v108", "v109", "v110", "v111", "v112", "v113", "v114", "v115", "v116", "v117", "v118", "v119", "v120", "v121", "v122", "v123", "v124", "v125", "v126", "v127", "v128", "v129", "v130", "v131", "v132", "v133", "v134", "v135", "v136", "v137", "v138", "v139", "v140", "v141", "v142", "v143", "v144", "v145", "v146", "v147", "v148", "v149", "v150", "v151", "v152", "v153", "v154", "v155", "v156", "v157", "v158", "v159", "v160", "v161", "v162", "v163", "v164", "v165", "v166", "v167", "v168", "v169", "v170", "v171", "v172", "v173", "v174", "v175", "v176", "v177", "v178", "v179", "v180", "v181", "v182", "v183", "v184", "v185", "v186", "v187", "v188", "v189", "v190", "v191", "v192", "v193", "v194", "v195", "v196", "v197", "v198", "v199", "v200", "v201", "v202", "v203", "v204", "v205", "v206", "v207", "v208", "v209", "v210", "v211", "v212", "v213", "v214", "v215", "v216", "v217", "v218", "v219", "v220", "v221", "v222", "v223", "v224", "v225", "v226", "v227", "v228", "v229", "v230", "v231", "v232", "v233", "v234", "v235", "v236", "v237", "v238", "v239", "v246", "v247", "v248", "v249", "v250", "v251", "v252", "s40", "s41", "s42", "s43", "s44", "s45", "s46", "s47", "s48", "s49", "s50", "s51", "s52", "s53", "s54", "s55", "s56", "s57", "s58", "s59", "s60", "s61", "s62", "s63", "s64", "s65", "s66", "s67", "s68", "s69", "s70", "s71", "s72", "s73", "s74", "s75", "s76", "s77", "s78", "s79"
__device__ __forceinline__ unsigned UNI(unsigned x) {
    unsigned r;
    asm volatile("s_nop 0\n\tv_readfirstlane_b32 %0, %1\n\ts_nop 4" : "=s"(r) : "v"(x));
    return r;
}
__device__ __forceinline__ void note_timeout(const Params& P, unsigned bud) {
    if (bud == 0) __builtin_amdgcn_raw_buffer_store_b32(1u, mkrsrc(P.ws + FL0, 512), 0x100, 0, 16);
}

#define LOOP_IO [bud] "=s"(bud), [mb] "+s"(mb), [pb] "+s"(pb), [fl] "+s"(fl), "+{v253}"(l16), "+{v240}"(W.a[0]), "+{v241}"(W.a[1]), "+{v242}"(W.a[2]), "+{v243}"(W.a[3]), "+{v244}"(W.a[4]), "+{v245}"(W.a[5])
#define LOOP_IN "{v254}"(b2), "{v255}"(dsto), [wave] "s"(wave), [ws] "s"(P.ws), [mo] "s"(FL_MB + blockIdx.x * 128 + wave * 4), [sz] "s"(RG_L), [lx] "s"(L << 13), [wr] "s"(wdesc(P, wave, isv, h)), [wm] "s"(UNI(wstage(wave, isv))), \
                [cj] "s"(REGS - 75 * WIN), [cs] "s"(5 * WIN)
struct WLoad { unsigned a[6]; };
__device__ __forceinline__ rsrc_t wdesc(const Params& P, int wave, bool isv, int h) {
    const unsigned wo = (isv && wave >= 4) ? h * DH * DM + (wave & 3) * 160 : h * DH;
    typedef const __attribute__((address_space(4))) unsigned long long* kptr;
    static_assert(__builtin_offsetof(Params, wq) == 24 && __builtin_offsetof(Params, wo) == 48, "argument order");
    const unsigned long long w = ((kptr)__builtin_amdgcn_kernarg_segment_ptr())[3 + (isv ? 2 : 0) + (wave >> 2)];
    return mkrsrc((const bf16_t*)w + wo, (DM * HD - wo) * 2);
}
__device__ __forceinline__ unsigned wstage(int wave, bool isv) { return (isv && wave >= 4) ? L_W + (wave & 3) * 20480 : wave * 3072; }
template <bool START>
__device__ __forceinline__ void wload_start(WLoad& W, const Params& P, int wave, bool isv, int h) {
    const int ln = lane_id(), w4 = wave & 3;
    const rsrc_t wr = wdesc(P, wave, isv, h);
    const unsigned wm = UNI(wstage(wave, isv));
    if (isv && wave >= 4) {
#pragma unroll
        for (int k = 0; k < 5; ++k) { const unsigned u = k * 64 + ln; W.a[k] = (u / 20) * (DM * 2) + (u % 20) * 16 - (k & 3) * 1024; }
#pragma unroll 1
        for (int k = 0; k < (START ? 4 : 0); ++k)
            asm volatile("s_mov_b32 m0, %7\n\ts_nop 0\n\tbuffer_load_dwordx4 %0, %5, %6 offen nt lds\n\tbuffer_load_dwordx4 %1, %5, %6 offen offset:1024 nt lds\n\tbuffer_load_dwordx4 %2, %5, %6 offen offset:2048 nt lds\n\t"
                         "buffer_load_dwordx4 %3, %5, %6 offen offset:3072 nt lds\n\ts_mov_b32 m0, %8\n\ts_nop 0\n\tbuffer_load_dwordx4 %4, %5, %6 offen nt lds"
                         :: "v"(W.a[0]), "v"(W.a[1]), "v"(W.a[2]), "v"(W.a[3]), "v"(W.a[4]), "s"(wr), "s"(k * 20480), "s"(wm + k * 5120), "s"(wm + k * 5120 + 4096) : "memory");
        W.a[5] = L_W + w4 * 20480 + (ln >> 2) * 320 + (ln & 3) * 8;
    } else {
        const unsigned sl = isv ? 12288 : 24576;
#pragma unroll
        for (int k = 0; k < 3; ++k) { const unsigned u = (w4 * 3 + k) * 64 + ln; W.a[k] = (u / 24) * (HD * 2) + (u % 24) * 16 - k * 1024; }
#pragma unroll 1
        for (int k = 0; k < (START ? 5 : 0); ++k)
            asm volatile("s_mov_b32 m0, %5\n\ts_nop 0\n\tbuffer_load_dwordx4 %0, %3, %4 offen nt lds\n\tbuffer_load_dwordx4 %1, %3, %4 offen offset:1024 nt lds\n\tbuffer_load_dwordx4 %2, %3, %4 offen offset:2048 nt lds"
                         :: "v"(W.a[0]), "v"(W.a[1]), "v"(W.a[2]), "s"(wr), "s"(k * 32 * HD * 2), "s"(wm + (k < 2 ? k + 4 : k - 2) * sl) : "memory");
        W.a[3] = (wave >> 2) * 12288 + ((ln >> 4) * 8 + ((ln >> 2) & 3)) * 384 + (ln & 3) * 8 + w4 * 96;
        W.a[4] = 0;
        W.a[5] = 0;
    }
}
__device__ __forceinline__ void s_role(const Params& P, int wave, int L, int h, int cpy) {
    const int ln = lane_id();
    unsigned l16 = ln * 16;
    constexpr bool isv = false;
    WLoad W;
    wload_start<false>(W, P, wave, isv, h);
    const int mt = (wave >> 1) & 1, hf = wave & 1;
    const unsigned xs = D_XN + mt * 4096 + hf * WIN, x1 = (2 - hf) * WIN + hf * 2048, m0 = L_X + mt * 20480 + hf * 4096, m1 = L_X + mt * 20480 + 8192 + hf * 2048;
    const float SCL = 0.07216878364870322f * 1.4426950408889634f;
    const unsigned px = RG_PX + (h >> 2) * WIN;
    unsigned mb = UNI(blk(10, cpy)), fl = UNI(blk(15, cpy)), pb = UNI(wave == 0 ? blk(32 + (h >> 2), 0) + (h & 3) * 640 : px + (wave == 2 ? (h & 3) * 1024 : 5120 + ((h >> 1) & 1) * 1024 + (h & 1) * 256));
    unsigned b2 = l16 + (wave < 3 ? L_Q : wave == 3 ? L_QX : L_W + wave * 10240), dsto = c1_dst(wave, 1 + (wave >> 2)), bud;
    asm volatile(ASM_S_LOOP : LOOP_IO
                 : LOOP_IN, [xs] "s"(xs), [x1] "s"(x1), [m0] "s"(m0), [m1] "s"(m1), [qs] "s"(UNI(L_KX + hf * 1024 + mt * 8)), [q2] "s"(UNI(L_KX + 2048 + (wave & 3) * 1024)),
                   [pm] "s"(UNI(px + (wave == 2 ? 4096 : 7168) + (h & 3) * 256)), [scl] "s"(__float_as_uint(SCL))
                 : LOOP_CLOB);
    note_timeout(P, bud);
}

__device__ __forceinline__ void v_role(const Params& P, int wave, int L, int h, int cpy) {
    const int ln = lane_id(), w4 = wave & 3;
    unsigned l16 = ln * 16;
    constexpr bool isv = true;
    WLoad W;
    wload_start<false>(W, P, wave, isv, h);
    unsigned b2 = l16 + (wave < 3 ? L_Q : wave == 3 ? L_QX : L_W + wave * 10240), dsto = wave < 4 ? c1_dst(wave, 0) : (l16 >> 8) * GRP + (l16 & 255), bud;
    unsigned mb = UNI(blk(10, cpy)), fl = UNI(wave < 4 ? blk(15, cpy) : blk(14, w4) + h * 128), pb = UNI(blk(h & 16, w4) + (h & 15) * 256);
    if (wave >= 4) l16 += L_Q;
    asm volatile(ASM_V_LOOP : LOOP_IO
                 : LOOP_IN, [xs] "s"(D_XN + w4 * WIN), [vm] "s"(VXD - 1), [vx] "s"(vxb(w4, 0) + (h >> 3) * WIN + (h & 7) * 1024 - L_Q)
                 : LOOP_CLOB);
    note_timeout(P, bud);
}

#define D_W0 4
#define D_A1 8
#define D_R1 9
#define D_W1 12
__device__ __forceinline__ void delay(int n) { for (int i = 0; i < n; ++i) __builtin_amdgcn_s_sleep(64); }
__device__ __forceinline__ unsigned handshake(rsrc_t rw, unsigned mo) {
    wait_vm<0>();
    unsigned v = SENT;
    for (int spins = 0; spins < SPIN_CAP && v == SENT; ++spins) {
        asm volatile("s_sleep 4" ::: "memory");
        v = __builtin_amdgcn_readfirstlane(__builtin_amdgcn_raw_buffer_load_b32(rw, 0, mo, 16));
    }
    __builtin_amdgcn_raw_buffer_store_b32(1u, rw, lane_id() ? OOB : 32768u, mo, 16);
    return v & 0x7000;
}
template <class F>
__device__ __forceinline__ void x_tile(const Params& P, int tile, F target) {
    const int ln = lane_id(), l15 = ln & 15, l4 = ln >> 4;
    u32x4 v[20];
    float s = 0.f, q = 0.f;
#pragma unroll
    for (int c = 0; c < 20; ++c) v[c] = *(const u32x4*)(P.x + (size_t)(tile * 16 + l15) * DM + c * 32 + l4 * 8);
#pragma unroll
    for (int c = 0; c < 20; ++c)
#pragma unroll
        for (int e = 0; e < 4; ++e) s += lo2f(v[c][e]) + hi2f(v[c][e]);
    sum2_rows(s, q);
#pragma unroll
    for (int c = 0; c < 20; ++c) asm volatile("" : "+v"(v[c]));
    const float mean = s * (1.0f / DM);
#pragma unroll
    for (int c = 0; c < 20; ++c)
#pragma unroll
        for (int e = 0; e < 4; ++e) { const float a = lo2f(v[c][e]) - mean, b = hi2f(v[c][e]) - mean; q += a * a + b * b; }
    sum2_rows(s, q);
#pragma unroll
    for (int c = 0; c < 20; ++c) asm volatile("" : "+v"(v[c]));
    const float m1 = rbf(mean), r = 1.0f / ln_den(q * (1.0f / DM));
    const unsigned sb = segb(tile >> 1) + (tile & 1) * 4096;
    u32x4 n[20];
#pragma unroll
    for (int c = 0; c < 20; ++c) {
#pragma unroll
        for (int e = 0; e < 4; ++e) { const unsigned d = pack2(lo2f(v[c][e]) - m1, hi2f(v[c][e]) - m1); n[c][e] = pack2(lo2f(d) * r, hi2f(d) * r); }
        asm volatile("" : "+v"(n[c]));
    }
    const rsrc_t rr = target();
#pragma unroll
    for (int c = 0; c < 20; ++c) {
        st128(n[c], rr, ln * 16 + (c & 3) * 1024, D_XN + sb + (c >> 2) * WIN);
        st128(v[c], rr, ln * 16 + (c & 3) * 1024, D_XR + sb + (c >> 2) * WIN);
    }
    wait_vm<0>();
}
__device__ __forceinline__ void wait_go(const Params& P, rsrc_t rd, int k) {
    for (int spins = 0;; ++spins) {
        if (__builtin_amdgcn_readfirstlane(__builtin_amdgcn_raw_buffer_load_b32(rd, 0, FL_VO + (k & 31) * 128, 16))) break;
        asm volatile("s_sleep 16" ::: "memory");
        if (spin_fail(spins * 4 + 3, P)) break;
    }
    asm volatile("" ::: "memory");
}

constexpr unsigned R_RED = 0;
constexpr unsigned R_PART = 8192;
__device__ __forceinline__ void r_role(const Params& P, int wave, int L, int j, int cpy) {
    const int lane = lane_id(), l15 = lane & 15, l4 = lane >> 4;
    const unsigned l16 = lane * 16;
    const rsrc_t rr = mkrsrc(lbase(P, L), RG_L);
    const int sidx = (lane & 2) ? 0xEEEE : 0x4444;
    unsigned offp[3];
    const unsigned sel = P.par & 0x5000;
    auto hq = [&](int q) { return blk(32 + q, cpy) ^ sel; };
#define ROT 1
    const int hg = (wave + ROT * j) & 7;
    offp[0] = hq(hg) + (lane >> 5) * 640 + 128 + (lane & 31) * 16;
    offp[1] = offp[0] + 1280;
    offp[2] = (lane & 2) ? OOB : hq(hg) + l4 * 640 + ((l15 >> 2) * 2 + (l15 & 1)) * 16;
    const int jq = j / 10, j10 = j % 10;
    const unsigned ae = wave < 3 ? ((j10 < 8 && wave * 1024 + l16 < 2560) ? (blk(32 + j10, jq) ^ sel) + wave * 1024 + l16 : OOB)
                      : wave == 3 ? (blk(10 + (j10 >> 2), jq) ^ sel) + (j10 & 3) * 1024 + l16
                      : wave < 6 ? (j < 32 ? RG_PX + (j >> 2) * WIN + (j & 3) * 2048 + (wave & 1) * 1024 + l16 : OOB)
                      : wave == 7 ? D_XA + (j >> 3) * WIN + (j & 7) * 512 + (lane >> 5) * 4096 + (lane & 31) * 16 : OOB;
    const unsigned as = wave < 4 ? CH_SLOT : PX_SLOT;
    static_assert(AH == 3 && VXD == 4, "generated loop");
    const unsigned offv = (blk((hg >> 2) * 16 + j10, jq) ^ sel) + (hg & 3) * 1024 + l16;
    const unsigned offo = (wave < (int)NCP) ? (blk(10 + (j >> 4), wave) ^ sel) + (j & 15) * 256 + lane * 4 : OOB;
    {
        const u32x4 s4 = sent4();
#pragma unroll
        for (int k = 0; k < (int)NSLOT; ++k) {
            st128(s4, rr, offv, k * CH_SLOT);
            if (wave < 6 && (k || wave != 3)) st128(s4, rr, ae, k * as);
        }
        for (int u = j; u < 126; u += 40) st128(s4, rr, wave * 1024 + l16, u < 108 ? D_XA + (u < 60 ? u / 20 : 3) * 4 * REG + (u < 60 ? u % 20 : u - 60) * WIN : u < 124 ? RG_ST + (u - 108) * WIN : D_SS + (u - 124) * WIN);
        wait_vm<0>();
        bar();
        const int row = l4 * 2 + ((lane >> 2) & 1), col = 16 * j + 4 * (lane & 3) + 2 * ((lane >> 3) & 1);
        __builtin_amdgcn_raw_buffer_store_b32(nosent(*(const unsigned*)(P.mem0 + row * DM + col)), rr, offo, 0, 16);
    }
    auto red_addr = [](int fl) { return R_RED + fl * 80 + (fl >> 4) * 16; };
    const unsigned rw0 = red_addr(l4 * 16 + ((l15 >> 1) & 1) * 8 + (l15 >> 2)) + (l15 & 1) * 4 + (hg ^ (l15 >> 3)) * 8;
    const unsigned rrd = red_addr(lane);
    unsigned bud;
    asm volatile(ASM_R_LOOP : [bud] "=s"(bud)
                 : [wave] "s"(wave), [rs] "s"(rr), [l16] "v"(l16), [ov] "v"(offv), [o0] "v"(offp[0]), [o1] "v"(offp[1]), [o2] "v"(offp[2]), [oo] "v"(offo), [sidx] "v"(sidx), [rw] "v"(rw0), [rd] "v"(rrd),
                   [xo] "v"((l4 >> 1) * 256 + l15 * 16 + (l4 & 1) * 8), [pw] "v"(R_PART + hg * 1024 + l16), [pr] "v"(R_PART + (wave & 1) * 8192 + l16), [fo] "v"((lane & 3) * 128), [ae] "v"(ae),
                   [fs] "s"(UNI((blk(14, jq) ^ sel) + hg * 512)), [vx] "s"(UNI(vxb(jq, 0) + (j10 * 4 + (hg >> 1)) * WIN + (hg & 1) * 4096)), [px] "s"(UNI(RG_PX + hg * WIN)),
                   [xr] "s"(UNI(D_XR + (j >> 3) * WIN + (wave & 1) * 4096 + (j & 7) * 512)), [ad] "s"(UNI(wave < 3 ? -1 : wave == 3 ? 0 : -2)), [as] "s"(UNI(as))
                 : "memory", "vcc", "scc", "v128", "v129", "v130", "v131", "v132", "v133", "v134", "v135", "v136", "v137", "v138", "v139", "v140", "v141", "v142", "v143", "v144", "v145", "v146", "v147", "v148", "v149", "v150", "v151", "v152", "v153", "v154", "v155", "v156", "v157", "v158", "v159", "v160", "v161", "v162", "v163", "v164", "v165", "v166", "v167", "v168", "v169", "v170", "v171", "v172", "v173", "v174", "v175", "v176", "v177", "v178", "v179", "v180", "v181", "v182", "v183", "v184", "v185", "v186", "v187", "v188", "v189", "v190", "v191", "v192", "v193", "v194", "v195", "v196", "v197", "v198", "v199", "v200", "v201", "v202", "v203", "v204", "v205", "v206", "v207", "v208", "v209", "v210", "v211", "v212", "v213", "v214", "v215", "v216", "v217", "v218", "v219", "v220", "v221", "v222", "v223", "v224", "v225", "v226", "v227", "v228", "v229", "v230", "v231", "v232", "v233", "v234", "v235", "v236", "v237", "v238", "v239", "v240", "v241", "v242", "v243", "v244", "v245", "v246", "v247", "v248", "v249", "v250", "v251", "v252", "v253", "v254", "v255", "s40", "s41", "s42", "s43", "s44", "s45", "s46", "s47", "s48", "s49", "s50", "s51", "s52", "s53", "s54", "s55", "s64", "s65", "s66", "s67", "s68", "s69", "s70", "s71", "s72", "s73", "s74", "s75", "s76", "s77", "s78", "s79");
    note_timeout(P, bud);
}

template <int WHICH>
__device__ __forceinline__ void w_role(const Params& P, int wave, int L, int cu) {
    constexpr int NT = (WHICH == 3) ? 2 : 1, N = (WHICH == 3) ? FF : DM, RB = NT * 256, RPI = 1024 / RB;
    const int ln = lane_id(), l15 = ln & 15, l4 = ln >> 4;
    const rsrc_t rd = mkrsrc(lbase(P, L), RG_L);
    const int nt0 = (cu * 8 + wave) * NT;
    const rsrc_t wr = mkrsrc((WHICH == 1 ? P.w1 : WHICH == 2 ? P.w2 : P.w3) + cu * (RB / 2), DM * N * 2 - cu * RB);
    auto swz = [](int row) { return ((row & 3) | ((row >> 1) & 4)) * 32; };
    const int dr = ln * 16 / RB, tr = l4 * 8 + ((ln >> 2) & 3);
    unsigned vo = dr * (N * 2) + ((ln * 16 % RB) ^ swz(wave * RPI + dr)), ta = tr * RB + ((wave * NT * 32 + (ln & 3) * 8) ^ swz(tr));
    const int grp = nt0 / 10, neg = grp & 1, sh = (WHICH == 1) ? (neg ? -1 : 1) * ((grp >> 1) + 1) : 0;
    unsigned ra = ln * 16, wa = ln * 16 + wave * 1024, sr = WHICH == 2 ? l15 * 8 : 24576 + l15 * 80, sw = sr + wave * 8, bud;
    const unsigned oo = WHICH == 3 ? (l4 & 1) * 512 + (l4 >> 1) * 256 + l15 * 16 : (nt0 >> 3) * WIN + (nt0 & 7) * 512 + (l4 >> 1) * 256 + ((l15 + sh) & 15) * 16 + (l4 & 1) * 8;
    unsigned up = ((unsigned)(l15 + sh) > 15u) ^ neg;
#define W_ARGS : [bud] "=s"(bud), "+{v176}"(vo), "+{v177}"(ta), "+{v249}"(ra), "+{v250}"(wa), "+{v251}"(sr), "+{v252}"(sw) \
               , "+{v255}"(up) : "{v253}"(ln * 16), "{v254}"(oo), [wave] "s"(wave), [rs] "s"(rd), [wr] "s"(wr), [wm] "s"(UNI(wave * 1024)), [wo] "s"(UNI(wave * RPI * N * 2)), [go] "s"(FL_VO + (WHICH * 8 + cu) * 128), \
                 [pi] "s"(UNI((WHICH == 1 ? D_XA : WHICH == 2 ? D_HS : D_X1) + wave * 1024)), \
                 [ng] "s"(UNI(WHICH == 3 ? D_H2 + (nt0 >> 3) * WIN + ((nt0 >> 1) & 3) * 1024 : neg)), [ss] "s"(UNI(cu == 0)) \
               : "memory", "vcc", "scc", "v0", "v1", "v2", "v3", "v4", "v5", "v6", "v7", "v8", "v9", "v10", "v11", "v12", "v13", "v14", "v15", "v16", "v17", "v18", "v19", "v20", "v21", "v22", "v23", "v24", "v25", "v26", "v27", "v28", "v29", "v30", "v31", "v32", "v33", "v34", "v35", "v36", "v37", "v38", "v39", "v40", "v41", "v42", "v43", "v44", "v45", "v46", "v47", "v48", "v49", "v50", "v51", "v52", "v53", "v54", "v55", "v56", "v57", "v58", "v59", "v60", "v61", "v62", "v63", "v64", "v65", "v66", "v67", "v68", "v69", "v70", "v71", "v72", "v73", "v74", "v75", "v76", "v77", "v78", "v79", "v80", "v81", "v82", "v83", "v84", "v85", "v86", "v87", "v88", "v89", "v90", "v91", "v92", "v93", "v94", "v95", "v96", "v97", "v98", "v99", "v100", "v101", "v102", "v103", "v104", "v105", "v106", "v107", "v108", "v109", "v110", "v111", "v112", "v113", "v114", "v115", "v116", "v117", "v118", "v119", "v120", "v121", "v122", "v123", "v124", "v125", "v126", "v127", "v128", "v129", "v130", "v131", "v132", "v133", "v134", "v135", "v136", "v137", "v138", "v139", "v140", "v141", "v142", "v143", "v144", "v145", "v146", "v147", "v148", "v149", "v150", "v151", "v152", "v153", "v154", "v155", "v156", "v157", "v158", "v159", "v160", "v161", "v162", "v163", "v164", "v165", "v166", "v167", "v168", "v169", "v170", "v171", "v172", "v173", "v174", "v175", "v178", "v179", "v180", "v181", "v182", "v183", "v184", "v185", "v186", "v187", "v188", "v189", "v190", "v191", "v192", "v193", "v194", "v195", "v196", "v197", "v198", "v199", "v200", "v201", "v202", "v203", "v204", "v205", "v206", "v207", "v208", "v209", "v210", "v211", "v212", "v213", "v214", "v215", "v216", "v217", "v218", "v219", "v220", "v221", "v222", "v223", "v224", "v225", "v226", "v227", "v228", "v229", "v230", "v231", "v232", "v233", "v234", "v235", "v236", "v237", "v238", "v239", "v240", "v241", "v242", "v243", "v244", "v245", "v246", "v247", "v248", "s40", "s41", "s42", "s43", "s44", "s45", "s46", "s47", "s48", "s49", "s50", "s51", "s52", "s53", "s54", "s55", "s56", "s57", "s58", "s59", "s60", "s61", "s62", "s63", "s64", "s65", "s66", "s67", "s68", "s69", "s70", "s71", "s72", "s73", "s74", "s75", "s76", "s77", "s78", "s79"
    if (WHICH == 1) asm volatile(ASM_W1_LOOP W_ARGS);
    if (WHICH == 2) asm volatile(ASM_W2_LOOP W_ARGS);
    if (WHICH == 3) asm volatile(ASM_W3_LOOP W_ARGS);
    note_timeout(P, bud);
}

constexpr unsigned W4_FL = 122880;
__device__ __forceinline__ unsigned lds_vld(unsigned a) { asm volatile("" ::: "memory"); const unsigned v = *(volatile LAS unsigned*)(size_t)a; asm volatile("" ::: "memory"); return v; }
__device__ __forceinline__ void lds_vst(unsigned a, unsigned v) { asm volatile("" ::: "memory"); *(volatile LAS unsigned*)(size_t)a = v; asm volatile("" ::: "memory"); }
__device__ __forceinline__ void w4_role(const Params& P, int wave, int L, int cu) {
    const int ln = lane_id(), l15 = ln & 15, l4 = ln >> 4;
    const unsigned l16 = ln * 16;
    const rsrc_t rd = mkrsrc(lbase(P, L), RG_L);
    unsigned bud;
    if (wave >= 6) wait_go(P, rd, 32 + cu);
    if (wave < 6) {
        u32x32 w[5];
        {
            const rsrc_t wr = mkrsrc(P.w4 + wave * 256 * DM + cu * 80, (FF * DM - wave * 256 * DM - cu * 80) * 2);
            const unsigned ta = wave * 20480 + (l4 * 8 + ((ln >> 2) & 3)) * 160 + (ln & 3) * 8;
            u32x4 f[5][8];
#pragma unroll
            for (int r = 0; r < 2; ++r) {
#pragma unroll
                for (int i = 0; i < 5; ++i) {
                    const unsigned u = i * 64 + ln, vo = (u / 10) * (DM * 2) + (u % 10) * 16;
#pragma unroll
                    for (int k = 0; k < 4; ++k) dma1(wr, vo, (r * 128 + k * 32) * (DM * 2), wave * 20480 + k * 5120 + i * 1024);
                }
                wait_vm<0>();
#pragma unroll
                for (int k = 0; k < 4; ++k)
#pragma unroll
                    for (int n = 0; n < 5; ++n) f[n][r * 4 + k] = tr_frag(ta + k * 5120 + n * 32, 640);
                asm volatile("s_waitcnt lgkmcnt(0)" ::: "memory");
                SB();
            }
#pragma unroll
            for (int n = 0; n < 5; ++n) {
                u32x8 g[4];
#pragma unroll
                for (int i = 0; i < 4; ++i) g[i] = __builtin_shufflevector(f[n][2 * i], f[n][2 * i + 1], 0, 1, 2, 3, 4, 5, 6, 7);
                w[n] = cat32(g[0], g[1], g[2], g[3]);
            }
            asm volatile("" : "+{v[0:31]}"(w[0]), "+{v[32:63]}"(w[1]), "+{v[64:95]}"(w[2]), "+{v[96:127]}"(w[3]), "+{v[128:159]}"(w[4]));
        }
        wait_go(P, rd, 32 + cu);
        bar();
        unsigned ld = l16 + wave * 1024;
        asm volatile(ASM_W4_MUL : [bud] "=s"(bud), [ld] "+v"(ld)
                     : "{v[0:31]}"(w[0]), "{v[32:63]}"(w[1]), "{v[64:95]}"(w[2]), "{v[96:127]}"(w[3]), "{v[128:159]}"(w[4]), [l2] "v"(2 * ld + 61440), [l16] "v"(l16), [fa] "v"(W4_FL + wave * 4), [rs] "s"(rd),
                       [hb] "s"(UNI(D_H2 + wave * 2 * WIN)), [wave] "s"(wave), [ro] "s"(UNI(wave * 5120)), [fp] "v"(W4_FL + (ln < 6 ? ln : 0) * 4)
                     : "memory", "vcc", "scc", "v160", "v161", "v162", "v163", "v164", "v165", "v166", "v167", "v168", "v169", "v170", "v171", "v172", "v173", "v174", "v175", "v176", "v177", "v178", "v179", "v180", "v181", "v182", "v183", "v184", "v185", "v186", "v187", "v188", "v189", "v190", "v191", "v192", "v193", "v194", "v195", "v196", "v197", "v198", "v199", "v200", "v201", "v202", "v203", "v204", "v205", "v206", "v207", "v208", "v209", "v210", "v211", "v212", "v213", "v214", "v215", "v216", "v217", "v218", "v219", "v220", "v221", "v222", "v223", "v224", "v225", "v226", "v227", "v228", "v229", "v230", "v231", "v232", "v233", "v234", "v235", "v236", "v237", "v238", "v239", "v240", "v241", "v242", "v243", "v244", "v245", "v246", "v247", "s40", "s41", "s42", "s43", "s44", "s45", "s46", "s47", "s48", "s49", "s50", "s51", "s52", "s53", "s54", "s55", "s64", "s65", "s66", "s67", "s68", "s69", "s70", "s71", "s72", "s73", "s74", "s75", "s76", "s77", "s78", "s79");
    } else {
        const int mt = wave - 6;
        if (ln < 16) lds_vst(W4_FL + ln * 4, 0u);
        bar();
        auto tl = [&](int n) { return (unsigned)((cu * 5 + n) >> 3) * WIN + ((cu * 5 + n) & 7) * 512 + mt * 4096; };
        const unsigned o8 = (l4 >> 1) * 256 + l15 * 16 + (l4 & 1) * 8;
        unsigned la = mt * 30720 + l16;
        static_assert(RG_ST == 112 * WIN && D_XN == 10 * REG && D_XR == 6 * REG, "generated loop");
#define F_IN [l2] "v"(2 * la + 61440), [fa] "v"(W4_FL + (ln < 6 ? ln : 0) * 4), [fb] "v"(W4_FL + 32 + (ln < 5 ? ln : 0) * 4), [fc] "v"(W4_FL + 24 + mt * 4), [rs] "s"(rd), [mt] "s"(mt), \
             [t0] "v"(o8 + tl(0)), [t1] "v"(o8 + tl(1)), [t2] "v"(o8 + tl(2)), [t3] "v"(o8 + tl(3)), [t4] "v"(o8 + tl(4))
#define F_CLOB "memory", "vcc", "scc", "v64", "v65", "v66", "v67", "v68", "v69", "v70", "v71", "v72", "v73", "v74", "v75", "v76", "v77", "v78", "v79", "v80", "v81", "v82", "v83", "v84", "v85", "v86", "v87", "v88", "v89", "v90", "v91", "v92", "v93", "v94", "v95", "v96", "v97", "v98", "v99", "v100", "v101", "v102", "v103", "v104", "v105", "v106", "v107", "v108", "v109", "v110", "v111", "v112", "v113", "v114", "v115", "v116", "v117", "v118", "v119", "v120", "v121", "v122", "v123", "v124", "v125", "v126", "v127", "v128", "v129", "v130", "v131", "v132", "v133", "v134", "v135", "v136", "v137", "v138", "v139", "v140", "v141", "v142", "v143", "v144", "v145", "v146", "v147", "v148", "v149", "v150", "v151", "v152", "v153", "v154", "v155", "v156", "v157", "v158", "v159", "v160", "v161", "v162", "v163", "v164", "v165", "v166", "v167", "v168", "v169", "v170", "v171", "v172", "v173", "v174", "v175", "v176", "v177", "v178", "v179", "v180", "v181", "v182", "v183", "v184", "v185", "v186", "v187", "v188", "v189", "v190", "v191", "v192", "v193", "v194", "v195", "v196", "v197", "v198", "v199", "v200", "v201", "v202", "v203", "v204", "v205", "v206", "v207", "v208", "v209", "v210", "v211", "v212", "v213", "v214", "v215", "v216", "v217", "v218", "v219", "v220", "v221", "v222", "v223", "v224", "v225", "v226", "v227", "v228", "v229", "v230", "v231", "v232", "v233", "v234", "v235", "v236", "v237", "v238", "v239", "s40", "s41", "s42", "s43", "s44", "s45", "s46", "s47", "s48", "s49", "s50", "s51", "s52", "s53", "s54", "s55", "s64", "s65", "s66", "s67", "s68", "s69", "s70", "s71", "s72", "s73", "s74", "s75", "s76", "s77", "s78", "s79"
        if (L) asm volatile(ASM_W4_FIN1 : [bud] "=s"(bud), [la] "+v"(la)
                            : F_IN, [ro] "s"(mkrsrc(P.out, ACT)), [co] "s"(cu * 160), [q8] "v"(l15 * (DM * 2) + l4 * 8), [q16] "v"(l15 * (DM * 2) + (l4 & 1) * 32 + (l4 >> 1) * 16) : F_CLOB);
        else asm volatile(ASM_W4_FIN0 : [bud] "=s"(bud), [la] "+v"(la)
                          : F_IN, [rn] "s"(mkrsrc(lbase(P, 1), RG_L)), [co] "s"(cu * 128), [l8] "v"(ln * 8), [o16] "v"(tl(l4 & 1) + (l4 >> 1) * 256 + l15 * 16), [o17] "v"(tl(2 + (l4 & 1)) + (l4 >> 1) * 256 + l15 * 16),
                            [fo] "v"(ln < 32 ? blk(0, ln >> 3) - CH0 + (ln & 7) * 16 : OOB), [fs] "s"(FL_XN + (cu * 2 + mt) * 128) : F_CLOB);
    }
    note_timeout(P, bud);
}

constexpr int LEADER = 252;
__device__ __forceinline__ unsigned leader(const Params& P, rsrc_t rw, int wave) {
    const int ln = lane_id();
    const u32x4 s4 = sent4(), z4 = s4 ^ s4;
    const unsigned mo = (wave * 32 + (ln >> 1)) * 128 + (ln & 1) * 16;
    __builtin_amdgcn_raw_buffer_store_b32(0u, rw, ln < 2 ? ln * 2048 : OOB, CAL0 + wave * 4096, 16);
    for (int k = 0; k < 6; ++k) st128(z4, rw, ln * 16, FL_VO + (k >> 2) * GRP + wave * 4096 + (k & 3) * 1024);
    st128(z4, rw, mo, FL_MB + 32768);
    if (wave == 0) st128(z4, rw, ln ? OOB : 256u, FL0);
    wait_vm<0>();
    bar();
    if (wave == 0) {
        const unsigned o = ln ? OOB : 0u;
        auto trip = [&](unsigned a) {
            unsigned long long t0, t1;
            asm volatile("s_memtime %0\n\ts_waitcnt lgkmcnt(0)\n\tbuffer_store_dword %2, %2, %3, %4 offen sc1\n\ts_waitcnt vmcnt(0)\n\ts_memtime %1\n\ts_waitcnt lgkmcnt(0)"
                         : "=&s"(t0), "=&s"(t1) : "v"(o), "s"(rw), "s"(CAL0 + a) : "memory");
            return (unsigned)(t1 - t0);
        };
        auto mn = [](unsigned a, unsigned b) { return a < b ? a : b; };
        const unsigned a0 = trip(0), b0 = trip(0x2000), a1 = trip(0x800), b1 = trip(0x2800);
        const unsigned h = mn(a0, a1) < mn(b0, b1) ? 0u : 0x2000u, t0 = h ? b0 + b1 : a0 + a1;
        const unsigned t14 = trip(h + 0x4000) + trip(h + 0x4800), t12 = trip(h + 0x1000) + trip(h + 0x1800);
        lds_vst(0, (h ^ 0x2000u) | (t14 < t0 ? 0x4000u : 0u) | (t12 < t0 ? 0x1000u : 0u));
    }
    bar();
    const unsigned par = __builtin_amdgcn_readfirstlane(lds_vld(0));
    for (int k = 0; k < 10; ++k) st128(s4, rw, ln * 16, CH0 + (10 + (k >> 2)) * GRP + wave * 4096 + (k & 3) * 1024);
    wait_vm<0>();
    bar();
    bool open = wave * 32 + (ln >> 1) != LEADER;
    for (int spins = 0; spins < SPIN_CAP; ++spins) {
        st128(s4 & par, rw, open ? mo : OOB, FL_MB);
        __builtin_amdgcn_s_sleep(20);
        const u32x4 d = LD128(rw, mo, FL_MB + 32768);
        open = open && !(d[0] && d[1] && d[2] && d[3]);
        if (!__any(open)) break;
    }
    bar();
    return par;
}
__global__ __launch_bounds__(512, 2) void mega_kernel(Params P) {
    const int wg = blockIdx.x;
    const int wave = __builtin_amdgcn_readfirstlane(threadIdx.x >> 6);
    const int L = (wg >> 2) & 1, x = wg & 3, r = wg >> 3, cpy = x;
    if (__builtin_expect(r < 16, 1)) {
        WLoad W;
        if (L) delay(D_A1);
        wload_start<true>(W, P, wave, r >= 8, x * 8 + (r & 7));
    }
    constexpr int nr = 10;
    const int j = x * 10 + r - 16, wk = r - 26;
    const int i = x == 0 ? 16 + wk : x == 2 ? 10 + wk : wk < 5 ? (x == 1 ? 5 : 0) + wk : 22 + (x >> 1);
    const rsrc_t rw = mkrsrc(P.ws, RG_L);
    const bool lead = wg == LEADER;
    if (!lead) __builtin_amdgcn_raw_buffer_store_b32(SENT, rw, lane_id() ? OOB : 0u, FL_MB + wg * 128 + wave * 4, 16);
#define FIN() __builtin_amdgcn_endpgm()
    if (r < 8) { s_role(P, wave, L, x * 8 + r, cpy); FIN(); }
    if (r < 16) { v_role(P, wave, L, x * 8 + r - 8, cpy); FIN(); }
    if (lead) P.par = leader(P, rw, wave);
    else {
        const unsigned mo = FL_MB + wg * 128 + wave * 4;
        const bool ln = r - 16 < nr && j < 16, first = ln && !L && wave == 7;
        if (!first) { P.par = handshake(rw, mo); if (L || r - 16 >= nr) delay(!L ? D_W0 : r - 16 < nr ? D_R1 : D_W1); }
        if (first || (ln && L && wave)) x_tile(P, (L ? wave * 16 : 0) + j, [&]() { if (first) P.par = handshake(rw, mo); return mkrsrc(lbase(P, 0), RG_L); });
        if (ln && L) bar();
        if (first || (ln && L && !wave)) {
            const rsrc_t r0 = mkrsrc(lbase(P, 0), RG_L);
            const int lane = lane_id();
            const unsigned fo = lane < 32 ? blk(0, lane >> 3) - CH0 + (lane & 7) * 16 : OOB;
            if (L) for (int spins = 0; spins < SPIN_CAP && __any(lane < 32 && LD128(r0, fo, FL_XN + j * 128)[0] == 0u); ++spins) asm volatile("s_sleep 8" ::: "memory");
            st128(sent4() & (L ? 64u : 8u), r0, fo, FL_XN + j * 128);
        }
    }
    if (r - 16 < nr) { r_role(P, wave, L, j, cpy); FIN(); }
    if (i < 5) { w_role<1>(P, wave, L, i); FIN(); }
    if (i < 10) { w_role<2>(P, wave, L, i - 5); FIN(); }
    if (i < 16) { w_role<3>(P, wave, L, i - 10); FIN(); }
    w4_role(P, wave, L, i - 16);
    FIN();
}

extern "C" void kernel_launch(void* const* d_in, const int* in_sizes, int n_in, void* d_out, int out_size, void* d_ws,
                              size_t ws_size, hipStream_t stream) {
    char* ws = (char*)d_ws + (-(size_t)d_ws & (REG - 1));
    Params P;
    P.par = 0; P.pad = 0; P.ws = ws; P.out = (bf16_t*)d_out;
    P.x = (const bf16_t*)d_in[0]; P.mem0 = (const bf16_t*)d_in[1]; P.wq = (const bf16_t*)d_in[2]; P.wk = (const bf16_t*)d_in[3]; P.wv = (const bf16_t*)d_in[4]; P.wo = (const bf16_t*)d_in[5];
    P.w1 = (const bf16_t*)d_in[8]; P.w2 = (const bf16_t*)d_in[9]; P.w3 = (const bf16_t*)d_in[10]; P.w4 = (const bf16_t*)d_in[11];
    (void)hipFuncSetAttribute((const void*)mega_kernel, hipFuncAttributeMaxDynamicSharedMemorySize, MEGA_LDS);

    mega_kernel<<<256, 512, MEGA_LDS, stream>>>(P);
}
```

```cpp
#include <hip/hip_runtime.h>
#include <stdint.h>

typedef unsigned short bf16_t;
using bf16x2 = __attribute__((ext_vector_type(2))) __bf16;
using f32x2 = __attribute__((ext_vector_type(2))) float;
using u32x2 = __attribute__((ext_vector_type(2))) unsigned;
using u32x4 = __attribute__((ext_vector_type(4))) unsigned;
typedef __amdgpu_buffer_rsrc_t rsrc_t;

__device__ __forceinline__ unsigned pack2(float a, float b) {
    bf16x2 r = __builtin_convertvector((f32x2){a, b}, bf16x2);
    return __builtin_bit_cast(unsigned, r);
}
__device__ __forceinline__ float rbf(float f) { return __uint_as_float(pack2(f, 0.f) << 16); }
__device__ __forceinline__ float lo2f(unsigned u) { return __uint_as_float(u << 16); }
__device__ __forceinline__ float hi2f(unsigned u) { return __uint_as_float(u & 0xFFFF0000u); }
__device__ __forceinline__ rsrc_t mkrsrc(const void* p, unsigned bytes) {
    return __builtin_amdgcn_make_buffer_rsrc((void*)p, 0, bytes, 0x00020000);
}

constexpr int S_ = 2048, DM = 640, NH = 32, DH = 192, HD = NH * DH  , SEG = 32, NSEG = 64;
constexpr int FF = 1536;

constexpr unsigned NSLOT = 4, NCP = 4;
constexpr unsigned WIN = 16384, REG = 2u << 20, REGS = REG + 8192;
constexpr unsigned GRP = 32768, CH0 = 48 * REG, CH_SLOT = 2 * REG;
__host__ __device__ constexpr unsigned blk(unsigned grp, unsigned x) { return CH0 + grp * GRP + (((x ^ (grp >> 4)) & 1) << 14) + ((((x >> 1) ^ (grp >> 5)) & 1) << 12); }
constexpr unsigned PX_SLOT = 8 * WIN;
constexpr unsigned RG_PX = 80 * WIN, RG_ST = RG_PX + NSLOT * PX_SLOT;
static_assert(RG_ST + 16 * WIN <= REG, "ring region");
constexpr unsigned FL0 = REG, FL_PAR = REG + 4096, FL_MB = REG + 65536;
constexpr unsigned FL_VO = blk(14, 0), FL_XN = blk(15, 0), D_SS = 2 * REG + 52 * WIN, CAL0 = blk(13, 0);
constexpr unsigned ACT = S_ * DM * 2;
constexpr unsigned D_XR = 6 * REG, D_XN = 10 * REG, D_XA = 14 * REG, D_HS = 18 * REG, D_X1 = 22 * REG, D_H2 = 26 * REG;
__host__ __device__ constexpr unsigned segb(unsigned seg) { return seg * (5 * WIN) + (seg >> 4) * (REGS - 80 * WIN); }
__host__ __device__ constexpr unsigned segh(unsigned seg) { return seg * (12 * WIN) + (seg >> 3) * (REGS - 96 * WIN); }
__host__ __device__ constexpr unsigned vxb(unsigned tile, unsigned hq, unsigned x) { return tile < 8 ? blk(tile * 8 + hq, x) + NSLOT * CH_SLOT : blk(tile * 8 - 16 + hq, x); }
constexpr int AH = 3;
constexpr unsigned RG_L = CH0 + 2 * NSLOT * CH_SLOT;

#define SB() __builtin_amdgcn_sched_barrier(0)
#define LAS __attribute__((address_space(3)))
constexpr unsigned OOB = 0x7FFFFF00u;
template <int N> __device__ __forceinline__ void wait_vm() { asm volatile("s_waitcnt vmcnt(%0)" :: "i"(N) : "memory"); }
__device__ __forceinline__ void bar() {
    asm volatile("s_waitcnt lgkmcnt(0)" ::: "memory");
    __builtin_amdgcn_s_barrier();
    asm volatile("" ::: "memory");
}
__device__ __forceinline__ int lane_id() {
    int ln;
    asm volatile("v_mbcnt_lo_u32_b32 %0, -1, 0\n\tv_mbcnt_hi_u32_b32 %0, -1, %0" : "=v"(ln));
    return ln;
}
__device__ __forceinline__ void sum2_rows(float& s, float& q) {
    unsigned a = __float_as_uint(s), b = a, c = __float_as_uint(q), d = c;
    asm volatile("s_nop 2\n\tv_permlane16_swap_b32 %0, %1\n\tv_permlane16_swap_b32 %2, %3\n\tv_add_f32 %0, %0, %1\n\tv_add_f32 %2, %2, %3\n\tv_mov_b32 %1, %0\n\tv_mov_b32 %3, %2\n\t"
                 "s_nop 1\n\tv_permlane32_swap_b32 %0, %1\n\tv_permlane32_swap_b32 %2, %3\n\tv_add_f32 %0, %0, %1\n\tv_add_f32 %2, %2, %3" : "+v"(a), "+v"(b), "+v"(c), "+v"(d));
    s = __uint_as_float(a); q = __uint_as_float(c);
}
template <int AUXV>
__device__ __forceinline__ void st128_(u32x4 v, rsrc_t r, unsigned voff, unsigned soff) {
    __builtin_amdgcn_raw_buffer_store_b128(v, r, voff, soff, AUXV);
    asm volatile("s_nop 1" :: "v"(v) : "memory");
}
#define st128(V, R, VO_, SO_) st128_<16>(V, R, VO_, SO_)
#define LD128(R, VO_, SO_) __builtin_amdgcn_raw_buffer_load_b128(R, VO_, SO_, 16)
__device__ __forceinline__ float ln_den(float var) { return rbf(__builtin_amdgcn_sqrtf(rbf(rbf(var) + 1e-5f))); }

struct Params {
    char* ws;
    const bf16_t *x, *mem0, *wq, *wk, *wv, *wo, *w1, *w2, *w3, *w4;
    bf16_t* out;
    unsigned par, pad;
};

#define SENT 0xFFFFFFFFu
constexpr int SPIN_CAP = 400000;
__device__ __forceinline__ bool spin_fail(int spins, const Params& P) {
    if ((spins & 1023) == 1023) {
        const rsrc_t rt = mkrsrc(P.ws + FL0, 512);
        unsigned o;
        asm volatile("v_mov_b32 %0, 0x100" : "=v"(o));
        if (__builtin_amdgcn_readfirstlane(__builtin_amdgcn_raw_buffer_load_b32(rt, o, 0, 16)) != 0u) return true;
        if (spins > SPIN_CAP) { __builtin_amdgcn_raw_buffer_store_b32(o, rt, o, 0, 16); return true; }
    }
    return false;
}
__device__ __forceinline__ unsigned nosent(unsigned d) { return d < 0xFFFFFFFEu ? d : 0xFFFFFFFEu; }
__device__ __forceinline__ u32x4 sent4() { unsigned sv; asm volatile("v_mov_b32 %0, -1" : "=v"(sv)); return (u32x4){sv, sv, sv, sv}; }
__device__ __forceinline__ char* lbase(const Params& P, int L) { return P.ws + ((P.par ^ (L << 13)) & 0x2000); }

constexpr unsigned L_X = 0;
constexpr unsigned L_MEM = 40960;
constexpr unsigned L_Q = 51200;
constexpr unsigned L_KM = 54272;
constexpr unsigned L_QX = 57344;
constexpr unsigned L_KX = 69632;
constexpr unsigned L_W = 81920;
constexpr int MEGA_LDS = 163840;

#define ASM_S_LOOP \
    "s_mov_b64 s[60:61], %[ws]\n\t" \
    "s_mov_b32 s62, %[sz]\n\t" \
    "s_mov_b32 s63, 0x20000\n\t" \
    "s_mov_b32 s40, 0\n\t" \
    "s_mov_b32 s41, 0x20000\n\t" \
    "s_cmp_eq_u32 %[wave], 0\n\t" \
    "s_cbranch_scc1 .Ls0_%=\n\t" \
    "s_cmp_eq_u32 %[wave], 1\n\t" \
    "s_cbranch_scc1 .Ls1_%=\n\t" \
    "s_cmp_eq_u32 %[wave], 2\n\t" \
    "s_cbranch_scc1 .Ls2_%=\n\t" \
    "s_cmp_eq_u32 %[wave], 3\n\t" \
    "s_cbranch_scc1 .Ls3_%=\n\t" \
    ".Ls4_%=:\n\t" \
    "s_mov_b32 s42, %[xs]\n\t" \
    "v_add_u32 v244, 0x18000, v243\n\t" \
    "s_waitcnt vmcnt(12) lgkmcnt(0)\n\t" \
    "s_barrier \n\t" \
    "s_add_u32 m0, %[wm], 0x12000\n\t" \
    "s_mov_b32 s44, 0x1e0000\n\t" \
    "buffer_load_dwordx4 v240, %[wr], s44 offen offset:0 nt lds\n\t" \
    "buffer_load_dwordx4 v241, %[wr], s44 offen offset:1024 nt lds\n\t" \
    "buffer_load_dwordx4 v242, %[wr], s44 offen offset:2048 nt lds\n\t" \
    "ds_read_b64_tr_b16 v[200:201], v244 offset:0\n\t" \
    "ds_read_b64_tr_b16 v[202:203], v244 offset:1536\n\t" \
    "ds_read_b64_tr_b16 v[0:1], v244 offset:32\n\t" \
    "ds_read_b64_tr_b16 v[2:3], v244 offset:1568\n\t" \
    "ds_read_b64_tr_b16 v[8:9], v244 offset:64\n\t" \
    "ds_read_b64_tr_b16 v[10:11], v244 offset:1600\n\t" \
    "v_add_u32 v244, 0x1e000, v243\n\t" \
    "s_waitcnt vmcnt(12) lgkmcnt(0)\n\t" \
    "s_barrier \n\t" \
    "s_add_u32 m0, %[wm], 0x18000\n\t" \
    "s_mov_b32 s44, 0x240000\n\t" \
    "buffer_load_dwordx4 v240, %[wr], s44 offen offset:0 nt lds\n\t" \
    "buffer_load_dwordx4 v241, %[wr], s44 offen offset:1024 nt lds\n\t" \
    "buffer_load_dwordx4 v242, %[wr], s44 offen offset:2048 nt lds\n\t" \
    "ds_read_b64_tr_b16 v[204:205], v244 offset:0\n\t" \
    "ds_read_b64_tr_b16 v[206:207], v244 offset:1536\n\t" \
    "ds_read_b64_tr_b16 v[4:5], v244 offset:32\n\t" \
    "ds_read_b64_tr_b16 v[6:7], v244 offset:1568\n\t" \
    "ds_read_b64_tr_b16 v[12:13], v244 offset:64\n\t" \
    "ds_read_b64_tr_b16 v[14:15], v244 offset:1600\n\t" \
    "v_add_u32 v244, 0x0, v243\n\t" \
    "s_waitcnt vmcnt(12) lgkmcnt(0)\n\t" \
    "s_barrier \n\t" \
    "s_add_u32 m0, %[wm], 0x1e000\n\t" \
    "s_mov_b32 s44, 0x2a0000\n\t" \
    "buffer_load_dwordx4 v240, %[wr], s44 offen offset:0 nt lds\n\t" \
    "buffer_load_dwordx4 v241, %[wr], s44 offen offset:1024 nt lds\n\t" \
    "buffer_load_dwordx4 v242, %[wr], s44 offen offset:2048 nt lds\n\t" \
    "ds_read_b64_tr_b16 v[16:17], v244 offset:0\n\t" \
    "ds_read_b64_tr_b16 v[18:19], v244 offset:1536\n\t" \
    "ds_read_b64_tr_b16 v[24:25], v244 offset:32\n\t" \
    "ds_read_b64_tr_b16 v[26:27], v244 offset:1568\n\t" \
    "ds_read_b64_tr_b16 v[32:33], v244 offset:64\n\t" \
    "ds_read_b64_tr_b16 v[34:35], v244 offset:1600\n\t" \
    "v_add_u32 v244, 0x6000, v243\n\t" \
    "s_waitcnt vmcnt(12) lgkmcnt(0)\n\t" \
    "s_barrier \n\t" \
    "s_add_u32 m0, %[wm], 0x0\n\t" \
    "s_mov_b32 s44, 0x300000\n\t" \
    "buffer_load_dwordx4 v240, %[wr], s44 offen offset:0 nt lds\n\t" \
    "buffer_load_dwordx4 v241, %[wr], s44 offen offset:1024 nt lds\n\t" \
    "buffer_load_dwordx4 v242, %[wr], s44 offen offset:2048 nt lds\n\t" \
    "ds_read_b64_tr_b16 v[20:21], v244 offset:0\n\t" \
    "ds_read_b64_tr_b16 v[22:23], v244 offset:1536\n\t" \
    "ds_read_b64_tr_b16 v[28:29], v244 offset:32\n\t" \
    "ds_read_b64_tr_b16 v[30:31], v244 offset:1568\n\t" \
    "ds_read_b64_tr_b16 v[36:37], v244 offset:64\n\t" \
    "ds_read_b64_tr_b16 v[38:39], v244 offset:1600\n\t" \
    "v_add_u32 v244, 0xc000, v243\n\t" \
    "s_waitcnt vmcnt(12) lgkmcnt(0)\n\t" \
    "s_barrier \n\t" \
    "s_add_u32 m0, %[wm], 0x6000\n\t" \
    "s_mov_b32 s44, 0x360000\n\t" \
    "buffer_load_dwordx4 v240, %[wr], s44 offen offset:0 nt lds\n\t" \
    "buffer_load_dwordx4 v241, %[wr], s44 offen offset:1024 nt lds\n\t" \
    "buffer_load_dwordx4 v242, %[wr], s44 offen offset:2048 nt lds\n\t" \
    "ds_read_b64_tr_b16 v[208:209], v244 offset:0\n\t" \
    "ds_read_b64_tr_b16 v[210:211], v244 offset:1536\n\t" \
    "ds_read_b64_tr_b16 v[40:41], v244 offset:32\n\t" \
    "ds_read_b64_tr_b16 v[42:43], v244 offset:1568\n\t" \
    "ds_read_b64_tr_b16 v[48:49], v244 offset:64\n\t" \
    "ds_read_b64_tr_b16 v[50:51], v244 offset:1600\n\t" \
    "v_add_u32 v244, 0x12000, v243\n\t" \
    "s_waitcnt vmcnt(12) lgkmcnt(0)\n\t" \
    "s_barrier \n\t" \
    "s_add_u32 m0, %[wm], 0xc000\n\t" \
    "s_mov_b32 s44, 0x3c0000\n\t" \
    "buffer_load_dwordx4 v240, %[wr], s44 offen offset:0 nt lds\n\t" \
    "buffer_load_dwordx4 v241, %[wr], s44 offen offset:1024 nt lds\n\t" \
    "buffer_load_dwordx4 v242, %[wr], s44 offen offset:2048 nt lds\n\t" \
    "ds_read_b64_tr_b16 v[212:213], v244 offset:0\n\t" \
    "ds_read_b64_tr_b16 v[214:215], v244 offset:1536\n\t" \
    "ds_read_b64_tr_b16 v[44:45], v244 offset:32\n\t" \
    "ds_read_b64_tr_b16 v[46:47], v244 offset:1568\n\t" \
    "ds_read_b64_tr_b16 v[52:53], v244 offset:64\n\t" \
    "ds_read_b64_tr_b16 v[54:55], v244 offset:1600\n\t" \
    "v_add_u32 v244, 0x18000, v243\n\t" \
    "s_waitcnt vmcnt(12) lgkmcnt(0)\n\t" \
    "s_barrier \n\t" \
    "s_add_u32 m0, %[wm], 0x12000\n\t" \
    "s_mov_b32 s44, 0x420000\n\t" \
    "buffer_load_dwordx4 v240, %[wr], s44 offen offset:0 nt lds\n\t" \
    "buffer_load_dwordx4 v241, %[wr], s44 offen offset:1024 nt lds\n\t" \
    "buffer_load_dwordx4 v242, %[wr], s44 offen offset:2048 nt lds\n\t" \
    "ds_read_b64_tr_b16 v[56:57], v244 offset:0\n\t" \
    "ds_read_b64_tr_b16 v[58:59], v244 offset:1536\n\t" \
    "ds_read_b64_tr_b16 v[64:65], v244 offset:32\n\t" \
    "ds_read_b64_tr_b16 v[66:67], v244 offset:1568\n\t" \
    "ds_read_b64_tr_b16 v[72:73], v244 offset:64\n\t" \
    "ds_read_b64_tr_b16 v[74:75], v244 offset:1600\n\t" \
    "v_add_u32 v244, 0x1e000, v243\n\t" \
    "s_waitcnt vmcnt(12) lgkmcnt(0)\n\t" \
    "s_barrier \n\t" \
    "s_add_u32 m0, %[wm], 0x18000\n\t" \
    "s_mov_b32 s44, 0x480000\n\t" \
    "buffer_load_dwordx4 v240, %[wr], s44 offen offset:0 nt lds\n\t" \
    "buffer_load_dwordx4 v241, %[wr], s44 offen offset:1024 nt lds\n\t" \
    "buffer_load_dwordx4 v242, %[wr], s44 offen offset:2048 nt lds\n\t" \
    "ds_read_b64_tr_b16 v[60:61], v244 offset:0\n\t" \
    "ds_read_b64_tr_b16 v[62:63], v244 offset:1536\n\t" \
    "ds_read_b64_tr_b16 v[68:69], v244 offset:32\n\t" \
    "ds_read_b64_tr_b16 v[70:71], v244 offset:1568\n\t" \
    "ds_read_b64_tr_b16 v[76:77], v244 offset:64\n\t" \
    "ds_read_b64_tr_b16 v[78:79], v244 offset:1600\n\t" \
    "v_add_u32 v244, 0x0, v243\n\t" \
    "s_waitcnt vmcnt(12) lgkmcnt(0)\n\t" \
    "s_barrier \n\t" \
    "v_mov_b32 v246, 0\n\t" \
    "buffer_load_dword v246, v246, s[60:63], %[mo] offen sc1\n\t" \
    "s_add_u32 m0, %[wm], 0x1e000\n\t" \
    "s_mov_b32 s44, 0x4e0000\n\t" \
    "buffer_load_dwordx4 v240, %[wr], s44 offen offset:0 nt lds\n\t" \
    "buffer_load_dwordx4 v241, %[wr], s44 offen offset:1024 nt lds\n\t" \
    "buffer_load_dwordx4 v242, %[wr], s44 offen offset:2048 nt lds\n\t" \
    "ds_read_b64_tr_b16 v[216:217], v244 offset:0\n\t" \
    "ds_read_b64_tr_b16 v[218:219], v244 offset:1536\n\t" \
    "ds_read_b64_tr_b16 v[80:81], v244 offset:32\n\t" \
    "ds_read_b64_tr_b16 v[82:83], v244 offset:1568\n\t" \
    "ds_read_b64_tr_b16 v[88:89], v244 offset:64\n\t" \
    "ds_read_b64_tr_b16 v[90:91], v244 offset:1600\n\t" \
    "v_add_u32 v244, 0x6000, v243\n\t" \
    "s_waitcnt vmcnt(12) lgkmcnt(0)\n\t" \
    "s_barrier \n\t" \
    "s_add_u32 m0, %[wm], 0x0\n\t" \
    "s_mov_b32 s44, 0x540000\n\t" \
    "buffer_load_dwordx4 v240, %[wr], s44 offen offset:0 nt lds\n\t" \
    "buffer_load_dwordx4 v241, %[wr], s44 offen offset:1024 nt lds\n\t" \
    "buffer_load_dwordx4 v242, %[wr], s44 offen offset:2048 nt lds\n\t" \
    "ds_read_b64_tr_b16 v[220:221], v244 offset:0\n\t" \
    "ds_read_b64_tr_b16 v[222:223], v244 offset:1536\n\t" \
    "ds_read_b64_tr_b16 v[84:85], v244 offset:32\n\t" \
    "ds_read_b64_tr_b16 v[86:87], v244 offset:1568\n\t" \
    "ds_read_b64_tr_b16 v[92:93], v244 offset:64\n\t" \
    "ds_read_b64_tr_b16 v[94:95], v244 offset:1600\n\t" \
    "v_add_u32 v244, 0xc000, v243\n\t" \
    "s_waitcnt vmcnt(12) lgkmcnt(0)\n\t" \
    "s_barrier \n\t" \
    "s_add_u32 m0, %[wm], 0x6000\n\t" \
    "s_mov_b32 s44, 0x5a0000\n\t" \
    "buffer_load_dwordx4 v240, %[wr], s44 offen offset:0 nt lds\n\t" \
    "buffer_load_dwordx4 v241, %[wr], s44 offen offset:1024 nt lds\n\t" \
    "buffer_load_dwordx4 v242, %[wr], s44 offen offset:2048 nt lds\n\t" \
    "ds_read_b64_tr_b16 v[96:97], v244 offset:0\n\t" \
    "ds_read_b64_tr_b16 v[98:99], v244 offset:1536\n\t" \
    "ds_read_b64_tr_b16 v[104:105], v244 offset:32\n\t" \
    "ds_read_b64_tr_b16 v[106:107], v244 offset:1568\n\t" \
    "ds_read_b64_tr_b16 v[112:113], v244 offset:64\n\t" \
    "ds_read_b64_tr_b16 v[114:115], v244 offset:1600\n\t" \
    "v_add_u32 v244, 0x12000, v243\n\t" \
    "s_waitcnt vmcnt(12) lgkmcnt(0)\n\t" \
    "s_barrier \n\t" \
    "s_add_u32 m0, %[wm], 0xc000\n\t" \
    "s_mov_b32 s44, 0x600000\n\t" \
    "buffer_load_dwordx4 v240, %[wr], s44 offen offset:0 nt lds\n\t" \
    "buffer_load_dwordx4 v241, %[wr], s44 offen offset:1024 nt lds\n\t" \
    "buffer_load_dwordx4 v242, %[wr], s44 offen offset:2048 nt lds\n\t" \
    "ds_read_b64_tr_b16 v[100:101], v244 offset:0\n\t" \
    "ds_read_b64_tr_b16 v[102:103], v244 offset:1536\n\t" \
    "ds_read_b64_tr_b16 v[108:109], v244 offset:32\n\t" \
    "ds_read_b64_tr_b16 v[110:111], v244 offset:1568\n\t" \
    "ds_read_b64_tr_b16 v[116:117], v244 offset:64\n\t" \
    "ds_read_b64_tr_b16 v[118:119], v244 offset:1600\n\t" \
    "v_add_u32 v244, 0x18000, v243\n\t" \
    "s_waitcnt vmcnt(12) lgkmcnt(0)\n\t" \
    "s_barrier \n\t" \
    "s_add_u32 m0, %[wm], 0x12000\n\t" \
    "s_mov_b32 s44, 0x660000\n\t" \
    "buffer_load_dwordx4 v240, %[wr], s44 offen offset:0 nt lds\n\t" \
    "buffer_load_dwordx4 v241, %[wr], s44 offen offset:1024 nt lds\n\t" \
    "buffer_load_dwordx4 v242, %[wr], s44 offen offset:2048 nt lds\n\t" \
    "ds_read_b64_tr_b16 v[224:225], v244 offset:0\n\t" \
    "ds_read_b64_tr_b16 v[226:227], v244 offset:1536\n\t" \
    "ds_read_b64_tr_b16 v[120:121], v244 offset:32\n\t" \
    "ds_read_b64_tr_b16 v[122:123], v244 offset:1568\n\t" \
    "ds_read_b64_tr_b16 v[128:129], v244 offset:64\n\t" \
    "ds_read_b64_tr_b16 v[130:131], v244 offset:1600\n\t" \
    "v_add_u32 v244, 0x1e000, v243\n\t" \
    "s_waitcnt vmcnt(12) lgkmcnt(0)\n\t" \
    "s_barrier \n\t" \
    ".Ls41_%=:\n\t" \
    "v_readfirstlane_b32 s43, v246\n\t" \
    "s_nop 0\n\t" \
    "s_cmp_lg_u32 s43, -1\n\t" \
    "s_cbranch_scc1 .Ls42_%=\n\t" \
    "s_cmp_eq_u32 s41, 0\n\t" \
    "s_cbranch_scc1 .Ls42_%=\n\t" \
    "s_sub_u32 s41, s41, 1\n\t" \
    "s_sleep 4\n\t" \
    "v_mov_b32 v246, 0\n\t" \
    "buffer_load_dword v246, v246, s[60:63], %[mo] offen sc1\n\t" \
    "s_waitcnt vmcnt(0)\n\t" \
    "s_branch .Ls41_%=\n\t" \
    ".Ls42_%=:\n\t" \
    "v_mov_b32 v246, 0x8000\n\t" \
    "s_mov_b64 exec, 1\n\t" \
    "buffer_store_dword v246, v246, s[60:63], %[mo] offen sc1\n\t" \
    "s_mov_b64 exec, -1\n\t" \
    "s_and_b32 s44, s43, 0x5000\n\t" \
    "s_xor_b32 %[mb], %[mb], s44\n\t" \
    "s_xor_b32 %[fl], %[fl], s44\n\t" \
    "s_and_b32 s43, s43, 0x2000\n\t" \
    "s_xor_b32 s43, s43, %[lx]\n\t" \
    "s_add_u32 s60, s60, s43\n\t" \
    "s_addc_u32 s61, s61, 0\n\t" \
    "v_and_b32 v252, 0xf0, v253\n\t" \
    "v_lshlrev_b32 v252, 3, v252\n\t" \
    "buffer_load_dword v252, v252, s[60:63], %[fl] offen sc1\n\t" \
    "s_add_u32 m0, %[wm], 0x18000\n\t" \
    "s_mov_b32 s44, 0x6c0000\n\t" \
    "buffer_load_dwordx4 v240, %[wr], s44 offen offset:0 nt lds\n\t" \
    "buffer_load_dwordx4 v241, %[wr], s44 offen offset:1024 nt lds\n\t" \
    "buffer_load_dwordx4 v242, %[wr], s44 offen offset:2048 nt lds\n\t" \
    "ds_read_b64_tr_b16 v[228:229], v244 offset:0\n\t" \
    "ds_read_b64_tr_b16 v[230:231], v244 offset:1536\n\t" \
    "ds_read_b64_tr_b16 v[124:125], v244 offset:32\n\t" \
    "ds_read_b64_tr_b16 v[126:127], v244 offset:1568\n\t" \
    "ds_read_b64_tr_b16 v[132:133], v244 offset:64\n\t" \
    "ds_read_b64_tr_b16 v[134:135], v244 offset:1600\n\t" \
    "v_add_u32 v244, 0x0, v243\n\t" \
    "s_waitcnt vmcnt(12) lgkmcnt(0)\n\t" \
    "s_barrier \n\t" \
    "s_add_u32 m0, %[wm], 0x1e000\n\t" \
    "s_mov_b32 s44, 0x720000\n\t" \
    "buffer_load_dwordx4 v240, %[wr], s44 offen offset:0 nt lds\n\t" \
    "buffer_load_dwordx4 v241, %[wr], s44 offen offset:1024 nt lds\n\t" \
    "buffer_load_dwordx4 v242, %[wr], s44 offen offset:2048 nt lds\n\t" \
    "ds_read_b64_tr_b16 v[136:137], v244 offset:0\n\t" \
    "ds_read_b64_tr_b16 v[138:139], v244 offset:1536\n\t" \
    "ds_read_b64_tr_b16 v[144:145], v244 offset:32\n\t" \
    "ds_read_b64_tr_b16 v[146:147], v244 offset:1568\n\t" \
    "ds_read_b64_tr_b16 v[152:153], v244 offset:64\n\t" \
    "ds_read_b64_tr_b16 v[154:155], v244 offset:1600\n\t" \
    "v_add_u32 v244, 0x6000, v243\n\t" \
    "s_waitcnt vmcnt(12) lgkmcnt(0)\n\t" \
    "s_barrier \n\t" \
    "ds_read_b64_tr_b16 v[140:141], v244 offset:0\n\t" \
    "ds_read_b64_tr_b16 v[142:143], v244 offset:1536\n\t" \
    "ds_read_b64_tr_b16 v[148:149], v244 offset:32\n\t" \
    "ds_read_b64_tr_b16 v[150:151], v244 offset:1568\n\t" \
    "ds_read_b64_tr_b16 v[156:157], v244 offset:64\n\t" \
    "ds_read_b64_tr_b16 v[158:159], v244 offset:1600\n\t" \
    "v_add_u32 v244, 0xc000, v243\n\t" \
    "s_waitcnt vmcnt(9) lgkmcnt(0)\n\t" \
    "s_barrier \n\t" \
    "ds_read_b64_tr_b16 v[232:233], v244 offset:0\n\t" \
    "ds_read_b64_tr_b16 v[234:235], v244 offset:1536\n\t" \
    "ds_read_b64_tr_b16 v[160:161], v244 offset:32\n\t" \
    "ds_read_b64_tr_b16 v[162:163], v244 offset:1568\n\t" \
    "ds_read_b64_tr_b16 v[168:169], v244 offset:64\n\t" \
    "ds_read_b64_tr_b16 v[170:171], v244 offset:1600\n\t" \
    "v_add_u32 v244, 0x12000, v243\n\t" \
    "s_waitcnt vmcnt(6) lgkmcnt(0)\n\t" \
    "s_barrier \n\t" \
    "ds_read_b64_tr_b16 v[236:237], v244 offset:0\n\t" \
    "ds_read_b64_tr_b16 v[238:239], v244 offset:1536\n\t" \
    "ds_read_b64_tr_b16 v[164:165], v244 offset:32\n\t" \
    "ds_read_b64_tr_b16 v[166:167], v244 offset:1568\n\t" \
    "ds_read_b64_tr_b16 v[172:173], v244 offset:64\n\t" \
    "ds_read_b64_tr_b16 v[174:175], v244 offset:1600\n\t" \
    "v_add_u32 v244, 0x18000, v243\n\t" \
    "s_waitcnt vmcnt(3) lgkmcnt(0)\n\t" \
    "s_barrier \n\t" \
    "ds_read_b64_tr_b16 v[176:177], v244 offset:0\n\t" \
    "ds_read_b64_tr_b16 v[178:179], v244 offset:1536\n\t" \
    "ds_read_b64_tr_b16 v[184:185], v244 offset:32\n\t" \
    "ds_read_b64_tr_b16 v[186:187], v244 offset:1568\n\t" \
    "ds_read_b64_tr_b16 v[192:193], v244 offset:64\n\t" \
    "ds_read_b64_tr_b16 v[194:195], v244 offset:1600\n\t" \
    "s_mov_b32 s43, 0\n\t" \
    "v_cmp_eq_u32 vcc, 0, v252\n\t" \
    "s_cbranch_vccnz .Ls43_%=\n\t" \
    "s_mov_b32 m0, %[m0]\n\t" \
    "s_add_u32 s45, s42, %[x1]\n\t" \
    "buffer_load_dwordx4 v253, s[60:63], s42 offen offset:0 sc1 lds\n\t" \
    "buffer_load_dwordx4 v253, s[60:63], s42 offen offset:1024 sc1 lds\n\t" \
    "buffer_load_dwordx4 v253, s[60:63], s42 offen offset:2048 sc1 lds\n\t" \
    "buffer_load_dwordx4 v253, s[60:63], s42 offen offset:3072 sc1 lds\n\t" \
    "s_mov_b32 m0, %[m1]\n\t" \
    "s_nop 0\n\t" \
    "buffer_load_dwordx4 v253, s[60:63], s45 offen offset:0 sc1 lds\n\t" \
    "buffer_load_dwordx4 v253, s[60:63], s45 offen offset:1024 sc1 lds\n\t" \
    "s_mov_b32 s43, 1\n\t" \
    ".Ls43_%=:\n\t" \
    "v_add_u32 v244, 0x1e000, v243\n\t" \
    "s_waitcnt vmcnt(0) lgkmcnt(0)\n\t" \
    "s_barrier \n\t" \
    "ds_read_b64_tr_b16 v[180:181], v244 offset:0\n\t" \
    "ds_read_b64_tr_b16 v[182:183], v244 offset:1536\n\t" \
    "ds_read_b64_tr_b16 v[188:189], v244 offset:32\n\t" \
    "ds_read_b64_tr_b16 v[190:191], v244 offset:1568\n\t" \
    "ds_read_b64_tr_b16 v[196:197], v244 offset:64\n\t" \
    "ds_read_b64_tr_b16 v[198:199], v244 offset:1600\n\t" \
    "s_waitcnt lgkmcnt(0)\n\t" \
    "s_barrier \n\t" \
    "ds_write_b128 v254, v[200:203] offset:0\n\t" \
    "ds_write_b128 v254, v[204:207] offset:1024\n\t" \
    "ds_write_b128 v254, v[208:211] offset:2048\n\t" \
    "ds_write_b128 v254, v[212:215] offset:3072\n\t" \
    "ds_write_b128 v254, v[216:219] offset:4096\n\t" \
    "ds_write_b128 v254, v[220:223] offset:5120\n\t" \
    "ds_write_b128 v254, v[224:227] offset:6144\n\t" \
    "ds_write_b128 v254, v[228:231] offset:7168\n\t" \
    "ds_write_b128 v254, v[232:235] offset:8192\n\t" \
    "ds_write_b128 v254, v[236:239] offset:9216\n\t" \
    "s_cmp_lg_u32 s43, 0\n\t" \
    "s_cbranch_scc1 .Ls44_%=\n\t" \
    "s_mov_b32 s44, 0\n\t" \
    ".Ls45_%=:\n\t" \
    "v_cmp_ge_u32 vcc, s44, v252\n\t" \
    "s_cbranch_vccz .Ls46_%=\n\t" \
    "s_cmp_eq_u32 s41, 0\n\t" \
    "s_cbranch_scc1 .Ls46_%=\n\t" \
    "s_sub_u32 s41, s41, 1\n\t" \
    "s_sleep 8\n\t" \
    "v_and_b32 v252, 0xf0, v253\n\t" \
    "v_lshlrev_b32 v252, 3, v252\n\t" \
    "buffer_load_dword v252, v252, s[60:63], %[fl] offen sc1\n\t" \
    "s_waitcnt vmcnt(0)\n\t" \
    "s_branch .Ls45_%=\n\t" \
    ".Ls46_%=:\n\t" \
    "s_mov_b32 m0, %[m0]\n\t" \
    "s_add_u32 s45, s42, %[x1]\n\t" \
    "buffer_load_dwordx4 v253, s[60:63], s42 offen offset:0 sc1 lds\n\t" \
    "buffer_load_dwordx4 v253, s[60:63], s42 offen offset:1024 sc1 lds\n\t" \
    "buffer_load_dwordx4 v253, s[60:63], s42 offen offset:2048 sc1 lds\n\t" \
    "buffer_load_dwordx4 v253, s[60:63], s42 offen offset:3072 sc1 lds\n\t" \
    "s_mov_b32 m0, %[m1]\n\t" \
    "s_nop 0\n\t" \
    "buffer_load_dwordx4 v253, s[60:63], s45 offen offset:0 sc1 lds\n\t" \
    "buffer_load_dwordx4 v253, s[60:63], s45 offen offset:1024 sc1 lds\n\t" \
    ".Ls44_%=:\n\t" \
    "s_waitcnt vmcnt(0) lgkmcnt(0)\n\t" \
    "s_barrier \n\t" \
    "ds_read_b128 v[224:227], v253 offset:0\n\t" \
    "ds_read_b128 v[244:247], v254 offset:0\n\t" \
    "ds_read_b128 v[228:231], v253 offset:1024\n\t" \
    "ds_read_b128 v[232:235], v253 offset:20480\n\t" \
    "ds_read_b128 v[236:239], v253 offset:21504\n\t" \
    "ds_read_b128 v[248:251], v254 offset:1024\n\t" \
    ".Ls47_%=:\n\t" \
    "s_cmp_lg_u32 %[wave], 4\n\t" \
    "s_cbranch_scc1 .Ls48_%=\n\t" \
    "s_sleep 8\n\t" \
    ".Ls48_%=:\n\t" \
    "s_waitcnt lgkmcnt(5)\n\t" \
    "v_mfma_f32_16x16x32_bf16 v[204:207], v[0:3], v[224:227], 0\n\t" \
    "s_add_u32 m0, %[m0], 0x3000\n\t" \
    "s_waitcnt lgkmcnt(3)\n\t" \
    "v_mfma_f32_16x16x32_bf16 v[204:207], v[4:7], v[228:231], v[204:207]\n\t" \
    "ds_read_b128 v[240:243], v253 offset:2048\n\t" \
    "s_add_u32 s45, s42, 0xc000\n\t" \
    "v_mfma_f32_16x16x32_bf16 v[208:211], v[8:11], v[224:227], 0\n\t" \
    "buffer_load_dwordx4 v253, s[60:63], s45 offen offset:0 sc1 lds\n\t" \
    "v_mfma_f32_16x16x32_bf16 v[208:211], v[12:15], v[228:231], v[208:211]\n\t" \
    "v_mfma_f32_16x16x32_bf16 v[200:203], v[244:247], v[224:227], 0\n\t" \
    "s_waitcnt lgkmcnt(1)\n\t" \
    "v_mfma_f32_16x16x32_bf16 v[200:203], v[248:251], v[228:231], v[200:203]\n\t" \
    "buffer_load_dwordx4 v253, s[60:63], s45 offen offset:1024 sc1 lds\n\t" \
    "v_mfma_f32_16x16x32_bf16 v[216:219], v[0:3], v[232:235], 0\n\t" \
    "ds_read_b128 v[224:227], v253 offset:3072\n\t" \
    "v_mfma_f32_16x16x32_bf16 v[216:219], v[4:7], v[236:239], v[216:219]\n\t" \
    "ds_read_b128 v[228:231], v253 offset:22528\n\t" \
    "v_mfma_f32_16x16x32_bf16 v[220:223], v[8:11], v[232:235], 0\n\t" \
    "buffer_load_dwordx4 v253, s[60:63], s45 offen offset:2048 sc1 lds\n\t" \
    "v_mfma_f32_16x16x32_bf16 v[220:223], v[12:15], v[236:239], v[220:223]\n\t" \
    "v_mfma_f32_16x16x32_bf16 v[212:215], v[244:247], v[232:235], 0\n\t" \
    "ds_read_b128 v[244:247], v254 offset:2048\n\t" \
    "v_mfma_f32_16x16x32_bf16 v[212:215], v[248:251], v[236:239], v[212:215]\n\t" \
    "ds_read_b128 v[248:251], v254 offset:3072\n\t" \
    "buffer_load_dwordx4 v253, s[60:63], s45 offen offset:3072 sc1 lds\n\t" \
    "s_waitcnt lgkmcnt(4)\n\t" \
    "v_mfma_f32_16x16x32_bf16 v[204:207], v[24:27], v[240:243], v[204:207]\n\t" \
    "ds_read_b128 v[232:235], v253 offset:23552\n\t" \
    "s_waitcnt lgkmcnt(4)\n\t" \
    "v_mfma_f32_16x16x32_bf16 v[204:207], v[28:31], v[224:227], v[204:207]\n\t" \
    "ds_read_b128 v[236:239], v253 offset:4096\n\t" \
    "v_mfma_f32_16x16x32_bf16 v[208:211], v[32:35], v[240:243], v[208:211]\n\t" \
    "v_mfma_f32_16x16x32_bf16 v[208:211], v[36:39], v[224:227], v[208:211]\n\t" \
    "v_mfma_f32_16x16x32_bf16 v[200:203], v[16:19], v[240:243], v[200:203]\n\t" \
    "v_mfma_f32_16x16x32_bf16 v[200:203], v[20:23], v[224:227], v[200:203]\n\t" \
    "s_waitcnt lgkmcnt(4)\n\t" \
    "v_mfma_f32_16x16x32_bf16 v[216:219], v[24:27], v[228:231], v[216:219]\n\t" \
    "ds_read_b128 v[240:243], v253 offset:5120\n\t" \
    "s_waitcnt lgkmcnt(2)\n\t" \
    "v_mfma_f32_16x16x32_bf16 v[216:219], v[28:31], v[232:235], v[216:219]\n\t" \
    "ds_read_b128 v[224:227], v253 offset:24576\n\t" \
    "v_mfma_f32_16x16x32_bf16 v[220:223], v[32:35], v[228:231], v[220:223]\n\t" \
    "v_mfma_f32_16x16x32_bf16 v[220:223], v[36:39], v[232:235], v[220:223]\n\t" \
    "v_mfma_f32_16x16x32_bf16 v[212:215], v[16:19], v[228:231], v[212:215]\n\t" \
    "v_mfma_f32_16x16x32_bf16 v[212:215], v[20:23], v[232:235], v[212:215]\n\t" \
    "s_waitcnt lgkmcnt(2)\n\t" \
    "v_mfma_f32_16x16x32_bf16 v[204:207], v[40:43], v[236:239], v[204:207]\n\t" \
    "ds_read_b128 v[228:231], v253 offset:25600\n\t" \
    "s_waitcnt lgkmcnt(2)\n\t" \
    "v_mfma_f32_16x16x32_bf16 v[204:207], v[44:47], v[240:243], v[204:207]\n\t" \
    "ds_read_b128 v[232:235], v253 offset:6144\n\t" \
    "v_mfma_f32_16x16x32_bf16 v[208:211], v[48:51], v[236:239], v[208:211]\n\t" \
    "v_mfma_f32_16x16x32_bf16 v[208:211], v[52:55], v[240:243], v[208:211]\n\t" \
    "v_mfma_f32_16x16x32_bf16 v[200:203], v[244:247], v[236:239], v[200:203]\n\t" \
    "v_mfma_f32_16x16x32_bf16 v[200:203], v[248:251], v[240:243], v[200:203]\n\t" \
    "s_waitcnt lgkmcnt(2)\n\t" \
    "v_mfma_f32_16x16x32_bf16 v[216:219], v[40:43], v[224:227], v[216:219]\n\t" \
    "ds_read_b128 v[236:239], v253 offset:7168\n\t" \
    "s_waitcnt lgkmcnt(2)\n\t" \
    "v_mfma_f32_16x16x32_bf16 v[216:219], v[44:47], v[228:231], v[216:219]\n\t" \
    "ds_read_b128 v[240:243], v253 offset:26624\n\t" \
    "v_mfma_f32_16x16x32_bf16 v[220:223], v[48:51], v[224:227], v[220:223]\n\t" \
    "v_mfma_f32_16x16x32_bf16 v[220:223], v[52:55], v[228:231], v[220:223]\n\t" \
    "v_mfma_f32_16x16x32_bf16 v[212:215], v[244:247], v[224:227], v[212:215]\n\t" \
    "ds_read_b128 v[244:247], v254 offset:4096\n\t" \
    "v_mfma_f32_16x16x32_bf16 v[212:215], v[248:251], v[228:231], v[212:215]\n\t" \
    "ds_read_b128 v[248:251], v254 offset:5120\n\t" \
    "s_waitcnt lgkmcnt(4)\n\t" \
    "v_mfma_f32_16x16x32_bf16 v[204:207], v[64:67], v[232:235], v[204:207]\n\t" \
    "ds_read_b128 v[224:227], v253 offset:27648\n\t" \
    "s_waitcnt lgkmcnt(4)\n\t" \
    "v_mfma_f32_16x16x32_bf16 v[204:207], v[68:71], v[236:239], v[204:207]\n\t" \
    "ds_read_b128 v[228:231], v253 offset:8192\n\t" \
    "v_mfma_f32_16x16x32_bf16 v[208:211], v[72:75], v[232:235], v[208:211]\n\t" \
    "v_mfma_f32_16x16x32_bf16 v[208:211], v[76:79], v[236:239], v[208:211]\n\t" \
    "v_mfma_f32_16x16x32_bf16 v[200:203], v[56:59], v[232:235], v[200:203]\n\t" \
    "v_mfma_f32_16x16x32_bf16 v[200:203], v[60:63], v[236:239], v[200:203]\n\t" \
    "v_and_b32 v252, 0xf0, v253\n\t" \
    "v_lshlrev_b32 v252, 3, v252\n\t" \
    "buffer_load_dword v252, v252, s[60:63], %[fl] offen sc1\n\t" \
    "s_waitcnt lgkmcnt(4)\n\t" \
    "v_mfma_f32_16x16x32_bf16 v[216:219], v[64:67], v[240:243], v[216:219]\n\t" \
    "ds_read_b128 v[232:235], v253 offset:9216\n\t" \
    "s_waitcnt lgkmcnt(2)\n\t" \
    "v_mfma_f32_16x16x32_bf16 v[216:219], v[68:71], v[224:227], v[216:219]\n\t" \
    "ds_read_b128 v[236:239], v253 offset:28672\n\t" \
    "v_mfma_f32_16x16x32_bf16 v[220:223], v[72:75], v[240:243], v[220:223]\n\t" \
    "v_mfma_f32_16x16x32_bf16 v[220:223], v[76:79], v[224:227], v[220:223]\n\t" \
    "v_mfma_f32_16x16x32_bf16 v[212:215], v[56:59], v[240:243], v[212:215]\n\t" \
    "v_mfma_f32_16x16x32_bf16 v[212:215], v[60:63], v[224:227], v[212:215]\n\t" \
    "s_waitcnt lgkmcnt(2)\n\t" \
    "v_mfma_f32_16x16x32_bf16 v[204:207], v[80:83], v[228:231], v[204:207]\n\t" \
    "ds_read_b128 v[240:243], v253 offset:29696\n\t" \
    "s_waitcnt lgkmcnt(2)\n\t" \
    "v_mfma_f32_16x16x32_bf16 v[204:207], v[84:87], v[232:235], v[204:207]\n\t" \
    "ds_read_b128 v[224:227], v253 offset:10240\n\t" \
    "v_mfma_f32_16x16x32_bf16 v[208:211], v[88:91], v[228:231], v[208:211]\n\t" \
    "v_mfma_f32_16x16x32_bf16 v[208:211], v[92:95], v[232:235], v[208:211]\n\t" \
    "v_mfma_f32_16x16x32_bf16 v[200:203], v[244:247], v[228:231], v[200:203]\n\t" \
    "v_mfma_f32_16x16x32_bf16 v[200:203], v[248:251], v[232:235], v[200:203]\n\t" \
    "s_waitcnt lgkmcnt(2)\n\t" \
    "v_mfma_f32_16x16x32_bf16 v[216:219], v[80:83], v[236:239], v[216:219]\n\t" \
    "ds_read_b128 v[228:231], v253 offset:11264\n\t" \
    "s_add_u32 s44, s40, 1\n\t" \
    "s_waitcnt lgkmcnt(2)\n\t" \
    "v_mfma_f32_16x16x32_bf16 v[216:219], v[84:87], v[240:243], v[216:219]\n\t" \
    "ds_read_b128 v[232:235], v253 offset:30720\n\t" \
    "s_and_b32 s45, s44, 15\n\t" \
    "v_mfma_f32_16x16x32_bf16 v[220:223], v[88:91], v[236:239], v[220:223]\n\t" \
    "s_cmp_eq_u32 s45, 0\n\t" \
    "v_mfma_f32_16x16x32_bf16 v[220:223], v[92:95], v[240:243], v[220:223]\n\t" \
    "s_cselect_b32 s45, %[cj], %[cs]\n\t" \
    "v_mfma_f32_16x16x32_bf16 v[212:215], v[244:247], v[236:239], v[212:215]\n\t" \
    "ds_read_b128 v[244:247], v254 offset:6144\n\t" \
    "s_cmp_eq_u32 s44, 64\n\t" \
    "v_mfma_f32_16x16x32_bf16 v[212:215], v[248:251], v[240:243], v[212:215]\n\t" \
    "ds_read_b128 v[248:251], v254 offset:7168\n\t" \
    "s_cselect_b32 s45, 0, s45\n\t" \
    "ds_read_b128 v[236:239], v253 offset:31744\n\t" \
    "s_setprio 1\n\t" \
    "s_waitcnt vmcnt(0) lgkmcnt(0)\n\t" \
    "s_barrier \n\t" \
    "s_add_u32 s42, s42, s45\n\t" \
    "s_add_u32 s44, s40, 1\n\t" \
    "s_min_u32 s44, s44, 63\n\t" \
    ".Ls49_%=:\n\t" \
    "v_cmp_ge_u32 vcc, s44, v252\n\t" \
    "s_cbranch_vccz .Ls410_%=\n\t" \
    "s_cmp_eq_u32 s41, 0\n\t" \
    "s_cbranch_scc1 .Ls410_%=\n\t" \
    "s_sub_u32 s41, s41, 1\n\t" \
    "s_sleep 2\n\t" \
    "v_and_b32 v252, 0xf0, v253\n\t" \
    "v_lshlrev_b32 v252, 3, v252\n\t" \
    "buffer_load_dword v252, v252, s[60:63], %[fl] offen sc1\n\t" \
    "s_waitcnt vmcnt(0)\n\t" \
    "s_branch .Ls49_%=\n\t" \
    ".Ls410_%=:\n\t" \
    "s_mov_b32 m0, %[m0]\n\t" \
    "s_add_u32 s45, s42, %[x1]\n\t" \
    "buffer_load_dwordx4 v253, s[60:63], s42 offen offset:0 sc1 lds\n\t" \
    "buffer_load_dwordx4 v253, s[60:63], s42 offen offset:1024 sc1 lds\n\t" \
    "buffer_load_dwordx4 v253, s[60:63], s42 offen offset:2048 sc1 lds\n\t" \
    "buffer_load_dwordx4 v253, s[60:63], s42 offen offset:3072 sc1 lds\n\t" \
    "s_mov_b32 m0, %[m1]\n\t" \
    "s_nop 0\n\t" \
    "buffer_load_dwordx4 v253, s[60:63], s45 offen offset:0 sc1 lds\n\t" \
    "buffer_load_dwordx4 v253, s[60:63], s45 offen offset:1024 sc1 lds\n\t" \
    "v_mfma_f32_16x16x32_bf16 v[204:207], v[104:107], v[224:227], v[204:207]\n\t" \
    "v_mfma_f32_16x16x32_bf16 v[204:207], v[108:111], v[228:231], v[204:207]\n\t" \
    "ds_read_b128 v[240:243], v253 offset:12288\n\t" \
    "v_mfma_f32_16x16x32_bf16 v[208:211], v[112:115], v[224:227], v[208:211]\n\t" \
    "v_mfma_f32_16x16x32_bf16 v[208:211], v[116:119], v[228:231], v[208:211]\n\t" \
    "v_mfma_f32_16x16x32_bf16 v[200:203], v[96:99], v[224:227], v[200:203]\n\t" \
    "v_mfma_f32_16x16x32_bf16 v[200:203], v[100:103], v[228:231], v[200:203]\n\t" \
    "v_mfma_f32_16x16x32_bf16 v[216:219], v[104:107], v[232:235], v[216:219]\n\t" \
    "ds_read_b128 v[224:227], v253 offset:13312\n\t" \
    "v_mfma_f32_16x16x32_bf16 v[216:219], v[108:111], v[236:239], v[216:219]\n\t" \
    "ds_read_b128 v[228:231], v253 offset:32768\n\t" \
    "v_mfma_f32_16x16x32_bf16 v[220:223], v[112:115], v[232:235], v[220:223]\n\t" \
    "v_mfma_f32_16x16x32_bf16 v[220:223], v[116:119], v[236:239], v[220:223]\n\t" \
    "v_mfma_f32_16x16x32_bf16 v[212:215], v[96:99], v[232:235], v[212:215]\n\t" \
    "v_mfma_f32_16x16x32_bf16 v[212:215], v[100:103], v[236:239], v[212:215]\n\t" \
    "s_waitcnt lgkmcnt(2)\n\t" \
    "v_mfma_f32_16x16x32_bf16 v[204:207], v[120:123], v[240:243], v[204:207]\n\t" \
    "ds_read_b128 v[232:235], v253 offset:33792\n\t" \
    "s_waitcnt lgkmcnt(2)\n\t" \
    "v_mfma_f32_16x16x32_bf16 v[204:207], v[124:127], v[224:227], v[204:207]\n\t" \
    "ds_read_b128 v[236:239], v253 offset:16384\n\t" \
    "v_mfma_f32_16x16x32_bf16 v[208:211], v[128:131], v[240:243], v[208:211]\n\t" \
    "v_mfma_f32_16x16x32_bf16 v[208:211], v[132:135], v[224:227], v[208:211]\n\t" \
    "v_mfma_f32_16x16x32_bf16 v[200:203], v[244:247], v[240:243], v[200:203]\n\t" \
    "v_mfma_f32_16x16x32_bf16 v[200:203], v[248:251], v[224:227], v[200:203]\n\t" \
    "s_waitcnt lgkmcnt(2)\n\t" \
    "v_mfma_f32_16x16x32_bf16 v[216:219], v[120:123], v[228:231], v[216:219]\n\t" \
    "ds_read_b128 v[240:243], v253 offset:17408\n\t" \
    "s_waitcnt lgkmcnt(2)\n\t" \
    "v_mfma_f32_16x16x32_bf16 v[216:219], v[124:127], v[232:235], v[216:219]\n\t" \
    "ds_read_b128 v[224:227], v253 offset:36864\n\t" \
    "v_mfma_f32_16x16x32_bf16 v[220:223], v[128:131], v[228:231], v[220:223]\n\t" \
    "v_mfma_f32_16x16x32_bf16 v[220:223], v[132:135], v[232:235], v[220:223]\n\t" \
    "v_mfma_f32_16x16x32_bf16 v[212:215], v[244:247], v[228:231], v[212:215]\n\t" \
    "ds_read_b128 v[244:247], v254 offset:8192\n\t" \
    "v_mfma_f32_16x16x32_bf16 v[212:215], v[248:251], v[232:235], v[212:215]\n\t" \
    "ds_read_b128 v[248:251], v254 offset:9216\n\t" \
    "s_waitcnt lgkmcnt(4)\n\t" \
    "v_mfma_f32_16x16x32_bf16 v[204:207], v[160:163], v[236:239], v[204:207]\n\t" \
    "ds_read_b128 v[228:231], v253 offset:37888\n\t" \
    "s_waitcnt lgkmcnt(4)\n\t" \
    "v_mfma_f32_16x16x32_bf16 v[204:207], v[164:167], v[240:243], v[204:207]\n\t" \
    "ds_read_b128 v[232:235], v253 offset:14336\n\t" \
    "v_mfma_f32_16x16x32_bf16 v[208:211], v[168:171], v[236:239], v[208:211]\n\t" \
    "v_mfma_f32_16x16x32_bf16 v[208:211], v[172:175], v[240:243], v[208:211]\n\t" \
    "s_waitcnt lgkmcnt(3)\n\t" \
    "v_mfma_f32_16x16x32_bf16 v[200:203], v[244:247], v[236:239], v[200:203]\n\t" \
    "s_waitcnt lgkmcnt(2)\n\t" \
    "v_mfma_f32_16x16x32_bf16 v[200:203], v[248:251], v[240:243], v[200:203]\n\t" \
    "v_mfma_f32_16x16x32_bf16 v[216:219], v[160:163], v[224:227], v[216:219]\n\t" \
    "ds_read_b128 v[236:239], v253 offset:15360\n\t" \
    "v_add_u32 v252, %[q2], v253\n\t" \
    "s_waitcnt lgkmcnt(2)\n\t" \
    "v_mfma_f32_16x16x32_bf16 v[216:219], v[164:167], v[228:231], v[216:219]\n\t" \
    "ds_read_b128 v[240:243], v253 offset:18432\n\t" \
    "v_mfma_f32_16x16x32_bf16 v[220:223], v[168:171], v[224:227], v[220:223]\n\t" \
    "v_mfma_f32_16x16x32_bf16 v[220:223], v[172:175], v[228:231], v[220:223]\n\t" \
    "v_mfma_f32_16x16x32_bf16 v[212:215], v[244:247], v[224:227], v[212:215]\n\t" \
    "v_mfma_f32_16x16x32_bf16 v[212:215], v[248:251], v[228:231], v[212:215]\n\t" \
    "s_waitcnt lgkmcnt(2)\n\t" \
    "v_mfma_f32_16x16x32_bf16 v[204:207], v[144:147], v[232:235], v[204:207]\n\t" \
    "ds_read_b128 v[224:227], v253 offset:19456\n\t" \
    "v_mov_b64 v[244:245], 0\n\t" \
    "s_waitcnt lgkmcnt(2)\n\t" \
    "v_mfma_f32_16x16x32_bf16 v[204:207], v[148:151], v[236:239], v[204:207]\n\t" \
    "ds_read_b128 v[228:231], v253 offset:34816\n\t" \
    "v_mov_b64 v[246:247], 0\n\t" \
    "v_mfma_f32_16x16x32_bf16 v[208:211], v[152:155], v[232:235], v[208:211]\n\t" \
    "v_mov_b64 v[248:249], 0\n\t" \
    "v_mfma_f32_16x16x32_bf16 v[208:211], v[156:159], v[236:239], v[208:211]\n\t" \
    "v_mov_b64 v[250:251], 0\n\t" \
    "v_mfma_f32_16x16x32_bf16 v[200:203], v[136:139], v[232:235], v[200:203]\n\t" \
    "v_mfma_f32_16x16x32_bf16 v[200:203], v[140:143], v[236:239], v[200:203]\n\t" \
    "s_waitcnt lgkmcnt(2)\n\t" \
    "v_mfma_f32_16x16x32_bf16 v[204:207], v[184:187], v[240:243], v[204:207]\n\t" \
    "ds_read_b128 v[232:235], v253 offset:35840\n\t" \
    "s_waitcnt lgkmcnt(2)\n\t" \
    "v_mfma_f32_16x16x32_bf16 v[204:207], v[188:191], v[224:227], v[204:207]\n\t" \
    "ds_read_b128 v[236:239], v253 offset:38912\n\t" \
    "v_mfma_f32_16x16x32_bf16 v[208:211], v[192:195], v[240:243], v[208:211]\n\t" \
    "v_mfma_f32_16x16x32_bf16 v[208:211], v[196:199], v[224:227], v[208:211]\n\t" \
    "v_mfma_f32_16x16x32_bf16 v[200:203], v[176:179], v[240:243], v[200:203]\n\t" \
    "v_mfma_f32_16x16x32_bf16 v[200:203], v[180:183], v[224:227], v[200:203]\n\t" \
    "ds_read_b128 v[240:243], v253 offset:39936\n\t" \
    "s_waitcnt lgkmcnt(3)\n\t" \
    "v_mfma_f32_16x16x32_bf16 v[216:219], v[144:147], v[228:231], v[216:219]\n\t" \
    "s_waitcnt lgkmcnt(2)\n\t" \
    "v_mfma_f32_16x16x32_bf16 v[216:219], v[148:151], v[232:235], v[216:219]\n\t" \
    "v_cvt_pk_bf16_f32 v204, v204, v208\n\t" \
    "v_cvt_pk_bf16_f32 v205, v205, v209\n\t" \
    "s_waitcnt lgkmcnt(1)\n\t" \
    "v_mfma_f32_16x16x32_bf16 v[216:219], v[184:187], v[236:239], v[216:219]\n\t" \
    "v_cvt_pk_bf16_f32 v206, v206, v210\n\t" \
    "v_cvt_pk_bf16_f32 v207, v207, v211\n\t" \
    "s_waitcnt lgkmcnt(0)\n\t" \
    "v_mfma_f32_16x16x32_bf16 v[216:219], v[188:191], v[240:243], v[216:219]\n\t" \
    "ds_write_b128 v252, v[204:207] offset:0\n\t" \
    "v_mfma_f32_16x16x32_bf16 v[220:223], v[152:155], v[228:231], v[220:223]\n\t" \
    "v_mfma_f32_16x16x32_bf16 v[220:223], v[156:159], v[232:235], v[220:223]\n\t" \
    "v_add_u32 v252, %[qs], v253\n\t" \
    "v_cvt_pk_bf16_f32 v200, v200, v201\n\t" \
    "v_mfma_f32_16x16x32_bf16 v[220:223], v[192:195], v[236:239], v[220:223]\n\t" \
    "v_cvt_pk_bf16_f32 v201, v202, v203\n\t" \
    "ds_write_b64 v252, v[200:201] offset:0\n\t" \
    "v_mfma_f32_16x16x32_bf16 v[220:223], v[196:199], v[240:243], v[220:223]\n\t" \
    "v_mfma_f32_16x16x32_bf16 v[212:215], v[136:139], v[228:231], v[212:215]\n\t" \
    "v_mov_b64 v[208:209], 0\n\t" \
    "v_mfma_f32_16x16x32_bf16 v[212:215], v[140:143], v[232:235], v[212:215]\n\t" \
    "v_mov_b64 v[210:211], 0\n\t" \
    "v_mfma_f32_16x16x32_bf16 v[212:215], v[176:179], v[236:239], v[212:215]\n\t" \
    "v_add_u32 v252, %[q2], v253\n\t" \
    "v_cvt_pk_bf16_f32 v216, v216, v220\n\t" \
    "v_mfma_f32_16x16x32_bf16 v[212:215], v[180:183], v[240:243], v[212:215]\n\t" \
    "v_cvt_pk_bf16_f32 v217, v217, v221\n\t" \
    "v_cvt_pk_bf16_f32 v218, v218, v222\n\t" \
    "v_cvt_pk_bf16_f32 v219, v219, v223\n\t" \
    "ds_write_b128 v252, v[216:219] offset:6144\n\t" \
    "v_add_u32 v252, %[qs], v253\n\t" \
    "s_nop 2\n\t" \
    "v_cvt_pk_bf16_f32 v212, v212, v213\n\t" \
    "v_cvt_pk_bf16_f32 v213, v214, v215\n\t" \
    "ds_write_b64 v252, v[212:213] offset:6144\n\t" \
    "s_setprio 2\n\t" \
    "s_waitcnt vmcnt(0) lgkmcnt(0)\n\t" \
    "s_barrier \n\t" \
    "ds_read_b128 v[224:227], v253 offset:40960\n\t" \
    "ds_read_b128 v[228:231], v253 offset:41984\n\t" \
    "ds_read_b128 v[232:235], v253 offset:43008\n\t" \
    "ds_read_b128 v[236:239], v253 offset:44032\n\t" \
    "ds_read_b128 v[240:243], v253 offset:45056\n\t" \
    "ds_read_b128 v[200:203], v254 offset:0\n\t" \
    "ds_read_b128 v[204:207], v254 offset:1024\n\t" \
    "ds_read_b128 v[216:219], v254 offset:2048\n\t" \
    "ds_read_b128 v[220:223], v254 offset:3072\n\t" \
    "v_bfe_i32 v212, v253, 5, 1\n\t" \
    "v_and_b32 v212, 0xaaaa, v212\n\t" \
    "v_or_b32 v212, 0x4444, v212\n\t" \
    "v_and_b32 v213, 0xffff, v255\n\t" \
    "v_lshrrev_b32 v214, 16, v255\n\t" \
    "s_waitcnt lgkmcnt(8)\n\t" \
    "v_smfmac_f32_16x16x64_bf16 v[244:247], v[224:227], v[0:7], v212\n\t" \
    "v_smfmac_f32_16x16x64_bf16 v[248:251], v[224:227], v[8:15], v212\n\t" \
    "s_waitcnt lgkmcnt(2)\n\t" \
    "v_smfmac_f32_16x16x64_bf16 v[208:211], v[224:227], v[200:207], v212\n\t" \
    "ds_read_b128 v[200:203], v254 offset:4096\n\t" \
    "ds_read_b128 v[204:207], v254 offset:5120\n\t" \
    "ds_read_b128 v[224:227], v253 offset:46080\n\t" \
    "v_smfmac_f32_16x16x64_bf16 v[208:211], v[228:231], v[16:23], v212\n\t" \
    "v_smfmac_f32_16x16x64_bf16 v[244:247], v[228:231], v[24:31], v212\n\t" \
    "v_smfmac_f32_16x16x64_bf16 v[248:251], v[228:231], v[32:39], v212\n\t" \
    "ds_read_b128 v[228:231], v253 offset:47104\n\t" \
    "s_waitcnt lgkmcnt(4)\n\t" \
    "v_smfmac_f32_16x16x64_bf16 v[208:211], v[232:235], v[216:223], v212\n\t" \
    "ds_read_b128 v[216:219], v254 offset:6144\n\t" \
    "ds_read_b128 v[220:223], v254 offset:7168\n\t" \
    "v_smfmac_f32_16x16x64_bf16 v[244:247], v[232:235], v[40:47], v212\n\t" \
    "v_smfmac_f32_16x16x64_bf16 v[248:251], v[232:235], v[48:55], v212\n\t" \
    "ds_read_b128 v[232:235], v253 offset:48128\n\t" \
    "v_smfmac_f32_16x16x64_bf16 v[208:211], v[236:239], v[56:63], v212\n\t" \
    "v_smfmac_f32_16x16x64_bf16 v[244:247], v[236:239], v[64:71], v212\n\t" \
    "v_smfmac_f32_16x16x64_bf16 v[248:251], v[236:239], v[72:79], v212\n\t" \
    "ds_read_b128 v[236:239], v253 offset:49152\n\t" \
    "s_waitcnt lgkmcnt(6)\n\t" \
    "v_smfmac_f32_16x16x64_bf16 v[208:211], v[240:243], v[200:207], v212\n\t" \
    "ds_read_b128 v[200:203], v254 offset:8192\n\t" \
    "ds_read_b128 v[204:207], v254 offset:9216\n\t" \
    "v_smfmac_f32_16x16x64_bf16 v[244:247], v[240:243], v[80:87], v212\n\t" \
    "v_smfmac_f32_16x16x64_bf16 v[248:251], v[240:243], v[88:95], v212\n\t" \
    "ds_read_b128 v[240:243], v253 offset:50176\n\t" \
    "s_waitcnt lgkmcnt(8)\n\t" \
    "v_smfmac_f32_16x16x64_bf16 v[208:211], v[224:227], v[96:103], v212\n\t" \
    "v_smfmac_f32_16x16x64_bf16 v[244:247], v[224:227], v[104:111], v212\n\t" \
    "v_smfmac_f32_16x16x64_bf16 v[248:251], v[224:227], v[112:119], v212\n\t" \
    "s_waitcnt lgkmcnt(5)\n\t" \
    "v_smfmac_f32_16x16x64_bf16 v[208:211], v[228:231], v[216:223], v212\n\t" \
    "s_waitcnt lgkmcnt(4)\n\t" \
    "v_smfmac_f32_16x16x64_bf16 v[208:211], v[232:235], v[136:143], v212\n\t" \
    "s_waitcnt lgkmcnt(1)\n\t" \
    "v_smfmac_f32_16x16x64_bf16 v[208:211], v[236:239], v[200:207], v212\n\t" \
    "s_waitcnt lgkmcnt(0)\n\t" \
    "v_smfmac_f32_16x16x64_bf16 v[208:211], v[240:243], v[176:183], v212\n\t" \
    "v_smfmac_f32_16x16x64_bf16 v[244:247], v[228:231], v[120:127], v212\n\t" \
    "v_smfmac_f32_16x16x64_bf16 v[244:247], v[232:235], v[144:151], v212\n\t" \
    "v_smfmac_f32_16x16x64_bf16 v[244:247], v[236:239], v[160:167], v212\n\t" \
    "v_add_f32 v208, v208, v210\n\t" \
    "v_add_f32 v209, v209, v211\n\t" \
    "v_cvt_pk_bf16_f32 v208, v208, v209\n\t" \
    "v_smfmac_f32_16x16x64_bf16 v[244:247], v[240:243], v[184:191], v212\n\t" \
    "ds_write_b16 v213, v208 offset:0\n\t" \
    "ds_write_b16_d16_hi v213, v208 offset:16\n\t" \
    "v_smfmac_f32_16x16x64_bf16 v[248:251], v[228:231], v[128:135], v212\n\t" \
    "v_smfmac_f32_16x16x64_bf16 v[248:251], v[232:235], v[152:159], v212\n\t" \
    "v_smfmac_f32_16x16x64_bf16 v[248:251], v[236:239], v[168:175], v212\n\t" \
    "v_add_f32 v244, v244, v246\n\t" \
    "v_add_f32 v245, v245, v247\n\t" \
    "v_smfmac_f32_16x16x64_bf16 v[248:251], v[240:243], v[192:199], v212\n\t" \
    "s_nop 7\n\t" \
    "v_add_f32 v248, v248, v250\n\t" \
    "v_add_f32 v249, v249, v251\n\t" \
    "v_cvt_pk_bf16_f32 v244, v244, v248\n\t" \
    "v_cvt_pk_bf16_f32 v245, v245, v249\n\t" \
    "ds_write2_b32 v214, v244, v245 offset1:4\n\t" \
    "s_setprio 0\n\t" \
    "s_waitcnt lgkmcnt(0)\n\t" \
    "s_barrier \n\t" \
    "ds_read_b128 v[224:227], v253 offset:0\n\t" \
    "ds_read_b128 v[244:247], v254 offset:0\n\t" \
    "ds_read_b128 v[228:231], v253 offset:1024\n\t" \
    "ds_read_b128 v[232:235], v253 offset:20480\n\t" \
    "ds_read_b128 v[236:239], v253 offset:21504\n\t" \
    "ds_read_b128 v[248:251], v254 offset:1024\n\t" \
    "s_setprio 1\n\t" \
    "s_add_u32 s40, s40, 1\n\t" \
    "s_cmp_lg_u32 s40, 64\n\t" \
    "s_cbranch_scc1 .Ls47_%=\n\t" \
    "s_branch .Lend%=\n\t" \
    ".Ls3_%=:\n\t" \
    "v_add_u32 v244, 0x18000, v243\n\t" \
    "s_waitcnt vmcnt(12) lgkmcnt(0)\n\t" \
    "s_barrier \n\t" \
    "s_add_u32 m0, %[wm], 0x12000\n\t" \
    "s_mov_b32 s44, 0x1e0000\n\t" \
    "buffer_load_dwordx4 v240, %[wr], s44 offen offset:0 nt lds\n\t" \
    "buffer_load_dwordx4 v241, %[wr], s44 offen offset:1024 nt lds\n\t" \
    "buffer_load_dwordx4 v242, %[wr], s44 offen offset:2048 nt lds\n\t" \
    "ds_read_b64_tr_b16 v[208:209], v244 offset:0\n\t" \
    "ds_read_b64_tr_b16 v[210:211], v244 offset:1536\n\t" \
    "ds_read_b64_tr_b16 v[40:41], v244 offset:32\n\t" \
    "ds_read_b64_tr_b16 v[42:43], v244 offset:1568\n\t" \
    "ds_read_b64_tr_b16 v[48:49], v244 offset:64\n\t" \
    "ds_read_b64_tr_b16 v[50:51], v244 offset:1600\n\t" \
    "v_add_u32 v244, 0x1e000, v243\n\t" \
    "s_waitcnt vmcnt(12) lgkmcnt(0)\n\t" \
    "s_barrier \n\t" \
    "s_add_u32 m0, %[wm], 0x18000\n\t" \
    "s_mov_b32 s44, 0x240000\n\t" \
    "buffer_load_dwordx4 v240, %[wr], s44 offen offset:0 nt lds\n\t" \
    "buffer_load_dwordx4 v241, %[wr], s44 offen offset:1024 nt lds\n\t" \
    "buffer_load_dwordx4 v242, %[wr], s44 offen offset:2048 nt lds\n\t" \
    "ds_read_b64_tr_b16 v[212:213], v244 offset:0\n\t" \
    "ds_read_b64_tr_b16 v[214:215], v244 offset:1536\n\t" \
    "ds_read_b64_tr_b16 v[44:45], v244 offset:32\n\t" \
    "ds_read_b64_tr_b16 v[46:47], v244 offset:1568\n\t" \
    "ds_read_b64_tr_b16 v[52:53], v244 offset:64\n\t" \
    "ds_read_b64_tr_b16 v[54:55], v244 offset:1600\n\t" \
    "v_add_u32 v244, 0x0, v243\n\t" \
    "s_waitcnt vmcnt(12) lgkmcnt(0)\n\t" \
    "s_barrier \n\t" \
    "s_add_u32 m0, %[wm], 0x1e000\n\t" \
    "s_mov_b32 s44, 0x2a0000\n\t" \
    "buffer_load_dwordx4 v240, %[wr], s44 offen offset:0 nt lds\n\t" \
    "buffer_load_dwordx4 v241, %[wr], s44 offen offset:1024 nt lds\n\t" \
    "buffer_load_dwordx4 v242, %[wr], s44 offen offset:2048 nt lds\n\t" \
    "ds_read_b64_tr_b16 v[56:57], v244 offset:0\n\t" \
    "ds_read_b64_tr_b16 v[58:59], v244 offset:1536\n\t" \
    "ds_read_b64_tr_b16 v[64:65], v244 offset:32\n\t" \
    "ds_read_b64_tr_b16 v[66:67], v244 offset:1568\n\t" \
    "ds_read_b64_tr_b16 v[72:73], v244 offset:64\n\t" \
    "ds_read_b64_tr_b16 v[74:75], v244 offset:1600\n\t" \
    "v_add_u32 v244, 0x6000, v243\n\t" \
    "s_waitcnt vmcnt(12) lgkmcnt(0)\n\t" \
    "s_barrier \n\t" \
    "s_add_u32 m0, %[wm], 0x0\n\t" \
    "s_mov_b32 s44, 0x300000\n\t" \
    "buffer_load_dwordx4 v240, %[wr], s44 offen offset:0 nt lds\n\t" \
    "buffer_load_dwordx4 v241, %[wr], s44 offen offset:1024 nt lds\n\t" \
    "buffer_load_dwordx4 v242, %[wr], s44 offen offset:2048 nt lds\n\t" \
    "ds_read_b64_tr_b16 v[60:61], v244 offset:0\n\t" \
    "ds_read_b64_tr_b16 v[62:63], v244 offset:1536\n\t" \
    "ds_read_b64_tr_b16 v[68:69], v244 offset:32\n\t" \
    "ds_read_b64_tr_b16 v[70:71], v244 offset:1568\n\t" \
    "ds_read_b64_tr_b16 v[76:77], v244 offset:64\n\t" \
    "ds_read_b64_tr_b16 v[78:79], v244 offset:1600\n\t" \
    "v_add_u32 v244, 0xc000, v243\n\t" \
    "s_waitcnt vmcnt(12) lgkmcnt(0)\n\t" \
    "s_barrier \n\t" \
    "s_add_u32 m0, %[wm], 0x6000\n\t" \
    "s_mov_b32 s44, 0x360000\n\t" \
    "buffer_load_dwordx4 v240, %[wr], s44 offen offset:0 nt lds\n\t" \
    "buffer_load_dwordx4 v241, %[wr], s44 offen offset:1024 nt lds\n\t" \
    "buffer_load_dwordx4 v242, %[wr], s44 offen offset:2048 nt lds\n\t" \
    "ds_read_b64_tr_b16 v[216:217], v244 offset:0\n\t" \
    "ds_read_b64_tr_b16 v[218:219], v244 offset:1536\n\t" \
    "ds_read_b64_tr_b16 v[80:81], v244 offset:32\n\t" \
    "ds_read_b64_tr_b16 v[82:83], v244 offset:1568\n\t" \
    "ds_read_b64_tr_b16 v[88:89], v244 offset:64\n\t" \
    "ds_read_b64_tr_b16 v[90:91], v244 offset:1600\n\t" \
    "v_add_u32 v244, 0x12000, v243\n\t" \
    "s_waitcnt vmcnt(12) lgkmcnt(0)\n\t" \
    "s_barrier \n\t" \
    "s_add_u32 m0, %[wm], 0xc000\n\t" \
    "s_mov_b32 s44, 0x3c0000\n\t" \
    "buffer_load_dwordx4 v240, %[wr], s44 offen offset:0 nt lds\n\t" \
    "buffer_load_dwordx4 v241, %[wr], s44 offen offset:1024 nt lds\n\t" \
    "buffer_load_dwordx4 v242, %[wr], s44 offen offset:2048 nt lds\n\t" \
    "ds_read_b64_tr_b16 v[220:221], v244 offset:0\n\t" \
    "ds_read_b64_tr_b16 v[222:223], v244 offset:1536\n\t" \
    "ds_read_b64_tr_b16 v[84:85], v244 offset:32\n\t" \
    "ds_read_b64_tr_b16 v[86:87], v244 offset:1568\n\t" \
    "ds_read_b64_tr_b16 v[92:93], v244 offset:64\n\t" \
    "ds_read_b64_tr_b16 v[94:95], v244 offset:1600\n\t" \
    "v_add_u32 v244, 0x18000, v243\n\t" \
    "s_waitcnt vmcnt(12) lgkmcnt(0)\n\t" \
    "s_barrier \n\t" \
    "s_add_u32 m0, %[wm], 0x12000\n\t" \
    "s_mov_b32 s44, 0x420000\n\t" \
    "buffer_load_dwordx4 v240, %[wr], s44 offen offset:0 nt lds\n\t" \
    "buffer_load_dwordx4 v241, %[wr], s44 offen offset:1024 nt lds\n\t" \
    "buffer_load_dwordx4 v242, %[wr], s44 offen offset:2048 nt lds\n\t" \
    "ds_read_b64_tr_b16 v[96:97], v244 offset:0\n\t" \
    "ds_read_b64_tr_b16 v[98:99], v244 offset:1536\n\t" \
    "ds_read_b64_tr_b16 v[104:105], v244 offset:32\n\t" \
    "ds_read_b64_tr_b16 v[106:107], v244 offset:1568\n\t" \
    "ds_read_b64_tr_b16 v[112:113], v244 offset:64\n\t" \
    "ds_read_b64_tr_b16 v[114:115], v244 offset:1600\n\t" \
    "v_add_u32 v244, 0x1e000, v243\n\t" \
    "s_waitcnt vmcnt(12) lgkmcnt(0)\n\t" \
    "s_barrier \n\t" \
    "s_add_u32 m0, %[wm], 0x18000\n\t" \
    "s_mov_b32 s44, 0x480000\n\t" \
    "buffer_load_dwordx4 v240, %[wr], s44 offen offset:0 nt lds\n\t" \
    "buffer_load_dwordx4 v241, %[wr], s44 offen offset:1024 nt lds\n\t" \
    "buffer_load_dwordx4 v242, %[wr], s44 offen offset:2048 nt lds\n\t" \
    "ds_read_b64_tr_b16 v[100:101], v244 offset:0\n\t" \
    "ds_read_b64_tr_b16 v[102:103], v244 offset:1536\n\t" \
    "ds_read_b64_tr_b16 v[108:109], v244 offset:32\n\t" \
    "ds_read_b64_tr_b16 v[110:111], v244 offset:1568\n\t" \
    "ds_read_b64_tr_b16 v[116:117], v244 offset:64\n\t" \
    "ds_read_b64_tr_b16 v[118:119], v244 offset:1600\n\t" \
    "v_add_u32 v244, 0x0, v243\n\t" \
    "s_waitcnt vmcnt(12) lgkmcnt(0)\n\t" \
    "s_barrier \n\t" \
    "v_mov_b32 v246, 0\n\t" \
    "buffer_load_dword v246, v246, s[60:63], %[mo] offen sc1\n\t" \
    "s_add_u32 m0, %[wm], 0x1e000\n\t" \
    "s_mov_b32 s44, 0x4e0000\n\t" \
    "buffer_load_dwordx4 v240, %[wr], s44 offen offset:0 nt lds\n\t" \
    "buffer_load_dwordx4 v241, %[wr], s44 offen offset:1024 nt lds\n\t" \
    "buffer_load_dwordx4 v242, %[wr], s44 offen offset:2048 nt lds\n\t" \
    "ds_read_b64_tr_b16 v[224:225], v244 offset:0\n\t" \
    "ds_read_b64_tr_b16 v[226:227], v244 offset:1536\n\t" \
    "ds_read_b64_tr_b16 v[120:121], v244 offset:32\n\t" \
    "ds_read_b64_tr_b16 v[122:123], v244 offset:1568\n\t" \
    "ds_read_b64_tr_b16 v[128:129], v244 offset:64\n\t" \
    "ds_read_b64_tr_b16 v[130:131], v244 offset:1600\n\t" \
    "v_add_u32 v244, 0x6000, v243\n\t" \
    "s_waitcnt vmcnt(12) lgkmcnt(0)\n\t" \
    "s_barrier \n\t" \
    "s_add_u32 m0, %[wm], 0x0\n\t" \
    "s_mov_b32 s44, 0x540000\n\t" \
    "buffer_load_dwordx4 v240, %[wr], s44 offen offset:0 nt lds\n\t" \
    "buffer_load_dwordx4 v241, %[wr], s44 offen offset:1024 nt lds\n\t" \
    "buffer_load_dwordx4 v242, %[wr], s44 offen offset:2048 nt lds\n\t" \
    "ds_read_b64_tr_b16 v[228:229], v244 offset:0\n\t" \
    "ds_read_b64_tr_b16 v[230:231], v244 offset:1536\n\t" \
    "ds_read_b64_tr_b16 v[124:125], v244 offset:32\n\t" \
    "ds_read_b64_tr_b16 v[126:127], v244 offset:1568\n\t" \
    "ds_read_b64_tr_b16 v[132:133], v244 offset:64\n\t" \
    "ds_read_b64_tr_b16 v[134:135], v244 offset:1600\n\t" \
    "v_add_u32 v244, 0xc000, v243\n\t" \
    "s_waitcnt vmcnt(12) lgkmcnt(0)\n\t" \
    "s_barrier \n\t" \
    "s_add_u32 m0, %[wm], 0x6000\n\t" \
    "s_mov_b32 s44, 0x5a0000\n\t" \
    "buffer_load_dwordx4 v240, %[wr], s44 offen offset:0 nt lds\n\t" \
    "buffer_load_dwordx4 v241, %[wr], s44 offen offset:1024 nt lds\n\t" \
    "buffer_load_dwordx4 v242, %[wr], s44 offen offset:2048 nt lds\n\t" \
    "ds_read_b64_tr_b16 v[136:137], v244 offset:0\n\t" \
    "ds_read_b64_tr_b16 v[138:139], v244 offset:1536\n\t" \
    "ds_read_b64_tr_b16 v[144:145], v244 offset:32\n\t" \
    "ds_read_b64_tr_b16 v[146:147], v244 offset:1568\n\t" \
    "ds_read_b64_tr_b16 v[152:153], v244 offset:64\n\t" \
    "ds_read_b64_tr_b16 v[154:155], v244 offset:1600\n\t" \
    "v_add_u32 v244, 0x12000, v243\n\t" \
    "s_waitcnt vmcnt(12) lgkmcnt(0)\n\t" \
    "s_barrier \n\t" \
    "s_add_u32 m0, %[wm], 0xc000\n\t" \
    "s_mov_b32 s44, 0x600000\n\t" \
    "buffer_load_dwordx4 v240, %[wr], s44 offen offset:0 nt lds\n\t" \
    "buffer_load_dwordx4 v241, %[wr], s44 offen offset:1024 nt lds\n\t" \
    "buffer_load_dwordx4 v242, %[wr], s44 offen offset:2048 nt lds\n\t" \
    "ds_read_b64_tr_b16 v[140:141], v244 offset:0\n\t" \
    "ds_read_b64_tr_b16 v[142:143], v244 offset:1536\n\t" \
    "ds_read_b64_tr_b16 v[148:149], v244 offset:32\n\t" \
    "ds_read_b64_tr_b16 v[150:151], v244 offset:1568\n\t" \
    "ds_read_b64_tr_b16 v[156:157], v244 offset:64\n\t" \
    "ds_read_b64_tr_b16 v[158:159], v244 offset:1600\n\t" \
    "v_add_u32 v244, 0x18000, v243\n\t" \
    "s_waitcnt vmcnt(12) lgkmcnt(0)\n\t" \
    "s_barrier \n\t" \
    "s_add_u32 m0, %[wm], 0x12000\n\t" \
    "s_mov_b32 s44, 0x660000\n\t" \
    "buffer_load_dwordx4 v240, %[wr], s44 offen offset:0 nt lds\n\t" \
    "buffer_load_dwordx4 v241, %[wr], s44 offen offset:1024 nt lds\n\t" \
    "buffer_load_dwordx4 v242, %[wr], s44 offen offset:2048 nt lds\n\t" \
    "ds_read_b64_tr_b16 v[232:233], v244 offset:0\n\t" \
    "ds_read_b64_tr_b16 v[234:235], v244 offset:1536\n\t" \
    "ds_read_b64_tr_b16 v[160:161], v244 offset:32\n\t" \
    "ds_read_b64_tr_b16 v[162:163], v244 offset:1568\n\t" \
    "ds_read_b64_tr_b16 v[168:169], v244 offset:64\n\t" \
    "ds_read_b64_tr_b16 v[170:171], v244 offset:1600\n\t" \
    "v_add_u32 v244, 0x1e000, v243\n\t" \
    "s_waitcnt vmcnt(12) lgkmcnt(0)\n\t" \
    "s_barrier \n\t" \
    ".Ls31_%=:\n\t" \
    "v_readfirstlane_b32 s43, v246\n\t" \
    "s_nop 0\n\t" \
    "s_cmp_lg_u32 s43, -1\n\t" \
    "s_cbranch_scc1 .Ls32_%=\n\t" \
    "s_cmp_eq_u32 s41, 0\n\t" \
    "s_cbranch_scc1 .Ls32_%=\n\t" \
    "s_sub_u32 s41, s41, 1\n\t" \
    "s_sleep 4\n\t" \
    "v_mov_b32 v246, 0\n\t" \
    "buffer_load_dword v246, v246, s[60:63], %[mo] offen sc1\n\t" \
    "s_waitcnt vmcnt(0)\n\t" \
    "s_branch .Ls31_%=\n\t" \
    ".Ls32_%=:\n\t" \
    "v_mov_b32 v246, 0x8000\n\t" \
    "s_mov_b64 exec, 1\n\t" \
    "buffer_store_dword v246, v246, s[60:63], %[mo] offen sc1\n\t" \
    "s_mov_b64 exec, -1\n\t" \
    "s_and_b32 s44, s43, 0x5000\n\t" \
    "s_xor_b32 %[mb], %[mb], s44\n\t" \
    "s_xor_b32 %[fl], %[fl], s44\n\t" \
    "s_and_b32 s43, s43, 0x2000\n\t" \
    "s_xor_b32 s43, s43, %[lx]\n\t" \
    "s_add_u32 s60, s60, s43\n\t" \
    "s_addc_u32 s61, s61, 0\n\t" \
    "s_add_u32 m0, %[wm], 0x18000\n\t" \
    "s_mov_b32 s44, 0x6c0000\n\t" \
    "buffer_load_dwordx4 v240, %[wr], s44 offen offset:0 nt lds\n\t" \
    "buffer_load_dwordx4 v241, %[wr], s44 offen offset:1024 nt lds\n\t" \
    "buffer_load_dwordx4 v242, %[wr], s44 offen offset:2048 nt lds\n\t" \
    "ds_read_b64_tr_b16 v[236:237], v244 offset:0\n\t" \
    "ds_read_b64_tr_b16 v[238:239], v244 offset:1536\n\t" \
    "ds_read_b64_tr_b16 v[164:165], v244 offset:32\n\t" \
    "ds_read_b64_tr_b16 v[166:167], v244 offset:1568\n\t" \
    "ds_read_b64_tr_b16 v[172:173], v244 offset:64\n\t" \
    "ds_read_b64_tr_b16 v[174:175], v244 offset:1600\n\t" \
    "v_add_u32 v244, 0x0, v243\n\t" \
    "s_waitcnt vmcnt(12) lgkmcnt(0)\n\t" \
    "s_barrier \n\t" \
    "s_add_u32 m0, %[wm], 0x1e000\n\t" \
    "s_mov_b32 s44, 0x720000\n\t" \
    "buffer_load_dwordx4 v240, %[wr], s44 offen offset:0 nt lds\n\t" \
    "buffer_load_dwordx4 v241, %[wr], s44 offen offset:1024 nt lds\n\t" \
    "buffer_load_dwordx4 v242, %[wr], s44 offen offset:2048 nt lds\n\t" \
    "ds_read_b64_tr_b16 v[176:177], v244 offset:0\n\t" \
    "ds_read_b64_tr_b16 v[178:179], v244 offset:1536\n\t" \
    "ds_read_b64_tr_b16 v[184:185], v244 offset:32\n\t" \
    "ds_read_b64_tr_b16 v[186:187], v244 offset:1568\n\t" \
    "ds_read_b64_tr_b16 v[192:193], v244 offset:64\n\t" \
    "ds_read_b64_tr_b16 v[194:195], v244 offset:1600\n\t" \
    "v_add_u32 v244, 0x6000, v243\n\t" \
    "s_waitcnt vmcnt(12) lgkmcnt(0)\n\t" \
    "s_barrier \n\t" \
    "ds_read_b64_tr_b16 v[180:181], v244 offset:0\n\t" \
    "ds_read_b64_tr_b16 v[182:183], v244 offset:1536\n\t" \
    "ds_read_b64_tr_b16 v[188:189], v244 offset:32\n\t" \
    "ds_read_b64_tr_b16 v[190:191], v244 offset:1568\n\t" \
    "ds_read_b64_tr_b16 v[196:197], v244 offset:64\n\t" \
    "ds_read_b64_tr_b16 v[198:199], v244 offset:1600\n\t" \
    "v_add_u32 v244, 0xc000, v243\n\t" \
    "s_waitcnt vmcnt(9) lgkmcnt(0)\n\t" \
    "s_barrier \n\t" \
    "ds_read_b64_tr_b16 v[200:201], v244 offset:0\n\t" \
    "ds_read_b64_tr_b16 v[202:203], v244 offset:1536\n\t" \
    "ds_read_b64_tr_b16 v[0:1], v244 offset:32\n\t" \
    "ds_read_b64_tr_b16 v[2:3], v244 offset:1568\n\t" \
    "ds_read_b64_tr_b16 v[8:9], v244 offset:64\n\t" \
    "ds_read_b64_tr_b16 v[10:11], v244 offset:1600\n\t" \
    "v_add_u32 v244, 0x12000, v243\n\t" \
    "s_waitcnt vmcnt(6) lgkmcnt(0)\n\t" \
    "s_barrier \n\t" \
    "ds_read_b64_tr_b16 v[204:205], v244 offset:0\n\t" \
    "ds_read_b64_tr_b16 v[206:207], v244 offset:1536\n\t" \
    "ds_read_b64_tr_b16 v[4:5], v244 offset:32\n\t" \
    "ds_read_b64_tr_b16 v[6:7], v244 offset:1568\n\t" \
    "ds_read_b64_tr_b16 v[12:13], v244 offset:64\n\t" \
    "ds_read_b64_tr_b16 v[14:15], v244 offset:1600\n\t" \
    "v_add_u32 v244, 0x18000, v243\n\t" \
    "s_waitcnt vmcnt(3) lgkmcnt(0)\n\t" \
    "s_barrier \n\t" \
    "ds_read_b64_tr_b16 v[16:17], v244 offset:0\n\t" \
    "ds_read_b64_tr_b16 v[18:19], v244 offset:1536\n\t" \
    "ds_read_b64_tr_b16 v[24:25], v244 offset:32\n\t" \
    "ds_read_b64_tr_b16 v[26:27], v244 offset:1568\n\t" \
    "ds_read_b64_tr_b16 v[32:33], v244 offset:64\n\t" \
    "ds_read_b64_tr_b16 v[34:35], v244 offset:1600\n\t" \
    "v_add_u32 v244, 0x1e000, v243\n\t" \
    "s_waitcnt vmcnt(0) lgkmcnt(0)\n\t" \
    "s_barrier \n\t" \
    "ds_read_b64_tr_b16 v[20:21], v244 offset:0\n\t" \
    "ds_read_b64_tr_b16 v[22:23], v244 offset:1536\n\t" \
    "ds_read_b64_tr_b16 v[28:29], v244 offset:32\n\t" \
    "ds_read_b64_tr_b16 v[30:31], v244 offset:1568\n\t" \
    "ds_read_b64_tr_b16 v[36:37], v244 offset:64\n\t" \
    "ds_read_b64_tr_b16 v[38:39], v244 offset:1600\n\t" \
    "s_waitcnt lgkmcnt(0)\n\t" \
    "s_barrier \n\t" \
    "ds_write_b128 v254, v[200:203] offset:55296\n\t" \
    "ds_write_b128 v254, v[204:207] offset:56320\n\t" \
    "ds_write_b128 v254, v[208:211] offset:57344\n\t" \
    "ds_write_b128 v254, v[212:215] offset:58368\n\t" \
    "ds_write_b128 v254, v[216:219] offset:59392\n\t" \
    "ds_write_b128 v254, v[220:223] offset:60416\n\t" \
    "ds_write_b128 v254, v[224:227] offset:61440\n\t" \
    "ds_write_b128 v254, v[228:231] offset:62464\n\t" \
    "ds_write_b128 v254, v[232:235] offset:63488\n\t" \
    "ds_write_b128 v254, v[236:239] offset:64512\n\t" \
    "s_waitcnt vmcnt(0) lgkmcnt(0)\n\t" \
    "s_barrier \n\t" \
    "ds_read_b128 v[208:211], v254 offset:24576\n\t" \
    "ds_read_b128 v[224:227], v253 offset:0\n\t" \
    "ds_read_b128 v[228:231], v253 offset:20480\n\t" \
    "ds_read_b128 v[212:215], v254 offset:25600\n\t" \
    "ds_read_b128 v[232:235], v253 offset:1024\n\t" \
    "ds_read_b128 v[236:239], v253 offset:21504\n\t" \
    "ds_read_b128 v[216:219], v254 offset:26624\n\t" \
    "ds_read_b128 v[240:243], v253 offset:2048\n\t" \
    "ds_read_b128 v[244:247], v253 offset:22528\n\t" \
    "ds_read_b128 v[220:223], v254 offset:27648\n\t" \
    "ds_read_b128 v[248:251], v253 offset:3072\n\t" \
    "s_waitcnt lgkmcnt(9)\n\t" \
    "v_mfma_f32_16x16x32_bf16 v[200:203], v[208:211], v[224:227], 0\n\t" \
    "ds_read_b128 v[224:227], v253 offset:23552\n\t" \
    "s_waitcnt lgkmcnt(9)\n\t" \
    "v_mfma_f32_16x16x32_bf16 v[204:207], v[208:211], v[228:231], 0\n\t" \
    "ds_read_b128 v[228:231], v253 offset:4096\n\t" \
    "ds_read_b128 v[208:211], v254 offset:28672\n\t" \
    "s_waitcnt lgkmcnt(9)\n\t" \
    "v_mfma_f32_16x16x32_bf16 v[200:203], v[212:215], v[232:235], v[200:203]\n\t" \
    "ds_read_b128 v[232:235], v253 offset:24576\n\t" \
    "s_waitcnt lgkmcnt(9)\n\t" \
    "v_mfma_f32_16x16x32_bf16 v[204:207], v[212:215], v[236:239], v[204:207]\n\t" \
    "ds_read_b128 v[236:239], v253 offset:5120\n\t" \
    "ds_read_b128 v[212:215], v254 offset:29696\n\t" \
    ".Ls33_%=:\n\t" \
    "s_waitcnt lgkmcnt(9)\n\t" \
    "v_mfma_f32_16x16x32_bf16 v[200:203], v[216:219], v[240:243], v[200:203]\n\t" \
    "ds_read_b128 v[240:243], v253 offset:25600\n\t" \
    "s_waitcnt lgkmcnt(9)\n\t" \
    "v_mfma_f32_16x16x32_bf16 v[204:207], v[216:219], v[244:247], v[204:207]\n\t" \
    "ds_read_b128 v[244:247], v253 offset:6144\n\t" \
    "ds_read_b128 v[216:219], v254 offset:30720\n\t" \
    "s_waitcnt lgkmcnt(9)\n\t" \
    "v_mfma_f32_16x16x32_bf16 v[200:203], v[220:223], v[248:251], v[200:203]\n\t" \
    "ds_read_b128 v[248:251], v253 offset:26624\n\t" \
    "s_waitcnt lgkmcnt(9)\n\t" \
    "v_mfma_f32_16x16x32_bf16 v[204:207], v[220:223], v[224:227], v[204:207]\n\t" \
    "ds_read_b128 v[224:227], v253 offset:7168\n\t" \
    "ds_read_b128 v[220:223], v254 offset:31744\n\t" \
    "s_waitcnt lgkmcnt(9)\n\t" \
    "v_mfma_f32_16x16x32_bf16 v[200:203], v[208:211], v[228:231], v[200:203]\n\t" \
    "ds_read_b128 v[228:231], v253 offset:27648\n\t" \
    "s_waitcnt lgkmcnt(9)\n\t" \
    "v_mfma_f32_16x16x32_bf16 v[204:207], v[208:211], v[232:235], v[204:207]\n\t" \
    "ds_read_b128 v[232:235], v253 offset:8192\n\t" \
    "ds_read_b128 v[208:211], v254 offset:32768\n\t" \
    "s_waitcnt lgkmcnt(9)\n\t" \
    "v_mfma_f32_16x16x32_bf16 v[200:203], v[212:215], v[236:239], v[200:203]\n\t" \
    "ds_read_b128 v[236:239], v253 offset:28672\n\t" \
    "s_waitcnt lgkmcnt(9)\n\t" \
    "v_mfma_f32_16x16x32_bf16 v[204:207], v[212:215], v[240:243], v[204:207]\n\t" \
    "ds_read_b128 v[240:243], v253 offset:9216\n\t" \
    "ds_read_b128 v[212:215], v254 offset:33792\n\t" \
    "s_waitcnt lgkmcnt(9)\n\t" \
    "v_mfma_f32_16x16x32_bf16 v[200:203], v[216:219], v[244:247], v[200:203]\n\t" \
    "ds_read_b128 v[244:247], v253 offset:29696\n\t" \
    "s_waitcnt lgkmcnt(9)\n\t" \
    "v_mfma_f32_16x16x32_bf16 v[204:207], v[216:219], v[248:251], v[204:207]\n\t" \
    "s_waitcnt lgkmcnt(7)\n\t" \
    "v_mfma_f32_16x16x32_bf16 v[200:203], v[220:223], v[224:227], v[200:203]\n\t" \
    "s_waitcnt lgkmcnt(6)\n\t" \
    "v_mfma_f32_16x16x32_bf16 v[204:207], v[220:223], v[228:231], v[204:207]\n\t" \
    "s_waitcnt lgkmcnt(4)\n\t" \
    "v_mfma_f32_16x16x32_bf16 v[200:203], v[208:211], v[232:235], v[200:203]\n\t" \
    "s_waitcnt lgkmcnt(3)\n\t" \
    "v_mfma_f32_16x16x32_bf16 v[204:207], v[208:211], v[236:239], v[204:207]\n\t" \
    "s_waitcnt lgkmcnt(1)\n\t" \
    "v_mfma_f32_16x16x32_bf16 v[200:203], v[212:215], v[240:243], v[200:203]\n\t" \
    "s_waitcnt lgkmcnt(0)\n\t" \
    "v_mfma_f32_16x16x32_bf16 v[204:207], v[212:215], v[244:247], v[204:207]\n\t" \
    "ds_read_b128 v[224:227], v253 offset:0\n\t" \
    "ds_read_b128 v[244:247], v254 offset:57344\n\t" \
    "ds_read_b128 v[228:231], v253 offset:1024\n\t" \
    "ds_read_b128 v[232:235], v253 offset:20480\n\t" \
    "ds_read_b128 v[236:239], v253 offset:21504\n\t" \
    "ds_read_b128 v[248:251], v254 offset:58368\n\t" \
    "s_nop 1\n\t" \
    "ds_write_b128 v253, v[200:203] offset:40960\n\t" \
    "ds_write_b128 v253, v[204:207] offset:41984\n\t" \
    "s_nop 1\n\t" \
    "s_waitcnt lgkmcnt(7)\n\t" \
    "v_mfma_f32_16x16x32_bf16 v[204:207], v[40:43], v[224:227], 0\n\t" \
    "s_waitcnt lgkmcnt(5)\n\t" \
    "v_mfma_f32_16x16x32_bf16 v[204:207], v[44:47], v[228:231], v[204:207]\n\t" \
    "ds_read_b128 v[240:243], v253 offset:2048\n\t" \
    "v_mfma_f32_16x16x32_bf16 v[208:211], v[48:51], v[224:227], 0\n\t" \
    "v_mfma_f32_16x16x32_bf16 v[208:211], v[52:55], v[228:231], v[208:211]\n\t" \
    "v_mfma_f32_16x16x32_bf16 v[200:203], v[244:247], v[224:227], 0\n\t" \
    "s_waitcnt lgkmcnt(3)\n\t" \
    "v_mfma_f32_16x16x32_bf16 v[200:203], v[248:251], v[228:231], v[200:203]\n\t" \
    "v_mfma_f32_16x16x32_bf16 v[216:219], v[40:43], v[232:235], 0\n\t" \
    "ds_read_b128 v[224:227], v253 offset:3072\n\t" \
    "v_mfma_f32_16x16x32_bf16 v[216:219], v[44:47], v[236:239], v[216:219]\n\t" \
    "ds_read_b128 v[228:231], v253 offset:22528\n\t" \
    "v_mfma_f32_16x16x32_bf16 v[220:223], v[48:51], v[232:235], 0\n\t" \
    "v_mfma_f32_16x16x32_bf16 v[220:223], v[52:55], v[236:239], v[220:223]\n\t" \
    "v_mfma_f32_16x16x32_bf16 v[212:215], v[244:247], v[232:235], 0\n\t" \
    "ds_read_b128 v[244:247], v254 offset:59392\n\t" \
    "v_mfma_f32_16x16x32_bf16 v[212:215], v[248:251], v[236:239], v[212:215]\n\t" \
    "ds_read_b128 v[248:251], v254 offset:60416\n\t" \
    "s_waitcnt lgkmcnt(4)\n\t" \
    "v_mfma_f32_16x16x32_bf16 v[204:207], v[64:67], v[240:243], v[204:207]\n\t" \
    "ds_read_b128 v[232:235], v253 offset:23552\n\t" \
    "s_waitcnt lgkmcnt(4)\n\t" \
    "v_mfma_f32_16x16x32_bf16 v[204:207], v[68:71], v[224:227], v[204:207]\n\t" \
    "ds_read_b128 v[236:239], v253 offset:4096\n\t" \
    "v_mfma_f32_16x16x32_bf16 v[208:211], v[72:75], v[240:243], v[208:211]\n\t" \
    "v_mfma_f32_16x16x32_bf16 v[208:211], v[76:79], v[224:227], v[208:211]\n\t" \
    "v_mfma_f32_16x16x32_bf16 v[200:203], v[56:59], v[240:243], v[200:203]\n\t" \
    "v_mfma_f32_16x16x32_bf16 v[200:203], v[60:63], v[224:227], v[200:203]\n\t" \
    "s_waitcnt lgkmcnt(4)\n\t" \
    "v_mfma_f32_16x16x32_bf16 v[216:219], v[64:67], v[228:231], v[216:219]\n\t" \
    "ds_read_b128 v[240:243], v253 offset:5120\n\t" \
    "s_waitcnt lgkmcnt(2)\n\t" \
    "v_mfma_f32_16x16x32_bf16 v[216:219], v[68:71], v[232:235], v[216:219]\n\t" \
    "ds_read_b128 v[224:227], v253 offset:24576\n\t" \
    "v_mfma_f32_16x16x32_bf16 v[220:223], v[72:75], v[228:231], v[220:223]\n\t" \
    "v_mfma_f32_16x16x32_bf16 v[220:223], v[76:79], v[232:235], v[220:223]\n\t" \
    "v_mfma_f32_16x16x32_bf16 v[212:215], v[56:59], v[228:231], v[212:215]\n\t" \
    "v_mfma_f32_16x16x32_bf16 v[212:215], v[60:63], v[232:235], v[212:215]\n\t" \
    "s_waitcnt lgkmcnt(2)\n\t" \
    "v_mfma_f32_16x16x32_bf16 v[204:207], v[80:83], v[236:239], v[204:207]\n\t" \
    "ds_read_b128 v[228:231], v253 offset:25600\n\t" \
    "s_waitcnt lgkmcnt(2)\n\t" \
    "v_mfma_f32_16x16x32_bf16 v[204:207], v[84:87], v[240:243], v[204:207]\n\t" \
    "ds_read_b128 v[232:235], v253 offset:6144\n\t" \
    "v_mfma_f32_16x16x32_bf16 v[208:211], v[88:91], v[236:239], v[208:211]\n\t" \
    "v_mfma_f32_16x16x32_bf16 v[208:211], v[92:95], v[240:243], v[208:211]\n\t" \
    "v_mfma_f32_16x16x32_bf16 v[200:203], v[244:247], v[236:239], v[200:203]\n\t" \
    "v_mfma_f32_16x16x32_bf16 v[200:203], v[248:251], v[240:243], v[200:203]\n\t" \
    "s_waitcnt lgkmcnt(2)\n\t" \
    "v_mfma_f32_16x16x32_bf16 v[216:219], v[80:83], v[224:227], v[216:219]\n\t" \
    "ds_read_b128 v[236:239], v253 offset:7168\n\t" \
    "s_waitcnt lgkmcnt(2)\n\t" \
    "v_mfma_f32_16x16x32_bf16 v[216:219], v[84:87], v[228:231], v[216:219]\n\t" \
    "ds_read_b128 v[240:243], v253 offset:26624\n\t" \
    "v_mfma_f32_16x16x32_bf16 v[220:223], v[88:91], v[224:227], v[220:223]\n\t" \
    "v_mfma_f32_16x16x32_bf16 v[220:223], v[92:95], v[228:231], v[220:223]\n\t" \
    "v_mfma_f32_16x16x32_bf16 v[212:215], v[244:247], v[224:227], v[212:215]\n\t" \
    "ds_read_b128 v[244:247], v254 offset:61440\n\t" \
    "v_mfma_f32_16x16x32_bf16 v[212:215], v[248:251], v[228:231], v[212:215]\n\t" \
    "ds_read_b128 v[248:251], v254 offset:62464\n\t" \
    "s_waitcnt lgkmcnt(4)\n\t" \
    "v_mfma_f32_16x16x32_bf16 v[204:207], v[104:107], v[232:235], v[204:207]\n\t" \
    "ds_read_b128 v[224:227], v253 offset:27648\n\t" \
    "s_waitcnt lgkmcnt(4)\n\t" \
    "v_mfma_f32_16x16x32_bf16 v[204:207], v[108:111], v[236:239], v[204:207]\n\t" \
    "ds_read_b128 v[228:231], v253 offset:8192\n\t" \
    "v_mfma_f32_16x16x32_bf16 v[208:211], v[112:115], v[232:235], v[208:211]\n\t" \
    "v_mfma_f32_16x16x32_bf16 v[208:211], v[116:119], v[236:239], v[208:211]\n\t" \
    "v_mfma_f32_16x16x32_bf16 v[200:203], v[96:99], v[232:235], v[200:203]\n\t" \
    "v_mfma_f32_16x16x32_bf16 v[200:203], v[100:103], v[236:239], v[200:203]\n\t" \
    "s_waitcnt lgkmcnt(4)\n\t" \
    "v_mfma_f32_16x16x32_bf16 v[216:219], v[104:107], v[240:243], v[216:219]\n\t" \
    "ds_read_b128 v[232:235], v253 offset:9216\n\t" \
    "s_waitcnt lgkmcnt(2)\n\t" \
    "v_mfma_f32_16x16x32_bf16 v[216:219], v[108:111], v[224:227], v[216:219]\n\t" \
    "ds_read_b128 v[236:239], v253 offset:28672\n\t" \
    "v_mfma_f32_16x16x32_bf16 v[220:223], v[112:115], v[240:243], v[220:223]\n\t" \
    "v_mfma_f32_16x16x32_bf16 v[220:223], v[116:119], v[224:227], v[220:223]\n\t" \
    "v_mfma_f32_16x16x32_bf16 v[212:215], v[96:99], v[240:243], v[212:215]\n\t" \
    "v_mfma_f32_16x16x32_bf16 v[212:215], v[100:103], v[224:227], v[212:215]\n\t" \
    "s_waitcnt lgkmcnt(2)\n\t" \
    "v_mfma_f32_16x16x32_bf16 v[204:207], v[120:123], v[228:231], v[204:207]\n\t" \
    "ds_read_b128 v[240:243], v253 offset:29696\n\t" \
    "s_waitcnt lgkmcnt(2)\n\t" \
    "v_mfma_f32_16x16x32_bf16 v[204:207], v[124:127], v[232:235], v[204:207]\n\t" \
    "ds_read_b128 v[224:227], v253 offset:10240\n\t" \
    "v_mfma_f32_16x16x32_bf16 v[208:211], v[128:131], v[228:231], v[208:211]\n\t" \
    "v_mfma_f32_16x16x32_bf16 v[208:211], v[132:135], v[232:235], v[208:211]\n\t" \
    "v_mfma_f32_16x16x32_bf16 v[200:203], v[244:247], v[228:231], v[200:203]\n\t" \
    "v_mfma_f32_16x16x32_bf16 v[200:203], v[248:251], v[232:235], v[200:203]\n\t" \
    "s_waitcnt lgkmcnt(2)\n\t" \
    "v_mfma_f32_16x16x32_bf16 v[216:219], v[120:123], v[236:239], v[216:219]\n\t" \
    "ds_read_b128 v[228:231], v253 offset:11264\n\t" \
    "s_waitcnt lgkmcnt(2)\n\t" \
    "v_mfma_f32_16x16x32_bf16 v[216:219], v[124:127], v[240:243], v[216:219]\n\t" \
    "ds_read_b128 v[232:235], v253 offset:30720\n\t" \
    "v_mfma_f32_16x16x32_bf16 v[220:223], v[128:131], v[236:239], v[220:223]\n\t" \
    "v_mfma_f32_16x16x32_bf16 v[220:223], v[132:135], v[240:243], v[220:223]\n\t" \
    "v_mfma_f32_16x16x32_bf16 v[212:215], v[244:247], v[236:239], v[212:215]\n\t" \
    "ds_read_b128 v[244:247], v254 offset:63488\n\t" \
    "v_mfma_f32_16x16x32_bf16 v[212:215], v[248:251], v[240:243], v[212:215]\n\t" \
    "ds_read_b128 v[248:251], v254 offset:64512\n\t" \
    "ds_read_b128 v[236:239], v253 offset:31744\n\t" \
    "s_setprio 2\n\t" \
    "s_waitcnt lgkmcnt(0)\n\t" \
    "s_barrier \n\t" \
    "v_mfma_f32_16x16x32_bf16 v[204:207], v[144:147], v[224:227], v[204:207]\n\t" \
    "v_mfma_f32_16x16x32_bf16 v[204:207], v[148:151], v[228:231], v[204:207]\n\t" \
    "ds_read_b128 v[240:243], v253 offset:12288\n\t" \
    "v_mfma_f32_16x16x32_bf16 v[208:211], v[152:155], v[224:227], v[208:211]\n\t" \
    "v_mfma_f32_16x16x32_bf16 v[208:211], v[156:159], v[228:231], v[208:211]\n\t" \
    "v_mfma_f32_16x16x32_bf16 v[200:203], v[136:139], v[224:227], v[200:203]\n\t" \
    "v_mfma_f32_16x16x32_bf16 v[200:203], v[140:143], v[228:231], v[200:203]\n\t" \
    "v_mfma_f32_16x16x32_bf16 v[216:219], v[144:147], v[232:235], v[216:219]\n\t" \
    "ds_read_b128 v[224:227], v253 offset:13312\n\t" \
    "s_and_b32 s44, s40, 3\n\t" \
    "v_mfma_f32_16x16x32_bf16 v[216:219], v[148:151], v[236:239], v[216:219]\n\t" \
    "ds_read_b128 v[228:231], v253 offset:32768\n\t" \
    "s_lshl_b32 s44, s44, 22\n\t" \
    "v_mfma_f32_16x16x32_bf16 v[220:223], v[152:155], v[232:235], v[220:223]\n\t" \
    "s_add_u32 s44, s44, %[mb]\n\t" \
    "v_mfma_f32_16x16x32_bf16 v[220:223], v[156:159], v[236:239], v[220:223]\n\t" \
    "s_add_u32 s46, s44, 0x10000\n\t" \
    "v_mfma_f32_16x16x32_bf16 v[212:215], v[136:139], v[232:235], v[212:215]\n\t" \
    "s_add_u32 s47, s44, 0x10400\n\t" \
    "v_mfma_f32_16x16x32_bf16 v[212:215], v[140:143], v[236:239], v[212:215]\n\t" \
    "s_waitcnt lgkmcnt(2)\n\t" \
    "v_mfma_f32_16x16x32_bf16 v[204:207], v[160:163], v[240:243], v[204:207]\n\t" \
    "ds_read_b128 v[232:235], v253 offset:33792\n\t" \
    "s_waitcnt lgkmcnt(2)\n\t" \
    "v_mfma_f32_16x16x32_bf16 v[204:207], v[164:167], v[224:227], v[204:207]\n\t" \
    "ds_read_b128 v[236:239], v253 offset:16384\n\t" \
    "v_mfma_f32_16x16x32_bf16 v[208:211], v[168:171], v[240:243], v[208:211]\n\t" \
    "v_mfma_f32_16x16x32_bf16 v[208:211], v[172:175], v[224:227], v[208:211]\n\t" \
    "v_mfma_f32_16x16x32_bf16 v[200:203], v[244:247], v[240:243], v[200:203]\n\t" \
    "v_mfma_f32_16x16x32_bf16 v[200:203], v[248:251], v[224:227], v[200:203]\n\t" \
    "s_waitcnt lgkmcnt(2)\n\t" \
    "v_mfma_f32_16x16x32_bf16 v[216:219], v[160:163], v[228:231], v[216:219]\n\t" \
    "ds_read_b128 v[240:243], v253 offset:17408\n\t" \
    "s_waitcnt lgkmcnt(2)\n\t" \
    "v_mfma_f32_16x16x32_bf16 v[216:219], v[164:167], v[232:235], v[216:219]\n\t" \
    "ds_read_b128 v[224:227], v253 offset:36864\n\t" \
    "v_mfma_f32_16x16x32_bf16 v[220:223], v[168:171], v[228:231], v[220:223]\n\t" \
    "v_mfma_f32_16x16x32_bf16 v[220:223], v[172:175], v[232:235], v[220:223]\n\t" \
    "v_mfma_f32_16x16x32_bf16 v[212:215], v[244:247], v[228:231], v[212:215]\n\t" \
    "ds_read_b128 v[244:247], v254 offset:55296\n\t" \
    "v_mfma_f32_16x16x32_bf16 v[212:215], v[248:251], v[232:235], v[212:215]\n\t" \
    "ds_read_b128 v[248:251], v254 offset:56320\n\t" \
    "s_waitcnt lgkmcnt(4)\n\t" \
    "v_mfma_f32_16x16x32_bf16 v[204:207], v[0:3], v[236:239], v[204:207]\n\t" \
    "ds_read_b128 v[228:231], v253 offset:37888\n\t" \
    "s_waitcnt lgkmcnt(4)\n\t" \
    "v_mfma_f32_16x16x32_bf16 v[204:207], v[4:7], v[240:243], v[204:207]\n\t" \
    "ds_read_b128 v[232:235], v253 offset:14336\n\t" \
    "v_mfma_f32_16x16x32_bf16 v[208:211], v[8:11], v[236:239], v[208:211]\n\t" \
    "v_mfma_f32_16x16x32_bf16 v[208:211], v[12:15], v[240:243], v[208:211]\n\t" \
    "s_waitcnt lgkmcnt(3)\n\t" \
    "v_mfma_f32_16x16x32_bf16 v[200:203], v[244:247], v[236:239], v[200:203]\n\t" \
    "s_waitcnt lgkmcnt(2)\n\t" \
    "v_mfma_f32_16x16x32_bf16 v[200:203], v[248:251], v[240:243], v[200:203]\n\t" \
    "v_mfma_f32_16x16x32_bf16 v[216:219], v[0:3], v[224:227], v[216:219]\n\t" \
    "ds_read_b128 v[236:239], v253 offset:15360\n\t" \
    "s_waitcnt lgkmcnt(2)\n\t" \
    "v_mfma_f32_16x16x32_bf16 v[216:219], v[4:7], v[228:231], v[216:219]\n\t" \
    "ds_read_b128 v[240:243], v253 offset:18432\n\t" \
    "v_mfma_f32_16x16x32_bf16 v[220:223], v[8:11], v[224:227], v[220:223]\n\t" \
    "v_mfma_f32_16x16x32_bf16 v[220:223], v[12:15], v[228:231], v[220:223]\n\t" \
    "v_mfma_f32_16x16x32_bf16 v[212:215], v[244:247], v[224:227], v[212:215]\n\t" \
    "v_mfma_f32_16x16x32_bf16 v[212:215], v[248:251], v[228:231], v[212:215]\n\t" \
    "s_waitcnt lgkmcnt(2)\n\t" \
    "v_mfma_f32_16x16x32_bf16 v[204:207], v[184:187], v[232:235], v[204:207]\n\t" \
    "ds_read_b128 v[224:227], v253 offset:19456\n\t" \
    "v_mov_b64 v[244:245], 0\n\t" \
    "s_waitcnt lgkmcnt(2)\n\t" \
    "v_mfma_f32_16x16x32_bf16 v[204:207], v[188:191], v[236:239], v[204:207]\n\t" \
    "ds_read_b128 v[228:231], v253 offset:34816\n\t" \
    "v_mov_b64 v[246:247], 0\n\t" \
    "v_mfma_f32_16x16x32_bf16 v[208:211], v[192:195], v[232:235], v[208:211]\n\t" \
    "v_mov_b64 v[248:249], 0\n\t" \
    "v_mfma_f32_16x16x32_bf16 v[208:211], v[196:199], v[236:239], v[208:211]\n\t" \
    "v_mov_b64 v[250:251], 0\n\t" \
    "v_mfma_f32_16x16x32_bf16 v[200:203], v[176:179], v[232:235], v[200:203]\n\t" \
    "v_mfma_f32_16x16x32_bf16 v[200:203], v[180:183], v[236:239], v[200:203]\n\t" \
    "s_waitcnt lgkmcnt(2)\n\t" \
    "v_mfma_f32_16x16x32_bf16 v[204:207], v[24:27], v[240:243], v[204:207]\n\t" \
    "ds_read_b128 v[232:235], v253 offset:35840\n\t" \
    "s_waitcnt lgkmcnt(2)\n\t" \
    "v_mfma_f32_16x16x32_bf16 v[204:207], v[28:31], v[224:227], v[204:207]\n\t" \
    "ds_read_b128 v[236:239], v253 offset:38912\n\t" \
    "v_mfma_f32_16x16x32_bf16 v[208:211], v[32:35], v[240:243], v[208:211]\n\t" \
    "v_mfma_f32_16x16x32_bf16 v[208:211], v[36:39], v[224:227], v[208:211]\n\t" \
    "v_mfma_f32_16x16x32_bf16 v[200:203], v[16:19], v[240:243], v[200:203]\n\t" \
    "v_mfma_f32_16x16x32_bf16 v[200:203], v[20:23], v[224:227], v[200:203]\n\t" \
    "ds_read_b128 v[240:243], v253 offset:39936\n\t" \
    "s_waitcnt lgkmcnt(3)\n\t" \
    "v_mfma_f32_16x16x32_bf16 v[216:219], v[184:187], v[228:231], v[216:219]\n\t" \
    "s_waitcnt lgkmcnt(2)\n\t" \
    "v_mfma_f32_16x16x32_bf16 v[216:219], v[188:191], v[232:235], v[216:219]\n\t" \
    "v_cvt_pk_bf16_f32 v204, v204, v208\n\t" \
    "v_cvt_pk_bf16_f32 v205, v205, v209\n\t" \
    "s_waitcnt lgkmcnt(1)\n\t" \
    "v_mfma_f32_16x16x32_bf16 v[216:219], v[24:27], v[236:239], v[216:219]\n\t" \
    "v_cvt_pk_bf16_f32 v206, v206, v210\n\t" \
    "v_cvt_pk_bf16_f32 v207, v207, v211\n\t" \
    "s_waitcnt lgkmcnt(0)\n\t" \
    "v_mfma_f32_16x16x32_bf16 v[216:219], v[28:31], v[240:243], v[216:219]\n\t" \
    "ds_write_b128 v254, v[204:207] offset:5120\n\t" \
    "v_mfma_f32_16x16x32_bf16 v[220:223], v[192:195], v[228:231], v[220:223]\n\t" \
    "v_mfma_f32_16x16x32_bf16 v[220:223], v[196:199], v[232:235], v[220:223]\n\t" \
    "v_cvt_pk_bf16_f32 v200, v200, v201\n\t" \
    "v_cvt_pk_bf16_f32 v201, v202, v203\n\t" \
    "v_mfma_f32_16x16x32_bf16 v[220:223], v[32:35], v[236:239], v[220:223]\n\t" \
    "ds_write_b64 v254, v[200:201] offset:1032\n\t" \
    "v_mfma_f32_16x16x32_bf16 v[220:223], v[36:39], v[240:243], v[220:223]\n\t" \
    "v_mfma_f32_16x16x32_bf16 v[212:215], v[176:179], v[228:231], v[212:215]\n\t" \
    "ds_read_b128 v[200:203], v254 offset:55296\n\t" \
    "ds_read_b128 v[204:207], v254 offset:56320\n\t" \
    "v_mfma_f32_16x16x32_bf16 v[212:215], v[180:183], v[232:235], v[212:215]\n\t" \
    "v_mov_b64 v[208:209], 0\n\t" \
    "v_mov_b64 v[210:211], 0\n\t" \
    "v_mfma_f32_16x16x32_bf16 v[212:215], v[16:19], v[236:239], v[212:215]\n\t" \
    "v_cvt_pk_bf16_f32 v216, v216, v220\n\t" \
    "v_cvt_pk_bf16_f32 v217, v217, v221\n\t" \
    "v_mfma_f32_16x16x32_bf16 v[212:215], v[20:23], v[240:243], v[212:215]\n\t" \
    "v_cvt_pk_bf16_f32 v218, v218, v222\n\t" \
    "v_cvt_pk_bf16_f32 v219, v219, v223\n\t" \
    "s_setprio 0\n\t" \
    "ds_write_b128 v254, v[216:219] offset:11264\n\t" \
    "ds_read_b128 v[216:219], v254 offset:57344\n\t" \
    "ds_read_b128 v[220:223], v254 offset:58368\n\t" \
    "s_nop 2\n\t" \
    "v_cvt_pk_bf16_f32 v212, v212, v213\n\t" \
    "v_cvt_pk_bf16_f32 v213, v214, v215\n\t" \
    "ds_write_b64 v254, v[212:213] offset:7176\n\t" \
    "s_sleep 3\n\t" \
    "s_mov_b32 m0, 0xc000\n\t" \
    "s_nop 0\n\t" \
    "buffer_load_dwordx4 v253, s[60:63], s46 offen sc1 lds\n\t" \
    "s_mov_b32 m0, 0xc400\n\t" \
    "s_nop 0\n\t" \
    "buffer_load_dwordx4 v253, s[60:63], s47 offen sc1 lds\n\t" \
    "s_waitcnt lgkmcnt(0)\n\t" \
    "v_bfe_i32 v212, v253, 5, 1\n\t" \
    "v_and_b32 v212, 0xaaaa, v212\n\t" \
    "v_or_b32 v212, 0x4444, v212\n\t" \
    "v_and_b32 v213, 0xffff, v255\n\t" \
    "v_lshrrev_b32 v214, 16, v255\n\t" \
    ".Ls34_%=:\n\t" \
    "s_waitcnt vmcnt(0)\n\t" \
    "ds_read_b128 v[224:227], v253 offset:49152\n\t" \
    "ds_read_b128 v[228:231], v253 offset:50176\n\t" \
    "s_waitcnt lgkmcnt(1)\n\t" \
    "v_max_u32 v215, v224, v225\n\t" \
    "v_max3_u32 v215, v226, v227, v215\n\t" \
    "s_waitcnt lgkmcnt(0)\n\t" \
    "v_max3_u32 v215, v228, v229, v215\n\t" \
    "v_max3_u32 v215, v230, v231, v215\n\t" \
    "v_cmp_eq_u32 vcc, -1, v215\n\t" \
    "s_cbranch_vccz .Ls35_%=\n\t" \
    "v_max_u32 v215, v224, v225\n\t" \
    "v_max3_u32 v215, v226, v227, v215\n\t" \
    "v_cmp_eq_u32 vcc, -1, v215\n\t" \
    "s_cbranch_vccz .Ls36_%=\n\t" \
    "s_mov_b32 m0, 0xc000\n\t" \
    "s_nop 0\n\t" \
    "buffer_load_dwordx4 v253, s[60:63], s46 offen sc1 lds\n\t" \
    ".Ls36_%=:\n\t" \
    "v_max_u32 v215, v228, v229\n\t" \
    "v_max3_u32 v215, v230, v231, v215\n\t" \
    "v_cmp_eq_u32 vcc, -1, v215\n\t" \
    "s_cbranch_vccz .Ls37_%=\n\t" \
    "s_mov_b32 m0, 0xc400\n\t" \
    "s_nop 0\n\t" \
    "buffer_load_dwordx4 v253, s[60:63], s47 offen sc1 lds\n\t" \
    ".Ls37_%=:\n\t" \
    "s_cmp_eq_u32 s41, 0\n\t" \
    "s_cbranch_scc1 .Ls35_%=\n\t" \
    "s_sub_u32 s41, s41, 1\n\t" \
    "s_branch .Ls34_%=\n\t" \
    ".Ls35_%=:\n\t" \
    "s_setprio 3\n\t" \
    "s_waitcnt lgkmcnt(0)\n\t" \
    "s_barrier \n\t" \
    "ds_read_b128 v[232:235], v253 offset:40960\n\t" \
    "ds_read_b128 v[236:239], v253 offset:41984\n\t" \
    "ds_read_b128 v[240:243], v253 offset:43008\n\t" \
    "v_smfmac_f32_16x16x64_bf16 v[244:247], v[224:227], v[0:7], v212\n\t" \
    "v_smfmac_f32_16x16x64_bf16 v[248:251], v[224:227], v[8:15], v212\n\t" \
    "v_smfmac_f32_16x16x64_bf16 v[208:211], v[224:227], v[200:207], v212\n\t" \
    "ds_read_b128 v[200:203], v254 offset:59392\n\t" \
    "ds_read_b128 v[204:207], v254 offset:60416\n\t" \
    "ds_read_b128 v[224:227], v253 offset:44032\n\t" \
    "v_smfmac_f32_16x16x64_bf16 v[208:211], v[228:231], v[16:23], v212\n\t" \
    "v_smfmac_f32_16x16x64_bf16 v[244:247], v[228:231], v[24:31], v212\n\t" \
    "v_smfmac_f32_16x16x64_bf16 v[248:251], v[228:231], v[32:39], v212\n\t" \
    "ds_read_b128 v[228:231], v253 offset:45056\n\t" \
    "s_waitcnt lgkmcnt(6)\n\t" \
    "v_smfmac_f32_16x16x64_bf16 v[208:211], v[232:235], v[216:223], v212\n\t" \
    "ds_read_b128 v[216:219], v254 offset:61440\n\t" \
    "ds_read_b128 v[220:223], v254 offset:62464\n\t" \
    "v_smfmac_f32_16x16x64_bf16 v[244:247], v[232:235], v[40:47], v212\n\t" \
    "v_smfmac_f32_16x16x64_bf16 v[248:251], v[232:235], v[48:55], v212\n\t" \
    "ds_read_b128 v[232:235], v253 offset:46080\n\t" \
    "s_waitcnt lgkmcnt(8)\n\t" \
    "v_smfmac_f32_16x16x64_bf16 v[208:211], v[236:239], v[56:63], v212\n\t" \
    "v_smfmac_f32_16x16x64_bf16 v[244:247], v[236:239], v[64:71], v212\n\t" \
    "v_smfmac_f32_16x16x64_bf16 v[248:251], v[236:239], v[72:79], v212\n\t" \
    "ds_read_b128 v[236:239], v253 offset:47104\n\t" \
    "s_waitcnt lgkmcnt(6)\n\t" \
    "v_smfmac_f32_16x16x64_bf16 v[208:211], v[240:243], v[200:207], v212\n\t" \
    "ds_read_b128 v[200:203], v254 offset:63488\n\t" \
    "ds_read_b128 v[204:207], v254 offset:64512\n\t" \
    "v_smfmac_f32_16x16x64_bf16 v[244:247], v[240:243], v[80:87], v212\n\t" \
    "v_smfmac_f32_16x16x64_bf16 v[248:251], v[240:243], v[88:95], v212\n\t" \
    "ds_read_b128 v[240:243], v253 offset:48128\n\t" \
    "s_waitcnt lgkmcnt(8)\n\t" \
    "v_smfmac_f32_16x16x64_bf16 v[208:211], v[224:227], v[96:103], v212\n\t" \
    "v_smfmac_f32_16x16x64_bf16 v[244:247], v[224:227], v[104:111], v212\n\t" \
    "v_smfmac_f32_16x16x64_bf16 v[248:251], v[224:227], v[112:119], v212\n\t" \
    "s_waitcnt lgkmcnt(5)\n\t" \
    "v_smfmac_f32_16x16x64_bf16 v[208:211], v[228:231], v[216:223], v212\n\t" \
    "s_waitcnt lgkmcnt(4)\n\t" \
    "v_smfmac_f32_16x16x64_bf16 v[208:211], v[232:235], v[136:143], v212\n\t" \
    "s_waitcnt lgkmcnt(1)\n\t" \
    "v_smfmac_f32_16x16x64_bf16 v[208:211], v[236:239], v[200:207], v212\n\t" \
    "s_waitcnt lgkmcnt(0)\n\t" \
    "v_smfmac_f32_16x16x64_bf16 v[208:211], v[240:243], v[176:183], v212\n\t" \
    "v_smfmac_f32_16x16x64_bf16 v[244:247], v[228:231], v[120:127], v212\n\t" \
    "v_smfmac_f32_16x16x64_bf16 v[244:247], v[232:235], v[144:151], v212\n\t" \
    "v_smfmac_f32_16x16x64_bf16 v[244:247], v[236:239], v[160:167], v212\n\t" \
    "v_add_f32 v208, v208, v210\n\t" \
    "v_add_f32 v209, v209, v211\n\t" \
    "v_cvt_pk_bf16_f32 v208, v208, v209\n\t" \
    "v_smfmac_f32_16x16x64_bf16 v[244:247], v[240:243], v[184:191], v212\n\t" \
    "ds_write_b16 v213, v208 offset:0\n\t" \
    "ds_write_b16_d16_hi v213, v208 offset:16\n\t" \
    "v_smfmac_f32_16x16x64_bf16 v[248:251], v[228:231], v[128:135], v212\n\t" \
    "v_smfmac_f32_16x16x64_bf16 v[248:251], v[232:235], v[152:159], v212\n\t" \
    "v_smfmac_f32_16x16x64_bf16 v[248:251], v[236:239], v[168:175], v212\n\t" \
    "v_add_f32 v244, v244, v246\n\t" \
    "v_add_f32 v245, v245, v247\n\t" \
    "v_smfmac_f32_16x16x64_bf16 v[248:251], v[240:243], v[192:199], v212\n\t" \
    "s_nop 7\n\t" \
    "v_add_f32 v248, v248, v250\n\t" \
    "v_add_f32 v249, v249, v251\n\t" \
    "v_cvt_pk_bf16_f32 v244, v244, v248\n\t" \
    "v_cvt_pk_bf16_f32 v245, v245, v249\n\t" \
    "ds_write2_b32 v214, v244, v245 offset1:4\n\t" \
    "s_setprio 0\n\t" \
    "s_sleep 1\n\t" \
    "ds_read_b128 v[208:211], v254 offset:24576\n\t" \
    "ds_read_b128 v[224:227], v253 offset:0\n\t" \
    "ds_read_b128 v[228:231], v253 offset:20480\n\t" \
    "ds_read_b128 v[212:215], v254 offset:25600\n\t" \
    "ds_read_b128 v[232:235], v253 offset:1024\n\t" \
    "ds_read_b128 v[236:239], v253 offset:21504\n\t" \
    "ds_read_b128 v[216:219], v254 offset:26624\n\t" \
    "ds_read_b128 v[240:243], v253 offset:2048\n\t" \
    "ds_read_b128 v[244:247], v253 offset:22528\n\t" \
    "ds_read_b128 v[220:223], v254 offset:27648\n\t" \
    "ds_read_b128 v[248:251], v253 offset:3072\n\t" \
    "s_waitcnt lgkmcnt(9)\n\t" \
    "v_mfma_f32_16x16x32_bf16 v[200:203], v[208:211], v[224:227], 0\n\t" \
    "ds_read_b128 v[224:227], v253 offset:23552\n\t" \
    "s_waitcnt lgkmcnt(9)\n\t" \
    "v_mfma_f32_16x16x32_bf16 v[204:207], v[208:211], v[228:231], 0\n\t" \
    "ds_read_b128 v[228:231], v253 offset:4096\n\t" \
    "ds_read_b128 v[208:211], v254 offset:28672\n\t" \
    "s_waitcnt lgkmcnt(9)\n\t" \
    "v_mfma_f32_16x16x32_bf16 v[200:203], v[212:215], v[232:235], v[200:203]\n\t" \
    "ds_read_b128 v[232:235], v253 offset:24576\n\t" \
    "s_waitcnt lgkmcnt(9)\n\t" \
    "v_mfma_f32_16x16x32_bf16 v[204:207], v[212:215], v[236:239], v[204:207]\n\t" \
    "ds_read_b128 v[236:239], v253 offset:5120\n\t" \
    "ds_read_b128 v[212:215], v254 offset:29696\n\t" \
    "s_waitcnt lgkmcnt(0)\n\t" \
    "s_barrier \n\t" \
    "s_add_u32 s40, s40, 1\n\t" \
    "s_cmp_lg_u32 s40, 64\n\t" \
    "s_cbranch_scc1 .Ls33_%=\n\t" \
    "s_branch .Lend%=\n\t" \
    ".Ls2_%=:\n\t" \
    "v_add_u32 v244, 0x18000, v243\n\t" \
    "s_waitcnt vmcnt(12) lgkmcnt(0)\n\t" \
    "s_barrier \n\t" \
    "s_add_u32 m0, %[wm], 0x12000\n\t" \
    "s_mov_b32 s44, 0x1e0000\n\t" \
    "buffer_load_dwordx4 v240, %[wr], s44 offen offset:0 nt lds\n\t" \
    "buffer_load_dwordx4 v241, %[wr], s44 offen offset:1024 nt lds\n\t" \
    "buffer_load_dwordx4 v242, %[wr], s44 offen offset:2048 nt lds\n\t" \
    "ds_read_b64_tr_b16 v[208:209], v244 offset:0\n\t" \
    "ds_read_b64_tr_b16 v[210:211], v244 offset:1536\n\t" \
    "ds_read_b64_tr_b16 v[40:41], v244 offset:32\n\t" \
    "ds_read_b64_tr_b16 v[42:43], v244 offset:1568\n\t" \
    "ds_read_b64_tr_b16 v[48:49], v244 offset:64\n\t" \
    "ds_read_b64_tr_b16 v[50:51], v244 offset:1600\n\t" \
    "v_add_u32 v244, 0x1e000, v243\n\t" \
    "s_waitcnt vmcnt(12) lgkmcnt(0)\n\t" \
    "s_barrier \n\t" \
    "s_add_u32 m0, %[wm], 0x18000\n\t" \
    "s_mov_b32 s44, 0x240000\n\t" \
    "buffer_load_dwordx4 v240, %[wr], s44 offen offset:0 nt lds\n\t" \
    "buffer_load_dwordx4 v241, %[wr], s44 offen offset:1024 nt lds\n\t" \
    "buffer_load_dwordx4 v242, %[wr], s44 offen offset:2048 nt lds\n\t" \
    "ds_read_b64_tr_b16 v[212:213], v244 offset:0\n\t" \
    "ds_read_b64_tr_b16 v[214:215], v244 offset:1536\n\t" \
    "ds_read_b64_tr_b16 v[44:45], v244 offset:32\n\t" \
    "ds_read_b64_tr_b16 v[46:47], v244 offset:1568\n\t" \
    "ds_read_b64_tr_b16 v[52:53], v244 offset:64\n\t" \
    "ds_read_b64_tr_b16 v[54:55], v244 offset:1600\n\t" \
    "v_add_u32 v244, 0x0, v243\n\t" \
    "s_waitcnt vmcnt(12) lgkmcnt(0)\n\t" \
    "s_barrier \n\t" \
    "s_add_u32 m0, %[wm], 0x1e000\n\t" \
    "s_mov_b32 s44, 0x2a0000\n\t" \
    "buffer_load_dwordx4 v240, %[wr], s44 offen offset:0 nt lds\n\t" \
    "buffer_load_dwordx4 v241, %[wr], s44 offen offset:1024 nt lds\n\t" \
    "buffer_load_dwordx4 v242, %[wr], s44 offen offset:2048 nt lds\n\t" \
    "ds_read_b64_tr_b16 v[56:57], v244 offset:0\n\t" \
    "ds_read_b64_tr_b16 v[58:59], v244 offset:1536\n\t" \
    "ds_read_b64_tr_b16 v[64:65], v244 offset:32\n\t" \
    "ds_read_b64_tr_b16 v[66:67], v244 offset:1568\n\t" \
    "ds_read_b64_tr_b16 v[72:73], v244 offset:64\n\t" \
    "ds_read_b64_tr_b16 v[74:75], v244 offset:1600\n\t" \
    "v_add_u32 v244, 0x6000, v243\n\t" \
    "s_waitcnt vmcnt(12) lgkmcnt(0)\n\t" \
    "s_barrier \n\t" \
    "s_add_u32 m0, %[wm], 0x0\n\t" \
    "s_mov_b32 s44, 0x300000\n\t" \
    "buffer_load_dwordx4 v240, %[wr], s44 offen offset:0 nt lds\n\t" \
    "buffer_load_dwordx4 v241, %[wr], s44 offen offset:1024 nt lds\n\t" \
    "buffer_load_dwordx4 v242, %[wr], s44 offen offset:2048 nt lds\n\t" \
    "ds_read_b64_tr_b16 v[60:61], v244 offset:0\n\t" \
    "ds_read_b64_tr_b16 v[62:63], v244 offset:1536\n\t" \
    "ds_read_b64_tr_b16 v[68:69], v244 offset:32\n\t" \
    "ds_read_b64_tr_b16 v[70:71], v244 offset:1568\n\t" \
    "ds_read_b64_tr_b16 v[76:77], v244 offset:64\n\t" \
    "ds_read_b64_tr_b16 v[78:79], v244 offset:1600\n\t" \
    "v_add_u32 v244, 0xc000, v243\n\t" \
    "s_waitcnt vmcnt(12) lgkmcnt(0)\n\t" \
    "s_barrier \n\t" \
    "s_add_u32 m0, %[wm], 0x6000\n\t" \
    "s_mov_b32 s44, 0x360000\n\t" \
    "buffer_load_dwordx4 v240, %[wr], s44 offen offset:0 nt lds\n\t" \
    "buffer_load_dwordx4 v241, %[wr], s44 offen offset:1024 nt lds\n\t" \
    "buffer_load_dwordx4 v242, %[wr], s44 offen offset:2048 nt lds\n\t" \
    "ds_read_b64_tr_b16 v[216:217], v244 offset:0\n\t" \
    "ds_read_b64_tr_b16 v[218:219], v244 offset:1536\n\t" \
    "ds_read_b64_tr_b16 v[80:81], v244 offset:32\n\t" \
    "ds_read_b64_tr_b16 v[82:83], v244 offset:1568\n\t" \
    "ds_read_b64_tr_b16 v[88:89], v244 offset:64\n\t" \
    "ds_read_b64_tr_b16 v[90:91], v244 offset:1600\n\t" \
    "v_add_u32 v244, 0x12000, v243\n\t" \
    "s_waitcnt vmcnt(12) lgkmcnt(0)\n\t" \
    "s_barrier \n\t" \
    "s_add_u32 m0, %[wm], 0xc000\n\t" \
    "s_mov_b32 s44, 0x3c0000\n\t" \
    "buffer_load_dwordx4 v240, %[wr], s44 offen offset:0 nt lds\n\t" \
    "buffer_load_dwordx4 v241, %[wr], s44 offen offset:1024 nt lds\n\t" \
    "buffer_load_dwordx4 v242, %[wr], s44 offen offset:2048 nt lds\n\t" \
    "ds_read_b64_tr_b16 v[220:221], v244 offset:0\n\t" \
    "ds_read_b64_tr_b16 v[222:223], v244 offset:1536\n\t" \
    "ds_read_b64_tr_b16 v[84:85], v244 offset:32\n\t" \
    "ds_read_b64_tr_b16 v[86:87], v244 offset:1568\n\t" \
    "ds_read_b64_tr_b16 v[92:93], v244 offset:64\n\t" \
    "ds_read_b64_tr_b16 v[94:95], v244 offset:1600\n\t" \
    "v_add_u32 v244, 0x18000, v243\n\t" \
    "s_waitcnt vmcnt(12) lgkmcnt(0)\n\t" \
    "s_barrier \n\t" \
    "s_add_u32 m0, %[wm], 0x12000\n\t" \
    "s_mov_b32 s44, 0x420000\n\t" \
    "buffer_load_dwordx4 v240, %[wr], s44 offen offset:0 nt lds\n\t" \
    "buffer_load_dwordx4 v241, %[wr], s44 offen offset:1024 nt lds\n\t" \
    "buffer_load_dwordx4 v242, %[wr], s44 offen offset:2048 nt lds\n\t" \
    "ds_read_b64_tr_b16 v[96:97], v244 offset:0\n\t" \
    "ds_read_b64_tr_b16 v[98:99], v244 offset:1536\n\t" \
    "ds_read_b64_tr_b16 v[104:105], v244 offset:32\n\t" \
    "ds_read_b64_tr_b16 v[106:107], v244 offset:1568\n\t" \
    "ds_read_b64_tr_b16 v[112:113], v244 offset:64\n\t" \
    "ds_read_b64_tr_b16 v[114:115], v244 offset:1600\n\t" \
    "v_add_u32 v244, 0x1e000, v243\n\t" \
    "s_waitcnt vmcnt(12) lgkmcnt(0)\n\t" \
    "s_barrier \n\t" \
    "s_add_u32 m0, %[wm], 0x18000\n\t" \
    "s_mov_b32 s44, 0x480000\n\t" \
    "buffer_load_dwordx4 v240, %[wr], s44 offen offset:0 nt lds\n\t" \
    "buffer_load_dwordx4 v241, %[wr], s44 offen offset:1024 nt lds\n\t" \
    "buffer_load_dwordx4 v242, %[wr], s44 offen offset:2048 nt lds\n\t" \
    "ds_read_b64_tr_b16 v[100:101], v244 offset:0\n\t" \
    "ds_read_b64_tr_b16 v[102:103], v244 offset:1536\n\t" \
    "ds_read_b64_tr_b16 v[108:109], v244 offset:32\n\t" \
    "ds_read_b64_tr_b16 v[110:111], v244 offset:1568\n\t" \
    "ds_read_b64_tr_b16 v[116:117], v244 offset:64\n\t" \
    "ds_read_b64_tr_b16 v[118:119], v244 offset:1600\n\t" \
    "v_add_u32 v244, 0x0, v243\n\t" \
    "s_waitcnt vmcnt(12) lgkmcnt(0)\n\t" \
    "s_barrier \n\t" \
    "v_mov_b32 v246, 0\n\t" \
    "buffer_load_dword v246, v246, s[60:63], %[mo] offen sc1\n\t" \
    "s_add_u32 m0, %[wm], 0x1e000\n\t" \
    "s_mov_b32 s44, 0x4e0000\n\t" \
    "buffer_load_dwordx4 v240, %[wr], s44 offen offset:0 nt lds\n\t" \
    "buffer_load_dwordx4 v241, %[wr], s44 offen offset:1024 nt lds\n\t" \
    "buffer_load_dwordx4 v242, %[wr], s44 offen offset:2048 nt lds\n\t" \
    "ds_read_b64_tr_b16 v[200:201], v244 offset:0\n\t" \
    "ds_read_b64_tr_b16 v[202:203], v244 offset:1536\n\t" \
    "ds_read_b64_tr_b16 v[0:1], v244 offset:32\n\t" \
    "ds_read_b64_tr_b16 v[2:3], v244 offset:1568\n\t" \
    "ds_read_b64_tr_b16 v[8:9], v244 offset:64\n\t" \
    "ds_read_b64_tr_b16 v[10:11], v244 offset:1600\n\t" \
    "v_add_u32 v244, 0x6000, v243\n\t" \
    "s_waitcnt vmcnt(12) lgkmcnt(0)\n\t" \
    "s_barrier \n\t" \
    "s_add_u32 m0, %[wm], 0x0\n\t" \
    "s_mov_b32 s44, 0x540000\n\t" \
    "buffer_load_dwordx4 v240, %[wr], s44 offen offset:0 nt lds\n\t" \
    "buffer_load_dwordx4 v241, %[wr], s44 offen offset:1024 nt lds\n\t" \
    "buffer_load_dwordx4 v242, %[wr], s44 offen offset:2048 nt lds\n\t" \
    "ds_read_b64_tr_b16 v[204:205], v244 offset:0\n\t" \
    "ds_read_b64_tr_b16 v[206:207], v244 offset:1536\n\t" \
    "ds_read_b64_tr_b16 v[4:5], v244 offset:32\n\t" \
    "ds_read_b64_tr_b16 v[6:7], v244 offset:1568\n\t" \
    "ds_read_b64_tr_b16 v[12:13], v244 offset:64\n\t" \
    "ds_read_b64_tr_b16 v[14:15], v244 offset:1600\n\t" \
    "v_add_u32 v244, 0xc000, v243\n\t" \
    "s_waitcnt vmcnt(12) lgkmcnt(0)\n\t" \
    "s_barrier \n\t" \
    "s_add_u32 m0, %[wm], 0x6000\n\t" \
    "s_mov_b32 s44, 0x5a0000\n\t" \
    "buffer_load_dwordx4 v240, %[wr], s44 offen offset:0 nt lds\n\t" \
    "buffer_load_dwordx4 v241, %[wr], s44 offen offset:1024 nt lds\n\t" \
    "buffer_load_dwordx4 v242, %[wr], s44 offen offset:2048 nt lds\n\t" \
    "ds_read_b64_tr_b16 v[224:225], v244 offset:0\n\t" \
    "ds_read_b64_tr_b16 v[226:227], v244 offset:1536\n\t" \
    "ds_read_b64_tr_b16 v[120:121], v244 offset:32\n\t" \
    "ds_read_b64_tr_b16 v[122:123], v244 offset:1568\n\t" \
    "ds_read_b64_tr_b16 v[128:129], v244 offset:64\n\t" \
    "ds_read_b64_tr_b16 v[130:131], v244 offset:1600\n\t" \
    "v_add_u32 v244, 0x12000, v243\n\t" \
    "s_waitcnt vmcnt(12) lgkmcnt(0)\n\t" \
    "s_barrier \n\t" \
    "s_add_u32 m0, %[wm], 0xc000\n\t" \
    "s_mov_b32 s44, 0x600000\n\t" \
    "buffer_load_dwordx4 v240, %[wr], s44 offen offset:0 nt lds\n\t" \
    "buffer_load_dwordx4 v241, %[wr], s44 offen offset:1024 nt lds\n\t" \
    "buffer_load_dwordx4 v242, %[wr], s44 offen offset:2048 nt lds\n\t" \
    "ds_read_b64_tr_b16 v[228:229], v244 offset:0\n\t" \
    "ds_read_b64_tr_b16 v[230:231], v244 offset:1536\n\t" \
    "ds_read_b64_tr_b16 v[124:125], v244 offset:32\n\t" \
    "ds_read_b64_tr_b16 v[126:127], v244 offset:1568\n\t" \
    "ds_read_b64_tr_b16 v[132:133], v244 offset:64\n\t" \
    "ds_read_b64_tr_b16 v[134:135], v244 offset:1600\n\t" \
    "v_add_u32 v244, 0x18000, v243\n\t" \
    "s_waitcnt vmcnt(12) lgkmcnt(0)\n\t" \
    "s_barrier \n\t" \
    "s_add_u32 m0, %[wm], 0x12000\n\t" \
    "s_mov_b32 s44, 0x660000\n\t" \
    "buffer_load_dwordx4 v240, %[wr], s44 offen offset:0 nt lds\n\t" \
    "buffer_load_dwordx4 v241, %[wr], s44 offen offset:1024 nt lds\n\t" \
    "buffer_load_dwordx4 v242, %[wr], s44 offen offset:2048 nt lds\n\t" \
    "ds_read_b64_tr_b16 v[136:137], v244 offset:0\n\t" \
    "ds_read_b64_tr_b16 v[138:139], v244 offset:1536\n\t" \
    "ds_read_b64_tr_b16 v[144:145], v244 offset:32\n\t" \
    "ds_read_b64_tr_b16 v[146:147], v244 offset:1568\n\t" \
    "ds_read_b64_tr_b16 v[152:153], v244 offset:64\n\t" \
    "ds_read_b64_tr_b16 v[154:155], v244 offset:1600\n\t" \
    "v_add_u32 v244, 0x1e000, v243\n\t" \
    "s_waitcnt vmcnt(12) lgkmcnt(0)\n\t" \
    "s_barrier \n\t" \
    ".Ls21_%=:\n\t" \
    "v_readfirstlane_b32 s43, v246\n\t" \
    "s_nop 0\n\t" \
    "s_cmp_lg_u32 s43, -1\n\t" \
    "s_cbranch_scc1 .Ls22_%=\n\t" \
    "s_cmp_eq_u32 s41, 0\n\t" \
    "s_cbranch_scc1 .Ls22_%=\n\t" \
    "s_sub_u32 s41, s41, 1\n\t" \
    "s_sleep 4\n\t" \
    "v_mov_b32 v246, 0\n\t" \
    "buffer_load_dword v246, v246, s[60:63], %[mo] offen sc1\n\t" \
    "s_waitcnt vmcnt(0)\n\t" \
    "s_branch .Ls21_%=\n\t" \
    ".Ls22_%=:\n\t" \
    "v_mov_b32 v246, 0x8000\n\t" \
    "s_mov_b64 exec, 1\n\t" \
    "buffer_store_dword v246, v246, s[60:63], %[mo] offen sc1\n\t" \
    "s_mov_b64 exec, -1\n\t" \
    "s_and_b32 s44, s43, 0x5000\n\t" \
    "s_xor_b32 %[mb], %[mb], s44\n\t" \
    "s_xor_b32 %[fl], %[fl], s44\n\t" \
    "s_and_b32 s43, s43, 0x2000\n\t" \
    "s_xor_b32 s43, s43, %[lx]\n\t" \
    "s_add_u32 s60, s60, s43\n\t" \
    "s_addc_u32 s61, s61, 0\n\t" \
    "s_add_u32 m0, %[wm], 0x18000\n\t" \
    "s_mov_b32 s44, 0x6c0000\n\t" \
    "buffer_load_dwordx4 v240, %[wr], s44 offen offset:0 nt lds\n\t" \
    "buffer_load_dwordx4 v241, %[wr], s44 offen offset:1024 nt lds\n\t" \
    "buffer_load_dwordx4 v242, %[wr], s44 offen offset:2048 nt lds\n\t" \
    "ds_read_b64_tr_b16 v[140:141], v244 offset:0\n\t" \
    "ds_read_b64_tr_b16 v[142:143], v244 offset:1536\n\t" \
    "ds_read_b64_tr_b16 v[148:149], v244 offset:32\n\t" \
    "ds_read_b64_tr_b16 v[150:151], v244 offset:1568\n\t" \
    "ds_read_b64_tr_b16 v[156:157], v244 offset:64\n\t" \
    "ds_read_b64_tr_b16 v[158:159], v244 offset:1600\n\t" \
    "v_add_u32 v244, 0x0, v243\n\t" \
    "s_waitcnt vmcnt(12) lgkmcnt(0)\n\t" \
    "s_barrier \n\t" \
    "s_add_u32 m0, %[wm], 0x1e000\n\t" \
    "s_mov_b32 s44, 0x720000\n\t" \
    "buffer_load_dwordx4 v240, %[wr], s44 offen offset:0 nt lds\n\t" \
    "buffer_load_dwordx4 v241, %[wr], s44 offen offset:1024 nt lds\n\t" \
    "buffer_load_dwordx4 v242, %[wr], s44 offen offset:2048 nt lds\n\t" \
    "ds_read_b64_tr_b16 v[16:17], v244 offset:0\n\t" \
    "ds_read_b64_tr_b16 v[18:19], v244 offset:1536\n\t" \
    "ds_read_b64_tr_b16 v[24:25], v244 offset:32\n\t" \
    "ds_read_b64_tr_b16 v[26:27], v244 offset:1568\n\t" \
    "ds_read_b64_tr_b16 v[32:33], v244 offset:64\n\t" \
    "ds_read_b64_tr_b16 v[34:35], v244 offset:1600\n\t" \
    "v_add_u32 v244, 0x6000, v243\n\t" \
    "s_waitcnt vmcnt(12) lgkmcnt(0)\n\t" \
    "s_barrier \n\t" \
    "ds_read_b64_tr_b16 v[20:21], v244 offset:0\n\t" \
    "ds_read_b64_tr_b16 v[22:23], v244 offset:1536\n\t" \
    "ds_read_b64_tr_b16 v[28:29], v244 offset:32\n\t" \
    "ds_read_b64_tr_b16 v[30:31], v244 offset:1568\n\t" \
    "ds_read_b64_tr_b16 v[36:37], v244 offset:64\n\t" \
    "ds_read_b64_tr_b16 v[38:39], v244 offset:1600\n\t" \
    "v_add_u32 v244, 0xc000, v243\n\t" \
    "s_waitcnt vmcnt(9) lgkmcnt(0)\n\t" \
    "s_barrier \n\t" \
    "ds_read_b64_tr_b16 v[232:233], v244 offset:0\n\t" \
    "ds_read_b64_tr_b16 v[234:235], v244 offset:1536\n\t" \
    "ds_read_b64_tr_b16 v[160:161], v244 offset:32\n\t" \
    "ds_read_b64_tr_b16 v[162:163], v244 offset:1568\n\t" \
    "ds_read_b64_tr_b16 v[168:169], v244 offset:64\n\t" \
    "ds_read_b64_tr_b16 v[170:171], v244 offset:1600\n\t" \
    "v_add_u32 v244, 0x12000, v243\n\t" \
    "s_waitcnt vmcnt(6) lgkmcnt(0)\n\t" \
    "s_barrier \n\t" \
    "ds_read_b64_tr_b16 v[236:237], v244 offset:0\n\t" \
    "ds_read_b64_tr_b16 v[238:239], v244 offset:1536\n\t" \
    "ds_read_b64_tr_b16 v[164:165], v244 offset:32\n\t" \
    "ds_read_b64_tr_b16 v[166:167], v244 offset:1568\n\t" \
    "ds_read_b64_tr_b16 v[172:173], v244 offset:64\n\t" \
    "ds_read_b64_tr_b16 v[174:175], v244 offset:1600\n\t" \
    "v_add_u32 v244, 0x18000, v243\n\t" \
    "s_waitcnt vmcnt(3) lgkmcnt(0)\n\t" \
    "s_barrier \n\t" \
    "ds_read_b64_tr_b16 v[176:177], v244 offset:0\n\t" \
    "ds_read_b64_tr_b16 v[178:179], v244 offset:1536\n\t" \
    "ds_read_b64_tr_b16 v[184:185], v244 offset:32\n\t" \
    "ds_read_b64_tr_b16 v[186:187], v244 offset:1568\n\t" \
    "ds_read_b64_tr_b16 v[192:193], v244 offset:64\n\t" \
    "ds_read_b64_tr_b16 v[194:195], v244 offset:1600\n\t" \
    "v_add_u32 v244, 0x1e000, v243\n\t" \
    "s_waitcnt vmcnt(0) lgkmcnt(0)\n\t" \
    "s_barrier \n\t" \
    "ds_read_b64_tr_b16 v[180:181], v244 offset:0\n\t" \
    "ds_read_b64_tr_b16 v[182:183], v244 offset:1536\n\t" \
    "ds_read_b64_tr_b16 v[188:189], v244 offset:32\n\t" \
    "ds_read_b64_tr_b16 v[190:191], v244 offset:1568\n\t" \
    "ds_read_b64_tr_b16 v[196:197], v244 offset:64\n\t" \
    "ds_read_b64_tr_b16 v[198:199], v244 offset:1600\n\t" \
    "s_waitcnt lgkmcnt(0)\n\t" \
    "s_barrier \n\t" \
    "ds_write_b128 v254, v[200:203] offset:51200\n\t" \
    "ds_write_b128 v254, v[204:207] offset:52224\n\t" \
    "ds_write_b128 v254, v[208:211] offset:53248\n\t" \
    "ds_write_b128 v254, v[212:215] offset:54272\n\t" \
    "ds_write_b128 v254, v[216:219] offset:55296\n\t" \
    "ds_write_b128 v254, v[220:223] offset:56320\n\t" \
    "ds_write_b128 v254, v[224:227] offset:57344\n\t" \
    "ds_write_b128 v254, v[228:231] offset:58368\n\t" \
    "ds_write_b128 v254, v[232:235] offset:59392\n\t" \
    "ds_write_b128 v254, v[236:239] offset:60416\n\t" \
    "s_waitcnt vmcnt(0) lgkmcnt(0)\n\t" \
    "s_barrier \n\t" \
    "ds_read_b128 v[224:227], v253 offset:0\n\t" \
    "ds_read_b128 v[244:247], v254 offset:53248\n\t" \
    "ds_read_b128 v[228:231], v253 offset:1024\n\t" \
    "ds_read_b128 v[232:235], v253 offset:20480\n\t" \
    "ds_read_b128 v[236:239], v253 offset:21504\n\t" \
    "ds_read_b128 v[248:251], v254 offset:54272\n\t" \
    ".Ls23_%=:\n\t" \
    "s_waitcnt lgkmcnt(5)\n\t" \
    "v_mfma_f32_16x16x32_bf16 v[204:207], v[40:43], v[224:227], 0\n\t" \
    "s_waitcnt lgkmcnt(3)\n\t" \
    "v_mfma_f32_16x16x32_bf16 v[204:207], v[44:47], v[228:231], v[204:207]\n\t" \
    "ds_read_b128 v[240:243], v253 offset:2048\n\t" \
    "v_mfma_f32_16x16x32_bf16 v[208:211], v[48:51], v[224:227], 0\n\t" \
    "v_mfma_f32_16x16x32_bf16 v[208:211], v[52:55], v[228:231], v[208:211]\n\t" \
    "v_mfma_f32_16x16x32_bf16 v[200:203], v[244:247], v[224:227], 0\n\t" \
    "s_waitcnt lgkmcnt(1)\n\t" \
    "v_mfma_f32_16x16x32_bf16 v[200:203], v[248:251], v[228:231], v[200:203]\n\t" \
    "v_mfma_f32_16x16x32_bf16 v[216:219], v[40:43], v[232:235], 0\n\t" \
    "ds_read_b128 v[224:227], v253 offset:3072\n\t" \
    "v_mfma_f32_16x16x32_bf16 v[216:219], v[44:47], v[236:239], v[216:219]\n\t" \
    "ds_read_b128 v[228:231], v253 offset:22528\n\t" \
    "v_mfma_f32_16x16x32_bf16 v[220:223], v[48:51], v[232:235], 0\n\t" \
    "v_mfma_f32_16x16x32_bf16 v[220:223], v[52:55], v[236:239], v[220:223]\n\t" \
    "v_mfma_f32_16x16x32_bf16 v[212:215], v[244:247], v[232:235], 0\n\t" \
    "ds_read_b128 v[244:247], v254 offset:55296\n\t" \
    "v_mfma_f32_16x16x32_bf16 v[212:215], v[248:251], v[236:239], v[212:215]\n\t" \
    "ds_read_b128 v[248:251], v254 offset:56320\n\t" \
    "s_waitcnt lgkmcnt(4)\n\t" \
    "v_mfma_f32_16x16x32_bf16 v[204:207], v[64:67], v[240:243], v[204:207]\n\t" \
    "ds_read_b128 v[232:235], v253 offset:23552\n\t" \
    "s_waitcnt lgkmcnt(4)\n\t" \
    "v_mfma_f32_16x16x32_bf16 v[204:207], v[68:71], v[224:227], v[204:207]\n\t" \
    "ds_read_b128 v[236:239], v253 offset:4096\n\t" \
    "v_mfma_f32_16x16x32_bf16 v[208:211], v[72:75], v[240:243], v[208:211]\n\t" \
    "v_mfma_f32_16x16x32_bf16 v[208:211], v[76:79], v[224:227], v[208:211]\n\t" \
    "v_mfma_f32_16x16x32_bf16 v[200:203], v[56:59], v[240:243], v[200:203]\n\t" \
    "v_mfma_f32_16x16x32_bf16 v[200:203], v[60:63], v[224:227], v[200:203]\n\t" \
    "s_waitcnt lgkmcnt(4)\n\t" \
    "v_mfma_f32_16x16x32_bf16 v[216:219], v[64:67], v[228:231], v[216:219]\n\t" \
    "ds_read_b128 v[240:243], v253 offset:5120\n\t" \
    "s_waitcnt lgkmcnt(2)\n\t" \
    "v_mfma_f32_16x16x32_bf16 v[216:219], v[68:71], v[232:235], v[216:219]\n\t" \
    "ds_read_b128 v[224:227], v253 offset:24576\n\t" \
    "v_mfma_f32_16x16x32_bf16 v[220:223], v[72:75], v[228:231], v[220:223]\n\t" \
    "v_mfma_f32_16x16x32_bf16 v[220:223], v[76:79], v[232:235], v[220:223]\n\t" \
    "v_mfma_f32_16x16x32_bf16 v[212:215], v[56:59], v[228:231], v[212:215]\n\t" \
    "v_mfma_f32_16x16x32_bf16 v[212:215], v[60:63], v[232:235], v[212:215]\n\t" \
    "s_waitcnt lgkmcnt(2)\n\t" \
    "v_mfma_f32_16x16x32_bf16 v[204:207], v[80:83], v[236:239], v[204:207]\n\t" \
    "ds_read_b128 v[228:231], v253 offset:25600\n\t" \
    "s_waitcnt lgkmcnt(2)\n\t" \
    "v_mfma_f32_16x16x32_bf16 v[204:207], v[84:87], v[240:243], v[204:207]\n\t" \
    "ds_read_b128 v[232:235], v253 offset:6144\n\t" \
    "v_mfma_f32_16x16x32_bf16 v[208:211], v[88:91], v[236:239], v[208:211]\n\t" \
    "v_mfma_f32_16x16x32_bf16 v[208:211], v[92:95], v[240:243], v[208:211]\n\t" \
    "v_mfma_f32_16x16x32_bf16 v[200:203], v[244:247], v[236:239], v[200:203]\n\t" \
    "v_mfma_f32_16x16x32_bf16 v[200:203], v[248:251], v[240:243], v[200:203]\n\t" \
    "s_waitcnt lgkmcnt(2)\n\t" \
    "v_mfma_f32_16x16x32_bf16 v[216:219], v[80:83], v[224:227], v[216:219]\n\t" \
    "ds_read_b128 v[236:239], v253 offset:7168\n\t" \
    "s_waitcnt lgkmcnt(2)\n\t" \
    "v_mfma_f32_16x16x32_bf16 v[216:219], v[84:87], v[228:231], v[216:219]\n\t" \
    "ds_read_b128 v[240:243], v253 offset:26624\n\t" \
    "v_mfma_f32_16x16x32_bf16 v[220:223], v[88:91], v[224:227], v[220:223]\n\t" \
    "v_mfma_f32_16x16x32_bf16 v[220:223], v[92:95], v[228:231], v[220:223]\n\t" \
    "v_mfma_f32_16x16x32_bf16 v[212:215], v[244:247], v[224:227], v[212:215]\n\t" \
    "ds_read_b128 v[244:247], v254 offset:51200\n\t" \
    "v_mfma_f32_16x16x32_bf16 v[212:215], v[248:251], v[228:231], v[212:215]\n\t" \
    "ds_read_b128 v[248:251], v254 offset:52224\n\t" \
    "s_waitcnt lgkmcnt(4)\n\t" \
    "v_mfma_f32_16x16x32_bf16 v[204:207], v[104:107], v[232:235], v[204:207]\n\t" \
    "ds_read_b128 v[224:227], v253 offset:27648\n\t" \
    "s_waitcnt lgkmcnt(4)\n\t" \
    "v_mfma_f32_16x16x32_bf16 v[204:207], v[108:111], v[236:239], v[204:207]\n\t" \
    "ds_read_b128 v[228:231], v253 offset:8192\n\t" \
    "v_mfma_f32_16x16x32_bf16 v[208:211], v[112:115], v[232:235], v[208:211]\n\t" \
    "v_mfma_f32_16x16x32_bf16 v[208:211], v[116:119], v[236:239], v[208:211]\n\t" \
    "v_mfma_f32_16x16x32_bf16 v[200:203], v[96:99], v[232:235], v[200:203]\n\t" \
    "v_mfma_f32_16x16x32_bf16 v[200:203], v[100:103], v[236:239], v[200:203]\n\t" \
    "s_waitcnt lgkmcnt(4)\n\t" \
    "v_mfma_f32_16x16x32_bf16 v[216:219], v[104:107], v[240:243], v[216:219]\n\t" \
    "ds_read_b128 v[232:235], v253 offset:9216\n\t" \
    "s_waitcnt lgkmcnt(2)\n\t" \
    "v_mfma_f32_16x16x32_bf16 v[216:219], v[108:111], v[224:227], v[216:219]\n\t" \
    "ds_read_b128 v[236:239], v253 offset:28672\n\t" \
    "v_mfma_f32_16x16x32_bf16 v[220:223], v[112:115], v[240:243], v[220:223]\n\t" \
    "v_mfma_f32_16x16x32_bf16 v[220:223], v[116:119], v[224:227], v[220:223]\n\t" \
    "v_mfma_f32_16x16x32_bf16 v[212:215], v[96:99], v[240:243], v[212:215]\n\t" \
    "v_mfma_f32_16x16x32_bf16 v[212:215], v[100:103], v[224:227], v[212:215]\n\t" \
    "s_waitcnt lgkmcnt(2)\n\t" \
    "v_mfma_f32_16x16x32_bf16 v[204:207], v[0:3], v[228:231], v[204:207]\n\t" \
    "ds_read_b128 v[240:243], v253 offset:29696\n\t" \
    "s_waitcnt lgkmcnt(2)\n\t" \
    "v_mfma_f32_16x16x32_bf16 v[204:207], v[4:7], v[232:235], v[204:207]\n\t" \
    "ds_read_b128 v[224:227], v253 offset:10240\n\t" \
    "v_mfma_f32_16x16x32_bf16 v[208:211], v[8:11], v[228:231], v[208:211]\n\t" \
    "v_mfma_f32_16x16x32_bf16 v[208:211], v[12:15], v[232:235], v[208:211]\n\t" \
    "v_mfma_f32_16x16x32_bf16 v[200:203], v[244:247], v[228:231], v[200:203]\n\t" \
    "v_mfma_f32_16x16x32_bf16 v[200:203], v[248:251], v[232:235], v[200:203]\n\t" \
    "s_waitcnt lgkmcnt(2)\n\t" \
    "v_mfma_f32_16x16x32_bf16 v[216:219], v[0:3], v[236:239], v[216:219]\n\t" \
    "ds_read_b128 v[228:231], v253 offset:11264\n\t" \
    "s_waitcnt lgkmcnt(2)\n\t" \
    "v_mfma_f32_16x16x32_bf16 v[216:219], v[4:7], v[240:243], v[216:219]\n\t" \
    "ds_read_b128 v[232:235], v253 offset:30720\n\t" \
    "v_mfma_f32_16x16x32_bf16 v[220:223], v[8:11], v[236:239], v[220:223]\n\t" \
    "v_mfma_f32_16x16x32_bf16 v[220:223], v[12:15], v[240:243], v[220:223]\n\t" \
    "v_mfma_f32_16x16x32_bf16 v[212:215], v[244:247], v[236:239], v[212:215]\n\t" \
    "ds_read_b128 v[244:247], v254 offset:57344\n\t" \
    "v_mfma_f32_16x16x32_bf16 v[212:215], v[248:251], v[240:243], v[212:215]\n\t" \
    "ds_read_b128 v[248:251], v254 offset:58368\n\t" \
    "ds_read_b128 v[236:239], v253 offset:31744\n\t" \
    "s_setprio 2\n\t" \
    "s_waitcnt lgkmcnt(0)\n\t" \
    "s_barrier \n\t" \
    "v_mfma_f32_16x16x32_bf16 v[204:207], v[120:123], v[224:227], v[204:207]\n\t" \
    "v_mfma_f32_16x16x32_bf16 v[204:207], v[124:127], v[228:231], v[204:207]\n\t" \
    "ds_read_b128 v[240:243], v253 offset:12288\n\t" \
    "v_mfma_f32_16x16x32_bf16 v[208:211], v[128:131], v[224:227], v[208:211]\n\t" \
    "v_mfma_f32_16x16x32_bf16 v[208:211], v[132:135], v[228:231], v[208:211]\n\t" \
    "v_mfma_f32_16x16x32_bf16 v[200:203], v[244:247], v[224:227], v[200:203]\n\t" \
    "v_mfma_f32_16x16x32_bf16 v[200:203], v[248:251], v[228:231], v[200:203]\n\t" \
    "v_mfma_f32_16x16x32_bf16 v[216:219], v[120:123], v[232:235], v[216:219]\n\t" \
    "ds_read_b128 v[224:227], v253 offset:13312\n\t" \
    "s_and_b32 s44, s40, 3\n\t" \
    "v_mfma_f32_16x16x32_bf16 v[216:219], v[124:127], v[236:239], v[216:219]\n\t" \
    "ds_read_b128 v[228:231], v253 offset:32768\n\t" \
    "s_lshl_b32 s44, s44, 22\n\t" \
    "v_mfma_f32_16x16x32_bf16 v[220:223], v[128:131], v[232:235], v[220:223]\n\t" \
    "s_add_u32 s44, s44, %[mb]\n\t" \
    "v_mfma_f32_16x16x32_bf16 v[220:223], v[132:135], v[236:239], v[220:223]\n\t" \
    "s_add_u32 s46, s44, 0x8000\n\t" \
    "v_mfma_f32_16x16x32_bf16 v[212:215], v[244:247], v[232:235], v[212:215]\n\t" \
    "ds_read_b128 v[244:247], v254 offset:59392\n\t" \
    "s_add_u32 s47, s44, 0x8c00\n\t" \
    "v_mfma_f32_16x16x32_bf16 v[212:215], v[248:251], v[236:239], v[212:215]\n\t" \
    "ds_read_b128 v[248:251], v254 offset:60416\n\t" \
    "s_waitcnt lgkmcnt(4)\n\t" \
    "v_mfma_f32_16x16x32_bf16 v[204:207], v[144:147], v[240:243], v[204:207]\n\t" \
    "ds_read_b128 v[232:235], v253 offset:33792\n\t" \
    "s_waitcnt lgkmcnt(4)\n\t" \
    "v_mfma_f32_16x16x32_bf16 v[204:207], v[148:151], v[224:227], v[204:207]\n\t" \
    "ds_read_b128 v[236:239], v253 offset:16384\n\t" \
    "v_mfma_f32_16x16x32_bf16 v[208:211], v[152:155], v[240:243], v[208:211]\n\t" \
    "v_mfma_f32_16x16x32_bf16 v[208:211], v[156:159], v[224:227], v[208:211]\n\t" \
    "v_mfma_f32_16x16x32_bf16 v[200:203], v[136:139], v[240:243], v[200:203]\n\t" \
    "v_mfma_f32_16x16x32_bf16 v[200:203], v[140:143], v[224:227], v[200:203]\n\t" \
    "s_waitcnt lgkmcnt(4)\n\t" \
    "v_mfma_f32_16x16x32_bf16 v[216:219], v[144:147], v[228:231], v[216:219]\n\t" \
    "ds_read_b128 v[240:243], v253 offset:17408\n\t" \
    "s_waitcnt lgkmcnt(2)\n\t" \
    "v_mfma_f32_16x16x32_bf16 v[216:219], v[148:151], v[232:235], v[216:219]\n\t" \
    "ds_read_b128 v[224:227], v253 offset:36864\n\t" \
    "v_mfma_f32_16x16x32_bf16 v[220:223], v[152:155], v[228:231], v[220:223]\n\t" \
    "v_mfma_f32_16x16x32_bf16 v[220:223], v[156:159], v[232:235], v[220:223]\n\t" \
    "v_mfma_f32_16x16x32_bf16 v[212:215], v[136:139], v[228:231], v[212:215]\n\t" \
    "v_mfma_f32_16x16x32_bf16 v[212:215], v[140:143], v[232:235], v[212:215]\n\t" \
    "s_waitcnt lgkmcnt(2)\n\t" \
    "v_mfma_f32_16x16x32_bf16 v[204:207], v[160:163], v[236:239], v[204:207]\n\t" \
    "ds_read_b128 v[228:231], v253 offset:37888\n\t" \
    "s_waitcnt lgkmcnt(2)\n\t" \
    "v_mfma_f32_16x16x32_bf16 v[204:207], v[164:167], v[240:243], v[204:207]\n\t" \
    "ds_read_b128 v[232:235], v253 offset:14336\n\t" \
    "v_mfma_f32_16x16x32_bf16 v[208:211], v[168:171], v[236:239], v[208:211]\n\t" \
    "v_mfma_f32_16x16x32_bf16 v[208:211], v[172:175], v[240:243], v[208:211]\n\t" \
    "v_mfma_f32_16x16x32_bf16 v[200:203], v[244:247], v[236:239], v[200:203]\n\t" \
    "v_mfma_f32_16x16x32_bf16 v[200:203], v[248:251], v[240:243], v[200:203]\n\t" \
    "s_waitcnt lgkmcnt(2)\n\t" \
    "v_mfma_f32_16x16x32_bf16 v[216:219], v[160:163], v[224:227], v[216:219]\n\t" \
    "ds_read_b128 v[236:239], v253 offset:15360\n\t" \
    "s_waitcnt lgkmcnt(2)\n\t" \
    "v_mfma_f32_16x16x32_bf16 v[216:219], v[164:167], v[228:231], v[216:219]\n\t" \
    "ds_read_b128 v[240:243], v253 offset:18432\n\t" \
    "v_mfma_f32_16x16x32_bf16 v[220:223], v[168:171], v[224:227], v[220:223]\n\t" \
    "v_mfma_f32_16x16x32_bf16 v[220:223], v[172:175], v[228:231], v[220:223]\n\t" \
    "v_mfma_f32_16x16x32_bf16 v[212:215], v[244:247], v[224:227], v[212:215]\n\t" \
    "v_mfma_f32_16x16x32_bf16 v[212:215], v[248:251], v[228:231], v[212:215]\n\t" \
    "s_waitcnt lgkmcnt(2)\n\t" \
    "v_mfma_f32_16x16x32_bf16 v[204:207], v[24:27], v[232:235], v[204:207]\n\t" \
    "ds_read_b128 v[224:227], v253 offset:19456\n\t" \
    "v_mov_b64 v[244:245], 0\n\t" \
    "s_waitcnt lgkmcnt(2)\n\t" \
    "v_mfma_f32_16x16x32_bf16 v[204:207], v[28:31], v[236:239], v[204:207]\n\t" \
    "ds_read_b128 v[228:231], v253 offset:34816\n\t" \
    "v_mov_b64 v[246:247], 0\n\t" \
    "v_mfma_f32_16x16x32_bf16 v[208:211], v[32:35], v[232:235], v[208:211]\n\t" \
    "v_mov_b64 v[248:249], 0\n\t" \
    "v_mfma_f32_16x16x32_bf16 v[208:211], v[36:39], v[236:239], v[208:211]\n\t" \
    "v_mov_b64 v[250:251], 0\n\t" \
    "v_mfma_f32_16x16x32_bf16 v[200:203], v[16:19], v[232:235], v[200:203]\n\t" \
    "v_mfma_f32_16x16x32_bf16 v[200:203], v[20:23], v[236:239], v[200:203]\n\t" \
    "s_waitcnt lgkmcnt(2)\n\t" \
    "v_mfma_f32_16x16x32_bf16 v[204:207], v[184:187], v[240:243], v[204:207]\n\t" \
    "ds_read_b128 v[232:235], v253 offset:35840\n\t" \
    "s_waitcnt lgkmcnt(2)\n\t" \
    "v_mfma_f32_16x16x32_bf16 v[204:207], v[188:191], v[224:227], v[204:207]\n\t" \
    "ds_read_b128 v[236:239], v253 offset:38912\n\t" \
    "v_mfma_f32_16x16x32_bf16 v[208:211], v[192:195], v[240:243], v[208:211]\n\t" \
    "v_mfma_f32_16x16x32_bf16 v[208:211], v[196:199], v[224:227], v[208:211]\n\t" \
    "v_mfma_f32_16x16x32_bf16 v[200:203], v[176:179], v[240:243], v[200:203]\n\t" \
    "v_mfma_f32_16x16x32_bf16 v[200:203], v[180:183], v[224:227], v[200:203]\n\t" \
    "ds_read_b128 v[240:243], v253 offset:39936\n\t" \
    "s_waitcnt lgkmcnt(3)\n\t" \
    "v_mfma_f32_16x16x32_bf16 v[216:219], v[24:27], v[228:231], v[216:219]\n\t" \
    "s_waitcnt lgkmcnt(2)\n\t" \
    "v_mfma_f32_16x16x32_bf16 v[216:219], v[28:31], v[232:235], v[216:219]\n\t" \
    "v_cvt_pk_bf16_f32 v204, v204, v208\n\t" \
    "v_cvt_pk_bf16_f32 v205, v205, v209\n\t" \
    "s_waitcnt lgkmcnt(1)\n\t" \
    "v_mfma_f32_16x16x32_bf16 v[216:219], v[184:187], v[236:239], v[216:219]\n\t" \
    "v_cvt_pk_bf16_f32 v206, v206, v210\n\t" \
    "v_cvt_pk_bf16_f32 v207, v207, v211\n\t" \
    "s_waitcnt lgkmcnt(0)\n\t" \
    "v_mfma_f32_16x16x32_bf16 v[216:219], v[188:191], v[240:243], v[216:219]\n\t" \
    "ds_write_b128 v254, v[204:207] offset:10240\n\t" \
    "v_mfma_f32_16x16x32_bf16 v[220:223], v[32:35], v[228:231], v[220:223]\n\t" \
    "v_mfma_f32_16x16x32_bf16 v[220:223], v[36:39], v[232:235], v[220:223]\n\t" \
    "v_cvt_pk_bf16_f32 v200, v200, v201\n\t" \
    "v_cvt_pk_bf16_f32 v201, v202, v203\n\t" \
    "v_mfma_f32_16x16x32_bf16 v[220:223], v[192:195], v[236:239], v[220:223]\n\t" \
    "ds_write_b64 v254, v[200:201] offset:6152\n\t" \
    "v_mfma_f32_16x16x32_bf16 v[220:223], v[196:199], v[240:243], v[220:223]\n\t" \
    "v_mfma_f32_16x16x32_bf16 v[212:215], v[16:19], v[228:231], v[212:215]\n\t" \
    "ds_read_b128 v[200:203], v254 offset:51200\n\t" \
    "ds_read_b128 v[204:207], v254 offset:52224\n\t" \
    "v_mfma_f32_16x16x32_bf16 v[212:215], v[20:23], v[232:235], v[212:215]\n\t" \
    "v_mov_b64 v[208:209], 0\n\t" \
    "v_mov_b64 v[210:211], 0\n\t" \
    "v_mfma_f32_16x16x32_bf16 v[212:215], v[176:179], v[236:239], v[212:215]\n\t" \
    "v_cvt_pk_bf16_f32 v216, v216, v220\n\t" \
    "v_cvt_pk_bf16_f32 v217, v217, v221\n\t" \
    "v_mfma_f32_16x16x32_bf16 v[212:215], v[180:183], v[240:243], v[212:215]\n\t" \
    "v_cvt_pk_bf16_f32 v218, v218, v222\n\t" \
    "v_cvt_pk_bf16_f32 v219, v219, v223\n\t" \
    "s_setprio 0\n\t" \
    "ds_write_b128 v254, v[216:219] offset:16384\n\t" \
    "ds_read_b128 v[216:219], v254 offset:53248\n\t" \
    "ds_read_b128 v[220:223], v254 offset:54272\n\t" \
    "s_nop 2\n\t" \
    "v_cvt_pk_bf16_f32 v212, v212, v213\n\t" \
    "v_cvt_pk_bf16_f32 v213, v214, v215\n\t" \
    "ds_write_b64 v254, v[212:213] offset:12296\n\t" \
    "s_sleep 3\n\t" \
    "s_mov_b32 m0, 0xb000\n\t" \
    "s_nop 0\n\t" \
    "buffer_load_dwordx4 v253, s[60:63], s46 offen sc1 lds\n\t" \
    "s_mov_b32 m0, 0xbc00\n\t" \
    "s_nop 0\n\t" \
    "buffer_load_dwordx4 v253, s[60:63], s47 offen sc1 lds\n\t" \
    "s_waitcnt lgkmcnt(0)\n\t" \
    "v_bfe_i32 v212, v253, 5, 1\n\t" \
    "v_and_b32 v212, 0xaaaa, v212\n\t" \
    "v_or_b32 v212, 0x4444, v212\n\t" \
    "v_and_b32 v213, 0xffff, v255\n\t" \
    "v_lshrrev_b32 v214, 16, v255\n\t" \
    ".Ls24_%=:\n\t" \
    "s_waitcnt vmcnt(0)\n\t" \
    "ds_read_b128 v[224:227], v253 offset:45056\n\t" \
    "ds_read_b128 v[228:231], v253 offset:48128\n\t" \
    "s_waitcnt lgkmcnt(1)\n\t" \
    "v_max_u32 v215, v224, v225\n\t" \
    "v_max3_u32 v215, v226, v227, v215\n\t" \
    "s_waitcnt lgkmcnt(0)\n\t" \
    "v_max3_u32 v215, v228, v229, v215\n\t" \
    "v_max3_u32 v215, v230, v231, v215\n\t" \
    "v_cmp_eq_u32 vcc, -1, v215\n\t" \
    "s_cbranch_vccz .Ls25_%=\n\t" \
    "v_max_u32 v215, v224, v225\n\t" \
    "v_max3_u32 v215, v226, v227, v215\n\t" \
    "v_cmp_eq_u32 vcc, -1, v215\n\t" \
    "s_cbranch_vccz .Ls26_%=\n\t" \
    "s_mov_b32 m0, 0xb000\n\t" \
    "s_nop 0\n\t" \
    "buffer_load_dwordx4 v253, s[60:63], s46 offen sc1 lds\n\t" \
    ".Ls26_%=:\n\t" \
    "v_max_u32 v215, v228, v229\n\t" \
    "v_max3_u32 v215, v230, v231, v215\n\t" \
    "v_cmp_eq_u32 vcc, -1, v215\n\t" \
    "s_cbranch_vccz .Ls27_%=\n\t" \
    "s_mov_b32 m0, 0xbc00\n\t" \
    "s_nop 0\n\t" \
    "buffer_load_dwordx4 v253, s[60:63], s47 offen sc1 lds\n\t" \
    ".Ls27_%=:\n\t" \
    "s_cmp_eq_u32 s41, 0\n\t" \
    "s_cbranch_scc1 .Ls25_%=\n\t" \
    "s_sub_u32 s41, s41, 1\n\t" \
    "s_branch .Ls24_%=\n\t" \
    ".Ls25_%=:\n\t" \
    "s_setprio 3\n\t" \
    "s_waitcnt lgkmcnt(0)\n\t" \
    "s_barrier \n\t" \
    "ds_read_b128 v[232:235], v253 offset:40960\n\t" \
    "ds_read_b128 v[236:239], v253 offset:41984\n\t" \
    "ds_read_b128 v[240:243], v253 offset:43008\n\t" \
    "v_smfmac_f32_16x16x64_bf16 v[244:247], v[224:227], v[0:7], v212\n\t" \
    "v_smfmac_f32_16x16x64_bf16 v[248:251], v[224:227], v[8:15], v212\n\t" \
    "v_smfmac_f32_16x16x64_bf16 v[208:211], v[224:227], v[200:207], v212\n\t" \
    "ds_read_b128 v[200:203], v254 offset:55296\n\t" \
    "ds_read_b128 v[204:207], v254 offset:56320\n\t" \
    "ds_read_b128 v[224:227], v253 offset:44032\n\t" \
    "v_smfmac_f32_16x16x64_bf16 v[208:211], v[228:231], v[16:23], v212\n\t" \
    "v_smfmac_f32_16x16x64_bf16 v[244:247], v[228:231], v[24:31], v212\n\t" \
    "v_smfmac_f32_16x16x64_bf16 v[248:251], v[228:231], v[32:39], v212\n\t" \
    "ds_read_b128 v[228:231], v253 offset:46080\n\t" \
    "s_waitcnt lgkmcnt(6)\n\t" \
    "v_smfmac_f32_16x16x64_bf16 v[208:211], v[232:235], v[216:223], v212\n\t" \
    "ds_read_b128 v[216:219], v254 offset:57344\n\t" \
    "ds_read_b128 v[220:223], v254 offset:58368\n\t" \
    "v_smfmac_f32_16x16x64_bf16 v[244:247], v[232:235], v[40:47], v212\n\t" \
    "v_smfmac_f32_16x16x64_bf16 v[248:251], v[232:235], v[48:55], v212\n\t" \
    "ds_read_b128 v[232:235], v253 offset:47104\n\t" \
    "s_waitcnt lgkmcnt(8)\n\t" \
    "v_smfmac_f32_16x16x64_bf16 v[208:211], v[236:239], v[56:63], v212\n\t" \
    "v_smfmac_f32_16x16x64_bf16 v[244:247], v[236:239], v[64:71], v212\n\t" \
    "v_smfmac_f32_16x16x64_bf16 v[248:251], v[236:239], v[72:79], v212\n\t" \
    "ds_read_b128 v[236:239], v253 offset:49152\n\t" \
    "s_waitcnt lgkmcnt(6)\n\t" \
    "v_smfmac_f32_16x16x64_bf16 v[208:211], v[240:243], v[200:207], v212\n\t" \
    "ds_read_b128 v[200:203], v254 offset:59392\n\t" \
    "ds_read_b128 v[204:207], v254 offset:60416\n\t" \
    "v_smfmac_f32_16x16x64_bf16 v[244:247], v[240:243], v[80:87], v212\n\t" \
    "v_smfmac_f32_16x16x64_bf16 v[248:251], v[240:243], v[88:95], v212\n\t" \
    "ds_read_b128 v[240:243], v253 offset:50176\n\t" \
    "s_waitcnt lgkmcnt(8)\n\t" \
    "v_smfmac_f32_16x16x64_bf16 v[208:211], v[224:227], v[96:103], v212\n\t" \
    "v_smfmac_f32_16x16x64_bf16 v[244:247], v[224:227], v[104:111], v212\n\t" \
    "v_smfmac_f32_16x16x64_bf16 v[248:251], v[224:227], v[112:119], v212\n\t" \
    "s_waitcnt lgkmcnt(5)\n\t" \
    "v_smfmac_f32_16x16x64_bf16 v[208:211], v[228:231], v[216:223], v212\n\t" \
    "s_waitcnt lgkmcnt(4)\n\t" \
    "v_smfmac_f32_16x16x64_bf16 v[208:211], v[232:235], v[136:143], v212\n\t" \
    "s_waitcnt lgkmcnt(1)\n\t" \
    "v_smfmac_f32_16x16x64_bf16 v[208:211], v[236:239], v[200:207], v212\n\t" \
    "s_waitcnt lgkmcnt(0)\n\t" \
    "v_smfmac_f32_16x16x64_bf16 v[208:211], v[240:243], v[176:183], v212\n\t" \
    "v_smfmac_f32_16x16x64_bf16 v[244:247], v[228:231], v[120:127], v212\n\t" \
    "v_smfmac_f32_16x16x64_bf16 v[244:247], v[232:235], v[144:151], v212\n\t" \
    "v_smfmac_f32_16x16x64_bf16 v[244:247], v[236:239], v[160:167], v212\n\t" \
    "v_add_f32 v208, v208, v210\n\t" \
    "v_add_f32 v209, v209, v211\n\t" \
    "v_cvt_pk_bf16_f32 v208, v208, v209\n\t" \
    "v_smfmac_f32_16x16x64_bf16 v[244:247], v[240:243], v[184:191], v212\n\t" \
    "ds_write_b16 v213, v208 offset:0\n\t" \
    "ds_write_b16_d16_hi v213, v208 offset:16\n\t" \
    "v_smfmac_f32_16x16x64_bf16 v[248:251], v[228:231], v[128:135], v212\n\t" \
    "v_smfmac_f32_16x16x64_bf16 v[248:251], v[232:235], v[152:159], v212\n\t" \
    "v_smfmac_f32_16x16x64_bf16 v[248:251], v[236:239], v[168:175], v212\n\t" \
    "v_add_f32 v244, v244, v246\n\t" \
    "v_add_f32 v245, v245, v247\n\t" \
    "v_smfmac_f32_16x16x64_bf16 v[248:251], v[240:243], v[192:199], v212\n\t" \
    "s_nop 7\n\t" \
    "v_add_f32 v248, v248, v250\n\t" \
    "v_add_f32 v249, v249, v251\n\t" \
    "v_cvt_pk_bf16_f32 v244, v244, v248\n\t" \
    "v_cvt_pk_bf16_f32 v245, v245, v249\n\t" \
    "ds_write2_b32 v214, v244, v245 offset1:4\n\t" \
    "s_setprio 0\n\t" \
    "s_sleep 1\n\t" \
    "ds_read_b128 v[200:203], v254 offset:12288\n\t" \
    "ds_read_b128 v[224:227], v254 offset:24576\n\t" \
    "ds_read_b128 v[204:207], v254 offset:13312\n\t" \
    "ds_read_b128 v[228:231], v254 offset:25600\n\t" \
    "ds_read_b128 v[208:211], v254 offset:14336\n\t" \
    "ds_read_b128 v[232:235], v254 offset:26624\n\t" \
    "ds_read_b128 v[212:215], v254 offset:15360\n\t" \
    "ds_read_b128 v[236:239], v254 offset:27648\n\t" \
    "ds_read_b128 v[216:219], v254 offset:16384\n\t" \
    "ds_read_b128 v[220:223], v254 offset:17408\n\t" \
    "s_and_b32 s44, s40, 3\n\t" \
    "s_lshl_b32 s44, s44, 17\n\t" \
    "s_add_u32 s50, s44, %[pb]\n\t" \
    "s_add_u32 s51, s44, %[pm]\n\t" \
    "s_mov_b32 s52, 0xf0000\n\t" \
    "s_mov_b32 s53, 0xfff00ff\n\t" \
    "s_mov_b32 s54, 0x1f0001\n\t" \
    "s_mov_b32 s55, 0x1fff01ff\n\t" \
    "s_mov_b32 s56, 0x3f0003\n\t" \
    "s_mov_b32 s57, 0x3fff03ff\n\t" \
    "s_mov_b32 s58, 0x7f0007\n\t" \
    "s_mov_b32 s59, 0x7fff07ff\n\t" \
    "s_waitcnt lgkmcnt(8)\n\t" \
    "v_mfma_f32_16x16x32_bf16 v[240:243], v[224:227], v[200:203], 0\n\t" \
    "ds_read_b128 v[224:227], v254 offset:28672\n\t" \
    "s_waitcnt lgkmcnt(7)\n\t" \
    "v_mfma_f32_16x16x32_bf16 v[240:243], v[228:231], v[204:207], v[240:243]\n\t" \
    "ds_read_b128 v[228:231], v254 offset:29696\n\t" \
    "s_waitcnt lgkmcnt(6)\n\t" \
    "v_mfma_f32_16x16x32_bf16 v[240:243], v[232:235], v[208:211], v[240:243]\n\t" \
    "ds_read_b128 v[232:235], v254 offset:18432\n\t" \
    "s_waitcnt lgkmcnt(5)\n\t" \
    "v_mfma_f32_16x16x32_bf16 v[240:243], v[236:239], v[212:215], v[240:243]\n\t" \
    "ds_read_b128 v[236:239], v254 offset:19456\n\t" \
    "s_waitcnt lgkmcnt(3)\n\t" \
    "v_mfma_f32_16x16x32_bf16 v[240:243], v[224:227], v[216:219], v[240:243]\n\t" \
    "ds_read_b128 v[224:227], v254 offset:20480\n\t" \
    "s_waitcnt lgkmcnt(3)\n\t" \
    "v_mfma_f32_16x16x32_bf16 v[240:243], v[228:231], v[220:223], v[240:243]\n\t" \
    "ds_read_b128 v[228:231], v254 offset:21504\n\t" \
    "v_and_b32 v252, 0xffffff7f, v254\n\t" \
    "s_waitcnt lgkmcnt(0)\n\t" \
    "s_barrier \n\t" \
    "v_mfma_f32_16x16x32_bf16 v[244:247], v[232:235], v[200:203], 0\n\t" \
    "ds_read_b128 v[232:235], v254 offset:22528\n\t" \
    "v_mfma_f32_16x16x32_bf16 v[244:247], v[236:239], v[204:207], v[244:247]\n\t" \
    "ds_read_b128 v[236:239], v254 offset:23552\n\t" \
    "v_mfma_f32_16x16x32_bf16 v[244:247], v[224:227], v[208:211], v[244:247]\n\t" \
    "ds_read_b128 v[224:227], v252 offset:3072\n\t" \
    "v_mfma_f32_16x16x32_bf16 v[244:247], v[228:231], v[212:215], v[244:247]\n\t" \
    "ds_read_b128 v[228:231], v252 offset:3200\n\t" \
    "s_waitcnt lgkmcnt(3)\n\t" \
    "v_mfma_f32_16x16x32_bf16 v[244:247], v[232:235], v[216:219], v[244:247]\n\t" \
    "ds_read_b128 v[232:235], v252 offset:4096\n\t" \
    "s_waitcnt lgkmcnt(3)\n\t" \
    "v_mfma_f32_16x16x32_bf16 v[244:247], v[236:239], v[220:223], v[244:247]\n\t" \
    "ds_read_b128 v[236:239], v252 offset:4224\n\t" \
    "s_waitcnt lgkmcnt(3)\n\t" \
    "v_mfma_f32_16x16x32_bf16 v[248:251], v[224:227], v[200:203], 0\n\t" \
    "ds_read_b128 v[224:227], v252 offset:5120\n\t" \
    "s_waitcnt lgkmcnt(3)\n\t" \
    "v_mfma_f32_16x16x32_bf16 v[248:251], v[228:231], v[204:207], v[248:251]\n\t" \
    "ds_read_b128 v[228:231], v252 offset:5248\n\t" \
    "s_waitcnt lgkmcnt(3)\n\t" \
    "v_mfma_f32_16x16x32_bf16 v[248:251], v[232:235], v[208:211], v[248:251]\n\t" \
    "s_waitcnt lgkmcnt(2)\n\t" \
    "v_mfma_f32_16x16x32_bf16 v[248:251], v[236:239], v[212:215], v[248:251]\n\t" \
    "s_waitcnt lgkmcnt(1)\n\t" \
    "v_mfma_f32_16x16x32_bf16 v[248:251], v[224:227], v[216:219], v[248:251]\n\t" \
    "s_waitcnt lgkmcnt(0)\n\t" \
    "v_mfma_f32_16x16x32_bf16 v[248:251], v[228:231], v[220:223], v[248:251]\n\t" \
    "v_mov_b32 v226, 0xff7fffff\n\t" \
    "v_cndmask_b32 v240, v240, v226, s[52:53]\n\t" \
    "v_cndmask_b32 v241, v241, v226, s[54:55]\n\t" \
    "v_cndmask_b32 v242, v242, v226, s[56:57]\n\t" \
    "v_cndmask_b32 v243, v243, v226, s[58:59]\n\t" \
    "v_max3_f32 v224, v240, v241, v242\n\t" \
    "v_max3_f32 v225, v244, v245, v246\n\t" \
    "v_max3_f32 v224, v224, v243, v247\n\t" \
    "v_lshrrev_b32 v200, 5, v253\n\t" \
    "v_and_b32 v201, 0xf0, v253\n\t" \
    "v_and_or_b32 v200, v200, 8, v201\n\t" \
    "v_max3_f32 v224, v224, v248, v249\n\t" \
    "v_max3_f32 v225, v225, v250, v251\n\t" \
    "v_max_f32 v224, v224, v225\n\t" \
    "v_mov_b32 v225, v224\n\t" \
    "s_nop 1\n\t" \
    "v_permlane16_swap_b32 v224, v225\n\t" \
    "v_max_f32 v224, v224, v225\n\t" \
    "v_mov_b32 v225, v224\n\t" \
    "s_nop 1\n\t" \
    "v_permlane32_swap_b32 v224, v225\n\t" \
    "v_max_f32 v224, v224, v225\n\t" \
    "v_mul_f32 v224, %[scl], v224\n\t" \
    "v_fma_f32 v240, v240, %[scl], -v224\n\t" \
    "v_fma_f32 v241, v241, %[scl], -v224\n\t" \
    "v_exp_f32 v240, v240\n\t" \
    "v_fma_f32 v242, v242, %[scl], -v224\n\t" \
    "v_exp_f32 v241, v241\n\t" \
    "v_fma_f32 v243, v243, %[scl], -v224\n\t" \
    "v_exp_f32 v242, v242\n\t" \
    "v_fma_f32 v244, v244, %[scl], -v224\n\t" \
    "v_exp_f32 v243, v243\n\t" \
    "v_fma_f32 v245, v245, %[scl], -v224\n\t" \
    "v_exp_f32 v244, v244\n\t" \
    "v_fma_f32 v246, v246, %[scl], -v224\n\t" \
    "v_exp_f32 v245, v245\n\t" \
    "v_fma_f32 v247, v247, %[scl], -v224\n\t" \
    "v_exp_f32 v246, v246\n\t" \
    "v_fma_f32 v248, v248, %[scl], -v224\n\t" \
    "v_exp_f32 v247, v247\n\t" \
    "v_fma_f32 v249, v249, %[scl], -v224\n\t" \
    "v_exp_f32 v248, v248\n\t" \
    "v_fma_f32 v250, v250, %[scl], -v224\n\t" \
    "v_exp_f32 v249, v249\n\t" \
    "v_fma_f32 v251, v251, %[scl], -v224\n\t" \
    "v_exp_f32 v250, v250\n\t" \
    "v_exp_f32 v251, v251\n\t" \
    "v_add_f32 v224, v240, v241\n\t" \
    "v_add_f32 v225, v242, v243\n\t" \
    "v_add_f32 v226, v244, v245\n\t" \
    "v_add_f32 v227, v246, v247\n\t" \
    "v_add_f32 v232, v248, v249\n\t" \
    "v_add_f32 v233, v250, v251\n\t" \
    "v_add_f32 v224, v224, v225\n\t" \
    "v_add_f32 v226, v226, v227\n\t" \
    "v_add_f32 v232, v232, v233\n\t" \
    "v_add_f32 v224, v224, v226\n\t" \
    "v_fma_f32 v224, v232, 0.5, v224\n\t" \
    "v_mov_b32 v225, v224\n\t" \
    "s_nop 1\n\t" \
    "v_permlane16_swap_b32 v224, v225\n\t" \
    "v_add_f32 v224, v224, v225\n\t" \
    "v_mov_b32 v225, v224\n\t" \
    "s_nop 1\n\t" \
    "v_permlane32_swap_b32 v224, v225\n\t" \
    "v_add_f32 v224, v224, v225\n\t" \
    "v_rcp_f32 v224, v224\n\t" \
    "s_nop 0\n\t" \
    "v_mul_f32 v240, v240, v224\n\t" \
    "v_mul_f32 v241, v241, v224\n\t" \
    "v_mul_f32 v242, v242, v224\n\t" \
    "v_mul_f32 v243, v243, v224\n\t" \
    "v_mul_f32 v244, v244, v224\n\t" \
    "v_mul_f32 v245, v245, v224\n\t" \
    "v_mul_f32 v246, v246, v224\n\t" \
    "v_mul_f32 v247, v247, v224\n\t" \
    "v_mul_f32 v248, v248, v224\n\t" \
    "v_mul_f32 v249, v249, v224\n\t" \
    "v_mul_f32 v250, v250, v224\n\t" \
    "v_mul_f32 v251, v251, v224\n\t" \
    "v_cvt_pk_bf16_f32 v204, |v244|, |v240|\n\t" \
    "v_cvt_pk_bf16_f32 v205, |v245|, |v241|\n\t" \
    "v_cvt_pk_bf16_f32 v206, |v246|, |v242|\n\t" \
    "v_cvt_pk_bf16_f32 v207, |v247|, |v243|\n\t" \
    "v_cvt_pk_bf16_f32 v208, |v248|, |v249|\n\t" \
    "v_cvt_pk_bf16_f32 v209, |v250|, |v251|\n\t" \
    "buffer_store_dwordx4 v[204:207], v253, s[60:63], s50 offen sc1\n\t" \
    "s_mov_b32 exec_lo, -1\n\t" \
    "s_mov_b32 exec_hi, 0\n\t" \
    "buffer_store_dwordx2 v[208:209], v200, s[60:63], s51 offen sc1\n\t" \
    "s_mov_b32 exec_hi, -1\n\t" \
    "ds_read_b128 v[224:227], v253 offset:0\n\t" \
    "ds_read_b128 v[244:247], v254 offset:53248\n\t" \
    "ds_read_b128 v[228:231], v253 offset:1024\n\t" \
    "ds_read_b128 v[232:235], v253 offset:20480\n\t" \
    "ds_read_b128 v[236:239], v253 offset:21504\n\t" \
    "ds_read_b128 v[248:251], v254 offset:54272\n\t" \
    "s_add_u32 s40, s40, 1\n\t" \
    "s_cmp_lg_u32 s40, 64\n\t" \
    "s_cbranch_scc1 .Ls23_%=\n\t" \
    "s_branch .Lend%=\n\t" \
    ".Ls1_%=:\n\t" \
    "v_add_u32 v244, 0x18000, v243\n\t" \
    "s_waitcnt vmcnt(12) lgkmcnt(0)\n\t" \
    "s_barrier \n\t" \
    "s_add_u32 m0, %[wm], 0x12000\n\t" \
    "s_mov_b32 s44, 0x1e0000\n\t" \
    "buffer_load_dwordx4 v240, %[wr], s44 offen offset:0 nt lds\n\t" \
    "buffer_load_dwordx4 v241, %[wr], s44 offen offset:1024 nt lds\n\t" \
    "buffer_load_dwordx4 v242, %[wr], s44 offen offset:2048 nt lds\n\t" \
    "ds_read_b64_tr_b16 v[56:57], v244 offset:0\n\t" \
    "ds_read_b64_tr_b16 v[58:59], v244 offset:1536\n\t" \
    "ds_read_b64_tr_b16 v[64:65], v244 offset:32\n\t" \
    "ds_read_b64_tr_b16 v[66:67], v244 offset:1568\n\t" \
    "ds_read_b64_tr_b16 v[72:73], v244 offset:64\n\t" \
    "ds_read_b64_tr_b16 v[74:75], v244 offset:1600\n\t" \
    "v_add_u32 v244, 0x1e000, v243\n\t" \
    "s_waitcnt vmcnt(12) lgkmcnt(0)\n\t" \
    "s_barrier \n\t" \
    "s_add_u32 m0, %[wm], 0x18000\n\t" \
    "s_mov_b32 s44, 0x240000\n\t" \
    "buffer_load_dwordx4 v240, %[wr], s44 offen offset:0 nt lds\n\t" \
    "buffer_load_dwordx4 v241, %[wr], s44 offen offset:1024 nt lds\n\t" \
    "buffer_load_dwordx4 v242, %[wr], s44 offen offset:2048 nt lds\n\t" \
    "ds_read_b64_tr_b16 v[60:61], v244 offset:0\n\t" \
    "ds_read_b64_tr_b16 v[62:63], v244 offset:1536\n\t" \
    "ds_read_b64_tr_b16 v[68:69], v244 offset:32\n\t" \
    "ds_read_b64_tr_b16 v[70:71], v244 offset:1568\n\t" \
    "ds_read_b64_tr_b16 v[76:77], v244 offset:64\n\t" \
    "ds_read_b64_tr_b16 v[78:79], v244 offset:1600\n\t" \
    "v_add_u32 v244, 0x0, v243\n\t" \
    "s_waitcnt vmcnt(12) lgkmcnt(0)\n\t" \
    "s_barrier \n\t" \
    "s_add_u32 m0, %[wm], 0x1e000\n\t" \
    "s_mov_b32 s44, 0x2a0000\n\t" \
    "buffer_load_dwordx4 v240, %[wr], s44 offen offset:0 nt lds\n\t" \
    "buffer_load_dwordx4 v241, %[wr], s44 offen offset:1024 nt lds\n\t" \
    "buffer_load_dwordx4 v242, %[wr], s44 offen offset:2048 nt lds\n\t" \
    "ds_read_b64_tr_b16 v[96:97], v244 offset:0\n\t" \
    "ds_read_b64_tr_b16 v[98:99], v244 offset:1536\n\t" \
    "ds_read_b64_tr_b16 v[104:105], v244 offset:32\n\t" \
    "ds_read_b64_tr_b16 v[106:107], v244 offset:1568\n\t" \
    "ds_read_b64_tr_b16 v[112:113], v244 offset:64\n\t" \
    "ds_read_b64_tr_b16 v[114:115], v244 offset:1600\n\t" \
    "v_add_u32 v244, 0x6000, v243\n\t" \
    "s_waitcnt vmcnt(12) lgkmcnt(0)\n\t" \
    "s_barrier \n\t" \
    "s_add_u32 m0, %[wm], 0x0\n\t" \
    "s_mov_b32 s44, 0x300000\n\t" \
    "buffer_load_dwordx4 v240, %[wr], s44 offen offset:0 nt lds\n\t" \
    "buffer_load_dwordx4 v241, %[wr], s44 offen offset:1024 nt lds\n\t" \
    "buffer_load_dwordx4 v242, %[wr], s44 offen offset:2048 nt lds\n\t" \
    "ds_read_b64_tr_b16 v[100:101], v244 offset:0\n\t" \
    "ds_read_b64_tr_b16 v[102:103], v244 offset:1536\n\t" \
    "ds_read_b64_tr_b16 v[108:109], v244 offset:32\n\t" \
    "ds_read_b64_tr_b16 v[110:111], v244 offset:1568\n\t" \
    "ds_read_b64_tr_b16 v[116:117], v244 offset:64\n\t" \
    "ds_read_b64_tr_b16 v[118:119], v244 offset:1600\n\t" \
    "v_add_u32 v244, 0xc000, v243\n\t" \
    "s_waitcnt vmcnt(12) lgkmcnt(0)\n\t" \
    "s_barrier \n\t" \
    "s_add_u32 m0, %[wm], 0x6000\n\t" \
    "s_mov_b32 s44, 0x360000\n\t" \
    "buffer_load_dwordx4 v240, %[wr], s44 offen offset:0 nt lds\n\t" \
    "buffer_load_dwordx4 v241, %[wr], s44 offen offset:1024 nt lds\n\t" \
    "buffer_load_dwordx4 v242, %[wr], s44 offen offset:2048 nt lds\n\t" \
    "ds_read_b64_tr_b16 v[200:201], v244 offset:0\n\t" \
    "ds_read_b64_tr_b16 v[202:203], v244 offset:1536\n\t" \
    "ds_read_b64_tr_b16 v[0:1], v244 offset:32\n\t" \
    "ds_read_b64_tr_b16 v[2:3], v244 offset:1568\n\t" \
    "ds_read_b64_tr_b16 v[8:9], v244 offset:64\n\t" \
    "ds_read_b64_tr_b16 v[10:11], v244 offset:1600\n\t" \
    "v_add_u32 v244, 0x12000, v243\n\t" \
    "s_waitcnt vmcnt(12) lgkmcnt(0)\n\t" \
    "s_barrier \n\t" \
    "s_add_u32 m0, %[wm], 0xc000\n\t" \
    "s_mov_b32 s44, 0x3c0000\n\t" \
    "buffer_load_dwordx4 v240, %[wr], s44 offen offset:0 nt lds\n\t" \
    "buffer_load_dwordx4 v241, %[wr], s44 offen offset:1024 nt lds\n\t" \
    "buffer_load_dwordx4 v242, %[wr], s44 offen offset:2048 nt lds\n\t" \
    "ds_read_b64_tr_b16 v[204:205], v244 offset:0\n\t" \
    "ds_read_b64_tr_b16 v[206:207], v244 offset:1536\n\t" \
    "ds_read_b64_tr_b16 v[4:5], v244 offset:32\n\t" \
    "ds_read_b64_tr_b16 v[6:7], v244 offset:1568\n\t" \
    "ds_read_b64_tr_b16 v[12:13], v244 offset:64\n\t" \
    "ds_read_b64_tr_b16 v[14:15], v244 offset:1600\n\t" \
    "v_add_u32 v244, 0x18000, v243\n\t" \
    "s_waitcnt vmcnt(12) lgkmcnt(0)\n\t" \
    "s_barrier \n\t" \
    "s_add_u32 m0, %[wm], 0x12000\n\t" \
    "s_mov_b32 s44, 0x420000\n\t" \
    "buffer_load_dwordx4 v240, %[wr], s44 offen offset:0 nt lds\n\t" \
    "buffer_load_dwordx4 v241, %[wr], s44 offen offset:1024 nt lds\n\t" \
    "buffer_load_dwordx4 v242, %[wr], s44 offen offset:2048 nt lds\n\t" \
    "ds_read_b64_tr_b16 v[208:209], v244 offset:0\n\t" \
    "ds_read_b64_tr_b16 v[210:211], v244 offset:1536\n\t" \
    "ds_read_b64_tr_b16 v[40:41], v244 offset:32\n\t" \
    "ds_read_b64_tr_b16 v[42:43], v244 offset:1568\n\t" \
    "ds_read_b64_tr_b16 v[48:49], v244 offset:64\n\t" \
    "ds_read_b64_tr_b16 v[50:51], v244 offset:1600\n\t" \
    "v_add_u32 v244, 0x1e000, v243\n\t" \
    "s_waitcnt vmcnt(12) lgkmcnt(0)\n\t" \
    "s_barrier \n\t" \
    "s_add_u32 m0, %[wm], 0x18000\n\t" \
    "s_mov_b32 s44, 0x480000\n\t" \
    "buffer_load_dwordx4 v240, %[wr], s44 offen offset:0 nt lds\n\t" \
    "buffer_load_dwordx4 v241, %[wr], s44 offen offset:1024 nt lds\n\t" \
    "buffer_load_dwordx4 v242, %[wr], s44 offen offset:2048 nt lds\n\t" \
    "ds_read_b64_tr_b16 v[212:213], v244 offset:0\n\t" \
    "ds_read_b64_tr_b16 v[214:215], v244 offset:1536\n\t" \
    "ds_read_b64_tr_b16 v[44:45], v244 offset:32\n\t" \
    "ds_read_b64_tr_b16 v[46:47], v244 offset:1568\n\t" \
    "ds_read_b64_tr_b16 v[52:53], v244 offset:64\n\t" \
    "ds_read_b64_tr_b16 v[54:55], v244 offset:1600\n\t" \
    "v_add_u32 v244, 0x0, v243\n\t" \
    "s_waitcnt vmcnt(12) lgkmcnt(0)\n\t" \
    "s_barrier \n\t" \
    "v_mov_b32 v246, 0\n\t" \
    "buffer_load_dword v246, v246, s[60:63], %[mo] offen sc1\n\t" \
    "s_add_u32 m0, %[wm], 0x1e000\n\t" \
    "s_mov_b32 s44, 0x4e0000\n\t" \
    "buffer_load_dwordx4 v240, %[wr], s44 offen offset:0 nt lds\n\t" \
    "buffer_load_dwordx4 v241, %[wr], s44 offen offset:1024 nt lds\n\t" \
    "buffer_load_dwordx4 v242, %[wr], s44 offen offset:2048 nt lds\n\t" \
    "ds_read_b64_tr_b16 v[136:137], v244 offset:0\n\t" \
    "ds_read_b64_tr_b16 v[138:139], v244 offset:1536\n\t" \
    "ds_read_b64_tr_b16 v[144:145], v244 offset:32\n\t" \
    "ds_read_b64_tr_b16 v[146:147], v244 offset:1568\n\t" \
    "ds_read_b64_tr_b16 v[152:153], v244 offset:64\n\t" \
    "ds_read_b64_tr_b16 v[154:155], v244 offset:1600\n\t" \
    "v_add_u32 v244, 0x6000, v243\n\t" \
    "s_waitcnt vmcnt(12) lgkmcnt(0)\n\t" \
    "s_barrier \n\t" \
    "s_add_u32 m0, %[wm], 0x0\n\t" \
    "s_mov_b32 s44, 0x540000\n\t" \
    "buffer_load_dwordx4 v240, %[wr], s44 offen offset:0 nt lds\n\t" \
    "buffer_load_dwordx4 v241, %[wr], s44 offen offset:1024 nt lds\n\t" \
    "buffer_load_dwordx4 v242, %[wr], s44 offen offset:2048 nt lds\n\t" \
    "ds_read_b64_tr_b16 v[140:141], v244 offset:0\n\t" \
    "ds_read_b64_tr_b16 v[142:143], v244 offset:1536\n\t" \
    "ds_read_b64_tr_b16 v[148:149], v244 offset:32\n\t" \
    "ds_read_b64_tr_b16 v[150:151], v244 offset:1568\n\t" \
    "ds_read_b64_tr_b16 v[156:157], v244 offset:64\n\t" \
    "ds_read_b64_tr_b16 v[158:159], v244 offset:1600\n\t" \
    "v_add_u32 v244, 0xc000, v243\n\t" \
    "s_waitcnt vmcnt(12) lgkmcnt(0)\n\t" \
    "s_barrier \n\t" \
    "s_add_u32 m0, %[wm], 0x6000\n\t" \
    "s_mov_b32 s44, 0x5a0000\n\t" \
    "buffer_load_dwordx4 v240, %[wr], s44 offen offset:0 nt lds\n\t" \
    "buffer_load_dwordx4 v241, %[wr], s44 offen offset:1024 nt lds\n\t" \
    "buffer_load_dwordx4 v242, %[wr], s44 offen offset:2048 nt lds\n\t" \
    "ds_read_b64_tr_b16 v[216:217], v244 offset:0\n\t" \
    "ds_read_b64_tr_b16 v[218:219], v244 offset:1536\n\t" \
    "ds_read_b64_tr_b16 v[80:81], v244 offset:32\n\t" \
    "ds_read_b64_tr_b16 v[82:83], v244 offset:1568\n\t" \
    "ds_read_b64_tr_b16 v[88:89], v244 offset:64\n\t" \
    "ds_read_b64_tr_b16 v[90:91], v244 offset:1600\n\t" \
    "v_add_u32 v244, 0x12000, v243\n\t" \
    "s_waitcnt vmcnt(12) lgkmcnt(0)\n\t" \
    "s_barrier \n\t" \
    "s_add_u32 m0, %[wm], 0xc000\n\t" \
    "s_mov_b32 s44, 0x600000\n\t" \
    "buffer_load_dwordx4 v240, %[wr], s44 offen offset:0 nt lds\n\t" \
    "buffer_load_dwordx4 v241, %[wr], s44 offen offset:1024 nt lds\n\t" \
    "buffer_load_dwordx4 v242, %[wr], s44 offen offset:2048 nt lds\n\t" \
    "ds_read_b64_tr_b16 v[220:221], v244 offset:0\n\t" \
    "ds_read_b64_tr_b16 v[222:223], v244 offset:1536\n\t" \
    "ds_read_b64_tr_b16 v[84:85], v244 offset:32\n\t" \
    "ds_read_b64_tr_b16 v[86:87], v244 offset:1568\n\t" \
    "ds_read_b64_tr_b16 v[92:93], v244 offset:64\n\t" \
    "ds_read_b64_tr_b16 v[94:95], v244 offset:1600\n\t" \
    "v_add_u32 v244, 0x18000, v243\n\t" \
    "s_waitcnt vmcnt(12) lgkmcnt(0)\n\t" \
    "s_barrier \n\t" \
    "s_add_u32 m0, %[wm], 0x12000\n\t" \
    "s_mov_b32 s44, 0x660000\n\t" \
    "buffer_load_dwordx4 v240, %[wr], s44 offen offset:0 nt lds\n\t" \
    "buffer_load_dwordx4 v241, %[wr], s44 offen offset:1024 nt lds\n\t" \
    "buffer_load_dwordx4 v242, %[wr], s44 offen offset:2048 nt lds\n\t" \
    "ds_read_b64_tr_b16 v[16:17], v244 offset:0\n\t" \
    "ds_read_b64_tr_b16 v[18:19], v244 offset:1536\n\t" \
    "ds_read_b64_tr_b16 v[24:25], v244 offset:32\n\t" \
    "ds_read_b64_tr_b16 v[26:27], v244 offset:1568\n\t" \
    "ds_read_b64_tr_b16 v[32:33], v244 offset:64\n\t" \
    "ds_read_b64_tr_b16 v[34:35], v244 offset:1600\n\t" \
    "v_add_u32 v244, 0x1e000, v243\n\t" \
    "s_waitcnt vmcnt(12) lgkmcnt(0)\n\t" \
    "s_barrier \n\t" \
    ".Ls11_%=:\n\t" \
    "v_readfirstlane_b32 s43, v246\n\t" \
    "s_nop 0\n\t" \
    "s_cmp_lg_u32 s43, -1\n\t" \
    "s_cbranch_scc1 .Ls12_%=\n\t" \
    "s_cmp_eq_u32 s41, 0\n\t" \
    "s_cbranch_scc1 .Ls12_%=\n\t" \
    "s_sub_u32 s41, s41, 1\n\t" \
    "s_sleep 4\n\t" \
    "v_mov_b32 v246, 0\n\t" \
    "buffer_load_dword v246, v246, s[60:63], %[mo] offen sc1\n\t" \
    "s_waitcnt vmcnt(0)\n\t" \
    "s_branch .Ls11_%=\n\t" \
    ".Ls12_%=:\n\t" \
    "v_mov_b32 v246, 0x8000\n\t" \
    "s_mov_b64 exec, 1\n\t" \
    "buffer_store_dword v246, v246, s[60:63], %[mo] offen sc1\n\t" \
    "s_mov_b64 exec, -1\n\t" \
    "s_and_b32 s44, s43, 0x5000\n\t" \
    "s_xor_b32 %[mb], %[mb], s44\n\t" \
    "s_xor_b32 %[fl], %[fl], s44\n\t" \
    "s_and_b32 s43, s43, 0x2000\n\t" \
    "s_xor_b32 s43, s43, %[lx]\n\t" \
    "s_add_u32 s60, s60, s43\n\t" \
    "s_addc_u32 s61, s61, 0\n\t" \
    "s_add_u32 m0, %[wm], 0x18000\n\t" \
    "s_mov_b32 s44, 0x6c0000\n\t" \
    "buffer_load_dwordx4 v240, %[wr], s44 offen offset:0 nt lds\n\t" \
    "buffer_load_dwordx4 v241, %[wr], s44 offen offset:1024 nt lds\n\t" \
    "buffer_load_dwordx4 v242, %[wr], s44 offen offset:2048 nt lds\n\t" \
    "ds_read_b64_tr_b16 v[20:21], v244 offset:0\n\t" \
    "ds_read_b64_tr_b16 v[22:23], v244 offset:1536\n\t" \
    "ds_read_b64_tr_b16 v[28:29], v244 offset:32\n\t" \
    "ds_read_b64_tr_b16 v[30:31], v244 offset:1568\n\t" \
    "ds_read_b64_tr_b16 v[36:37], v244 offset:64\n\t" \
    "ds_read_b64_tr_b16 v[38:39], v244 offset:1600\n\t" \
    "v_add_u32 v244, 0x0, v243\n\t" \
    "s_waitcnt vmcnt(12) lgkmcnt(0)\n\t" \
    "s_barrier \n\t" \
    "s_add_u32 m0, %[wm], 0x1e000\n\t" \
    "s_mov_b32 s44, 0x720000\n\t" \
    "buffer_load_dwordx4 v240, %[wr], s44 offen offset:0 nt lds\n\t" \
    "buffer_load_dwordx4 v241, %[wr], s44 offen offset:1024 nt lds\n\t" \
    "buffer_load_dwordx4 v242, %[wr], s44 offen offset:2048 nt lds\n\t" \
    "ds_read_b64_tr_b16 v[224:225], v244 offset:0\n\t" \
    "ds_read_b64_tr_b16 v[226:227], v244 offset:1536\n\t" \
    "ds_read_b64_tr_b16 v[120:121], v244 offset:32\n\t" \
    "ds_read_b64_tr_b16 v[122:123], v244 offset:1568\n\t" \
    "ds_read_b64_tr_b16 v[128:129], v244 offset:64\n\t" \
    "ds_read_b64_tr_b16 v[130:131], v244 offset:1600\n\t" \
    "v_add_u32 v244, 0x6000, v243\n\t" \
    "s_waitcnt vmcnt(12) lgkmcnt(0)\n\t" \
    "s_barrier \n\t" \
    "ds_read_b64_tr_b16 v[228:229], v244 offset:0\n\t" \
    "ds_read_b64_tr_b16 v[230:231], v244 offset:1536\n\t" \
    "ds_read_b64_tr_b16 v[124:125], v244 offset:32\n\t" \
    "ds_read_b64_tr_b16 v[126:127], v244 offset:1568\n\t" \
    "ds_read_b64_tr_b16 v[132:133], v244 offset:64\n\t" \
    "ds_read_b64_tr_b16 v[134:135], v244 offset:1600\n\t" \
    "v_add_u32 v244, 0xc000, v243\n\t" \
    "s_waitcnt vmcnt(9) lgkmcnt(0)\n\t" \
    "s_barrier \n\t" \
    "ds_read_b64_tr_b16 v[232:233], v244 offset:0\n\t" \
    "ds_read_b64_tr_b16 v[234:235], v244 offset:1536\n\t" \
    "ds_read_b64_tr_b16 v[160:161], v244 offset:32\n\t" \
    "ds_read_b64_tr_b16 v[162:163], v244 offset:1568\n\t" \
    "ds_read_b64_tr_b16 v[168:169], v244 offset:64\n\t" \
    "ds_read_b64_tr_b16 v[170:171], v244 offset:1600\n\t" \
    "v_add_u32 v244, 0x12000, v243\n\t" \
    "s_waitcnt vmcnt(6) lgkmcnt(0)\n\t" \
    "s_barrier \n\t" \
    "ds_read_b64_tr_b16 v[236:237], v244 offset:0\n\t" \
    "ds_read_b64_tr_b16 v[238:239], v244 offset:1536\n\t" \
    "ds_read_b64_tr_b16 v[164:165], v244 offset:32\n\t" \
    "ds_read_b64_tr_b16 v[166:167], v244 offset:1568\n\t" \
    "ds_read_b64_tr_b16 v[172:173], v244 offset:64\n\t" \
    "ds_read_b64_tr_b16 v[174:175], v244 offset:1600\n\t" \
    "v_add_u32 v244, 0x18000, v243\n\t" \
    "s_waitcnt vmcnt(3) lgkmcnt(0)\n\t" \
    "s_barrier \n\t" \
    "ds_read_b64_tr_b16 v[176:177], v244 offset:0\n\t" \
    "ds_read_b64_tr_b16 v[178:179], v244 offset:1536\n\t" \
    "ds_read_b64_tr_b16 v[184:185], v244 offset:32\n\t" \
    "ds_read_b64_tr_b16 v[186:187], v244 offset:1568\n\t" \
    "ds_read_b64_tr_b16 v[192:193], v244 offset:64\n\t" \
    "ds_read_b64_tr_b16 v[194:195], v244 offset:1600\n\t" \
    "v_add_u32 v244, 0x1e000, v243\n\t" \
    "s_waitcnt vmcnt(0) lgkmcnt(0)\n\t" \
    "s_barrier \n\t" \
    "ds_read_b64_tr_b16 v[180:181], v244 offset:0\n\t" \
    "ds_read_b64_tr_b16 v[182:183], v244 offset:1536\n\t" \
    "ds_read_b64_tr_b16 v[188:189], v244 offset:32\n\t" \
    "ds_read_b64_tr_b16 v[190:191], v244 offset:1568\n\t" \
    "ds_read_b64_tr_b16 v[196:197], v244 offset:64\n\t" \
    "ds_read_b64_tr_b16 v[198:199], v244 offset:1600\n\t" \
    "s_waitcnt lgkmcnt(0)\n\t" \
    "s_barrier \n\t" \
    "ds_write_b128 v254, v[200:203] offset:40960\n\t" \
    "ds_write_b128 v254, v[204:207] offset:41984\n\t" \
    "ds_write_b128 v254, v[208:211] offset:43008\n\t" \
    "ds_write_b128 v254, v[212:215] offset:44032\n\t" \
    "ds_write_b128 v254, v[216:219] offset:45056\n\t" \
    "ds_write_b128 v254, v[220:223] offset:46080\n\t" \
    "ds_write_b128 v254, v[224:227] offset:47104\n\t" \
    "ds_write_b128 v254, v[228:231] offset:48128\n\t" \
    "ds_write_b128 v254, v[232:235] offset:49152\n\t" \
    "ds_write_b128 v254, v[236:239] offset:50176\n\t" \
    "s_waitcnt vmcnt(0) lgkmcnt(0)\n\t" \
    "s_barrier \n\t" \
    "ds_read_b128 v[224:227], v253 offset:0\n\t" \
    "ds_read_b128 v[244:247], v254 offset:40960\n\t" \
    "ds_read_b128 v[228:231], v253 offset:1024\n\t" \
    "ds_read_b128 v[232:235], v253 offset:20480\n\t" \
    "ds_read_b128 v[236:239], v253 offset:21504\n\t" \
    "ds_read_b128 v[248:251], v254 offset:41984\n\t" \
    ".Ls13_%=:\n\t" \
    "s_waitcnt lgkmcnt(5)\n\t" \
    "v_mfma_f32_16x16x32_bf16 v[204:207], v[64:67], v[224:227], 0\n\t" \
    "s_waitcnt lgkmcnt(3)\n\t" \
    "v_mfma_f32_16x16x32_bf16 v[204:207], v[68:71], v[228:231], v[204:207]\n\t" \
    "ds_read_b128 v[240:243], v253 offset:2048\n\t" \
    "v_mfma_f32_16x16x32_bf16 v[208:211], v[72:75], v[224:227], 0\n\t" \
    "v_mfma_f32_16x16x32_bf16 v[208:211], v[76:79], v[228:231], v[208:211]\n\t" \
    "v_mfma_f32_16x16x32_bf16 v[200:203], v[56:59], v[224:227], 0\n\t" \
    "v_mfma_f32_16x16x32_bf16 v[200:203], v[60:63], v[228:231], v[200:203]\n\t" \
    "s_waitcnt lgkmcnt(3)\n\t" \
    "v_mfma_f32_16x16x32_bf16 v[216:219], v[64:67], v[232:235], 0\n\t" \
    "ds_read_b128 v[224:227], v253 offset:3072\n\t" \
    "s_waitcnt lgkmcnt(3)\n\t" \
    "v_mfma_f32_16x16x32_bf16 v[216:219], v[68:71], v[236:239], v[216:219]\n\t" \
    "ds_read_b128 v[228:231], v253 offset:22528\n\t" \
    "v_mfma_f32_16x16x32_bf16 v[220:223], v[72:75], v[232:235], 0\n\t" \
    "v_mfma_f32_16x16x32_bf16 v[220:223], v[76:79], v[236:239], v[220:223]\n\t" \
    "v_mfma_f32_16x16x32_bf16 v[212:215], v[56:59], v[232:235], 0\n\t" \
    "v_mfma_f32_16x16x32_bf16 v[212:215], v[60:63], v[236:239], v[212:215]\n\t" \
    "s_waitcnt lgkmcnt(2)\n\t" \
    "v_mfma_f32_16x16x32_bf16 v[204:207], v[104:107], v[240:243], v[204:207]\n\t" \
    "ds_read_b128 v[232:235], v253 offset:23552\n\t" \
    "s_waitcnt lgkmcnt(2)\n\t" \
    "v_mfma_f32_16x16x32_bf16 v[204:207], v[108:111], v[224:227], v[204:207]\n\t" \
    "ds_read_b128 v[236:239], v253 offset:4096\n\t" \
    "v_mfma_f32_16x16x32_bf16 v[208:211], v[112:115], v[240:243], v[208:211]\n\t" \
    "v_mfma_f32_16x16x32_bf16 v[208:211], v[116:119], v[224:227], v[208:211]\n\t" \
    "v_mfma_f32_16x16x32_bf16 v[200:203], v[96:99], v[240:243], v[200:203]\n\t" \
    "v_mfma_f32_16x16x32_bf16 v[200:203], v[100:103], v[224:227], v[200:203]\n\t" \
    "s_waitcnt lgkmcnt(2)\n\t" \
    "v_mfma_f32_16x16x32_bf16 v[216:219], v[104:107], v[228:231], v[216:219]\n\t" \
    "ds_read_b128 v[240:243], v253 offset:5120\n\t" \
    "s_waitcnt lgkmcnt(2)\n\t" \
    "v_mfma_f32_16x16x32_bf16 v[216:219], v[108:111], v[232:235], v[216:219]\n\t" \
    "ds_read_b128 v[224:227], v253 offset:24576\n\t" \
    "v_mfma_f32_16x16x32_bf16 v[220:223], v[112:115], v[228:231], v[220:223]\n\t" \
    "v_mfma_f32_16x16x32_bf16 v[220:223], v[116:119], v[232:235], v[220:223]\n\t" \
    "v_mfma_f32_16x16x32_bf16 v[212:215], v[96:99], v[228:231], v[212:215]\n\t" \
    "v_mfma_f32_16x16x32_bf16 v[212:215], v[100:103], v[232:235], v[212:215]\n\t" \
    "s_waitcnt lgkmcnt(2)\n\t" \
    "v_mfma_f32_16x16x32_bf16 v[204:207], v[0:3], v[236:239], v[204:207]\n\t" \
    "ds_read_b128 v[228:231], v253 offset:25600\n\t" \
    "s_waitcnt lgkmcnt(2)\n\t" \
    "v_mfma_f32_16x16x32_bf16 v[204:207], v[4:7], v[240:243], v[204:207]\n\t" \
    "ds_read_b128 v[232:235], v253 offset:6144\n\t" \
    "v_mfma_f32_16x16x32_bf16 v[208:211], v[8:11], v[236:239], v[208:211]\n\t" \
    "v_mfma_f32_16x16x32_bf16 v[208:211], v[12:15], v[240:243], v[208:211]\n\t" \
    "v_mfma_f32_16x16x32_bf16 v[200:203], v[244:247], v[236:239], v[200:203]\n\t" \
    "v_mfma_f32_16x16x32_bf16 v[200:203], v[248:251], v[240:243], v[200:203]\n\t" \
    "s_waitcnt lgkmcnt(2)\n\t" \
    "v_mfma_f32_16x16x32_bf16 v[216:219], v[0:3], v[224:227], v[216:219]\n\t" \
    "ds_read_b128 v[236:239], v253 offset:7168\n\t" \
    "s_waitcnt lgkmcnt(2)\n\t" \
    "v_mfma_f32_16x16x32_bf16 v[216:219], v[4:7], v[228:231], v[216:219]\n\t" \
    "ds_read_b128 v[240:243], v253 offset:26624\n\t" \
    "v_mfma_f32_16x16x32_bf16 v[220:223], v[8:11], v[224:227], v[220:223]\n\t" \
    "v_mfma_f32_16x16x32_bf16 v[220:223], v[12:15], v[228:231], v[220:223]\n\t" \
    "v_mfma_f32_16x16x32_bf16 v[212:215], v[244:247], v[224:227], v[212:215]\n\t" \
    "ds_read_b128 v[244:247], v254 offset:43008\n\t" \
    "v_mfma_f32_16x16x32_bf16 v[212:215], v[248:251], v[228:231], v[212:215]\n\t" \
    "ds_read_b128 v[248:251], v254 offset:44032\n\t" \
    "s_waitcnt lgkmcnt(4)\n\t" \
    "v_mfma_f32_16x16x32_bf16 v[204:207], v[40:43], v[232:235], v[204:207]\n\t" \
    "ds_read_b128 v[224:227], v253 offset:27648\n\t" \
    "s_waitcnt lgkmcnt(4)\n\t" \
    "v_mfma_f32_16x16x32_bf16 v[204:207], v[44:47], v[236:239], v[204:207]\n\t" \
    "ds_read_b128 v[228:231], v253 offset:8192\n\t" \
    "v_mfma_f32_16x16x32_bf16 v[208:211], v[48:51], v[232:235], v[208:211]\n\t" \
    "v_mfma_f32_16x16x32_bf16 v[208:211], v[52:55], v[236:239], v[208:211]\n\t" \
    "s_waitcnt lgkmcnt(3)\n\t" \
    "v_mfma_f32_16x16x32_bf16 v[200:203], v[244:247], v[232:235], v[200:203]\n\t" \
    "s_waitcnt lgkmcnt(2)\n\t" \
    "v_mfma_f32_16x16x32_bf16 v[200:203], v[248:251], v[236:239], v[200:203]\n\t" \
    "v_mfma_f32_16x16x32_bf16 v[216:219], v[40:43], v[240:243], v[216:219]\n\t" \
    "ds_read_b128 v[232:235], v253 offset:9216\n\t" \
    "s_waitcnt lgkmcnt(2)\n\t" \
    "v_mfma_f32_16x16x32_bf16 v[216:219], v[44:47], v[224:227], v[216:219]\n\t" \
    "ds_read_b128 v[236:239], v253 offset:28672\n\t" \
    "v_mfma_f32_16x16x32_bf16 v[220:223], v[48:51], v[240:243], v[220:223]\n\t" \
    "v_mfma_f32_16x16x32_bf16 v[220:223], v[52:55], v[224:227], v[220:223]\n\t" \
    "v_mfma_f32_16x16x32_bf16 v[212:215], v[244:247], v[240:243], v[212:215]\n\t" \
    "ds_read_b128 v[244:247], v254 offset:45056\n\t" \
    "v_mfma_f32_16x16x32_bf16 v[212:215], v[248:251], v[224:227], v[212:215]\n\t" \
    "ds_read_b128 v[248:251], v254 offset:46080\n\t" \
    "s_waitcnt lgkmcnt(4)\n\t" \
    "v_mfma_f32_16x16x32_bf16 v[204:207], v[144:147], v[228:231], v[204:207]\n\t" \
    "ds_read_b128 v[240:243], v253 offset:29696\n\t" \
    "s_waitcnt lgkmcnt(4)\n\t" \
    "v_mfma_f32_16x16x32_bf16 v[204:207], v[148:151], v[232:235], v[204:207]\n\t" \
    "ds_read_b128 v[224:227], v253 offset:10240\n\t" \
    "v_mfma_f32_16x16x32_bf16 v[208:211], v[152:155], v[228:231], v[208:211]\n\t" \
    "v_mfma_f32_16x16x32_bf16 v[208:211], v[156:159], v[232:235], v[208:211]\n\t" \
    "v_mfma_f32_16x16x32_bf16 v[200:203], v[136:139], v[228:231], v[200:203]\n\t" \
    "v_mfma_f32_16x16x32_bf16 v[200:203], v[140:143], v[232:235], v[200:203]\n\t" \
    "s_waitcnt lgkmcnt(4)\n\t" \
    "v_mfma_f32_16x16x32_bf16 v[216:219], v[144:147], v[236:239], v[216:219]\n\t" \
    "ds_read_b128 v[228:231], v253 offset:11264\n\t" \
    "s_waitcnt lgkmcnt(2)\n\t" \
    "v_mfma_f32_16x16x32_bf16 v[216:219], v[148:151], v[240:243], v[216:219]\n\t" \
    "ds_read_b128 v[232:235], v253 offset:30720\n\t" \
    "v_mfma_f32_16x16x32_bf16 v[220:223], v[152:155], v[236:239], v[220:223]\n\t" \
    "v_mfma_f32_16x16x32_bf16 v[220:223], v[156:159], v[240:243], v[220:223]\n\t" \
    "v_mfma_f32_16x16x32_bf16 v[212:215], v[136:139], v[236:239], v[212:215]\n\t" \
    "v_mfma_f32_16x16x32_bf16 v[212:215], v[140:143], v[240:243], v[212:215]\n\t" \
    "ds_read_b128 v[236:239], v253 offset:31744\n\t" \
    "s_setprio 2\n\t" \
    "s_waitcnt lgkmcnt(0)\n\t" \
    "s_barrier \n\t" \
    "v_mfma_f32_16x16x32_bf16 v[204:207], v[80:83], v[224:227], v[204:207]\n\t" \
    "v_mfma_f32_16x16x32_bf16 v[204:207], v[84:87], v[228:231], v[204:207]\n\t" \
    "ds_read_b128 v[240:243], v253 offset:14336\n\t" \
    "v_mfma_f32_16x16x32_bf16 v[208:211], v[88:91], v[224:227], v[208:211]\n\t" \
    "v_mfma_f32_16x16x32_bf16 v[208:211], v[92:95], v[228:231], v[208:211]\n\t" \
    "v_mfma_f32_16x16x32_bf16 v[200:203], v[244:247], v[224:227], v[200:203]\n\t" \
    "v_mfma_f32_16x16x32_bf16 v[200:203], v[248:251], v[228:231], v[200:203]\n\t" \
    "v_mfma_f32_16x16x32_bf16 v[216:219], v[80:83], v[232:235], v[216:219]\n\t" \
    "ds_read_b128 v[224:227], v253 offset:15360\n\t" \
    "s_and_b32 s44, s40, 3\n\t" \
    "v_mfma_f32_16x16x32_bf16 v[216:219], v[84:87], v[236:239], v[216:219]\n\t" \
    "ds_read_b128 v[228:231], v253 offset:34816\n\t" \
    "s_lshl_b32 s44, s44, 22\n\t" \
    "v_mfma_f32_16x16x32_bf16 v[220:223], v[88:91], v[232:235], v[220:223]\n\t" \
    "s_add_u32 s44, s44, %[mb]\n\t" \
    "v_mfma_f32_16x16x32_bf16 v[220:223], v[92:95], v[236:239], v[220:223]\n\t" \
    "s_add_u32 s46, s44, 0x800\n\t" \
    "v_mfma_f32_16x16x32_bf16 v[212:215], v[244:247], v[232:235], v[212:215]\n\t" \
    "ds_read_b128 v[244:247], v254 offset:47104\n\t" \
    "s_add_u32 s47, s44, 0x8800\n\t" \
    "v_mfma_f32_16x16x32_bf16 v[212:215], v[248:251], v[236:239], v[212:215]\n\t" \
    "ds_read_b128 v[248:251], v254 offset:48128\n\t" \
    "s_add_u32 s48, s44, 0xc00\n\t" \
    "s_waitcnt lgkmcnt(4)\n\t" \
    "v_mfma_f32_16x16x32_bf16 v[204:207], v[120:123], v[240:243], v[204:207]\n\t" \
    "ds_read_b128 v[232:235], v253 offset:35840\n\t" \
    "s_waitcnt lgkmcnt(4)\n\t" \
    "v_mfma_f32_16x16x32_bf16 v[204:207], v[124:127], v[224:227], v[204:207]\n\t" \
    "ds_read_b128 v[236:239], v253 offset:16384\n\t" \
    "v_mfma_f32_16x16x32_bf16 v[208:211], v[128:131], v[240:243], v[208:211]\n\t" \
    "v_mfma_f32_16x16x32_bf16 v[208:211], v[132:135], v[224:227], v[208:211]\n\t" \
    "s_waitcnt lgkmcnt(3)\n\t" \
    "v_mfma_f32_16x16x32_bf16 v[200:203], v[244:247], v[240:243], v[200:203]\n\t" \
    "s_waitcnt lgkmcnt(2)\n\t" \
    "v_mfma_f32_16x16x32_bf16 v[200:203], v[248:251], v[224:227], v[200:203]\n\t" \
    "v_mfma_f32_16x16x32_bf16 v[216:219], v[120:123], v[228:231], v[216:219]\n\t" \
    "ds_read_b128 v[240:243], v253 offset:17408\n\t" \
    "s_waitcnt lgkmcnt(2)\n\t" \
    "v_mfma_f32_16x16x32_bf16 v[216:219], v[124:127], v[232:235], v[216:219]\n\t" \
    "ds_read_b128 v[224:227], v253 offset:36864\n\t" \
    "v_mfma_f32_16x16x32_bf16 v[220:223], v[128:131], v[228:231], v[220:223]\n\t" \
    "v_mfma_f32_16x16x32_bf16 v[220:223], v[132:135], v[232:235], v[220:223]\n\t" \
    "v_mfma_f32_16x16x32_bf16 v[212:215], v[244:247], v[228:231], v[212:215]\n\t" \
    "ds_read_b128 v[244:247], v254 offset:49152\n\t" \
    "v_mfma_f32_16x16x32_bf16 v[212:215], v[248:251], v[232:235], v[212:215]\n\t" \
    "ds_read_b128 v[248:251], v254 offset:50176\n\t" \
    "s_waitcnt lgkmcnt(4)\n\t" \
    "v_mfma_f32_16x16x32_bf16 v[204:207], v[160:163], v[236:239], v[204:207]\n\t" \
    "ds_read_b128 v[228:231], v253 offset:37888\n\t" \
    "s_waitcnt lgkmcnt(4)\n\t" \
    "v_mfma_f32_16x16x32_bf16 v[204:207], v[164:167], v[240:243], v[204:207]\n\t" \
    "ds_read_b128 v[232:235], v253 offset:12288\n\t" \
    "v_mfma_f32_16x16x32_bf16 v[208:211], v[168:171], v[236:239], v[208:211]\n\t" \
    "v_mfma_f32_16x16x32_bf16 v[208:211], v[172:175], v[240:243], v[208:211]\n\t" \
    "s_waitcnt lgkmcnt(3)\n\t" \
    "v_mfma_f32_16x16x32_bf16 v[200:203], v[244:247], v[236:239], v[200:203]\n\t" \
    "s_waitcnt lgkmcnt(2)\n\t" \
    "v_mfma_f32_16x16x32_bf16 v[200:203], v[248:251], v[240:243], v[200:203]\n\t" \
    "v_mfma_f32_16x16x32_bf16 v[216:219], v[160:163], v[224:227], v[216:219]\n\t" \
    "ds_read_b128 v[236:239], v253 offset:13312\n\t" \
    "s_waitcnt lgkmcnt(2)\n\t" \
    "v_mfma_f32_16x16x32_bf16 v[216:219], v[164:167], v[228:231], v[216:219]\n\t" \
    "ds_read_b128 v[240:243], v253 offset:18432\n\t" \
    "v_mfma_f32_16x16x32_bf16 v[220:223], v[168:171], v[224:227], v[220:223]\n\t" \
    "v_mfma_f32_16x16x32_bf16 v[220:223], v[172:175], v[228:231], v[220:223]\n\t" \
    "v_mfma_f32_16x16x32_bf16 v[212:215], v[244:247], v[224:227], v[212:215]\n\t" \
    "v_mfma_f32_16x16x32_bf16 v[212:215], v[248:251], v[228:231], v[212:215]\n\t" \
    "s_waitcnt lgkmcnt(2)\n\t" \
    "v_mfma_f32_16x16x32_bf16 v[204:207], v[24:27], v[232:235], v[204:207]\n\t" \
    "ds_read_b128 v[224:227], v253 offset:19456\n\t" \
    "v_mov_b64 v[244:245], 0\n\t" \
    "s_waitcnt lgkmcnt(2)\n\t" \
    "v_mfma_f32_16x16x32_bf16 v[204:207], v[28:31], v[236:239], v[204:207]\n\t" \
    "ds_read_b128 v[228:231], v253 offset:32768\n\t" \
    "v_mov_b64 v[246:247], 0\n\t" \
    "v_mfma_f32_16x16x32_bf16 v[208:211], v[32:35], v[232:235], v[208:211]\n\t" \
    "v_mov_b64 v[248:249], 0\n\t" \
    "v_mfma_f32_16x16x32_bf16 v[208:211], v[36:39], v[236:239], v[208:211]\n\t" \
    "v_mov_b64 v[250:251], 0\n\t" \
    "v_mfma_f32_16x16x32_bf16 v[200:203], v[16:19], v[232:235], v[200:203]\n\t" \
    "v_mfma_f32_16x16x32_bf16 v[200:203], v[20:23], v[236:239], v[200:203]\n\t" \
    "s_waitcnt lgkmcnt(2)\n\t" \
    "v_mfma_f32_16x16x32_bf16 v[204:207], v[184:187], v[240:243], v[204:207]\n\t" \
    "ds_read_b128 v[232:235], v253 offset:33792\n\t" \
    "s_waitcnt lgkmcnt(2)\n\t" \
    "v_mfma_f32_16x16x32_bf16 v[204:207], v[188:191], v[224:227], v[204:207]\n\t" \
    "ds_read_b128 v[236:239], v253 offset:38912\n\t" \
    "v_mfma_f32_16x16x32_bf16 v[208:211], v[192:195], v[240:243], v[208:211]\n\t" \
    "v_mfma_f32_16x16x32_bf16 v[208:211], v[196:199], v[224:227], v[208:211]\n\t" \
    "v_mfma_f32_16x16x32_bf16 v[200:203], v[176:179], v[240:243], v[200:203]\n\t" \
    "v_mfma_f32_16x16x32_bf16 v[200:203], v[180:183], v[224:227], v[200:203]\n\t" \
    "ds_read_b128 v[240:243], v253 offset:39936\n\t" \
    "s_waitcnt lgkmcnt(3)\n\t" \
    "v_mfma_f32_16x16x32_bf16 v[216:219], v[24:27], v[228:231], v[216:219]\n\t" \
    "s_waitcnt lgkmcnt(2)\n\t" \
    "v_mfma_f32_16x16x32_bf16 v[216:219], v[28:31], v[232:235], v[216:219]\n\t" \
    "v_cvt_pk_bf16_f32 v204, v204, v208\n\t" \
    "v_cvt_pk_bf16_f32 v205, v205, v209\n\t" \
    "s_waitcnt lgkmcnt(1)\n\t" \
    "v_mfma_f32_16x16x32_bf16 v[216:219], v[184:187], v[236:239], v[216:219]\n\t" \
    "v_cvt_pk_bf16_f32 v206, v206, v210\n\t" \
    "v_cvt_pk_bf16_f32 v207, v207, v211\n\t" \
    "s_waitcnt lgkmcnt(0)\n\t" \
    "v_mfma_f32_16x16x32_bf16 v[216:219], v[188:191], v[240:243], v[216:219]\n\t" \
    "ds_write_b128 v254, v[204:207] offset:9216\n\t" \
    "v_mfma_f32_16x16x32_bf16 v[220:223], v[32:35], v[228:231], v[220:223]\n\t" \
    "v_mfma_f32_16x16x32_bf16 v[220:223], v[36:39], v[232:235], v[220:223]\n\t" \
    "v_cvt_pk_bf16_f32 v200, v200, v201\n\t" \
    "v_cvt_pk_bf16_f32 v201, v202, v203\n\t" \
    "v_mfma_f32_16x16x32_bf16 v[220:223], v[192:195], v[236:239], v[220:223]\n\t" \
    "ds_write_b64 v254, v[200:201] offset:7168\n\t" \
    "v_mfma_f32_16x16x32_bf16 v[220:223], v[196:199], v[240:243], v[220:223]\n\t" \
    "v_mfma_f32_16x16x32_bf16 v[212:215], v[16:19], v[228:231], v[212:215]\n\t" \
    "ds_read_b128 v[200:203], v254 offset:40960\n\t" \
    "ds_read_b128 v[204:207], v254 offset:41984\n\t" \
    "v_mfma_f32_16x16x32_bf16 v[212:215], v[20:23], v[232:235], v[212:215]\n\t" \
    "v_mov_b64 v[208:209], 0\n\t" \
    "v_mov_b64 v[210:211], 0\n\t" \
    "v_mfma_f32_16x16x32_bf16 v[212:215], v[176:179], v[236:239], v[212:215]\n\t" \
    "v_cvt_pk_bf16_f32 v216, v216, v220\n\t" \
    "v_cvt_pk_bf16_f32 v217, v217, v221\n\t" \
    "v_mfma_f32_16x16x32_bf16 v[212:215], v[180:183], v[240:243], v[212:215]\n\t" \
    "v_cvt_pk_bf16_f32 v218, v218, v222\n\t" \
    "v_cvt_pk_bf16_f32 v219, v219, v223\n\t" \
    "s_setprio 0\n\t" \
    "ds_write_b128 v254, v[216:219] offset:15360\n\t" \
    "ds_read_b128 v[216:219], v254 offset:43008\n\t" \
    "ds_read_b128 v[220:223], v254 offset:44032\n\t" \
    "s_nop 2\n\t" \
    "v_cvt_pk_bf16_f32 v212, v212, v213\n\t" \
    "v_cvt_pk_bf16_f32 v213, v214, v215\n\t" \
    "ds_write_b64 v254, v[212:213] offset:13312\n\t" \
    "s_sleep 3\n\t" \
    "s_mov_b32 m0, 0xa800\n\t" \
    "s_nop 0\n\t" \
    "buffer_load_dwordx4 v253, s[60:63], s46 offen sc1 lds\n\t" \
    "s_mov_b32 m0, 0xb800\n\t" \
    "s_nop 0\n\t" \
    "buffer_load_dwordx4 v253, s[60:63], s47 offen sc1 lds\n\t" \
    "s_mov_b32 m0, 0xac00\n\t" \
    "s_nop 0\n\t" \
    "buffer_load_dwordx4 v253, s[60:63], s48 offen sc1 lds\n\t" \
    "s_waitcnt lgkmcnt(0)\n\t" \
    "v_bfe_i32 v212, v253, 5, 1\n\t" \
    "v_and_b32 v212, 0xaaaa, v212\n\t" \
    "v_or_b32 v212, 0x4444, v212\n\t" \
    "v_and_b32 v213, 0xffff, v255\n\t" \
    "v_lshrrev_b32 v214, 16, v255\n\t" \
    ".Ls14_%=:\n\t" \
    "s_waitcnt vmcnt(0)\n\t" \
    "ds_read_b128 v[224:227], v253 offset:43008\n\t" \
    "ds_read_b128 v[228:231], v253 offset:47104\n\t" \
    "ds_read_b128 v[232:235], v253 offset:44032\n\t" \
    "s_waitcnt lgkmcnt(2)\n\t" \
    "v_max_u32 v215, v224, v225\n\t" \
    "v_max3_u32 v215, v226, v227, v215\n\t" \
    "s_waitcnt lgkmcnt(1)\n\t" \
    "v_max3_u32 v215, v228, v229, v215\n\t" \
    "v_max3_u32 v215, v230, v231, v215\n\t" \
    "s_waitcnt lgkmcnt(0)\n\t" \
    "v_max3_u32 v215, v232, v233, v215\n\t" \
    "v_max3_u32 v215, v234, v235, v215\n\t" \
    "v_cmp_eq_u32 vcc, -1, v215\n\t" \
    "s_cbranch_vccz .Ls15_%=\n\t" \
    "v_max_u32 v215, v224, v225\n\t" \
    "v_max3_u32 v215, v226, v227, v215\n\t" \
    "v_cmp_eq_u32 vcc, -1, v215\n\t" \
    "s_cbranch_vccz .Ls16_%=\n\t" \
    "s_mov_b32 m0, 0xa800\n\t" \
    "s_nop 0\n\t" \
    "buffer_load_dwordx4 v253, s[60:63], s46 offen sc1 lds\n\t" \
    ".Ls16_%=:\n\t" \
    "v_max_u32 v215, v228, v229\n\t" \
    "v_max3_u32 v215, v230, v231, v215\n\t" \
    "v_cmp_eq_u32 vcc, -1, v215\n\t" \
    "s_cbranch_vccz .Ls17_%=\n\t" \
    "s_mov_b32 m0, 0xb800\n\t" \
    "s_nop 0\n\t" \
    "buffer_load_dwordx4 v253, s[60:63], s47 offen sc1 lds\n\t" \
    ".Ls17_%=:\n\t" \
    "v_max_u32 v215, v232, v233\n\t" \
    "v_max3_u32 v215, v234, v235, v215\n\t" \
    "v_cmp_eq_u32 vcc, -1, v215\n\t" \
    "s_cbranch_vccz .Ls18_%=\n\t" \
    "s_mov_b32 m0, 0xac00\n\t" \
    "s_nop 0\n\t" \
    "buffer_load_dwordx4 v253, s[60:63], s48 offen sc1 lds\n\t" \
    ".Ls18_%=:\n\t" \
    "s_cmp_eq_u32 s41, 0\n\t" \
    "s_cbranch_scc1 .Ls15_%=\n\t" \
    "s_sub_u32 s41, s41, 1\n\t" \
    "s_branch .Ls14_%=\n\t" \
    ".Ls15_%=:\n\t" \
    "s_setprio 3\n\t" \
    "s_waitcnt lgkmcnt(0)\n\t" \
    "s_barrier \n\t" \
    "ds_read_b128 v[236:239], v253 offset:40960\n\t" \
    "ds_read_b128 v[240:243], v253 offset:46080\n\t" \
    "v_smfmac_f32_16x16x64_bf16 v[244:247], v[224:227], v[0:7], v212\n\t" \
    "v_smfmac_f32_16x16x64_bf16 v[248:251], v[224:227], v[8:15], v212\n\t" \
    "v_smfmac_f32_16x16x64_bf16 v[208:211], v[224:227], v[200:207], v212\n\t" \
    "ds_read_b128 v[200:203], v254 offset:45056\n\t" \
    "ds_read_b128 v[204:207], v254 offset:46080\n\t" \
    "ds_read_b128 v[224:227], v253 offset:41984\n\t" \
    "v_smfmac_f32_16x16x64_bf16 v[208:211], v[228:231], v[16:23], v212\n\t" \
    "v_smfmac_f32_16x16x64_bf16 v[244:247], v[228:231], v[24:31], v212\n\t" \
    "v_smfmac_f32_16x16x64_bf16 v[248:251], v[228:231], v[32:39], v212\n\t" \
    "ds_read_b128 v[228:231], v253 offset:48128\n\t" \
    "v_smfmac_f32_16x16x64_bf16 v[208:211], v[232:235], v[216:223], v212\n\t" \
    "ds_read_b128 v[216:219], v254 offset:47104\n\t" \
    "ds_read_b128 v[220:223], v254 offset:48128\n\t" \
    "v_smfmac_f32_16x16x64_bf16 v[244:247], v[232:235], v[40:47], v212\n\t" \
    "v_smfmac_f32_16x16x64_bf16 v[248:251], v[232:235], v[48:55], v212\n\t" \
    "ds_read_b128 v[232:235], v253 offset:45056\n\t" \
    "s_waitcnt lgkmcnt(8)\n\t" \
    "v_smfmac_f32_16x16x64_bf16 v[208:211], v[236:239], v[56:63], v212\n\t" \
    "v_smfmac_f32_16x16x64_bf16 v[244:247], v[236:239], v[64:71], v212\n\t" \
    "v_smfmac_f32_16x16x64_bf16 v[248:251], v[236:239], v[72:79], v212\n\t" \
    "ds_read_b128 v[236:239], v253 offset:49152\n\t" \
    "s_waitcnt lgkmcnt(6)\n\t" \
    "v_smfmac_f32_16x16x64_bf16 v[208:211], v[240:243], v[200:207], v212\n\t" \
    "ds_read_b128 v[200:203], v254 offset:49152\n\t" \
    "ds_read_b128 v[204:207], v254 offset:50176\n\t" \
    "v_smfmac_f32_16x16x64_bf16 v[244:247], v[240:243], v[80:87], v212\n\t" \
    "v_smfmac_f32_16x16x64_bf16 v[248:251], v[240:243], v[88:95], v212\n\t" \
    "ds_read_b128 v[240:243], v253 offset:50176\n\t" \
    "s_waitcnt lgkmcnt(8)\n\t" \
    "v_smfmac_f32_16x16x64_bf16 v[208:211], v[224:227], v[96:103], v212\n\t" \
    "v_smfmac_f32_16x16x64_bf16 v[244:247], v[224:227], v[104:111], v212\n\t" \
    "v_smfmac_f32_16x16x64_bf16 v[248:251], v[224:227], v[112:119], v212\n\t" \
    "s_waitcnt lgkmcnt(5)\n\t" \
    "v_smfmac_f32_16x16x64_bf16 v[208:211], v[228:231], v[216:223], v212\n\t" \
    "s_waitcnt lgkmcnt(4)\n\t" \
    "v_smfmac_f32_16x16x64_bf16 v[208:211], v[232:235], v[136:143], v212\n\t" \
    "s_waitcnt lgkmcnt(1)\n\t" \
    "v_smfmac_f32_16x16x64_bf16 v[208:211], v[236:239], v[200:207], v212\n\t" \
    "s_waitcnt lgkmcnt(0)\n\t" \
    "v_smfmac_f32_16x16x64_bf16 v[208:211], v[240:243], v[176:183], v212\n\t" \
    "v_smfmac_f32_16x16x64_bf16 v[244:247], v[228:231], v[120:127], v212\n\t" \
    "v_smfmac_f32_16x16x64_bf16 v[244:247], v[232:235], v[144:151], v212\n\t" \
    "v_smfmac_f32_16x16x64_bf16 v[244:247], v[236:239], v[160:167], v212\n\t" \
    "v_add_f32 v208, v208, v210\n\t" \
    "v_add_f32 v209, v209, v211\n\t" \
    "v_cvt_pk_bf16_f32 v208, v208, v209\n\t" \
    "v_smfmac_f32_16x16x64_bf16 v[244:247], v[240:243], v[184:191], v212\n\t" \
    "ds_write_b16 v213, v208 offset:0\n\t" \
    "ds_write_b16_d16_hi v213, v208 offset:16\n\t" \
    "v_smfmac_f32_16x16x64_bf16 v[248:251], v[228:231], v[128:135], v212\n\t" \
    "v_smfmac_f32_16x16x64_bf16 v[248:251], v[232:235], v[152:159], v212\n\t" \
    "v_smfmac_f32_16x16x64_bf16 v[248:251], v[236:239], v[168:175], v212\n\t" \
    "v_add_f32 v244, v244, v246\n\t" \
    "v_add_f32 v245, v245, v247\n\t" \
    "v_smfmac_f32_16x16x64_bf16 v[248:251], v[240:243], v[192:199], v212\n\t" \
    "s_nop 7\n\t" \
    "v_add_f32 v248, v248, v250\n\t" \
    "v_add_f32 v249, v249, v251\n\t" \
    "v_cvt_pk_bf16_f32 v244, v244, v248\n\t" \
    "v_cvt_pk_bf16_f32 v245, v245, v249\n\t" \
    "ds_write2_b32 v214, v244, v245 offset1:4\n\t" \
    "s_setprio 0\n\t" \
    "s_sleep 1\n\t" \
    "ds_read_b128 v[200:203], v254 offset:6144\n\t" \
    "ds_read_b128 v[224:227], v254 offset:18432\n\t" \
    "ds_read_b128 v[204:207], v254 offset:7168\n\t" \
    "ds_read_b128 v[228:231], v254 offset:19456\n\t" \
    "ds_read_b128 v[208:211], v254 offset:8192\n\t" \
    "ds_read_b128 v[232:235], v254 offset:20480\n\t" \
    "ds_read_b128 v[212:215], v254 offset:9216\n\t" \
    "ds_read_b128 v[236:239], v254 offset:21504\n\t" \
    "ds_read_b128 v[216:219], v254 offset:10240\n\t" \
    "ds_read_b128 v[220:223], v254 offset:11264\n\t" \
    "s_and_b32 s44, s40, 3\n\t" \
    "s_lshl_b32 s44, s44, 17\n\t" \
    "s_add_u32 s50, s44, %[pb]\n\t" \
    "s_add_u32 s51, s44, %[pm]\n\t" \
    "s_mov_b32 s52, 0xf0000\n\t" \
    "s_mov_b32 s53, 0xfff00ff\n\t" \
    "s_mov_b32 s54, 0x1f0001\n\t" \
    "s_mov_b32 s55, 0x1fff01ff\n\t" \
    "s_mov_b32 s56, 0x3f0003\n\t" \
    "s_mov_b32 s57, 0x3fff03ff\n\t" \
    "s_mov_b32 s58, 0x7f0007\n\t" \
    "s_mov_b32 s59, 0x7fff07ff\n\t" \
    "s_waitcnt lgkmcnt(8)\n\t" \
    "v_mfma_f32_16x16x32_bf16 v[240:243], v[224:227], v[200:203], 0\n\t" \
    "ds_read_b128 v[224:227], v254 offset:22528\n\t" \
    "s_waitcnt lgkmcnt(7)\n\t" \
    "v_mfma_f32_16x16x32_bf16 v[240:243], v[228:231], v[204:207], v[240:243]\n\t" \
    "ds_read_b128 v[228:231], v254 offset:23552\n\t" \
    "s_waitcnt lgkmcnt(6)\n\t" \
    "v_mfma_f32_16x16x32_bf16 v[240:243], v[232:235], v[208:211], v[240:243]\n\t" \
    "s_waitcnt lgkmcnt(4)\n\t" \
    "v_mfma_f32_16x16x32_bf16 v[240:243], v[236:239], v[212:215], v[240:243]\n\t" \
    "v_and_b32 v252, 0xffffff7f, v254\n\t" \
    "s_waitcnt lgkmcnt(0)\n\t" \
    "s_barrier \n\t" \
    "ds_read_b128 v[232:235], v252 offset:3072\n\t" \
    "ds_read_b128 v[236:239], v252 offset:3200\n\t" \
    "v_mfma_f32_16x16x32_bf16 v[240:243], v[224:227], v[216:219], v[240:243]\n\t" \
    "ds_read_b128 v[224:227], v252 offset:4096\n\t" \
    "v_mfma_f32_16x16x32_bf16 v[240:243], v[228:231], v[220:223], v[240:243]\n\t" \
    "ds_read_b128 v[228:231], v252 offset:4224\n\t" \
    "s_waitcnt lgkmcnt(3)\n\t" \
    "v_mfma_f32_16x16x32_bf16 v[248:251], v[232:235], v[200:203], 0\n\t" \
    "ds_read_b128 v[232:235], v252 offset:5120\n\t" \
    "s_waitcnt lgkmcnt(3)\n\t" \
    "v_mfma_f32_16x16x32_bf16 v[248:251], v[236:239], v[204:207], v[248:251]\n\t" \
    "ds_read_b128 v[236:239], v252 offset:5248\n\t" \
    "s_waitcnt lgkmcnt(3)\n\t" \
    "v_mfma_f32_16x16x32_bf16 v[248:251], v[224:227], v[208:211], v[248:251]\n\t" \
    "s_waitcnt lgkmcnt(2)\n\t" \
    "v_mfma_f32_16x16x32_bf16 v[248:251], v[228:231], v[212:215], v[248:251]\n\t" \
    "s_waitcnt lgkmcnt(1)\n\t" \
    "v_mfma_f32_16x16x32_bf16 v[248:251], v[232:235], v[216:219], v[248:251]\n\t" \
    "s_waitcnt lgkmcnt(0)\n\t" \
    "v_mfma_f32_16x16x32_bf16 v[248:251], v[236:239], v[220:223], v[248:251]\n\t" \
    "v_mov_b32 v226, 0xff7fffff\n\t" \
    "v_cndmask_b32 v240, v240, v226, s[52:53]\n\t" \
    "v_cndmask_b32 v241, v241, v226, s[54:55]\n\t" \
    "v_cndmask_b32 v242, v242, v226, s[56:57]\n\t" \
    "v_cndmask_b32 v243, v243, v226, s[58:59]\n\t" \
    "v_max3_f32 v224, v240, v241, v242\n\t" \
    "v_mov_b32 v225, v243\n\t" \
    "v_lshrrev_b32 v200, 5, v253\n\t" \
    "v_and_b32 v201, 0xf0, v253\n\t" \
    "v_and_or_b32 v200, v200, 8, v201\n\t" \
    "v_max3_f32 v224, v224, v248, v249\n\t" \
    "v_max3_f32 v225, v225, v250, v251\n\t" \
    "v_max_f32 v224, v224, v225\n\t" \
    "v_mov_b32 v225, v224\n\t" \
    "s_nop 1\n\t" \
    "v_permlane16_swap_b32 v224, v225\n\t" \
    "v_max_f32 v224, v224, v225\n\t" \
    "v_mov_b32 v225, v224\n\t" \
    "s_nop 1\n\t" \
    "v_permlane32_swap_b32 v224, v225\n\t" \
    "v_max_f32 v224, v224, v225\n\t" \
    "v_mul_f32 v224, %[scl], v224\n\t" \
    "v_fma_f32 v240, v240, %[scl], -v224\n\t" \
    "v_fma_f32 v241, v241, %[scl], -v224\n\t" \
    "v_exp_f32 v240, v240\n\t" \
    "v_fma_f32 v242, v242, %[scl], -v224\n\t" \
    "v_exp_f32 v241, v241\n\t" \
    "v_fma_f32 v243, v243, %[scl], -v224\n\t" \
    "v_exp_f32 v242, v242\n\t" \
    "v_fma_f32 v248, v248, %[scl], -v224\n\t" \
    "v_exp_f32 v243, v243\n\t" \
    "v_fma_f32 v249, v249, %[scl], -v224\n\t" \
    "v_exp_f32 v248, v248\n\t" \
    "v_fma_f32 v250, v250, %[scl], -v224\n\t" \
    "v_exp_f32 v249, v249\n\t" \
    "v_fma_f32 v251, v251, %[scl], -v224\n\t" \
    "v_exp_f32 v250, v250\n\t" \
    "v_exp_f32 v251, v251\n\t" \
    "v_add_f32 v224, v240, v241\n\t" \
    "v_add_f32 v225, v242, v243\n\t" \
    "v_add_f32 v232, v248, v249\n\t" \
    "v_add_f32 v233, v250, v251\n\t" \
    "v_add_f32 v224, v224, v225\n\t" \
    "v_add_f32 v232, v232, v233\n\t" \
    "v_fma_f32 v224, v232, 0.5, v224\n\t" \
    "v_mov_b32 v225, v224\n\t" \
    "s_nop 1\n\t" \
    "v_permlane16_swap_b32 v224, v225\n\t" \
    "v_add_f32 v224, v224, v225\n\t" \
    "v_mov_b32 v225, v224\n\t" \
    "s_nop 1\n\t" \
    "v_permlane32_swap_b32 v224, v225\n\t" \
    "v_add_f32 v224, v224, v225\n\t" \
    "v_rcp_f32 v224, v224\n\t" \
    "s_nop 0\n\t" \
    "v_mul_f32 v240, v240, v224\n\t" \
    "v_mul_f32 v241, v241, v224\n\t" \
    "v_mul_f32 v242, v242, v224\n\t" \
    "v_mul_f32 v243, v243, v224\n\t" \
    "v_mul_f32 v248, v248, v224\n\t" \
    "v_mul_f32 v249, v249, v224\n\t" \
    "v_mul_f32 v250, v250, v224\n\t" \
    "v_mul_f32 v251, v251, v224\n\t" \
    "v_cvt_pk_bf16_f32 v204, |v240|, |v241|\n\t" \
    "v_cvt_pk_bf16_f32 v205, |v242|, |v243|\n\t" \
    "v_cvt_pk_bf16_f32 v208, |v248|, |v249|\n\t" \
    "v_cvt_pk_bf16_f32 v209, |v250|, |v251|\n\t" \
    "v_permlane16_swap_b32 v204, v206\n\t" \
    "v_permlane16_swap_b32 v205, v207\n\t" \
    "s_mov_b32 exec_lo, 0xffff\n\t" \
    "s_mov_b32 exec_hi, 0xffff\n\t" \
    "buffer_store_dwordx4 v[204:207], v253, s[60:63], s50 offen sc1\n\t" \
    "s_mov_b32 exec_lo, -1\n\t" \
    "s_mov_b32 exec_hi, 0\n\t" \
    "buffer_store_dwordx2 v[208:209], v200, s[60:63], s51 offen sc1\n\t" \
    "s_mov_b32 exec_hi, -1\n\t" \
    "ds_read_b128 v[224:227], v253 offset:0\n\t" \
    "ds_read_b128 v[244:247], v254 offset:40960\n\t" \
    "ds_read_b128 v[228:231], v253 offset:1024\n\t" \
    "ds_read_b128 v[232:235], v253 offset:20480\n\t" \
    "ds_read_b128 v[236:239], v253 offset:21504\n\t" \
    "ds_read_b128 v[248:251], v254 offset:41984\n\t" \
    "s_add_u32 s40, s40, 1\n\t" \
    "s_cmp_lg_u32 s40, 64\n\t" \
    "s_cbranch_scc1 .Ls13_%=\n\t" \
    "s_branch .Lend%=\n\t" \
    ".Ls0_%=:\n\t" \
    "v_add_u32 v244, 0x18000, v243\n\t" \
    "s_waitcnt vmcnt(12) lgkmcnt(0)\n\t" \
    "s_barrier \n\t" \
    "s_add_u32 m0, %[wm], 0x12000\n\t" \
    "s_mov_b32 s44, 0x1e0000\n\t" \
    "buffer_load_dwordx4 v240, %[wr], s44 offen offset:0 nt lds\n\t" \
    "buffer_load_dwordx4 v241, %[wr], s44 offen offset:1024 nt lds\n\t" \
    "buffer_load_dwordx4 v242, %[wr], s44 offen offset:2048 nt lds\n\t" \
    "ds_read_b64_tr_b16 v[200:201], v244 offset:0\n\t" \
    "ds_read_b64_tr_b16 v[202:203], v244 offset:1536\n\t" \
    "ds_read_b64_tr_b16 v[0:1], v244 offset:32\n\t" \
    "ds_read_b64_tr_b16 v[2:3], v244 offset:1568\n\t" \
    "ds_read_b64_tr_b16 v[8:9], v244 offset:64\n\t" \
    "ds_read_b64_tr_b16 v[10:11], v244 offset:1600\n\t" \
    "v_add_u32 v244, 0x1e000, v243\n\t" \
    "s_waitcnt vmcnt(12) lgkmcnt(0)\n\t" \
    "s_barrier \n\t" \
    "s_add_u32 m0, %[wm], 0x18000\n\t" \
    "s_mov_b32 s44, 0x240000\n\t" \
    "buffer_load_dwordx4 v240, %[wr], s44 offen offset:0 nt lds\n\t" \
    "buffer_load_dwordx4 v241, %[wr], s44 offen offset:1024 nt lds\n\t" \
    "buffer_load_dwordx4 v242, %[wr], s44 offen offset:2048 nt lds\n\t" \
    "ds_read_b64_tr_b16 v[204:205], v244 offset:0\n\t" \
    "ds_read_b64_tr_b16 v[206:207], v244 offset:1536\n\t" \
    "ds_read_b64_tr_b16 v[4:5], v244 offset:32\n\t" \
    "ds_read_b64_tr_b16 v[6:7], v244 offset:1568\n\t" \
    "ds_read_b64_tr_b16 v[12:13], v244 offset:64\n\t" \
    "ds_read_b64_tr_b16 v[14:15], v244 offset:1600\n\t" \
    "v_add_u32 v244, 0x0, v243\n\t" \
    "s_waitcnt vmcnt(12) lgkmcnt(0)\n\t" \
    "s_barrier \n\t" \
    "s_add_u32 m0, %[wm], 0x1e000\n\t" \
    "s_mov_b32 s44, 0x2a0000\n\t" \
    "buffer_load_dwordx4 v240, %[wr], s44 offen offset:0 nt lds\n\t" \
    "buffer_load_dwordx4 v241, %[wr], s44 offen offset:1024 nt lds\n\t" \
    "buffer_load_dwordx4 v242, %[wr], s44 offen offset:2048 nt lds\n\t" \
    "ds_read_b64_tr_b16 v[208:209], v244 offset:0\n\t" \
    "ds_read_b64_tr_b16 v[210:211], v244 offset:1536\n\t" \
    "ds_read_b64_tr_b16 v[40:41], v244 offset:32\n\t" \
    "ds_read_b64_tr_b16 v[42:43], v244 offset:1568\n\t" \
    "ds_read_b64_tr_b16 v[48:49], v244 offset:64\n\t" \
    "ds_read_b64_tr_b16 v[50:51], v244 offset:1600\n\t" \
    "v_add_u32 v244, 0x6000, v243\n\t" \
    "s_waitcnt vmcnt(12) lgkmcnt(0)\n\t" \
    "s_barrier \n\t" \
    "s_add_u32 m0, %[wm], 0x0\n\t" \
    "s_mov_b32 s44, 0x300000\n\t" \
    "buffer_load_dwordx4 v240, %[wr], s44 offen offset:0 nt lds\n\t" \
    "buffer_load_dwordx4 v241, %[wr], s44 offen offset:1024 nt lds\n\t" \
    "buffer_load_dwordx4 v242, %[wr], s44 offen offset:2048 nt lds\n\t" \
    "ds_read_b64_tr_b16 v[212:213], v244 offset:0\n\t" \
    "ds_read_b64_tr_b16 v[214:215], v244 offset:1536\n\t" \
    "ds_read_b64_tr_b16 v[44:45], v244 offset:32\n\t" \
    "ds_read_b64_tr_b16 v[46:47], v244 offset:1568\n\t" \
    "ds_read_b64_tr_b16 v[52:53], v244 offset:64\n\t" \
    "ds_read_b64_tr_b16 v[54:55], v244 offset:1600\n\t" \
    "v_add_u32 v244, 0xc000, v243\n\t" \
    "s_waitcnt vmcnt(12) lgkmcnt(0)\n\t" \
    "s_barrier \n\t" \
    "s_add_u32 m0, %[wm], 0x6000\n\t" \
    "s_mov_b32 s44, 0x360000\n\t" \
    "buffer_load_dwordx4 v240, %[wr], s44 offen offset:0 nt lds\n\t" \
    "buffer_load_dwordx4 v241, %[wr], s44 offen offset:1024 nt lds\n\t" \
    "buffer_load_dwordx4 v242, %[wr], s44 offen offset:2048 nt lds\n\t" \
    "ds_read_b64_tr_b16 v[216:217], v244 offset:0\n\t" \
    "ds_read_b64_tr_b16 v[218:219], v244 offset:1536\n\t" \
    "ds_read_b64_tr_b16 v[80:81], v244 offset:32\n\t" \
    "ds_read_b64_tr_b16 v[82:83], v244 offset:1568\n\t" \
    "ds_read_b64_tr_b16 v[88:89], v244 offset:64\n\t" \
    "ds_read_b64_tr_b16 v[90:91], v244 offset:1600\n\t" \
    "v_add_u32 v244, 0x12000, v243\n\t" \
    "s_waitcnt vmcnt(12) lgkmcnt(0)\n\t" \
    "s_barrier \n\t" \
    "s_add_u32 m0, %[wm], 0xc000\n\t" \
    "s_mov_b32 s44, 0x3c0000\n\t" \
    "buffer_load_dwordx4 v240, %[wr], s44 offen offset:0 nt lds\n\t" \
    "buffer_load_dwordx4 v241, %[wr], s44 offen offset:1024 nt lds\n\t" \
    "buffer_load_dwordx4 v242, %[wr], s44 offen offset:2048 nt lds\n\t" \
    "ds_read_b64_tr_b16 v[220:221], v244 offset:0\n\t" \
    "ds_read_b64_tr_b16 v[222:223], v244 offset:1536\n\t" \
    "ds_read_b64_tr_b16 v[84:85], v244 offset:32\n\t" \
    "ds_read_b64_tr_b16 v[86:87], v244 offset:1568\n\t" \
    "ds_read_b64_tr_b16 v[92:93], v244 offset:64\n\t" \
    "ds_read_b64_tr_b16 v[94:95], v244 offset:1600\n\t" \
    "v_add_u32 v244, 0x18000, v243\n\t" \
    "s_waitcnt vmcnt(12) lgkmcnt(0)\n\t" \
    "s_barrier \n\t" \
    "s_add_u32 m0, %[wm], 0x12000\n\t" \
    "s_mov_b32 s44, 0x420000\n\t" \
    "buffer_load_dwordx4 v240, %[wr], s44 offen offset:0 nt lds\n\t" \
    "buffer_load_dwordx4 v241, %[wr], s44 offen offset:1024 nt lds\n\t" \
    "buffer_load_dwordx4 v242, %[wr], s44 offen offset:2048 nt lds\n\t" \
    "ds_read_b64_tr_b16 v[224:225], v244 offset:0\n\t" \
    "ds_read_b64_tr_b16 v[226:227], v244 offset:1536\n\t" \
    "ds_read_b64_tr_b16 v[120:121], v244 offset:32\n\t" \
    "ds_read_b64_tr_b16 v[122:123], v244 offset:1568\n\t" \
    "ds_read_b64_tr_b16 v[128:129], v244 offset:64\n\t" \
    "ds_read_b64_tr_b16 v[130:131], v244 offset:1600\n\t" \
    "v_add_u32 v244, 0x1e000, v243\n\t" \
    "s_waitcnt vmcnt(12) lgkmcnt(0)\n\t" \
    "s_barrier \n\t" \
    "s_add_u32 m0, %[wm], 0x18000\n\t" \
    "s_mov_b32 s44, 0x480000\n\t" \
    "buffer_load_dwordx4 v240, %[wr], s44 offen offset:0 nt lds\n\t" \
    "buffer_load_dwordx4 v241, %[wr], s44 offen offset:1024 nt lds\n\t" \
    "buffer_load_dwordx4 v242, %[wr], s44 offen offset:2048 nt lds\n\t" \
    "ds_read_b64_tr_b16 v[228:229], v244 offset:0\n\t" \
    "ds_read_b64_tr_b16 v[230:231], v244 offset:1536\n\t" \
    "ds_read_b64_tr_b16 v[124:125], v244 offset:32\n\t" \
    "ds_read_b64_tr_b16 v[126:127], v244 offset:1568\n\t" \
    "ds_read_b64_tr_b16 v[132:133], v244 offset:64\n\t" \
    "ds_read_b64_tr_b16 v[134:135], v244 offset:1600\n\t" \
    "v_add_u32 v244, 0x0, v243\n\t" \
    "s_waitcnt vmcnt(12) lgkmcnt(0)\n\t" \
    "s_barrier \n\t" \
    "v_mov_b32 v246, 0\n\t" \
    "buffer_load_dword v246, v246, s[60:63], %[mo] offen sc1\n\t" \
    "s_add_u32 m0, %[wm], 0x1e000\n\t" \
    "s_mov_b32 s44, 0x4e0000\n\t" \
    "buffer_load_dwordx4 v240, %[wr], s44 offen offset:0 nt lds\n\t" \
    "buffer_load_dwordx4 v241, %[wr], s44 offen offset:1024 nt lds\n\t" \
    "buffer_load_dwordx4 v242, %[wr], s44 offen offset:2048 nt lds\n\t" \
    "ds_read_b64_tr_b16 v[232:233], v244 offset:0\n\t" \
    "ds_read_b64_tr_b16 v[234:235], v244 offset:1536\n\t" \
    "ds_read_b64_tr_b16 v[160:161], v244 offset:32\n\t" \
    "ds_read_b64_tr_b16 v[162:163], v244 offset:1568\n\t" \
    "ds_read_b64_tr_b16 v[168:169], v244 offset:64\n\t" \
    "ds_read_b64_tr_b16 v[170:171], v244 offset:1600\n\t" \
    "v_add_u32 v244, 0x6000, v243\n\t" \
    "s_waitcnt vmcnt(12) lgkmcnt(0)\n\t" \
    "s_barrier \n\t" \
    "s_add_u32 m0, %[wm], 0x0\n\t" \
    "s_mov_b32 s44, 0x540000\n\t" \
    "buffer_load_dwordx4 v240, %[wr], s44 offen offset:0 nt lds\n\t" \
    "buffer_load_dwordx4 v241, %[wr], s44 offen offset:1024 nt lds\n\t" \
    "buffer_load_dwordx4 v242, %[wr], s44 offen offset:2048 nt lds\n\t" \
    "ds_read_b64_tr_b16 v[236:237], v244 offset:0\n\t" \
    "ds_read_b64_tr_b16 v[238:239], v244 offset:1536\n\t" \
    "ds_read_b64_tr_b16 v[164:165], v244 offset:32\n\t" \
    "ds_read_b64_tr_b16 v[166:167], v244 offset:1568\n\t" \
    "ds_read_b64_tr_b16 v[172:173], v244 offset:64\n\t" \
    "ds_read_b64_tr_b16 v[174:175], v244 offset:1600\n\t" \
    "v_add_u32 v244, 0xc000, v243\n\t" \
    "s_waitcnt vmcnt(12) lgkmcnt(0)\n\t" \
    "s_barrier \n\t" \
    "s_add_u32 m0, %[wm], 0x6000\n\t" \
    "s_mov_b32 s44, 0x5a0000\n\t" \
    "buffer_load_dwordx4 v240, %[wr], s44 offen offset:0 nt lds\n\t" \
    "buffer_load_dwordx4 v241, %[wr], s44 offen offset:1024 nt lds\n\t" \
    "buffer_load_dwordx4 v242, %[wr], s44 offen offset:2048 nt lds\n\t" \
    "ds_read_b64_tr_b16 v[16:17], v244 offset:0\n\t" \
    "ds_read_b64_tr_b16 v[18:19], v244 offset:1536\n\t" \
    "ds_read_b64_tr_b16 v[24:25], v244 offset:32\n\t" \
    "ds_read_b64_tr_b16 v[26:27], v244 offset:1568\n\t" \
    "ds_read_b64_tr_b16 v[32:33], v244 offset:64\n\t" \
    "ds_read_b64_tr_b16 v[34:35], v244 offset:1600\n\t" \
    "v_add_u32 v244, 0x12000, v243\n\t" \
    "s_waitcnt vmcnt(12) lgkmcnt(0)\n\t" \
    "s_barrier \n\t" \
    "s_add_u32 m0, %[wm], 0xc000\n\t" \
    "s_mov_b32 s44, 0x600000\n\t" \
    "buffer_load_dwordx4 v240, %[wr], s44 offen offset:0 nt lds\n\t" \
    "buffer_load_dwordx4 v241, %[wr], s44 offen offset:1024 nt lds\n\t" \
    "buffer_load_dwordx4 v242, %[wr], s44 offen offset:2048 nt lds\n\t" \
    "ds_read_b64_tr_b16 v[20:21], v244 offset:0\n\t" \
    "ds_read_b64_tr_b16 v[22:23], v244 offset:1536\n\t" \
    "ds_read_b64_tr_b16 v[28:29], v244 offset:32\n\t" \
    "ds_read_b64_tr_b16 v[30:31], v244 offset:1568\n\t" \
    "ds_read_b64_tr_b16 v[36:37], v244 offset:64\n\t" \
    "ds_read_b64_tr_b16 v[38:39], v244 offset:1600\n\t" \
    "v_add_u32 v244, 0x18000, v243\n\t" \
    "s_waitcnt vmcnt(12) lgkmcnt(0)\n\t" \
    "s_barrier \n\t" \
    "s_add_u32 m0, %[wm], 0x12000\n\t" \
    "s_mov_b32 s44, 0x660000\n\t" \
    "buffer_load_dwordx4 v240, %[wr], s44 offen offset:0 nt lds\n\t" \
    "buffer_load_dwordx4 v241, %[wr], s44 offen offset:1024 nt lds\n\t" \
    "buffer_load_dwordx4 v242, %[wr], s44 offen offset:2048 nt lds\n\t" \
    "ds_read_b64_tr_b16 v[56:57], v244 offset:0\n\t" \
    "ds_read_b64_tr_b16 v[58:59], v244 offset:1536\n\t" \
    "ds_read_b64_tr_b16 v[64:65], v244 offset:32\n\t" \
    "ds_read_b64_tr_b16 v[66:67], v244 offset:1568\n\t" \
    "ds_read_b64_tr_b16 v[72:73], v244 offset:64\n\t" \
    "ds_read_b64_tr_b16 v[74:75], v244 offset:1600\n\t" \
    "v_add_u32 v244, 0x1e000, v243\n\t" \
    "s_waitcnt vmcnt(12) lgkmcnt(0)\n\t" \
    "s_barrier \n\t" \
    ".Ls01_%=:\n\t" \
    "v_readfirstlane_b32 s43, v246\n\t" \
    "s_nop 0\n\t" \
    "s_cmp_lg_u32 s43, -1\n\t" \
    "s_cbranch_scc1 .Ls02_%=\n\t" \
    "s_cmp_eq_u32 s41, 0\n\t" \
    "s_cbranch_scc1 .Ls02_%=\n\t" \
    "s_sub_u32 s41, s41, 1\n\t" \
    "s_sleep 4\n\t" \
    "v_mov_b32 v246, 0\n\t" \
    "buffer_load_dword v246, v246, s[60:63], %[mo] offen sc1\n\t" \
    "s_waitcnt vmcnt(0)\n\t" \
    "s_branch .Ls01_%=\n\t" \
    ".Ls02_%=:\n\t" \
    "v_mov_b32 v246, 0x8000\n\t" \
    "s_mov_b64 exec, 1\n\t" \
    "buffer_store_dword v246, v246, s[60:63], %[mo] offen sc1\n\t" \
    "s_mov_b64 exec, -1\n\t" \
    "s_and_b32 s44, s43, 0x5000\n\t" \
    "s_xor_b32 %[mb], %[mb], s44\n\t" \
    "s_xor_b32 %[fl], %[fl], s44\n\t" \
    "s_xor_b32 %[pb], %[pb], s44\n\t" \
    "s_and_b32 s43, s43, 0x2000\n\t" \
    "s_xor_b32 s43, s43, %[lx]\n\t" \
    "s_add_u32 s60, s60, s43\n\t" \
    "s_addc_u32 s61, s61, 0\n\t" \
    "s_add_u32 m0, %[wm], 0x18000\n\t" \
    "s_mov_b32 s44, 0x6c0000\n\t" \
    "buffer_load_dwordx4 v240, %[wr], s44 offen offset:0 nt lds\n\t" \
    "buffer_load_dwordx4 v241, %[wr], s44 offen offset:1024 nt lds\n\t" \
    "buffer_load_dwordx4 v242, %[wr], s44 offen offset:2048 nt lds\n\t" \
    "ds_read_b64_tr_b16 v[60:61], v244 offset:0\n\t" \
    "ds_read_b64_tr_b16 v[62:63], v244 offset:1536\n\t" \
    "ds_read_b64_tr_b16 v[68:69], v244 offset:32\n\t" \
    "ds_read_b64_tr_b16 v[70:71], v244 offset:1568\n\t" \
    "ds_read_b64_tr_b16 v[76:77], v244 offset:64\n\t" \
    "ds_read_b64_tr_b16 v[78:79], v244 offset:1600\n\t" \
    "v_add_u32 v244, 0x0, v243\n\t" \
    "s_waitcnt vmcnt(12) lgkmcnt(0)\n\t" \
    "s_barrier \n\t" \
    "s_add_u32 m0, %[wm], 0x1e000\n\t" \
    "s_mov_b32 s44, 0x720000\n\t" \
    "buffer_load_dwordx4 v240, %[wr], s44 offen offset:0 nt lds\n\t" \
    "buffer_load_dwordx4 v241, %[wr], s44 offen offset:1024 nt lds\n\t" \
    "buffer_load_dwordx4 v242, %[wr], s44 offen offset:2048 nt lds\n\t" \
    "ds_read_b64_tr_b16 v[96:97], v244 offset:0\n\t" \
    "ds_read_b64_tr_b16 v[98:99], v244 offset:1536\n\t" \
    "ds_read_b64_tr_b16 v[104:105], v244 offset:32\n\t" \
    "ds_read_b64_tr_b16 v[106:107], v244 offset:1568\n\t" \
    "ds_read_b64_tr_b16 v[112:113], v244 offset:64\n\t" \
    "ds_read_b64_tr_b16 v[114:115], v244 offset:1600\n\t" \
    "v_add_u32 v244, 0x6000, v243\n\t" \
    "s_waitcnt vmcnt(12) lgkmcnt(0)\n\t" \
    "s_barrier \n\t" \
    "ds_read_b64_tr_b16 v[100:101], v244 offset:0\n\t" \
    "ds_read_b64_tr_b16 v[102:103], v244 offset:1536\n\t" \
    "ds_read_b64_tr_b16 v[108:109], v244 offset:32\n\t" \
    "ds_read_b64_tr_b16 v[110:111], v244 offset:1568\n\t" \
    "ds_read_b64_tr_b16 v[116:117], v244 offset:64\n\t" \
    "ds_read_b64_tr_b16 v[118:119], v244 offset:1600\n\t" \
    "v_add_u32 v244, 0xc000, v243\n\t" \
    "s_waitcnt vmcnt(9) lgkmcnt(0)\n\t" \
    "s_barrier \n\t" \
    "ds_read_b64_tr_b16 v[136:137], v244 offset:0\n\t" \
    "ds_read_b64_tr_b16 v[138:139], v244 offset:1536\n\t" \
    "ds_read_b64_tr_b16 v[144:145], v244 offset:32\n\t" \
    "ds_read_b64_tr_b16 v[146:147], v244 offset:1568\n\t" \
    "ds_read_b64_tr_b16 v[152:153], v244 offset:64\n\t" \
    "ds_read_b64_tr_b16 v[154:155], v244 offset:1600\n\t" \
    "v_add_u32 v244, 0x12000, v243\n\t" \
    "s_waitcnt vmcnt(6) lgkmcnt(0)\n\t" \
    "s_barrier \n\t" \
    "ds_read_b64_tr_b16 v[140:141], v244 offset:0\n\t" \
    "ds_read_b64_tr_b16 v[142:143], v244 offset:1536\n\t" \
    "ds_read_b64_tr_b16 v[148:149], v244 offset:32\n\t" \
    "ds_read_b64_tr_b16 v[150:151], v244 offset:1568\n\t" \
    "ds_read_b64_tr_b16 v[156:157], v244 offset:64\n\t" \
    "ds_read_b64_tr_b16 v[158:159], v244 offset:1600\n\t" \
    "v_add_u32 v244, 0x18000, v243\n\t" \
    "s_waitcnt vmcnt(3) lgkmcnt(0)\n\t" \
    "s_barrier \n\t" \
    "ds_read_b64_tr_b16 v[176:177], v244 offset:0\n\t" \
    "ds_read_b64_tr_b16 v[178:179], v244 offset:1536\n\t" \
    "ds_read_b64_tr_b16 v[184:185], v244 offset:32\n\t" \
    "ds_read_b64_tr_b16 v[186:187], v244 offset:1568\n\t" \
    "ds_read_b64_tr_b16 v[192:193], v244 offset:64\n\t" \
    "ds_read_b64_tr_b16 v[194:195], v244 offset:1600\n\t" \
    "v_add_u32 v244, 0x1e000, v243\n\t" \
    "s_waitcnt vmcnt(0) lgkmcnt(0)\n\t" \
    "s_barrier \n\t" \
    "ds_read_b64_tr_b16 v[180:181], v244 offset:0\n\t" \
    "ds_read_b64_tr_b16 v[182:183], v244 offset:1536\n\t" \
    "ds_read_b64_tr_b16 v[188:189], v244 offset:32\n\t" \
    "ds_read_b64_tr_b16 v[190:191], v244 offset:1568\n\t" \
    "ds_read_b64_tr_b16 v[196:197], v244 offset:64\n\t" \
    "ds_read_b64_tr_b16 v[198:199], v244 offset:1600\n\t" \
    "s_waitcnt lgkmcnt(0)\n\t" \
    "s_barrier \n\t" \
    "ds_write_b128 v254, v[200:203] offset:30720\n\t" \
    "ds_write_b128 v254, v[204:207] offset:31744\n\t" \
    "ds_write_b128 v254, v[208:211] offset:32768\n\t" \
    "ds_write_b128 v254, v[212:215] offset:33792\n\t" \
    "ds_write_b128 v254, v[216:219] offset:34816\n\t" \
    "ds_write_b128 v254, v[220:223] offset:35840\n\t" \
    "ds_write_b128 v254, v[224:227] offset:36864\n\t" \
    "ds_write_b128 v254, v[228:231] offset:37888\n\t" \
    "ds_write_b128 v254, v[232:235] offset:38912\n\t" \
    "ds_write_b128 v254, v[236:239] offset:39936\n\t" \
    "s_waitcnt vmcnt(0) lgkmcnt(0)\n\t" \
    "s_barrier \n\t" \
    "ds_read_b128 v[224:227], v253 offset:0\n\t" \
    "ds_read_b128 v[228:231], v253 offset:1024\n\t" \
    "ds_read_b128 v[232:235], v253 offset:20480\n\t" \
    "ds_read_b128 v[236:239], v253 offset:21504\n\t" \
    ".Ls03_%=:\n\t" \
    "s_waitcnt lgkmcnt(3)\n\t" \
    "v_mfma_f32_16x16x32_bf16 v[204:207], v[0:3], v[224:227], 0\n\t" \
    "s_waitcnt lgkmcnt(2)\n\t" \
    "v_mfma_f32_16x16x32_bf16 v[204:207], v[4:7], v[228:231], v[204:207]\n\t" \
    "ds_read_b128 v[240:243], v253 offset:2048\n\t" \
    "v_mfma_f32_16x16x32_bf16 v[208:211], v[8:11], v[224:227], 0\n\t" \
    "v_mfma_f32_16x16x32_bf16 v[208:211], v[12:15], v[228:231], v[208:211]\n\t" \
    "s_waitcnt lgkmcnt(2)\n\t" \
    "v_mfma_f32_16x16x32_bf16 v[216:219], v[0:3], v[232:235], 0\n\t" \
    "ds_read_b128 v[224:227], v253 offset:3072\n\t" \
    "s_waitcnt lgkmcnt(2)\n\t" \
    "v_mfma_f32_16x16x32_bf16 v[216:219], v[4:7], v[236:239], v[216:219]\n\t" \
    "ds_read_b128 v[228:231], v253 offset:22528\n\t" \
    "v_mfma_f32_16x16x32_bf16 v[220:223], v[8:11], v[232:235], 0\n\t" \
    "v_mfma_f32_16x16x32_bf16 v[220:223], v[12:15], v[236:239], v[220:223]\n\t" \
    "s_waitcnt lgkmcnt(2)\n\t" \
    "v_mfma_f32_16x16x32_bf16 v[204:207], v[40:43], v[240:243], v[204:207]\n\t" \
    "ds_read_b128 v[232:235], v253 offset:23552\n\t" \
    "s_waitcnt lgkmcnt(2)\n\t" \
    "v_mfma_f32_16x16x32_bf16 v[204:207], v[44:47], v[224:227], v[204:207]\n\t" \
    "ds_read_b128 v[236:239], v253 offset:4096\n\t" \
    "v_mfma_f32_16x16x32_bf16 v[208:211], v[48:51], v[240:243], v[208:211]\n\t" \
    "v_mfma_f32_16x16x32_bf16 v[208:211], v[52:55], v[224:227], v[208:211]\n\t" \
    "s_waitcnt lgkmcnt(2)\n\t" \
    "v_mfma_f32_16x16x32_bf16 v[216:219], v[40:43], v[228:231], v[216:219]\n\t" \
    "ds_read_b128 v[240:243], v253 offset:5120\n\t" \
    "s_waitcnt lgkmcnt(2)\n\t" \
    "v_mfma_f32_16x16x32_bf16 v[216:219], v[44:47], v[232:235], v[216:219]\n\t" \
    "ds_read_b128 v[224:227], v253 offset:24576\n\t" \
    "v_mfma_f32_16x16x32_bf16 v[220:223], v[48:51], v[228:231], v[220:223]\n\t" \
    "v_mfma_f32_16x16x32_bf16 v[220:223], v[52:55], v[232:235], v[220:223]\n\t" \
    "s_waitcnt lgkmcnt(2)\n\t" \
    "v_mfma_f32_16x16x32_bf16 v[204:207], v[80:83], v[236:239], v[204:207]\n\t" \
    "ds_read_b128 v[228:231], v253 offset:25600\n\t" \
    "s_waitcnt lgkmcnt(2)\n\t" \
    "v_mfma_f32_16x16x32_bf16 v[204:207], v[84:87], v[240:243], v[204:207]\n\t" \
    "ds_read_b128 v[232:235], v253 offset:6144\n\t" \
    "v_mfma_f32_16x16x32_bf16 v[208:211], v[88:91], v[236:239], v[208:211]\n\t" \
    "v_mfma_f32_16x16x32_bf16 v[208:211], v[92:95], v[240:243], v[208:211]\n\t" \
    "s_waitcnt lgkmcnt(2)\n\t" \
    "v_mfma_f32_16x16x32_bf16 v[216:219], v[80:83], v[224:227], v[216:219]\n\t" \
    "ds_read_b128 v[236:239], v253 offset:7168\n\t" \
    "s_waitcnt lgkmcnt(2)\n\t" \
    "v_mfma_f32_16x16x32_bf16 v[216:219], v[84:87], v[228:231], v[216:219]\n\t" \
    "ds_read_b128 v[240:243], v253 offset:26624\n\t" \
    "v_mfma_f32_16x16x32_bf16 v[220:223], v[88:91], v[224:227], v[220:223]\n\t" \
    "v_mfma_f32_16x16x32_bf16 v[220:223], v[92:95], v[228:231], v[220:223]\n\t" \
    "s_waitcnt lgkmcnt(2)\n\t" \
    "v_mfma_f32_16x16x32_bf16 v[204:207], v[120:123], v[232:235], v[204:207]\n\t" \
    "ds_read_b128 v[224:227], v253 offset:27648\n\t" \
    "s_waitcnt lgkmcnt(2)\n\t" \
    "v_mfma_f32_16x16x32_bf16 v[204:207], v[124:127], v[236:239], v[204:207]\n\t" \
    "ds_read_b128 v[228:231], v253 offset:8192\n\t" \
    "v_mfma_f32_16x16x32_bf16 v[208:211], v[128:131], v[232:235], v[208:211]\n\t" \
    "v_mfma_f32_16x16x32_bf16 v[208:211], v[132:135], v[236:239], v[208:211]\n\t" \
    "s_waitcnt lgkmcnt(2)\n\t" \
    "v_mfma_f32_16x16x32_bf16 v[216:219], v[120:123], v[240:243], v[216:219]\n\t" \
    "ds_read_b128 v[232:235], v253 offset:9216\n\t" \
    "s_waitcnt lgkmcnt(2)\n\t" \
    "v_mfma_f32_16x16x32_bf16 v[216:219], v[124:127], v[224:227], v[216:219]\n\t" \
    "ds_read_b128 v[236:239], v253 offset:28672\n\t" \
    "v_mfma_f32_16x16x32_bf16 v[220:223], v[128:131], v[240:243], v[220:223]\n\t" \
    "v_mfma_f32_16x16x32_bf16 v[220:223], v[132:135], v[224:227], v[220:223]\n\t" \
    "s_waitcnt lgkmcnt(2)\n\t" \
    "v_mfma_f32_16x16x32_bf16 v[204:207], v[160:163], v[228:231], v[204:207]\n\t" \
    "ds_read_b128 v[240:243], v253 offset:29696\n\t" \
    "s_waitcnt lgkmcnt(2)\n\t" \
    "v_mfma_f32_16x16x32_bf16 v[204:207], v[164:167], v[232:235], v[204:207]\n\t" \
    "ds_read_b128 v[224:227], v253 offset:10240\n\t" \
    "v_mfma_f32_16x16x32_bf16 v[208:211], v[168:171], v[228:231], v[208:211]\n\t" \
    "v_mfma_f32_16x16x32_bf16 v[208:211], v[172:175], v[232:235], v[208:211]\n\t" \
    "s_waitcnt lgkmcnt(2)\n\t" \
    "v_mfma_f32_16x16x32_bf16 v[216:219], v[160:163], v[236:239], v[216:219]\n\t" \
    "ds_read_b128 v[228:231], v253 offset:11264\n\t" \
    "s_waitcnt lgkmcnt(2)\n\t" \
    "v_mfma_f32_16x16x32_bf16 v[216:219], v[164:167], v[240:243], v[216:219]\n\t" \
    "ds_read_b128 v[232:235], v253 offset:30720\n\t" \
    "v_mfma_f32_16x16x32_bf16 v[220:223], v[168:171], v[236:239], v[220:223]\n\t" \
    "v_mfma_f32_16x16x32_bf16 v[220:223], v[172:175], v[240:243], v[220:223]\n\t" \
    "ds_read_b128 v[236:239], v253 offset:31744\n\t" \
    "s_setprio 2\n\t" \
    "s_waitcnt lgkmcnt(0)\n\t" \
    "s_barrier \n\t" \
    "ds_read_b128 v[200:203], v253 offset:40960\n\t" \
    "ds_read_b128 v[212:215], v253 offset:41984\n\t" \
    "v_mfma_f32_16x16x32_bf16 v[204:207], v[24:27], v[224:227], v[204:207]\n\t" \
    "v_mfma_f32_16x16x32_bf16 v[204:207], v[28:31], v[228:231], v[204:207]\n\t" \
    "ds_read_b128 v[240:243], v253 offset:12288\n\t" \
    "v_mfma_f32_16x16x32_bf16 v[208:211], v[32:35], v[224:227], v[208:211]\n\t" \
    "v_mfma_f32_16x16x32_bf16 v[208:211], v[36:39], v[228:231], v[208:211]\n\t" \
    "s_waitcnt lgkmcnt(2)\n\t" \
    "v_mfma_f32_16x16x32_bf16 v[200:203], v[16:19], v[224:227], v[200:203]\n\t" \
    "v_mfma_f32_16x16x32_bf16 v[200:203], v[20:23], v[228:231], v[200:203]\n\t" \
    "v_mfma_f32_16x16x32_bf16 v[216:219], v[24:27], v[232:235], v[216:219]\n\t" \
    "ds_read_b128 v[224:227], v253 offset:13312\n\t" \
    "s_and_b32 s44, s40, 3\n\t" \
    "v_mfma_f32_16x16x32_bf16 v[216:219], v[28:31], v[236:239], v[216:219]\n\t" \
    "ds_read_b128 v[228:231], v253 offset:32768\n\t" \
    "s_lshl_b32 s44, s44, 22\n\t" \
    "v_mfma_f32_16x16x32_bf16 v[220:223], v[32:35], v[232:235], v[220:223]\n\t" \
    "s_add_u32 s44, s44, %[mb]\n\t" \
    "v_mfma_f32_16x16x32_bf16 v[220:223], v[36:39], v[236:239], v[220:223]\n\t" \
    "s_add_u32 s46, s44, 0x0\n\t" \
    "s_waitcnt lgkmcnt(3)\n\t" \
    "v_mfma_f32_16x16x32_bf16 v[212:215], v[16:19], v[232:235], v[212:215]\n\t" \
    "s_add_u32 s47, s44, 0x8400\n\t" \
    "v_mfma_f32_16x16x32_bf16 v[212:215], v[20:23], v[236:239], v[212:215]\n\t" \
    "s_add_u32 s48, s44, 0x400\n\t" \
    "s_waitcnt lgkmcnt(2)\n\t" \
    "v_mfma_f32_16x16x32_bf16 v[204:207], v[64:67], v[240:243], v[204:207]\n\t" \
    "ds_read_b128 v[232:235], v253 offset:33792\n\t" \
    "s_waitcnt lgkmcnt(2)\n\t" \
    "v_mfma_f32_16x16x32_bf16 v[204:207], v[68:71], v[224:227], v[204:207]\n\t" \
    "ds_read_b128 v[236:239], v253 offset:14336\n\t" \
    "v_mfma_f32_16x16x32_bf16 v[208:211], v[72:75], v[240:243], v[208:211]\n\t" \
    "v_mfma_f32_16x16x32_bf16 v[208:211], v[76:79], v[224:227], v[208:211]\n\t" \
    "v_mfma_f32_16x16x32_bf16 v[200:203], v[56:59], v[240:243], v[200:203]\n\t" \
    "v_mfma_f32_16x16x32_bf16 v[200:203], v[60:63], v[224:227], v[200:203]\n\t" \
    "s_waitcnt lgkmcnt(2)\n\t" \
    "v_mfma_f32_16x16x32_bf16 v[216:219], v[64:67], v[228:231], v[216:219]\n\t" \
    "ds_read_b128 v[240:243], v253 offset:15360\n\t" \
    "s_waitcnt lgkmcnt(2)\n\t" \
    "v_mfma_f32_16x16x32_bf16 v[216:219], v[68:71], v[232:235], v[216:219]\n\t" \
    "ds_read_b128 v[224:227], v253 offset:34816\n\t" \
    "v_mfma_f32_16x16x32_bf16 v[220:223], v[72:75], v[228:231], v[220:223]\n\t" \
    "v_mfma_f32_16x16x32_bf16 v[220:223], v[76:79], v[232:235], v[220:223]\n\t" \
    "v_mfma_f32_16x16x32_bf16 v[212:215], v[56:59], v[228:231], v[212:215]\n\t" \
    "v_mfma_f32_16x16x32_bf16 v[212:215], v[60:63], v[232:235], v[212:215]\n\t" \
    "s_waitcnt lgkmcnt(2)\n\t" \
    "v_mfma_f32_16x16x32_bf16 v[204:207], v[104:107], v[236:239], v[204:207]\n\t" \
    "ds_read_b128 v[228:231], v253 offset:35840\n\t" \
    "s_waitcnt lgkmcnt(2)\n\t" \
    "v_mfma_f32_16x16x32_bf16 v[204:207], v[108:111], v[240:243], v[204:207]\n\t" \
    "ds_read_b128 v[232:235], v253 offset:16384\n\t" \
    "v_mfma_f32_16x16x32_bf16 v[208:211], v[112:115], v[236:239], v[208:211]\n\t" \
    "v_mfma_f32_16x16x32_bf16 v[208:211], v[116:119], v[240:243], v[208:211]\n\t" \
    "v_mfma_f32_16x16x32_bf16 v[200:203], v[96:99], v[236:239], v[200:203]\n\t" \
    "v_mfma_f32_16x16x32_bf16 v[200:203], v[100:103], v[240:243], v[200:203]\n\t" \
    "s_waitcnt lgkmcnt(2)\n\t" \
    "v_mfma_f32_16x16x32_bf16 v[216:219], v[104:107], v[224:227], v[216:219]\n\t" \
    "ds_read_b128 v[236:239], v253 offset:17408\n\t" \
    "s_waitcnt lgkmcnt(2)\n\t" \
    "v_mfma_f32_16x16x32_bf16 v[216:219], v[108:111], v[228:231], v[216:219]\n\t" \
    "ds_read_b128 v[240:243], v253 offset:18432\n\t" \
    "v_mfma_f32_16x16x32_bf16 v[220:223], v[112:115], v[224:227], v[220:223]\n\t" \
    "v_mfma_f32_16x16x32_bf16 v[220:223], v[116:119], v[228:231], v[220:223]\n\t" \
    "v_mfma_f32_16x16x32_bf16 v[212:215], v[96:99], v[224:227], v[212:215]\n\t" \
    "v_mfma_f32_16x16x32_bf16 v[212:215], v[100:103], v[228:231], v[212:215]\n\t" \
    "s_waitcnt lgkmcnt(2)\n\t" \
    "v_mfma_f32_16x16x32_bf16 v[204:207], v[144:147], v[232:235], v[204:207]\n\t" \
    "ds_read_b128 v[224:227], v253 offset:19456\n\t" \
    "v_mov_b64 v[244:245], 0\n\t" \
    "s_waitcnt lgkmcnt(2)\n\t" \
    "v_mfma_f32_16x16x32_bf16 v[204:207], v[148:151], v[236:239], v[204:207]\n\t" \
    "ds_read_b128 v[228:231], v253 offset:36864\n\t" \
    "v_mov_b64 v[246:247], 0\n\t" \
    "v_mfma_f32_16x16x32_bf16 v[208:211], v[152:155], v[232:235], v[208:211]\n\t" \
    "v_mov_b64 v[248:249], 0\n\t" \
    "v_mfma_f32_16x16x32_bf16 v[208:211], v[156:159], v[236:239], v[208:211]\n\t" \
    "v_mov_b64 v[250:251], 0\n\t" \
    "v_mfma_f32_16x16x32_bf16 v[200:203], v[136:139], v[232:235], v[200:203]\n\t" \
    "v_mfma_f32_16x16x32_bf16 v[200:203], v[140:143], v[236:239], v[200:203]\n\t" \
    "s_waitcnt lgkmcnt(2)\n\t" \
    "v_mfma_f32_16x16x32_bf16 v[204:207], v[184:187], v[240:243], v[204:207]\n\t" \
    "ds_read_b128 v[232:235], v253 offset:37888\n\t" \
    "s_waitcnt lgkmcnt(2)\n\t" \
    "v_mfma_f32_16x16x32_bf16 v[204:207], v[188:191], v[224:227], v[204:207]\n\t" \
    "ds_read_b128 v[236:239], v253 offset:38912\n\t" \
    "v_mfma_f32_16x16x32_bf16 v[208:211], v[192:195], v[240:243], v[208:211]\n\t" \
    "v_mfma_f32_16x16x32_bf16 v[208:211], v[196:199], v[224:227], v[208:211]\n\t" \
    "v_mfma_f32_16x16x32_bf16 v[200:203], v[176:179], v[240:243], v[200:203]\n\t" \
    "v_mfma_f32_16x16x32_bf16 v[200:203], v[180:183], v[224:227], v[200:203]\n\t" \
    "ds_read_b128 v[240:243], v253 offset:39936\n\t" \
    "s_waitcnt lgkmcnt(3)\n\t" \
    "v_mfma_f32_16x16x32_bf16 v[216:219], v[144:147], v[228:231], v[216:219]\n\t" \
    "s_waitcnt lgkmcnt(2)\n\t" \
    "v_mfma_f32_16x16x32_bf16 v[216:219], v[148:151], v[232:235], v[216:219]\n\t" \
    "v_cvt_pk_bf16_f32 v204, v204, v208\n\t" \
    "v_cvt_pk_bf16_f32 v205, v205, v209\n\t" \
    "s_waitcnt lgkmcnt(1)\n\t" \
    "v_mfma_f32_16x16x32_bf16 v[216:219], v[184:187], v[236:239], v[216:219]\n\t" \
    "v_cvt_pk_bf16_f32 v206, v206, v210\n\t" \
    "v_cvt_pk_bf16_f32 v207, v207, v211\n\t" \
    "s_waitcnt lgkmcnt(0)\n\t" \
    "v_mfma_f32_16x16x32_bf16 v[216:219], v[188:191], v[240:243], v[216:219]\n\t" \
    "ds_write_b128 v254, v[204:207] offset:8192\n\t" \
    "v_mfma_f32_16x16x32_bf16 v[220:223], v[152:155], v[228:231], v[220:223]\n\t" \
    "v_mfma_f32_16x16x32_bf16 v[220:223], v[156:159], v[232:235], v[220:223]\n\t" \
    "v_cvt_pk_bf16_f32 v200, v200, v201\n\t" \
    "v_cvt_pk_bf16_f32 v201, v202, v203\n\t" \
    "v_mfma_f32_16x16x32_bf16 v[220:223], v[192:195], v[236:239], v[220:223]\n\t" \
    "ds_write_b64 v254, v[200:201] offset:6144\n\t" \
    "v_mfma_f32_16x16x32_bf16 v[220:223], v[196:199], v[240:243], v[220:223]\n\t" \
    "v_mfma_f32_16x16x32_bf16 v[212:215], v[136:139], v[228:231], v[212:215]\n\t" \
    "ds_read_b128 v[200:203], v254 offset:30720\n\t" \
    "ds_read_b128 v[204:207], v254 offset:31744\n\t" \
    "v_mfma_f32_16x16x32_bf16 v[212:215], v[140:143], v[232:235], v[212:215]\n\t" \
    "v_mov_b64 v[208:209], 0\n\t" \
    "v_mov_b64 v[210:211], 0\n\t" \
    "v_mfma_f32_16x16x32_bf16 v[212:215], v[176:179], v[236:239], v[212:215]\n\t" \
    "v_cvt_pk_bf16_f32 v216, v216, v220\n\t" \
    "v_cvt_pk_bf16_f32 v217, v217, v221\n\t" \
    "v_mfma_f32_16x16x32_bf16 v[212:215], v[180:183], v[240:243], v[212:215]\n\t" \
    "v_cvt_pk_bf16_f32 v218, v218, v222\n\t" \
    "v_cvt_pk_bf16_f32 v219, v219, v223\n\t" \
    "s_setprio 0\n\t" \
    "ds_write_b128 v254, v[216:219] offset:14336\n\t" \
    "ds_read_b128 v[216:219], v254 offset:32768\n\t" \
    "ds_read_b128 v[220:223], v254 offset:33792\n\t" \
    "s_nop 2\n\t" \
    "v_cvt_pk_bf16_f32 v212, v212, v213\n\t" \
    "v_cvt_pk_bf16_f32 v213, v214, v215\n\t" \
    "ds_write_b64 v254, v[212:213] offset:12288\n\t" \
    "s_sleep 3\n\t" \
    "s_mov_b32 m0, 0xa000\n\t" \
    "s_nop 0\n\t" \
    "buffer_load_dwordx4 v253, s[60:63], s46 offen sc1 lds\n\t" \
    "s_mov_b32 m0, 0xb400\n\t" \
    "s_nop 0\n\t" \
    "buffer_load_dwordx4 v253, s[60:63], s47 offen sc1 lds\n\t" \
    "s_mov_b32 m0, 0xa400\n\t" \
    "s_nop 0\n\t" \
    "buffer_load_dwordx4 v253, s[60:63], s48 offen sc1 lds\n\t" \
    "s_waitcnt lgkmcnt(0)\n\t" \
    "v_bfe_i32 v212, v253, 5, 1\n\t" \
    "v_and_b32 v212, 0xaaaa, v212\n\t" \
    "v_or_b32 v212, 0x4444, v212\n\t" \
    "v_and_b32 v213, 0xffff, v255\n\t" \
    "v_lshrrev_b32 v214, 16, v255\n\t" \
    ".Ls04_%=:\n\t" \
    "s_waitcnt vmcnt(0)\n\t" \
    "ds_read_b128 v[224:227], v253 offset:40960\n\t" \
    "ds_read_b128 v[228:231], v253 offset:46080\n\t" \
    "ds_read_b128 v[232:235], v253 offset:41984\n\t" \
    "s_waitcnt lgkmcnt(2)\n\t" \
    "v_max_u32 v215, v224, v225\n\t" \
    "v_max3_u32 v215, v226, v227, v215\n\t" \
    "s_waitcnt lgkmcnt(1)\n\t" \
    "v_max3_u32 v215, v228, v229, v215\n\t" \
    "v_max3_u32 v215, v230, v231, v215\n\t" \
    "s_waitcnt lgkmcnt(0)\n\t" \
    "v_max3_u32 v215, v232, v233, v215\n\t" \
    "v_max3_u32 v215, v234, v235, v215\n\t" \
    "v_cmp_eq_u32 vcc, -1, v215\n\t" \
    "s_cbranch_vccz .Ls05_%=\n\t" \
    "v_max_u32 v215, v224, v225\n\t" \
    "v_max3_u32 v215, v226, v227, v215\n\t" \
    "v_cmp_eq_u32 vcc, -1, v215\n\t" \
    "s_cbranch_vccz .Ls06_%=\n\t" \
    "s_mov_b32 m0, 0xa000\n\t" \
    "s_nop 0\n\t" \
    "buffer_load_dwordx4 v253, s[60:63], s46 offen sc1 lds\n\t" \
    ".Ls06_%=:\n\t" \
    "v_max_u32 v215, v228, v229\n\t" \
    "v_max3_u32 v215, v230, v231, v215\n\t" \
    "v_cmp_eq_u32 vcc, -1, v215\n\t" \
    "s_cbranch_vccz .Ls07_%=\n\t" \
    "s_mov_b32 m0, 0xb400\n\t" \
    "s_nop 0\n\t" \
    "buffer_load_dwordx4 v253, s[60:63], s47 offen sc1 lds\n\t" \
    ".Ls07_%=:\n\t" \
    "v_max_u32 v215, v232, v233\n\t" \
    "v_max3_u32 v215, v234, v235, v215\n\t" \
    "v_cmp_eq_u32 vcc, -1, v215\n\t" \
    "s_cbranch_vccz .Ls08_%=\n\t" \
    "s_mov_b32 m0, 0xa400\n\t" \
    "s_nop 0\n\t" \
    "buffer_load_dwordx4 v253, s[60:63], s48 offen sc1 lds\n\t" \
    ".Ls08_%=:\n\t" \
    "s_cmp_eq_u32 s41, 0\n\t" \
    "s_cbranch_scc1 .Ls05_%=\n\t" \
    "s_sub_u32 s41, s41, 1\n\t" \
    "s_branch .Ls04_%=\n\t" \
    ".Ls05_%=:\n\t" \
    "s_setprio 3\n\t" \
    "s_waitcnt lgkmcnt(0)\n\t" \
    "s_barrier \n\t" \
    "ds_read_b128 v[236:239], v253 offset:47104\n\t" \
    "ds_read_b128 v[240:243], v253 offset:43008\n\t" \
    "v_smfmac_f32_16x16x64_bf16 v[244:247], v[224:227], v[0:7], v212\n\t" \
    "v_smfmac_f32_16x16x64_bf16 v[248:251], v[224:227], v[8:15], v212\n\t" \
    "v_smfmac_f32_16x16x64_bf16 v[208:211], v[224:227], v[200:207], v212\n\t" \
    "ds_read_b128 v[200:203], v254 offset:34816\n\t" \
    "ds_read_b128 v[204:207], v254 offset:35840\n\t" \
    "ds_read_b128 v[224:227], v253 offset:48128\n\t" \
    "v_smfmac_f32_16x16x64_bf16 v[208:211], v[228:231], v[16:23], v212\n\t" \
    "v_smfmac_f32_16x16x64_bf16 v[244:247], v[228:231], v[24:31], v212\n\t" \
    "v_smfmac_f32_16x16x64_bf16 v[248:251], v[228:231], v[32:39], v212\n\t" \
    "ds_read_b128 v[228:231], v253 offset:44032\n\t" \
    "v_smfmac_f32_16x16x64_bf16 v[208:211], v[232:235], v[216:223], v212\n\t" \
    "ds_read_b128 v[216:219], v254 offset:36864\n\t" \
    "ds_read_b128 v[220:223], v254 offset:37888\n\t" \
    "v_smfmac_f32_16x16x64_bf16 v[244:247], v[232:235], v[40:47], v212\n\t" \
    "v_smfmac_f32_16x16x64_bf16 v[248:251], v[232:235], v[48:55], v212\n\t" \
    "ds_read_b128 v[232:235], v253 offset:49152\n\t" \
    "s_waitcnt lgkmcnt(8)\n\t" \
    "v_smfmac_f32_16x16x64_bf16 v[208:211], v[236:239], v[56:63], v212\n\t" \
    "v_smfmac_f32_16x16x64_bf16 v[244:247], v[236:239], v[64:71], v212\n\t" \
    "v_smfmac_f32_16x16x64_bf16 v[248:251], v[236:239], v[72:79], v212\n\t" \
    "ds_read_b128 v[236:239], v253 offset:45056\n\t" \
    "s_waitcnt lgkmcnt(6)\n\t" \
    "v_smfmac_f32_16x16x64_bf16 v[208:211], v[240:243], v[200:207], v212\n\t" \
    "ds_read_b128 v[200:203], v254 offset:38912\n\t" \
    "ds_read_b128 v[204:207], v254 offset:39936\n\t" \
    "v_smfmac_f32_16x16x64_bf16 v[244:247], v[240:243], v[80:87], v212\n\t" \
    "v_smfmac_f32_16x16x64_bf16 v[248:251], v[240:243], v[88:95], v212\n\t" \
    "ds_read_b128 v[240:243], v253 offset:50176\n\t" \
    "s_waitcnt lgkmcnt(8)\n\t" \
    "v_smfmac_f32_16x16x64_bf16 v[208:211], v[224:227], v[96:103], v212\n\t" \
    "v_smfmac_f32_16x16x64_bf16 v[244:247], v[224:227], v[104:111], v212\n\t" \
    "v_smfmac_f32_16x16x64_bf16 v[248:251], v[224:227], v[112:119], v212\n\t" \
    "s_waitcnt lgkmcnt(5)\n\t" \
    "v_smfmac_f32_16x16x64_bf16 v[208:211], v[228:231], v[216:223], v212\n\t" \
    "s_waitcnt lgkmcnt(4)\n\t" \
    "v_smfmac_f32_16x16x64_bf16 v[208:211], v[232:235], v[136:143], v212\n\t" \
    "s_waitcnt lgkmcnt(1)\n\t" \
    "v_smfmac_f32_16x16x64_bf16 v[208:211], v[236:239], v[200:207], v212\n\t" \
    "s_waitcnt lgkmcnt(0)\n\t" \
    "v_smfmac_f32_16x16x64_bf16 v[208:211], v[240:243], v[176:183], v212\n\t" \
    "v_smfmac_f32_16x16x64_bf16 v[244:247], v[228:231], v[120:127], v212\n\t" \
    "v_smfmac_f32_16x16x64_bf16 v[244:247], v[232:235], v[144:151], v212\n\t" \
    "v_smfmac_f32_16x16x64_bf16 v[244:247], v[236:239], v[160:167], v212\n\t" \
    "v_add_f32 v208, v208, v210\n\t" \
    "v_add_f32 v209, v209, v211\n\t" \
    "v_cvt_pk_bf16_f32 v208, v208, v209\n\t" \
    "v_smfmac_f32_16x16x64_bf16 v[244:247], v[240:243], v[184:191], v212\n\t" \
    "ds_write_b16 v213, v208 offset:0\n\t" \
    "ds_write_b16_d16_hi v213, v208 offset:16\n\t" \
    "v_smfmac_f32_16x16x64_bf16 v[248:251], v[228:231], v[128:135], v212\n\t" \
    "v_smfmac_f32_16x16x64_bf16 v[248:251], v[232:235], v[152:159], v212\n\t" \
    "v_smfmac_f32_16x16x64_bf16 v[248:251], v[236:239], v[168:175], v212\n\t" \
    "v_add_f32 v244, v244, v246\n\t" \
    "v_add_f32 v245, v245, v247\n\t" \
    "v_smfmac_f32_16x16x64_bf16 v[248:251], v[240:243], v[192:199], v212\n\t" \
    "s_nop 7\n\t" \
    "v_add_f32 v248, v248, v250\n\t" \
    "v_add_f32 v249, v249, v251\n\t" \
    "v_cvt_pk_bf16_f32 v244, v244, v248\n\t" \
    "v_cvt_pk_bf16_f32 v245, v245, v249\n\t" \
    "ds_write2_b32 v214, v244, v245 offset1:4\n\t" \
    "s_setprio 0\n\t" \
    "s_sleep 3\n\t" \
    "ds_read_b128 v[200:203], v254 offset:18432\n\t" \
    "ds_read_b128 v[204:207], v254 offset:19456\n\t" \
    "ds_read_b128 v[214:217], v254 offset:24576\n\t" \
    "ds_read_b128 v[218:221], v254 offset:25600\n\t" \
    "ds_read_b128 v[222:225], v254 offset:20480\n\t" \
    "ds_read_b128 v[226:229], v254 offset:21504\n\t" \
    "ds_read_b128 v[230:233], v254 offset:26624\n\t" \
    "ds_read_b128 v[234:237], v254 offset:27648\n\t" \
    "v_and_b32 v213, 0xffffff7f, v254\n\t" \
    "v_mov_b64 v[242:243], 0\n\t" \
    "v_mov_b64 v[244:245], 0\n\t" \
    "v_mov_b64 v[246:247], 0\n\t" \
    "v_mov_b64 v[248:249], 0\n\t" \
    "v_mov_b64 v[250:251], 0\n\t" \
    "v_mov_b64 v[252:253], 0\n\t" \
    "s_and_b32 s44, s40, 3\n\t" \
    "s_lshl_b32 s44, s44, 22\n\t" \
    "s_add_u32 s50, s44, %[pb]\n\t" \
    "s_xor_b32 s51, s50, 0x4000\n\t" \
    "s_xor_b32 s52, s50, 0x1000\n\t" \
    "s_xor_b32 s53, s50, 0x5000\n\t" \
    "s_setprio 3\n\t" \
    "s_waitcnt lgkmcnt(0)\n\t" \
    "s_barrier \n\t" \
    "ds_read_b128 v[208:211], v254 offset:0\n\t" \
    "ds_read_b128 v[238:241], v254 offset:1024\n\t" \
    "s_waitcnt lgkmcnt(1)\n\t" \
    "v_smfmac_f32_16x16x64_bf16 v[242:245], v[208:211], v[200:207], v212\n\t" \
    "ds_read_b128 v[200:203], v213 offset:3072\n\t" \
    "ds_read_b128 v[204:207], v213 offset:3200\n\t" \
    "v_smfmac_f32_16x16x64_bf16 v[246:249], v[208:211], v[214:221], v212\n\t" \
    "ds_read_b128 v[214:217], v213 offset:4096\n\t" \
    "ds_read_b128 v[218:221], v213 offset:4224\n\t" \
    "s_waitcnt lgkmcnt(4)\n\t" \
    "v_smfmac_f32_16x16x64_bf16 v[242:245], v[238:241], v[222:229], v212\n\t" \
    "ds_read_b128 v[222:225], v254 offset:22528\n\t" \
    "ds_read_b128 v[226:229], v254 offset:23552\n\t" \
    "v_smfmac_f32_16x16x64_bf16 v[246:249], v[238:241], v[230:237], v212\n\t" \
    "ds_read_b128 v[230:233], v254 offset:28672\n\t" \
    "ds_read_b128 v[234:237], v254 offset:29696\n\t" \
    "s_waitcnt lgkmcnt(6)\n\t" \
    "v_smfmac_f32_16x16x64_bf16 v[250:253], v[208:211], v[200:207], v212\n\t" \
    "ds_read_b128 v[208:211], v254 offset:2048\n\t" \
    "ds_read_b128 v[200:203], v213 offset:5120\n\t" \
    "ds_read_b128 v[204:207], v213 offset:5248\n\t" \
    "s_waitcnt lgkmcnt(7)\n\t" \
    "v_smfmac_f32_16x16x64_bf16 v[250:253], v[238:241], v[214:221], v212\n\t" \
    "v_lshrrev_b32 v238, 3, v254\n\t" \
    "v_and_b32 v238, 0x6e, v238\n\t" \
    "v_lshrrev_b32 v239, 16, v255\n\t" \
    "s_waitcnt lgkmcnt(2)\n\t" \
    "v_smfmac_f32_16x16x64_bf16 v[242:245], v[208:211], v[222:229], v212\n\t" \
    "v_smfmac_f32_16x16x64_bf16 v[246:249], v[208:211], v[230:237], v212\n\t" \
    "s_waitcnt lgkmcnt(0)\n\t" \
    "v_smfmac_f32_16x16x64_bf16 v[250:253], v[208:211], v[200:207], v212\n\t" \
    "s_nop 1\n\t" \
    "v_add_f32 v242, v242, v244\n\t" \
    "v_add_f32 v243, v243, v245\n\t" \
    "s_nop 0\n\t" \
    "v_add_f32 v246, v246, v248\n\t" \
    "v_add_f32 v247, v247, v249\n\t" \
    "s_nop 0\n\t" \
    "v_add_f32 v250, v250, v252\n\t" \
    "v_add_f32 v251, v251, v253\n\t" \
    "v_max3_f32 v244, v242, v246, v250\n\t" \
    "v_max3_f32 v245, v243, v247, v251\n\t" \
    "s_nop 0\n\t" \
    "v_max_f32_dpp v244, v244, v244 quad_perm:[1,0,3,2] row_mask:0xf bank_mask:0xf\n\t" \
    "v_max_f32_dpp v245, v245, v245 quad_perm:[1,0,3,2] row_mask:0xf bank_mask:0xf\n\t" \
    "s_nop 0\n\t" \
    "v_max_f32_dpp v244, v244, v244 quad_perm:[2,3,0,1] row_mask:0xf bank_mask:0xf\n\t" \
    "v_max_f32_dpp v245, v245, v245 quad_perm:[2,3,0,1] row_mask:0xf bank_mask:0xf\n\t" \
    "s_nop 0\n\t" \
    "v_max_f32_dpp v244, v244, v244 row_half_mirror row_mask:0xf bank_mask:0xf\n\t" \
    "v_max_f32_dpp v245, v245, v245 row_half_mirror row_mask:0xf bank_mask:0xf\n\t" \
    "s_nop 0\n\t" \
    "v_max_f32_dpp v244, v244, v244 row_mirror row_mask:0xf bank_mask:0xf\n\t" \
    "v_max_f32_dpp v245, v245, v245 row_mirror row_mask:0xf bank_mask:0xf\n\t" \
    "v_mul_f32 v244, %[scl], v244\n\t" \
    "v_mul_f32 v245, %[scl], v245\n\t" \
    "v_fma_f32 v242, v242, %[scl], -v244\n\t" \
    "v_fma_f32 v243, v243, %[scl], -v245\n\t" \
    "v_fma_f32 v246, v246, %[scl], -v244\n\t" \
    "v_fma_f32 v247, v247, %[scl], -v245\n\t" \
    "v_exp_f32 v242, v242\n\t" \
    "v_exp_f32 v243, v243\n\t" \
    "v_fma_f32 v250, v250, %[scl], -v244\n\t" \
    "v_fma_f32 v251, v251, %[scl], -v245\n\t" \
    "v_exp_f32 v246, v246\n\t" \
    "v_exp_f32 v247, v247\n\t" \
    "v_exp_f32 v250, v250\n\t" \
    "v_exp_f32 v251, v251\n\t" \
    "v_add_f32 v244, v242, v246\n\t" \
    "v_add_f32 v245, v243, v247\n\t" \
    "v_fma_f32 v244, v250, 0.5, v244\n\t" \
    "v_fma_f32 v245, v251, 0.5, v245\n\t" \
    "s_nop 0\n\t" \
    "v_add_f32_dpp v244, v244, v244 quad_perm:[1,0,3,2] row_mask:0xf bank_mask:0xf\n\t" \
    "v_add_f32_dpp v245, v245, v245 quad_perm:[1,0,3,2] row_mask:0xf bank_mask:0xf\n\t" \
    "s_nop 0\n\t" \
    "v_add_f32_dpp v244, v244, v244 quad_perm:[2,3,0,1] row_mask:0xf bank_mask:0xf\n\t" \
    "v_add_f32_dpp v245, v245, v245 quad_perm:[2,3,0,1] row_mask:0xf bank_mask:0xf\n\t" \
    "s_nop 0\n\t" \
    "v_add_f32_dpp v244, v244, v244 row_half_mirror row_mask:0xf bank_mask:0xf\n\t" \
    "v_add_f32_dpp v245, v245, v245 row_half_mirror row_mask:0xf bank_mask:0xf\n\t" \
    "s_nop 0\n\t" \
    "v_add_f32_dpp v244, v244, v244 row_mirror row_mask:0xf bank_mask:0xf\n\t" \
    "v_add_f32_dpp v245, v245, v245 row_mirror row_mask:0xf bank_mask:0xf\n\t" \
    "v_rcp_f32 v244, v244\n\t" \
    "v_rcp_f32 v245, v245\n\t" \
    "s_wakeup\n\t" \
    "v_mul_f32 v250, v250, v244\n\t" \
    "v_mul_f32 v251, v251, v245\n\t" \
    "v_mul_f32 v242, v242, v244\n\t" \
    "v_mul_f32 v243, v243, v245\n\t" \
    "v_mul_f32 v246, v246, v244\n\t" \
    "v_mul_f32 v247, v247, v245\n\t" \
    "v_cvt_pk_bf16_f32 v250, |v250|, |v251|\n\t" \
    "ds_write_b16 v238, v250 offset:52096\n\t" \
    "ds_write_b16_d16_hi v238, v250 offset:52112\n\t" \
    "v_cvt_pk_bf16_f32 v242, |v242|, |v246|\n\t" \
    "v_cvt_pk_bf16_f32 v243, |v243|, |v247|\n\t" \
    "ds_write2_b32 v239, v242, v243 offset1:4\n\t" \
    "ds_read_b128 v[200:203], v254 offset:896\n\t" \
    "v_and_b32 v253, 0x3ff, v254\n\t" \
    "s_mov_b32 exec_hi, 0xff\n\t" \
    "s_waitcnt lgkmcnt(0)\n\t" \
    "buffer_store_dwordx4 v[200:203], v253, s[60:63], s50 offen sc1\n\t" \
    "buffer_store_dwordx4 v[200:203], v253, s[60:63], s51 offen sc1\n\t" \
    "buffer_store_dwordx4 v[200:203], v253, s[60:63], s52 offen sc1\n\t" \
    "buffer_store_dwordx4 v[200:203], v253, s[60:63], s53 offen sc1\n\t" \
    "s_mov_b32 exec_hi, -1\n\t" \
    "s_setprio 0\n\t" \
    "ds_read_b128 v[224:227], v253 offset:0\n\t" \
    "ds_read_b128 v[228:231], v253 offset:1024\n\t" \
    "ds_read_b128 v[232:235], v253 offset:20480\n\t" \
    "ds_read_b128 v[236:239], v253 offset:21504\n\t" \
    "s_add_u32 s40, s40, 1\n\t" \
    "s_cmp_lg_u32 s40, 64\n\t" \
    "s_cbranch_scc1 .Ls03_%=\n\t" \
    "s_branch .Lend%=\n\t" \
    ".Lend%=:\n\t" \
    "s_waitcnt vmcnt(0) lgkmcnt(0)\n\t" \
    "s_mov_b32 %[bud], s41\n\t"
#define ASM_V_LOOP \
    "s_mov_b64 s[60:61], %[ws]\n\t" \
    "s_mov_b32 s62, %[sz]\n\t" \
    "s_mov_b32 s63, 0x20000\n\t" \
    "s_mov_b32 s40, 0\n\t" \
    "s_mov_b32 s41, 0x20000\n\t" \
    "s_cmp_eq_u32 %[wave], 0\n\t" \
    "s_cbranch_scc1 .Lv0_%=\n\t" \
    "s_cmp_eq_u32 %[wave], 1\n\t" \
    "s_cbranch_scc1 .Lv1_%=\n\t" \
    "s_cmp_eq_u32 %[wave], 2\n\t" \
    "s_cbranch_scc1 .Lv2_%=\n\t" \
    "s_cmp_eq_u32 %[wave], 3\n\t" \
    "s_cbranch_scc1 .Lv3_%=\n\t" \
    ".Lv4_%=:\n\t" \
    "s_barrier \n\t" \
    "s_waitcnt vmcnt(15)\n\t" \
    "ds_read_b64_tr_b16 v[200:201], v245 offset:0\n\t" \
    "ds_read_b64_tr_b16 v[16:17], v245 offset:32\n\t" \
    "ds_read_b64_tr_b16 v[208:209], v245 offset:64\n\t" \
    "ds_read_b64_tr_b16 v[56:57], v245 offset:96\n\t" \
    "ds_read_b64_tr_b16 v[216:217], v245 offset:128\n\t" \
    "ds_read_b64_tr_b16 v[96:97], v245 offset:160\n\t" \
    "ds_read_b64_tr_b16 v[224:225], v245 offset:192\n\t" \
    "ds_read_b64_tr_b16 v[136:137], v245 offset:224\n\t" \
    "ds_read_b64_tr_b16 v[232:233], v245 offset:256\n\t" \
    "ds_read_b64_tr_b16 v[176:177], v245 offset:288\n\t" \
    "s_waitcnt lgkmcnt(0)\n\t" \
    "s_add_u32 m0, %[wm], 0x0\n\t" \
    "s_mov_b32 s44, 0x14000\n\t" \
    "buffer_load_dwordx4 v240, %[wr], s44 offen offset:0 nt lds\n\t" \
    "buffer_load_dwordx4 v241, %[wr], s44 offen offset:1024 nt lds\n\t" \
    "buffer_load_dwordx4 v242, %[wr], s44 offen offset:2048 nt lds\n\t" \
    "buffer_load_dwordx4 v243, %[wr], s44 offen offset:3072 nt lds\n\t" \
    "s_add_u32 m0, %[wm], 0x1000\n\t" \
    "s_nop 0\n\t" \
    "buffer_load_dwordx4 v244, %[wr], s44 offen nt lds\n\t" \
    "s_barrier \n\t" \
    "s_barrier \n\t" \
    "s_waitcnt vmcnt(15)\n\t" \
    "ds_read_b64_tr_b16 v[204:205], v245 offset:5120\n\t" \
    "ds_read_b64_tr_b16 v[20:21], v245 offset:5152\n\t" \
    "ds_read_b64_tr_b16 v[212:213], v245 offset:5184\n\t" \
    "ds_read_b64_tr_b16 v[60:61], v245 offset:5216\n\t" \
    "ds_read_b64_tr_b16 v[220:221], v245 offset:5248\n\t" \
    "ds_read_b64_tr_b16 v[100:101], v245 offset:5280\n\t" \
    "ds_read_b64_tr_b16 v[228:229], v245 offset:5312\n\t" \
    "ds_read_b64_tr_b16 v[140:141], v245 offset:5344\n\t" \
    "ds_read_b64_tr_b16 v[236:237], v245 offset:5376\n\t" \
    "ds_read_b64_tr_b16 v[180:181], v245 offset:5408\n\t" \
    "s_waitcnt lgkmcnt(0)\n\t" \
    "s_add_u32 m0, %[wm], 0x1400\n\t" \
    "s_mov_b32 s44, 0x19000\n\t" \
    "buffer_load_dwordx4 v240, %[wr], s44 offen offset:0 nt lds\n\t" \
    "buffer_load_dwordx4 v241, %[wr], s44 offen offset:1024 nt lds\n\t" \
    "buffer_load_dwordx4 v242, %[wr], s44 offen offset:2048 nt lds\n\t" \
    "buffer_load_dwordx4 v243, %[wr], s44 offen offset:3072 nt lds\n\t" \
    "s_add_u32 m0, %[wm], 0x2400\n\t" \
    "s_nop 0\n\t" \
    "buffer_load_dwordx4 v244, %[wr], s44 offen nt lds\n\t" \
    "s_barrier \n\t" \
    "s_barrier \n\t" \
    "s_waitcnt vmcnt(15)\n\t" \
    "ds_read_b64_tr_b16 v[0:1], v245 offset:10240\n\t" \
    "ds_read_b64_tr_b16 v[24:25], v245 offset:10272\n\t" \
    "ds_read_b64_tr_b16 v[40:41], v245 offset:10304\n\t" \
    "ds_read_b64_tr_b16 v[64:65], v245 offset:10336\n\t" \
    "ds_read_b64_tr_b16 v[80:81], v245 offset:10368\n\t" \
    "ds_read_b64_tr_b16 v[104:105], v245 offset:10400\n\t" \
    "ds_read_b64_tr_b16 v[120:121], v245 offset:10432\n\t" \
    "ds_read_b64_tr_b16 v[144:145], v245 offset:10464\n\t" \
    "ds_read_b64_tr_b16 v[160:161], v245 offset:10496\n\t" \
    "ds_read_b64_tr_b16 v[184:185], v245 offset:10528\n\t" \
    "s_waitcnt lgkmcnt(0)\n\t" \
    "s_add_u32 m0, %[wm], 0x2800\n\t" \
    "s_mov_b32 s44, 0x1e000\n\t" \
    "buffer_load_dwordx4 v240, %[wr], s44 offen offset:0 nt lds\n\t" \
    "buffer_load_dwordx4 v241, %[wr], s44 offen offset:1024 nt lds\n\t" \
    "buffer_load_dwordx4 v242, %[wr], s44 offen offset:2048 nt lds\n\t" \
    "buffer_load_dwordx4 v243, %[wr], s44 offen offset:3072 nt lds\n\t" \
    "s_add_u32 m0, %[wm], 0x3800\n\t" \
    "s_nop 0\n\t" \
    "buffer_load_dwordx4 v244, %[wr], s44 offen nt lds\n\t" \
    "s_barrier \n\t" \
    "s_waitcnt vmcnt(15)\n\t" \
    "ds_read_b64_tr_b16 v[4:5], v245 offset:15360\n\t" \
    "ds_read_b64_tr_b16 v[28:29], v245 offset:15392\n\t" \
    "ds_read_b64_tr_b16 v[44:45], v245 offset:15424\n\t" \
    "ds_read_b64_tr_b16 v[68:69], v245 offset:15456\n\t" \
    "ds_read_b64_tr_b16 v[84:85], v245 offset:15488\n\t" \
    "ds_read_b64_tr_b16 v[108:109], v245 offset:15520\n\t" \
    "ds_read_b64_tr_b16 v[124:125], v245 offset:15552\n\t" \
    "ds_read_b64_tr_b16 v[148:149], v245 offset:15584\n\t" \
    "ds_read_b64_tr_b16 v[164:165], v245 offset:15616\n\t" \
    "ds_read_b64_tr_b16 v[188:189], v245 offset:15648\n\t" \
    "s_waitcnt lgkmcnt(0)\n\t" \
    "s_add_u32 m0, %[wm], 0x3c00\n\t" \
    "s_mov_b32 s44, 0x23000\n\t" \
    "buffer_load_dwordx4 v240, %[wr], s44 offen offset:0 nt lds\n\t" \
    "buffer_load_dwordx4 v241, %[wr], s44 offen offset:1024 nt lds\n\t" \
    "buffer_load_dwordx4 v242, %[wr], s44 offen offset:2048 nt lds\n\t" \
    "buffer_load_dwordx4 v243, %[wr], s44 offen offset:3072 nt lds\n\t" \
    "s_add_u32 m0, %[wm], 0x4c00\n\t" \
    "s_nop 0\n\t" \
    "buffer_load_dwordx4 v244, %[wr], s44 offen nt lds\n\t" \
    "s_barrier \n\t" \
    "s_barrier \n\t" \
    "s_waitcnt vmcnt(15)\n\t" \
    "ds_read_b64_tr_b16 v[8:9], v245 offset:0\n\t" \
    "ds_read_b64_tr_b16 v[32:33], v245 offset:32\n\t" \
    "ds_read_b64_tr_b16 v[48:49], v245 offset:64\n\t" \
    "ds_read_b64_tr_b16 v[72:73], v245 offset:96\n\t" \
    "ds_read_b64_tr_b16 v[88:89], v245 offset:128\n\t" \
    "ds_read_b64_tr_b16 v[112:113], v245 offset:160\n\t" \
    "ds_read_b64_tr_b16 v[128:129], v245 offset:192\n\t" \
    "ds_read_b64_tr_b16 v[152:153], v245 offset:224\n\t" \
    "ds_read_b64_tr_b16 v[168:169], v245 offset:256\n\t" \
    "ds_read_b64_tr_b16 v[192:193], v245 offset:288\n\t" \
    "s_waitcnt lgkmcnt(0)\n\t" \
    "s_add_u32 m0, %[wm], 0x0\n\t" \
    "s_mov_b32 s44, 0x28000\n\t" \
    "buffer_load_dwordx4 v240, %[wr], s44 offen offset:0 nt lds\n\t" \
    "buffer_load_dwordx4 v241, %[wr], s44 offen offset:1024 nt lds\n\t" \
    "buffer_load_dwordx4 v242, %[wr], s44 offen offset:2048 nt lds\n\t" \
    "buffer_load_dwordx4 v243, %[wr], s44 offen offset:3072 nt lds\n\t" \
    "s_add_u32 m0, %[wm], 0x1000\n\t" \
    "s_nop 0\n\t" \
    "buffer_load_dwordx4 v244, %[wr], s44 offen nt lds\n\t" \
    "s_barrier \n\t" \
    "s_barrier \n\t" \
    "s_waitcnt vmcnt(15)\n\t" \
    "ds_read_b64_tr_b16 v[12:13], v245 offset:5120\n\t" \
    "ds_read_b64_tr_b16 v[36:37], v245 offset:5152\n\t" \
    "ds_read_b64_tr_b16 v[52:53], v245 offset:5184\n\t" \
    "ds_read_b64_tr_b16 v[76:77], v245 offset:5216\n\t" \
    "ds_read_b64_tr_b16 v[92:93], v245 offset:5248\n\t" \
    "ds_read_b64_tr_b16 v[116:117], v245 offset:5280\n\t" \
    "ds_read_b64_tr_b16 v[132:133], v245 offset:5312\n\t" \
    "ds_read_b64_tr_b16 v[156:157], v245 offset:5344\n\t" \
    "ds_read_b64_tr_b16 v[172:173], v245 offset:5376\n\t" \
    "ds_read_b64_tr_b16 v[196:197], v245 offset:5408\n\t" \
    "s_waitcnt lgkmcnt(0)\n\t" \
    "s_add_u32 m0, %[wm], 0x1400\n\t" \
    "s_mov_b32 s44, 0x2d000\n\t" \
    "buffer_load_dwordx4 v240, %[wr], s44 offen offset:0 nt lds\n\t" \
    "buffer_load_dwordx4 v241, %[wr], s44 offen offset:1024 nt lds\n\t" \
    "buffer_load_dwordx4 v242, %[wr], s44 offen offset:2048 nt lds\n\t" \
    "buffer_load_dwordx4 v243, %[wr], s44 offen offset:3072 nt lds\n\t" \
    "s_add_u32 m0, %[wm], 0x2400\n\t" \
    "s_nop 0\n\t" \
    "buffer_load_dwordx4 v244, %[wr], s44 offen nt lds\n\t" \
    "s_barrier \n\t" \
    "s_waitcnt vmcnt(15)\n\t" \
    "ds_read_b64_tr_b16 v[202:203], v245 offset:10240\n\t" \
    "ds_read_b64_tr_b16 v[18:19], v245 offset:10272\n\t" \
    "ds_read_b64_tr_b16 v[210:211], v245 offset:10304\n\t" \
    "ds_read_b64_tr_b16 v[58:59], v245 offset:10336\n\t" \
    "ds_read_b64_tr_b16 v[218:219], v245 offset:10368\n\t" \
    "ds_read_b64_tr_b16 v[98:99], v245 offset:10400\n\t" \
    "ds_read_b64_tr_b16 v[226:227], v245 offset:10432\n\t" \
    "ds_read_b64_tr_b16 v[138:139], v245 offset:10464\n\t" \
    "ds_read_b64_tr_b16 v[234:235], v245 offset:10496\n\t" \
    "ds_read_b64_tr_b16 v[178:179], v245 offset:10528\n\t" \
    "v_mov_b32 v246, 0\n\t" \
    "buffer_load_dword v246, v246, s[60:63], %[mo] offen sc1\n\t" \
    "s_waitcnt lgkmcnt(0)\n\t" \
    "s_add_u32 m0, %[wm], 0x2800\n\t" \
    "s_mov_b32 s44, 0x32000\n\t" \
    "buffer_load_dwordx4 v240, %[wr], s44 offen offset:0 nt lds\n\t" \
    "buffer_load_dwordx4 v241, %[wr], s44 offen offset:1024 nt lds\n\t" \
    "buffer_load_dwordx4 v242, %[wr], s44 offen offset:2048 nt lds\n\t" \
    "buffer_load_dwordx4 v243, %[wr], s44 offen offset:3072 nt lds\n\t" \
    "s_add_u32 m0, %[wm], 0x3800\n\t" \
    "s_nop 0\n\t" \
    "buffer_load_dwordx4 v244, %[wr], s44 offen nt lds\n\t" \
    "s_barrier \n\t" \
    "s_barrier \n\t" \
    "s_waitcnt vmcnt(15)\n\t" \
    "ds_read_b64_tr_b16 v[206:207], v245 offset:15360\n\t" \
    "ds_read_b64_tr_b16 v[22:23], v245 offset:15392\n\t" \
    "ds_read_b64_tr_b16 v[214:215], v245 offset:15424\n\t" \
    "ds_read_b64_tr_b16 v[62:63], v245 offset:15456\n\t" \
    "ds_read_b64_tr_b16 v[222:223], v245 offset:15488\n\t" \
    "ds_read_b64_tr_b16 v[102:103], v245 offset:15520\n\t" \
    "ds_read_b64_tr_b16 v[230:231], v245 offset:15552\n\t" \
    "ds_read_b64_tr_b16 v[142:143], v245 offset:15584\n\t" \
    "ds_read_b64_tr_b16 v[238:239], v245 offset:15616\n\t" \
    "ds_read_b64_tr_b16 v[182:183], v245 offset:15648\n\t" \
    "s_waitcnt lgkmcnt(0)\n\t" \
    "s_add_u32 m0, %[wm], 0x3c00\n\t" \
    "s_mov_b32 s44, 0x37000\n\t" \
    "buffer_load_dwordx4 v240, %[wr], s44 offen offset:0 nt lds\n\t" \
    "buffer_load_dwordx4 v241, %[wr], s44 offen offset:1024 nt lds\n\t" \
    "buffer_load_dwordx4 v242, %[wr], s44 offen offset:2048 nt lds\n\t" \
    "buffer_load_dwordx4 v243, %[wr], s44 offen offset:3072 nt lds\n\t" \
    "s_add_u32 m0, %[wm], 0x4c00\n\t" \
    "s_nop 0\n\t" \
    "buffer_load_dwordx4 v244, %[wr], s44 offen nt lds\n\t" \
    "s_barrier \n\t" \
    "s_barrier \n\t" \
    "s_waitcnt vmcnt(15)\n\t" \
    "ds_read_b64_tr_b16 v[2:3], v245 offset:0\n\t" \
    "ds_read_b64_tr_b16 v[26:27], v245 offset:32\n\t" \
    "ds_read_b64_tr_b16 v[42:43], v245 offset:64\n\t" \
    "ds_read_b64_tr_b16 v[66:67], v245 offset:96\n\t" \
    "ds_read_b64_tr_b16 v[82:83], v245 offset:128\n\t" \
    "ds_read_b64_tr_b16 v[106:107], v245 offset:160\n\t" \
    "ds_read_b64_tr_b16 v[122:123], v245 offset:192\n\t" \
    "ds_read_b64_tr_b16 v[146:147], v245 offset:224\n\t" \
    "ds_read_b64_tr_b16 v[162:163], v245 offset:256\n\t" \
    "ds_read_b64_tr_b16 v[186:187], v245 offset:288\n\t" \
    "s_barrier \n\t" \
    "s_waitcnt vmcnt(10)\n\t" \
    "ds_read_b64_tr_b16 v[6:7], v245 offset:5120\n\t" \
    "ds_read_b64_tr_b16 v[30:31], v245 offset:5152\n\t" \
    "ds_read_b64_tr_b16 v[46:47], v245 offset:5184\n\t" \
    "ds_read_b64_tr_b16 v[70:71], v245 offset:5216\n\t" \
    "ds_read_b64_tr_b16 v[86:87], v245 offset:5248\n\t" \
    "ds_read_b64_tr_b16 v[110:111], v245 offset:5280\n\t" \
    "ds_read_b64_tr_b16 v[126:127], v245 offset:5312\n\t" \
    "ds_read_b64_tr_b16 v[150:151], v245 offset:5344\n\t" \
    "ds_read_b64_tr_b16 v[166:167], v245 offset:5376\n\t" \
    "ds_read_b64_tr_b16 v[190:191], v245 offset:5408\n\t" \
    "s_barrier \n\t" \
    "s_barrier \n\t" \
    "s_waitcnt vmcnt(5)\n\t" \
    "ds_read_b64_tr_b16 v[10:11], v245 offset:10240\n\t" \
    "ds_read_b64_tr_b16 v[34:35], v245 offset:10272\n\t" \
    "ds_read_b64_tr_b16 v[50:51], v245 offset:10304\n\t" \
    "ds_read_b64_tr_b16 v[74:75], v245 offset:10336\n\t" \
    "ds_read_b64_tr_b16 v[90:91], v245 offset:10368\n\t" \
    "ds_read_b64_tr_b16 v[114:115], v245 offset:10400\n\t" \
    "ds_read_b64_tr_b16 v[130:131], v245 offset:10432\n\t" \
    "ds_read_b64_tr_b16 v[154:155], v245 offset:10464\n\t" \
    "ds_read_b64_tr_b16 v[170:171], v245 offset:10496\n\t" \
    "ds_read_b64_tr_b16 v[194:195], v245 offset:10528\n\t" \
    ".Lv41_%=:\n\t" \
    "v_readfirstlane_b32 s43, v246\n\t" \
    "s_nop 0\n\t" \
    "s_cmp_lg_u32 s43, -1\n\t" \
    "s_cbranch_scc1 .Lv42_%=\n\t" \
    "s_cmp_eq_u32 s41, 0\n\t" \
    "s_cbranch_scc1 .Lv42_%=\n\t" \
    "s_sub_u32 s41, s41, 1\n\t" \
    "s_sleep 4\n\t" \
    "v_mov_b32 v246, 0\n\t" \
    "buffer_load_dword v246, v246, s[60:63], %[mo] offen sc1\n\t" \
    "s_waitcnt vmcnt(0)\n\t" \
    "s_branch .Lv41_%=\n\t" \
    ".Lv42_%=:\n\t" \
    "v_mov_b32 v246, 0x8000\n\t" \
    "s_mov_b64 exec, 1\n\t" \
    "buffer_store_dword v246, v246, s[60:63], %[mo] offen sc1\n\t" \
    "s_mov_b64 exec, -1\n\t" \
    "s_and_b32 s44, s43, 0x5000\n\t" \
    "s_xor_b32 %[mb], %[mb], s44\n\t" \
    "s_xor_b32 %[fl], %[fl], s44\n\t" \
    "s_sub_u32 %[fl], %[fl], 0xc800\n\t" \
    "s_xor_b32 %[pb], %[pb], s44\n\t" \
    "s_and_b32 s43, s43, 0x2000\n\t" \
    "s_xor_b32 s43, s43, %[lx]\n\t" \
    "s_add_u32 s60, s60, s43\n\t" \
    "s_addc_u32 s61, s61, 0\n\t" \
    "s_barrier \n\t" \
    "s_barrier \n\t" \
    "s_waitcnt vmcnt(0)\n\t" \
    "ds_read_b64_tr_b16 v[14:15], v245 offset:15360\n\t" \
    "ds_read_b64_tr_b16 v[38:39], v245 offset:15392\n\t" \
    "ds_read_b64_tr_b16 v[54:55], v245 offset:15424\n\t" \
    "ds_read_b64_tr_b16 v[78:79], v245 offset:15456\n\t" \
    "ds_read_b64_tr_b16 v[94:95], v245 offset:15488\n\t" \
    "ds_read_b64_tr_b16 v[118:119], v245 offset:15520\n\t" \
    "ds_read_b64_tr_b16 v[134:135], v245 offset:15552\n\t" \
    "ds_read_b64_tr_b16 v[158:159], v245 offset:15584\n\t" \
    "ds_read_b64_tr_b16 v[174:175], v245 offset:15616\n\t" \
    "ds_read_b64_tr_b16 v[198:199], v245 offset:15648\n\t" \
    "s_waitcnt lgkmcnt(0)\n\t" \
    "s_barrier \n\t" \
    "ds_write_b128 v254, v[200:203] offset:0\n\t" \
    "ds_write_b128 v254, v[204:207] offset:1024\n\t" \
    "ds_write_b128 v254, v[208:211] offset:2048\n\t" \
    "ds_write_b128 v254, v[212:215] offset:3072\n\t" \
    "ds_write_b128 v254, v[216:219] offset:4096\n\t" \
    "ds_write_b128 v254, v[220:223] offset:5120\n\t" \
    "ds_write_b128 v254, v[224:227] offset:6144\n\t" \
    "ds_write_b128 v254, v[228:231] offset:7168\n\t" \
    "ds_write_b128 v254, v[232:235] offset:8192\n\t" \
    "ds_write_b128 v254, v[236:239] offset:9216\n\t" \
    "s_waitcnt vmcnt(0) lgkmcnt(0)\n\t" \
    "s_barrier \n\t" \
    "v_mov_b64 v[200:201], 0\n\t" \
    "v_mov_b64 v[202:203], 0\n\t" \
    "ds_write_b128 v253, v[200:203] offset:3072\n\t" \
    "ds_write_b128 v253, v[200:203] offset:4096\n\t" \
    "v_bfe_i32 v252, v253, 5, 1\n\t" \
    "v_and_b32 v252, 0xaaaa, v252\n\t" \
    "v_or_b32 v252, 0x4444, v252\n\t" \
    ".Lv43_%=:\n\t" \
    "ds_read_b128 v[248:251], v254 offset:0\n\t" \
    "s_and_b32 s44, s40, 3\n\t" \
    "s_lshl_b32 s44, s44, 22\n\t" \
    "s_add_u32 s54, s44, %[mb]\n\t" \
    "s_xor_b32 s55, s54, 0x4000\n\t" \
    "s_xor_b32 s56, s54, 0x1000\n\t" \
    "s_xor_b32 s57, s54, 0x5000\n\t" \
    "s_waitcnt lgkmcnt(0)\n\t" \
    "s_barrier \n\t" \
    "ds_read_b128 v[224:227], v253 offset:6144\n\t" \
    "ds_read_b128 v[232:235], v253 offset:8192\n\t" \
    "ds_read_b128 v[236:239], v253 offset:9216\n\t" \
    "ds_read_b128 v[240:243], v253 offset:10240\n\t" \
    "ds_read_b128 v[244:247], v253 offset:11264\n\t" \
    "ds_read_b128 v[228:231], v253 offset:7168\n\t" \
    "s_waitcnt lgkmcnt(5)\n\t" \
    "v_mfma_f32_16x16x32_bf16 v[200:203], v[224:227], v[16:19], 0\n\t" \
    "s_waitcnt lgkmcnt(4)\n\t" \
    "v_mfma_f32_16x16x32_bf16 v[200:203], v[232:235], v[24:27], v[200:203]\n\t" \
    "s_waitcnt lgkmcnt(3)\n\t" \
    "v_mfma_f32_16x16x32_bf16 v[200:203], v[236:239], v[28:31], v[200:203]\n\t" \
    "s_waitcnt lgkmcnt(2)\n\t" \
    "v_mfma_f32_16x16x32_bf16 v[200:203], v[240:243], v[32:35], v[200:203]\n\t" \
    "s_waitcnt lgkmcnt(1)\n\t" \
    "v_mfma_f32_16x16x32_bf16 v[200:203], v[244:247], v[36:39], v[200:203]\n\t" \
    "s_waitcnt lgkmcnt(0)\n\t" \
    "v_mfma_f32_16x16x32_bf16 v[200:203], v[228:231], v[20:23], v[200:203]\n\t" \
    "v_mfma_f32_16x16x32_bf16 v[204:207], v[224:227], v[248:251], 0\n\t" \
    "ds_read_b128 v[248:251], v254 offset:1024\n\t" \
    "v_mfma_f32_16x16x32_bf16 v[204:207], v[232:235], v[0:3], v[204:207]\n\t" \
    "v_mfma_f32_16x16x32_bf16 v[204:207], v[236:239], v[4:7], v[204:207]\n\t" \
    "v_mfma_f32_16x16x32_bf16 v[204:207], v[240:243], v[8:11], v[204:207]\n\t" \
    "v_mfma_f32_16x16x32_bf16 v[204:207], v[244:247], v[12:15], v[204:207]\n\t" \
    "s_waitcnt lgkmcnt(0)\n\t" \
    "v_mfma_f32_16x16x32_bf16 v[204:207], v[228:231], v[248:251], v[204:207]\n\t" \
    "ds_read_b128 v[248:251], v254 offset:2048\n\t" \
    "s_barrier \n\t" \
    "v_mfma_f32_16x16x32_bf16 v[208:211], v[224:227], v[56:59], 0\n\t" \
    "v_mfma_f32_16x16x32_bf16 v[208:211], v[232:235], v[64:67], v[208:211]\n\t" \
    "v_mfma_f32_16x16x32_bf16 v[208:211], v[236:239], v[68:71], v[208:211]\n\t" \
    "v_mfma_f32_16x16x32_bf16 v[208:211], v[240:243], v[72:75], v[208:211]\n\t" \
    "v_mfma_f32_16x16x32_bf16 v[208:211], v[244:247], v[76:79], v[208:211]\n\t" \
    "v_mfma_f32_16x16x32_bf16 v[208:211], v[228:231], v[60:63], v[208:211]\n\t" \
    "s_waitcnt lgkmcnt(0)\n\t" \
    "v_mfma_f32_16x16x32_bf16 v[212:215], v[224:227], v[248:251], 0\n\t" \
    "ds_read_b128 v[248:251], v254 offset:3072\n\t" \
    "ds_read_b128 v[224:227], v253 offset:12288\n\t" \
    "v_mfma_f32_16x16x32_bf16 v[212:215], v[232:235], v[40:43], v[212:215]\n\t" \
    "ds_read_b128 v[232:235], v253 offset:14336\n\t" \
    "v_mfma_f32_16x16x32_bf16 v[212:215], v[236:239], v[44:47], v[212:215]\n\t" \
    "ds_read_b128 v[236:239], v253 offset:15360\n\t" \
    "v_mfma_f32_16x16x32_bf16 v[212:215], v[240:243], v[48:51], v[212:215]\n\t" \
    "ds_read_b128 v[240:243], v253 offset:16384\n\t" \
    "v_mfma_f32_16x16x32_bf16 v[212:215], v[244:247], v[52:55], v[212:215]\n\t" \
    "ds_read_b128 v[244:247], v253 offset:17408\n\t" \
    "s_waitcnt lgkmcnt(5)\n\t" \
    "v_mfma_f32_16x16x32_bf16 v[212:215], v[228:231], v[248:251], v[212:215]\n\t" \
    "ds_read_b128 v[248:251], v254 offset:4096\n\t" \
    "ds_read_b128 v[228:231], v253 offset:13312\n\t" \
    "s_waitcnt lgkmcnt(6)\n\t" \
    "v_mfma_f32_16x16x32_bf16 v[216:219], v[224:227], v[16:19], 0\n\t" \
    "s_waitcnt lgkmcnt(5)\n\t" \
    "v_mfma_f32_16x16x32_bf16 v[216:219], v[232:235], v[24:27], v[216:219]\n\t" \
    "s_waitcnt lgkmcnt(4)\n\t" \
    "v_mfma_f32_16x16x32_bf16 v[216:219], v[236:239], v[28:31], v[216:219]\n\t" \
    "s_waitcnt lgkmcnt(3)\n\t" \
    "v_mfma_f32_16x16x32_bf16 v[216:219], v[240:243], v[32:35], v[216:219]\n\t" \
    "s_waitcnt lgkmcnt(2)\n\t" \
    "v_mfma_f32_16x16x32_bf16 v[216:219], v[244:247], v[36:39], v[216:219]\n\t" \
    "s_waitcnt lgkmcnt(0)\n\t" \
    "v_mfma_f32_16x16x32_bf16 v[216:219], v[228:231], v[20:23], v[216:219]\n\t" \
    "v_mfma_f32_16x16x32_bf16 v[220:223], v[224:227], v[248:251], 0\n\t" \
    "ds_read_b128 v[248:251], v254 offset:5120\n\t" \
    "v_mfma_f32_16x16x32_bf16 v[220:223], v[232:235], v[80:83], v[220:223]\n\t" \
    "v_mfma_f32_16x16x32_bf16 v[220:223], v[236:239], v[84:87], v[220:223]\n\t" \
    "v_cvt_pk_bf16_f32 v200, v200, v216\n\t" \
    "v_cvt_pk_bf16_f32 v201, v201, v217\n\t" \
    "v_mfma_f32_16x16x32_bf16 v[220:223], v[240:243], v[88:91], v[220:223]\n\t" \
    "v_cvt_pk_bf16_f32 v202, v202, v218\n\t" \
    "v_cvt_pk_bf16_f32 v203, v203, v219\n\t" \
    "v_mfma_f32_16x16x32_bf16 v[220:223], v[244:247], v[92:95], v[220:223]\n\t" \
    "s_add_u32 s44, s54, 0x1033800\n\t" \
    "s_waitcnt lgkmcnt(0)\n\t" \
    "v_mfma_f32_16x16x32_bf16 v[220:223], v[228:231], v[248:251], v[220:223]\n\t" \
    "ds_read_b128 v[248:251], v254 offset:0\n\t" \
    "buffer_store_dwordx4 v[200:203], v253, s[60:63], s44 offen sc1\n\t" \
    "v_mfma_f32_16x16x32_bf16 v[216:219], v[224:227], v[56:59], 0\n\t" \
    "v_mfma_f32_16x16x32_bf16 v[216:219], v[232:235], v[64:67], v[216:219]\n\t" \
    "v_mfma_f32_16x16x32_bf16 v[216:219], v[236:239], v[68:71], v[216:219]\n\t" \
    "v_mfma_f32_16x16x32_bf16 v[216:219], v[240:243], v[72:75], v[216:219]\n\t" \
    "v_mfma_f32_16x16x32_bf16 v[216:219], v[244:247], v[76:79], v[216:219]\n\t" \
    "v_mfma_f32_16x16x32_bf16 v[216:219], v[228:231], v[60:63], v[216:219]\n\t" \
    "s_waitcnt lgkmcnt(0)\n\t" \
    "v_mfma_f32_16x16x32_bf16 v[200:203], v[224:227], v[248:251], 0\n\t" \
    "ds_read_b128 v[248:251], v254 offset:1024\n\t" \
    "v_mfma_f32_16x16x32_bf16 v[200:203], v[232:235], v[0:3], v[200:203]\n\t" \
    "v_mfma_f32_16x16x32_bf16 v[200:203], v[236:239], v[4:7], v[200:203]\n\t" \
    "v_cvt_pk_bf16_f32 v208, v208, v216\n\t" \
    "v_cvt_pk_bf16_f32 v209, v209, v217\n\t" \
    "v_mfma_f32_16x16x32_bf16 v[200:203], v[240:243], v[8:11], v[200:203]\n\t" \
    "v_cvt_pk_bf16_f32 v210, v210, v218\n\t" \
    "v_cvt_pk_bf16_f32 v211, v211, v219\n\t" \
    "v_mfma_f32_16x16x32_bf16 v[200:203], v[244:247], v[12:15], v[200:203]\n\t" \
    "s_add_u32 s44, s55, 0x10b3800\n\t" \
    "s_waitcnt lgkmcnt(0)\n\t" \
    "v_mfma_f32_16x16x32_bf16 v[200:203], v[228:231], v[248:251], v[200:203]\n\t" \
    "ds_read_b128 v[248:251], v254 offset:2048\n\t" \
    "buffer_store_dwordx4 v[208:211], v253, s[60:63], s44 offen sc1\n\t" \
    "v_mfma_f32_16x16x32_bf16 v[216:219], v[224:227], v[96:99], 0\n\t" \
    "v_mfma_f32_16x16x32_bf16 v[216:219], v[232:235], v[104:107], v[216:219]\n\t" \
    "v_mfma_f32_16x16x32_bf16 v[216:219], v[236:239], v[108:111], v[216:219]\n\t" \
    "v_cvt_pk_bf16_f32 v204, v204, v200\n\t" \
    "v_mfma_f32_16x16x32_bf16 v[216:219], v[240:243], v[112:115], v[216:219]\n\t" \
    "v_cvt_pk_bf16_f32 v205, v205, v201\n\t" \
    "v_cvt_pk_bf16_f32 v206, v206, v202\n\t" \
    "v_mfma_f32_16x16x32_bf16 v[216:219], v[244:247], v[116:119], v[216:219]\n\t" \
    "v_cvt_pk_bf16_f32 v207, v207, v203\n\t" \
    "v_mfma_f32_16x16x32_bf16 v[216:219], v[228:231], v[100:103], v[216:219]\n\t" \
    "s_add_u32 s44, s54, 0xff3800\n\t" \
    "buffer_store_dwordx4 v[204:207], v253, s[60:63], s44 offen sc1\n\t" \
    "s_waitcnt lgkmcnt(0)\n\t" \
    "v_mfma_f32_16x16x32_bf16 v[208:211], v[224:227], v[248:251], 0\n\t" \
    "ds_read_b128 v[248:251], v254 offset:3072\n\t" \
    "v_mfma_f32_16x16x32_bf16 v[208:211], v[232:235], v[40:43], v[208:211]\n\t" \
    "v_mfma_f32_16x16x32_bf16 v[208:211], v[236:239], v[44:47], v[208:211]\n\t" \
    "v_mfma_f32_16x16x32_bf16 v[208:211], v[240:243], v[48:51], v[208:211]\n\t" \
    "v_mfma_f32_16x16x32_bf16 v[208:211], v[244:247], v[52:55], v[208:211]\n\t" \
    "s_waitcnt lgkmcnt(0)\n\t" \
    "v_mfma_f32_16x16x32_bf16 v[208:211], v[228:231], v[248:251], v[208:211]\n\t" \
    "ds_read_b128 v[248:251], v254 offset:6144\n\t" \
    "v_mfma_f32_16x16x32_bf16 v[200:203], v[224:227], v[136:139], 0\n\t" \
    "v_mfma_f32_16x16x32_bf16 v[200:203], v[232:235], v[144:147], v[200:203]\n\t" \
    "v_mfma_f32_16x16x32_bf16 v[200:203], v[236:239], v[148:151], v[200:203]\n\t" \
    "v_cvt_pk_bf16_f32 v212, v212, v208\n\t" \
    "v_cvt_pk_bf16_f32 v213, v213, v209\n\t" \
    "v_mfma_f32_16x16x32_bf16 v[200:203], v[240:243], v[152:155], v[200:203]\n\t" \
    "v_cvt_pk_bf16_f32 v214, v214, v210\n\t" \
    "v_cvt_pk_bf16_f32 v215, v215, v211\n\t" \
    "v_mfma_f32_16x16x32_bf16 v[200:203], v[244:247], v[156:159], v[200:203]\n\t" \
    "s_add_u32 s44, s55, 0x1073800\n\t" \
    "v_mfma_f32_16x16x32_bf16 v[200:203], v[228:231], v[140:143], v[200:203]\n\t" \
    "buffer_store_dwordx4 v[212:215], v253, s[60:63], s44 offen sc1\n\t" \
    "s_waitcnt lgkmcnt(0)\n\t" \
    "v_mfma_f32_16x16x32_bf16 v[204:207], v[224:227], v[248:251], 0\n\t" \
    "ds_read_b128 v[248:251], v254 offset:7168\n\t" \
    "ds_read_b128 v[224:227], v253 offset:6144\n\t" \
    "v_mfma_f32_16x16x32_bf16 v[204:207], v[232:235], v[120:123], v[204:207]\n\t" \
    "ds_read_b128 v[232:235], v253 offset:8192\n\t" \
    "v_mfma_f32_16x16x32_bf16 v[204:207], v[236:239], v[124:127], v[204:207]\n\t" \
    "ds_read_b128 v[236:239], v253 offset:9216\n\t" \
    "v_mfma_f32_16x16x32_bf16 v[204:207], v[240:243], v[128:131], v[204:207]\n\t" \
    "ds_read_b128 v[240:243], v253 offset:10240\n\t" \
    "v_mfma_f32_16x16x32_bf16 v[204:207], v[244:247], v[132:135], v[204:207]\n\t" \
    "ds_read_b128 v[244:247], v253 offset:11264\n\t" \
    "s_waitcnt lgkmcnt(5)\n\t" \
    "v_mfma_f32_16x16x32_bf16 v[204:207], v[228:231], v[248:251], v[204:207]\n\t" \
    "ds_read_b128 v[248:251], v254 offset:4096\n\t" \
    "ds_read_b128 v[228:231], v253 offset:7168\n\t" \
    "s_waitcnt lgkmcnt(6)\n\t" \
    "v_mfma_f32_16x16x32_bf16 v[208:211], v[224:227], v[96:99], 0\n\t" \
    "s_waitcnt lgkmcnt(5)\n\t" \
    "v_mfma_f32_16x16x32_bf16 v[208:211], v[232:235], v[104:107], v[208:211]\n\t" \
    "s_waitcnt lgkmcnt(4)\n\t" \
    "v_mfma_f32_16x16x32_bf16 v[208:211], v[236:239], v[108:111], v[208:211]\n\t" \
    "s_waitcnt lgkmcnt(3)\n\t" \
    "v_mfma_f32_16x16x32_bf16 v[208:211], v[240:243], v[112:115], v[208:211]\n\t" \
    "s_waitcnt lgkmcnt(2)\n\t" \
    "v_mfma_f32_16x16x32_bf16 v[208:211], v[244:247], v[116:119], v[208:211]\n\t" \
    "s_waitcnt lgkmcnt(0)\n\t" \
    "v_mfma_f32_16x16x32_bf16 v[208:211], v[228:231], v[100:103], v[208:211]\n\t" \
    "v_mfma_f32_16x16x32_bf16 v[212:215], v[224:227], v[248:251], 0\n\t" \
    "ds_read_b128 v[248:251], v254 offset:5120\n\t" \
    "v_mfma_f32_16x16x32_bf16 v[212:215], v[232:235], v[80:83], v[212:215]\n\t" \
    "v_mfma_f32_16x16x32_bf16 v[212:215], v[236:239], v[84:87], v[212:215]\n\t" \
    "v_cvt_pk_bf16_f32 v216, v208, v216\n\t" \
    "v_cvt_pk_bf16_f32 v217, v209, v217\n\t" \
    "v_mfma_f32_16x16x32_bf16 v[212:215], v[240:243], v[88:91], v[212:215]\n\t" \
    "v_cvt_pk_bf16_f32 v218, v210, v218\n\t" \
    "v_cvt_pk_bf16_f32 v219, v211, v219\n\t" \
    "v_mfma_f32_16x16x32_bf16 v[212:215], v[244:247], v[92:95], v[212:215]\n\t" \
    "s_add_u32 s44, s56, 0x1133800\n\t" \
    "s_waitcnt lgkmcnt(0)\n\t" \
    "v_mfma_f32_16x16x32_bf16 v[212:215], v[228:231], v[248:251], v[212:215]\n\t" \
    "ds_read_b128 v[248:251], v254 offset:6144\n\t" \
    "buffer_store_dwordx4 v[216:219], v253, s[60:63], s44 offen sc1\n\t" \
    "v_mfma_f32_16x16x32_bf16 v[208:211], v[224:227], v[176:179], 0\n\t" \
    "v_mfma_f32_16x16x32_bf16 v[208:211], v[232:235], v[184:187], v[208:211]\n\t" \
    "v_mfma_f32_16x16x32_bf16 v[208:211], v[236:239], v[188:191], v[208:211]\n\t" \
    "v_cvt_pk_bf16_f32 v220, v212, v220\n\t" \
    "v_mfma_f32_16x16x32_bf16 v[208:211], v[240:243], v[192:195], v[208:211]\n\t" \
    "v_cvt_pk_bf16_f32 v221, v213, v221\n\t" \
    "v_cvt_pk_bf16_f32 v222, v214, v222\n\t" \
    "v_mfma_f32_16x16x32_bf16 v[208:211], v[244:247], v[196:199], v[208:211]\n\t" \
    "v_cvt_pk_bf16_f32 v223, v215, v223\n\t" \
    "v_mfma_f32_16x16x32_bf16 v[208:211], v[228:231], v[180:183], v[208:211]\n\t" \
    "s_add_u32 s44, s56, 0x10f3800\n\t" \
    "buffer_store_dwordx4 v[220:223], v253, s[60:63], s44 offen sc1\n\t" \
    "s_waitcnt lgkmcnt(0)\n\t" \
    "v_mfma_f32_16x16x32_bf16 v[216:219], v[224:227], v[248:251], 0\n\t" \
    "ds_read_b128 v[248:251], v254 offset:7168\n\t" \
    "v_mfma_f32_16x16x32_bf16 v[216:219], v[232:235], v[120:123], v[216:219]\n\t" \
    "v_mfma_f32_16x16x32_bf16 v[216:219], v[236:239], v[124:127], v[216:219]\n\t" \
    "v_mfma_f32_16x16x32_bf16 v[216:219], v[240:243], v[128:131], v[216:219]\n\t" \
    "v_mfma_f32_16x16x32_bf16 v[216:219], v[244:247], v[132:135], v[216:219]\n\t" \
    "s_waitcnt lgkmcnt(0)\n\t" \
    "v_mfma_f32_16x16x32_bf16 v[216:219], v[228:231], v[248:251], v[216:219]\n\t" \
    "ds_read_b128 v[248:251], v254 offset:8192\n\t" \
    "v_mfma_f32_16x16x32_bf16 v[212:215], v[224:227], v[136:139], 0\n\t" \
    "v_mfma_f32_16x16x32_bf16 v[212:215], v[232:235], v[144:147], v[212:215]\n\t" \
    "v_mfma_f32_16x16x32_bf16 v[212:215], v[236:239], v[148:151], v[212:215]\n\t" \
    "v_cvt_pk_bf16_f32 v204, v216, v204\n\t" \
    "v_cvt_pk_bf16_f32 v205, v217, v205\n\t" \
    "v_mfma_f32_16x16x32_bf16 v[212:215], v[240:243], v[152:155], v[212:215]\n\t" \
    "v_cvt_pk_bf16_f32 v206, v218, v206\n\t" \
    "v_cvt_pk_bf16_f32 v207, v219, v207\n\t" \
    "v_mfma_f32_16x16x32_bf16 v[212:215], v[244:247], v[156:159], v[212:215]\n\t" \
    "s_add_u32 s44, s57, 0x1173800\n\t" \
    "v_mfma_f32_16x16x32_bf16 v[212:215], v[228:231], v[140:143], v[212:215]\n\t" \
    "buffer_store_dwordx4 v[204:207], v253, s[60:63], s44 offen sc1\n\t" \
    "s_waitcnt lgkmcnt(0)\n\t" \
    "v_mfma_f32_16x16x32_bf16 v[220:223], v[224:227], v[248:251], 0\n\t" \
    "ds_read_b128 v[248:251], v254 offset:9216\n\t" \
    "ds_read_b128 v[224:227], v253 offset:12288\n\t" \
    "v_mfma_f32_16x16x32_bf16 v[220:223], v[232:235], v[160:163], v[220:223]\n\t" \
    "ds_read_b128 v[232:235], v253 offset:14336\n\t" \
    "v_mfma_f32_16x16x32_bf16 v[220:223], v[236:239], v[164:167], v[220:223]\n\t" \
    "ds_read_b128 v[236:239], v253 offset:15360\n\t" \
    "v_cvt_pk_bf16_f32 v200, v212, v200\n\t" \
    "v_mfma_f32_16x16x32_bf16 v[220:223], v[240:243], v[168:171], v[220:223]\n\t" \
    "ds_read_b128 v[240:243], v253 offset:16384\n\t" \
    "v_cvt_pk_bf16_f32 v201, v213, v201\n\t" \
    "v_cvt_pk_bf16_f32 v202, v214, v202\n\t" \
    "v_mfma_f32_16x16x32_bf16 v[220:223], v[244:247], v[172:175], v[220:223]\n\t" \
    "ds_read_b128 v[244:247], v253 offset:17408\n\t" \
    "v_cvt_pk_bf16_f32 v203, v215, v203\n\t" \
    "s_waitcnt lgkmcnt(5)\n\t" \
    "v_mfma_f32_16x16x32_bf16 v[220:223], v[228:231], v[248:251], v[220:223]\n\t" \
    "ds_read_b128 v[248:251], v254 offset:8192\n\t" \
    "ds_read_b128 v[228:231], v253 offset:13312\n\t" \
    "s_add_u32 s44, s57, 0x11b3800\n\t" \
    "buffer_store_dwordx4 v[200:203], v253, s[60:63], s44 offen sc1\n\t" \
    "s_waitcnt lgkmcnt(6)\n\t" \
    "v_mfma_f32_16x16x32_bf16 v[216:219], v[224:227], v[176:179], 0\n\t" \
    "s_waitcnt lgkmcnt(5)\n\t" \
    "v_mfma_f32_16x16x32_bf16 v[216:219], v[232:235], v[184:187], v[216:219]\n\t" \
    "s_waitcnt lgkmcnt(4)\n\t" \
    "v_mfma_f32_16x16x32_bf16 v[216:219], v[236:239], v[188:191], v[216:219]\n\t" \
    "s_waitcnt lgkmcnt(3)\n\t" \
    "v_mfma_f32_16x16x32_bf16 v[216:219], v[240:243], v[192:195], v[216:219]\n\t" \
    "s_waitcnt lgkmcnt(2)\n\t" \
    "v_mfma_f32_16x16x32_bf16 v[216:219], v[244:247], v[196:199], v[216:219]\n\t" \
    "s_waitcnt lgkmcnt(0)\n\t" \
    "v_mfma_f32_16x16x32_bf16 v[216:219], v[228:231], v[180:183], v[216:219]\n\t" \
    "v_mfma_f32_16x16x32_bf16 v[204:207], v[224:227], v[248:251], 0\n\t" \
    "ds_read_b128 v[248:251], v254 offset:9216\n\t" \
    "v_mfma_f32_16x16x32_bf16 v[204:207], v[232:235], v[160:163], v[204:207]\n\t" \
    "v_mfma_f32_16x16x32_bf16 v[204:207], v[236:239], v[164:167], v[204:207]\n\t" \
    "v_cvt_pk_bf16_f32 v208, v208, v216\n\t" \
    "v_cvt_pk_bf16_f32 v209, v209, v217\n\t" \
    "v_mfma_f32_16x16x32_bf16 v[204:207], v[240:243], v[168:171], v[204:207]\n\t" \
    "v_cvt_pk_bf16_f32 v210, v210, v218\n\t" \
    "v_cvt_pk_bf16_f32 v211, v211, v219\n\t" \
    "v_mfma_f32_16x16x32_bf16 v[204:207], v[244:247], v[172:175], v[204:207]\n\t" \
    "s_add_u32 s44, s57, 0x1b3800\n\t" \
    "s_waitcnt lgkmcnt(0)\n\t" \
    "v_mfma_f32_16x16x32_bf16 v[204:207], v[228:231], v[248:251], v[204:207]\n\t" \
    "buffer_store_dwordx4 v[208:211], v253, s[60:63], s44 offen sc1\n\t" \
    "s_nop 6\n\t" \
    "v_cvt_pk_bf16_f32 v220, v220, v204\n\t" \
    "v_cvt_pk_bf16_f32 v221, v221, v205\n\t" \
    "v_cvt_pk_bf16_f32 v222, v222, v206\n\t" \
    "v_cvt_pk_bf16_f32 v223, v223, v207\n\t" \
    "s_add_u32 s44, s57, 0x173800\n\t" \
    "buffer_store_dwordx4 v[220:223], v253, s[60:63], s44 offen sc1\n\t" \
    "s_add_u32 s44, s40, 1\n\t" \
    "s_mov_b32 s45, s44\n\t" \
    "v_mov_b64 v[200:201], s[44:45]\n\t" \
    "v_mov_b64 v[202:203], s[44:45]\n\t" \
    "s_waitcnt lgkmcnt(0)\n\t" \
    "s_barrier \n\t" \
    "ds_read_b128 v[220:223], v253 offset:3072\n\t" \
    "ds_read_b128 v[224:227], v253 offset:3072\n\t" \
    "ds_read_b128 v[228:231], v253 offset:3072\n\t" \
    "ds_read_b128 v[232:235], v253 offset:3072\n\t" \
    "ds_read_b128 v[236:239], v253 offset:3072\n\t" \
    "ds_read_b128 v[240:243], v253 offset:3072\n\t" \
    "ds_read_b128 v[244:247], v253 offset:3072\n\t" \
    "ds_read_b128 v[248:251], v253 offset:3072\n\t" \
    "ds_read_b128 v[212:215], v254 offset:0\n\t" \
    "ds_read_b128 v[216:219], v254 offset:1024\n\t" \
    "s_and_b32 s44, s40, 3\n\t" \
    "s_lshl_b32 s44, s44, 22\n\t" \
    "s_add_u32 s42, s44, %[pb]\n\t" \
    "s_mov_b64 exec, 0xff\n\t" \
    "s_waitcnt vmcnt(0)\n\t" \
    "buffer_store_dwordx4 v[200:203], v253, s[60:63], %[fl] offen sc1\n\t" \
    "s_mov_b64 exec, -1\n\t" \
    "s_setprio 3\n\t" \
    "s_waitcnt lgkmcnt(0)\n\t" \
    "s_barrier \n\t" \
    "ds_read_b128 v[204:207], v253 offset:1024\n\t" \
    "ds_read_b128 v[208:211], v253 offset:2048\n\t" \
    "ds_read_b128 v[200:203], v253 offset:0\n\t" \
    "s_waitcnt lgkmcnt(2)\n\t" \
    "v_smfmac_f32_16x16x64_bf16 v[224:227], v[204:207], v[24:31], v252\n\t" \
    "v_smfmac_f32_16x16x64_bf16 v[220:223], v[204:207], v[0:7], v252\n\t" \
    "s_waitcnt lgkmcnt(1)\n\t" \
    "v_smfmac_f32_16x16x64_bf16 v[224:227], v[208:211], v[32:39], v252\n\t" \
    "v_smfmac_f32_16x16x64_bf16 v[220:223], v[208:211], v[8:15], v252\n\t" \
    "s_waitcnt lgkmcnt(0)\n\t" \
    "v_smfmac_f32_16x16x64_bf16 v[224:227], v[200:203], v[16:23], v252\n\t" \
    "v_smfmac_f32_16x16x64_bf16 v[220:223], v[200:203], v[212:219], v252\n\t" \
    "ds_read_b128 v[212:215], v254 offset:2048\n\t" \
    "ds_read_b128 v[216:219], v254 offset:3072\n\t" \
    "v_smfmac_f32_16x16x64_bf16 v[232:235], v[204:207], v[64:71], v252\n\t" \
    "v_smfmac_f32_16x16x64_bf16 v[228:231], v[204:207], v[40:47], v252\n\t" \
    "v_smfmac_f32_16x16x64_bf16 v[232:235], v[208:211], v[72:79], v252\n\t" \
    "v_add_f32 v220, v220, v222\n\t" \
    "v_add_f32 v221, v221, v223\n\t" \
    "v_cvt_pk_bf16_f32 v220, v220, v221\n\t" \
    "v_smfmac_f32_16x16x64_bf16 v[228:231], v[208:211], v[48:55], v252\n\t" \
    "v_min_u32 v220, -2, v220\n\t" \
    "v_add_f32 v224, v224, v226\n\t" \
    "v_add_f32 v225, v225, v227\n\t" \
    "v_smfmac_f32_16x16x64_bf16 v[232:235], v[200:203], v[56:63], v252\n\t" \
    "v_cvt_pk_bf16_f32 v221, v224, v225\n\t" \
    "v_min_u32 v221, -2, v221\n\t" \
    "s_waitcnt lgkmcnt(0)\n\t" \
    "v_smfmac_f32_16x16x64_bf16 v[228:231], v[200:203], v[212:219], v252\n\t" \
    "ds_read_b128 v[212:215], v254 offset:4096\n\t" \
    "ds_read_b128 v[216:219], v254 offset:5120\n\t" \
    "v_smfmac_f32_16x16x64_bf16 v[240:243], v[204:207], v[104:111], v252\n\t" \
    "v_smfmac_f32_16x16x64_bf16 v[236:239], v[204:207], v[80:87], v252\n\t" \
    "v_smfmac_f32_16x16x64_bf16 v[240:243], v[208:211], v[112:119], v252\n\t" \
    "v_add_f32 v228, v228, v230\n\t" \
    "v_add_f32 v229, v229, v231\n\t" \
    "v_cvt_pk_bf16_f32 v222, v228, v229\n\t" \
    "v_smfmac_f32_16x16x64_bf16 v[236:239], v[208:211], v[88:95], v252\n\t" \
    "v_min_u32 v222, -2, v222\n\t" \
    "v_add_f32 v232, v232, v234\n\t" \
    "v_add_f32 v233, v233, v235\n\t" \
    "v_smfmac_f32_16x16x64_bf16 v[240:243], v[200:203], v[96:103], v252\n\t" \
    "v_cvt_pk_bf16_f32 v223, v232, v233\n\t" \
    "v_min_u32 v223, -2, v223\n\t" \
    "v_permlane32_swap_b32 v220, v222\n\t" \
    "s_waitcnt lgkmcnt(0)\n\t" \
    "v_smfmac_f32_16x16x64_bf16 v[236:239], v[200:203], v[212:219], v252\n\t" \
    "ds_read_b128 v[212:215], v254 offset:6144\n\t" \
    "ds_read_b128 v[216:219], v254 offset:7168\n\t" \
    "s_nop 0\n\t" \
    "v_permlane32_swap_b32 v221, v223\n\t" \
    "s_nop 1\n\t" \
    "v_permlane16_swap_b32 v220, v221\n\t" \
    "v_permlane16_swap_b32 v222, v223\n\t" \
    "buffer_store_dwordx4 v[220:223], v255, s[60:63], s42 offen sc1\n\t" \
    "s_add_u32 s42, s42, 0x20000\n\t" \
    "ds_read_b128 v[224:227], v253 offset:3072\n\t" \
    "ds_read_b128 v[228:231], v253 offset:3072\n\t" \
    "v_smfmac_f32_16x16x64_bf16 v[248:251], v[204:207], v[144:151], v252\n\t" \
    "v_smfmac_f32_16x16x64_bf16 v[244:247], v[204:207], v[120:127], v252\n\t" \
    "v_smfmac_f32_16x16x64_bf16 v[248:251], v[208:211], v[152:159], v252\n\t" \
    "v_add_f32 v236, v236, v238\n\t" \
    "v_add_f32 v237, v237, v239\n\t" \
    "v_cvt_pk_bf16_f32 v236, v236, v237\n\t" \
    "v_smfmac_f32_16x16x64_bf16 v[244:247], v[208:211], v[128:135], v252\n\t" \
    "v_min_u32 v236, -2, v236\n\t" \
    "v_add_f32 v240, v240, v242\n\t" \
    "v_add_f32 v241, v241, v243\n\t" \
    "v_smfmac_f32_16x16x64_bf16 v[248:251], v[200:203], v[136:143], v252\n\t" \
    "v_cvt_pk_bf16_f32 v237, v240, v241\n\t" \
    "v_min_u32 v237, -2, v237\n\t" \
    "s_waitcnt lgkmcnt(2)\n\t" \
    "v_smfmac_f32_16x16x64_bf16 v[244:247], v[200:203], v[212:219], v252\n\t" \
    "ds_read_b128 v[212:215], v254 offset:8192\n\t" \
    "ds_read_b128 v[216:219], v254 offset:9216\n\t" \
    "s_waitcnt lgkmcnt(2)\n\t" \
    "v_smfmac_f32_16x16x64_bf16 v[228:231], v[204:207], v[184:191], v252\n\t" \
    "v_smfmac_f32_16x16x64_bf16 v[224:227], v[204:207], v[160:167], v252\n\t" \
    "v_smfmac_f32_16x16x64_bf16 v[228:231], v[208:211], v[192:199], v252\n\t" \
    "v_and_b32 v232, 0xffff, v255\n\t" \
    "v_lshrrev_b32 v233, 16, v255\n\t" \
    "v_lshl_add_u32 v232, v233, 3, v232\n\t" \
    "v_smfmac_f32_16x16x64_bf16 v[224:227], v[208:211], v[168:175], v252\n\t" \
    "v_add_f32 v244, v244, v246\n\t" \
    "v_add_f32 v245, v245, v247\n\t" \
    "v_cvt_pk_bf16_f32 v238, v244, v245\n\t" \
    "v_smfmac_f32_16x16x64_bf16 v[228:231], v[200:203], v[176:183], v252\n\t" \
    "v_min_u32 v238, -2, v238\n\t" \
    "v_add_f32 v248, v248, v250\n\t" \
    "v_add_f32 v249, v249, v251\n\t" \
    "s_waitcnt lgkmcnt(0)\n\t" \
    "v_smfmac_f32_16x16x64_bf16 v[224:227], v[200:203], v[212:219], v252\n\t" \
    "v_cvt_pk_bf16_f32 v239, v248, v249\n\t" \
    "v_min_u32 v239, -2, v239\n\t" \
    "v_permlane32_swap_b32 v236, v238\n\t" \
    "s_nop 0\n\t" \
    "v_permlane32_swap_b32 v237, v239\n\t" \
    "s_nop 1\n\t" \
    "v_permlane16_swap_b32 v236, v237\n\t" \
    "v_permlane16_swap_b32 v238, v239\n\t" \
    "buffer_store_dwordx4 v[236:239], v255, s[60:63], s42 offen sc1\n\t" \
    "s_add_u32 s42, s42, 0x20000\n\t" \
    "v_add_f32 v228, v228, v230\n\t" \
    "v_add_f32 v229, v229, v231\n\t" \
    "v_add_f32 v224, v224, v226\n\t" \
    "v_add_f32 v225, v225, v227\n\t" \
    "v_cvt_pk_bf16_f32 v229, v228, v229\n\t" \
    "v_cvt_pk_bf16_f32 v228, v224, v225\n\t" \
    "v_min_u32 v229, -2, v229\n\t" \
    "v_min_u32 v228, -2, v228\n\t" \
    "s_nop 1\n\t" \
    "v_permlane16_swap_b32 v228, v229\n\t" \
    "buffer_store_dwordx2 v[228:229], v232, s[60:63], s42 offen sc1\n\t" \
    "s_setprio 0\n\t" \
    "s_add_u32 s40, s40, 1\n\t" \
    "s_cmp_lg_u32 s40, 64\n\t" \
    "s_cbranch_scc1 .Lv43_%=\n\t" \
    "s_branch .Lend%=\n\t" \
    ".Lv3_%=:\n\t" \
    "s_mov_b32 s42, %[xs]\n\t" \
    "v_add_u32 v244, 0xc000, v243\n\t" \
    "s_waitcnt vmcnt(12) lgkmcnt(0)\n\t" \
    "s_barrier \n\t" \
    "s_add_u32 m0, %[wm], 0x9000\n\t" \
    "s_mov_b32 s44, 0x1e0000\n\t" \
    "buffer_load_dwordx4 v240, %[wr], s44 offen offset:0 nt lds\n\t" \
    "buffer_load_dwordx4 v241, %[wr], s44 offen offset:1024 nt lds\n\t" \
    "buffer_load_dwordx4 v242, %[wr], s44 offen offset:2048 nt lds\n\t" \
    "ds_read_b64_tr_b16 v[208:209], v244 offset:0\n\t" \
    "ds_read_b64_tr_b16 v[210:211], v244 offset:1536\n\t" \
    "ds_read_b64_tr_b16 v[40:41], v244 offset:32\n\t" \
    "ds_read_b64_tr_b16 v[42:43], v244 offset:1568\n\t" \
    "ds_read_b64_tr_b16 v[48:49], v244 offset:64\n\t" \
    "ds_read_b64_tr_b16 v[50:51], v244 offset:1600\n\t" \
    "v_add_u32 v244, 0xf000, v243\n\t" \
    "s_waitcnt vmcnt(12) lgkmcnt(0)\n\t" \
    "s_barrier \n\t" \
    "s_add_u32 m0, %[wm], 0xc000\n\t" \
    "s_mov_b32 s44, 0x240000\n\t" \
    "buffer_load_dwordx4 v240, %[wr], s44 offen offset:0 nt lds\n\t" \
    "buffer_load_dwordx4 v241, %[wr], s44 offen offset:1024 nt lds\n\t" \
    "buffer_load_dwordx4 v242, %[wr], s44 offen offset:2048 nt lds\n\t" \
    "ds_read_b64_tr_b16 v[212:213], v244 offset:0\n\t" \
    "ds_read_b64_tr_b16 v[214:215], v244 offset:1536\n\t" \
    "ds_read_b64_tr_b16 v[44:45], v244 offset:32\n\t" \
    "ds_read_b64_tr_b16 v[46:47], v244 offset:1568\n\t" \
    "ds_read_b64_tr_b16 v[52:53], v244 offset:64\n\t" \
    "ds_read_b64_tr_b16 v[54:55], v244 offset:1600\n\t" \
    "v_add_u32 v244, 0x0, v243\n\t" \
    "s_waitcnt vmcnt(12) lgkmcnt(0)\n\t" \
    "s_barrier \n\t" \
    "s_add_u32 m0, %[wm], 0xf000\n\t" \
    "s_mov_b32 s44, 0x2a0000\n\t" \
    "buffer_load_dwordx4 v240, %[wr], s44 offen offset:0 nt lds\n\t" \
    "buffer_load_dwordx4 v241, %[wr], s44 offen offset:1024 nt lds\n\t" \
    "buffer_load_dwordx4 v242, %[wr], s44 offen offset:2048 nt lds\n\t" \
    "ds_read_b64_tr_b16 v[56:57], v244 offset:0\n\t" \
    "ds_read_b64_tr_b16 v[58:59], v244 offset:1536\n\t" \
    "ds_read_b64_tr_b16 v[64:65], v244 offset:32\n\t" \
    "ds_read_b64_tr_b16 v[66:67], v244 offset:1568\n\t" \
    "ds_read_b64_tr_b16 v[72:73], v244 offset:64\n\t" \
    "ds_read_b64_tr_b16 v[74:75], v244 offset:1600\n\t" \
    "v_add_u32 v244, 0x3000, v243\n\t" \
    "s_waitcnt vmcnt(12) lgkmcnt(0)\n\t" \
    "s_barrier \n\t" \
    "s_add_u32 m0, %[wm], 0x0\n\t" \
    "s_mov_b32 s44, 0x300000\n\t" \
    "buffer_load_dwordx4 v240, %[wr], s44 offen offset:0 nt lds\n\t" \
    "buffer_load_dwordx4 v241, %[wr], s44 offen offset:1024 nt lds\n\t" \
    "buffer_load_dwordx4 v242, %[wr], s44 offen offset:2048 nt lds\n\t" \
    "ds_read_b64_tr_b16 v[60:61], v244 offset:0\n\t" \
    "ds_read_b64_tr_b16 v[62:63], v244 offset:1536\n\t" \
    "ds_read_b64_tr_b16 v[68:69], v244 offset:32\n\t" \
    "ds_read_b64_tr_b16 v[70:71], v244 offset:1568\n\t" \
    "ds_read_b64_tr_b16 v[76:77], v244 offset:64\n\t" \
    "ds_read_b64_tr_b16 v[78:79], v244 offset:1600\n\t" \
    "v_add_u32 v244, 0x6000, v243\n\t" \
    "s_waitcnt vmcnt(12) lgkmcnt(0)\n\t" \
    "s_barrier \n\t" \
    "s_add_u32 m0, %[wm], 0x3000\n\t" \
    "s_mov_b32 s44, 0x360000\n\t" \
    "buffer_load_dwordx4 v240, %[wr], s44 offen offset:0 nt lds\n\t" \
    "buffer_load_dwordx4 v241, %[wr], s44 offen offset:1024 nt lds\n\t" \
    "buffer_load_dwordx4 v242, %[wr], s44 offen offset:2048 nt lds\n\t" \
    "ds_read_b64_tr_b16 v[216:217], v244 offset:0\n\t" \
    "ds_read_b64_tr_b16 v[218:219], v244 offset:1536\n\t" \
    "ds_read_b64_tr_b16 v[80:81], v244 offset:32\n\t" \
    "ds_read_b64_tr_b16 v[82:83], v244 offset:1568\n\t" \
    "ds_read_b64_tr_b16 v[88:89], v244 offset:64\n\t" \
    "ds_read_b64_tr_b16 v[90:91], v244 offset:1600\n\t" \
    "v_add_u32 v244, 0x9000, v243\n\t" \
    "s_waitcnt vmcnt(12) lgkmcnt(0)\n\t" \
    "s_barrier \n\t" \
    "s_add_u32 m0, %[wm], 0x6000\n\t" \
    "s_mov_b32 s44, 0x3c0000\n\t" \
    "buffer_load_dwordx4 v240, %[wr], s44 offen offset:0 nt lds\n\t" \
    "buffer_load_dwordx4 v241, %[wr], s44 offen offset:1024 nt lds\n\t" \
    "buffer_load_dwordx4 v242, %[wr], s44 offen offset:2048 nt lds\n\t" \
    "ds_read_b64_tr_b16 v[220:221], v244 offset:0\n\t" \
    "ds_read_b64_tr_b16 v[222:223], v244 offset:1536\n\t" \
    "ds_read_b64_tr_b16 v[84:85], v244 offset:32\n\t" \
    "ds_read_b64_tr_b16 v[86:87], v244 offset:1568\n\t" \
    "ds_read_b64_tr_b16 v[92:93], v244 offset:64\n\t" \
    "ds_read_b64_tr_b16 v[94:95], v244 offset:1600\n\t" \
    "v_add_u32 v244, 0xc000, v243\n\t" \
    "s_waitcnt vmcnt(12) lgkmcnt(0)\n\t" \
    "s_barrier \n\t" \
    "s_add_u32 m0, %[wm], 0x9000\n\t" \
    "s_mov_b32 s44, 0x420000\n\t" \
    "buffer_load_dwordx4 v240, %[wr], s44 offen offset:0 nt lds\n\t" \
    "buffer_load_dwordx4 v241, %[wr], s44 offen offset:1024 nt lds\n\t" \
    "buffer_load_dwordx4 v242, %[wr], s44 offen offset:2048 nt lds\n\t" \
    "ds_read_b64_tr_b16 v[96:97], v244 offset:0\n\t" \
    "ds_read_b64_tr_b16 v[98:99], v244 offset:1536\n\t" \
    "ds_read_b64_tr_b16 v[104:105], v244 offset:32\n\t" \
    "ds_read_b64_tr_b16 v[106:107], v244 offset:1568\n\t" \
    "ds_read_b64_tr_b16 v[112:113], v244 offset:64\n\t" \
    "ds_read_b64_tr_b16 v[114:115], v244 offset:1600\n\t" \
    "v_add_u32 v244, 0xf000, v243\n\t" \
    "s_waitcnt vmcnt(12) lgkmcnt(0)\n\t" \
    "s_barrier \n\t" \
    "s_add_u32 m0, %[wm], 0xc000\n\t" \
    "s_mov_b32 s44, 0x480000\n\t" \
    "buffer_load_dwordx4 v240, %[wr], s44 offen offset:0 nt lds\n\t" \
    "buffer_load_dwordx4 v241, %[wr], s44 offen offset:1024 nt lds\n\t" \
    "buffer_load_dwordx4 v242, %[wr], s44 offen offset:2048 nt lds\n\t" \
    "ds_read_b64_tr_b16 v[100:101], v244 offset:0\n\t" \
    "ds_read_b64_tr_b16 v[102:103], v244 offset:1536\n\t" \
    "ds_read_b64_tr_b16 v[108:109], v244 offset:32\n\t" \
    "ds_read_b64_tr_b16 v[110:111], v244 offset:1568\n\t" \
    "ds_read_b64_tr_b16 v[116:117], v244 offset:64\n\t" \
    "ds_read_b64_tr_b16 v[118:119], v244 offset:1600\n\t" \
    "v_add_u32 v244, 0x0, v243\n\t" \
    "s_waitcnt vmcnt(12) lgkmcnt(0)\n\t" \
    "s_barrier \n\t" \
    "v_mov_b32 v246, 0\n\t" \
    "buffer_load_dword v246, v246, s[60:63], %[mo] offen sc1\n\t" \
    "s_add_u32 m0, %[wm], 0xf000\n\t" \
    "s_mov_b32 s44, 0x4e0000\n\t" \
    "buffer_load_dwordx4 v240, %[wr], s44 offen offset:0 nt lds\n\t" \
    "buffer_load_dwordx4 v241, %[wr], s44 offen offset:1024 nt lds\n\t" \
    "buffer_load_dwordx4 v242, %[wr], s44 offen offset:2048 nt lds\n\t" \
    "ds_read_b64_tr_b16 v[224:225], v244 offset:0\n\t" \
    "ds_read_b64_tr_b16 v[226:227], v244 offset:1536\n\t" \
    "ds_read_b64_tr_b16 v[120:121], v244 offset:32\n\t" \
    "ds_read_b64_tr_b16 v[122:123], v244 offset:1568\n\t" \
    "ds_read_b64_tr_b16 v[128:129], v244 offset:64\n\t" \
    "ds_read_b64_tr_b16 v[130:131], v244 offset:1600\n\t" \
    "v_add_u32 v244, 0x3000, v243\n\t" \
    "s_waitcnt vmcnt(12) lgkmcnt(0)\n\t" \
    "s_barrier \n\t" \
    "s_add_u32 m0, %[wm], 0x0\n\t" \
    "s_mov_b32 s44, 0x540000\n\t" \
    "buffer_load_dwordx4 v240, %[wr], s44 offen offset:0 nt lds\n\t" \
    "buffer_load_dwordx4 v241, %[wr], s44 offen offset:1024 nt lds\n\t" \
    "buffer_load_dwordx4 v242, %[wr], s44 offen offset:2048 nt lds\n\t" \
    "ds_read_b64_tr_b16 v[228:229], v244 offset:0\n\t" \
    "ds_read_b64_tr_b16 v[230:231], v244 offset:1536\n\t" \
    "ds_read_b64_tr_b16 v[124:125], v244 offset:32\n\t" \
    "ds_read_b64_tr_b16 v[126:127], v244 offset:1568\n\t" \
    "ds_read_b64_tr_b16 v[132:133], v244 offset:64\n\t" \
    "ds_read_b64_tr_b16 v[134:135], v244 offset:1600\n\t" \
    "v_add_u32 v244, 0x6000, v243\n\t" \
    "s_waitcnt vmcnt(12) lgkmcnt(0)\n\t" \
    "s_barrier \n\t" \
    "s_add_u32 m0, %[wm], 0x3000\n\t" \
    "s_mov_b32 s44, 0x5a0000\n\t" \
    "buffer_load_dwordx4 v240, %[wr], s44 offen offset:0 nt lds\n\t" \
    "buffer_load_dwordx4 v241, %[wr], s44 offen offset:1024 nt lds\n\t" \
    "buffer_load_dwordx4 v242, %[wr], s44 offen offset:2048 nt lds\n\t" \
    "ds_read_b64_tr_b16 v[136:137], v244 offset:0\n\t" \
    "ds_read_b64_tr_b16 v[138:139], v244 offset:1536\n\t" \
    "ds_read_b64_tr_b16 v[144:145], v244 offset:32\n\t" \
    "ds_read_b64_tr_b16 v[146:147], v244 offset:1568\n\t" \
    "ds_read_b64_tr_b16 v[152:153], v244 offset:64\n\t" \
    "ds_read_b64_tr_b16 v[154:155], v244 offset:1600\n\t" \
    "v_add_u32 v244, 0x9000, v243\n\t" \
    "s_waitcnt vmcnt(12) lgkmcnt(0)\n\t" \
    "s_barrier \n\t" \
    "s_add_u32 m0, %[wm], 0x6000\n\t" \
    "s_mov_b32 s44, 0x600000\n\t" \
    "buffer_load_dwordx4 v240, %[wr], s44 offen offset:0 nt lds\n\t" \
    "buffer_load_dwordx4 v241, %[wr], s44 offen offset:1024 nt lds\n\t" \
    "buffer_load_dwordx4 v242, %[wr], s44 offen offset:2048 nt lds\n\t" \
    "ds_read_b64_tr_b16 v[140:141], v244 offset:0\n\t" \
    "ds_read_b64_tr_b16 v[142:143], v244 offset:1536\n\t" \
    "ds_read_b64_tr_b16 v[148:149], v244 offset:32\n\t" \
    "ds_read_b64_tr_b16 v[150:151], v244 offset:1568\n\t" \
    "ds_read_b64_tr_b16 v[156:157], v244 offset:64\n\t" \
    "ds_read_b64_tr_b16 v[158:159], v244 offset:1600\n\t" \
    "v_add_u32 v244, 0xc000, v243\n\t" \
    "s_waitcnt vmcnt(12) lgkmcnt(0)\n\t" \
    "s_barrier \n\t" \
    "s_add_u32 m0, %[wm], 0x9000\n\t" \
    "s_mov_b32 s44, 0x660000\n\t" \
    "buffer_load_dwordx4 v240, %[wr], s44 offen offset:0 nt lds\n\t" \
    "buffer_load_dwordx4 v241, %[wr], s44 offen offset:1024 nt lds\n\t" \
    "buffer_load_dwordx4 v242, %[wr], s44 offen offset:2048 nt lds\n\t" \
    "ds_read_b64_tr_b16 v[232:233], v244 offset:0\n\t" \
    "ds_read_b64_tr_b16 v[234:235], v244 offset:1536\n\t" \
    "ds_read_b64_tr_b16 v[160:161], v244 offset:32\n\t" \
    "ds_read_b64_tr_b16 v[162:163], v244 offset:1568\n\t" \
    "ds_read_b64_tr_b16 v[168:169], v244 offset:64\n\t" \
    "ds_read_b64_tr_b16 v[170:171], v244 offset:1600\n\t" \
    "v_add_u32 v244, 0xf000, v243\n\t" \
    "s_waitcnt vmcnt(12) lgkmcnt(0)\n\t" \
    "s_barrier \n\t" \
    ".Lv31_%=:\n\t" \
    "v_readfirstlane_b32 s43, v246\n\t" \
    "s_nop 0\n\t" \
    "s_cmp_lg_u32 s43, -1\n\t" \
    "s_cbranch_scc1 .Lv32_%=\n\t" \
    "s_cmp_eq_u32 s41, 0\n\t" \
    "s_cbranch_scc1 .Lv32_%=\n\t" \
    "s_sub_u32 s41, s41, 1\n\t" \
    "s_sleep 4\n\t" \
    "v_mov_b32 v246, 0\n\t" \
    "buffer_load_dword v246, v246, s[60:63], %[mo] offen sc1\n\t" \
    "s_waitcnt vmcnt(0)\n\t" \
    "s_branch .Lv31_%=\n\t" \
    ".Lv32_%=:\n\t" \
    "v_mov_b32 v246, 0x8000\n\t" \
    "s_mov_b64 exec, 1\n\t" \
    "buffer_store_dword v246, v246, s[60:63], %[mo] offen sc1\n\t" \
    "s_mov_b64 exec, -1\n\t" \
    "s_and_b32 s44, s43, 0x5000\n\t" \
    "s_xor_b32 %[mb], %[mb], s44\n\t" \
    "s_xor_b32 %[fl], %[fl], s44\n\t" \
    "s_and_b32 s43, s43, 0x2000\n\t" \
    "s_xor_b32 s43, s43, %[lx]\n\t" \
    "s_add_u32 s60, s60, s43\n\t" \
    "s_addc_u32 s61, s61, 0\n\t" \
    "v_and_b32 v252, 0xf0, v253\n\t" \
    "v_lshlrev_b32 v252, 3, v252\n\t" \
    "buffer_load_dword v252, v252, s[60:63], %[fl] offen sc1\n\t" \
    "s_add_u32 m0, %[wm], 0xc000\n\t" \
    "s_mov_b32 s44, 0x6c0000\n\t" \
    "buffer_load_dwordx4 v240, %[wr], s44 offen offset:0 nt lds\n\t" \
    "buffer_load_dwordx4 v241, %[wr], s44 offen offset:1024 nt lds\n\t" \
    "buffer_load_dwordx4 v242, %[wr], s44 offen offset:2048 nt lds\n\t" \
    "ds_read_b64_tr_b16 v[236:237], v244 offset:0\n\t" \
    "ds_read_b64_tr_b16 v[238:239], v244 offset:1536\n\t" \
    "ds_read_b64_tr_b16 v[164:165], v244 offset:32\n\t" \
    "ds_read_b64_tr_b16 v[166:167], v244 offset:1568\n\t" \
    "ds_read_b64_tr_b16 v[172:173], v244 offset:64\n\t" \
    "ds_read_b64_tr_b16 v[174:175], v244 offset:1600\n\t" \
    "v_add_u32 v244, 0x0, v243\n\t" \
    "s_waitcnt vmcnt(12) lgkmcnt(0)\n\t" \
    "s_barrier \n\t" \
    "s_add_u32 m0, %[wm], 0xf000\n\t" \
    "s_mov_b32 s44, 0x720000\n\t" \
    "buffer_load_dwordx4 v240, %[wr], s44 offen offset:0 nt lds\n\t" \
    "buffer_load_dwordx4 v241, %[wr], s44 offen offset:1024 nt lds\n\t" \
    "buffer_load_dwordx4 v242, %[wr], s44 offen offset:2048 nt lds\n\t" \
    "ds_read_b64_tr_b16 v[176:177], v244 offset:0\n\t" \
    "ds_read_b64_tr_b16 v[178:179], v244 offset:1536\n\t" \
    "ds_read_b64_tr_b16 v[184:185], v244 offset:32\n\t" \
    "ds_read_b64_tr_b16 v[186:187], v244 offset:1568\n\t" \
    "ds_read_b64_tr_b16 v[192:193], v244 offset:64\n\t" \
    "ds_read_b64_tr_b16 v[194:195], v244 offset:1600\n\t" \
    "v_add_u32 v244, 0x3000, v243\n\t" \
    "s_waitcnt vmcnt(12) lgkmcnt(0)\n\t" \
    "s_barrier \n\t" \
    "ds_read_b64_tr_b16 v[180:181], v244 offset:0\n\t" \
    "ds_read_b64_tr_b16 v[182:183], v244 offset:1536\n\t" \
    "ds_read_b64_tr_b16 v[188:189], v244 offset:32\n\t" \
    "ds_read_b64_tr_b16 v[190:191], v244 offset:1568\n\t" \
    "ds_read_b64_tr_b16 v[196:197], v244 offset:64\n\t" \
    "ds_read_b64_tr_b16 v[198:199], v244 offset:1600\n\t" \
    "v_add_u32 v244, 0x6000, v243\n\t" \
    "s_waitcnt vmcnt(9) lgkmcnt(0)\n\t" \
    "s_barrier \n\t" \
    "ds_read_b64_tr_b16 v[200:201], v244 offset:0\n\t" \
    "ds_read_b64_tr_b16 v[202:203], v244 offset:1536\n\t" \
    "ds_read_b64_tr_b16 v[0:1], v244 offset:32\n\t" \
    "ds_read_b64_tr_b16 v[2:3], v244 offset:1568\n\t" \
    "ds_read_b64_tr_b16 v[8:9], v244 offset:64\n\t" \
    "ds_read_b64_tr_b16 v[10:11], v244 offset:1600\n\t" \
    "v_add_u32 v244, 0x9000, v243\n\t" \
    "s_waitcnt vmcnt(6) lgkmcnt(0)\n\t" \
    "s_barrier \n\t" \
    "ds_read_b64_tr_b16 v[204:205], v244 offset:0\n\t" \
    "ds_read_b64_tr_b16 v[206:207], v244 offset:1536\n\t" \
    "ds_read_b64_tr_b16 v[4:5], v244 offset:32\n\t" \
    "ds_read_b64_tr_b16 v[6:7], v244 offset:1568\n\t" \
    "ds_read_b64_tr_b16 v[12:13], v244 offset:64\n\t" \
    "ds_read_b64_tr_b16 v[14:15], v244 offset:1600\n\t" \
    "v_add_u32 v244, 0xc000, v243\n\t" \
    "s_waitcnt vmcnt(3) lgkmcnt(0)\n\t" \
    "s_barrier \n\t" \
    "ds_read_b64_tr_b16 v[16:17], v244 offset:0\n\t" \
    "ds_read_b64_tr_b16 v[18:19], v244 offset:1536\n\t" \
    "ds_read_b64_tr_b16 v[24:25], v244 offset:32\n\t" \
    "ds_read_b64_tr_b16 v[26:27], v244 offset:1568\n\t" \
    "ds_read_b64_tr_b16 v[32:33], v244 offset:64\n\t" \
    "ds_read_b64_tr_b16 v[34:35], v244 offset:1600\n\t" \
    "s_mov_b32 s43, 0\n\t" \
    "v_cmp_eq_u32 vcc, 0, v252\n\t" \
    "s_cbranch_vccnz .Lv33_%=\n\t" \
    "s_mov_b32 m0, 0x3000\n\t" \
    "s_add_u32 s49, s42, 0x0\n\t" \
    "buffer_load_dwordx4 v253, s[60:63], s49 offen offset:0 sc1 lds\n\t" \
    "buffer_load_dwordx4 v253, s[60:63], s49 offen offset:1024 sc1 lds\n\t" \
    "buffer_load_dwordx4 v253, s[60:63], s49 offen offset:2048 sc1 lds\n\t" \
    "buffer_load_dwordx4 v253, s[60:63], s49 offen offset:3072 sc1 lds\n\t" \
    "s_mov_b32 m0, 0x4c00\n\t" \
    "s_add_u32 s49, s42, 0x4c00\n\t" \
    "buffer_load_dwordx4 v253, s[60:63], s49 offen offset:0 sc1 lds\n\t" \
    "s_mov_b32 m0, 0x8000\n\t" \
    "s_add_u32 s49, s42, 0x1000\n\t" \
    "buffer_load_dwordx4 v253, s[60:63], s49 offen offset:0 sc1 lds\n\t" \
    "buffer_load_dwordx4 v253, s[60:63], s49 offen offset:1024 sc1 lds\n\t" \
    "buffer_load_dwordx4 v253, s[60:63], s49 offen offset:2048 sc1 lds\n\t" \
    "buffer_load_dwordx4 v253, s[60:63], s49 offen offset:3072 sc1 lds\n\t" \
    "s_mov_b32 m0, 0x9c00\n\t" \
    "s_add_u32 s49, s42, 0x5c00\n\t" \
    "buffer_load_dwordx4 v253, s[60:63], s49 offen offset:0 sc1 lds\n\t" \
    "s_mov_b32 s43, 1\n\t" \
    ".Lv33_%=:\n\t" \
    "v_add_u32 v244, 0xf000, v243\n\t" \
    "s_waitcnt vmcnt(0) lgkmcnt(0)\n\t" \
    "s_barrier \n\t" \
    "ds_read_b64_tr_b16 v[20:21], v244 offset:0\n\t" \
    "ds_read_b64_tr_b16 v[22:23], v244 offset:1536\n\t" \
    "ds_read_b64_tr_b16 v[28:29], v244 offset:32\n\t" \
    "ds_read_b64_tr_b16 v[30:31], v244 offset:1568\n\t" \
    "ds_read_b64_tr_b16 v[36:37], v244 offset:64\n\t" \
    "ds_read_b64_tr_b16 v[38:39], v244 offset:1600\n\t" \
    "s_waitcnt lgkmcnt(0)\n\t" \
    "s_barrier \n\t" \
    "ds_write_b128 v254, v[200:203] offset:55296\n\t" \
    "ds_write_b128 v254, v[204:207] offset:56320\n\t" \
    "ds_write_b128 v254, v[208:211] offset:57344\n\t" \
    "ds_write_b128 v254, v[212:215] offset:58368\n\t" \
    "ds_write_b128 v254, v[216:219] offset:59392\n\t" \
    "ds_write_b128 v254, v[220:223] offset:60416\n\t" \
    "ds_write_b128 v254, v[224:227] offset:61440\n\t" \
    "ds_write_b128 v254, v[228:231] offset:62464\n\t" \
    "ds_write_b128 v254, v[232:235] offset:63488\n\t" \
    "ds_write_b128 v254, v[236:239] offset:64512\n\t" \
    "s_cmp_lg_u32 s43, 0\n\t" \
    "s_cbranch_scc1 .Lv34_%=\n\t" \
    "s_mov_b32 s44, 0\n\t" \
    ".Lv35_%=:\n\t" \
    "v_cmp_ge_u32 vcc, s44, v252\n\t" \
    "s_cbranch_vccz .Lv36_%=\n\t" \
    "s_cmp_eq_u32 s41, 0\n\t" \
    "s_cbranch_scc1 .Lv36_%=\n\t" \
    "s_sub_u32 s41, s41, 1\n\t" \
    "s_sleep 8\n\t" \
    "v_and_b32 v252, 0xf0, v253\n\t" \
    "v_lshlrev_b32 v252, 3, v252\n\t" \
    "buffer_load_dword v252, v252, s[60:63], %[fl] offen sc1\n\t" \
    "s_waitcnt vmcnt(0)\n\t" \
    "s_branch .Lv35_%=\n\t" \
    ".Lv36_%=:\n\t" \
    "s_mov_b32 m0, 0x3000\n\t" \
    "s_add_u32 s49, s42, 0x0\n\t" \
    "buffer_load_dwordx4 v253, s[60:63], s49 offen offset:0 sc1 lds\n\t" \
    "buffer_load_dwordx4 v253, s[60:63], s49 offen offset:1024 sc1 lds\n\t" \
    "buffer_load_dwordx4 v253, s[60:63], s49 offen offset:2048 sc1 lds\n\t" \
    "buffer_load_dwordx4 v253, s[60:63], s49 offen offset:3072 sc1 lds\n\t" \
    "s_mov_b32 m0, 0x4c00\n\t" \
    "s_add_u32 s49, s42, 0x4c00\n\t" \
    "buffer_load_dwordx4 v253, s[60:63], s49 offen offset:0 sc1 lds\n\t" \
    "s_mov_b32 m0, 0x8000\n\t" \
    "s_add_u32 s49, s42, 0x1000\n\t" \
    "buffer_load_dwordx4 v253, s[60:63], s49 offen offset:0 sc1 lds\n\t" \
    "buffer_load_dwordx4 v253, s[60:63], s49 offen offset:1024 sc1 lds\n\t" \
    "buffer_load_dwordx4 v253, s[60:63], s49 offen offset:2048 sc1 lds\n\t" \
    "buffer_load_dwordx4 v253, s[60:63], s49 offen offset:3072 sc1 lds\n\t" \
    "s_mov_b32 m0, 0x9c00\n\t" \
    "s_add_u32 s49, s42, 0x5c00\n\t" \
    "buffer_load_dwordx4 v253, s[60:63], s49 offen offset:0 sc1 lds\n\t" \
    ".Lv34_%=:\n\t" \
    "s_waitcnt vmcnt(0) lgkmcnt(0)\n\t" \
    "s_barrier \n\t" \
    "ds_read_b128 v[224:227], v253 offset:16384\n\t" \
    "ds_read_b128 v[244:247], v254 offset:55296\n\t" \
    "ds_read_b128 v[228:231], v253 offset:17408\n\t" \
    "ds_read_b128 v[232:235], v253 offset:36864\n\t" \
    "ds_read_b128 v[236:239], v253 offset:37888\n\t" \
    "ds_read_b128 v[248:251], v254 offset:56320\n\t" \
    ".Lv37_%=:\n\t" \
    "s_waitcnt lgkmcnt(5)\n\t" \
    "v_mfma_f32_16x16x32_bf16 v[204:207], v[0:3], v[224:227], 0\n\t" \
    "s_waitcnt lgkmcnt(3)\n\t" \
    "v_mfma_f32_16x16x32_bf16 v[204:207], v[4:7], v[228:231], v[204:207]\n\t" \
    "ds_read_b128 v[240:243], v253 offset:18432\n\t" \
    "v_mfma_f32_16x16x32_bf16 v[208:211], v[8:11], v[224:227], 0\n\t" \
    "v_mfma_f32_16x16x32_bf16 v[208:211], v[12:15], v[228:231], v[208:211]\n\t" \
    "v_mfma_f32_16x16x32_bf16 v[200:203], v[244:247], v[224:227], 0\n\t" \
    "s_waitcnt lgkmcnt(1)\n\t" \
    "v_mfma_f32_16x16x32_bf16 v[200:203], v[248:251], v[228:231], v[200:203]\n\t" \
    "v_mfma_f32_16x16x32_bf16 v[216:219], v[0:3], v[232:235], 0\n\t" \
    "ds_read_b128 v[224:227], v253 offset:19456\n\t" \
    "v_mfma_f32_16x16x32_bf16 v[216:219], v[4:7], v[236:239], v[216:219]\n\t" \
    "ds_read_b128 v[228:231], v253 offset:0\n\t" \
    "v_mfma_f32_16x16x32_bf16 v[220:223], v[8:11], v[232:235], 0\n\t" \
    "v_mfma_f32_16x16x32_bf16 v[220:223], v[12:15], v[236:239], v[220:223]\n\t" \
    "v_mfma_f32_16x16x32_bf16 v[212:215], v[244:247], v[232:235], 0\n\t" \
    "ds_read_b128 v[244:247], v254 offset:57344\n\t" \
    "v_mfma_f32_16x16x32_bf16 v[212:215], v[248:251], v[236:239], v[212:215]\n\t" \
    "ds_read_b128 v[248:251], v254 offset:58368\n\t" \
    "s_waitcnt lgkmcnt(4)\n\t" \
    "v_mfma_f32_16x16x32_bf16 v[204:207], v[24:27], v[240:243], v[204:207]\n\t" \
    "ds_read_b128 v[232:235], v253 offset:1024\n\t" \
    "s_waitcnt lgkmcnt(4)\n\t" \
    "v_mfma_f32_16x16x32_bf16 v[204:207], v[28:31], v[224:227], v[204:207]\n\t" \
    "ds_read_b128 v[236:239], v253 offset:20480\n\t" \
    "v_mfma_f32_16x16x32_bf16 v[208:211], v[32:35], v[240:243], v[208:211]\n\t" \
    "v_mfma_f32_16x16x32_bf16 v[208:211], v[36:39], v[224:227], v[208:211]\n\t" \
    "v_mfma_f32_16x16x32_bf16 v[200:203], v[16:19], v[240:243], v[200:203]\n\t" \
    "v_mfma_f32_16x16x32_bf16 v[200:203], v[20:23], v[224:227], v[200:203]\n\t" \
    "s_waitcnt lgkmcnt(4)\n\t" \
    "v_mfma_f32_16x16x32_bf16 v[204:207], v[40:43], v[228:231], v[204:207]\n\t" \
    "ds_read_b128 v[240:243], v253 offset:21504\n\t" \
    "s_waitcnt lgkmcnt(2)\n\t" \
    "v_mfma_f32_16x16x32_bf16 v[204:207], v[44:47], v[232:235], v[204:207]\n\t" \
    "ds_read_b128 v[224:227], v253 offset:2048\n\t" \
    "v_mfma_f32_16x16x32_bf16 v[208:211], v[48:51], v[228:231], v[208:211]\n\t" \
    "v_mfma_f32_16x16x32_bf16 v[208:211], v[52:55], v[232:235], v[208:211]\n\t" \
    "v_mfma_f32_16x16x32_bf16 v[200:203], v[244:247], v[228:231], v[200:203]\n\t" \
    "v_mfma_f32_16x16x32_bf16 v[200:203], v[248:251], v[232:235], v[200:203]\n\t" \
    "s_waitcnt lgkmcnt(2)\n\t" \
    "v_mfma_f32_16x16x32_bf16 v[216:219], v[40:43], v[236:239], v[216:219]\n\t" \
    "ds_read_b128 v[228:231], v253 offset:3072\n\t" \
    "s_add_u32 s44, s40, 1\n\t" \
    "s_waitcnt lgkmcnt(2)\n\t" \
    "v_mfma_f32_16x16x32_bf16 v[216:219], v[44:47], v[240:243], v[216:219]\n\t" \
    "ds_read_b128 v[232:235], v253 offset:4096\n\t" \
    "s_and_b32 s45, s44, 15\n\t" \
    "v_mfma_f32_16x16x32_bf16 v[220:223], v[48:51], v[236:239], v[220:223]\n\t" \
    "s_cmp_eq_u32 s45, 0\n\t" \
    "v_mfma_f32_16x16x32_bf16 v[220:223], v[52:55], v[240:243], v[220:223]\n\t" \
    "s_cselect_b32 s45, %[cj], %[cs]\n\t" \
    "v_mfma_f32_16x16x32_bf16 v[212:215], v[244:247], v[236:239], v[212:215]\n\t" \
    "ds_read_b128 v[244:247], v254 offset:59392\n\t" \
    "s_cmp_eq_u32 s44, 64\n\t" \
    "v_mfma_f32_16x16x32_bf16 v[212:215], v[248:251], v[240:243], v[212:215]\n\t" \
    "ds_read_b128 v[248:251], v254 offset:60416\n\t" \
    "s_cselect_b32 s45, 0, s45\n\t" \
    "s_waitcnt lgkmcnt(4)\n\t" \
    "v_mfma_f32_16x16x32_bf16 v[204:207], v[64:67], v[224:227], v[204:207]\n\t" \
    "ds_read_b128 v[236:239], v253 offset:5120\n\t" \
    "s_add_u32 s42, s42, s45\n\t" \
    "s_waitcnt lgkmcnt(4)\n\t" \
    "v_mfma_f32_16x16x32_bf16 v[204:207], v[68:71], v[228:231], v[204:207]\n\t" \
    "ds_read_b128 v[240:243], v253 offset:24576\n\t" \
    "v_mfma_f32_16x16x32_bf16 v[208:211], v[72:75], v[224:227], v[208:211]\n\t" \
    "v_mfma_f32_16x16x32_bf16 v[208:211], v[76:79], v[228:231], v[208:211]\n\t" \
    "v_mfma_f32_16x16x32_bf16 v[200:203], v[56:59], v[224:227], v[200:203]\n\t" \
    "v_mfma_f32_16x16x32_bf16 v[200:203], v[60:63], v[228:231], v[200:203]\n\t" \
    "s_waitcnt lgkmcnt(4)\n\t" \
    "v_mfma_f32_16x16x32_bf16 v[204:207], v[80:83], v[232:235], v[204:207]\n\t" \
    "ds_read_b128 v[224:227], v253 offset:25600\n\t" \
    "s_waitcnt lgkmcnt(2)\n\t" \
    "v_mfma_f32_16x16x32_bf16 v[204:207], v[84:87], v[236:239], v[204:207]\n\t" \
    "ds_read_b128 v[228:231], v253 offset:6144\n\t" \
    "v_mfma_f32_16x16x32_bf16 v[208:211], v[88:91], v[232:235], v[208:211]\n\t" \
    "v_mfma_f32_16x16x32_bf16 v[208:211], v[92:95], v[236:239], v[208:211]\n\t" \
    "v_mfma_f32_16x16x32_bf16 v[200:203], v[244:247], v[232:235], v[200:203]\n\t" \
    "v_mfma_f32_16x16x32_bf16 v[200:203], v[248:251], v[236:239], v[200:203]\n\t" \
    "s_waitcnt lgkmcnt(2)\n\t" \
    "v_mfma_f32_16x16x32_bf16 v[216:219], v[80:83], v[240:243], v[216:219]\n\t" \
    "ds_read_b128 v[232:235], v253 offset:7168\n\t" \
    "s_and_b32 s44, s40, 3\n\t" \
    "s_waitcnt lgkmcnt(2)\n\t" \
    "v_mfma_f32_16x16x32_bf16 v[216:219], v[84:87], v[224:227], v[216:219]\n\t" \
    "ds_read_b128 v[236:239], v253 offset:8192\n\t" \
    "s_lshl_b32 s44, s44, 22\n\t" \
    "v_mfma_f32_16x16x32_bf16 v[220:223], v[88:91], v[240:243], v[220:223]\n\t" \
    "s_add_u32 s44, s44, %[mb]\n\t" \
    "v_mfma_f32_16x16x32_bf16 v[220:223], v[92:95], v[224:227], v[220:223]\n\t" \
    "s_add_u32 s46, s44, 0x10000\n\t" \
    "v_mfma_f32_16x16x32_bf16 v[212:215], v[244:247], v[240:243], v[212:215]\n\t" \
    "ds_read_b128 v[244:247], v254 offset:61440\n\t" \
    "s_add_u32 s47, s44, 0x10400\n\t" \
    "v_mfma_f32_16x16x32_bf16 v[212:215], v[248:251], v[224:227], v[212:215]\n\t" \
    "ds_read_b128 v[248:251], v254 offset:62464\n\t" \
    "s_waitcnt lgkmcnt(4)\n\t" \
    "v_mfma_f32_16x16x32_bf16 v[204:207], v[104:107], v[228:231], v[204:207]\n\t" \
    "ds_read_b128 v[240:243], v253 offset:9216\n\t" \
    "s_waitcnt lgkmcnt(4)\n\t" \
    "v_mfma_f32_16x16x32_bf16 v[204:207], v[108:111], v[232:235], v[204:207]\n\t" \
    "ds_read_b128 v[224:227], v253 offset:28672\n\t" \
    "v_mfma_f32_16x16x32_bf16 v[208:211], v[112:115], v[228:231], v[208:211]\n\t" \
    "v_mfma_f32_16x16x32_bf16 v[208:211], v[116:119], v[232:235], v[208:211]\n\t" \
    "v_mfma_f32_16x16x32_bf16 v[200:203], v[96:99], v[228:231], v[200:203]\n\t" \
    "v_mfma_f32_16x16x32_bf16 v[200:203], v[100:103], v[232:235], v[200:203]\n\t" \
    "s_waitcnt lgkmcnt(4)\n\t" \
    "v_mfma_f32_16x16x32_bf16 v[204:207], v[120:123], v[236:239], v[204:207]\n\t" \
    "ds_read_b128 v[228:231], v253 offset:29696\n\t" \
    "s_waitcnt lgkmcnt(2)\n\t" \
    "v_mfma_f32_16x16x32_bf16 v[204:207], v[124:127], v[240:243], v[204:207]\n\t" \
    "ds_read_b128 v[232:235], v253 offset:10240\n\t" \
    "v_mfma_f32_16x16x32_bf16 v[208:211], v[128:131], v[236:239], v[208:211]\n\t" \
    "v_mfma_f32_16x16x32_bf16 v[208:211], v[132:135], v[240:243], v[208:211]\n\t" \
    "v_mfma_f32_16x16x32_bf16 v[200:203], v[244:247], v[236:239], v[200:203]\n\t" \
    "v_mfma_f32_16x16x32_bf16 v[200:203], v[248:251], v[240:243], v[200:203]\n\t" \
    "s_waitcnt lgkmcnt(2)\n\t" \
    "v_mfma_f32_16x16x32_bf16 v[216:219], v[120:123], v[224:227], v[216:219]\n\t" \
    "ds_read_b128 v[236:239], v253 offset:11264\n\t" \
    "s_waitcnt lgkmcnt(2)\n\t" \
    "v_mfma_f32_16x16x32_bf16 v[216:219], v[124:127], v[228:231], v[216:219]\n\t" \
    "ds_read_b128 v[240:243], v253 offset:12288\n\t" \
    "v_mfma_f32_16x16x32_bf16 v[220:223], v[128:131], v[224:227], v[220:223]\n\t" \
    "v_mfma_f32_16x16x32_bf16 v[220:223], v[132:135], v[228:231], v[220:223]\n\t" \
    "v_mfma_f32_16x16x32_bf16 v[212:215], v[244:247], v[224:227], v[212:215]\n\t" \
    "ds_read_b128 v[244:247], v254 offset:63488\n\t" \
    "v_mfma_f32_16x16x32_bf16 v[212:215], v[248:251], v[228:231], v[212:215]\n\t" \
    "ds_read_b128 v[248:251], v254 offset:64512\n\t" \
    "v_and_b32 v252, 0xf0, v253\n\t" \
    "v_lshlrev_b32 v252, 3, v252\n\t" \
    "buffer_load_dword v252, v252, s[60:63], %[fl] offen sc1\n\t" \
    "s_waitcnt lgkmcnt(4)\n\t" \
    "v_mfma_f32_16x16x32_bf16 v[204:207], v[144:147], v[232:235], v[204:207]\n\t" \
    "ds_read_b128 v[224:227], v253 offset:13312\n\t" \
    "s_waitcnt lgkmcnt(4)\n\t" \
    "v_mfma_f32_16x16x32_bf16 v[204:207], v[148:151], v[236:239], v[204:207]\n\t" \
    "ds_read_b128 v[228:231], v253 offset:32768\n\t" \
    "v_mfma_f32_16x16x32_bf16 v[208:211], v[152:155], v[232:235], v[208:211]\n\t" \
    "v_mfma_f32_16x16x32_bf16 v[208:211], v[156:159], v[236:239], v[208:211]\n\t" \
    "v_mfma_f32_16x16x32_bf16 v[200:203], v[136:139], v[232:235], v[200:203]\n\t" \
    "v_mfma_f32_16x16x32_bf16 v[200:203], v[140:143], v[236:239], v[200:203]\n\t" \
    "s_waitcnt lgkmcnt(4)\n\t" \
    "v_mfma_f32_16x16x32_bf16 v[204:207], v[160:163], v[240:243], v[204:207]\n\t" \
    "ds_read_b128 v[232:235], v253 offset:33792\n\t" \
    "s_waitcnt lgkmcnt(2)\n\t" \
    "v_mfma_f32_16x16x32_bf16 v[204:207], v[164:167], v[224:227], v[204:207]\n\t" \
    "ds_read_b128 v[236:239], v253 offset:14336\n\t" \
    "v_mfma_f32_16x16x32_bf16 v[208:211], v[168:171], v[240:243], v[208:211]\n\t" \
    "v_mfma_f32_16x16x32_bf16 v[208:211], v[172:175], v[224:227], v[208:211]\n\t" \
    "v_mfma_f32_16x16x32_bf16 v[200:203], v[244:247], v[240:243], v[200:203]\n\t" \
    "v_mfma_f32_16x16x32_bf16 v[200:203], v[248:251], v[224:227], v[200:203]\n\t" \
    "s_waitcnt lgkmcnt(2)\n\t" \
    "v_mfma_f32_16x16x32_bf16 v[216:219], v[160:163], v[228:231], v[216:219]\n\t" \
    "ds_read_b128 v[240:243], v253 offset:15360\n\t" \
    "s_waitcnt lgkmcnt(2)\n\t" \
    "v_mfma_f32_16x16x32_bf16 v[216:219], v[164:167], v[232:235], v[216:219]\n\t" \
    "ds_read_b128 v[224:227], v253 offset:38912\n\t" \
    "v_mfma_f32_16x16x32_bf16 v[220:223], v[168:171], v[228:231], v[220:223]\n\t" \
    "v_mfma_f32_16x16x32_bf16 v[220:223], v[172:175], v[232:235], v[220:223]\n\t" \
    "v_mfma_f32_16x16x32_bf16 v[212:215], v[244:247], v[228:231], v[212:215]\n\t" \
    "v_mfma_f32_16x16x32_bf16 v[212:215], v[248:251], v[232:235], v[212:215]\n\t" \
    "s_waitcnt lgkmcnt(2)\n\t" \
    "v_mfma_f32_16x16x32_bf16 v[204:207], v[184:187], v[236:239], v[204:207]\n\t" \
    "ds_read_b128 v[228:231], v253 offset:39936\n\t" \
    "s_waitcnt lgkmcnt(2)\n\t" \
    "v_mfma_f32_16x16x32_bf16 v[204:207], v[188:191], v[240:243], v[204:207]\n\t" \
    "ds_read_b128 v[232:235], v253 offset:22528\n\t" \
    "v_mfma_f32_16x16x32_bf16 v[208:211], v[192:195], v[236:239], v[208:211]\n\t" \
    "v_mfma_f32_16x16x32_bf16 v[208:211], v[196:199], v[240:243], v[208:211]\n\t" \
    "v_mfma_f32_16x16x32_bf16 v[200:203], v[176:179], v[236:239], v[200:203]\n\t" \
    "v_mfma_f32_16x16x32_bf16 v[200:203], v[180:183], v[240:243], v[200:203]\n\t" \
    "s_waitcnt lgkmcnt(2)\n\t" \
    "v_mfma_f32_16x16x32_bf16 v[216:219], v[24:27], v[224:227], v[216:219]\n\t" \
    "ds_read_b128 v[236:239], v253 offset:23552\n\t" \
    "s_waitcnt lgkmcnt(2)\n\t" \
    "v_mfma_f32_16x16x32_bf16 v[216:219], v[28:31], v[228:231], v[216:219]\n\t" \
    "ds_read_b128 v[240:243], v253 offset:26624\n\t" \
    "v_mfma_f32_16x16x32_bf16 v[220:223], v[32:35], v[224:227], v[220:223]\n\t" \
    "v_cvt_pk_bf16_f32 v204, v204, v205\n\t" \
    "v_mfma_f32_16x16x32_bf16 v[220:223], v[36:39], v[228:231], v[220:223]\n\t" \
    "v_cvt_pk_bf16_f32 v205, v206, v207\n\t" \
    "v_mfma_f32_16x16x32_bf16 v[212:215], v[16:19], v[224:227], v[212:215]\n\t" \
    "ds_write_b64 v254, v[204:205] offset:4104\n\t" \
    "v_mfma_f32_16x16x32_bf16 v[212:215], v[20:23], v[228:231], v[212:215]\n\t" \
    "v_cvt_pk_bf16_f32 v208, v208, v209\n\t" \
    "s_waitcnt lgkmcnt(3)\n\t" \
    "v_mfma_f32_16x16x32_bf16 v[216:219], v[64:67], v[232:235], v[216:219]\n\t" \
    "ds_read_b128 v[224:227], v253 offset:27648\n\t" \
    "v_cvt_pk_bf16_f32 v209, v210, v211\n\t" \
    "s_waitcnt lgkmcnt(3)\n\t" \
    "v_mfma_f32_16x16x32_bf16 v[216:219], v[68:71], v[236:239], v[216:219]\n\t" \
    "ds_read_b128 v[228:231], v253 offset:30720\n\t" \
    "ds_write_b64 v254, v[208:209] offset:5128\n\t" \
    "v_mfma_f32_16x16x32_bf16 v[220:223], v[72:75], v[232:235], v[220:223]\n\t" \
    "v_cvt_pk_bf16_f32 v200, v200, v201\n\t" \
    "v_mfma_f32_16x16x32_bf16 v[220:223], v[76:79], v[236:239], v[220:223]\n\t" \
    "v_cvt_pk_bf16_f32 v201, v202, v203\n\t" \
    "v_mfma_f32_16x16x32_bf16 v[212:215], v[56:59], v[232:235], v[212:215]\n\t" \
    "ds_write_b64 v254, v[200:201] offset:3080\n\t" \
    "v_mfma_f32_16x16x32_bf16 v[212:215], v[60:63], v[236:239], v[212:215]\n\t" \
    "s_waitcnt lgkmcnt(5)\n\t" \
    "v_mfma_f32_16x16x32_bf16 v[216:219], v[104:107], v[240:243], v[216:219]\n\t" \
    "ds_read_b128 v[232:235], v253 offset:31744\n\t" \
    "s_waitcnt lgkmcnt(4)\n\t" \
    "v_mfma_f32_16x16x32_bf16 v[216:219], v[108:111], v[224:227], v[216:219]\n\t" \
    "ds_read_b128 v[236:239], v253 offset:34816\n\t" \
    "v_mfma_f32_16x16x32_bf16 v[220:223], v[112:115], v[240:243], v[220:223]\n\t" \
    "v_mfma_f32_16x16x32_bf16 v[220:223], v[116:119], v[224:227], v[220:223]\n\t" \
    "v_mfma_f32_16x16x32_bf16 v[212:215], v[96:99], v[240:243], v[212:215]\n\t" \
    "v_mfma_f32_16x16x32_bf16 v[212:215], v[100:103], v[224:227], v[212:215]\n\t" \
    "ds_read_b128 v[240:243], v253 offset:35840\n\t" \
    "s_waitcnt lgkmcnt(3)\n\t" \
    "s_barrier \n\t" \
    "v_mfma_f32_16x16x32_bf16 v[216:219], v[144:147], v[228:231], v[216:219]\n\t" \
    "s_waitcnt lgkmcnt(2)\n\t" \
    "v_mfma_f32_16x16x32_bf16 v[216:219], v[148:151], v[232:235], v[216:219]\n\t" \
    "s_waitcnt lgkmcnt(1)\n\t" \
    "v_mfma_f32_16x16x32_bf16 v[216:219], v[184:187], v[236:239], v[216:219]\n\t" \
    "s_waitcnt lgkmcnt(0)\n\t" \
    "v_mfma_f32_16x16x32_bf16 v[216:219], v[188:191], v[240:243], v[216:219]\n\t" \
    "v_mfma_f32_16x16x32_bf16 v[220:223], v[152:155], v[228:231], v[220:223]\n\t" \
    "v_mfma_f32_16x16x32_bf16 v[220:223], v[156:159], v[232:235], v[220:223]\n\t" \
    "v_mfma_f32_16x16x32_bf16 v[220:223], v[192:195], v[236:239], v[220:223]\n\t" \
    "v_cvt_pk_bf16_f32 v216, v216, v217\n\t" \
    "v_cvt_pk_bf16_f32 v217, v218, v219\n\t" \
    "v_mfma_f32_16x16x32_bf16 v[220:223], v[196:199], v[240:243], v[220:223]\n\t" \
    "ds_write_b64 v254, v[216:217] offset:10248\n\t" \
    "v_mfma_f32_16x16x32_bf16 v[212:215], v[136:139], v[228:231], v[212:215]\n\t" \
    "v_mfma_f32_16x16x32_bf16 v[212:215], v[140:143], v[232:235], v[212:215]\n\t" \
    "v_mfma_f32_16x16x32_bf16 v[212:215], v[176:179], v[236:239], v[212:215]\n\t" \
    "v_cvt_pk_bf16_f32 v220, v220, v221\n\t" \
    "v_cvt_pk_bf16_f32 v221, v222, v223\n\t" \
    "v_mfma_f32_16x16x32_bf16 v[212:215], v[180:183], v[240:243], v[212:215]\n\t" \
    "ds_write_b64 v254, v[220:221] offset:11272\n\t" \
    "s_nop 6\n\t" \
    "v_cvt_pk_bf16_f32 v212, v212, v213\n\t" \
    "v_cvt_pk_bf16_f32 v213, v214, v215\n\t" \
    "ds_write_b64 v254, v[212:213] offset:9224\n\t" \
    "s_waitcnt vmcnt(0) lgkmcnt(0)\n\t" \
    "s_barrier \n\t" \
    "s_setprio 2\n\t" \
    "s_add_u32 s44, s40, 1\n\t" \
    "s_min_u32 s44, s44, 63\n\t" \
    ".Lv38_%=:\n\t" \
    "v_cmp_ge_u32 vcc, s44, v252\n\t" \
    "s_cbranch_vccz .Lv39_%=\n\t" \
    "s_cmp_eq_u32 s41, 0\n\t" \
    "s_cbranch_scc1 .Lv39_%=\n\t" \
    "s_sub_u32 s41, s41, 1\n\t" \
    "s_sleep 2\n\t" \
    "v_and_b32 v252, 0xf0, v253\n\t" \
    "v_lshlrev_b32 v252, 3, v252\n\t" \
    "buffer_load_dword v252, v252, s[60:63], %[fl] offen sc1\n\t" \
    "s_waitcnt vmcnt(0)\n\t" \
    "s_branch .Lv38_%=\n\t" \
    ".Lv39_%=:\n\t" \
    "s_mov_b32 m0, 0x3000\n\t" \
    "s_add_u32 s49, s42, 0x0\n\t" \
    "buffer_load_dwordx4 v253, s[60:63], s49 offen offset:0 sc1 lds\n\t" \
    "s_sleep 1\n\t" \
    "buffer_load_dwordx4 v253, s[60:63], s49 offen offset:1024 sc1 lds\n\t" \
    "s_sleep 1\n\t" \
    "buffer_load_dwordx4 v253, s[60:63], s49 offen offset:2048 sc1 lds\n\t" \
    "s_sleep 1\n\t" \
    "buffer_load_dwordx4 v253, s[60:63], s49 offen offset:3072 sc1 lds\n\t" \
    "s_sleep 1\n\t" \
    "s_mov_b32 m0, 0x4c00\n\t" \
    "s_add_u32 s49, s42, 0x4c00\n\t" \
    "buffer_load_dwordx4 v253, s[60:63], s49 offen offset:0 sc1 lds\n\t" \
    "s_sleep 1\n\t" \
    "s_mov_b32 m0, 0x8000\n\t" \
    "s_add_u32 s49, s42, 0x1000\n\t" \
    "buffer_load_dwordx4 v253, s[60:63], s49 offen offset:0 sc1 lds\n\t" \
    "s_sleep 1\n\t" \
    "buffer_load_dwordx4 v253, s[60:63], s49 offen offset:1024 sc1 lds\n\t" \
    "s_sleep 1\n\t" \
    "buffer_load_dwordx4 v253, s[60:63], s49 offen offset:2048 sc1 lds\n\t" \
    "s_sleep 1\n\t" \
    "buffer_load_dwordx4 v253, s[60:63], s49 offen offset:3072 sc1 lds\n\t" \
    "s_sleep 1\n\t" \
    "s_mov_b32 m0, 0x9c00\n\t" \
    "s_add_u32 s49, s42, 0x5c00\n\t" \
    "buffer_load_dwordx4 v253, s[60:63], s49 offen offset:0 sc1 lds\n\t" \
    "s_sleep 1\n\t" \
    "ds_read_b128 v[200:203], v254 offset:55296\n\t" \
    "ds_read_b128 v[204:207], v254 offset:56320\n\t" \
    "ds_read_b128 v[216:219], v254 offset:57344\n\t" \
    "ds_read_b128 v[220:223], v254 offset:58368\n\t" \
    "v_mov_b64 v[208:209], 0\n\t" \
    "v_mov_b64 v[210:211], 0\n\t" \
    "v_mov_b64 v[244:245], 0\n\t" \
    "v_mov_b64 v[246:247], 0\n\t" \
    "v_mov_b64 v[248:249], 0\n\t" \
    "v_mov_b64 v[250:251], 0\n\t" \
    "s_sleep 5\n\t" \
    "s_mov_b32 m0, 0xc000\n\t" \
    "s_nop 0\n\t" \
    "buffer_load_dwordx4 v253, s[60:63], s46 offen sc1 lds\n\t" \
    "s_mov_b32 m0, 0xc400\n\t" \
    "s_nop 0\n\t" \
    "buffer_load_dwordx4 v253, s[60:63], s47 offen sc1 lds\n\t" \
    "s_waitcnt lgkmcnt(0)\n\t" \
    "v_bfe_i32 v212, v253, 5, 1\n\t" \
    "v_and_b32 v212, 0xaaaa, v212\n\t" \
    "v_or_b32 v212, 0x4444, v212\n\t" \
    "v_and_b32 v213, 0xffff, v255\n\t" \
    "v_lshrrev_b32 v214, 16, v255\n\t" \
    ".Lv310_%=:\n\t" \
    "s_waitcnt vmcnt(0)\n\t" \
    "ds_read_b128 v[224:227], v253 offset:49152\n\t" \
    "ds_read_b128 v[228:231], v253 offset:50176\n\t" \
    "s_waitcnt lgkmcnt(1)\n\t" \
    "v_max_u32 v215, v224, v225\n\t" \
    "v_max3_u32 v215, v226, v227, v215\n\t" \
    "s_waitcnt lgkmcnt(0)\n\t" \
    "v_max3_u32 v215, v228, v229, v215\n\t" \
    "v_max3_u32 v215, v230, v231, v215\n\t" \
    "v_cmp_eq_u32 vcc, -1, v215\n\t" \
    "s_cbranch_vccz .Lv311_%=\n\t" \
    "v_max_u32 v215, v224, v225\n\t" \
    "v_max3_u32 v215, v226, v227, v215\n\t" \
    "v_cmp_eq_u32 vcc, -1, v215\n\t" \
    "s_cbranch_vccz .Lv312_%=\n\t" \
    "s_mov_b32 m0, 0xc000\n\t" \
    "s_nop 0\n\t" \
    "buffer_load_dwordx4 v253, s[60:63], s46 offen sc1 lds\n\t" \
    ".Lv312_%=:\n\t" \
    "v_max_u32 v215, v228, v229\n\t" \
    "v_max3_u32 v215, v230, v231, v215\n\t" \
    "v_cmp_eq_u32 vcc, -1, v215\n\t" \
    "s_cbranch_vccz .Lv313_%=\n\t" \
    "s_mov_b32 m0, 0xc400\n\t" \
    "s_nop 0\n\t" \
    "buffer_load_dwordx4 v253, s[60:63], s47 offen sc1 lds\n\t" \
    ".Lv313_%=:\n\t" \
    "s_cmp_eq_u32 s41, 0\n\t" \
    "s_cbranch_scc1 .Lv311_%=\n\t" \
    "s_sub_u32 s41, s41, 1\n\t" \
    "s_branch .Lv310_%=\n\t" \
    ".Lv311_%=:\n\t" \
    "s_waitcnt lgkmcnt(0)\n\t" \
    "s_barrier \n\t" \
    "ds_read_b128 v[232:235], v253 offset:40960\n\t" \
    "ds_read_b128 v[236:239], v253 offset:41984\n\t" \
    "ds_read_b128 v[240:243], v253 offset:43008\n\t" \
    "v_smfmac_f32_16x16x64_bf16 v[244:247], v[224:227], v[0:7], v212\n\t" \
    "v_smfmac_f32_16x16x64_bf16 v[248:251], v[224:227], v[8:15], v212\n\t" \
    "v_smfmac_f32_16x16x64_bf16 v[208:211], v[224:227], v[200:207], v212\n\t" \
    "ds_read_b128 v[200:203], v254 offset:59392\n\t" \
    "ds_read_b128 v[204:207], v254 offset:60416\n\t" \
    "ds_read_b128 v[224:227], v253 offset:44032\n\t" \
    "v_smfmac_f32_16x16x64_bf16 v[208:211], v[228:231], v[16:23], v212\n\t" \
    "v_smfmac_f32_16x16x64_bf16 v[244:247], v[228:231], v[24:31], v212\n\t" \
    "v_smfmac_f32_16x16x64_bf16 v[248:251], v[228:231], v[32:39], v212\n\t" \
    "ds_read_b128 v[228:231], v253 offset:45056\n\t" \
    "s_waitcnt lgkmcnt(6)\n\t" \
    "v_smfmac_f32_16x16x64_bf16 v[208:211], v[232:235], v[216:223], v212\n\t" \
    "ds_read_b128 v[216:219], v254 offset:61440\n\t" \
    "ds_read_b128 v[220:223], v254 offset:62464\n\t" \
    "v_smfmac_f32_16x16x64_bf16 v[244:247], v[232:235], v[40:47], v212\n\t" \
    "v_smfmac_f32_16x16x64_bf16 v[248:251], v[232:235], v[48:55], v212\n\t" \
    "ds_read_b128 v[232:235], v253 offset:46080\n\t" \
    "s_waitcnt lgkmcnt(8)\n\t" \
    "v_smfmac_f32_16x16x64_bf16 v[208:211], v[236:239], v[56:63], v212\n\t" \
    "v_smfmac_f32_16x16x64_bf16 v[244:247], v[236:239], v[64:71], v212\n\t" \
    "v_smfmac_f32_16x16x64_bf16 v[248:251], v[236:239], v[72:79], v212\n\t" \
    "ds_read_b128 v[236:239], v253 offset:47104\n\t" \
    "s_waitcnt lgkmcnt(6)\n\t" \
    "v_smfmac_f32_16x16x64_bf16 v[208:211], v[240:243], v[200:207], v212\n\t" \
    "ds_read_b128 v[200:203], v254 offset:63488\n\t" \
    "ds_read_b128 v[204:207], v254 offset:64512\n\t" \
    "v_smfmac_f32_16x16x64_bf16 v[244:247], v[240:243], v[80:87], v212\n\t" \
    "v_smfmac_f32_16x16x64_bf16 v[248:251], v[240:243], v[88:95], v212\n\t" \
    "ds_read_b128 v[240:243], v253 offset:48128\n\t" \
    "s_waitcnt lgkmcnt(8)\n\t" \
    "v_smfmac_f32_16x16x64_bf16 v[208:211], v[224:227], v[96:103], v212\n\t" \
    "v_smfmac_f32_16x16x64_bf16 v[244:247], v[224:227], v[104:111], v212\n\t" \
    "v_smfmac_f32_16x16x64_bf16 v[248:251], v[224:227], v[112:119], v212\n\t" \
    "s_waitcnt lgkmcnt(7)\n\t" \
    "v_smfmac_f32_16x16x64_bf16 v[244:247], v[228:231], v[120:127], v212\n\t" \
    "s_waitcnt lgkmcnt(4)\n\t" \
    "v_smfmac_f32_16x16x64_bf16 v[244:247], v[232:235], v[144:151], v212\n\t" \
    "s_waitcnt lgkmcnt(3)\n\t" \
    "v_smfmac_f32_16x16x64_bf16 v[244:247], v[236:239], v[160:167], v212\n\t" \
    "s_waitcnt lgkmcnt(0)\n\t" \
    "v_smfmac_f32_16x16x64_bf16 v[244:247], v[240:243], v[184:191], v212\n\t" \
    "v_smfmac_f32_16x16x64_bf16 v[248:251], v[228:231], v[128:135], v212\n\t" \
    "v_smfmac_f32_16x16x64_bf16 v[248:251], v[232:235], v[152:159], v212\n\t" \
    "v_smfmac_f32_16x16x64_bf16 v[248:251], v[236:239], v[168:175], v212\n\t" \
    "v_add_f32 v244, v244, v246\n\t" \
    "v_add_f32 v245, v245, v247\n\t" \
    "v_cvt_pk_bf16_f32 v244, v244, v245\n\t" \
    "v_smfmac_f32_16x16x64_bf16 v[248:251], v[240:243], v[192:199], v212\n\t" \
    "ds_write_b16 v213, v244 offset:512\n\t" \
    "ds_write_b16_d16_hi v213, v244 offset:528\n\t" \
    "v_smfmac_f32_16x16x64_bf16 v[208:211], v[228:231], v[216:223], v212\n\t" \
    "v_smfmac_f32_16x16x64_bf16 v[208:211], v[232:235], v[136:143], v212\n\t" \
    "v_smfmac_f32_16x16x64_bf16 v[208:211], v[236:239], v[200:207], v212\n\t" \
    "v_add_f32 v248, v248, v250\n\t" \
    "v_add_f32 v249, v249, v251\n\t" \
    "v_cvt_pk_bf16_f32 v248, v248, v249\n\t" \
    "v_smfmac_f32_16x16x64_bf16 v[208:211], v[240:243], v[176:183], v212\n\t" \
    "ds_write_b16 v213, v248 offset:1024\n\t" \
    "ds_write_b16_d16_hi v213, v248 offset:1040\n\t" \
    "s_nop 5\n\t" \
    "v_add_f32 v208, v208, v210\n\t" \
    "v_add_f32 v209, v209, v211\n\t" \
    "v_cvt_pk_bf16_f32 v208, v208, v209\n\t" \
    "ds_write_b16 v213, v208 offset:0\n\t" \
    "ds_write_b16_d16_hi v213, v208 offset:16\n\t" \
    "s_setprio 0\n\t" \
    "s_waitcnt lgkmcnt(0)\n\t" \
    "s_barrier \n\t" \
    "ds_read_b128 v[224:227], v253 offset:16384\n\t" \
    "ds_read_b128 v[244:247], v254 offset:55296\n\t" \
    "ds_read_b128 v[228:231], v253 offset:17408\n\t" \
    "ds_read_b128 v[232:235], v253 offset:36864\n\t" \
    "ds_read_b128 v[236:239], v253 offset:37888\n\t" \
    "ds_read_b128 v[248:251], v254 offset:56320\n\t" \
    "s_add_u32 s40, s40, 1\n\t" \
    "s_cmp_lg_u32 s40, 64\n\t" \
    "s_cbranch_scc1 .Lv37_%=\n\t" \
    "s_branch .Lend%=\n\t" \
    ".Lv2_%=:\n\t" \
    "s_mov_b32 s42, %[xs]\n\t" \
    "v_add_u32 v244, 0xc000, v243\n\t" \
    "s_waitcnt vmcnt(12) lgkmcnt(0)\n\t" \
    "s_barrier \n\t" \
    "s_add_u32 m0, %[wm], 0x9000\n\t" \
    "s_mov_b32 s44, 0x1e0000\n\t" \
    "buffer_load_dwordx4 v240, %[wr], s44 offen offset:0 nt lds\n\t" \
    "buffer_load_dwordx4 v241, %[wr], s44 offen offset:1024 nt lds\n\t" \
    "buffer_load_dwordx4 v242, %[wr], s44 offen offset:2048 nt lds\n\t" \
    "ds_read_b64_tr_b16 v[208:209], v244 offset:0\n\t" \
    "ds_read_b64_tr_b16 v[210:211], v244 offset:1536\n\t" \
    "ds_read_b64_tr_b16 v[40:41], v244 offset:32\n\t" \
    "ds_read_b64_tr_b16 v[42:43], v244 offset:1568\n\t" \
    "ds_read_b64_tr_b16 v[48:49], v244 offset:64\n\t" \
    "ds_read_b64_tr_b16 v[50:51], v244 offset:1600\n\t" \
    "v_add_u32 v244, 0xf000, v243\n\t" \
    "s_waitcnt vmcnt(12) lgkmcnt(0)\n\t" \
    "s_barrier \n\t" \
    "s_add_u32 m0, %[wm], 0xc000\n\t" \
    "s_mov_b32 s44, 0x240000\n\t" \
    "buffer_load_dwordx4 v240, %[wr], s44 offen offset:0 nt lds\n\t" \
    "buffer_load_dwordx4 v241, %[wr], s44 offen offset:1024 nt lds\n\t" \
    "buffer_load_dwordx4 v242, %[wr], s44 offen offset:2048 nt lds\n\t" \
    "ds_read_b64_tr_b16 v[212:213], v244 offset:0\n\t" \
    "ds_read_b64_tr_b16 v[214:215], v244 offset:1536\n\t" \
    "ds_read_b64_tr_b16 v[44:45], v244 offset:32\n\t" \
    "ds_read_b64_tr_b16 v[46:47], v244 offset:1568\n\t" \
    "ds_read_b64_tr_b16 v[52:53], v244 offset:64\n\t" \
    "ds_read_b64_tr_b16 v[54:55], v244 offset:1600\n\t" \
    "v_add_u32 v244, 0x0, v243\n\t" \
    "s_waitcnt vmcnt(12) lgkmcnt(0)\n\t" \
    "s_barrier \n\t" \
    "s_add_u32 m0, %[wm], 0xf000\n\t" \
    "s_mov_b32 s44, 0x2a0000\n\t" \
    "buffer_load_dwordx4 v240, %[wr], s44 offen offset:0 nt lds\n\t" \
    "buffer_load_dwordx4 v241, %[wr], s44 offen offset:1024 nt lds\n\t" \
    "buffer_load_dwordx4 v242, %[wr], s44 offen offset:2048 nt lds\n\t" \
    "ds_read_b64_tr_b16 v[56:57], v244 offset:0\n\t" \
    "ds_read_b64_tr_b16 v[58:59], v244 offset:1536\n\t" \
    "ds_read_b64_tr_b16 v[64:65], v244 offset:32\n\t" \
    "ds_read_b64_tr_b16 v[66:67], v244 offset:1568\n\t" \
    "ds_read_b64_tr_b16 v[72:73], v244 offset:64\n\t" \
    "ds_read_b64_tr_b16 v[74:75], v244 offset:1600\n\t" \
    "v_add_u32 v244, 0x3000, v243\n\t" \
    "s_waitcnt vmcnt(12) lgkmcnt(0)\n\t" \
    "s_barrier \n\t" \
    "s_add_u32 m0, %[wm], 0x0\n\t" \
    "s_mov_b32 s44, 0x300000\n\t" \
    "buffer_load_dwordx4 v240, %[wr], s44 offen offset:0 nt lds\n\t" \
    "buffer_load_dwordx4 v241, %[wr], s44 offen offset:1024 nt lds\n\t" \
    "buffer_load_dwordx4 v242, %[wr], s44 offen offset:2048 nt lds\n\t" \
    "ds_read_b64_tr_b16 v[60:61], v244 offset:0\n\t" \
    "ds_read_b64_tr_b16 v[62:63], v244 offset:1536\n\t" \
    "ds_read_b64_tr_b16 v[68:69], v244 offset:32\n\t" \
    "ds_read_b64_tr_b16 v[70:71], v244 offset:1568\n\t" \
    "ds_read_b64_tr_b16 v[76:77], v244 offset:64\n\t" \
    "ds_read_b64_tr_b16 v[78:79], v244 offset:1600\n\t" \
    "v_add_u32 v244, 0x6000, v243\n\t" \
    "s_waitcnt vmcnt(12) lgkmcnt(0)\n\t" \
    "s_barrier \n\t" \
    "s_add_u32 m0, %[wm], 0x3000\n\t" \
    "s_mov_b32 s44, 0x360000\n\t" \
    "buffer_load_dwordx4 v240, %[wr], s44 offen offset:0 nt lds\n\t" \
    "buffer_load_dwordx4 v241, %[wr], s44 offen offset:1024 nt lds\n\t" \
    "buffer_load_dwordx4 v242, %[wr], s44 offen offset:2048 nt lds\n\t" \
    "ds_read_b64_tr_b16 v[216:217], v244 offset:0\n\t" \
    "ds_read_b64_tr_b16 v[218:219], v244 offset:1536\n\t" \
    "ds_read_b64_tr_b16 v[80:81], v244 offset:32\n\t" \
    "ds_read_b64_tr_b16 v[82:83], v244 offset:1568\n\t" \
    "ds_read_b64_tr_b16 v[88:89], v244 offset:64\n\t" \
    "ds_read_b64_tr_b16 v[90:91], v244 offset:1600\n\t" \
    "v_add_u32 v244, 0x9000, v243\n\t" \
    "s_waitcnt vmcnt(12) lgkmcnt(0)\n\t" \
    "s_barrier \n\t" \
    "s_add_u32 m0, %[wm], 0x6000\n\t" \
    "s_mov_b32 s44, 0x3c0000\n\t" \
    "buffer_load_dwordx4 v240, %[wr], s44 offen offset:0 nt lds\n\t" \
    "buffer_load_dwordx4 v241, %[wr], s44 offen offset:1024 nt lds\n\t" \
    "buffer_load_dwordx4 v242, %[wr], s44 offen offset:2048 nt lds\n\t" \
    "ds_read_b64_tr_b16 v[220:221], v244 offset:0\n\t" \
    "ds_read_b64_tr_b16 v[222:223], v244 offset:1536\n\t" \
    "ds_read_b64_tr_b16 v[84:85], v244 offset:32\n\t" \
    "ds_read_b64_tr_b16 v[86:87], v244 offset:1568\n\t" \
    "ds_read_b64_tr_b16 v[92:93], v244 offset:64\n\t" \
    "ds_read_b64_tr_b16 v[94:95], v244 offset:1600\n\t" \
    "v_add_u32 v244, 0xc000, v243\n\t" \
    "s_waitcnt vmcnt(12) lgkmcnt(0)\n\t" \
    "s_barrier \n\t" \
    "s_add_u32 m0, %[wm], 0x9000\n\t" \
    "s_mov_b32 s44, 0x420000\n\t" \
    "buffer_load_dwordx4 v240, %[wr], s44 offen offset:0 nt lds\n\t" \
    "buffer_load_dwordx4 v241, %[wr], s44 offen offset:1024 nt lds\n\t" \
    "buffer_load_dwordx4 v242, %[wr], s44 offen offset:2048 nt lds\n\t" \
    "ds_read_b64_tr_b16 v[96:97], v244 offset:0\n\t" \
    "ds_read_b64_tr_b16 v[98:99], v244 offset:1536\n\t" \
    "ds_read_b64_tr_b16 v[104:105], v244 offset:32\n\t" \
    "ds_read_b64_tr_b16 v[106:107], v244 offset:1568\n\t" \
    "ds_read_b64_tr_b16 v[112:113], v244 offset:64\n\t" \
    "ds_read_b64_tr_b16 v[114:115], v244 offset:1600\n\t" \
    "v_add_u32 v244, 0xf000, v243\n\t" \
    "s_waitcnt vmcnt(12) lgkmcnt(0)\n\t" \
    "s_barrier \n\t" \
    "s_add_u32 m0, %[wm], 0xc000\n\t" \
    "s_mov_b32 s44, 0x480000\n\t" \
    "buffer_load_dwordx4 v240, %[wr], s44 offen offset:0 nt lds\n\t" \
    "buffer_load_dwordx4 v241, %[wr], s44 offen offset:1024 nt lds\n\t" \
    "buffer_load_dwordx4 v242, %[wr], s44 offen offset:2048 nt lds\n\t" \
    "ds_read_b64_tr_b16 v[100:101], v244 offset:0\n\t" \
    "ds_read_b64_tr_b16 v[102:103], v244 offset:1536\n\t" \
    "ds_read_b64_tr_b16 v[108:109], v244 offset:32\n\t" \
    "ds_read_b64_tr_b16 v[110:111], v244 offset:1568\n\t" \
    "ds_read_b64_tr_b16 v[116:117], v244 offset:64\n\t" \
    "ds_read_b64_tr_b16 v[118:119], v244 offset:1600\n\t" \
    "v_add_u32 v244, 0x0, v243\n\t" \
    "s_waitcnt vmcnt(12) lgkmcnt(0)\n\t" \
    "s_barrier \n\t" \
    "v_mov_b32 v246, 0\n\t" \
    "buffer_load_dword v246, v246, s[60:63], %[mo] offen sc1\n\t" \
    "s_add_u32 m0, %[wm], 0xf000\n\t" \
    "s_mov_b32 s44, 0x4e0000\n\t" \
    "buffer_load_dwordx4 v240, %[wr], s44 offen offset:0 nt lds\n\t" \
    "buffer_load_dwordx4 v241, %[wr], s44 offen offset:1024 nt lds\n\t" \
    "buffer_load_dwordx4 v242, %[wr], s44 offen offset:2048 nt lds\n\t" \
    "ds_read_b64_tr_b16 v[200:201], v244 offset:0\n\t" \
    "ds_read_b64_tr_b16 v[202:203], v244 offset:1536\n\t" \
    "ds_read_b64_tr_b16 v[0:1], v244 offset:32\n\t" \
    "ds_read_b64_tr_b16 v[2:3], v244 offset:1568\n\t" \
    "ds_read_b64_tr_b16 v[8:9], v244 offset:64\n\t" \
    "ds_read_b64_tr_b16 v[10:11], v244 offset:1600\n\t" \
    "v_add_u32 v244, 0x3000, v243\n\t" \
    "s_waitcnt vmcnt(12) lgkmcnt(0)\n\t" \
    "s_barrier \n\t" \
    "s_add_u32 m0, %[wm], 0x0\n\t" \
    "s_mov_b32 s44, 0x540000\n\t" \
    "buffer_load_dwordx4 v240, %[wr], s44 offen offset:0 nt lds\n\t" \
    "buffer_load_dwordx4 v241, %[wr], s44 offen offset:1024 nt lds\n\t" \
    "buffer_load_dwordx4 v242, %[wr], s44 offen offset:2048 nt lds\n\t" \
    "ds_read_b64_tr_b16 v[204:205], v244 offset:0\n\t" \
    "ds_read_b64_tr_b16 v[206:207], v244 offset:1536\n\t" \
    "ds_read_b64_tr_b16 v[4:5], v244 offset:32\n\t" \
    "ds_read_b64_tr_b16 v[6:7], v244 offset:1568\n\t" \
    "ds_read_b64_tr_b16 v[12:13], v244 offset:64\n\t" \
    "ds_read_b64_tr_b16 v[14:15], v244 offset:1600\n\t" \
    "v_add_u32 v244, 0x6000, v243\n\t" \
    "s_waitcnt vmcnt(12) lgkmcnt(0)\n\t" \
    "s_barrier \n\t" \
    "s_add_u32 m0, %[wm], 0x3000\n\t" \
    "s_mov_b32 s44, 0x5a0000\n\t" \
    "buffer_load_dwordx4 v240, %[wr], s44 offen offset:0 nt lds\n\t" \
    "buffer_load_dwordx4 v241, %[wr], s44 offen offset:1024 nt lds\n\t" \
    "buffer_load_dwordx4 v242, %[wr], s44 offen offset:2048 nt lds\n\t" \
    "ds_read_b64_tr_b16 v[224:225], v244 offset:0\n\t" \
    "ds_read_b64_tr_b16 v[226:227], v244 offset:1536\n\t" \
    "ds_read_b64_tr_b16 v[120:121], v244 offset:32\n\t" \
    "ds_read_b64_tr_b16 v[122:123], v244 offset:1568\n\t" \
    "ds_read_b64_tr_b16 v[128:129], v244 offset:64\n\t" \
    "ds_read_b64_tr_b16 v[130:131], v244 offset:1600\n\t" \
    "v_add_u32 v244, 0x9000, v243\n\t" \
    "s_waitcnt vmcnt(12) lgkmcnt(0)\n\t" \
    "s_barrier \n\t" \
    "s_add_u32 m0, %[wm], 0x6000\n\t" \
    "s_mov_b32 s44, 0x600000\n\t" \
    "buffer_load_dwordx4 v240, %[wr], s44 offen offset:0 nt lds\n\t" \
    "buffer_load_dwordx4 v241, %[wr], s44 offen offset:1024 nt lds\n\t" \
    "buffer_load_dwordx4 v242, %[wr], s44 offen offset:2048 nt lds\n\t" \
    "ds_read_b64_tr_b16 v[228:229], v244 offset:0\n\t" \
    "ds_read_b64_tr_b16 v[230:231], v244 offset:1536\n\t" \
    "ds_read_b64_tr_b16 v[124:125], v244 offset:32\n\t" \
    "ds_read_b64_tr_b16 v[126:127], v244 offset:1568\n\t" \
    "ds_read_b64_tr_b16 v[132:133], v244 offset:64\n\t" \
    "ds_read_b64_tr_b16 v[134:135], v244 offset:1600\n\t" \
    "v_add_u32 v244, 0xc000, v243\n\t" \
    "s_waitcnt vmcnt(12) lgkmcnt(0)\n\t" \
    "s_barrier \n\t" \
    "s_add_u32 m0, %[wm], 0x9000\n\t" \
    "s_mov_b32 s44, 0x660000\n\t" \
    "buffer_load_dwordx4 v240, %[wr], s44 offen offset:0 nt lds\n\t" \
    "buffer_load_dwordx4 v241, %[wr], s44 offen offset:1024 nt lds\n\t" \
    "buffer_load_dwordx4 v242, %[wr], s44 offen offset:2048 nt lds\n\t" \
    "ds_read_b64_tr_b16 v[136:137], v244 offset:0\n\t" \
    "ds_read_b64_tr_b16 v[138:139], v244 offset:1536\n\t" \
    "ds_read_b64_tr_b16 v[144:145], v244 offset:32\n\t" \
    "ds_read_b64_tr_b16 v[146:147], v244 offset:1568\n\t" \
    "ds_read_b64_tr_b16 v[152:153], v244 offset:64\n\t" \
    "ds_read_b64_tr_b16 v[154:155], v244 offset:1600\n\t" \
    "v_add_u32 v244, 0xf000, v243\n\t" \
    "s_waitcnt vmcnt(12) lgkmcnt(0)\n\t" \
    "s_barrier \n\t" \
    ".Lv21_%=:\n\t" \
    "v_readfirstlane_b32 s43, v246\n\t" \
    "s_nop 0\n\t" \
    "s_cmp_lg_u32 s43, -1\n\t" \
    "s_cbranch_scc1 .Lv22_%=\n\t" \
    "s_cmp_eq_u32 s41, 0\n\t" \
    "s_cbranch_scc1 .Lv22_%=\n\t" \
    "s_sub_u32 s41, s41, 1\n\t" \
    "s_sleep 4\n\t" \
    "v_mov_b32 v246, 0\n\t" \
    "buffer_load_dword v246, v246, s[60:63], %[mo] offen sc1\n\t" \
    "s_waitcnt vmcnt(0)\n\t" \
    "s_branch .Lv21_%=\n\t" \
    ".Lv22_%=:\n\t" \
    "v_mov_b32 v246, 0x8000\n\t" \
    "s_mov_b64 exec, 1\n\t" \
    "buffer_store_dword v246, v246, s[60:63], %[mo] offen sc1\n\t" \
    "s_mov_b64 exec, -1\n\t" \
    "s_and_b32 s44, s43, 0x5000\n\t" \
    "s_xor_b32 %[mb], %[mb], s44\n\t" \
    "s_xor_b32 %[fl], %[fl], s44\n\t" \
    "s_and_b32 s43, s43, 0x2000\n\t" \
    "s_xor_b32 s43, s43, %[lx]\n\t" \
    "s_add_u32 s60, s60, s43\n\t" \
    "s_addc_u32 s61, s61, 0\n\t" \
    "v_and_b32 v252, 0xf0, v253\n\t" \
    "v_lshlrev_b32 v252, 3, v252\n\t" \
    "buffer_load_dword v252, v252, s[60:63], %[fl] offen sc1\n\t" \
    "s_add_u32 m0, %[wm], 0xc000\n\t" \
    "s_mov_b32 s44, 0x6c0000\n\t" \
    "buffer_load_dwordx4 v240, %[wr], s44 offen offset:0 nt lds\n\t" \
    "buffer_load_dwordx4 v241, %[wr], s44 offen offset:1024 nt lds\n\t" \
    "buffer_load_dwordx4 v242, %[wr], s44 offen offset:2048 nt lds\n\t" \
    "ds_read_b64_tr_b16 v[140:141], v244 offset:0\n\t" \
    "ds_read_b64_tr_b16 v[142:143], v244 offset:1536\n\t" \
    "ds_read_b64_tr_b16 v[148:149], v244 offset:32\n\t" \
    "ds_read_b64_tr_b16 v[150:151], v244 offset:1568\n\t" \
    "ds_read_b64_tr_b16 v[156:157], v244 offset:64\n\t" \
    "ds_read_b64_tr_b16 v[158:159], v244 offset:1600\n\t" \
    "v_add_u32 v244, 0x0, v243\n\t" \
    "s_waitcnt vmcnt(12) lgkmcnt(0)\n\t" \
    "s_barrier \n\t" \
    "s_add_u32 m0, %[wm], 0xf000\n\t" \
    "s_mov_b32 s44, 0x720000\n\t" \
    "buffer_load_dwordx4 v240, %[wr], s44 offen offset:0 nt lds\n\t" \
    "buffer_load_dwordx4 v241, %[wr], s44 offen offset:1024 nt lds\n\t" \
    "buffer_load_dwordx4 v242, %[wr], s44 offen offset:2048 nt lds\n\t" \
    "ds_read_b64_tr_b16 v[16:17], v244 offset:0\n\t" \
    "ds_read_b64_tr_b16 v[18:19], v244 offset:1536\n\t" \
    "ds_read_b64_tr_b16 v[24:25], v244 offset:32\n\t" \
    "ds_read_b64_tr_b16 v[26:27], v244 offset:1568\n\t" \
    "ds_read_b64_tr_b16 v[32:33], v244 offset:64\n\t" \
    "ds_read_b64_tr_b16 v[34:35], v244 offset:1600\n\t" \
    "v_add_u32 v244, 0x3000, v243\n\t" \
    "s_waitcnt vmcnt(12) lgkmcnt(0)\n\t" \
    "s_barrier \n\t" \
    "ds_read_b64_tr_b16 v[20:21], v244 offset:0\n\t" \
    "ds_read_b64_tr_b16 v[22:23], v244 offset:1536\n\t" \
    "ds_read_b64_tr_b16 v[28:29], v244 offset:32\n\t" \
    "ds_read_b64_tr_b16 v[30:31], v244 offset:1568\n\t" \
    "ds_read_b64_tr_b16 v[36:37], v244 offset:64\n\t" \
    "ds_read_b64_tr_b16 v[38:39], v244 offset:1600\n\t" \
    "v_add_u32 v244, 0x6000, v243\n\t" \
    "s_waitcnt vmcnt(9) lgkmcnt(0)\n\t" \
    "s_barrier \n\t" \
    "ds_read_b64_tr_b16 v[232:233], v244 offset:0\n\t" \
    "ds_read_b64_tr_b16 v[234:235], v244 offset:1536\n\t" \
    "ds_read_b64_tr_b16 v[160:161], v244 offset:32\n\t" \
    "ds_read_b64_tr_b16 v[162:163], v244 offset:1568\n\t" \
    "ds_read_b64_tr_b16 v[168:169], v244 offset:64\n\t" \
    "ds_read_b64_tr_b16 v[170:171], v244 offset:1600\n\t" \
    "v_add_u32 v244, 0x9000, v243\n\t" \
    "s_waitcnt vmcnt(6) lgkmcnt(0)\n\t" \
    "s_barrier \n\t" \
    "ds_read_b64_tr_b16 v[236:237], v244 offset:0\n\t" \
    "ds_read_b64_tr_b16 v[238:239], v244 offset:1536\n\t" \
    "ds_read_b64_tr_b16 v[164:165], v244 offset:32\n\t" \
    "ds_read_b64_tr_b16 v[166:167], v244 offset:1568\n\t" \
    "ds_read_b64_tr_b16 v[172:173], v244 offset:64\n\t" \
    "ds_read_b64_tr_b16 v[174:175], v244 offset:1600\n\t" \
    "v_add_u32 v244, 0xc000, v243\n\t" \
    "s_waitcnt vmcnt(3) lgkmcnt(0)\n\t" \
    "s_barrier \n\t" \
    "ds_read_b64_tr_b16 v[176:177], v244 offset:0\n\t" \
    "ds_read_b64_tr_b16 v[178:179], v244 offset:1536\n\t" \
    "ds_read_b64_tr_b16 v[184:185], v244 offset:32\n\t" \
    "ds_read_b64_tr_b16 v[186:187], v244 offset:1568\n\t" \
    "ds_read_b64_tr_b16 v[192:193], v244 offset:64\n\t" \
    "ds_read_b64_tr_b16 v[194:195], v244 offset:1600\n\t" \
    "s_mov_b32 s43, 0\n\t" \
    "v_cmp_eq_u32 vcc, 0, v252\n\t" \
    "s_cbranch_vccnz .Lv23_%=\n\t" \
    "s_mov_b32 m0, 0x2000\n\t" \
    "s_add_u32 s49, s42, 0x0\n\t" \
    "buffer_load_dwordx4 v253, s[60:63], s49 offen offset:0 sc1 lds\n\t" \
    "buffer_load_dwordx4 v253, s[60:63], s49 offen offset:1024 sc1 lds\n\t" \
    "buffer_load_dwordx4 v253, s[60:63], s49 offen offset:2048 sc1 lds\n\t" \
    "buffer_load_dwordx4 v253, s[60:63], s49 offen offset:3072 sc1 lds\n\t" \
    "s_mov_b32 m0, 0x4800\n\t" \
    "s_add_u32 s49, s42, 0x8800\n\t" \
    "buffer_load_dwordx4 v253, s[60:63], s49 offen offset:0 sc1 lds\n\t" \
    "s_mov_b32 m0, 0x7000\n\t" \
    "s_add_u32 s49, s42, 0x1000\n\t" \
    "buffer_load_dwordx4 v253, s[60:63], s49 offen offset:0 sc1 lds\n\t" \
    "buffer_load_dwordx4 v253, s[60:63], s49 offen offset:1024 sc1 lds\n\t" \
    "buffer_load_dwordx4 v253, s[60:63], s49 offen offset:2048 sc1 lds\n\t" \
    "buffer_load_dwordx4 v253, s[60:63], s49 offen offset:3072 sc1 lds\n\t" \
    "s_mov_b32 m0, 0x9800\n\t" \
    "s_add_u32 s49, s42, 0x9800\n\t" \
    "buffer_load_dwordx4 v253, s[60:63], s49 offen offset:0 sc1 lds\n\t" \
    "s_mov_b32 s43, 1\n\t" \
    ".Lv23_%=:\n\t" \
    "v_add_u32 v244, 0xf000, v243\n\t" \
    "s_waitcnt vmcnt(0) lgkmcnt(0)\n\t" \
    "s_barrier \n\t" \
    "ds_read_b64_tr_b16 v[180:181], v244 offset:0\n\t" \
    "ds_read_b64_tr_b16 v[182:183], v244 offset:1536\n\t" \
    "ds_read_b64_tr_b16 v[188:189], v244 offset:32\n\t" \
    "ds_read_b64_tr_b16 v[190:191], v244 offset:1568\n\t" \
    "ds_read_b64_tr_b16 v[196:197], v244 offset:64\n\t" \
    "ds_read_b64_tr_b16 v[198:199], v244 offset:1600\n\t" \
    "s_waitcnt lgkmcnt(0)\n\t" \
    "s_barrier \n\t" \
    "ds_write_b128 v254, v[200:203] offset:51200\n\t" \
    "ds_write_b128 v254, v[204:207] offset:52224\n\t" \
    "ds_write_b128 v254, v[208:211] offset:53248\n\t" \
    "ds_write_b128 v254, v[212:215] offset:54272\n\t" \
    "ds_write_b128 v254, v[216:219] offset:55296\n\t" \
    "ds_write_b128 v254, v[220:223] offset:56320\n\t" \
    "ds_write_b128 v254, v[224:227] offset:57344\n\t" \
    "ds_write_b128 v254, v[228:231] offset:58368\n\t" \
    "ds_write_b128 v254, v[232:235] offset:59392\n\t" \
    "ds_write_b128 v254, v[236:239] offset:60416\n\t" \
    "s_cmp_lg_u32 s43, 0\n\t" \
    "s_cbranch_scc1 .Lv24_%=\n\t" \
    "s_mov_b32 s44, 0\n\t" \
    ".Lv25_%=:\n\t" \
    "v_cmp_ge_u32 vcc, s44, v252\n\t" \
    "s_cbranch_vccz .Lv26_%=\n\t" \
    "s_cmp_eq_u32 s41, 0\n\t" \
    "s_cbranch_scc1 .Lv26_%=\n\t" \
    "s_sub_u32 s41, s41, 1\n\t" \
    "s_sleep 8\n\t" \
    "v_and_b32 v252, 0xf0, v253\n\t" \
    "v_lshlrev_b32 v252, 3, v252\n\t" \
    "buffer_load_dword v252, v252, s[60:63], %[fl] offen sc1\n\t" \
    "s_waitcnt vmcnt(0)\n\t" \
    "s_branch .Lv25_%=\n\t" \
    ".Lv26_%=:\n\t" \
    "s_mov_b32 m0, 0x2000\n\t" \
    "s_add_u32 s49, s42, 0x0\n\t" \
    "buffer_load_dwordx4 v253, s[60:63], s49 offen offset:0 sc1 lds\n\t" \
    "buffer_load_dwordx4 v253, s[60:63], s49 offen offset:1024 sc1 lds\n\t" \
    "buffer_load_dwordx4 v253, s[60:63], s49 offen offset:2048 sc1 lds\n\t" \
    "buffer_load_dwordx4 v253, s[60:63], s49 offen offset:3072 sc1 lds\n\t" \
    "s_mov_b32 m0, 0x4800\n\t" \
    "s_add_u32 s49, s42, 0x8800\n\t" \
    "buffer_load_dwordx4 v253, s[60:63], s49 offen offset:0 sc1 lds\n\t" \
    "s_mov_b32 m0, 0x7000\n\t" \
    "s_add_u32 s49, s42, 0x1000\n\t" \
    "buffer_load_dwordx4 v253, s[60:63], s49 offen offset:0 sc1 lds\n\t" \
    "buffer_load_dwordx4 v253, s[60:63], s49 offen offset:1024 sc1 lds\n\t" \
    "buffer_load_dwordx4 v253, s[60:63], s49 offen offset:2048 sc1 lds\n\t" \
    "buffer_load_dwordx4 v253, s[60:63], s49 offen offset:3072 sc1 lds\n\t" \
    "s_mov_b32 m0, 0x9800\n\t" \
    "s_add_u32 s49, s42, 0x9800\n\t" \
    "buffer_load_dwordx4 v253, s[60:63], s49 offen offset:0 sc1 lds\n\t" \
    ".Lv24_%=:\n\t" \
    "s_waitcnt vmcnt(0) lgkmcnt(0)\n\t" \
    "s_barrier \n\t" \
    "ds_read_b128 v[224:227], v253 offset:8192\n\t" \
    "ds_read_b128 v[244:247], v254 offset:51200\n\t" \
    "ds_read_b128 v[228:231], v253 offset:9216\n\t" \
    "ds_read_b128 v[232:235], v253 offset:28672\n\t" \
    "ds_read_b128 v[236:239], v253 offset:29696\n\t" \
    "ds_read_b128 v[248:251], v254 offset:52224\n\t" \
    ".Lv27_%=:\n\t" \
    "s_waitcnt lgkmcnt(5)\n\t" \
    "v_mfma_f32_16x16x32_bf16 v[204:207], v[0:3], v[224:227], 0\n\t" \
    "s_waitcnt lgkmcnt(3)\n\t" \
    "v_mfma_f32_16x16x32_bf16 v[204:207], v[4:7], v[228:231], v[204:207]\n\t" \
    "ds_read_b128 v[240:243], v253 offset:14336\n\t" \
    "v_mfma_f32_16x16x32_bf16 v[208:211], v[8:11], v[224:227], 0\n\t" \
    "v_mfma_f32_16x16x32_bf16 v[208:211], v[12:15], v[228:231], v[208:211]\n\t" \
    "v_mfma_f32_16x16x32_bf16 v[200:203], v[244:247], v[224:227], 0\n\t" \
    "s_waitcnt lgkmcnt(1)\n\t" \
    "v_mfma_f32_16x16x32_bf16 v[200:203], v[248:251], v[228:231], v[200:203]\n\t" \
    "v_mfma_f32_16x16x32_bf16 v[216:219], v[0:3], v[232:235], 0\n\t" \
    "ds_read_b128 v[224:227], v253 offset:15360\n\t" \
    "v_mfma_f32_16x16x32_bf16 v[216:219], v[4:7], v[236:239], v[216:219]\n\t" \
    "ds_read_b128 v[228:231], v253 offset:0\n\t" \
    "v_mfma_f32_16x16x32_bf16 v[220:223], v[8:11], v[232:235], 0\n\t" \
    "v_mfma_f32_16x16x32_bf16 v[220:223], v[12:15], v[236:239], v[220:223]\n\t" \
    "v_mfma_f32_16x16x32_bf16 v[212:215], v[244:247], v[232:235], 0\n\t" \
    "ds_read_b128 v[244:247], v254 offset:53248\n\t" \
    "v_mfma_f32_16x16x32_bf16 v[212:215], v[248:251], v[236:239], v[212:215]\n\t" \
    "ds_read_b128 v[248:251], v254 offset:54272\n\t" \
    "s_waitcnt lgkmcnt(4)\n\t" \
    "v_mfma_f32_16x16x32_bf16 v[204:207], v[24:27], v[240:243], v[204:207]\n\t" \
    "ds_read_b128 v[232:235], v253 offset:1024\n\t" \
    "s_waitcnt lgkmcnt(4)\n\t" \
    "v_mfma_f32_16x16x32_bf16 v[204:207], v[28:31], v[224:227], v[204:207]\n\t" \
    "ds_read_b128 v[236:239], v253 offset:20480\n\t" \
    "v_mfma_f32_16x16x32_bf16 v[208:211], v[32:35], v[240:243], v[208:211]\n\t" \
    "v_mfma_f32_16x16x32_bf16 v[208:211], v[36:39], v[224:227], v[208:211]\n\t" \
    "v_mfma_f32_16x16x32_bf16 v[200:203], v[16:19], v[240:243], v[200:203]\n\t" \
    "v_mfma_f32_16x16x32_bf16 v[200:203], v[20:23], v[224:227], v[200:203]\n\t" \
    "s_waitcnt lgkmcnt(4)\n\t" \
    "v_mfma_f32_16x16x32_bf16 v[204:207], v[40:43], v[228:231], v[204:207]\n\t" \
    "ds_read_b128 v[240:243], v253 offset:21504\n\t" \
    "s_waitcnt lgkmcnt(2)\n\t" \
    "v_mfma_f32_16x16x32_bf16 v[204:207], v[44:47], v[232:235], v[204:207]\n\t" \
    "ds_read_b128 v[224:227], v253 offset:2048\n\t" \
    "v_mfma_f32_16x16x32_bf16 v[208:211], v[48:51], v[228:231], v[208:211]\n\t" \
    "v_mfma_f32_16x16x32_bf16 v[208:211], v[52:55], v[232:235], v[208:211]\n\t" \
    "v_mfma_f32_16x16x32_bf16 v[200:203], v[244:247], v[228:231], v[200:203]\n\t" \
    "v_mfma_f32_16x16x32_bf16 v[200:203], v[248:251], v[232:235], v[200:203]\n\t" \
    "s_waitcnt lgkmcnt(2)\n\t" \
    "v_mfma_f32_16x16x32_bf16 v[216:219], v[40:43], v[236:239], v[216:219]\n\t" \
    "ds_read_b128 v[228:231], v253 offset:3072\n\t" \
    "s_add_u32 s44, s40, 1\n\t" \
    "s_waitcnt lgkmcnt(2)\n\t" \
    "v_mfma_f32_16x16x32_bf16 v[216:219], v[44:47], v[240:243], v[216:219]\n\t" \
    "ds_read_b128 v[232:235], v253 offset:4096\n\t" \
    "s_and_b32 s45, s44, 15\n\t" \
    "v_mfma_f32_16x16x32_bf16 v[220:223], v[48:51], v[236:239], v[220:223]\n\t" \
    "s_cmp_eq_u32 s45, 0\n\t" \
    "v_mfma_f32_16x16x32_bf16 v[220:223], v[52:55], v[240:243], v[220:223]\n\t" \
    "s_cselect_b32 s45, %[cj], %[cs]\n\t" \
    "v_mfma_f32_16x16x32_bf16 v[212:215], v[244:247], v[236:239], v[212:215]\n\t" \
    "ds_read_b128 v[244:247], v254 offset:55296\n\t" \
    "s_cmp_eq_u32 s44, 64\n\t" \
    "v_mfma_f32_16x16x32_bf16 v[212:215], v[248:251], v[240:243], v[212:215]\n\t" \
    "ds_read_b128 v[248:251], v254 offset:56320\n\t" \
    "s_cselect_b32 s45, 0, s45\n\t" \
    "s_waitcnt lgkmcnt(4)\n\t" \
    "v_mfma_f32_16x16x32_bf16 v[204:207], v[64:67], v[224:227], v[204:207]\n\t" \
    "ds_read_b128 v[236:239], v253 offset:5120\n\t" \
    "s_add_u32 s42, s42, s45\n\t" \
    "s_waitcnt lgkmcnt(4)\n\t" \
    "v_mfma_f32_16x16x32_bf16 v[204:207], v[68:71], v[228:231], v[204:207]\n\t" \
    "ds_read_b128 v[240:243], v253 offset:24576\n\t" \
    "v_mfma_f32_16x16x32_bf16 v[208:211], v[72:75], v[224:227], v[208:211]\n\t" \
    "v_mfma_f32_16x16x32_bf16 v[208:211], v[76:79], v[228:231], v[208:211]\n\t" \
    "v_mfma_f32_16x16x32_bf16 v[200:203], v[56:59], v[224:227], v[200:203]\n\t" \
    "v_mfma_f32_16x16x32_bf16 v[200:203], v[60:63], v[228:231], v[200:203]\n\t" \
    "s_waitcnt lgkmcnt(4)\n\t" \
    "v_mfma_f32_16x16x32_bf16 v[204:207], v[80:83], v[232:235], v[204:207]\n\t" \
    "ds_read_b128 v[224:227], v253 offset:25600\n\t" \
    "s_waitcnt lgkmcnt(2)\n\t" \
    "v_mfma_f32_16x16x32_bf16 v[204:207], v[84:87], v[236:239], v[204:207]\n\t" \
    "ds_read_b128 v[228:231], v253 offset:6144\n\t" \
    "v_mfma_f32_16x16x32_bf16 v[208:211], v[88:91], v[232:235], v[208:211]\n\t" \
    "v_mfma_f32_16x16x32_bf16 v[208:211], v[92:95], v[236:239], v[208:211]\n\t" \
    "v_mfma_f32_16x16x32_bf16 v[200:203], v[244:247], v[232:235], v[200:203]\n\t" \
    "v_mfma_f32_16x16x32_bf16 v[200:203], v[248:251], v[236:239], v[200:203]\n\t" \
    "s_waitcnt lgkmcnt(2)\n\t" \
    "v_mfma_f32_16x16x32_bf16 v[216:219], v[80:83], v[240:243], v[216:219]\n\t" \
    "ds_read_b128 v[232:235], v253 offset:7168\n\t" \
    "s_and_b32 s44, s40, 3\n\t" \
    "s_waitcnt lgkmcnt(2)\n\t" \
    "v_mfma_f32_16x16x32_bf16 v[216:219], v[84:87], v[224:227], v[216:219]\n\t" \
    "ds_read_b128 v[236:239], v253 offset:10240\n\t" \
    "s_lshl_b32 s44, s44, 22\n\t" \
    "v_mfma_f32_16x16x32_bf16 v[220:223], v[88:91], v[240:243], v[220:223]\n\t" \
    "s_add_u32 s44, s44, %[mb]\n\t" \
    "v_mfma_f32_16x16x32_bf16 v[220:223], v[92:95], v[224:227], v[220:223]\n\t" \
    "s_add_u32 s46, s44, 0x8000\n\t" \
    "v_mfma_f32_16x16x32_bf16 v[212:215], v[244:247], v[240:243], v[212:215]\n\t" \
    "ds_read_b128 v[244:247], v254 offset:57344\n\t" \
    "s_add_u32 s47, s44, 0x8c00\n\t" \
    "v_mfma_f32_16x16x32_bf16 v[212:215], v[248:251], v[224:227], v[212:215]\n\t" \
    "ds_read_b128 v[248:251], v254 offset:58368\n\t" \
    "s_waitcnt lgkmcnt(4)\n\t" \
    "v_mfma_f32_16x16x32_bf16 v[204:207], v[104:107], v[228:231], v[204:207]\n\t" \
    "ds_read_b128 v[240:243], v253 offset:11264\n\t" \
    "s_waitcnt lgkmcnt(4)\n\t" \
    "v_mfma_f32_16x16x32_bf16 v[204:207], v[108:111], v[232:235], v[204:207]\n\t" \
    "ds_read_b128 v[224:227], v253 offset:30720\n\t" \
    "v_mfma_f32_16x16x32_bf16 v[208:211], v[112:115], v[228:231], v[208:211]\n\t" \
    "v_mfma_f32_16x16x32_bf16 v[208:211], v[116:119], v[232:235], v[208:211]\n\t" \
    "v_mfma_f32_16x16x32_bf16 v[200:203], v[96:99], v[228:231], v[200:203]\n\t" \
    "v_mfma_f32_16x16x32_bf16 v[200:203], v[100:103], v[232:235], v[200:203]\n\t" \
    "s_waitcnt lgkmcnt(4)\n\t" \
    "v_mfma_f32_16x16x32_bf16 v[204:207], v[120:123], v[236:239], v[204:207]\n\t" \
    "ds_read_b128 v[228:231], v253 offset:31744\n\t" \
    "s_waitcnt lgkmcnt(2)\n\t" \
    "v_mfma_f32_16x16x32_bf16 v[204:207], v[124:127], v[240:243], v[204:207]\n\t" \
    "ds_read_b128 v[232:235], v253 offset:12288\n\t" \
    "v_mfma_f32_16x16x32_bf16 v[208:211], v[128:131], v[236:239], v[208:211]\n\t" \
    "v_mfma_f32_16x16x32_bf16 v[208:211], v[132:135], v[240:243], v[208:211]\n\t" \
    "v_mfma_f32_16x16x32_bf16 v[200:203], v[244:247], v[236:239], v[200:203]\n\t" \
    "v_mfma_f32_16x16x32_bf16 v[200:203], v[248:251], v[240:243], v[200:203]\n\t" \
    "s_waitcnt lgkmcnt(2)\n\t" \
    "v_mfma_f32_16x16x32_bf16 v[216:219], v[120:123], v[224:227], v[216:219]\n\t" \
    "ds_read_b128 v[236:239], v253 offset:13312\n\t" \
    "s_waitcnt lgkmcnt(2)\n\t" \
    "v_mfma_f32_16x16x32_bf16 v[216:219], v[124:127], v[228:231], v[216:219]\n\t" \
    "ds_read_b128 v[240:243], v253 offset:16384\n\t" \
    "v_mfma_f32_16x16x32_bf16 v[220:223], v[128:131], v[224:227], v[220:223]\n\t" \
    "v_mfma_f32_16x16x32_bf16 v[220:223], v[132:135], v[228:231], v[220:223]\n\t" \
    "v_mfma_f32_16x16x32_bf16 v[212:215], v[244:247], v[224:227], v[212:215]\n\t" \
    "ds_read_b128 v[244:247], v254 offset:59392\n\t" \
    "v_mfma_f32_16x16x32_bf16 v[212:215], v[248:251], v[228:231], v[212:215]\n\t" \
    "ds_read_b128 v[248:251], v254 offset:60416\n\t" \
    "v_and_b32 v252, 0xf0, v253\n\t" \
    "v_lshlrev_b32 v252, 3, v252\n\t" \
    "buffer_load_dword v252, v252, s[60:63], %[fl] offen sc1\n\t" \
    "s_waitcnt lgkmcnt(4)\n\t" \
    "v_mfma_f32_16x16x32_bf16 v[204:207], v[144:147], v[232:235], v[204:207]\n\t" \
    "ds_read_b128 v[224:227], v253 offset:17408\n\t" \
    "s_waitcnt lgkmcnt(4)\n\t" \
    "v_mfma_f32_16x16x32_bf16 v[204:207], v[148:151], v[236:239], v[204:207]\n\t" \
    "ds_read_b128 v[228:231], v253 offset:36864\n\t" \
    "v_mfma_f32_16x16x32_bf16 v[208:211], v[152:155], v[232:235], v[208:211]\n\t" \
    "v_mfma_f32_16x16x32_bf16 v[208:211], v[156:159], v[236:239], v[208:211]\n\t" \
    "v_mfma_f32_16x16x32_bf16 v[200:203], v[136:139], v[232:235], v[200:203]\n\t" \
    "v_mfma_f32_16x16x32_bf16 v[200:203], v[140:143], v[236:239], v[200:203]\n\t" \
    "s_waitcnt lgkmcnt(4)\n\t" \
    "v_mfma_f32_16x16x32_bf16 v[204:207], v[160:163], v[240:243], v[204:207]\n\t" \
    "ds_read_b128 v[232:235], v253 offset:37888\n\t" \
    "s_waitcnt lgkmcnt(2)\n\t" \
    "v_mfma_f32_16x16x32_bf16 v[204:207], v[164:167], v[224:227], v[204:207]\n\t" \
    "ds_read_b128 v[236:239], v253 offset:18432\n\t" \
    "v_mfma_f32_16x16x32_bf16 v[208:211], v[168:171], v[240:243], v[208:211]\n\t" \
    "v_mfma_f32_16x16x32_bf16 v[208:211], v[172:175], v[224:227], v[208:211]\n\t" \
    "v_mfma_f32_16x16x32_bf16 v[200:203], v[244:247], v[240:243], v[200:203]\n\t" \
    "v_mfma_f32_16x16x32_bf16 v[200:203], v[248:251], v[224:227], v[200:203]\n\t" \
    "s_waitcnt lgkmcnt(2)\n\t" \
    "v_mfma_f32_16x16x32_bf16 v[216:219], v[160:163], v[228:231], v[216:219]\n\t" \
    "ds_read_b128 v[240:243], v253 offset:19456\n\t" \
    "s_waitcnt lgkmcnt(2)\n\t" \
    "v_mfma_f32_16x16x32_bf16 v[216:219], v[164:167], v[232:235], v[216:219]\n\t" \
    "ds_read_b128 v[224:227], v253 offset:34816\n\t" \
    "v_mfma_f32_16x16x32_bf16 v[220:223], v[168:171], v[228:231], v[220:223]\n\t" \
    "v_mfma_f32_16x16x32_bf16 v[220:223], v[172:175], v[232:235], v[220:223]\n\t" \
    "v_mfma_f32_16x16x32_bf16 v[212:215], v[244:247], v[228:231], v[212:215]\n\t" \
    "v_mfma_f32_16x16x32_bf16 v[212:215], v[248:251], v[232:235], v[212:215]\n\t" \
    "s_waitcnt lgkmcnt(2)\n\t" \
    "v_mfma_f32_16x16x32_bf16 v[204:207], v[184:187], v[236:239], v[204:207]\n\t" \
    "ds_read_b128 v[228:231], v253 offset:35840\n\t" \
    "s_waitcnt lgkmcnt(2)\n\t" \
    "v_mfma_f32_16x16x32_bf16 v[204:207], v[188:191], v[240:243], v[204:207]\n\t" \
    "ds_read_b128 v[232:235], v253 offset:22528\n\t" \
    "v_mfma_f32_16x16x32_bf16 v[208:211], v[192:195], v[236:239], v[208:211]\n\t" \
    "v_mfma_f32_16x16x32_bf16 v[208:211], v[196:199], v[240:243], v[208:211]\n\t" \
    "v_mfma_f32_16x16x32_bf16 v[200:203], v[176:179], v[236:239], v[200:203]\n\t" \
    "v_mfma_f32_16x16x32_bf16 v[200:203], v[180:183], v[240:243], v[200:203]\n\t" \
    "s_waitcnt lgkmcnt(2)\n\t" \
    "v_mfma_f32_16x16x32_bf16 v[216:219], v[24:27], v[224:227], v[216:219]\n\t" \
    "ds_read_b128 v[236:239], v253 offset:23552\n\t" \
    "s_waitcnt lgkmcnt(2)\n\t" \
    "v_mfma_f32_16x16x32_bf16 v[216:219], v[28:31], v[228:231], v[216:219]\n\t" \
    "ds_read_b128 v[240:243], v253 offset:26624\n\t" \
    "v_mfma_f32_16x16x32_bf16 v[220:223], v[32:35], v[224:227], v[220:223]\n\t" \
    "v_cvt_pk_bf16_f32 v204, v204, v205\n\t" \
    "v_mfma_f32_16x16x32_bf16 v[220:223], v[36:39], v[228:231], v[220:223]\n\t" \
    "v_cvt_pk_bf16_f32 v205, v206, v207\n\t" \
    "v_mfma_f32_16x16x32_bf16 v[212:215], v[16:19], v[224:227], v[212:215]\n\t" \
    "ds_write_b64 v254, v[204:205] offset:7176\n\t" \
    "v_mfma_f32_16x16x32_bf16 v[212:215], v[20:23], v[228:231], v[212:215]\n\t" \
    "v_cvt_pk_bf16_f32 v208, v208, v209\n\t" \
    "s_waitcnt lgkmcnt(3)\n\t" \
    "v_mfma_f32_16x16x32_bf16 v[216:219], v[64:67], v[232:235], v[216:219]\n\t" \
    "ds_read_b128 v[224:227], v253 offset:27648\n\t" \
    "v_cvt_pk_bf16_f32 v209, v210, v211\n\t" \
    "s_waitcnt lgkmcnt(3)\n\t" \
    "v_mfma_f32_16x16x32_bf16 v[216:219], v[68:71], v[236:239], v[216:219]\n\t" \
    "ds_read_b128 v[228:231], v253 offset:32768\n\t" \
    "ds_write_b64 v254, v[208:209] offset:8200\n\t" \
    "v_mfma_f32_16x16x32_bf16 v[220:223], v[72:75], v[232:235], v[220:223]\n\t" \
    "v_cvt_pk_bf16_f32 v200, v200, v201\n\t" \
    "v_mfma_f32_16x16x32_bf16 v[220:223], v[76:79], v[236:239], v[220:223]\n\t" \
    "v_cvt_pk_bf16_f32 v201, v202, v203\n\t" \
    "v_mfma_f32_16x16x32_bf16 v[212:215], v[56:59], v[232:235], v[212:215]\n\t" \
    "ds_write_b64 v254, v[200:201] offset:6152\n\t" \
    "v_mfma_f32_16x16x32_bf16 v[212:215], v[60:63], v[236:239], v[212:215]\n\t" \
    "s_waitcnt lgkmcnt(5)\n\t" \
    "v_mfma_f32_16x16x32_bf16 v[216:219], v[104:107], v[240:243], v[216:219]\n\t" \
    "ds_read_b128 v[232:235], v253 offset:33792\n\t" \
    "s_waitcnt lgkmcnt(4)\n\t" \
    "v_mfma_f32_16x16x32_bf16 v[216:219], v[108:111], v[224:227], v[216:219]\n\t" \
    "ds_read_b128 v[236:239], v253 offset:38912\n\t" \
    "v_mfma_f32_16x16x32_bf16 v[220:223], v[112:115], v[240:243], v[220:223]\n\t" \
    "v_mfma_f32_16x16x32_bf16 v[220:223], v[116:119], v[224:227], v[220:223]\n\t" \
    "v_mfma_f32_16x16x32_bf16 v[212:215], v[96:99], v[240:243], v[212:215]\n\t" \
    "v_mfma_f32_16x16x32_bf16 v[212:215], v[100:103], v[224:227], v[212:215]\n\t" \
    "ds_read_b128 v[240:243], v253 offset:39936\n\t" \
    "s_waitcnt lgkmcnt(3)\n\t" \
    "s_barrier \n\t" \
    "v_mfma_f32_16x16x32_bf16 v[216:219], v[144:147], v[228:231], v[216:219]\n\t" \
    "s_waitcnt lgkmcnt(2)\n\t" \
    "v_mfma_f32_16x16x32_bf16 v[216:219], v[148:151], v[232:235], v[216:219]\n\t" \
    "s_waitcnt lgkmcnt(1)\n\t" \
    "v_mfma_f32_16x16x32_bf16 v[216:219], v[184:187], v[236:239], v[216:219]\n\t" \
    "s_waitcnt lgkmcnt(0)\n\t" \
    "v_mfma_f32_16x16x32_bf16 v[216:219], v[188:191], v[240:243], v[216:219]\n\t" \
    "v_mfma_f32_16x16x32_bf16 v[220:223], v[152:155], v[228:231], v[220:223]\n\t" \
    "v_mfma_f32_16x16x32_bf16 v[220:223], v[156:159], v[232:235], v[220:223]\n\t" \
    "v_mfma_f32_16x16x32_bf16 v[220:223], v[192:195], v[236:239], v[220:223]\n\t" \
    "v_cvt_pk_bf16_f32 v216, v216, v217\n\t" \
    "v_cvt_pk_bf16_f32 v217, v218, v219\n\t" \
    "v_mfma_f32_16x16x32_bf16 v[220:223], v[196:199], v[240:243], v[220:223]\n\t" \
    "ds_write_b64 v254, v[216:217] offset:13320\n\t" \
    "v_mfma_f32_16x16x32_bf16 v[212:215], v[136:139], v[228:231], v[212:215]\n\t" \
    "v_mfma_f32_16x16x32_bf16 v[212:215], v[140:143], v[232:235], v[212:215]\n\t" \
    "v_mfma_f32_16x16x32_bf16 v[212:215], v[176:179], v[236:239], v[212:215]\n\t" \
    "v_cvt_pk_bf16_f32 v220, v220, v221\n\t" \
    "v_cvt_pk_bf16_f32 v221, v222, v223\n\t" \
    "v_mfma_f32_16x16x32_bf16 v[212:215], v[180:183], v[240:243], v[212:215]\n\t" \
    "ds_write_b64 v254, v[220:221] offset:14344\n\t" \
    "s_nop 6\n\t" \
    "v_cvt_pk_bf16_f32 v212, v212, v213\n\t" \
    "v_cvt_pk_bf16_f32 v213, v214, v215\n\t" \
    "ds_write_b64 v254, v[212:213] offset:12296\n\t" \
    "s_waitcnt vmcnt(0) lgkmcnt(0)\n\t" \
    "s_barrier \n\t" \
    "s_setprio 2\n\t" \
    "s_add_u32 s44, s40, 1\n\t" \
    "s_min_u32 s44, s44, 63\n\t" \
    ".Lv28_%=:\n\t" \
    "v_cmp_ge_u32 vcc, s44, v252\n\t" \
    "s_cbranch_vccz .Lv29_%=\n\t" \
    "s_cmp_eq_u32 s41, 0\n\t" \
    "s_cbranch_scc1 .Lv29_%=\n\t" \
    "s_sub_u32 s41, s41, 1\n\t" \
    "s_sleep 2\n\t" \
    "v_and_b32 v252, 0xf0, v253\n\t" \
    "v_lshlrev_b32 v252, 3, v252\n\t" \
    "buffer_load_dword v252, v252, s[60:63], %[fl] offen sc1\n\t" \
    "s_waitcnt vmcnt(0)\n\t" \
    "s_branch .Lv28_%=\n\t" \
    ".Lv29_%=:\n\t" \
    "s_mov_b32 m0, 0x2000\n\t" \
    "s_add_u32 s49, s42, 0x0\n\t" \
    "buffer_load_dwordx4 v253, s[60:63], s49 offen offset:0 sc1 lds\n\t" \
    "s_sleep 1\n\t" \
    "buffer_load_dwordx4 v253, s[60:63], s49 offen offset:1024 sc1 lds\n\t" \
    "s_sleep 1\n\t" \
    "buffer_load_dwordx4 v253, s[60:63], s49 offen offset:2048 sc1 lds\n\t" \
    "s_sleep 1\n\t" \
    "buffer_load_dwordx4 v253, s[60:63], s49 offen offset:3072 sc1 lds\n\t" \
    "s_sleep 1\n\t" \
    "s_mov_b32 m0, 0x4800\n\t" \
    "s_add_u32 s49, s42, 0x8800\n\t" \
    "buffer_load_dwordx4 v253, s[60:63], s49 offen offset:0 sc1 lds\n\t" \
    "s_sleep 1\n\t" \
    "s_mov_b32 m0, 0x7000\n\t" \
    "s_add_u32 s49, s42, 0x1000\n\t" \
    "buffer_load_dwordx4 v253, s[60:63], s49 offen offset:0 sc1 lds\n\t" \
    "s_sleep 1\n\t" \
    "buffer_load_dwordx4 v253, s[60:63], s49 offen offset:1024 sc1 lds\n\t" \
    "s_sleep 1\n\t" \
    "buffer_load_dwordx4 v253, s[60:63], s49 offen offset:2048 sc1 lds\n\t" \
    "s_sleep 1\n\t" \
    "buffer_load_dwordx4 v253, s[60:63], s49 offen offset:3072 sc1 lds\n\t" \
    "s_sleep 1\n\t" \
    "s_mov_b32 m0, 0x9800\n\t" \
    "s_add_u32 s49, s42, 0x9800\n\t" \
    "buffer_load_dwordx4 v253, s[60:63], s49 offen offset:0 sc1 lds\n\t" \
    "s_sleep 1\n\t" \
    "ds_read_b128 v[200:203], v254 offset:51200\n\t" \
    "ds_read_b128 v[204:207], v254 offset:52224\n\t" \
    "ds_read_b128 v[216:219], v254 offset:53248\n\t" \
    "ds_read_b128 v[220:223], v254 offset:54272\n\t" \
    "v_mov_b64 v[208:209], 0\n\t" \
    "v_mov_b64 v[210:211], 0\n\t" \
    "v_mov_b64 v[244:245], 0\n\t" \
    "v_mov_b64 v[246:247], 0\n\t" \
    "v_mov_b64 v[248:249], 0\n\t" \
    "v_mov_b64 v[250:251], 0\n\t" \
    "s_sleep 5\n\t" \
    "s_mov_b32 m0, 0xb000\n\t" \
    "s_nop 0\n\t" \
    "buffer_load_dwordx4 v253, s[60:63], s46 offen sc1 lds\n\t" \
    "s_mov_b32 m0, 0xbc00\n\t" \
    "s_nop 0\n\t" \
    "buffer_load_dwordx4 v253, s[60:63], s47 offen sc1 lds\n\t" \
    "s_waitcnt lgkmcnt(0)\n\t" \
    "v_bfe_i32 v212, v253, 5, 1\n\t" \
    "v_and_b32 v212, 0xaaaa, v212\n\t" \
    "v_or_b32 v212, 0x4444, v212\n\t" \
    "v_and_b32 v213, 0xffff, v255\n\t" \
    "v_lshrrev_b32 v214, 16, v255\n\t" \
    ".Lv210_%=:\n\t" \
    "s_waitcnt vmcnt(0)\n\t" \
    "ds_read_b128 v[224:227], v253 offset:45056\n\t" \
    "ds_read_b128 v[228:231], v253 offset:48128\n\t" \
    "s_waitcnt lgkmcnt(1)\n\t" \
    "v_max_u32 v215, v224, v225\n\t" \
    "v_max3_u32 v215, v226, v227, v215\n\t" \
    "s_waitcnt lgkmcnt(0)\n\t" \
    "v_max3_u32 v215, v228, v229, v215\n\t" \
    "v_max3_u32 v215, v230, v231, v215\n\t" \
    "v_cmp_eq_u32 vcc, -1, v215\n\t" \
    "s_cbranch_vccz .Lv211_%=\n\t" \
    "v_max_u32 v215, v224, v225\n\t" \
    "v_max3_u32 v215, v226, v227, v215\n\t" \
    "v_cmp_eq_u32 vcc, -1, v215\n\t" \
    "s_cbranch_vccz .Lv212_%=\n\t" \
    "s_mov_b32 m0, 0xb000\n\t" \
    "s_nop 0\n\t" \
    "buffer_load_dwordx4 v253, s[60:63], s46 offen sc1 lds\n\t" \
    ".Lv212_%=:\n\t" \
    "v_max_u32 v215, v228, v229\n\t" \
    "v_max3_u32 v215, v230, v231, v215\n\t" \
    "v_cmp_eq_u32 vcc, -1, v215\n\t" \
    "s_cbranch_vccz .Lv213_%=\n\t" \
    "s_mov_b32 m0, 0xbc00\n\t" \
    "s_nop 0\n\t" \
    "buffer_load_dwordx4 v253, s[60:63], s47 offen sc1 lds\n\t" \
    ".Lv213_%=:\n\t" \
    "s_cmp_eq_u32 s41, 0\n\t" \
    "s_cbranch_scc1 .Lv211_%=\n\t" \
    "s_sub_u32 s41, s41, 1\n\t" \
    "s_branch .Lv210_%=\n\t" \
    ".Lv211_%=:\n\t" \
    "s_waitcnt lgkmcnt(0)\n\t" \
    "s_barrier \n\t" \
    "ds_read_b128 v[232:235], v253 offset:40960\n\t" \
    "ds_read_b128 v[236:239], v253 offset:41984\n\t" \
    "ds_read_b128 v[240:243], v253 offset:43008\n\t" \
    "v_smfmac_f32_16x16x64_bf16 v[244:247], v[224:227], v[0:7], v212\n\t" \
    "v_smfmac_f32_16x16x64_bf16 v[248:251], v[224:227], v[8:15], v212\n\t" \
    "v_smfmac_f32_16x16x64_bf16 v[208:211], v[224:227], v[200:207], v212\n\t" \
    "ds_read_b128 v[200:203], v254 offset:55296\n\t" \
    "ds_read_b128 v[204:207], v254 offset:56320\n\t" \
    "ds_read_b128 v[224:227], v253 offset:44032\n\t" \
    "v_smfmac_f32_16x16x64_bf16 v[208:211], v[228:231], v[16:23], v212\n\t" \
    "v_smfmac_f32_16x16x64_bf16 v[244:247], v[228:231], v[24:31], v212\n\t" \
    "v_smfmac_f32_16x16x64_bf16 v[248:251], v[228:231], v[32:39], v212\n\t" \
    "ds_read_b128 v[228:231], v253 offset:46080\n\t" \
    "s_waitcnt lgkmcnt(6)\n\t" \
    "v_smfmac_f32_16x16x64_bf16 v[208:211], v[232:235], v[216:223], v212\n\t" \
    "ds_read_b128 v[216:219], v254 offset:57344\n\t" \
    "ds_read_b128 v[220:223], v254 offset:58368\n\t" \
    "v_smfmac_f32_16x16x64_bf16 v[244:247], v[232:235], v[40:47], v212\n\t" \
    "v_smfmac_f32_16x16x64_bf16 v[248:251], v[232:235], v[48:55], v212\n\t" \
    "ds_read_b128 v[232:235], v253 offset:47104\n\t" \
    "s_waitcnt lgkmcnt(8)\n\t" \
    "v_smfmac_f32_16x16x64_bf16 v[208:211], v[236:239], v[56:63], v212\n\t" \
    "v_smfmac_f32_16x16x64_bf16 v[244:247], v[236:239], v[64:71], v212\n\t" \
    "v_smfmac_f32_16x16x64_bf16 v[248:251], v[236:239], v[72:79], v212\n\t" \
    "ds_read_b128 v[236:239], v253 offset:49152\n\t" \
    "s_waitcnt lgkmcnt(6)\n\t" \
    "v_smfmac_f32_16x16x64_bf16 v[208:211], v[240:243], v[200:207], v212\n\t" \
    "ds_read_b128 v[200:203], v254 offset:59392\n\t" \
    "ds_read_b128 v[204:207], v254 offset:60416\n\t" \
    "v_smfmac_f32_16x16x64_bf16 v[244:247], v[240:243], v[80:87], v212\n\t" \
    "v_smfmac_f32_16x16x64_bf16 v[248:251], v[240:243], v[88:95], v212\n\t" \
    "ds_read_b128 v[240:243], v253 offset:50176\n\t" \
    "s_waitcnt lgkmcnt(8)\n\t" \
    "v_smfmac_f32_16x16x64_bf16 v[208:211], v[224:227], v[96:103], v212\n\t" \
    "v_smfmac_f32_16x16x64_bf16 v[244:247], v[224:227], v[104:111], v212\n\t" \
    "v_smfmac_f32_16x16x64_bf16 v[248:251], v[224:227], v[112:119], v212\n\t" \
    "s_waitcnt lgkmcnt(7)\n\t" \
    "v_smfmac_f32_16x16x64_bf16 v[244:247], v[228:231], v[120:127], v212\n\t" \
    "s_waitcnt lgkmcnt(4)\n\t" \
    "v_smfmac_f32_16x16x64_bf16 v[244:247], v[232:235], v[144:151], v212\n\t" \
    "s_waitcnt lgkmcnt(3)\n\t" \
    "v_smfmac_f32_16x16x64_bf16 v[244:247], v[236:239], v[160:167], v212\n\t" \
    "s_waitcnt lgkmcnt(0)\n\t" \
    "v_smfmac_f32_16x16x64_bf16 v[244:247], v[240:243], v[184:191], v212\n\t" \
    "v_smfmac_f32_16x16x64_bf16 v[248:251], v[228:231], v[128:135], v212\n\t" \
    "v_smfmac_f32_16x16x64_bf16 v[248:251], v[232:235], v[152:159], v212\n\t" \
    "v_smfmac_f32_16x16x64_bf16 v[248:251], v[236:239], v[168:175], v212\n\t" \
    "v_add_f32 v244, v244, v246\n\t" \
    "v_add_f32 v245, v245, v247\n\t" \
    "v_cvt_pk_bf16_f32 v244, v244, v245\n\t" \
    "v_smfmac_f32_16x16x64_bf16 v[248:251], v[240:243], v[192:199], v212\n\t" \
    "ds_write_b16 v213, v244 offset:512\n\t" \
    "ds_write_b16_d16_hi v213, v244 offset:528\n\t" \
    "v_smfmac_f32_16x16x64_bf16 v[208:211], v[228:231], v[216:223], v212\n\t" \
    "v_smfmac_f32_16x16x64_bf16 v[208:211], v[232:235], v[136:143], v212\n\t" \
    "v_smfmac_f32_16x16x64_bf16 v[208:211], v[236:239], v[200:207], v212\n\t" \
    "v_add_f32 v248, v248, v250\n\t" \
    "v_add_f32 v249, v249, v251\n\t" \
    "v_cvt_pk_bf16_f32 v248, v248, v249\n\t" \
    "v_smfmac_f32_16x16x64_bf16 v[208:211], v[240:243], v[176:183], v212\n\t" \
    "ds_write_b16 v213, v248 offset:1024\n\t" \
    "ds_write_b16_d16_hi v213, v248 offset:1040\n\t" \
    "s_nop 5\n\t" \
    "v_add_f32 v208, v208, v210\n\t" \
    "v_add_f32 v209, v209, v211\n\t" \
    "v_cvt_pk_bf16_f32 v208, v208, v209\n\t" \
    "ds_write_b16 v213, v208 offset:0\n\t" \
    "ds_write_b16_d16_hi v213, v208 offset:16\n\t" \
    "s_setprio 0\n\t" \
    "s_waitcnt lgkmcnt(0)\n\t" \
    "s_barrier \n\t" \
    "ds_read_b128 v[224:227], v253 offset:8192\n\t" \
    "ds_read_b128 v[244:247], v254 offset:51200\n\t" \
    "ds_read_b128 v[228:231], v253 offset:9216\n\t" \
    "ds_read_b128 v[232:235], v253 offset:28672\n\t" \
    "ds_read_b128 v[236:239], v253 offset:29696\n\t" \
    "ds_read_b128 v[248:251], v254 offset:52224\n\t" \
    "s_add_u32 s40, s40, 1\n\t" \
    "s_cmp_lg_u32 s40, 64\n\t" \
    "s_cbranch_scc1 .Lv27_%=\n\t" \
    "s_branch .Lend%=\n\t" \
    ".Lv1_%=:\n\t" \
    "s_mov_b32 s42, %[xs]\n\t" \
    "v_add_u32 v244, 0xc000, v243\n\t" \
    "s_waitcnt vmcnt(12) lgkmcnt(0)\n\t" \
    "s_barrier \n\t" \
    "s_add_u32 m0, %[wm], 0x9000\n\t" \
    "s_mov_b32 s44, 0x1e0000\n\t" \
    "buffer_load_dwordx4 v240, %[wr], s44 offen offset:0 nt lds\n\t" \
    "buffer_load_dwordx4 v241, %[wr], s44 offen offset:1024 nt lds\n\t" \
    "buffer_load_dwordx4 v242, %[wr], s44 offen offset:2048 nt lds\n\t" \
    "ds_read_b64_tr_b16 v[56:57], v244 offset:0\n\t" \
    "ds_read_b64_tr_b16 v[58:59], v244 offset:1536\n\t" \
    "ds_read_b64_tr_b16 v[64:65], v244 offset:32\n\t" \
    "ds_read_b64_tr_b16 v[66:67], v244 offset:1568\n\t" \
    "ds_read_b64_tr_b16 v[72:73], v244 offset:64\n\t" \
    "ds_read_b64_tr_b16 v[74:75], v244 offset:1600\n\t" \
    "v_add_u32 v244, 0xf000, v243\n\t" \
    "s_waitcnt vmcnt(12) lgkmcnt(0)\n\t" \
    "s_barrier \n\t" \
    "s_add_u32 m0, %[wm], 0xc000\n\t" \
    "s_mov_b32 s44, 0x240000\n\t" \
    "buffer_load_dwordx4 v240, %[wr], s44 offen offset:0 nt lds\n\t" \
    "buffer_load_dwordx4 v241, %[wr], s44 offen offset:1024 nt lds\n\t" \
    "buffer_load_dwordx4 v242, %[wr], s44 offen offset:2048 nt lds\n\t" \
    "ds_read_b64_tr_b16 v[60:61], v244 offset:0\n\t" \
    "ds_read_b64_tr_b16 v[62:63], v244 offset:1536\n\t" \
    "ds_read_b64_tr_b16 v[68:69], v244 offset:32\n\t" \
    "ds_read_b64_tr_b16 v[70:71], v244 offset:1568\n\t" \
    "ds_read_b64_tr_b16 v[76:77], v244 offset:64\n\t" \
    "ds_read_b64_tr_b16 v[78:79], v244 offset:1600\n\t" \
    "v_add_u32 v244, 0x0, v243\n\t" \
    "s_waitcnt vmcnt(12) lgkmcnt(0)\n\t" \
    "s_barrier \n\t" \
    "s_add_u32 m0, %[wm], 0xf000\n\t" \
    "s_mov_b32 s44, 0x2a0000\n\t" \
    "buffer_load_dwordx4 v240, %[wr], s44 offen offset:0 nt lds\n\t" \
    "buffer_load_dwordx4 v241, %[wr], s44 offen offset:1024 nt lds\n\t" \
    "buffer_load_dwordx4 v242, %[wr], s44 offen offset:2048 nt lds\n\t" \
    "ds_read_b64_tr_b16 v[96:97], v244 offset:0\n\t" \
    "ds_read_b64_tr_b16 v[98:99], v244 offset:1536\n\t" \
    "ds_read_b64_tr_b16 v[104:105], v244 offset:32\n\t" \
    "ds_read_b64_tr_b16 v[106:107], v244 offset:1568\n\t" \
    "ds_read_b64_tr_b16 v[112:113], v244 offset:64\n\t" \
    "ds_read_b64_tr_b16 v[114:115], v244 offset:1600\n\t" \
    "v_add_u32 v244, 0x3000, v243\n\t" \
    "s_waitcnt vmcnt(12) lgkmcnt(0)\n\t" \
    "s_barrier \n\t" \
    "s_add_u32 m0, %[wm], 0x0\n\t" \
    "s_mov_b32 s44, 0x300000\n\t" \
    "buffer_load_dwordx4 v240, %[wr], s44 offen offset:0 nt lds\n\t" \
    "buffer_load_dwordx4 v241, %[wr], s44 offen offset:1024 nt lds\n\t" \
    "buffer_load_dwordx4 v242, %[wr], s44 offen offset:2048 nt lds\n\t" \
    "ds_read_b64_tr_b16 v[100:101], v244 offset:0\n\t" \
    "ds_read_b64_tr_b16 v[102:103], v244 offset:1536\n\t" \
    "ds_read_b64_tr_b16 v[108:109], v244 offset:32\n\t" \
    "ds_read_b64_tr_b16 v[110:111], v244 offset:1568\n\t" \
    "ds_read_b64_tr_b16 v[116:117], v244 offset:64\n\t" \
    "ds_read_b64_tr_b16 v[118:119], v244 offset:1600\n\t" \
    "v_add_u32 v244, 0x6000, v243\n\t" \
    "s_waitcnt vmcnt(12) lgkmcnt(0)\n\t" \
    "s_barrier \n\t" \
    "s_add_u32 m0, %[wm], 0x3000\n\t" \
    "s_mov_b32 s44, 0x360000\n\t" \
    "buffer_load_dwordx4 v240, %[wr], s44 offen offset:0 nt lds\n\t" \
    "buffer_load_dwordx4 v241, %[wr], s44 offen offset:1024 nt lds\n\t" \
    "buffer_load_dwordx4 v242, %[wr], s44 offen offset:2048 nt lds\n\t" \
    "ds_read_b64_tr_b16 v[200:201], v244 offset:0\n\t" \
    "ds_read_b64_tr_b16 v[202:203], v244 offset:1536\n\t" \
    "ds_read_b64_tr_b16 v[0:1], v244 offset:32\n\t" \
    "ds_read_b64_tr_b16 v[2:3], v244 offset:1568\n\t" \
    "ds_read_b64_tr_b16 v[8:9], v244 offset:64\n\t" \
    "ds_read_b64_tr_b16 v[10:11], v244 offset:1600\n\t" \
    "v_add_u32 v244, 0x9000, v243\n\t" \
    "s_waitcnt vmcnt(12) lgkmcnt(0)\n\t" \
    "s_barrier \n\t" \
    "s_add_u32 m0, %[wm], 0x6000\n\t" \
    "s_mov_b32 s44, 0x3c0000\n\t" \
    "buffer_load_dwordx4 v240, %[wr], s44 offen offset:0 nt lds\n\t" \
    "buffer_load_dwordx4 v241, %[wr], s44 offen offset:1024 nt lds\n\t" \
    "buffer_load_dwordx4 v242, %[wr], s44 offen offset:2048 nt lds\n\t" \
    "ds_read_b64_tr_b16 v[204:205], v244 offset:0\n\t" \
    "ds_read_b64_tr_b16 v[206:207], v244 offset:1536\n\t" \
    "ds_read_b64_tr_b16 v[4:5], v244 offset:32\n\t" \
    "ds_read_b64_tr_b16 v[6:7], v244 offset:1568\n\t" \
    "ds_read_b64_tr_b16 v[12:13], v244 offset:64\n\t" \
    "ds_read_b64_tr_b16 v[14:15], v244 offset:1600\n\t" \
    "v_add_u32 v244, 0xc000, v243\n\t" \
    "s_waitcnt vmcnt(12) lgkmcnt(0)\n\t" \
    "s_barrier \n\t" \
    "s_add_u32 m0, %[wm], 0x9000\n\t" \
    "s_mov_b32 s44, 0x420000\n\t" \
    "buffer_load_dwordx4 v240, %[wr], s44 offen offset:0 nt lds\n\t" \
    "buffer_load_dwordx4 v241, %[wr], s44 offen offset:1024 nt lds\n\t" \
    "buffer_load_dwordx4 v242, %[wr], s44 offen offset:2048 nt lds\n\t" \
    "ds_read_b64_tr_b16 v[208:209], v244 offset:0\n\t" \
    "ds_read_b64_tr_b16 v[210:211], v244 offset:1536\n\t" \
    "ds_read_b64_tr_b16 v[40:41], v244 offset:32\n\t" \
    "ds_read_b64_tr_b16 v[42:43], v244 offset:1568\n\t" \
    "ds_read_b64_tr_b16 v[48:49], v244 offset:64\n\t" \
    "ds_read_b64_tr_b16 v[50:51], v244 offset:1600\n\t" \
    "v_add_u32 v244, 0xf000, v243\n\t" \
    "s_waitcnt vmcnt(12) lgkmcnt(0)\n\t" \
    "s_barrier \n\t" \
    "s_add_u32 m0, %[wm], 0xc000\n\t" \
    "s_mov_b32 s44, 0x480000\n\t" \
    "buffer_load_dwordx4 v240, %[wr], s44 offen offset:0 nt lds\n\t" \
    "buffer_load_dwordx4 v241, %[wr], s44 offen offset:1024 nt lds\n\t" \
    "buffer_load_dwordx4 v242, %[wr], s44 offen offset:2048 nt lds\n\t" \
    "ds_read_b64_tr_b16 v[212:213], v244 offset:0\n\t" \
    "ds_read_b64_tr_b16 v[214:215], v244 offset:1536\n\t" \
    "ds_read_b64_tr_b16 v[44:45], v244 offset:32\n\t" \
    "ds_read_b64_tr_b16 v[46:47], v244 offset:1568\n\t" \
    "ds_read_b64_tr_b16 v[52:53], v244 offset:64\n\t" \
    "ds_read_b64_tr_b16 v[54:55], v244 offset:1600\n\t" \
    "v_add_u32 v244, 0x0, v243\n\t" \
    "s_waitcnt vmcnt(12) lgkmcnt(0)\n\t" \
    "s_barrier \n\t" \
    "v_mov_b32 v246, 0\n\t" \
    "buffer_load_dword v246, v246, s[60:63], %[mo] offen sc1\n\t" \
    "s_add_u32 m0, %[wm], 0xf000\n\t" \
    "s_mov_b32 s44, 0x4e0000\n\t" \
    "buffer_load_dwordx4 v240, %[wr], s44 offen offset:0 nt lds\n\t" \
    "buffer_load_dwordx4 v241, %[wr], s44 offen offset:1024 nt lds\n\t" \
    "buffer_load_dwordx4 v242, %[wr], s44 offen offset:2048 nt lds\n\t" \
    "ds_read_b64_tr_b16 v[136:137], v244 offset:0\n\t" \
    "ds_read_b64_tr_b16 v[138:139], v244 offset:1536\n\t" \
    "ds_read_b64_tr_b16 v[144:145], v244 offset:32\n\t" \
    "ds_read_b64_tr_b16 v[146:147], v244 offset:1568\n\t" \
    "ds_read_b64_tr_b16 v[152:153], v244 offset:64\n\t" \
    "ds_read_b64_tr_b16 v[154:155], v244 offset:1600\n\t" \
    "v_add_u32 v244, 0x3000, v243\n\t" \
    "s_waitcnt vmcnt(12) lgkmcnt(0)\n\t" \
    "s_barrier \n\t" \
    "s_add_u32 m0, %[wm], 0x0\n\t" \
    "s_mov_b32 s44, 0x540000\n\t" \
    "buffer_load_dwordx4 v240, %[wr], s44 offen offset:0 nt lds\n\t" \
    "buffer_load_dwordx4 v241, %[wr], s44 offen offset:1024 nt lds\n\t" \
    "buffer_load_dwordx4 v242, %[wr], s44 offen offset:2048 nt lds\n\t" \
    "ds_read_b64_tr_b16 v[140:141], v244 offset:0\n\t" \
    "ds_read_b64_tr_b16 v[142:143], v244 offset:1536\n\t" \
    "ds_read_b64_tr_b16 v[148:149], v244 offset:32\n\t" \
    "ds_read_b64_tr_b16 v[150:151], v244 offset:1568\n\t" \
    "ds_read_b64_tr_b16 v[156:157], v244 offset:64\n\t" \
    "ds_read_b64_tr_b16 v[158:159], v244 offset:1600\n\t" \
    "v_add_u32 v244, 0x6000, v243\n\t" \
    "s_waitcnt vmcnt(12) lgkmcnt(0)\n\t" \
    "s_barrier \n\t" \
    "s_add_u32 m0, %[wm], 0x3000\n\t" \
    "s_mov_b32 s44, 0x5a0000\n\t" \
    "buffer_load_dwordx4 v240, %[wr], s44 offen offset:0 nt lds\n\t" \
    "buffer_load_dwordx4 v241, %[wr], s44 offen offset:1024 nt lds\n\t" \
    "buffer_load_dwordx4 v242, %[wr], s44 offen offset:2048 nt lds\n\t" \
    "ds_read_b64_tr_b16 v[216:217], v244 offset:0\n\t" \
    "ds_read_b64_tr_b16 v[218:219], v244 offset:1536\n\t" \
    "ds_read_b64_tr_b16 v[80:81], v244 offset:32\n\t" \
    "ds_read_b64_tr_b16 v[82:83], v244 offset:1568\n\t" \
    "ds_read_b64_tr_b16 v[88:89], v244 offset:64\n\t" \
    "ds_read_b64_tr_b16 v[90:91], v244 offset:1600\n\t" \
    "v_add_u32 v244, 0x9000, v243\n\t" \
    "s_waitcnt vmcnt(12) lgkmcnt(0)\n\t" \
    "s_barrier \n\t" \
    "s_add_u32 m0, %[wm], 0x6000\n\t" \
    "s_mov_b32 s44, 0x600000\n\t" \
    "buffer_load_dwordx4 v240, %[wr], s44 offen offset:0 nt lds\n\t" \
    "buffer_load_dwordx4 v241, %[wr], s44 offen offset:1024 nt lds\n\t" \
    "buffer_load_dwordx4 v242, %[wr], s44 offen offset:2048 nt lds\n\t" \
    "ds_read_b64_tr_b16 v[220:221], v244 offset:0\n\t" \
    "ds_read_b64_tr_b16 v[222:223], v244 offset:1536\n\t" \
    "ds_read_b64_tr_b16 v[84:85], v244 offset:32\n\t" \
    "ds_read_b64_tr_b16 v[86:87], v244 offset:1568\n\t" \
    "ds_read_b64_tr_b16 v[92:93], v244 offset:64\n\t" \
    "ds_read_b64_tr_b16 v[94:95], v244 offset:1600\n\t" \
    "v_add_u32 v244, 0xc000, v243\n\t" \
    "s_waitcnt vmcnt(12) lgkmcnt(0)\n\t" \
    "s_barrier \n\t" \
    "s_add_u32 m0, %[wm], 0x9000\n\t" \
    "s_mov_b32 s44, 0x660000\n\t" \
    "buffer_load_dwordx4 v240, %[wr], s44 offen offset:0 nt lds\n\t" \
    "buffer_load_dwordx4 v241, %[wr], s44 offen offset:1024 nt lds\n\t" \
    "buffer_load_dwordx4 v242, %[wr], s44 offen offset:2048 nt lds\n\t" \
    "ds_read_b64_tr_b16 v[16:17], v244 offset:0\n\t" \
    "ds_read_b64_tr_b16 v[18:19], v244 offset:1536\n\t" \
    "ds_read_b64_tr_b16 v[24:25], v244 offset:32\n\t" \
    "ds_read_b64_tr_b16 v[26:27], v244 offset:1568\n\t" \
    "ds_read_b64_tr_b16 v[32:33], v244 offset:64\n\t" \
    "ds_read_b64_tr_b16 v[34:35], v244 offset:1600\n\t" \
    "v_add_u32 v244, 0xf000, v243\n\t" \
    "s_waitcnt vmcnt(12) lgkmcnt(0)\n\t" \
    "s_barrier \n\t" \
    ".Lv11_%=:\n\t" \
    "v_readfirstlane_b32 s43, v246\n\t" \
    "s_nop 0\n\t" \
    "s_cmp_lg_u32 s43, -1\n\t" \
    "s_cbranch_scc1 .Lv12_%=\n\t" \
    "s_cmp_eq_u32 s41, 0\n\t" \
    "s_cbranch_scc1 .Lv12_%=\n\t" \
    "s_sub_u32 s41, s41, 1\n\t" \
    "s_sleep 4\n\t" \
    "v_mov_b32 v246, 0\n\t" \
    "buffer_load_dword v246, v246, s[60:63], %[mo] offen sc1\n\t" \
    "s_waitcnt vmcnt(0)\n\t" \
    "s_branch .Lv11_%=\n\t" \
    ".Lv12_%=:\n\t" \
    "v_mov_b32 v246, 0x8000\n\t" \
    "s_mov_b64 exec, 1\n\t" \
    "buffer_store_dword v246, v246, s[60:63], %[mo] offen sc1\n\t" \
    "s_mov_b64 exec, -1\n\t" \
    "s_and_b32 s44, s43, 0x5000\n\t" \
    "s_xor_b32 %[mb], %[mb], s44\n\t" \
    "s_xor_b32 %[fl], %[fl], s44\n\t" \
    "s_and_b32 s43, s43, 0x2000\n\t" \
    "s_xor_b32 s43, s43, %[lx]\n\t" \
    "s_add_u32 s60, s60, s43\n\t" \
    "s_addc_u32 s61, s61, 0\n\t" \
    "v_and_b32 v252, 0xf0, v253\n\t" \
    "v_lshlrev_b32 v252, 3, v252\n\t" \
    "buffer_load_dword v252, v252, s[60:63], %[fl] offen sc1\n\t" \
    "s_add_u32 m0, %[wm], 0xc000\n\t" \
    "s_mov_b32 s44, 0x6c0000\n\t" \
    "buffer_load_dwordx4 v240, %[wr], s44 offen offset:0 nt lds\n\t" \
    "buffer_load_dwordx4 v241, %[wr], s44 offen offset:1024 nt lds\n\t" \
    "buffer_load_dwordx4 v242, %[wr], s44 offen offset:2048 nt lds\n\t" \
    "ds_read_b64_tr_b16 v[20:21], v244 offset:0\n\t" \
    "ds_read_b64_tr_b16 v[22:23], v244 offset:1536\n\t" \
    "ds_read_b64_tr_b16 v[28:29], v244 offset:32\n\t" \
    "ds_read_b64_tr_b16 v[30:31], v244 offset:1568\n\t" \
    "ds_read_b64_tr_b16 v[36:37], v244 offset:64\n\t" \
    "ds_read_b64_tr_b16 v[38:39], v244 offset:1600\n\t" \
    "v_add_u32 v244, 0x0, v243\n\t" \
    "s_waitcnt vmcnt(12) lgkmcnt(0)\n\t" \
    "s_barrier \n\t" \
    "s_add_u32 m0, %[wm], 0xf000\n\t" \
    "s_mov_b32 s44, 0x720000\n\t" \
    "buffer_load_dwordx4 v240, %[wr], s44 offen offset:0 nt lds\n\t" \
    "buffer_load_dwordx4 v241, %[wr], s44 offen offset:1024 nt lds\n\t" \
    "buffer_load_dwordx4 v242, %[wr], s44 offen offset:2048 nt lds\n\t" \
    "ds_read_b64_tr_b16 v[224:225], v244 offset:0\n\t" \
    "ds_read_b64_tr_b16 v[226:227], v244 offset:1536\n\t" \
    "ds_read_b64_tr_b16 v[120:121], v244 offset:32\n\t" \
    "ds_read_b64_tr_b16 v[122:123], v244 offset:1568\n\t" \
    "ds_read_b64_tr_b16 v[128:129], v244 offset:64\n\t" \
    "ds_read_b64_tr_b16 v[130:131], v244 offset:1600\n\t" \
    "v_add_u32 v244, 0x3000, v243\n\t" \
    "s_waitcnt vmcnt(12) lgkmcnt(0)\n\t" \
    "s_barrier \n\t" \
    "ds_read_b64_tr_b16 v[228:229], v244 offset:0\n\t" \
    "ds_read_b64_tr_b16 v[230:231], v244 offset:1536\n\t" \
    "ds_read_b64_tr_b16 v[124:125], v244 offset:32\n\t" \
    "ds_read_b64_tr_b16 v[126:127], v244 offset:1568\n\t" \
    "ds_read_b64_tr_b16 v[132:133], v244 offset:64\n\t" \
    "ds_read_b64_tr_b16 v[134:135], v244 offset:1600\n\t" \
    "v_add_u32 v244, 0x6000, v243\n\t" \
    "s_waitcnt vmcnt(9) lgkmcnt(0)\n\t" \
    "s_barrier \n\t" \
    "ds_read_b64_tr_b16 v[232:233], v244 offset:0\n\t" \
    "ds_read_b64_tr_b16 v[234:235], v244 offset:1536\n\t" \
    "ds_read_b64_tr_b16 v[160:161], v244 offset:32\n\t" \
    "ds_read_b64_tr_b16 v[162:163], v244 offset:1568\n\t" \
    "ds_read_b64_tr_b16 v[168:169], v244 offset:64\n\t" \
    "ds_read_b64_tr_b16 v[170:171], v244 offset:1600\n\t" \
    "v_add_u32 v244, 0x9000, v243\n\t" \
    "s_waitcnt vmcnt(6) lgkmcnt(0)\n\t" \
    "s_barrier \n\t" \
    "ds_read_b64_tr_b16 v[236:237], v244 offset:0\n\t" \
    "ds_read_b64_tr_b16 v[238:239], v244 offset:1536\n\t" \
    "ds_read_b64_tr_b16 v[164:165], v244 offset:32\n\t" \
    "ds_read_b64_tr_b16 v[166:167], v244 offset:1568\n\t" \
    "ds_read_b64_tr_b16 v[172:173], v244 offset:64\n\t" \
    "ds_read_b64_tr_b16 v[174:175], v244 offset:1600\n\t" \
    "v_add_u32 v244, 0xc000, v243\n\t" \
    "s_waitcnt vmcnt(3) lgkmcnt(0)\n\t" \
    "s_barrier \n\t" \
    "ds_read_b64_tr_b16 v[176:177], v244 offset:0\n\t" \
    "ds_read_b64_tr_b16 v[178:179], v244 offset:1536\n\t" \
    "ds_read_b64_tr_b16 v[184:185], v244 offset:32\n\t" \
    "ds_read_b64_tr_b16 v[186:187], v244 offset:1568\n\t" \
    "ds_read_b64_tr_b16 v[192:193], v244 offset:64\n\t" \
    "ds_read_b64_tr_b16 v[194:195], v244 offset:1600\n\t" \
    "s_mov_b32 s43, 0\n\t" \
    "v_cmp_eq_u32 vcc, 0, v252\n\t" \
    "s_cbranch_vccnz .Lv13_%=\n\t" \
    "s_mov_b32 m0, 0x1000\n\t" \
    "s_add_u32 s49, s42, 0x0\n\t" \
    "buffer_load_dwordx4 v253, s[60:63], s49 offen offset:0 sc1 lds\n\t" \
    "buffer_load_dwordx4 v253, s[60:63], s49 offen offset:1024 sc1 lds\n\t" \
    "buffer_load_dwordx4 v253, s[60:63], s49 offen offset:2048 sc1 lds\n\t" \
    "buffer_load_dwordx4 v253, s[60:63], s49 offen offset:3072 sc1 lds\n\t" \
    "s_mov_b32 m0, 0x4400\n\t" \
    "s_add_u32 s49, s42, 0xc400\n\t" \
    "buffer_load_dwordx4 v253, s[60:63], s49 offen offset:0 sc1 lds\n\t" \
    "s_mov_b32 m0, 0x6000\n\t" \
    "s_add_u32 s49, s42, 0x1000\n\t" \
    "buffer_load_dwordx4 v253, s[60:63], s49 offen offset:0 sc1 lds\n\t" \
    "buffer_load_dwordx4 v253, s[60:63], s49 offen offset:1024 sc1 lds\n\t" \
    "buffer_load_dwordx4 v253, s[60:63], s49 offen offset:2048 sc1 lds\n\t" \
    "buffer_load_dwordx4 v253, s[60:63], s49 offen offset:3072 sc1 lds\n\t" \
    "s_mov_b32 m0, 0x9400\n\t" \
    "s_add_u32 s49, s42, 0xd400\n\t" \
    "buffer_load_dwordx4 v253, s[60:63], s49 offen offset:0 sc1 lds\n\t" \
    "s_mov_b32 s43, 1\n\t" \
    ".Lv13_%=:\n\t" \
    "v_add_u32 v244, 0xf000, v243\n\t" \
    "s_waitcnt vmcnt(0) lgkmcnt(0)\n\t" \
    "s_barrier \n\t" \
    "ds_read_b64_tr_b16 v[180:181], v244 offset:0\n\t" \
    "ds_read_b64_tr_b16 v[182:183], v244 offset:1536\n\t" \
    "ds_read_b64_tr_b16 v[188:189], v244 offset:32\n\t" \
    "ds_read_b64_tr_b16 v[190:191], v244 offset:1568\n\t" \
    "ds_read_b64_tr_b16 v[196:197], v244 offset:64\n\t" \
    "ds_read_b64_tr_b16 v[198:199], v244 offset:1600\n\t" \
    "s_waitcnt lgkmcnt(0)\n\t" \
    "s_barrier \n\t" \
    "ds_write_b128 v254, v[200:203] offset:40960\n\t" \
    "ds_write_b128 v254, v[204:207] offset:41984\n\t" \
    "ds_write_b128 v254, v[208:211] offset:43008\n\t" \
    "ds_write_b128 v254, v[212:215] offset:44032\n\t" \
    "ds_write_b128 v254, v[216:219] offset:45056\n\t" \
    "ds_write_b128 v254, v[220:223] offset:46080\n\t" \
    "ds_write_b128 v254, v[224:227] offset:47104\n\t" \
    "ds_write_b128 v254, v[228:231] offset:48128\n\t" \
    "ds_write_b128 v254, v[232:235] offset:49152\n\t" \
    "ds_write_b128 v254, v[236:239] offset:50176\n\t" \
    "s_cmp_lg_u32 s43, 0\n\t" \
    "s_cbranch_scc1 .Lv14_%=\n\t" \
    "s_mov_b32 s44, 0\n\t" \
    ".Lv15_%=:\n\t" \
    "v_cmp_ge_u32 vcc, s44, v252\n\t" \
    "s_cbranch_vccz .Lv16_%=\n\t" \
    "s_cmp_eq_u32 s41, 0\n\t" \
    "s_cbranch_scc1 .Lv16_%=\n\t" \
    "s_sub_u32 s41, s41, 1\n\t" \
    "s_sleep 8\n\t" \
    "v_and_b32 v252, 0xf0, v253\n\t" \
    "v_lshlrev_b32 v252, 3, v252\n\t" \
    "buffer_load_dword v252, v252, s[60:63], %[fl] offen sc1\n\t" \
    "s_waitcnt vmcnt(0)\n\t" \
    "s_branch .Lv15_%=\n\t" \
    ".Lv16_%=:\n\t" \
    "s_mov_b32 m0, 0x1000\n\t" \
    "s_add_u32 s49, s42, 0x0\n\t" \
    "buffer_load_dwordx4 v253, s[60:63], s49 offen offset:0 sc1 lds\n\t" \
    "buffer_load_dwordx4 v253, s[60:63], s49 offen offset:1024 sc1 lds\n\t" \
    "buffer_load_dwordx4 v253, s[60:63], s49 offen offset:2048 sc1 lds\n\t" \
    "buffer_load_dwordx4 v253, s[60:63], s49 offen offset:3072 sc1 lds\n\t" \
    "s_mov_b32 m0, 0x4400\n\t" \
    "s_add_u32 s49, s42, 0xc400\n\t" \
    "buffer_load_dwordx4 v253, s[60:63], s49 offen offset:0 sc1 lds\n\t" \
    "s_mov_b32 m0, 0x6000\n\t" \
    "s_add_u32 s49, s42, 0x1000\n\t" \
    "buffer_load_dwordx4 v253, s[60:63], s49 offen offset:0 sc1 lds\n\t" \
    "buffer_load_dwordx4 v253, s[60:63], s49 offen offset:1024 sc1 lds\n\t" \
    "buffer_load_dwordx4 v253, s[60:63], s49 offen offset:2048 sc1 lds\n\t" \
    "buffer_load_dwordx4 v253, s[60:63], s49 offen offset:3072 sc1 lds\n\t" \
    "s_mov_b32 m0, 0x9400\n\t" \
    "s_add_u32 s49, s42, 0xd400\n\t" \
    "buffer_load_dwordx4 v253, s[60:63], s49 offen offset:0 sc1 lds\n\t" \
    ".Lv14_%=:\n\t" \
    "s_waitcnt vmcnt(0) lgkmcnt(0)\n\t" \
    "s_barrier \n\t" \
    "ds_read_b128 v[224:227], v253 offset:4096\n\t" \
    "ds_read_b128 v[244:247], v254 offset:40960\n\t" \
    "ds_read_b128 v[228:231], v253 offset:5120\n\t" \
    "ds_read_b128 v[232:235], v253 offset:24576\n\t" \
    "ds_read_b128 v[236:239], v253 offset:25600\n\t" \
    "ds_read_b128 v[248:251], v254 offset:41984\n\t" \
    ".Lv17_%=:\n\t" \
    "s_waitcnt lgkmcnt(5)\n\t" \
    "v_mfma_f32_16x16x32_bf16 v[204:207], v[0:3], v[224:227], 0\n\t" \
    "s_waitcnt lgkmcnt(3)\n\t" \
    "v_mfma_f32_16x16x32_bf16 v[204:207], v[4:7], v[228:231], v[204:207]\n\t" \
    "ds_read_b128 v[240:243], v253 offset:12288\n\t" \
    "v_mfma_f32_16x16x32_bf16 v[208:211], v[8:11], v[224:227], 0\n\t" \
    "v_mfma_f32_16x16x32_bf16 v[208:211], v[12:15], v[228:231], v[208:211]\n\t" \
    "v_mfma_f32_16x16x32_bf16 v[200:203], v[244:247], v[224:227], 0\n\t" \
    "s_waitcnt lgkmcnt(1)\n\t" \
    "v_mfma_f32_16x16x32_bf16 v[200:203], v[248:251], v[228:231], v[200:203]\n\t" \
    "v_mfma_f32_16x16x32_bf16 v[216:219], v[0:3], v[232:235], 0\n\t" \
    "ds_read_b128 v[224:227], v253 offset:13312\n\t" \
    "v_mfma_f32_16x16x32_bf16 v[216:219], v[4:7], v[236:239], v[216:219]\n\t" \
    "ds_read_b128 v[228:231], v253 offset:6144\n\t" \
    "v_mfma_f32_16x16x32_bf16 v[220:223], v[8:11], v[232:235], 0\n\t" \
    "v_mfma_f32_16x16x32_bf16 v[220:223], v[12:15], v[236:239], v[220:223]\n\t" \
    "v_mfma_f32_16x16x32_bf16 v[212:215], v[244:247], v[232:235], 0\n\t" \
    "ds_read_b128 v[244:247], v254 offset:43008\n\t" \
    "v_mfma_f32_16x16x32_bf16 v[212:215], v[248:251], v[236:239], v[212:215]\n\t" \
    "ds_read_b128 v[248:251], v254 offset:44032\n\t" \
    "s_waitcnt lgkmcnt(4)\n\t" \
    "v_mfma_f32_16x16x32_bf16 v[204:207], v[24:27], v[240:243], v[204:207]\n\t" \
    "ds_read_b128 v[232:235], v253 offset:7168\n\t" \
    "s_waitcnt lgkmcnt(4)\n\t" \
    "v_mfma_f32_16x16x32_bf16 v[204:207], v[28:31], v[224:227], v[204:207]\n\t" \
    "ds_read_b128 v[236:239], v253 offset:26624\n\t" \
    "v_mfma_f32_16x16x32_bf16 v[208:211], v[32:35], v[240:243], v[208:211]\n\t" \
    "v_mfma_f32_16x16x32_bf16 v[208:211], v[36:39], v[224:227], v[208:211]\n\t" \
    "v_mfma_f32_16x16x32_bf16 v[200:203], v[16:19], v[240:243], v[200:203]\n\t" \
    "v_mfma_f32_16x16x32_bf16 v[200:203], v[20:23], v[224:227], v[200:203]\n\t" \
    "s_waitcnt lgkmcnt(4)\n\t" \
    "v_mfma_f32_16x16x32_bf16 v[204:207], v[40:43], v[228:231], v[204:207]\n\t" \
    "ds_read_b128 v[240:243], v253 offset:27648\n\t" \
    "s_waitcnt lgkmcnt(2)\n\t" \
    "v_mfma_f32_16x16x32_bf16 v[204:207], v[44:47], v[232:235], v[204:207]\n\t" \
    "ds_read_b128 v[224:227], v253 offset:0\n\t" \
    "v_mfma_f32_16x16x32_bf16 v[208:211], v[48:51], v[228:231], v[208:211]\n\t" \
    "v_mfma_f32_16x16x32_bf16 v[208:211], v[52:55], v[232:235], v[208:211]\n\t" \
    "v_mfma_f32_16x16x32_bf16 v[200:203], v[244:247], v[228:231], v[200:203]\n\t" \
    "v_mfma_f32_16x16x32_bf16 v[200:203], v[248:251], v[232:235], v[200:203]\n\t" \
    "s_waitcnt lgkmcnt(2)\n\t" \
    "v_mfma_f32_16x16x32_bf16 v[216:219], v[40:43], v[236:239], v[216:219]\n\t" \
    "ds_read_b128 v[228:231], v253 offset:1024\n\t" \
    "s_add_u32 s44, s40, 1\n\t" \
    "s_waitcnt lgkmcnt(2)\n\t" \
    "v_mfma_f32_16x16x32_bf16 v[216:219], v[44:47], v[240:243], v[216:219]\n\t" \
    "ds_read_b128 v[232:235], v253 offset:10240\n\t" \
    "s_and_b32 s45, s44, 15\n\t" \
    "v_mfma_f32_16x16x32_bf16 v[220:223], v[48:51], v[236:239], v[220:223]\n\t" \
    "s_cmp_eq_u32 s45, 0\n\t" \
    "v_mfma_f32_16x16x32_bf16 v[220:223], v[52:55], v[240:243], v[220:223]\n\t" \
    "s_cselect_b32 s45, %[cj], %[cs]\n\t" \
    "v_mfma_f32_16x16x32_bf16 v[212:215], v[244:247], v[236:239], v[212:215]\n\t" \
    "ds_read_b128 v[244:247], v254 offset:45056\n\t" \
    "s_cmp_eq_u32 s44, 64\n\t" \
    "v_mfma_f32_16x16x32_bf16 v[212:215], v[248:251], v[240:243], v[212:215]\n\t" \
    "ds_read_b128 v[248:251], v254 offset:46080\n\t" \
    "s_cselect_b32 s45, 0, s45\n\t" \
    "s_waitcnt lgkmcnt(4)\n\t" \
    "v_mfma_f32_16x16x32_bf16 v[204:207], v[64:67], v[224:227], v[204:207]\n\t" \
    "ds_read_b128 v[236:239], v253 offset:11264\n\t" \
    "s_add_u32 s42, s42, s45\n\t" \
    "s_waitcnt lgkmcnt(4)\n\t" \
    "v_mfma_f32_16x16x32_bf16 v[204:207], v[68:71], v[228:231], v[204:207]\n\t" \
    "ds_read_b128 v[240:243], v253 offset:30720\n\t" \
    "v_mfma_f32_16x16x32_bf16 v[208:211], v[72:75], v[224:227], v[208:211]\n\t" \
    "v_mfma_f32_16x16x32_bf16 v[208:211], v[76:79], v[228:231], v[208:211]\n\t" \
    "v_mfma_f32_16x16x32_bf16 v[200:203], v[56:59], v[224:227], v[200:203]\n\t" \
    "v_mfma_f32_16x16x32_bf16 v[200:203], v[60:63], v[228:231], v[200:203]\n\t" \
    "s_waitcnt lgkmcnt(4)\n\t" \
    "v_mfma_f32_16x16x32_bf16 v[204:207], v[80:83], v[232:235], v[204:207]\n\t" \
    "ds_read_b128 v[224:227], v253 offset:31744\n\t" \
    "s_waitcnt lgkmcnt(2)\n\t" \
    "v_mfma_f32_16x16x32_bf16 v[204:207], v[84:87], v[236:239], v[204:207]\n\t" \
    "ds_read_b128 v[228:231], v253 offset:2048\n\t" \
    "v_mfma_f32_16x16x32_bf16 v[208:211], v[88:91], v[232:235], v[208:211]\n\t" \
    "v_mfma_f32_16x16x32_bf16 v[208:211], v[92:95], v[236:239], v[208:211]\n\t" \
    "v_mfma_f32_16x16x32_bf16 v[200:203], v[244:247], v[232:235], v[200:203]\n\t" \
    "v_mfma_f32_16x16x32_bf16 v[200:203], v[248:251], v[236:239], v[200:203]\n\t" \
    "s_waitcnt lgkmcnt(2)\n\t" \
    "v_mfma_f32_16x16x32_bf16 v[216:219], v[80:83], v[240:243], v[216:219]\n\t" \
    "ds_read_b128 v[232:235], v253 offset:3072\n\t" \
    "s_and_b32 s44, s40, 3\n\t" \
    "s_waitcnt lgkmcnt(2)\n\t" \
    "v_mfma_f32_16x16x32_bf16 v[216:219], v[84:87], v[224:227], v[216:219]\n\t" \
    "ds_read_b128 v[236:239], v253 offset:14336\n\t" \
    "s_lshl_b32 s44, s44, 22\n\t" \
    "v_mfma_f32_16x16x32_bf16 v[220:223], v[88:91], v[240:243], v[220:223]\n\t" \
    "s_add_u32 s44, s44, %[mb]\n\t" \
    "v_mfma_f32_16x16x32_bf16 v[220:223], v[92:95], v[224:227], v[220:223]\n\t" \
    "s_add_u32 s46, s44, 0x800\n\t" \
    "v_mfma_f32_16x16x32_bf16 v[212:215], v[244:247], v[240:243], v[212:215]\n\t" \
    "ds_read_b128 v[244:247], v254 offset:47104\n\t" \
    "s_add_u32 s47, s44, 0x8800\n\t" \
    "v_mfma_f32_16x16x32_bf16 v[212:215], v[248:251], v[224:227], v[212:215]\n\t" \
    "ds_read_b128 v[248:251], v254 offset:48128\n\t" \
    "s_add_u32 s48, s44, 0xc00\n\t" \
    "s_waitcnt lgkmcnt(4)\n\t" \
    "v_mfma_f32_16x16x32_bf16 v[204:207], v[104:107], v[228:231], v[204:207]\n\t" \
    "ds_read_b128 v[240:243], v253 offset:15360\n\t" \
    "s_waitcnt lgkmcnt(4)\n\t" \
    "v_mfma_f32_16x16x32_bf16 v[204:207], v[108:111], v[232:235], v[204:207]\n\t" \
    "ds_read_b128 v[224:227], v253 offset:34816\n\t" \
    "v_mfma_f32_16x16x32_bf16 v[208:211], v[112:115], v[228:231], v[208:211]\n\t" \
    "v_mfma_f32_16x16x32_bf16 v[208:211], v[116:119], v[232:235], v[208:211]\n\t" \
    "v_mfma_f32_16x16x32_bf16 v[200:203], v[96:99], v[228:231], v[200:203]\n\t" \
    "v_mfma_f32_16x16x32_bf16 v[200:203], v[100:103], v[232:235], v[200:203]\n\t" \
    "s_waitcnt lgkmcnt(4)\n\t" \
    "v_mfma_f32_16x16x32_bf16 v[204:207], v[120:123], v[236:239], v[204:207]\n\t" \
    "ds_read_b128 v[228:231], v253 offset:35840\n\t" \
    "s_waitcnt lgkmcnt(2)\n\t" \
    "v_mfma_f32_16x16x32_bf16 v[204:207], v[124:127], v[240:243], v[204:207]\n\t" \
    "ds_read_b128 v[232:235], v253 offset:8192\n\t" \
    "v_mfma_f32_16x16x32_bf16 v[208:211], v[128:131], v[236:239], v[208:211]\n\t" \
    "v_mfma_f32_16x16x32_bf16 v[208:211], v[132:135], v[240:243], v[208:211]\n\t" \
    "v_mfma_f32_16x16x32_bf16 v[200:203], v[244:247], v[236:239], v[200:203]\n\t" \
    "v_mfma_f32_16x16x32_bf16 v[200:203], v[248:251], v[240:243], v[200:203]\n\t" \
    "s_waitcnt lgkmcnt(2)\n\t" \
    "v_mfma_f32_16x16x32_bf16 v[216:219], v[120:123], v[224:227], v[216:219]\n\t" \
    "ds_read_b128 v[236:239], v253 offset:9216\n\t" \
    "s_waitcnt lgkmcnt(2)\n\t" \
    "v_mfma_f32_16x16x32_bf16 v[216:219], v[124:127], v[228:231], v[216:219]\n\t" \
    "ds_read_b128 v[240:243], v253 offset:16384\n\t" \
    "v_mfma_f32_16x16x32_bf16 v[220:223], v[128:131], v[224:227], v[220:223]\n\t" \
    "v_mfma_f32_16x16x32_bf16 v[220:223], v[132:135], v[228:231], v[220:223]\n\t" \
    "v_mfma_f32_16x16x32_bf16 v[212:215], v[244:247], v[224:227], v[212:215]\n\t" \
    "ds_read_b128 v[244:247], v254 offset:49152\n\t" \
    "v_mfma_f32_16x16x32_bf16 v[212:215], v[248:251], v[228:231], v[212:215]\n\t" \
    "ds_read_b128 v[248:251], v254 offset:50176\n\t" \
    "v_and_b32 v252, 0xf0, v253\n\t" \
    "v_lshlrev_b32 v252, 3, v252\n\t" \
    "buffer_load_dword v252, v252, s[60:63], %[fl] offen sc1\n\t" \
    "s_waitcnt lgkmcnt(4)\n\t" \
    "v_mfma_f32_16x16x32_bf16 v[204:207], v[144:147], v[232:235], v[204:207]\n\t" \
    "ds_read_b128 v[224:227], v253 offset:17408\n\t" \
    "s_waitcnt lgkmcnt(4)\n\t" \
    "v_mfma_f32_16x16x32_bf16 v[204:207], v[148:151], v[236:239], v[204:207]\n\t" \
    "ds_read_b128 v[228:231], v253 offset:36864\n\t" \
    "v_mfma_f32_16x16x32_bf16 v[208:211], v[152:155], v[232:235], v[208:211]\n\t" \
    "v_mfma_f32_16x16x32_bf16 v[208:211], v[156:159], v[236:239], v[208:211]\n\t" \
    "v_mfma_f32_16x16x32_bf16 v[200:203], v[136:139], v[232:235], v[200:203]\n\t" \
    "v_mfma_f32_16x16x32_bf16 v[200:203], v[140:143], v[236:239], v[200:203]\n\t" \
    "s_waitcnt lgkmcnt(4)\n\t" \
    "v_mfma_f32_16x16x32_bf16 v[204:207], v[160:163], v[240:243], v[204:207]\n\t" \
    "ds_read_b128 v[232:235], v253 offset:37888\n\t" \
    "s_waitcnt lgkmcnt(2)\n\t" \
    "v_mfma_f32_16x16x32_bf16 v[204:207], v[164:167], v[224:227], v[204:207]\n\t" \
    "ds_read_b128 v[236:239], v253 offset:18432\n\t" \
    "v_mfma_f32_16x16x32_bf16 v[208:211], v[168:171], v[240:243], v[208:211]\n\t" \
    "v_mfma_f32_16x16x32_bf16 v[208:211], v[172:175], v[224:227], v[208:211]\n\t" \
    "v_mfma_f32_16x16x32_bf16 v[200:203], v[244:247], v[240:243], v[200:203]\n\t" \
    "v_mfma_f32_16x16x32_bf16 v[200:203], v[248:251], v[224:227], v[200:203]\n\t" \
    "s_waitcnt lgkmcnt(2)\n\t" \
    "v_mfma_f32_16x16x32_bf16 v[216:219], v[160:163], v[228:231], v[216:219]\n\t" \
    "ds_read_b128 v[240:243], v253 offset:19456\n\t" \
    "s_waitcnt lgkmcnt(2)\n\t" \
    "v_mfma_f32_16x16x32_bf16 v[216:219], v[164:167], v[232:235], v[216:219]\n\t" \
    "ds_read_b128 v[224:227], v253 offset:32768\n\t" \
    "v_mfma_f32_16x16x32_bf16 v[220:223], v[168:171], v[228:231], v[220:223]\n\t" \
    "v_mfma_f32_16x16x32_bf16 v[220:223], v[172:175], v[232:235], v[220:223]\n\t" \
    "v_mfma_f32_16x16x32_bf16 v[212:215], v[244:247], v[228:231], v[212:215]\n\t" \
    "v_mfma_f32_16x16x32_bf16 v[212:215], v[248:251], v[232:235], v[212:215]\n\t" \
    "s_waitcnt lgkmcnt(2)\n\t" \
    "v_mfma_f32_16x16x32_bf16 v[204:207], v[184:187], v[236:239], v[204:207]\n\t" \
    "ds_read_b128 v[228:231], v253 offset:33792\n\t" \
    "s_waitcnt lgkmcnt(2)\n\t" \
    "v_mfma_f32_16x16x32_bf16 v[204:207], v[188:191], v[240:243], v[204:207]\n\t" \
    "ds_read_b128 v[232:235], v253 offset:20480\n\t" \
    "v_mfma_f32_16x16x32_bf16 v[208:211], v[192:195], v[236:239], v[208:211]\n\t" \
    "v_mfma_f32_16x16x32_bf16 v[208:211], v[196:199], v[240:243], v[208:211]\n\t" \
    "v_mfma_f32_16x16x32_bf16 v[200:203], v[176:179], v[236:239], v[200:203]\n\t" \
    "v_mfma_f32_16x16x32_bf16 v[200:203], v[180:183], v[240:243], v[200:203]\n\t" \
    "s_waitcnt lgkmcnt(2)\n\t" \
    "v_mfma_f32_16x16x32_bf16 v[216:219], v[24:27], v[224:227], v[216:219]\n\t" \
    "ds_read_b128 v[236:239], v253 offset:21504\n\t" \
    "s_waitcnt lgkmcnt(2)\n\t" \
    "v_mfma_f32_16x16x32_bf16 v[216:219], v[28:31], v[228:231], v[216:219]\n\t" \
    "ds_read_b128 v[240:243], v253 offset:22528\n\t" \
    "v_mfma_f32_16x16x32_bf16 v[220:223], v[32:35], v[224:227], v[220:223]\n\t" \
    "v_cvt_pk_bf16_f32 v204, v204, v205\n\t" \
    "v_mfma_f32_16x16x32_bf16 v[220:223], v[36:39], v[228:231], v[220:223]\n\t" \
    "v_cvt_pk_bf16_f32 v205, v206, v207\n\t" \
    "v_mfma_f32_16x16x32_bf16 v[212:215], v[16:19], v[224:227], v[212:215]\n\t" \
    "ds_write_b64 v254, v[204:205] offset:10240\n\t" \
    "v_mfma_f32_16x16x32_bf16 v[212:215], v[20:23], v[228:231], v[212:215]\n\t" \
    "v_cvt_pk_bf16_f32 v208, v208, v209\n\t" \
    "s_waitcnt lgkmcnt(3)\n\t" \
    "v_mfma_f32_16x16x32_bf16 v[216:219], v[64:67], v[232:235], v[216:219]\n\t" \
    "ds_read_b128 v[224:227], v253 offset:23552\n\t" \
    "v_cvt_pk_bf16_f32 v209, v210, v211\n\t" \
    "s_waitcnt lgkmcnt(3)\n\t" \
    "v_mfma_f32_16x16x32_bf16 v[216:219], v[68:71], v[236:239], v[216:219]\n\t" \
    "ds_read_b128 v[228:231], v253 offset:28672\n\t" \
    "ds_write_b64 v254, v[208:209] offset:11264\n\t" \
    "v_mfma_f32_16x16x32_bf16 v[220:223], v[72:75], v[232:235], v[220:223]\n\t" \
    "v_cvt_pk_bf16_f32 v200, v200, v201\n\t" \
    "v_mfma_f32_16x16x32_bf16 v[220:223], v[76:79], v[236:239], v[220:223]\n\t" \
    "v_cvt_pk_bf16_f32 v201, v202, v203\n\t" \
    "v_mfma_f32_16x16x32_bf16 v[212:215], v[56:59], v[232:235], v[212:215]\n\t" \
    "ds_write_b64 v254, v[200:201] offset:9216\n\t" \
    "v_mfma_f32_16x16x32_bf16 v[212:215], v[60:63], v[236:239], v[212:215]\n\t" \
    "s_waitcnt lgkmcnt(5)\n\t" \
    "v_mfma_f32_16x16x32_bf16 v[216:219], v[104:107], v[240:243], v[216:219]\n\t" \
    "ds_read_b128 v[232:235], v253 offset:29696\n\t" \
    "s_waitcnt lgkmcnt(4)\n\t" \
    "v_mfma_f32_16x16x32_bf16 v[216:219], v[108:111], v[224:227], v[216:219]\n\t" \
    "ds_read_b128 v[236:239], v253 offset:38912\n\t" \
    "v_mfma_f32_16x16x32_bf16 v[220:223], v[112:115], v[240:243], v[220:223]\n\t" \
    "v_mfma_f32_16x16x32_bf16 v[220:223], v[116:119], v[224:227], v[220:223]\n\t" \
    "v_mfma_f32_16x16x32_bf16 v[212:215], v[96:99], v[240:243], v[212:215]\n\t" \
    "v_mfma_f32_16x16x32_bf16 v[212:215], v[100:103], v[224:227], v[212:215]\n\t" \
    "ds_read_b128 v[240:243], v253 offset:39936\n\t" \
    "s_waitcnt lgkmcnt(3)\n\t" \
    "s_barrier \n\t" \
    "v_mfma_f32_16x16x32_bf16 v[216:219], v[144:147], v[228:231], v[216:219]\n\t" \
    "s_waitcnt lgkmcnt(2)\n\t" \
    "v_mfma_f32_16x16x32_bf16 v[216:219], v[148:151], v[232:235], v[216:219]\n\t" \
    "s_waitcnt lgkmcnt(1)\n\t" \
    "v_mfma_f32_16x16x32_bf16 v[216:219], v[184:187], v[236:239], v[216:219]\n\t" \
    "s_waitcnt lgkmcnt(0)\n\t" \
    "v_mfma_f32_16x16x32_bf16 v[216:219], v[188:191], v[240:243], v[216:219]\n\t" \
    "v_mfma_f32_16x16x32_bf16 v[220:223], v[152:155], v[228:231], v[220:223]\n\t" \
    "v_mfma_f32_16x16x32_bf16 v[220:223], v[156:159], v[232:235], v[220:223]\n\t" \
    "v_mfma_f32_16x16x32_bf16 v[220:223], v[192:195], v[236:239], v[220:223]\n\t" \
    "v_cvt_pk_bf16_f32 v216, v216, v217\n\t" \
    "v_cvt_pk_bf16_f32 v217, v218, v219\n\t" \
    "v_mfma_f32_16x16x32_bf16 v[220:223], v[196:199], v[240:243], v[220:223]\n\t" \
    "ds_write_b64 v254, v[216:217] offset:16384\n\t" \
    "v_mfma_f32_16x16x32_bf16 v[212:215], v[136:139], v[228:231], v[212:215]\n\t" \
    "v_mfma_f32_16x16x32_bf16 v[212:215], v[140:143], v[232:235], v[212:215]\n\t" \
    "v_mfma_f32_16x16x32_bf16 v[212:215], v[176:179], v[236:239], v[212:215]\n\t" \
    "v_cvt_pk_bf16_f32 v220, v220, v221\n\t" \
    "v_cvt_pk_bf16_f32 v221, v222, v223\n\t" \
    "v_mfma_f32_16x16x32_bf16 v[212:215], v[180:183], v[240:243], v[212:215]\n\t" \
    "ds_write_b64 v254, v[220:221] offset:17408\n\t" \
    "s_nop 6\n\t" \
    "v_cvt_pk_bf16_f32 v212, v212, v213\n\t" \
    "v_cvt_pk_bf16_f32 v213, v214, v215\n\t" \
    "ds_write_b64 v254, v[212:213] offset:15360\n\t" \
    "s_waitcnt vmcnt(0) lgkmcnt(0)\n\t" \
    "s_barrier \n\t" \
    "s_setprio 2\n\t" \
    "s_add_u32 s44, s40, 1\n\t" \
    "s_min_u32 s44, s44, 63\n\t" \
    ".Lv18_%=:\n\t" \
    "v_cmp_ge_u32 vcc, s44, v252\n\t" \
    "s_cbranch_vccz .Lv19_%=\n\t" \
    "s_cmp_eq_u32 s41, 0\n\t" \
    "s_cbranch_scc1 .Lv19_%=\n\t" \
    "s_sub_u32 s41, s41, 1\n\t" \
    "s_sleep 2\n\t" \
    "v_and_b32 v252, 0xf0, v253\n\t" \
    "v_lshlrev_b32 v252, 3, v252\n\t" \
    "buffer_load_dword v252, v252, s[60:63], %[fl] offen sc1\n\t" \
    "s_waitcnt vmcnt(0)\n\t" \
    "s_branch .Lv18_%=\n\t" \
    ".Lv19_%=:\n\t" \
    "s_mov_b32 m0, 0x1000\n\t" \
    "s_add_u32 s49, s42, 0x0\n\t" \
    "buffer_load_dwordx4 v253, s[60:63], s49 offen offset:0 sc1 lds\n\t" \
    "s_sleep 1\n\t" \
    "buffer_load_dwordx4 v253, s[60:63], s49 offen offset:1024 sc1 lds\n\t" \
    "s_sleep 1\n\t" \
    "buffer_load_dwordx4 v253, s[60:63], s49 offen offset:2048 sc1 lds\n\t" \
    "s_sleep 1\n\t" \
    "buffer_load_dwordx4 v253, s[60:63], s49 offen offset:3072 sc1 lds\n\t" \
    "s_sleep 1\n\t" \
    "s_mov_b32 m0, 0x4400\n\t" \
    "s_add_u32 s49, s42, 0xc400\n\t" \
    "buffer_load_dwordx4 v253, s[60:63], s49 offen offset:0 sc1 lds\n\t" \
    "s_sleep 1\n\t" \
    "s_mov_b32 m0, 0x6000\n\t" \
    "s_add_u32 s49, s42, 0x1000\n\t" \
    "buffer_load_dwordx4 v253, s[60:63], s49 offen offset:0 sc1 lds\n\t" \
    "s_sleep 1\n\t" \
    "buffer_load_dwordx4 v253, s[60:63], s49 offen offset:1024 sc1 lds\n\t" \
    "s_sleep 1\n\t" \
    "buffer_load_dwordx4 v253, s[60:63], s49 offen offset:2048 sc1 lds\n\t" \
    "s_sleep 1\n\t" \
    "buffer_load_dwordx4 v253, s[60:63], s49 offen offset:3072 sc1 lds\n\t" \
    "s_sleep 1\n\t" \
    "s_mov_b32 m0, 0x9400\n\t" \
    "s_add_u32 s49, s42, 0xd400\n\t" \
    "buffer_load_dwordx4 v253, s[60:63], s49 offen offset:0 sc1 lds\n\t" \
    "s_sleep 1\n\t" \
    "ds_read_b128 v[200:203], v254 offset:40960\n\t" \
    "ds_read_b128 v[204:207], v254 offset:41984\n\t" \
    "ds_read_b128 v[216:219], v254 offset:43008\n\t" \
    "ds_read_b128 v[220:223], v254 offset:44032\n\t" \
    "v_mov_b64 v[208:209], 0\n\t" \
    "v_mov_b64 v[210:211], 0\n\t" \
    "v_mov_b64 v[244:245], 0\n\t" \
    "v_mov_b64 v[246:247], 0\n\t" \
    "v_mov_b64 v[248:249], 0\n\t" \
    "v_mov_b64 v[250:251], 0\n\t" \
    "s_sleep 5\n\t" \
    "s_mov_b32 m0, 0xa800\n\t" \
    "s_nop 0\n\t" \
    "buffer_load_dwordx4 v253, s[60:63], s46 offen sc1 lds\n\t" \
    "s_mov_b32 m0, 0xb800\n\t" \
    "s_nop 0\n\t" \
    "buffer_load_dwordx4 v253, s[60:63], s47 offen sc1 lds\n\t" \
    "s_mov_b32 m0, 0xac00\n\t" \
    "s_nop 0\n\t" \
    "buffer_load_dwordx4 v253, s[60:63], s48 offen sc1 lds\n\t" \
    "s_waitcnt lgkmcnt(0)\n\t" \
    "v_bfe_i32 v212, v253, 5, 1\n\t" \
    "v_and_b32 v212, 0xaaaa, v212\n\t" \
    "v_or_b32 v212, 0x4444, v212\n\t" \
    "v_and_b32 v213, 0xffff, v255\n\t" \
    "v_lshrrev_b32 v214, 16, v255\n\t" \
    ".Lv110_%=:\n\t" \
    "s_waitcnt vmcnt(0)\n\t" \
    "ds_read_b128 v[224:227], v253 offset:43008\n\t" \
    "ds_read_b128 v[228:231], v253 offset:47104\n\t" \
    "ds_read_b128 v[232:235], v253 offset:44032\n\t" \
    "s_waitcnt lgkmcnt(2)\n\t" \
    "v_max_u32 v215, v224, v225\n\t" \
    "v_max3_u32 v215, v226, v227, v215\n\t" \
    "s_waitcnt lgkmcnt(1)\n\t" \
    "v_max3_u32 v215, v228, v229, v215\n\t" \
    "v_max3_u32 v215, v230, v231, v215\n\t" \
    "s_waitcnt lgkmcnt(0)\n\t" \
    "v_max3_u32 v215, v232, v233, v215\n\t" \
    "v_max3_u32 v215, v234, v235, v215\n\t" \
    "v_cmp_eq_u32 vcc, -1, v215\n\t" \
    "s_cbranch_vccz .Lv111_%=\n\t" \
    "v_max_u32 v215, v224, v225\n\t" \
    "v_max3_u32 v215, v226, v227, v215\n\t" \
    "v_cmp_eq_u32 vcc, -1, v215\n\t" \
    "s_cbranch_vccz .Lv112_%=\n\t" \
    "s_mov_b32 m0, 0xa800\n\t" \
    "s_nop 0\n\t" \
    "buffer_load_dwordx4 v253, s[60:63], s46 offen sc1 lds\n\t" \
    ".Lv112_%=:\n\t" \
    "v_max_u32 v215, v228, v229\n\t" \
    "v_max3_u32 v215, v230, v231, v215\n\t" \
    "v_cmp_eq_u32 vcc, -1, v215\n\t" \
    "s_cbranch_vccz .Lv113_%=\n\t" \
    "s_mov_b32 m0, 0xb800\n\t" \
    "s_nop 0\n\t" \
    "buffer_load_dwordx4 v253, s[60:63], s47 offen sc1 lds\n\t" \
    ".Lv113_%=:\n\t" \
    "v_max_u32 v215, v232, v233\n\t" \
    "v_max3_u32 v215, v234, v235, v215\n\t" \
    "v_cmp_eq_u32 vcc, -1, v215\n\t" \
    "s_cbranch_vccz .Lv114_%=\n\t" \
    "s_mov_b32 m0, 0xac00\n\t" \
    "s_nop 0\n\t" \
    "buffer_load_dwordx4 v253, s[60:63], s48 offen sc1 lds\n\t" \
    ".Lv114_%=:\n\t" \
    "s_cmp_eq_u32 s41, 0\n\t" \
    "s_cbranch_scc1 .Lv111_%=\n\t" \
    "s_sub_u32 s41, s41, 1\n\t" \
    "s_branch .Lv110_%=\n\t" \
    ".Lv111_%=:\n\t" \
    "s_waitcnt lgkmcnt(0)\n\t" \
    "s_barrier \n\t" \
    "ds_read_b128 v[236:239], v253 offset:40960\n\t" \
    "ds_read_b128 v[240:243], v253 offset:46080\n\t" \
    "v_smfmac_f32_16x16x64_bf16 v[244:247], v[224:227], v[0:7], v212\n\t" \
    "v_smfmac_f32_16x16x64_bf16 v[248:251], v[224:227], v[8:15], v212\n\t" \
    "v_smfmac_f32_16x16x64_bf16 v[208:211], v[224:227], v[200:207], v212\n\t" \
    "ds_read_b128 v[200:203], v254 offset:45056\n\t" \
    "ds_read_b128 v[204:207], v254 offset:46080\n\t" \
    "ds_read_b128 v[224:227], v253 offset:41984\n\t" \
    "v_smfmac_f32_16x16x64_bf16 v[208:211], v[228:231], v[16:23], v212\n\t" \
    "v_smfmac_f32_16x16x64_bf16 v[244:247], v[228:231], v[24:31], v212\n\t" \
    "v_smfmac_f32_16x16x64_bf16 v[248:251], v[228:231], v[32:39], v212\n\t" \
    "ds_read_b128 v[228:231], v253 offset:48128\n\t" \
    "v_smfmac_f32_16x16x64_bf16 v[208:211], v[232:235], v[216:223], v212\n\t" \
    "ds_read_b128 v[216:219], v254 offset:47104\n\t" \
    "ds_read_b128 v[220:223], v254 offset:48128\n\t" \
    "v_smfmac_f32_16x16x64_bf16 v[244:247], v[232:235], v[40:47], v212\n\t" \
    "v_smfmac_f32_16x16x64_bf16 v[248:251], v[232:235], v[48:55], v212\n\t" \
    "ds_read_b128 v[232:235], v253 offset:45056\n\t" \
    "s_waitcnt lgkmcnt(8)\n\t" \
    "v_smfmac_f32_16x16x64_bf16 v[208:211], v[236:239], v[56:63], v212\n\t" \
    "v_smfmac_f32_16x16x64_bf16 v[244:247], v[236:239], v[64:71], v212\n\t" \
    "v_smfmac_f32_16x16x64_bf16 v[248:251], v[236:239], v[72:79], v212\n\t" \
    "ds_read_b128 v[236:239], v253 offset:49152\n\t" \
    "s_waitcnt lgkmcnt(6)\n\t" \
    "v_smfmac_f32_16x16x64_bf16 v[208:211], v[240:243], v[200:207], v212\n\t" \
    "ds_read_b128 v[200:203], v254 offset:49152\n\t" \
    "ds_read_b128 v[204:207], v254 offset:50176\n\t" \
    "v_smfmac_f32_16x16x64_bf16 v[244:247], v[240:243], v[80:87], v212\n\t" \
    "v_smfmac_f32_16x16x64_bf16 v[248:251], v[240:243], v[88:95], v212\n\t" \
    "ds_read_b128 v[240:243], v253 offset:50176\n\t" \
    "s_waitcnt lgkmcnt(8)\n\t" \
    "v_smfmac_f32_16x16x64_bf16 v[208:211], v[224:227], v[96:103], v212\n\t" \
    "v_smfmac_f32_16x16x64_bf16 v[244:247], v[224:227], v[104:111], v212\n\t" \
    "v_smfmac_f32_16x16x64_bf16 v[248:251], v[224:227], v[112:119], v212\n\t" \
    "s_waitcnt lgkmcnt(7)\n\t" \
    "v_smfmac_f32_16x16x64_bf16 v[244:247], v[228:231], v[120:127], v212\n\t" \
    "s_waitcnt lgkmcnt(4)\n\t" \
    "v_smfmac_f32_16x16x64_bf16 v[244:247], v[232:235], v[144:151], v212\n\t" \
    "s_waitcnt lgkmcnt(3)\n\t" \
    "v_smfmac_f32_16x16x64_bf16 v[244:247], v[236:239], v[160:167], v212\n\t" \
    "s_waitcnt lgkmcnt(0)\n\t" \
    "v_smfmac_f32_16x16x64_bf16 v[244:247], v[240:243], v[184:191], v212\n\t" \
    "v_smfmac_f32_16x16x64_bf16 v[248:251], v[228:231], v[128:135], v212\n\t" \
    "v_smfmac_f32_16x16x64_bf16 v[248:251], v[232:235], v[152:159], v212\n\t" \
    "v_smfmac_f32_16x16x64_bf16 v[248:251], v[236:239], v[168:175], v212\n\t" \
    "v_add_f32 v244, v244, v246\n\t" \
    "v_add_f32 v245, v245, v247\n\t" \
    "v_cvt_pk_bf16_f32 v244, v244, v245\n\t" \
    "v_smfmac_f32_16x16x64_bf16 v[248:251], v[240:243], v[192:199], v212\n\t" \
    "ds_write_b16 v213, v244 offset:512\n\t" \
    "ds_write_b16_d16_hi v213, v244 offset:528\n\t" \
    "v_smfmac_f32_16x16x64_bf16 v[208:211], v[228:231], v[216:223], v212\n\t" \
    "v_smfmac_f32_16x16x64_bf16 v[208:211], v[232:235], v[136:143], v212\n\t" \
    "v_smfmac_f32_16x16x64_bf16 v[208:211], v[236:239], v[200:207], v212\n\t" \
    "v_add_f32 v248, v248, v250\n\t" \
    "v_add_f32 v249, v249, v251\n\t" \
    "v_cvt_pk_bf16_f32 v248, v248, v249\n\t" \
    "v_smfmac_f32_16x16x64_bf16 v[208:211], v[240:243], v[176:183], v212\n\t" \
    "ds_write_b16 v213, v248 offset:1024\n\t" \
    "ds_write_b16_d16_hi v213, v248 offset:1040\n\t" \
    "s_nop 5\n\t" \
    "v_add_f32 v208, v208, v210\n\t" \
    "v_add_f32 v209, v209, v211\n\t" \
    "v_cvt_pk_bf16_f32 v208, v208, v209\n\t" \
    "ds_write_b16 v213, v208 offset:0\n\t" \
    "ds_write_b16_d16_hi v213, v208 offset:16\n\t" \
    "s_setprio 0\n\t" \
    "s_waitcnt lgkmcnt(0)\n\t" \
    "s_barrier \n\t" \
    "ds_read_b128 v[224:227], v253 offset:4096\n\t" \
    "ds_read_b128 v[244:247], v254 offset:40960\n\t" \
    "ds_read_b128 v[228:231], v253 offset:5120\n\t" \
    "ds_read_b128 v[232:235], v253 offset:24576\n\t" \
    "ds_read_b128 v[236:239], v253 offset:25600\n\t" \
    "ds_read_b128 v[248:251], v254 offset:41984\n\t" \
    "s_add_u32 s40, s40, 1\n\t" \
    "s_cmp_lg_u32 s40, 64\n\t" \
    "s_cbranch_scc1 .Lv17_%=\n\t" \
    "s_branch .Lend%=\n\t" \
    ".Lv0_%=:\n\t" \
    "s_mov_b32 s42, %[xs]\n\t" \
    "v_add_u32 v244, 0xc000, v243\n\t" \
    "s_waitcnt vmcnt(12) lgkmcnt(0)\n\t" \
    "s_barrier \n\t" \
    "s_add_u32 m0, %[wm], 0x9000\n\t" \
    "s_mov_b32 s44, 0x1e0000\n\t" \
    "buffer_load_dwordx4 v240, %[wr], s44 offen offset:0 nt lds\n\t" \
    "buffer_load_dwordx4 v241, %[wr], s44 offen offset:1024 nt lds\n\t" \
    "buffer_load_dwordx4 v242, %[wr], s44 offen offset:2048 nt lds\n\t" \
    "ds_read_b64_tr_b16 v[200:201], v244 offset:0\n\t" \
    "ds_read_b64_tr_b16 v[202:203], v244 offset:1536\n\t" \
    "ds_read_b64_tr_b16 v[0:1], v244 offset:32\n\t" \
    "ds_read_b64_tr_b16 v[2:3], v244 offset:1568\n\t" \
    "ds_read_b64_tr_b16 v[8:9], v244 offset:64\n\t" \
    "ds_read_b64_tr_b16 v[10:11], v244 offset:1600\n\t" \
    "v_add_u32 v244, 0xf000, v243\n\t" \
    "s_waitcnt vmcnt(12) lgkmcnt(0)\n\t" \
    "s_barrier \n\t" \
    "s_add_u32 m0, %[wm], 0xc000\n\t" \
    "s_mov_b32 s44, 0x240000\n\t" \
    "buffer_load_dwordx4 v240, %[wr], s44 offen offset:0 nt lds\n\t" \
    "buffer_load_dwordx4 v241, %[wr], s44 offen offset:1024 nt lds\n\t" \
    "buffer_load_dwordx4 v242, %[wr], s44 offen offset:2048 nt lds\n\t" \
    "ds_read_b64_tr_b16 v[204:205], v244 offset:0\n\t" \
    "ds_read_b64_tr_b16 v[206:207], v244 offset:1536\n\t" \
    "ds_read_b64_tr_b16 v[4:5], v244 offset:32\n\t" \
    "ds_read_b64_tr_b16 v[6:7], v244 offset:1568\n\t" \
    "ds_read_b64_tr_b16 v[12:13], v244 offset:64\n\t" \
    "ds_read_b64_tr_b16 v[14:15], v244 offset:1600\n\t" \
    "v_add_u32 v244, 0x0, v243\n\t" \
    "s_waitcnt vmcnt(12) lgkmcnt(0)\n\t" \
    "s_barrier \n\t" \
    "s_add_u32 m0, %[wm], 0xf000\n\t" \
    "s_mov_b32 s44, 0x2a0000\n\t" \
    "buffer_load_dwordx4 v240, %[wr], s44 offen offset:0 nt lds\n\t" \
    "buffer_load_dwordx4 v241, %[wr], s44 offen offset:1024 nt lds\n\t" \
    "buffer_load_dwordx4 v242, %[wr], s44 offen offset:2048 nt lds\n\t" \
    "ds_read_b64_tr_b16 v[208:209], v244 offset:0\n\t" \
    "ds_read_b64_tr_b16 v[210:211], v244 offset:1536\n\t" \
    "ds_read_b64_tr_b16 v[40:41], v244 offset:32\n\t" \
    "ds_read_b64_tr_b16 v[42:43], v244 offset:1568\n\t" \
    "ds_read_b64_tr_b16 v[48:49], v244 offset:64\n\t" \
    "ds_read_b64_tr_b16 v[50:51], v244 offset:1600\n\t" \
    "v_add_u32 v244, 0x3000, v243\n\t" \
    "s_waitcnt vmcnt(12) lgkmcnt(0)\n\t" \
    "s_barrier \n\t" \
    "s_add_u32 m0, %[wm], 0x0\n\t" \
    "s_mov_b32 s44, 0x300000\n\t" \
    "buffer_load_dwordx4 v240, %[wr], s44 offen offset:0 nt lds\n\t" \
    "buffer_load_dwordx4 v241, %[wr], s44 offen offset:1024 nt lds\n\t" \
    "buffer_load_dwordx4 v242, %[wr], s44 offen offset:2048 nt lds\n\t" \
    "ds_read_b64_tr_b16 v[212:213], v244 offset:0\n\t" \
    "ds_read_b64_tr_b16 v[214:215], v244 offset:1536\n\t" \
    "ds_read_b64_tr_b16 v[44:45], v244 offset:32\n\t" \
    "ds_read_b64_tr_b16 v[46:47], v244 offset:1568\n\t" \
    "ds_read_b64_tr_b16 v[52:53], v244 offset:64\n\t" \
    "ds_read_b64_tr_b16 v[54:55], v244 offset:1600\n\t" \
    "v_add_u32 v244, 0x6000, v243\n\t" \
    "s_waitcnt vmcnt(12) lgkmcnt(0)\n\t" \
    "s_barrier \n\t" \
    "s_add_u32 m0, %[wm], 0x3000\n\t" \
    "s_mov_b32 s44, 0x360000\n\t" \
    "buffer_load_dwordx4 v240, %[wr], s44 offen offset:0 nt lds\n\t" \
    "buffer_load_dwordx4 v241, %[wr], s44 offen offset:1024 nt lds\n\t" \
    "buffer_load_dwordx4 v242, %[wr], s44 offen offset:2048 nt lds\n\t" \
    "ds_read_b64_tr_b16 v[216:217], v244 offset:0\n\t" \
    "ds_read_b64_tr_b16 v[218:219], v244 offset:1536\n\t" \
    "ds_read_b64_tr_b16 v[80:81], v244 offset:32\n\t" \
    "ds_read_b64_tr_b16 v[82:83], v244 offset:1568\n\t" \
    "ds_read_b64_tr_b16 v[88:89], v244 offset:64\n\t" \
    "ds_read_b64_tr_b16 v[90:91], v244 offset:1600\n\t" \
    "v_add_u32 v244, 0x9000, v243\n\t" \
    "s_waitcnt vmcnt(12) lgkmcnt(0)\n\t" \
    "s_barrier \n\t" \
    "s_add_u32 m0, %[wm], 0x6000\n\t" \
    "s_mov_b32 s44, 0x3c0000\n\t" \
    "buffer_load_dwordx4 v240, %[wr], s44 offen offset:0 nt lds\n\t" \
    "buffer_load_dwordx4 v241, %[wr], s44 offen offset:1024 nt lds\n\t" \
    "buffer_load_dwordx4 v242, %[wr], s44 offen offset:2048 nt lds\n\t" \
    "ds_read_b64_tr_b16 v[220:221], v244 offset:0\n\t" \
    "ds_read_b64_tr_b16 v[222:223], v244 offset:1536\n\t" \
    "ds_read_b64_tr_b16 v[84:85], v244 offset:32\n\t" \
    "ds_read_b64_tr_b16 v[86:87], v244 offset:1568\n\t" \
    "ds_read_b64_tr_b16 v[92:93], v244 offset:64\n\t" \
    "ds_read_b64_tr_b16 v[94:95], v244 offset:1600\n\t" \
    "v_add_u32 v244, 0xc000, v243\n\t" \
    "s_waitcnt vmcnt(12) lgkmcnt(0)\n\t" \
    "s_barrier \n\t" \
    "s_add_u32 m0, %[wm], 0x9000\n\t" \
    "s_mov_b32 s44, 0x420000\n\t" \
    "buffer_load_dwordx4 v240, %[wr], s44 offen offset:0 nt lds\n\t" \
    "buffer_load_dwordx4 v241, %[wr], s44 offen offset:1024 nt lds\n\t" \
    "buffer_load_dwordx4 v242, %[wr], s44 offen offset:2048 nt lds\n\t" \
    "ds_read_b64_tr_b16 v[224:225], v244 offset:0\n\t" \
    "ds_read_b64_tr_b16 v[226:227], v244 offset:1536\n\t" \
    "ds_read_b64_tr_b16 v[120:121], v244 offset:32\n\t" \
    "ds_read_b64_tr_b16 v[122:123], v244 offset:1568\n\t" \
    "ds_read_b64_tr_b16 v[128:129], v244 offset:64\n\t" \
    "ds_read_b64_tr_b16 v[130:131], v244 offset:1600\n\t" \
    "v_add_u32 v244, 0xf000, v243\n\t" \
    "s_waitcnt vmcnt(12) lgkmcnt(0)\n\t" \
    "s_barrier \n\t" \
    "s_add_u32 m0, %[wm], 0xc000\n\t" \
    "s_mov_b32 s44, 0x480000\n\t" \
    "buffer_load_dwordx4 v240, %[wr], s44 offen offset:0 nt lds\n\t" \
    "buffer_load_dwordx4 v241, %[wr], s44 offen offset:1024 nt lds\n\t" \
    "buffer_load_dwordx4 v242, %[wr], s44 offen offset:2048 nt lds\n\t" \
    "ds_read_b64_tr_b16 v[228:229], v244 offset:0\n\t" \
    "ds_read_b64_tr_b16 v[230:231], v244 offset:1536\n\t" \
    "ds_read_b64_tr_b16 v[124:125], v244 offset:32\n\t" \
    "ds_read_b64_tr_b16 v[126:127], v244 offset:1568\n\t" \
    "ds_read_b64_tr_b16 v[132:133], v244 offset:64\n\t" \
    "ds_read_b64_tr_b16 v[134:135], v244 offset:1600\n\t" \
    "v_add_u32 v244, 0x0, v243\n\t" \
    "s_waitcnt vmcnt(12) lgkmcnt(0)\n\t" \
    "s_barrier \n\t" \
    "v_mov_b32 v246, 0\n\t" \
    "buffer_load_dword v246, v246, s[60:63], %[mo] offen sc1\n\t" \
    "s_add_u32 m0, %[wm], 0xf000\n\t" \
    "s_mov_b32 s44, 0x4e0000\n\t" \
    "buffer_load_dwordx4 v240, %[wr], s44 offen offset:0 nt lds\n\t" \
    "buffer_load_dwordx4 v241, %[wr], s44 offen offset:1024 nt lds\n\t" \
    "buffer_load_dwordx4 v242, %[wr], s44 offen offset:2048 nt lds\n\t" \
    "ds_read_b64_tr_b16 v[232:233], v244 offset:0\n\t" \
    "ds_read_b64_tr_b16 v[234:235], v244 offset:1536\n\t" \
    "ds_read_b64_tr_b16 v[160:161], v244 offset:32\n\t" \
    "ds_read_b64_tr_b16 v[162:163], v244 offset:1568\n\t" \
    "ds_read_b64_tr_b16 v[168:169], v244 offset:64\n\t" \
    "ds_read_b64_tr_b16 v[170:171], v244 offset:1600\n\t" \
    "v_add_u32 v244, 0x3000, v243\n\t" \
    "s_waitcnt vmcnt(12) lgkmcnt(0)\n\t" \
    "s_barrier \n\t" \
    "s_add_u32 m0, %[wm], 0x0\n\t" \
    "s_mov_b32 s44, 0x540000\n\t" \
    "buffer_load_dwordx4 v240, %[wr], s44 offen offset:0 nt lds\n\t" \
    "buffer_load_dwordx4 v241, %[wr], s44 offen offset:1024 nt lds\n\t" \
    "buffer_load_dwordx4 v242, %[wr], s44 offen offset:2048 nt lds\n\t" \
    "ds_read_b64_tr_b16 v[236:237], v244 offset:0\n\t" \
    "ds_read_b64_tr_b16 v[238:239], v244 offset:1536\n\t" \
    "ds_read_b64_tr_b16 v[164:165], v244 offset:32\n\t" \
    "ds_read_b64_tr_b16 v[166:167], v244 offset:1568\n\t" \
    "ds_read_b64_tr_b16 v[172:173], v244 offset:64\n\t" \
    "ds_read_b64_tr_b16 v[174:175], v244 offset:1600\n\t" \
    "v_add_u32 v244, 0x6000, v243\n\t" \
    "s_waitcnt vmcnt(12) lgkmcnt(0)\n\t" \
    "s_barrier \n\t" \
    "s_add_u32 m0, %[wm], 0x3000\n\t" \
    "s_mov_b32 s44, 0x5a0000\n\t" \
    "buffer_load_dwordx4 v240, %[wr], s44 offen offset:0 nt lds\n\t" \
    "buffer_load_dwordx4 v241, %[wr], s44 offen offset:1024 nt lds\n\t" \
    "buffer_load_dwordx4 v242, %[wr], s44 offen offset:2048 nt lds\n\t" \
    "ds_read_b64_tr_b16 v[16:17], v244 offset:0\n\t" \
    "ds_read_b64_tr_b16 v[18:19], v244 offset:1536\n\t" \
    "ds_read_b64_tr_b16 v[24:25], v244 offset:32\n\t" \
    "ds_read_b64_tr_b16 v[26:27], v244 offset:1568\n\t" \
    "ds_read_b64_tr_b16 v[32:33], v244 offset:64\n\t" \
    "ds_read_b64_tr_b16 v[34:35], v244 offset:1600\n\t" \
    "v_add_u32 v244, 0x9000, v243\n\t" \
    "s_waitcnt vmcnt(12) lgkmcnt(0)\n\t" \
    "s_barrier \n\t" \
    "s_add_u32 m0, %[wm], 0x6000\n\t" \
    "s_mov_b32 s44, 0x600000\n\t" \
    "buffer_load_dwordx4 v240, %[wr], s44 offen offset:0 nt lds\n\t" \
    "buffer_load_dwordx4 v241, %[wr], s44 offen offset:1024 nt lds\n\t" \
    "buffer_load_dwordx4 v242, %[wr], s44 offen offset:2048 nt lds\n\t" \
    "ds_read_b64_tr_b16 v[20:21], v244 offset:0\n\t" \
    "ds_read_b64_tr_b16 v[22:23], v244 offset:1536\n\t" \
    "ds_read_b64_tr_b16 v[28:29], v244 offset:32\n\t" \
    "ds_read_b64_tr_b16 v[30:31], v244 offset:1568\n\t" \
    "ds_read_b64_tr_b16 v[36:37], v244 offset:64\n\t" \
    "ds_read_b64_tr_b16 v[38:39], v244 offset:1600\n\t" \
    "v_add_u32 v244, 0xc000, v243\n\t" \
    "s_waitcnt vmcnt(12) lgkmcnt(0)\n\t" \
    "s_barrier \n\t" \
    "s_add_u32 m0, %[wm], 0x9000\n\t" \
    "s_mov_b32 s44, 0x660000\n\t" \
    "buffer_load_dwordx4 v240, %[wr], s44 offen offset:0 nt lds\n\t" \
    "buffer_load_dwordx4 v241, %[wr], s44 offen offset:1024 nt lds\n\t" \
    "buffer_load_dwordx4 v242, %[wr], s44 offen offset:2048 nt lds\n\t" \
    "ds_read_b64_tr_b16 v[56:57], v244 offset:0\n\t" \
    "ds_read_b64_tr_b16 v[58:59], v244 offset:1536\n\t" \
    "ds_read_b64_tr_b16 v[64:65], v244 offset:32\n\t" \
    "ds_read_b64_tr_b16 v[66:67], v244 offset:1568\n\t" \
    "ds_read_b64_tr_b16 v[72:73], v244 offset:64\n\t" \
    "ds_read_b64_tr_b16 v[74:75], v244 offset:1600\n\t" \
    "v_add_u32 v244, 0xf000, v243\n\t" \
    "s_waitcnt vmcnt(12) lgkmcnt(0)\n\t" \
    "s_barrier \n\t" \
    ".Lv01_%=:\n\t" \
    "v_readfirstlane_b32 s43, v246\n\t" \
    "s_nop 0\n\t" \
    "s_cmp_lg_u32 s43, -1\n\t" \
    "s_cbranch_scc1 .Lv02_%=\n\t" \
    "s_cmp_eq_u32 s41, 0\n\t" \
    "s_cbranch_scc1 .Lv02_%=\n\t" \
    "s_sub_u32 s41, s41, 1\n\t" \
    "s_sleep 4\n\t" \
    "v_mov_b32 v246, 0\n\t" \
    "buffer_load_dword v246, v246, s[60:63], %[mo] offen sc1\n\t" \
    "s_waitcnt vmcnt(0)\n\t" \
    "s_branch .Lv01_%=\n\t" \
    ".Lv02_%=:\n\t" \
    "v_mov_b32 v246, 0x8000\n\t" \
    "s_mov_b64 exec, 1\n\t" \
    "buffer_store_dword v246, v246, s[60:63], %[mo] offen sc1\n\t" \
    "s_mov_b64 exec, -1\n\t" \
    "s_and_b32 s44, s43, 0x5000\n\t" \
    "s_xor_b32 %[mb], %[mb], s44\n\t" \
    "s_xor_b32 %[fl], %[fl], s44\n\t" \
    "s_and_b32 s43, s43, 0x2000\n\t" \
    "s_xor_b32 s43, s43, %[lx]\n\t" \
    "s_add_u32 s60, s60, s43\n\t" \
    "s_addc_u32 s61, s61, 0\n\t" \
    "v_and_b32 v252, 0xf0, v253\n\t" \
    "v_lshlrev_b32 v252, 3, v252\n\t" \
    "buffer_load_dword v252, v252, s[60:63], %[fl] offen sc1\n\t" \
    "s_add_u32 m0, %[wm], 0xc000\n\t" \
    "s_mov_b32 s44, 0x6c0000\n\t" \
    "buffer_load_dwordx4 v240, %[wr], s44 offen offset:0 nt lds\n\t" \
    "buffer_load_dwordx4 v241, %[wr], s44 offen offset:1024 nt lds\n\t" \
    "buffer_load_dwordx4 v242, %[wr], s44 offen offset:2048 nt lds\n\t" \
    "ds_read_b64_tr_b16 v[60:61], v244 offset:0\n\t" \
    "ds_read_b64_tr_b16 v[62:63], v244 offset:1536\n\t" \
    "ds_read_b64_tr_b16 v[68:69], v244 offset:32\n\t" \
    "ds_read_b64_tr_b16 v[70:71], v244 offset:1568\n\t" \
    "ds_read_b64_tr_b16 v[76:77], v244 offset:64\n\t" \
    "ds_read_b64_tr_b16 v[78:79], v244 offset:1600\n\t" \
    "v_add_u32 v244, 0x0, v243\n\t" \
    "s_waitcnt vmcnt(12) lgkmcnt(0)\n\t" \
    "s_barrier \n\t" \
    "s_add_u32 m0, %[wm], 0xf000\n\t" \
    "s_mov_b32 s44, 0x720000\n\t" \
    "buffer_load_dwordx4 v240, %[wr], s44 offen offset:0 nt lds\n\t" \
    "buffer_load_dwordx4 v241, %[wr], s44 offen offset:1024 nt lds\n\t" \
    "buffer_load_dwordx4 v242, %[wr], s44 offen offset:2048 nt lds\n\t" \
    "ds_read_b64_tr_b16 v[96:97], v244 offset:0\n\t" \
    "ds_read_b64_tr_b16 v[98:99], v244 offset:1536\n\t" \
    "ds_read_b64_tr_b16 v[104:105], v244 offset:32\n\t" \
    "ds_read_b64_tr_b16 v[106:107], v244 offset:1568\n\t" \
    "ds_read_b64_tr_b16 v[112:113], v244 offset:64\n\t" \
    "ds_read_b64_tr_b16 v[114:115], v244 offset:1600\n\t" \
    "v_add_u32 v244, 0x3000, v243\n\t" \
    "s_waitcnt vmcnt(12) lgkmcnt(0)\n\t" \
    "s_barrier \n\t" \
    "ds_read_b64_tr_b16 v[100:101], v244 offset:0\n\t" \
    "ds_read_b64_tr_b16 v[102:103], v244 offset:1536\n\t" \
    "ds_read_b64_tr_b16 v[108:109], v244 offset:32\n\t" \
    "ds_read_b64_tr_b16 v[110:111], v244 offset:1568\n\t" \
    "ds_read_b64_tr_b16 v[116:117], v244 offset:64\n\t" \
    "ds_read_b64_tr_b16 v[118:119], v244 offset:1600\n\t" \
    "v_add_u32 v244, 0x6000, v243\n\t" \
    "s_waitcnt vmcnt(9) lgkmcnt(0)\n\t" \
    "s_barrier \n\t" \
    "ds_read_b64_tr_b16 v[136:137], v244 offset:0\n\t" \
    "ds_read_b64_tr_b16 v[138:139], v244 offset:1536\n\t" \
    "ds_read_b64_tr_b16 v[144:145], v244 offset:32\n\t" \
    "ds_read_b64_tr_b16 v[146:147], v244 offset:1568\n\t" \
    "ds_read_b64_tr_b16 v[152:153], v244 offset:64\n\t" \
    "ds_read_b64_tr_b16 v[154:155], v244 offset:1600\n\t" \
    "v_add_u32 v244, 0x9000, v243\n\t" \
    "s_waitcnt vmcnt(6) lgkmcnt(0)\n\t" \
    "s_barrier \n\t" \
    "ds_read_b64_tr_b16 v[140:141], v244 offset:0\n\t" \
    "ds_read_b64_tr_b16 v[142:143], v244 offset:1536\n\t" \
    "ds_read_b64_tr_b16 v[148:149], v244 offset:32\n\t" \
    "ds_read_b64_tr_b16 v[150:151], v244 offset:1568\n\t" \
    "ds_read_b64_tr_b16 v[156:157], v244 offset:64\n\t" \
    "ds_read_b64_tr_b16 v[158:159], v244 offset:1600\n\t" \
    "v_add_u32 v244, 0xc000, v243\n\t" \
    "s_waitcnt vmcnt(3) lgkmcnt(0)\n\t" \
    "s_barrier \n\t" \
    "ds_read_b64_tr_b16 v[176:177], v244 offset:0\n\t" \
    "ds_read_b64_tr_b16 v[178:179], v244 offset:1536\n\t" \
    "ds_read_b64_tr_b16 v[184:185], v244 offset:32\n\t" \
    "ds_read_b64_tr_b16 v[186:187], v244 offset:1568\n\t" \
    "ds_read_b64_tr_b16 v[192:193], v244 offset:64\n\t" \
    "ds_read_b64_tr_b16 v[194:195], v244 offset:1600\n\t" \
    "s_mov_b32 s43, 0\n\t" \
    "v_cmp_eq_u32 vcc, 0, v252\n\t" \
    "s_cbranch_vccnz .Lv03_%=\n\t" \
    "s_mov_b32 m0, 0x0\n\t" \
    "s_add_u32 s49, s42, 0x0\n\t" \
    "buffer_load_dwordx4 v253, s[60:63], s49 offen offset:0 sc1 lds\n\t" \
    "buffer_load_dwordx4 v253, s[60:63], s49 offen offset:1024 sc1 lds\n\t" \
    "buffer_load_dwordx4 v253, s[60:63], s49 offen offset:2048 sc1 lds\n\t" \
    "buffer_load_dwordx4 v253, s[60:63], s49 offen offset:3072 sc1 lds\n\t" \
    "s_mov_b32 m0, 0x4000\n\t" \
    "s_add_u32 s49, s42, 0x10000\n\t" \
    "buffer_load_dwordx4 v253, s[60:63], s49 offen offset:0 sc1 lds\n\t" \
    "s_mov_b32 m0, 0x5000\n\t" \
    "s_add_u32 s49, s42, 0x1000\n\t" \
    "buffer_load_dwordx4 v253, s[60:63], s49 offen offset:0 sc1 lds\n\t" \
    "buffer_load_dwordx4 v253, s[60:63], s49 offen offset:1024 sc1 lds\n\t" \
    "buffer_load_dwordx4 v253, s[60:63], s49 offen offset:2048 sc1 lds\n\t" \
    "buffer_load_dwordx4 v253, s[60:63], s49 offen offset:3072 sc1 lds\n\t" \
    "s_mov_b32 m0, 0x9000\n\t" \
    "s_add_u32 s49, s42, 0x11000\n\t" \
    "buffer_load_dwordx4 v253, s[60:63], s49 offen offset:0 sc1 lds\n\t" \
    "s_mov_b32 s43, 1\n\t" \
    ".Lv03_%=:\n\t" \
    "v_add_u32 v244, 0xf000, v243\n\t" \
    "s_waitcnt vmcnt(0) lgkmcnt(0)\n\t" \
    "s_barrier \n\t" \
    "ds_read_b64_tr_b16 v[180:181], v244 offset:0\n\t" \
    "ds_read_b64_tr_b16 v[182:183], v244 offset:1536\n\t" \
    "ds_read_b64_tr_b16 v[188:189], v244 offset:32\n\t" \
    "ds_read_b64_tr_b16 v[190:191], v244 offset:1568\n\t" \
    "ds_read_b64_tr_b16 v[196:197], v244 offset:64\n\t" \
    "ds_read_b64_tr_b16 v[198:199], v244 offset:1600\n\t" \
    "s_waitcnt lgkmcnt(0)\n\t" \
    "s_barrier \n\t" \
    "ds_write_b128 v254, v[200:203] offset:30720\n\t" \
    "ds_write_b128 v254, v[204:207] offset:31744\n\t" \
    "ds_write_b128 v254, v[208:211] offset:32768\n\t" \
    "ds_write_b128 v254, v[212:215] offset:33792\n\t" \
    "ds_write_b128 v254, v[216:219] offset:34816\n\t" \
    "ds_write_b128 v254, v[220:223] offset:35840\n\t" \
    "ds_write_b128 v254, v[224:227] offset:36864\n\t" \
    "ds_write_b128 v254, v[228:231] offset:37888\n\t" \
    "ds_write_b128 v254, v[232:235] offset:38912\n\t" \
    "ds_write_b128 v254, v[236:239] offset:39936\n\t" \
    "s_cmp_lg_u32 s43, 0\n\t" \
    "s_cbranch_scc1 .Lv04_%=\n\t" \
    "s_mov_b32 s44, 0\n\t" \
    ".Lv05_%=:\n\t" \
    "v_cmp_ge_u32 vcc, s44, v252\n\t" \
    "s_cbranch_vccz .Lv06_%=\n\t" \
    "s_cmp_eq_u32 s41, 0\n\t" \
    "s_cbranch_scc1 .Lv06_%=\n\t" \
    "s_sub_u32 s41, s41, 1\n\t" \
    "s_sleep 8\n\t" \
    "v_and_b32 v252, 0xf0, v253\n\t" \
    "v_lshlrev_b32 v252, 3, v252\n\t" \
    "buffer_load_dword v252, v252, s[60:63], %[fl] offen sc1\n\t" \
    "s_waitcnt vmcnt(0)\n\t" \
    "s_branch .Lv05_%=\n\t" \
    ".Lv06_%=:\n\t" \
    "s_mov_b32 m0, 0x0\n\t" \
    "s_add_u32 s49, s42, 0x0\n\t" \
    "buffer_load_dwordx4 v253, s[60:63], s49 offen offset:0 sc1 lds\n\t" \
    "buffer_load_dwordx4 v253, s[60:63], s49 offen offset:1024 sc1 lds\n\t" \
    "buffer_load_dwordx4 v253, s[60:63], s49 offen offset:2048 sc1 lds\n\t" \
    "buffer_load_dwordx4 v253, s[60:63], s49 offen offset:3072 sc1 lds\n\t" \
    "s_mov_b32 m0, 0x4000\n\t" \
    "s_add_u32 s49, s42, 0x10000\n\t" \
    "buffer_load_dwordx4 v253, s[60:63], s49 offen offset:0 sc1 lds\n\t" \
    "s_mov_b32 m0, 0x5000\n\t" \
    "s_add_u32 s49, s42, 0x1000\n\t" \
    "buffer_load_dwordx4 v253, s[60:63], s49 offen offset:0 sc1 lds\n\t" \
    "buffer_load_dwordx4 v253, s[60:63], s49 offen offset:1024 sc1 lds\n\t" \
    "buffer_load_dwordx4 v253, s[60:63], s49 offen offset:2048 sc1 lds\n\t" \
    "buffer_load_dwordx4 v253, s[60:63], s49 offen offset:3072 sc1 lds\n\t" \
    "s_mov_b32 m0, 0x9000\n\t" \
    "s_add_u32 s49, s42, 0x11000\n\t" \
    "buffer_load_dwordx4 v253, s[60:63], s49 offen offset:0 sc1 lds\n\t" \
    ".Lv04_%=:\n\t" \
    "s_waitcnt vmcnt(0) lgkmcnt(0)\n\t" \
    "s_barrier \n\t" \
    "ds_read_b128 v[224:227], v253 offset:0\n\t" \
    "ds_read_b128 v[244:247], v254 offset:30720\n\t" \
    "ds_read_b128 v[228:231], v253 offset:1024\n\t" \
    "ds_read_b128 v[232:235], v253 offset:20480\n\t" \
    "ds_read_b128 v[236:239], v253 offset:21504\n\t" \
    "ds_read_b128 v[248:251], v254 offset:31744\n\t" \
    ".Lv07_%=:\n\t" \
    "s_waitcnt lgkmcnt(5)\n\t" \
    "v_mfma_f32_16x16x32_bf16 v[204:207], v[0:3], v[224:227], 0\n\t" \
    "s_waitcnt lgkmcnt(3)\n\t" \
    "v_mfma_f32_16x16x32_bf16 v[204:207], v[4:7], v[228:231], v[204:207]\n\t" \
    "ds_read_b128 v[240:243], v253 offset:10240\n\t" \
    "v_mfma_f32_16x16x32_bf16 v[208:211], v[8:11], v[224:227], 0\n\t" \
    "v_mfma_f32_16x16x32_bf16 v[208:211], v[12:15], v[228:231], v[208:211]\n\t" \
    "v_mfma_f32_16x16x32_bf16 v[200:203], v[244:247], v[224:227], 0\n\t" \
    "s_waitcnt lgkmcnt(1)\n\t" \
    "v_mfma_f32_16x16x32_bf16 v[200:203], v[248:251], v[228:231], v[200:203]\n\t" \
    "v_mfma_f32_16x16x32_bf16 v[216:219], v[0:3], v[232:235], 0\n\t" \
    "ds_read_b128 v[224:227], v253 offset:11264\n\t" \
    "v_mfma_f32_16x16x32_bf16 v[216:219], v[4:7], v[236:239], v[216:219]\n\t" \
    "ds_read_b128 v[228:231], v253 offset:2048\n\t" \
    "v_mfma_f32_16x16x32_bf16 v[220:223], v[8:11], v[232:235], 0\n\t" \
    "v_mfma_f32_16x16x32_bf16 v[220:223], v[12:15], v[236:239], v[220:223]\n\t" \
    "v_mfma_f32_16x16x32_bf16 v[212:215], v[244:247], v[232:235], 0\n\t" \
    "ds_read_b128 v[244:247], v254 offset:32768\n\t" \
    "v_mfma_f32_16x16x32_bf16 v[212:215], v[248:251], v[236:239], v[212:215]\n\t" \
    "ds_read_b128 v[248:251], v254 offset:33792\n\t" \
    "s_waitcnt lgkmcnt(4)\n\t" \
    "v_mfma_f32_16x16x32_bf16 v[204:207], v[24:27], v[240:243], v[204:207]\n\t" \
    "ds_read_b128 v[232:235], v253 offset:3072\n\t" \
    "s_waitcnt lgkmcnt(4)\n\t" \
    "v_mfma_f32_16x16x32_bf16 v[204:207], v[28:31], v[224:227], v[204:207]\n\t" \
    "ds_read_b128 v[236:239], v253 offset:22528\n\t" \
    "v_mfma_f32_16x16x32_bf16 v[208:211], v[32:35], v[240:243], v[208:211]\n\t" \
    "v_mfma_f32_16x16x32_bf16 v[208:211], v[36:39], v[224:227], v[208:211]\n\t" \
    "v_mfma_f32_16x16x32_bf16 v[200:203], v[16:19], v[240:243], v[200:203]\n\t" \
    "v_mfma_f32_16x16x32_bf16 v[200:203], v[20:23], v[224:227], v[200:203]\n\t" \
    "s_waitcnt lgkmcnt(4)\n\t" \
    "v_mfma_f32_16x16x32_bf16 v[204:207], v[40:43], v[228:231], v[204:207]\n\t" \
    "ds_read_b128 v[240:243], v253 offset:23552\n\t" \
    "s_waitcnt lgkmcnt(2)\n\t" \
    "v_mfma_f32_16x16x32_bf16 v[204:207], v[44:47], v[232:235], v[204:207]\n\t" \
    "ds_read_b128 v[224:227], v253 offset:12288\n\t" \
    "v_mfma_f32_16x16x32_bf16 v[208:211], v[48:51], v[228:231], v[208:211]\n\t" \
    "v_mfma_f32_16x16x32_bf16 v[208:211], v[52:55], v[232:235], v[208:211]\n\t" \
    "v_mfma_f32_16x16x32_bf16 v[200:203], v[244:247], v[228:231], v[200:203]\n\t" \
    "v_mfma_f32_16x16x32_bf16 v[200:203], v[248:251], v[232:235], v[200:203]\n\t" \
    "s_waitcnt lgkmcnt(2)\n\t" \
    "v_mfma_f32_16x16x32_bf16 v[216:219], v[40:43], v[236:239], v[216:219]\n\t" \
    "ds_read_b128 v[228:231], v253 offset:13312\n\t" \
    "s_add_u32 s44, s40, 1\n\t" \
    "s_waitcnt lgkmcnt(2)\n\t" \
    "v_mfma_f32_16x16x32_bf16 v[216:219], v[44:47], v[240:243], v[216:219]\n\t" \
    "ds_read_b128 v[232:235], v253 offset:4096\n\t" \
    "s_and_b32 s45, s44, 15\n\t" \
    "v_mfma_f32_16x16x32_bf16 v[220:223], v[48:51], v[236:239], v[220:223]\n\t" \
    "s_cmp_eq_u32 s45, 0\n\t" \
    "v_mfma_f32_16x16x32_bf16 v[220:223], v[52:55], v[240:243], v[220:223]\n\t" \
    "s_cselect_b32 s45, %[cj], %[cs]\n\t" \
    "v_mfma_f32_16x16x32_bf16 v[212:215], v[244:247], v[236:239], v[212:215]\n\t" \
    "ds_read_b128 v[244:247], v254 offset:34816\n\t" \
    "s_cmp_eq_u32 s44, 64\n\t" \
    "v_mfma_f32_16x16x32_bf16 v[212:215], v[248:251], v[240:243], v[212:215]\n\t" \
    "ds_read_b128 v[248:251], v254 offset:35840\n\t" \
    "s_cselect_b32 s45, 0, s45\n\t" \
    "s_waitcnt lgkmcnt(4)\n\t" \
    "v_mfma_f32_16x16x32_bf16 v[204:207], v[64:67], v[224:227], v[204:207]\n\t" \
    "ds_read_b128 v[236:239], v253 offset:5120\n\t" \
    "s_add_u32 s42, s42, s45\n\t" \
    "s_waitcnt lgkmcnt(4)\n\t" \
    "v_mfma_f32_16x16x32_bf16 v[204:207], v[68:71], v[228:231], v[204:207]\n\t" \
    "ds_read_b128 v[240:243], v253 offset:24576\n\t" \
    "v_mfma_f32_16x16x32_bf16 v[208:211], v[72:75], v[224:227], v[208:211]\n\t" \
    "v_mfma_f32_16x16x32_bf16 v[208:211], v[76:79], v[228:231], v[208:211]\n\t" \
    "v_mfma_f32_16x16x32_bf16 v[200:203], v[56:59], v[224:227], v[200:203]\n\t" \
    "v_mfma_f32_16x16x32_bf16 v[200:203], v[60:63], v[228:231], v[200:203]\n\t" \
    "s_waitcnt lgkmcnt(4)\n\t" \
    "v_mfma_f32_16x16x32_bf16 v[204:207], v[80:83], v[232:235], v[204:207]\n\t" \
    "ds_read_b128 v[224:227], v253 offset:25600\n\t" \
    "s_waitcnt lgkmcnt(2)\n\t" \
    "v_mfma_f32_16x16x32_bf16 v[204:207], v[84:87], v[236:239], v[204:207]\n\t" \
    "ds_read_b128 v[228:231], v253 offset:14336\n\t" \
    "v_mfma_f32_16x16x32_bf16 v[208:211], v[88:91], v[232:235], v[208:211]\n\t" \
    "v_mfma_f32_16x16x32_bf16 v[208:211], v[92:95], v[236:239], v[208:211]\n\t" \
    "v_mfma_f32_16x16x32_bf16 v[200:203], v[244:247], v[232:235], v[200:203]\n\t" \
    "v_mfma_f32_16x16x32_bf16 v[200:203], v[248:251], v[236:239], v[200:203]\n\t" \
    "s_waitcnt lgkmcnt(2)\n\t" \
    "v_mfma_f32_16x16x32_bf16 v[216:219], v[80:83], v[240:243], v[216:219]\n\t" \
    "ds_read_b128 v[232:235], v253 offset:15360\n\t" \
    "s_and_b32 s44, s40, 3\n\t" \
    "s_waitcnt lgkmcnt(2)\n\t" \
    "v_mfma_f32_16x16x32_bf16 v[216:219], v[84:87], v[224:227], v[216:219]\n\t" \
    "ds_read_b128 v[236:239], v253 offset:6144\n\t" \
    "s_lshl_b32 s44, s44, 22\n\t" \
    "v_mfma_f32_16x16x32_bf16 v[220:223], v[88:91], v[240:243], v[220:223]\n\t" \
    "s_add_u32 s44, s44, %[mb]\n\t" \
    "v_mfma_f32_16x16x32_bf16 v[220:223], v[92:95], v[224:227], v[220:223]\n\t" \
    "s_add_u32 s46, s44, 0x0\n\t" \
    "v_mfma_f32_16x16x32_bf16 v[212:215], v[244:247], v[240:243], v[212:215]\n\t" \
    "ds_read_b128 v[244:247], v254 offset:36864\n\t" \
    "s_add_u32 s47, s44, 0x8400\n\t" \
    "v_mfma_f32_16x16x32_bf16 v[212:215], v[248:251], v[224:227], v[212:215]\n\t" \
    "ds_read_b128 v[248:251], v254 offset:37888\n\t" \
    "s_add_u32 s48, s44, 0x400\n\t" \
    "s_waitcnt lgkmcnt(4)\n\t" \
    "v_mfma_f32_16x16x32_bf16 v[204:207], v[104:107], v[228:231], v[204:207]\n\t" \
    "ds_read_b128 v[240:243], v253 offset:7168\n\t" \
    "s_waitcnt lgkmcnt(4)\n\t" \
    "v_mfma_f32_16x16x32_bf16 v[204:207], v[108:111], v[232:235], v[204:207]\n\t" \
    "ds_read_b128 v[224:227], v253 offset:26624\n\t" \
    "v_mfma_f32_16x16x32_bf16 v[208:211], v[112:115], v[228:231], v[208:211]\n\t" \
    "v_mfma_f32_16x16x32_bf16 v[208:211], v[116:119], v[232:235], v[208:211]\n\t" \
    "v_mfma_f32_16x16x32_bf16 v[200:203], v[96:99], v[228:231], v[200:203]\n\t" \
    "v_mfma_f32_16x16x32_bf16 v[200:203], v[100:103], v[232:235], v[200:203]\n\t" \
    "s_waitcnt lgkmcnt(4)\n\t" \
    "v_mfma_f32_16x16x32_bf16 v[204:207], v[120:123], v[236:239], v[204:207]\n\t" \
    "ds_read_b128 v[228:231], v253 offset:27648\n\t" \
    "s_waitcnt lgkmcnt(2)\n\t" \
    "v_mfma_f32_16x16x32_bf16 v[204:207], v[124:127], v[240:243], v[204:207]\n\t" \
    "ds_read_b128 v[232:235], v253 offset:16384\n\t" \
    "v_mfma_f32_16x16x32_bf16 v[208:211], v[128:131], v[236:239], v[208:211]\n\t" \
    "v_mfma_f32_16x16x32_bf16 v[208:211], v[132:135], v[240:243], v[208:211]\n\t" \
    "v_mfma_f32_16x16x32_bf16 v[200:203], v[244:247], v[236:239], v[200:203]\n\t" \
    "v_mfma_f32_16x16x32_bf16 v[200:203], v[248:251], v[240:243], v[200:203]\n\t" \
    "s_waitcnt lgkmcnt(2)\n\t" \
    "v_mfma_f32_16x16x32_bf16 v[216:219], v[120:123], v[224:227], v[216:219]\n\t" \
    "ds_read_b128 v[236:239], v253 offset:17408\n\t" \
    "s_waitcnt lgkmcnt(2)\n\t" \
    "v_mfma_f32_16x16x32_bf16 v[216:219], v[124:127], v[228:231], v[216:219]\n\t" \
    "ds_read_b128 v[240:243], v253 offset:8192\n\t" \
    "v_mfma_f32_16x16x32_bf16 v[220:223], v[128:131], v[224:227], v[220:223]\n\t" \
    "v_mfma_f32_16x16x32_bf16 v[220:223], v[132:135], v[228:231], v[220:223]\n\t" \
    "v_mfma_f32_16x16x32_bf16 v[212:215], v[244:247], v[224:227], v[212:215]\n\t" \
    "ds_read_b128 v[244:247], v254 offset:38912\n\t" \
    "v_mfma_f32_16x16x32_bf16 v[212:215], v[248:251], v[228:231], v[212:215]\n\t" \
    "ds_read_b128 v[248:251], v254 offset:39936\n\t" \
    "v_and_b32 v252, 0xf0, v253\n\t" \
    "v_lshlrev_b32 v252, 3, v252\n\t" \
    "buffer_load_dword v252, v252, s[60:63], %[fl] offen sc1\n\t" \
    "s_waitcnt lgkmcnt(4)\n\t" \
    "v_mfma_f32_16x16x32_bf16 v[204:207], v[144:147], v[232:235], v[204:207]\n\t" \
    "ds_read_b128 v[224:227], v253 offset:9216\n\t" \
    "s_waitcnt lgkmcnt(4)\n\t" \
    "v_mfma_f32_16x16x32_bf16 v[204:207], v[148:151], v[236:239], v[204:207]\n\t" \
    "ds_read_b128 v[228:231], v253 offset:28672\n\t" \
    "v_mfma_f32_16x16x32_bf16 v[208:211], v[152:155], v[232:235], v[208:211]\n\t" \
    "v_mfma_f32_16x16x32_bf16 v[208:211], v[156:159], v[236:239], v[208:211]\n\t" \
    "v_mfma_f32_16x16x32_bf16 v[200:203], v[136:139], v[232:235], v[200:203]\n\t" \
    "v_mfma_f32_16x16x32_bf16 v[200:203], v[140:143], v[236:239], v[200:203]\n\t" \
    "s_waitcnt lgkmcnt(4)\n\t" \
    "v_mfma_f32_16x16x32_bf16 v[204:207], v[160:163], v[240:243], v[204:207]\n\t" \
    "ds_read_b128 v[232:235], v253 offset:29696\n\t" \
    "s_waitcnt lgkmcnt(2)\n\t" \
    "v_mfma_f32_16x16x32_bf16 v[204:207], v[164:167], v[224:227], v[204:207]\n\t" \
    "ds_read_b128 v[236:239], v253 offset:18432\n\t" \
    "v_mfma_f32_16x16x32_bf16 v[208:211], v[168:171], v[240:243], v[208:211]\n\t" \
    "v_mfma_f32_16x16x32_bf16 v[208:211], v[172:175], v[224:227], v[208:211]\n\t" \
    "v_mfma_f32_16x16x32_bf16 v[200:203], v[244:247], v[240:243], v[200:203]\n\t" \
    "v_mfma_f32_16x16x32_bf16 v[200:203], v[248:251], v[224:227], v[200:203]\n\t" \
    "s_waitcnt lgkmcnt(2)\n\t" \
    "v_mfma_f32_16x16x32_bf16 v[216:219], v[160:163], v[228:231], v[216:219]\n\t" \
    "ds_read_b128 v[240:243], v253 offset:19456\n\t" \
    "s_waitcnt lgkmcnt(2)\n\t" \
    "v_mfma_f32_16x16x32_bf16 v[216:219], v[164:167], v[232:235], v[216:219]\n\t" \
    "ds_read_b128 v[224:227], v253 offset:30720\n\t" \
    "v_mfma_f32_16x16x32_bf16 v[220:223], v[168:171], v[228:231], v[220:223]\n\t" \
    "v_mfma_f32_16x16x32_bf16 v[220:223], v[172:175], v[232:235], v[220:223]\n\t" \
    "v_mfma_f32_16x16x32_bf16 v[212:215], v[244:247], v[228:231], v[212:215]\n\t" \
    "v_mfma_f32_16x16x32_bf16 v[212:215], v[248:251], v[232:235], v[212:215]\n\t" \
    "s_waitcnt lgkmcnt(2)\n\t" \
    "v_mfma_f32_16x16x32_bf16 v[204:207], v[184:187], v[236:239], v[204:207]\n\t" \
    "ds_read_b128 v[228:231], v253 offset:31744\n\t" \
    "s_waitcnt lgkmcnt(2)\n\t" \
    "v_mfma_f32_16x16x32_bf16 v[204:207], v[188:191], v[240:243], v[204:207]\n\t" \
    "ds_read_b128 v[232:235], v253 offset:32768\n\t" \
    "v_mfma_f32_16x16x32_bf16 v[208:211], v[192:195], v[236:239], v[208:211]\n\t" \
    "v_mfma_f32_16x16x32_bf16 v[208:211], v[196:199], v[240:243], v[208:211]\n\t" \
    "v_mfma_f32_16x16x32_bf16 v[200:203], v[176:179], v[236:239], v[200:203]\n\t" \
    "v_mfma_f32_16x16x32_bf16 v[200:203], v[180:183], v[240:243], v[200:203]\n\t" \
    "s_waitcnt lgkmcnt(2)\n\t" \
    "v_mfma_f32_16x16x32_bf16 v[216:219], v[24:27], v[224:227], v[216:219]\n\t" \
    "ds_read_b128 v[236:239], v253 offset:33792\n\t" \
    "s_waitcnt lgkmcnt(2)\n\t" \
    "v_mfma_f32_16x16x32_bf16 v[216:219], v[28:31], v[228:231], v[216:219]\n\t" \
    "ds_read_b128 v[240:243], v253 offset:34816\n\t" \
    "v_mfma_f32_16x16x32_bf16 v[220:223], v[32:35], v[224:227], v[220:223]\n\t" \
    "v_cvt_pk_bf16_f32 v204, v204, v205\n\t" \
    "v_mfma_f32_16x16x32_bf16 v[220:223], v[36:39], v[228:231], v[220:223]\n\t" \
    "v_cvt_pk_bf16_f32 v205, v206, v207\n\t" \
    "v_mfma_f32_16x16x32_bf16 v[212:215], v[16:19], v[224:227], v[212:215]\n\t" \
    "ds_write_b64 v254, v[204:205] offset:7168\n\t" \
    "v_mfma_f32_16x16x32_bf16 v[212:215], v[20:23], v[228:231], v[212:215]\n\t" \
    "v_cvt_pk_bf16_f32 v208, v208, v209\n\t" \
    "s_waitcnt lgkmcnt(3)\n\t" \
    "v_mfma_f32_16x16x32_bf16 v[216:219], v[64:67], v[232:235], v[216:219]\n\t" \
    "ds_read_b128 v[224:227], v253 offset:35840\n\t" \
    "v_cvt_pk_bf16_f32 v209, v210, v211\n\t" \
    "s_waitcnt lgkmcnt(3)\n\t" \
    "v_mfma_f32_16x16x32_bf16 v[216:219], v[68:71], v[236:239], v[216:219]\n\t" \
    "ds_read_b128 v[228:231], v253 offset:36864\n\t" \
    "ds_write_b64 v254, v[208:209] offset:8192\n\t" \
    "v_mfma_f32_16x16x32_bf16 v[220:223], v[72:75], v[232:235], v[220:223]\n\t" \
    "v_cvt_pk_bf16_f32 v200, v200, v201\n\t" \
    "v_mfma_f32_16x16x32_bf16 v[220:223], v[76:79], v[236:239], v[220:223]\n\t" \
    "v_cvt_pk_bf16_f32 v201, v202, v203\n\t" \
    "v_mfma_f32_16x16x32_bf16 v[212:215], v[56:59], v[232:235], v[212:215]\n\t" \
    "ds_write_b64 v254, v[200:201] offset:6144\n\t" \
    "v_mfma_f32_16x16x32_bf16 v[212:215], v[60:63], v[236:239], v[212:215]\n\t" \
    "s_waitcnt lgkmcnt(5)\n\t" \
    "v_mfma_f32_16x16x32_bf16 v[216:219], v[104:107], v[240:243], v[216:219]\n\t" \
    "ds_read_b128 v[232:235], v253 offset:37888\n\t" \
    "s_waitcnt lgkmcnt(4)\n\t" \
    "v_mfma_f32_16x16x32_bf16 v[216:219], v[108:111], v[224:227], v[216:219]\n\t" \
    "ds_read_b128 v[236:239], v253 offset:38912\n\t" \
    "v_mfma_f32_16x16x32_bf16 v[220:223], v[112:115], v[240:243], v[220:223]\n\t" \
    "v_mfma_f32_16x16x32_bf16 v[220:223], v[116:119], v[224:227], v[220:223]\n\t" \
    "v_mfma_f32_16x16x32_bf16 v[212:215], v[96:99], v[240:243], v[212:215]\n\t" \
    "v_mfma_f32_16x16x32_bf16 v[212:215], v[100:103], v[224:227], v[212:215]\n\t" \
    "ds_read_b128 v[240:243], v253 offset:39936\n\t" \
    "s_waitcnt lgkmcnt(3)\n\t" \
    "s_barrier \n\t" \
    "v_mfma_f32_16x16x32_bf16 v[216:219], v[144:147], v[228:231], v[216:219]\n\t" \
    "s_waitcnt lgkmcnt(2)\n\t" \
    "v_mfma_f32_16x16x32_bf16 v[216:219], v[148:151], v[232:235], v[216:219]\n\t" \
    "s_waitcnt lgkmcnt(1)\n\t" \
    "v_mfma_f32_16x16x32_bf16 v[216:219], v[184:187], v[236:239], v[216:219]\n\t" \
    "s_waitcnt lgkmcnt(0)\n\t" \
    "v_mfma_f32_16x16x32_bf16 v[216:219], v[188:191], v[240:243], v[216:219]\n\t" \
    "v_mfma_f32_16x16x32_bf16 v[220:223], v[152:155], v[228:231], v[220:223]\n\t" \
    "v_mfma_f32_16x16x32_bf16 v[220:223], v[156:159], v[232:235], v[220:223]\n\t" \
    "v_mfma_f32_16x16x32_bf16 v[220:223], v[192:195], v[236:239], v[220:223]\n\t" \
    "v_cvt_pk_bf16_f32 v216, v216, v217\n\t" \
    "v_cvt_pk_bf16_f32 v217, v218, v219\n\t" \
    "v_mfma_f32_16x16x32_bf16 v[220:223], v[196:199], v[240:243], v[220:223]\n\t" \
    "ds_write_b64 v254, v[216:217] offset:13312\n\t" \
    "v_mfma_f32_16x16x32_bf16 v[212:215], v[136:139], v[228:231], v[212:215]\n\t" \
    "v_mfma_f32_16x16x32_bf16 v[212:215], v[140:143], v[232:235], v[212:215]\n\t" \
    "v_mfma_f32_16x16x32_bf16 v[212:215], v[176:179], v[236:239], v[212:215]\n\t" \
    "v_cvt_pk_bf16_f32 v220, v220, v221\n\t" \
    "v_cvt_pk_bf16_f32 v221, v222, v223\n\t" \
    "v_mfma_f32_16x16x32_bf16 v[212:215], v[180:183], v[240:243], v[212:215]\n\t" \
    "ds_write_b64 v254, v[220:221] offset:14336\n\t" \
    "s_nop 6\n\t" \
    "v_cvt_pk_bf16_f32 v212, v212, v213\n\t" \
    "v_cvt_pk_bf16_f32 v213, v214, v215\n\t" \
    "ds_write_b64 v254, v[212:213] offset:12288\n\t" \
    "s_waitcnt vmcnt(0) lgkmcnt(0)\n\t" \
    "s_barrier \n\t" \
    "s_setprio 2\n\t" \
    "s_add_u32 s44, s40, 1\n\t" \
    "s_min_u32 s44, s44, 63\n\t" \
    ".Lv08_%=:\n\t" \
    "v_cmp_ge_u32 vcc, s44, v252\n\t" \
    "s_cbranch_vccz .Lv09_%=\n\t" \
    "s_cmp_eq_u32 s41, 0\n\t" \
    "s_cbranch_scc1 .Lv09_%=\n\t" \
    "s_sub_u32 s41, s41, 1\n\t" \
    "s_sleep 2\n\t" \
    "v_and_b32 v252, 0xf0, v253\n\t" \
    "v_lshlrev_b32 v252, 3, v252\n\t" \
    "buffer_load_dword v252, v252, s[60:63], %[fl] offen sc1\n\t" \
    "s_waitcnt vmcnt(0)\n\t" \
    "s_branch .Lv08_%=\n\t" \
    ".Lv09_%=:\n\t" \
    "s_mov_b32 m0, 0x0\n\t" \
    "s_add_u32 s49, s42, 0x0\n\t" \
    "buffer_load_dwordx4 v253, s[60:63], s49 offen offset:0 sc1 lds\n\t" \
    "s_sleep 1\n\t" \
    "buffer_load_dwordx4 v253, s[60:63], s49 offen offset:1024 sc1 lds\n\t" \
    "s_sleep 1\n\t" \
    "buffer_load_dwordx4 v253, s[60:63], s49 offen offset:2048 sc1 lds\n\t" \
    "s_sleep 1\n\t" \
    "buffer_load_dwordx4 v253, s[60:63], s49 offen offset:3072 sc1 lds\n\t" \
    "s_sleep 1\n\t" \
    "s_mov_b32 m0, 0x4000\n\t" \
    "s_add_u32 s49, s42, 0x10000\n\t" \
    "buffer_load_dwordx4 v253, s[60:63], s49 offen offset:0 sc1 lds\n\t" \
    "s_sleep 1\n\t" \
    "s_mov_b32 m0, 0x5000\n\t" \
    "s_add_u32 s49, s42, 0x1000\n\t" \
    "buffer_load_dwordx4 v253, s[60:63], s49 offen offset:0 sc1 lds\n\t" \
    "s_sleep 1\n\t" \
    "buffer_load_dwordx4 v253, s[60:63], s49 offen offset:1024 sc1 lds\n\t" \
    "s_sleep 1\n\t" \
    "buffer_load_dwordx4 v253, s[60:63], s49 offen offset:2048 sc1 lds\n\t" \
    "s_sleep 1\n\t" \
    "buffer_load_dwordx4 v253, s[60:63], s49 offen offset:3072 sc1 lds\n\t" \
    "s_sleep 1\n\t" \
    "s_mov_b32 m0, 0x9000\n\t" \
    "s_add_u32 s49, s42, 0x11000\n\t" \
    "buffer_load_dwordx4 v253, s[60:63], s49 offen offset:0 sc1 lds\n\t" \
    "s_sleep 1\n\t" \
    "ds_read_b128 v[200:203], v254 offset:30720\n\t" \
    "ds_read_b128 v[204:207], v254 offset:31744\n\t" \
    "ds_read_b128 v[216:219], v254 offset:32768\n\t" \
    "ds_read_b128 v[220:223], v254 offset:33792\n\t" \
    "v_mov_b64 v[208:209], 0\n\t" \
    "v_mov_b64 v[210:211], 0\n\t" \
    "v_mov_b64 v[244:245], 0\n\t" \
    "v_mov_b64 v[246:247], 0\n\t" \
    "v_mov_b64 v[248:249], 0\n\t" \
    "v_mov_b64 v[250:251], 0\n\t" \
    "s_sleep 5\n\t" \
    "s_mov_b32 m0, 0xa000\n\t" \
    "s_nop 0\n\t" \
    "buffer_load_dwordx4 v253, s[60:63], s46 offen sc1 lds\n\t" \
    "s_mov_b32 m0, 0xb400\n\t" \
    "s_nop 0\n\t" \
    "buffer_load_dwordx4 v253, s[60:63], s47 offen sc1 lds\n\t" \
    "s_mov_b32 m0, 0xa400\n\t" \
    "s_nop 0\n\t" \
    "buffer_load_dwordx4 v253, s[60:63], s48 offen sc1 lds\n\t" \
    "s_waitcnt lgkmcnt(0)\n\t" \
    "v_bfe_i32 v212, v253, 5, 1\n\t" \
    "v_and_b32 v212, 0xaaaa, v212\n\t" \
    "v_or_b32 v212, 0x4444, v212\n\t" \
    "v_and_b32 v213, 0xffff, v255\n\t" \
    "v_lshrrev_b32 v214, 16, v255\n\t" \
    ".Lv010_%=:\n\t" \
    "s_waitcnt vmcnt(0)\n\t" \
    "ds_read_b128 v[224:227], v253 offset:40960\n\t" \
    "ds_read_b128 v[228:231], v253 offset:46080\n\t" \
    "ds_read_b128 v[232:235], v253 offset:41984\n\t" \
    "s_waitcnt lgkmcnt(2)\n\t" \
    "v_max_u32 v215, v224, v225\n\t" \
    "v_max3_u32 v215, v226, v227, v215\n\t" \
    "s_waitcnt lgkmcnt(1)\n\t" \
    "v_max3_u32 v215, v228, v229, v215\n\t" \
    "v_max3_u32 v215, v230, v231, v215\n\t" \
    "s_waitcnt lgkmcnt(0)\n\t" \
    "v_max3_u32 v215, v232, v233, v215\n\t" \
    "v_max3_u32 v215, v234, v235, v215\n\t" \
    "v_cmp_eq_u32 vcc, -1, v215\n\t" \
    "s_cbranch_vccz .Lv011_%=\n\t" \
    "v_max_u32 v215, v224, v225\n\t" \
    "v_max3_u32 v215, v226, v227, v215\n\t" \
    "v_cmp_eq_u32 vcc, -1, v215\n\t" \
    "s_cbranch_vccz .Lv012_%=\n\t" \
    "s_mov_b32 m0, 0xa000\n\t" \
    "s_nop 0\n\t" \
    "buffer_load_dwordx4 v253, s[60:63], s46 offen sc1 lds\n\t" \
    ".Lv012_%=:\n\t" \
    "v_max_u32 v215, v228, v229\n\t" \
    "v_max3_u32 v215, v230, v231, v215\n\t" \
    "v_cmp_eq_u32 vcc, -1, v215\n\t" \
    "s_cbranch_vccz .Lv013_%=\n\t" \
    "s_mov_b32 m0, 0xb400\n\t" \
    "s_nop 0\n\t" \
    "buffer_load_dwordx4 v253, s[60:63], s47 offen sc1 lds\n\t" \
    ".Lv013_%=:\n\t" \
    "v_max_u32 v215, v232, v233\n\t" \
    "v_max3_u32 v215, v234, v235, v215\n\t" \
    "v_cmp_eq_u32 vcc, -1, v215\n\t" \
    "s_cbranch_vccz .Lv014_%=\n\t" \
    "s_mov_b32 m0, 0xa400\n\t" \
    "s_nop 0\n\t" \
    "buffer_load_dwordx4 v253, s[60:63], s48 offen sc1 lds\n\t" \
    ".Lv014_%=:\n\t" \
    "s_cmp_eq_u32 s41, 0\n\t" \
    "s_cbranch_scc1 .Lv011_%=\n\t" \
    "s_sub_u32 s41, s41, 1\n\t" \
    "s_branch .Lv010_%=\n\t" \
    ".Lv011_%=:\n\t" \
    "s_waitcnt lgkmcnt(0)\n\t" \
    "s_barrier \n\t" \
    "ds_read_b128 v[236:239], v253 offset:47104\n\t" \
    "ds_read_b128 v[240:243], v253 offset:43008\n\t" \
    "v_smfmac_f32_16x16x64_bf16 v[244:247], v[224:227], v[0:7], v212\n\t" \
    "v_smfmac_f32_16x16x64_bf16 v[248:251], v[224:227], v[8:15], v212\n\t" \
    "v_smfmac_f32_16x16x64_bf16 v[208:211], v[224:227], v[200:207], v212\n\t" \
    "ds_read_b128 v[200:203], v254 offset:34816\n\t" \
    "ds_read_b128 v[204:207], v254 offset:35840\n\t" \
    "ds_read_b128 v[224:227], v253 offset:48128\n\t" \
    "v_smfmac_f32_16x16x64_bf16 v[208:211], v[228:231], v[16:23], v212\n\t" \
    "v_smfmac_f32_16x16x64_bf16 v[244:247], v[228:231], v[24:31], v212\n\t" \
    "v_smfmac_f32_16x16x64_bf16 v[248:251], v[228:231], v[32:39], v212\n\t" \
    "ds_read_b128 v[228:231], v253 offset:44032\n\t" \
    "v_smfmac_f32_16x16x64_bf16 v[208:211], v[232:235], v[216:223], v212\n\t" \
    "ds_read_b128 v[216:219], v254 offset:36864\n\t" \
    "ds_read_b128 v[220:223], v254 offset:37888\n\t" \
    "v_smfmac_f32_16x16x64_bf16 v[244:247], v[232:235], v[40:47], v212\n\t" \
    "v_smfmac_f32_16x16x64_bf16 v[248:251], v[232:235], v[48:55], v212\n\t" \
    "ds_read_b128 v[232:235], v253 offset:49152\n\t" \
    "s_waitcnt lgkmcnt(8)\n\t" \
    "v_smfmac_f32_16x16x64_bf16 v[208:211], v[236:239], v[56:63], v212\n\t" \
    "v_smfmac_f32_16x16x64_bf16 v[244:247], v[236:239], v[64:71], v212\n\t" \
    "v_smfmac_f32_16x16x64_bf16 v[248:251], v[236:239], v[72:79], v212\n\t" \
    "ds_read_b128 v[236:239], v253 offset:45056\n\t" \
    "s_waitcnt lgkmcnt(6)\n\t" \
    "v_smfmac_f32_16x16x64_bf16 v[208:211], v[240:243], v[200:207], v212\n\t" \
    "ds_read_b128 v[200:203], v254 offset:38912\n\t" \
    "ds_read_b128 v[204:207], v254 offset:39936\n\t" \
    "v_smfmac_f32_16x16x64_bf16 v[244:247], v[240:243], v[80:87], v212\n\t" \
    "v_smfmac_f32_16x16x64_bf16 v[248:251], v[240:243], v[88:95], v212\n\t" \
    "ds_read_b128 v[240:243], v253 offset:50176\n\t" \
    "s_waitcnt lgkmcnt(8)\n\t" \
    "v_smfmac_f32_16x16x64_bf16 v[208:211], v[224:227], v[96:103], v212\n\t" \
    "v_smfmac_f32_16x16x64_bf16 v[244:247], v[224:227], v[104:111], v212\n\t" \
    "v_smfmac_f32_16x16x64_bf16 v[248:251], v[224:227], v[112:119], v212\n\t" \
    "s_waitcnt lgkmcnt(7)\n\t" \
    "v_smfmac_f32_16x16x64_bf16 v[244:247], v[228:231], v[120:127], v212\n\t" \
    "s_waitcnt lgkmcnt(4)\n\t" \
    "v_smfmac_f32_16x16x64_bf16 v[244:247], v[232:235], v[144:151], v212\n\t" \
    "s_waitcnt lgkmcnt(3)\n\t" \
    "v_smfmac_f32_16x16x64_bf16 v[244:247], v[236:239], v[160:167], v212\n\t" \
    "s_waitcnt lgkmcnt(0)\n\t" \
    "v_smfmac_f32_16x16x64_bf16 v[244:247], v[240:243], v[184:191], v212\n\t" \
    "v_smfmac_f32_16x16x64_bf16 v[248:251], v[228:231], v[128:135], v212\n\t" \
    "v_smfmac_f32_16x16x64_bf16 v[248:251], v[232:235], v[152:159], v212\n\t" \
    "v_smfmac_f32_16x16x64_bf16 v[248:251], v[236:239], v[168:175], v212\n\t" \
    "v_add_f32 v244, v244, v246\n\t" \
    "v_add_f32 v245, v245, v247\n\t" \
    "v_cvt_pk_bf16_f32 v244, v244, v245\n\t" \
    "v_smfmac_f32_16x16x64_bf16 v[248:251], v[240:243], v[192:199], v212\n\t" \
    "ds_write_b16 v213, v244 offset:512\n\t" \
    "ds_write_b16_d16_hi v213, v244 offset:528\n\t" \
    "v_smfmac_f32_16x16x64_bf16 v[208:211], v[228:231], v[216:223], v212\n\t" \
    "v_smfmac_f32_16x16x64_bf16 v[208:211], v[232:235], v[136:143], v212\n\t" \
    "v_smfmac_f32_16x16x64_bf16 v[208:211], v[236:239], v[200:207], v212\n\t" \
    "v_add_f32 v248, v248, v250\n\t" \
    "v_add_f32 v249, v249, v251\n\t" \
    "v_cvt_pk_bf16_f32 v248, v248, v249\n\t" \
    "v_smfmac_f32_16x16x64_bf16 v[208:211], v[240:243], v[176:183], v212\n\t" \
    "ds_write_b16 v213, v248 offset:1024\n\t" \
    "ds_write_b16_d16_hi v213, v248 offset:1040\n\t" \
    "s_nop 5\n\t" \
    "v_add_f32 v208, v208, v210\n\t" \
    "v_add_f32 v209, v209, v211\n\t" \
    "v_cvt_pk_bf16_f32 v208, v208, v209\n\t" \
    "ds_write_b16 v213, v208 offset:0\n\t" \
    "ds_write_b16_d16_hi v213, v208 offset:16\n\t" \
    "s_setprio 0\n\t" \
    "s_waitcnt lgkmcnt(0)\n\t" \
    "s_barrier \n\t" \
    "ds_read_b128 v[224:227], v253 offset:0\n\t" \
    "ds_read_b128 v[244:247], v254 offset:30720\n\t" \
    "ds_read_b128 v[228:231], v253 offset:1024\n\t" \
    "ds_read_b128 v[232:235], v253 offset:20480\n\t" \
    "ds_read_b128 v[236:239], v253 offset:21504\n\t" \
    "ds_read_b128 v[248:251], v254 offset:31744\n\t" \
    "s_add_u32 s40, s40, 1\n\t" \
    "s_cmp_lg_u32 s40, 64\n\t" \
    "s_cbranch_scc1 .Lv07_%=\n\t" \
    "s_branch .Lend%=\n\t" \
    ".Lend%=:\n\t" \
    "s_waitcnt vmcnt(0) lgkmcnt(0)\n\t" \
    "s_mov_b32 %[bud], s41\n\t"
#define ASM_R_LOOP \
    "s_mov_b32 s40, 0\n\t" \
    "s_mov_b32 s41, 0x40000\n\t" \
    "v_mov_b64 v[252:253], -1\n\t" \
    "v_mov_b64 v[254:255], -1\n\t" \
    "v_mov_b32 v248, 0\n\t" \
    "s_mov_b32 s53, 0x05040100\n\t" \
    ".Lr1_%=:\n\t" \
    "s_mov_b32 s43, s40\n\t" \
    "s_and_b32 s44, s40, 3\n\t" \
    "s_lshl_b32 s42, s44, 22\n\t" \
    "s_add_u32 s42, s42, %[vx]\n\t" \
    "s_lshl_b32 s46, s44, 22\n\t" \
    "s_mov_b32 s47, s46\n\t" \
    "s_lshl_b32 s49, s44, 17\n\t" \
    "s_add_u32 s49, s49, %[px]\n\t" \
    "s_add_u32 s50, s49, 0x1000\n\t" \
    "s_add_u32 s44, s40, 1\n\t" \
    "s_and_b32 s44, s44, 3\n\t" \
    "s_lshl_b32 s48, s44, 22\n\t" \
    "s_mul_i32 s52, s40, 0x14000\n\t" \
    "s_lshr_b32 s44, s40, 4\n\t" \
    "s_mul_i32 s44, s44, 0xc2000\n\t" \
    "s_add_u32 s52, s52, s44\n\t" \
    "s_add_u32 s52, s52, %[xr]\n\t" \
    "s_add_i32 s44, s40, %[ad]\n\t" \
    "s_and_b32 s45, s44, 3\n\t" \
    "s_mul_i32 s51, s45, %[as]\n\t" \
    "s_cmp_lt_i32 s44, 0\n\t" \
    "s_cselect_b32 s55, 0x40000000, 0\n\t" \
    "s_add_u32 s44, s40, 3\n\t" \
    "s_mul_i32 s54, s44, 0x14000\n\t" \
    "s_lshr_b32 s45, s44, 4\n\t" \
    "s_mul_i32 s45, s45, 0xc2000\n\t" \
    "s_add_u32 s54, s54, s45\n\t" \
    "s_cmp_eq_u32 %[wave], 7\n\t" \
    "s_cselect_b32 s51, s54, s51\n\t" \
    "s_cselect_b32 s55, 0, s55\n\t" \
    "s_cselect_b32 s44, 60, 62\n\t" \
    "s_cmp_gt_u32 s40, s44\n\t" \
    "s_cselect_b32 s55, 0x40000000, s55\n\t" \
    "v_or_b32 v197, s55, %[ae]\n\t" \
    "v_mov_b64 v[180:181], -1\n\t" \
    "v_mov_b64 v[182:183], -1\n\t" \
    "v_mov_b64 v[184:185], -1\n\t" \
    "v_mov_b64 v[186:187], -1\n\t" \
    "v_mov_b64 v[188:189], -1\n\t" \
    "v_mov_b64 v[190:191], -1\n\t" \
    "v_mov_b64 v[192:193], -1\n\t" \
    "v_mov_b64 v[194:195], -1\n\t" \
    "s_waitcnt vmcnt(0)\n\t" \
    ".Lr2_%=:\n\t" \
    "buffer_load_dword v248, %[fo], %[rs], %[fs] offen sc1\n\t" \
    "s_waitcnt vmcnt(0)\n\t" \
    "v_cmp_ge_u32 vcc, s43, v248\n\t" \
    "s_cbranch_vccz .Lr3_%=\n\t" \
    "s_sleep 2\n\t" \
    "s_cmp_eq_u32 s41, 0\n\t" \
    "s_cbranch_scc1 .Lr3_%=\n\t" \
    "s_sub_u32 s41, s41, 1\n\t" \
    "s_branch .Lr2_%=\n\t" \
    ".Lr3_%=:\n\t" \
    "buffer_load_dwordx4 v[160:163], %[l16], %[rs], s42 offen offset:0 sc1\n\t" \
    "buffer_load_dwordx4 v[164:167], %[l16], %[rs], s42 offen offset:1024 sc1\n\t" \
    "buffer_load_dwordx4 v[168:171], %[l16], %[rs], s42 offen offset:2048 sc1\n\t" \
    "buffer_load_dwordx4 v[172:175], %[l16], %[rs], s42 offen offset:3072 sc1\n\t" \
    "s_cmp_lg_u32 s40, 63\n\t" \
    "s_cbranch_scc1 .Lr6_%=\n\t" \
    "buffer_load_dwordx4 v[180:183], %[ov], %[rs], s46 offen sc1\n\t" \
    ".Lr7_%=:\n\t" \
    "s_waitcnt vmcnt(12)\n\t" \
    "v_max_u32 v196, v180, v181\n\t" \
    "v_max3_u32 v196, v182, v183, v196\n\t" \
    "v_cmp_eq_u32 vcc, -1, v196\n\t" \
    "s_cbranch_vccz .Lr8_%=\n\t" \
    "s_nop 7\n\t" \
    "v_max_u32 v196, v180, v181\n\t" \
    "v_max3_u32 v196, v182, v183, v196\n\t" \
    "v_cmp_eq_u32 vcc, -1, v196\n\t" \
    "s_cbranch_vccz .Lr8_%=\n\t" \
    "s_nop 7\n\t" \
    "v_max_u32 v196, v180, v181\n\t" \
    "v_max3_u32 v196, v182, v183, v196\n\t" \
    "v_cmp_eq_u32 vcc, -1, v196\n\t" \
    "s_cbranch_vccz .Lr8_%=\n\t" \
    "s_nop 7\n\t" \
    "v_max_u32 v196, v180, v181\n\t" \
    "v_max3_u32 v196, v182, v183, v196\n\t" \
    "v_cmp_eq_u32 vcc, -1, v196\n\t" \
    "s_cbranch_vccz .Lr8_%=\n\t" \
    "s_nop 7\n\t" \
    "s_cmp_eq_u32 s41, 0\n\t" \
    "s_cbranch_scc1 .Lr8_%=\n\t" \
    "s_sub_u32 s41, s41, 1\n\t" \
    "v_max_u32 v196, v180, v181\n\t" \
    "v_max3_u32 v196, v182, v183, v196\n\t" \
    "v_cmp_eq_u32 vcc, -1, v196\n\t" \
    "s_cbranch_vccz .Lr9_%=\n\t" \
    "buffer_load_dwordx4 v[180:183], %[ov], %[rs], s46 offen sc1\n\t" \
    ".Lr9_%=:\n\t" \
    "s_branch .Lr7_%=\n\t" \
    ".Lr8_%=:\n\t" \
    "s_branch .Lr5_%=\n\t" \
    ".Lr6_%=:\n\t" \
    "s_sleep 8\n\t" \
    "buffer_load_dwordx4 v[184:187], %[o0], %[rs], s47 offen sc1\n\t" \
    "buffer_load_dwordx4 v[188:191], %[o1], %[rs], s47 offen sc1\n\t" \
    "buffer_load_dwordx4 v[192:195], %[o2], %[rs], s47 offen sc1\n\t" \
    "buffer_load_dwordx4 v[180:183], %[ov], %[rs], s46 offen sc1\n\t" \
    ".Lr10_%=:\n\t" \
    "s_waitcnt vmcnt(12)\n\t" \
    "v_max_u32 v196, v184, v185\n\t" \
    "v_max3_u32 v196, v186, v187, v196\n\t" \
    "v_max3_u32 v196, v188, v189, v196\n\t" \
    "v_max3_u32 v196, v190, v191, v196\n\t" \
    "v_max3_u32 v196, v192, v193, v196\n\t" \
    "v_max3_u32 v196, v194, v195, v196\n\t" \
    "v_max3_u32 v196, v180, v181, v196\n\t" \
    "v_max3_u32 v196, v182, v183, v196\n\t" \
    "v_cmp_eq_u32 vcc, -1, v196\n\t" \
    "s_cbranch_vccz .Lr11_%=\n\t" \
    "s_nop 7\n\t" \
    "v_max_u32 v196, v184, v185\n\t" \
    "v_max3_u32 v196, v186, v187, v196\n\t" \
    "v_max3_u32 v196, v188, v189, v196\n\t" \
    "v_max3_u32 v196, v190, v191, v196\n\t" \
    "v_max3_u32 v196, v192, v193, v196\n\t" \
    "v_max3_u32 v196, v194, v195, v196\n\t" \
    "v_max3_u32 v196, v180, v181, v196\n\t" \
    "v_max3_u32 v196, v182, v183, v196\n\t" \
    "v_cmp_eq_u32 vcc, -1, v196\n\t" \
    "s_cbranch_vccz .Lr11_%=\n\t" \
    "s_nop 7\n\t" \
    "v_max_u32 v196, v184, v185\n\t" \
    "v_max3_u32 v196, v186, v187, v196\n\t" \
    "v_max3_u32 v196, v188, v189, v196\n\t" \
    "v_max3_u32 v196, v190, v191, v196\n\t" \
    "v_max3_u32 v196, v192, v193, v196\n\t" \
    "v_max3_u32 v196, v194, v195, v196\n\t" \
    "v_max3_u32 v196, v180, v181, v196\n\t" \
    "v_max3_u32 v196, v182, v183, v196\n\t" \
    "v_cmp_eq_u32 vcc, -1, v196\n\t" \
    "s_cbranch_vccz .Lr11_%=\n\t" \
    "s_nop 7\n\t" \
    "v_max_u32 v196, v184, v185\n\t" \
    "v_max3_u32 v196, v186, v187, v196\n\t" \
    "v_max3_u32 v196, v188, v189, v196\n\t" \
    "v_max3_u32 v196, v190, v191, v196\n\t" \
    "v_max3_u32 v196, v192, v193, v196\n\t" \
    "v_max3_u32 v196, v194, v195, v196\n\t" \
    "v_max3_u32 v196, v180, v181, v196\n\t" \
    "v_max3_u32 v196, v182, v183, v196\n\t" \
    "v_cmp_eq_u32 vcc, -1, v196\n\t" \
    "s_cbranch_vccz .Lr11_%=\n\t" \
    "s_nop 7\n\t" \
    "v_max_u32 v196, v184, v185\n\t" \
    "v_max3_u32 v196, v186, v187, v196\n\t" \
    "v_max3_u32 v196, v188, v189, v196\n\t" \
    "v_max3_u32 v196, v190, v191, v196\n\t" \
    "v_max3_u32 v196, v192, v193, v196\n\t" \
    "v_max3_u32 v196, v194, v195, v196\n\t" \
    "v_max3_u32 v196, v180, v181, v196\n\t" \
    "v_max3_u32 v196, v182, v183, v196\n\t" \
    "v_cmp_eq_u32 vcc, -1, v196\n\t" \
    "s_cbranch_vccz .Lr11_%=\n\t" \
    "s_nop 7\n\t" \
    "v_max_u32 v196, v184, v185\n\t" \
    "v_max3_u32 v196, v186, v187, v196\n\t" \
    "v_max3_u32 v196, v188, v189, v196\n\t" \
    "v_max3_u32 v196, v190, v191, v196\n\t" \
    "v_max3_u32 v196, v192, v193, v196\n\t" \
    "v_max3_u32 v196, v194, v195, v196\n\t" \
    "v_max3_u32 v196, v180, v181, v196\n\t" \
    "v_max3_u32 v196, v182, v183, v196\n\t" \
    "v_cmp_eq_u32 vcc, -1, v196\n\t" \
    "s_cbranch_vccz .Lr11_%=\n\t" \
    "s_nop 7\n\t" \
    "v_max_u32 v196, v184, v185\n\t" \
    "v_max3_u32 v196, v186, v187, v196\n\t" \
    "v_max3_u32 v196, v188, v189, v196\n\t" \
    "v_max3_u32 v196, v190, v191, v196\n\t" \
    "v_max3_u32 v196, v192, v193, v196\n\t" \
    "v_max3_u32 v196, v194, v195, v196\n\t" \
    "v_max3_u32 v196, v180, v181, v196\n\t" \
    "v_max3_u32 v196, v182, v183, v196\n\t" \
    "v_cmp_eq_u32 vcc, -1, v196\n\t" \
    "s_cbranch_vccz .Lr11_%=\n\t" \
    "s_nop 7\n\t" \
    "v_max_u32 v196, v184, v185\n\t" \
    "v_max3_u32 v196, v186, v187, v196\n\t" \
    "v_max3_u32 v196, v188, v189, v196\n\t" \
    "v_max3_u32 v196, v190, v191, v196\n\t" \
    "v_max3_u32 v196, v192, v193, v196\n\t" \
    "v_max3_u32 v196, v194, v195, v196\n\t" \
    "v_max3_u32 v196, v180, v181, v196\n\t" \
    "v_max3_u32 v196, v182, v183, v196\n\t" \
    "v_cmp_eq_u32 vcc, -1, v196\n\t" \
    "s_cbranch_vccz .Lr11_%=\n\t" \
    "s_nop 7\n\t" \
    "s_cmp_eq_u32 s41, 0\n\t" \
    "s_cbranch_scc1 .Lr11_%=\n\t" \
    "s_sub_u32 s41, s41, 1\n\t" \
    "v_max_u32 v196, v184, v185\n\t" \
    "v_max3_u32 v196, v186, v187, v196\n\t" \
    "v_cmp_eq_u32 vcc, -1, v196\n\t" \
    "s_cbranch_vccz .Lr12_%=\n\t" \
    "buffer_load_dwordx4 v[184:187], %[o0], %[rs], s47 offen sc1\n\t" \
    ".Lr12_%=:\n\t" \
    "v_max_u32 v196, v188, v189\n\t" \
    "v_max3_u32 v196, v190, v191, v196\n\t" \
    "v_cmp_eq_u32 vcc, -1, v196\n\t" \
    "s_cbranch_vccz .Lr13_%=\n\t" \
    "buffer_load_dwordx4 v[188:191], %[o1], %[rs], s47 offen sc1\n\t" \
    ".Lr13_%=:\n\t" \
    "v_max_u32 v196, v192, v193\n\t" \
    "v_max3_u32 v196, v194, v195, v196\n\t" \
    "v_cmp_eq_u32 vcc, -1, v196\n\t" \
    "s_cbranch_vccz .Lr14_%=\n\t" \
    "buffer_load_dwordx4 v[192:195], %[o2], %[rs], s47 offen sc1\n\t" \
    ".Lr14_%=:\n\t" \
    "v_max_u32 v196, v180, v181\n\t" \
    "v_max3_u32 v196, v182, v183, v196\n\t" \
    "v_cmp_eq_u32 vcc, -1, v196\n\t" \
    "s_cbranch_vccz .Lr15_%=\n\t" \
    "buffer_load_dwordx4 v[180:183], %[ov], %[rs], s46 offen sc1\n\t" \
    ".Lr15_%=:\n\t" \
    "s_branch .Lr10_%=\n\t" \
    ".Lr11_%=:\n\t" \
    "v_mfma_f32_16x16x32_bf16 v[176:179], v[192:195], v[180:183], 0\n\t" \
    "v_smfmac_f32_16x16x64_bf16 v[176:179], v[184:187], v[160:167], %[sidx]\n\t" \
    "v_smfmac_f32_16x16x64_bf16 v[176:179], v[188:191], v[168:175], %[sidx]\n\t" \
    "s_cmp_gt_u32 %[wave], 3\n\t" \
    "s_nop 7\n\t" \
    "v_add_f32 v176, v176, v178\n\t" \
    "v_add_f32 v177, v177, v179\n\t" \
    "ds_write2_b32 %[rw], v176, v177 offset1:80\n\t" \
    "s_waitcnt lgkmcnt(0)\n\t" \
    "s_barrier \n\t" \
    "s_cbranch_scc1 .Lr4_%=\n\t" \
    "ds_read_b128 v[128:131], %[rd] offset:0\n\t" \
    "ds_read_b128 v[132:135], %[rd] offset:16\n\t" \
    "ds_read_b128 v[136:139], %[rd] offset:32\n\t" \
    "ds_read_b128 v[140:143], %[rd] offset:48\n\t" \
    "s_waitcnt lgkmcnt(2)\n\t" \
    "v_pk_add_f32 v[128:129], v[128:129], v[130:131]\n\t" \
    "v_pk_add_f32 v[132:133], v[132:133], v[134:135]\n\t" \
    "s_waitcnt lgkmcnt(0)\n\t" \
    "v_pk_add_f32 v[136:137], v[136:137], v[138:139]\n\t" \
    "v_pk_add_f32 v[140:141], v[140:141], v[142:143]\n\t" \
    "v_pk_add_f32 v[128:129], v[128:129], v[132:133]\n\t" \
    "v_pk_add_f32 v[136:137], v[136:137], v[140:141]\n\t" \
    "v_pk_add_f32 v[128:129], v[128:129], v[136:137]\n\t" \
    "v_cvt_pk_bf16_f32 v128, v128, v129\n\t" \
    "v_min_u32 v128, -2, v128\n\t" \
    "buffer_store_dword v128, %[oo], %[rs], s48 offen sc1\n\t" \
    ".Lr4_%=:\n\t" \
    "s_cmp_lt_u32 %[wave], 4\n\t" \
    "s_cbranch_scc1 .Lr17_%=\n\t" \
    "s_sleep 4\n\t" \
    ".Lr17_%=:\n\t" \
    "s_sleep 2\n\t" \
    ".Lr5_%=:\n\t" \
    "s_cmp_lt_u32 %[wave], 6\n\t" \
    "s_cbranch_scc1 .Lr16_%=\n\t" \
    "buffer_load_dwordx2 v[250:251], %[xo], %[rs], s52 offen sc1\n\t" \
    ".Lr16_%=:\n\t" \
    "buffer_load_dwordx4 v[220:223], %[l16], %[rs], s50 offen offset:1024 sc1\n\t" \
    "buffer_load_dwordx4 v[224:227], %[l16], %[rs], s50 offen offset:2048 sc1\n\t" \
    "buffer_load_dwordx4 v[228:231], %[l16], %[rs], s50 offen offset:3072 sc1\n\t" \
    "v_perm_b32 v232, v161, v160, s53\n\t" \
    "v_perm_b32 v233, v163, v162, s53\n\t" \
    "v_perm_b32 v234, v165, v164, s53\n\t" \
    "v_perm_b32 v235, v167, v166, s53\n\t" \
    "v_perm_b32 v236, v169, v168, s53\n\t" \
    "v_perm_b32 v237, v171, v170, s53\n\t" \
    "v_perm_b32 v238, v173, v172, s53\n\t" \
    "v_perm_b32 v239, v175, v174, s53\n\t" \
    "s_nop 1\n\t" \
    "v_permlane16_swap_b32 v232, v234\n\t" \
    "v_permlane16_swap_b32 v233, v235\n\t" \
    "v_permlane16_swap_b32 v236, v238\n\t" \
    "v_permlane16_swap_b32 v237, v239\n\t" \
    "s_waitcnt vmcnt(0)\n\t" \
    ".Lr18_%=:\n\t" \
    "v_max_u32 v196, v220, v221\n\t" \
    "v_max3_u32 v196, v222, v223, v196\n\t" \
    "v_max3_u32 v196, v224, v225, v196\n\t" \
    "v_max3_u32 v196, v226, v227, v196\n\t" \
    "v_max3_u32 v196, v228, v229, v196\n\t" \
    "v_max3_u32 v196, v230, v231, v196\n\t" \
    "v_cmp_eq_u32 vcc, -1, v196\n\t" \
    "s_cbranch_vccz .Lr19_%=\n\t" \
    "s_cmp_eq_u32 s41, 0\n\t" \
    "s_cbranch_scc1 .Lr19_%=\n\t" \
    "s_sub_u32 s41, s41, 1\n\t" \
    "v_max_u32 v196, v220, v221\n\t" \
    "v_max3_u32 v196, v222, v223, v196\n\t" \
    "v_cmp_eq_u32 vcc, -1, v196\n\t" \
    "s_cbranch_vccz .Lr20_%=\n\t" \
    "buffer_load_dwordx4 v[220:223], %[l16], %[rs], s50 offen offset:1024 sc1\n\t" \
    ".Lr20_%=:\n\t" \
    "v_max_u32 v196, v224, v225\n\t" \
    "v_max3_u32 v196, v226, v227, v196\n\t" \
    "v_cmp_eq_u32 vcc, -1, v196\n\t" \
    "s_cbranch_vccz .Lr21_%=\n\t" \
    "buffer_load_dwordx4 v[224:227], %[l16], %[rs], s50 offen offset:2048 sc1\n\t" \
    ".Lr21_%=:\n\t" \
    "v_max_u32 v196, v228, v229\n\t" \
    "v_max3_u32 v196, v230, v231, v196\n\t" \
    "v_cmp_eq_u32 vcc, -1, v196\n\t" \
    "s_cbranch_vccz .Lr22_%=\n\t" \
    "buffer_load_dwordx4 v[228:231], %[l16], %[rs], s50 offen offset:3072 sc1\n\t" \
    ".Lr22_%=:\n\t" \
    "s_waitcnt vmcnt(0)\n\t" \
    "s_branch .Lr18_%=\n\t" \
    ".Lr19_%=:\n\t" \
    "v_mfma_f32_16x16x32_bf16 v[240:243], v[232:235], v[220:223], 0\n\t" \
    "v_mfma_f32_16x16x32_bf16 v[240:243], v[236:239], v[224:227], v[240:243]\n\t" \
    "v_mfma_f32_16x16x32_bf16 v[240:243], v[180:183], v[228:231], v[240:243]\n\t" \
    "s_nop 7\n\t" \
    "ds_write_b128 %[pw], v[240:243]\n\t" \
    "s_waitcnt lgkmcnt(0)\n\t" \
    "s_barrier \n\t" \
    "buffer_load_dwordx4 v[200:203], %[l16], %[rs], s49 offen offset:0 sc1\n\t" \
    "buffer_load_dwordx4 v[204:207], %[l16], %[rs], s49 offen offset:1024 sc1\n\t" \
    "buffer_load_dwordx4 v[208:211], %[l16], %[rs], s49 offen offset:2048 sc1\n\t" \
    "buffer_load_dwordx4 v[212:215], %[l16], %[rs], s49 offen offset:3072 sc1\n\t" \
    "buffer_load_dwordx4 v[216:219], %[l16], %[rs], s50 offen offset:0 sc1\n\t" \
    "s_cmp_lg_u32 %[wave], 6\n\t" \
    "s_cbranch_scc1 .Lr23_%=\n\t" \
    "ds_read_b128 v[128:131], %[pr] offset:0\n\t" \
    "ds_read_b128 v[132:135], %[pr] offset:1024\n\t" \
    "ds_read_b128 v[136:139], %[pr] offset:2048\n\t" \
    "ds_read_b128 v[140:143], %[pr] offset:3072\n\t" \
    "ds_read_b128 v[144:147], %[pr] offset:4096\n\t" \
    "ds_read_b128 v[148:151], %[pr] offset:5120\n\t" \
    "ds_read_b128 v[152:155], %[pr] offset:6144\n\t" \
    "ds_read_b128 v[156:159], %[pr] offset:7168\n\t" \
    "v_lshlrev_b32 v176, 16, v250\n\t" \
    "v_and_b32 v177, 0xffff0000, v250\n\t" \
    "v_lshlrev_b32 v178, 16, v251\n\t" \
    "v_and_b32 v179, 0xffff0000, v251\n\t" \
    "s_waitcnt lgkmcnt(6)\n\t" \
    "v_pk_add_f32 v[128:129], v[128:129], v[132:133]\n\t" \
    "v_pk_add_f32 v[130:131], v[130:131], v[134:135]\n\t" \
    "s_waitcnt lgkmcnt(4)\n\t" \
    "v_pk_add_f32 v[136:137], v[136:137], v[140:141]\n\t" \
    "v_pk_add_f32 v[138:139], v[138:139], v[142:143]\n\t" \
    "v_pk_add_f32 v[128:129], v[128:129], v[136:137]\n\t" \
    "v_pk_add_f32 v[130:131], v[130:131], v[138:139]\n\t" \
    "s_waitcnt lgkmcnt(2)\n\t" \
    "v_pk_add_f32 v[144:145], v[144:145], v[148:149]\n\t" \
    "v_pk_add_f32 v[146:147], v[146:147], v[150:151]\n\t" \
    "s_waitcnt lgkmcnt(0)\n\t" \
    "v_pk_add_f32 v[152:153], v[152:153], v[156:157]\n\t" \
    "v_pk_add_f32 v[154:155], v[154:155], v[158:159]\n\t" \
    "v_pk_add_f32 v[144:145], v[144:145], v[152:153]\n\t" \
    "v_pk_add_f32 v[146:147], v[146:147], v[154:155]\n\t" \
    "v_pk_add_f32 v[128:129], v[128:129], v[144:145]\n\t" \
    "v_pk_add_f32 v[130:131], v[130:131], v[146:147]\n\t" \
    "v_pk_add_f32 v[128:129], v[128:129], v[176:177]\n\t" \
    "v_pk_add_f32 v[130:131], v[130:131], v[178:179]\n\t" \
    "v_cvt_pk_bf16_f32 v128, v128, v129\n\t" \
    "v_cvt_pk_bf16_f32 v129, v130, v131\n\t" \
    "v_min_u32 v128, -2, v128\n\t" \
    "v_min_u32 v129, -2, v129\n\t" \
    "s_add_u32 s44, s52, 0x1000000\n\t" \
    "buffer_store_dwordx2 v[128:129], %[xo], %[rs], s44 offen sc1\n\t" \
    "s_waitcnt vmcnt(1)\n\t" \
    "s_branch .Lr24_%=\n\t" \
    ".Lr23_%=:\n\t" \
    "s_waitcnt vmcnt(0)\n\t" \
    ".Lr24_%=:\n\t" \
    ".Lr25_%=:\n\t" \
    "v_max_u32 v196, v200, v201\n\t" \
    "v_max3_u32 v196, v202, v203, v196\n\t" \
    "v_max3_u32 v196, v204, v205, v196\n\t" \
    "v_max3_u32 v196, v206, v207, v196\n\t" \
    "v_max3_u32 v196, v208, v209, v196\n\t" \
    "v_max3_u32 v196, v210, v211, v196\n\t" \
    "v_max3_u32 v196, v212, v213, v196\n\t" \
    "v_max3_u32 v196, v214, v215, v196\n\t" \
    "v_max3_u32 v196, v216, v217, v196\n\t" \
    "v_max3_u32 v196, v218, v219, v196\n\t" \
    "v_cmp_eq_u32 vcc, -1, v196\n\t" \
    "s_cbranch_vccz .Lr26_%=\n\t" \
    "s_cmp_eq_u32 s41, 0\n\t" \
    "s_cbranch_scc1 .Lr26_%=\n\t" \
    "s_sub_u32 s41, s41, 1\n\t" \
    "v_max_u32 v196, v200, v201\n\t" \
    "v_max3_u32 v196, v202, v203, v196\n\t" \
    "v_cmp_eq_u32 vcc, -1, v196\n\t" \
    "s_cbranch_vccz .Lr27_%=\n\t" \
    "buffer_load_dwordx4 v[200:203], %[l16], %[rs], s49 offen offset:0 sc1\n\t" \
    ".Lr27_%=:\n\t" \
    "v_max_u32 v196, v204, v205\n\t" \
    "v_max3_u32 v196, v206, v207, v196\n\t" \
    "v_cmp_eq_u32 vcc, -1, v196\n\t" \
    "s_cbranch_vccz .Lr28_%=\n\t" \
    "buffer_load_dwordx4 v[204:207], %[l16], %[rs], s49 offen offset:1024 sc1\n\t" \
    ".Lr28_%=:\n\t" \
    "v_max_u32 v196, v208, v209\n\t" \
    "v_max3_u32 v196, v210, v211, v196\n\t" \
    "v_cmp_eq_u32 vcc, -1, v196\n\t" \
    "s_cbranch_vccz .Lr29_%=\n\t" \
    "buffer_load_dwordx4 v[208:211], %[l16], %[rs], s49 offen offset:2048 sc1\n\t" \
    ".Lr29_%=:\n\t" \
    "v_max_u32 v196, v212, v213\n\t" \
    "v_max3_u32 v196, v214, v215, v196\n\t" \
    "v_cmp_eq_u32 vcc, -1, v196\n\t" \
    "s_cbranch_vccz .Lr30_%=\n\t" \
    "buffer_load_dwordx4 v[212:215], %[l16], %[rs], s49 offen offset:3072 sc1\n\t" \
    ".Lr30_%=:\n\t" \
    "v_max_u32 v196, v216, v217\n\t" \
    "v_max3_u32 v196, v218, v219, v196\n\t" \
    "v_cmp_eq_u32 vcc, -1, v196\n\t" \
    "s_cbranch_vccz .Lr31_%=\n\t" \
    "buffer_load_dwordx4 v[216:219], %[l16], %[rs], s50 offen offset:0 sc1\n\t" \
    ".Lr31_%=:\n\t" \
    "s_waitcnt vmcnt(0)\n\t" \
    "s_branch .Lr25_%=\n\t" \
    ".Lr26_%=:\n\t" \
    "v_mfma_f32_16x16x32_bf16 v[244:247], v[160:163], v[200:203], 0\n\t" \
    "v_mfma_f32_16x16x32_bf16 v[244:247], v[164:167], v[204:207], v[244:247]\n\t" \
    "v_mfma_f32_16x16x32_bf16 v[244:247], v[168:171], v[208:211], v[244:247]\n\t" \
    "v_mfma_f32_16x16x32_bf16 v[244:247], v[172:175], v[212:215], v[244:247]\n\t" \
    "v_mfma_f32_16x16x32_bf16 v[244:247], v[180:183], v[216:219], v[244:247]\n\t" \
    "s_nop 7\n\t" \
    "ds_write_b128 %[pw], v[244:247] offset:8192\n\t" \
    "s_waitcnt lgkmcnt(0)\n\t" \
    "s_barrier \n\t" \
    "s_cmp_lg_u32 %[wave], 7\n\t" \
    "s_cbranch_scc1 .Lr32_%=\n\t" \
    "ds_read_b128 v[128:131], %[pr] offset:0\n\t" \
    "ds_read_b128 v[132:135], %[pr] offset:1024\n\t" \
    "ds_read_b128 v[136:139], %[pr] offset:2048\n\t" \
    "ds_read_b128 v[140:143], %[pr] offset:3072\n\t" \
    "ds_read_b128 v[144:147], %[pr] offset:4096\n\t" \
    "ds_read_b128 v[148:151], %[pr] offset:5120\n\t" \
    "ds_read_b128 v[152:155], %[pr] offset:6144\n\t" \
    "ds_read_b128 v[156:159], %[pr] offset:7168\n\t" \
    "v_lshlrev_b32 v176, 16, v250\n\t" \
    "v_and_b32 v177, 0xffff0000, v250\n\t" \
    "v_lshlrev_b32 v178, 16, v251\n\t" \
    "v_and_b32 v179, 0xffff0000, v251\n\t" \
    "s_waitcnt lgkmcnt(6)\n\t" \
    "v_pk_add_f32 v[128:129], v[128:129], v[132:133]\n\t" \
    "v_pk_add_f32 v[130:131], v[130:131], v[134:135]\n\t" \
    "s_waitcnt lgkmcnt(4)\n\t" \
    "v_pk_add_f32 v[136:137], v[136:137], v[140:141]\n\t" \
    "v_pk_add_f32 v[138:139], v[138:139], v[142:143]\n\t" \
    "v_pk_add_f32 v[128:129], v[128:129], v[136:137]\n\t" \
    "v_pk_add_f32 v[130:131], v[130:131], v[138:139]\n\t" \
    "s_waitcnt lgkmcnt(2)\n\t" \
    "v_pk_add_f32 v[144:145], v[144:145], v[148:149]\n\t" \
    "v_pk_add_f32 v[146:147], v[146:147], v[150:151]\n\t" \
    "s_waitcnt lgkmcnt(0)\n\t" \
    "v_pk_add_f32 v[152:153], v[152:153], v[156:157]\n\t" \
    "v_pk_add_f32 v[154:155], v[154:155], v[158:159]\n\t" \
    "v_pk_add_f32 v[144:145], v[144:145], v[152:153]\n\t" \
    "v_pk_add_f32 v[146:147], v[146:147], v[154:155]\n\t" \
    "v_pk_add_f32 v[128:129], v[128:129], v[144:145]\n\t" \
    "v_pk_add_f32 v[130:131], v[130:131], v[146:147]\n\t" \
    "v_pk_add_f32 v[128:129], v[128:129], v[176:177]\n\t" \
    "v_pk_add_f32 v[130:131], v[130:131], v[178:179]\n\t" \
    "v_cvt_pk_bf16_f32 v128, v128, v129\n\t" \
    "v_cvt_pk_bf16_f32 v129, v130, v131\n\t" \
    "v_min_u32 v128, -2, v128\n\t" \
    "v_min_u32 v129, -2, v129\n\t" \
    "s_add_u32 s44, s52, 0x1000000\n\t" \
    "buffer_store_dwordx2 v[128:129], %[xo], %[rs], s44 offen sc1\n\t" \
    ".Lr32_%=:\n\t" \
    "buffer_store_dwordx4 v[252:255], %[ov], %[rs], s46 offen sc1\n\t" \
    "buffer_store_dwordx4 v[252:255], v197, %[rs], s51 offen sc1\n\t" \
    "s_add_u32 s40, s40, 1\n\t" \
    "s_cmp_lg_u32 s40, 64\n\t" \
    "s_cbranch_scc1 .Lr1_%=\n\t" \
    "s_branch .Lend%=\n\t" \
    ".Lend%=:\n\t" \
    "s_waitcnt vmcnt(0) lgkmcnt(0)\n\t" \
    "s_mov_b32 %[bud], s41\n\t"
#define ASM_W4_MUL \
    "s_mov_b32 s40, 0\n\t" \
    "s_mov_b32 s41, 0x40000\n\t" \
    "s_mov_b32 s43, 0\n\t" \
    "s_mov_b32 s46, %[hb]\n\t" \
    "s_add_u32 s47, s46, 0x4000\n\t" \
    "v_mov_b64 v[188:189], -1\n\t" \
    "v_mov_b32 v246, 0x1e000\n\t" \
    ".Lm1_%=:\n\t" \
    ".Lm2_%=:\n\t" \
    "ds_read_b32 v245, v246 offset:24\n\t" \
    "s_sub_i32 s44, s40, 3\n\t" \
    "s_waitcnt lgkmcnt(0)\n\t" \
    "v_cmp_gt_i32 vcc, s44, v245\n\t" \
    "s_cbranch_vccnz .Lm2_%=\n\t" \
    "s_add_u32 s44, s40, 1\n\t" \
    "s_min_u32 s44, s44, 127\n\t" \
    "s_lshr_b32 s45, s44, 1\n\t" \
    "s_and_b32 s44, s44, 1\n\t" \
    "s_lshl_b32 s44, s44, 12\n\t" \
    "s_mul_i32 s48, s45, 0x30000\n\t" \
    "s_add_u32 s48, s48, s44\n\t" \
    "s_lshr_b32 s45, s45, 3\n\t" \
    "s_mul_i32 s45, s45, 0x82000\n\t" \
    "s_add_u32 s48, s48, s45\n\t" \
    "s_add_u32 s48, s48, %[hb]\n\t" \
    "s_add_u32 s49, s48, 0x4000\n\t" \
    "s_cmp_lg_u32 s43, 0\n\t" \
    "s_cbranch_scc1 .Lm5_%=\n\t" \
    ".Lm3_%=:\n\t" \
    "v_max_u32 v244, v188, v189\n\t" \
    "v_max3_u32 v244, v190, v191, v244\n\t" \
    "v_cmp_eq_u32 vcc, -1, v244\n\t" \
    "s_cbranch_vccz .Lm4_%=\n\t" \
    "s_cmp_eq_u32 s41, 0\n\t" \
    "s_cbranch_scc1 .Lm4_%=\n\t" \
    "s_sub_u32 s41, s41, 1\n\t" \
    "s_sleep 3\n\t" \
    "buffer_load_dwordx4 v[188:191], %[l16], %[rs], s47 offen offset:3072 sc1\n\t" \
    "s_waitcnt vmcnt(0)\n\t" \
    "s_branch .Lm3_%=\n\t" \
    ".Lm4_%=:\n\t" \
    "buffer_load_dwordx4 v[184:187], %[l16], %[rs], s47 offen offset:2048 sc1\n\t" \
    "buffer_load_dwordx4 v[180:183], %[l16], %[rs], s47 offen offset:1024 sc1\n\t" \
    "buffer_load_dwordx4 v[176:179], %[l16], %[rs], s47 offen offset:0 sc1\n\t" \
    "buffer_load_dwordx4 v[172:175], %[l16], %[rs], s46 offen offset:3072 sc1\n\t" \
    "buffer_load_dwordx4 v[168:171], %[l16], %[rs], s46 offen offset:2048 sc1\n\t" \
    "buffer_load_dwordx4 v[164:167], %[l16], %[rs], s46 offen offset:1024 sc1\n\t" \
    "buffer_load_dwordx4 v[160:163], %[l16], %[rs], s46 offen offset:0 sc1\n\t" \
    ".Lm5_%=:\n\t" \
    "v_mfma_f32_16x16x32_bf16 v[224:227], v[28:31], v[188:191], 0\n\t" \
    "v_mfma_f32_16x16x32_bf16 v[228:231], v[60:63], v[188:191], 0\n\t" \
    "v_mfma_f32_16x16x32_bf16 v[232:235], v[92:95], v[188:191], 0\n\t" \
    "v_mfma_f32_16x16x32_bf16 v[236:239], v[124:127], v[188:191], 0\n\t" \
    "v_mfma_f32_16x16x32_bf16 v[240:243], v[156:159], v[188:191], 0\n\t" \
    "s_waitcnt vmcnt(6)\n\t" \
    ".Lm6_%=:\n\t" \
    "v_max_u32 v244, v184, v185\n\t" \
    "v_max3_u32 v244, v186, v187, v244\n\t" \
    "v_cmp_eq_u32 vcc, -1, v244\n\t" \
    "s_cbranch_vccnz .Lm7_%=\n\t" \
    ".Lm8_%=:\n\t" \
    "v_mfma_f32_16x16x32_bf16 v[224:227], v[24:27], v[184:187], v[224:227]\n\t" \
    "v_mfma_f32_16x16x32_bf16 v[228:231], v[56:59], v[184:187], v[228:231]\n\t" \
    "v_mfma_f32_16x16x32_bf16 v[232:235], v[88:91], v[184:187], v[232:235]\n\t" \
    "v_mfma_f32_16x16x32_bf16 v[236:239], v[120:123], v[184:187], v[236:239]\n\t" \
    "v_mfma_f32_16x16x32_bf16 v[240:243], v[152:155], v[184:187], v[240:243]\n\t" \
    "s_waitcnt vmcnt(5)\n\t" \
    ".Lm9_%=:\n\t" \
    "v_max_u32 v244, v180, v181\n\t" \
    "v_max3_u32 v244, v182, v183, v244\n\t" \
    "v_cmp_eq_u32 vcc, -1, v244\n\t" \
    "s_cbranch_vccnz .Lm10_%=\n\t" \
    ".Lm11_%=:\n\t" \
    "v_mfma_f32_16x16x32_bf16 v[224:227], v[20:23], v[180:183], v[224:227]\n\t" \
    "v_mfma_f32_16x16x32_bf16 v[228:231], v[52:55], v[180:183], v[228:231]\n\t" \
    "v_mfma_f32_16x16x32_bf16 v[232:235], v[84:87], v[180:183], v[232:235]\n\t" \
    "v_mfma_f32_16x16x32_bf16 v[236:239], v[116:119], v[180:183], v[236:239]\n\t" \
    "v_mfma_f32_16x16x32_bf16 v[240:243], v[148:151], v[180:183], v[240:243]\n\t" \
    "s_waitcnt vmcnt(4)\n\t" \
    ".Lm12_%=:\n\t" \
    "v_max_u32 v244, v176, v177\n\t" \
    "v_max3_u32 v244, v178, v179, v244\n\t" \
    "v_cmp_eq_u32 vcc, -1, v244\n\t" \
    "s_cbranch_vccnz .Lm13_%=\n\t" \
    ".Lm14_%=:\n\t" \
    "v_mfma_f32_16x16x32_bf16 v[224:227], v[16:19], v[176:179], v[224:227]\n\t" \
    "v_mfma_f32_16x16x32_bf16 v[228:231], v[48:51], v[176:179], v[228:231]\n\t" \
    "v_mfma_f32_16x16x32_bf16 v[232:235], v[80:83], v[176:179], v[232:235]\n\t" \
    "v_mfma_f32_16x16x32_bf16 v[236:239], v[112:115], v[176:179], v[236:239]\n\t" \
    "v_mfma_f32_16x16x32_bf16 v[240:243], v[144:147], v[176:179], v[240:243]\n\t" \
    "s_waitcnt vmcnt(3)\n\t" \
    ".Lm15_%=:\n\t" \
    "v_max_u32 v244, v172, v173\n\t" \
    "v_max3_u32 v244, v174, v175, v244\n\t" \
    "v_cmp_eq_u32 vcc, -1, v244\n\t" \
    "s_cbranch_vccnz .Lm16_%=\n\t" \
    ".Lm17_%=:\n\t" \
    "v_mfma_f32_16x16x32_bf16 v[224:227], v[12:15], v[172:175], v[224:227]\n\t" \
    "v_mfma_f32_16x16x32_bf16 v[228:231], v[44:47], v[172:175], v[228:231]\n\t" \
    "v_mfma_f32_16x16x32_bf16 v[232:235], v[76:79], v[172:175], v[232:235]\n\t" \
    "v_mfma_f32_16x16x32_bf16 v[236:239], v[108:111], v[172:175], v[236:239]\n\t" \
    "v_mfma_f32_16x16x32_bf16 v[240:243], v[140:143], v[172:175], v[240:243]\n\t" \
    "s_waitcnt vmcnt(2)\n\t" \
    ".Lm18_%=:\n\t" \
    "v_max_u32 v244, v168, v169\n\t" \
    "v_max3_u32 v244, v170, v171, v244\n\t" \
    "v_cmp_eq_u32 vcc, -1, v244\n\t" \
    "s_cbranch_vccnz .Lm19_%=\n\t" \
    ".Lm20_%=:\n\t" \
    "buffer_load_dwordx4 v[220:223], %[l16], %[rs], s49 offen offset:3072 sc1\n\t" \
    "v_mfma_f32_16x16x32_bf16 v[224:227], v[8:11], v[168:171], v[224:227]\n\t" \
    "v_mfma_f32_16x16x32_bf16 v[228:231], v[40:43], v[168:171], v[228:231]\n\t" \
    "v_mfma_f32_16x16x32_bf16 v[232:235], v[72:75], v[168:171], v[232:235]\n\t" \
    "v_mfma_f32_16x16x32_bf16 v[236:239], v[104:107], v[168:171], v[236:239]\n\t" \
    "v_mfma_f32_16x16x32_bf16 v[240:243], v[136:139], v[168:171], v[240:243]\n\t" \
    "s_waitcnt vmcnt(2)\n\t" \
    ".Lm21_%=:\n\t" \
    "v_max_u32 v244, v164, v165\n\t" \
    "v_max3_u32 v244, v166, v167, v244\n\t" \
    "v_cmp_eq_u32 vcc, -1, v244\n\t" \
    "s_cbranch_vccnz .Lm22_%=\n\t" \
    ".Lm23_%=:\n\t" \
    "v_mfma_f32_16x16x32_bf16 v[224:227], v[4:7], v[164:167], v[224:227]\n\t" \
    "v_mfma_f32_16x16x32_bf16 v[228:231], v[36:39], v[164:167], v[228:231]\n\t" \
    "v_mfma_f32_16x16x32_bf16 v[232:235], v[68:71], v[164:167], v[232:235]\n\t" \
    "v_mfma_f32_16x16x32_bf16 v[236:239], v[100:103], v[164:167], v[236:239]\n\t" \
    "v_mfma_f32_16x16x32_bf16 v[240:243], v[132:135], v[164:167], v[240:243]\n\t" \
    "s_waitcnt vmcnt(1)\n\t" \
    ".Lm24_%=:\n\t" \
    "v_max_u32 v244, v160, v161\n\t" \
    "v_max3_u32 v244, v162, v163, v244\n\t" \
    "v_cmp_eq_u32 vcc, -1, v244\n\t" \
    "s_cbranch_vccnz .Lm25_%=\n\t" \
    ".Lm26_%=:\n\t" \
    "v_mfma_f32_16x16x32_bf16 v[224:227], v[0:3], v[160:163], v[224:227]\n\t" \
    "v_mfma_f32_16x16x32_bf16 v[228:231], v[32:35], v[160:163], v[228:231]\n\t" \
    "v_mfma_f32_16x16x32_bf16 v[232:235], v[64:67], v[160:163], v[232:235]\n\t" \
    "v_mfma_f32_16x16x32_bf16 v[236:239], v[96:99], v[160:163], v[236:239]\n\t" \
    "v_mfma_f32_16x16x32_bf16 v[240:243], v[128:131], v[160:163], v[240:243]\n\t" \
    "s_add_u32 s40, s40, 1\n\t" \
    "v_mov_b32 v245, s40\n\t" \
    "ds_write_b128 %[ld], v[224:227] offset:0\n\t" \
    "ds_write_b128 %[ld], v[228:231] offset:6144\n\t" \
    "ds_write_b128 %[ld], v[232:235] offset:12288\n\t" \
    "s_nop 0\n\t" \
    "ds_write_b128 %[ld], v[236:239] offset:18432\n\t" \
    "s_nop 1\n\t" \
    "ds_write_b128 %[ld], v[240:243] offset:24576\n\t" \
    "ds_write_b32 %[fa], v245\n\t" \
    "s_mov_b32 s43, 0\n\t" \
    "s_waitcnt vmcnt(0)\n\t" \
    "v_max_u32 v244, v220, v221\n\t" \
    "v_max3_u32 v244, v222, v223, v244\n\t" \
    "v_cmp_eq_u32 vcc, -1, v244\n\t" \
    "s_cbranch_vccnz .Lm27_%=\n\t" \
    "buffer_load_dwordx4 v[216:219], %[l16], %[rs], s49 offen offset:2048 sc1\n\t" \
    "buffer_load_dwordx4 v[212:215], %[l16], %[rs], s49 offen offset:1024 sc1\n\t" \
    "buffer_load_dwordx4 v[208:211], %[l16], %[rs], s49 offen offset:0 sc1\n\t" \
    "buffer_load_dwordx4 v[204:207], %[l16], %[rs], s48 offen offset:3072 sc1\n\t" \
    "buffer_load_dwordx4 v[200:203], %[l16], %[rs], s48 offen offset:2048 sc1\n\t" \
    "buffer_load_dwordx4 v[196:199], %[l16], %[rs], s48 offen offset:1024 sc1\n\t" \
    "buffer_load_dwordx4 v[192:195], %[l16], %[rs], s48 offen offset:0 sc1\n\t" \
    "s_mov_b32 s43, 1\n\t" \
    ".Lm27_%=:\n\t" \
    ".Lm28_%=:\n\t" \
    "ds_read_b32 v245, v246 offset:28\n\t" \
    "s_sub_i32 s44, s40, 3\n\t" \
    "s_waitcnt lgkmcnt(0)\n\t" \
    "v_cmp_gt_i32 vcc, s44, v245\n\t" \
    "s_cbranch_vccnz .Lm28_%=\n\t" \
    "s_add_u32 s44, s40, 1\n\t" \
    "s_min_u32 s44, s44, 127\n\t" \
    "s_lshr_b32 s45, s44, 1\n\t" \
    "s_and_b32 s44, s44, 1\n\t" \
    "s_lshl_b32 s44, s44, 12\n\t" \
    "s_mul_i32 s46, s45, 0x30000\n\t" \
    "s_add_u32 s46, s46, s44\n\t" \
    "s_lshr_b32 s45, s45, 3\n\t" \
    "s_mul_i32 s45, s45, 0x82000\n\t" \
    "s_add_u32 s46, s46, s45\n\t" \
    "s_add_u32 s46, s46, %[hb]\n\t" \
    "s_add_u32 s47, s46, 0x4000\n\t" \
    "s_cmp_lg_u32 s43, 0\n\t" \
    "s_cbranch_scc1 .Lm31_%=\n\t" \
    ".Lm29_%=:\n\t" \
    "v_max_u32 v244, v220, v221\n\t" \
    "v_max3_u32 v244, v222, v223, v244\n\t" \
    "v_cmp_eq_u32 vcc, -1, v244\n\t" \
    "s_cbranch_vccz .Lm30_%=\n\t" \
    "s_cmp_eq_u32 s41, 0\n\t" \
    "s_cbranch_scc1 .Lm30_%=\n\t" \
    "s_sub_u32 s41, s41, 1\n\t" \
    "s_sleep 3\n\t" \
    "buffer_load_dwordx4 v[220:223], %[l16], %[rs], s49 offen offset:3072 sc1\n\t" \
    "s_waitcnt vmcnt(0)\n\t" \
    "s_branch .Lm29_%=\n\t" \
    ".Lm30_%=:\n\t" \
    "buffer_load_dwordx4 v[216:219], %[l16], %[rs], s49 offen offset:2048 sc1\n\t" \
    "buffer_load_dwordx4 v[212:215], %[l16], %[rs], s49 offen offset:1024 sc1\n\t" \
    "buffer_load_dwordx4 v[208:211], %[l16], %[rs], s49 offen offset:0 sc1\n\t" \
    "buffer_load_dwordx4 v[204:207], %[l16], %[rs], s48 offen offset:3072 sc1\n\t" \
    "buffer_load_dwordx4 v[200:203], %[l16], %[rs], s48 offen offset:2048 sc1\n\t" \
    "buffer_load_dwordx4 v[196:199], %[l16], %[rs], s48 offen offset:1024 sc1\n\t" \
    "buffer_load_dwordx4 v[192:195], %[l16], %[rs], s48 offen offset:0 sc1\n\t" \
    ".Lm31_%=:\n\t" \
    "v_mfma_f32_16x16x32_bf16 v[224:227], v[28:31], v[220:223], 0\n\t" \
    "v_mfma_f32_16x16x32_bf16 v[228:231], v[60:63], v[220:223], 0\n\t" \
    "v_mfma_f32_16x16x32_bf16 v[232:235], v[92:95], v[220:223], 0\n\t" \
    "v_mfma_f32_16x16x32_bf16 v[236:239], v[124:127], v[220:223], 0\n\t" \
    "v_mfma_f32_16x16x32_bf16 v[240:243], v[156:159], v[220:223], 0\n\t" \
    "s_waitcnt vmcnt(6)\n\t" \
    ".Lm32_%=:\n\t" \
    "v_max_u32 v244, v216, v217\n\t" \
    "v_max3_u32 v244, v218, v219, v244\n\t" \
    "v_cmp_eq_u32 vcc, -1, v244\n\t" \
    "s_cbranch_vccnz .Lm33_%=\n\t" \
    ".Lm34_%=:\n\t" \
    "v_mfma_f32_16x16x32_bf16 v[224:227], v[24:27], v[216:219], v[224:227]\n\t" \
    "v_mfma_f32_16x16x32_bf16 v[228:231], v[56:59], v[216:219], v[228:231]\n\t" \
    "v_mfma_f32_16x16x32_bf16 v[232:235], v[88:91], v[216:219], v[232:235]\n\t" \
    "v_mfma_f32_16x16x32_bf16 v[236:239], v[120:123], v[216:219], v[236:239]\n\t" \
    "v_mfma_f32_16x16x32_bf16 v[240:243], v[152:155], v[216:219], v[240:243]\n\t" \
    "s_waitcnt vmcnt(5)\n\t" \
    ".Lm35_%=:\n\t" \
    "v_max_u32 v244, v212, v213\n\t" \
    "v_max3_u32 v244, v214, v215, v244\n\t" \
    "v_cmp_eq_u32 vcc, -1, v244\n\t" \
    "s_cbranch_vccnz .Lm36_%=\n\t" \
    ".Lm37_%=:\n\t" \
    "v_mfma_f32_16x16x32_bf16 v[224:227], v[20:23], v[212:215], v[224:227]\n\t" \
    "v_mfma_f32_16x16x32_bf16 v[228:231], v[52:55], v[212:215], v[228:231]\n\t" \
    "v_mfma_f32_16x16x32_bf16 v[232:235], v[84:87], v[212:215], v[232:235]\n\t" \
    "v_mfma_f32_16x16x32_bf16 v[236:239], v[116:119], v[212:215], v[236:239]\n\t" \
    "v_mfma_f32_16x16x32_bf16 v[240:243], v[148:151], v[212:215], v[240:243]\n\t" \
    "s_waitcnt vmcnt(4)\n\t" \
    ".Lm38_%=:\n\t" \
    "v_max_u32 v244, v208, v209\n\t" \
    "v_max3_u32 v244, v210, v211, v244\n\t" \
    "v_cmp_eq_u32 vcc, -1, v244\n\t" \
    "s_cbranch_vccnz .Lm39_%=\n\t" \
    ".Lm40_%=:\n\t" \
    "v_mfma_f32_16x16x32_bf16 v[224:227], v[16:19], v[208:211], v[224:227]\n\t" \
    "v_mfma_f32_16x16x32_bf16 v[228:231], v[48:51], v[208:211], v[228:231]\n\t" \
    "v_mfma_f32_16x16x32_bf16 v[232:235], v[80:83], v[208:211], v[232:235]\n\t" \
    "v_mfma_f32_16x16x32_bf16 v[236:239], v[112:115], v[208:211], v[236:239]\n\t" \
    "v_mfma_f32_16x16x32_bf16 v[240:243], v[144:147], v[208:211], v[240:243]\n\t" \
    "s_waitcnt vmcnt(3)\n\t" \
    ".Lm41_%=:\n\t" \
    "v_max_u32 v244, v204, v205\n\t" \
    "v_max3_u32 v244, v206, v207, v244\n\t" \
    "v_cmp_eq_u32 vcc, -1, v244\n\t" \
    "s_cbranch_vccnz .Lm42_%=\n\t" \
    ".Lm43_%=:\n\t" \
    "v_mfma_f32_16x16x32_bf16 v[224:227], v[12:15], v[204:207], v[224:227]\n\t" \
    "v_mfma_f32_16x16x32_bf16 v[228:231], v[44:47], v[204:207], v[228:231]\n\t" \
    "v_mfma_f32_16x16x32_bf16 v[232:235], v[76:79], v[204:207], v[232:235]\n\t" \
    "v_mfma_f32_16x16x32_bf16 v[236:239], v[108:111], v[204:207], v[236:239]\n\t" \
    "v_mfma_f32_16x16x32_bf16 v[240:243], v[140:143], v[204:207], v[240:243]\n\t" \
    "s_waitcnt vmcnt(2)\n\t" \
    ".Lm44_%=:\n\t" \
    "v_max_u32 v244, v200, v201\n\t" \
    "v_max3_u32 v244, v202, v203, v244\n\t" \
    "v_cmp_eq_u32 vcc, -1, v244\n\t" \
    "s_cbranch_vccnz .Lm45_%=\n\t" \
    ".Lm46_%=:\n\t" \
    "buffer_load_dwordx4 v[188:191], %[l16], %[rs], s47 offen offset:3072 sc1\n\t" \
    "v_mfma_f32_16x16x32_bf16 v[224:227], v[8:11], v[200:203], v[224:227]\n\t" \
    "v_mfma_f32_16x16x32_bf16 v[228:231], v[40:43], v[200:203], v[228:231]\n\t" \
    "v_mfma_f32_16x16x32_bf16 v[232:235], v[72:75], v[200:203], v[232:235]\n\t" \
    "v_mfma_f32_16x16x32_bf16 v[236:239], v[104:107], v[200:203], v[236:239]\n\t" \
    "v_mfma_f32_16x16x32_bf16 v[240:243], v[136:139], v[200:203], v[240:243]\n\t" \
    "s_waitcnt vmcnt(2)\n\t" \
    ".Lm47_%=:\n\t" \
    "v_max_u32 v244, v196, v197\n\t" \
    "v_max3_u32 v244, v198, v199, v244\n\t" \
    "v_cmp_eq_u32 vcc, -1, v244\n\t" \
    "s_cbranch_vccnz .Lm48_%=\n\t" \
    ".Lm49_%=:\n\t" \
    "v_mfma_f32_16x16x32_bf16 v[224:227], v[4:7], v[196:199], v[224:227]\n\t" \
    "v_mfma_f32_16x16x32_bf16 v[228:231], v[36:39], v[196:199], v[228:231]\n\t" \
    "v_mfma_f32_16x16x32_bf16 v[232:235], v[68:71], v[196:199], v[232:235]\n\t" \
    "v_mfma_f32_16x16x32_bf16 v[236:239], v[100:103], v[196:199], v[236:239]\n\t" \
    "v_mfma_f32_16x16x32_bf16 v[240:243], v[132:135], v[196:199], v[240:243]\n\t" \
    "s_waitcnt vmcnt(1)\n\t" \
    ".Lm50_%=:\n\t" \
    "v_max_u32 v244, v192, v193\n\t" \
    "v_max3_u32 v244, v194, v195, v244\n\t" \
    "v_cmp_eq_u32 vcc, -1, v244\n\t" \
    "s_cbranch_vccnz .Lm51_%=\n\t" \
    ".Lm52_%=:\n\t" \
    "v_mfma_f32_16x16x32_bf16 v[224:227], v[0:3], v[192:195], v[224:227]\n\t" \
    "v_mfma_f32_16x16x32_bf16 v[228:231], v[32:35], v[192:195], v[228:231]\n\t" \
    "v_mfma_f32_16x16x32_bf16 v[232:235], v[64:67], v[192:195], v[232:235]\n\t" \
    "v_mfma_f32_16x16x32_bf16 v[236:239], v[96:99], v[192:195], v[236:239]\n\t" \
    "v_mfma_f32_16x16x32_bf16 v[240:243], v[128:131], v[192:195], v[240:243]\n\t" \
    "s_add_u32 s40, s40, 1\n\t" \
    "v_mov_b32 v245, s40\n\t" \
    "ds_write_b128 %[ld], v[224:227] offset:30720\n\t" \
    "ds_write_b128 %[ld], v[228:231] offset:36864\n\t" \
    "ds_write_b128 %[ld], v[232:235] offset:43008\n\t" \
    "s_nop 0\n\t" \
    "ds_write_b128 %[ld], v[236:239] offset:49152\n\t" \
    "s_nop 1\n\t" \
    "ds_write_b128 %[ld], v[240:243] offset:55296\n\t" \
    "ds_write_b32 %[fa], v245\n\t" \
    "s_mov_b32 s43, 0\n\t" \
    "s_cmp_eq_u32 s40, 128\n\t" \
    "s_cbranch_scc1 .Lm53_%=\n\t" \
    "s_waitcnt vmcnt(0)\n\t" \
    "v_max_u32 v244, v188, v189\n\t" \
    "v_max3_u32 v244, v190, v191, v244\n\t" \
    "v_cmp_eq_u32 vcc, -1, v244\n\t" \
    "s_cbranch_vccnz .Lm53_%=\n\t" \
    "buffer_load_dwordx4 v[184:187], %[l16], %[rs], s47 offen offset:2048 sc1\n\t" \
    "buffer_load_dwordx4 v[180:183], %[l16], %[rs], s47 offen offset:1024 sc1\n\t" \
    "buffer_load_dwordx4 v[176:179], %[l16], %[rs], s47 offen offset:0 sc1\n\t" \
    "buffer_load_dwordx4 v[172:175], %[l16], %[rs], s46 offen offset:3072 sc1\n\t" \
    "buffer_load_dwordx4 v[168:171], %[l16], %[rs], s46 offen offset:2048 sc1\n\t" \
    "buffer_load_dwordx4 v[164:167], %[l16], %[rs], s46 offen offset:1024 sc1\n\t" \
    "buffer_load_dwordx4 v[160:163], %[l16], %[rs], s46 offen offset:0 sc1\n\t" \
    "s_mov_b32 s43, 1\n\t" \
    ".Lm53_%=:\n\t" \
    "v_sub_u32 %[ld], %[l2], %[ld]\n\t" \
    "s_nop 0\n\t" \
    "s_cmp_lg_u32 s40, 128\n\t" \
    "s_cbranch_scc1 .Lm1_%=\n\t" \
    "s_branch .Lend%=\n\t" \
    ".Lm7_%=:\n\t" \
    "s_cmp_eq_u32 s41, 0\n\t" \
    "s_cbranch_scc1 .Lm8_%=\n\t" \
    "s_sub_u32 s41, s41, 1\n\t" \
    "buffer_load_dwordx4 v[184:187], %[l16], %[rs], s47 offen offset:2048 sc1\n\t" \
    "s_waitcnt vmcnt(0)\n\t" \
    "s_branch .Lm6_%=\n\t" \
    ".Lm10_%=:\n\t" \
    "s_cmp_eq_u32 s41, 0\n\t" \
    "s_cbranch_scc1 .Lm11_%=\n\t" \
    "s_sub_u32 s41, s41, 1\n\t" \
    "buffer_load_dwordx4 v[180:183], %[l16], %[rs], s47 offen offset:1024 sc1\n\t" \
    "s_waitcnt vmcnt(0)\n\t" \
    "s_branch .Lm9_%=\n\t" \
    ".Lm13_%=:\n\t" \
    "s_cmp_eq_u32 s41, 0\n\t" \
    "s_cbranch_scc1 .Lm14_%=\n\t" \
    "s_sub_u32 s41, s41, 1\n\t" \
    "buffer_load_dwordx4 v[176:179], %[l16], %[rs], s47 offen offset:0 sc1\n\t" \
    "s_waitcnt vmcnt(0)\n\t" \
    "s_branch .Lm12_%=\n\t" \
    ".Lm16_%=:\n\t" \
    "s_cmp_eq_u32 s41, 0\n\t" \
    "s_cbranch_scc1 .Lm17_%=\n\t" \
    "s_sub_u32 s41, s41, 1\n\t" \
    "buffer_load_dwordx4 v[172:175], %[l16], %[rs], s46 offen offset:3072 sc1\n\t" \
    "s_waitcnt vmcnt(0)\n\t" \
    "s_branch .Lm15_%=\n\t" \
    ".Lm19_%=:\n\t" \
    "s_cmp_eq_u32 s41, 0\n\t" \
    "s_cbranch_scc1 .Lm20_%=\n\t" \
    "s_sub_u32 s41, s41, 1\n\t" \
    "buffer_load_dwordx4 v[168:171], %[l16], %[rs], s46 offen offset:2048 sc1\n\t" \
    "s_waitcnt vmcnt(0)\n\t" \
    "s_branch .Lm18_%=\n\t" \
    ".Lm22_%=:\n\t" \
    "s_cmp_eq_u32 s41, 0\n\t" \
    "s_cbranch_scc1 .Lm23_%=\n\t" \
    "s_sub_u32 s41, s41, 1\n\t" \
    "buffer_load_dwordx4 v[164:167], %[l16], %[rs], s46 offen offset:1024 sc1\n\t" \
    "s_waitcnt vmcnt(0)\n\t" \
    "s_branch .Lm21_%=\n\t" \
    ".Lm25_%=:\n\t" \
    "s_cmp_eq_u32 s41, 0\n\t" \
    "s_cbranch_scc1 .Lm26_%=\n\t" \
    "s_sub_u32 s41, s41, 1\n\t" \
    "buffer_load_dwordx4 v[160:163], %[l16], %[rs], s46 offen offset:0 sc1\n\t" \
    "s_waitcnt vmcnt(0)\n\t" \
    "s_branch .Lm24_%=\n\t" \
    ".Lm33_%=:\n\t" \
    "s_cmp_eq_u32 s41, 0\n\t" \
    "s_cbranch_scc1 .Lm34_%=\n\t" \
    "s_sub_u32 s41, s41, 1\n\t" \
    "buffer_load_dwordx4 v[216:219], %[l16], %[rs], s49 offen offset:2048 sc1\n\t" \
    "s_waitcnt vmcnt(0)\n\t" \
    "s_branch .Lm32_%=\n\t" \
    ".Lm36_%=:\n\t" \
    "s_cmp_eq_u32 s41, 0\n\t" \
    "s_cbranch_scc1 .Lm37_%=\n\t" \
    "s_sub_u32 s41, s41, 1\n\t" \
    "buffer_load_dwordx4 v[212:215], %[l16], %[rs], s49 offen offset:1024 sc1\n\t" \
    "s_waitcnt vmcnt(0)\n\t" \
    "s_branch .Lm35_%=\n\t" \
    ".Lm39_%=:\n\t" \
    "s_cmp_eq_u32 s41, 0\n\t" \
    "s_cbranch_scc1 .Lm40_%=\n\t" \
    "s_sub_u32 s41, s41, 1\n\t" \
    "buffer_load_dwordx4 v[208:211], %[l16], %[rs], s49 offen offset:0 sc1\n\t" \
    "s_waitcnt vmcnt(0)\n\t" \
    "s_branch .Lm38_%=\n\t" \
    ".Lm42_%=:\n\t" \
    "s_cmp_eq_u32 s41, 0\n\t" \
    "s_cbranch_scc1 .Lm43_%=\n\t" \
    "s_sub_u32 s41, s41, 1\n\t" \
    "buffer_load_dwordx4 v[204:207], %[l16], %[rs], s48 offen offset:3072 sc1\n\t" \
    "s_waitcnt vmcnt(0)\n\t" \
    "s_branch .Lm41_%=\n\t" \
    ".Lm45_%=:\n\t" \
    "s_cmp_eq_u32 s41, 0\n\t" \
    "s_cbranch_scc1 .Lm46_%=\n\t" \
    "s_sub_u32 s41, s41, 1\n\t" \
    "buffer_load_dwordx4 v[200:203], %[l16], %[rs], s48 offen offset:2048 sc1\n\t" \
    "s_waitcnt vmcnt(0)\n\t" \
    "s_branch .Lm44_%=\n\t" \
    ".Lm48_%=:\n\t" \
    "s_cmp_eq_u32 s41, 0\n\t" \
    "s_cbranch_scc1 .Lm49_%=\n\t" \
    "s_sub_u32 s41, s41, 1\n\t" \
    "buffer_load_dwordx4 v[196:199], %[l16], %[rs], s48 offen offset:1024 sc1\n\t" \
    "s_waitcnt vmcnt(0)\n\t" \
    "s_branch .Lm47_%=\n\t" \
    ".Lm51_%=:\n\t" \
    "s_cmp_eq_u32 s41, 0\n\t" \
    "s_cbranch_scc1 .Lm52_%=\n\t" \
    "s_sub_u32 s41, s41, 1\n\t" \
    "buffer_load_dwordx4 v[192:195], %[l16], %[rs], s48 offen offset:0 sc1\n\t" \
    "s_waitcnt vmcnt(0)\n\t" \
    "s_branch .Lm50_%=\n\t" \
    ".Lend%=:\n\t" \
    "s_waitcnt vmcnt(0) lgkmcnt(0)\n\t" \
    "s_mov_b32 %[bud], s41\n\t"
#define ASM_W4_FIN0 \
    "s_mov_b32 s40, %[mt]\n\t" \
    "s_mov_b32 s41, 0x40000\n\t" \
    "s_mov_b32 s43, 0\n\t" \
    "v_mov_b32 v99, 0x3f803f80\n\t" \
    "s_setprio 3\n\t" \
    "s_add_u32 s46, s43, 0x2c00000\n\t" \
    "s_add_u32 s47, s43, 0x1c00000\n\t" \
    "buffer_load_dwordx2 v[120:121], %[t0], %[rs], s46 offen sc1\n\t" \
    "buffer_load_dwordx2 v[122:123], %[t0], %[rs], s47 offen sc1\n\t" \
    "buffer_load_dwordx2 v[124:125], %[t1], %[rs], s46 offen sc1\n\t" \
    "buffer_load_dwordx2 v[126:127], %[t1], %[rs], s47 offen sc1\n\t" \
    "buffer_load_dwordx2 v[128:129], %[t2], %[rs], s46 offen sc1\n\t" \
    "buffer_load_dwordx2 v[130:131], %[t2], %[rs], s47 offen sc1\n\t" \
    "buffer_load_dwordx2 v[132:133], %[t3], %[rs], s46 offen sc1\n\t" \
    "buffer_load_dwordx2 v[134:135], %[t3], %[rs], s47 offen sc1\n\t" \
    "buffer_load_dwordx2 v[136:137], %[t4], %[rs], s46 offen sc1\n\t" \
    "buffer_load_dwordx2 v[138:139], %[t4], %[rs], s47 offen sc1\n\t" \
    "s_waitcnt vmcnt(0)\n\t" \
    ".Lf01_%=:\n\t" \
    ".Lf02_%=:\n\t" \
    "s_waitcnt vmcnt(1)\n\t" \
    "v_max_u32 v80, v120, v121\n\t" \
    "v_max3_u32 v80, v122, v123, v80\n\t" \
    "v_max3_u32 v80, v124, v125, v80\n\t" \
    "v_max3_u32 v80, v126, v127, v80\n\t" \
    "v_max3_u32 v80, v128, v129, v80\n\t" \
    "v_max3_u32 v80, v130, v131, v80\n\t" \
    "v_max3_u32 v80, v132, v133, v80\n\t" \
    "v_max3_u32 v80, v134, v135, v80\n\t" \
    "v_max3_u32 v80, v136, v137, v80\n\t" \
    "v_max3_u32 v80, v138, v139, v80\n\t" \
    "v_cmp_eq_u32 vcc, -1, v80\n\t" \
    "s_cbranch_vccz .Lf03_%=\n\t" \
    "s_cmp_eq_u32 s41, 0\n\t" \
    "s_cbranch_scc1 .Lf03_%=\n\t" \
    "s_sub_u32 s41, s41, 1\n\t" \
    "s_sleep 8\n\t" \
    "s_add_u32 s46, s43, 0x2c00000\n\t" \
    "s_add_u32 s47, s43, 0x1c00000\n\t" \
    "buffer_load_dwordx2 v[120:121], %[t0], %[rs], s46 offen sc1\n\t" \
    "buffer_load_dwordx2 v[122:123], %[t0], %[rs], s47 offen sc1\n\t" \
    "buffer_load_dwordx2 v[124:125], %[t1], %[rs], s46 offen sc1\n\t" \
    "buffer_load_dwordx2 v[126:127], %[t1], %[rs], s47 offen sc1\n\t" \
    "buffer_load_dwordx2 v[128:129], %[t2], %[rs], s46 offen sc1\n\t" \
    "buffer_load_dwordx2 v[130:131], %[t2], %[rs], s47 offen sc1\n\t" \
    "buffer_load_dwordx2 v[132:133], %[t3], %[rs], s46 offen sc1\n\t" \
    "buffer_load_dwordx2 v[134:135], %[t3], %[rs], s47 offen sc1\n\t" \
    "buffer_load_dwordx2 v[136:137], %[t4], %[rs], s46 offen sc1\n\t" \
    "buffer_load_dwordx2 v[138:139], %[t4], %[rs], s47 offen sc1\n\t" \
    "s_waitcnt vmcnt(0)\n\t" \
    "s_branch .Lf02_%=\n\t" \
    ".Lf03_%=:\n\t" \
    "v_lshlrev_b32 v140, 16, v120\n\t" \
    "v_and_b32 v141, 0xffff0000, v120\n\t" \
    "v_lshlrev_b32 v142, 16, v121\n\t" \
    "v_and_b32 v143, 0xffff0000, v121\n\t" \
    "v_lshlrev_b32 v80, 16, v122\n\t" \
    "v_and_b32 v81, 0xffff0000, v122\n\t" \
    "v_lshlrev_b32 v82, 16, v123\n\t" \
    "v_and_b32 v83, 0xffff0000, v123\n\t" \
    "v_add_f32 v140, v140, v80\n\t" \
    "v_add_f32 v141, v141, v81\n\t" \
    "v_add_f32 v142, v142, v82\n\t" \
    "v_add_f32 v143, v143, v83\n\t" \
    "v_lshlrev_b32 v144, 16, v124\n\t" \
    "v_and_b32 v145, 0xffff0000, v124\n\t" \
    "v_lshlrev_b32 v146, 16, v125\n\t" \
    "v_and_b32 v147, 0xffff0000, v125\n\t" \
    "v_lshlrev_b32 v80, 16, v126\n\t" \
    "v_and_b32 v81, 0xffff0000, v126\n\t" \
    "v_lshlrev_b32 v82, 16, v127\n\t" \
    "v_and_b32 v83, 0xffff0000, v127\n\t" \
    "v_add_f32 v144, v144, v80\n\t" \
    "v_add_f32 v145, v145, v81\n\t" \
    "v_add_f32 v146, v146, v82\n\t" \
    "v_add_f32 v147, v147, v83\n\t" \
    "v_lshlrev_b32 v148, 16, v128\n\t" \
    "v_and_b32 v149, 0xffff0000, v128\n\t" \
    "v_lshlrev_b32 v150, 16, v129\n\t" \
    "v_and_b32 v151, 0xffff0000, v129\n\t" \
    "v_lshlrev_b32 v80, 16, v130\n\t" \
    "v_and_b32 v81, 0xffff0000, v130\n\t" \
    "v_lshlrev_b32 v82, 16, v131\n\t" \
    "v_and_b32 v83, 0xffff0000, v131\n\t" \
    "v_add_f32 v148, v148, v80\n\t" \
    "v_add_f32 v149, v149, v81\n\t" \
    "v_add_f32 v150, v150, v82\n\t" \
    "v_add_f32 v151, v151, v83\n\t" \
    "v_lshlrev_b32 v152, 16, v132\n\t" \
    "v_and_b32 v153, 0xffff0000, v132\n\t" \
    "v_lshlrev_b32 v154, 16, v133\n\t" \
    "v_and_b32 v155, 0xffff0000, v133\n\t" \
    "v_lshlrev_b32 v80, 16, v134\n\t" \
    "v_and_b32 v81, 0xffff0000, v134\n\t" \
    "v_lshlrev_b32 v82, 16, v135\n\t" \
    "v_and_b32 v83, 0xffff0000, v135\n\t" \
    "v_add_f32 v152, v152, v80\n\t" \
    "v_add_f32 v153, v153, v81\n\t" \
    "v_add_f32 v154, v154, v82\n\t" \
    "v_add_f32 v155, v155, v83\n\t" \
    "v_lshlrev_b32 v156, 16, v136\n\t" \
    "v_and_b32 v157, 0xffff0000, v136\n\t" \
    "v_lshlrev_b32 v158, 16, v137\n\t" \
    "v_and_b32 v159, 0xffff0000, v137\n\t" \
    "v_lshlrev_b32 v80, 16, v138\n\t" \
    "v_and_b32 v81, 0xffff0000, v138\n\t" \
    "v_lshlrev_b32 v82, 16, v139\n\t" \
    "v_and_b32 v83, 0xffff0000, v139\n\t" \
    "v_add_f32 v156, v156, v80\n\t" \
    "v_add_f32 v157, v157, v81\n\t" \
    "v_add_f32 v158, v158, v82\n\t" \
    "v_add_f32 v159, v159, v83\n\t" \
    "s_mov_b32 s42, s43\n\t" \
    "s_lshr_b32 s48, s40, 1\n\t" \
    "s_add_u32 s48, s48, 1\n\t" \
    "s_mul_i32 s43, s48, 0x14000\n\t" \
    "s_lshr_b32 s44, s48, 4\n\t" \
    "s_mul_i32 s44, s44, 0xc2000\n\t" \
    "s_add_u32 s43, s43, s44\n\t" \
    "s_lshr_b32 s44, s40, 3\n\t" \
    "s_lshl_b32 s44, s44, 14\n\t" \
    "s_and_b32 s45, s40, 7\n\t" \
    "s_lshl_b32 s45, s45, 10\n\t" \
    "s_add_u32 s49, s44, s45\n\t" \
    "s_add_u32 s49, s49, 0x1c0000\n\t" \
    "s_add_u32 s50, s49, %[co]\n\t" \
    "s_add_u32 s51, s42, 0x1400000\n\t" \
    "s_add_u32 s52, s42, 0xc00000\n\t" \
    "s_mov_b32 s44, s48\n\t" \
    "s_mov_b32 s45, s48\n\t" \
    "v_mov_b64 v[76:77], s[44:45]\n\t" \
    "v_mov_b64 v[78:79], s[44:45]\n\t" \
    "s_add_u32 s44, s40, 1\n\t" \
    "v_mov_b32 v98, s44\n\t" \
    ".Lf04_%=:\n\t" \
    "s_nop 7\n\t" \
    "ds_read_b32 v80, %[fa]\n\t" \
    "s_waitcnt lgkmcnt(0)\n\t" \
    "v_cmp_ge_u32 vcc, s40, v80\n\t" \
    "s_cbranch_vccnz .Lf04_%=\n\t" \
    "ds_read_b128 v[160:163], %[la] offset:0\n\t" \
    "ds_read_b128 v[164:167], %[la] offset:6144\n\t" \
    "ds_read_b128 v[168:171], %[la] offset:12288\n\t" \
    "ds_read_b128 v[172:175], %[la] offset:18432\n\t" \
    "ds_read_b128 v[176:179], %[la] offset:24576\n\t" \
    "ds_read_b128 v[180:183], %[la] offset:1024\n\t" \
    "ds_read_b128 v[184:187], %[la] offset:7168\n\t" \
    "ds_read_b128 v[188:191], %[la] offset:13312\n\t" \
    "ds_read_b128 v[192:195], %[la] offset:19456\n\t" \
    "ds_read_b128 v[196:199], %[la] offset:25600\n\t" \
    "ds_read_b128 v[200:203], %[la] offset:2048\n\t" \
    "ds_read_b128 v[204:207], %[la] offset:8192\n\t" \
    "ds_read_b128 v[208:211], %[la] offset:14336\n\t" \
    "ds_read_b128 v[212:215], %[la] offset:20480\n\t" \
    "ds_read_b128 v[216:219], %[la] offset:26624\n\t" \
    "s_waitcnt lgkmcnt(14)\n\t" \
    "v_pk_add_f32 v[100:101], v[140:141], v[160:161]\n\t" \
    "v_pk_add_f32 v[102:103], v[142:143], v[162:163]\n\t" \
    "ds_read_b128 v[220:223], %[la] offset:3072\n\t" \
    "s_waitcnt lgkmcnt(14)\n\t" \
    "v_pk_add_f32 v[104:105], v[144:145], v[164:165]\n\t" \
    "v_pk_add_f32 v[106:107], v[146:147], v[166:167]\n\t" \
    "ds_read_b128 v[224:227], %[la] offset:9216\n\t" \
    "s_waitcnt lgkmcnt(14)\n\t" \
    "v_pk_add_f32 v[108:109], v[148:149], v[168:169]\n\t" \
    "v_pk_add_f32 v[110:111], v[150:151], v[170:171]\n\t" \
    "ds_read_b128 v[228:231], %[la] offset:15360\n\t" \
    "s_waitcnt lgkmcnt(14)\n\t" \
    "v_pk_add_f32 v[112:113], v[152:153], v[172:173]\n\t" \
    "v_pk_add_f32 v[114:115], v[154:155], v[174:175]\n\t" \
    "ds_read_b128 v[232:235], %[la] offset:21504\n\t" \
    "s_waitcnt lgkmcnt(14)\n\t" \
    "v_pk_add_f32 v[116:117], v[156:157], v[176:177]\n\t" \
    "v_pk_add_f32 v[118:119], v[158:159], v[178:179]\n\t" \
    "ds_read_b128 v[236:239], %[la] offset:27648\n\t" \
    "s_waitcnt lgkmcnt(14)\n\t" \
    "v_pk_add_f32 v[100:101], v[100:101], v[180:181]\n\t" \
    "v_pk_add_f32 v[102:103], v[102:103], v[182:183]\n\t" \
    "ds_read_b128 v[160:163], %[la] offset:4096\n\t" \
    "s_waitcnt lgkmcnt(14)\n\t" \
    "v_pk_add_f32 v[104:105], v[104:105], v[184:185]\n\t" \
    "v_pk_add_f32 v[106:107], v[106:107], v[186:187]\n\t" \
    "ds_read_b128 v[164:167], %[la] offset:10240\n\t" \
    "s_waitcnt lgkmcnt(14)\n\t" \
    "v_pk_add_f32 v[108:109], v[108:109], v[188:189]\n\t" \
    "v_pk_add_f32 v[110:111], v[110:111], v[190:191]\n\t" \
    "ds_read_b128 v[168:171], %[la] offset:16384\n\t" \
    "s_waitcnt lgkmcnt(14)\n\t" \
    "v_pk_add_f32 v[112:113], v[112:113], v[192:193]\n\t" \
    "v_pk_add_f32 v[114:115], v[114:115], v[194:195]\n\t" \
    "ds_read_b128 v[172:175], %[la] offset:22528\n\t" \
    "s_waitcnt lgkmcnt(14)\n\t" \
    "v_pk_add_f32 v[116:117], v[116:117], v[196:197]\n\t" \
    "v_pk_add_f32 v[118:119], v[118:119], v[198:199]\n\t" \
    "ds_read_b128 v[176:179], %[la] offset:28672\n\t" \
    "s_waitcnt lgkmcnt(14)\n\t" \
    "v_pk_add_f32 v[100:101], v[100:101], v[200:201]\n\t" \
    "v_pk_add_f32 v[102:103], v[102:103], v[202:203]\n\t" \
    "ds_read_b128 v[180:183], %[la] offset:5120\n\t" \
    "s_waitcnt lgkmcnt(14)\n\t" \
    "v_pk_add_f32 v[104:105], v[104:105], v[204:205]\n\t" \
    "v_pk_add_f32 v[106:107], v[106:107], v[206:207]\n\t" \
    "ds_read_b128 v[184:187], %[la] offset:11264\n\t" \
    "s_waitcnt lgkmcnt(14)\n\t" \
    "v_pk_add_f32 v[108:109], v[108:109], v[208:209]\n\t" \
    "v_pk_add_f32 v[110:111], v[110:111], v[210:211]\n\t" \
    "ds_read_b128 v[188:191], %[la] offset:17408\n\t" \
    "s_waitcnt lgkmcnt(14)\n\t" \
    "v_pk_add_f32 v[112:113], v[112:113], v[212:213]\n\t" \
    "v_pk_add_f32 v[114:115], v[114:115], v[214:215]\n\t" \
    "ds_read_b128 v[192:195], %[la] offset:23552\n\t" \
    "s_waitcnt lgkmcnt(14)\n\t" \
    "v_pk_add_f32 v[116:117], v[116:117], v[216:217]\n\t" \
    "v_pk_add_f32 v[118:119], v[118:119], v[218:219]\n\t" \
    "ds_read_b128 v[196:199], %[la] offset:29696\n\t" \
    "s_waitcnt lgkmcnt(14)\n\t" \
    "v_pk_add_f32 v[100:101], v[100:101], v[220:221]\n\t" \
    "v_pk_add_f32 v[102:103], v[102:103], v[222:223]\n\t" \
    "s_waitcnt lgkmcnt(13)\n\t" \
    "v_pk_add_f32 v[104:105], v[104:105], v[224:225]\n\t" \
    "v_pk_add_f32 v[106:107], v[106:107], v[226:227]\n\t" \
    "s_waitcnt lgkmcnt(12)\n\t" \
    "v_pk_add_f32 v[108:109], v[108:109], v[228:229]\n\t" \
    "v_pk_add_f32 v[110:111], v[110:111], v[230:231]\n\t" \
    "s_waitcnt lgkmcnt(11)\n\t" \
    "v_pk_add_f32 v[112:113], v[112:113], v[232:233]\n\t" \
    "v_pk_add_f32 v[114:115], v[114:115], v[234:235]\n\t" \
    "s_waitcnt lgkmcnt(10)\n\t" \
    "v_pk_add_f32 v[116:117], v[116:117], v[236:237]\n\t" \
    "v_pk_add_f32 v[118:119], v[118:119], v[238:239]\n\t" \
    "s_waitcnt lgkmcnt(9)\n\t" \
    "v_pk_add_f32 v[100:101], v[100:101], v[160:161]\n\t" \
    "v_pk_add_f32 v[102:103], v[102:103], v[162:163]\n\t" \
    "s_waitcnt lgkmcnt(8)\n\t" \
    "v_pk_add_f32 v[104:105], v[104:105], v[164:165]\n\t" \
    "v_pk_add_f32 v[106:107], v[106:107], v[166:167]\n\t" \
    "s_waitcnt lgkmcnt(7)\n\t" \
    "v_pk_add_f32 v[108:109], v[108:109], v[168:169]\n\t" \
    "v_pk_add_f32 v[110:111], v[110:111], v[170:171]\n\t" \
    "s_waitcnt lgkmcnt(6)\n\t" \
    "v_pk_add_f32 v[112:113], v[112:113], v[172:173]\n\t" \
    "v_pk_add_f32 v[114:115], v[114:115], v[174:175]\n\t" \
    "s_waitcnt lgkmcnt(5)\n\t" \
    "v_pk_add_f32 v[116:117], v[116:117], v[176:177]\n\t" \
    "v_pk_add_f32 v[118:119], v[118:119], v[178:179]\n\t" \
    "s_waitcnt lgkmcnt(4)\n\t" \
    "v_pk_add_f32 v[100:101], v[100:101], v[180:181]\n\t" \
    "v_pk_add_f32 v[102:103], v[102:103], v[182:183]\n\t" \
    "s_waitcnt lgkmcnt(3)\n\t" \
    "v_pk_add_f32 v[104:105], v[104:105], v[184:185]\n\t" \
    "v_pk_add_f32 v[106:107], v[106:107], v[186:187]\n\t" \
    "s_waitcnt lgkmcnt(2)\n\t" \
    "v_pk_add_f32 v[108:109], v[108:109], v[188:189]\n\t" \
    "v_pk_add_f32 v[110:111], v[110:111], v[190:191]\n\t" \
    "s_waitcnt lgkmcnt(1)\n\t" \
    "v_pk_add_f32 v[112:113], v[112:113], v[192:193]\n\t" \
    "v_pk_add_f32 v[114:115], v[114:115], v[194:195]\n\t" \
    "s_waitcnt lgkmcnt(0)\n\t" \
    "v_pk_add_f32 v[116:117], v[116:117], v[196:197]\n\t" \
    "v_pk_add_f32 v[118:119], v[118:119], v[198:199]\n\t" \
    "ds_write_b32 %[fc], v98\n\t" \
    "v_sub_u32 %[la], %[l2], %[la]\n\t" \
    "v_cvt_pk_bf16_f32 v84, v100, v101\n\t" \
    "v_cvt_pk_bf16_f32 v85, v102, v103\n\t" \
    "v_cvt_pk_bf16_f32 v86, v104, v105\n\t" \
    "v_cvt_pk_bf16_f32 v87, v106, v107\n\t" \
    "v_cvt_pk_bf16_f32 v88, v108, v109\n\t" \
    "v_cvt_pk_bf16_f32 v89, v110, v111\n\t" \
    "v_cvt_pk_bf16_f32 v90, v112, v113\n\t" \
    "v_cvt_pk_bf16_f32 v91, v114, v115\n\t" \
    "v_cvt_pk_bf16_f32 v92, v116, v117\n\t" \
    "v_cvt_pk_bf16_f32 v93, v118, v119\n\t" \
    "v_mov_b32 v80, 0\n\t" \
    "v_mov_b32 v82, 0\n\t" \
    "v_dot2c_f32_bf16 v80, v84, v99\n\t" \
    "v_dot2c_f32_bf16 v82, v84, v84\n\t" \
    "v_dot2c_f32_bf16 v80, v85, v99\n\t" \
    "v_dot2c_f32_bf16 v82, v85, v85\n\t" \
    "v_dot2c_f32_bf16 v80, v86, v99\n\t" \
    "v_dot2c_f32_bf16 v82, v86, v86\n\t" \
    "v_dot2c_f32_bf16 v80, v87, v99\n\t" \
    "v_dot2c_f32_bf16 v82, v87, v87\n\t" \
    "v_dot2c_f32_bf16 v80, v88, v99\n\t" \
    "v_dot2c_f32_bf16 v82, v88, v88\n\t" \
    "v_dot2c_f32_bf16 v80, v89, v99\n\t" \
    "v_dot2c_f32_bf16 v82, v89, v89\n\t" \
    "v_dot2c_f32_bf16 v80, v90, v99\n\t" \
    "v_dot2c_f32_bf16 v82, v90, v90\n\t" \
    "v_dot2c_f32_bf16 v80, v91, v99\n\t" \
    "v_dot2c_f32_bf16 v82, v91, v91\n\t" \
    "v_dot2c_f32_bf16 v80, v92, v99\n\t" \
    "v_dot2c_f32_bf16 v82, v92, v92\n\t" \
    "v_dot2c_f32_bf16 v80, v93, v99\n\t" \
    "v_dot2c_f32_bf16 v82, v93, v93\n\t" \
    "s_nop 2\n\t" \
    "v_mov_b32 v81, v80\n\t" \
    "v_mov_b32 v83, v82\n\t" \
    "s_nop 1\n\t" \
    "v_permlane16_swap_b32 v80, v81\n\t" \
    "v_permlane16_swap_b32 v82, v83\n\t" \
    "v_add_f32 v80, v80, v81\n\t" \
    "v_add_f32 v82, v82, v83\n\t" \
    "v_mov_b32 v81, v80\n\t" \
    "v_mov_b32 v83, v82\n\t" \
    "s_nop 1\n\t" \
    "v_permlane32_swap_b32 v80, v81\n\t" \
    "v_permlane32_swap_b32 v82, v83\n\t" \
    "v_add_f32 v80, v80, v81\n\t" \
    "v_add_f32 v82, v82, v83\n\t" \
    "v_min_u32 v80, -2, v80\n\t" \
    "v_min_u32 v81, -2, v82\n\t" \
    "s_mov_b64 exec, 0xffff\n\t" \
    "buffer_store_dwordx2 v[80:81], %[l8], %[rs], s50 offen sc1\n\t" \
    "s_mov_b64 exec, -1\n\t" \
    "s_add_u32 s46, s43, 0x2c00000\n\t" \
    "s_add_u32 s47, s43, 0x1c00000\n\t" \
    "buffer_load_dwordx2 v[120:121], %[t0], %[rs], s46 offen sc1\n\t" \
    "buffer_load_dwordx2 v[122:123], %[t0], %[rs], s47 offen sc1\n\t" \
    "buffer_load_dwordx2 v[124:125], %[t1], %[rs], s46 offen sc1\n\t" \
    "buffer_load_dwordx2 v[126:127], %[t1], %[rs], s47 offen sc1\n\t" \
    "buffer_load_dwordx2 v[128:129], %[t2], %[rs], s46 offen sc1\n\t" \
    "buffer_load_dwordx2 v[130:131], %[t2], %[rs], s47 offen sc1\n\t" \
    "buffer_load_dwordx2 v[132:133], %[t3], %[rs], s46 offen sc1\n\t" \
    "buffer_load_dwordx2 v[134:135], %[t3], %[rs], s47 offen sc1\n\t" \
    "buffer_load_dwordx2 v[136:137], %[t4], %[rs], s46 offen sc1\n\t" \
    "buffer_load_dwordx2 v[138:139], %[t4], %[rs], s47 offen sc1\n\t" \
    "s_nop 0\n\t" \
    "v_permlane16_swap_b32 v84, v86\n\t" \
    "v_permlane16_swap_b32 v85, v87\n\t" \
    "v_permlane16_swap_b32 v88, v90\n\t" \
    "v_permlane16_swap_b32 v89, v91\n\t" \
    "s_sleep 4\n\t" \
    ".Lf05_%=:\n\t" \
    "buffer_load_dwordx2 v[94:95], %[l8], %[rs], s49 offen sc1\n\t" \
    "buffer_load_dwordx2 v[96:97], %[l8], %[rs], s49 offen offset:512 sc1\n\t" \
    "s_waitcnt vmcnt(0)\n\t" \
    "v_max_u32 v80, v94, v95\n\t" \
    "v_max3_u32 v80, v96, v97, v80\n\t" \
    "v_cmp_eq_u32 vcc, -1, v80\n\t" \
    "s_cbranch_vccz .Lf06_%=\n\t" \
    "s_cmp_eq_u32 s41, 0\n\t" \
    "s_cbranch_scc1 .Lf06_%=\n\t" \
    "s_sub_u32 s41, s41, 1\n\t" \
    "s_branch .Lf05_%=\n\t" \
    ".Lf06_%=:\n\t" \
    "v_add_f32 v80, v94, v96\n\t" \
    "v_add_f32 v82, v95, v97\n\t" \
    "v_mov_b32 v81, v80\n\t" \
    "v_mov_b32 v83, v82\n\t" \
    "s_nop 1\n\t" \
    "v_permlane16_swap_b32 v80, v81\n\t" \
    "v_permlane16_swap_b32 v82, v83\n\t" \
    "v_add_f32 v80, v80, v81\n\t" \
    "v_add_f32 v82, v82, v83\n\t" \
    "v_mov_b32 v81, v80\n\t" \
    "v_mov_b32 v83, v82\n\t" \
    "s_nop 1\n\t" \
    "v_permlane32_swap_b32 v80, v81\n\t" \
    "v_permlane32_swap_b32 v82, v83\n\t" \
    "v_add_f32 v80, v80, v81\n\t" \
    "v_add_f32 v82, v82, v83\n\t" \
    "v_mul_f32 v80, 0x3acccccd, v80\n\t" \
    "v_mul_f32 v82, 0x3acccccd, v82\n\t" \
    "v_fma_f32 v82, -v80, v80, v82\n\t" \
    "v_cvt_pk_bf16_f32 v80, v80, v80\n\t" \
    "v_and_b32 v80, 0xffff0000, v80\n\t" \
    "v_max_f32 v82, 0, v82\n\t" \
    "v_cvt_pk_bf16_f32 v82, v82, v82\n\t" \
    "v_and_b32 v82, 0xffff0000, v82\n\t" \
    "v_add_f32 v82, 0x3727c5ac, v82\n\t" \
    "v_cvt_pk_bf16_f32 v82, v82, v82\n\t" \
    "v_and_b32 v82, 0xffff0000, v82\n\t" \
    "v_sqrt_f32 v82, v82\n\t" \
    "s_nop 0\n\t" \
    "v_cvt_pk_bf16_f32 v82, v82, v82\n\t" \
    "v_and_b32 v82, 0xffff0000, v82\n\t" \
    "v_rcp_f32 v82, v82\n\t" \
    "s_nop 0\n\t" \
    "v_mul_f32 v80, -v80, v82\n\t" \
    "v_fma_f32 v100, v100, v82, v80\n\t" \
    "v_fma_f32 v101, v101, v82, v80\n\t" \
    "v_fma_f32 v102, v102, v82, v80\n\t" \
    "v_fma_f32 v103, v103, v82, v80\n\t" \
    "v_fma_f32 v104, v104, v82, v80\n\t" \
    "v_fma_f32 v105, v105, v82, v80\n\t" \
    "v_fma_f32 v106, v106, v82, v80\n\t" \
    "v_fma_f32 v107, v107, v82, v80\n\t" \
    "v_fma_f32 v108, v108, v82, v80\n\t" \
    "v_fma_f32 v109, v109, v82, v80\n\t" \
    "v_fma_f32 v110, v110, v82, v80\n\t" \
    "v_fma_f32 v111, v111, v82, v80\n\t" \
    "v_fma_f32 v112, v112, v82, v80\n\t" \
    "v_fma_f32 v113, v113, v82, v80\n\t" \
    "v_fma_f32 v114, v114, v82, v80\n\t" \
    "v_fma_f32 v115, v115, v82, v80\n\t" \
    "v_fma_f32 v116, v116, v82, v80\n\t" \
    "v_fma_f32 v117, v117, v82, v80\n\t" \
    "v_fma_f32 v118, v118, v82, v80\n\t" \
    "v_fma_f32 v119, v119, v82, v80\n\t" \
    "v_cvt_pk_bf16_f32 v64, v100, v101\n\t" \
    "v_cvt_pk_bf16_f32 v65, v102, v103\n\t" \
    "v_cvt_pk_bf16_f32 v66, v104, v105\n\t" \
    "v_cvt_pk_bf16_f32 v67, v106, v107\n\t" \
    "v_cvt_pk_bf16_f32 v68, v108, v109\n\t" \
    "v_cvt_pk_bf16_f32 v69, v110, v111\n\t" \
    "v_cvt_pk_bf16_f32 v70, v112, v113\n\t" \
    "v_cvt_pk_bf16_f32 v71, v114, v115\n\t" \
    "v_cvt_pk_bf16_f32 v72, v116, v117\n\t" \
    "v_cvt_pk_bf16_f32 v73, v118, v119\n\t" \
    "s_nop 0\n\t" \
    "v_permlane16_swap_b32 v64, v66\n\t" \
    "v_permlane16_swap_b32 v65, v67\n\t" \
    "v_permlane16_swap_b32 v68, v70\n\t" \
    "v_permlane16_swap_b32 v69, v71\n\t" \
    "buffer_store_dwordx4 v[64:67], %[o16], %[rn], s51 offen sc1\n\t" \
    "buffer_store_dwordx4 v[68:71], %[o17], %[rn], s51 offen sc1\n\t" \
    "buffer_store_dwordx2 v[72:73], %[t4], %[rn], s51 offen sc1\n\t" \
    "buffer_store_dwordx4 v[84:87], %[o16], %[rn], s52 offen sc1\n\t" \
    "buffer_store_dwordx4 v[88:91], %[o17], %[rn], s52 offen sc1\n\t" \
    "buffer_store_dwordx2 v[92:93], %[t4], %[rn], s52 offen sc1\n\t" \
    "s_waitcnt vmcnt(0)\n\t" \
    "buffer_store_dwordx4 v[76:79], %[fo], %[rn], %[fs] offen sc1\n\t" \
    "s_add_u32 s40, s40, 2\n\t" \
    "s_cmp_lt_u32 s40, 128\n\t" \
    "s_cbranch_scc1 .Lf01_%=\n\t" \
    "s_branch .Lend%=\n\t" \
    ".Lend%=:\n\t" \
    "s_waitcnt vmcnt(0) lgkmcnt(0)\n\t" \
    "s_mov_b32 %[bud], s41\n\t"
#define ASM_W4_FIN1 \
    "s_mov_b32 s40, %[mt]\n\t" \
    "s_mov_b32 s41, 0x40000\n\t" \
    "s_mov_b32 s43, 0\n\t" \
    "v_mov_b32 v99, 0x3f803f80\n\t" \
    "s_setprio 3\n\t" \
    "s_add_u32 s46, s43, 0x2c00000\n\t" \
    "s_add_u32 s47, s43, 0x1c00000\n\t" \
    "buffer_load_dwordx2 v[120:121], %[t0], %[rs], s46 offen sc1\n\t" \
    "buffer_load_dwordx2 v[122:123], %[t0], %[rs], s47 offen sc1\n\t" \
    "buffer_load_dwordx2 v[124:125], %[t1], %[rs], s46 offen sc1\n\t" \
    "buffer_load_dwordx2 v[126:127], %[t1], %[rs], s47 offen sc1\n\t" \
    "buffer_load_dwordx2 v[128:129], %[t2], %[rs], s46 offen sc1\n\t" \
    "buffer_load_dwordx2 v[130:131], %[t2], %[rs], s47 offen sc1\n\t" \
    "buffer_load_dwordx2 v[132:133], %[t3], %[rs], s46 offen sc1\n\t" \
    "buffer_load_dwordx2 v[134:135], %[t3], %[rs], s47 offen sc1\n\t" \
    "buffer_load_dwordx2 v[136:137], %[t4], %[rs], s46 offen sc1\n\t" \
    "buffer_load_dwordx2 v[138:139], %[t4], %[rs], s47 offen sc1\n\t" \
    ".Lf11_%=:\n\t" \
    ".Lf12_%=:\n\t" \
    "s_waitcnt vmcnt(0)\n\t" \
    "v_max_u32 v80, v120, v121\n\t" \
    "v_max3_u32 v80, v122, v123, v80\n\t" \
    "v_max3_u32 v80, v124, v125, v80\n\t" \
    "v_max3_u32 v80, v126, v127, v80\n\t" \
    "v_max3_u32 v80, v128, v129, v80\n\t" \
    "v_max3_u32 v80, v130, v131, v80\n\t" \
    "v_max3_u32 v80, v132, v133, v80\n\t" \
    "v_max3_u32 v80, v134, v135, v80\n\t" \
    "v_max3_u32 v80, v136, v137, v80\n\t" \
    "v_max3_u32 v80, v138, v139, v80\n\t" \
    "v_cmp_eq_u32 vcc, -1, v80\n\t" \
    "s_cbranch_vccz .Lf13_%=\n\t" \
    "s_cmp_eq_u32 s41, 0\n\t" \
    "s_cbranch_scc1 .Lf13_%=\n\t" \
    "s_sub_u32 s41, s41, 1\n\t" \
    "s_sleep 8\n\t" \
    "s_add_u32 s46, s43, 0x2c00000\n\t" \
    "s_add_u32 s47, s43, 0x1c00000\n\t" \
    "buffer_load_dwordx2 v[120:121], %[t0], %[rs], s46 offen sc1\n\t" \
    "buffer_load_dwordx2 v[122:123], %[t0], %[rs], s47 offen sc1\n\t" \
    "buffer_load_dwordx2 v[124:125], %[t1], %[rs], s46 offen sc1\n\t" \
    "buffer_load_dwordx2 v[126:127], %[t1], %[rs], s47 offen sc1\n\t" \
    "buffer_load_dwordx2 v[128:129], %[t2], %[rs], s46 offen sc1\n\t" \
    "buffer_load_dwordx2 v[130:131], %[t2], %[rs], s47 offen sc1\n\t" \
    "buffer_load_dwordx2 v[132:133], %[t3], %[rs], s46 offen sc1\n\t" \
    "buffer_load_dwordx2 v[134:135], %[t3], %[rs], s47 offen sc1\n\t" \
    "buffer_load_dwordx2 v[136:137], %[t4], %[rs], s46 offen sc1\n\t" \
    "buffer_load_dwordx2 v[138:139], %[t4], %[rs], s47 offen sc1\n\t" \
    "s_waitcnt vmcnt(0)\n\t" \
    "s_branch .Lf12_%=\n\t" \
    ".Lf13_%=:\n\t" \
    "v_lshlrev_b32 v140, 16, v120\n\t" \
    "v_and_b32 v141, 0xffff0000, v120\n\t" \
    "v_lshlrev_b32 v142, 16, v121\n\t" \
    "v_and_b32 v143, 0xffff0000, v121\n\t" \
    "v_lshlrev_b32 v80, 16, v122\n\t" \
    "v_and_b32 v81, 0xffff0000, v122\n\t" \
    "v_lshlrev_b32 v82, 16, v123\n\t" \
    "v_and_b32 v83, 0xffff0000, v123\n\t" \
    "v_add_f32 v140, v140, v80\n\t" \
    "v_add_f32 v141, v141, v81\n\t" \
    "v_add_f32 v142, v142, v82\n\t" \
    "v_add_f32 v143, v143, v83\n\t" \
    "v_lshlrev_b32 v144, 16, v124\n\t" \
    "v_and_b32 v145, 0xffff0000, v124\n\t" \
    "v_lshlrev_b32 v146, 16, v125\n\t" \
    "v_and_b32 v147, 0xffff0000, v125\n\t" \
    "v_lshlrev_b32 v80, 16, v126\n\t" \
    "v_and_b32 v81, 0xffff0000, v126\n\t" \
    "v_lshlrev_b32 v82, 16, v127\n\t" \
    "v_and_b32 v83, 0xffff0000, v127\n\t" \
    "v_add_f32 v144, v144, v80\n\t" \
    "v_add_f32 v145, v145, v81\n\t" \
    "v_add_f32 v146, v146, v82\n\t" \
    "v_add_f32 v147, v147, v83\n\t" \
    "v_lshlrev_b32 v148, 16, v128\n\t" \
    "v_and_b32 v149, 0xffff0000, v128\n\t" \
    "v_lshlrev_b32 v150, 16, v129\n\t" \
    "v_and_b32 v151, 0xffff0000, v129\n\t" \
    "v_lshlrev_b32 v80, 16, v130\n\t" \
    "v_and_b32 v81, 0xffff0000, v130\n\t" \
    "v_lshlrev_b32 v82, 16, v131\n\t" \
    "v_and_b32 v83, 0xffff0000, v131\n\t" \
    "v_add_f32 v148, v148, v80\n\t" \
    "v_add_f32 v149, v149, v81\n\t" \
    "v_add_f32 v150, v150, v82\n\t" \
    "v_add_f32 v151, v151, v83\n\t" \
    "v_lshlrev_b32 v152, 16, v132\n\t" \
    "v_and_b32 v153, 0xffff0000, v132\n\t" \
    "v_lshlrev_b32 v154, 16, v133\n\t" \
    "v_and_b32 v155, 0xffff0000, v133\n\t" \
    "v_lshlrev_b32 v80, 16, v134\n\t" \
    "v_and_b32 v81, 0xffff0000, v134\n\t" \
    "v_lshlrev_b32 v82, 16, v135\n\t" \
    "v_and_b32 v83, 0xffff0000, v135\n\t" \
    "v_add_f32 v152, v152, v80\n\t" \
    "v_add_f32 v153, v153, v81\n\t" \
    "v_add_f32 v154, v154, v82\n\t" \
    "v_add_f32 v155, v155, v83\n\t" \
    "v_lshlrev_b32 v156, 16, v136\n\t" \
    "v_and_b32 v157, 0xffff0000, v136\n\t" \
    "v_lshlrev_b32 v158, 16, v137\n\t" \
    "v_and_b32 v159, 0xffff0000, v137\n\t" \
    "v_lshlrev_b32 v80, 16, v138\n\t" \
    "v_and_b32 v81, 0xffff0000, v138\n\t" \
    "v_lshlrev_b32 v82, 16, v139\n\t" \
    "v_and_b32 v83, 0xffff0000, v139\n\t" \
    "v_add_f32 v156, v156, v80\n\t" \
    "v_add_f32 v157, v157, v81\n\t" \
    "v_add_f32 v158, v158, v82\n\t" \
    "v_add_f32 v159, v159, v83\n\t" \
    "s_mov_b32 s42, s43\n\t" \
    "s_lshr_b32 s48, s40, 1\n\t" \
    "s_add_u32 s48, s48, 1\n\t" \
    "s_mul_i32 s43, s48, 0x14000\n\t" \
    "s_lshr_b32 s44, s48, 4\n\t" \
    "s_mul_i32 s44, s44, 0xc2000\n\t" \
    "s_add_u32 s43, s43, s44\n\t" \
    "s_mul_i32 s49, s40, 20480\n\t" \
    "s_add_u32 s49, s49, %[co]\n\t" \
    "s_add_u32 s44, s40, 1\n\t" \
    "v_mov_b32 v98, s44\n\t" \
    ".Lf14_%=:\n\t" \
    "s_nop 7\n\t" \
    "ds_read_b32 v80, %[fa]\n\t" \
    "s_waitcnt lgkmcnt(0)\n\t" \
    "v_cmp_ge_u32 vcc, s40, v80\n\t" \
    "s_cbranch_vccnz .Lf14_%=\n\t" \
    "ds_read_b128 v[160:163], %[la] offset:0\n\t" \
    "ds_read_b128 v[164:167], %[la] offset:6144\n\t" \
    "ds_read_b128 v[168:171], %[la] offset:12288\n\t" \
    "ds_read_b128 v[172:175], %[la] offset:18432\n\t" \
    "ds_read_b128 v[176:179], %[la] offset:24576\n\t" \
    "ds_read_b128 v[180:183], %[la] offset:1024\n\t" \
    "ds_read_b128 v[184:187], %[la] offset:7168\n\t" \
    "ds_read_b128 v[188:191], %[la] offset:13312\n\t" \
    "ds_read_b128 v[192:195], %[la] offset:19456\n\t" \
    "ds_read_b128 v[196:199], %[la] offset:25600\n\t" \
    "ds_read_b128 v[200:203], %[la] offset:2048\n\t" \
    "ds_read_b128 v[204:207], %[la] offset:8192\n\t" \
    "ds_read_b128 v[208:211], %[la] offset:14336\n\t" \
    "ds_read_b128 v[212:215], %[la] offset:20480\n\t" \
    "ds_read_b128 v[216:219], %[la] offset:26624\n\t" \
    "s_waitcnt lgkmcnt(14)\n\t" \
    "v_pk_add_f32 v[100:101], v[140:141], v[160:161]\n\t" \
    "v_pk_add_f32 v[102:103], v[142:143], v[162:163]\n\t" \
    "ds_read_b128 v[220:223], %[la] offset:3072\n\t" \
    "s_waitcnt lgkmcnt(14)\n\t" \
    "v_pk_add_f32 v[104:105], v[144:145], v[164:165]\n\t" \
    "v_pk_add_f32 v[106:107], v[146:147], v[166:167]\n\t" \
    "ds_read_b128 v[224:227], %[la] offset:9216\n\t" \
    "s_waitcnt lgkmcnt(14)\n\t" \
    "v_pk_add_f32 v[108:109], v[148:149], v[168:169]\n\t" \
    "v_pk_add_f32 v[110:111], v[150:151], v[170:171]\n\t" \
    "ds_read_b128 v[228:231], %[la] offset:15360\n\t" \
    "s_waitcnt lgkmcnt(14)\n\t" \
    "v_pk_add_f32 v[112:113], v[152:153], v[172:173]\n\t" \
    "v_pk_add_f32 v[114:115], v[154:155], v[174:175]\n\t" \
    "ds_read_b128 v[232:235], %[la] offset:21504\n\t" \
    "s_waitcnt lgkmcnt(14)\n\t" \
    "v_pk_add_f32 v[116:117], v[156:157], v[176:177]\n\t" \
    "v_pk_add_f32 v[118:119], v[158:159], v[178:179]\n\t" \
    "ds_read_b128 v[236:239], %[la] offset:27648\n\t" \
    "s_waitcnt lgkmcnt(14)\n\t" \
    "v_pk_add_f32 v[100:101], v[100:101], v[180:181]\n\t" \
    "v_pk_add_f32 v[102:103], v[102:103], v[182:183]\n\t" \
    "ds_read_b128 v[160:163], %[la] offset:4096\n\t" \
    "s_waitcnt lgkmcnt(14)\n\t" \
    "v_pk_add_f32 v[104:105], v[104:105], v[184:185]\n\t" \
    "v_pk_add_f32 v[106:107], v[106:107], v[186:187]\n\t" \
    "ds_read_b128 v[164:167], %[la] offset:10240\n\t" \
    "s_waitcnt lgkmcnt(14)\n\t" \
    "v_pk_add_f32 v[108:109], v[108:109], v[188:189]\n\t" \
    "v_pk_add_f32 v[110:111], v[110:111], v[190:191]\n\t" \
    "ds_read_b128 v[168:171], %[la] offset:16384\n\t" \
    "s_waitcnt lgkmcnt(14)\n\t" \
    "v_pk_add_f32 v[112:113], v[112:113], v[192:193]\n\t" \
    "v_pk_add_f32 v[114:115], v[114:115], v[194:195]\n\t" \
    "ds_read_b128 v[172:175], %[la] offset:22528\n\t" \
    "s_waitcnt lgkmcnt(14)\n\t" \
    "v_pk_add_f32 v[116:117], v[116:117], v[196:197]\n\t" \
    "v_pk_add_f32 v[118:119], v[118:119], v[198:199]\n\t" \
    "ds_read_b128 v[176:179], %[la] offset:28672\n\t" \
    "s_waitcnt lgkmcnt(14)\n\t" \
    "v_pk_add_f32 v[100:101], v[100:101], v[200:201]\n\t" \
    "v_pk_add_f32 v[102:103], v[102:103], v[202:203]\n\t" \
    "ds_read_b128 v[180:183], %[la] offset:5120\n\t" \
    "s_waitcnt lgkmcnt(14)\n\t" \
    "v_pk_add_f32 v[104:105], v[104:105], v[204:205]\n\t" \
    "v_pk_add_f32 v[106:107], v[106:107], v[206:207]\n\t" \
    "ds_read_b128 v[184:187], %[la] offset:11264\n\t" \
    "s_waitcnt lgkmcnt(14)\n\t" \
    "v_pk_add_f32 v[108:109], v[108:109], v[208:209]\n\t" \
    "v_pk_add_f32 v[110:111], v[110:111], v[210:211]\n\t" \
    "ds_read_b128 v[188:191], %[la] offset:17408\n\t" \
    "s_waitcnt lgkmcnt(14)\n\t" \
    "v_pk_add_f32 v[112:113], v[112:113], v[212:213]\n\t" \
    "v_pk_add_f32 v[114:115], v[114:115], v[214:215]\n\t" \
    "ds_read_b128 v[192:195], %[la] offset:23552\n\t" \
    "s_waitcnt lgkmcnt(14)\n\t" \
    "v_pk_add_f32 v[116:117], v[116:117], v[216:217]\n\t" \
    "v_pk_add_f32 v[118:119], v[118:119], v[218:219]\n\t" \
    "ds_read_b128 v[196:199], %[la] offset:29696\n\t" \
    "s_waitcnt lgkmcnt(14)\n\t" \
    "v_pk_add_f32 v[100:101], v[100:101], v[220:221]\n\t" \
    "v_pk_add_f32 v[102:103], v[102:103], v[222:223]\n\t" \
    "s_waitcnt lgkmcnt(13)\n\t" \
    "v_pk_add_f32 v[104:105], v[104:105], v[224:225]\n\t" \
    "v_pk_add_f32 v[106:107], v[106:107], v[226:227]\n\t" \
    "s_waitcnt lgkmcnt(12)\n\t" \
    "v_pk_add_f32 v[108:109], v[108:109], v[228:229]\n\t" \
    "v_pk_add_f32 v[110:111], v[110:111], v[230:231]\n\t" \
    "s_waitcnt lgkmcnt(11)\n\t" \
    "v_pk_add_f32 v[112:113], v[112:113], v[232:233]\n\t" \
    "v_pk_add_f32 v[114:115], v[114:115], v[234:235]\n\t" \
    "s_waitcnt lgkmcnt(10)\n\t" \
    "v_pk_add_f32 v[116:117], v[116:117], v[236:237]\n\t" \
    "v_pk_add_f32 v[118:119], v[118:119], v[238:239]\n\t" \
    "s_waitcnt lgkmcnt(9)\n\t" \
    "v_pk_add_f32 v[100:101], v[100:101], v[160:161]\n\t" \
    "v_pk_add_f32 v[102:103], v[102:103], v[162:163]\n\t" \
    "s_waitcnt lgkmcnt(8)\n\t" \
    "v_pk_add_f32 v[104:105], v[104:105], v[164:165]\n\t" \
    "v_pk_add_f32 v[106:107], v[106:107], v[166:167]\n\t" \
    "s_waitcnt lgkmcnt(7)\n\t" \
    "v_pk_add_f32 v[108:109], v[108:109], v[168:169]\n\t" \
    "v_pk_add_f32 v[110:111], v[110:111], v[170:171]\n\t" \
    "s_waitcnt lgkmcnt(6)\n\t" \
    "v_pk_add_f32 v[112:113], v[112:113], v[172:173]\n\t" \
    "v_pk_add_f32 v[114:115], v[114:115], v[174:175]\n\t" \
    "s_waitcnt lgkmcnt(5)\n\t" \
    "v_pk_add_f32 v[116:117], v[116:117], v[176:177]\n\t" \
    "v_pk_add_f32 v[118:119], v[118:119], v[178:179]\n\t" \
    "s_waitcnt lgkmcnt(4)\n\t" \
    "v_pk_add_f32 v[100:101], v[100:101], v[180:181]\n\t" \
    "v_pk_add_f32 v[102:103], v[102:103], v[182:183]\n\t" \
    "s_waitcnt lgkmcnt(3)\n\t" \
    "v_pk_add_f32 v[104:105], v[104:105], v[184:185]\n\t" \
    "v_pk_add_f32 v[106:107], v[106:107], v[186:187]\n\t" \
    "s_waitcnt lgkmcnt(2)\n\t" \
    "v_pk_add_f32 v[108:109], v[108:109], v[188:189]\n\t" \
    "v_pk_add_f32 v[110:111], v[110:111], v[190:191]\n\t" \
    "s_waitcnt lgkmcnt(1)\n\t" \
    "v_pk_add_f32 v[112:113], v[112:113], v[192:193]\n\t" \
    "v_pk_add_f32 v[114:115], v[114:115], v[194:195]\n\t" \
    "s_waitcnt lgkmcnt(0)\n\t" \
    "v_pk_add_f32 v[116:117], v[116:117], v[196:197]\n\t" \
    "v_pk_add_f32 v[118:119], v[118:119], v[198:199]\n\t" \
    "ds_write_b32 %[fc], v98\n\t" \
    "v_sub_u32 %[la], %[l2], %[la]\n\t" \
    "v_cvt_pk_bf16_f32 v84, v100, v101\n\t" \
    "v_cvt_pk_bf16_f32 v85, v102, v103\n\t" \
    "v_cvt_pk_bf16_f32 v86, v104, v105\n\t" \
    "v_cvt_pk_bf16_f32 v87, v106, v107\n\t" \
    "v_cvt_pk_bf16_f32 v88, v108, v109\n\t" \
    "v_cvt_pk_bf16_f32 v89, v110, v111\n\t" \
    "v_cvt_pk_bf16_f32 v90, v112, v113\n\t" \
    "v_cvt_pk_bf16_f32 v91, v114, v115\n\t" \
    "v_cvt_pk_bf16_f32 v92, v116, v117\n\t" \
    "v_cvt_pk_bf16_f32 v93, v118, v119\n\t" \
    "s_nop 0\n\t" \
    "v_permlane16_swap_b32 v84, v86\n\t" \
    "v_permlane16_swap_b32 v85, v87\n\t" \
    "v_permlane16_swap_b32 v88, v90\n\t" \
    "v_permlane16_swap_b32 v89, v91\n\t" \
    "buffer_store_dwordx4 v[84:87], %[q16], %[ro], s49 offen nt\n\t" \
    "buffer_store_dwordx4 v[88:91], %[q16], %[ro], s49 offen offset:64 nt\n\t" \
    "buffer_store_dwordx2 v[92:93], %[q8], %[ro], s49 offen offset:128 nt\n\t" \
    "s_cmp_gt_u32 s40, 125\n\t" \
    "s_cbranch_scc1 .Lf15_%=\n\t" \
    "s_add_u32 s46, s43, 0x2c00000\n\t" \
    "s_add_u32 s47, s43, 0x1c00000\n\t" \
    "buffer_load_dwordx2 v[120:121], %[t0], %[rs], s46 offen sc1\n\t" \
    "buffer_load_dwordx2 v[122:123], %[t0], %[rs], s47 offen sc1\n\t" \
    "buffer_load_dwordx2 v[124:125], %[t1], %[rs], s46 offen sc1\n\t" \
    "buffer_load_dwordx2 v[126:127], %[t1], %[rs], s47 offen sc1\n\t" \
    "buffer_load_dwordx2 v[128:129], %[t2], %[rs], s46 offen sc1\n\t" \
    "buffer_load_dwordx2 v[130:131], %[t2], %[rs], s47 offen sc1\n\t" \
    "buffer_load_dwordx2 v[132:133], %[t3], %[rs], s46 offen sc1\n\t" \
    "buffer_load_dwordx2 v[134:135], %[t3], %[rs], s47 offen sc1\n\t" \
    "buffer_load_dwordx2 v[136:137], %[t4], %[rs], s46 offen sc1\n\t" \
    "buffer_load_dwordx2 v[138:139], %[t4], %[rs], s47 offen sc1\n\t" \
    ".Lf15_%=:\n\t" \
    "s_add_u32 s40, s40, 2\n\t" \
    "s_cmp_lt_u32 s40, 128\n\t" \
    "s_cbranch_scc1 .Lf11_%=\n\t" \
    "s_branch .Lend%=\n\t" \
    ".Lend%=:\n\t" \
    "s_waitcnt vmcnt(0) lgkmcnt(0)\n\t" \
    "s_mov_b32 %[bud], s41\n\t"
#define ASM_W1_LOOP \
    "s_mov_b32 s40, 0\n\t" \
    "s_mov_b32 s41, 0x20000\n\t" \
    "s_add_u32 m0, %[wm], 0x0\n\t" \
    "s_add_u32 s44, %[wo], 0x0\n\t" \
    "buffer_load_dwordx4 v176, %[wr], s44 offen nt lds\n\t" \
    "s_add_u32 m0, %[wm], 0x2000\n\t" \
    "s_add_u32 s44, %[wo], 0xa000\n\t" \
    "buffer_load_dwordx4 v176, %[wr], s44 offen nt lds\n\t" \
    "s_add_u32 m0, %[wm], 0x4000\n\t" \
    "s_add_u32 s44, %[wo], 0x14000\n\t" \
    "buffer_load_dwordx4 v176, %[wr], s44 offen nt lds\n\t" \
    "s_add_u32 m0, %[wm], 0x6000\n\t" \
    "s_add_u32 s44, %[wo], 0x1e000\n\t" \
    "buffer_load_dwordx4 v176, %[wr], s44 offen nt lds\n\t" \
    "s_add_u32 m0, %[wm], 0x8000\n\t" \
    "s_add_u32 s44, %[wo], 0x28000\n\t" \
    "buffer_load_dwordx4 v176, %[wr], s44 offen nt lds\n\t" \
    "s_add_u32 m0, %[wm], 0xa000\n\t" \
    "s_add_u32 s44, %[wo], 0x32000\n\t" \
    "buffer_load_dwordx4 v176, %[wr], s44 offen nt lds\n\t" \
    "s_add_u32 m0, %[wm], 0xc000\n\t" \
    "s_add_u32 s44, %[wo], 0x3c000\n\t" \
    "buffer_load_dwordx4 v176, %[wr], s44 offen nt lds\n\t" \
    "s_add_u32 m0, %[wm], 0xe000\n\t" \
    "s_add_u32 s44, %[wo], 0x46000\n\t" \
    "buffer_load_dwordx4 v176, %[wr], s44 offen nt lds\n\t" \
    "s_add_u32 m0, %[wm], 0x10000\n\t" \
    "s_add_u32 s44, %[wo], 0x50000\n\t" \
    "buffer_load_dwordx4 v176, %[wr], s44 offen nt lds\n\t" \
    "s_add_u32 m0, %[wm], 0x12000\n\t" \
    "s_add_u32 s44, %[wo], 0x5a000\n\t" \
    "buffer_load_dwordx4 v176, %[wr], s44 offen nt lds\n\t" \
    "s_add_u32 m0, %[wm], 0x14000\n\t" \
    "s_add_u32 s44, %[wo], 0x64000\n\t" \
    "buffer_load_dwordx4 v176, %[wr], s44 offen nt lds\n\t" \
    "s_add_u32 m0, %[wm], 0x16000\n\t" \
    "s_add_u32 s44, %[wo], 0x6e000\n\t" \
    "buffer_load_dwordx4 v176, %[wr], s44 offen nt lds\n\t" \
    "s_add_u32 m0, %[wm], 0x18000\n\t" \
    "s_add_u32 s44, %[wo], 0x78000\n\t" \
    "buffer_load_dwordx4 v176, %[wr], s44 offen nt lds\n\t" \
    "s_add_u32 m0, %[wm], 0x1a000\n\t" \
    "s_add_u32 s44, %[wo], 0x82000\n\t" \
    "buffer_load_dwordx4 v176, %[wr], s44 offen nt lds\n\t" \
    "s_add_u32 m0, %[wm], 0x1c000\n\t" \
    "s_add_u32 s44, %[wo], 0x8c000\n\t" \
    "buffer_load_dwordx4 v176, %[wr], s44 offen nt lds\n\t" \
    "s_add_u32 m0, %[wm], 0x1e000\n\t" \
    "s_add_u32 s44, %[wo], 0x96000\n\t" \
    "buffer_load_dwordx4 v176, %[wr], s44 offen nt lds\n\t" \
    "s_add_u32 m0, %[wm], 0x20000\n\t" \
    "s_add_u32 s44, %[wo], 0xa0000\n\t" \
    "buffer_load_dwordx4 v176, %[wr], s44 offen nt lds\n\t" \
    "s_add_u32 m0, %[wm], 0x22000\n\t" \
    "s_add_u32 s44, %[wo], 0xaa000\n\t" \
    "buffer_load_dwordx4 v176, %[wr], s44 offen nt lds\n\t" \
    "s_add_u32 m0, %[wm], 0x24000\n\t" \
    "s_add_u32 s44, %[wo], 0xb4000\n\t" \
    "buffer_load_dwordx4 v176, %[wr], s44 offen nt lds\n\t" \
    "s_add_u32 m0, %[wm], 0x26000\n\t" \
    "s_add_u32 s44, %[wo], 0xbe000\n\t" \
    "buffer_load_dwordx4 v176, %[wr], s44 offen nt lds\n\t" \
    "s_waitcnt vmcnt(0) lgkmcnt(0)\n\t" \
    "s_barrier \n\t" \
    "v_add_u32 v178, 0x0, v177\n\t" \
    "ds_read_b64_tr_b16 v[0:1], v178 offset:0\n\t" \
    "ds_read_b64_tr_b16 v[2:3], v178 offset:1024\n\t" \
    "ds_read_b64_tr_b16 v[4:5], v178 offset:8192\n\t" \
    "ds_read_b64_tr_b16 v[6:7], v178 offset:9216\n\t" \
    "ds_read_b64_tr_b16 v[8:9], v178 offset:16384\n\t" \
    "ds_read_b64_tr_b16 v[10:11], v178 offset:17408\n\t" \
    "ds_read_b64_tr_b16 v[12:13], v178 offset:24576\n\t" \
    "ds_read_b64_tr_b16 v[14:15], v178 offset:25600\n\t" \
    "v_add_u32 v178, 0x8000, v177\n\t" \
    "ds_read_b64_tr_b16 v[16:17], v178 offset:0\n\t" \
    "ds_read_b64_tr_b16 v[18:19], v178 offset:1024\n\t" \
    "ds_read_b64_tr_b16 v[20:21], v178 offset:8192\n\t" \
    "ds_read_b64_tr_b16 v[22:23], v178 offset:9216\n\t" \
    "ds_read_b64_tr_b16 v[24:25], v178 offset:16384\n\t" \
    "ds_read_b64_tr_b16 v[26:27], v178 offset:17408\n\t" \
    "ds_read_b64_tr_b16 v[28:29], v178 offset:24576\n\t" \
    "ds_read_b64_tr_b16 v[30:31], v178 offset:25600\n\t" \
    "v_add_u32 v178, 0x10000, v177\n\t" \
    "ds_read_b64_tr_b16 v[32:33], v178 offset:0\n\t" \
    "ds_read_b64_tr_b16 v[34:35], v178 offset:1024\n\t" \
    "ds_read_b64_tr_b16 v[36:37], v178 offset:8192\n\t" \
    "ds_read_b64_tr_b16 v[38:39], v178 offset:9216\n\t" \
    "ds_read_b64_tr_b16 v[40:41], v178 offset:16384\n\t" \
    "ds_read_b64_tr_b16 v[42:43], v178 offset:17408\n\t" \
    "ds_read_b64_tr_b16 v[44:45], v178 offset:24576\n\t" \
    "ds_read_b64_tr_b16 v[46:47], v178 offset:25600\n\t" \
    "v_add_u32 v178, 0x18000, v177\n\t" \
    "ds_read_b64_tr_b16 v[48:49], v178 offset:0\n\t" \
    "ds_read_b64_tr_b16 v[50:51], v178 offset:1024\n\t" \
    "ds_read_b64_tr_b16 v[52:53], v178 offset:8192\n\t" \
    "ds_read_b64_tr_b16 v[54:55], v178 offset:9216\n\t" \
    "ds_read_b64_tr_b16 v[56:57], v178 offset:16384\n\t" \
    "ds_read_b64_tr_b16 v[58:59], v178 offset:17408\n\t" \
    "ds_read_b64_tr_b16 v[60:61], v178 offset:24576\n\t" \
    "ds_read_b64_tr_b16 v[62:63], v178 offset:25600\n\t" \
    "v_add_u32 v178, 0x20000, v177\n\t" \
    "ds_read_b64_tr_b16 v[64:65], v178 offset:0\n\t" \
    "ds_read_b64_tr_b16 v[66:67], v178 offset:1024\n\t" \
    "ds_read_b64_tr_b16 v[68:69], v178 offset:8192\n\t" \
    "ds_read_b64_tr_b16 v[70:71], v178 offset:9216\n\t" \
    "ds_read_b64_tr_b16 v[72:73], v178 offset:16384\n\t" \
    "ds_read_b64_tr_b16 v[74:75], v178 offset:17408\n\t" \
    "ds_read_b64_tr_b16 v[76:77], v178 offset:24576\n\t" \
    "ds_read_b64_tr_b16 v[78:79], v178 offset:25600\n\t" \
    "v_mov_b32 v228, 0x3f803f80\n\t" \
    "v_mov_b32 v229, 0x3f803f80\n\t" \
    "v_mov_b32 v230, 0x3f803f80\n\t" \
    "v_mov_b32 v231, 0x3f803f80\n\t" \
    "v_mov_b64 v[216:217], 0\n\t" \
    "v_mov_b32 v219, 0\n\t" \
    "v_bfe_u32 v83, v253, 2, 5\n\t" \
    "v_add_u32 v83, 0x7000, v83\n\t" \
    "v_mov_b32 v218, 0\n\t" \
    "s_waitcnt lgkmcnt(0)\n\t" \
    "s_barrier \n\t" \
    "ds_write_b32 v251, v228 offset:72\n\t" \
    "ds_write_b32 v251, v228 offset:2120\n\t" \
    "v_mfma_f32_16x16x32_bf16 v[160:163], v[0:3], v[228:231], 0\n\t" \
    "v_mfma_f32_16x16x32_bf16 v[160:163], v[4:7], v[228:231], v[160:163]\n\t" \
    "v_mfma_f32_16x16x32_bf16 v[160:163], v[8:11], v[228:231], v[160:163]\n\t" \
    "v_mfma_f32_16x16x32_bf16 v[160:163], v[12:15], v[228:231], v[160:163]\n\t" \
    "v_mfma_f32_16x16x32_bf16 v[160:163], v[16:19], v[228:231], v[160:163]\n\t" \
    "v_mfma_f32_16x16x32_bf16 v[160:163], v[20:23], v[228:231], v[160:163]\n\t" \
    "v_mfma_f32_16x16x32_bf16 v[160:163], v[24:27], v[228:231], v[160:163]\n\t" \
    "v_mfma_f32_16x16x32_bf16 v[160:163], v[28:31], v[228:231], v[160:163]\n\t" \
    "v_mfma_f32_16x16x32_bf16 v[160:163], v[32:35], v[228:231], v[160:163]\n\t" \
    "v_mfma_f32_16x16x32_bf16 v[160:163], v[36:39], v[228:231], v[160:163]\n\t" \
    "v_mfma_f32_16x16x32_bf16 v[160:163], v[40:43], v[228:231], v[160:163]\n\t" \
    "v_mfma_f32_16x16x32_bf16 v[160:163], v[44:47], v[228:231], v[160:163]\n\t" \
    "v_mfma_f32_16x16x32_bf16 v[160:163], v[48:51], v[228:231], v[160:163]\n\t" \
    "v_mfma_f32_16x16x32_bf16 v[160:163], v[52:55], v[228:231], v[160:163]\n\t" \
    "v_mfma_f32_16x16x32_bf16 v[160:163], v[56:59], v[228:231], v[160:163]\n\t" \
    "v_mfma_f32_16x16x32_bf16 v[160:163], v[60:63], v[228:231], v[160:163]\n\t" \
    "v_mfma_f32_16x16x32_bf16 v[160:163], v[64:67], v[228:231], v[160:163]\n\t" \
    "v_mfma_f32_16x16x32_bf16 v[160:163], v[68:71], v[228:231], v[160:163]\n\t" \
    "v_mfma_f32_16x16x32_bf16 v[160:163], v[72:75], v[228:231], v[160:163]\n\t" \
    "v_mfma_f32_16x16x32_bf16 v[160:163], v[76:79], v[228:231], v[160:163]\n\t" \
    "s_mov_b32 s52, 0x22222222\n\t" \
    "s_mov_b32 s53, 0x22222222\n\t" \
    "s_mov_b32 s54, 0x44444444\n\t" \
    "s_mov_b32 s55, 0x44444444\n\t" \
    "s_mov_b32 s56, 0x88888888\n\t" \
    "s_mov_b32 s57, 0x88888888\n\t" \
    "ds_write_b32 v83, v218\n\t" \
    "s_waitcnt lgkmcnt(0)\n\t" \
    "s_barrier \n\t" \
    ".Lw11_%=:\n\t" \
    "v_mov_b32 v248, 0\n\t" \
    "buffer_load_dword v248, v248, %[rs], %[go] offen sc1\n\t" \
    "s_waitcnt vmcnt(0)\n\t" \
    "v_cmp_ne_u32 vcc, 0, v248\n\t" \
    "s_cbranch_vccnz .Lw12_%=\n\t" \
    "s_cmp_eq_u32 s41, 0\n\t" \
    "s_cbranch_scc1 .Lw12_%=\n\t" \
    "s_sub_u32 s41, s41, 1\n\t" \
    "s_sleep 16\n\t" \
    "s_branch .Lw11_%=\n\t" \
    ".Lw12_%=:\n\t" \
    "s_sub_i32 s60, 0, %[ng]\n\t" \
    "s_sub_i32 s61, 1, %[ng]\n\t" \
    "s_add_i32 s62, s60, 6\n\t" \
    "s_add_i32 s63, s61, 6\n\t" \
    "s_mul_i32 s42, s61, 127\n\t" \
    "s_mul_i32 s44, %[ng], 0x733000\n\t" \
    "s_add_u32 s44, s44, 0x2400000\n\t" \
    "v_cmp_ne_u32 s[58:59], %[ng], v255\n\t" \
    "s_nop 1\n\t" \
    "s_mov_b64 exec, s[58:59]\n\t" \
    "buffer_store_dwordx2 v[216:217], v254, %[rs], s44 offen sc1\n\t" \
    "s_mov_b64 exec, -1\n\t" \
    "s_lshr_b32 s48, %[wave], 2\n\t" \
    "s_mul_i32 s48, s48, 0x3000\n\t" \
    "s_add_u32 s48, s48, %[pi]\n\t" \
    "s_mov_b32 s43, 0\n\t" \
    "s_add_u32 s46, s43, s48\n\t" \
    "s_add_u32 s47, s46, 0x0\n\t" \
    "buffer_load_dwordx4 v[208:211], v253, %[rs], s47 offen sc1\n\t" \
    "s_add_u32 s47, s46, 0x8000\n\t" \
    "buffer_load_dwordx4 v[212:215], v253, %[rs], s47 offen sc1\n\t" \
    "s_cmp_gt_u32 %[wave], 3\n\t" \
    "s_cbranch_scc1 .Lw13_%=\n\t" \
    "s_add_u32 s47, s46, 0x10000\n\t" \
    "buffer_load_dwordx4 v[216:219], v253, %[rs], s47 offen sc1\n\t" \
    ".Lw13_%=:\n\t" \
    "s_waitcnt vmcnt(0)\n\t" \
    "v_lshrrev_b32 v82, 1, v253\n\t" \
    ".Lw14_%=:\n\t" \
    "s_add_i32 s44, s40, s60\n\t" \
    "s_max_i32 s44, s44, 0\n\t" \
    "s_lshr_b32 s45, s44, 1\n\t" \
    "s_and_b32 s44, s44, 1\n\t" \
    "s_lshl_b32 s44, s44, 12\n\t" \
    "s_mul_i32 s49, s45, 0x14000\n\t" \
    "s_add_u32 s49, s49, s44\n\t" \
    "s_lshr_b32 s45, s45, 4\n\t" \
    "s_mul_i32 s45, s45, 0xc2000\n\t" \
    "s_add_u32 s49, s49, s45\n\t" \
    "s_add_i32 s44, s40, s61\n\t" \
    "s_max_i32 s44, s44, 0\n\t" \
    "s_lshr_b32 s45, s44, 1\n\t" \
    "s_and_b32 s44, s44, 1\n\t" \
    "s_lshl_b32 s44, s44, 12\n\t" \
    "s_mul_i32 s50, s45, 0x14000\n\t" \
    "s_add_u32 s50, s50, s44\n\t" \
    "s_lshr_b32 s45, s45, 4\n\t" \
    "s_mul_i32 s45, s45, 0xc2000\n\t" \
    "s_add_u32 s50, s50, s45\n\t" \
    "s_sub_u32 s50, s50, s49\n\t" \
    "s_add_u32 s49, s49, 0x2400000\n\t" \
    "v_mad_u32_u24 v80, v255, s50, v254\n\t" \
    "s_lshr_b32 s44, s40, 6\n\t" \
    "s_lshl_b32 s44, s44, 14\n\t" \
    "s_and_b32 s45, s40, 63\n\t" \
    "s_lshl_b32 s45, s45, 7\n\t" \
    "s_add_u32 s44, s44, s45\n\t" \
    "s_add_u32 s50, s44, 0x4d0000\n\t" \
    "s_add_i32 s44, s40, 1\n\t" \
    "s_max_i32 s44, s44, 0\n\t" \
    "s_lshr_b32 s45, s44, 1\n\t" \
    "s_and_b32 s44, s44, 1\n\t" \
    "s_lshl_b32 s44, s44, 12\n\t" \
    "s_mul_i32 s43, s45, 0x14000\n\t" \
    "s_add_u32 s43, s43, s44\n\t" \
    "s_lshr_b32 s45, s45, 4\n\t" \
    "s_mul_i32 s45, s45, 0xc2000\n\t" \
    "s_add_u32 s43, s43, s45\n\t" \
    "s_setprio 2\n\t" \
    "v_max_u32 v248, v208, v209\n\t" \
    "v_max3_u32 v248, v210, v211, v248\n\t" \
    "v_max3_u32 v248, v212, v213, v248\n\t" \
    "v_max3_u32 v248, v214, v215, v248\n\t" \
    "v_max3_u32 v248, v216, v217, v248\n\t" \
    "v_max3_u32 v248, v218, v219, v248\n\t" \
    "v_cmp_eq_u32 vcc, -1, v248\n\t" \
    "s_cbranch_vccz .Lw16_%=\n\t" \
    "s_and_b32 s44, s40, 1\n\t" \
    "s_cmp_eq_u32 s44, 1\n\t" \
    "s_cbranch_scc1 .Lw15_%=\n\t" \
    "s_cmp_gt_u32 s40, 123\n\t" \
    "s_cbranch_scc1 .Lw15_%=\n\t" \
    "s_sleep 10\n\t" \
    ".Lw15_%=:\n\t" \
    "v_max_u32 v248, v208, v209\n\t" \
    "v_max3_u32 v248, v210, v211, v248\n\t" \
    "v_cmp_eq_u32 vcc, -1, v248\n\t" \
    "s_cbranch_vccz .Lw18_%=\n\t" \
    "s_add_u32 s47, s46, 0x0\n\t" \
    "buffer_load_dwordx4 v[208:211], v253, %[rs], s47 offen sc1\n\t" \
    ".Lw18_%=:\n\t" \
    "v_max_u32 v248, v212, v213\n\t" \
    "v_max3_u32 v248, v214, v215, v248\n\t" \
    "v_cmp_eq_u32 vcc, -1, v248\n\t" \
    "s_cbranch_vccz .Lw19_%=\n\t" \
    "s_add_u32 s47, s46, 0x8000\n\t" \
    "buffer_load_dwordx4 v[212:215], v253, %[rs], s47 offen sc1\n\t" \
    ".Lw19_%=:\n\t" \
    "v_max_u32 v248, v216, v217\n\t" \
    "v_max3_u32 v248, v218, v219, v248\n\t" \
    "v_cmp_eq_u32 vcc, -1, v248\n\t" \
    "s_cbranch_vccz .Lw110_%=\n\t" \
    "s_add_u32 s47, s46, 0x10000\n\t" \
    "buffer_load_dwordx4 v[216:219], v253, %[rs], s47 offen sc1\n\t" \
    ".Lw110_%=:\n\t" \
    "s_waitcnt vmcnt(0)\n\t" \
    "v_max_u32 v248, v208, v209\n\t" \
    "v_max3_u32 v248, v210, v211, v248\n\t" \
    "v_max3_u32 v248, v212, v213, v248\n\t" \
    "v_max3_u32 v248, v214, v215, v248\n\t" \
    "v_max3_u32 v248, v216, v217, v248\n\t" \
    "v_max3_u32 v248, v218, v219, v248\n\t" \
    "v_cmp_eq_u32 vcc, -1, v248\n\t" \
    "s_cbranch_vccz .Lw16_%=\n\t" \
    "s_cmp_eq_u32 s41, 0\n\t" \
    "s_cbranch_scc1 .Lw16_%=\n\t" \
    "s_sub_u32 s41, s41, 1\n\t" \
    "s_branch .Lw15_%=\n\t" \
    ".Lw16_%=:\n\t" \
    "ds_write_b128 v250, v[208:211] offset:0\n\t" \
    "v_mfma_f32_16x16x32_bf16 v[236:239], v[208:211], v[208:211], 0\n\t" \
    "v_mfma_f32_16x16x32_bf16 v[232:235], v[228:231], v[208:211], 0\n\t" \
    "ds_write_b128 v250, v[212:215] offset:8192\n\t" \
    "v_mfma_f32_16x16x32_bf16 v[236:239], v[212:215], v[212:215], v[236:239]\n\t" \
    "v_mfma_f32_16x16x32_bf16 v[232:235], v[228:231], v[212:215], v[232:235]\n\t" \
    "s_cmp_gt_u32 %[wave], 3\n\t" \
    "s_cbranch_scc1 .Lw111_%=\n\t" \
    "ds_write_b128 v250, v[216:219] offset:16384\n\t" \
    ".Lw111_%=:\n\t" \
    "v_mfma_f32_16x16x32_bf16 v[236:239], v[216:219], v[216:219], v[236:239]\n\t" \
    "v_mfma_f32_16x16x32_bf16 v[232:235], v[228:231], v[216:219], v[232:235]\n\t" \
    "s_add_u32 s46, s43, s48\n\t" \
    "s_nop 4\n\t" \
    "v_cndmask_b32 v236, v236, v237, s[52:53]\n\t" \
    "v_cndmask_b32 v236, v236, v238, s[54:55]\n\t" \
    "v_cndmask_b32 v236, v236, v239, s[56:57]\n\t" \
    "s_mov_b32 exec_lo, 0xf0000f\n\t" \
    "s_mov_b32 exec_hi, 0xf0000f00\n\t" \
    "ds_write2_b32 v252, v232, v236 offset1:1\n\t" \
    "s_mov_b64 exec, -1\n\t" \
    "s_waitcnt lgkmcnt(0)\n\t" \
    "s_barrier \n\t" \
    "s_setprio 0\n\t" \
    "s_cmp_lg_u32 %[wave], 4\n\t" \
    "s_cbranch_scc1 .Lw112_%=\n\t" \
    "s_setprio 1\n\t" \
    "ds_read_b128 v[232:235], v251 offset:0\n\t" \
    "ds_read_b128 v[236:239], v251 offset:16\n\t" \
    "ds_read_b128 v[240:243], v251 offset:32\n\t" \
    "ds_read_b128 v[244:247], v251 offset:48\n\t" \
    "s_waitcnt lgkmcnt(0)\n\t" \
    "v_add_f32 v232, v232, v240\n\t" \
    "v_add_f32 v233, v233, v241\n\t" \
    "v_add_f32 v234, v234, v242\n\t" \
    "v_add_f32 v235, v235, v243\n\t" \
    "v_add_f32 v236, v236, v244\n\t" \
    "v_add_f32 v237, v237, v245\n\t" \
    "v_add_f32 v238, v238, v246\n\t" \
    "v_add_f32 v239, v239, v247\n\t" \
    "v_add_f32 v232, v232, v236\n\t" \
    "v_add_f32 v233, v233, v237\n\t" \
    "v_add_f32 v234, v234, v238\n\t" \
    "v_add_f32 v235, v235, v239\n\t" \
    "v_add_f32 v232, v232, v234\n\t" \
    "v_add_f32 v233, v233, v235\n\t" \
    "v_mul_f32 v232, 0x3acccccd, v232\n\t" \
    "v_mul_f32 v233, 0x3acccccd, v233\n\t" \
    "v_fma_f32 v233, -v232, v232, v233\n\t" \
    "v_mov_b32 v234, v232\n\t" \
    "v_max_f32 v233, 0, v233\n\t" \
    "v_cvt_pk_bf16_f32 v234, v234, v234\n\t" \
    "v_and_b32 v234, 0xffff0000, v234\n\t" \
    "v_mov_b32 v236, v233\n\t" \
    "v_cvt_pk_bf16_f32 v236, v236, v236\n\t" \
    "v_and_b32 v236, 0xffff0000, v236\n\t" \
    "v_add_f32 v236, 0x3727c5ac, v236\n\t" \
    "v_cvt_pk_bf16_f32 v236, v236, v236\n\t" \
    "v_and_b32 v236, 0xffff0000, v236\n\t" \
    "v_sqrt_f32 v236, v236\n\t" \
    "s_nop 0\n\t" \
    "v_cvt_pk_bf16_f32 v236, v236, v236\n\t" \
    "v_and_b32 v236, 0xffff0000, v236\n\t" \
    "v_rcp_f32 v235, v236\n\t" \
    "s_nop 0\n\t" \
    "v_sub_f32 v232, v232, v234\n\t" \
    "v_mul_f32 v233, v233, v235\n\t" \
    "v_mul_f32 v232, v232, v235\n\t" \
    "v_mul_f32 v233, v233, v235\n\t" \
    "v_cvt_pk_bf16_f32 v232, v232, v232\n\t" \
    "v_and_b32 v232, 0xffff0000, v232\n\t" \
    "v_cvt_pk_bf16_f32 v233, v233, v233\n\t" \
    "v_and_b32 v233, 0xffff0000, v233\n\t" \
    "v_add_f32 v233, 0x3727c5ac, v233\n\t" \
    "v_cvt_pk_bf16_f32 v233, v233, v233\n\t" \
    "v_and_b32 v233, 0xffff0000, v233\n\t" \
    "v_sqrt_f32 v233, v233\n\t" \
    "s_nop 0\n\t" \
    "v_cvt_pk_bf16_f32 v233, v233, v233\n\t" \
    "v_and_b32 v233, 0xffff0000, v233\n\t" \
    "v_rcp_f32 v233, v233\n\t" \
    "v_fma_f32 v232, v232, v236, v234\n\t" \
    "v_mul_f32 v237, v235, v233\n\t" \
    "v_mul_f32 v238, -v232, v237\n\t" \
    "v_min_u32 v234, -2, v234\n\t" \
    "v_min_u32 v235, -2, v235\n\t" \
    "v_mov_b32 v239, s40\n\t" \
    "ds_write2_b32 v251, v237, v238 offset0:16 offset1:17\n\t" \
    "ds_write_b32 v251, v239 offset:72\n\t" \
    "s_setprio 0\n\t" \
    "s_cmp_eq_u32 %[ss], 0\n\t" \
    "s_cbranch_scc1 .Lw113_%=\n\t" \
    "s_mov_b64 exec, 0xffff\n\t" \
    "buffer_store_dwordx2 v[234:235], v82, %[rs], s50 offen sc1\n\t" \
    "s_mov_b64 exec, -1\n\t" \
    ".Lw113_%=:\n\t" \
    ".Lw112_%=:\n\t" \
    "ds_read_b128 v[176:179], v249 offset:0\n\t" \
    "ds_read_b128 v[180:183], v249 offset:1024\n\t" \
    "ds_read_b128 v[184:187], v249 offset:2048\n\t" \
    "ds_read_b128 v[188:191], v249 offset:3072\n\t" \
    "ds_read_b128 v[192:195], v249 offset:4096\n\t" \
    "ds_read_b128 v[196:199], v249 offset:5120\n\t" \
    "ds_read_b128 v[200:203], v249 offset:6144\n\t" \
    "s_waitcnt lgkmcnt(6)\n\t" \
    "v_mfma_f32_16x16x32_bf16 v[168:171], v[0:3], v[176:179], 0\n\t" \
    "ds_read_b128 v[204:207], v249 offset:7168\n\t" \
    "s_waitcnt lgkmcnt(6)\n\t" \
    "v_mfma_f32_16x16x32_bf16 v[168:171], v[4:7], v[180:183], v[168:171]\n\t" \
    "ds_read_b128 v[176:179], v249 offset:8192\n\t" \
    "s_waitcnt lgkmcnt(6)\n\t" \
    "v_mfma_f32_16x16x32_bf16 v[168:171], v[8:11], v[184:187], v[168:171]\n\t" \
    "ds_read_b128 v[180:183], v249 offset:9216\n\t" \
    "s_waitcnt lgkmcnt(6)\n\t" \
    "v_mfma_f32_16x16x32_bf16 v[168:171], v[12:15], v[188:191], v[168:171]\n\t" \
    "ds_read_b128 v[184:187], v249 offset:10240\n\t" \
    "s_add_u32 s47, s46, 0x0\n\t" \
    "buffer_load_dwordx4 v[208:211], v253, %[rs], s47 offen sc1\n\t" \
    "s_add_u32 s47, s46, 0x8000\n\t" \
    "buffer_load_dwordx4 v[212:215], v253, %[rs], s47 offen sc1\n\t" \
    "s_cmp_gt_u32 %[wave], 3\n\t" \
    "s_cbranch_scc1 .Lw114_%=\n\t" \
    "s_add_u32 s47, s46, 0x10000\n\t" \
    "buffer_load_dwordx4 v[216:219], v253, %[rs], s47 offen sc1\n\t" \
    ".Lw114_%=:\n\t" \
    "s_waitcnt lgkmcnt(6)\n\t" \
    "v_mfma_f32_16x16x32_bf16 v[168:171], v[16:19], v[192:195], v[168:171]\n\t" \
    "ds_read_b128 v[188:191], v249 offset:11264\n\t" \
    "s_waitcnt lgkmcnt(6)\n\t" \
    "v_mfma_f32_16x16x32_bf16 v[168:171], v[20:23], v[196:199], v[168:171]\n\t" \
    "ds_read_b128 v[192:195], v249 offset:12288\n\t" \
    "s_waitcnt lgkmcnt(6)\n\t" \
    "v_mfma_f32_16x16x32_bf16 v[168:171], v[24:27], v[200:203], v[168:171]\n\t" \
    "ds_read_b128 v[196:199], v249 offset:13312\n\t" \
    "s_waitcnt lgkmcnt(6)\n\t" \
    "v_mfma_f32_16x16x32_bf16 v[168:171], v[28:31], v[204:207], v[168:171]\n\t" \
    "ds_read_b128 v[200:203], v249 offset:14336\n\t" \
    "s_waitcnt lgkmcnt(6)\n\t" \
    "v_mfma_f32_16x16x32_bf16 v[168:171], v[32:35], v[176:179], v[168:171]\n\t" \
    "ds_read_b128 v[204:207], v249 offset:15360\n\t" \
    "s_waitcnt lgkmcnt(6)\n\t" \
    "v_mfma_f32_16x16x32_bf16 v[168:171], v[36:39], v[180:183], v[168:171]\n\t" \
    "ds_read_b128 v[176:179], v249 offset:16384\n\t" \
    "s_waitcnt lgkmcnt(6)\n\t" \
    "v_mfma_f32_16x16x32_bf16 v[168:171], v[40:43], v[184:187], v[168:171]\n\t" \
    "ds_read_b128 v[180:183], v249 offset:17408\n\t" \
    "s_waitcnt lgkmcnt(6)\n\t" \
    "v_mfma_f32_16x16x32_bf16 v[168:171], v[44:47], v[188:191], v[168:171]\n\t" \
    "ds_read_b128 v[184:187], v249 offset:18432\n\t" \
    "s_waitcnt lgkmcnt(6)\n\t" \
    "v_mfma_f32_16x16x32_bf16 v[168:171], v[48:51], v[192:195], v[168:171]\n\t" \
    "ds_read_b128 v[188:191], v249 offset:19456\n\t" \
    "s_waitcnt lgkmcnt(6)\n\t" \
    "v_mfma_f32_16x16x32_bf16 v[168:171], v[52:55], v[196:199], v[168:171]\n\t" \
    "ds_read_b32 v236, v251 offset:72\n\t" \
    "ds_read_b64 v[238:239], v251 offset:64\n\t" \
    "s_waitcnt lgkmcnt(7)\n\t" \
    "v_mfma_f32_16x16x32_bf16 v[168:171], v[56:59], v[200:203], v[168:171]\n\t" \
    "s_waitcnt lgkmcnt(6)\n\t" \
    "v_mfma_f32_16x16x32_bf16 v[168:171], v[60:63], v[204:207], v[168:171]\n\t" \
    "s_waitcnt lgkmcnt(5)\n\t" \
    "v_mfma_f32_16x16x32_bf16 v[168:171], v[64:67], v[176:179], v[168:171]\n\t" \
    "s_waitcnt lgkmcnt(4)\n\t" \
    "v_mfma_f32_16x16x32_bf16 v[168:171], v[68:71], v[180:183], v[168:171]\n\t" \
    ".Lw115_%=:\n\t" \
    "s_waitcnt lgkmcnt(0)\n\t" \
    "v_cmp_ne_u32 vcc, s40, v236\n\t" \
    "s_cbranch_vccz .Lw116_%=\n\t" \
    "ds_read_b32 v236, v251 offset:72\n\t" \
    "ds_read_b64 v[238:239], v251 offset:64\n\t" \
    "s_branch .Lw115_%=\n\t" \
    ".Lw116_%=:\n\t" \
    "s_waitcnt lgkmcnt(0)\n\t" \
    "v_mul_f32 v240, v160, v239\n\t" \
    "v_mul_f32 v241, v161, v239\n\t" \
    "v_mfma_f32_16x16x32_bf16 v[168:171], v[72:75], v[184:187], v[168:171]\n\t" \
    "v_mul_f32 v242, v162, v239\n\t" \
    "v_mul_f32 v243, v163, v239\n\t" \
    "v_mfma_f32_16x16x32_bf16 v[168:171], v[76:79], v[188:191], v[168:171]\n\t" \
    "v_mov_b32 v237, 0xc0135761\n\t" \
    "s_nop 6\n\t" \
    "v_fma_f32 v168, v168, v238, v240\n\t" \
    "v_fma_f32 v169, v169, v238, v241\n\t" \
    "v_fma_f32 v170, v170, v238, v242\n\t" \
    "v_fma_f32 v171, v171, v238, v243\n\t" \
    "v_mul_f32 v176, v168, v168\n\t" \
    "v_mul_f32 v177, v169, v169\n\t" \
    "v_mul_f32 v178, v170, v170\n\t" \
    "v_mul_f32 v179, v171, v171\n\t" \
    "v_fmamk_f32 v176, v176, 0xbdd2d3e7, v237\n\t" \
    "v_fmamk_f32 v177, v177, 0xbdd2d3e7, v237\n\t" \
    "v_fmamk_f32 v178, v178, 0xbdd2d3e7, v237\n\t" \
    "v_fmamk_f32 v179, v179, 0xbdd2d3e7, v237\n\t" \
    "v_mul_f32 v176, v168, v176\n\t" \
    "v_mul_f32 v177, v169, v177\n\t" \
    "v_mul_f32 v178, v170, v178\n\t" \
    "v_mul_f32 v179, v171, v179\n\t" \
    "v_exp_f32 v176, v176\n\t" \
    "v_exp_f32 v177, v177\n\t" \
    "v_exp_f32 v178, v178\n\t" \
    "v_exp_f32 v179, v179\n\t" \
    "v_add_f32 v176, 1.0, v176\n\t" \
    "v_add_f32 v177, 1.0, v177\n\t" \
    "v_add_f32 v178, 1.0, v178\n\t" \
    "v_add_f32 v179, 1.0, v179\n\t" \
    "v_rcp_f32 v176, v176\n\t" \
    "v_rcp_f32 v177, v177\n\t" \
    "v_rcp_f32 v178, v178\n\t" \
    "v_rcp_f32 v179, v179\n\t" \
    "v_mul_f32 v168, v168, v176\n\t" \
    "v_mul_f32 v169, v169, v177\n\t" \
    "v_mul_f32 v170, v170, v178\n\t" \
    "v_mul_f32 v171, v171, v179\n\t" \
    "s_waitcnt vmcnt(0)\n\t" \
    "v_cvt_pk_bf16_f32 v184, v168, v169\n\t" \
    "v_cvt_pk_bf16_f32 v185, v170, v171\n\t" \
    "v_min_u32 v184, -2, v184\n\t" \
    "v_min_u32 v185, -2, v185\n\t" \
    "s_cmp_eq_u32 s40, s42\n\t" \
    "s_cselect_b64 s[44:45], s[58:59], 0\n\t" \
    "s_andn2_b64 exec, -1, s[44:45]\n\t" \
    "buffer_store_dwordx2 v[184:185], v80, %[rs], s49 offen sc1\n\t" \
    "s_mov_b64 exec, -1\n\t" \
    "s_cmp_ge_u32 s40, 122\n\t" \
    "s_cbranch_scc1 .Lw117_%=\n\t" \
    "s_add_i32 s44, s40, s62\n\t" \
    "s_max_i32 s44, s44, 0\n\t" \
    "s_lshr_b32 s45, s44, 1\n\t" \
    "s_and_b32 s44, s44, 1\n\t" \
    "s_lshl_b32 s44, s44, 12\n\t" \
    "s_mul_i32 s51, s45, 0x14000\n\t" \
    "s_add_u32 s51, s51, s44\n\t" \
    "s_lshr_b32 s45, s45, 4\n\t" \
    "s_mul_i32 s45, s45, 0xc2000\n\t" \
    "s_add_u32 s51, s51, s45\n\t" \
    "s_add_i32 s44, s40, s63\n\t" \
    "s_max_i32 s44, s44, 0\n\t" \
    "s_lshr_b32 s45, s44, 1\n\t" \
    "s_and_b32 s44, s44, 1\n\t" \
    "s_lshl_b32 s44, s44, 12\n\t" \
    "s_mul_i32 s50, s45, 0x14000\n\t" \
    "s_add_u32 s50, s50, s44\n\t" \
    "s_lshr_b32 s45, s45, 4\n\t" \
    "s_mul_i32 s45, s45, 0xc2000\n\t" \
    "s_add_u32 s50, s50, s45\n\t" \
    "s_sub_u32 s50, s50, s51\n\t" \
    "s_add_u32 s51, s51, 0x2400000\n\t" \
    "v_mad_u32_u24 v81, v255, s50, v254\n\t" \
    "v_mov_b64 v[188:189], -1\n\t" \
    "s_add_u32 s44, s40, 6\n\t" \
    "s_cmp_eq_u32 s44, s42\n\t" \
    "s_cselect_b64 s[44:45], s[58:59], 0\n\t" \
    "s_andn2_b64 exec, -1, s[44:45]\n\t" \
    "buffer_store_dwordx2 v[188:189], v81, %[rs], s51 offen sc1\n\t" \
    "s_mov_b64 exec, -1\n\t" \
    ".Lw117_%=:\n\t" \
    "v_xor_b32 v249, 0x8000, v249\n\t" \
    "v_xor_b32 v250, 0x8000, v250\n\t" \
    "v_xor_b32 v251, 0x800, v251\n\t" \
    "v_xor_b32 v252, 0x800, v252\n\t" \
    "s_add_u32 s40, s40, 1\n\t" \
    "s_cmp_lg_u32 s40, 128\n\t" \
    "s_cbranch_scc1 .Lw14_%=\n\t" \
    "s_branch .Lend%=\n\t" \
    ".Lend%=:\n\t" \
    "s_waitcnt vmcnt(0) lgkmcnt(0)\n\t" \
    "s_mov_b32 %[bud], s41\n\t"
#define ASM_W2_LOOP \
    "s_mov_b32 s40, 0\n\t" \
    "s_mov_b32 s41, 0x20000\n\t" \
    "s_add_u32 m0, %[wm], 0x0\n\t" \
    "s_add_u32 s44, %[wo], 0x0\n\t" \
    "buffer_load_dwordx4 v176, %[wr], s44 offen nt lds\n\t" \
    "s_add_u32 m0, %[wm], 0x2000\n\t" \
    "s_add_u32 s44, %[wo], 0xa000\n\t" \
    "buffer_load_dwordx4 v176, %[wr], s44 offen nt lds\n\t" \
    "s_add_u32 m0, %[wm], 0x4000\n\t" \
    "s_add_u32 s44, %[wo], 0x14000\n\t" \
    "buffer_load_dwordx4 v176, %[wr], s44 offen nt lds\n\t" \
    "s_add_u32 m0, %[wm], 0x6000\n\t" \
    "s_add_u32 s44, %[wo], 0x1e000\n\t" \
    "buffer_load_dwordx4 v176, %[wr], s44 offen nt lds\n\t" \
    "s_add_u32 m0, %[wm], 0x8000\n\t" \
    "s_add_u32 s44, %[wo], 0x28000\n\t" \
    "buffer_load_dwordx4 v176, %[wr], s44 offen nt lds\n\t" \
    "s_add_u32 m0, %[wm], 0xa000\n\t" \
    "s_add_u32 s44, %[wo], 0x32000\n\t" \
    "buffer_load_dwordx4 v176, %[wr], s44 offen nt lds\n\t" \
    "s_add_u32 m0, %[wm], 0xc000\n\t" \
    "s_add_u32 s44, %[wo], 0x3c000\n\t" \
    "buffer_load_dwordx4 v176, %[wr], s44 offen nt lds\n\t" \
    "s_add_u32 m0, %[wm], 0xe000\n\t" \
    "s_add_u32 s44, %[wo], 0x46000\n\t" \
    "buffer_load_dwordx4 v176, %[wr], s44 offen nt lds\n\t" \
    "s_add_u32 m0, %[wm], 0x10000\n\t" \
    "s_add_u32 s44, %[wo], 0x50000\n\t" \
    "buffer_load_dwordx4 v176, %[wr], s44 offen nt lds\n\t" \
    "s_add_u32 m0, %[wm], 0x12000\n\t" \
    "s_add_u32 s44, %[wo], 0x5a000\n\t" \
    "buffer_load_dwordx4 v176, %[wr], s44 offen nt lds\n\t" \
    "s_add_u32 m0, %[wm], 0x14000\n\t" \
    "s_add_u32 s44, %[wo], 0x64000\n\t" \
    "buffer_load_dwordx4 v176, %[wr], s44 offen nt lds\n\t" \
    "s_add_u32 m0, %[wm], 0x16000\n\t" \
    "s_add_u32 s44, %[wo], 0x6e000\n\t" \
    "buffer_load_dwordx4 v176, %[wr], s44 offen nt lds\n\t" \
    "s_add_u32 m0, %[wm], 0x18000\n\t" \
    "s_add_u32 s44, %[wo], 0x78000\n\t" \
    "buffer_load_dwordx4 v176, %[wr], s44 offen nt lds\n\t" \
    "s_add_u32 m0, %[wm], 0x1a000\n\t" \
    "s_add_u32 s44, %[wo], 0x82000\n\t" \
    "buffer_load_dwordx4 v176, %[wr], s44 offen nt lds\n\t" \
    "s_add_u32 m0, %[wm], 0x1c000\n\t" \
    "s_add_u32 s44, %[wo], 0x8c000\n\t" \
    "buffer_load_dwordx4 v176, %[wr], s44 offen nt lds\n\t" \
    "s_add_u32 m0, %[wm], 0x1e000\n\t" \
    "s_add_u32 s44, %[wo], 0x96000\n\t" \
    "buffer_load_dwordx4 v176, %[wr], s44 offen nt lds\n\t" \
    "s_add_u32 m0, %[wm], 0x20000\n\t" \
    "s_add_u32 s44, %[wo], 0xa0000\n\t" \
    "buffer_load_dwordx4 v176, %[wr], s44 offen nt lds\n\t" \
    "s_add_u32 m0, %[wm], 0x22000\n\t" \
    "s_add_u32 s44, %[wo], 0xaa000\n\t" \
    "buffer_load_dwordx4 v176, %[wr], s44 offen nt lds\n\t" \
    "s_add_u32 m0, %[wm], 0x24000\n\t" \
    "s_add_u32 s44, %[wo], 0xb4000\n\t" \
    "buffer_load_dwordx4 v176, %[wr], s44 offen nt lds\n\t" \
    "s_add_u32 m0, %[wm], 0x26000\n\t" \
    "s_add_u32 s44, %[wo], 0xbe000\n\t" \
    "buffer_load_dwordx4 v176, %[wr], s44 offen nt lds\n\t" \
    "s_waitcnt vmcnt(0) lgkmcnt(0)\n\t" \
    "s_barrier \n\t" \
    "v_add_u32 v178, 0x0, v177\n\t" \
    "ds_read_b64_tr_b16 v[0:1], v178 offset:0\n\t" \
    "ds_read_b64_tr_b16 v[2:3], v178 offset:1024\n\t" \
    "ds_read_b64_tr_b16 v[4:5], v178 offset:8192\n\t" \
    "ds_read_b64_tr_b16 v[6:7], v178 offset:9216\n\t" \
    "ds_read_b64_tr_b16 v[8:9], v178 offset:16384\n\t" \
    "ds_read_b64_tr_b16 v[10:11], v178 offset:17408\n\t" \
    "ds_read_b64_tr_b16 v[12:13], v178 offset:24576\n\t" \
    "ds_read_b64_tr_b16 v[14:15], v178 offset:25600\n\t" \
    "v_add_u32 v178, 0x8000, v177\n\t" \
    "ds_read_b64_tr_b16 v[16:17], v178 offset:0\n\t" \
    "ds_read_b64_tr_b16 v[18:19], v178 offset:1024\n\t" \
    "ds_read_b64_tr_b16 v[20:21], v178 offset:8192\n\t" \
    "ds_read_b64_tr_b16 v[22:23], v178 offset:9216\n\t" \
    "ds_read_b64_tr_b16 v[24:25], v178 offset:16384\n\t" \
    "ds_read_b64_tr_b16 v[26:27], v178 offset:17408\n\t" \
    "ds_read_b64_tr_b16 v[28:29], v178 offset:24576\n\t" \
    "ds_read_b64_tr_b16 v[30:31], v178 offset:25600\n\t" \
    "v_add_u32 v178, 0x10000, v177\n\t" \
    "ds_read_b64_tr_b16 v[32:33], v178 offset:0\n\t" \
    "ds_read_b64_tr_b16 v[34:35], v178 offset:1024\n\t" \
    "ds_read_b64_tr_b16 v[36:37], v178 offset:8192\n\t" \
    "ds_read_b64_tr_b16 v[38:39], v178 offset:9216\n\t" \
    "ds_read_b64_tr_b16 v[40:41], v178 offset:16384\n\t" \
    "ds_read_b64_tr_b16 v[42:43], v178 offset:17408\n\t" \
    "ds_read_b64_tr_b16 v[44:45], v178 offset:24576\n\t" \
    "ds_read_b64_tr_b16 v[46:47], v178 offset:25600\n\t" \
    "v_add_u32 v178, 0x18000, v177\n\t" \
    "ds_read_b64_tr_b16 v[48:49], v178 offset:0\n\t" \
    "ds_read_b64_tr_b16 v[50:51], v178 offset:1024\n\t" \
    "ds_read_b64_tr_b16 v[52:53], v178 offset:8192\n\t" \
    "ds_read_b64_tr_b16 v[54:55], v178 offset:9216\n\t" \
    "ds_read_b64_tr_b16 v[56:57], v178 offset:16384\n\t" \
    "ds_read_b64_tr_b16 v[58:59], v178 offset:17408\n\t" \
    "ds_read_b64_tr_b16 v[60:61], v178 offset:24576\n\t" \
    "ds_read_b64_tr_b16 v[62:63], v178 offset:25600\n\t" \
    "v_add_u32 v178, 0x20000, v177\n\t" \
    "ds_read_b64_tr_b16 v[64:65], v178 offset:0\n\t" \
    "ds_read_b64_tr_b16 v[66:67], v178 offset:1024\n\t" \
    "ds_read_b64_tr_b16 v[68:69], v178 offset:8192\n\t" \
    "ds_read_b64_tr_b16 v[70:71], v178 offset:9216\n\t" \
    "ds_read_b64_tr_b16 v[72:73], v178 offset:16384\n\t" \
    "ds_read_b64_tr_b16 v[74:75], v178 offset:17408\n\t" \
    "ds_read_b64_tr_b16 v[76:77], v178 offset:24576\n\t" \
    "ds_read_b64_tr_b16 v[78:79], v178 offset:25600\n\t" \
    "v_mov_b32 v228, 0x3f803f80\n\t" \
    "v_mov_b32 v229, 0x3f803f80\n\t" \
    "v_mov_b32 v230, 0x3f803f80\n\t" \
    "v_mov_b32 v231, 0x3f803f80\n\t" \
    "v_mov_b64 v[216:217], 0\n\t" \
    "v_mov_b32 v219, 0\n\t" \
    "v_bfe_u32 v255, v253, 2, 5\n\t" \
    "v_add_u32 v255, 0x7000, v255\n\t" \
    "v_mov_b32 v218, 0\n\t" \
    "s_waitcnt lgkmcnt(0)\n\t" \
    "s_barrier \n\t" \
    "ds_write_b32 v255, v218\n\t" \
    "s_waitcnt lgkmcnt(0)\n\t" \
    "s_barrier \n\t" \
    ".Lw21_%=:\n\t" \
    "v_mov_b32 v248, 0\n\t" \
    "buffer_load_dword v248, v248, %[rs], %[go] offen sc1\n\t" \
    "s_waitcnt vmcnt(0)\n\t" \
    "v_cmp_ne_u32 vcc, 0, v248\n\t" \
    "s_cbranch_vccnz .Lw22_%=\n\t" \
    "s_cmp_eq_u32 s41, 0\n\t" \
    "s_cbranch_scc1 .Lw22_%=\n\t" \
    "s_sub_u32 s41, s41, 1\n\t" \
    "s_sleep 16\n\t" \
    "s_branch .Lw21_%=\n\t" \
    ".Lw22_%=:\n\t" \
    "s_lshr_b32 s48, %[wave], 2\n\t" \
    "s_mul_i32 s48, s48, 0x3000\n\t" \
    "s_add_u32 s48, s48, %[pi]\n\t" \
    "s_mov_b32 s43, 0\n\t" \
    "s_add_u32 s46, s43, s48\n\t" \
    "s_add_u32 s47, s46, 0x0\n\t" \
    "buffer_load_dwordx4 v[208:211], v253, %[rs], s47 offen sc1\n\t" \
    "s_add_u32 s47, s46, 0x8000\n\t" \
    "buffer_load_dwordx4 v[212:215], v253, %[rs], s47 offen sc1\n\t" \
    "s_cmp_gt_u32 %[wave], 3\n\t" \
    "s_cbranch_scc1 .Lw23_%=\n\t" \
    "s_add_u32 s47, s46, 0x10000\n\t" \
    "buffer_load_dwordx4 v[216:219], v253, %[rs], s47 offen sc1\n\t" \
    ".Lw23_%=:\n\t" \
    "s_waitcnt vmcnt(0)\n\t" \
    ".Lw24_%=:\n\t" \
    "s_add_u32 s49, s43, 0x2c00000\n\t" \
    "s_add_u32 s50, s43, 0x1c00000\n\t" \
    "s_lshr_b32 s44, s40, 6\n\t" \
    "s_lshl_b32 s44, s44, 14\n\t" \
    "s_and_b32 s45, s40, 63\n\t" \
    "s_lshl_b32 s45, s45, 7\n\t" \
    "s_add_u32 s44, s44, s45\n\t" \
    "s_add_u32 s51, s44, 0x4d0000\n\t" \
    "s_add_i32 s44, s40, 1\n\t" \
    "s_max_i32 s44, s44, 0\n\t" \
    "s_lshr_b32 s45, s44, 1\n\t" \
    "s_and_b32 s44, s44, 1\n\t" \
    "s_lshl_b32 s44, s44, 12\n\t" \
    "s_mul_i32 s43, s45, 0x14000\n\t" \
    "s_add_u32 s43, s43, s44\n\t" \
    "s_lshr_b32 s45, s45, 4\n\t" \
    "s_mul_i32 s45, s45, 0xc2000\n\t" \
    "s_add_u32 s43, s43, s45\n\t" \
    "s_setprio 2\n\t" \
    "v_max_u32 v248, v208, v209\n\t" \
    "v_max3_u32 v248, v210, v211, v248\n\t" \
    "v_max3_u32 v248, v212, v213, v248\n\t" \
    "v_max3_u32 v248, v214, v215, v248\n\t" \
    "v_max3_u32 v248, v216, v217, v248\n\t" \
    "v_max3_u32 v248, v218, v219, v248\n\t" \
    "v_cmp_eq_u32 vcc, -1, v248\n\t" \
    "s_cbranch_vccz .Lw26_%=\n\t" \
    "s_and_b32 s44, s40, 1\n\t" \
    "s_cmp_eq_u32 s44, 0\n\t" \
    "s_cbranch_scc1 .Lw25_%=\n\t" \
    "s_cmp_gt_u32 s40, 123\n\t" \
    "s_cbranch_scc1 .Lw25_%=\n\t" \
    "s_sleep 6\n\t" \
    ".Lw25_%=:\n\t" \
    "v_max_u32 v248, v208, v209\n\t" \
    "v_max3_u32 v248, v210, v211, v248\n\t" \
    "v_cmp_eq_u32 vcc, -1, v248\n\t" \
    "s_cbranch_vccz .Lw28_%=\n\t" \
    "s_add_u32 s47, s46, 0x0\n\t" \
    "buffer_load_dwordx4 v[208:211], v253, %[rs], s47 offen sc1\n\t" \
    ".Lw28_%=:\n\t" \
    "v_max_u32 v248, v212, v213\n\t" \
    "v_max3_u32 v248, v214, v215, v248\n\t" \
    "v_cmp_eq_u32 vcc, -1, v248\n\t" \
    "s_cbranch_vccz .Lw29_%=\n\t" \
    "s_add_u32 s47, s46, 0x8000\n\t" \
    "buffer_load_dwordx4 v[212:215], v253, %[rs], s47 offen sc1\n\t" \
    ".Lw29_%=:\n\t" \
    "v_max_u32 v248, v216, v217\n\t" \
    "v_max3_u32 v248, v218, v219, v248\n\t" \
    "v_cmp_eq_u32 vcc, -1, v248\n\t" \
    "s_cbranch_vccz .Lw210_%=\n\t" \
    "s_add_u32 s47, s46, 0x10000\n\t" \
    "buffer_load_dwordx4 v[216:219], v253, %[rs], s47 offen sc1\n\t" \
    ".Lw210_%=:\n\t" \
    "s_waitcnt vmcnt(0)\n\t" \
    "v_max_u32 v248, v208, v209\n\t" \
    "v_max3_u32 v248, v210, v211, v248\n\t" \
    "v_max3_u32 v248, v212, v213, v248\n\t" \
    "v_max3_u32 v248, v214, v215, v248\n\t" \
    "v_max3_u32 v248, v216, v217, v248\n\t" \
    "v_max3_u32 v248, v218, v219, v248\n\t" \
    "v_cmp_eq_u32 vcc, -1, v248\n\t" \
    "s_cbranch_vccz .Lw26_%=\n\t" \
    "s_cmp_eq_u32 s41, 0\n\t" \
    "s_cbranch_scc1 .Lw26_%=\n\t" \
    "s_sub_u32 s41, s41, 1\n\t" \
    "s_branch .Lw25_%=\n\t" \
    ".Lw26_%=:\n\t" \
    "ds_write_b128 v250, v[208:211] offset:0\n\t" \
    "ds_write_b128 v250, v[212:215] offset:8192\n\t" \
    "s_cmp_gt_u32 %[wave], 3\n\t" \
    "s_cbranch_scc1 .Lw211_%=\n\t" \
    "ds_write_b128 v250, v[216:219] offset:16384\n\t" \
    ".Lw211_%=:\n\t" \
    "s_add_u32 s46, s43, s48\n\t" \
    "s_waitcnt lgkmcnt(0)\n\t" \
    "s_barrier \n\t" \
    "s_setprio 0\n\t" \
    "ds_read_b128 v[176:179], v249 offset:0\n\t" \
    "buffer_load_dwordx2 v[232:233], v254, %[rs], s50 offen sc1\n\t" \
    "buffer_load_dwordx2 v[234:235], v251, %[rs], s51 offen sc1\n\t" \
    "ds_read_b128 v[180:183], v249 offset:1024\n\t" \
    "ds_read_b128 v[184:187], v249 offset:2048\n\t" \
    "ds_read_b128 v[188:191], v249 offset:3072\n\t" \
    "ds_read_b128 v[192:195], v249 offset:4096\n\t" \
    "ds_read_b128 v[196:199], v249 offset:5120\n\t" \
    "ds_read_b128 v[200:203], v249 offset:6144\n\t" \
    "s_waitcnt lgkmcnt(6)\n\t" \
    "v_mfma_f32_16x16x32_bf16 v[168:171], v[0:3], v[176:179], 0\n\t" \
    "ds_read_b128 v[204:207], v249 offset:7168\n\t" \
    "s_waitcnt lgkmcnt(6)\n\t" \
    "v_mfma_f32_16x16x32_bf16 v[168:171], v[4:7], v[180:183], v[168:171]\n\t" \
    "ds_read_b128 v[176:179], v249 offset:8192\n\t" \
    "s_waitcnt lgkmcnt(6)\n\t" \
    "v_mfma_f32_16x16x32_bf16 v[168:171], v[8:11], v[184:187], v[168:171]\n\t" \
    "ds_read_b128 v[180:183], v249 offset:9216\n\t" \
    "s_waitcnt lgkmcnt(6)\n\t" \
    "v_mfma_f32_16x16x32_bf16 v[168:171], v[12:15], v[188:191], v[168:171]\n\t" \
    "ds_read_b128 v[184:187], v249 offset:10240\n\t" \
    "s_add_u32 s47, s46, 0x0\n\t" \
    "buffer_load_dwordx4 v[208:211], v253, %[rs], s47 offen sc1\n\t" \
    "s_add_u32 s47, s46, 0x8000\n\t" \
    "buffer_load_dwordx4 v[212:215], v253, %[rs], s47 offen sc1\n\t" \
    "s_cmp_gt_u32 %[wave], 3\n\t" \
    "s_cbranch_scc1 .Lw212_%=\n\t" \
    "s_add_u32 s47, s46, 0x10000\n\t" \
    "buffer_load_dwordx4 v[216:219], v253, %[rs], s47 offen sc1\n\t" \
    ".Lw212_%=:\n\t" \
    "s_waitcnt lgkmcnt(6)\n\t" \
    "v_mfma_f32_16x16x32_bf16 v[168:171], v[16:19], v[192:195], v[168:171]\n\t" \
    "ds_read_b128 v[188:191], v249 offset:11264\n\t" \
    "s_waitcnt lgkmcnt(6)\n\t" \
    "v_mfma_f32_16x16x32_bf16 v[168:171], v[20:23], v[196:199], v[168:171]\n\t" \
    "ds_read_b128 v[192:195], v249 offset:12288\n\t" \
    "s_waitcnt lgkmcnt(6)\n\t" \
    "v_mfma_f32_16x16x32_bf16 v[168:171], v[24:27], v[200:203], v[168:171]\n\t" \
    "ds_read_b128 v[196:199], v249 offset:13312\n\t" \
    "s_waitcnt lgkmcnt(6)\n\t" \
    "v_mfma_f32_16x16x32_bf16 v[168:171], v[28:31], v[204:207], v[168:171]\n\t" \
    "ds_read_b128 v[200:203], v249 offset:14336\n\t" \
    "s_waitcnt lgkmcnt(6)\n\t" \
    "v_mfma_f32_16x16x32_bf16 v[168:171], v[32:35], v[176:179], v[168:171]\n\t" \
    "ds_read_b128 v[204:207], v249 offset:15360\n\t" \
    "s_waitcnt lgkmcnt(6)\n\t" \
    "v_mfma_f32_16x16x32_bf16 v[168:171], v[36:39], v[180:183], v[168:171]\n\t" \
    "ds_read_b128 v[176:179], v249 offset:16384\n\t" \
    "s_waitcnt lgkmcnt(6)\n\t" \
    "v_mfma_f32_16x16x32_bf16 v[168:171], v[40:43], v[184:187], v[168:171]\n\t" \
    "ds_read_b128 v[180:183], v249 offset:17408\n\t" \
    "s_waitcnt lgkmcnt(6)\n\t" \
    "v_mfma_f32_16x16x32_bf16 v[168:171], v[44:47], v[188:191], v[168:171]\n\t" \
    "ds_read_b128 v[184:187], v249 offset:18432\n\t" \
    "s_waitcnt lgkmcnt(6)\n\t" \
    "v_mfma_f32_16x16x32_bf16 v[168:171], v[48:51], v[192:195], v[168:171]\n\t" \
    "ds_read_b128 v[188:191], v249 offset:19456\n\t" \
    "s_waitcnt lgkmcnt(6)\n\t" \
    "v_mfma_f32_16x16x32_bf16 v[168:171], v[52:55], v[196:199], v[168:171]\n\t" \
    "s_waitcnt lgkmcnt(5)\n\t" \
    "v_mfma_f32_16x16x32_bf16 v[168:171], v[56:59], v[200:203], v[168:171]\n\t" \
    "s_waitcnt lgkmcnt(4)\n\t" \
    "v_mfma_f32_16x16x32_bf16 v[168:171], v[60:63], v[204:207], v[168:171]\n\t" \
    "s_waitcnt lgkmcnt(3)\n\t" \
    "v_mfma_f32_16x16x32_bf16 v[168:171], v[64:67], v[176:179], v[168:171]\n\t" \
    "s_waitcnt lgkmcnt(2)\n\t" \
    "v_mfma_f32_16x16x32_bf16 v[168:171], v[68:71], v[180:183], v[168:171]\n\t" \
    "s_waitcnt lgkmcnt(1)\n\t" \
    "v_mfma_f32_16x16x32_bf16 v[168:171], v[72:75], v[184:187], v[168:171]\n\t" \
    "s_waitcnt lgkmcnt(0)\n\t" \
    "v_mfma_f32_16x16x32_bf16 v[168:171], v[76:79], v[188:191], v[168:171]\n\t" \
    "s_waitcnt vmcnt(0)\n\t" \
    ".Lw213_%=:\n\t" \
    "v_max_u32 v248, v234, v235\n\t" \
    "v_cmp_eq_u32 vcc, -1, v248\n\t" \
    "s_cbranch_vccz .Lw214_%=\n\t" \
    "s_cmp_eq_u32 s41, 0\n\t" \
    "s_cbranch_scc1 .Lw214_%=\n\t" \
    "s_sub_u32 s41, s41, 1\n\t" \
    "buffer_load_dwordx2 v[234:235], v251, %[rs], s51 offen sc1\n\t" \
    "s_waitcnt vmcnt(0)\n\t" \
    "s_branch .Lw213_%=\n\t" \
    ".Lw214_%=:\n\t" \
    "v_lshlrev_b32 v176, 16, v232\n\t" \
    "v_and_b32 v177, 0xffff0000, v232\n\t" \
    "v_lshlrev_b32 v178, 16, v233\n\t" \
    "v_and_b32 v179, 0xffff0000, v233\n\t" \
    "v_sub_f32 v176, v176, v234\n\t" \
    "v_sub_f32 v177, v177, v234\n\t" \
    "v_sub_f32 v178, v178, v234\n\t" \
    "v_sub_f32 v179, v179, v234\n\t" \
    "v_mul_f32 v176, v176, v235\n\t" \
    "v_mul_f32 v177, v177, v235\n\t" \
    "v_mul_f32 v178, v178, v235\n\t" \
    "v_mul_f32 v179, v179, v235\n\t" \
    "v_cvt_pk_bf16_f32 v180, v176, v177\n\t" \
    "v_cvt_pk_bf16_f32 v181, v178, v179\n\t" \
    "v_lshlrev_b32 v176, 16, v180\n\t" \
    "v_and_b32 v177, 0xffff0000, v180\n\t" \
    "v_lshlrev_b32 v178, 16, v181\n\t" \
    "v_and_b32 v179, 0xffff0000, v181\n\t" \
    "v_add_f32 v168, v176, v168\n\t" \
    "v_add_f32 v169, v177, v169\n\t" \
    "v_add_f32 v170, v178, v170\n\t" \
    "v_add_f32 v171, v179, v171\n\t" \
    "v_cvt_pk_bf16_f32 v184, v168, v169\n\t" \
    "v_cvt_pk_bf16_f32 v185, v170, v171\n\t" \
    "v_min_u32 v184, -2, v184\n\t" \
    "v_min_u32 v185, -2, v185\n\t" \
    "buffer_store_dwordx2 v[184:185], v254, %[rs], s49 offen sc1\n\t" \
    "s_cmp_ge_u32 s40, 122\n\t" \
    "s_cbranch_scc1 .Lw215_%=\n\t" \
    "s_add_i32 s44, s40, 6\n\t" \
    "s_max_i32 s44, s44, 0\n\t" \
    "s_lshr_b32 s45, s44, 1\n\t" \
    "s_and_b32 s44, s44, 1\n\t" \
    "s_lshl_b32 s44, s44, 12\n\t" \
    "s_mul_i32 s50, s45, 0x14000\n\t" \
    "s_add_u32 s50, s50, s44\n\t" \
    "s_lshr_b32 s45, s45, 4\n\t" \
    "s_mul_i32 s45, s45, 0xc2000\n\t" \
    "s_add_u32 s50, s50, s45\n\t" \
    "s_add_u32 s50, s50, 0x2c00000\n\t" \
    "v_mov_b64 v[188:189], -1\n\t" \
    "buffer_store_dwordx2 v[188:189], v254, %[rs], s50 offen sc1\n\t" \
    ".Lw215_%=:\n\t" \
    "v_xor_b32 v249, 0x8000, v249\n\t" \
    "v_xor_b32 v250, 0x8000, v250\n\t" \
    "s_add_u32 s40, s40, 1\n\t" \
    "s_cmp_lg_u32 s40, 128\n\t" \
    "s_cbranch_scc1 .Lw24_%=\n\t" \
    "s_branch .Lend%=\n\t" \
    ".Lend%=:\n\t" \
    "s_waitcnt vmcnt(0) lgkmcnt(0)\n\t" \
    "s_mov_b32 %[bud], s41\n\t"
#define ASM_W3_LOOP \
    "s_mov_b32 s40, 0\n\t" \
    "s_mov_b32 s41, 0x20000\n\t" \
    "s_add_u32 m0, %[wm], 0x0\n\t" \
    "s_add_u32 s44, %[wo], 0x0\n\t" \
    "buffer_load_dwordx4 v176, %[wr], s44 offen nt lds\n\t" \
    "s_add_u32 m0, %[wm], 0x2000\n\t" \
    "s_add_u32 s44, %[wo], 0xc000\n\t" \
    "buffer_load_dwordx4 v176, %[wr], s44 offen nt lds\n\t" \
    "s_add_u32 m0, %[wm], 0x4000\n\t" \
    "s_add_u32 s44, %[wo], 0x18000\n\t" \
    "buffer_load_dwordx4 v176, %[wr], s44 offen nt lds\n\t" \
    "s_add_u32 m0, %[wm], 0x6000\n\t" \
    "s_add_u32 s44, %[wo], 0x24000\n\t" \
    "buffer_load_dwordx4 v176, %[wr], s44 offen nt lds\n\t" \
    "s_add_u32 m0, %[wm], 0x8000\n\t" \
    "s_add_u32 s44, %[wo], 0x30000\n\t" \
    "buffer_load_dwordx4 v176, %[wr], s44 offen nt lds\n\t" \
    "s_add_u32 m0, %[wm], 0xa000\n\t" \
    "s_add_u32 s44, %[wo], 0x3c000\n\t" \
    "buffer_load_dwordx4 v176, %[wr], s44 offen nt lds\n\t" \
    "s_add_u32 m0, %[wm], 0xc000\n\t" \
    "s_add_u32 s44, %[wo], 0x48000\n\t" \
    "buffer_load_dwordx4 v176, %[wr], s44 offen nt lds\n\t" \
    "s_add_u32 m0, %[wm], 0xe000\n\t" \
    "s_add_u32 s44, %[wo], 0x54000\n\t" \
    "buffer_load_dwordx4 v176, %[wr], s44 offen nt lds\n\t" \
    "s_add_u32 m0, %[wm], 0x10000\n\t" \
    "s_add_u32 s44, %[wo], 0x60000\n\t" \
    "buffer_load_dwordx4 v176, %[wr], s44 offen nt lds\n\t" \
    "s_add_u32 m0, %[wm], 0x12000\n\t" \
    "s_add_u32 s44, %[wo], 0x6c000\n\t" \
    "buffer_load_dwordx4 v176, %[wr], s44 offen nt lds\n\t" \
    "s_add_u32 m0, %[wm], 0x14000\n\t" \
    "s_add_u32 s44, %[wo], 0x78000\n\t" \
    "buffer_load_dwordx4 v176, %[wr], s44 offen nt lds\n\t" \
    "s_add_u32 m0, %[wm], 0x16000\n\t" \
    "s_add_u32 s44, %[wo], 0x84000\n\t" \
    "buffer_load_dwordx4 v176, %[wr], s44 offen nt lds\n\t" \
    "s_add_u32 m0, %[wm], 0x18000\n\t" \
    "s_add_u32 s44, %[wo], 0x90000\n\t" \
    "buffer_load_dwordx4 v176, %[wr], s44 offen nt lds\n\t" \
    "s_add_u32 m0, %[wm], 0x1a000\n\t" \
    "s_add_u32 s44, %[wo], 0x9c000\n\t" \
    "buffer_load_dwordx4 v176, %[wr], s44 offen nt lds\n\t" \
    "s_add_u32 m0, %[wm], 0x1c000\n\t" \
    "s_add_u32 s44, %[wo], 0xa8000\n\t" \
    "buffer_load_dwordx4 v176, %[wr], s44 offen nt lds\n\t" \
    "s_add_u32 m0, %[wm], 0x1e000\n\t" \
    "s_add_u32 s44, %[wo], 0xb4000\n\t" \
    "buffer_load_dwordx4 v176, %[wr], s44 offen nt lds\n\t" \
    "s_add_u32 m0, %[wm], 0x20000\n\t" \
    "s_add_u32 s44, %[wo], 0xc0000\n\t" \
    "buffer_load_dwordx4 v176, %[wr], s44 offen nt lds\n\t" \
    "s_add_u32 m0, %[wm], 0x22000\n\t" \
    "s_add_u32 s44, %[wo], 0xcc000\n\t" \
    "buffer_load_dwordx4 v176, %[wr], s44 offen nt lds\n\t" \
    "s_add_u32 m0, %[wm], 0x24000\n\t" \
    "s_add_u32 s44, %[wo], 0xd8000\n\t" \
    "buffer_load_dwordx4 v176, %[wr], s44 offen nt lds\n\t" \
    "s_add_u32 m0, %[wm], 0x26000\n\t" \
    "s_add_u32 s44, %[wo], 0xe4000\n\t" \
    "buffer_load_dwordx4 v176, %[wr], s44 offen nt lds\n\t" \
    "s_waitcnt vmcnt(0) lgkmcnt(0)\n\t" \
    "s_barrier \n\t" \
    "v_add_u32 v178, 0x0, v177\n\t" \
    "v_xor_b32 v179, 32, v178\n\t" \
    "ds_read_b64_tr_b16 v[0:1], v178 offset:0\n\t" \
    "ds_read_b64_tr_b16 v[2:3], v178 offset:2048\n\t" \
    "ds_read_b64_tr_b16 v[80:81], v179 offset:0\n\t" \
    "ds_read_b64_tr_b16 v[82:83], v179 offset:2048\n\t" \
    "ds_read_b64_tr_b16 v[4:5], v178 offset:16384\n\t" \
    "ds_read_b64_tr_b16 v[6:7], v178 offset:18432\n\t" \
    "ds_read_b64_tr_b16 v[84:85], v179 offset:16384\n\t" \
    "ds_read_b64_tr_b16 v[86:87], v179 offset:18432\n\t" \
    "v_add_u32 v178, 0x8000, v177\n\t" \
    "v_xor_b32 v179, 32, v178\n\t" \
    "ds_read_b64_tr_b16 v[8:9], v178 offset:0\n\t" \
    "ds_read_b64_tr_b16 v[10:11], v178 offset:2048\n\t" \
    "ds_read_b64_tr_b16 v[88:89], v179 offset:0\n\t" \
    "ds_read_b64_tr_b16 v[90:91], v179 offset:2048\n\t" \
    "ds_read_b64_tr_b16 v[12:13], v178 offset:16384\n\t" \
    "ds_read_b64_tr_b16 v[14:15], v178 offset:18432\n\t" \
    "ds_read_b64_tr_b16 v[92:93], v179 offset:16384\n\t" \
    "ds_read_b64_tr_b16 v[94:95], v179 offset:18432\n\t" \
    "v_add_u32 v178, 0x10000, v177\n\t" \
    "v_xor_b32 v179, 32, v178\n\t" \
    "ds_read_b64_tr_b16 v[16:17], v178 offset:0\n\t" \
    "ds_read_b64_tr_b16 v[18:19], v178 offset:2048\n\t" \
    "ds_read_b64_tr_b16 v[96:97], v179 offset:0\n\t" \
    "ds_read_b64_tr_b16 v[98:99], v179 offset:2048\n\t" \
    "ds_read_b64_tr_b16 v[20:21], v178 offset:16384\n\t" \
    "ds_read_b64_tr_b16 v[22:23], v178 offset:18432\n\t" \
    "ds_read_b64_tr_b16 v[100:101], v179 offset:16384\n\t" \
    "ds_read_b64_tr_b16 v[102:103], v179 offset:18432\n\t" \
    "v_add_u32 v178, 0x18000, v177\n\t" \
    "v_xor_b32 v179, 32, v178\n\t" \
    "ds_read_b64_tr_b16 v[24:25], v178 offset:0\n\t" \
    "ds_read_b64_tr_b16 v[26:27], v178 offset:2048\n\t" \
    "ds_read_b64_tr_b16 v[104:105], v179 offset:0\n\t" \
    "ds_read_b64_tr_b16 v[106:107], v179 offset:2048\n\t" \
    "ds_read_b64_tr_b16 v[28:29], v178 offset:16384\n\t" \
    "ds_read_b64_tr_b16 v[30:31], v178 offset:18432\n\t" \
    "ds_read_b64_tr_b16 v[108:109], v179 offset:16384\n\t" \
    "ds_read_b64_tr_b16 v[110:111], v179 offset:18432\n\t" \
    "v_add_u32 v178, 0x20000, v177\n\t" \
    "v_xor_b32 v179, 32, v178\n\t" \
    "ds_read_b64_tr_b16 v[32:33], v178 offset:0\n\t" \
    "ds_read_b64_tr_b16 v[34:35], v178 offset:2048\n\t" \
    "ds_read_b64_tr_b16 v[112:113], v179 offset:0\n\t" \
    "ds_read_b64_tr_b16 v[114:115], v179 offset:2048\n\t" \
    "ds_read_b64_tr_b16 v[36:37], v178 offset:16384\n\t" \
    "ds_read_b64_tr_b16 v[38:39], v178 offset:18432\n\t" \
    "ds_read_b64_tr_b16 v[116:117], v179 offset:16384\n\t" \
    "ds_read_b64_tr_b16 v[118:119], v179 offset:18432\n\t" \
    "s_waitcnt lgkmcnt(0)\n\t" \
    "s_barrier \n\t" \
    "s_add_u32 m0, %[wm], 0x0\n\t" \
    "s_add_u32 s44, %[wo], 0xf0000\n\t" \
    "buffer_load_dwordx4 v176, %[wr], s44 offen nt lds\n\t" \
    "s_add_u32 m0, %[wm], 0x2000\n\t" \
    "s_add_u32 s44, %[wo], 0xfc000\n\t" \
    "buffer_load_dwordx4 v176, %[wr], s44 offen nt lds\n\t" \
    "s_add_u32 m0, %[wm], 0x4000\n\t" \
    "s_add_u32 s44, %[wo], 0x108000\n\t" \
    "buffer_load_dwordx4 v176, %[wr], s44 offen nt lds\n\t" \
    "s_add_u32 m0, %[wm], 0x6000\n\t" \
    "s_add_u32 s44, %[wo], 0x114000\n\t" \
    "buffer_load_dwordx4 v176, %[wr], s44 offen nt lds\n\t" \
    "s_add_u32 m0, %[wm], 0x8000\n\t" \
    "s_add_u32 s44, %[wo], 0x120000\n\t" \
    "buffer_load_dwordx4 v176, %[wr], s44 offen nt lds\n\t" \
    "s_add_u32 m0, %[wm], 0xa000\n\t" \
    "s_add_u32 s44, %[wo], 0x12c000\n\t" \
    "buffer_load_dwordx4 v176, %[wr], s44 offen nt lds\n\t" \
    "s_add_u32 m0, %[wm], 0xc000\n\t" \
    "s_add_u32 s44, %[wo], 0x138000\n\t" \
    "buffer_load_dwordx4 v176, %[wr], s44 offen nt lds\n\t" \
    "s_add_u32 m0, %[wm], 0xe000\n\t" \
    "s_add_u32 s44, %[wo], 0x144000\n\t" \
    "buffer_load_dwordx4 v176, %[wr], s44 offen nt lds\n\t" \
    "s_add_u32 m0, %[wm], 0x10000\n\t" \
    "s_add_u32 s44, %[wo], 0x150000\n\t" \
    "buffer_load_dwordx4 v176, %[wr], s44 offen nt lds\n\t" \
    "s_add_u32 m0, %[wm], 0x12000\n\t" \
    "s_add_u32 s44, %[wo], 0x15c000\n\t" \
    "buffer_load_dwordx4 v176, %[wr], s44 offen nt lds\n\t" \
    "s_add_u32 m0, %[wm], 0x14000\n\t" \
    "s_add_u32 s44, %[wo], 0x168000\n\t" \
    "buffer_load_dwordx4 v176, %[wr], s44 offen nt lds\n\t" \
    "s_add_u32 m0, %[wm], 0x16000\n\t" \
    "s_add_u32 s44, %[wo], 0x174000\n\t" \
    "buffer_load_dwordx4 v176, %[wr], s44 offen nt lds\n\t" \
    "s_add_u32 m0, %[wm], 0x18000\n\t" \
    "s_add_u32 s44, %[wo], 0x180000\n\t" \
    "buffer_load_dwordx4 v176, %[wr], s44 offen nt lds\n\t" \
    "s_add_u32 m0, %[wm], 0x1a000\n\t" \
    "s_add_u32 s44, %[wo], 0x18c000\n\t" \
    "buffer_load_dwordx4 v176, %[wr], s44 offen nt lds\n\t" \
    "s_add_u32 m0, %[wm], 0x1c000\n\t" \
    "s_add_u32 s44, %[wo], 0x198000\n\t" \
    "buffer_load_dwordx4 v176, %[wr], s44 offen nt lds\n\t" \
    "s_add_u32 m0, %[wm], 0x1e000\n\t" \
    "s_add_u32 s44, %[wo], 0x1a4000\n\t" \
    "buffer_load_dwordx4 v176, %[wr], s44 offen nt lds\n\t" \
    "s_add_u32 m0, %[wm], 0x20000\n\t" \
    "s_add_u32 s44, %[wo], 0x1b0000\n\t" \
    "buffer_load_dwordx4 v176, %[wr], s44 offen nt lds\n\t" \
    "s_add_u32 m0, %[wm], 0x22000\n\t" \
    "s_add_u32 s44, %[wo], 0x1bc000\n\t" \
    "buffer_load_dwordx4 v176, %[wr], s44 offen nt lds\n\t" \
    "s_add_u32 m0, %[wm], 0x24000\n\t" \
    "s_add_u32 s44, %[wo], 0x1c8000\n\t" \
    "buffer_load_dwordx4 v176, %[wr], s44 offen nt lds\n\t" \
    "s_add_u32 m0, %[wm], 0x26000\n\t" \
    "s_add_u32 s44, %[wo], 0x1d4000\n\t" \
    "buffer_load_dwordx4 v176, %[wr], s44 offen nt lds\n\t" \
    "s_waitcnt vmcnt(0) lgkmcnt(0)\n\t" \
    "s_barrier \n\t" \
    "v_add_u32 v178, 0x0, v177\n\t" \
    "v_xor_b32 v179, 32, v178\n\t" \
    "ds_read_b64_tr_b16 v[40:41], v178 offset:0\n\t" \
    "ds_read_b64_tr_b16 v[42:43], v178 offset:2048\n\t" \
    "ds_read_b64_tr_b16 v[120:121], v179 offset:0\n\t" \
    "ds_read_b64_tr_b16 v[122:123], v179 offset:2048\n\t" \
    "ds_read_b64_tr_b16 v[44:45], v178 offset:16384\n\t" \
    "ds_read_b64_tr_b16 v[46:47], v178 offset:18432\n\t" \
    "ds_read_b64_tr_b16 v[124:125], v179 offset:16384\n\t" \
    "ds_read_b64_tr_b16 v[126:127], v179 offset:18432\n\t" \
    "v_add_u32 v178, 0x8000, v177\n\t" \
    "v_xor_b32 v179, 32, v178\n\t" \
    "ds_read_b64_tr_b16 v[48:49], v178 offset:0\n\t" \
    "ds_read_b64_tr_b16 v[50:51], v178 offset:2048\n\t" \
    "ds_read_b64_tr_b16 v[128:129], v179 offset:0\n\t" \
    "ds_read_b64_tr_b16 v[130:131], v179 offset:2048\n\t" \
    "ds_read_b64_tr_b16 v[52:53], v178 offset:16384\n\t" \
    "ds_read_b64_tr_b16 v[54:55], v178 offset:18432\n\t" \
    "ds_read_b64_tr_b16 v[132:133], v179 offset:16384\n\t" \
    "ds_read_b64_tr_b16 v[134:135], v179 offset:18432\n\t" \
    "v_add_u32 v178, 0x10000, v177\n\t" \
    "v_xor_b32 v179, 32, v178\n\t" \
    "ds_read_b64_tr_b16 v[56:57], v178 offset:0\n\t" \
    "ds_read_b64_tr_b16 v[58:59], v178 offset:2048\n\t" \
    "ds_read_b64_tr_b16 v[136:137], v179 offset:0\n\t" \
    "ds_read_b64_tr_b16 v[138:139], v179 offset:2048\n\t" \
    "ds_read_b64_tr_b16 v[60:61], v178 offset:16384\n\t" \
    "ds_read_b64_tr_b16 v[62:63], v178 offset:18432\n\t" \
    "ds_read_b64_tr_b16 v[140:141], v179 offset:16384\n\t" \
    "ds_read_b64_tr_b16 v[142:143], v179 offset:18432\n\t" \
    "v_add_u32 v178, 0x18000, v177\n\t" \
    "v_xor_b32 v179, 32, v178\n\t" \
    "ds_read_b64_tr_b16 v[64:65], v178 offset:0\n\t" \
    "ds_read_b64_tr_b16 v[66:67], v178 offset:2048\n\t" \
    "ds_read_b64_tr_b16 v[144:145], v179 offset:0\n\t" \
    "ds_read_b64_tr_b16 v[146:147], v179 offset:2048\n\t" \
    "ds_read_b64_tr_b16 v[68:69], v178 offset:16384\n\t" \
    "ds_read_b64_tr_b16 v[70:71], v178 offset:18432\n\t" \
    "ds_read_b64_tr_b16 v[148:149], v179 offset:16384\n\t" \
    "ds_read_b64_tr_b16 v[150:151], v179 offset:18432\n\t" \
    "v_add_u32 v178, 0x20000, v177\n\t" \
    "v_xor_b32 v179, 32, v178\n\t" \
    "ds_read_b64_tr_b16 v[72:73], v178 offset:0\n\t" \
    "ds_read_b64_tr_b16 v[74:75], v178 offset:2048\n\t" \
    "ds_read_b64_tr_b16 v[152:153], v179 offset:0\n\t" \
    "ds_read_b64_tr_b16 v[154:155], v179 offset:2048\n\t" \
    "ds_read_b64_tr_b16 v[76:77], v178 offset:16384\n\t" \
    "ds_read_b64_tr_b16 v[78:79], v178 offset:18432\n\t" \
    "ds_read_b64_tr_b16 v[156:157], v179 offset:16384\n\t" \
    "ds_read_b64_tr_b16 v[158:159], v179 offset:18432\n\t" \
    "v_mov_b32 v228, 0x3f803f80\n\t" \
    "v_mov_b32 v229, 0x3f803f80\n\t" \
    "v_mov_b32 v230, 0x3f803f80\n\t" \
    "v_mov_b32 v231, 0x3f803f80\n\t" \
    "v_mov_b64 v[216:217], 0\n\t" \
    "v_mov_b32 v219, 0\n\t" \
    "v_bfe_u32 v255, v253, 2, 5\n\t" \
    "v_add_u32 v255, 0x7000, v255\n\t" \
    "v_bfe_u32 v218, v253, 6, 1\n\t" \
    "s_waitcnt lgkmcnt(0)\n\t" \
    "s_barrier \n\t" \
    "ds_write_b32 v251, v228 offset:72\n\t" \
    "ds_write_b32 v251, v228 offset:2120\n\t" \
    "v_mfma_f32_16x16x32_bf16 v[160:163], v[0:3], v[228:231], 0\n\t" \
    "v_mfma_f32_16x16x32_bf16 v[164:167], v[80:83], v[228:231], 0\n\t" \
    "v_mfma_f32_16x16x32_bf16 v[160:163], v[4:7], v[228:231], v[160:163]\n\t" \
    "v_mfma_f32_16x16x32_bf16 v[164:167], v[84:87], v[228:231], v[164:167]\n\t" \
    "v_mfma_f32_16x16x32_bf16 v[160:163], v[8:11], v[228:231], v[160:163]\n\t" \
    "v_mfma_f32_16x16x32_bf16 v[164:167], v[88:91], v[228:231], v[164:167]\n\t" \
    "v_mfma_f32_16x16x32_bf16 v[160:163], v[12:15], v[228:231], v[160:163]\n\t" \
    "v_mfma_f32_16x16x32_bf16 v[164:167], v[92:95], v[228:231], v[164:167]\n\t" \
    "v_mfma_f32_16x16x32_bf16 v[160:163], v[16:19], v[228:231], v[160:163]\n\t" \
    "v_mfma_f32_16x16x32_bf16 v[164:167], v[96:99], v[228:231], v[164:167]\n\t" \
    "v_mfma_f32_16x16x32_bf16 v[160:163], v[20:23], v[228:231], v[160:163]\n\t" \
    "v_mfma_f32_16x16x32_bf16 v[164:167], v[100:103], v[228:231], v[164:167]\n\t" \
    "v_mfma_f32_16x16x32_bf16 v[160:163], v[24:27], v[228:231], v[160:163]\n\t" \
    "v_mfma_f32_16x16x32_bf16 v[164:167], v[104:107], v[228:231], v[164:167]\n\t" \
    "v_mfma_f32_16x16x32_bf16 v[160:163], v[28:31], v[228:231], v[160:163]\n\t" \
    "v_mfma_f32_16x16x32_bf16 v[164:167], v[108:111], v[228:231], v[164:167]\n\t" \
    "v_mfma_f32_16x16x32_bf16 v[160:163], v[32:35], v[228:231], v[160:163]\n\t" \
    "v_mfma_f32_16x16x32_bf16 v[164:167], v[112:115], v[228:231], v[164:167]\n\t" \
    "v_mfma_f32_16x16x32_bf16 v[160:163], v[36:39], v[228:231], v[160:163]\n\t" \
    "v_mfma_f32_16x16x32_bf16 v[164:167], v[116:119], v[228:231], v[164:167]\n\t" \
    "v_mfma_f32_16x16x32_bf16 v[160:163], v[40:43], v[228:231], v[160:163]\n\t" \
    "v_mfma_f32_16x16x32_bf16 v[164:167], v[120:123], v[228:231], v[164:167]\n\t" \
    "v_mfma_f32_16x16x32_bf16 v[160:163], v[44:47], v[228:231], v[160:163]\n\t" \
    "v_mfma_f32_16x16x32_bf16 v[164:167], v[124:127], v[228:231], v[164:167]\n\t" \
    "v_mfma_f32_16x16x32_bf16 v[160:163], v[48:51], v[228:231], v[160:163]\n\t" \
    "v_mfma_f32_16x16x32_bf16 v[164:167], v[128:131], v[228:231], v[164:167]\n\t" \
    "v_mfma_f32_16x16x32_bf16 v[160:163], v[52:55], v[228:231], v[160:163]\n\t" \
    "v_mfma_f32_16x16x32_bf16 v[164:167], v[132:135], v[228:231], v[164:167]\n\t" \
    "v_mfma_f32_16x16x32_bf16 v[160:163], v[56:59], v[228:231], v[160:163]\n\t" \
    "v_mfma_f32_16x16x32_bf16 v[164:167], v[136:139], v[228:231], v[164:167]\n\t" \
    "v_mfma_f32_16x16x32_bf16 v[160:163], v[60:63], v[228:231], v[160:163]\n\t" \
    "v_mfma_f32_16x16x32_bf16 v[164:167], v[140:143], v[228:231], v[164:167]\n\t" \
    "v_mfma_f32_16x16x32_bf16 v[160:163], v[64:67], v[228:231], v[160:163]\n\t" \
    "v_mfma_f32_16x16x32_bf16 v[164:167], v[144:147], v[228:231], v[164:167]\n\t" \
    "v_mfma_f32_16x16x32_bf16 v[160:163], v[68:71], v[228:231], v[160:163]\n\t" \
    "v_mfma_f32_16x16x32_bf16 v[164:167], v[148:151], v[228:231], v[164:167]\n\t" \
    "v_mfma_f32_16x16x32_bf16 v[160:163], v[72:75], v[228:231], v[160:163]\n\t" \
    "v_mfma_f32_16x16x32_bf16 v[164:167], v[152:155], v[228:231], v[164:167]\n\t" \
    "v_mfma_f32_16x16x32_bf16 v[160:163], v[76:79], v[228:231], v[160:163]\n\t" \
    "v_mfma_f32_16x16x32_bf16 v[164:167], v[156:159], v[228:231], v[164:167]\n\t" \
    "s_mov_b32 s52, 0x22222222\n\t" \
    "s_mov_b32 s53, 0x22222222\n\t" \
    "s_mov_b32 s54, 0x44444444\n\t" \
    "s_mov_b32 s55, 0x44444444\n\t" \
    "s_mov_b32 s56, 0x88888888\n\t" \
    "s_mov_b32 s57, 0x88888888\n\t" \
    "ds_write_b32 v255, v218\n\t" \
    "s_waitcnt lgkmcnt(0)\n\t" \
    "s_barrier \n\t" \
    ".Lw31_%=:\n\t" \
    "v_mov_b32 v248, 0\n\t" \
    "buffer_load_dword v248, v248, %[rs], %[go] offen sc1\n\t" \
    "s_waitcnt vmcnt(0)\n\t" \
    "v_cmp_ne_u32 vcc, 0, v248\n\t" \
    "s_cbranch_vccnz .Lw32_%=\n\t" \
    "s_cmp_eq_u32 s41, 0\n\t" \
    "s_cbranch_scc1 .Lw32_%=\n\t" \
    "s_sub_u32 s41, s41, 1\n\t" \
    "s_sleep 16\n\t" \
    "s_branch .Lw31_%=\n\t" \
    ".Lw32_%=:\n\t" \
    "s_mov_b32 s43, 0\n\t" \
    "s_add_u32 s46, s43, %[pi]\n\t" \
    "s_cmp_gt_u32 %[wave], 3\n\t" \
    "s_cbranch_scc1 .Lw33_%=\n\t" \
    "s_add_u32 s47, s46, 0x0\n\t" \
    "buffer_load_dwordx4 v[208:211], v253, %[rs], s47 offen sc1\n\t" \
    "s_add_u32 s47, s46, 0x4000\n\t" \
    "buffer_load_dwordx4 v[212:215], v253, %[rs], s47 offen sc1\n\t" \
    "s_add_u32 s47, s46, 0x8000\n\t" \
    "buffer_load_dwordx4 v[216:219], v253, %[rs], s47 offen sc1\n\t" \
    "s_add_u32 s47, s46, 0xc000\n\t" \
    "buffer_load_dwordx4 v[220:223], v253, %[rs], s47 offen sc1\n\t" \
    "s_add_u32 s47, s46, 0x10000\n\t" \
    "buffer_load_dwordx4 v[224:227], v253, %[rs], s47 offen sc1\n\t" \
    ".Lw33_%=:\n\t" \
    "s_waitcnt vmcnt(0)\n\t" \
    ".Lw34_%=:\n\t" \
    "s_add_i32 s44, s40, 0\n\t" \
    "s_max_i32 s44, s44, 0\n\t" \
    "s_lshr_b32 s45, s44, 1\n\t" \
    "s_and_b32 s44, s44, 1\n\t" \
    "s_lshl_b32 s44, s44, 12\n\t" \
    "s_mul_i32 s49, s45, 0x30000\n\t" \
    "s_add_u32 s49, s49, s44\n\t" \
    "s_lshr_b32 s45, s45, 3\n\t" \
    "s_mul_i32 s45, s45, 0x82000\n\t" \
    "s_add_u32 s49, s49, s45\n\t" \
    "s_add_u32 s49, s49, %[ng]\n\t" \
    "s_add_i32 s44, s40, 1\n\t" \
    "s_max_i32 s44, s44, 0\n\t" \
    "s_lshr_b32 s45, s44, 1\n\t" \
    "s_and_b32 s44, s44, 1\n\t" \
    "s_lshl_b32 s44, s44, 12\n\t" \
    "s_mul_i32 s43, s45, 0x14000\n\t" \
    "s_add_u32 s43, s43, s44\n\t" \
    "s_lshr_b32 s45, s45, 4\n\t" \
    "s_mul_i32 s45, s45, 0xc2000\n\t" \
    "s_add_u32 s43, s43, s45\n\t" \
    "s_cmp_gt_u32 %[wave], 3\n\t" \
    "s_cbranch_scc1 .Lw37_%=\n\t" \
    "s_setprio 2\n\t" \
    "v_max_u32 v248, v208, v209\n\t" \
    "v_max3_u32 v248, v210, v211, v248\n\t" \
    "v_max3_u32 v248, v212, v213, v248\n\t" \
    "v_max3_u32 v248, v214, v215, v248\n\t" \
    "v_max3_u32 v248, v216, v217, v248\n\t" \
    "v_max3_u32 v248, v218, v219, v248\n\t" \
    "v_max3_u32 v248, v220, v221, v248\n\t" \
    "v_max3_u32 v248, v222, v223, v248\n\t" \
    "v_max3_u32 v248, v224, v225, v248\n\t" \
    "v_max3_u32 v248, v226, v227, v248\n\t" \
    "v_cmp_eq_u32 vcc, -1, v248\n\t" \
    "s_cbranch_vccz .Lw36_%=\n\t" \
    ".Lw35_%=:\n\t" \
    "v_max_u32 v248, v208, v209\n\t" \
    "v_max3_u32 v248, v210, v211, v248\n\t" \
    "v_cmp_eq_u32 vcc, -1, v248\n\t" \
    "s_cbranch_vccz .Lw38_%=\n\t" \
    "s_add_u32 s47, s46, 0x0\n\t" \
    "buffer_load_dwordx4 v[208:211], v253, %[rs], s47 offen sc1\n\t" \
    ".Lw38_%=:\n\t" \
    "v_max_u32 v248, v212, v213\n\t" \
    "v_max3_u32 v248, v214, v215, v248\n\t" \
    "v_cmp_eq_u32 vcc, -1, v248\n\t" \
    "s_cbranch_vccz .Lw39_%=\n\t" \
    "s_add_u32 s47, s46, 0x4000\n\t" \
    "buffer_load_dwordx4 v[212:215], v253, %[rs], s47 offen sc1\n\t" \
    ".Lw39_%=:\n\t" \
    "v_max_u32 v248, v216, v217\n\t" \
    "v_max3_u32 v248, v218, v219, v248\n\t" \
    "v_cmp_eq_u32 vcc, -1, v248\n\t" \
    "s_cbranch_vccz .Lw310_%=\n\t" \
    "s_add_u32 s47, s46, 0x8000\n\t" \
    "buffer_load_dwordx4 v[216:219], v253, %[rs], s47 offen sc1\n\t" \
    ".Lw310_%=:\n\t" \
    "v_max_u32 v248, v220, v221\n\t" \
    "v_max3_u32 v248, v222, v223, v248\n\t" \
    "v_cmp_eq_u32 vcc, -1, v248\n\t" \
    "s_cbranch_vccz .Lw311_%=\n\t" \
    "s_add_u32 s47, s46, 0xc000\n\t" \
    "buffer_load_dwordx4 v[220:223], v253, %[rs], s47 offen sc1\n\t" \
    ".Lw311_%=:\n\t" \
    "v_max_u32 v248, v224, v225\n\t" \
    "v_max3_u32 v248, v226, v227, v248\n\t" \
    "v_cmp_eq_u32 vcc, -1, v248\n\t" \
    "s_cbranch_vccz .Lw312_%=\n\t" \
    "s_add_u32 s47, s46, 0x10000\n\t" \
    "buffer_load_dwordx4 v[224:227], v253, %[rs], s47 offen sc1\n\t" \
    ".Lw312_%=:\n\t" \
    "s_waitcnt vmcnt(0)\n\t" \
    "v_max_u32 v248, v208, v209\n\t" \
    "v_max3_u32 v248, v210, v211, v248\n\t" \
    "v_max3_u32 v248, v212, v213, v248\n\t" \
    "v_max3_u32 v248, v214, v215, v248\n\t" \
    "v_max3_u32 v248, v216, v217, v248\n\t" \
    "v_max3_u32 v248, v218, v219, v248\n\t" \
    "v_max3_u32 v248, v220, v221, v248\n\t" \
    "v_max3_u32 v248, v222, v223, v248\n\t" \
    "v_max3_u32 v248, v224, v225, v248\n\t" \
    "v_max3_u32 v248, v226, v227, v248\n\t" \
    "v_cmp_eq_u32 vcc, -1, v248\n\t" \
    "s_cbranch_vccz .Lw36_%=\n\t" \
    "s_cmp_eq_u32 s41, 0\n\t" \
    "s_cbranch_scc1 .Lw36_%=\n\t" \
    "s_sub_u32 s41, s41, 1\n\t" \
    "s_branch .Lw35_%=\n\t" \
    ".Lw36_%=:\n\t" \
    "ds_write_b128 v250, v[208:211] offset:0\n\t" \
    "v_mfma_f32_16x16x32_bf16 v[236:239], v[208:211], v[208:211], 0\n\t" \
    "v_mfma_f32_16x16x32_bf16 v[232:235], v[228:231], v[208:211], 0\n\t" \
    "ds_write_b128 v250, v[212:215] offset:4096\n\t" \
    "v_mfma_f32_16x16x32_bf16 v[236:239], v[212:215], v[212:215], v[236:239]\n\t" \
    "v_mfma_f32_16x16x32_bf16 v[232:235], v[228:231], v[212:215], v[232:235]\n\t" \
    "ds_write_b128 v250, v[216:219] offset:8192\n\t" \
    "v_mfma_f32_16x16x32_bf16 v[236:239], v[216:219], v[216:219], v[236:239]\n\t" \
    "v_mfma_f32_16x16x32_bf16 v[232:235], v[228:231], v[216:219], v[232:235]\n\t" \
    "ds_write_b128 v250, v[220:223] offset:12288\n\t" \
    "v_mfma_f32_16x16x32_bf16 v[236:239], v[220:223], v[220:223], v[236:239]\n\t" \
    "v_mfma_f32_16x16x32_bf16 v[232:235], v[228:231], v[220:223], v[232:235]\n\t" \
    "ds_write_b128 v250, v[224:227] offset:16384\n\t" \
    "v_mfma_f32_16x16x32_bf16 v[236:239], v[224:227], v[224:227], v[236:239]\n\t" \
    "v_mfma_f32_16x16x32_bf16 v[232:235], v[228:231], v[224:227], v[232:235]\n\t" \
    "s_add_u32 s46, s43, %[pi]\n\t" \
    "s_nop 4\n\t" \
    "v_cndmask_b32 v236, v236, v237, s[52:53]\n\t" \
    "v_cndmask_b32 v236, v236, v238, s[54:55]\n\t" \
    "v_cndmask_b32 v236, v236, v239, s[56:57]\n\t" \
    "s_mov_b32 exec_lo, 0xf0000f\n\t" \
    "s_mov_b32 exec_hi, 0xf0000f00\n\t" \
    "ds_write2_b32 v252, v232, v236 offset1:1\n\t" \
    "s_mov_b64 exec, -1\n\t" \
    "s_add_u32 s44, s40, 1\n\t" \
    "v_mov_b32 v248, s44\n\t" \
    "s_lshl_b64 exec, 1, %[wave]\n\t" \
    "ds_write_b32 v255, v248\n\t" \
    "s_mov_b64 exec, -1\n\t" \
    ".Lw37_%=:\n\t" \
    "ds_read_b32 v248, v255\n\t" \
    "s_waitcnt lgkmcnt(0)\n\t" \
    "v_cmp_ge_u32 vcc, s40, v248\n\t" \
    "s_cbranch_vccz .Lw313_%=\n\t" \
    "s_cmp_lt_u32 %[wave], 4\n\t" \
    "s_cbranch_scc1 .Lw37_%=\n\t" \
    "s_sleep 1\n\t" \
    "s_branch .Lw37_%=\n\t" \
    ".Lw313_%=:\n\t" \
    "s_setprio 0\n\t" \
    "s_cmp_lg_u32 %[wave], 4\n\t" \
    "s_cbranch_scc1 .Lw314_%=\n\t" \
    "s_setprio 1\n\t" \
    "ds_read_b128 v[232:235], v251 offset:0\n\t" \
    "ds_read_b128 v[236:239], v251 offset:16\n\t" \
    "s_waitcnt lgkmcnt(0)\n\t" \
    "v_add_f32 v232, v232, v236\n\t" \
    "v_add_f32 v233, v233, v237\n\t" \
    "v_add_f32 v234, v234, v238\n\t" \
    "v_add_f32 v235, v235, v239\n\t" \
    "v_add_f32 v232, v232, v234\n\t" \
    "v_add_f32 v233, v233, v235\n\t" \
    "v_mul_f32 v232, 0x3acccccd, v232\n\t" \
    "v_mul_f32 v233, 0x3acccccd, v233\n\t" \
    "v_fma_f32 v233, -v232, v232, v233\n\t" \
    "v_mov_b32 v234, v232\n\t" \
    "v_max_f32 v233, 0, v233\n\t" \
    "v_cvt_pk_bf16_f32 v234, v234, v234\n\t" \
    "v_and_b32 v234, 0xffff0000, v234\n\t" \
    "v_mov_b32 v236, v233\n\t" \
    "v_cvt_pk_bf16_f32 v236, v236, v236\n\t" \
    "v_and_b32 v236, 0xffff0000, v236\n\t" \
    "v_add_f32 v236, 0x3727c5ac, v236\n\t" \
    "v_cvt_pk_bf16_f32 v236, v236, v236\n\t" \
    "v_and_b32 v236, 0xffff0000, v236\n\t" \
    "v_sqrt_f32 v236, v236\n\t" \
    "s_nop 0\n\t" \
    "v_cvt_pk_bf16_f32 v236, v236, v236\n\t" \
    "v_and_b32 v236, 0xffff0000, v236\n\t" \
    "v_rcp_f32 v235, v236\n\t" \
    "s_nop 0\n\t" \
    "v_mov_b32 v237, v235\n\t" \
    "v_mul_f32 v238, -v234, v235\n\t" \
    "v_mov_b32 v239, s40\n\t" \
    "ds_write2_b32 v251, v237, v238 offset0:16 offset1:17\n\t" \
    "ds_write_b32 v251, v239 offset:72\n\t" \
    "s_setprio 0\n\t" \
    ".Lw314_%=:\n\t" \
    "ds_read_b128 v[176:179], v249 offset:0\n\t" \
    "ds_read_b128 v[180:183], v249 offset:1024\n\t" \
    "ds_read_b128 v[184:187], v249 offset:2048\n\t" \
    "ds_read_b128 v[188:191], v249 offset:3072\n\t" \
    "ds_read_b128 v[192:195], v249 offset:4096\n\t" \
    "ds_read_b128 v[196:199], v249 offset:5120\n\t" \
    "ds_read_b128 v[200:203], v249 offset:6144\n\t" \
    "s_waitcnt lgkmcnt(6)\n\t" \
    "v_mfma_f32_16x16x32_bf16 v[168:171], v[0:3], v[176:179], 0\n\t" \
    "ds_read_b128 v[204:207], v249 offset:7168\n\t" \
    "v_mfma_f32_16x16x32_bf16 v[172:175], v[80:83], v[176:179], 0\n\t" \
    "s_waitcnt lgkmcnt(6)\n\t" \
    "v_mfma_f32_16x16x32_bf16 v[168:171], v[4:7], v[180:183], v[168:171]\n\t" \
    "ds_read_b128 v[176:179], v249 offset:8192\n\t" \
    "v_mfma_f32_16x16x32_bf16 v[172:175], v[84:87], v[180:183], v[172:175]\n\t" \
    "s_waitcnt lgkmcnt(6)\n\t" \
    "v_mfma_f32_16x16x32_bf16 v[168:171], v[8:11], v[184:187], v[168:171]\n\t" \
    "ds_read_b128 v[180:183], v249 offset:9216\n\t" \
    "v_mfma_f32_16x16x32_bf16 v[172:175], v[88:91], v[184:187], v[172:175]\n\t" \
    "s_waitcnt lgkmcnt(6)\n\t" \
    "v_mfma_f32_16x16x32_bf16 v[168:171], v[12:15], v[188:191], v[168:171]\n\t" \
    "ds_read_b128 v[184:187], v249 offset:10240\n\t" \
    "v_mfma_f32_16x16x32_bf16 v[172:175], v[92:95], v[188:191], v[172:175]\n\t" \
    "s_waitcnt lgkmcnt(6)\n\t" \
    "v_mfma_f32_16x16x32_bf16 v[168:171], v[16:19], v[192:195], v[168:171]\n\t" \
    "ds_read_b128 v[188:191], v249 offset:11264\n\t" \
    "v_mfma_f32_16x16x32_bf16 v[172:175], v[96:99], v[192:195], v[172:175]\n\t" \
    "s_waitcnt lgkmcnt(6)\n\t" \
    "v_mfma_f32_16x16x32_bf16 v[168:171], v[20:23], v[196:199], v[168:171]\n\t" \
    "ds_read_b128 v[192:195], v249 offset:12288\n\t" \
    "v_mfma_f32_16x16x32_bf16 v[172:175], v[100:103], v[196:199], v[172:175]\n\t" \
    "s_waitcnt lgkmcnt(6)\n\t" \
    "v_mfma_f32_16x16x32_bf16 v[168:171], v[24:27], v[200:203], v[168:171]\n\t" \
    "ds_read_b128 v[196:199], v249 offset:13312\n\t" \
    "v_mfma_f32_16x16x32_bf16 v[172:175], v[104:107], v[200:203], v[172:175]\n\t" \
    "s_waitcnt lgkmcnt(6)\n\t" \
    "v_mfma_f32_16x16x32_bf16 v[168:171], v[28:31], v[204:207], v[168:171]\n\t" \
    "ds_read_b128 v[200:203], v249 offset:14336\n\t" \
    "v_mfma_f32_16x16x32_bf16 v[172:175], v[108:111], v[204:207], v[172:175]\n\t" \
    "s_waitcnt lgkmcnt(6)\n\t" \
    "v_mfma_f32_16x16x32_bf16 v[168:171], v[32:35], v[176:179], v[168:171]\n\t" \
    "ds_read_b128 v[204:207], v249 offset:15360\n\t" \
    "v_mfma_f32_16x16x32_bf16 v[172:175], v[112:115], v[176:179], v[172:175]\n\t" \
    "s_waitcnt lgkmcnt(6)\n\t" \
    "v_mfma_f32_16x16x32_bf16 v[168:171], v[36:39], v[180:183], v[168:171]\n\t" \
    "ds_read_b128 v[176:179], v249 offset:16384\n\t" \
    "v_mfma_f32_16x16x32_bf16 v[172:175], v[116:119], v[180:183], v[172:175]\n\t" \
    "s_waitcnt lgkmcnt(6)\n\t" \
    "v_mfma_f32_16x16x32_bf16 v[168:171], v[40:43], v[184:187], v[168:171]\n\t" \
    "ds_read_b128 v[180:183], v249 offset:17408\n\t" \
    "v_mfma_f32_16x16x32_bf16 v[172:175], v[120:123], v[184:187], v[172:175]\n\t" \
    "s_waitcnt lgkmcnt(6)\n\t" \
    "v_mfma_f32_16x16x32_bf16 v[168:171], v[44:47], v[188:191], v[168:171]\n\t" \
    "ds_read_b128 v[184:187], v249 offset:18432\n\t" \
    "v_mfma_f32_16x16x32_bf16 v[172:175], v[124:127], v[188:191], v[172:175]\n\t" \
    "s_waitcnt lgkmcnt(6)\n\t" \
    "v_mfma_f32_16x16x32_bf16 v[168:171], v[48:51], v[192:195], v[168:171]\n\t" \
    "ds_read_b128 v[188:191], v249 offset:19456\n\t" \
    "v_mfma_f32_16x16x32_bf16 v[172:175], v[128:131], v[192:195], v[172:175]\n\t" \
    "s_waitcnt lgkmcnt(6)\n\t" \
    "v_mfma_f32_16x16x32_bf16 v[168:171], v[52:55], v[196:199], v[168:171]\n\t" \
    "v_mfma_f32_16x16x32_bf16 v[172:175], v[132:135], v[196:199], v[172:175]\n\t" \
    "s_waitcnt lgkmcnt(5)\n\t" \
    "v_mfma_f32_16x16x32_bf16 v[168:171], v[56:59], v[200:203], v[168:171]\n\t" \
    "v_mfma_f32_16x16x32_bf16 v[172:175], v[136:139], v[200:203], v[172:175]\n\t" \
    "s_cmp_gt_u32 %[wave], 3\n\t" \
    "s_cbranch_scc1 .Lw316_%=\n\t" \
    "s_add_u32 s47, s46, 0x0\n\t" \
    "buffer_load_dwordx4 v[208:211], v253, %[rs], s47 offen sc1\n\t" \
    "s_add_u32 s47, s46, 0x4000\n\t" \
    "buffer_load_dwordx4 v[212:215], v253, %[rs], s47 offen sc1\n\t" \
    "s_add_u32 s47, s46, 0x8000\n\t" \
    "buffer_load_dwordx4 v[216:219], v253, %[rs], s47 offen sc1\n\t" \
    "s_add_u32 s47, s46, 0xc000\n\t" \
    "buffer_load_dwordx4 v[220:223], v253, %[rs], s47 offen sc1\n\t" \
    "s_add_u32 s47, s46, 0x10000\n\t" \
    "buffer_load_dwordx4 v[224:227], v253, %[rs], s47 offen sc1\n\t" \
    ".Lw316_%=:\n\t" \
    "s_waitcnt lgkmcnt(4)\n\t" \
    "v_mfma_f32_16x16x32_bf16 v[168:171], v[60:63], v[204:207], v[168:171]\n\t" \
    "v_mfma_f32_16x16x32_bf16 v[172:175], v[140:143], v[204:207], v[172:175]\n\t" \
    "ds_read_b32 v236, v251 offset:72\n\t" \
    "ds_read_b64 v[238:239], v251 offset:64\n\t" \
    "s_waitcnt lgkmcnt(5)\n\t" \
    "v_mfma_f32_16x16x32_bf16 v[168:171], v[64:67], v[176:179], v[168:171]\n\t" \
    "v_mfma_f32_16x16x32_bf16 v[172:175], v[144:147], v[176:179], v[172:175]\n\t" \
    "s_waitcnt lgkmcnt(4)\n\t" \
    "v_mfma_f32_16x16x32_bf16 v[168:171], v[68:71], v[180:183], v[168:171]\n\t" \
    "v_mfma_f32_16x16x32_bf16 v[172:175], v[148:151], v[180:183], v[172:175]\n\t" \
    ".Lw317_%=:\n\t" \
    "s_waitcnt lgkmcnt(0)\n\t" \
    "v_cmp_ne_u32 vcc, s40, v236\n\t" \
    "s_cbranch_vccz .Lw318_%=\n\t" \
    "ds_read_b32 v236, v251 offset:72\n\t" \
    "ds_read_b64 v[238:239], v251 offset:64\n\t" \
    "s_branch .Lw317_%=\n\t" \
    ".Lw318_%=:\n\t" \
    "s_waitcnt lgkmcnt(0)\n\t" \
    "v_mul_f32 v240, v160, v239\n\t" \
    "v_mul_f32 v241, v161, v239\n\t" \
    "v_mfma_f32_16x16x32_bf16 v[168:171], v[72:75], v[184:187], v[168:171]\n\t" \
    "v_mul_f32 v242, v162, v239\n\t" \
    "v_mul_f32 v243, v163, v239\n\t" \
    "v_mfma_f32_16x16x32_bf16 v[172:175], v[152:155], v[184:187], v[172:175]\n\t" \
    "v_mul_f32 v244, v164, v239\n\t" \
    "v_mul_f32 v245, v165, v239\n\t" \
    "v_mfma_f32_16x16x32_bf16 v[168:171], v[76:79], v[188:191], v[168:171]\n\t" \
    "v_mul_f32 v246, v166, v239\n\t" \
    "v_mul_f32 v247, v167, v239\n\t" \
    "v_mfma_f32_16x16x32_bf16 v[172:175], v[156:159], v[188:191], v[172:175]\n\t" \
    "v_mov_b32 v237, 0xc0135761\n\t" \
    "s_cmp_lt_u32 %[wave], 4\n\t" \
    "s_cbranch_scc1 .Lw319_%=\n\t" \
    "s_add_u32 s44, s40, 2\n\t" \
    "v_mov_b32 v248, s44\n\t" \
    "s_lshl_b64 exec, 1, %[wave]\n\t" \
    "ds_write_b32 v255, v248\n\t" \
    "s_mov_b64 exec, -1\n\t" \
    ".Lw319_%=:\n\t" \
    "s_nop 1\n\t" \
    "s_nop 2\n\t" \
    "v_fma_f32 v168, v168, v238, v240\n\t" \
    "v_fma_f32 v169, v169, v238, v241\n\t" \
    "v_fma_f32 v170, v170, v238, v242\n\t" \
    "v_fma_f32 v171, v171, v238, v243\n\t" \
    "v_fma_f32 v172, v172, v238, v244\n\t" \
    "v_fma_f32 v173, v173, v238, v245\n\t" \
    "v_fma_f32 v174, v174, v238, v246\n\t" \
    "v_fma_f32 v175, v175, v238, v247\n\t" \
    "v_mul_f32 v176, v168, v168\n\t" \
    "v_mul_f32 v177, v169, v169\n\t" \
    "v_mul_f32 v178, v170, v170\n\t" \
    "v_mul_f32 v179, v171, v171\n\t" \
    "v_mul_f32 v180, v172, v172\n\t" \
    "v_mul_f32 v181, v173, v173\n\t" \
    "v_mul_f32 v182, v174, v174\n\t" \
    "v_mul_f32 v183, v175, v175\n\t" \
    "v_fmamk_f32 v176, v176, 0xbdd2d3e7, v237\n\t" \
    "v_fmamk_f32 v177, v177, 0xbdd2d3e7, v237\n\t" \
    "v_fmamk_f32 v178, v178, 0xbdd2d3e7, v237\n\t" \
    "v_fmamk_f32 v179, v179, 0xbdd2d3e7, v237\n\t" \
    "v_fmamk_f32 v180, v180, 0xbdd2d3e7, v237\n\t" \
    "v_fmamk_f32 v181, v181, 0xbdd2d3e7, v237\n\t" \
    "v_fmamk_f32 v182, v182, 0xbdd2d3e7, v237\n\t" \
    "v_fmamk_f32 v183, v183, 0xbdd2d3e7, v237\n\t" \
    "v_mul_f32 v176, v168, v176\n\t" \
    "v_mul_f32 v177, v169, v177\n\t" \
    "v_mul_f32 v178, v170, v178\n\t" \
    "v_mul_f32 v179, v171, v179\n\t" \
    "v_mul_f32 v180, v172, v180\n\t" \
    "v_mul_f32 v181, v173, v181\n\t" \
    "v_mul_f32 v182, v174, v182\n\t" \
    "v_mul_f32 v183, v175, v183\n\t" \
    "v_exp_f32 v176, v176\n\t" \
    "v_exp_f32 v177, v177\n\t" \
    "v_exp_f32 v178, v178\n\t" \
    "v_exp_f32 v179, v179\n\t" \
    "v_exp_f32 v180, v180\n\t" \
    "v_exp_f32 v181, v181\n\t" \
    "v_exp_f32 v182, v182\n\t" \
    "v_exp_f32 v183, v183\n\t" \
    "v_add_f32 v176, 1.0, v176\n\t" \
    "v_add_f32 v177, 1.0, v177\n\t" \
    "v_add_f32 v178, 1.0, v178\n\t" \
    "v_add_f32 v179, 1.0, v179\n\t" \
    "v_add_f32 v180, 1.0, v180\n\t" \
    "v_add_f32 v181, 1.0, v181\n\t" \
    "v_add_f32 v182, 1.0, v182\n\t" \
    "v_add_f32 v183, 1.0, v183\n\t" \
    "v_rcp_f32 v176, v176\n\t" \
    "v_rcp_f32 v177, v177\n\t" \
    "v_rcp_f32 v178, v178\n\t" \
    "v_rcp_f32 v179, v179\n\t" \
    "v_rcp_f32 v180, v180\n\t" \
    "v_rcp_f32 v181, v181\n\t" \
    "v_rcp_f32 v182, v182\n\t" \
    "v_rcp_f32 v183, v183\n\t" \
    "v_mul_f32 v168, v168, v176\n\t" \
    "v_mul_f32 v169, v169, v177\n\t" \
    "v_mul_f32 v170, v170, v178\n\t" \
    "v_mul_f32 v171, v171, v179\n\t" \
    "v_mul_f32 v172, v172, v180\n\t" \
    "v_mul_f32 v173, v173, v181\n\t" \
    "v_mul_f32 v174, v174, v182\n\t" \
    "v_mul_f32 v175, v175, v183\n\t" \
    "s_waitcnt vmcnt(0)\n\t" \
    "v_cvt_pk_bf16_f32 v184, v168, v169\n\t" \
    "v_cvt_pk_bf16_f32 v185, v170, v171\n\t" \
    "v_cvt_pk_bf16_f32 v186, v172, v173\n\t" \
    "v_cvt_pk_bf16_f32 v187, v174, v175\n\t" \
    "v_min_u32 v184, -2, v184\n\t" \
    "v_min_u32 v185, -2, v185\n\t" \
    "v_min_u32 v186, -2, v186\n\t" \
    "v_min_u32 v187, -2, v187\n\t" \
    "s_nop 0\n\t" \
    "v_permlane16_swap_b32 v184, v186\n\t" \
    "v_permlane16_swap_b32 v185, v187\n\t" \
    "buffer_store_dwordx4 v[184:187], v254, %[rs], s49 offen sc1\n\t" \
    "s_cmp_ge_u32 s40, 122\n\t" \
    "s_cbranch_scc1 .Lw320_%=\n\t" \
    "s_add_i32 s44, s40, 6\n\t" \
    "s_max_i32 s44, s44, 0\n\t" \
    "s_lshr_b32 s45, s44, 1\n\t" \
    "s_and_b32 s44, s44, 1\n\t" \
    "s_lshl_b32 s44, s44, 12\n\t" \
    "s_mul_i32 s50, s45, 0x30000\n\t" \
    "s_add_u32 s50, s50, s44\n\t" \
    "s_lshr_b32 s45, s45, 3\n\t" \
    "s_mul_i32 s45, s45, 0x82000\n\t" \
    "s_add_u32 s50, s50, s45\n\t" \
    "s_add_u32 s50, s50, %[ng]\n\t" \
    "v_mov_b32 v188, -1\n\t" \
    "v_mov_b32 v189, -1\n\t" \
    "v_mov_b32 v190, -1\n\t" \
    "v_mov_b32 v191, -1\n\t" \
    "buffer_store_dwordx4 v[188:191], v253, %[rs], s50 offen sc1\n\t" \
    ".Lw320_%=:\n\t" \
    "v_xor_b32 v249, 0x8000, v249\n\t" \
    "v_xor_b32 v250, 0x8000, v250\n\t" \
    "v_xor_b32 v251, 0x800, v251\n\t" \
    "v_xor_b32 v252, 0x800, v252\n\t" \
    "s_add_u32 s40, s40, 1\n\t" \
    "s_cmp_lg_u32 s40, 128\n\t" \
    "s_cbranch_scc1 .Lw34_%=\n\t" \
    "s_branch .Lend%=\n\t" \
    ".Lend%=:\n\t" \
    "s_waitcnt vmcnt(0) lgkmcnt(0)\n\t" \
    "s_mov_b32 %[bud], s41\n\t"
using u32x8 = __attribute__((ext_vector_type(8))) unsigned;
using u32x16 = __attribute__((ext_vector_type(16))) unsigned;
using u32x32 = __attribute__((ext_vector_type(32))) unsigned;
__device__ __forceinline__ u32x32 cat32(u32x8 a, u32x8 b, u32x8 c, u32x8 d) {
    const u32x16 lo = __builtin_shufflevector(a, b, 0, 1, 2, 3, 4, 5, 6, 7, 8, 9, 10, 11, 12, 13, 14, 15);
    const u32x16 hi = __builtin_shufflevector(c, d, 0, 1, 2, 3, 4, 5, 6, 7, 8, 9, 10, 11, 12, 13, 14, 15);
    return __builtin_shufflevector(lo, hi, 0, 1, 2, 3, 4, 5, 6, 7, 8, 9, 10, 11, 12, 13, 14, 15, 16, 17, 18, 19, 20, 21, 22, 23, 24, 25, 26, 27, 28, 29, 30, 31);
}
__device__ __forceinline__ unsigned c1_dst(int wave, int kind) {
    const int ln = lane_id(), c = ln & 15, g = ln >> 4, hf = (wave >> 1) & 1;
    if (kind == 2) {
        const unsigned b = L_KM + (c >> 2) * 256 + (wave & 1) * 128 + g * 32;
        return (b + hf * 8 + (c & 3) * 2) | (b + 1024 + hf * 1024 + (c & 3) * 4) << 16;
    }
    const unsigned b = L_Q + (c >> 3) * 256 + g * 64 + ((c >> 2) & 1) * 8;
    return (b + (wave & 1) * (kind ? 512 : 1536) + ((c >> 1) & 1) * 32 + hf * 4 + (c & 1) * 2) | (b + 1024 + (wave & 3) * 512 + (c & 1) * 32 + ((c >> 1) & 1) * 4) << 16;
}
__device__ __forceinline__ void dma1(rsrc_t rs, unsigned vo, unsigned so, unsigned dst) {
    asm volatile("s_mov_b32 m0, %3\n\ts_nop 0\n\tbuffer_load_dwordx4 %0, %1, %2 offen nt lds" :: "v"(vo), "s"(rs), "s"(so), "s"(dst) : "memory");
}
using s16x4 = __attribute__((ext_vector_type(4))) short;
__device__ __forceinline__ u32x4 tr_frag(unsigned a, unsigned step) {
    const u32x2 x = __builtin_bit_cast(u32x2, __builtin_amdgcn_ds_read_tr16_b64_v4i16((LAS s16x4*)(size_t)a)), y = __builtin_bit_cast(u32x2, __builtin_amdgcn_ds_read_tr16_b64_v4i16((LAS s16x4*)(size_t)(a + step)));
    return (u32x4){x[0], x[1], y[0], y[1]};
}
#define LOOP_CLOB "memory", "vcc", "scc", "v0", "v1", "v2", "v3", "v4", "v5", "v6", "v7", "v8", "v9", "v10", "v11", "v12", "v13", "v14", "v15", "v16", "v17", "v18", "v19", "v20", "v21", "v22", "v23", "v24", "v25", "v26", "v27", "v28", "v29", "v30", "v31", "v32", "v33", "v34", "v35", "v36", "v37", "v38", "v39", "v40", "v41", "v42", "v43", "v44", "v45", "v46", "v47", "v48", "v49", "v50", "v51", "v52", "v53", "v54", "v55", "v56", "v57", "v58", "v59", "v60", "v61", "v62", "v63", "v64", "v65", "v66", "v67", "v68", "v69", "v70", "v71", "v72", "v73", "v74", "v75", "v76", "v77", "v78", "v79", "v80", "v81", "v82", "v83", "v84", "v85", "v86", "v87", "v88", "v89", "v90", "v91", "v92", "v93", "v94", "v95", "v96", "v97", "v98", "v99", "v100", "v101", "v102", "v103", "v104", "v105", "v106", "v107", "v108", "v109", "v110", "v111", "v112", "v113", "v114", "v115", "v116", "v117", "v118", "v119", "v120", "v121", "v122", "v123", "v124", "v125", "v126", "v127", "v128", "v129", "v130", "v131", "v132", "v133", "v134", "v135", "v136", "v137", "v138", "v139", "v140", "v141", "v142", "v143", "v144", "v145", "v146", "v147", "v148", "v149", "v150", "v151", "v152", "v153", "v154", "v155", "v156", "v157", "v158", "v159", "v160", "v161", "v162", "v163", "v164", "v165", "v166", "v167", "v168", "v169", "v170", "v171", "v172", "v173", "v174", "v175", "v176", "v177", "v178", "v179", "v180", "v181", "v182", "v183", "v184", "v185", "v186", "v187", "v188", "v189", "v190", "v191", "v192", "v193", "v194", "v195", "v196", "v197", "v198", "v199", "v200", "v201", "v202", "v203", "v204", "v205", "v206", "v207", "v208", "v209", "v210", "v211", "v212", "v213", "v214", "v215", "v216", "v217", "v218", "v219", "v220", "v221", "v222", "v223", "v224", "v225", "v226", "v227", "v228", "v229", "v230", "v231", "v232", "v233", "v234", "v235", "v236", "v237", "v238", "v239", "v246", "v247", "v248", "v249", "v250", "v251", "v252", "s40", "s41", "s42", "s43", "s44", "s45", "s46", "s47", "s48", "s49", "s50", "s51", "s52", "s53", "s54", "s55", "s56", "s57", "s58", "s59", "s60", "s61", "s62", "s63", "s64", "s65", "s66", "s67", "s68", "s69", "s70", "s71", "s72", "s73", "s74", "s75", "s76", "s77", "s78", "s79"
__device__ __forceinline__ unsigned UNI(unsigned x) {
    unsigned r;
    asm volatile("s_nop 0\n\tv_readfirstlane_b32 %0, %1\n\ts_nop 4" : "=s"(r) : "v"(x));
    return r;
}
__device__ __forceinline__ void note_timeout(const Params& P, unsigned bud) {
    if (bud == 0) __builtin_amdgcn_raw_buffer_store_b32(1u, mkrsrc(P.ws + FL0, 512), 0x100, 0, 16);
}

#define LOOP_IO [bud] "=s"(bud), [mb] "+s"(mb), [pb] "+s"(pb), [fl] "+s"(fl), "+{v253}"(l16), "+{v240}"(W.a[0]), "+{v241}"(W.a[1]), "+{v242}"(W.a[2]), "+{v243}"(W.a[3]), "+{v244}"(W.a[4]), "+{v245}"(W.a[5])
#define LOOP_IN "{v254}"(b2), "{v255}"(dsto), [wave] "s"(wave), [ws] "s"(P.ws), [mo] "s"(FL_MB + blockIdx.x * 128 + wave * 4), [sz] "s"(RG_L), [lx] "s"(L << 13), [wr] "s"(wdesc(P, wave, isv, h)), [wm] "s"(UNI(wstage(wave, isv))), \
                [cj] "s"(REGS - 75 * WIN), [cs] "s"(5 * WIN)
struct WLoad { unsigned a[6]; };
__device__ __forceinline__ rsrc_t wdesc(const Params& P, int wave, bool isv, int h) {
    const unsigned wo = (isv && wave >= 4) ? h * DH * DM + (wave & 3) * 160 : h * DH;
    typedef const __attribute__((address_space(4))) unsigned long long* kptr;
    static_assert(__builtin_offsetof(Params, wq) == 24 && __builtin_offsetof(Params, wo) == 48, "argument order");
    const unsigned long long w = ((kptr)__builtin_amdgcn_kernarg_segment_ptr())[3 + (isv ? 2 : 0) + (wave >> 2)];
    return mkrsrc((const bf16_t*)w + wo, (DM * HD - wo) * 2);
}
__device__ __forceinline__ unsigned wstage(int wave, bool isv) { return (isv && wave >= 4) ? L_W + (wave & 3) * 20480 : wave * 3072; }
template <bool START>
__device__ __forceinline__ void wload_start(WLoad& W, const Params& P, int wave, bool isv, int h) {
    const int ln = lane_id(), w4 = wave & 3;
    const rsrc_t wr = wdesc(P, wave, isv, h);
    const unsigned wm = UNI(wstage(wave, isv));
    if (isv && wave >= 4) {
#pragma unroll
        for (int k = 0; k < 5; ++k) { const unsigned u = k * 64 + ln; W.a[k] = (u / 20) * (DM * 2) + (u % 20) * 16 - (k & 3) * 1024; }
#pragma unroll 1
        for (int k = 0; k < (START ? 4 : 0); ++k)
            asm volatile("s_mov_b32 m0, %7\n\ts_nop 0\n\tbuffer_load_dwordx4 %0, %5, %6 offen nt lds\n\tbuffer_load_dwordx4 %1, %5, %6 offen offset:1024 nt lds\n\tbuffer_load_dwordx4 %2, %5, %6 offen offset:2048 nt lds\n\t"
                         "buffer_load_dwordx4 %3, %5, %6 offen offset:3072 nt lds\n\ts_mov_b32 m0, %8\n\ts_nop 0\n\tbuffer_load_dwordx4 %4, %5, %6 offen nt lds"
                         :: "v"(W.a[0]), "v"(W.a[1]), "v"(W.a[2]), "v"(W.a[3]), "v"(W.a[4]), "s"(wr), "s"(k * 20480), "s"(wm + k * 5120), "s"(wm + k * 5120 + 4096) : "memory");
        W.a[5] = L_W + w4 * 20480 + (ln >> 2) * 320 + (ln & 3) * 8;
    } else {
        const unsigned sl = isv ? 12288 : 24576;
#pragma unroll
        for (int k = 0; k < 3; ++k) { const unsigned u = (w4 * 3 + k) * 64 + ln; W.a[k] = (u / 24) * (HD * 2) + (u % 24) * 16 - k * 1024; }
#pragma unroll 1
        for (int k = 0; k < (START ? 5 : 0); ++k)
            asm volatile("s_mov_b32 m0, %5\n\ts_nop 0\n\tbuffer_load_dwordx4 %0, %3, %4 offen nt lds\n\tbuffer_load_dwordx4 %1, %3, %4 offen offset:1024 nt lds\n\tbuffer_load_dwordx4 %2, %3, %4 offen offset:2048 nt lds"
                         :: "v"(W.a[0]), "v"(W.a[1]), "v"(W.a[2]), "s"(wr), "s"(k * 32 * HD * 2), "s"(wm + (k < 2 ? k + 4 : k - 2) * sl) : "memory");
        W.a[3] = (wave >> 2) * 12288 + ((ln >> 4) * 8 + ((ln >> 2) & 3)) * 384 + (ln & 3) * 8 + w4 * 96;
        W.a[4] = 0;
        W.a[5] = 0;
    }
}
__device__ __forceinline__ void s_role(const Params& P, int wave, int L, int h, int cpy) {
    const int ln = lane_id();
    unsigned l16 = ln * 16;
    constexpr bool isv = false;
    WLoad W;
    wload_start<false>(W, P, wave, isv, h);
    const int mt = (wave >> 1) & 1, hf = wave & 1;
    const unsigned xs = D_XN + mt * 4096 + hf * WIN, x1 = (2 - hf) * WIN + hf * 2048, m0 = L_X + mt * 20480 + hf * 4096, m1 = L_X + mt * 20480 + 8192 + hf * 2048;
    const float SCL = 0.07216878364870322f * 1.4426950408889634f;
    const unsigned px = RG_PX + (h >> 2) * WIN;
    unsigned mb = UNI(blk(10, cpy)), fl = UNI(blk(15, cpy)), pb = UNI(wave == 0 ? blk(32 + (h >> 2), 0) + (h & 3) * 640 : px + (wave == 2 ? (h & 3) * 1024 : 5120 + ((h >> 1) & 1) * 1024 + (h & 1) * 256));
    unsigned b2 = l16 + (wave < 3 ? L_Q : wave == 3 ? L_QX : L_W + wave * 10240), dsto = c1_dst(wave, 1 + (wave >> 2)), bud;
    asm volatile(ASM_S_LOOP : LOOP_IO
                 : LOOP_IN, [xs] "s"(xs), [x1] "s"(x1), [m0] "s"(m0), [m1] "s"(m1), [qs] "s"(UNI(L_KX + hf * 1024 + mt * 8)), [q2] "s"(UNI(L_KX + 2048 + (wave & 3) * 1024)),
                   [pm] "s"(UNI(px + (wave == 2 ? 4096 : 7168) + (h & 3) * 256)), [scl] "s"(__float_as_uint(SCL))
                 : LOOP_CLOB);
    note_timeout(P, bud);
}

__device__ __forceinline__ void v_role(const Params& P, int wave, int L, int h, int cpy) {
    const int ln = lane_id(), w4 = wave & 3;
    unsigned l16 = ln * 16;
    constexpr bool isv = true;
    WLoad W;
    wload_start<false>(W, P, wave, isv, h);
    unsigned b2 = l16 + (wave < 3 ? L_Q : wave == 3 ? L_QX : L_W + wave * 10240), dsto = wave < 4 ? c1_dst(wave, 0) : (l16 >> 8) * GRP + (l16 & 255), bud;
    unsigned mb = UNI(wave < 4 ? blk(10, cpy) : blk(h >> 2, w4) + (h & 3) * 1024), fl = UNI(wave < 4 ? blk(15, cpy) : blk(14, w4) + h * 128), pb = UNI(blk(h & 16, w4) + (h & 15) * 256);
    if (wave >= 4) l16 += L_Q;
    asm volatile(ASM_V_LOOP : LOOP_IO
                 : LOOP_IN, [xs] "s"(D_XN + w4 * WIN)
                 : LOOP_CLOB);
    note_timeout(P, bud);
}

#define D_W0 4
#define D_A1 8
#define D_R1 9
#define D_W1 12
__device__ __forceinline__ void delay(int n) { for (int i = 0; i < n; ++i) __builtin_amdgcn_s_sleep(64); }
__device__ __forceinline__ unsigned handshake(rsrc_t rw, unsigned mo) {
    wait_vm<0>();
    unsigned v = SENT;
    for (int spins = 0; spins < SPIN_CAP && v == SENT; ++spins) {
        asm volatile("s_sleep 4" ::: "memory");
        v = __builtin_amdgcn_readfirstlane(__builtin_amdgcn_raw_buffer_load_b32(rw, 0, mo, 16));
    }
    __builtin_amdgcn_raw_buffer_store_b32(1u, rw, lane_id() ? OOB : 32768u, mo, 16);
    return v & 0x7000;
}
template <class F>
__device__ __forceinline__ void x_tile(const Params& P, int tile, F target) {
    const int ln = lane_id(), l15 = ln & 15, l4 = ln >> 4;
    u32x4 v[20];
    float s = 0.f, q = 0.f;
#pragma unroll
    for (int c = 0; c < 20; ++c) v[c] = *(const u32x4*)(P.x + (size_t)(tile * 16 + l15) * DM + c * 32 + l4 * 8);
#pragma unroll
    for (int c = 0; c < 20; ++c)
#pragma unroll
        for (int e = 0; e < 4; ++e) s += lo2f(v[c][e]) + hi2f(v[c][e]);
    sum2_rows(s, q);
#pragma unroll
    for (int c = 0; c < 20; ++c) asm volatile("" : "+v"(v[c]));
    const float mean = s * (1.0f / DM);
#pragma unroll
    for (int c = 0; c < 20; ++c)
#pragma unroll
        for (int e = 0; e < 4; ++e) { const float a = lo2f(v[c][e]) - mean, b = hi2f(v[c][e]) - mean; q += a * a + b * b; }
    sum2_rows(s, q);
#pragma unroll
    for (int c = 0; c < 20; ++c) asm volatile("" : "+v"(v[c]));
    const float m1 = rbf(mean), r = 1.0f / ln_den(q * (1.0f / DM));
    const unsigned sb = segb(tile >> 1) + (tile & 1) * 4096;
    u32x4 n[20];
#pragma unroll
    for (int c = 0; c < 20; ++c) {
#pragma unroll
        for (int e = 0; e < 4; ++e) { const unsigned d = pack2(lo2f(v[c][e]) - m1, hi2f(v[c][e]) - m1); n[c][e] = pack2(lo2f(d) * r, hi2f(d) * r); }
        asm volatile("" : "+v"(n[c]));
    }
    const rsrc_t rr = target();
#pragma unroll
    for (int c = 0; c < 20; ++c) {
        st128(n[c], rr, ln * 16 + (c & 3) * 1024, D_XN + sb + (c >> 2) * WIN);
        st128(v[c], rr, ln * 16 + (c & 3) * 1024, D_XR + sb + (c >> 2) * WIN);
    }
    wait_vm<0>();
}
__device__ __forceinline__ void wait_go(const Params& P, rsrc_t rd, int k) {
    for (int spins = 0;; ++spins) {
        if (__builtin_amdgcn_readfirstlane(__builtin_amdgcn_raw_buffer_load_b32(rd, 0, FL_VO + (k & 31) * 128, 16))) break;
        asm volatile("s_sleep 16" ::: "memory");
        if (spin_fail(spins * 4 + 3, P)) break;
    }
    asm volatile("" ::: "memory");
}

constexpr unsigned R_RED = 0;
constexpr unsigned R_PART = 8192;
__device__ __forceinline__ void r_role(const Params& P, int wave, int L, int j, int cpy) {
    const int lane = lane_id(), l15 = lane & 15, l4 = lane >> 4;
    const unsigned l16 = lane * 16;
    const rsrc_t rr = mkrsrc(lbase(P, L), RG_L);
    const int sidx = (lane & 2) ? 0xEEEE : 0x4444;
    unsigned offp[3];
    const unsigned sel = P.par & 0x5000;
    auto hq = [&](int q) { return blk(32 + q, cpy) ^ sel; };
#define ROT 1
    const int hg = (wave + ROT * j) & 7;
    offp[0] = hq(hg) + (lane >> 5) * 640 + 128 + (lane & 31) * 16;
    offp[1] = offp[0] + 1280;
    offp[2] = (lane & 2) ? OOB : hq(hg) + l4 * 640 + ((l15 >> 2) * 2 + (l15 & 1)) * 16;
    const int jq = j / 10, j10 = j % 10;
    const unsigned ae = wave < 3 ? ((j10 < 8 && wave * 1024 + l16 < 2560) ? (blk(32 + j10, jq) ^ sel) + wave * 1024 + l16 : OOB)
                      : wave == 3 ? (blk(10 + (j10 >> 2), jq) ^ sel) + (j10 & 3) * 1024 + l16
                      : wave < 6 ? (j < 32 ? RG_PX + (j >> 2) * WIN + (j & 3) * 2048 + (wave & 1) * 1024 + l16 : OOB)
                      : wave == 7 ? D_XA + (j >> 3) * WIN + (j & 7) * 512 + (lane >> 5) * 4096 + (lane & 31) * 16 : OOB;
    const unsigned as = wave < 4 ? CH_SLOT : PX_SLOT;
    static_assert(AH == 3, "generated loop");
    const unsigned offv = (blk((hg >> 2) * 16 + j10, jq) ^ sel) + (hg & 3) * 1024 + l16;
    const unsigned offo = (wave < (int)NCP) ? (blk(10 + (j >> 4), wave) ^ sel) + (j & 15) * 256 + lane * 4 : OOB;
    {
        const u32x4 s4 = sent4();
#pragma unroll
        for (int k = 0; k < (int)NSLOT; ++k) {
            st128(s4, rr, offv, k * CH_SLOT);
            if (wave < 6 && (k || wave != 3)) st128(s4, rr, ae, k * as);
        }
        for (int u = j; u < 126; u += 40) st128(s4, rr, wave * 1024 + l16, u < 108 ? D_XA + (u < 60 ? u / 20 : 3) * 4 * REG + (u < 60 ? u % 20 : u - 60) * WIN : u < 124 ? RG_ST + (u - 108) * WIN : D_SS + (u - 124) * WIN);
        wait_vm<0>();
        bar();
        const int row = l4 * 2 + ((lane >> 2) & 1), col = 16 * j + 4 * (lane & 3) + 2 * ((lane >> 3) & 1);
        __builtin_amdgcn_raw_buffer_store_b32(nosent(*(const unsigned*)(P.mem0 + row * DM + col)), rr, offo, 0, 16);
    }
    auto red_addr = [](int fl) { return R_RED + fl * 80 + (fl >> 4) * 16; };
    const unsigned rw0 = red_addr(l4 * 16 + ((l15 >> 1) & 1) * 8 + (l15 >> 2)) + (l15 & 1) * 4 + (hg ^ (l15 >> 3)) * 8;
    const unsigned rrd = red_addr(lane);
    unsigned bud;
    asm volatile(ASM_R_LOOP : [bud] "=s"(bud)
                 : [wave] "s"(wave), [rs] "s"(rr), [l16] "v"(l16), [ov] "v"(offv), [o0] "v"(offp[0]), [o1] "v"(offp[1]), [o2] "v"(offp[2]), [oo] "v"(offo), [sidx] "v"(sidx), [rw] "v"(rw0), [rd] "v"(rrd),
                   [xo] "v"((l4 >> 1) * 256 + l15 * 16 + (l4 & 1) * 8), [pw] "v"(R_PART + hg * 1024 + l16), [pr] "v"(R_PART + (wave & 1) * 8192 + l16), [fo] "v"((lane & 3) * 128), [ae] "v"(ae),
                   [fs] "s"(UNI((blk(14, jq) ^ sel) + hg * 512)), [vx] "s"(UNI(vxb(j10, hg, jq) ^ sel)), [px] "s"(UNI(RG_PX + hg * WIN)),
                   [xr] "s"(UNI(D_XR + (j >> 3) * WIN + (wave & 1) * 4096 + (j & 7) * 512)), [ad] "s"(UNI(wave < 3 ? -1 : wave == 3 ? 0 : -2)), [as] "s"(UNI(as))
                 : "memory", "vcc", "scc", "v128", "v129", "v130", "v131", "v132", "v133", "v134", "v135", "v136", "v137", "v138", "v139", "v140", "v141", "v142", "v143", "v144", "v145", "v146", "v147", "v148", "v149", "v150", "v151", "v152", "v153", "v154", "v155", "v156", "v157", "v158", "v159", "v160", "v161", "v162", "v163", "v164", "v165", "v166", "v167", "v168", "v169", "v170", "v171", "v172", "v173", "v174", "v175", "v176", "v177", "v178", "v179", "v180", "v181", "v182", "v183", "v184", "v185", "v186", "v187", "v188", "v189", "v190", "v191", "v192", "v193", "v194", "v195", "v196", "v197", "v198", "v199", "v200", "v201", "v202", "v203", "v204", "v205", "v206", "v207", "v208", "v209", "v210", "v211", "v212", "v213", "v214", "v215", "v216", "v217", "v218", "v219", "v220", "v221", "v222", "v223", "v224", "v225", "v226", "v227", "v228", "v229", "v230", "v231", "v232", "v233", "v234", "v235", "v236", "v237", "v238", "v239", "v240", "v241", "v242", "v243", "v244", "v245", "v246", "v247", "v248", "v249", "v250", "v251", "v252", "v253", "v254", "v255", "s40", "s41", "s42", "s43", "s44", "s45", "s46", "s47", "s48", "s49", "s50", "s51", "s52", "s53", "s54", "s55", "s64", "s65", "s66", "s67", "s68", "s69", "s70", "s71", "s72", "s73", "s74", "s75", "s76", "s77", "s78", "s79");
    note_timeout(P, bud);
}

template <int WHICH>
__device__ __forceinline__ void w_role(const Params& P, int wave, int L, int cu) {
    constexpr int NT = (WHICH == 3) ? 2 : 1, N = (WHICH == 3) ? FF : DM, RB = NT * 256, RPI = 1024 / RB;
    const int ln = lane_id(), l15 = ln & 15, l4 = ln >> 4;
    const rsrc_t rd = mkrsrc(lbase(P, L), RG_L);
    const int nt0 = (cu * 8 + wave) * NT;
    const rsrc_t wr = mkrsrc((WHICH == 1 ? P.w1 : WHICH == 2 ? P.w2 : P.w3) + cu * (RB / 2), DM * N * 2 - cu * RB);
    auto swz = [](int row) { return ((row & 3) | ((row >> 1) & 4)) * 32; };
    const int dr = ln * 16 / RB, tr = l4 * 8 + ((ln >> 2) & 3);
    unsigned vo = dr * (N * 2) + ((ln * 16 % RB) ^ swz(wave * RPI + dr)), ta = tr * RB + ((wave * NT * 32 + (ln & 3) * 8) ^ swz(tr));
    const int grp = nt0 / 10, neg = grp & 1, sh = (WHICH == 1) ? (neg ? -1 : 1) * ((grp >> 1) + 1) : 0;
    unsigned ra = ln * 16, wa = ln * 16 + wave * 1024, sr = WHICH == 2 ? l15 * 8 : 24576 + l15 * 80, sw = sr + wave * 8, bud;
    const unsigned oo = WHICH == 3 ? (l4 & 1) * 512 + (l4 >> 1) * 256 + l15 * 16 : (nt0 >> 3) * WIN + (nt0 & 7) * 512 + (l4 >> 1) * 256 + ((l15 + sh) & 15) * 16 + (l4 & 1) * 8;
    unsigned up = ((unsigned)(l15 + sh) > 15u) ^ neg;
#define W_ARGS : [bud] "=s"(bud), "+{v176}"(vo), "+{v177}"(ta), "+{v249}"(ra), "+{v250}"(wa), "+{v251}"(sr), "+{v252}"(sw) \
               , "+{v255}"(up) : "{v253}"(ln * 16), "{v254}"(oo), [wave] "s"(wave), [rs] "s"(rd), [wr] "s"(wr), [wm] "s"(UNI(wave * 1024)), [wo] "s"(UNI(wave * RPI * N * 2)), [go] "s"(FL_VO + (WHICH * 8 + cu) * 128), \
                 [pi] "s"(UNI((WHICH == 1 ? D_XA : WHICH == 2 ? D_HS : D_X1) + wave * 1024)), \
                 [ng] "s"(UNI(WHICH == 3 ? D_H2 + (nt0 >> 3) * WIN + ((nt0 >> 1) & 3) * 1024 : neg)), [ss] "s"(UNI(cu == 0)) \
               : "memory", "vcc", "scc", "v0", "v1", "v2", "v3", "v4", "v5", "v6", "v7", "v8", "v9", "v10", "v11", "v12", "v13", "v14", "v15", "v16", "v17", "v18", "v19", "v20", "v21", "v22", "v23", "v24", "v25", "v26", "v27", "v28", "v29", "v30", "v31", "v32", "v33", "v34", "v35", "v36", "v37", "v38", "v39", "v40", "v41", "v42", "v43", "v44", "v45", "v46", "v47", "v48", "v49", "v50", "v51", "v52", "v53", "v54", "v55", "v56", "v57", "v58", "v59", "v60", "v61", "v62", "v63", "v64", "v65", "v66", "v67", "v68", "v69", "v70", "v71", "v72", "v73", "v74", "v75", "v76", "v77", "v78", "v79", "v80", "v81", "v82", "v83", "v84", "v85", "v86", "v87", "v88", "v89", "v90", "v91", "v92", "v93", "v94", "v95", "v96", "v97", "v98", "v99", "v100", "v101", "v102", "v103", "v104", "v105", "v106", "v107", "v108", "v109", "v110", "v111", "v112", "v113", "v114", "v115", "v116", "v117", "v118", "v119", "v120", "v121", "v122", "v123", "v124", "v125", "v126", "v127", "v128", "v129", "v130", "v131", "v132", "v133", "v134", "v135", "v136", "v137", "v138", "v139", "v140", "v141", "v142", "v143", "v144", "v145", "v146", "v147", "v148", "v149", "v150", "v151", "v152", "v153", "v154", "v155", "v156", "v157", "v158", "v159", "v160", "v161", "v162", "v163", "v164", "v165", "v166", "v167", "v168", "v169", "v170", "v171", "v172", "v173", "v174", "v175", "v178", "v179", "v180", "v181", "v182", "v183", "v184", "v185", "v186", "v187", "v188", "v189", "v190", "v191", "v192", "v193", "v194", "v195", "v196", "v197", "v198", "v199", "v200", "v201", "v202", "v203", "v204", "v205", "v206", "v207", "v208", "v209", "v210", "v211", "v212", "v213", "v214", "v215", "v216", "v217", "v218", "v219", "v220", "v221", "v222", "v223", "v224", "v225", "v226", "v227", "v228", "v229", "v230", "v231", "v232", "v233", "v234", "v235", "v236", "v237", "v238", "v239", "v240", "v241", "v242", "v243", "v244", "v245", "v246", "v247", "v248", "s40", "s41", "s42", "s43", "s44", "s45", "s46", "s47", "s48", "s49", "s50", "s51", "s52", "s53", "s54", "s55", "s56", "s57", "s58", "s59", "s60", "s61", "s62", "s63", "s64", "s65", "s66", "s67", "s68", "s69", "s70", "s71", "s72", "s73", "s74", "s75", "s76", "s77", "s78", "s79"
    if (WHICH == 1) asm volatile(ASM_W1_LOOP W_ARGS);
    if (WHICH == 2) asm volatile(ASM_W2_LOOP W_ARGS);
    if (WHICH == 3) asm volatile(ASM_W3_LOOP W_ARGS);
    note_timeout(P, bud);
}

constexpr unsigned W4_FL = 122880;
__device__ __forceinline__ unsigned lds_vld(unsigned a) { asm volatile("" ::: "memory"); const unsigned v = *(volatile LAS unsigned*)(size_t)a; asm volatile("" ::: "memory"); return v; }
__device__ __forceinline__ void lds_vst(unsigned a, unsigned v) { asm volatile("" ::: "memory"); *(volatile LAS unsigned*)(size_t)a = v; asm volatile("" ::: "memory"); }
__device__ __forceinline__ void w4_role(const Params& P, int wave, int L, int cu) {
    const int ln = lane_id(), l15 = ln & 15, l4 = ln >> 4;
    const unsigned l16 = ln * 16;
    const rsrc_t rd = mkrsrc(lbase(P, L), RG_L);
    unsigned bud;
    if (wave >= 6) wait_go(P, rd, 32 + cu);
    if (wave < 6) {
        u32x32 w[5];
        {
            const rsrc_t wr = mkrsrc(P.w4 + wave * 256 * DM + cu * 80, (FF * DM - wave * 256 * DM - cu * 80) * 2);
            const unsigned ta = wave * 20480 + (l4 * 8 + ((ln >> 2) & 3)) * 160 + (ln & 3) * 8;
            u32x4 f[5][8];
#pragma unroll
            for (int r = 0; r < 2; ++r) {
#pragma unroll
                for (int i = 0; i < 5; ++i) {
                    const unsigned u = i * 64 + ln, vo = (u / 10) * (DM * 2) + (u % 10) * 16;
#pragma unroll
                    for (int k = 0; k < 4; ++k) dma1(wr, vo, (r * 128 + k * 32) * (DM * 2), wave * 20480 + k * 5120 + i * 1024);
                }
                wait_vm<0>();
#pragma unroll
                for (int k = 0; k < 4; ++k)
#pragma unroll
                    for (int n = 0; n < 5; ++n) f[n][r * 4 + k] = tr_frag(ta + k * 5120 + n * 32, 640);
                asm volatile("s_waitcnt lgkmcnt(0)" ::: "memory");
                SB();
            }
#pragma unroll
            for (int n = 0; n < 5; ++n) {
                u32x8 g[4];
#pragma unroll
                for (int i = 0; i < 4; ++i) g[i] = __builtin_shufflevector(f[n][2 * i], f[n][2 * i + 1], 0, 1, 2, 3, 4, 5, 6, 7);
                w[n] = cat32(g[0], g[1], g[2], g[3]);
            }
            asm volatile("" : "+{v[0:31]}"(w[0]), "+{v[32:63]}"(w[1]), "+{v[64:95]}"(w[2]), "+{v[96:127]}"(w[3]), "+{v[128:159]}"(w[4]));
        }
        wait_go(P, rd, 32 + cu);
        bar();
        unsigned ld = l16 + wave * 1024;
        asm volatile(ASM_W4_MUL : [bud] "=s"(bud), [ld] "+v"(ld)
                     : "{v[0:31]}"(w[0]), "{v[32:63]}"(w[1]), "{v[64:95]}"(w[2]), "{v[96:127]}"(w[3]), "{v[128:159]}"(w[4]), [l2] "v"(2 * ld + 61440), [l16] "v"(l16), [fa] "v"(W4_FL + wave * 4), [rs] "s"(rd),
                       [hb] "s"(UNI(D_H2 + wave * 2 * WIN)), [wave] "s"(wave), [ro] "s"(UNI(wave * 5120)), [fp] "v"(W4_FL + (ln < 6 ? ln : 0) * 4)
                     : "memory", "vcc", "scc", "v160", "v161", "v162", "v163", "v164", "v165", "v166", "v167", "v168", "v169", "v170", "v171", "v172", "v173", "v174", "v175", "v176", "v177", "v178", "v179", "v180", "v181", "v182", "v183", "v184", "v185", "v186", "v187", "v188", "v189", "v190", "v191", "v192", "v193", "v194", "v195", "v196", "v197", "v198", "v199", "v200", "v201", "v202", "v203", "v204", "v205", "v206", "v207", "v208", "v209", "v210", "v211", "v212", "v213", "v214", "v215", "v216", "v217", "v218", "v219", "v220", "v221", "v222", "v223", "v224", "v225", "v226", "v227", "v228", "v229", "v230", "v231", "v232", "v233", "v234", "v235", "v236", "v237", "v238", "v239", "v240", "v241", "v242", "v243", "v244", "v245", "v246", "v247", "s40", "s41", "s42", "s43", "s44", "s45", "s46", "s47", "s48", "s49", "s50", "s51", "s52", "s53", "s54", "s55", "s64", "s65", "s66", "s67", "s68", "s69", "s70", "s71", "s72", "s73", "s74", "s75", "s76", "s77", "s78", "s79");
    } else {
        const int mt = wave - 6;
        if (ln < 16) lds_vst(W4_FL + ln * 4, 0u);
        bar();
        auto tl = [&](int n) { return (unsigned)((cu * 5 + n) >> 3) * WIN + ((cu * 5 + n) & 7) * 512 + mt * 4096; };
        const unsigned o8 = (l4 >> 1) * 256 + l15 * 16 + (l4 & 1) * 8;
        unsigned la = mt * 30720 + l16;
        static_assert(RG_ST == 112 * WIN && D_XN == 10 * REG && D_XR == 6 * REG, "generated loop");
#define F_IN [l2] "v"(2 * la + 61440), [fa] "v"(W4_FL + (ln < 6 ? ln : 0) * 4), [fb] "v"(W4_FL + 32 + (ln < 5 ? ln : 0) * 4), [fc] "v"(W4_FL + 24 + mt * 4), [rs] "s"(rd), [mt] "s"(mt), \
             [t0] "v"(o8 + tl(0)), [t1] "v"(o8 + tl(1)), [t2] "v"(o8 + tl(2)), [t3] "v"(o8 + tl(3)), [t4] "v"(o8 + tl(4))
#define F_CLOB "memory", "vcc", "scc", "v64", "v65", "v66", "v67", "v68", "v69", "v70", "v71", "v72", "v73", "v74", "v75", "v76", "v77", "v78", "v79", "v80", "v81", "v82", "v83", "v84", "v85", "v86", "v87", "v88", "v89", "v90", "v91", "v92", "v93", "v94", "v95", "v96", "v97", "v98", "v99", "v100", "v101", "v102", "v103", "v104", "v105", "v106", "v107", "v108", "v109", "v110", "v111", "v112", "v113", "v114", "v115", "v116", "v117", "v118", "v119", "v120", "v121", "v122", "v123", "v124", "v125", "v126", "v127", "v128", "v129", "v130", "v131", "v132", "v133", "v134", "v135", "v136", "v137", "v138", "v139", "v140", "v141", "v142", "v143", "v144", "v145", "v146", "v147", "v148", "v149", "v150", "v151", "v152", "v153", "v154", "v155", "v156", "v157", "v158", "v159", "v160", "v161", "v162", "v163", "v164", "v165", "v166", "v167", "v168", "v169", "v170", "v171", "v172", "v173", "v174", "v175", "v176", "v177", "v178", "v179", "v180", "v181", "v182", "v183", "v184", "v185", "v186", "v187", "v188", "v189", "v190", "v191", "v192", "v193", "v194", "v195", "v196", "v197", "v198", "v199", "v200", "v201", "v202", "v203", "v204", "v205", "v206", "v207", "v208", "v209", "v210", "v211", "v212", "v213", "v214", "v215", "v216", "v217", "v218", "v219", "v220", "v221", "v222", "v223", "v224", "v225", "v226", "v227", "v228", "v229", "v230", "v231", "v232", "v233", "v234", "v235", "v236", "v237", "v238", "v239", "s40", "s41", "s42", "s43", "s44", "s45", "s46", "s47", "s48", "s49", "s50", "s51", "s52", "s53", "s54", "s55", "s64", "s65", "s66", "s67", "s68", "s69", "s70", "s71", "s72", "s73", "s74", "s75", "s76", "s77", "s78", "s79"
        if (L) asm volatile(ASM_W4_FIN1 : [bud] "=s"(bud), [la] "+v"(la)
                            : F_IN, [ro] "s"(mkrsrc(P.out, ACT)), [co] "s"(cu * 160), [q8] "v"(l15 * (DM * 2) + l4 * 8), [q16] "v"(l15 * (DM * 2) + (l4 & 1) * 32 + (l4 >> 1) * 16) : F_CLOB);
        else asm volatile(ASM_W4_FIN0 : [bud] "=s"(bud), [la] "+v"(la)
                          : F_IN, [rn] "s"(mkrsrc(lbase(P, 1), RG_L)), [co] "s"(cu * 128), [l8] "v"(ln * 8), [o16] "v"(tl(l4 & 1) + (l4 >> 1) * 256 + l15 * 16), [o17] "v"(tl(2 + (l4 & 1)) + (l4 >> 1) * 256 + l15 * 16),
                            [fo] "v"(ln < 32 ? blk(0, ln >> 3) - CH0 + (ln & 7) * 16 : OOB), [fs] "s"(FL_XN + (cu * 2 + mt) * 128) : F_CLOB);
    }
    note_timeout(P, bud);
}

constexpr int LEADER = 252;
__device__ __forceinline__ unsigned leader(const Params& P, rsrc_t rw, int wave) {
    const int ln = lane_id();
    const u32x4 s4 = sent4(), z4 = s4 ^ s4;
    const unsigned mo = (wave * 32 + (ln >> 1)) * 128 + (ln & 1) * 16;
    __builtin_amdgcn_raw_buffer_store_b32(0u, rw, ln < 2 ? ln * 2048 : OOB, CAL0 + wave * 4096, 16);
    for (int k = 0; k < 6; ++k) st128(z4, rw, ln * 16, FL_VO + (k >> 2) * GRP + wave * 4096 + (k & 3) * 1024);
    st128(z4, rw, mo, FL_MB + 32768);
    if (wave == 0) st128(z4, rw, ln ? OOB : 256u, FL0);
    wait_vm<0>();
    bar();
    if (wave == 0) {
        const unsigned o = ln ? OOB : 0u;
        auto trip = [&](unsigned a) {
            unsigned long long t0, t1;
            asm volatile("s_memtime %0\n\ts_waitcnt lgkmcnt(0)\n\tbuffer_store_dword %2, %2, %3, %4 offen sc1\n\ts_waitcnt vmcnt(0)\n\ts_memtime %1\n\ts_waitcnt lgkmcnt(0)"
                         : "=&s"(t0), "=&s"(t1) : "v"(o), "s"(rw), "s"(CAL0 + a) : "memory");
            return (unsigned)(t1 - t0);
        };
        auto mn = [](unsigned a, unsigned b) { return a < b ? a : b; };
        const unsigned a0 = trip(0), b0 = trip(0x2000), a1 = trip(0x800), b1 = trip(0x2800);
        const unsigned h = mn(a0, a1) < mn(b0, b1) ? 0u : 0x2000u, t0 = h ? b0 + b1 : a0 + a1;
        const unsigned t14 = trip(h + 0x4000) + trip(h + 0x4800), t12 = trip(h + 0x1000) + trip(h + 0x1800);
        lds_vst(0, (h ^ 0x2000u) | (t14 < t0 ? 0x4000u : 0u) | (t12 < t0 ? 0x1000u : 0u));
    }
    bar();
    const unsigned par = __builtin_amdgcn_readfirstlane(lds_vld(0));
    for (int k = 0; k < 10; ++k) st128(s4, rw, ln * 16, CH0 + (10 + (k >> 2)) * GRP + wave * 4096 + (k & 3) * 1024);
    wait_vm<0>();
    bar();
    bool open = wave * 32 + (ln >> 1) != LEADER;
    for (int spins = 0; spins < SPIN_CAP; ++spins) {
        st128(s4 & par, rw, open ? mo : OOB, FL_MB);
        __builtin_amdgcn_s_sleep(20);
        const u32x4 d = LD128(rw, mo, FL_MB + 32768);
        open = open && !(d[0] && d[1] && d[2] && d[3]);
        if (!__any(open)) break;
    }
    bar();
    return par;
}
__global__ __launch_bounds__(512, 2) void mega_kernel(Params P) {
    const int wg = blockIdx.x;
    const int wave = __builtin_amdgcn_readfirstlane(threadIdx.x >> 6);
    const int L = (wg >> 2) & 1, x = wg & 3, r = wg >> 3, cpy = x;
    if (__builtin_expect(r < 16, 1)) {
        WLoad W;
        if (L) delay(D_A1);
        wload_start<true>(W, P, wave, r >= 8, x * 8 + (r & 7));
    }
    constexpr int nr = 10;
    const int j = x * 10 + r - 16, wk = r - 26;
    const int i = x == 0 ? 16 + wk : x == 2 ? 10 + wk : wk < 5 ? (x == 1 ? 5 : 0) + wk : 22 + (x >> 1);
    const rsrc_t rw = mkrsrc(P.ws, RG_L);
    const bool lead = wg == LEADER;
    if (!lead) __builtin_amdgcn_raw_buffer_store_b32(SENT, rw, lane_id() ? OOB : 0u, FL_MB + wg * 128 + wave * 4, 16);
#define FIN() __builtin_amdgcn_endpgm()
    if (r < 8) { s_role(P, wave, L, x * 8 + r, cpy); FIN(); }
    if (r < 16) { v_role(P, wave, L, x * 8 + r - 8, cpy); FIN(); }
    if (lead) P.par = leader(P, rw, wave);
    else {
        const unsigned mo = FL_MB + wg * 128 + wave * 4;
        const bool ln = r - 16 < nr && j < 16, first = ln && !L && wave == 7;
        if (!first) { P.par = handshake(rw, mo); if (L || r - 16 >= nr) delay(!L ? D_W0 : r - 16 < nr ? D_R1 : D_W1); }
        if (first || (ln && L && wave)) x_tile(P, (L ? wave * 16 : 0) + j, [&]() { if (first) P.par = handshake(rw, mo); return mkrsrc(lbase(P, 0), RG_L); });
        if (ln && L) bar();
        if (first || (ln && L && !wave)) {
            const rsrc_t r0 = mkrsrc(lbase(P, 0), RG_L);
            const int lane = lane_id();
            const unsigned fo = lane < 32 ? blk(0, lane >> 3) - CH0 + (lane & 7) * 16 : OOB;
            if (L) for (int spins = 0; spins < SPIN_CAP && __any(lane < 32 && LD128(r0, fo, FL_XN + j * 128)[0] == 0u); ++spins) asm volatile("s_sleep 8" ::: "memory");
            st128(sent4() & (L ? 64u : 8u), r0, fo, FL_XN + j * 128);
        }
    }
    if (r - 16 < nr) { r_role(P, wave, L, j, cpy); FIN(); }
    if (i < 5) { w_role<1>(P, wave, L, i); FIN(); }
    if (i < 10) { w_role<2>(P, wave, L, i - 5); FIN(); }
    if (i < 16) { w_role<3>(P, wave, L, i - 10); FIN(); }
    w4_role(P, wave, L, i - 16);
    FIN();
}

extern "C" void kernel_launch(void* const* d_in, const int* in_sizes, int n_in, void* d_out, int out_size, void* d_ws,
                              size_t ws_size, hipStream_t stream) {
    char* ws = (char*)d_ws + (-(size_t)d_ws & (REG - 1));
    Params P;
    P.par = 0; P.pad = 0; P.ws = ws; P.out = (bf16_t*)d_out;
    P.x = (const bf16_t*)d_in[0]; P.mem0 = (const bf16_t*)d_in[1]; P.wq = (const bf16_t*)d_in[2]; P.wk = (const bf16_t*)d_in[3]; P.wv = (const bf16_t*)d_in[4]; P.wo = (const bf16_t*)d_in[5];
    P.w1 = (const bf16_t*)d_in[8]; P.w2 = (const bf16_t*)d_in[9]; P.w3 = (const bf16_t*)d_in[10]; P.w4 = (const bf16_t*)d_in[11];
    (void)hipFuncSetAttribute((const void*)mega_kernel, hipFuncAttributeMaxDynamicSharedMemorySize, MEGA_LDS);

    mega_kernel<<<256, 512, MEGA_LDS, stream>>>(P);
}
```

```cpp
#include <hip/hip_runtime.h>
#include <stdint.h>

typedef unsigned short bf16_t;
using bf16x2 = __attribute__((ext_vector_type(2))) __bf16;
using f32x2 = __attribute__((ext_vector_type(2))) float;
using u32x2 = __attribute__((ext_vector_type(2))) unsigned;
using u32x4 = __attribute__((ext_vector_type(4))) unsigned;
typedef __amdgpu_buffer_rsrc_t rsrc_t;

__device__ __forceinline__ unsigned pack2(float a, float b) {
    bf16x2 r = __builtin_convertvector((f32x2){a, b}, bf16x2);
    return __builtin_bit_cast(unsigned, r);
}
__device__ __forceinline__ float rbf(float f) { return __uint_as_float(pack2(f, 0.f) << 16); }
__device__ __forceinline__ float lo2f(unsigned u) { return __uint_as_float(u << 16); }
__device__ __forceinline__ float hi2f(unsigned u) { return __uint_as_float(u & 0xFFFF0000u); }
__device__ __forceinline__ rsrc_t mkrsrc(const void* p, unsigned bytes) {
    return __builtin_amdgcn_make_buffer_rsrc((void*)p, 0, bytes, 0x00020000);
}

constexpr int S_ = 2048, DM = 640, NH = 32, DH = 192, HD = NH * DH  , SEG = 32, NSEG = 64;
constexpr int FF = 1536;

constexpr unsigned NSLOT = 4, NCP = 4;
constexpr unsigned WIN = 16384, REG = 2u << 20, REGS = REG + 8192;
constexpr unsigned GRP = 32768, CH0 = 48 * REG, CH_SLOT = 2 * REG;
__host__ __device__ constexpr unsigned blk(unsigned grp, unsigned x) { return CH0 + grp * GRP + (((x ^ (grp >> 4)) & 1) << 14) + ((((x >> 1) ^ (grp >> 5)) & 1) << 12); }
constexpr unsigned PX_SLOT = 8 * WIN;
constexpr unsigned RG_PX = 80 * WIN, RG_ST = RG_PX + NSLOT * PX_SLOT;
static_assert(RG_ST + 16 * WIN <= REG, "ring region");
constexpr unsigned FL0 = REG, FL_PAR = REG + 4096, FL_MB = REG + 65536;
constexpr unsigned FL_VO = blk(14, 0), FL_XN = blk(15, 0), D_SS = 2 * REG + 52 * WIN, CAL0 = blk(13, 0);
constexpr unsigned ACT = S_ * DM * 2;
constexpr unsigned D_XR = 6 * REG, D_XN = 10 * REG, D_XA = 14 * REG, D_HS = 18 * REG, D_X1 = 22 * REG, D_H2 = 26 * REG;
__host__ __device__ constexpr unsigned segb(unsigned seg) { return seg * (5 * WIN) + (seg >> 4) * (REGS - 80 * WIN); }
__host__ __device__ constexpr unsigned segh(unsigned seg) { return seg * (12 * WIN) + (seg >> 3) * (REGS - 96 * WIN); }
__host__ __device__ constexpr unsigned vxb(unsigned tile, unsigned hq, unsigned x) { return tile < 8 ? blk(tile * 8 + hq, x) + NSLOT * CH_SLOT : blk(tile * 8 - 16 + hq, x); }
constexpr int AH = 3;
constexpr unsigned RG_L = CH0 + 2 * NSLOT * CH_SLOT;

#define SB() __builtin_amdgcn_sched_barrier(0)
#define LAS __attribute__((address_space(3)))
__device__ __forceinline__ unsigned lds_vld(unsigned a) { asm volatile("" ::: "memory"); const unsigned v = *(volatile LAS unsigned*)(size_t)a; asm volatile("" ::: "memory"); return v; }
__device__ __forceinline__ void lds_vst(unsigned a, unsigned v) { asm volatile("" ::: "memory"); *(volatile LAS unsigned*)(size_t)a = v; asm volatile("" ::: "memory"); }
constexpr unsigned OOB = 0x7FFFFF00u;
template <int N> __device__ __forceinline__ void wait_vm() { asm volatile("s_waitcnt vmcnt(%0)" :: "i"(N) : "memory"); }
__device__ __forceinline__ void bar() {
    asm volatile("s_waitcnt lgkmcnt(0)" ::: "memory");
    __builtin_amdgcn_s_barrier();
    asm volatile("" ::: "memory");
}
__device__ __forceinline__ int lane_id() {
    int ln;
    asm volatile("v_mbcnt_lo_u32_b32 %0, -1, 0\n\tv_mbcnt_hi_u32_b32 %0, -1, %0" : "=v"(ln));
    return ln;
}
__device__ __forceinline__ void sum2_rows(float& s, float& q) {
    unsigned a = __float_as_uint(s), b = a, c = __float_as_uint(q), d = c;
    asm volatile("s_nop 2\n\tv_permlane16_swap_b32 %0, %1\n\tv_permlane16_swap_b32 %2, %3\n\tv_add_f32 %0, %0, %1\n\tv_add_f32 %2, %2, %3\n\tv_mov_b32 %1, %0\n\tv_mov_b32 %3, %2\n\t"
                 "s_nop 1\n\tv_permlane32_swap_b32 %0, %1\n\tv_permlane32_swap_b32 %2, %3\n\tv_add_f32 %0, %0, %1\n\tv_add_f32 %2, %2, %3" : "+v"(a), "+v"(b), "+v"(c), "+v"(d));
    s = __uint_as_float(a); q = __uint_as_float(c);
}
template <int AUXV>
__device__ __forceinline__ void st128_(u32x4 v, rsrc_t r, unsigned voff, unsigned soff) {
    __builtin_amdgcn_raw_buffer_store_b128(v, r, voff, soff, AUXV);
    asm volatile("s_nop 1" :: "v"(v) : "memory");
}
#define st128(V, R, VO_, SO_) st128_<16>(V, R, VO_, SO_)
#define LD128(R, VO_, SO_) __builtin_amdgcn_raw_buffer_load_b128(R, VO_, SO_, 16)
__device__ __forceinline__ float ln_den(float var) { return rbf(__builtin_amdgcn_sqrtf(rbf(rbf(var) + 1e-5f))); }

struct Params {
    char* ws;
    const bf16_t *x, *mem0, *wq, *wk, *wv, *wo, *w1, *w2, *w3, *w4;
    bf16_t* out;
    unsigned par, pad;
};

#define SENT 0xFFFFFFFFu
constexpr int SPIN_CAP = 400000;
__device__ __forceinline__ bool spin_fail(int spins, const Params& P) {
    if ((spins & 1023) == 1023) {
        const rsrc_t rt = mkrsrc(P.ws + FL0, 512);
        unsigned o;
        asm volatile("v_mov_b32 %0, 0x100" : "=v"(o));
        if (__builtin_amdgcn_readfirstlane(__builtin_amdgcn_raw_buffer_load_b32(rt, o, 0, 16)) != 0u) return true;
        if (spins > SPIN_CAP) { __builtin_amdgcn_raw_buffer_store_b32(o, rt, o, 0, 16); return true; }
    }
    return false;
}
__device__ __forceinline__ unsigned nosent(unsigned d) { return d < 0xFFFFFFFEu ? d : 0xFFFFFFFEu; }
__device__ __forceinline__ u32x4 sent4() { unsigned sv; asm volatile("v_mov_b32 %0, -1" : "=v"(sv)); return (u32x4){sv, sv, sv, sv}; }
__device__ __forceinline__ char* lbase(const Params& P, int L) { return P.ws + ((P.par ^ (L << 13)) & 0x2000); }

#define LST0
#define LST1
#define LST2
#define LST3
#define LST4
#define LST5
#define LST6
#define LST7
#define LSTINIT
#define LFLUSH200
#define LFLUSH212
#define LFLUSHM
#define LFLUSHF
#define LFLUSHR
#define LFLUSHW
#define LST_INW
#define LST_IN

#define LJIT
#define CJIT(m)
constexpr unsigned L_X = 0;
constexpr unsigned L_MEM = 40960;
constexpr unsigned L_Q = 51200;
constexpr unsigned L_KM = 54272;
constexpr unsigned L_QX = 57344;
constexpr unsigned L_KX = 69632;
constexpr unsigned L_W = 81920;
constexpr int MEGA_LDS = 163840;

#define ASM_S_LOOP \
    "s_mov_b64 s[60:61], %[ws]\n\t" \
    "s_mov_b32 s62, %[sz]\n\t" \
    "s_mov_b32 s63, 0x20000\n\t" \
    "s_mov_b32 s40, 0\n\t" \
    "s_mov_b32 s41, 0x20000\n\t" \
    LSTINIT \
    "s_cmp_eq_u32 %[wave], 0\n\t" \
    "s_cbranch_scc1 .Ls0_%=\n\t" \
    "s_cmp_eq_u32 %[wave], 1\n\t" \
    "s_cbranch_scc1 .Ls1_%=\n\t" \
    "s_cmp_eq_u32 %[wave], 2\n\t" \
    "s_cbranch_scc1 .Ls2_%=\n\t" \
    "s_cmp_eq_u32 %[wave], 3\n\t" \
    "s_cbranch_scc1 .Ls3_%=\n\t" \
    ".Ls4_%=:\n\t" \
    "s_mov_b32 s42, %[xs]\n\t" \
    "v_add_u32 v244, 0x18000, v243\n\t" \
    "s_waitcnt vmcnt(12) lgkmcnt(0)\n\t" \
    "s_barrier \n\t" \
    "s_add_u32 m0, %[wm], 0x12000\n\t" \
    "s_mov_b32 s44, 0x1e0000\n\t" \
    "buffer_load_dwordx4 v240, %[wr], s44 offen offset:0 nt lds\n\t" \
    "buffer_load_dwordx4 v241, %[wr], s44 offen offset:1024 nt lds\n\t" \
    "buffer_load_dwordx4 v242, %[wr], s44 offen offset:2048 nt lds\n\t" \
    "ds_read_b64_tr_b16 v[200:201], v244 offset:0\n\t" \
    "ds_read_b64_tr_b16 v[202:203], v244 offset:1536\n\t" \
    "ds_read_b64_tr_b16 v[0:1], v244 offset:32\n\t" \
    "ds_read_b64_tr_b16 v[2:3], v244 offset:1568\n\t" \
    "ds_read_b64_tr_b16 v[8:9], v244 offset:64\n\t" \
    "ds_read_b64_tr_b16 v[10:11], v244 offset:1600\n\t" \
    "v_add_u32 v244, 0x1e000, v243\n\t" \
    "s_waitcnt vmcnt(12) lgkmcnt(0)\n\t" \
    "s_barrier \n\t" \
    "s_add_u32 m0, %[wm], 0x18000\n\t" \
    "s_mov_b32 s44, 0x240000\n\t" \
    "buffer_load_dwordx4 v240, %[wr], s44 offen offset:0 nt lds\n\t" \
    "buffer_load_dwordx4 v241, %[wr], s44 offen offset:1024 nt lds\n\t" \
    "buffer_load_dwordx4 v242, %[wr], s44 offen offset:2048 nt lds\n\t" \
    "ds_read_b64_tr_b16 v[204:205], v244 offset:0\n\t" \
    "ds_read_b64_tr_b16 v[206:207], v244 offset:1536\n\t" \
    "ds_read_b64_tr_b16 v[4:5], v244 offset:32\n\t" \
    "ds_read_b64_tr_b16 v[6:7], v244 offset:1568\n\t" \
    "ds_read_b64_tr_b16 v[12:13], v244 offset:64\n\t" \
    "ds_read_b64_tr_b16 v[14:15], v244 offset:1600\n\t" \
    "v_add_u32 v244, 0x0, v243\n\t" \
    "s_waitcnt vmcnt(12) lgkmcnt(0)\n\t" \
    "s_barrier \n\t" \
    "s_add_u32 m0, %[wm], 0x1e000\n\t" \
    "s_mov_b32 s44, 0x2a0000\n\t" \
    "buffer_load_dwordx4 v240, %[wr], s44 offen offset:0 nt lds\n\t" \
    "buffer_load_dwordx4 v241, %[wr], s44 offen offset:1024 nt lds\n\t" \
    "buffer_load_dwordx4 v242, %[wr], s44 offen offset:2048 nt lds\n\t" \
    "ds_read_b64_tr_b16 v[16:17], v244 offset:0\n\t" \
    "ds_read_b64_tr_b16 v[18:19], v244 offset:1536\n\t" \
    "ds_read_b64_tr_b16 v[24:25], v244 offset:32\n\t" \
    "ds_read_b64_tr_b16 v[26:27], v244 offset:1568\n\t" \
    "ds_read_b64_tr_b16 v[32:33], v244 offset:64\n\t" \
    "ds_read_b64_tr_b16 v[34:35], v244 offset:1600\n\t" \
    "v_add_u32 v244, 0x6000, v243\n\t" \
    "s_waitcnt vmcnt(12) lgkmcnt(0)\n\t" \
    "s_barrier \n\t" \
    "s_add_u32 m0, %[wm], 0x0\n\t" \
    "s_mov_b32 s44, 0x300000\n\t" \
    "buffer_load_dwordx4 v240, %[wr], s44 offen offset:0 nt lds\n\t" \
    "buffer_load_dwordx4 v241, %[wr], s44 offen offset:1024 nt lds\n\t" \
    "buffer_load_dwordx4 v242, %[wr], s44 offen offset:2048 nt lds\n\t" \
    "ds_read_b64_tr_b16 v[20:21], v244 offset:0\n\t" \
    "ds_read_b64_tr_b16 v[22:23], v244 offset:1536\n\t" \
    "ds_read_b64_tr_b16 v[28:29], v244 offset:32\n\t" \
    "ds_read_b64_tr_b16 v[30:31], v244 offset:1568\n\t" \
    "ds_read_b64_tr_b16 v[36:37], v244 offset:64\n\t" \
    "ds_read_b64_tr_b16 v[38:39], v244 offset:1600\n\t" \
    "v_add_u32 v244, 0xc000, v243\n\t" \
    "s_waitcnt vmcnt(12) lgkmcnt(0)\n\t" \
    "s_barrier \n\t" \
    "s_add_u32 m0, %[wm], 0x6000\n\t" \
    "s_mov_b32 s44, 0x360000\n\t" \
    "buffer_load_dwordx4 v240, %[wr], s44 offen offset:0 nt lds\n\t" \
    "buffer_load_dwordx4 v241, %[wr], s44 offen offset:1024 nt lds\n\t" \
    "buffer_load_dwordx4 v242, %[wr], s44 offen offset:2048 nt lds\n\t" \
    "ds_read_b64_tr_b16 v[208:209], v244 offset:0\n\t" \
    "ds_read_b64_tr_b16 v[210:211], v244 offset:1536\n\t" \
    "ds_read_b64_tr_b16 v[40:41], v244 offset:32\n\t" \
    "ds_read_b64_tr_b16 v[42:43], v244 offset:1568\n\t" \
    "ds_read_b64_tr_b16 v[48:49], v244 offset:64\n\t" \
    "ds_read_b64_tr_b16 v[50:51], v244 offset:1600\n\t" \
    "v_add_u32 v244, 0x12000, v243\n\t" \
    "s_waitcnt vmcnt(12) lgkmcnt(0)\n\t" \
    "s_barrier \n\t" \
    "s_add_u32 m0, %[wm], 0xc000\n\t" \
    "s_mov_b32 s44, 0x3c0000\n\t" \
    "buffer_load_dwordx4 v240, %[wr], s44 offen offset:0 nt lds\n\t" \
    "buffer_load_dwordx4 v241, %[wr], s44 offen offset:1024 nt lds\n\t" \
    "buffer_load_dwordx4 v242, %[wr], s44 offen offset:2048 nt lds\n\t" \
    "ds_read_b64_tr_b16 v[212:213], v244 offset:0\n\t" \
    "ds_read_b64_tr_b16 v[214:215], v244 offset:1536\n\t" \
    "ds_read_b64_tr_b16 v[44:45], v244 offset:32\n\t" \
    "ds_read_b64_tr_b16 v[46:47], v244 offset:1568\n\t" \
    "ds_read_b64_tr_b16 v[52:53], v244 offset:64\n\t" \
    "ds_read_b64_tr_b16 v[54:55], v244 offset:1600\n\t" \
    "v_add_u32 v244, 0x18000, v243\n\t" \
    "s_waitcnt vmcnt(12) lgkmcnt(0)\n\t" \
    "s_barrier \n\t" \
    "s_add_u32 m0, %[wm], 0x12000\n\t" \
    "s_mov_b32 s44, 0x420000\n\t" \
    "buffer_load_dwordx4 v240, %[wr], s44 offen offset:0 nt lds\n\t" \
    "buffer_load_dwordx4 v241, %[wr], s44 offen offset:1024 nt lds\n\t" \
    "buffer_load_dwordx4 v242, %[wr], s44 offen offset:2048 nt lds\n\t" \
    "ds_read_b64_tr_b16 v[56:57], v244 offset:0\n\t" \
    "ds_read_b64_tr_b16 v[58:59], v244 offset:1536\n\t" \
    "ds_read_b64_tr_b16 v[64:65], v244 offset:32\n\t" \
    "ds_read_b64_tr_b16 v[66:67], v244 offset:1568\n\t" \
    "ds_read_b64_tr_b16 v[72:73], v244 offset:64\n\t" \
    "ds_read_b64_tr_b16 v[74:75], v244 offset:1600\n\t" \
    "v_add_u32 v244, 0x1e000, v243\n\t" \
    "s_waitcnt vmcnt(12) lgkmcnt(0)\n\t" \
    "s_barrier \n\t" \
    "s_add_u32 m0, %[wm], 0x18000\n\t" \
    "s_mov_b32 s44, 0x480000\n\t" \
    "buffer_load_dwordx4 v240, %[wr], s44 offen offset:0 nt lds\n\t" \
    "buffer_load_dwordx4 v241, %[wr], s44 offen offset:1024 nt lds\n\t" \
    "buffer_load_dwordx4 v242, %[wr], s44 offen offset:2048 nt lds\n\t" \
    "ds_read_b64_tr_b16 v[60:61], v244 offset:0\n\t" \
    "ds_read_b64_tr_b16 v[62:63], v244 offset:1536\n\t" \
    "ds_read_b64_tr_b16 v[68:69], v244 offset:32\n\t" \
    "ds_read_b64_tr_b16 v[70:71], v244 offset:1568\n\t" \
    "ds_read_b64_tr_b16 v[76:77], v244 offset:64\n\t" \
    "ds_read_b64_tr_b16 v[78:79], v244 offset:1600\n\t" \
    "v_add_u32 v244, 0x0, v243\n\t" \
    "s_waitcnt vmcnt(12) lgkmcnt(0)\n\t" \
    "s_barrier \n\t" \
    "v_mov_b32 v246, 0\n\t" \
    "buffer_load_dword v246, v246, s[60:63], %[mo] offen sc1\n\t" \
    "s_add_u32 m0, %[wm], 0x1e000\n\t" \
    "s_mov_b32 s44, 0x4e0000\n\t" \
    "buffer_load_dwordx4 v240, %[wr], s44 offen offset:0 nt lds\n\t" \
    "buffer_load_dwordx4 v241, %[wr], s44 offen offset:1024 nt lds\n\t" \
    "buffer_load_dwordx4 v242, %[wr], s44 offen offset:2048 nt lds\n\t" \
    "ds_read_b64_tr_b16 v[216:217], v244 offset:0\n\t" \
    "ds_read_b64_tr_b16 v[218:219], v244 offset:1536\n\t" \
    "ds_read_b64_tr_b16 v[80:81], v244 offset:32\n\t" \
    "ds_read_b64_tr_b16 v[82:83], v244 offset:1568\n\t" \
    "ds_read_b64_tr_b16 v[88:89], v244 offset:64\n\t" \
    "ds_read_b64_tr_b16 v[90:91], v244 offset:1600\n\t" \
    "v_add_u32 v244, 0x6000, v243\n\t" \
    "s_waitcnt vmcnt(12) lgkmcnt(0)\n\t" \
    "s_barrier \n\t" \
    "s_add_u32 m0, %[wm], 0x0\n\t" \
    "s_mov_b32 s44, 0x540000\n\t" \
    "buffer_load_dwordx4 v240, %[wr], s44 offen offset:0 nt lds\n\t" \
    "buffer_load_dwordx4 v241, %[wr], s44 offen offset:1024 nt lds\n\t" \
    "buffer_load_dwordx4 v242, %[wr], s44 offen offset:2048 nt lds\n\t" \
    "ds_read_b64_tr_b16 v[220:221], v244 offset:0\n\t" \
    "ds_read_b64_tr_b16 v[222:223], v244 offset:1536\n\t" \
    "ds_read_b64_tr_b16 v[84:85], v244 offset:32\n\t" \
    "ds_read_b64_tr_b16 v[86:87], v244 offset:1568\n\t" \
    "ds_read_b64_tr_b16 v[92:93], v244 offset:64\n\t" \
    "ds_read_b64_tr_b16 v[94:95], v244 offset:1600\n\t" \
    "v_add_u32 v244, 0xc000, v243\n\t" \
    "s_waitcnt vmcnt(12) lgkmcnt(0)\n\t" \
    "s_barrier \n\t" \
    "s_add_u32 m0, %[wm], 0x6000\n\t" \
    "s_mov_b32 s44, 0x5a0000\n\t" \
    "buffer_load_dwordx4 v240, %[wr], s44 offen offset:0 nt lds\n\t" \
    "buffer_load_dwordx4 v241, %[wr], s44 offen offset:1024 nt lds\n\t" \
    "buffer_load_dwordx4 v242, %[wr], s44 offen offset:2048 nt lds\n\t" \
    "ds_read_b64_tr_b16 v[96:97], v244 offset:0\n\t" \
    "ds_read_b64_tr_b16 v[98:99], v244 offset:1536\n\t" \
    "ds_read_b64_tr_b16 v[104:105], v244 offset:32\n\t" \
    "ds_read_b64_tr_b16 v[106:107], v244 offset:1568\n\t" \
    "ds_read_b64_tr_b16 v[112:113], v244 offset:64\n\t" \
    "ds_read_b64_tr_b16 v[114:115], v244 offset:1600\n\t" \
    "v_add_u32 v244, 0x12000, v243\n\t" \
    "s_waitcnt vmcnt(12) lgkmcnt(0)\n\t" \
    "s_barrier \n\t" \
    "s_add_u32 m0, %[wm], 0xc000\n\t" \
    "s_mov_b32 s44, 0x600000\n\t" \
    "buffer_load_dwordx4 v240, %[wr], s44 offen offset:0 nt lds\n\t" \
    "buffer_load_dwordx4 v241, %[wr], s44 offen offset:1024 nt lds\n\t" \
    "buffer_load_dwordx4 v242, %[wr], s44 offen offset:2048 nt lds\n\t" \
    "ds_read_b64_tr_b16 v[100:101], v244 offset:0\n\t" \
    "ds_read_b64_tr_b16 v[102:103], v244 offset:1536\n\t" \
    "ds_read_b64_tr_b16 v[108:109], v244 offset:32\n\t" \
    "ds_read_b64_tr_b16 v[110:111], v244 offset:1568\n\t" \
    "ds_read_b64_tr_b16 v[116:117], v244 offset:64\n\t" \
    "ds_read_b64_tr_b16 v[118:119], v244 offset:1600\n\t" \
    "v_add_u32 v244, 0x18000, v243\n\t" \
    "s_waitcnt vmcnt(12) lgkmcnt(0)\n\t" \
    "s_barrier \n\t" \
    "s_add_u32 m0, %[wm], 0x12000\n\t" \
    "s_mov_b32 s44, 0x660000\n\t" \
    "buffer_load_dwordx4 v240, %[wr], s44 offen offset:0 nt lds\n\t" \
    "buffer_load_dwordx4 v241, %[wr], s44 offen offset:1024 nt lds\n\t" \
    "buffer_load_dwordx4 v242, %[wr], s44 offen offset:2048 nt lds\n\t" \
    "ds_read_b64_tr_b16 v[224:225], v244 offset:0\n\t" \
    "ds_read_b64_tr_b16 v[226:227], v244 offset:1536\n\t" \
    "ds_read_b64_tr_b16 v[120:121], v244 offset:32\n\t" \
    "ds_read_b64_tr_b16 v[122:123], v244 offset:1568\n\t" \
    "ds_read_b64_tr_b16 v[128:129], v244 offset:64\n\t" \
    "ds_read_b64_tr_b16 v[130:131], v244 offset:1600\n\t" \
    "v_add_u32 v244, 0x1e000, v243\n\t" \
    "s_waitcnt vmcnt(12) lgkmcnt(0)\n\t" \
    "s_barrier \n\t" \
    ".Ls41_%=:\n\t" \
    "v_readfirstlane_b32 s43, v246\n\t" \
    "s_nop 0\n\t" \
    "s_cmp_lg_u32 s43, -1\n\t" \
    "s_cbranch_scc1 .Ls42_%=\n\t" \
    "s_cmp_eq_u32 s41, 0\n\t" \
    "s_cbranch_scc1 .Ls42_%=\n\t" \
    "s_sub_u32 s41, s41, 1\n\t" \
    "s_sleep 4\n\t" \
    "v_mov_b32 v246, 0\n\t" \
    "buffer_load_dword v246, v246, s[60:63], %[mo] offen sc1\n\t" \
    "s_waitcnt vmcnt(0)\n\t" \
    "s_branch .Ls41_%=\n\t" \
    ".Ls42_%=:\n\t" \
    "v_mov_b32 v246, 0x8000\n\t" \
    "s_mov_b64 exec, 1\n\t" \
    "buffer_store_dword v246, v246, s[60:63], %[mo] offen sc1\n\t" \
    "s_mov_b64 exec, -1\n\t" \
    "s_and_b32 s44, s43, 0x5000\n\t" \
    "s_xor_b32 %[mb], %[mb], s44\n\t" \
    "s_xor_b32 %[fl], %[fl], s44\n\t" \
    "s_and_b32 s43, s43, 0x2000\n\t" \
    "s_xor_b32 s43, s43, %[lx]\n\t" \
    "s_add_u32 s60, s60, s43\n\t" \
    "s_addc_u32 s61, s61, 0\n\t" \
    "v_and_b32 v252, 0xf0, v253\n\t" \
    "v_lshlrev_b32 v252, 3, v252\n\t" \
    "buffer_load_dword v252, v252, s[60:63], %[fl] offen sc1\n\t" \
    "s_add_u32 m0, %[wm], 0x18000\n\t" \
    "s_mov_b32 s44, 0x6c0000\n\t" \
    "buffer_load_dwordx4 v240, %[wr], s44 offen offset:0 nt lds\n\t" \
    "buffer_load_dwordx4 v241, %[wr], s44 offen offset:1024 nt lds\n\t" \
    "buffer_load_dwordx4 v242, %[wr], s44 offen offset:2048 nt lds\n\t" \
    "ds_read_b64_tr_b16 v[228:229], v244 offset:0\n\t" \
    "ds_read_b64_tr_b16 v[230:231], v244 offset:1536\n\t" \
    "ds_read_b64_tr_b16 v[124:125], v244 offset:32\n\t" \
    "ds_read_b64_tr_b16 v[126:127], v244 offset:1568\n\t" \
    "ds_read_b64_tr_b16 v[132:133], v244 offset:64\n\t" \
    "ds_read_b64_tr_b16 v[134:135], v244 offset:1600\n\t" \
    "v_add_u32 v244, 0x0, v243\n\t" \
    "s_waitcnt vmcnt(12) lgkmcnt(0)\n\t" \
    "s_barrier \n\t" \
    "s_add_u32 m0, %[wm], 0x1e000\n\t" \
    "s_mov_b32 s44, 0x720000\n\t" \
    "buffer_load_dwordx4 v240, %[wr], s44 offen offset:0 nt lds\n\t" \
    "buffer_load_dwordx4 v241, %[wr], s44 offen offset:1024 nt lds\n\t" \
    "buffer_load_dwordx4 v242, %[wr], s44 offen offset:2048 nt lds\n\t" \
    "ds_read_b64_tr_b16 v[136:137], v244 offset:0\n\t" \
    "ds_read_b64_tr_b16 v[138:139], v244 offset:1536\n\t" \
    "ds_read_b64_tr_b16 v[144:145], v244 offset:32\n\t" \
    "ds_read_b64_tr_b16 v[146:147], v244 offset:1568\n\t" \
    "ds_read_b64_tr_b16 v[152:153], v244 offset:64\n\t" \
    "ds_read_b64_tr_b16 v[154:155], v244 offset:1600\n\t" \
    "v_add_u32 v244, 0x6000, v243\n\t" \
    "s_waitcnt vmcnt(12) lgkmcnt(0)\n\t" \
    "s_barrier \n\t" \
    "ds_read_b64_tr_b16 v[140:141], v244 offset:0\n\t" \
    "ds_read_b64_tr_b16 v[142:143], v244 offset:1536\n\t" \
    "ds_read_b64_tr_b16 v[148:149], v244 offset:32\n\t" \
    "ds_read_b64_tr_b16 v[150:151], v244 offset:1568\n\t" \
    "ds_read_b64_tr_b16 v[156:157], v244 offset:64\n\t" \
    "ds_read_b64_tr_b16 v[158:159], v244 offset:1600\n\t" \
    "v_add_u32 v244, 0xc000, v243\n\t" \
    "s_waitcnt vmcnt(9) lgkmcnt(0)\n\t" \
    "s_barrier \n\t" \
    "ds_read_b64_tr_b16 v[232:233], v244 offset:0\n\t" \
    "ds_read_b64_tr_b16 v[234:235], v244 offset:1536\n\t" \
    "ds_read_b64_tr_b16 v[160:161], v244 offset:32\n\t" \
    "ds_read_b64_tr_b16 v[162:163], v244 offset:1568\n\t" \
    "ds_read_b64_tr_b16 v[168:169], v244 offset:64\n\t" \
    "ds_read_b64_tr_b16 v[170:171], v244 offset:1600\n\t" \
    "v_add_u32 v244, 0x12000, v243\n\t" \
    "s_waitcnt vmcnt(6) lgkmcnt(0)\n\t" \
    "s_barrier \n\t" \
    "ds_read_b64_tr_b16 v[236:237], v244 offset:0\n\t" \
    "ds_read_b64_tr_b16 v[238:239], v244 offset:1536\n\t" \
    "ds_read_b64_tr_b16 v[164:165], v244 offset:32\n\t" \
    "ds_read_b64_tr_b16 v[166:167], v244 offset:1568\n\t" \
    "ds_read_b64_tr_b16 v[172:173], v244 offset:64\n\t" \
    "ds_read_b64_tr_b16 v[174:175], v244 offset:1600\n\t" \
    "v_add_u32 v244, 0x18000, v243\n\t" \
    "s_waitcnt vmcnt(3) lgkmcnt(0)\n\t" \
    "s_barrier \n\t" \
    "ds_read_b64_tr_b16 v[176:177], v244 offset:0\n\t" \
    "ds_read_b64_tr_b16 v[178:179], v244 offset:1536\n\t" \
    "ds_read_b64_tr_b16 v[184:185], v244 offset:32\n\t" \
    "ds_read_b64_tr_b16 v[186:187], v244 offset:1568\n\t" \
    "ds_read_b64_tr_b16 v[192:193], v244 offset:64\n\t" \
    "ds_read_b64_tr_b16 v[194:195], v244 offset:1600\n\t" \
    "s_mov_b32 s43, 0\n\t" \
    "v_cmp_eq_u32 vcc, 0, v252\n\t" \
    "s_cbranch_vccnz .Ls43_%=\n\t" \
    "s_mov_b32 m0, %[m0]\n\t" \
    "s_add_u32 s45, s42, %[x1]\n\t" \
    "buffer_load_dwordx4 v253, s[60:63], s42 offen offset:0 sc1 lds\n\t" \
    "buffer_load_dwordx4 v253, s[60:63], s42 offen offset:1024 sc1 lds\n\t" \
    "buffer_load_dwordx4 v253, s[60:63], s42 offen offset:2048 sc1 lds\n\t" \
    "buffer_load_dwordx4 v253, s[60:63], s42 offen offset:3072 sc1 lds\n\t" \
    "s_mov_b32 m0, %[m1]\n\t" \
    "s_nop 0\n\t" \
    "buffer_load_dwordx4 v253, s[60:63], s45 offen offset:0 sc1 lds\n\t" \
    "buffer_load_dwordx4 v253, s[60:63], s45 offen offset:1024 sc1 lds\n\t" \
    "s_mov_b32 s43, 1\n\t" \
    ".Ls43_%=:\n\t" \
    "v_add_u32 v244, 0x1e000, v243\n\t" \
    "s_waitcnt vmcnt(0) lgkmcnt(0)\n\t" \
    "s_barrier \n\t" \
    "ds_read_b64_tr_b16 v[180:181], v244 offset:0\n\t" \
    "ds_read_b64_tr_b16 v[182:183], v244 offset:1536\n\t" \
    "ds_read_b64_tr_b16 v[188:189], v244 offset:32\n\t" \
    "ds_read_b64_tr_b16 v[190:191], v244 offset:1568\n\t" \
    "ds_read_b64_tr_b16 v[196:197], v244 offset:64\n\t" \
    "ds_read_b64_tr_b16 v[198:199], v244 offset:1600\n\t" \
    "s_waitcnt lgkmcnt(0)\n\t" \
    "s_barrier \n\t" \
    LST0 \
    LJIT \
    "ds_write_b128 v254, v[200:203] offset:0\n\t" \
    "ds_write_b128 v254, v[204:207] offset:1024\n\t" \
    "ds_write_b128 v254, v[208:211] offset:2048\n\t" \
    "ds_write_b128 v254, v[212:215] offset:3072\n\t" \
    "ds_write_b128 v254, v[216:219] offset:4096\n\t" \
    "ds_write_b128 v254, v[220:223] offset:5120\n\t" \
    "ds_write_b128 v254, v[224:227] offset:6144\n\t" \
    "ds_write_b128 v254, v[228:231] offset:7168\n\t" \
    "ds_write_b128 v254, v[232:235] offset:8192\n\t" \
    "ds_write_b128 v254, v[236:239] offset:9216\n\t" \
    "s_cmp_lg_u32 s43, 0\n\t" \
    "s_cbranch_scc1 .Ls44_%=\n\t" \
    "s_mov_b32 s44, 0\n\t" \
    ".Ls45_%=:\n\t" \
    "v_cmp_ge_u32 vcc, s44, v252\n\t" \
    "s_cbranch_vccz .Ls46_%=\n\t" \
    "s_cmp_eq_u32 s41, 0\n\t" \
    "s_cbranch_scc1 .Ls46_%=\n\t" \
    "s_sub_u32 s41, s41, 1\n\t" \
    "s_sleep 8\n\t" \
    "v_and_b32 v252, 0xf0, v253\n\t" \
    "v_lshlrev_b32 v252, 3, v252\n\t" \
    "buffer_load_dword v252, v252, s[60:63], %[fl] offen sc1\n\t" \
    "s_waitcnt vmcnt(0)\n\t" \
    "s_branch .Ls45_%=\n\t" \
    ".Ls46_%=:\n\t" \
    "s_mov_b32 m0, %[m0]\n\t" \
    "s_add_u32 s45, s42, %[x1]\n\t" \
    "buffer_load_dwordx4 v253, s[60:63], s42 offen offset:0 sc1 lds\n\t" \
    "buffer_load_dwordx4 v253, s[60:63], s42 offen offset:1024 sc1 lds\n\t" \
    "buffer_load_dwordx4 v253, s[60:63], s42 offen offset:2048 sc1 lds\n\t" \
    "buffer_load_dwordx4 v253, s[60:63], s42 offen offset:3072 sc1 lds\n\t" \
    "s_mov_b32 m0, %[m1]\n\t" \
    "s_nop 0\n\t" \
    "buffer_load_dwordx4 v253, s[60:63], s45 offen offset:0 sc1 lds\n\t" \
    "buffer_load_dwordx4 v253, s[60:63], s45 offen offset:1024 sc1 lds\n\t" \
    ".Ls44_%=:\n\t" \
    "s_waitcnt vmcnt(0) lgkmcnt(0)\n\t" \
    "s_barrier \n\t" \
    "ds_read_b128 v[224:227], v253 offset:0\n\t" \
    "ds_read_b128 v[228:231], v253 offset:20480\n\t" \
    "ds_read_b128 v[232:235], v253 offset:1024\n\t" \
    "ds_read_b128 v[236:239], v253 offset:21504\n\t" \
    "ds_read_b128 v[244:247], v254 offset:0\n\t" \
    "ds_read_b128 v[248:251], v254 offset:1024\n\t" \
    ".p2align 8\n\t" \
    ".Ls47_%=:\n\t" \
    "s_cmp_lg_u32 %[wave], 4\n\t" \
    "s_cbranch_scc1 .Ls48_%=\n\t" \
    "s_sleep 6\n\t" \
    ".Ls48_%=:\n\t" \
    "s_waitcnt lgkmcnt(5)\n\t" \
    "v_mfma_f32_16x16x32_bf16 v[204:207], v[0:3], v[224:227], 0\n\t" \
    "ds_read_b128 v[240:243], v253 offset:2048\n\t" \
    "v_mfma_f32_16x16x32_bf16 v[208:211], v[8:11], v[224:227], 0\n\t" \
    "s_waitcnt lgkmcnt(2)\n\t" \
    "v_mfma_f32_16x16x32_bf16 v[200:203], v[244:247], v[224:227], 0\n\t" \
    "v_mfma_f32_16x16x32_bf16 v[216:219], v[0:3], v[228:231], 0\n\t" \
    "ds_read_b128 v[224:227], v253 offset:22528\n\t" \
    "v_mfma_f32_16x16x32_bf16 v[220:223], v[8:11], v[228:231], 0\n\t" \
    "v_mfma_f32_16x16x32_bf16 v[212:215], v[244:247], v[228:231], 0\n\t" \
    "ds_read_b128 v[244:247], v254 offset:2048\n\t" \
    "v_mfma_f32_16x16x32_bf16 v[204:207], v[4:7], v[232:235], v[204:207]\n\t" \
    "ds_read_b128 v[228:231], v253 offset:3072\n\t" \
    "s_add_u32 m0, %[m0], 0x3000\n\t" \
    "v_mfma_f32_16x16x32_bf16 v[208:211], v[12:15], v[232:235], v[208:211]\n\t" \
    "s_add_u32 s45, s42, 0xc000\n\t" \
    "s_waitcnt lgkmcnt(4)\n\t" \
    "v_mfma_f32_16x16x32_bf16 v[200:203], v[248:251], v[232:235], v[200:203]\n\t" \
    "buffer_load_dwordx4 v253, s[60:63], s45 offen offset:0 sc1 lds\n\t" \
    "v_mfma_f32_16x16x32_bf16 v[216:219], v[4:7], v[236:239], v[216:219]\n\t" \
    "ds_read_b128 v[232:235], v253 offset:23552\n\t" \
    "v_mfma_f32_16x16x32_bf16 v[220:223], v[12:15], v[236:239], v[220:223]\n\t" \
    "v_mfma_f32_16x16x32_bf16 v[212:215], v[248:251], v[236:239], v[212:215]\n\t" \
    "ds_read_b128 v[248:251], v254 offset:3072\n\t" \
    "s_waitcnt lgkmcnt(5)\n\t" \
    "v_mfma_f32_16x16x32_bf16 v[204:207], v[24:27], v[240:243], v[204:207]\n\t" \
    "ds_read_b128 v[236:239], v253 offset:4096\n\t" \
    "buffer_load_dwordx4 v253, s[60:63], s45 offen offset:1024 sc1 lds\n\t" \
    "v_mfma_f32_16x16x32_bf16 v[208:211], v[32:35], v[240:243], v[208:211]\n\t" \
    "v_mfma_f32_16x16x32_bf16 v[200:203], v[16:19], v[240:243], v[200:203]\n\t" \
    "s_waitcnt lgkmcnt(5)\n\t" \
    "v_mfma_f32_16x16x32_bf16 v[216:219], v[24:27], v[224:227], v[216:219]\n\t" \
    "ds_read_b128 v[240:243], v253 offset:24576\n\t" \
    "v_mfma_f32_16x16x32_bf16 v[220:223], v[32:35], v[224:227], v[220:223]\n\t" \
    "buffer_load_dwordx4 v253, s[60:63], s45 offen offset:2048 sc1 lds\n\t" \
    "v_mfma_f32_16x16x32_bf16 v[212:215], v[16:19], v[224:227], v[212:215]\n\t" \
    "s_waitcnt lgkmcnt(4)\n\t" \
    "v_mfma_f32_16x16x32_bf16 v[204:207], v[28:31], v[228:231], v[204:207]\n\t" \
    "ds_read_b128 v[224:227], v253 offset:5120\n\t" \
    "v_mfma_f32_16x16x32_bf16 v[208:211], v[36:39], v[228:231], v[208:211]\n\t" \
    "v_mfma_f32_16x16x32_bf16 v[200:203], v[20:23], v[228:231], v[200:203]\n\t" \
    "buffer_load_dwordx4 v253, s[60:63], s45 offen offset:3072 sc1 lds\n\t" \
    "s_waitcnt lgkmcnt(4)\n\t" \
    "v_mfma_f32_16x16x32_bf16 v[216:219], v[28:31], v[232:235], v[216:219]\n\t" \
    "ds_read_b128 v[228:231], v253 offset:25600\n\t" \
    "v_mfma_f32_16x16x32_bf16 v[220:223], v[36:39], v[232:235], v[220:223]\n\t" \
    "v_mfma_f32_16x16x32_bf16 v[212:215], v[20:23], v[232:235], v[212:215]\n\t" \
    "s_waitcnt lgkmcnt(3)\n\t" \
    "v_mfma_f32_16x16x32_bf16 v[204:207], v[40:43], v[236:239], v[204:207]\n\t" \
    "ds_read_b128 v[232:235], v253 offset:6144\n\t" \
    "v_mfma_f32_16x16x32_bf16 v[208:211], v[48:51], v[236:239], v[208:211]\n\t" \
    "v_mfma_f32_16x16x32_bf16 v[200:203], v[244:247], v[236:239], v[200:203]\n\t" \
    "s_waitcnt lgkmcnt(3)\n\t" \
    "v_mfma_f32_16x16x32_bf16 v[216:219], v[40:43], v[240:243], v[216:219]\n\t" \
    "ds_read_b128 v[236:239], v253 offset:26624\n\t" \
    "v_mfma_f32_16x16x32_bf16 v[220:223], v[48:51], v[240:243], v[220:223]\n\t" \
    "v_mfma_f32_16x16x32_bf16 v[212:215], v[244:247], v[240:243], v[212:215]\n\t" \
    "ds_read_b128 v[244:247], v254 offset:4096\n\t" \
    "s_waitcnt lgkmcnt(4)\n\t" \
    "v_mfma_f32_16x16x32_bf16 v[204:207], v[44:47], v[224:227], v[204:207]\n\t" \
    "ds_read_b128 v[240:243], v253 offset:7168\n\t" \
    "v_mfma_f32_16x16x32_bf16 v[208:211], v[52:55], v[224:227], v[208:211]\n\t" \
    "v_mfma_f32_16x16x32_bf16 v[200:203], v[248:251], v[224:227], v[200:203]\n\t" \
    "s_waitcnt lgkmcnt(4)\n\t" \
    "v_mfma_f32_16x16x32_bf16 v[216:219], v[44:47], v[228:231], v[216:219]\n\t" \
    "ds_read_b128 v[224:227], v253 offset:27648\n\t" \
    "v_mfma_f32_16x16x32_bf16 v[220:223], v[52:55], v[228:231], v[220:223]\n\t" \
    "v_mfma_f32_16x16x32_bf16 v[212:215], v[248:251], v[228:231], v[212:215]\n\t" \
    "ds_read_b128 v[248:251], v254 offset:5120\n\t" \
    "s_waitcnt lgkmcnt(5)\n\t" \
    "v_mfma_f32_16x16x32_bf16 v[204:207], v[64:67], v[232:235], v[204:207]\n\t" \
    "ds_read_b128 v[228:231], v253 offset:8192\n\t" \
    "v_mfma_f32_16x16x32_bf16 v[208:211], v[72:75], v[232:235], v[208:211]\n\t" \
    "v_mfma_f32_16x16x32_bf16 v[200:203], v[56:59], v[232:235], v[200:203]\n\t" \
    "s_waitcnt lgkmcnt(5)\n\t" \
    "v_mfma_f32_16x16x32_bf16 v[216:219], v[64:67], v[236:239], v[216:219]\n\t" \
    "ds_read_b128 v[232:235], v253 offset:28672\n\t" \
    "v_mfma_f32_16x16x32_bf16 v[220:223], v[72:75], v[236:239], v[220:223]\n\t" \
    "v_mfma_f32_16x16x32_bf16 v[212:215], v[56:59], v[236:239], v[212:215]\n\t" \
    "s_waitcnt lgkmcnt(4)\n\t" \
    "v_mfma_f32_16x16x32_bf16 v[204:207], v[68:71], v[240:243], v[204:207]\n\t" \
    "ds_read_b128 v[236:239], v253 offset:9216\n\t" \
    "v_mfma_f32_16x16x32_bf16 v[208:211], v[76:79], v[240:243], v[208:211]\n\t" \
    "v_mfma_f32_16x16x32_bf16 v[200:203], v[60:63], v[240:243], v[200:203]\n\t" \
    "v_and_b32 v252, 0xf0, v253\n\t" \
    "v_lshlrev_b32 v252, 3, v252\n\t" \
    "buffer_load_dword v252, v252, s[60:63], %[fl] offen sc1\n\t" \
    "s_waitcnt lgkmcnt(4)\n\t" \
    "v_mfma_f32_16x16x32_bf16 v[216:219], v[68:71], v[224:227], v[216:219]\n\t" \
    "ds_read_b128 v[240:243], v253 offset:29696\n\t" \
    "v_mfma_f32_16x16x32_bf16 v[220:223], v[76:79], v[224:227], v[220:223]\n\t" \
    "v_mfma_f32_16x16x32_bf16 v[212:215], v[60:63], v[224:227], v[212:215]\n\t" \
    "s_waitcnt lgkmcnt(3)\n\t" \
    "v_mfma_f32_16x16x32_bf16 v[204:207], v[80:83], v[228:231], v[204:207]\n\t" \
    "ds_read_b128 v[224:227], v253 offset:10240\n\t" \
    "v_mfma_f32_16x16x32_bf16 v[208:211], v[88:91], v[228:231], v[208:211]\n\t" \
    "v_mfma_f32_16x16x32_bf16 v[200:203], v[244:247], v[228:231], v[200:203]\n\t" \
    "s_waitcnt lgkmcnt(3)\n\t" \
    "v_mfma_f32_16x16x32_bf16 v[216:219], v[80:83], v[232:235], v[216:219]\n\t" \
    "ds_read_b128 v[228:231], v253 offset:30720\n\t" \
    "s_add_u32 s44, s40, 1\n\t" \
    "v_mfma_f32_16x16x32_bf16 v[220:223], v[88:91], v[232:235], v[220:223]\n\t" \
    "s_and_b32 s45, s44, 15\n\t" \
    "v_mfma_f32_16x16x32_bf16 v[212:215], v[244:247], v[232:235], v[212:215]\n\t" \
    "ds_read_b128 v[244:247], v254 offset:6144\n\t" \
    "s_cmp_eq_u32 s45, 0\n\t" \
    "s_waitcnt lgkmcnt(4)\n\t" \
    "v_mfma_f32_16x16x32_bf16 v[204:207], v[84:87], v[236:239], v[204:207]\n\t" \
    "ds_read_b128 v[232:235], v253 offset:11264\n\t" \
    "s_cselect_b32 s45, %[cj], %[cs]\n\t" \
    "v_mfma_f32_16x16x32_bf16 v[208:211], v[92:95], v[236:239], v[208:211]\n\t" \
    "s_cmp_eq_u32 s44, 64\n\t" \
    "v_mfma_f32_16x16x32_bf16 v[200:203], v[248:251], v[236:239], v[200:203]\n\t" \
    "s_cselect_b32 s45, 0, s45\n\t" \
    "s_waitcnt lgkmcnt(4)\n\t" \
    "v_mfma_f32_16x16x32_bf16 v[216:219], v[84:87], v[240:243], v[216:219]\n\t" \
    "ds_read_b128 v[236:239], v253 offset:31744\n\t" \
    "s_add_u32 s42, s42, s45\n\t" \
    "v_mfma_f32_16x16x32_bf16 v[220:223], v[92:95], v[240:243], v[220:223]\n\t" \
    "v_mfma_f32_16x16x32_bf16 v[212:215], v[248:251], v[240:243], v[212:215]\n\t" \
    "ds_read_b128 v[248:251], v254 offset:7168\n\t" \
    "s_setprio 1\n\t" \
    "s_waitcnt vmcnt(0) lgkmcnt(0)\n\t" \
    "s_barrier \n\t" \
    LST7 \
    LJIT \
    "s_add_u32 s44, s40, 1\n\t" \
    "s_min_u32 s44, s44, 63\n\t" \
    ".Ls49_%=:\n\t" \
    "v_cmp_ge_u32 vcc, s44, v252\n\t" \
    "s_cbranch_vccz .Ls410_%=\n\t" \
    "s_cmp_eq_u32 s41, 0\n\t" \
    "s_cbranch_scc1 .Ls410_%=\n\t" \
    "s_sub_u32 s41, s41, 1\n\t" \
    "s_sleep 2\n\t" \
    "v_and_b32 v252, 0xf0, v253\n\t" \
    "v_lshlrev_b32 v252, 3, v252\n\t" \
    "buffer_load_dword v252, v252, s[60:63], %[fl] offen sc1\n\t" \
    "s_waitcnt vmcnt(0)\n\t" \
    "s_branch .Ls49_%=\n\t" \
    ".Ls410_%=:\n\t" \
    "s_mov_b32 m0, %[m0]\n\t" \
    "s_add_u32 s45, s42, %[x1]\n\t" \
    "buffer_load_dwordx4 v253, s[60:63], s42 offen offset:0 sc1 lds\n\t" \
    "buffer_load_dwordx4 v253, s[60:63], s42 offen offset:1024 sc1 lds\n\t" \
    "buffer_load_dwordx4 v253, s[60:63], s42 offen offset:2048 sc1 lds\n\t" \
    "buffer_load_dwordx4 v253, s[60:63], s42 offen offset:3072 sc1 lds\n\t" \
    "s_mov_b32 m0, %[m1]\n\t" \
    "s_nop 0\n\t" \
    "buffer_load_dwordx4 v253, s[60:63], s45 offen offset:0 sc1 lds\n\t" \
    "buffer_load_dwordx4 v253, s[60:63], s45 offen offset:1024 sc1 lds\n\t" \
    "v_mfma_f32_16x16x32_bf16 v[204:207], v[104:107], v[224:227], v[204:207]\n\t" \
    "ds_read_b128 v[240:243], v253 offset:12288\n\t" \
    "v_mfma_f32_16x16x32_bf16 v[208:211], v[112:115], v[224:227], v[208:211]\n\t" \
    "v_mfma_f32_16x16x32_bf16 v[200:203], v[96:99], v[224:227], v[200:203]\n\t" \
    "v_mfma_f32_16x16x32_bf16 v[216:219], v[104:107], v[228:231], v[216:219]\n\t" \
    "ds_read_b128 v[224:227], v253 offset:32768\n\t" \
    "v_mfma_f32_16x16x32_bf16 v[220:223], v[112:115], v[228:231], v[220:223]\n\t" \
    "v_mfma_f32_16x16x32_bf16 v[212:215], v[96:99], v[228:231], v[212:215]\n\t" \
    "v_mfma_f32_16x16x32_bf16 v[204:207], v[108:111], v[232:235], v[204:207]\n\t" \
    "ds_read_b128 v[228:231], v253 offset:13312\n\t" \
    "v_mfma_f32_16x16x32_bf16 v[208:211], v[116:119], v[232:235], v[208:211]\n\t" \
    "v_mfma_f32_16x16x32_bf16 v[200:203], v[100:103], v[232:235], v[200:203]\n\t" \
    "v_mfma_f32_16x16x32_bf16 v[216:219], v[108:111], v[236:239], v[216:219]\n\t" \
    "ds_read_b128 v[232:235], v253 offset:33792\n\t" \
    "v_mfma_f32_16x16x32_bf16 v[220:223], v[116:119], v[236:239], v[220:223]\n\t" \
    "v_mfma_f32_16x16x32_bf16 v[212:215], v[100:103], v[236:239], v[212:215]\n\t" \
    "s_waitcnt lgkmcnt(3)\n\t" \
    "v_mfma_f32_16x16x32_bf16 v[204:207], v[120:123], v[240:243], v[204:207]\n\t" \
    "ds_read_b128 v[236:239], v253 offset:16384\n\t" \
    "v_mfma_f32_16x16x32_bf16 v[208:211], v[128:131], v[240:243], v[208:211]\n\t" \
    "v_mfma_f32_16x16x32_bf16 v[200:203], v[244:247], v[240:243], v[200:203]\n\t" \
    "s_waitcnt lgkmcnt(3)\n\t" \
    "v_mfma_f32_16x16x32_bf16 v[216:219], v[120:123], v[224:227], v[216:219]\n\t" \
    "ds_read_b128 v[240:243], v253 offset:36864\n\t" \
    "v_mfma_f32_16x16x32_bf16 v[220:223], v[128:131], v[224:227], v[220:223]\n\t" \
    "v_mfma_f32_16x16x32_bf16 v[212:215], v[244:247], v[224:227], v[212:215]\n\t" \
    "ds_read_b128 v[244:247], v254 offset:8192\n\t" \
    "s_waitcnt lgkmcnt(4)\n\t" \
    "v_mfma_f32_16x16x32_bf16 v[204:207], v[124:127], v[228:231], v[204:207]\n\t" \
    "ds_read_b128 v[224:227], v253 offset:17408\n\t" \
    "v_mfma_f32_16x16x32_bf16 v[208:211], v[132:135], v[228:231], v[208:211]\n\t" \
    "v_mfma_f32_16x16x32_bf16 v[200:203], v[248:251], v[228:231], v[200:203]\n\t" \
    "s_waitcnt lgkmcnt(4)\n\t" \
    "v_mfma_f32_16x16x32_bf16 v[216:219], v[124:127], v[232:235], v[216:219]\n\t" \
    "ds_read_b128 v[228:231], v253 offset:37888\n\t" \
    "v_mfma_f32_16x16x32_bf16 v[220:223], v[132:135], v[232:235], v[220:223]\n\t" \
    "v_mfma_f32_16x16x32_bf16 v[212:215], v[248:251], v[232:235], v[212:215]\n\t" \
    "ds_read_b128 v[248:251], v254 offset:9216\n\t" \
    "s_waitcnt lgkmcnt(5)\n\t" \
    "v_mfma_f32_16x16x32_bf16 v[204:207], v[160:163], v[236:239], v[204:207]\n\t" \
    "ds_read_b128 v[232:235], v253 offset:14336\n\t" \
    "v_mfma_f32_16x16x32_bf16 v[208:211], v[168:171], v[236:239], v[208:211]\n\t" \
    "s_waitcnt lgkmcnt(4)\n\t" \
    "v_mfma_f32_16x16x32_bf16 v[200:203], v[244:247], v[236:239], v[200:203]\n\t" \
    "v_mfma_f32_16x16x32_bf16 v[216:219], v[160:163], v[240:243], v[216:219]\n\t" \
    "ds_read_b128 v[236:239], v253 offset:15360\n\t" \
    "v_mfma_f32_16x16x32_bf16 v[220:223], v[168:171], v[240:243], v[220:223]\n\t" \
    "v_mfma_f32_16x16x32_bf16 v[212:215], v[244:247], v[240:243], v[212:215]\n\t" \
    "s_waitcnt lgkmcnt(4)\n\t" \
    "v_mfma_f32_16x16x32_bf16 v[204:207], v[164:167], v[224:227], v[204:207]\n\t" \
    "ds_read_b128 v[240:243], v253 offset:18432\n\t" \
    "v_add_u32 v252, %[q2], v253\n\t" \
    "v_mfma_f32_16x16x32_bf16 v[208:211], v[172:175], v[224:227], v[208:211]\n\t" \
    "s_waitcnt lgkmcnt(3)\n\t" \
    "v_mfma_f32_16x16x32_bf16 v[200:203], v[248:251], v[224:227], v[200:203]\n\t" \
    "v_mfma_f32_16x16x32_bf16 v[216:219], v[164:167], v[228:231], v[216:219]\n\t" \
    "ds_read_b128 v[224:227], v253 offset:19456\n\t" \
    "v_mfma_f32_16x16x32_bf16 v[220:223], v[172:175], v[228:231], v[220:223]\n\t" \
    "v_mfma_f32_16x16x32_bf16 v[212:215], v[248:251], v[228:231], v[212:215]\n\t" \
    "s_waitcnt lgkmcnt(3)\n\t" \
    "v_mfma_f32_16x16x32_bf16 v[204:207], v[144:147], v[232:235], v[204:207]\n\t" \
    "ds_read_b128 v[228:231], v253 offset:34816\n\t" \
    "v_mov_b64 v[244:245], 0\n\t" \
    "v_mfma_f32_16x16x32_bf16 v[208:211], v[152:155], v[232:235], v[208:211]\n\t" \
    "v_mov_b64 v[246:247], 0\n\t" \
    "v_mfma_f32_16x16x32_bf16 v[200:203], v[136:139], v[232:235], v[200:203]\n\t" \
    "v_mov_b64 v[248:249], 0\n\t" \
    "s_waitcnt lgkmcnt(3)\n\t" \
    "v_mfma_f32_16x16x32_bf16 v[204:207], v[148:151], v[236:239], v[204:207]\n\t" \
    "ds_read_b128 v[232:235], v253 offset:35840\n\t" \
    "v_mov_b64 v[250:251], 0\n\t" \
    "v_mfma_f32_16x16x32_bf16 v[208:211], v[156:159], v[236:239], v[208:211]\n\t" \
    "v_mfma_f32_16x16x32_bf16 v[200:203], v[140:143], v[236:239], v[200:203]\n\t" \
    "s_waitcnt lgkmcnt(3)\n\t" \
    "v_mfma_f32_16x16x32_bf16 v[204:207], v[184:187], v[240:243], v[204:207]\n\t" \
    "ds_read_b128 v[236:239], v253 offset:38912\n\t" \
    "v_mfma_f32_16x16x32_bf16 v[208:211], v[192:195], v[240:243], v[208:211]\n\t" \
    "v_mfma_f32_16x16x32_bf16 v[200:203], v[176:179], v[240:243], v[200:203]\n\t" \
    "s_waitcnt lgkmcnt(3)\n\t" \
    "v_mfma_f32_16x16x32_bf16 v[204:207], v[188:191], v[224:227], v[204:207]\n\t" \
    "ds_read_b128 v[240:243], v253 offset:39936\n\t" \
    "v_mfma_f32_16x16x32_bf16 v[208:211], v[196:199], v[224:227], v[208:211]\n\t" \
    "v_mfma_f32_16x16x32_bf16 v[200:203], v[180:183], v[224:227], v[200:203]\n\t" \
    "s_waitcnt lgkmcnt(3)\n\t" \
    "v_mfma_f32_16x16x32_bf16 v[216:219], v[144:147], v[228:231], v[216:219]\n\t" \
    "s_waitcnt lgkmcnt(2)\n\t" \
    "v_mfma_f32_16x16x32_bf16 v[216:219], v[148:151], v[232:235], v[216:219]\n\t" \
    "v_cvt_pk_bf16_f32 v204, v204, v208\n\t" \
    "v_cvt_pk_bf16_f32 v205, v205, v209\n\t" \
    "s_waitcnt lgkmcnt(1)\n\t" \
    "v_mfma_f32_16x16x32_bf16 v[216:219], v[184:187], v[236:239], v[216:219]\n\t" \
    "v_cvt_pk_bf16_f32 v206, v206, v210\n\t" \
    "v_cvt_pk_bf16_f32 v207, v207, v211\n\t" \
    "s_waitcnt lgkmcnt(0)\n\t" \
    "v_mfma_f32_16x16x32_bf16 v[216:219], v[188:191], v[240:243], v[216:219]\n\t" \
    "ds_write_b128 v252, v[204:207] offset:0\n\t" \
    "v_mfma_f32_16x16x32_bf16 v[220:223], v[152:155], v[228:231], v[220:223]\n\t" \
    "v_mfma_f32_16x16x32_bf16 v[220:223], v[156:159], v[232:235], v[220:223]\n\t" \
    "v_add_u32 v252, %[qs], v253\n\t" \
    "v_mfma_f32_16x16x32_bf16 v[220:223], v[192:195], v[236:239], v[220:223]\n\t" \
    "v_cvt_pk_bf16_f32 v200, v200, v201\n\t" \
    "v_cvt_pk_bf16_f32 v201, v202, v203\n\t" \
    "v_mfma_f32_16x16x32_bf16 v[220:223], v[196:199], v[240:243], v[220:223]\n\t" \
    "ds_write_b64 v252, v[200:201] offset:0\n\t" \
    "v_mfma_f32_16x16x32_bf16 v[212:215], v[136:139], v[228:231], v[212:215]\n\t" \
    "v_mfma_f32_16x16x32_bf16 v[212:215], v[140:143], v[232:235], v[212:215]\n\t" \
    "v_mov_b64 v[208:209], 0\n\t" \
    "v_mfma_f32_16x16x32_bf16 v[212:215], v[176:179], v[236:239], v[212:215]\n\t" \
    "v_mov_b64 v[210:211], 0\n\t" \
    "v_add_u32 v252, %[q2], v253\n\t" \
    "v_mfma_f32_16x16x32_bf16 v[212:215], v[180:183], v[240:243], v[212:215]\n\t" \
    "v_cvt_pk_bf16_f32 v216, v216, v220\n\t" \
    "v_cvt_pk_bf16_f32 v217, v217, v221\n\t" \
    "v_cvt_pk_bf16_f32 v218, v218, v222\n\t" \
    "v_cvt_pk_bf16_f32 v219, v219, v223\n\t" \
    "ds_write_b128 v252, v[216:219] offset:6144\n\t" \
    "v_add_u32 v252, %[qs], v253\n\t" \
    "s_nop 1\n\t" \
    "v_cvt_pk_bf16_f32 v212, v212, v213\n\t" \
    "v_cvt_pk_bf16_f32 v213, v214, v215\n\t" \
    "ds_write_b64 v252, v[212:213] offset:6144\n\t" \
    LST6 \
    LJIT \
    "s_setprio 2\n\t" \
    "s_waitcnt vmcnt(0) lgkmcnt(0)\n\t" \
    "s_barrier \n\t" \
    LST3 \
    LJIT \
    "ds_read_b128 v[224:227], v253 offset:40960\n\t" \
    "ds_read_b128 v[228:231], v253 offset:41984\n\t" \
    "ds_read_b128 v[232:235], v253 offset:43008\n\t" \
    "ds_read_b128 v[236:239], v253 offset:44032\n\t" \
    "ds_read_b128 v[240:243], v253 offset:45056\n\t" \
    "ds_read_b128 v[200:203], v254 offset:0\n\t" \
    "ds_read_b128 v[204:207], v254 offset:1024\n\t" \
    "ds_read_b128 v[216:219], v254 offset:2048\n\t" \
    "ds_read_b128 v[220:223], v254 offset:3072\n\t" \
    LFLUSH212 \
    "v_bfe_i32 v212, v253, 5, 1\n\t" \
    "v_and_b32 v212, 0xaaaa, v212\n\t" \
    "v_or_b32 v212, 0x4444, v212\n\t" \
    "v_and_b32 v213, 0xffff, v255\n\t" \
    "v_lshrrev_b32 v214, 16, v255\n\t" \
    "s_waitcnt lgkmcnt(8)\n\t" \
    "v_smfmac_f32_16x16x64_bf16 v[244:247], v[224:227], v[0:7], v212\n\t" \
    "v_smfmac_f32_16x16x64_bf16 v[248:251], v[224:227], v[8:15], v212\n\t" \
    "s_waitcnt lgkmcnt(2)\n\t" \
    "v_smfmac_f32_16x16x64_bf16 v[208:211], v[224:227], v[200:207], v212\n\t" \
    "ds_read_b128 v[200:203], v254 offset:4096\n\t" \
    "ds_read_b128 v[204:207], v254 offset:5120\n\t" \
    "ds_read_b128 v[224:227], v253 offset:46080\n\t" \
    "v_smfmac_f32_16x16x64_bf16 v[208:211], v[228:231], v[16:23], v212\n\t" \
    "v_smfmac_f32_16x16x64_bf16 v[244:247], v[228:231], v[24:31], v212\n\t" \
    "v_smfmac_f32_16x16x64_bf16 v[248:251], v[228:231], v[32:39], v212\n\t" \
    "ds_read_b128 v[228:231], v253 offset:47104\n\t" \
    "s_waitcnt lgkmcnt(4)\n\t" \
    "v_smfmac_f32_16x16x64_bf16 v[208:211], v[232:235], v[216:223], v212\n\t" \
    "ds_read_b128 v[216:219], v254 offset:6144\n\t" \
    "ds_read_b128 v[220:223], v254 offset:7168\n\t" \
    "v_smfmac_f32_16x16x64_bf16 v[244:247], v[232:235], v[40:47], v212\n\t" \
    "v_smfmac_f32_16x16x64_bf16 v[248:251], v[232:235], v[48:55], v212\n\t" \
    "ds_read_b128 v[232:235], v253 offset:48128\n\t" \
    "v_smfmac_f32_16x16x64_bf16 v[208:211], v[236:239], v[56:63], v212\n\t" \
    "v_smfmac_f32_16x16x64_bf16 v[244:247], v[236:239], v[64:71], v212\n\t" \
    "v_smfmac_f32_16x16x64_bf16 v[248:251], v[236:239], v[72:79], v212\n\t" \
    "ds_read_b128 v[236:239], v253 offset:49152\n\t" \
    "s_waitcnt lgkmcnt(6)\n\t" \
    "v_smfmac_f32_16x16x64_bf16 v[208:211], v[240:243], v[200:207], v212\n\t" \
    "ds_read_b128 v[200:203], v254 offset:8192\n\t" \
    "ds_read_b128 v[204:207], v254 offset:9216\n\t" \
    "v_smfmac_f32_16x16x64_bf16 v[244:247], v[240:243], v[80:87], v212\n\t" \
    "v_smfmac_f32_16x16x64_bf16 v[248:251], v[240:243], v[88:95], v212\n\t" \
    "ds_read_b128 v[240:243], v253 offset:50176\n\t" \
    "s_waitcnt lgkmcnt(8)\n\t" \
    "v_smfmac_f32_16x16x64_bf16 v[208:211], v[224:227], v[96:103], v212\n\t" \
    "v_smfmac_f32_16x16x64_bf16 v[244:247], v[224:227], v[104:111], v212\n\t" \
    "v_smfmac_f32_16x16x64_bf16 v[248:251], v[224:227], v[112:119], v212\n\t" \
    "s_waitcnt lgkmcnt(5)\n\t" \
    "v_smfmac_f32_16x16x64_bf16 v[208:211], v[228:231], v[216:223], v212\n\t" \
    "s_waitcnt lgkmcnt(4)\n\t" \
    "v_smfmac_f32_16x16x64_bf16 v[208:211], v[232:235], v[136:143], v212\n\t" \
    "s_waitcnt lgkmcnt(1)\n\t" \
    "v_smfmac_f32_16x16x64_bf16 v[208:211], v[236:239], v[200:207], v212\n\t" \
    "s_waitcnt lgkmcnt(0)\n\t" \
    "v_smfmac_f32_16x16x64_bf16 v[208:211], v[240:243], v[176:183], v212\n\t" \
    "v_smfmac_f32_16x16x64_bf16 v[244:247], v[228:231], v[120:127], v212\n\t" \
    "v_smfmac_f32_16x16x64_bf16 v[244:247], v[232:235], v[144:151], v212\n\t" \
    "v_smfmac_f32_16x16x64_bf16 v[244:247], v[236:239], v[160:167], v212\n\t" \
    "v_add_f32 v208, v208, v210\n\t" \
    "v_add_f32 v209, v209, v211\n\t" \
    "v_cvt_pk_bf16_f32 v208, v208, v209\n\t" \
    "v_smfmac_f32_16x16x64_bf16 v[244:247], v[240:243], v[184:191], v212\n\t" \
    "ds_write_b16 v213, v208 offset:0\n\t" \
    "ds_write_b16_d16_hi v213, v208 offset:16\n\t" \
    "v_smfmac_f32_16x16x64_bf16 v[248:251], v[228:231], v[128:135], v212\n\t" \
    "v_smfmac_f32_16x16x64_bf16 v[248:251], v[232:235], v[152:159], v212\n\t" \
    "v_smfmac_f32_16x16x64_bf16 v[248:251], v[236:239], v[168:175], v212\n\t" \
    "v_add_f32 v244, v244, v246\n\t" \
    "v_add_f32 v245, v245, v247\n\t" \
    "v_smfmac_f32_16x16x64_bf16 v[248:251], v[240:243], v[192:199], v212\n\t" \
    "ds_read_b128 v[224:227], v253 offset:0\n\t" \
    "ds_read_b128 v[228:231], v253 offset:20480\n\t" \
    "s_nop 5\n\t" \
    "v_add_f32 v248, v248, v250\n\t" \
    "v_add_f32 v249, v249, v251\n\t" \
    "v_cvt_pk_bf16_f32 v244, v244, v248\n\t" \
    "v_cvt_pk_bf16_f32 v245, v245, v249\n\t" \
    "ds_write2_b32 v214, v244, v245 offset1:4\n\t" \
    "s_setprio 0\n\t" \
    "s_add_u32 s40, s40, 1\n\t" \
    "s_cmp_lg_u32 s40, 64\n\t" \
    "s_waitcnt lgkmcnt(0)\n\t" \
    "s_barrier \n\t" \
    LST4 \
    LJIT \
    "ds_read_b128 v[232:235], v253 offset:1024\n\t" \
    "ds_read_b128 v[236:239], v253 offset:21504\n\t" \
    "ds_read_b128 v[244:247], v254 offset:0\n\t" \
    "ds_read_b128 v[248:251], v254 offset:1024\n\t" \
    "s_setprio 1\n\t" \
    "s_nop 0\n\t" \
    "s_nop 0\n\t" \
    "s_cbranch_scc1 .Ls47_%=\n\t" \
    "s_branch .Lend%=\n\t" \
    ".Ls3_%=:\n\t" \
    "v_add_u32 v244, 0x18000, v243\n\t" \
    "s_waitcnt vmcnt(12) lgkmcnt(0)\n\t" \
    "s_barrier \n\t" \
    "s_add_u32 m0, %[wm], 0x12000\n\t" \
    "s_mov_b32 s44, 0x1e0000\n\t" \
    "buffer_load_dwordx4 v240, %[wr], s44 offen offset:0 nt lds\n\t" \
    "buffer_load_dwordx4 v241, %[wr], s44 offen offset:1024 nt lds\n\t" \
    "buffer_load_dwordx4 v242, %[wr], s44 offen offset:2048 nt lds\n\t" \
    "ds_read_b64_tr_b16 v[208:209], v244 offset:0\n\t" \
    "ds_read_b64_tr_b16 v[210:211], v244 offset:1536\n\t" \
    "ds_read_b64_tr_b16 v[40:41], v244 offset:32\n\t" \
    "ds_read_b64_tr_b16 v[42:43], v244 offset:1568\n\t" \
    "ds_read_b64_tr_b16 v[48:49], v244 offset:64\n\t" \
    "ds_read_b64_tr_b16 v[50:51], v244 offset:1600\n\t" \
    "v_add_u32 v244, 0x1e000, v243\n\t" \
    "s_waitcnt vmcnt(12) lgkmcnt(0)\n\t" \
    "s_barrier \n\t" \
    "s_add_u32 m0, %[wm], 0x18000\n\t" \
    "s_mov_b32 s44, 0x240000\n\t" \
    "buffer_load_dwordx4 v240, %[wr], s44 offen offset:0 nt lds\n\t" \
    "buffer_load_dwordx4 v241, %[wr], s44 offen offset:1024 nt lds\n\t" \
    "buffer_load_dwordx4 v242, %[wr], s44 offen offset:2048 nt lds\n\t" \
    "ds_read_b64_tr_b16 v[212:213], v244 offset:0\n\t" \
    "ds_read_b64_tr_b16 v[214:215], v244 offset:1536\n\t" \
    "ds_read_b64_tr_b16 v[44:45], v244 offset:32\n\t" \
    "ds_read_b64_tr_b16 v[46:47], v244 offset:1568\n\t" \
    "ds_read_b64_tr_b16 v[52:53], v244 offset:64\n\t" \
    "ds_read_b64_tr_b16 v[54:55], v244 offset:1600\n\t" \
    "v_add_u32 v244, 0x0, v243\n\t" \
    "s_waitcnt vmcnt(12) lgkmcnt(0)\n\t" \
    "s_barrier \n\t" \
    "s_add_u32 m0, %[wm], 0x1e000\n\t" \
    "s_mov_b32 s44, 0x2a0000\n\t" \
    "buffer_load_dwordx4 v240, %[wr], s44 offen offset:0 nt lds\n\t" \
    "buffer_load_dwordx4 v241, %[wr], s44 offen offset:1024 nt lds\n\t" \
    "buffer_load_dwordx4 v242, %[wr], s44 offen offset:2048 nt lds\n\t" \
    "ds_read_b64_tr_b16 v[56:57], v244 offset:0\n\t" \
    "ds_read_b64_tr_b16 v[58:59], v244 offset:1536\n\t" \
    "ds_read_b64_tr_b16 v[64:65], v244 offset:32\n\t" \
    "ds_read_b64_tr_b16 v[66:67], v244 offset:1568\n\t" \
    "ds_read_b64_tr_b16 v[72:73], v244 offset:64\n\t" \
    "ds_read_b64_tr_b16 v[74:75], v244 offset:1600\n\t" \
    "v_add_u32 v244, 0x6000, v243\n\t" \
    "s_waitcnt vmcnt(12) lgkmcnt(0)\n\t" \
    "s_barrier \n\t" \
    "s_add_u32 m0, %[wm], 0x0\n\t" \
    "s_mov_b32 s44, 0x300000\n\t" \
    "buffer_load_dwordx4 v240, %[wr], s44 offen offset:0 nt lds\n\t" \
    "buffer_load_dwordx4 v241, %[wr], s44 offen offset:1024 nt lds\n\t" \
    "buffer_load_dwordx4 v242, %[wr], s44 offen offset:2048 nt lds\n\t" \
    "ds_read_b64_tr_b16 v[60:61], v244 offset:0\n\t" \
    "ds_read_b64_tr_b16 v[62:63], v244 offset:1536\n\t" \
    "ds_read_b64_tr_b16 v[68:69], v244 offset:32\n\t" \
    "ds_read_b64_tr_b16 v[70:71], v244 offset:1568\n\t" \
    "ds_read_b64_tr_b16 v[76:77], v244 offset:64\n\t" \
    "ds_read_b64_tr_b16 v[78:79], v244 offset:1600\n\t" \
    "v_add_u32 v244, 0xc000, v243\n\t" \
    "s_waitcnt vmcnt(12) lgkmcnt(0)\n\t" \
    "s_barrier \n\t" \
    "s_add_u32 m0, %[wm], 0x6000\n\t" \
    "s_mov_b32 s44, 0x360000\n\t" \
    "buffer_load_dwordx4 v240, %[wr], s44 offen offset:0 nt lds\n\t" \
    "buffer_load_dwordx4 v241, %[wr], s44 offen offset:1024 nt lds\n\t" \
    "buffer_load_dwordx4 v242, %[wr], s44 offen offset:2048 nt lds\n\t" \
    "ds_read_b64_tr_b16 v[216:217], v244 offset:0\n\t" \
    "ds_read_b64_tr_b16 v[218:219], v244 offset:1536\n\t" \
    "ds_read_b64_tr_b16 v[80:81], v244 offset:32\n\t" \
    "ds_read_b64_tr_b16 v[82:83], v244 offset:1568\n\t" \
    "ds_read_b64_tr_b16 v[88:89], v244 offset:64\n\t" \
    "ds_read_b64_tr_b16 v[90:91], v244 offset:1600\n\t" \
    "v_add_u32 v244, 0x12000, v243\n\t" \
    "s_waitcnt vmcnt(12) lgkmcnt(0)\n\t" \
    "s_barrier \n\t" \
    "s_add_u32 m0, %[wm], 0xc000\n\t" \
    "s_mov_b32 s44, 0x3c0000\n\t" \
    "buffer_load_dwordx4 v240, %[wr], s44 offen offset:0 nt lds\n\t" \
    "buffer_load_dwordx4 v241, %[wr], s44 offen offset:1024 nt lds\n\t" \
    "buffer_load_dwordx4 v242, %[wr], s44 offen offset:2048 nt lds\n\t" \
    "ds_read_b64_tr_b16 v[220:221], v244 offset:0\n\t" \
    "ds_read_b64_tr_b16 v[222:223], v244 offset:1536\n\t" \
    "ds_read_b64_tr_b16 v[84:85], v244 offset:32\n\t" \
    "ds_read_b64_tr_b16 v[86:87], v244 offset:1568\n\t" \
    "ds_read_b64_tr_b16 v[92:93], v244 offset:64\n\t" \
    "ds_read_b64_tr_b16 v[94:95], v244 offset:1600\n\t" \
    "v_add_u32 v244, 0x18000, v243\n\t" \
    "s_waitcnt vmcnt(12) lgkmcnt(0)\n\t" \
    "s_barrier \n\t" \
    "s_add_u32 m0, %[wm], 0x12000\n\t" \
    "s_mov_b32 s44, 0x420000\n\t" \
    "buffer_load_dwordx4 v240, %[wr], s44 offen offset:0 nt lds\n\t" \
    "buffer_load_dwordx4 v241, %[wr], s44 offen offset:1024 nt lds\n\t" \
    "buffer_load_dwordx4 v242, %[wr], s44 offen offset:2048 nt lds\n\t" \
    "ds_read_b64_tr_b16 v[96:97], v244 offset:0\n\t" \
    "ds_read_b64_tr_b16 v[98:99], v244 offset:1536\n\t" \
    "ds_read_b64_tr_b16 v[104:105], v244 offset:32\n\t" \
    "ds_read_b64_tr_b16 v[106:107], v244 offset:1568\n\t" \
    "ds_read_b64_tr_b16 v[112:113], v244 offset:64\n\t" \
    "ds_read_b64_tr_b16 v[114:115], v244 offset:1600\n\t" \
    "v_add_u32 v244, 0x1e000, v243\n\t" \
    "s_waitcnt vmcnt(12) lgkmcnt(0)\n\t" \
    "s_barrier \n\t" \
    "s_add_u32 m0, %[wm], 0x18000\n\t" \
    "s_mov_b32 s44, 0x480000\n\t" \
    "buffer_load_dwordx4 v240, %[wr], s44 offen offset:0 nt lds\n\t" \
    "buffer_load_dwordx4 v241, %[wr], s44 offen offset:1024 nt lds\n\t" \
    "buffer_load_dwordx4 v242, %[wr], s44 offen offset:2048 nt lds\n\t" \
    "ds_read_b64_tr_b16 v[100:101], v244 offset:0\n\t" \
    "ds_read_b64_tr_b16 v[102:103], v244 offset:1536\n\t" \
    "ds_read_b64_tr_b16 v[108:109], v244 offset:32\n\t" \
    "ds_read_b64_tr_b16 v[110:111], v244 offset:1568\n\t" \
    "ds_read_b64_tr_b16 v[116:117], v244 offset:64\n\t" \
    "ds_read_b64_tr_b16 v[118:119], v244 offset:1600\n\t" \
    "v_add_u32 v244, 0x0, v243\n\t" \
    "s_waitcnt vmcnt(12) lgkmcnt(0)\n\t" \
    "s_barrier \n\t" \
    "v_mov_b32 v246, 0\n\t" \
    "buffer_load_dword v246, v246, s[60:63], %[mo] offen sc1\n\t" \
    "s_add_u32 m0, %[wm], 0x1e000\n\t" \
    "s_mov_b32 s44, 0x4e0000\n\t" \
    "buffer_load_dwordx4 v240, %[wr], s44 offen offset:0 nt lds\n\t" \
    "buffer_load_dwordx4 v241, %[wr], s44 offen offset:1024 nt lds\n\t" \
    "buffer_load_dwordx4 v242, %[wr], s44 offen offset:2048 nt lds\n\t" \
    "ds_read_b64_tr_b16 v[224:225], v244 offset:0\n\t" \
    "ds_read_b64_tr_b16 v[226:227], v244 offset:1536\n\t" \
    "ds_read_b64_tr_b16 v[120:121], v244 offset:32\n\t" \
    "ds_read_b64_tr_b16 v[122:123], v244 offset:1568\n\t" \
    "ds_read_b64_tr_b16 v[128:129], v244 offset:64\n\t" \
    "ds_read_b64_tr_b16 v[130:131], v244 offset:1600\n\t" \
    "v_add_u32 v244, 0x6000, v243\n\t" \
    "s_waitcnt vmcnt(12) lgkmcnt(0)\n\t" \
    "s_barrier \n\t" \
    "s_add_u32 m0, %[wm], 0x0\n\t" \
    "s_mov_b32 s44, 0x540000\n\t" \
    "buffer_load_dwordx4 v240, %[wr], s44 offen offset:0 nt lds\n\t" \
    "buffer_load_dwordx4 v241, %[wr], s44 offen offset:1024 nt lds\n\t" \
    "buffer_load_dwordx4 v242, %[wr], s44 offen offset:2048 nt lds\n\t" \
    "ds_read_b64_tr_b16 v[228:229], v244 offset:0\n\t" \
    "ds_read_b64_tr_b16 v[230:231], v244 offset:1536\n\t" \
    "ds_read_b64_tr_b16 v[124:125], v244 offset:32\n\t" \
    "ds_read_b64_tr_b16 v[126:127], v244 offset:1568\n\t" \
    "ds_read_b64_tr_b16 v[132:133], v244 offset:64\n\t" \
    "ds_read_b64_tr_b16 v[134:135], v244 offset:1600\n\t" \
    "v_add_u32 v244, 0xc000, v243\n\t" \
    "s_waitcnt vmcnt(12) lgkmcnt(0)\n\t" \
    "s_barrier \n\t" \
    "s_add_u32 m0, %[wm], 0x6000\n\t" \
    "s_mov_b32 s44, 0x5a0000\n\t" \
    "buffer_load_dwordx4 v240, %[wr], s44 offen offset:0 nt lds\n\t" \
    "buffer_load_dwordx4 v241, %[wr], s44 offen offset:1024 nt lds\n\t" \
    "buffer_load_dwordx4 v242, %[wr], s44 offen offset:2048 nt lds\n\t" \
    "ds_read_b64_tr_b16 v[136:137], v244 offset:0\n\t" \
    "ds_read_b64_tr_b16 v[138:139], v244 offset:1536\n\t" \
    "ds_read_b64_tr_b16 v[144:145], v244 offset:32\n\t" \
    "ds_read_b64_tr_b16 v[146:147], v244 offset:1568\n\t" \
    "ds_read_b64_tr_b16 v[152:153], v244 offset:64\n\t" \
    "ds_read_b64_tr_b16 v[154:155], v244 offset:1600\n\t" \
    "v_add_u32 v244, 0x12000, v243\n\t" \
    "s_waitcnt vmcnt(12) lgkmcnt(0)\n\t" \
    "s_barrier \n\t" \
    "s_add_u32 m0, %[wm], 0xc000\n\t" \
    "s_mov_b32 s44, 0x600000\n\t" \
    "buffer_load_dwordx4 v240, %[wr], s44 offen offset:0 nt lds\n\t" \
    "buffer_load_dwordx4 v241, %[wr], s44 offen offset:1024 nt lds\n\t" \
    "buffer_load_dwordx4 v242, %[wr], s44 offen offset:2048 nt lds\n\t" \
    "ds_read_b64_tr_b16 v[140:141], v244 offset:0\n\t" \
    "ds_read_b64_tr_b16 v[142:143], v244 offset:1536\n\t" \
    "ds_read_b64_tr_b16 v[148:149], v244 offset:32\n\t" \
    "ds_read_b64_tr_b16 v[150:151], v244 offset:1568\n\t" \
    "ds_read_b64_tr_b16 v[156:157], v244 offset:64\n\t" \
    "ds_read_b64_tr_b16 v[158:159], v244 offset:1600\n\t" \
    "v_add_u32 v244, 0x18000, v243\n\t" \
    "s_waitcnt vmcnt(12) lgkmcnt(0)\n\t" \
    "s_barrier \n\t" \
    "s_add_u32 m0, %[wm], 0x12000\n\t" \
    "s_mov_b32 s44, 0x660000\n\t" \
    "buffer_load_dwordx4 v240, %[wr], s44 offen offset:0 nt lds\n\t" \
    "buffer_load_dwordx4 v241, %[wr], s44 offen offset:1024 nt lds\n\t" \
    "buffer_load_dwordx4 v242, %[wr], s44 offen offset:2048 nt lds\n\t" \
    "ds_read_b64_tr_b16 v[232:233], v244 offset:0\n\t" \
    "ds_read_b64_tr_b16 v[234:235], v244 offset:1536\n\t" \
    "ds_read_b64_tr_b16 v[160:161], v244 offset:32\n\t" \
    "ds_read_b64_tr_b16 v[162:163], v244 offset:1568\n\t" \
    "ds_read_b64_tr_b16 v[168:169], v244 offset:64\n\t" \
    "ds_read_b64_tr_b16 v[170:171], v244 offset:1600\n\t" \
    "v_add_u32 v244, 0x1e000, v243\n\t" \
    "s_waitcnt vmcnt(12) lgkmcnt(0)\n\t" \
    "s_barrier \n\t" \
    ".Ls31_%=:\n\t" \
    "v_readfirstlane_b32 s43, v246\n\t" \
    "s_nop 0\n\t" \
    "s_cmp_lg_u32 s43, -1\n\t" \
    "s_cbranch_scc1 .Ls32_%=\n\t" \
    "s_cmp_eq_u32 s41, 0\n\t" \
    "s_cbranch_scc1 .Ls32_%=\n\t" \
    "s_sub_u32 s41, s41, 1\n\t" \
    "s_sleep 4\n\t" \
    "v_mov_b32 v246, 0\n\t" \
    "buffer_load_dword v246, v246, s[60:63], %[mo] offen sc1\n\t" \
    "s_waitcnt vmcnt(0)\n\t" \
    "s_branch .Ls31_%=\n\t" \
    ".Ls32_%=:\n\t" \
    "v_mov_b32 v246, 0x8000\n\t" \
    "s_mov_b64 exec, 1\n\t" \
    "buffer_store_dword v246, v246, s[60:63], %[mo] offen sc1\n\t" \
    "s_mov_b64 exec, -1\n\t" \
    "s_and_b32 s44, s43, 0x5000\n\t" \
    "s_xor_b32 %[mb], %[mb], s44\n\t" \
    "s_xor_b32 %[fl], %[fl], s44\n\t" \
    "s_and_b32 s43, s43, 0x2000\n\t" \
    "s_xor_b32 s43, s43, %[lx]\n\t" \
    "s_add_u32 s60, s60, s43\n\t" \
    "s_addc_u32 s61, s61, 0\n\t" \
    "s_add_u32 m0, %[wm], 0x18000\n\t" \
    "s_mov_b32 s44, 0x6c0000\n\t" \
    "buffer_load_dwordx4 v240, %[wr], s44 offen offset:0 nt lds\n\t" \
    "buffer_load_dwordx4 v241, %[wr], s44 offen offset:1024 nt lds\n\t" \
    "buffer_load_dwordx4 v242, %[wr], s44 offen offset:2048 nt lds\n\t" \
    "ds_read_b64_tr_b16 v[236:237], v244 offset:0\n\t" \
    "ds_read_b64_tr_b16 v[238:239], v244 offset:1536\n\t" \
    "ds_read_b64_tr_b16 v[164:165], v244 offset:32\n\t" \
    "ds_read_b64_tr_b16 v[166:167], v244 offset:1568\n\t" \
    "ds_read_b64_tr_b16 v[172:173], v244 offset:64\n\t" \
    "ds_read_b64_tr_b16 v[174:175], v244 offset:1600\n\t" \
    "v_add_u32 v244, 0x0, v243\n\t" \
    "s_waitcnt vmcnt(12) lgkmcnt(0)\n\t" \
    "s_barrier \n\t" \
    "s_add_u32 m0, %[wm], 0x1e000\n\t" \
    "s_mov_b32 s44, 0x720000\n\t" \
    "buffer_load_dwordx4 v240, %[wr], s44 offen offset:0 nt lds\n\t" \
    "buffer_load_dwordx4 v241, %[wr], s44 offen offset:1024 nt lds\n\t" \
    "buffer_load_dwordx4 v242, %[wr], s44 offen offset:2048 nt lds\n\t" \
    "ds_read_b64_tr_b16 v[176:177], v244 offset:0\n\t" \
    "ds_read_b64_tr_b16 v[178:179], v244 offset:1536\n\t" \
    "ds_read_b64_tr_b16 v[184:185], v244 offset:32\n\t" \
    "ds_read_b64_tr_b16 v[186:187], v244 offset:1568\n\t" \
    "ds_read_b64_tr_b16 v[192:193], v244 offset:64\n\t" \
    "ds_read_b64_tr_b16 v[194:195], v244 offset:1600\n\t" \
    "v_add_u32 v244, 0x6000, v243\n\t" \
    "s_waitcnt vmcnt(12) lgkmcnt(0)\n\t" \
    "s_barrier \n\t" \
    "ds_read_b64_tr_b16 v[180:181], v244 offset:0\n\t" \
    "ds_read_b64_tr_b16 v[182:183], v244 offset:1536\n\t" \
    "ds_read_b64_tr_b16 v[188:189], v244 offset:32\n\t" \
    "ds_read_b64_tr_b16 v[190:191], v244 offset:1568\n\t" \
    "ds_read_b64_tr_b16 v[196:197], v244 offset:64\n\t" \
    "ds_read_b64_tr_b16 v[198:199], v244 offset:1600\n\t" \
    "v_add_u32 v244, 0xc000, v243\n\t" \
    "s_waitcnt vmcnt(9) lgkmcnt(0)\n\t" \
    "s_barrier \n\t" \
    "ds_read_b64_tr_b16 v[200:201], v244 offset:0\n\t" \
    "ds_read_b64_tr_b16 v[202:203], v244 offset:1536\n\t" \
    "ds_read_b64_tr_b16 v[0:1], v244 offset:32\n\t" \
    "ds_read_b64_tr_b16 v[2:3], v244 offset:1568\n\t" \
    "ds_read_b64_tr_b16 v[8:9], v244 offset:64\n\t" \
    "ds_read_b64_tr_b16 v[10:11], v244 offset:1600\n\t" \
    "v_add_u32 v244, 0x12000, v243\n\t" \
    "s_waitcnt vmcnt(6) lgkmcnt(0)\n\t" \
    "s_barrier \n\t" \
    "ds_read_b64_tr_b16 v[204:205], v244 offset:0\n\t" \
    "ds_read_b64_tr_b16 v[206:207], v244 offset:1536\n\t" \
    "ds_read_b64_tr_b16 v[4:5], v244 offset:32\n\t" \
    "ds_read_b64_tr_b16 v[6:7], v244 offset:1568\n\t" \
    "ds_read_b64_tr_b16 v[12:13], v244 offset:64\n\t" \
    "ds_read_b64_tr_b16 v[14:15], v244 offset:1600\n\t" \
    "v_add_u32 v244, 0x18000, v243\n\t" \
    "s_waitcnt vmcnt(3) lgkmcnt(0)\n\t" \
    "s_barrier \n\t" \
    "ds_read_b64_tr_b16 v[16:17], v244 offset:0\n\t" \
    "ds_read_b64_tr_b16 v[18:19], v244 offset:1536\n\t" \
    "ds_read_b64_tr_b16 v[24:25], v244 offset:32\n\t" \
    "ds_read_b64_tr_b16 v[26:27], v244 offset:1568\n\t" \
    "ds_read_b64_tr_b16 v[32:33], v244 offset:64\n\t" \
    "ds_read_b64_tr_b16 v[34:35], v244 offset:1600\n\t" \
    "v_add_u32 v244, 0x1e000, v243\n\t" \
    "s_waitcnt vmcnt(0) lgkmcnt(0)\n\t" \
    "s_barrier \n\t" \
    "ds_read_b64_tr_b16 v[20:21], v244 offset:0\n\t" \
    "ds_read_b64_tr_b16 v[22:23], v244 offset:1536\n\t" \
    "ds_read_b64_tr_b16 v[28:29], v244 offset:32\n\t" \
    "ds_read_b64_tr_b16 v[30:31], v244 offset:1568\n\t" \
    "ds_read_b64_tr_b16 v[36:37], v244 offset:64\n\t" \
    "ds_read_b64_tr_b16 v[38:39], v244 offset:1600\n\t" \
    "s_waitcnt lgkmcnt(0)\n\t" \
    "s_barrier \n\t" \
    LST0 \
    LJIT \
    "ds_write_b128 v254, v[200:203] offset:55296\n\t" \
    "ds_write_b128 v254, v[204:207] offset:56320\n\t" \
    "ds_write_b128 v254, v[208:211] offset:57344\n\t" \
    "ds_write_b128 v254, v[212:215] offset:58368\n\t" \
    "ds_write_b128 v254, v[216:219] offset:59392\n\t" \
    "ds_write_b128 v254, v[220:223] offset:60416\n\t" \
    "ds_write_b128 v254, v[224:227] offset:61440\n\t" \
    "ds_write_b128 v254, v[228:231] offset:62464\n\t" \
    "ds_write_b128 v254, v[232:235] offset:63488\n\t" \
    "ds_write_b128 v254, v[236:239] offset:64512\n\t" \
    "s_waitcnt vmcnt(0) lgkmcnt(0)\n\t" \
    "s_barrier \n\t" \
    "ds_read_b128 v[208:211], v254 offset:24576\n\t" \
    "ds_read_b128 v[224:227], v253 offset:0\n\t" \
    "ds_read_b128 v[228:231], v253 offset:20480\n\t" \
    "ds_read_b128 v[212:215], v254 offset:25600\n\t" \
    "ds_read_b128 v[232:235], v253 offset:1024\n\t" \
    "ds_read_b128 v[236:239], v253 offset:21504\n\t" \
    "ds_read_b128 v[216:219], v254 offset:26624\n\t" \
    "ds_read_b128 v[240:243], v253 offset:2048\n\t" \
    "ds_read_b128 v[244:247], v253 offset:22528\n\t" \
    "ds_read_b128 v[220:223], v254 offset:27648\n\t" \
    "ds_read_b128 v[248:251], v253 offset:3072\n\t" \
    "s_waitcnt lgkmcnt(9)\n\t" \
    "v_mfma_f32_16x16x32_bf16 v[200:203], v[208:211], v[224:227], 0\n\t" \
    "ds_read_b128 v[224:227], v253 offset:23552\n\t" \
    "s_waitcnt lgkmcnt(9)\n\t" \
    "v_mfma_f32_16x16x32_bf16 v[204:207], v[208:211], v[228:231], 0\n\t" \
    "ds_read_b128 v[228:231], v253 offset:4096\n\t" \
    "ds_read_b128 v[208:211], v254 offset:28672\n\t" \
    "s_waitcnt lgkmcnt(9)\n\t" \
    "v_mfma_f32_16x16x32_bf16 v[200:203], v[212:215], v[232:235], v[200:203]\n\t" \
    "ds_read_b128 v[232:235], v253 offset:24576\n\t" \
    "s_waitcnt lgkmcnt(9)\n\t" \
    "v_mfma_f32_16x16x32_bf16 v[204:207], v[212:215], v[236:239], v[204:207]\n\t" \
    "ds_read_b128 v[236:239], v253 offset:5120\n\t" \
    "ds_read_b128 v[212:215], v254 offset:29696\n\t" \
    "s_waitcnt lgkmcnt(9)\n\t" \
    "v_mfma_f32_16x16x32_bf16 v[200:203], v[216:219], v[240:243], v[200:203]\n\t" \
    "ds_read_b128 v[240:243], v253 offset:25600\n\t" \
    "s_waitcnt lgkmcnt(9)\n\t" \
    "v_mfma_f32_16x16x32_bf16 v[204:207], v[216:219], v[244:247], v[204:207]\n\t" \
    "ds_read_b128 v[244:247], v253 offset:6144\n\t" \
    "ds_read_b128 v[216:219], v254 offset:30720\n\t" \
    "s_waitcnt lgkmcnt(9)\n\t" \
    "v_mfma_f32_16x16x32_bf16 v[200:203], v[220:223], v[248:251], v[200:203]\n\t" \
    "ds_read_b128 v[248:251], v253 offset:26624\n\t" \
    ".p2align 8\n\t" \
    ".Ls33_%=:\n\t" \
    "s_waitcnt lgkmcnt(9)\n\t" \
    "v_mfma_f32_16x16x32_bf16 v[204:207], v[220:223], v[224:227], v[204:207]\n\t" \
    "ds_read_b128 v[224:227], v253 offset:7168\n\t" \
    "ds_read_b128 v[220:223], v254 offset:31744\n\t" \
    "s_waitcnt lgkmcnt(9)\n\t" \
    "v_mfma_f32_16x16x32_bf16 v[200:203], v[208:211], v[228:231], v[200:203]\n\t" \
    "ds_read_b128 v[228:231], v253 offset:27648\n\t" \
    "s_waitcnt lgkmcnt(9)\n\t" \
    "v_mfma_f32_16x16x32_bf16 v[204:207], v[208:211], v[232:235], v[204:207]\n\t" \
    "ds_read_b128 v[232:235], v253 offset:8192\n\t" \
    "ds_read_b128 v[208:211], v254 offset:32768\n\t" \
    "s_waitcnt lgkmcnt(9)\n\t" \
    "v_mfma_f32_16x16x32_bf16 v[200:203], v[212:215], v[236:239], v[200:203]\n\t" \
    "ds_read_b128 v[236:239], v253 offset:28672\n\t" \
    "s_waitcnt lgkmcnt(9)\n\t" \
    "v_mfma_f32_16x16x32_bf16 v[204:207], v[212:215], v[240:243], v[204:207]\n\t" \
    "ds_read_b128 v[240:243], v253 offset:9216\n\t" \
    "ds_read_b128 v[212:215], v254 offset:33792\n\t" \
    "s_waitcnt lgkmcnt(9)\n\t" \
    "v_mfma_f32_16x16x32_bf16 v[200:203], v[216:219], v[244:247], v[200:203]\n\t" \
    "ds_read_b128 v[244:247], v253 offset:29696\n\t" \
    "s_waitcnt lgkmcnt(9)\n\t" \
    "v_mfma_f32_16x16x32_bf16 v[204:207], v[216:219], v[248:251], v[204:207]\n\t" \
    "s_waitcnt lgkmcnt(7)\n\t" \
    "v_mfma_f32_16x16x32_bf16 v[200:203], v[220:223], v[224:227], v[200:203]\n\t" \
    "s_waitcnt lgkmcnt(6)\n\t" \
    "v_mfma_f32_16x16x32_bf16 v[204:207], v[220:223], v[228:231], v[204:207]\n\t" \
    "s_waitcnt lgkmcnt(4)\n\t" \
    "v_mfma_f32_16x16x32_bf16 v[200:203], v[208:211], v[232:235], v[200:203]\n\t" \
    "s_waitcnt lgkmcnt(3)\n\t" \
    "v_mfma_f32_16x16x32_bf16 v[204:207], v[208:211], v[236:239], v[204:207]\n\t" \
    "s_waitcnt lgkmcnt(1)\n\t" \
    "v_mfma_f32_16x16x32_bf16 v[200:203], v[212:215], v[240:243], v[200:203]\n\t" \
    "s_waitcnt lgkmcnt(0)\n\t" \
    "v_mfma_f32_16x16x32_bf16 v[204:207], v[212:215], v[244:247], v[204:207]\n\t" \
    "ds_read_b128 v[224:227], v253 offset:0\n\t" \
    "ds_read_b128 v[244:247], v254 offset:57344\n\t" \
    "ds_read_b128 v[228:231], v253 offset:20480\n\t" \
    "ds_read_b128 v[232:235], v253 offset:1024\n\t" \
    "ds_read_b128 v[236:239], v253 offset:21504\n\t" \
    "ds_read_b128 v[248:251], v254 offset:58368\n\t" \
    "ds_write_b128 v253, v[200:203] offset:40960\n\t" \
    "s_nop 0\n\t" \
    "ds_write_b128 v253, v[204:207] offset:41984\n\t" \
    "s_nop 1\n\t" \
    "s_waitcnt lgkmcnt(7)\n\t" \
    "v_mfma_f32_16x16x32_bf16 v[204:207], v[40:43], v[224:227], 0\n\t" \
    "ds_read_b128 v[240:243], v253 offset:2048\n\t" \
    "v_mfma_f32_16x16x32_bf16 v[208:211], v[48:51], v[224:227], 0\n\t" \
    "s_waitcnt lgkmcnt(7)\n\t" \
    "v_mfma_f32_16x16x32_bf16 v[200:203], v[244:247], v[224:227], 0\n\t" \
    "s_waitcnt lgkmcnt(6)\n\t" \
    "v_mfma_f32_16x16x32_bf16 v[216:219], v[40:43], v[228:231], 0\n\t" \
    "ds_read_b128 v[224:227], v253 offset:22528\n\t" \
    "v_mfma_f32_16x16x32_bf16 v[220:223], v[48:51], v[228:231], 0\n\t" \
    "v_mfma_f32_16x16x32_bf16 v[212:215], v[244:247], v[228:231], 0\n\t" \
    "ds_read_b128 v[244:247], v254 offset:59392\n\t" \
    "s_waitcnt lgkmcnt(7)\n\t" \
    "v_mfma_f32_16x16x32_bf16 v[204:207], v[44:47], v[232:235], v[204:207]\n\t" \
    "ds_read_b128 v[228:231], v253 offset:3072\n\t" \
    "v_mfma_f32_16x16x32_bf16 v[208:211], v[52:55], v[232:235], v[208:211]\n\t" \
    "s_waitcnt lgkmcnt(6)\n\t" \
    "v_mfma_f32_16x16x32_bf16 v[200:203], v[248:251], v[232:235], v[200:203]\n\t" \
    "v_mfma_f32_16x16x32_bf16 v[216:219], v[44:47], v[236:239], v[216:219]\n\t" \
    "ds_read_b128 v[232:235], v253 offset:23552\n\t" \
    "v_mfma_f32_16x16x32_bf16 v[220:223], v[52:55], v[236:239], v[220:223]\n\t" \
    "v_mfma_f32_16x16x32_bf16 v[212:215], v[248:251], v[236:239], v[212:215]\n\t" \
    "ds_read_b128 v[248:251], v254 offset:60416\n\t" \
    "s_waitcnt lgkmcnt(5)\n\t" \
    "v_mfma_f32_16x16x32_bf16 v[204:207], v[64:67], v[240:243], v[204:207]\n\t" \
    "ds_read_b128 v[236:239], v253 offset:4096\n\t" \
    "v_mfma_f32_16x16x32_bf16 v[208:211], v[72:75], v[240:243], v[208:211]\n\t" \
    "v_mfma_f32_16x16x32_bf16 v[200:203], v[56:59], v[240:243], v[200:203]\n\t" \
    "s_waitcnt lgkmcnt(5)\n\t" \
    "v_mfma_f32_16x16x32_bf16 v[216:219], v[64:67], v[224:227], v[216:219]\n\t" \
    "ds_read_b128 v[240:243], v253 offset:24576\n\t" \
    "v_mfma_f32_16x16x32_bf16 v[220:223], v[72:75], v[224:227], v[220:223]\n\t" \
    "v_mfma_f32_16x16x32_bf16 v[212:215], v[56:59], v[224:227], v[212:215]\n\t" \
    "s_waitcnt lgkmcnt(4)\n\t" \
    "v_mfma_f32_16x16x32_bf16 v[204:207], v[68:71], v[228:231], v[204:207]\n\t" \
    "ds_read_b128 v[224:227], v253 offset:5120\n\t" \
    "v_mfma_f32_16x16x32_bf16 v[208:211], v[76:79], v[228:231], v[208:211]\n\t" \
    "v_mfma_f32_16x16x32_bf16 v[200:203], v[60:63], v[228:231], v[200:203]\n\t" \
    "s_waitcnt lgkmcnt(4)\n\t" \
    "v_mfma_f32_16x16x32_bf16 v[216:219], v[68:71], v[232:235], v[216:219]\n\t" \
    "ds_read_b128 v[228:231], v253 offset:25600\n\t" \
    "v_mfma_f32_16x16x32_bf16 v[220:223], v[76:79], v[232:235], v[220:223]\n\t" \
    "v_mfma_f32_16x16x32_bf16 v[212:215], v[60:63], v[232:235], v[212:215]\n\t" \
    "s_waitcnt lgkmcnt(3)\n\t" \
    "v_mfma_f32_16x16x32_bf16 v[204:207], v[80:83], v[236:239], v[204:207]\n\t" \
    "ds_read_b128 v[232:235], v253 offset:6144\n\t" \
    "v_mfma_f32_16x16x32_bf16 v[208:211], v[88:91], v[236:239], v[208:211]\n\t" \
    "v_mfma_f32_16x16x32_bf16 v[200:203], v[244:247], v[236:239], v[200:203]\n\t" \
    "s_waitcnt lgkmcnt(3)\n\t" \
    "v_mfma_f32_16x16x32_bf16 v[216:219], v[80:83], v[240:243], v[216:219]\n\t" \
    "ds_read_b128 v[236:239], v253 offset:26624\n\t" \
    "v_mfma_f32_16x16x32_bf16 v[220:223], v[88:91], v[240:243], v[220:223]\n\t" \
    "v_mfma_f32_16x16x32_bf16 v[212:215], v[244:247], v[240:243], v[212:215]\n\t" \
    "ds_read_b128 v[244:247], v254 offset:61440\n\t" \
    "s_waitcnt lgkmcnt(4)\n\t" \
    "v_mfma_f32_16x16x32_bf16 v[204:207], v[84:87], v[224:227], v[204:207]\n\t" \
    "ds_read_b128 v[240:243], v253 offset:7168\n\t" \
    "v_mfma_f32_16x16x32_bf16 v[208:211], v[92:95], v[224:227], v[208:211]\n\t" \
    "v_mfma_f32_16x16x32_bf16 v[200:203], v[248:251], v[224:227], v[200:203]\n\t" \
    "s_waitcnt lgkmcnt(4)\n\t" \
    "v_mfma_f32_16x16x32_bf16 v[216:219], v[84:87], v[228:231], v[216:219]\n\t" \
    "ds_read_b128 v[224:227], v253 offset:27648\n\t" \
    "v_mfma_f32_16x16x32_bf16 v[220:223], v[92:95], v[228:231], v[220:223]\n\t" \
    "v_mfma_f32_16x16x32_bf16 v[212:215], v[248:251], v[228:231], v[212:215]\n\t" \
    "ds_read_b128 v[248:251], v254 offset:62464\n\t" \
    "s_waitcnt lgkmcnt(5)\n\t" \
    "v_mfma_f32_16x16x32_bf16 v[204:207], v[104:107], v[232:235], v[204:207]\n\t" \
    "ds_read_b128 v[228:231], v253 offset:8192\n\t" \
    "v_mfma_f32_16x16x32_bf16 v[208:211], v[112:115], v[232:235], v[208:211]\n\t" \
    "v_mfma_f32_16x16x32_bf16 v[200:203], v[96:99], v[232:235], v[200:203]\n\t" \
    "s_waitcnt lgkmcnt(5)\n\t" \
    "v_mfma_f32_16x16x32_bf16 v[216:219], v[104:107], v[236:239], v[216:219]\n\t" \
    "ds_read_b128 v[232:235], v253 offset:28672\n\t" \
    "v_mfma_f32_16x16x32_bf16 v[220:223], v[112:115], v[236:239], v[220:223]\n\t" \
    "v_mfma_f32_16x16x32_bf16 v[212:215], v[96:99], v[236:239], v[212:215]\n\t" \
    "s_waitcnt lgkmcnt(4)\n\t" \
    "v_mfma_f32_16x16x32_bf16 v[204:207], v[108:111], v[240:243], v[204:207]\n\t" \
    "ds_read_b128 v[236:239], v253 offset:9216\n\t" \
    "v_mfma_f32_16x16x32_bf16 v[208:211], v[116:119], v[240:243], v[208:211]\n\t" \
    "v_mfma_f32_16x16x32_bf16 v[200:203], v[100:103], v[240:243], v[200:203]\n\t" \
    "s_waitcnt lgkmcnt(4)\n\t" \
    "v_mfma_f32_16x16x32_bf16 v[216:219], v[108:111], v[224:227], v[216:219]\n\t" \
    "ds_read_b128 v[240:243], v253 offset:29696\n\t" \
    "v_mfma_f32_16x16x32_bf16 v[220:223], v[116:119], v[224:227], v[220:223]\n\t" \
    "v_mfma_f32_16x16x32_bf16 v[212:215], v[100:103], v[224:227], v[212:215]\n\t" \
    "s_waitcnt lgkmcnt(3)\n\t" \
    "v_mfma_f32_16x16x32_bf16 v[204:207], v[120:123], v[228:231], v[204:207]\n\t" \
    "ds_read_b128 v[224:227], v253 offset:10240\n\t" \
    "v_mfma_f32_16x16x32_bf16 v[208:211], v[128:131], v[228:231], v[208:211]\n\t" \
    "v_mfma_f32_16x16x32_bf16 v[200:203], v[244:247], v[228:231], v[200:203]\n\t" \
    "s_waitcnt lgkmcnt(3)\n\t" \
    "v_mfma_f32_16x16x32_bf16 v[216:219], v[120:123], v[232:235], v[216:219]\n\t" \
    "ds_read_b128 v[228:231], v253 offset:30720\n\t" \
    "v_mfma_f32_16x16x32_bf16 v[220:223], v[128:131], v[232:235], v[220:223]\n\t" \
    "v_mfma_f32_16x16x32_bf16 v[212:215], v[244:247], v[232:235], v[212:215]\n\t" \
    "ds_read_b128 v[244:247], v254 offset:63488\n\t" \
    "s_waitcnt lgkmcnt(4)\n\t" \
    "v_mfma_f32_16x16x32_bf16 v[204:207], v[124:127], v[236:239], v[204:207]\n\t" \
    "ds_read_b128 v[232:235], v253 offset:11264\n\t" \
    "v_mfma_f32_16x16x32_bf16 v[208:211], v[132:135], v[236:239], v[208:211]\n\t" \
    "v_mfma_f32_16x16x32_bf16 v[200:203], v[248:251], v[236:239], v[200:203]\n\t" \
    "s_waitcnt lgkmcnt(4)\n\t" \
    "v_mfma_f32_16x16x32_bf16 v[216:219], v[124:127], v[240:243], v[216:219]\n\t" \
    "ds_read_b128 v[236:239], v253 offset:31744\n\t" \
    "v_mfma_f32_16x16x32_bf16 v[220:223], v[132:135], v[240:243], v[220:223]\n\t" \
    "v_mfma_f32_16x16x32_bf16 v[212:215], v[248:251], v[240:243], v[212:215]\n\t" \
    "ds_read_b128 v[248:251], v254 offset:64512\n\t" \
    "s_setprio 2\n\t" \
    "s_waitcnt lgkmcnt(0)\n\t" \
    "s_barrier \n\t" \
    LST7 \
    LJIT \
    "v_mfma_f32_16x16x32_bf16 v[204:207], v[144:147], v[224:227], v[204:207]\n\t" \
    "ds_read_b128 v[240:243], v253 offset:12288\n\t" \
    "v_mfma_f32_16x16x32_bf16 v[208:211], v[152:155], v[224:227], v[208:211]\n\t" \
    "v_mfma_f32_16x16x32_bf16 v[200:203], v[136:139], v[224:227], v[200:203]\n\t" \
    "v_mfma_f32_16x16x32_bf16 v[216:219], v[144:147], v[228:231], v[216:219]\n\t" \
    "ds_read_b128 v[224:227], v253 offset:32768\n\t" \
    "s_and_b32 s44, s40, 3\n\t" \
    "v_mfma_f32_16x16x32_bf16 v[220:223], v[152:155], v[228:231], v[220:223]\n\t" \
    "s_lshl_b32 s44, s44, 22\n\t" \
    "v_mfma_f32_16x16x32_bf16 v[212:215], v[136:139], v[228:231], v[212:215]\n\t" \
    "s_add_u32 s44, s44, %[mb]\n\t" \
    "v_mfma_f32_16x16x32_bf16 v[204:207], v[148:151], v[232:235], v[204:207]\n\t" \
    "ds_read_b128 v[228:231], v253 offset:13312\n\t" \
    "s_add_u32 s46, s44, 0x10000\n\t" \
    "v_mfma_f32_16x16x32_bf16 v[208:211], v[156:159], v[232:235], v[208:211]\n\t" \
    "s_add_u32 s47, s44, 0x10400\n\t" \
    "v_mfma_f32_16x16x32_bf16 v[200:203], v[140:143], v[232:235], v[200:203]\n\t" \
    "v_mfma_f32_16x16x32_bf16 v[216:219], v[148:151], v[236:239], v[216:219]\n\t" \
    "ds_read_b128 v[232:235], v253 offset:33792\n\t" \
    "v_mfma_f32_16x16x32_bf16 v[220:223], v[156:159], v[236:239], v[220:223]\n\t" \
    "v_mfma_f32_16x16x32_bf16 v[212:215], v[140:143], v[236:239], v[212:215]\n\t" \
    "s_waitcnt lgkmcnt(3)\n\t" \
    "v_mfma_f32_16x16x32_bf16 v[204:207], v[160:163], v[240:243], v[204:207]\n\t" \
    "ds_read_b128 v[236:239], v253 offset:16384\n\t" \
    "v_mfma_f32_16x16x32_bf16 v[208:211], v[168:171], v[240:243], v[208:211]\n\t" \
    "v_mfma_f32_16x16x32_bf16 v[200:203], v[244:247], v[240:243], v[200:203]\n\t" \
    "s_waitcnt lgkmcnt(3)\n\t" \
    "v_mfma_f32_16x16x32_bf16 v[216:219], v[160:163], v[224:227], v[216:219]\n\t" \
    "ds_read_b128 v[240:243], v253 offset:36864\n\t" \
    "v_mfma_f32_16x16x32_bf16 v[220:223], v[168:171], v[224:227], v[220:223]\n\t" \
    "v_mfma_f32_16x16x32_bf16 v[212:215], v[244:247], v[224:227], v[212:215]\n\t" \
    "ds_read_b128 v[244:247], v254 offset:55296\n\t" \
    "s_waitcnt lgkmcnt(4)\n\t" \
    "v_mfma_f32_16x16x32_bf16 v[204:207], v[164:167], v[228:231], v[204:207]\n\t" \
    "ds_read_b128 v[224:227], v253 offset:17408\n\t" \
    "v_mfma_f32_16x16x32_bf16 v[208:211], v[172:175], v[228:231], v[208:211]\n\t" \
    "v_mfma_f32_16x16x32_bf16 v[200:203], v[248:251], v[228:231], v[200:203]\n\t" \
    "s_waitcnt lgkmcnt(4)\n\t" \
    "v_mfma_f32_16x16x32_bf16 v[216:219], v[164:167], v[232:235], v[216:219]\n\t" \
    "ds_read_b128 v[228:231], v253 offset:37888\n\t" \
    "v_mfma_f32_16x16x32_bf16 v[220:223], v[172:175], v[232:235], v[220:223]\n\t" \
    "v_mfma_f32_16x16x32_bf16 v[212:215], v[248:251], v[232:235], v[212:215]\n\t" \
    "ds_read_b128 v[248:251], v254 offset:56320\n\t" \
    "s_waitcnt lgkmcnt(5)\n\t" \
    "v_mfma_f32_16x16x32_bf16 v[204:207], v[0:3], v[236:239], v[204:207]\n\t" \
    "ds_read_b128 v[232:235], v253 offset:14336\n\t" \
    "v_mfma_f32_16x16x32_bf16 v[208:211], v[8:11], v[236:239], v[208:211]\n\t" \
    "s_waitcnt lgkmcnt(4)\n\t" \
    "v_mfma_f32_16x16x32_bf16 v[200:203], v[244:247], v[236:239], v[200:203]\n\t" \
    "v_mfma_f32_16x16x32_bf16 v[216:219], v[0:3], v[240:243], v[216:219]\n\t" \
    "ds_read_b128 v[236:239], v253 offset:15360\n\t" \
    "v_mfma_f32_16x16x32_bf16 v[220:223], v[8:11], v[240:243], v[220:223]\n\t" \
    "v_mfma_f32_16x16x32_bf16 v[212:215], v[244:247], v[240:243], v[212:215]\n\t" \
    "s_waitcnt lgkmcnt(4)\n\t" \
    "v_mfma_f32_16x16x32_bf16 v[204:207], v[4:7], v[224:227], v[204:207]\n\t" \
    "ds_read_b128 v[240:243], v253 offset:18432\n\t" \
    "v_mfma_f32_16x16x32_bf16 v[208:211], v[12:15], v[224:227], v[208:211]\n\t" \
    "s_waitcnt lgkmcnt(3)\n\t" \
    "v_mfma_f32_16x16x32_bf16 v[200:203], v[248:251], v[224:227], v[200:203]\n\t" \
    "v_mfma_f32_16x16x32_bf16 v[216:219], v[4:7], v[228:231], v[216:219]\n\t" \
    "ds_read_b128 v[224:227], v253 offset:19456\n\t" \
    "v_mfma_f32_16x16x32_bf16 v[220:223], v[12:15], v[228:231], v[220:223]\n\t" \
    "v_mfma_f32_16x16x32_bf16 v[212:215], v[248:251], v[228:231], v[212:215]\n\t" \
    "s_waitcnt lgkmcnt(3)\n\t" \
    "v_mfma_f32_16x16x32_bf16 v[204:207], v[184:187], v[232:235], v[204:207]\n\t" \
    "ds_read_b128 v[228:231], v253 offset:34816\n\t" \
    "v_mov_b64 v[244:245], 0\n\t" \
    "v_mfma_f32_16x16x32_bf16 v[208:211], v[192:195], v[232:235], v[208:211]\n\t" \
    "v_mov_b64 v[246:247], 0\n\t" \
    "v_mfma_f32_16x16x32_bf16 v[200:203], v[176:179], v[232:235], v[200:203]\n\t" \
    "v_mov_b64 v[248:249], 0\n\t" \
    "s_waitcnt lgkmcnt(3)\n\t" \
    "v_mfma_f32_16x16x32_bf16 v[204:207], v[188:191], v[236:239], v[204:207]\n\t" \
    "ds_read_b128 v[232:235], v253 offset:35840\n\t" \
    "v_mov_b64 v[250:251], 0\n\t" \
    "v_mfma_f32_16x16x32_bf16 v[208:211], v[196:199], v[236:239], v[208:211]\n\t" \
    "v_mfma_f32_16x16x32_bf16 v[200:203], v[180:183], v[236:239], v[200:203]\n\t" \
    "s_waitcnt lgkmcnt(3)\n\t" \
    "v_mfma_f32_16x16x32_bf16 v[204:207], v[24:27], v[240:243], v[204:207]\n\t" \
    "ds_read_b128 v[236:239], v253 offset:38912\n\t" \
    "v_mfma_f32_16x16x32_bf16 v[208:211], v[32:35], v[240:243], v[208:211]\n\t" \
    "v_mfma_f32_16x16x32_bf16 v[200:203], v[16:19], v[240:243], v[200:203]\n\t" \
    "s_waitcnt lgkmcnt(3)\n\t" \
    "v_mfma_f32_16x16x32_bf16 v[204:207], v[28:31], v[224:227], v[204:207]\n\t" \
    "ds_read_b128 v[240:243], v253 offset:39936\n\t" \
    "v_mfma_f32_16x16x32_bf16 v[208:211], v[36:39], v[224:227], v[208:211]\n\t" \
    "v_mfma_f32_16x16x32_bf16 v[200:203], v[20:23], v[224:227], v[200:203]\n\t" \
    "s_waitcnt lgkmcnt(3)\n\t" \
    "v_mfma_f32_16x16x32_bf16 v[216:219], v[184:187], v[228:231], v[216:219]\n\t" \
    "s_waitcnt lgkmcnt(2)\n\t" \
    "v_mfma_f32_16x16x32_bf16 v[216:219], v[188:191], v[232:235], v[216:219]\n\t" \
    "v_cvt_pk_bf16_f32 v204, v204, v208\n\t" \
    "v_cvt_pk_bf16_f32 v205, v205, v209\n\t" \
    "s_waitcnt lgkmcnt(1)\n\t" \
    "v_mfma_f32_16x16x32_bf16 v[216:219], v[24:27], v[236:239], v[216:219]\n\t" \
    "v_cvt_pk_bf16_f32 v206, v206, v210\n\t" \
    "v_cvt_pk_bf16_f32 v207, v207, v211\n\t" \
    "s_waitcnt lgkmcnt(0)\n\t" \
    "v_mfma_f32_16x16x32_bf16 v[216:219], v[28:31], v[240:243], v[216:219]\n\t" \
    "ds_write_b128 v254, v[204:207] offset:5120\n\t" \
    "v_mfma_f32_16x16x32_bf16 v[220:223], v[192:195], v[228:231], v[220:223]\n\t" \
    "v_mfma_f32_16x16x32_bf16 v[220:223], v[196:199], v[232:235], v[220:223]\n\t" \
    "v_cvt_pk_bf16_f32 v200, v200, v201\n\t" \
    "v_mfma_f32_16x16x32_bf16 v[220:223], v[32:35], v[236:239], v[220:223]\n\t" \
    "v_cvt_pk_bf16_f32 v201, v202, v203\n\t" \
    "ds_write_b64 v254, v[200:201] offset:1032\n\t" \
    "v_mfma_f32_16x16x32_bf16 v[220:223], v[36:39], v[240:243], v[220:223]\n\t" \
    "v_mfma_f32_16x16x32_bf16 v[212:215], v[176:179], v[228:231], v[212:215]\n\t" \
    "v_mfma_f32_16x16x32_bf16 v[212:215], v[180:183], v[232:235], v[212:215]\n\t" \
    "ds_read_b128 v[200:203], v254 offset:55296\n\t" \
    "ds_read_b128 v[204:207], v254 offset:56320\n\t" \
    "v_mfma_f32_16x16x32_bf16 v[212:215], v[16:19], v[236:239], v[212:215]\n\t" \
    "v_mov_b64 v[208:209], 0\n\t" \
    "v_mov_b64 v[210:211], 0\n\t" \
    "v_mfma_f32_16x16x32_bf16 v[212:215], v[20:23], v[240:243], v[212:215]\n\t" \
    "v_cvt_pk_bf16_f32 v216, v216, v220\n\t" \
    "v_cvt_pk_bf16_f32 v217, v217, v221\n\t" \
    "s_setprio 0\n\t" \
    "v_cvt_pk_bf16_f32 v218, v218, v222\n\t" \
    "v_cvt_pk_bf16_f32 v219, v219, v223\n\t" \
    "ds_write_b128 v254, v[216:219] offset:11264\n\t" \
    "ds_read_b128 v[216:219], v254 offset:57344\n\t" \
    "ds_read_b128 v[220:223], v254 offset:58368\n\t" \
    "s_nop 0\n\t" \
    "v_cvt_pk_bf16_f32 v212, v212, v213\n\t" \
    "v_cvt_pk_bf16_f32 v213, v214, v215\n\t" \
    "ds_write_b64 v254, v[212:213] offset:7176\n\t" \
    LST6 \
    LJIT \
    "s_sleep 3\n\t" \
    "s_mov_b32 m0, 0xc000\n\t" \
    "s_nop 0\n\t" \
    "buffer_load_dwordx4 v253, s[60:63], s46 offen sc1 lds\n\t" \
    "s_mov_b32 m0, 0xc400\n\t" \
    "s_nop 0\n\t" \
    "buffer_load_dwordx4 v253, s[60:63], s47 offen sc1 lds\n\t" \
    "s_waitcnt lgkmcnt(0)\n\t" \
    LFLUSH212 \
    "v_bfe_i32 v212, v253, 5, 1\n\t" \
    "v_and_b32 v212, 0xaaaa, v212\n\t" \
    "v_or_b32 v212, 0x4444, v212\n\t" \
    "v_and_b32 v213, 0xffff, v255\n\t" \
    "v_lshrrev_b32 v214, 16, v255\n\t" \
    ".Ls34_%=:\n\t" \
    "s_waitcnt vmcnt(0)\n\t" \
    "ds_read_b128 v[224:227], v253 offset:49152\n\t" \
    "ds_read_b128 v[228:231], v253 offset:50176\n\t" \
    "s_waitcnt lgkmcnt(1)\n\t" \
    "v_max_u32 v215, v224, v225\n\t" \
    "v_max3_u32 v215, v226, v227, v215\n\t" \
    "s_waitcnt lgkmcnt(0)\n\t" \
    "v_max3_u32 v215, v228, v229, v215\n\t" \
    "v_max3_u32 v215, v230, v231, v215\n\t" \
    "v_cmp_eq_u32 vcc, -1, v215\n\t" \
    "s_cbranch_vccz .Ls35_%=\n\t" \
    "v_max_u32 v215, v224, v225\n\t" \
    "v_max3_u32 v215, v226, v227, v215\n\t" \
    "v_cmp_eq_u32 vcc, -1, v215\n\t" \
    "s_cbranch_vccz .Ls36_%=\n\t" \
    "s_mov_b32 m0, 0xc000\n\t" \
    "s_nop 0\n\t" \
    "buffer_load_dwordx4 v253, s[60:63], s46 offen sc1 lds\n\t" \
    ".Ls36_%=:\n\t" \
    "v_max_u32 v215, v228, v229\n\t" \
    "v_max3_u32 v215, v230, v231, v215\n\t" \
    "v_cmp_eq_u32 vcc, -1, v215\n\t" \
    "s_cbranch_vccz .Ls37_%=\n\t" \
    "s_mov_b32 m0, 0xc400\n\t" \
    "s_nop 0\n\t" \
    "buffer_load_dwordx4 v253, s[60:63], s47 offen sc1 lds\n\t" \
    ".Ls37_%=:\n\t" \
    "s_cmp_eq_u32 s41, 0\n\t" \
    "s_cbranch_scc1 .Ls35_%=\n\t" \
    "s_sub_u32 s41, s41, 1\n\t" \
    "s_branch .Ls34_%=\n\t" \
    ".Ls35_%=:\n\t" \
    LST2 \
    LJIT \
    "s_setprio 3\n\t" \
    "s_waitcnt lgkmcnt(0)\n\t" \
    "s_barrier \n\t" \
    LST3 \
    LJIT \
    "ds_read_b128 v[232:235], v253 offset:40960\n\t" \
    "ds_read_b128 v[236:239], v253 offset:41984\n\t" \
    "ds_read_b128 v[240:243], v253 offset:43008\n\t" \
    "v_smfmac_f32_16x16x64_bf16 v[244:247], v[224:227], v[0:7], v212\n\t" \
    "v_smfmac_f32_16x16x64_bf16 v[248:251], v[224:227], v[8:15], v212\n\t" \
    "v_smfmac_f32_16x16x64_bf16 v[208:211], v[224:227], v[200:207], v212\n\t" \
    "ds_read_b128 v[200:203], v254 offset:59392\n\t" \
    "ds_read_b128 v[204:207], v254 offset:60416\n\t" \
    "ds_read_b128 v[224:227], v253 offset:44032\n\t" \
    "v_smfmac_f32_16x16x64_bf16 v[208:211], v[228:231], v[16:23], v212\n\t" \
    "v_smfmac_f32_16x16x64_bf16 v[244:247], v[228:231], v[24:31], v212\n\t" \
    "v_smfmac_f32_16x16x64_bf16 v[248:251], v[228:231], v[32:39], v212\n\t" \
    "ds_read_b128 v[228:231], v253 offset:45056\n\t" \
    "s_waitcnt lgkmcnt(6)\n\t" \
    "v_smfmac_f32_16x16x64_bf16 v[208:211], v[232:235], v[216:223], v212\n\t" \
    "ds_read_b128 v[216:219], v254 offset:61440\n\t" \
    "ds_read_b128 v[220:223], v254 offset:62464\n\t" \
    "v_smfmac_f32_16x16x64_bf16 v[244:247], v[232:235], v[40:47], v212\n\t" \
    "v_smfmac_f32_16x16x64_bf16 v[248:251], v[232:235], v[48:55], v212\n\t" \
    "ds_read_b128 v[232:235], v253 offset:46080\n\t" \
    "s_waitcnt lgkmcnt(8)\n\t" \
    "v_smfmac_f32_16x16x64_bf16 v[208:211], v[236:239], v[56:63], v212\n\t" \
    "v_smfmac_f32_16x16x64_bf16 v[244:247], v[236:239], v[64:71], v212\n\t" \
    "v_smfmac_f32_16x16x64_bf16 v[248:251], v[236:239], v[72:79], v212\n\t" \
    "ds_read_b128 v[236:239], v253 offset:47104\n\t" \
    "s_waitcnt lgkmcnt(6)\n\t" \
    "v_smfmac_f32_16x16x64_bf16 v[208:211], v[240:243], v[200:207], v212\n\t" \
    "ds_read_b128 v[200:203], v254 offset:63488\n\t" \
    "ds_read_b128 v[204:207], v254 offset:64512\n\t" \
    "v_smfmac_f32_16x16x64_bf16 v[244:247], v[240:243], v[80:87], v212\n\t" \
    "v_smfmac_f32_16x16x64_bf16 v[248:251], v[240:243], v[88:95], v212\n\t" \
    "ds_read_b128 v[240:243], v253 offset:48128\n\t" \
    "s_waitcnt lgkmcnt(8)\n\t" \
    "v_smfmac_f32_16x16x64_bf16 v[208:211], v[224:227], v[96:103], v212\n\t" \
    "v_smfmac_f32_16x16x64_bf16 v[244:247], v[224:227], v[104:111], v212\n\t" \
    "v_smfmac_f32_16x16x64_bf16 v[248:251], v[224:227], v[112:119], v212\n\t" \
    "s_waitcnt lgkmcnt(5)\n\t" \
    "v_smfmac_f32_16x16x64_bf16 v[208:211], v[228:231], v[216:223], v212\n\t" \
    "s_waitcnt lgkmcnt(4)\n\t" \
    "v_smfmac_f32_16x16x64_bf16 v[208:211], v[232:235], v[136:143], v212\n\t" \
    "s_waitcnt lgkmcnt(1)\n\t" \
    "v_smfmac_f32_16x16x64_bf16 v[208:211], v[236:239], v[200:207], v212\n\t" \
    "s_waitcnt lgkmcnt(0)\n\t" \
    "v_smfmac_f32_16x16x64_bf16 v[208:211], v[240:243], v[176:183], v212\n\t" \
    "v_smfmac_f32_16x16x64_bf16 v[244:247], v[228:231], v[120:127], v212\n\t" \
    "v_smfmac_f32_16x16x64_bf16 v[244:247], v[232:235], v[144:151], v212\n\t" \
    "v_smfmac_f32_16x16x64_bf16 v[244:247], v[236:239], v[160:167], v212\n\t" \
    "v_add_f32 v208, v208, v210\n\t" \
    "v_add_f32 v209, v209, v211\n\t" \
    "v_cvt_pk_bf16_f32 v208, v208, v209\n\t" \
    "v_smfmac_f32_16x16x64_bf16 v[244:247], v[240:243], v[184:191], v212\n\t" \
    "ds_write_b16 v213, v208 offset:0\n\t" \
    "ds_write_b16_d16_hi v213, v208 offset:16\n\t" \
    "v_smfmac_f32_16x16x64_bf16 v[248:251], v[228:231], v[128:135], v212\n\t" \
    "v_smfmac_f32_16x16x64_bf16 v[248:251], v[232:235], v[152:159], v212\n\t" \
    "v_smfmac_f32_16x16x64_bf16 v[248:251], v[236:239], v[168:175], v212\n\t" \
    "v_add_f32 v244, v244, v246\n\t" \
    "v_add_f32 v245, v245, v247\n\t" \
    "v_smfmac_f32_16x16x64_bf16 v[248:251], v[240:243], v[192:199], v212\n\t" \
    "s_nop 7\n\t" \
    "v_add_f32 v248, v248, v250\n\t" \
    "v_add_f32 v249, v249, v251\n\t" \
    "v_cvt_pk_bf16_f32 v244, v244, v248\n\t" \
    "v_cvt_pk_bf16_f32 v245, v245, v249\n\t" \
    "ds_write2_b32 v214, v244, v245 offset1:4\n\t" \
    "s_setprio 0\n\t" \
    "s_sleep 1\n\t" \
    "ds_read_b128 v[208:211], v254 offset:24576\n\t" \
    "ds_read_b128 v[224:227], v253 offset:0\n\t" \
    "ds_read_b128 v[228:231], v253 offset:20480\n\t" \
    "ds_read_b128 v[212:215], v254 offset:25600\n\t" \
    "ds_read_b128 v[232:235], v253 offset:1024\n\t" \
    "ds_read_b128 v[236:239], v253 offset:21504\n\t" \
    "ds_read_b128 v[216:219], v254 offset:26624\n\t" \
    "ds_read_b128 v[240:243], v253 offset:2048\n\t" \
    "ds_read_b128 v[244:247], v253 offset:22528\n\t" \
    "ds_read_b128 v[220:223], v254 offset:27648\n\t" \
    "ds_read_b128 v[248:251], v253 offset:3072\n\t" \
    "s_waitcnt lgkmcnt(9)\n\t" \
    "v_mfma_f32_16x16x32_bf16 v[200:203], v[208:211], v[224:227], 0\n\t" \
    "ds_read_b128 v[224:227], v253 offset:23552\n\t" \
    "s_waitcnt lgkmcnt(9)\n\t" \
    "v_mfma_f32_16x16x32_bf16 v[204:207], v[208:211], v[228:231], 0\n\t" \
    "ds_read_b128 v[228:231], v253 offset:4096\n\t" \
    "ds_read_b128 v[208:211], v254 offset:28672\n\t" \
    "s_waitcnt lgkmcnt(9)\n\t" \
    "v_mfma_f32_16x16x32_bf16 v[200:203], v[212:215], v[232:235], v[200:203]\n\t" \
    "ds_read_b128 v[232:235], v253 offset:24576\n\t" \
    "s_waitcnt lgkmcnt(9)\n\t" \
    "v_mfma_f32_16x16x32_bf16 v[204:207], v[212:215], v[236:239], v[204:207]\n\t" \
    "ds_read_b128 v[236:239], v253 offset:5120\n\t" \
    "ds_read_b128 v[212:215], v254 offset:29696\n\t" \
    "s_waitcnt lgkmcnt(9)\n\t" \
    "v_mfma_f32_16x16x32_bf16 v[200:203], v[216:219], v[240:243], v[200:203]\n\t" \
    "ds_read_b128 v[240:243], v253 offset:25600\n\t" \
    "s_waitcnt lgkmcnt(9)\n\t" \
    "v_mfma_f32_16x16x32_bf16 v[204:207], v[216:219], v[244:247], v[204:207]\n\t" \
    "ds_read_b128 v[244:247], v253 offset:6144\n\t" \
    "ds_read_b128 v[216:219], v254 offset:30720\n\t" \
    "s_waitcnt lgkmcnt(9)\n\t" \
    "v_mfma_f32_16x16x32_bf16 v[200:203], v[220:223], v[248:251], v[200:203]\n\t" \
    "ds_read_b128 v[248:251], v253 offset:26624\n\t" \
    "s_waitcnt lgkmcnt(0)\n\t" \
    "s_barrier \n\t" \
    LST4 \
    LJIT \
    "s_add_u32 s40, s40, 1\n\t" \
    "s_cmp_lg_u32 s40, 64\n\t" \
    "s_cbranch_scc1 .Ls33_%=\n\t" \
    "s_branch .Lend%=\n\t" \
    ".Ls2_%=:\n\t" \
    "v_add_u32 v244, 0x18000, v243\n\t" \
    "s_waitcnt vmcnt(12) lgkmcnt(0)\n\t" \
    "s_barrier \n\t" \
    "s_add_u32 m0, %[wm], 0x12000\n\t" \
    "s_mov_b32 s44, 0x1e0000\n\t" \
    "buffer_load_dwordx4 v240, %[wr], s44 offen offset:0 nt lds\n\t" \
    "buffer_load_dwordx4 v241, %[wr], s44 offen offset:1024 nt lds\n\t" \
    "buffer_load_dwordx4 v242, %[wr], s44 offen offset:2048 nt lds\n\t" \
    "ds_read_b64_tr_b16 v[208:209], v244 offset:0\n\t" \
    "ds_read_b64_tr_b16 v[210:211], v244 offset:1536\n\t" \
    "ds_read_b64_tr_b16 v[40:41], v244 offset:32\n\t" \
    "ds_read_b64_tr_b16 v[42:43], v244 offset:1568\n\t" \
    "ds_read_b64_tr_b16 v[48:49], v244 offset:64\n\t" \
    "ds_read_b64_tr_b16 v[50:51], v244 offset:1600\n\t" \
    "v_add_u32 v244, 0x1e000, v243\n\t" \
    "s_waitcnt vmcnt(12) lgkmcnt(0)\n\t" \
    "s_barrier \n\t" \
    "s_add_u32 m0, %[wm], 0x18000\n\t" \
    "s_mov_b32 s44, 0x240000\n\t" \
    "buffer_load_dwordx4 v240, %[wr], s44 offen offset:0 nt lds\n\t" \
    "buffer_load_dwordx4 v241, %[wr], s44 offen offset:1024 nt lds\n\t" \
    "buffer_load_dwordx4 v242, %[wr], s44 offen offset:2048 nt lds\n\t" \
    "ds_read_b64_tr_b16 v[212:213], v244 offset:0\n\t" \
    "ds_read_b64_tr_b16 v[214:215], v244 offset:1536\n\t" \
    "ds_read_b64_tr_b16 v[44:45], v244 offset:32\n\t" \
    "ds_read_b64_tr_b16 v[46:47], v244 offset:1568\n\t" \
    "ds_read_b64_tr_b16 v[52:53], v244 offset:64\n\t" \
    "ds_read_b64_tr_b16 v[54:55], v244 offset:1600\n\t" \
    "v_add_u32 v244, 0x0, v243\n\t" \
    "s_waitcnt vmcnt(12) lgkmcnt(0)\n\t" \
    "s_barrier \n\t" \
    "s_add_u32 m0, %[wm], 0x1e000\n\t" \
    "s_mov_b32 s44, 0x2a0000\n\t" \
    "buffer_load_dwordx4 v240, %[wr], s44 offen offset:0 nt lds\n\t" \
    "buffer_load_dwordx4 v241, %[wr], s44 offen offset:1024 nt lds\n\t" \
    "buffer_load_dwordx4 v242, %[wr], s44 offen offset:2048 nt lds\n\t" \
    "ds_read_b64_tr_b16 v[56:57], v244 offset:0\n\t" \
    "ds_read_b64_tr_b16 v[58:59], v244 offset:1536\n\t" \
    "ds_read_b64_tr_b16 v[64:65], v244 offset:32\n\t" \
    "ds_read_b64_tr_b16 v[66:67], v244 offset:1568\n\t" \
    "ds_read_b64_tr_b16 v[72:73], v244 offset:64\n\t" \
    "ds_read_b64_tr_b16 v[74:75], v244 offset:1600\n\t" \
    "v_add_u32 v244, 0x6000, v243\n\t" \
    "s_waitcnt vmcnt(12) lgkmcnt(0)\n\t" \
    "s_barrier \n\t" \
    "s_add_u32 m0, %[wm], 0x0\n\t" \
    "s_mov_b32 s44, 0x300000\n\t" \
    "buffer_load_dwordx4 v240, %[wr], s44 offen offset:0 nt lds\n\t" \
    "buffer_load_dwordx4 v241, %[wr], s44 offen offset:1024 nt lds\n\t" \
    "buffer_load_dwordx4 v242, %[wr], s44 offen offset:2048 nt lds\n\t" \
    "ds_read_b64_tr_b16 v[60:61], v244 offset:0\n\t" \
    "ds_read_b64_tr_b16 v[62:63], v244 offset:1536\n\t" \
    "ds_read_b64_tr_b16 v[68:69], v244 offset:32\n\t" \
    "ds_read_b64_tr_b16 v[70:71], v244 offset:1568\n\t" \
    "ds_read_b64_tr_b16 v[76:77], v244 offset:64\n\t" \
    "ds_read_b64_tr_b16 v[78:79], v244 offset:1600\n\t" \
    "v_add_u32 v244, 0xc000, v243\n\t" \
    "s_waitcnt vmcnt(12) lgkmcnt(0)\n\t" \
    "s_barrier \n\t" \
    "s_add_u32 m0, %[wm], 0x6000\n\t" \
    "s_mov_b32 s44, 0x360000\n\t" \
    "buffer_load_dwordx4 v240, %[wr], s44 offen offset:0 nt lds\n\t" \
    "buffer_load_dwordx4 v241, %[wr], s44 offen offset:1024 nt lds\n\t" \
    "buffer_load_dwordx4 v242, %[wr], s44 offen offset:2048 nt lds\n\t" \
    "ds_read_b64_tr_b16 v[216:217], v244 offset:0\n\t" \
    "ds_read_b64_tr_b16 v[218:219], v244 offset:1536\n\t" \
    "ds_read_b64_tr_b16 v[80:81], v244 offset:32\n\t" \
    "ds_read_b64_tr_b16 v[82:83], v244 offset:1568\n\t" \
    "ds_read_b64_tr_b16 v[88:89], v244 offset:64\n\t" \
    "ds_read_b64_tr_b16 v[90:91], v244 offset:1600\n\t" \
    "v_add_u32 v244, 0x12000, v243\n\t" \
    "s_waitcnt vmcnt(12) lgkmcnt(0)\n\t" \
    "s_barrier \n\t" \
    "s_add_u32 m0, %[wm], 0xc000\n\t" \
    "s_mov_b32 s44, 0x3c0000\n\t" \
    "buffer_load_dwordx4 v240, %[wr], s44 offen offset:0 nt lds\n\t" \
    "buffer_load_dwordx4 v241, %[wr], s44 offen offset:1024 nt lds\n\t" \
    "buffer_load_dwordx4 v242, %[wr], s44 offen offset:2048 nt lds\n\t" \
    "ds_read_b64_tr_b16 v[220:221], v244 offset:0\n\t" \
    "ds_read_b64_tr_b16 v[222:223], v244 offset:1536\n\t" \
    "ds_read_b64_tr_b16 v[84:85], v244 offset:32\n\t" \
    "ds_read_b64_tr_b16 v[86:87], v244 offset:1568\n\t" \
    "ds_read_b64_tr_b16 v[92:93], v244 offset:64\n\t" \
    "ds_read_b64_tr_b16 v[94:95], v244 offset:1600\n\t" \
    "v_add_u32 v244, 0x18000, v243\n\t" \
    "s_waitcnt vmcnt(12) lgkmcnt(0)\n\t" \
    "s_barrier \n\t" \
    "s_add_u32 m0, %[wm], 0x12000\n\t" \
    "s_mov_b32 s44, 0x420000\n\t" \
    "buffer_load_dwordx4 v240, %[wr], s44 offen offset:0 nt lds\n\t" \
    "buffer_load_dwordx4 v241, %[wr], s44 offen offset:1024 nt lds\n\t" \
    "buffer_load_dwordx4 v242, %[wr], s44 offen offset:2048 nt lds\n\t" \
    "ds_read_b64_tr_b16 v[96:97], v244 offset:0\n\t" \
    "ds_read_b64_tr_b16 v[98:99], v244 offset:1536\n\t" \
    "ds_read_b64_tr_b16 v[104:105], v244 offset:32\n\t" \
    "ds_read_b64_tr_b16 v[106:107], v244 offset:1568\n\t" \
    "ds_read_b64_tr_b16 v[112:113], v244 offset:64\n\t" \
    "ds_read_b64_tr_b16 v[114:115], v244 offset:1600\n\t" \
    "v_add_u32 v244, 0x1e000, v243\n\t" \
    "s_waitcnt vmcnt(12) lgkmcnt(0)\n\t" \
    "s_barrier \n\t" \
    "s_add_u32 m0, %[wm], 0x18000\n\t" \
    "s_mov_b32 s44, 0x480000\n\t" \
    "buffer_load_dwordx4 v240, %[wr], s44 offen offset:0 nt lds\n\t" \
    "buffer_load_dwordx4 v241, %[wr], s44 offen offset:1024 nt lds\n\t" \
    "buffer_load_dwordx4 v242, %[wr], s44 offen offset:2048 nt lds\n\t" \
    "ds_read_b64_tr_b16 v[100:101], v244 offset:0\n\t" \
    "ds_read_b64_tr_b16 v[102:103], v244 offset:1536\n\t" \
    "ds_read_b64_tr_b16 v[108:109], v244 offset:32\n\t" \
    "ds_read_b64_tr_b16 v[110:111], v244 offset:1568\n\t" \
    "ds_read_b64_tr_b16 v[116:117], v244 offset:64\n\t" \
    "ds_read_b64_tr_b16 v[118:119], v244 offset:1600\n\t" \
    "v_add_u32 v244, 0x0, v243\n\t" \
    "s_waitcnt vmcnt(12) lgkmcnt(0)\n\t" \
    "s_barrier \n\t" \
    "v_mov_b32 v246, 0\n\t" \
    "buffer_load_dword v246, v246, s[60:63], %[mo] offen sc1\n\t" \
    "s_add_u32 m0, %[wm], 0x1e000\n\t" \
    "s_mov_b32 s44, 0x4e0000\n\t" \
    "buffer_load_dwordx4 v240, %[wr], s44 offen offset:0 nt lds\n\t" \
    "buffer_load_dwordx4 v241, %[wr], s44 offen offset:1024 nt lds\n\t" \
    "buffer_load_dwordx4 v242, %[wr], s44 offen offset:2048 nt lds\n\t" \
    "ds_read_b64_tr_b16 v[200:201], v244 offset:0\n\t" \
    "ds_read_b64_tr_b16 v[202:203], v244 offset:1536\n\t" \
    "ds_read_b64_tr_b16 v[0:1], v244 offset:32\n\t" \
    "ds_read_b64_tr_b16 v[2:3], v244 offset:1568\n\t" \
    "ds_read_b64_tr_b16 v[8:9], v244 offset:64\n\t" \
    "ds_read_b64_tr_b16 v[10:11], v244 offset:1600\n\t" \
    "v_add_u32 v244, 0x6000, v243\n\t" \
    "s_waitcnt vmcnt(12) lgkmcnt(0)\n\t" \
    "s_barrier \n\t" \
    "s_add_u32 m0, %[wm], 0x0\n\t" \
    "s_mov_b32 s44, 0x540000\n\t" \
    "buffer_load_dwordx4 v240, %[wr], s44 offen offset:0 nt lds\n\t" \
    "buffer_load_dwordx4 v241, %[wr], s44 offen offset:1024 nt lds\n\t" \
    "buffer_load_dwordx4 v242, %[wr], s44 offen offset:2048 nt lds\n\t" \
    "ds_read_b64_tr_b16 v[204:205], v244 offset:0\n\t" \
    "ds_read_b64_tr_b16 v[206:207], v244 offset:1536\n\t" \
    "ds_read_b64_tr_b16 v[4:5], v244 offset:32\n\t" \
    "ds_read_b64_tr_b16 v[6:7], v244 offset:1568\n\t" \
    "ds_read_b64_tr_b16 v[12:13], v244 offset:64\n\t" \
    "ds_read_b64_tr_b16 v[14:15], v244 offset:1600\n\t" \
    "v_add_u32 v244, 0xc000, v243\n\t" \
    "s_waitcnt vmcnt(12) lgkmcnt(0)\n\t" \
    "s_barrier \n\t" \
    "s_add_u32 m0, %[wm], 0x6000\n\t" \
    "s_mov_b32 s44, 0x5a0000\n\t" \
    "buffer_load_dwordx4 v240, %[wr], s44 offen offset:0 nt lds\n\t" \
    "buffer_load_dwordx4 v241, %[wr], s44 offen offset:1024 nt lds\n\t" \
    "buffer_load_dwordx4 v242, %[wr], s44 offen offset:2048 nt lds\n\t" \
    "ds_read_b64_tr_b16 v[224:225], v244 offset:0\n\t" \
    "ds_read_b64_tr_b16 v[226:227], v244 offset:1536\n\t" \
    "ds_read_b64_tr_b16 v[120:121], v244 offset:32\n\t" \
    "ds_read_b64_tr_b16 v[122:123], v244 offset:1568\n\t" \
    "ds_read_b64_tr_b16 v[128:129], v244 offset:64\n\t" \
    "ds_read_b64_tr_b16 v[130:131], v244 offset:1600\n\t" \
    "v_add_u32 v244, 0x12000, v243\n\t" \
    "s_waitcnt vmcnt(12) lgkmcnt(0)\n\t" \
    "s_barrier \n\t" \
    "s_add_u32 m0, %[wm], 0xc000\n\t" \
    "s_mov_b32 s44, 0x600000\n\t" \
    "buffer_load_dwordx4 v240, %[wr], s44 offen offset:0 nt lds\n\t" \
    "buffer_load_dwordx4 v241, %[wr], s44 offen offset:1024 nt lds\n\t" \
    "buffer_load_dwordx4 v242, %[wr], s44 offen offset:2048 nt lds\n\t" \
    "ds_read_b64_tr_b16 v[228:229], v244 offset:0\n\t" \
    "ds_read_b64_tr_b16 v[230:231], v244 offset:1536\n\t" \
    "ds_read_b64_tr_b16 v[124:125], v244 offset:32\n\t" \
    "ds_read_b64_tr_b16 v[126:127], v244 offset:1568\n\t" \
    "ds_read_b64_tr_b16 v[132:133], v244 offset:64\n\t" \
    "ds_read_b64_tr_b16 v[134:135], v244 offset:1600\n\t" \
    "v_add_u32 v244, 0x18000, v243\n\t" \
    "s_waitcnt vmcnt(12) lgkmcnt(0)\n\t" \
    "s_barrier \n\t" \
    "s_add_u32 m0, %[wm], 0x12000\n\t" \
    "s_mov_b32 s44, 0x660000\n\t" \
    "buffer_load_dwordx4 v240, %[wr], s44 offen offset:0 nt lds\n\t" \
    "buffer_load_dwordx4 v241, %[wr], s44 offen offset:1024 nt lds\n\t" \
    "buffer_load_dwordx4 v242, %[wr], s44 offen offset:2048 nt lds\n\t" \
    "ds_read_b64_tr_b16 v[136:137], v244 offset:0\n\t" \
    "ds_read_b64_tr_b16 v[138:139], v244 offset:1536\n\t" \
    "ds_read_b64_tr_b16 v[144:145], v244 offset:32\n\t" \
    "ds_read_b64_tr_b16 v[146:147], v244 offset:1568\n\t" \
    "ds_read_b64_tr_b16 v[152:153], v244 offset:64\n\t" \
    "ds_read_b64_tr_b16 v[154:155], v244 offset:1600\n\t" \
    "v_add_u32 v244, 0x1e000, v243\n\t" \
    "s_waitcnt vmcnt(12) lgkmcnt(0)\n\t" \
    "s_barrier \n\t" \
    ".Ls21_%=:\n\t" \
    "v_readfirstlane_b32 s43, v246\n\t" \
    "s_nop 0\n\t" \
    "s_cmp_lg_u32 s43, -1\n\t" \
    "s_cbranch_scc1 .Ls22_%=\n\t" \
    "s_cmp_eq_u32 s41, 0\n\t" \
    "s_cbranch_scc1 .Ls22_%=\n\t" \
    "s_sub_u32 s41, s41, 1\n\t" \
    "s_sleep 4\n\t" \
    "v_mov_b32 v246, 0\n\t" \
    "buffer_load_dword v246, v246, s[60:63], %[mo] offen sc1\n\t" \
    "s_waitcnt vmcnt(0)\n\t" \
    "s_branch .Ls21_%=\n\t" \
    ".Ls22_%=:\n\t" \
    "v_mov_b32 v246, 0x8000\n\t" \
    "s_mov_b64 exec, 1\n\t" \
    "buffer_store_dword v246, v246, s[60:63], %[mo] offen sc1\n\t" \
    "s_mov_b64 exec, -1\n\t" \
    "s_and_b32 s44, s43, 0x5000\n\t" \
    "s_xor_b32 %[mb], %[mb], s44\n\t" \
    "s_xor_b32 %[fl], %[fl], s44\n\t" \
    "s_and_b32 s43, s43, 0x2000\n\t" \
    "s_xor_b32 s43, s43, %[lx]\n\t" \
    "s_add_u32 s60, s60, s43\n\t" \
    "s_addc_u32 s61, s61, 0\n\t" \
    "s_add_u32 m0, %[wm], 0x18000\n\t" \
    "s_mov_b32 s44, 0x6c0000\n\t" \
    "buffer_load_dwordx4 v240, %[wr], s44 offen offset:0 nt lds\n\t" \
    "buffer_load_dwordx4 v241, %[wr], s44 offen offset:1024 nt lds\n\t" \
    "buffer_load_dwordx4 v242, %[wr], s44 offen offset:2048 nt lds\n\t" \
    "ds_read_b64_tr_b16 v[140:141], v244 offset:0\n\t" \
    "ds_read_b64_tr_b16 v[142:143], v244 offset:1536\n\t" \
    "ds_read_b64_tr_b16 v[148:149], v244 offset:32\n\t" \
    "ds_read_b64_tr_b16 v[150:151], v244 offset:1568\n\t" \
    "ds_read_b64_tr_b16 v[156:157], v244 offset:64\n\t" \
    "ds_read_b64_tr_b16 v[158:159], v244 offset:1600\n\t" \
    "v_add_u32 v244, 0x0, v243\n\t" \
    "s_waitcnt vmcnt(12) lgkmcnt(0)\n\t" \
    "s_barrier \n\t" \
    "s_add_u32 m0, %[wm], 0x1e000\n\t" \
    "s_mov_b32 s44, 0x720000\n\t" \
    "buffer_load_dwordx4 v240, %[wr], s44 offen offset:0 nt lds\n\t" \
    "buffer_load_dwordx4 v241, %[wr], s44 offen offset:1024 nt lds\n\t" \
    "buffer_load_dwordx4 v242, %[wr], s44 offen offset:2048 nt lds\n\t" \
    "ds_read_b64_tr_b16 v[16:17], v244 offset:0\n\t" \
    "ds_read_b64_tr_b16 v[18:19], v244 offset:1536\n\t" \
    "ds_read_b64_tr_b16 v[24:25], v244 offset:32\n\t" \
    "ds_read_b64_tr_b16 v[26:27], v244 offset:1568\n\t" \
    "ds_read_b64_tr_b16 v[32:33], v244 offset:64\n\t" \
    "ds_read_b64_tr_b16 v[34:35], v244 offset:1600\n\t" \
    "v_add_u32 v244, 0x6000, v243\n\t" \
    "s_waitcnt vmcnt(12) lgkmcnt(0)\n\t" \
    "s_barrier \n\t" \
    "ds_read_b64_tr_b16 v[20:21], v244 offset:0\n\t" \
    "ds_read_b64_tr_b16 v[22:23], v244 offset:1536\n\t" \
    "ds_read_b64_tr_b16 v[28:29], v244 offset:32\n\t" \
    "ds_read_b64_tr_b16 v[30:31], v244 offset:1568\n\t" \
    "ds_read_b64_tr_b16 v[36:37], v244 offset:64\n\t" \
    "ds_read_b64_tr_b16 v[38:39], v244 offset:1600\n\t" \
    "v_add_u32 v244, 0xc000, v243\n\t" \
    "s_waitcnt vmcnt(9) lgkmcnt(0)\n\t" \
    "s_barrier \n\t" \
    "ds_read_b64_tr_b16 v[232:233], v244 offset:0\n\t" \
    "ds_read_b64_tr_b16 v[234:235], v244 offset:1536\n\t" \
    "ds_read_b64_tr_b16 v[160:161], v244 offset:32\n\t" \
    "ds_read_b64_tr_b16 v[162:163], v244 offset:1568\n\t" \
    "ds_read_b64_tr_b16 v[168:169], v244 offset:64\n\t" \
    "ds_read_b64_tr_b16 v[170:171], v244 offset:1600\n\t" \
    "v_add_u32 v244, 0x12000, v243\n\t" \
    "s_waitcnt vmcnt(6) lgkmcnt(0)\n\t" \
    "s_barrier \n\t" \
    "ds_read_b64_tr_b16 v[236:237], v244 offset:0\n\t" \
    "ds_read_b64_tr_b16 v[238:239], v244 offset:1536\n\t" \
    "ds_read_b64_tr_b16 v[164:165], v244 offset:32\n\t" \
    "ds_read_b64_tr_b16 v[166:167], v244 offset:1568\n\t" \
    "ds_read_b64_tr_b16 v[172:173], v244 offset:64\n\t" \
    "ds_read_b64_tr_b16 v[174:175], v244 offset:1600\n\t" \
    "v_add_u32 v244, 0x18000, v243\n\t" \
    "s_waitcnt vmcnt(3) lgkmcnt(0)\n\t" \
    "s_barrier \n\t" \
    "ds_read_b64_tr_b16 v[176:177], v244 offset:0\n\t" \
    "ds_read_b64_tr_b16 v[178:179], v244 offset:1536\n\t" \
    "ds_read_b64_tr_b16 v[184:185], v244 offset:32\n\t" \
    "ds_read_b64_tr_b16 v[186:187], v244 offset:1568\n\t" \
    "ds_read_b64_tr_b16 v[192:193], v244 offset:64\n\t" \
    "ds_read_b64_tr_b16 v[194:195], v244 offset:1600\n\t" \
    "v_add_u32 v244, 0x1e000, v243\n\t" \
    "s_waitcnt vmcnt(0) lgkmcnt(0)\n\t" \
    "s_barrier \n\t" \
    "ds_read_b64_tr_b16 v[180:181], v244 offset:0\n\t" \
    "ds_read_b64_tr_b16 v[182:183], v244 offset:1536\n\t" \
    "ds_read_b64_tr_b16 v[188:189], v244 offset:32\n\t" \
    "ds_read_b64_tr_b16 v[190:191], v244 offset:1568\n\t" \
    "ds_read_b64_tr_b16 v[196:197], v244 offset:64\n\t" \
    "ds_read_b64_tr_b16 v[198:199], v244 offset:1600\n\t" \
    "s_waitcnt lgkmcnt(0)\n\t" \
    "s_barrier \n\t" \
    LST0 \
    LJIT \
    "ds_write_b128 v254, v[200:203] offset:51200\n\t" \
    "ds_write_b128 v254, v[204:207] offset:52224\n\t" \
    "ds_write_b128 v254, v[208:211] offset:53248\n\t" \
    "ds_write_b128 v254, v[212:215] offset:54272\n\t" \
    "ds_write_b128 v254, v[216:219] offset:55296\n\t" \
    "ds_write_b128 v254, v[220:223] offset:56320\n\t" \
    "ds_write_b128 v254, v[224:227] offset:57344\n\t" \
    "ds_write_b128 v254, v[228:231] offset:58368\n\t" \
    "ds_write_b128 v254, v[232:235] offset:59392\n\t" \
    "ds_write_b128 v254, v[236:239] offset:60416\n\t" \
    "s_waitcnt vmcnt(0) lgkmcnt(0)\n\t" \
    "s_barrier \n\t" \
    "ds_read_b128 v[224:227], v253 offset:0\n\t" \
    "ds_read_b128 v[244:247], v254 offset:53248\n\t" \
    "ds_read_b128 v[228:231], v253 offset:20480\n\t" \
    "ds_read_b128 v[232:235], v253 offset:1024\n\t" \
    "ds_read_b128 v[236:239], v253 offset:21504\n\t" \
    "ds_read_b128 v[248:251], v254 offset:54272\n\t" \
    ".p2align 8\n\t" \
    ".Ls23_%=:\n\t" \
    "s_waitcnt lgkmcnt(5)\n\t" \
    "v_mfma_f32_16x16x32_bf16 v[204:207], v[40:43], v[224:227], 0\n\t" \
    "ds_read_b128 v[240:243], v253 offset:2048\n\t" \
    "v_mfma_f32_16x16x32_bf16 v[208:211], v[48:51], v[224:227], 0\n\t" \
    "s_waitcnt lgkmcnt(5)\n\t" \
    "v_mfma_f32_16x16x32_bf16 v[200:203], v[244:247], v[224:227], 0\n\t" \
    "s_waitcnt lgkmcnt(4)\n\t" \
    "v_mfma_f32_16x16x32_bf16 v[216:219], v[40:43], v[228:231], 0\n\t" \
    "ds_read_b128 v[224:227], v253 offset:22528\n\t" \
    "v_mfma_f32_16x16x32_bf16 v[220:223], v[48:51], v[228:231], 0\n\t" \
    "v_mfma_f32_16x16x32_bf16 v[212:215], v[244:247], v[228:231], 0\n\t" \
    "ds_read_b128 v[244:247], v254 offset:55296\n\t" \
    "s_waitcnt lgkmcnt(5)\n\t" \
    "v_mfma_f32_16x16x32_bf16 v[204:207], v[44:47], v[232:235], v[204:207]\n\t" \
    "ds_read_b128 v[228:231], v253 offset:3072\n\t" \
    "v_mfma_f32_16x16x32_bf16 v[208:211], v[52:55], v[232:235], v[208:211]\n\t" \
    "s_waitcnt lgkmcnt(4)\n\t" \
    "v_mfma_f32_16x16x32_bf16 v[200:203], v[248:251], v[232:235], v[200:203]\n\t" \
    "v_mfma_f32_16x16x32_bf16 v[216:219], v[44:47], v[236:239], v[216:219]\n\t" \
    "ds_read_b128 v[232:235], v253 offset:23552\n\t" \
    "v_mfma_f32_16x16x32_bf16 v[220:223], v[52:55], v[236:239], v[220:223]\n\t" \
    "v_mfma_f32_16x16x32_bf16 v[212:215], v[248:251], v[236:239], v[212:215]\n\t" \
    "ds_read_b128 v[248:251], v254 offset:56320\n\t" \
    "s_waitcnt lgkmcnt(5)\n\t" \
    "v_mfma_f32_16x16x32_bf16 v[204:207], v[64:67], v[240:243], v[204:207]\n\t" \
    "ds_read_b128 v[236:239], v253 offset:4096\n\t" \
    "v_mfma_f32_16x16x32_bf16 v[208:211], v[72:75], v[240:243], v[208:211]\n\t" \
    "v_mfma_f32_16x16x32_bf16 v[200:203], v[56:59], v[240:243], v[200:203]\n\t" \
    "s_waitcnt lgkmcnt(5)\n\t" \
    "v_mfma_f32_16x16x32_bf16 v[216:219], v[64:67], v[224:227], v[216:219]\n\t" \
    "ds_read_b128 v[240:243], v253 offset:24576\n\t" \
    "v_mfma_f32_16x16x32_bf16 v[220:223], v[72:75], v[224:227], v[220:223]\n\t" \
    "v_mfma_f32_16x16x32_bf16 v[212:215], v[56:59], v[224:227], v[212:215]\n\t" \
    "s_waitcnt lgkmcnt(4)\n\t" \
    "v_mfma_f32_16x16x32_bf16 v[204:207], v[68:71], v[228:231], v[204:207]\n\t" \
    "ds_read_b128 v[224:227], v253 offset:5120\n\t" \
    "v_mfma_f32_16x16x32_bf16 v[208:211], v[76:79], v[228:231], v[208:211]\n\t" \
    "v_mfma_f32_16x16x32_bf16 v[200:203], v[60:63], v[228:231], v[200:203]\n\t" \
    "s_waitcnt lgkmcnt(4)\n\t" \
    "v_mfma_f32_16x16x32_bf16 v[216:219], v[68:71], v[232:235], v[216:219]\n\t" \
    "ds_read_b128 v[228:231], v253 offset:25600\n\t" \
    "v_mfma_f32_16x16x32_bf16 v[220:223], v[76:79], v[232:235], v[220:223]\n\t" \
    "v_mfma_f32_16x16x32_bf16 v[212:215], v[60:63], v[232:235], v[212:215]\n\t" \
    "s_waitcnt lgkmcnt(3)\n\t" \
    "v_mfma_f32_16x16x32_bf16 v[204:207], v[80:83], v[236:239], v[204:207]\n\t" \
    "ds_read_b128 v[232:235], v253 offset:6144\n\t" \
    "v_mfma_f32_16x16x32_bf16 v[208:211], v[88:91], v[236:239], v[208:211]\n\t" \
    "v_mfma_f32_16x16x32_bf16 v[200:203], v[244:247], v[236:239], v[200:203]\n\t" \
    "s_waitcnt lgkmcnt(3)\n\t" \
    "v_mfma_f32_16x16x32_bf16 v[216:219], v[80:83], v[240:243], v[216:219]\n\t" \
    "ds_read_b128 v[236:239], v253 offset:26624\n\t" \
    "v_mfma_f32_16x16x32_bf16 v[220:223], v[88:91], v[240:243], v[220:223]\n\t" \
    "v_mfma_f32_16x16x32_bf16 v[212:215], v[244:247], v[240:243], v[212:215]\n\t" \
    "ds_read_b128 v[244:247], v254 offset:51200\n\t" \
    "s_waitcnt lgkmcnt(4)\n\t" \
    "v_mfma_f32_16x16x32_bf16 v[204:207], v[84:87], v[224:227], v[204:207]\n\t" \
    "ds_read_b128 v[240:243], v253 offset:7168\n\t" \
    "v_mfma_f32_16x16x32_bf16 v[208:211], v[92:95], v[224:227], v[208:211]\n\t" \
    "v_mfma_f32_16x16x32_bf16 v[200:203], v[248:251], v[224:227], v[200:203]\n\t" \
    "s_waitcnt lgkmcnt(4)\n\t" \
    "v_mfma_f32_16x16x32_bf16 v[216:219], v[84:87], v[228:231], v[216:219]\n\t" \
    "ds_read_b128 v[224:227], v253 offset:27648\n\t" \
    "v_mfma_f32_16x16x32_bf16 v[220:223], v[92:95], v[228:231], v[220:223]\n\t" \
    "v_mfma_f32_16x16x32_bf16 v[212:215], v[248:251], v[228:231], v[212:215]\n\t" \
    "ds_read_b128 v[248:251], v254 offset:52224\n\t" \
    "s_waitcnt lgkmcnt(5)\n\t" \
    "v_mfma_f32_16x16x32_bf16 v[204:207], v[104:107], v[232:235], v[204:207]\n\t" \
    "ds_read_b128 v[228:231], v253 offset:8192\n\t" \
    "v_mfma_f32_16x16x32_bf16 v[208:211], v[112:115], v[232:235], v[208:211]\n\t" \
    "v_mfma_f32_16x16x32_bf16 v[200:203], v[96:99], v[232:235], v[200:203]\n\t" \
    "s_waitcnt lgkmcnt(5)\n\t" \
    "v_mfma_f32_16x16x32_bf16 v[216:219], v[104:107], v[236:239], v[216:219]\n\t" \
    "ds_read_b128 v[232:235], v253 offset:28672\n\t" \
    "v_mfma_f32_16x16x32_bf16 v[220:223], v[112:115], v[236:239], v[220:223]\n\t" \
    "v_mfma_f32_16x16x32_bf16 v[212:215], v[96:99], v[236:239], v[212:215]\n\t" \
    "s_waitcnt lgkmcnt(4)\n\t" \
    "v_mfma_f32_16x16x32_bf16 v[204:207], v[108:111], v[240:243], v[204:207]\n\t" \
    "ds_read_b128 v[236:239], v253 offset:9216\n\t" \
    "v_mfma_f32_16x16x32_bf16 v[208:211], v[116:119], v[240:243], v[208:211]\n\t" \
    "v_mfma_f32_16x16x32_bf16 v[200:203], v[100:103], v[240:243], v[200:203]\n\t" \
    "s_waitcnt lgkmcnt(4)\n\t" \
    "v_mfma_f32_16x16x32_bf16 v[216:219], v[108:111], v[224:227], v[216:219]\n\t" \
    "ds_read_b128 v[240:243], v253 offset:29696\n\t" \
    "v_mfma_f32_16x16x32_bf16 v[220:223], v[116:119], v[224:227], v[220:223]\n\t" \
    "v_mfma_f32_16x16x32_bf16 v[212:215], v[100:103], v[224:227], v[212:215]\n\t" \
    "s_waitcnt lgkmcnt(3)\n\t" \
    "v_mfma_f32_16x16x32_bf16 v[204:207], v[0:3], v[228:231], v[204:207]\n\t" \
    "ds_read_b128 v[224:227], v253 offset:10240\n\t" \
    "v_mfma_f32_16x16x32_bf16 v[208:211], v[8:11], v[228:231], v[208:211]\n\t" \
    "v_mfma_f32_16x16x32_bf16 v[200:203], v[244:247], v[228:231], v[200:203]\n\t" \
    "s_waitcnt lgkmcnt(3)\n\t" \
    "v_mfma_f32_16x16x32_bf16 v[216:219], v[0:3], v[232:235], v[216:219]\n\t" \
    "ds_read_b128 v[228:231], v253 offset:30720\n\t" \
    "v_mfma_f32_16x16x32_bf16 v[220:223], v[8:11], v[232:235], v[220:223]\n\t" \
    "v_mfma_f32_16x16x32_bf16 v[212:215], v[244:247], v[232:235], v[212:215]\n\t" \
    "ds_read_b128 v[244:247], v254 offset:57344\n\t" \
    "s_waitcnt lgkmcnt(4)\n\t" \
    "v_mfma_f32_16x16x32_bf16 v[204:207], v[4:7], v[236:239], v[204:207]\n\t" \
    "ds_read_b128 v[232:235], v253 offset:11264\n\t" \
    "v_mfma_f32_16x16x32_bf16 v[208:211], v[12:15], v[236:239], v[208:211]\n\t" \
    "v_mfma_f32_16x16x32_bf16 v[200:203], v[248:251], v[236:239], v[200:203]\n\t" \
    "s_waitcnt lgkmcnt(4)\n\t" \
    "v_mfma_f32_16x16x32_bf16 v[216:219], v[4:7], v[240:243], v[216:219]\n\t" \
    "ds_read_b128 v[236:239], v253 offset:31744\n\t" \
    "v_mfma_f32_16x16x32_bf16 v[220:223], v[12:15], v[240:243], v[220:223]\n\t" \
    "v_mfma_f32_16x16x32_bf16 v[212:215], v[248:251], v[240:243], v[212:215]\n\t" \
    "ds_read_b128 v[248:251], v254 offset:58368\n\t" \
    "s_setprio 2\n\t" \
    "s_waitcnt lgkmcnt(0)\n\t" \
    "s_barrier \n\t" \
    LST7 \
    LJIT \
    "v_mfma_f32_16x16x32_bf16 v[204:207], v[120:123], v[224:227], v[204:207]\n\t" \
    "ds_read_b128 v[240:243], v253 offset:12288\n\t" \
    "v_mfma_f32_16x16x32_bf16 v[208:211], v[128:131], v[224:227], v[208:211]\n\t" \
    "v_mfma_f32_16x16x32_bf16 v[200:203], v[244:247], v[224:227], v[200:203]\n\t" \
    "v_mfma_f32_16x16x32_bf16 v[216:219], v[120:123], v[228:231], v[216:219]\n\t" \
    "ds_read_b128 v[224:227], v253 offset:32768\n\t" \
    "s_and_b32 s44, s40, 3\n\t" \
    "v_mfma_f32_16x16x32_bf16 v[220:223], v[128:131], v[228:231], v[220:223]\n\t" \
    "s_lshl_b32 s44, s44, 22\n\t" \
    "v_mfma_f32_16x16x32_bf16 v[212:215], v[244:247], v[228:231], v[212:215]\n\t" \
    "ds_read_b128 v[244:247], v254 offset:59392\n\t" \
    "s_add_u32 s44, s44, %[mb]\n\t" \
    "v_mfma_f32_16x16x32_bf16 v[204:207], v[124:127], v[232:235], v[204:207]\n\t" \
    "ds_read_b128 v[228:231], v253 offset:13312\n\t" \
    "s_add_u32 s46, s44, 0x8000\n\t" \
    "v_mfma_f32_16x16x32_bf16 v[208:211], v[132:135], v[232:235], v[208:211]\n\t" \
    "s_add_u32 s47, s44, 0x8c00\n\t" \
    "v_mfma_f32_16x16x32_bf16 v[200:203], v[248:251], v[232:235], v[200:203]\n\t" \
    "v_mfma_f32_16x16x32_bf16 v[216:219], v[124:127], v[236:239], v[216:219]\n\t" \
    "ds_read_b128 v[232:235], v253 offset:33792\n\t" \
    "v_mfma_f32_16x16x32_bf16 v[220:223], v[132:135], v[236:239], v[220:223]\n\t" \
    "v_mfma_f32_16x16x32_bf16 v[212:215], v[248:251], v[236:239], v[212:215]\n\t" \
    "ds_read_b128 v[248:251], v254 offset:60416\n\t" \
    "s_waitcnt lgkmcnt(5)\n\t" \
    "v_mfma_f32_16x16x32_bf16 v[204:207], v[144:147], v[240:243], v[204:207]\n\t" \
    "ds_read_b128 v[236:239], v253 offset:16384\n\t" \
    "v_mfma_f32_16x16x32_bf16 v[208:211], v[152:155], v[240:243], v[208:211]\n\t" \
    "v_mfma_f32_16x16x32_bf16 v[200:203], v[136:139], v[240:243], v[200:203]\n\t" \
    "s_waitcnt lgkmcnt(5)\n\t" \
    "v_mfma_f32_16x16x32_bf16 v[216:219], v[144:147], v[224:227], v[216:219]\n\t" \
    "ds_read_b128 v[240:243], v253 offset:36864\n\t" \
    "v_mfma_f32_16x16x32_bf16 v[220:223], v[152:155], v[224:227], v[220:223]\n\t" \
    "v_mfma_f32_16x16x32_bf16 v[212:215], v[136:139], v[224:227], v[212:215]\n\t" \
    "s_waitcnt lgkmcnt(4)\n\t" \
    "v_mfma_f32_16x16x32_bf16 v[204:207], v[148:151], v[228:231], v[204:207]\n\t" \
    "ds_read_b128 v[224:227], v253 offset:17408\n\t" \
    "v_mfma_f32_16x16x32_bf16 v[208:211], v[156:159], v[228:231], v[208:211]\n\t" \
    "v_mfma_f32_16x16x32_bf16 v[200:203], v[140:143], v[228:231], v[200:203]\n\t" \
    "s_waitcnt lgkmcnt(4)\n\t" \
    "v_mfma_f32_16x16x32_bf16 v[216:219], v[148:151], v[232:235], v[216:219]\n\t" \
    "ds_read_b128 v[228:231], v253 offset:37888\n\t" \
    "v_mfma_f32_16x16x32_bf16 v[220:223], v[156:159], v[232:235], v[220:223]\n\t" \
    "v_mfma_f32_16x16x32_bf16 v[212:215], v[140:143], v[232:235], v[212:215]\n\t" \
    "s_waitcnt lgkmcnt(3)\n\t" \
    "v_mfma_f32_16x16x32_bf16 v[204:207], v[160:163], v[236:239], v[204:207]\n\t" \
    "ds_read_b128 v[232:235], v253 offset:14336\n\t" \
    "v_mfma_f32_16x16x32_bf16 v[208:211], v[168:171], v[236:239], v[208:211]\n\t" \
    "v_mfma_f32_16x16x32_bf16 v[200:203], v[244:247], v[236:239], v[200:203]\n\t" \
    "s_waitcnt lgkmcnt(3)\n\t" \
    "v_mfma_f32_16x16x32_bf16 v[216:219], v[160:163], v[240:243], v[216:219]\n\t" \
    "ds_read_b128 v[236:239], v253 offset:15360\n\t" \
    "v_mfma_f32_16x16x32_bf16 v[220:223], v[168:171], v[240:243], v[220:223]\n\t" \
    "v_mfma_f32_16x16x32_bf16 v[212:215], v[244:247], v[240:243], v[212:215]\n\t" \
    "s_waitcnt lgkmcnt(3)\n\t" \
    "v_mfma_f32_16x16x32_bf16 v[204:207], v[164:167], v[224:227], v[204:207]\n\t" \
    "ds_read_b128 v[240:243], v253 offset:18432\n\t" \
    "v_mfma_f32_16x16x32_bf16 v[208:211], v[172:175], v[224:227], v[208:211]\n\t" \
    "v_mfma_f32_16x16x32_bf16 v[200:203], v[248:251], v[224:227], v[200:203]\n\t" \
    "s_waitcnt lgkmcnt(3)\n\t" \
    "v_mfma_f32_16x16x32_bf16 v[216:219], v[164:167], v[228:231], v[216:219]\n\t" \
    "ds_read_b128 v[224:227], v253 offset:19456\n\t" \
    "v_mfma_f32_16x16x32_bf16 v[220:223], v[172:175], v[228:231], v[220:223]\n\t" \
    "v_mfma_f32_16x16x32_bf16 v[212:215], v[248:251], v[228:231], v[212:215]\n\t" \
    "s_waitcnt lgkmcnt(3)\n\t" \
    "v_mfma_f32_16x16x32_bf16 v[204:207], v[24:27], v[232:235], v[204:207]\n\t" \
    "ds_read_b128 v[228:231], v253 offset:34816\n\t" \
    "v_mov_b64 v[244:245], 0\n\t" \
    "v_mfma_f32_16x16x32_bf16 v[208:211], v[32:35], v[232:235], v[208:211]\n\t" \
    "v_mov_b64 v[246:247], 0\n\t" \
    "v_mfma_f32_16x16x32_bf16 v[200:203], v[16:19], v[232:235], v[200:203]\n\t" \
    "v_mov_b64 v[248:249], 0\n\t" \
    "s_waitcnt lgkmcnt(3)\n\t" \
    "v_mfma_f32_16x16x32_bf16 v[204:207], v[28:31], v[236:239], v[204:207]\n\t" \
    "ds_read_b128 v[232:235], v253 offset:35840\n\t" \
    "v_mov_b64 v[250:251], 0\n\t" \
    "v_mfma_f32_16x16x32_bf16 v[208:211], v[36:39], v[236:239], v[208:211]\n\t" \
    "v_mfma_f32_16x16x32_bf16 v[200:203], v[20:23], v[236:239], v[200:203]\n\t" \
    "s_waitcnt lgkmcnt(3)\n\t" \
    "v_mfma_f32_16x16x32_bf16 v[204:207], v[184:187], v[240:243], v[204:207]\n\t" \
    "ds_read_b128 v[236:239], v253 offset:38912\n\t" \
    "v_mfma_f32_16x16x32_bf16 v[208:211], v[192:195], v[240:243], v[208:211]\n\t" \
    "v_mfma_f32_16x16x32_bf16 v[200:203], v[176:179], v[240:243], v[200:203]\n\t" \
    "s_waitcnt lgkmcnt(3)\n\t" \
    "v_mfma_f32_16x16x32_bf16 v[204:207], v[188:191], v[224:227], v[204:207]\n\t" \
    "ds_read_b128 v[240:243], v253 offset:39936\n\t" \
    "v_mfma_f32_16x16x32_bf16 v[208:211], v[196:199], v[224:227], v[208:211]\n\t" \
    "v_mfma_f32_16x16x32_bf16 v[200:203], v[180:183], v[224:227], v[200:203]\n\t" \
    "s_waitcnt lgkmcnt(3)\n\t" \
    "v_mfma_f32_16x16x32_bf16 v[216:219], v[24:27], v[228:231], v[216:219]\n\t" \
    "s_waitcnt lgkmcnt(2)\n\t" \
    "v_mfma_f32_16x16x32_bf16 v[216:219], v[28:31], v[232:235], v[216:219]\n\t" \
    "v_cvt_pk_bf16_f32 v204, v204, v208\n\t" \
    "v_cvt_pk_bf16_f32 v205, v205, v209\n\t" \
    "s_waitcnt lgkmcnt(1)\n\t" \
    "v_mfma_f32_16x16x32_bf16 v[216:219], v[184:187], v[236:239], v[216:219]\n\t" \
    "v_cvt_pk_bf16_f32 v206, v206, v210\n\t" \
    "v_cvt_pk_bf16_f32 v207, v207, v211\n\t" \
    "s_waitcnt lgkmcnt(0)\n\t" \
    "v_mfma_f32_16x16x32_bf16 v[216:219], v[188:191], v[240:243], v[216:219]\n\t" \
    "ds_write_b128 v254, v[204:207] offset:10240\n\t" \
    "v_mfma_f32_16x16x32_bf16 v[220:223], v[32:35], v[228:231], v[220:223]\n\t" \
    "v_mfma_f32_16x16x32_bf16 v[220:223], v[36:39], v[232:235], v[220:223]\n\t" \
    "v_cvt_pk_bf16_f32 v200, v200, v201\n\t" \
    "v_mfma_f32_16x16x32_bf16 v[220:223], v[192:195], v[236:239], v[220:223]\n\t" \
    "v_cvt_pk_bf16_f32 v201, v202, v203\n\t" \
    "ds_write_b64 v254, v[200:201] offset:6152\n\t" \
    "v_mfma_f32_16x16x32_bf16 v[220:223], v[196:199], v[240:243], v[220:223]\n\t" \
    "v_mfma_f32_16x16x32_bf16 v[212:215], v[16:19], v[228:231], v[212:215]\n\t" \
    "v_mfma_f32_16x16x32_bf16 v[212:215], v[20:23], v[232:235], v[212:215]\n\t" \
    "ds_read_b128 v[200:203], v254 offset:51200\n\t" \
    "ds_read_b128 v[204:207], v254 offset:52224\n\t" \
    "v_mfma_f32_16x16x32_bf16 v[212:215], v[176:179], v[236:239], v[212:215]\n\t" \
    "v_mov_b64 v[208:209], 0\n\t" \
    "v_mov_b64 v[210:211], 0\n\t" \
    "v_mfma_f32_16x16x32_bf16 v[212:215], v[180:183], v[240:243], v[212:215]\n\t" \
    "v_cvt_pk_bf16_f32 v216, v216, v220\n\t" \
    "v_cvt_pk_bf16_f32 v217, v217, v221\n\t" \
    "s_setprio 0\n\t" \
    "v_cvt_pk_bf16_f32 v218, v218, v222\n\t" \
    "v_cvt_pk_bf16_f32 v219, v219, v223\n\t" \
    "ds_write_b128 v254, v[216:219] offset:16384\n\t" \
    "ds_read_b128 v[216:219], v254 offset:53248\n\t" \
    "ds_read_b128 v[220:223], v254 offset:54272\n\t" \
    "s_nop 0\n\t" \
    "v_cvt_pk_bf16_f32 v212, v212, v213\n\t" \
    "v_cvt_pk_bf16_f32 v213, v214, v215\n\t" \
    "ds_write_b64 v254, v[212:213] offset:12296\n\t" \
    LST6 \
    LJIT \
    "s_sleep 3\n\t" \
    "s_mov_b32 m0, 0xb000\n\t" \
    "s_nop 0\n\t" \
    "buffer_load_dwordx4 v253, s[60:63], s46 offen sc1 lds\n\t" \
    "s_mov_b32 m0, 0xbc00\n\t" \
    "s_nop 0\n\t" \
    "buffer_load_dwordx4 v253, s[60:63], s47 offen sc1 lds\n\t" \
    "s_waitcnt lgkmcnt(0)\n\t" \
    LFLUSH212 \
    "v_bfe_i32 v212, v253, 5, 1\n\t" \
    "v_and_b32 v212, 0xaaaa, v212\n\t" \
    "v_or_b32 v212, 0x4444, v212\n\t" \
    "v_and_b32 v213, 0xffff, v255\n\t" \
    "v_lshrrev_b32 v214, 16, v255\n\t" \
    ".Ls24_%=:\n\t" \
    "s_waitcnt vmcnt(0)\n\t" \
    "ds_read_b128 v[224:227], v253 offset:45056\n\t" \
    "ds_read_b128 v[228:231], v253 offset:48128\n\t" \
    "s_waitcnt lgkmcnt(1)\n\t" \
    "v_max_u32 v215, v224, v225\n\t" \
    "v_max3_u32 v215, v226, v227, v215\n\t" \
    "s_waitcnt lgkmcnt(0)\n\t" \
    "v_max3_u32 v215, v228, v229, v215\n\t" \
    "v_max3_u32 v215, v230, v231, v215\n\t" \
    "v_cmp_eq_u32 vcc, -1, v215\n\t" \
    "s_cbranch_vccz .Ls25_%=\n\t" \
    "v_max_u32 v215, v224, v225\n\t" \
    "v_max3_u32 v215, v226, v227, v215\n\t" \
    "v_cmp_eq_u32 vcc, -1, v215\n\t" \
    "s_cbranch_vccz .Ls26_%=\n\t" \
    "s_mov_b32 m0, 0xb000\n\t" \
    "s_nop 0\n\t" \
    "buffer_load_dwordx4 v253, s[60:63], s46 offen sc1 lds\n\t" \
    ".Ls26_%=:\n\t" \
    "v_max_u32 v215, v228, v229\n\t" \
    "v_max3_u32 v215, v230, v231, v215\n\t" \
    "v_cmp_eq_u32 vcc, -1, v215\n\t" \
    "s_cbranch_vccz .Ls27_%=\n\t" \
    "s_mov_b32 m0, 0xbc00\n\t" \
    "s_nop 0\n\t" \
    "buffer_load_dwordx4 v253, s[60:63], s47 offen sc1 lds\n\t" \
    ".Ls27_%=:\n\t" \
    "s_cmp_eq_u32 s41, 0\n\t" \
    "s_cbranch_scc1 .Ls25_%=\n\t" \
    "s_sub_u32 s41, s41, 1\n\t" \
    "s_branch .Ls24_%=\n\t" \
    ".Ls25_%=:\n\t" \
    LST2 \
    LJIT \
    "s_setprio 3\n\t" \
    "s_waitcnt lgkmcnt(0)\n\t" \
    "s_barrier \n\t" \
    LST3 \
    LJIT \
    "ds_read_b128 v[232:235], v253 offset:40960\n\t" \
    "ds_read_b128 v[236:239], v253 offset:41984\n\t" \
    "ds_read_b128 v[240:243], v253 offset:43008\n\t" \
    "v_smfmac_f32_16x16x64_bf16 v[244:247], v[224:227], v[0:7], v212\n\t" \
    "v_smfmac_f32_16x16x64_bf16 v[248:251], v[224:227], v[8:15], v212\n\t" \
    "v_smfmac_f32_16x16x64_bf16 v[208:211], v[224:227], v[200:207], v212\n\t" \
    "ds_read_b128 v[200:203], v254 offset:55296\n\t" \
    "ds_read_b128 v[204:207], v254 offset:56320\n\t" \
    "ds_read_b128 v[224:227], v253 offset:44032\n\t" \
    "v_smfmac_f32_16x16x64_bf16 v[208:211], v[228:231], v[16:23], v212\n\t" \
    "v_smfmac_f32_16x16x64_bf16 v[244:247], v[228:231], v[24:31], v212\n\t" \
    "v_smfmac_f32_16x16x64_bf16 v[248:251], v[228:231], v[32:39], v212\n\t" \
    "ds_read_b128 v[228:231], v253 offset:46080\n\t" \
    "s_waitcnt lgkmcnt(6)\n\t" \
    "v_smfmac_f32_16x16x64_bf16 v[208:211], v[232:235], v[216:223], v212\n\t" \
    "ds_read_b128 v[216:219], v254 offset:57344\n\t" \
    "ds_read_b128 v[220:223], v254 offset:58368\n\t" \
    "v_smfmac_f32_16x16x64_bf16 v[244:247], v[232:235], v[40:47], v212\n\t" \
    "v_smfmac_f32_16x16x64_bf16 v[248:251], v[232:235], v[48:55], v212\n\t" \
    "ds_read_b128 v[232:235], v253 offset:47104\n\t" \
    "s_waitcnt lgkmcnt(8)\n\t" \
    "v_smfmac_f32_16x16x64_bf16 v[208:211], v[236:239], v[56:63], v212\n\t" \
    "v_smfmac_f32_16x16x64_bf16 v[244:247], v[236:239], v[64:71], v212\n\t" \
    "v_smfmac_f32_16x16x64_bf16 v[248:251], v[236:239], v[72:79], v212\n\t" \
    "ds_read_b128 v[236:239], v253 offset:49152\n\t" \
    "s_waitcnt lgkmcnt(6)\n\t" \
    "v_smfmac_f32_16x16x64_bf16 v[208:211], v[240:243], v[200:207], v212\n\t" \
    "ds_read_b128 v[200:203], v254 offset:59392\n\t" \
    "ds_read_b128 v[204:207], v254 offset:60416\n\t" \
    "v_smfmac_f32_16x16x64_bf16 v[244:247], v[240:243], v[80:87], v212\n\t" \
    "v_smfmac_f32_16x16x64_bf16 v[248:251], v[240:243], v[88:95], v212\n\t" \
    "ds_read_b128 v[240:243], v253 offset:50176\n\t" \
    "s_waitcnt lgkmcnt(8)\n\t" \
    "v_smfmac_f32_16x16x64_bf16 v[208:211], v[224:227], v[96:103], v212\n\t" \
    "v_smfmac_f32_16x16x64_bf16 v[244:247], v[224:227], v[104:111], v212\n\t" \
    "v_smfmac_f32_16x16x64_bf16 v[248:251], v[224:227], v[112:119], v212\n\t" \
    "s_waitcnt lgkmcnt(5)\n\t" \
    "v_smfmac_f32_16x16x64_bf16 v[208:211], v[228:231], v[216:223], v212\n\t" \
    "s_waitcnt lgkmcnt(4)\n\t" \
    "v_smfmac_f32_16x16x64_bf16 v[208:211], v[232:235], v[136:143], v212\n\t" \
    "s_waitcnt lgkmcnt(1)\n\t" \
    "v_smfmac_f32_16x16x64_bf16 v[208:211], v[236:239], v[200:207], v212\n\t" \
    "s_waitcnt lgkmcnt(0)\n\t" \
    "v_smfmac_f32_16x16x64_bf16 v[208:211], v[240:243], v[176:183], v212\n\t" \
    "v_smfmac_f32_16x16x64_bf16 v[244:247], v[228:231], v[120:127], v212\n\t" \
    "v_smfmac_f32_16x16x64_bf16 v[244:247], v[232:235], v[144:151], v212\n\t" \
    "v_smfmac_f32_16x16x64_bf16 v[244:247], v[236:239], v[160:167], v212\n\t" \
    "v_add_f32 v208, v208, v210\n\t" \
    "v_add_f32 v209, v209, v211\n\t" \
    "v_cvt_pk_bf16_f32 v208, v208, v209\n\t" \
    "v_smfmac_f32_16x16x64_bf16 v[244:247], v[240:243], v[184:191], v212\n\t" \
    "ds_write_b16 v213, v208 offset:0\n\t" \
    "ds_write_b16_d16_hi v213, v208 offset:16\n\t" \
    "v_smfmac_f32_16x16x64_bf16 v[248:251], v[228:231], v[128:135], v212\n\t" \
    "v_smfmac_f32_16x16x64_bf16 v[248:251], v[232:235], v[152:159], v212\n\t" \
    "v_smfmac_f32_16x16x64_bf16 v[248:251], v[236:239], v[168:175], v212\n\t" \
    "v_add_f32 v244, v244, v246\n\t" \
    "v_add_f32 v245, v245, v247\n\t" \
    "v_smfmac_f32_16x16x64_bf16 v[248:251], v[240:243], v[192:199], v212\n\t" \
    "s_nop 7\n\t" \
    "v_add_f32 v248, v248, v250\n\t" \
    "v_add_f32 v249, v249, v251\n\t" \
    "v_cvt_pk_bf16_f32 v244, v244, v248\n\t" \
    "v_cvt_pk_bf16_f32 v245, v245, v249\n\t" \
    "ds_write2_b32 v214, v244, v245 offset1:4\n\t" \
    "s_setprio 0\n\t" \
    "s_sleep 1\n\t" \
    "ds_read_b128 v[200:203], v254 offset:12288\n\t" \
    "ds_read_b128 v[224:227], v254 offset:24576\n\t" \
    "ds_read_b128 v[204:207], v254 offset:13312\n\t" \
    "ds_read_b128 v[228:231], v254 offset:25600\n\t" \
    "ds_read_b128 v[208:211], v254 offset:14336\n\t" \
    "ds_read_b128 v[232:235], v254 offset:26624\n\t" \
    "ds_read_b128 v[212:215], v254 offset:15360\n\t" \
    "ds_read_b128 v[236:239], v254 offset:27648\n\t" \
    "ds_read_b128 v[216:219], v254 offset:16384\n\t" \
    "ds_read_b128 v[220:223], v254 offset:17408\n\t" \
    "s_and_b32 s44, s40, 3\n\t" \
    "s_lshl_b32 s44, s44, 17\n\t" \
    "s_add_u32 s50, s44, %[pb]\n\t" \
    "s_add_u32 s51, s44, %[pm]\n\t" \
    "s_mov_b32 s52, 0xf0000\n\t" \
    "s_mov_b32 s53, 0xfff00ff\n\t" \
    "s_mov_b32 s54, 0x1f0001\n\t" \
    "s_mov_b32 s55, 0x1fff01ff\n\t" \
    "s_mov_b32 s56, 0x3f0003\n\t" \
    "s_mov_b32 s57, 0x3fff03ff\n\t" \
    "s_mov_b32 s58, 0x7f0007\n\t" \
    "s_mov_b32 s59, 0x7fff07ff\n\t" \
    "s_waitcnt lgkmcnt(8)\n\t" \
    "v_mfma_f32_16x16x32_bf16 v[240:243], v[224:227], v[200:203], 0\n\t" \
    "ds_read_b128 v[224:227], v254 offset:28672\n\t" \
    "s_waitcnt lgkmcnt(7)\n\t" \
    "v_mfma_f32_16x16x32_bf16 v[240:243], v[228:231], v[204:207], v[240:243]\n\t" \
    "ds_read_b128 v[228:231], v254 offset:29696\n\t" \
    "s_waitcnt lgkmcnt(6)\n\t" \
    "v_mfma_f32_16x16x32_bf16 v[240:243], v[232:235], v[208:211], v[240:243]\n\t" \
    "ds_read_b128 v[232:235], v254 offset:18432\n\t" \
    "s_waitcnt lgkmcnt(5)\n\t" \
    "v_mfma_f32_16x16x32_bf16 v[240:243], v[236:239], v[212:215], v[240:243]\n\t" \
    "ds_read_b128 v[236:239], v254 offset:19456\n\t" \
    "s_waitcnt lgkmcnt(3)\n\t" \
    "v_mfma_f32_16x16x32_bf16 v[240:243], v[224:227], v[216:219], v[240:243]\n\t" \
    "ds_read_b128 v[224:227], v254 offset:20480\n\t" \
    "s_waitcnt lgkmcnt(3)\n\t" \
    "v_mfma_f32_16x16x32_bf16 v[240:243], v[228:231], v[220:223], v[240:243]\n\t" \
    "ds_read_b128 v[228:231], v254 offset:21504\n\t" \
    "s_waitcnt lgkmcnt(3)\n\t" \
    "v_mfma_f32_16x16x32_bf16 v[244:247], v[232:235], v[200:203], 0\n\t" \
    "ds_read_b128 v[232:235], v254 offset:22528\n\t" \
    "s_waitcnt lgkmcnt(3)\n\t" \
    "v_mfma_f32_16x16x32_bf16 v[244:247], v[236:239], v[204:207], v[244:247]\n\t" \
    "ds_read_b128 v[236:239], v254 offset:23552\n\t" \
    "v_and_b32 v252, 0xffffff7f, v254\n\t" \
    "s_waitcnt lgkmcnt(0)\n\t" \
    "s_barrier \n\t" \
    LST4 \
    LJIT \
    "v_mfma_f32_16x16x32_bf16 v[244:247], v[224:227], v[208:211], v[244:247]\n\t" \
    "ds_read_b128 v[224:227], v252 offset:3072\n\t" \
    "v_mfma_f32_16x16x32_bf16 v[244:247], v[228:231], v[212:215], v[244:247]\n\t" \
    "ds_read_b128 v[228:231], v252 offset:3200\n\t" \
    "v_mfma_f32_16x16x32_bf16 v[244:247], v[232:235], v[216:219], v[244:247]\n\t" \
    "ds_read_b128 v[232:235], v252 offset:4096\n\t" \
    "v_mfma_f32_16x16x32_bf16 v[244:247], v[236:239], v[220:223], v[244:247]\n\t" \
    "ds_read_b128 v[236:239], v252 offset:4224\n\t" \
    "s_waitcnt lgkmcnt(3)\n\t" \
    "v_mfma_f32_16x16x32_bf16 v[248:251], v[224:227], v[200:203], 0\n\t" \
    "ds_read_b128 v[224:227], v252 offset:5120\n\t" \
    "s_waitcnt lgkmcnt(3)\n\t" \
    "v_mfma_f32_16x16x32_bf16 v[248:251], v[228:231], v[204:207], v[248:251]\n\t" \
    "ds_read_b128 v[228:231], v252 offset:5248\n\t" \
    "s_waitcnt lgkmcnt(3)\n\t" \
    "v_mfma_f32_16x16x32_bf16 v[248:251], v[232:235], v[208:211], v[248:251]\n\t" \
    "s_waitcnt lgkmcnt(2)\n\t" \
    "v_mfma_f32_16x16x32_bf16 v[248:251], v[236:239], v[212:215], v[248:251]\n\t" \
    "s_waitcnt lgkmcnt(1)\n\t" \
    "v_mfma_f32_16x16x32_bf16 v[248:251], v[224:227], v[216:219], v[248:251]\n\t" \
    "s_waitcnt lgkmcnt(0)\n\t" \
    "v_mfma_f32_16x16x32_bf16 v[248:251], v[228:231], v[220:223], v[248:251]\n\t" \
    "v_mov_b32 v226, 0xff7fffff\n\t" \
    "v_cndmask_b32 v240, v240, v226, s[52:53]\n\t" \
    "v_cndmask_b32 v241, v241, v226, s[54:55]\n\t" \
    "v_cndmask_b32 v242, v242, v226, s[56:57]\n\t" \
    "v_cndmask_b32 v243, v243, v226, s[58:59]\n\t" \
    "v_max3_f32 v224, v240, v241, v242\n\t" \
    "v_max3_f32 v225, v244, v245, v246\n\t" \
    "v_max3_f32 v224, v224, v243, v247\n\t" \
    "v_lshrrev_b32 v200, 5, v253\n\t" \
    "v_and_b32 v201, 0xf0, v253\n\t" \
    "v_and_or_b32 v200, v200, 8, v201\n\t" \
    "v_max3_f32 v224, v224, v248, v249\n\t" \
    "v_max3_f32 v225, v225, v250, v251\n\t" \
    "v_max_f32 v224, v224, v225\n\t" \
    "v_mov_b32 v225, v224\n\t" \
    "s_nop 1\n\t" \
    "v_permlane16_swap_b32 v224, v225\n\t" \
    "v_max_f32 v224, v224, v225\n\t" \
    "v_mov_b32 v225, v224\n\t" \
    "s_nop 1\n\t" \
    "v_permlane32_swap_b32 v224, v225\n\t" \
    "v_max_f32 v224, v224, v225\n\t" \
    "v_mul_f32 v224, %[scl], v224\n\t" \
    "v_fma_f32 v240, v240, %[scl], -v224\n\t" \
    "v_fma_f32 v241, v241, %[scl], -v224\n\t" \
    "v_exp_f32 v240, v240\n\t" \
    "v_fma_f32 v242, v242, %[scl], -v224\n\t" \
    "v_exp_f32 v241, v241\n\t" \
    "v_fma_f32 v243, v243, %[scl], -v224\n\t" \
    "v_exp_f32 v242, v242\n\t" \
    "v_fma_f32 v244, v244, %[scl], -v224\n\t" \
    "v_exp_f32 v243, v243\n\t" \
    "v_fma_f32 v245, v245, %[scl], -v224\n\t" \
    "v_exp_f32 v244, v244\n\t" \
    "v_fma_f32 v246, v246, %[scl], -v224\n\t" \
    "v_exp_f32 v245, v245\n\t" \
    "v_fma_f32 v247, v247, %[scl], -v224\n\t" \
    "v_exp_f32 v246, v246\n\t" \
    "v_fma_f32 v248, v248, %[scl], -v224\n\t" \
    "v_exp_f32 v247, v247\n\t" \
    "v_fma_f32 v249, v249, %[scl], -v224\n\t" \
    "v_exp_f32 v248, v248\n\t" \
    "v_fma_f32 v250, v250, %[scl], -v224\n\t" \
    "v_exp_f32 v249, v249\n\t" \
    "v_fma_f32 v251, v251, %[scl], -v224\n\t" \
    "v_exp_f32 v250, v250\n\t" \
    "v_exp_f32 v251, v251\n\t" \
    "v_add_f32 v224, v240, v241\n\t" \
    "v_add_f32 v225, v242, v243\n\t" \
    "v_add_f32 v226, v244, v245\n\t" \
    "v_add_f32 v227, v246, v247\n\t" \
    "v_add_f32 v232, v248, v249\n\t" \
    "v_add_f32 v233, v250, v251\n\t" \
    "v_add_f32 v224, v224, v225\n\t" \
    "v_add_f32 v226, v226, v227\n\t" \
    "v_add_f32 v232, v232, v233\n\t" \
    "v_add_f32 v224, v224, v226\n\t" \
    "v_fma_f32 v224, v232, 0.5, v224\n\t" \
    "v_mov_b32 v225, v224\n\t" \
    "s_nop 1\n\t" \
    "v_permlane16_swap_b32 v224, v225\n\t" \
    "v_add_f32 v224, v224, v225\n\t" \
    "v_mov_b32 v225, v224\n\t" \
    "s_nop 1\n\t" \
    "v_permlane32_swap_b32 v224, v225\n\t" \
    "v_add_f32 v224, v224, v225\n\t" \
    "v_rcp_f32 v224, v224\n\t" \
    "s_nop 0\n\t" \
    "v_mul_f32 v240, v240, v224\n\t" \
    "v_mul_f32 v241, v241, v224\n\t" \
    "v_mul_f32 v242, v242, v224\n\t" \
    "v_mul_f32 v243, v243, v224\n\t" \
    "v_mul_f32 v244, v244, v224\n\t" \
    "v_mul_f32 v245, v245, v224\n\t" \
    "v_mul_f32 v246, v246, v224\n\t" \
    "v_mul_f32 v247, v247, v224\n\t" \
    "v_mul_f32 v248, v248, v224\n\t" \
    "v_mul_f32 v249, v249, v224\n\t" \
    "v_mul_f32 v250, v250, v224\n\t" \
    "v_mul_f32 v251, v251, v224\n\t" \
    "v_cvt_pk_bf16_f32 v204, |v244|, |v240|\n\t" \
    "v_cvt_pk_bf16_f32 v205, |v245|, |v241|\n\t" \
    "v_cvt_pk_bf16_f32 v206, |v246|, |v242|\n\t" \
    "v_cvt_pk_bf16_f32 v207, |v247|, |v243|\n\t" \
    "v_cvt_pk_bf16_f32 v208, |v248|, |v249|\n\t" \
    "v_cvt_pk_bf16_f32 v209, |v250|, |v251|\n\t" \
    "buffer_store_dwordx4 v[204:207], v253, s[60:63], s50 offen sc1\n\t" \
    "s_mov_b32 exec_lo, -1\n\t" \
    "s_mov_b32 exec_hi, 0\n\t" \
    "buffer_store_dwordx2 v[208:209], v200, s[60:63], s51 offen sc1\n\t" \
    "s_mov_b32 exec_hi, -1\n\t" \
    LST5 \
    LJIT \
    "ds_read_b128 v[224:227], v253 offset:0\n\t" \
    "ds_read_b128 v[244:247], v254 offset:53248\n\t" \
    "ds_read_b128 v[228:231], v253 offset:20480\n\t" \
    "ds_read_b128 v[232:235], v253 offset:1024\n\t" \
    "ds_read_b128 v[236:239], v253 offset:21504\n\t" \
    "ds_read_b128 v[248:251], v254 offset:54272\n\t" \
    "s_add_u32 s40, s40, 1\n\t" \
    "s_cmp_lg_u32 s40, 64\n\t" \
    "s_cbranch_scc1 .Ls23_%=\n\t" \
    "s_branch .Lend%=\n\t" \
    ".Ls1_%=:\n\t" \
    "v_add_u32 v244, 0x18000, v243\n\t" \
    "s_waitcnt vmcnt(12) lgkmcnt(0)\n\t" \
    "s_barrier \n\t" \
    "s_add_u32 m0, %[wm], 0x12000\n\t" \
    "s_mov_b32 s44, 0x1e0000\n\t" \
    "buffer_load_dwordx4 v240, %[wr], s44 offen offset:0 nt lds\n\t" \
    "buffer_load_dwordx4 v241, %[wr], s44 offen offset:1024 nt lds\n\t" \
    "buffer_load_dwordx4 v242, %[wr], s44 offen offset:2048 nt lds\n\t" \
    "ds_read_b64_tr_b16 v[56:57], v244 offset:0\n\t" \
    "ds_read_b64_tr_b16 v[58:59], v244 offset:1536\n\t" \
    "ds_read_b64_tr_b16 v[64:65], v244 offset:32\n\t" \
    "ds_read_b64_tr_b16 v[66:67], v244 offset:1568\n\t" \
    "ds_read_b64_tr_b16 v[72:73], v244 offset:64\n\t" \
    "ds_read_b64_tr_b16 v[74:75], v244 offset:1600\n\t" \
    "v_add_u32 v244, 0x1e000, v243\n\t" \
    "s_waitcnt vmcnt(12) lgkmcnt(0)\n\t" \
    "s_barrier \n\t" \
    "s_add_u32 m0, %[wm], 0x18000\n\t" \
    "s_mov_b32 s44, 0x240000\n\t" \
    "buffer_load_dwordx4 v240, %[wr], s44 offen offset:0 nt lds\n\t" \
    "buffer_load_dwordx4 v241, %[wr], s44 offen offset:1024 nt lds\n\t" \
    "buffer_load_dwordx4 v242, %[wr], s44 offen offset:2048 nt lds\n\t" \
    "ds_read_b64_tr_b16 v[60:61], v244 offset:0\n\t" \
    "ds_read_b64_tr_b16 v[62:63], v244 offset:1536\n\t" \
    "ds_read_b64_tr_b16 v[68:69], v244 offset:32\n\t" \
    "ds_read_b64_tr_b16 v[70:71], v244 offset:1568\n\t" \
    "ds_read_b64_tr_b16 v[76:77], v244 offset:64\n\t" \
    "ds_read_b64_tr_b16 v[78:79], v244 offset:1600\n\t" \
    "v_add_u32 v244, 0x0, v243\n\t" \
    "s_waitcnt vmcnt(12) lgkmcnt(0)\n\t" \
    "s_barrier \n\t" \
    "s_add_u32 m0, %[wm], 0x1e000\n\t" \
    "s_mov_b32 s44, 0x2a0000\n\t" \
    "buffer_load_dwordx4 v240, %[wr], s44 offen offset:0 nt lds\n\t" \
    "buffer_load_dwordx4 v241, %[wr], s44 offen offset:1024 nt lds\n\t" \
    "buffer_load_dwordx4 v242, %[wr], s44 offen offset:2048 nt lds\n\t" \
    "ds_read_b64_tr_b16 v[96:97], v244 offset:0\n\t" \
    "ds_read_b64_tr_b16 v[98:99], v244 offset:1536\n\t" \
    "ds_read_b64_tr_b16 v[104:105], v244 offset:32\n\t" \
    "ds_read_b64_tr_b16 v[106:107], v244 offset:1568\n\t" \
    "ds_read_b64_tr_b16 v[112:113], v244 offset:64\n\t" \
    "ds_read_b64_tr_b16 v[114:115], v244 offset:1600\n\t" \
    "v_add_u32 v244, 0x6000, v243\n\t" \
    "s_waitcnt vmcnt(12) lgkmcnt(0)\n\t" \
    "s_barrier \n\t" \
    "s_add_u32 m0, %[wm], 0x0\n\t" \
    "s_mov_b32 s44, 0x300000\n\t" \
    "buffer_load_dwordx4 v240, %[wr], s44 offen offset:0 nt lds\n\t" \
    "buffer_load_dwordx4 v241, %[wr], s44 offen offset:1024 nt lds\n\t" \
    "buffer_load_dwordx4 v242, %[wr], s44 offen offset:2048 nt lds\n\t" \
    "ds_read_b64_tr_b16 v[100:101], v244 offset:0\n\t" \
    "ds_read_b64_tr_b16 v[102:103], v244 offset:1536\n\t" \
    "ds_read_b64_tr_b16 v[108:109], v244 offset:32\n\t" \
    "ds_read_b64_tr_b16 v[110:111], v244 offset:1568\n\t" \
    "ds_read_b64_tr_b16 v[116:117], v244 offset:64\n\t" \
    "ds_read_b64_tr_b16 v[118:119], v244 offset:1600\n\t" \
    "v_add_u32 v244, 0xc000, v243\n\t" \
    "s_waitcnt vmcnt(12) lgkmcnt(0)\n\t" \
    "s_barrier \n\t" \
    "s_add_u32 m0, %[wm], 0x6000\n\t" \
    "s_mov_b32 s44, 0x360000\n\t" \
    "buffer_load_dwordx4 v240, %[wr], s44 offen offset:0 nt lds\n\t" \
    "buffer_load_dwordx4 v241, %[wr], s44 offen offset:1024 nt lds\n\t" \
    "buffer_load_dwordx4 v242, %[wr], s44 offen offset:2048 nt lds\n\t" \
    "ds_read_b64_tr_b16 v[200:201], v244 offset:0\n\t" \
    "ds_read_b64_tr_b16 v[202:203], v244 offset:1536\n\t" \
    "ds_read_b64_tr_b16 v[0:1], v244 offset:32\n\t" \
    "ds_read_b64_tr_b16 v[2:3], v244 offset:1568\n\t" \
    "ds_read_b64_tr_b16 v[8:9], v244 offset:64\n\t" \
    "ds_read_b64_tr_b16 v[10:11], v244 offset:1600\n\t" \
    "v_add_u32 v244, 0x12000, v243\n\t" \
    "s_waitcnt vmcnt(12) lgkmcnt(0)\n\t" \
    "s_barrier \n\t" \
    "s_add_u32 m0, %[wm], 0xc000\n\t" \
    "s_mov_b32 s44, 0x3c0000\n\t" \
    "buffer_load_dwordx4 v240, %[wr], s44 offen offset:0 nt lds\n\t" \
    "buffer_load_dwordx4 v241, %[wr], s44 offen offset:1024 nt lds\n\t" \
    "buffer_load_dwordx4 v242, %[wr], s44 offen offset:2048 nt lds\n\t" \
    "ds_read_b64_tr_b16 v[204:205], v244 offset:0\n\t" \
    "ds_read_b64_tr_b16 v[206:207], v244 offset:1536\n\t" \
    "ds_read_b64_tr_b16 v[4:5], v244 offset:32\n\t" \
    "ds_read_b64_tr_b16 v[6:7], v244 offset:1568\n\t" \
    "ds_read_b64_tr_b16 v[12:13], v244 offset:64\n\t" \
    "ds_read_b64_tr_b16 v[14:15], v244 offset:1600\n\t" \
    "v_add_u32 v244, 0x18000, v243\n\t" \
    "s_waitcnt vmcnt(12) lgkmcnt(0)\n\t" \
    "s_barrier \n\t" \
    "s_add_u32 m0, %[wm], 0x12000\n\t" \
    "s_mov_b32 s44, 0x420000\n\t" \
    "buffer_load_dwordx4 v240, %[wr], s44 offen offset:0 nt lds\n\t" \
    "buffer_load_dwordx4 v241, %[wr], s44 offen offset:1024 nt lds\n\t" \
    "buffer_load_dwordx4 v242, %[wr], s44 offen offset:2048 nt lds\n\t" \
    "ds_read_b64_tr_b16 v[208:209], v244 offset:0\n\t" \
    "ds_read_b64_tr_b16 v[210:211], v244 offset:1536\n\t" \
    "ds_read_b64_tr_b16 v[40:41], v244 offset:32\n\t" \
    "ds_read_b64_tr_b16 v[42:43], v244 offset:1568\n\t" \
    "ds_read_b64_tr_b16 v[48:49], v244 offset:64\n\t" \
    "ds_read_b64_tr_b16 v[50:51], v244 offset:1600\n\t" \
    "v_add_u32 v244, 0x1e000, v243\n\t" \
    "s_waitcnt vmcnt(12) lgkmcnt(0)\n\t" \
    "s_barrier \n\t" \
    "s_add_u32 m0, %[wm], 0x18000\n\t" \
    "s_mov_b32 s44, 0x480000\n\t" \
    "buffer_load_dwordx4 v240, %[wr], s44 offen offset:0 nt lds\n\t" \
    "buffer_load_dwordx4 v241, %[wr], s44 offen offset:1024 nt lds\n\t" \
    "buffer_load_dwordx4 v242, %[wr], s44 offen offset:2048 nt lds\n\t" \
    "ds_read_b64_tr_b16 v[212:213], v244 offset:0\n\t" \
    "ds_read_b64_tr_b16 v[214:215], v244 offset:1536\n\t" \
    "ds_read_b64_tr_b16 v[44:45], v244 offset:32\n\t" \
    "ds_read_b64_tr_b16 v[46:47], v244 offset:1568\n\t" \
    "ds_read_b64_tr_b16 v[52:53], v244 offset:64\n\t" \
    "ds_read_b64_tr_b16 v[54:55], v244 offset:1600\n\t" \
    "v_add_u32 v244, 0x0, v243\n\t" \
    "s_waitcnt vmcnt(12) lgkmcnt(0)\n\t" \
    "s_barrier \n\t" \
    "v_mov_b32 v246, 0\n\t" \
    "buffer_load_dword v246, v246, s[60:63], %[mo] offen sc1\n\t" \
    "s_add_u32 m0, %[wm], 0x1e000\n\t" \
    "s_mov_b32 s44, 0x4e0000\n\t" \
    "buffer_load_dwordx4 v240, %[wr], s44 offen offset:0 nt lds\n\t" \
    "buffer_load_dwordx4 v241, %[wr], s44 offen offset:1024 nt lds\n\t" \
    "buffer_load_dwordx4 v242, %[wr], s44 offen offset:2048 nt lds\n\t" \
    "ds_read_b64_tr_b16 v[136:137], v244 offset:0\n\t" \
    "ds_read_b64_tr_b16 v[138:139], v244 offset:1536\n\t" \
    "ds_read_b64_tr_b16 v[144:145], v244 offset:32\n\t" \
    "ds_read_b64_tr_b16 v[146:147], v244 offset:1568\n\t" \
    "ds_read_b64_tr_b16 v[152:153], v244 offset:64\n\t" \
    "ds_read_b64_tr_b16 v[154:155], v244 offset:1600\n\t" \
    "v_add_u32 v244, 0x6000, v243\n\t" \
    "s_waitcnt vmcnt(12) lgkmcnt(0)\n\t" \
    "s_barrier \n\t" \
    "s_add_u32 m0, %[wm], 0x0\n\t" \
    "s_mov_b32 s44, 0x540000\n\t" \
    "buffer_load_dwordx4 v240, %[wr], s44 offen offset:0 nt lds\n\t" \
    "buffer_load_dwordx4 v241, %[wr], s44 offen offset:1024 nt lds\n\t" \
    "buffer_load_dwordx4 v242, %[wr], s44 offen offset:2048 nt lds\n\t" \
    "ds_read_b64_tr_b16 v[140:141], v244 offset:0\n\t" \
    "ds_read_b64_tr_b16 v[142:143], v244 offset:1536\n\t" \
    "ds_read_b64_tr_b16 v[148:149], v244 offset:32\n\t" \
    "ds_read_b64_tr_b16 v[150:151], v244 offset:1568\n\t" \
    "ds_read_b64_tr_b16 v[156:157], v244 offset:64\n\t" \
    "ds_read_b64_tr_b16 v[158:159], v244 offset:1600\n\t" \
    "v_add_u32 v244, 0xc000, v243\n\t" \
    "s_waitcnt vmcnt(12) lgkmcnt(0)\n\t" \
    "s_barrier \n\t" \
    "s_add_u32 m0, %[wm], 0x6000\n\t" \
    "s_mov_b32 s44, 0x5a0000\n\t" \
    "buffer_load_dwordx4 v240, %[wr], s44 offen offset:0 nt lds\n\t" \
    "buffer_load_dwordx4 v241, %[wr], s44 offen offset:1024 nt lds\n\t" \
    "buffer_load_dwordx4 v242, %[wr], s44 offen offset:2048 nt lds\n\t" \
    "ds_read_b64_tr_b16 v[216:217], v244 offset:0\n\t" \
    "ds_read_b64_tr_b16 v[218:219], v244 offset:1536\n\t" \
    "ds_read_b64_tr_b16 v[80:81], v244 offset:32\n\t" \
    "ds_read_b64_tr_b16 v[82:83], v244 offset:1568\n\t" \
    "ds_read_b64_tr_b16 v[88:89], v244 offset:64\n\t" \
    "ds_read_b64_tr_b16 v[90:91], v244 offset:1600\n\t" \
    "v_add_u32 v244, 0x12000, v243\n\t" \
    "s_waitcnt vmcnt(12) lgkmcnt(0)\n\t" \
    "s_barrier \n\t" \
    "s_add_u32 m0, %[wm], 0xc000\n\t" \
    "s_mov_b32 s44, 0x600000\n\t" \
    "buffer_load_dwordx4 v240, %[wr], s44 offen offset:0 nt lds\n\t" \
    "buffer_load_dwordx4 v241, %[wr], s44 offen offset:1024 nt lds\n\t" \
    "buffer_load_dwordx4 v242, %[wr], s44 offen offset:2048 nt lds\n\t" \
    "ds_read_b64_tr_b16 v[220:221], v244 offset:0\n\t" \
    "ds_read_b64_tr_b16 v[222:223], v244 offset:1536\n\t" \
    "ds_read_b64_tr_b16 v[84:85], v244 offset:32\n\t" \
    "ds_read_b64_tr_b16 v[86:87], v244 offset:1568\n\t" \
    "ds_read_b64_tr_b16 v[92:93], v244 offset:64\n\t" \
    "ds_read_b64_tr_b16 v[94:95], v244 offset:1600\n\t" \
    "v_add_u32 v244, 0x18000, v243\n\t" \
    "s_waitcnt vmcnt(12) lgkmcnt(0)\n\t" \
    "s_barrier \n\t" \
    "s_add_u32 m0, %[wm], 0x12000\n\t" \
    "s_mov_b32 s44, 0x660000\n\t" \
    "buffer_load_dwordx4 v240, %[wr], s44 offen offset:0 nt lds\n\t" \
    "buffer_load_dwordx4 v241, %[wr], s44 offen offset:1024 nt lds\n\t" \
    "buffer_load_dwordx4 v242, %[wr], s44 offen offset:2048 nt lds\n\t" \
    "ds_read_b64_tr_b16 v[16:17], v244 offset:0\n\t" \
    "ds_read_b64_tr_b16 v[18:19], v244 offset:1536\n\t" \
    "ds_read_b64_tr_b16 v[24:25], v244 offset:32\n\t" \
    "ds_read_b64_tr_b16 v[26:27], v244 offset:1568\n\t" \
    "ds_read_b64_tr_b16 v[32:33], v244 offset:64\n\t" \
    "ds_read_b64_tr_b16 v[34:35], v244 offset:1600\n\t" \
    "v_add_u32 v244, 0x1e000, v243\n\t" \
    "s_waitcnt vmcnt(12) lgkmcnt(0)\n\t" \
    "s_barrier \n\t" \
    ".Ls11_%=:\n\t" \
    "v_readfirstlane_b32 s43, v246\n\t" \
    "s_nop 0\n\t" \
    "s_cmp_lg_u32 s43, -1\n\t" \
    "s_cbranch_scc1 .Ls12_%=\n\t" \
    "s_cmp_eq_u32 s41, 0\n\t" \
    "s_cbranch_scc1 .Ls12_%=\n\t" \
    "s_sub_u32 s41, s41, 1\n\t" \
    "s_sleep 4\n\t" \
    "v_mov_b32 v246, 0\n\t" \
    "buffer_load_dword v246, v246, s[60:63], %[mo] offen sc1\n\t" \
    "s_waitcnt vmcnt(0)\n\t" \
    "s_branch .Ls11_%=\n\t" \
    ".Ls12_%=:\n\t" \
    "v_mov_b32 v246, 0x8000\n\t" \
    "s_mov_b64 exec, 1\n\t" \
    "buffer_store_dword v246, v246, s[60:63], %[mo] offen sc1\n\t" \
    "s_mov_b64 exec, -1\n\t" \
    "s_and_b32 s44, s43, 0x5000\n\t" \
    "s_xor_b32 %[mb], %[mb], s44\n\t" \
    "s_xor_b32 %[fl], %[fl], s44\n\t" \
    "s_and_b32 s43, s43, 0x2000\n\t" \
    "s_xor_b32 s43, s43, %[lx]\n\t" \
    "s_add_u32 s60, s60, s43\n\t" \
    "s_addc_u32 s61, s61, 0\n\t" \
    "s_add_u32 m0, %[wm], 0x18000\n\t" \
    "s_mov_b32 s44, 0x6c0000\n\t" \
    "buffer_load_dwordx4 v240, %[wr], s44 offen offset:0 nt lds\n\t" \
    "buffer_load_dwordx4 v241, %[wr], s44 offen offset:1024 nt lds\n\t" \
    "buffer_load_dwordx4 v242, %[wr], s44 offen offset:2048 nt lds\n\t" \
    "ds_read_b64_tr_b16 v[20:21], v244 offset:0\n\t" \
    "ds_read_b64_tr_b16 v[22:23], v244 offset:1536\n\t" \
    "ds_read_b64_tr_b16 v[28:29], v244 offset:32\n\t" \
    "ds_read_b64_tr_b16 v[30:31], v244 offset:1568\n\t" \
    "ds_read_b64_tr_b16 v[36:37], v244 offset:64\n\t" \
    "ds_read_b64_tr_b16 v[38:39], v244 offset:1600\n\t" \
    "v_add_u32 v244, 0x0, v243\n\t" \
    "s_waitcnt vmcnt(12) lgkmcnt(0)\n\t" \
    "s_barrier \n\t" \
    "s_add_u32 m0, %[wm], 0x1e000\n\t" \
    "s_mov_b32 s44, 0x720000\n\t" \
    "buffer_load_dwordx4 v240, %[wr], s44 offen offset:0 nt lds\n\t" \
    "buffer_load_dwordx4 v241, %[wr], s44 offen offset:1024 nt lds\n\t" \
    "buffer_load_dwordx4 v242, %[wr], s44 offen offset:2048 nt lds\n\t" \
    "ds_read_b64_tr_b16 v[224:225], v244 offset:0\n\t" \
    "ds_read_b64_tr_b16 v[226:227], v244 offset:1536\n\t" \
    "ds_read_b64_tr_b16 v[120:121], v244 offset:32\n\t" \
    "ds_read_b64_tr_b16 v[122:123], v244 offset:1568\n\t" \
    "ds_read_b64_tr_b16 v[128:129], v244 offset:64\n\t" \
    "ds_read_b64_tr_b16 v[130:131], v244 offset:1600\n\t" \
    "v_add_u32 v244, 0x6000, v243\n\t" \
    "s_waitcnt vmcnt(12) lgkmcnt(0)\n\t" \
    "s_barrier \n\t" \
    "ds_read_b64_tr_b16 v[228:229], v244 offset:0\n\t" \
    "ds_read_b64_tr_b16 v[230:231], v244 offset:1536\n\t" \
    "ds_read_b64_tr_b16 v[124:125], v244 offset:32\n\t" \
    "ds_read_b64_tr_b16 v[126:127], v244 offset:1568\n\t" \
    "ds_read_b64_tr_b16 v[132:133], v244 offset:64\n\t" \
    "ds_read_b64_tr_b16 v[134:135], v244 offset:1600\n\t" \
    "v_add_u32 v244, 0xc000, v243\n\t" \
    "s_waitcnt vmcnt(9) lgkmcnt(0)\n\t" \
    "s_barrier \n\t" \
    "ds_read_b64_tr_b16 v[232:233], v244 offset:0\n\t" \
    "ds_read_b64_tr_b16 v[234:235], v244 offset:1536\n\t" \
    "ds_read_b64_tr_b16 v[160:161], v244 offset:32\n\t" \
    "ds_read_b64_tr_b16 v[162:163], v244 offset:1568\n\t" \
    "ds_read_b64_tr_b16 v[168:169], v244 offset:64\n\t" \
    "ds_read_b64_tr_b16 v[170:171], v244 offset:1600\n\t" \
    "v_add_u32 v244, 0x12000, v243\n\t" \
    "s_waitcnt vmcnt(6) lgkmcnt(0)\n\t" \
    "s_barrier \n\t" \
    "ds_read_b64_tr_b16 v[236:237], v244 offset:0\n\t" \
    "ds_read_b64_tr_b16 v[238:239], v244 offset:1536\n\t" \
    "ds_read_b64_tr_b16 v[164:165], v244 offset:32\n\t" \
    "ds_read_b64_tr_b16 v[166:167], v244 offset:1568\n\t" \
    "ds_read_b64_tr_b16 v[172:173], v244 offset:64\n\t" \
    "ds_read_b64_tr_b16 v[174:175], v244 offset:1600\n\t" \
    "v_add_u32 v244, 0x18000, v243\n\t" \
    "s_waitcnt vmcnt(3) lgkmcnt(0)\n\t" \
    "s_barrier \n\t" \
    "ds_read_b64_tr_b16 v[176:177], v244 offset:0\n\t" \
    "ds_read_b64_tr_b16 v[178:179], v244 offset:1536\n\t" \
    "ds_read_b64_tr_b16 v[184:185], v244 offset:32\n\t" \
    "ds_read_b64_tr_b16 v[186:187], v244 offset:1568\n\t" \
    "ds_read_b64_tr_b16 v[192:193], v244 offset:64\n\t" \
    "ds_read_b64_tr_b16 v[194:195], v244 offset:1600\n\t" \
    "v_add_u32 v244, 0x1e000, v243\n\t" \
    "s_waitcnt vmcnt(0) lgkmcnt(0)\n\t" \
    "s_barrier \n\t" \
    "ds_read_b64_tr_b16 v[180:181], v244 offset:0\n\t" \
    "ds_read_b64_tr_b16 v[182:183], v244 offset:1536\n\t" \
    "ds_read_b64_tr_b16 v[188:189], v244 offset:32\n\t" \
    "ds_read_b64_tr_b16 v[190:191], v244 offset:1568\n\t" \
    "ds_read_b64_tr_b16 v[196:197], v244 offset:64\n\t" \
    "ds_read_b64_tr_b16 v[198:199], v244 offset:1600\n\t" \
    "s_waitcnt lgkmcnt(0)\n\t" \
    "s_barrier \n\t" \
    LST0 \
    LJIT \
    "ds_write_b128 v254, v[200:203] offset:40960\n\t" \
    "ds_write_b128 v254, v[204:207] offset:41984\n\t" \
    "ds_write_b128 v254, v[208:211] offset:43008\n\t" \
    "ds_write_b128 v254, v[212:215] offset:44032\n\t" \
    "ds_write_b128 v254, v[216:219] offset:45056\n\t" \
    "ds_write_b128 v254, v[220:223] offset:46080\n\t" \
    "ds_write_b128 v254, v[224:227] offset:47104\n\t" \
    "ds_write_b128 v254, v[228:231] offset:48128\n\t" \
    "ds_write_b128 v254, v[232:235] offset:49152\n\t" \
    "ds_write_b128 v254, v[236:239] offset:50176\n\t" \
    "s_waitcnt vmcnt(0) lgkmcnt(0)\n\t" \
    "s_barrier \n\t" \
    "ds_read_b128 v[224:227], v253 offset:0\n\t" \
    "ds_read_b128 v[244:247], v254 offset:40960\n\t" \
    "ds_read_b128 v[228:231], v253 offset:20480\n\t" \
    "ds_read_b128 v[232:235], v253 offset:1024\n\t" \
    "ds_read_b128 v[236:239], v253 offset:21504\n\t" \
    "ds_read_b128 v[248:251], v254 offset:41984\n\t" \
    ".p2align 8\n\t" \
    ".Ls13_%=:\n\t" \
    "s_waitcnt lgkmcnt(5)\n\t" \
    "v_mfma_f32_16x16x32_bf16 v[204:207], v[64:67], v[224:227], 0\n\t" \
    "ds_read_b128 v[240:243], v253 offset:2048\n\t" \
    "v_mfma_f32_16x16x32_bf16 v[208:211], v[72:75], v[224:227], 0\n\t" \
    "v_mfma_f32_16x16x32_bf16 v[200:203], v[56:59], v[224:227], 0\n\t" \
    "s_waitcnt lgkmcnt(4)\n\t" \
    "v_mfma_f32_16x16x32_bf16 v[216:219], v[64:67], v[228:231], 0\n\t" \
    "ds_read_b128 v[224:227], v253 offset:22528\n\t" \
    "v_mfma_f32_16x16x32_bf16 v[220:223], v[72:75], v[228:231], 0\n\t" \
    "v_mfma_f32_16x16x32_bf16 v[212:215], v[56:59], v[228:231], 0\n\t" \
    "s_waitcnt lgkmcnt(4)\n\t" \
    "v_mfma_f32_16x16x32_bf16 v[204:207], v[68:71], v[232:235], v[204:207]\n\t" \
    "ds_read_b128 v[228:231], v253 offset:3072\n\t" \
    "v_mfma_f32_16x16x32_bf16 v[208:211], v[76:79], v[232:235], v[208:211]\n\t" \
    "v_mfma_f32_16x16x32_bf16 v[200:203], v[60:63], v[232:235], v[200:203]\n\t" \
    "s_waitcnt lgkmcnt(4)\n\t" \
    "v_mfma_f32_16x16x32_bf16 v[216:219], v[68:71], v[236:239], v[216:219]\n\t" \
    "ds_read_b128 v[232:235], v253 offset:23552\n\t" \
    "v_mfma_f32_16x16x32_bf16 v[220:223], v[76:79], v[236:239], v[220:223]\n\t" \
    "v_mfma_f32_16x16x32_bf16 v[212:215], v[60:63], v[236:239], v[212:215]\n\t" \
    "s_waitcnt lgkmcnt(3)\n\t" \
    "v_mfma_f32_16x16x32_bf16 v[204:207], v[104:107], v[240:243], v[204:207]\n\t" \
    "ds_read_b128 v[236:239], v253 offset:4096\n\t" \
    "v_mfma_f32_16x16x32_bf16 v[208:211], v[112:115], v[240:243], v[208:211]\n\t" \
    "v_mfma_f32_16x16x32_bf16 v[200:203], v[96:99], v[240:243], v[200:203]\n\t" \
    "s_waitcnt lgkmcnt(3)\n\t" \
    "v_mfma_f32_16x16x32_bf16 v[216:219], v[104:107], v[224:227], v[216:219]\n\t" \
    "ds_read_b128 v[240:243], v253 offset:24576\n\t" \
    "v_mfma_f32_16x16x32_bf16 v[220:223], v[112:115], v[224:227], v[220:223]\n\t" \
    "v_mfma_f32_16x16x32_bf16 v[212:215], v[96:99], v[224:227], v[212:215]\n\t" \
    "s_waitcnt lgkmcnt(3)\n\t" \
    "v_mfma_f32_16x16x32_bf16 v[204:207], v[108:111], v[228:231], v[204:207]\n\t" \
    "ds_read_b128 v[224:227], v253 offset:5120\n\t" \
    "v_mfma_f32_16x16x32_bf16 v[208:211], v[116:119], v[228:231], v[208:211]\n\t" \
    "v_mfma_f32_16x16x32_bf16 v[200:203], v[100:103], v[228:231], v[200:203]\n\t" \
    "s_waitcnt lgkmcnt(3)\n\t" \
    "v_mfma_f32_16x16x32_bf16 v[216:219], v[108:111], v[232:235], v[216:219]\n\t" \
    "ds_read_b128 v[228:231], v253 offset:25600\n\t" \
    "v_mfma_f32_16x16x32_bf16 v[220:223], v[116:119], v[232:235], v[220:223]\n\t" \
    "v_mfma_f32_16x16x32_bf16 v[212:215], v[100:103], v[232:235], v[212:215]\n\t" \
    "s_waitcnt lgkmcnt(3)\n\t" \
    "v_mfma_f32_16x16x32_bf16 v[204:207], v[0:3], v[236:239], v[204:207]\n\t" \
    "ds_read_b128 v[232:235], v253 offset:6144\n\t" \
    "v_mfma_f32_16x16x32_bf16 v[208:211], v[8:11], v[236:239], v[208:211]\n\t" \
    "v_mfma_f32_16x16x32_bf16 v[200:203], v[244:247], v[236:239], v[200:203]\n\t" \
    "s_waitcnt lgkmcnt(3)\n\t" \
    "v_mfma_f32_16x16x32_bf16 v[216:219], v[0:3], v[240:243], v[216:219]\n\t" \
    "ds_read_b128 v[236:239], v253 offset:26624\n\t" \
    "v_mfma_f32_16x16x32_bf16 v[220:223], v[8:11], v[240:243], v[220:223]\n\t" \
    "v_mfma_f32_16x16x32_bf16 v[212:215], v[244:247], v[240:243], v[212:215]\n\t" \
    "ds_read_b128 v[244:247], v254 offset:43008\n\t" \
    "s_waitcnt lgkmcnt(4)\n\t" \
    "v_mfma_f32_16x16x32_bf16 v[204:207], v[4:7], v[224:227], v[204:207]\n\t" \
    "ds_read_b128 v[240:243], v253 offset:7168\n\t" \
    "v_mfma_f32_16x16x32_bf16 v[208:211], v[12:15], v[224:227], v[208:211]\n\t" \
    "v_mfma_f32_16x16x32_bf16 v[200:203], v[248:251], v[224:227], v[200:203]\n\t" \
    "s_waitcnt lgkmcnt(4)\n\t" \
    "v_mfma_f32_16x16x32_bf16 v[216:219], v[4:7], v[228:231], v[216:219]\n\t" \
    "ds_read_b128 v[224:227], v253 offset:27648\n\t" \
    "v_mfma_f32_16x16x32_bf16 v[220:223], v[12:15], v[228:231], v[220:223]\n\t" \
    "v_mfma_f32_16x16x32_bf16 v[212:215], v[248:251], v[228:231], v[212:215]\n\t" \
    "ds_read_b128 v[248:251], v254 offset:44032\n\t" \
    "s_waitcnt lgkmcnt(5)\n\t" \
    "v_mfma_f32_16x16x32_bf16 v[204:207], v[40:43], v[232:235], v[204:207]\n\t" \
    "ds_read_b128 v[228:231], v253 offset:8192\n\t" \
    "v_mfma_f32_16x16x32_bf16 v[208:211], v[48:51], v[232:235], v[208:211]\n\t" \
    "s_waitcnt lgkmcnt(4)\n\t" \
    "v_mfma_f32_16x16x32_bf16 v[200:203], v[244:247], v[232:235], v[200:203]\n\t" \
    "v_mfma_f32_16x16x32_bf16 v[216:219], v[40:43], v[236:239], v[216:219]\n\t" \
    "ds_read_b128 v[232:235], v253 offset:28672\n\t" \
    "v_mfma_f32_16x16x32_bf16 v[220:223], v[48:51], v[236:239], v[220:223]\n\t" \
    "v_mfma_f32_16x16x32_bf16 v[212:215], v[244:247], v[236:239], v[212:215]\n\t" \
    "ds_read_b128 v[244:247], v254 offset:45056\n\t" \
    "s_waitcnt lgkmcnt(5)\n\t" \
    "v_mfma_f32_16x16x32_bf16 v[204:207], v[44:47], v[240:243], v[204:207]\n\t" \
    "ds_read_b128 v[236:239], v253 offset:9216\n\t" \
    "v_mfma_f32_16x16x32_bf16 v[208:211], v[52:55], v[240:243], v[208:211]\n\t" \
    "s_waitcnt lgkmcnt(4)\n\t" \
    "v_mfma_f32_16x16x32_bf16 v[200:203], v[248:251], v[240:243], v[200:203]\n\t" \
    "v_mfma_f32_16x16x32_bf16 v[216:219], v[44:47], v[224:227], v[216:219]\n\t" \
    "ds_read_b128 v[240:243], v253 offset:29696\n\t" \
    "v_mfma_f32_16x16x32_bf16 v[220:223], v[52:55], v[224:227], v[220:223]\n\t" \
    "v_mfma_f32_16x16x32_bf16 v[212:215], v[248:251], v[224:227], v[212:215]\n\t" \
    "ds_read_b128 v[248:251], v254 offset:46080\n\t" \
    "s_waitcnt lgkmcnt(5)\n\t" \
    "v_mfma_f32_16x16x32_bf16 v[204:207], v[144:147], v[228:231], v[204:207]\n\t" \
    "ds_read_b128 v[224:227], v253 offset:10240\n\t" \
    "v_mfma_f32_16x16x32_bf16 v[208:211], v[152:155], v[228:231], v[208:211]\n\t" \
    "v_mfma_f32_16x16x32_bf16 v[200:203], v[136:139], v[228:231], v[200:203]\n\t" \
    "s_waitcnt lgkmcnt(5)\n\t" \
    "v_mfma_f32_16x16x32_bf16 v[216:219], v[144:147], v[232:235], v[216:219]\n\t" \
    "ds_read_b128 v[228:231], v253 offset:30720\n\t" \
    "v_mfma_f32_16x16x32_bf16 v[220:223], v[152:155], v[232:235], v[220:223]\n\t" \
    "v_mfma_f32_16x16x32_bf16 v[212:215], v[136:139], v[232:235], v[212:215]\n\t" \
    "s_waitcnt lgkmcnt(4)\n\t" \
    "v_mfma_f32_16x16x32_bf16 v[204:207], v[148:151], v[236:239], v[204:207]\n\t" \
    "ds_read_b128 v[232:235], v253 offset:11264\n\t" \
    "v_mfma_f32_16x16x32_bf16 v[208:211], v[156:159], v[236:239], v[208:211]\n\t" \
    "v_mfma_f32_16x16x32_bf16 v[200:203], v[140:143], v[236:239], v[200:203]\n\t" \
    "s_waitcnt lgkmcnt(4)\n\t" \
    "v_mfma_f32_16x16x32_bf16 v[216:219], v[148:151], v[240:243], v[216:219]\n\t" \
    "ds_read_b128 v[236:239], v253 offset:31744\n\t" \
    "v_mfma_f32_16x16x32_bf16 v[220:223], v[156:159], v[240:243], v[220:223]\n\t" \
    "v_mfma_f32_16x16x32_bf16 v[212:215], v[140:143], v[240:243], v[212:215]\n\t" \
    "s_setprio 2\n\t" \
    "s_waitcnt lgkmcnt(0)\n\t" \
    "s_barrier \n\t" \
    LST7 \
    LJIT \
    "v_mfma_f32_16x16x32_bf16 v[204:207], v[80:83], v[224:227], v[204:207]\n\t" \
    "ds_read_b128 v[240:243], v253 offset:14336\n\t" \
    "v_mfma_f32_16x16x32_bf16 v[208:211], v[88:91], v[224:227], v[208:211]\n\t" \
    "v_mfma_f32_16x16x32_bf16 v[200:203], v[244:247], v[224:227], v[200:203]\n\t" \
    "v_mfma_f32_16x16x32_bf16 v[216:219], v[80:83], v[228:231], v[216:219]\n\t" \
    "ds_read_b128 v[224:227], v253 offset:34816\n\t" \
    "s_and_b32 s44, s40, 3\n\t" \
    "v_mfma_f32_16x16x32_bf16 v[220:223], v[88:91], v[228:231], v[220:223]\n\t" \
    "s_lshl_b32 s44, s44, 22\n\t" \
    "v_mfma_f32_16x16x32_bf16 v[212:215], v[244:247], v[228:231], v[212:215]\n\t" \
    "ds_read_b128 v[244:247], v254 offset:47104\n\t" \
    "s_add_u32 s44, s44, %[mb]\n\t" \
    "v_mfma_f32_16x16x32_bf16 v[204:207], v[84:87], v[232:235], v[204:207]\n\t" \
    "ds_read_b128 v[228:231], v253 offset:15360\n\t" \
    "s_add_u32 s46, s44, 0x800\n\t" \
    "v_mfma_f32_16x16x32_bf16 v[208:211], v[92:95], v[232:235], v[208:211]\n\t" \
    "s_add_u32 s47, s44, 0x8800\n\t" \
    "v_mfma_f32_16x16x32_bf16 v[200:203], v[248:251], v[232:235], v[200:203]\n\t" \
    "s_add_u32 s48, s44, 0xc00\n\t" \
    "v_mfma_f32_16x16x32_bf16 v[216:219], v[84:87], v[236:239], v[216:219]\n\t" \
    "ds_read_b128 v[232:235], v253 offset:35840\n\t" \
    "v_mfma_f32_16x16x32_bf16 v[220:223], v[92:95], v[236:239], v[220:223]\n\t" \
    "v_mfma_f32_16x16x32_bf16 v[212:215], v[248:251], v[236:239], v[212:215]\n\t" \
    "ds_read_b128 v[248:251], v254 offset:48128\n\t" \
    "s_waitcnt lgkmcnt(5)\n\t" \
    "v_mfma_f32_16x16x32_bf16 v[204:207], v[120:123], v[240:243], v[204:207]\n\t" \
    "ds_read_b128 v[236:239], v253 offset:16384\n\t" \
    "v_mfma_f32_16x16x32_bf16 v[208:211], v[128:131], v[240:243], v[208:211]\n\t" \
    "s_waitcnt lgkmcnt(4)\n\t" \
    "v_mfma_f32_16x16x32_bf16 v[200:203], v[244:247], v[240:243], v[200:203]\n\t" \
    "v_mfma_f32_16x16x32_bf16 v[216:219], v[120:123], v[224:227], v[216:219]\n\t" \
    "ds_read_b128 v[240:243], v253 offset:36864\n\t" \
    "v_mfma_f32_16x16x32_bf16 v[220:223], v[128:131], v[224:227], v[220:223]\n\t" \
    "v_mfma_f32_16x16x32_bf16 v[212:215], v[244:247], v[224:227], v[212:215]\n\t" \
    "ds_read_b128 v[244:247], v254 offset:49152\n\t" \
    "s_waitcnt lgkmcnt(5)\n\t" \
    "v_mfma_f32_16x16x32_bf16 v[204:207], v[124:127], v[228:231], v[204:207]\n\t" \
    "ds_read_b128 v[224:227], v253 offset:17408\n\t" \
    "v_mfma_f32_16x16x32_bf16 v[208:211], v[132:135], v[228:231], v[208:211]\n\t" \
    "s_waitcnt lgkmcnt(4)\n\t" \
    "v_mfma_f32_16x16x32_bf16 v[200:203], v[248:251], v[228:231], v[200:203]\n\t" \
    "v_mfma_f32_16x16x32_bf16 v[216:219], v[124:127], v[232:235], v[216:219]\n\t" \
    "ds_read_b128 v[228:231], v253 offset:37888\n\t" \
    "v_mfma_f32_16x16x32_bf16 v[220:223], v[132:135], v[232:235], v[220:223]\n\t" \
    "v_mfma_f32_16x16x32_bf16 v[212:215], v[248:251], v[232:235], v[212:215]\n\t" \
    "ds_read_b128 v[248:251], v254 offset:50176\n\t" \
    "s_waitcnt lgkmcnt(5)\n\t" \
    "v_mfma_f32_16x16x32_bf16 v[204:207], v[160:163], v[236:239], v[204:207]\n\t" \
    "ds_read_b128 v[232:235], v253 offset:12288\n\t" \
    "v_mfma_f32_16x16x32_bf16 v[208:211], v[168:171], v[236:239], v[208:211]\n\t" \
    "s_waitcnt lgkmcnt(4)\n\t" \
    "v_mfma_f32_16x16x32_bf16 v[200:203], v[244:247], v[236:239], v[200:203]\n\t" \
    "v_mfma_f32_16x16x32_bf16 v[216:219], v[160:163], v[240:243], v[216:219]\n\t" \
    "ds_read_b128 v[236:239], v253 offset:13312\n\t" \
    "v_mfma_f32_16x16x32_bf16 v[220:223], v[168:171], v[240:243], v[220:223]\n\t" \
    "v_mfma_f32_16x16x32_bf16 v[212:215], v[244:247], v[240:243], v[212:215]\n\t" \
    "s_waitcnt lgkmcnt(4)\n\t" \
    "v_mfma_f32_16x16x32_bf16 v[204:207], v[164:167], v[224:227], v[204:207]\n\t" \
    "ds_read_b128 v[240:243], v253 offset:18432\n\t" \
    "v_mfma_f32_16x16x32_bf16 v[208:211], v[172:175], v[224:227], v[208:211]\n\t" \
    "s_waitcnt lgkmcnt(3)\n\t" \
    "v_mfma_f32_16x16x32_bf16 v[200:203], v[248:251], v[224:227], v[200:203]\n\t" \
    "v_mfma_f32_16x16x32_bf16 v[216:219], v[164:167], v[228:231], v[216:219]\n\t" \
    "ds_read_b128 v[224:227], v253 offset:19456\n\t" \
    "v_mfma_f32_16x16x32_bf16 v[220:223], v[172:175], v[228:231], v[220:223]\n\t" \
    "v_mfma_f32_16x16x32_bf16 v[212:215], v[248:251], v[228:231], v[212:215]\n\t" \
    "s_waitcnt lgkmcnt(3)\n\t" \
    "v_mfma_f32_16x16x32_bf16 v[204:207], v[24:27], v[232:235], v[204:207]\n\t" \
    "ds_read_b128 v[228:231], v253 offset:32768\n\t" \
    "v_mov_b64 v[244:245], 0\n\t" \
    "v_mfma_f32_16x16x32_bf16 v[208:211], v[32:35], v[232:235], v[208:211]\n\t" \
    "v_mov_b64 v[246:247], 0\n\t" \
    "v_mfma_f32_16x16x32_bf16 v[200:203], v[16:19], v[232:235], v[200:203]\n\t" \
    "v_mov_b64 v[248:249], 0\n\t" \
    "s_waitcnt lgkmcnt(3)\n\t" \
    "v_mfma_f32_16x16x32_bf16 v[204:207], v[28:31], v[236:239], v[204:207]\n\t" \
    "ds_read_b128 v[232:235], v253 offset:33792\n\t" \
    "v_mov_b64 v[250:251], 0\n\t" \
    "v_mfma_f32_16x16x32_bf16 v[208:211], v[36:39], v[236:239], v[208:211]\n\t" \
    "v_mfma_f32_16x16x32_bf16 v[200:203], v[20:23], v[236:239], v[200:203]\n\t" \
    "s_waitcnt lgkmcnt(3)\n\t" \
    "v_mfma_f32_16x16x32_bf16 v[204:207], v[184:187], v[240:243], v[204:207]\n\t" \
    "ds_read_b128 v[236:239], v253 offset:38912\n\t" \
    "v_mfma_f32_16x16x32_bf16 v[208:211], v[192:195], v[240:243], v[208:211]\n\t" \
    "v_mfma_f32_16x16x32_bf16 v[200:203], v[176:179], v[240:243], v[200:203]\n\t" \
    "s_waitcnt lgkmcnt(3)\n\t" \
    "v_mfma_f32_16x16x32_bf16 v[204:207], v[188:191], v[224:227], v[204:207]\n\t" \
    "ds_read_b128 v[240:243], v253 offset:39936\n\t" \
    "v_mfma_f32_16x16x32_bf16 v[208:211], v[196:199], v[224:227], v[208:211]\n\t" \
    "v_mfma_f32_16x16x32_bf16 v[200:203], v[180:183], v[224:227], v[200:203]\n\t" \
    "s_waitcnt lgkmcnt(3)\n\t" \
    "v_mfma_f32_16x16x32_bf16 v[216:219], v[24:27], v[228:231], v[216:219]\n\t" \
    "s_waitcnt lgkmcnt(2)\n\t" \
    "v_mfma_f32_16x16x32_bf16 v[216:219], v[28:31], v[232:235], v[216:219]\n\t" \
    "v_cvt_pk_bf16_f32 v204, v204, v208\n\t" \
    "v_cvt_pk_bf16_f32 v205, v205, v209\n\t" \
    "s_waitcnt lgkmcnt(1)\n\t" \
    "v_mfma_f32_16x16x32_bf16 v[216:219], v[184:187], v[236:239], v[216:219]\n\t" \
    "v_cvt_pk_bf16_f32 v206, v206, v210\n\t" \
    "v_cvt_pk_bf16_f32 v207, v207, v211\n\t" \
    "s_waitcnt lgkmcnt(0)\n\t" \
    "v_mfma_f32_16x16x32_bf16 v[216:219], v[188:191], v[240:243], v[216:219]\n\t" \
    "ds_write_b128 v254, v[204:207] offset:9216\n\t" \
    "v_mfma_f32_16x16x32_bf16 v[220:223], v[32:35], v[228:231], v[220:223]\n\t" \
    "v_mfma_f32_16x16x32_bf16 v[220:223], v[36:39], v[232:235], v[220:223]\n\t" \
    "v_cvt_pk_bf16_f32 v200, v200, v201\n\t" \
    "v_mfma_f32_16x16x32_bf16 v[220:223], v[192:195], v[236:239], v[220:223]\n\t" \
    "v_cvt_pk_bf16_f32 v201, v202, v203\n\t" \
    "ds_write_b64 v254, v[200:201] offset:7168\n\t" \
    "v_mfma_f32_16x16x32_bf16 v[220:223], v[196:199], v[240:243], v[220:223]\n\t" \
    "v_mfma_f32_16x16x32_bf16 v[212:215], v[16:19], v[228:231], v[212:215]\n\t" \
    "v_mfma_f32_16x16x32_bf16 v[212:215], v[20:23], v[232:235], v[212:215]\n\t" \
    "ds_read_b128 v[200:203], v254 offset:40960\n\t" \
    "ds_read_b128 v[204:207], v254 offset:41984\n\t" \
    "v_mfma_f32_16x16x32_bf16 v[212:215], v[176:179], v[236:239], v[212:215]\n\t" \
    "v_mov_b64 v[208:209], 0\n\t" \
    "v_mov_b64 v[210:211], 0\n\t" \
    "v_mfma_f32_16x16x32_bf16 v[212:215], v[180:183], v[240:243], v[212:215]\n\t" \
    "v_cvt_pk_bf16_f32 v216, v216, v220\n\t" \
    "v_cvt_pk_bf16_f32 v217, v217, v221\n\t" \
    "s_setprio 0\n\t" \
    "v_cvt_pk_bf16_f32 v218, v218, v222\n\t" \
    "v_cvt_pk_bf16_f32 v219, v219, v223\n\t" \
    "ds_write_b128 v254, v[216:219] offset:15360\n\t" \
    "ds_read_b128 v[216:219], v254 offset:43008\n\t" \
    "ds_read_b128 v[220:223], v254 offset:44032\n\t" \
    "s_nop 0\n\t" \
    "v_cvt_pk_bf16_f32 v212, v212, v213\n\t" \
    "v_cvt_pk_bf16_f32 v213, v214, v215\n\t" \
    "ds_write_b64 v254, v[212:213] offset:13312\n\t" \
    LST6 \
    LJIT \
    "s_sleep 3\n\t" \
    "s_mov_b32 m0, 0xa800\n\t" \
    "s_nop 0\n\t" \
    "buffer_load_dwordx4 v253, s[60:63], s46 offen sc1 lds\n\t" \
    "s_mov_b32 m0, 0xb800\n\t" \
    "s_nop 0\n\t" \
    "buffer_load_dwordx4 v253, s[60:63], s47 offen sc1 lds\n\t" \
    "s_mov_b32 m0, 0xac00\n\t" \
    "s_nop 0\n\t" \
    "buffer_load_dwordx4 v253, s[60:63], s48 offen sc1 lds\n\t" \
    "s_waitcnt lgkmcnt(0)\n\t" \
    LFLUSH212 \
    "v_bfe_i32 v212, v253, 5, 1\n\t" \
    "v_and_b32 v212, 0xaaaa, v212\n\t" \
    "v_or_b32 v212, 0x4444, v212\n\t" \
    "v_and_b32 v213, 0xffff, v255\n\t" \
    "v_lshrrev_b32 v214, 16, v255\n\t" \
    ".Ls14_%=:\n\t" \
    "s_waitcnt vmcnt(0)\n\t" \
    "ds_read_b128 v[224:227], v253 offset:43008\n\t" \
    "ds_read_b128 v[228:231], v253 offset:47104\n\t" \
    "ds_read_b128 v[232:235], v253 offset:44032\n\t" \
    "s_waitcnt lgkmcnt(2)\n\t" \
    "v_max_u32 v215, v224, v225\n\t" \
    "v_max3_u32 v215, v226, v227, v215\n\t" \
    "s_waitcnt lgkmcnt(1)\n\t" \
    "v_max3_u32 v215, v228, v229, v215\n\t" \
    "v_max3_u32 v215, v230, v231, v215\n\t" \
    "s_waitcnt lgkmcnt(0)\n\t" \
    "v_max3_u32 v215, v232, v233, v215\n\t" \
    "v_max3_u32 v215, v234, v235, v215\n\t" \
    "v_cmp_eq_u32 vcc, -1, v215\n\t" \
    "s_cbranch_vccz .Ls15_%=\n\t" \
    "v_max_u32 v215, v224, v225\n\t" \
    "v_max3_u32 v215, v226, v227, v215\n\t" \
    "v_cmp_eq_u32 vcc, -1, v215\n\t" \
    "s_cbranch_vccz .Ls16_%=\n\t" \
    "s_mov_b32 m0, 0xa800\n\t" \
    "s_nop 0\n\t" \
    "buffer_load_dwordx4 v253, s[60:63], s46 offen sc1 lds\n\t" \
    ".Ls16_%=:\n\t" \
    "v_max_u32 v215, v228, v229\n\t" \
    "v_max3_u32 v215, v230, v231, v215\n\t" \
    "v_cmp_eq_u32 vcc, -1, v215\n\t" \
    "s_cbranch_vccz .Ls17_%=\n\t" \
    "s_mov_b32 m0, 0xb800\n\t" \
    "s_nop 0\n\t" \
    "buffer_load_dwordx4 v253, s[60:63], s47 offen sc1 lds\n\t" \
    ".Ls17_%=:\n\t" \
    "v_max_u32 v215, v232, v233\n\t" \
    "v_max3_u32 v215, v234, v235, v215\n\t" \
    "v_cmp_eq_u32 vcc, -1, v215\n\t" \
    "s_cbranch_vccz .Ls18_%=\n\t" \
    "s_mov_b32 m0, 0xac00\n\t" \
    "s_nop 0\n\t" \
    "buffer_load_dwordx4 v253, s[60:63], s48 offen sc1 lds\n\t" \
    ".Ls18_%=:\n\t" \
    "s_cmp_eq_u32 s41, 0\n\t" \
    "s_cbranch_scc1 .Ls15_%=\n\t" \
    "s_sub_u32 s41, s41, 1\n\t" \
    "s_branch .Ls14_%=\n\t" \
    ".Ls15_%=:\n\t" \
    LST2 \
    LJIT \
    "s_setprio 3\n\t" \
    "s_waitcnt lgkmcnt(0)\n\t" \
    "s_barrier \n\t" \
    LST3 \
    LJIT \
    "ds_read_b128 v[236:239], v253 offset:40960\n\t" \
    "ds_read_b128 v[240:243], v253 offset:46080\n\t" \
    "v_smfmac_f32_16x16x64_bf16 v[244:247], v[224:227], v[0:7], v212\n\t" \
    "v_smfmac_f32_16x16x64_bf16 v[248:251], v[224:227], v[8:15], v212\n\t" \
    "v_smfmac_f32_16x16x64_bf16 v[208:211], v[224:227], v[200:207], v212\n\t" \
    "ds_read_b128 v[200:203], v254 offset:45056\n\t" \
    "ds_read_b128 v[204:207], v254 offset:46080\n\t" \
    "ds_read_b128 v[224:227], v253 offset:41984\n\t" \
    "v_smfmac_f32_16x16x64_bf16 v[208:211], v[228:231], v[16:23], v212\n\t" \
    "v_smfmac_f32_16x16x64_bf16 v[244:247], v[228:231], v[24:31], v212\n\t" \
    "v_smfmac_f32_16x16x64_bf16 v[248:251], v[228:231], v[32:39], v212\n\t" \
    "ds_read_b128 v[228:231], v253 offset:48128\n\t" \
    "v_smfmac_f32_16x16x64_bf16 v[208:211], v[232:235], v[216:223], v212\n\t" \
    "ds_read_b128 v[216:219], v254 offset:47104\n\t" \
    "ds_read_b128 v[220:223], v254 offset:48128\n\t" \
    "v_smfmac_f32_16x16x64_bf16 v[244:247], v[232:235], v[40:47], v212\n\t" \
    "v_smfmac_f32_16x16x64_bf16 v[248:251], v[232:235], v[48:55], v212\n\t" \
    "ds_read_b128 v[232:235], v253 offset:45056\n\t" \
    "s_waitcnt lgkmcnt(8)\n\t" \
    "v_smfmac_f32_16x16x64_bf16 v[208:211], v[236:239], v[56:63], v212\n\t" \
    "v_smfmac_f32_16x16x64_bf16 v[244:247], v[236:239], v[64:71], v212\n\t" \
    "v_smfmac_f32_16x16x64_bf16 v[248:251], v[236:239], v[72:79], v212\n\t" \
    "ds_read_b128 v[236:239], v253 offset:49152\n\t" \
    "s_waitcnt lgkmcnt(6)\n\t" \
    "v_smfmac_f32_16x16x64_bf16 v[208:211], v[240:243], v[200:207], v212\n\t" \
    "ds_read_b128 v[200:203], v254 offset:49152\n\t" \
    "ds_read_b128 v[204:207], v254 offset:50176\n\t" \
    "v_smfmac_f32_16x16x64_bf16 v[244:247], v[240:243], v[80:87], v212\n\t" \
    "v_smfmac_f32_16x16x64_bf16 v[248:251], v[240:243], v[88:95], v212\n\t" \
    "ds_read_b128 v[240:243], v253 offset:50176\n\t" \
    "s_waitcnt lgkmcnt(8)\n\t" \
    "v_smfmac_f32_16x16x64_bf16 v[208:211], v[224:227], v[96:103], v212\n\t" \
    "v_smfmac_f32_16x16x64_bf16 v[244:247], v[224:227], v[104:111], v212\n\t" \
    "v_smfmac_f32_16x16x64_bf16 v[248:251], v[224:227], v[112:119], v212\n\t" \
    "s_waitcnt lgkmcnt(5)\n\t" \
    "v_smfmac_f32_16x16x64_bf16 v[208:211], v[228:231], v[216:223], v212\n\t" \
    "s_waitcnt lgkmcnt(4)\n\t" \
    "v_smfmac_f32_16x16x64_bf16 v[208:211], v[232:235], v[136:143], v212\n\t" \
    "s_waitcnt lgkmcnt(1)\n\t" \
    "v_smfmac_f32_16x16x64_bf16 v[208:211], v[236:239], v[200:207], v212\n\t" \
    "s_waitcnt lgkmcnt(0)\n\t" \
    "v_smfmac_f32_16x16x64_bf16 v[208:211], v[240:243], v[176:183], v212\n\t" \
    "v_smfmac_f32_16x16x64_bf16 v[244:247], v[228:231], v[120:127], v212\n\t" \
    "v_smfmac_f32_16x16x64_bf16 v[244:247], v[232:235], v[144:151], v212\n\t" \
    "v_smfmac_f32_16x16x64_bf16 v[244:247], v[236:239], v[160:167], v212\n\t" \
    "v_add_f32 v208, v208, v210\n\t" \
    "v_add_f32 v209, v209, v211\n\t" \
    "v_cvt_pk_bf16_f32 v208, v208, v209\n\t" \
    "v_smfmac_f32_16x16x64_bf16 v[244:247], v[240:243], v[184:191], v212\n\t" \
    "ds_write_b16 v213, v208 offset:0\n\t" \
    "ds_write_b16_d16_hi v213, v208 offset:16\n\t" \
    "v_smfmac_f32_16x16x64_bf16 v[248:251], v[228:231], v[128:135], v212\n\t" \
    "v_smfmac_f32_16x16x64_bf16 v[248:251], v[232:235], v[152:159], v212\n\t" \
    "v_smfmac_f32_16x16x64_bf16 v[248:251], v[236:239], v[168:175], v212\n\t" \
    "v_add_f32 v244, v244, v246\n\t" \
    "v_add_f32 v245, v245, v247\n\t" \
    "v_smfmac_f32_16x16x64_bf16 v[248:251], v[240:243], v[192:199], v212\n\t" \
    "s_nop 7\n\t" \
    "v_add_f32 v248, v248, v250\n\t" \
    "v_add_f32 v249, v249, v251\n\t" \
    "v_cvt_pk_bf16_f32 v244, v244, v248\n\t" \
    "v_cvt_pk_bf16_f32 v245, v245, v249\n\t" \
    "ds_write2_b32 v214, v244, v245 offset1:4\n\t" \
    "s_setprio 0\n\t" \
    "s_sleep 1\n\t" \
    "ds_read_b128 v[200:203], v254 offset:6144\n\t" \
    "ds_read_b128 v[224:227], v254 offset:18432\n\t" \
    "ds_read_b128 v[204:207], v254 offset:7168\n\t" \
    "ds_read_b128 v[228:231], v254 offset:19456\n\t" \
    "ds_read_b128 v[208:211], v254 offset:8192\n\t" \
    "ds_read_b128 v[232:235], v254 offset:20480\n\t" \
    "ds_read_b128 v[212:215], v254 offset:9216\n\t" \
    "ds_read_b128 v[236:239], v254 offset:21504\n\t" \
    "ds_read_b128 v[216:219], v254 offset:10240\n\t" \
    "ds_read_b128 v[220:223], v254 offset:11264\n\t" \
    "s_and_b32 s44, s40, 3\n\t" \
    "s_lshl_b32 s44, s44, 17\n\t" \
    "s_add_u32 s50, s44, %[pb]\n\t" \
    "s_add_u32 s51, s44, %[pm]\n\t" \
    "s_mov_b32 s52, 0xf0000\n\t" \
    "s_mov_b32 s53, 0xfff00ff\n\t" \
    "s_mov_b32 s54, 0x1f0001\n\t" \
    "s_mov_b32 s55, 0x1fff01ff\n\t" \
    "s_mov_b32 s56, 0x3f0003\n\t" \
    "s_mov_b32 s57, 0x3fff03ff\n\t" \
    "s_mov_b32 s58, 0x7f0007\n\t" \
    "s_mov_b32 s59, 0x7fff07ff\n\t" \
    "s_waitcnt lgkmcnt(8)\n\t" \
    "v_mfma_f32_16x16x32_bf16 v[240:243], v[224:227], v[200:203], 0\n\t" \
    "ds_read_b128 v[224:227], v254 offset:22528\n\t" \
    "s_waitcnt lgkmcnt(7)\n\t" \
    "v_mfma_f32_16x16x32_bf16 v[240:243], v[228:231], v[204:207], v[240:243]\n\t" \
    "ds_read_b128 v[228:231], v254 offset:23552\n\t" \
    "v_and_b32 v252, 0xffffff7f, v254\n\t" \
    "s_waitcnt lgkmcnt(0)\n\t" \
    "s_barrier \n\t" \
    LST4 \
    LJIT \
    "v_mfma_f32_16x16x32_bf16 v[240:243], v[232:235], v[208:211], v[240:243]\n\t" \
    "ds_read_b128 v[232:235], v252 offset:3072\n\t" \
    "v_mfma_f32_16x16x32_bf16 v[240:243], v[236:239], v[212:215], v[240:243]\n\t" \
    "ds_read_b128 v[236:239], v252 offset:3200\n\t" \
    "v_mfma_f32_16x16x32_bf16 v[240:243], v[224:227], v[216:219], v[240:243]\n\t" \
    "ds_read_b128 v[224:227], v252 offset:4096\n\t" \
    "v_mfma_f32_16x16x32_bf16 v[240:243], v[228:231], v[220:223], v[240:243]\n\t" \
    "ds_read_b128 v[228:231], v252 offset:4224\n\t" \
    "s_waitcnt lgkmcnt(3)\n\t" \
    "v_mfma_f32_16x16x32_bf16 v[248:251], v[232:235], v[200:203], 0\n\t" \
    "ds_read_b128 v[232:235], v252 offset:5120\n\t" \
    "s_waitcnt lgkmcnt(3)\n\t" \
    "v_mfma_f32_16x16x32_bf16 v[248:251], v[236:239], v[204:207], v[248:251]\n\t" \
    "ds_read_b128 v[236:239], v252 offset:5248\n\t" \
    "s_waitcnt lgkmcnt(3)\n\t" \
    "v_mfma_f32_16x16x32_bf16 v[248:251], v[224:227], v[208:211], v[248:251]\n\t" \
    "s_waitcnt lgkmcnt(2)\n\t" \
    "v_mfma_f32_16x16x32_bf16 v[248:251], v[228:231], v[212:215], v[248:251]\n\t" \
    "s_waitcnt lgkmcnt(1)\n\t" \
    "v_mfma_f32_16x16x32_bf16 v[248:251], v[232:235], v[216:219], v[248:251]\n\t" \
    "s_waitcnt lgkmcnt(0)\n\t" \
    "v_mfma_f32_16x16x32_bf16 v[248:251], v[236:239], v[220:223], v[248:251]\n\t" \
    "v_mov_b32 v226, 0xff7fffff\n\t" \
    "v_cndmask_b32 v240, v240, v226, s[52:53]\n\t" \
    "v_cndmask_b32 v241, v241, v226, s[54:55]\n\t" \
    "v_cndmask_b32 v242, v242, v226, s[56:57]\n\t" \
    "v_cndmask_b32 v243, v243, v226, s[58:59]\n\t" \
    "v_max3_f32 v224, v240, v241, v242\n\t" \
    "v_mov_b32 v225, v243\n\t" \
    "v_lshrrev_b32 v200, 5, v253\n\t" \
    "v_and_b32 v201, 0xf0, v253\n\t" \
    "v_and_or_b32 v200, v200, 8, v201\n\t" \
    "v_max3_f32 v224, v224, v248, v249\n\t" \
    "v_max3_f32 v225, v225, v250, v251\n\t" \
    "v_max_f32 v224, v224, v225\n\t" \
    "v_mov_b32 v225, v224\n\t" \
    "s_nop 1\n\t" \
    "v_permlane16_swap_b32 v224, v225\n\t" \
    "v_max_f32 v224, v224, v225\n\t" \
    "v_mov_b32 v225, v224\n\t" \
    "s_nop 1\n\t" \
    "v_permlane32_swap_b32 v224, v225\n\t" \
    "v_max_f32 v224, v224, v225\n\t" \
    "v_mul_f32 v224, %[scl], v224\n\t" \
    "v_fma_f32 v240, v240, %[scl], -v224\n\t" \
    "v_fma_f32 v241, v241, %[scl], -v224\n\t" \
    "v_exp_f32 v240, v240\n\t" \
    "v_fma_f32 v242, v242, %[scl], -v224\n\t" \
    "v_exp_f32 v241, v241\n\t" \
    "v_fma_f32 v243, v243, %[scl], -v224\n\t" \
    "v_exp_f32 v242, v242\n\t" \
    "v_fma_f32 v248, v248, %[scl], -v224\n\t" \
    "v_exp_f32 v243, v243\n\t" \
    "v_fma_f32 v249, v249, %[scl], -v224\n\t" \
    "v_exp_f32 v248, v248\n\t" \
    "v_fma_f32 v250, v250, %[scl], -v224\n\t" \
    "v_exp_f32 v249, v249\n\t" \
    "v_fma_f32 v251, v251, %[scl], -v224\n\t" \
    "v_exp_f32 v250, v250\n\t" \
    "v_exp_f32 v251, v251\n\t" \
    "v_add_f32 v224, v240, v241\n\t" \
    "v_add_f32 v225, v242, v243\n\t" \
    "v_add_f32 v232, v248, v249\n\t" \
    "v_add_f32 v233, v250, v251\n\t" \
    "v_add_f32 v224, v224, v225\n\t" \
    "v_add_f32 v232, v232, v233\n\t" \
    "v_fma_f32 v224, v232, 0.5, v224\n\t" \
    "v_mov_b32 v225, v224\n\t" \
    "s_nop 1\n\t" \
    "v_permlane16_swap_b32 v224, v225\n\t" \
    "v_add_f32 v224, v224, v225\n\t" \
    "v_mov_b32 v225, v224\n\t" \
    "s_nop 1\n\t" \
    "v_permlane32_swap_b32 v224, v225\n\t" \
    "v_add_f32 v224, v224, v225\n\t" \
    "v_rcp_f32 v224, v224\n\t" \
    "s_nop 0\n\t" \
    "v_mul_f32 v240, v240, v224\n\t" \
    "v_mul_f32 v241, v241, v224\n\t" \
    "v_mul_f32 v242, v242, v224\n\t" \
    "v_mul_f32 v243, v243, v224\n\t" \
    "v_mul_f32 v248, v248, v224\n\t" \
    "v_mul_f32 v249, v249, v224\n\t" \
    "v_mul_f32 v250, v250, v224\n\t" \
    "v_mul_f32 v251, v251, v224\n\t" \
    "v_cvt_pk_bf16_f32 v204, |v240|, |v241|\n\t" \
    "v_cvt_pk_bf16_f32 v205, |v242|, |v243|\n\t" \
    "v_cvt_pk_bf16_f32 v208, |v248|, |v249|\n\t" \
    "v_cvt_pk_bf16_f32 v209, |v250|, |v251|\n\t" \
    "v_permlane16_swap_b32 v204, v206\n\t" \
    "v_permlane16_swap_b32 v205, v207\n\t" \
    "s_mov_b32 exec_lo, 0xffff\n\t" \
    "s_mov_b32 exec_hi, 0xffff\n\t" \
    "buffer_store_dwordx4 v[204:207], v253, s[60:63], s50 offen sc1\n\t" \
    "s_mov_b32 exec_lo, -1\n\t" \
    "s_mov_b32 exec_hi, 0\n\t" \
    "buffer_store_dwordx2 v[208:209], v200, s[60:63], s51 offen sc1\n\t" \
    "s_mov_b32 exec_hi, -1\n\t" \
    LST5 \
    LJIT \
    "ds_read_b128 v[224:227], v253 offset:0\n\t" \
    "ds_read_b128 v[244:247], v254 offset:40960\n\t" \
    "ds_read_b128 v[228:231], v253 offset:20480\n\t" \
    "ds_read_b128 v[232:235], v253 offset:1024\n\t" \
    "ds_read_b128 v[236:239], v253 offset:21504\n\t" \
    "ds_read_b128 v[248:251], v254 offset:41984\n\t" \
    "s_add_u32 s40, s40, 1\n\t" \
    "s_cmp_lg_u32 s40, 64\n\t" \
    "s_cbranch_scc1 .Ls13_%=\n\t" \
    "s_branch .Lend%=\n\t" \
    ".Ls0_%=:\n\t" \
    "v_add_u32 v244, 0x18000, v243\n\t" \
    "s_waitcnt vmcnt(12) lgkmcnt(0)\n\t" \
    "s_barrier \n\t" \
    "s_add_u32 m0, %[wm], 0x12000\n\t" \
    "s_mov_b32 s44, 0x1e0000\n\t" \
    "buffer_load_dwordx4 v240, %[wr], s44 offen offset:0 nt lds\n\t" \
    "buffer_load_dwordx4 v241, %[wr], s44 offen offset:1024 nt lds\n\t" \
    "buffer_load_dwordx4 v242, %[wr], s44 offen offset:2048 nt lds\n\t" \
    "ds_read_b64_tr_b16 v[200:201], v244 offset:0\n\t" \
    "ds_read_b64_tr_b16 v[202:203], v244 offset:1536\n\t" \
    "ds_read_b64_tr_b16 v[0:1], v244 offset:32\n\t" \
    "ds_read_b64_tr_b16 v[2:3], v244 offset:1568\n\t" \
    "ds_read_b64_tr_b16 v[8:9], v244 offset:64\n\t" \
    "ds_read_b64_tr_b16 v[10:11], v244 offset:1600\n\t" \
    "v_add_u32 v244, 0x1e000, v243\n\t" \
    "s_waitcnt vmcnt(12) lgkmcnt(0)\n\t" \
    "s_barrier \n\t" \
    "s_add_u32 m0, %[wm], 0x18000\n\t" \
    "s_mov_b32 s44, 0x240000\n\t" \
    "buffer_load_dwordx4 v240, %[wr], s44 offen offset:0 nt lds\n\t" \
    "buffer_load_dwordx4 v241, %[wr], s44 offen offset:1024 nt lds\n\t" \
    "buffer_load_dwordx4 v242, %[wr], s44 offen offset:2048 nt lds\n\t" \
    "ds_read_b64_tr_b16 v[204:205], v244 offset:0\n\t" \
    "ds_read_b64_tr_b16 v[206:207], v244 offset:1536\n\t" \
    "ds_read_b64_tr_b16 v[4:5], v244 offset:32\n\t" \
    "ds_read_b64_tr_b16 v[6:7], v244 offset:1568\n\t" \
    "ds_read_b64_tr_b16 v[12:13], v244 offset:64\n\t" \
    "ds_read_b64_tr_b16 v[14:15], v244 offset:1600\n\t" \
    "v_add_u32 v244, 0x0, v243\n\t" \
    "s_waitcnt vmcnt(12) lgkmcnt(0)\n\t" \
    "s_barrier \n\t" \
    "s_add_u32 m0, %[wm], 0x1e000\n\t" \
    "s_mov_b32 s44, 0x2a0000\n\t" \
    "buffer_load_dwordx4 v240, %[wr], s44 offen offset:0 nt lds\n\t" \
    "buffer_load_dwordx4 v241, %[wr], s44 offen offset:1024 nt lds\n\t" \
    "buffer_load_dwordx4 v242, %[wr], s44 offen offset:2048 nt lds\n\t" \
    "ds_read_b64_tr_b16 v[208:209], v244 offset:0\n\t" \
    "ds_read_b64_tr_b16 v[210:211], v244 offset:1536\n\t" \
    "ds_read_b64_tr_b16 v[40:41], v244 offset:32\n\t" \
    "ds_read_b64_tr_b16 v[42:43], v244 offset:1568\n\t" \
    "ds_read_b64_tr_b16 v[48:49], v244 offset:64\n\t" \
    "ds_read_b64_tr_b16 v[50:51], v244 offset:1600\n\t" \
    "v_add_u32 v244, 0x6000, v243\n\t" \
    "s_waitcnt vmcnt(12) lgkmcnt(0)\n\t" \
    "s_barrier \n\t" \
    "s_add_u32 m0, %[wm], 0x0\n\t" \
    "s_mov_b32 s44, 0x300000\n\t" \
    "buffer_load_dwordx4 v240, %[wr], s44 offen offset:0 nt lds\n\t" \
    "buffer_load_dwordx4 v241, %[wr], s44 offen offset:1024 nt lds\n\t" \
    "buffer_load_dwordx4 v242, %[wr], s44 offen offset:2048 nt lds\n\t" \
    "ds_read_b64_tr_b16 v[212:213], v244 offset:0\n\t" \
    "ds_read_b64_tr_b16 v[214:215], v244 offset:1536\n\t" \
    "ds_read_b64_tr_b16 v[44:45], v244 offset:32\n\t" \
    "ds_read_b64_tr_b16 v[46:47], v244 offset:1568\n\t" \
    "ds_read_b64_tr_b16 v[52:53], v244 offset:64\n\t" \
    "ds_read_b64_tr_b16 v[54:55], v244 offset:1600\n\t" \
    "v_add_u32 v244, 0xc000, v243\n\t" \
    "s_waitcnt vmcnt(12) lgkmcnt(0)\n\t" \
    "s_barrier \n\t" \
    "s_add_u32 m0, %[wm], 0x6000\n\t" \
    "s_mov_b32 s44, 0x360000\n\t" \
    "buffer_load_dwordx4 v240, %[wr], s44 offen offset:0 nt lds\n\t" \
    "buffer_load_dwordx4 v241, %[wr], s44 offen offset:1024 nt lds\n\t" \
    "buffer_load_dwordx4 v242, %[wr], s44 offen offset:2048 nt lds\n\t" \
    "ds_read_b64_tr_b16 v[216:217], v244 offset:0\n\t" \
    "ds_read_b64_tr_b16 v[218:219], v244 offset:1536\n\t" \
    "ds_read_b64_tr_b16 v[80:81], v244 offset:32\n\t" \
    "ds_read_b64_tr_b16 v[82:83], v244 offset:1568\n\t" \
    "ds_read_b64_tr_b16 v[88:89], v244 offset:64\n\t" \
    "ds_read_b64_tr_b16 v[90:91], v244 offset:1600\n\t" \
    "v_add_u32 v244, 0x12000, v243\n\t" \
    "s_waitcnt vmcnt(12) lgkmcnt(0)\n\t" \
    "s_barrier \n\t" \
    "s_add_u32 m0, %[wm], 0xc000\n\t" \
    "s_mov_b32 s44, 0x3c0000\n\t" \
    "buffer_load_dwordx4 v240, %[wr], s44 offen offset:0 nt lds\n\t" \
    "buffer_load_dwordx4 v241, %[wr], s44 offen offset:1024 nt lds\n\t" \
    "buffer_load_dwordx4 v242, %[wr], s44 offen offset:2048 nt lds\n\t" \
    "ds_read_b64_tr_b16 v[220:221], v244 offset:0\n\t" \
    "ds_read_b64_tr_b16 v[222:223], v244 offset:1536\n\t" \
    "ds_read_b64_tr_b16 v[84:85], v244 offset:32\n\t" \
    "ds_read_b64_tr_b16 v[86:87], v244 offset:1568\n\t" \
    "ds_read_b64_tr_b16 v[92:93], v244 offset:64\n\t" \
    "ds_read_b64_tr_b16 v[94:95], v244 offset:1600\n\t" \
    "v_add_u32 v244, 0x18000, v243\n\t" \
    "s_waitcnt vmcnt(12) lgkmcnt(0)\n\t" \
    "s_barrier \n\t" \
    "s_add_u32 m0, %[wm], 0x12000\n\t" \
    "s_mov_b32 s44, 0x420000\n\t" \
    "buffer_load_dwordx4 v240, %[wr], s44 offen offset:0 nt lds\n\t" \
    "buffer_load_dwordx4 v241, %[wr], s44 offen offset:1024 nt lds\n\t" \
    "buffer_load_dwordx4 v242, %[wr], s44 offen offset:2048 nt lds\n\t" \
    "ds_read_b64_tr_b16 v[224:225], v244 offset:0\n\t" \
    "ds_read_b64_tr_b16 v[226:227], v244 offset:1536\n\t" \
    "ds_read_b64_tr_b16 v[120:121], v244 offset:32\n\t" \
    "ds_read_b64_tr_b16 v[122:123], v244 offset:1568\n\t" \
    "ds_read_b64_tr_b16 v[128:129], v244 offset:64\n\t" \
    "ds_read_b64_tr_b16 v[130:131], v244 offset:1600\n\t" \
    "v_add_u32 v244, 0x1e000, v243\n\t" \
    "s_waitcnt vmcnt(12) lgkmcnt(0)\n\t" \
    "s_barrier \n\t" \
    "s_add_u32 m0, %[wm], 0x18000\n\t" \
    "s_mov_b32 s44, 0x480000\n\t" \
    "buffer_load_dwordx4 v240, %[wr], s44 offen offset:0 nt lds\n\t" \
    "buffer_load_dwordx4 v241, %[wr], s44 offen offset:1024 nt lds\n\t" \
    "buffer_load_dwordx4 v242, %[wr], s44 offen offset:2048 nt lds\n\t" \
    "ds_read_b64_tr_b16 v[228:229], v244 offset:0\n\t" \
    "ds_read_b64_tr_b16 v[230:231], v244 offset:1536\n\t" \
    "ds_read_b64_tr_b16 v[124:125], v244 offset:32\n\t" \
    "ds_read_b64_tr_b16 v[126:127], v244 offset:1568\n\t" \
    "ds_read_b64_tr_b16 v[132:133], v244 offset:64\n\t" \
    "ds_read_b64_tr_b16 v[134:135], v244 offset:1600\n\t" \
    "v_add_u32 v244, 0x0, v243\n\t" \
    "s_waitcnt vmcnt(12) lgkmcnt(0)\n\t" \
    "s_barrier \n\t" \
    "v_mov_b32 v246, 0\n\t" \
    "buffer_load_dword v246, v246, s[60:63], %[mo] offen sc1\n\t" \
    "s_add_u32 m0, %[wm], 0x1e000\n\t" \
    "s_mov_b32 s44, 0x4e0000\n\t" \
    "buffer_load_dwordx4 v240, %[wr], s44 offen offset:0 nt lds\n\t" \
    "buffer_load_dwordx4 v241, %[wr], s44 offen offset:1024 nt lds\n\t" \
    "buffer_load_dwordx4 v242, %[wr], s44 offen offset:2048 nt lds\n\t" \
    "ds_read_b64_tr_b16 v[232:233], v244 offset:0\n\t" \
    "ds_read_b64_tr_b16 v[234:235], v244 offset:1536\n\t" \
    "ds_read_b64_tr_b16 v[160:161], v244 offset:32\n\t" \
    "ds_read_b64_tr_b16 v[162:163], v244 offset:1568\n\t" \
    "ds_read_b64_tr_b16 v[168:169], v244 offset:64\n\t" \
    "ds_read_b64_tr_b16 v[170:171], v244 offset:1600\n\t" \
    "v_add_u32 v244, 0x6000, v243\n\t" \
    "s_waitcnt vmcnt(12) lgkmcnt(0)\n\t" \
    "s_barrier \n\t" \
    "s_add_u32 m0, %[wm], 0x0\n\t" \
    "s_mov_b32 s44, 0x540000\n\t" \
    "buffer_load_dwordx4 v240, %[wr], s44 offen offset:0 nt lds\n\t" \
    "buffer_load_dwordx4 v241, %[wr], s44 offen offset:1024 nt lds\n\t" \
    "buffer_load_dwordx4 v242, %[wr], s44 offen offset:2048 nt lds\n\t" \
    "ds_read_b64_tr_b16 v[236:237], v244 offset:0\n\t" \
    "ds_read_b64_tr_b16 v[238:239], v244 offset:1536\n\t" \
    "ds_read_b64_tr_b16 v[164:165], v244 offset:32\n\t" \
    "ds_read_b64_tr_b16 v[166:167], v244 offset:1568\n\t" \
    "ds_read_b64_tr_b16 v[172:173], v244 offset:64\n\t" \
    "ds_read_b64_tr_b16 v[174:175], v244 offset:1600\n\t" \
    "v_add_u32 v244, 0xc000, v243\n\t" \
    "s_waitcnt vmcnt(12) lgkmcnt(0)\n\t" \
    "s_barrier \n\t" \
    "s_add_u32 m0, %[wm], 0x6000\n\t" \
    "s_mov_b32 s44, 0x5a0000\n\t" \
    "buffer_load_dwordx4 v240, %[wr], s44 offen offset:0 nt lds\n\t" \
    "buffer_load_dwordx4 v241, %[wr], s44 offen offset:1024 nt lds\n\t" \
    "buffer_load_dwordx4 v242, %[wr], s44 offen offset:2048 nt lds\n\t" \
    "ds_read_b64_tr_b16 v[16:17], v244 offset:0\n\t" \
    "ds_read_b64_tr_b16 v[18:19], v244 offset:1536\n\t" \
    "ds_read_b64_tr_b16 v[24:25], v244 offset:32\n\t" \
    "ds_read_b64_tr_b16 v[26:27], v244 offset:1568\n\t" \
    "ds_read_b64_tr_b16 v[32:33], v244 offset:64\n\t" \
    "ds_read_b64_tr_b16 v[34:35], v244 offset:1600\n\t" \
    "v_add_u32 v244, 0x12000, v243\n\t" \
    "s_waitcnt vmcnt(12) lgkmcnt(0)\n\t" \
    "s_barrier \n\t" \
    "s_add_u32 m0, %[wm], 0xc000\n\t" \
    "s_mov_b32 s44, 0x600000\n\t" \
    "buffer_load_dwordx4 v240, %[wr], s44 offen offset:0 nt lds\n\t" \
    "buffer_load_dwordx4 v241, %[wr], s44 offen offset:1024 nt lds\n\t" \
    "buffer_load_dwordx4 v242, %[wr], s44 offen offset:2048 nt lds\n\t" \
    "ds_read_b64_tr_b16 v[20:21], v244 offset:0\n\t" \
    "ds_read_b64_tr_b16 v[22:23], v244 offset:1536\n\t" \
    "ds_read_b64_tr_b16 v[28:29], v244 offset:32\n\t" \
    "ds_read_b64_tr_b16 v[30:31], v244 offset:1568\n\t" \
    "ds_read_b64_tr_b16 v[36:37], v244 offset:64\n\t" \
    "ds_read_b64_tr_b16 v[38:39], v244 offset:1600\n\t" \
    "v_add_u32 v244, 0x18000, v243\n\t" \
    "s_waitcnt vmcnt(12) lgkmcnt(0)\n\t" \
    "s_barrier \n\t" \
    "s_add_u32 m0, %[wm], 0x12000\n\t" \
    "s_mov_b32 s44, 0x660000\n\t" \
    "buffer_load_dwordx4 v240, %[wr], s44 offen offset:0 nt lds\n\t" \
    "buffer_load_dwordx4 v241, %[wr], s44 offen offset:1024 nt lds\n\t" \
    "buffer_load_dwordx4 v242, %[wr], s44 offen offset:2048 nt lds\n\t" \
    "ds_read_b64_tr_b16 v[56:57], v244 offset:0\n\t" \
    "ds_read_b64_tr_b16 v[58:59], v244 offset:1536\n\t" \
    "ds_read_b64_tr_b16 v[64:65], v244 offset:32\n\t" \
    "ds_read_b64_tr_b16 v[66:67], v244 offset:1568\n\t" \
    "ds_read_b64_tr_b16 v[72:73], v244 offset:64\n\t" \
    "ds_read_b64_tr_b16 v[74:75], v244 offset:1600\n\t" \
    "v_add_u32 v244, 0x1e000, v243\n\t" \
    "s_waitcnt vmcnt(12) lgkmcnt(0)\n\t" \
    "s_barrier \n\t" \
    ".Ls01_%=:\n\t" \
    "v_readfirstlane_b32 s43, v246\n\t" \
    "s_nop 0\n\t" \
    "s_cmp_lg_u32 s43, -1\n\t" \
    "s_cbranch_scc1 .Ls02_%=\n\t" \
    "s_cmp_eq_u32 s41, 0\n\t" \
    "s_cbranch_scc1 .Ls02_%=\n\t" \
    "s_sub_u32 s41, s41, 1\n\t" \
    "s_sleep 4\n\t" \
    "v_mov_b32 v246, 0\n\t" \
    "buffer_load_dword v246, v246, s[60:63], %[mo] offen sc1\n\t" \
    "s_waitcnt vmcnt(0)\n\t" \
    "s_branch .Ls01_%=\n\t" \
    ".Ls02_%=:\n\t" \
    "v_mov_b32 v246, 0x8000\n\t" \
    "s_mov_b64 exec, 1\n\t" \
    "buffer_store_dword v246, v246, s[60:63], %[mo] offen sc1\n\t" \
    "s_mov_b64 exec, -1\n\t" \
    "s_and_b32 s44, s43, 0x5000\n\t" \
    "s_xor_b32 %[mb], %[mb], s44\n\t" \
    "s_xor_b32 %[fl], %[fl], s44\n\t" \
    "s_xor_b32 %[pb], %[pb], s44\n\t" \
    "s_and_b32 s43, s43, 0x2000\n\t" \
    "s_xor_b32 s43, s43, %[lx]\n\t" \
    "s_add_u32 s60, s60, s43\n\t" \
    "s_addc_u32 s61, s61, 0\n\t" \
    "s_add_u32 m0, %[wm], 0x18000\n\t" \
    "s_mov_b32 s44, 0x6c0000\n\t" \
    "buffer_load_dwordx4 v240, %[wr], s44 offen offset:0 nt lds\n\t" \
    "buffer_load_dwordx4 v241, %[wr], s44 offen offset:1024 nt lds\n\t" \
    "buffer_load_dwordx4 v242, %[wr], s44 offen offset:2048 nt lds\n\t" \
    "ds_read_b64_tr_b16 v[60:61], v244 offset:0\n\t" \
    "ds_read_b64_tr_b16 v[62:63], v244 offset:1536\n\t" \
    "ds_read_b64_tr_b16 v[68:69], v244 offset:32\n\t" \
    "ds_read_b64_tr_b16 v[70:71], v244 offset:1568\n\t" \
    "ds_read_b64_tr_b16 v[76:77], v244 offset:64\n\t" \
    "ds_read_b64_tr_b16 v[78:79], v244 offset:1600\n\t" \
    "v_add_u32 v244, 0x0, v243\n\t" \
    "s_waitcnt vmcnt(12) lgkmcnt(0)\n\t" \
    "s_barrier \n\t" \
    "s_add_u32 m0, %[wm], 0x1e000\n\t" \
    "s_mov_b32 s44, 0x720000\n\t" \
    "buffer_load_dwordx4 v240, %[wr], s44 offen offset:0 nt lds\n\t" \
    "buffer_load_dwordx4 v241, %[wr], s44 offen offset:1024 nt lds\n\t" \
    "buffer_load_dwordx4 v242, %[wr], s44 offen offset:2048 nt lds\n\t" \
    "ds_read_b64_tr_b16 v[96:97], v244 offset:0\n\t" \
    "ds_read_b64_tr_b16 v[98:99], v244 offset:1536\n\t" \
    "ds_read_b64_tr_b16 v[104:105], v244 offset:32\n\t" \
    "ds_read_b64_tr_b16 v[106:107], v244 offset:1568\n\t" \
    "ds_read_b64_tr_b16 v[112:113], v244 offset:64\n\t" \
    "ds_read_b64_tr_b16 v[114:115], v244 offset:1600\n\t" \
    "v_add_u32 v244, 0x6000, v243\n\t" \
    "s_waitcnt vmcnt(12) lgkmcnt(0)\n\t" \
    "s_barrier \n\t" \
    "ds_read_b64_tr_b16 v[100:101], v244 offset:0\n\t" \
    "ds_read_b64_tr_b16 v[102:103], v244 offset:1536\n\t" \
    "ds_read_b64_tr_b16 v[108:109], v244 offset:32\n\t" \
    "ds_read_b64_tr_b16 v[110:111], v244 offset:1568\n\t" \
    "ds_read_b64_tr_b16 v[116:117], v244 offset:64\n\t" \
    "ds_read_b64_tr_b16 v[118:119], v244 offset:1600\n\t" \
    "v_add_u32 v244, 0xc000, v243\n\t" \
    "s_waitcnt vmcnt(9) lgkmcnt(0)\n\t" \
    "s_barrier \n\t" \
    "ds_read_b64_tr_b16 v[136:137], v244 offset:0\n\t" \
    "ds_read_b64_tr_b16 v[138:139], v244 offset:1536\n\t" \
    "ds_read_b64_tr_b16 v[144:145], v244 offset:32\n\t" \
    "ds_read_b64_tr_b16 v[146:147], v244 offset:1568\n\t" \
    "ds_read_b64_tr_b16 v[152:153], v244 offset:64\n\t" \
    "ds_read_b64_tr_b16 v[154:155], v244 offset:1600\n\t" \
    "v_add_u32 v244, 0x12000, v243\n\t" \
    "s_waitcnt vmcnt(6) lgkmcnt(0)\n\t" \
    "s_barrier \n\t" \
    "ds_read_b64_tr_b16 v[140:141], v244 offset:0\n\t" \
    "ds_read_b64_tr_b16 v[142:143], v244 offset:1536\n\t" \
    "ds_read_b64_tr_b16 v[148:149], v244 offset:32\n\t" \
    "ds_read_b64_tr_b16 v[150:151], v244 offset:1568\n\t" \
    "ds_read_b64_tr_b16 v[156:157], v244 offset:64\n\t" \
    "ds_read_b64_tr_b16 v[158:159], v244 offset:1600\n\t" \
    "v_add_u32 v244, 0x18000, v243\n\t" \
    "s_waitcnt vmcnt(3) lgkmcnt(0)\n\t" \
    "s_barrier \n\t" \
    "ds_read_b64_tr_b16 v[176:177], v244 offset:0\n\t" \
    "ds_read_b64_tr_b16 v[178:179], v244 offset:1536\n\t" \
    "ds_read_b64_tr_b16 v[184:185], v244 offset:32\n\t" \
    "ds_read_b64_tr_b16 v[186:187], v244 offset:1568\n\t" \
    "ds_read_b64_tr_b16 v[192:193], v244 offset:64\n\t" \
    "ds_read_b64_tr_b16 v[194:195], v244 offset:1600\n\t" \
    "v_add_u32 v244, 0x1e000, v243\n\t" \
    "s_waitcnt vmcnt(0) lgkmcnt(0)\n\t" \
    "s_barrier \n\t" \
    "ds_read_b64_tr_b16 v[180:181], v244 offset:0\n\t" \
    "ds_read_b64_tr_b16 v[182:183], v244 offset:1536\n\t" \
    "ds_read_b64_tr_b16 v[188:189], v244 offset:32\n\t" \
    "ds_read_b64_tr_b16 v[190:191], v244 offset:1568\n\t" \
    "ds_read_b64_tr_b16 v[196:197], v244 offset:64\n\t" \
    "ds_read_b64_tr_b16 v[198:199], v244 offset:1600\n\t" \
    "s_waitcnt lgkmcnt(0)\n\t" \
    "s_barrier \n\t" \
    LST0 \
    LJIT \
    "ds_write_b128 v254, v[200:203] offset:30720\n\t" \
    "ds_write_b128 v254, v[204:207] offset:31744\n\t" \
    "ds_write_b128 v254, v[208:211] offset:32768\n\t" \
    "ds_write_b128 v254, v[212:215] offset:33792\n\t" \
    "ds_write_b128 v254, v[216:219] offset:34816\n\t" \
    "ds_write_b128 v254, v[220:223] offset:35840\n\t" \
    "ds_write_b128 v254, v[224:227] offset:36864\n\t" \
    "ds_write_b128 v254, v[228:231] offset:37888\n\t" \
    "ds_write_b128 v254, v[232:235] offset:38912\n\t" \
    "ds_write_b128 v254, v[236:239] offset:39936\n\t" \
    "s_waitcnt vmcnt(0) lgkmcnt(0)\n\t" \
    "s_barrier \n\t" \
    "ds_read_b128 v[224:227], v253 offset:0\n\t" \
    "ds_read_b128 v[228:231], v253 offset:20480\n\t" \
    "ds_read_b128 v[232:235], v253 offset:1024\n\t" \
    "ds_read_b128 v[236:239], v253 offset:21504\n\t" \
    ".p2align 8\n\t" \
    ".Ls03_%=:\n\t" \
    "s_waitcnt lgkmcnt(3)\n\t" \
    "v_mfma_f32_16x16x32_bf16 v[204:207], v[0:3], v[224:227], 0\n\t" \
    "ds_read_b128 v[240:243], v253 offset:2048\n\t" \
    "v_mfma_f32_16x16x32_bf16 v[208:211], v[8:11], v[224:227], 0\n\t" \
    "s_waitcnt lgkmcnt(3)\n\t" \
    "v_mfma_f32_16x16x32_bf16 v[216:219], v[0:3], v[228:231], 0\n\t" \
    "ds_read_b128 v[224:227], v253 offset:22528\n\t" \
    "v_mfma_f32_16x16x32_bf16 v[220:223], v[8:11], v[228:231], 0\n\t" \
    "s_waitcnt lgkmcnt(3)\n\t" \
    "v_mfma_f32_16x16x32_bf16 v[204:207], v[4:7], v[232:235], v[204:207]\n\t" \
    "ds_read_b128 v[228:231], v253 offset:3072\n\t" \
    "v_mfma_f32_16x16x32_bf16 v[208:211], v[12:15], v[232:235], v[208:211]\n\t" \
    "s_waitcnt lgkmcnt(3)\n\t" \
    "v_mfma_f32_16x16x32_bf16 v[216:219], v[4:7], v[236:239], v[216:219]\n\t" \
    "ds_read_b128 v[232:235], v253 offset:23552\n\t" \
    "v_mfma_f32_16x16x32_bf16 v[220:223], v[12:15], v[236:239], v[220:223]\n\t" \
    "s_waitcnt lgkmcnt(3)\n\t" \
    "v_mfma_f32_16x16x32_bf16 v[204:207], v[40:43], v[240:243], v[204:207]\n\t" \
    "ds_read_b128 v[236:239], v253 offset:4096\n\t" \
    "v_mfma_f32_16x16x32_bf16 v[208:211], v[48:51], v[240:243], v[208:211]\n\t" \
    "s_waitcnt lgkmcnt(3)\n\t" \
    "v_mfma_f32_16x16x32_bf16 v[216:219], v[40:43], v[224:227], v[216:219]\n\t" \
    "ds_read_b128 v[240:243], v253 offset:24576\n\t" \
    "v_mfma_f32_16x16x32_bf16 v[220:223], v[48:51], v[224:227], v[220:223]\n\t" \
    "s_waitcnt lgkmcnt(3)\n\t" \
    "v_mfma_f32_16x16x32_bf16 v[204:207], v[44:47], v[228:231], v[204:207]\n\t" \
    "ds_read_b128 v[224:227], v253 offset:5120\n\t" \
    "v_mfma_f32_16x16x32_bf16 v[208:211], v[52:55], v[228:231], v[208:211]\n\t" \
    "s_waitcnt lgkmcnt(3)\n\t" \
    "v_mfma_f32_16x16x32_bf16 v[216:219], v[44:47], v[232:235], v[216:219]\n\t" \
    "ds_read_b128 v[228:231], v253 offset:25600\n\t" \
    "v_mfma_f32_16x16x32_bf16 v[220:223], v[52:55], v[232:235], v[220:223]\n\t" \
    "s_waitcnt lgkmcnt(3)\n\t" \
    "v_mfma_f32_16x16x32_bf16 v[204:207], v[80:83], v[236:239], v[204:207]\n\t" \
    "ds_read_b128 v[232:235], v253 offset:6144\n\t" \
    "v_mfma_f32_16x16x32_bf16 v[208:211], v[88:91], v[236:239], v[208:211]\n\t" \
    "s_waitcnt lgkmcnt(3)\n\t" \
    "v_mfma_f32_16x16x32_bf16 v[216:219], v[80:83], v[240:243], v[216:219]\n\t" \
    "ds_read_b128 v[236:239], v253 offset:26624\n\t" \
    "v_mfma_f32_16x16x32_bf16 v[220:223], v[88:91], v[240:243], v[220:223]\n\t" \
    "s_waitcnt lgkmcnt(3)\n\t" \
    "v_mfma_f32_16x16x32_bf16 v[204:207], v[84:87], v[224:227], v[204:207]\n\t" \
    "ds_read_b128 v[240:243], v253 offset:7168\n\t" \
    "v_mfma_f32_16x16x32_bf16 v[208:211], v[92:95], v[224:227], v[208:211]\n\t" \
    "s_waitcnt lgkmcnt(3)\n\t" \
    "v_mfma_f32_16x16x32_bf16 v[216:219], v[84:87], v[228:231], v[216:219]\n\t" \
    "ds_read_b128 v[224:227], v253 offset:27648\n\t" \
    "v_mfma_f32_16x16x32_bf16 v[220:223], v[92:95], v[228:231], v[220:223]\n\t" \
    "s_waitcnt lgkmcnt(3)\n\t" \
    "v_mfma_f32_16x16x32_bf16 v[204:207], v[120:123], v[232:235], v[204:207]\n\t" \
    "ds_read_b128 v[228:231], v253 offset:8192\n\t" \
    "v_mfma_f32_16x16x32_bf16 v[208:211], v[128:131], v[232:235], v[208:211]\n\t" \
    "s_waitcnt lgkmcnt(3)\n\t" \
    "v_mfma_f32_16x16x32_bf16 v[216:219], v[120:123], v[236:239], v[216:219]\n\t" \
    "ds_read_b128 v[232:235], v253 offset:28672\n\t" \
    "v_mfma_f32_16x16x32_bf16 v[220:223], v[128:131], v[236:239], v[220:223]\n\t" \
    "s_waitcnt lgkmcnt(3)\n\t" \
    "v_mfma_f32_16x16x32_bf16 v[204:207], v[124:127], v[240:243], v[204:207]\n\t" \
    "ds_read_b128 v[236:239], v253 offset:9216\n\t" \
    "v_mfma_f32_16x16x32_bf16 v[208:211], v[132:135], v[240:243], v[208:211]\n\t" \
    "s_waitcnt lgkmcnt(3)\n\t" \
    "v_mfma_f32_16x16x32_bf16 v[216:219], v[124:127], v[224:227], v[216:219]\n\t" \
    "ds_read_b128 v[240:243], v253 offset:29696\n\t" \
    "v_mfma_f32_16x16x32_bf16 v[220:223], v[132:135], v[224:227], v[220:223]\n\t" \
    "s_waitcnt lgkmcnt(3)\n\t" \
    "v_mfma_f32_16x16x32_bf16 v[204:207], v[160:163], v[228:231], v[204:207]\n\t" \
    "ds_read_b128 v[224:227], v253 offset:10240\n\t" \
    "v_mfma_f32_16x16x32_bf16 v[208:211], v[168:171], v[228:231], v[208:211]\n\t" \
    "s_waitcnt lgkmcnt(3)\n\t" \
    "v_mfma_f32_16x16x32_bf16 v[216:219], v[160:163], v[232:235], v[216:219]\n\t" \
    "ds_read_b128 v[228:231], v253 offset:30720\n\t" \
    "v_mfma_f32_16x16x32_bf16 v[220:223], v[168:171], v[232:235], v[220:223]\n\t" \
    "s_waitcnt lgkmcnt(3)\n\t" \
    "v_mfma_f32_16x16x32_bf16 v[204:207], v[164:167], v[236:239], v[204:207]\n\t" \
    "ds_read_b128 v[232:235], v253 offset:11264\n\t" \
    "v_mfma_f32_16x16x32_bf16 v[208:211], v[172:175], v[236:239], v[208:211]\n\t" \
    "s_waitcnt lgkmcnt(3)\n\t" \
    "v_mfma_f32_16x16x32_bf16 v[216:219], v[164:167], v[240:243], v[216:219]\n\t" \
    "ds_read_b128 v[236:239], v253 offset:31744\n\t" \
    "v_mfma_f32_16x16x32_bf16 v[220:223], v[172:175], v[240:243], v[220:223]\n\t" \
    "s_setprio 2\n\t" \
    "s_waitcnt lgkmcnt(0)\n\t" \
    "s_barrier \n\t" \
    LST7 \
    LJIT \
    "ds_read_b128 v[200:203], v253 offset:40960\n\t" \
    "ds_read_b128 v[212:215], v253 offset:41984\n\t" \
    "v_mfma_f32_16x16x32_bf16 v[204:207], v[24:27], v[224:227], v[204:207]\n\t" \
    "ds_read_b128 v[240:243], v253 offset:12288\n\t" \
    "v_mfma_f32_16x16x32_bf16 v[208:211], v[32:35], v[224:227], v[208:211]\n\t" \
    "s_waitcnt lgkmcnt(2)\n\t" \
    "v_mfma_f32_16x16x32_bf16 v[200:203], v[16:19], v[224:227], v[200:203]\n\t" \
    "v_mfma_f32_16x16x32_bf16 v[216:219], v[24:27], v[228:231], v[216:219]\n\t" \
    "ds_read_b128 v[224:227], v253 offset:32768\n\t" \
    "s_and_b32 s44, s40, 3\n\t" \
    "v_mfma_f32_16x16x32_bf16 v[220:223], v[32:35], v[228:231], v[220:223]\n\t" \
    "s_lshl_b32 s44, s44, 22\n\t" \
    "s_waitcnt lgkmcnt(2)\n\t" \
    "v_mfma_f32_16x16x32_bf16 v[212:215], v[16:19], v[228:231], v[212:215]\n\t" \
    "s_add_u32 s44, s44, %[mb]\n\t" \
    "v_mfma_f32_16x16x32_bf16 v[204:207], v[28:31], v[232:235], v[204:207]\n\t" \
    "ds_read_b128 v[228:231], v253 offset:13312\n\t" \
    "s_add_u32 s46, s44, 0x0\n\t" \
    "v_mfma_f32_16x16x32_bf16 v[208:211], v[36:39], v[232:235], v[208:211]\n\t" \
    "s_add_u32 s47, s44, 0x8400\n\t" \
    "v_mfma_f32_16x16x32_bf16 v[200:203], v[20:23], v[232:235], v[200:203]\n\t" \
    "s_add_u32 s48, s44, 0x400\n\t" \
    "v_mfma_f32_16x16x32_bf16 v[216:219], v[28:31], v[236:239], v[216:219]\n\t" \
    "ds_read_b128 v[232:235], v253 offset:33792\n\t" \
    "v_mfma_f32_16x16x32_bf16 v[220:223], v[36:39], v[236:239], v[220:223]\n\t" \
    "v_mfma_f32_16x16x32_bf16 v[212:215], v[20:23], v[236:239], v[212:215]\n\t" \
    "s_waitcnt lgkmcnt(3)\n\t" \
    "v_mfma_f32_16x16x32_bf16 v[204:207], v[64:67], v[240:243], v[204:207]\n\t" \
    "ds_read_b128 v[236:239], v253 offset:14336\n\t" \
    "v_mfma_f32_16x16x32_bf16 v[208:211], v[72:75], v[240:243], v[208:211]\n\t" \
    "v_mfma_f32_16x16x32_bf16 v[200:203], v[56:59], v[240:243], v[200:203]\n\t" \
    "s_waitcnt lgkmcnt(3)\n\t" \
    "v_mfma_f32_16x16x32_bf16 v[216:219], v[64:67], v[224:227], v[216:219]\n\t" \
    "ds_read_b128 v[240:243], v253 offset:34816\n\t" \
    "v_mfma_f32_16x16x32_bf16 v[220:223], v[72:75], v[224:227], v[220:223]\n\t" \
    "v_mfma_f32_16x16x32_bf16 v[212:215], v[56:59], v[224:227], v[212:215]\n\t" \
    "s_waitcnt lgkmcnt(3)\n\t" \
    "v_mfma_f32_16x16x32_bf16 v[204:207], v[68:71], v[228:231], v[204:207]\n\t" \
    "ds_read_b128 v[224:227], v253 offset:15360\n\t" \
    "v_mfma_f32_16x16x32_bf16 v[208:211], v[76:79], v[228:231], v[208:211]\n\t" \
    "v_mfma_f32_16x16x32_bf16 v[200:203], v[60:63], v[228:231], v[200:203]\n\t" \
    "s_waitcnt lgkmcnt(3)\n\t" \
    "v_mfma_f32_16x16x32_bf16 v[216:219], v[68:71], v[232:235], v[216:219]\n\t" \
    "ds_read_b128 v[228:231], v253 offset:35840\n\t" \
    "v_mfma_f32_16x16x32_bf16 v[220:223], v[76:79], v[232:235], v[220:223]\n\t" \
    "v_mfma_f32_16x16x32_bf16 v[212:215], v[60:63], v[232:235], v[212:215]\n\t" \
    "s_waitcnt lgkmcnt(3)\n\t" \
    "v_mfma_f32_16x16x32_bf16 v[204:207], v[104:107], v[236:239], v[204:207]\n\t" \
    "ds_read_b128 v[232:235], v253 offset:16384\n\t" \
    "v_mfma_f32_16x16x32_bf16 v[208:211], v[112:115], v[236:239], v[208:211]\n\t" \
    "v_mfma_f32_16x16x32_bf16 v[200:203], v[96:99], v[236:239], v[200:203]\n\t" \
    "s_waitcnt lgkmcnt(3)\n\t" \
    "v_mfma_f32_16x16x32_bf16 v[216:219], v[104:107], v[240:243], v[216:219]\n\t" \
    "ds_read_b128 v[236:239], v253 offset:17408\n\t" \
    "v_mfma_f32_16x16x32_bf16 v[220:223], v[112:115], v[240:243], v[220:223]\n\t" \
    "v_mfma_f32_16x16x32_bf16 v[212:215], v[96:99], v[240:243], v[212:215]\n\t" \
    "s_waitcnt lgkmcnt(3)\n\t" \
    "v_mfma_f32_16x16x32_bf16 v[204:207], v[108:111], v[224:227], v[204:207]\n\t" \
    "ds_read_b128 v[240:243], v253 offset:18432\n\t" \
    "v_mfma_f32_16x16x32_bf16 v[208:211], v[116:119], v[224:227], v[208:211]\n\t" \
    "v_mfma_f32_16x16x32_bf16 v[200:203], v[100:103], v[224:227], v[200:203]\n\t" \
    "s_waitcnt lgkmcnt(3)\n\t" \
    "v_mfma_f32_16x16x32_bf16 v[216:219], v[108:111], v[228:231], v[216:219]\n\t" \
    "ds_read_b128 v[224:227], v253 offset:19456\n\t" \
    "v_mfma_f32_16x16x32_bf16 v[220:223], v[116:119], v[228:231], v[220:223]\n\t" \
    "v_mfma_f32_16x16x32_bf16 v[212:215], v[100:103], v[228:231], v[212:215]\n\t" \
    "s_waitcnt lgkmcnt(3)\n\t" \
    "v_mfma_f32_16x16x32_bf16 v[204:207], v[144:147], v[232:235], v[204:207]\n\t" \
    "ds_read_b128 v[228:231], v253 offset:36864\n\t" \
    "v_mov_b64 v[244:245], 0\n\t" \
    "v_mfma_f32_16x16x32_bf16 v[208:211], v[152:155], v[232:235], v[208:211]\n\t" \
    "v_mov_b64 v[246:247], 0\n\t" \
    "v_mfma_f32_16x16x32_bf16 v[200:203], v[136:139], v[232:235], v[200:203]\n\t" \
    "v_mov_b64 v[248:249], 0\n\t" \
    "s_waitcnt lgkmcnt(3)\n\t" \
    "v_mfma_f32_16x16x32_bf16 v[204:207], v[148:151], v[236:239], v[204:207]\n\t" \
    "ds_read_b128 v[232:235], v253 offset:37888\n\t" \
    "v_mov_b64 v[250:251], 0\n\t" \
    "v_mfma_f32_16x16x32_bf16 v[208:211], v[156:159], v[236:239], v[208:211]\n\t" \
    "v_mfma_f32_16x16x32_bf16 v[200:203], v[140:143], v[236:239], v[200:203]\n\t" \
    "s_waitcnt lgkmcnt(3)\n\t" \
    "v_mfma_f32_16x16x32_bf16 v[204:207], v[184:187], v[240:243], v[204:207]\n\t" \
    "ds_read_b128 v[236:239], v253 offset:38912\n\t" \
    "v_mfma_f32_16x16x32_bf16 v[208:211], v[192:195], v[240:243], v[208:211]\n\t" \
    "v_mfma_f32_16x16x32_bf16 v[200:203], v[176:179], v[240:243], v[200:203]\n\t" \
    "s_waitcnt lgkmcnt(3)\n\t" \
    "v_mfma_f32_16x16x32_bf16 v[204:207], v[188:191], v[224:227], v[204:207]\n\t" \
    "ds_read_b128 v[240:243], v253 offset:39936\n\t" \
    "v_mfma_f32_16x16x32_bf16 v[208:211], v[196:199], v[224:227], v[208:211]\n\t" \
    "v_mfma_f32_16x16x32_bf16 v[200:203], v[180:183], v[224:227], v[200:203]\n\t" \
    "s_waitcnt lgkmcnt(3)\n\t" \
    "v_mfma_f32_16x16x32_bf16 v[216:219], v[144:147], v[228:231], v[216:219]\n\t" \
    "s_waitcnt lgkmcnt(2)\n\t" \
    "v_mfma_f32_16x16x32_bf16 v[216:219], v[148:151], v[232:235], v[216:219]\n\t" \
    "v_cvt_pk_bf16_f32 v204, v204, v208\n\t" \
    "v_cvt_pk_bf16_f32 v205, v205, v209\n\t" \
    "s_waitcnt lgkmcnt(1)\n\t" \
    "v_mfma_f32_16x16x32_bf16 v[216:219], v[184:187], v[236:239], v[216:219]\n\t" \
    "v_cvt_pk_bf16_f32 v206, v206, v210\n\t" \
    "v_cvt_pk_bf16_f32 v207, v207, v211\n\t" \
    "s_waitcnt lgkmcnt(0)\n\t" \
    "v_mfma_f32_16x16x32_bf16 v[216:219], v[188:191], v[240:243], v[216:219]\n\t" \
    "ds_write_b128 v254, v[204:207] offset:8192\n\t" \
    "v_mfma_f32_16x16x32_bf16 v[220:223], v[152:155], v[228:231], v[220:223]\n\t" \
    "v_mfma_f32_16x16x32_bf16 v[220:223], v[156:159], v[232:235], v[220:223]\n\t" \
    "v_cvt_pk_bf16_f32 v200, v200, v201\n\t" \
    "v_mfma_f32_16x16x32_bf16 v[220:223], v[192:195], v[236:239], v[220:223]\n\t" \
    "v_cvt_pk_bf16_f32 v201, v202, v203\n\t" \
    "ds_write_b64 v254, v[200:201] offset:6144\n\t" \
    "v_mfma_f32_16x16x32_bf16 v[220:223], v[196:199], v[240:243], v[220:223]\n\t" \
    "v_mfma_f32_16x16x32_bf16 v[212:215], v[136:139], v[228:231], v[212:215]\n\t" \
    "v_mfma_f32_16x16x32_bf16 v[212:215], v[140:143], v[232:235], v[212:215]\n\t" \
    "ds_read_b128 v[200:203], v254 offset:30720\n\t" \
    "ds_read_b128 v[204:207], v254 offset:31744\n\t" \
    "v_mfma_f32_16x16x32_bf16 v[212:215], v[176:179], v[236:239], v[212:215]\n\t" \
    "v_mov_b64 v[208:209], 0\n\t" \
    "v_mov_b64 v[210:211], 0\n\t" \
    "v_mfma_f32_16x16x32_bf16 v[212:215], v[180:183], v[240:243], v[212:215]\n\t" \
    "v_cvt_pk_bf16_f32 v216, v216, v220\n\t" \
    "v_cvt_pk_bf16_f32 v217, v217, v221\n\t" \
    "s_setprio 0\n\t" \
    "v_cvt_pk_bf16_f32 v218, v218, v222\n\t" \
    "v_cvt_pk_bf16_f32 v219, v219, v223\n\t" \
    "ds_write_b128 v254, v[216:219] offset:14336\n\t" \
    "ds_read_b128 v[216:219], v254 offset:32768\n\t" \
    "ds_read_b128 v[220:223], v254 offset:33792\n\t" \
    "s_nop 0\n\t" \
    "v_cvt_pk_bf16_f32 v212, v212, v213\n\t" \
    "v_cvt_pk_bf16_f32 v213, v214, v215\n\t" \
    "ds_write_b64 v254, v[212:213] offset:12288\n\t" \
    LST6 \
    LJIT \
    "s_sleep 3\n\t" \
    "s_mov_b32 m0, 0xa000\n\t" \
    "s_nop 0\n\t" \
    "buffer_load_dwordx4 v253, s[60:63], s46 offen sc1 lds\n\t" \
    "s_mov_b32 m0, 0xb400\n\t" \
    "s_nop 0\n\t" \
    "buffer_load_dwordx4 v253, s[60:63], s47 offen sc1 lds\n\t" \
    "s_mov_b32 m0, 0xa400\n\t" \
    "s_nop 0\n\t" \
    "buffer_load_dwordx4 v253, s[60:63], s48 offen sc1 lds\n\t" \
    "s_waitcnt lgkmcnt(0)\n\t" \
    LFLUSH212 \
    "v_bfe_i32 v212, v253, 5, 1\n\t" \
    "v_and_b32 v212, 0xaaaa, v212\n\t" \
    "v_or_b32 v212, 0x4444, v212\n\t" \
    "v_and_b32 v213, 0xffff, v255\n\t" \
    "v_lshrrev_b32 v214, 16, v255\n\t" \
    ".Ls04_%=:\n\t" \
    "s_waitcnt vmcnt(0)\n\t" \
    "ds_read_b128 v[224:227], v253 offset:40960\n\t" \
    "ds_read_b128 v[228:231], v253 offset:46080\n\t" \
    "ds_read_b128 v[232:235], v253 offset:41984\n\t" \
    "s_waitcnt lgkmcnt(2)\n\t" \
    "v_max_u32 v215, v224, v225\n\t" \
    "v_max3_u32 v215, v226, v227, v215\n\t" \
    "s_waitcnt lgkmcnt(1)\n\t" \
    "v_max3_u32 v215, v228, v229, v215\n\t" \
    "v_max3_u32 v215, v230, v231, v215\n\t" \
    "s_waitcnt lgkmcnt(0)\n\t" \
    "v_max3_u32 v215, v232, v233, v215\n\t" \
    "v_max3_u32 v215, v234, v235, v215\n\t" \
    "v_cmp_eq_u32 vcc, -1, v215\n\t" \
    "s_cbranch_vccz .Ls05_%=\n\t" \
    "v_max_u32 v215, v224, v225\n\t" \
    "v_max3_u32 v215, v226, v227, v215\n\t" \
    "v_cmp_eq_u32 vcc, -1, v215\n\t" \
    "s_cbranch_vccz .Ls06_%=\n\t" \
    "s_mov_b32 m0, 0xa000\n\t" \
    "s_nop 0\n\t" \
    "buffer_load_dwordx4 v253, s[60:63], s46 offen sc1 lds\n\t" \
    ".Ls06_%=:\n\t" \
    "v_max_u32 v215, v228, v229\n\t" \
    "v_max3_u32 v215, v230, v231, v215\n\t" \
    "v_cmp_eq_u32 vcc, -1, v215\n\t" \
    "s_cbranch_vccz .Ls07_%=\n\t" \
    "s_mov_b32 m0, 0xb400\n\t" \
    "s_nop 0\n\t" \
    "buffer_load_dwordx4 v253, s[60:63], s47 offen sc1 lds\n\t" \
    ".Ls07_%=:\n\t" \
    "v_max_u32 v215, v232, v233\n\t" \
    "v_max3_u32 v215, v234, v235, v215\n\t" \
    "v_cmp_eq_u32 vcc, -1, v215\n\t" \
    "s_cbranch_vccz .Ls08_%=\n\t" \
    "s_mov_b32 m0, 0xa400\n\t" \
    "s_nop 0\n\t" \
    "buffer_load_dwordx4 v253, s[60:63], s48 offen sc1 lds\n\t" \
    ".Ls08_%=:\n\t" \
    "s_cmp_eq_u32 s41, 0\n\t" \
    "s_cbranch_scc1 .Ls05_%=\n\t" \
    "s_sub_u32 s41, s41, 1\n\t" \
    "s_branch .Ls04_%=\n\t" \
    ".Ls05_%=:\n\t" \
    LST2 \
    LJIT \
    "s_setprio 3\n\t" \
    "s_waitcnt lgkmcnt(0)\n\t" \
    "s_barrier \n\t" \
    LST3 \
    LJIT \
    "ds_read_b128 v[236:239], v253 offset:47104\n\t" \
    "ds_read_b128 v[240:243], v253 offset:43008\n\t" \
    "v_smfmac_f32_16x16x64_bf16 v[244:247], v[224:227], v[0:7], v212\n\t" \
    "v_smfmac_f32_16x16x64_bf16 v[248:251], v[224:227], v[8:15], v212\n\t" \
    "v_smfmac_f32_16x16x64_bf16 v[208:211], v[224:227], v[200:207], v212\n\t" \
    "ds_read_b128 v[200:203], v254 offset:34816\n\t" \
    "ds_read_b128 v[204:207], v254 offset:35840\n\t" \
    "ds_read_b128 v[224:227], v253 offset:48128\n\t" \
    "v_smfmac_f32_16x16x64_bf16 v[208:211], v[228:231], v[16:23], v212\n\t" \
    "v_smfmac_f32_16x16x64_bf16 v[244:247], v[228:231], v[24:31], v212\n\t" \
    "v_smfmac_f32_16x16x64_bf16 v[248:251], v[228:231], v[32:39], v212\n\t" \
    "ds_read_b128 v[228:231], v253 offset:44032\n\t" \
    "v_smfmac_f32_16x16x64_bf16 v[208:211], v[232:235], v[216:223], v212\n\t" \
    "ds_read_b128 v[216:219], v254 offset:36864\n\t" \
    "ds_read_b128 v[220:223], v254 offset:37888\n\t" \
    "v_smfmac_f32_16x16x64_bf16 v[244:247], v[232:235], v[40:47], v212\n\t" \
    "v_smfmac_f32_16x16x64_bf16 v[248:251], v[232:235], v[48:55], v212\n\t" \
    "ds_read_b128 v[232:235], v253 offset:49152\n\t" \
    "s_waitcnt lgkmcnt(8)\n\t" \
    "v_smfmac_f32_16x16x64_bf16 v[208:211], v[236:239], v[56:63], v212\n\t" \
    "v_smfmac_f32_16x16x64_bf16 v[244:247], v[236:239], v[64:71], v212\n\t" \
    "v_smfmac_f32_16x16x64_bf16 v[248:251], v[236:239], v[72:79], v212\n\t" \
    "ds_read_b128 v[236:239], v253 offset:45056\n\t" \
    "s_waitcnt lgkmcnt(6)\n\t" \
    "v_smfmac_f32_16x16x64_bf16 v[208:211], v[240:243], v[200:207], v212\n\t" \
    "ds_read_b128 v[200:203], v254 offset:38912\n\t" \
    "ds_read_b128 v[204:207], v254 offset:39936\n\t" \
    "v_smfmac_f32_16x16x64_bf16 v[244:247], v[240:243], v[80:87], v212\n\t" \
    "v_smfmac_f32_16x16x64_bf16 v[248:251], v[240:243], v[88:95], v212\n\t" \
    "ds_read_b128 v[240:243], v253 offset:50176\n\t" \
    "s_waitcnt lgkmcnt(8)\n\t" \
    "v_smfmac_f32_16x16x64_bf16 v[208:211], v[224:227], v[96:103], v212\n\t" \
    "v_smfmac_f32_16x16x64_bf16 v[244:247], v[224:227], v[104:111], v212\n\t" \
    "v_smfmac_f32_16x16x64_bf16 v[248:251], v[224:227], v[112:119], v212\n\t" \
    "s_waitcnt lgkmcnt(5)\n\t" \
    "v_smfmac_f32_16x16x64_bf16 v[208:211], v[228:231], v[216:223], v212\n\t" \
    "s_waitcnt lgkmcnt(4)\n\t" \
    "v_smfmac_f32_16x16x64_bf16 v[208:211], v[232:235], v[136:143], v212\n\t" \
    "s_waitcnt lgkmcnt(1)\n\t" \
    "v_smfmac_f32_16x16x64_bf16 v[208:211], v[236:239], v[200:207], v212\n\t" \
    "s_waitcnt lgkmcnt(0)\n\t" \
    "v_smfmac_f32_16x16x64_bf16 v[208:211], v[240:243], v[176:183], v212\n\t" \
    "v_smfmac_f32_16x16x64_bf16 v[244:247], v[228:231], v[120:127], v212\n\t" \
    "v_smfmac_f32_16x16x64_bf16 v[244:247], v[232:235], v[144:151], v212\n\t" \
    "v_smfmac_f32_16x16x64_bf16 v[244:247], v[236:239], v[160:167], v212\n\t" \
    "v_add_f32 v208, v208, v210\n\t" \
    "v_add_f32 v209, v209, v211\n\t" \
    "v_cvt_pk_bf16_f32 v208, v208, v209\n\t" \
    "v_smfmac_f32_16x16x64_bf16 v[244:247], v[240:243], v[184:191], v212\n\t" \
    "ds_write_b16 v213, v208 offset:0\n\t" \
    "ds_write_b16_d16_hi v213, v208 offset:16\n\t" \
    "v_smfmac_f32_16x16x64_bf16 v[248:251], v[228:231], v[128:135], v212\n\t" \
    "v_smfmac_f32_16x16x64_bf16 v[248:251], v[232:235], v[152:159], v212\n\t" \
    "v_smfmac_f32_16x16x64_bf16 v[248:251], v[236:239], v[168:175], v212\n\t" \
    "v_add_f32 v244, v244, v246\n\t" \
    "v_add_f32 v245, v245, v247\n\t" \
    "v_smfmac_f32_16x16x64_bf16 v[248:251], v[240:243], v[192:199], v212\n\t" \
    "s_nop 7\n\t" \
    "v_add_f32 v248, v248, v250\n\t" \
    "v_add_f32 v249, v249, v251\n\t" \
    "v_cvt_pk_bf16_f32 v244, v244, v248\n\t" \
    "v_cvt_pk_bf16_f32 v245, v245, v249\n\t" \
    "ds_write2_b32 v214, v244, v245 offset1:4\n\t" \
    "s_setprio 0\n\t" \
    "s_sleep 3\n\t" \
    "ds_read_b128 v[200:203], v254 offset:18432\n\t" \
    "ds_read_b128 v[204:207], v254 offset:19456\n\t" \
    "ds_read_b128 v[214:217], v254 offset:24576\n\t" \
    "ds_read_b128 v[218:221], v254 offset:25600\n\t" \
    "ds_read_b128 v[222:225], v254 offset:20480\n\t" \
    "ds_read_b128 v[226:229], v254 offset:21504\n\t" \
    "ds_read_b128 v[230:233], v254 offset:26624\n\t" \
    "ds_read_b128 v[234:237], v254 offset:27648\n\t" \
    "v_and_b32 v213, 0xffffff7f, v254\n\t" \
    "v_mov_b64 v[242:243], 0\n\t" \
    "v_mov_b64 v[244:245], 0\n\t" \
    "v_mov_b64 v[246:247], 0\n\t" \
    "v_mov_b64 v[248:249], 0\n\t" \
    "v_mov_b64 v[250:251], 0\n\t" \
    "v_mov_b64 v[252:253], 0\n\t" \
    "s_and_b32 s44, s40, 3\n\t" \
    "s_lshl_b32 s44, s44, 22\n\t" \
    "s_add_u32 s50, s44, %[pb]\n\t" \
    "s_xor_b32 s51, s50, 0x4000\n\t" \
    "s_xor_b32 s52, s50, 0x1000\n\t" \
    "s_xor_b32 s53, s50, 0x5000\n\t" \
    "s_setprio 3\n\t" \
    "s_waitcnt lgkmcnt(0)\n\t" \
    "s_barrier \n\t" \
    LST4 \
    LJIT \
    "ds_read_b128 v[208:211], v254 offset:0\n\t" \
    "ds_read_b128 v[238:241], v254 offset:1024\n\t" \
    "s_waitcnt lgkmcnt(1)\n\t" \
    "v_smfmac_f32_16x16x64_bf16 v[242:245], v[208:211], v[200:207], v212\n\t" \
    "ds_read_b128 v[200:203], v213 offset:3072\n\t" \
    "ds_read_b128 v[204:207], v213 offset:3200\n\t" \
    "v_smfmac_f32_16x16x64_bf16 v[246:249], v[208:211], v[214:221], v212\n\t" \
    "ds_read_b128 v[214:217], v213 offset:4096\n\t" \
    "ds_read_b128 v[218:221], v213 offset:4224\n\t" \
    "s_waitcnt lgkmcnt(4)\n\t" \
    "v_smfmac_f32_16x16x64_bf16 v[242:245], v[238:241], v[222:229], v212\n\t" \
    "ds_read_b128 v[222:225], v254 offset:22528\n\t" \
    "ds_read_b128 v[226:229], v254 offset:23552\n\t" \
    "v_smfmac_f32_16x16x64_bf16 v[246:249], v[238:241], v[230:237], v212\n\t" \
    "ds_read_b128 v[230:233], v254 offset:28672\n\t" \
    "ds_read_b128 v[234:237], v254 offset:29696\n\t" \
    "s_waitcnt lgkmcnt(6)\n\t" \
    "v_smfmac_f32_16x16x64_bf16 v[250:253], v[208:211], v[200:207], v212\n\t" \
    "ds_read_b128 v[208:211], v254 offset:2048\n\t" \
    "ds_read_b128 v[200:203], v213 offset:5120\n\t" \
    "ds_read_b128 v[204:207], v213 offset:5248\n\t" \
    "s_waitcnt lgkmcnt(7)\n\t" \
    "v_smfmac_f32_16x16x64_bf16 v[250:253], v[238:241], v[214:221], v212\n\t" \
    "v_lshrrev_b32 v238, 3, v254\n\t" \
    "v_and_b32 v238, 0x6e, v238\n\t" \
    "v_lshrrev_b32 v239, 16, v255\n\t" \
    "s_waitcnt lgkmcnt(2)\n\t" \
    "v_smfmac_f32_16x16x64_bf16 v[242:245], v[208:211], v[222:229], v212\n\t" \
    "v_smfmac_f32_16x16x64_bf16 v[246:249], v[208:211], v[230:237], v212\n\t" \
    "s_waitcnt lgkmcnt(0)\n\t" \
    "v_smfmac_f32_16x16x64_bf16 v[250:253], v[208:211], v[200:207], v212\n\t" \
    "s_nop 1\n\t" \
    "v_add_f32 v242, v242, v244\n\t" \
    "v_add_f32 v243, v243, v245\n\t" \
    "s_nop 0\n\t" \
    "v_add_f32 v246, v246, v248\n\t" \
    "v_add_f32 v247, v247, v249\n\t" \
    "s_nop 0\n\t" \
    "v_add_f32 v250, v250, v252\n\t" \
    "v_add_f32 v251, v251, v253\n\t" \
    "v_max3_f32 v244, v242, v246, v250\n\t" \
    "v_max3_f32 v245, v243, v247, v251\n\t" \
    "s_nop 0\n\t" \
    "v_max_f32_dpp v244, v244, v244 quad_perm:[1,0,3,2] row_mask:0xf bank_mask:0xf\n\t" \
    "v_max_f32_dpp v245, v245, v245 quad_perm:[1,0,3,2] row_mask:0xf bank_mask:0xf\n\t" \
    "s_nop 0\n\t" \
    "v_max_f32_dpp v244, v244, v244 quad_perm:[2,3,0,1] row_mask:0xf bank_mask:0xf\n\t" \
    "v_max_f32_dpp v245, v245, v245 quad_perm:[2,3,0,1] row_mask:0xf bank_mask:0xf\n\t" \
    "s_nop 0\n\t" \
    "v_max_f32_dpp v244, v244, v244 row_half_mirror row_mask:0xf bank_mask:0xf\n\t" \
    "v_max_f32_dpp v245, v245, v245 row_half_mirror row_mask:0xf bank_mask:0xf\n\t" \
    "s_nop 0\n\t" \
    "v_max_f32_dpp v244, v244, v244 row_mirror row_mask:0xf bank_mask:0xf\n\t" \
    "v_max_f32_dpp v245, v245, v245 row_mirror row_mask:0xf bank_mask:0xf\n\t" \
    "v_mul_f32 v244, %[scl], v244\n\t" \
    "v_mul_f32 v245, %[scl], v245\n\t" \
    "v_fma_f32 v242, v242, %[scl], -v244\n\t" \
    "v_fma_f32 v243, v243, %[scl], -v245\n\t" \
    "v_fma_f32 v246, v246, %[scl], -v244\n\t" \
    "v_fma_f32 v247, v247, %[scl], -v245\n\t" \
    "v_exp_f32 v242, v242\n\t" \
    "v_exp_f32 v243, v243\n\t" \
    "v_fma_f32 v250, v250, %[scl], -v244\n\t" \
    "v_fma_f32 v251, v251, %[scl], -v245\n\t" \
    "v_exp_f32 v246, v246\n\t" \
    "v_exp_f32 v247, v247\n\t" \
    "v_exp_f32 v250, v250\n\t" \
    "v_exp_f32 v251, v251\n\t" \
    "s_wakeup\n\t" \
    "v_add_f32 v244, v242, v246\n\t" \
    "v_add_f32 v245, v243, v247\n\t" \
    "v_fma_f32 v244, v250, 0.5, v244\n\t" \
    "v_fma_f32 v245, v251, 0.5, v245\n\t" \
    "s_nop 0\n\t" \
    "v_add_f32_dpp v244, v244, v244 quad_perm:[1,0,3,2] row_mask:0xf bank_mask:0xf\n\t" \
    "v_add_f32_dpp v245, v245, v245 quad_perm:[1,0,3,2] row_mask:0xf bank_mask:0xf\n\t" \
    "s_nop 0\n\t" \
    "v_add_f32_dpp v244, v244, v244 quad_perm:[2,3,0,1] row_mask:0xf bank_mask:0xf\n\t" \
    "v_add_f32_dpp v245, v245, v245 quad_perm:[2,3,0,1] row_mask:0xf bank_mask:0xf\n\t" \
    "s_nop 0\n\t" \
    "v_add_f32_dpp v244, v244, v244 row_half_mirror row_mask:0xf bank_mask:0xf\n\t" \
    "v_add_f32_dpp v245, v245, v245 row_half_mirror row_mask:0xf bank_mask:0xf\n\t" \
    "s_nop 0\n\t" \
    "v_add_f32_dpp v244, v244, v244 row_mirror row_mask:0xf bank_mask:0xf\n\t" \
    "v_add_f32_dpp v245, v245, v245 row_mirror row_mask:0xf bank_mask:0xf\n\t" \
    "v_rcp_f32 v244, v244\n\t" \
    "v_rcp_f32 v245, v245\n\t" \
    "v_mul_f32 v250, v250, v244\n\t" \
    "v_mul_f32 v251, v251, v245\n\t" \
    "v_mul_f32 v242, v242, v244\n\t" \
    "v_mul_f32 v243, v243, v245\n\t" \
    "v_mul_f32 v246, v246, v244\n\t" \
    "v_mul_f32 v247, v247, v245\n\t" \
    "v_cvt_pk_bf16_f32 v250, |v250|, |v251|\n\t" \
    "ds_write_b16 v238, v250 offset:52096\n\t" \
    "ds_write_b16_d16_hi v238, v250 offset:52112\n\t" \
    "v_cvt_pk_bf16_f32 v242, |v242|, |v246|\n\t" \
    "v_cvt_pk_bf16_f32 v243, |v243|, |v247|\n\t" \
    "ds_write2_b32 v239, v242, v243 offset1:4\n\t" \
    "ds_read_b128 v[200:203], v254 offset:896\n\t" \
    "v_and_b32 v253, 0x3ff, v254\n\t" \
    "s_mov_b32 exec_hi, 0xff\n\t" \
    "s_waitcnt lgkmcnt(0)\n\t" \
    "buffer_store_dwordx4 v[200:203], v253, s[60:63], s50 offen sc1\n\t" \
    "buffer_store_dwordx4 v[200:203], v253, s[60:63], s51 offen sc1\n\t" \
    "buffer_store_dwordx4 v[200:203], v253, s[60:63], s52 offen sc1\n\t" \
    "buffer_store_dwordx4 v[200:203], v253, s[60:63], s53 offen sc1\n\t" \
    "s_mov_b32 exec_hi, -1\n\t" \
    "s_setprio 0\n\t" \
    LST5 \
    LJIT \
    "ds_read_b128 v[224:227], v253 offset:0\n\t" \
    "ds_read_b128 v[228:231], v253 offset:20480\n\t" \
    "ds_read_b128 v[232:235], v253 offset:1024\n\t" \
    "ds_read_b128 v[236:239], v253 offset:21504\n\t" \
    "s_add_u32 s40, s40, 1\n\t" \
    "s_cmp_lg_u32 s40, 64\n\t" \
    "s_cbranch_scc1 .Ls03_%=\n\t" \
    "s_branch .Lend%=\n\t" \
    ".Lend%=:\n\t" \
    "s_waitcnt vmcnt(0) lgkmcnt(0)\n\t" \
    "s_mov_b32 %[bud], s41\n\t"
#define ASM_V_LOOP \
    "s_mov_b64 s[60:61], %[ws]\n\t" \
    "s_mov_b32 s62, %[sz]\n\t" \
    "s_mov_b32 s63, 0x20000\n\t" \
    "s_mov_b32 s40, 0\n\t" \
    "s_mov_b32 s41, 0x20000\n\t" \
    LSTINIT \
    "s_cmp_eq_u32 %[wave], 0\n\t" \
    "s_cbranch_scc1 .Lv0_%=\n\t" \
    "s_cmp_eq_u32 %[wave], 1\n\t" \
    "s_cbranch_scc1 .Lv1_%=\n\t" \
    "s_cmp_eq_u32 %[wave], 2\n\t" \
    "s_cbranch_scc1 .Lv2_%=\n\t" \
    "s_cmp_eq_u32 %[wave], 3\n\t" \
    "s_cbranch_scc1 .Lv3_%=\n\t" \
    ".Lv4_%=:\n\t" \
    "s_barrier \n\t" \
    "s_waitcnt vmcnt(15)\n\t" \
    "ds_read_b64_tr_b16 v[200:201], v245 offset:0\n\t" \
    "ds_read_b64_tr_b16 v[16:17], v245 offset:32\n\t" \
    "ds_read_b64_tr_b16 v[208:209], v245 offset:64\n\t" \
    "ds_read_b64_tr_b16 v[56:57], v245 offset:96\n\t" \
    "ds_read_b64_tr_b16 v[216:217], v245 offset:128\n\t" \
    "ds_read_b64_tr_b16 v[96:97], v245 offset:160\n\t" \
    "ds_read_b64_tr_b16 v[224:225], v245 offset:192\n\t" \
    "ds_read_b64_tr_b16 v[136:137], v245 offset:224\n\t" \
    "ds_read_b64_tr_b16 v[232:233], v245 offset:256\n\t" \
    "ds_read_b64_tr_b16 v[176:177], v245 offset:288\n\t" \
    "s_waitcnt lgkmcnt(0)\n\t" \
    "s_add_u32 m0, %[wm], 0x0\n\t" \
    "s_mov_b32 s44, 0x14000\n\t" \
    "buffer_load_dwordx4 v240, %[wr], s44 offen offset:0 nt lds\n\t" \
    "buffer_load_dwordx4 v241, %[wr], s44 offen offset:1024 nt lds\n\t" \
    "buffer_load_dwordx4 v242, %[wr], s44 offen offset:2048 nt lds\n\t" \
    "buffer_load_dwordx4 v243, %[wr], s44 offen offset:3072 nt lds\n\t" \
    "s_add_u32 m0, %[wm], 0x1000\n\t" \
    "s_nop 0\n\t" \
    "buffer_load_dwordx4 v244, %[wr], s44 offen nt lds\n\t" \
    "s_barrier \n\t" \
    "s_barrier \n\t" \
    "s_waitcnt vmcnt(15)\n\t" \
    "ds_read_b64_tr_b16 v[204:205], v245 offset:5120\n\t" \
    "ds_read_b64_tr_b16 v[20:21], v245 offset:5152\n\t" \
    "ds_read_b64_tr_b16 v[212:213], v245 offset:5184\n\t" \
    "ds_read_b64_tr_b16 v[60:61], v245 offset:5216\n\t" \
    "ds_read_b64_tr_b16 v[220:221], v245 offset:5248\n\t" \
    "ds_read_b64_tr_b16 v[100:101], v245 offset:5280\n\t" \
    "ds_read_b64_tr_b16 v[228:229], v245 offset:5312\n\t" \
    "ds_read_b64_tr_b16 v[140:141], v245 offset:5344\n\t" \
    "ds_read_b64_tr_b16 v[236:237], v245 offset:5376\n\t" \
    "ds_read_b64_tr_b16 v[180:181], v245 offset:5408\n\t" \
    "s_waitcnt lgkmcnt(0)\n\t" \
    "s_add_u32 m0, %[wm], 0x1400\n\t" \
    "s_mov_b32 s44, 0x19000\n\t" \
    "buffer_load_dwordx4 v240, %[wr], s44 offen offset:0 nt lds\n\t" \
    "buffer_load_dwordx4 v241, %[wr], s44 offen offset:1024 nt lds\n\t" \
    "buffer_load_dwordx4 v242, %[wr], s44 offen offset:2048 nt lds\n\t" \
    "buffer_load_dwordx4 v243, %[wr], s44 offen offset:3072 nt lds\n\t" \
    "s_add_u32 m0, %[wm], 0x2400\n\t" \
    "s_nop 0\n\t" \
    "buffer_load_dwordx4 v244, %[wr], s44 offen nt lds\n\t" \
    "s_barrier \n\t" \
    "s_barrier \n\t" \
    "s_waitcnt vmcnt(15)\n\t" \
    "ds_read_b64_tr_b16 v[0:1], v245 offset:10240\n\t" \
    "ds_read_b64_tr_b16 v[24:25], v245 offset:10272\n\t" \
    "ds_read_b64_tr_b16 v[40:41], v245 offset:10304\n\t" \
    "ds_read_b64_tr_b16 v[64:65], v245 offset:10336\n\t" \
    "ds_read_b64_tr_b16 v[80:81], v245 offset:10368\n\t" \
    "ds_read_b64_tr_b16 v[104:105], v245 offset:10400\n\t" \
    "ds_read_b64_tr_b16 v[120:121], v245 offset:10432\n\t" \
    "ds_read_b64_tr_b16 v[144:145], v245 offset:10464\n\t" \
    "ds_read_b64_tr_b16 v[160:161], v245 offset:10496\n\t" \
    "ds_read_b64_tr_b16 v[184:185], v245 offset:10528\n\t" \
    "s_waitcnt lgkmcnt(0)\n\t" \
    "s_add_u32 m0, %[wm], 0x2800\n\t" \
    "s_mov_b32 s44, 0x1e000\n\t" \
    "buffer_load_dwordx4 v240, %[wr], s44 offen offset:0 nt lds\n\t" \
    "buffer_load_dwordx4 v241, %[wr], s44 offen offset:1024 nt lds\n\t" \
    "buffer_load_dwordx4 v242, %[wr], s44 offen offset:2048 nt lds\n\t" \
    "buffer_load_dwordx4 v243, %[wr], s44 offen offset:3072 nt lds\n\t" \
    "s_add_u32 m0, %[wm], 0x3800\n\t" \
    "s_nop 0\n\t" \
    "buffer_load_dwordx4 v244, %[wr], s44 offen nt lds\n\t" \
    "s_barrier \n\t" \
    "s_waitcnt vmcnt(15)\n\t" \
    "ds_read_b64_tr_b16 v[4:5], v245 offset:15360\n\t" \
    "ds_read_b64_tr_b16 v[28:29], v245 offset:15392\n\t" \
    "ds_read_b64_tr_b16 v[44:45], v245 offset:15424\n\t" \
    "ds_read_b64_tr_b16 v[68:69], v245 offset:15456\n\t" \
    "ds_read_b64_tr_b16 v[84:85], v245 offset:15488\n\t" \
    "ds_read_b64_tr_b16 v[108:109], v245 offset:15520\n\t" \
    "ds_read_b64_tr_b16 v[124:125], v245 offset:15552\n\t" \
    "ds_read_b64_tr_b16 v[148:149], v245 offset:15584\n\t" \
    "ds_read_b64_tr_b16 v[164:165], v245 offset:15616\n\t" \
    "ds_read_b64_tr_b16 v[188:189], v245 offset:15648\n\t" \
    "s_waitcnt lgkmcnt(0)\n\t" \
    "s_add_u32 m0, %[wm], 0x3c00\n\t" \
    "s_mov_b32 s44, 0x23000\n\t" \
    "buffer_load_dwordx4 v240, %[wr], s44 offen offset:0 nt lds\n\t" \
    "buffer_load_dwordx4 v241, %[wr], s44 offen offset:1024 nt lds\n\t" \
    "buffer_load_dwordx4 v242, %[wr], s44 offen offset:2048 nt lds\n\t" \
    "buffer_load_dwordx4 v243, %[wr], s44 offen offset:3072 nt lds\n\t" \
    "s_add_u32 m0, %[wm], 0x4c00\n\t" \
    "s_nop 0\n\t" \
    "buffer_load_dwordx4 v244, %[wr], s44 offen nt lds\n\t" \
    "s_barrier \n\t" \
    "s_barrier \n\t" \
    "s_waitcnt vmcnt(15)\n\t" \
    "ds_read_b64_tr_b16 v[8:9], v245 offset:0\n\t" \
    "ds_read_b64_tr_b16 v[32:33], v245 offset:32\n\t" \
    "ds_read_b64_tr_b16 v[48:49], v245 offset:64\n\t" \
    "ds_read_b64_tr_b16 v[72:73], v245 offset:96\n\t" \
    "ds_read_b64_tr_b16 v[88:89], v245 offset:128\n\t" \
    "ds_read_b64_tr_b16 v[112:113], v245 offset:160\n\t" \
    "ds_read_b64_tr_b16 v[128:129], v245 offset:192\n\t" \
    "ds_read_b64_tr_b16 v[152:153], v245 offset:224\n\t" \
    "ds_read_b64_tr_b16 v[168:169], v245 offset:256\n\t" \
    "ds_read_b64_tr_b16 v[192:193], v245 offset:288\n\t" \
    "s_waitcnt lgkmcnt(0)\n\t" \
    "s_add_u32 m0, %[wm], 0x0\n\t" \
    "s_mov_b32 s44, 0x28000\n\t" \
    "buffer_load_dwordx4 v240, %[wr], s44 offen offset:0 nt lds\n\t" \
    "buffer_load_dwordx4 v241, %[wr], s44 offen offset:1024 nt lds\n\t" \
    "buffer_load_dwordx4 v242, %[wr], s44 offen offset:2048 nt lds\n\t" \
    "buffer_load_dwordx4 v243, %[wr], s44 offen offset:3072 nt lds\n\t" \
    "s_add_u32 m0, %[wm], 0x1000\n\t" \
    "s_nop 0\n\t" \
    "buffer_load_dwordx4 v244, %[wr], s44 offen nt lds\n\t" \
    "s_barrier \n\t" \
    "s_barrier \n\t" \
    "s_waitcnt vmcnt(15)\n\t" \
    "ds_read_b64_tr_b16 v[12:13], v245 offset:5120\n\t" \
    "ds_read_b64_tr_b16 v[36:37], v245 offset:5152\n\t" \
    "ds_read_b64_tr_b16 v[52:53], v245 offset:5184\n\t" \
    "ds_read_b64_tr_b16 v[76:77], v245 offset:5216\n\t" \
    "ds_read_b64_tr_b16 v[92:93], v245 offset:5248\n\t" \
    "ds_read_b64_tr_b16 v[116:117], v245 offset:5280\n\t" \
    "ds_read_b64_tr_b16 v[132:133], v245 offset:5312\n\t" \
    "ds_read_b64_tr_b16 v[156:157], v245 offset:5344\n\t" \
    "ds_read_b64_tr_b16 v[172:173], v245 offset:5376\n\t" \
    "ds_read_b64_tr_b16 v[196:197], v245 offset:5408\n\t" \
    "s_waitcnt lgkmcnt(0)\n\t" \
    "s_add_u32 m0, %[wm], 0x1400\n\t" \
    "s_mov_b32 s44, 0x2d000\n\t" \
    "buffer_load_dwordx4 v240, %[wr], s44 offen offset:0 nt lds\n\t" \
    "buffer_load_dwordx4 v241, %[wr], s44 offen offset:1024 nt lds\n\t" \
    "buffer_load_dwordx4 v242, %[wr], s44 offen offset:2048 nt lds\n\t" \
    "buffer_load_dwordx4 v243, %[wr], s44 offen offset:3072 nt lds\n\t" \
    "s_add_u32 m0, %[wm], 0x2400\n\t" \
    "s_nop 0\n\t" \
    "buffer_load_dwordx4 v244, %[wr], s44 offen nt lds\n\t" \
    "s_barrier \n\t" \
    "s_waitcnt vmcnt(15)\n\t" \
    "ds_read_b64_tr_b16 v[202:203], v245 offset:10240\n\t" \
    "ds_read_b64_tr_b16 v[18:19], v245 offset:10272\n\t" \
    "ds_read_b64_tr_b16 v[210:211], v245 offset:10304\n\t" \
    "ds_read_b64_tr_b16 v[58:59], v245 offset:10336\n\t" \
    "ds_read_b64_tr_b16 v[218:219], v245 offset:10368\n\t" \
    "ds_read_b64_tr_b16 v[98:99], v245 offset:10400\n\t" \
    "ds_read_b64_tr_b16 v[226:227], v245 offset:10432\n\t" \
    "ds_read_b64_tr_b16 v[138:139], v245 offset:10464\n\t" \
    "ds_read_b64_tr_b16 v[234:235], v245 offset:10496\n\t" \
    "ds_read_b64_tr_b16 v[178:179], v245 offset:10528\n\t" \
    "v_mov_b32 v246, 0\n\t" \
    "buffer_load_dword v246, v246, s[60:63], %[mo] offen sc1\n\t" \
    "s_waitcnt lgkmcnt(0)\n\t" \
    "s_add_u32 m0, %[wm], 0x2800\n\t" \
    "s_mov_b32 s44, 0x32000\n\t" \
    "buffer_load_dwordx4 v240, %[wr], s44 offen offset:0 nt lds\n\t" \
    "buffer_load_dwordx4 v241, %[wr], s44 offen offset:1024 nt lds\n\t" \
    "buffer_load_dwordx4 v242, %[wr], s44 offen offset:2048 nt lds\n\t" \
    "buffer_load_dwordx4 v243, %[wr], s44 offen offset:3072 nt lds\n\t" \
    "s_add_u32 m0, %[wm], 0x3800\n\t" \
    "s_nop 0\n\t" \
    "buffer_load_dwordx4 v244, %[wr], s44 offen nt lds\n\t" \
    "s_barrier \n\t" \
    "s_barrier \n\t" \
    "s_waitcnt vmcnt(15)\n\t" \
    "ds_read_b64_tr_b16 v[206:207], v245 offset:15360\n\t" \
    "ds_read_b64_tr_b16 v[22:23], v245 offset:15392\n\t" \
    "ds_read_b64_tr_b16 v[214:215], v245 offset:15424\n\t" \
    "ds_read_b64_tr_b16 v[62:63], v245 offset:15456\n\t" \
    "ds_read_b64_tr_b16 v[222:223], v245 offset:15488\n\t" \
    "ds_read_b64_tr_b16 v[102:103], v245 offset:15520\n\t" \
    "ds_read_b64_tr_b16 v[230:231], v245 offset:15552\n\t" \
    "ds_read_b64_tr_b16 v[142:143], v245 offset:15584\n\t" \
    "ds_read_b64_tr_b16 v[238:239], v245 offset:15616\n\t" \
    "ds_read_b64_tr_b16 v[182:183], v245 offset:15648\n\t" \
    "s_waitcnt lgkmcnt(0)\n\t" \
    "s_add_u32 m0, %[wm], 0x3c00\n\t" \
    "s_mov_b32 s44, 0x37000\n\t" \
    "buffer_load_dwordx4 v240, %[wr], s44 offen offset:0 nt lds\n\t" \
    "buffer_load_dwordx4 v241, %[wr], s44 offen offset:1024 nt lds\n\t" \
    "buffer_load_dwordx4 v242, %[wr], s44 offen offset:2048 nt lds\n\t" \
    "buffer_load_dwordx4 v243, %[wr], s44 offen offset:3072 nt lds\n\t" \
    "s_add_u32 m0, %[wm], 0x4c00\n\t" \
    "s_nop 0\n\t" \
    "buffer_load_dwordx4 v244, %[wr], s44 offen nt lds\n\t" \
    "s_barrier \n\t" \
    "s_barrier \n\t" \
    "s_waitcnt vmcnt(15)\n\t" \
    "ds_read_b64_tr_b16 v[2:3], v245 offset:0\n\t" \
    "ds_read_b64_tr_b16 v[26:27], v245 offset:32\n\t" \
    "ds_read_b64_tr_b16 v[42:43], v245 offset:64\n\t" \
    "ds_read_b64_tr_b16 v[66:67], v245 offset:96\n\t" \
    "ds_read_b64_tr_b16 v[82:83], v245 offset:128\n\t" \
    "ds_read_b64_tr_b16 v[106:107], v245 offset:160\n\t" \
    "ds_read_b64_tr_b16 v[122:123], v245 offset:192\n\t" \
    "ds_read_b64_tr_b16 v[146:147], v245 offset:224\n\t" \
    "ds_read_b64_tr_b16 v[162:163], v245 offset:256\n\t" \
    "ds_read_b64_tr_b16 v[186:187], v245 offset:288\n\t" \
    "s_barrier \n\t" \
    "s_waitcnt vmcnt(10)\n\t" \
    "ds_read_b64_tr_b16 v[6:7], v245 offset:5120\n\t" \
    "ds_read_b64_tr_b16 v[30:31], v245 offset:5152\n\t" \
    "ds_read_b64_tr_b16 v[46:47], v245 offset:5184\n\t" \
    "ds_read_b64_tr_b16 v[70:71], v245 offset:5216\n\t" \
    "ds_read_b64_tr_b16 v[86:87], v245 offset:5248\n\t" \
    "ds_read_b64_tr_b16 v[110:111], v245 offset:5280\n\t" \
    "ds_read_b64_tr_b16 v[126:127], v245 offset:5312\n\t" \
    "ds_read_b64_tr_b16 v[150:151], v245 offset:5344\n\t" \
    "ds_read_b64_tr_b16 v[166:167], v245 offset:5376\n\t" \
    "ds_read_b64_tr_b16 v[190:191], v245 offset:5408\n\t" \
    "s_barrier \n\t" \
    "s_barrier \n\t" \
    "s_waitcnt vmcnt(5)\n\t" \
    "ds_read_b64_tr_b16 v[10:11], v245 offset:10240\n\t" \
    "ds_read_b64_tr_b16 v[34:35], v245 offset:10272\n\t" \
    "ds_read_b64_tr_b16 v[50:51], v245 offset:10304\n\t" \
    "ds_read_b64_tr_b16 v[74:75], v245 offset:10336\n\t" \
    "ds_read_b64_tr_b16 v[90:91], v245 offset:10368\n\t" \
    "ds_read_b64_tr_b16 v[114:115], v245 offset:10400\n\t" \
    "ds_read_b64_tr_b16 v[130:131], v245 offset:10432\n\t" \
    "ds_read_b64_tr_b16 v[154:155], v245 offset:10464\n\t" \
    "ds_read_b64_tr_b16 v[170:171], v245 offset:10496\n\t" \
    "ds_read_b64_tr_b16 v[194:195], v245 offset:10528\n\t" \
    ".Lv41_%=:\n\t" \
    "v_readfirstlane_b32 s43, v246\n\t" \
    "s_nop 0\n\t" \
    "s_cmp_lg_u32 s43, -1\n\t" \
    "s_cbranch_scc1 .Lv42_%=\n\t" \
    "s_cmp_eq_u32 s41, 0\n\t" \
    "s_cbranch_scc1 .Lv42_%=\n\t" \
    "s_sub_u32 s41, s41, 1\n\t" \
    "s_sleep 4\n\t" \
    "v_mov_b32 v246, 0\n\t" \
    "buffer_load_dword v246, v246, s[60:63], %[mo] offen sc1\n\t" \
    "s_waitcnt vmcnt(0)\n\t" \
    "s_branch .Lv41_%=\n\t" \
    ".Lv42_%=:\n\t" \
    "v_mov_b32 v246, 0x8000\n\t" \
    "s_mov_b64 exec, 1\n\t" \
    "buffer_store_dword v246, v246, s[60:63], %[mo] offen sc1\n\t" \
    "s_mov_b64 exec, -1\n\t" \
    "s_and_b32 s44, s43, 0x5000\n\t" \
    "s_xor_b32 %[mb], %[mb], s44\n\t" \
    "s_xor_b32 %[fl], %[fl], s44\n\t" \
    "s_sub_u32 %[fl], %[fl], 0xc800\n\t" \
    "s_xor_b32 %[pb], %[pb], s44\n\t" \
    "s_and_b32 s43, s43, 0x2000\n\t" \
    "s_xor_b32 s43, s43, %[lx]\n\t" \
    "s_add_u32 s60, s60, s43\n\t" \
    "s_addc_u32 s61, s61, 0\n\t" \
    "s_barrier \n\t" \
    "s_barrier \n\t" \
    "s_waitcnt vmcnt(0)\n\t" \
    "ds_read_b64_tr_b16 v[14:15], v245 offset:15360\n\t" \
    "ds_read_b64_tr_b16 v[38:39], v245 offset:15392\n\t" \
    "ds_read_b64_tr_b16 v[54:55], v245 offset:15424\n\t" \
    "ds_read_b64_tr_b16 v[78:79], v245 offset:15456\n\t" \
    "ds_read_b64_tr_b16 v[94:95], v245 offset:15488\n\t" \
    "ds_read_b64_tr_b16 v[118:119], v245 offset:15520\n\t" \
    "ds_read_b64_tr_b16 v[134:135], v245 offset:15552\n\t" \
    "ds_read_b64_tr_b16 v[158:159], v245 offset:15584\n\t" \
    "ds_read_b64_tr_b16 v[174:175], v245 offset:15616\n\t" \
    "ds_read_b64_tr_b16 v[198:199], v245 offset:15648\n\t" \
    "s_waitcnt lgkmcnt(0)\n\t" \
    "s_barrier \n\t" \
    LST0 \
    LJIT \
    "ds_write_b128 v254, v[200:203] offset:0\n\t" \
    "ds_write_b128 v254, v[204:207] offset:1024\n\t" \
    "ds_write_b128 v254, v[208:211] offset:2048\n\t" \
    "ds_write_b128 v254, v[212:215] offset:3072\n\t" \
    "ds_write_b128 v254, v[216:219] offset:4096\n\t" \
    "ds_write_b128 v254, v[220:223] offset:5120\n\t" \
    "ds_write_b128 v254, v[224:227] offset:6144\n\t" \
    "ds_write_b128 v254, v[228:231] offset:7168\n\t" \
    "ds_write_b128 v254, v[232:235] offset:8192\n\t" \
    "ds_write_b128 v254, v[236:239] offset:9216\n\t" \
    "s_waitcnt vmcnt(0) lgkmcnt(0)\n\t" \
    "s_barrier \n\t" \
    "v_mov_b64 v[200:201], 0\n\t" \
    "v_mov_b64 v[202:203], 0\n\t" \
    "ds_write_b128 v253, v[200:203] offset:3072\n\t" \
    "ds_write_b128 v253, v[200:203] offset:4096\n\t" \
    "v_bfe_i32 v252, v253, 5, 1\n\t" \
    "v_and_b32 v252, 0xaaaa, v252\n\t" \
    "v_or_b32 v252, 0x4444, v252\n\t" \
    ".p2align 8\n\t" \
    ".Lv43_%=:\n\t" \
    "ds_read_b128 v[248:251], v254 offset:0\n\t" \
    "s_and_b32 s44, s40, 3\n\t" \
    "s_lshl_b32 s44, s44, 22\n\t" \
    "s_add_u32 s54, s44, %[mb]\n\t" \
    "s_xor_b32 s55, s54, 0x4000\n\t" \
    "s_xor_b32 s56, s54, 0x1000\n\t" \
    "s_xor_b32 s57, s54, 0x5000\n\t" \
    "s_waitcnt lgkmcnt(0)\n\t" \
    "s_barrier \n\t" \
    "ds_read_b128 v[224:227], v253 offset:6144\n\t" \
    "ds_read_b128 v[232:235], v253 offset:8192\n\t" \
    "ds_read_b128 v[236:239], v253 offset:9216\n\t" \
    "ds_read_b128 v[240:243], v253 offset:10240\n\t" \
    "ds_read_b128 v[244:247], v253 offset:11264\n\t" \
    "ds_read_b128 v[228:231], v253 offset:7168\n\t" \
    "s_waitcnt lgkmcnt(5)\n\t" \
    "v_mfma_f32_16x16x32_bf16 v[200:203], v[224:227], v[16:19], 0\n\t" \
    "s_waitcnt lgkmcnt(4)\n\t" \
    "v_mfma_f32_16x16x32_bf16 v[200:203], v[232:235], v[24:27], v[200:203]\n\t" \
    "s_waitcnt lgkmcnt(3)\n\t" \
    "v_mfma_f32_16x16x32_bf16 v[200:203], v[236:239], v[28:31], v[200:203]\n\t" \
    "s_waitcnt lgkmcnt(2)\n\t" \
    "v_mfma_f32_16x16x32_bf16 v[200:203], v[240:243], v[32:35], v[200:203]\n\t" \
    "s_waitcnt lgkmcnt(1)\n\t" \
    "v_mfma_f32_16x16x32_bf16 v[200:203], v[244:247], v[36:39], v[200:203]\n\t" \
    "s_waitcnt lgkmcnt(0)\n\t" \
    "v_mfma_f32_16x16x32_bf16 v[200:203], v[228:231], v[20:23], v[200:203]\n\t" \
    "v_mfma_f32_16x16x32_bf16 v[204:207], v[224:227], v[248:251], 0\n\t" \
    "ds_read_b128 v[248:251], v254 offset:1024\n\t" \
    "v_mfma_f32_16x16x32_bf16 v[204:207], v[232:235], v[0:3], v[204:207]\n\t" \
    "v_mfma_f32_16x16x32_bf16 v[204:207], v[236:239], v[4:7], v[204:207]\n\t" \
    "v_mfma_f32_16x16x32_bf16 v[204:207], v[240:243], v[8:11], v[204:207]\n\t" \
    "v_mfma_f32_16x16x32_bf16 v[204:207], v[244:247], v[12:15], v[204:207]\n\t" \
    "s_waitcnt lgkmcnt(0)\n\t" \
    "v_mfma_f32_16x16x32_bf16 v[204:207], v[228:231], v[248:251], v[204:207]\n\t" \
    "ds_read_b128 v[248:251], v254 offset:2048\n\t" \
    "s_barrier \n\t" \
    LST7 \
    LJIT \
    "v_mfma_f32_16x16x32_bf16 v[208:211], v[224:227], v[56:59], 0\n\t" \
    "v_mfma_f32_16x16x32_bf16 v[208:211], v[232:235], v[64:67], v[208:211]\n\t" \
    "v_mfma_f32_16x16x32_bf16 v[208:211], v[236:239], v[68:71], v[208:211]\n\t" \
    "v_mfma_f32_16x16x32_bf16 v[208:211], v[240:243], v[72:75], v[208:211]\n\t" \
    "v_mfma_f32_16x16x32_bf16 v[208:211], v[244:247], v[76:79], v[208:211]\n\t" \
    "v_mfma_f32_16x16x32_bf16 v[208:211], v[228:231], v[60:63], v[208:211]\n\t" \
    "s_waitcnt lgkmcnt(0)\n\t" \
    "v_mfma_f32_16x16x32_bf16 v[212:215], v[224:227], v[248:251], 0\n\t" \
    "ds_read_b128 v[248:251], v254 offset:3072\n\t" \
    "ds_read_b128 v[224:227], v253 offset:12288\n\t" \
    "v_mfma_f32_16x16x32_bf16 v[212:215], v[232:235], v[40:43], v[212:215]\n\t" \
    "ds_read_b128 v[232:235], v253 offset:14336\n\t" \
    "v_mfma_f32_16x16x32_bf16 v[212:215], v[236:239], v[44:47], v[212:215]\n\t" \
    "ds_read_b128 v[236:239], v253 offset:15360\n\t" \
    "v_mfma_f32_16x16x32_bf16 v[212:215], v[240:243], v[48:51], v[212:215]\n\t" \
    "ds_read_b128 v[240:243], v253 offset:16384\n\t" \
    "v_mfma_f32_16x16x32_bf16 v[212:215], v[244:247], v[52:55], v[212:215]\n\t" \
    "ds_read_b128 v[244:247], v253 offset:17408\n\t" \
    "s_waitcnt lgkmcnt(5)\n\t" \
    "v_mfma_f32_16x16x32_bf16 v[212:215], v[228:231], v[248:251], v[212:215]\n\t" \
    "ds_read_b128 v[248:251], v254 offset:4096\n\t" \
    "ds_read_b128 v[228:231], v253 offset:13312\n\t" \
    "s_waitcnt lgkmcnt(6)\n\t" \
    "v_mfma_f32_16x16x32_bf16 v[216:219], v[224:227], v[16:19], 0\n\t" \
    "s_waitcnt lgkmcnt(5)\n\t" \
    "v_mfma_f32_16x16x32_bf16 v[216:219], v[232:235], v[24:27], v[216:219]\n\t" \
    "s_waitcnt lgkmcnt(4)\n\t" \
    "v_mfma_f32_16x16x32_bf16 v[216:219], v[236:239], v[28:31], v[216:219]\n\t" \
    "s_waitcnt lgkmcnt(3)\n\t" \
    "v_mfma_f32_16x16x32_bf16 v[216:219], v[240:243], v[32:35], v[216:219]\n\t" \
    "s_waitcnt lgkmcnt(2)\n\t" \
    "v_mfma_f32_16x16x32_bf16 v[216:219], v[244:247], v[36:39], v[216:219]\n\t" \
    "s_waitcnt lgkmcnt(0)\n\t" \
    "v_mfma_f32_16x16x32_bf16 v[216:219], v[228:231], v[20:23], v[216:219]\n\t" \
    "v_mfma_f32_16x16x32_bf16 v[220:223], v[224:227], v[248:251], 0\n\t" \
    "ds_read_b128 v[248:251], v254 offset:5120\n\t" \
    "v_mfma_f32_16x16x32_bf16 v[220:223], v[232:235], v[80:83], v[220:223]\n\t" \
    "v_mfma_f32_16x16x32_bf16 v[220:223], v[236:239], v[84:87], v[220:223]\n\t" \
    "v_mfma_f32_16x16x32_bf16 v[220:223], v[240:243], v[88:91], v[220:223]\n\t" \
    "v_cvt_pk_bf16_f32 v200, v200, v216\n\t" \
    "v_cvt_pk_bf16_f32 v201, v201, v217\n\t" \
    "v_mfma_f32_16x16x32_bf16 v[220:223], v[244:247], v[92:95], v[220:223]\n\t" \
    "v_cvt_pk_bf16_f32 v202, v202, v218\n\t" \
    "v_cvt_pk_bf16_f32 v203, v203, v219\n\t" \
    "s_waitcnt lgkmcnt(0)\n\t" \
    "v_mfma_f32_16x16x32_bf16 v[220:223], v[228:231], v[248:251], v[220:223]\n\t" \
    "ds_read_b128 v[248:251], v254 offset:0\n\t" \
    "s_add_u32 s44, s54, 0x1033800\n\t" \
    "v_mfma_f32_16x16x32_bf16 v[216:219], v[224:227], v[56:59], 0\n\t" \
    "buffer_store_dwordx4 v[200:203], v253, s[60:63], s44 offen sc1\n\t" \
    "v_mfma_f32_16x16x32_bf16 v[216:219], v[232:235], v[64:67], v[216:219]\n\t" \
    "v_mfma_f32_16x16x32_bf16 v[216:219], v[236:239], v[68:71], v[216:219]\n\t" \
    "v_mfma_f32_16x16x32_bf16 v[216:219], v[240:243], v[72:75], v[216:219]\n\t" \
    "v_mfma_f32_16x16x32_bf16 v[216:219], v[244:247], v[76:79], v[216:219]\n\t" \
    "v_mfma_f32_16x16x32_bf16 v[216:219], v[228:231], v[60:63], v[216:219]\n\t" \
    "s_waitcnt lgkmcnt(0)\n\t" \
    "v_mfma_f32_16x16x32_bf16 v[200:203], v[224:227], v[248:251], 0\n\t" \
    "ds_read_b128 v[248:251], v254 offset:1024\n\t" \
    "v_mfma_f32_16x16x32_bf16 v[200:203], v[232:235], v[0:3], v[200:203]\n\t" \
    "v_mfma_f32_16x16x32_bf16 v[200:203], v[236:239], v[4:7], v[200:203]\n\t" \
    "v_mfma_f32_16x16x32_bf16 v[200:203], v[240:243], v[8:11], v[200:203]\n\t" \
    "v_cvt_pk_bf16_f32 v208, v208, v216\n\t" \
    "v_cvt_pk_bf16_f32 v209, v209, v217\n\t" \
    "v_mfma_f32_16x16x32_bf16 v[200:203], v[244:247], v[12:15], v[200:203]\n\t" \
    "v_cvt_pk_bf16_f32 v210, v210, v218\n\t" \
    "v_cvt_pk_bf16_f32 v211, v211, v219\n\t" \
    "s_waitcnt lgkmcnt(0)\n\t" \
    "v_mfma_f32_16x16x32_bf16 v[200:203], v[228:231], v[248:251], v[200:203]\n\t" \
    "ds_read_b128 v[248:251], v254 offset:2048\n\t" \
    "s_add_u32 s44, s55, 0x10b3800\n\t" \
    "v_mfma_f32_16x16x32_bf16 v[216:219], v[224:227], v[96:99], 0\n\t" \
    "buffer_store_dwordx4 v[208:211], v253, s[60:63], s44 offen sc1\n\t" \
    "v_mfma_f32_16x16x32_bf16 v[216:219], v[232:235], v[104:107], v[216:219]\n\t" \
    "v_mfma_f32_16x16x32_bf16 v[216:219], v[236:239], v[108:111], v[216:219]\n\t" \
    "v_mfma_f32_16x16x32_bf16 v[216:219], v[240:243], v[112:115], v[216:219]\n\t" \
    "v_mfma_f32_16x16x32_bf16 v[216:219], v[244:247], v[116:119], v[216:219]\n\t" \
    "v_cvt_pk_bf16_f32 v204, v204, v200\n\t" \
    "v_mfma_f32_16x16x32_bf16 v[216:219], v[228:231], v[100:103], v[216:219]\n\t" \
    "v_cvt_pk_bf16_f32 v205, v205, v201\n\t" \
    "v_cvt_pk_bf16_f32 v206, v206, v202\n\t" \
    "s_waitcnt lgkmcnt(0)\n\t" \
    "v_mfma_f32_16x16x32_bf16 v[208:211], v[224:227], v[248:251], 0\n\t" \
    "ds_read_b128 v[248:251], v254 offset:3072\n\t" \
    "v_cvt_pk_bf16_f32 v207, v207, v203\n\t" \
    "v_mfma_f32_16x16x32_bf16 v[208:211], v[232:235], v[40:43], v[208:211]\n\t" \
    "v_mfma_f32_16x16x32_bf16 v[208:211], v[236:239], v[44:47], v[208:211]\n\t" \
    "s_add_u32 s44, s54, 0xff3800\n\t" \
    "v_mfma_f32_16x16x32_bf16 v[208:211], v[240:243], v[48:51], v[208:211]\n\t" \
    "buffer_store_dwordx4 v[204:207], v253, s[60:63], s44 offen sc1\n\t" \
    "v_mfma_f32_16x16x32_bf16 v[208:211], v[244:247], v[52:55], v[208:211]\n\t" \
    "s_waitcnt lgkmcnt(0)\n\t" \
    "v_mfma_f32_16x16x32_bf16 v[208:211], v[228:231], v[248:251], v[208:211]\n\t" \
    "ds_read_b128 v[248:251], v254 offset:6144\n\t" \
    "v_mfma_f32_16x16x32_bf16 v[200:203], v[224:227], v[136:139], 0\n\t" \
    "v_mfma_f32_16x16x32_bf16 v[200:203], v[232:235], v[144:147], v[200:203]\n\t" \
    "v_mfma_f32_16x16x32_bf16 v[200:203], v[236:239], v[148:151], v[200:203]\n\t" \
    "v_mfma_f32_16x16x32_bf16 v[200:203], v[240:243], v[152:155], v[200:203]\n\t" \
    "v_cvt_pk_bf16_f32 v212, v212, v208\n\t" \
    "v_cvt_pk_bf16_f32 v213, v213, v209\n\t" \
    "v_mfma_f32_16x16x32_bf16 v[200:203], v[244:247], v[156:159], v[200:203]\n\t" \
    "v_cvt_pk_bf16_f32 v214, v214, v210\n\t" \
    "v_cvt_pk_bf16_f32 v215, v215, v211\n\t" \
    "v_mfma_f32_16x16x32_bf16 v[200:203], v[228:231], v[140:143], v[200:203]\n\t" \
    "s_add_u32 s44, s55, 0x1073800\n\t" \
    "s_waitcnt lgkmcnt(0)\n\t" \
    "v_mfma_f32_16x16x32_bf16 v[204:207], v[224:227], v[248:251], 0\n\t" \
    "ds_read_b128 v[248:251], v254 offset:7168\n\t" \
    "ds_read_b128 v[224:227], v253 offset:6144\n\t" \
    "buffer_store_dwordx4 v[212:215], v253, s[60:63], s44 offen sc1\n\t" \
    "v_mfma_f32_16x16x32_bf16 v[204:207], v[232:235], v[120:123], v[204:207]\n\t" \
    "ds_read_b128 v[232:235], v253 offset:8192\n\t" \
    "v_mfma_f32_16x16x32_bf16 v[204:207], v[236:239], v[124:127], v[204:207]\n\t" \
    "ds_read_b128 v[236:239], v253 offset:9216\n\t" \
    "v_mfma_f32_16x16x32_bf16 v[204:207], v[240:243], v[128:131], v[204:207]\n\t" \
    "ds_read_b128 v[240:243], v253 offset:10240\n\t" \
    "v_mfma_f32_16x16x32_bf16 v[204:207], v[244:247], v[132:135], v[204:207]\n\t" \
    "ds_read_b128 v[244:247], v253 offset:11264\n\t" \
    "s_waitcnt lgkmcnt(5)\n\t" \
    "v_mfma_f32_16x16x32_bf16 v[204:207], v[228:231], v[248:251], v[204:207]\n\t" \
    "ds_read_b128 v[248:251], v254 offset:4096\n\t" \
    "ds_read_b128 v[228:231], v253 offset:7168\n\t" \
    "s_waitcnt lgkmcnt(6)\n\t" \
    "v_mfma_f32_16x16x32_bf16 v[208:211], v[224:227], v[96:99], 0\n\t" \
    "s_waitcnt lgkmcnt(5)\n\t" \
    "v_mfma_f32_16x16x32_bf16 v[208:211], v[232:235], v[104:107], v[208:211]\n\t" \
    "s_waitcnt lgkmcnt(4)\n\t" \
    "v_mfma_f32_16x16x32_bf16 v[208:211], v[236:239], v[108:111], v[208:211]\n\t" \
    "s_waitcnt lgkmcnt(3)\n\t" \
    "v_mfma_f32_16x16x32_bf16 v[208:211], v[240:243], v[112:115], v[208:211]\n\t" \
    "s_waitcnt lgkmcnt(2)\n\t" \
    "v_mfma_f32_16x16x32_bf16 v[208:211], v[244:247], v[116:119], v[208:211]\n\t" \
    "s_waitcnt lgkmcnt(0)\n\t" \
    "v_mfma_f32_16x16x32_bf16 v[208:211], v[228:231], v[100:103], v[208:211]\n\t" \
    "v_mfma_f32_16x16x32_bf16 v[212:215], v[224:227], v[248:251], 0\n\t" \
    "ds_read_b128 v[248:251], v254 offset:5120\n\t" \
    "v_mfma_f32_16x16x32_bf16 v[212:215], v[232:235], v[80:83], v[212:215]\n\t" \
    "v_mfma_f32_16x16x32_bf16 v[212:215], v[236:239], v[84:87], v[212:215]\n\t" \
    "v_mfma_f32_16x16x32_bf16 v[212:215], v[240:243], v[88:91], v[212:215]\n\t" \
    "v_cvt_pk_bf16_f32 v216, v208, v216\n\t" \
    "v_cvt_pk_bf16_f32 v217, v209, v217\n\t" \
    "v_mfma_f32_16x16x32_bf16 v[212:215], v[244:247], v[92:95], v[212:215]\n\t" \
    "v_cvt_pk_bf16_f32 v218, v210, v218\n\t" \
    "v_cvt_pk_bf16_f32 v219, v211, v219\n\t" \
    "s_waitcnt lgkmcnt(0)\n\t" \
    "v_mfma_f32_16x16x32_bf16 v[212:215], v[228:231], v[248:251], v[212:215]\n\t" \
    "ds_read_b128 v[248:251], v254 offset:6144\n\t" \
    "s_add_u32 s44, s56, 0x1133800\n\t" \
    "v_mfma_f32_16x16x32_bf16 v[208:211], v[224:227], v[176:179], 0\n\t" \
    "buffer_store_dwordx4 v[216:219], v253, s[60:63], s44 offen sc1\n\t" \
    "v_mfma_f32_16x16x32_bf16 v[208:211], v[232:235], v[184:187], v[208:211]\n\t" \
    "v_mfma_f32_16x16x32_bf16 v[208:211], v[236:239], v[188:191], v[208:211]\n\t" \
    "v_mfma_f32_16x16x32_bf16 v[208:211], v[240:243], v[192:195], v[208:211]\n\t" \
    "v_mfma_f32_16x16x32_bf16 v[208:211], v[244:247], v[196:199], v[208:211]\n\t" \
    "v_cvt_pk_bf16_f32 v220, v212, v220\n\t" \
    "v_mfma_f32_16x16x32_bf16 v[208:211], v[228:231], v[180:183], v[208:211]\n\t" \
    "v_cvt_pk_bf16_f32 v221, v213, v221\n\t" \
    "v_cvt_pk_bf16_f32 v222, v214, v222\n\t" \
    "s_waitcnt lgkmcnt(0)\n\t" \
    "v_mfma_f32_16x16x32_bf16 v[216:219], v[224:227], v[248:251], 0\n\t" \
    "ds_read_b128 v[248:251], v254 offset:7168\n\t" \
    "v_cvt_pk_bf16_f32 v223, v215, v223\n\t" \
    "v_mfma_f32_16x16x32_bf16 v[216:219], v[232:235], v[120:123], v[216:219]\n\t" \
    "v_mfma_f32_16x16x32_bf16 v[216:219], v[236:239], v[124:127], v[216:219]\n\t" \
    "s_add_u32 s44, s56, 0x10f3800\n\t" \
    "v_mfma_f32_16x16x32_bf16 v[216:219], v[240:243], v[128:131], v[216:219]\n\t" \
    "buffer_store_dwordx4 v[220:223], v253, s[60:63], s44 offen sc1\n\t" \
    "v_mfma_f32_16x16x32_bf16 v[216:219], v[244:247], v[132:135], v[216:219]\n\t" \
    "s_waitcnt lgkmcnt(0)\n\t" \
    "v_mfma_f32_16x16x32_bf16 v[216:219], v[228:231], v[248:251], v[216:219]\n\t" \
    "ds_read_b128 v[248:251], v254 offset:8192\n\t" \
    "v_mfma_f32_16x16x32_bf16 v[212:215], v[224:227], v[136:139], 0\n\t" \
    "v_mfma_f32_16x16x32_bf16 v[212:215], v[232:235], v[144:147], v[212:215]\n\t" \
    "v_mfma_f32_16x16x32_bf16 v[212:215], v[236:239], v[148:151], v[212:215]\n\t" \
    "v_mfma_f32_16x16x32_bf16 v[212:215], v[240:243], v[152:155], v[212:215]\n\t" \
    "v_cvt_pk_bf16_f32 v204, v216, v204\n\t" \
    "v_cvt_pk_bf16_f32 v205, v217, v205\n\t" \
    "v_mfma_f32_16x16x32_bf16 v[212:215], v[244:247], v[156:159], v[212:215]\n\t" \
    "v_cvt_pk_bf16_f32 v206, v218, v206\n\t" \
    "v_cvt_pk_bf16_f32 v207, v219, v207\n\t" \
    "v_mfma_f32_16x16x32_bf16 v[212:215], v[228:231], v[140:143], v[212:215]\n\t" \
    "s_add_u32 s44, s57, 0x1173800\n\t" \
    "s_waitcnt lgkmcnt(0)\n\t" \
    "v_mfma_f32_16x16x32_bf16 v[220:223], v[224:227], v[248:251], 0\n\t" \
    "ds_read_b128 v[248:251], v254 offset:9216\n\t" \
    "ds_read_b128 v[224:227], v253 offset:12288\n\t" \
    "buffer_store_dwordx4 v[204:207], v253, s[60:63], s44 offen sc1\n\t" \
    "v_mfma_f32_16x16x32_bf16 v[220:223], v[232:235], v[160:163], v[220:223]\n\t" \
    "ds_read_b128 v[232:235], v253 offset:14336\n\t" \
    "v_mfma_f32_16x16x32_bf16 v[220:223], v[236:239], v[164:167], v[220:223]\n\t" \
    "ds_read_b128 v[236:239], v253 offset:15360\n\t" \
    "v_mfma_f32_16x16x32_bf16 v[220:223], v[240:243], v[168:171], v[220:223]\n\t" \
    "ds_read_b128 v[240:243], v253 offset:16384\n\t" \
    "v_mfma_f32_16x16x32_bf16 v[220:223], v[244:247], v[172:175], v[220:223]\n\t" \
    "ds_read_b128 v[244:247], v253 offset:17408\n\t" \
    "v_cvt_pk_bf16_f32 v200, v212, v200\n\t" \
    "s_waitcnt lgkmcnt(5)\n\t" \
    "v_mfma_f32_16x16x32_bf16 v[220:223], v[228:231], v[248:251], v[220:223]\n\t" \
    "ds_read_b128 v[248:251], v254 offset:8192\n\t" \
    "ds_read_b128 v[228:231], v253 offset:13312\n\t" \
    "v_cvt_pk_bf16_f32 v201, v213, v201\n\t" \
    "v_cvt_pk_bf16_f32 v202, v214, v202\n\t" \
    "s_waitcnt lgkmcnt(6)\n\t" \
    "v_mfma_f32_16x16x32_bf16 v[216:219], v[224:227], v[176:179], 0\n\t" \
    "v_cvt_pk_bf16_f32 v203, v215, v203\n\t" \
    "s_waitcnt lgkmcnt(5)\n\t" \
    "v_mfma_f32_16x16x32_bf16 v[216:219], v[232:235], v[184:187], v[216:219]\n\t" \
    "s_waitcnt lgkmcnt(4)\n\t" \
    "v_mfma_f32_16x16x32_bf16 v[216:219], v[236:239], v[188:191], v[216:219]\n\t" \
    "s_add_u32 s44, s57, 0x11b3800\n\t" \
    "s_waitcnt lgkmcnt(3)\n\t" \
    "v_mfma_f32_16x16x32_bf16 v[216:219], v[240:243], v[192:195], v[216:219]\n\t" \
    "buffer_store_dwordx4 v[200:203], v253, s[60:63], s44 offen sc1\n\t" \
    "s_waitcnt lgkmcnt(2)\n\t" \
    "v_mfma_f32_16x16x32_bf16 v[216:219], v[244:247], v[196:199], v[216:219]\n\t" \
    "s_waitcnt lgkmcnt(0)\n\t" \
    "v_mfma_f32_16x16x32_bf16 v[216:219], v[228:231], v[180:183], v[216:219]\n\t" \
    "v_mfma_f32_16x16x32_bf16 v[204:207], v[224:227], v[248:251], 0\n\t" \
    "ds_read_b128 v[248:251], v254 offset:9216\n\t" \
    "v_mfma_f32_16x16x32_bf16 v[204:207], v[232:235], v[160:163], v[204:207]\n\t" \
    "v_mfma_f32_16x16x32_bf16 v[204:207], v[236:239], v[164:167], v[204:207]\n\t" \
    "v_mfma_f32_16x16x32_bf16 v[204:207], v[240:243], v[168:171], v[204:207]\n\t" \
    "v_cvt_pk_bf16_f32 v208, v208, v216\n\t" \
    "v_cvt_pk_bf16_f32 v209, v209, v217\n\t" \
    "v_mfma_f32_16x16x32_bf16 v[204:207], v[244:247], v[172:175], v[204:207]\n\t" \
    "v_cvt_pk_bf16_f32 v210, v210, v218\n\t" \
    "v_cvt_pk_bf16_f32 v211, v211, v219\n\t" \
    "s_waitcnt lgkmcnt(0)\n\t" \
    "v_mfma_f32_16x16x32_bf16 v[204:207], v[228:231], v[248:251], v[204:207]\n\t" \
    "s_add_u32 s44, s57, 0x1b3800\n\t" \
    "buffer_store_dwordx4 v[208:211], v253, s[60:63], s44 offen sc1\n\t" \
    "s_nop 6\n\t" \
    "v_cvt_pk_bf16_f32 v220, v220, v204\n\t" \
    "v_cvt_pk_bf16_f32 v221, v221, v205\n\t" \
    "v_cvt_pk_bf16_f32 v222, v222, v206\n\t" \
    "v_cvt_pk_bf16_f32 v223, v223, v207\n\t" \
    "s_add_u32 s44, s57, 0x173800\n\t" \
    "buffer_store_dwordx4 v[220:223], v253, s[60:63], s44 offen sc1\n\t" \
    LST0 \
    LJIT \
    "s_add_u32 s44, s40, 1\n\t" \
    "s_mov_b32 s45, s44\n\t" \
    "v_mov_b64 v[200:201], s[44:45]\n\t" \
    "v_mov_b64 v[202:203], s[44:45]\n\t" \
    "s_waitcnt lgkmcnt(0)\n\t" \
    "s_barrier \n\t" \
    LST3 \
    LJIT \
    "ds_read_b128 v[220:223], v253 offset:3072\n\t" \
    "ds_read_b128 v[224:227], v253 offset:3072\n\t" \
    "ds_read_b128 v[228:231], v253 offset:3072\n\t" \
    "ds_read_b128 v[232:235], v253 offset:3072\n\t" \
    "ds_read_b128 v[236:239], v253 offset:3072\n\t" \
    "ds_read_b128 v[240:243], v253 offset:3072\n\t" \
    "ds_read_b128 v[244:247], v253 offset:3072\n\t" \
    "ds_read_b128 v[248:251], v253 offset:3072\n\t" \
    "ds_read_b128 v[212:215], v254 offset:0\n\t" \
    "ds_read_b128 v[216:219], v254 offset:1024\n\t" \
    "s_and_b32 s44, s40, 3\n\t" \
    "s_lshl_b32 s44, s44, 22\n\t" \
    "s_add_u32 s42, s44, %[pb]\n\t" \
    "s_mov_b64 exec, 0xff\n\t" \
    "s_waitcnt vmcnt(0)\n\t" \
    "buffer_store_dwordx4 v[200:203], v253, s[60:63], %[fl] offen sc1\n\t" \
    "s_mov_b64 exec, -1\n\t" \
    LST1 \
    LJIT \
    "s_setprio 3\n\t" \
    "s_waitcnt lgkmcnt(0)\n\t" \
    "s_barrier \n\t" \
    LST4 \
    LJIT \
    "ds_read_b128 v[204:207], v253 offset:1024\n\t" \
    "ds_read_b128 v[208:211], v253 offset:2048\n\t" \
    "ds_read_b128 v[200:203], v253 offset:0\n\t" \
    "s_waitcnt lgkmcnt(2)\n\t" \
    "v_smfmac_f32_16x16x64_bf16 v[224:227], v[204:207], v[24:31], v252\n\t" \
    "v_smfmac_f32_16x16x64_bf16 v[220:223], v[204:207], v[0:7], v252\n\t" \
    "s_waitcnt lgkmcnt(1)\n\t" \
    "v_smfmac_f32_16x16x64_bf16 v[224:227], v[208:211], v[32:39], v252\n\t" \
    "v_smfmac_f32_16x16x64_bf16 v[220:223], v[208:211], v[8:15], v252\n\t" \
    "s_waitcnt lgkmcnt(0)\n\t" \
    "v_smfmac_f32_16x16x64_bf16 v[224:227], v[200:203], v[16:23], v252\n\t" \
    "v_smfmac_f32_16x16x64_bf16 v[220:223], v[200:203], v[212:219], v252\n\t" \
    "ds_read_b128 v[212:215], v254 offset:2048\n\t" \
    "ds_read_b128 v[216:219], v254 offset:3072\n\t" \
    "v_smfmac_f32_16x16x64_bf16 v[232:235], v[204:207], v[64:71], v252\n\t" \
    "v_smfmac_f32_16x16x64_bf16 v[228:231], v[204:207], v[40:47], v252\n\t" \
    "v_smfmac_f32_16x16x64_bf16 v[232:235], v[208:211], v[72:79], v252\n\t" \
    "v_add_f32 v220, v220, v222\n\t" \
    "v_add_f32 v221, v221, v223\n\t" \
    "v_cvt_pk_bf16_f32 v220, v220, v221\n\t" \
    "v_smfmac_f32_16x16x64_bf16 v[228:231], v[208:211], v[48:55], v252\n\t" \
    "v_min_u32 v220, -2, v220\n\t" \
    "v_add_f32 v224, v224, v226\n\t" \
    "v_add_f32 v225, v225, v227\n\t" \
    "v_smfmac_f32_16x16x64_bf16 v[232:235], v[200:203], v[56:63], v252\n\t" \
    "v_cvt_pk_bf16_f32 v221, v224, v225\n\t" \
    "v_min_u32 v221, -2, v221\n\t" \
    "s_waitcnt lgkmcnt(0)\n\t" \
    "v_smfmac_f32_16x16x64_bf16 v[228:231], v[200:203], v[212:219], v252\n\t" \
    "ds_read_b128 v[212:215], v254 offset:4096\n\t" \
    "ds_read_b128 v[216:219], v254 offset:5120\n\t" \
    "v_smfmac_f32_16x16x64_bf16 v[240:243], v[204:207], v[104:111], v252\n\t" \
    "v_smfmac_f32_16x16x64_bf16 v[236:239], v[204:207], v[80:87], v252\n\t" \
    "v_smfmac_f32_16x16x64_bf16 v[240:243], v[208:211], v[112:119], v252\n\t" \
    "v_add_f32 v228, v228, v230\n\t" \
    "v_add_f32 v229, v229, v231\n\t" \
    "v_cvt_pk_bf16_f32 v222, v228, v229\n\t" \
    "v_smfmac_f32_16x16x64_bf16 v[236:239], v[208:211], v[88:95], v252\n\t" \
    "v_min_u32 v222, -2, v222\n\t" \
    "v_add_f32 v232, v232, v234\n\t" \
    "v_add_f32 v233, v233, v235\n\t" \
    "v_smfmac_f32_16x16x64_bf16 v[240:243], v[200:203], v[96:103], v252\n\t" \
    "v_cvt_pk_bf16_f32 v223, v232, v233\n\t" \
    "v_min_u32 v223, -2, v223\n\t" \
    "v_permlane32_swap_b32 v220, v222\n\t" \
    "s_waitcnt lgkmcnt(0)\n\t" \
    "v_smfmac_f32_16x16x64_bf16 v[236:239], v[200:203], v[212:219], v252\n\t" \
    "ds_read_b128 v[212:215], v254 offset:6144\n\t" \
    "ds_read_b128 v[216:219], v254 offset:7168\n\t" \
    "s_nop 0\n\t" \
    "v_permlane32_swap_b32 v221, v223\n\t" \
    "s_nop 1\n\t" \
    "v_permlane16_swap_b32 v220, v221\n\t" \
    "v_permlane16_swap_b32 v222, v223\n\t" \
    "buffer_store_dwordx4 v[220:223], v255, s[60:63], s42 offen sc1\n\t" \
    "s_add_u32 s42, s42, 0x20000\n\t" \
    "ds_read_b128 v[224:227], v253 offset:3072\n\t" \
    "ds_read_b128 v[228:231], v253 offset:3072\n\t" \
    "v_smfmac_f32_16x16x64_bf16 v[248:251], v[204:207], v[144:151], v252\n\t" \
    "v_smfmac_f32_16x16x64_bf16 v[244:247], v[204:207], v[120:127], v252\n\t" \
    "v_smfmac_f32_16x16x64_bf16 v[248:251], v[208:211], v[152:159], v252\n\t" \
    "v_add_f32 v236, v236, v238\n\t" \
    "v_add_f32 v237, v237, v239\n\t" \
    "v_cvt_pk_bf16_f32 v236, v236, v237\n\t" \
    "v_smfmac_f32_16x16x64_bf16 v[244:247], v[208:211], v[128:135], v252\n\t" \
    "v_min_u32 v236, -2, v236\n\t" \
    "v_add_f32 v240, v240, v242\n\t" \
    "v_add_f32 v241, v241, v243\n\t" \
    "v_smfmac_f32_16x16x64_bf16 v[248:251], v[200:203], v[136:143], v252\n\t" \
    "v_cvt_pk_bf16_f32 v237, v240, v241\n\t" \
    "v_min_u32 v237, -2, v237\n\t" \
    "s_waitcnt lgkmcnt(2)\n\t" \
    "v_smfmac_f32_16x16x64_bf16 v[244:247], v[200:203], v[212:219], v252\n\t" \
    "ds_read_b128 v[212:215], v254 offset:8192\n\t" \
    "ds_read_b128 v[216:219], v254 offset:9216\n\t" \
    "s_waitcnt lgkmcnt(2)\n\t" \
    "v_smfmac_f32_16x16x64_bf16 v[228:231], v[204:207], v[184:191], v252\n\t" \
    "v_smfmac_f32_16x16x64_bf16 v[224:227], v[204:207], v[160:167], v252\n\t" \
    "v_smfmac_f32_16x16x64_bf16 v[228:231], v[208:211], v[192:199], v252\n\t" \
    "v_and_b32 v232, 0xffff, v255\n\t" \
    "v_lshrrev_b32 v233, 16, v255\n\t" \
    "v_lshl_add_u32 v232, v233, 3, v232\n\t" \
    "v_smfmac_f32_16x16x64_bf16 v[224:227], v[208:211], v[168:175], v252\n\t" \
    "v_add_f32 v244, v244, v246\n\t" \
    "v_add_f32 v245, v245, v247\n\t" \
    "v_cvt_pk_bf16_f32 v238, v244, v245\n\t" \
    "v_smfmac_f32_16x16x64_bf16 v[228:231], v[200:203], v[176:183], v252\n\t" \
    "v_min_u32 v238, -2, v238\n\t" \
    "v_add_f32 v248, v248, v250\n\t" \
    "v_add_f32 v249, v249, v251\n\t" \
    "s_waitcnt lgkmcnt(0)\n\t" \
    "v_smfmac_f32_16x16x64_bf16 v[224:227], v[200:203], v[212:219], v252\n\t" \
    "v_cvt_pk_bf16_f32 v239, v248, v249\n\t" \
    "v_min_u32 v239, -2, v239\n\t" \
    "v_permlane32_swap_b32 v236, v238\n\t" \
    "s_nop 0\n\t" \
    "v_permlane32_swap_b32 v237, v239\n\t" \
    "s_nop 1\n\t" \
    "v_permlane16_swap_b32 v236, v237\n\t" \
    "v_permlane16_swap_b32 v238, v239\n\t" \
    "buffer_store_dwordx4 v[236:239], v255, s[60:63], s42 offen sc1\n\t" \
    "s_add_u32 s42, s42, 0x20000\n\t" \
    "v_add_f32 v228, v228, v230\n\t" \
    "v_add_f32 v229, v229, v231\n\t" \
    "v_add_f32 v224, v224, v226\n\t" \
    "v_add_f32 v225, v225, v227\n\t" \
    "v_cvt_pk_bf16_f32 v229, v228, v229\n\t" \
    "v_cvt_pk_bf16_f32 v228, v224, v225\n\t" \
    "v_min_u32 v229, -2, v229\n\t" \
    "v_min_u32 v228, -2, v228\n\t" \
    "s_nop 1\n\t" \
    "v_permlane16_swap_b32 v228, v229\n\t" \
    "buffer_store_dwordx2 v[228:229], v232, s[60:63], s42 offen sc1\n\t" \
    "s_setprio 0\n\t" \
    LST5 \
    LJIT \
    LFLUSH200 \
    "s_add_u32 s40, s40, 1\n\t" \
    "s_cmp_lg_u32 s40, 64\n\t" \
    "s_cbranch_scc1 .Lv43_%=\n\t" \
    "s_branch .Lend%=\n\t" \
    ".Lv3_%=:\n\t" \
    "s_mov_b32 s42, %[xs]\n\t" \
    "v_add_u32 v244, 0xc000, v243\n\t" \
    "s_waitcnt vmcnt(12) lgkmcnt(0)\n\t" \
    "s_barrier \n\t" \
    "s_add_u32 m0, %[wm], 0x9000\n\t" \
    "s_mov_b32 s44, 0x1e0000\n\t" \
    "buffer_load_dwordx4 v240, %[wr], s44 offen offset:0 nt lds\n\t" \
    "buffer_load_dwordx4 v241, %[wr], s44 offen offset:1024 nt lds\n\t" \
    "buffer_load_dwordx4 v242, %[wr], s44 offen offset:2048 nt lds\n\t" \
    "ds_read_b64_tr_b16 v[208:209], v244 offset:0\n\t" \
    "ds_read_b64_tr_b16 v[210:211], v244 offset:1536\n\t" \
    "ds_read_b64_tr_b16 v[40:41], v244 offset:32\n\t" \
    "ds_read_b64_tr_b16 v[42:43], v244 offset:1568\n\t" \
    "ds_read_b64_tr_b16 v[48:49], v244 offset:64\n\t" \
    "ds_read_b64_tr_b16 v[50:51], v244 offset:1600\n\t" \
    "v_add_u32 v244, 0xf000, v243\n\t" \
    "s_waitcnt vmcnt(12) lgkmcnt(0)\n\t" \
    "s_barrier \n\t" \
    "s_add_u32 m0, %[wm], 0xc000\n\t" \
    "s_mov_b32 s44, 0x240000\n\t" \
    "buffer_load_dwordx4 v240, %[wr], s44 offen offset:0 nt lds\n\t" \
    "buffer_load_dwordx4 v241, %[wr], s44 offen offset:1024 nt lds\n\t" \
    "buffer_load_dwordx4 v242, %[wr], s44 offen offset:2048 nt lds\n\t" \
    "ds_read_b64_tr_b16 v[212:213], v244 offset:0\n\t" \
    "ds_read_b64_tr_b16 v[214:215], v244 offset:1536\n\t" \
    "ds_read_b64_tr_b16 v[44:45], v244 offset:32\n\t" \
    "ds_read_b64_tr_b16 v[46:47], v244 offset:1568\n\t" \
    "ds_read_b64_tr_b16 v[52:53], v244 offset:64\n\t" \
    "ds_read_b64_tr_b16 v[54:55], v244 offset:1600\n\t" \
    "v_add_u32 v244, 0x0, v243\n\t" \
    "s_waitcnt vmcnt(12) lgkmcnt(0)\n\t" \
    "s_barrier \n\t" \
    "s_add_u32 m0, %[wm], 0xf000\n\t" \
    "s_mov_b32 s44, 0x2a0000\n\t" \
    "buffer_load_dwordx4 v240, %[wr], s44 offen offset:0 nt lds\n\t" \
    "buffer_load_dwordx4 v241, %[wr], s44 offen offset:1024 nt lds\n\t" \
    "buffer_load_dwordx4 v242, %[wr], s44 offen offset:2048 nt lds\n\t" \
    "ds_read_b64_tr_b16 v[56:57], v244 offset:0\n\t" \
    "ds_read_b64_tr_b16 v[58:59], v244 offset:1536\n\t" \
    "ds_read_b64_tr_b16 v[64:65], v244 offset:32\n\t" \
    "ds_read_b64_tr_b16 v[66:67], v244 offset:1568\n\t" \
    "ds_read_b64_tr_b16 v[72:73], v244 offset:64\n\t" \
    "ds_read_b64_tr_b16 v[74:75], v244 offset:1600\n\t" \
    "v_add_u32 v244, 0x3000, v243\n\t" \
    "s_waitcnt vmcnt(12) lgkmcnt(0)\n\t" \
    "s_barrier \n\t" \
    "s_add_u32 m0, %[wm], 0x0\n\t" \
    "s_mov_b32 s44, 0x300000\n\t" \
    "buffer_load_dwordx4 v240, %[wr], s44 offen offset:0 nt lds\n\t" \
    "buffer_load_dwordx4 v241, %[wr], s44 offen offset:1024 nt lds\n\t" \
    "buffer_load_dwordx4 v242, %[wr], s44 offen offset:2048 nt lds\n\t" \
    "ds_read_b64_tr_b16 v[60:61], v244 offset:0\n\t" \
    "ds_read_b64_tr_b16 v[62:63], v244 offset:1536\n\t" \
    "ds_read_b64_tr_b16 v[68:69], v244 offset:32\n\t" \
    "ds_read_b64_tr_b16 v[70:71], v244 offset:1568\n\t" \
    "ds_read_b64_tr_b16 v[76:77], v244 offset:64\n\t" \
    "ds_read_b64_tr_b16 v[78:79], v244 offset:1600\n\t" \
    "v_add_u32 v244, 0x6000, v243\n\t" \
    "s_waitcnt vmcnt(12) lgkmcnt(0)\n\t" \
    "s_barrier \n\t" \
    "s_add_u32 m0, %[wm], 0x3000\n\t" \
    "s_mov_b32 s44, 0x360000\n\t" \
    "buffer_load_dwordx4 v240, %[wr], s44 offen offset:0 nt lds\n\t" \
    "buffer_load_dwordx4 v241, %[wr], s44 offen offset:1024 nt lds\n\t" \
    "buffer_load_dwordx4 v242, %[wr], s44 offen offset:2048 nt lds\n\t" \
    "ds_read_b64_tr_b16 v[216:217], v244 offset:0\n\t" \
    "ds_read_b64_tr_b16 v[218:219], v244 offset:1536\n\t" \
    "ds_read_b64_tr_b16 v[80:81], v244 offset:32\n\t" \
    "ds_read_b64_tr_b16 v[82:83], v244 offset:1568\n\t" \
    "ds_read_b64_tr_b16 v[88:89], v244 offset:64\n\t" \
    "ds_read_b64_tr_b16 v[90:91], v244 offset:1600\n\t" \
    "v_add_u32 v244, 0x9000, v243\n\t" \
    "s_waitcnt vmcnt(12) lgkmcnt(0)\n\t" \
    "s_barrier \n\t" \
    "s_add_u32 m0, %[wm], 0x6000\n\t" \
    "s_mov_b32 s44, 0x3c0000\n\t" \
    "buffer_load_dwordx4 v240, %[wr], s44 offen offset:0 nt lds\n\t" \
    "buffer_load_dwordx4 v241, %[wr], s44 offen offset:1024 nt lds\n\t" \
    "buffer_load_dwordx4 v242, %[wr], s44 offen offset:2048 nt lds\n\t" \
    "ds_read_b64_tr_b16 v[220:221], v244 offset:0\n\t" \
    "ds_read_b64_tr_b16 v[222:223], v244 offset:1536\n\t" \
    "ds_read_b64_tr_b16 v[84:85], v244 offset:32\n\t" \
    "ds_read_b64_tr_b16 v[86:87], v244 offset:1568\n\t" \
    "ds_read_b64_tr_b16 v[92:93], v244 offset:64\n\t" \
    "ds_read_b64_tr_b16 v[94:95], v244 offset:1600\n\t" \
    "v_add_u32 v244, 0xc000, v243\n\t" \
    "s_waitcnt vmcnt(12) lgkmcnt(0)\n\t" \
    "s_barrier \n\t" \
    "s_add_u32 m0, %[wm], 0x9000\n\t" \
    "s_mov_b32 s44, 0x420000\n\t" \
    "buffer_load_dwordx4 v240, %[wr], s44 offen offset:0 nt lds\n\t" \
    "buffer_load_dwordx4 v241, %[wr], s44 offen offset:1024 nt lds\n\t" \
    "buffer_load_dwordx4 v242, %[wr], s44 offen offset:2048 nt lds\n\t" \
    "ds_read_b64_tr_b16 v[96:97], v244 offset:0\n\t" \
    "ds_read_b64_tr_b16 v[98:99], v244 offset:1536\n\t" \
    "ds_read_b64_tr_b16 v[104:105], v244 offset:32\n\t" \
    "ds_read_b64_tr_b16 v[106:107], v244 offset:1568\n\t" \
    "ds_read_b64_tr_b16 v[112:113], v244 offset:64\n\t" \
    "ds_read_b64_tr_b16 v[114:115], v244 offset:1600\n\t" \
    "v_add_u32 v244, 0xf000, v243\n\t" \
    "s_waitcnt vmcnt(12) lgkmcnt(0)\n\t" \
    "s_barrier \n\t" \
    "s_add_u32 m0, %[wm], 0xc000\n\t" \
    "s_mov_b32 s44, 0x480000\n\t" \
    "buffer_load_dwordx4 v240, %[wr], s44 offen offset:0 nt lds\n\t" \
    "buffer_load_dwordx4 v241, %[wr], s44 offen offset:1024 nt lds\n\t" \
    "buffer_load_dwordx4 v242, %[wr], s44 offen offset:2048 nt lds\n\t" \
    "ds_read_b64_tr_b16 v[100:101], v244 offset:0\n\t" \
    "ds_read_b64_tr_b16 v[102:103], v244 offset:1536\n\t" \
    "ds_read_b64_tr_b16 v[108:109], v244 offset:32\n\t" \
    "ds_read_b64_tr_b16 v[110:111], v244 offset:1568\n\t" \
    "ds_read_b64_tr_b16 v[116:117], v244 offset:64\n\t" \
    "ds_read_b64_tr_b16 v[118:119], v244 offset:1600\n\t" \
    "v_add_u32 v244, 0x0, v243\n\t" \
    "s_waitcnt vmcnt(12) lgkmcnt(0)\n\t" \
    "s_barrier \n\t" \
    "v_mov_b32 v246, 0\n\t" \
    "buffer_load_dword v246, v246, s[60:63], %[mo] offen sc1\n\t" \
    "s_add_u32 m0, %[wm], 0xf000\n\t" \
    "s_mov_b32 s44, 0x4e0000\n\t" \
    "buffer_load_dwordx4 v240, %[wr], s44 offen offset:0 nt lds\n\t" \
    "buffer_load_dwordx4 v241, %[wr], s44 offen offset:1024 nt lds\n\t" \
    "buffer_load_dwordx4 v242, %[wr], s44 offen offset:2048 nt lds\n\t" \
    "ds_read_b64_tr_b16 v[224:225], v244 offset:0\n\t" \
    "ds_read_b64_tr_b16 v[226:227], v244 offset:1536\n\t" \
    "ds_read_b64_tr_b16 v[120:121], v244 offset:32\n\t" \
    "ds_read_b64_tr_b16 v[122:123], v244 offset:1568\n\t" \
    "ds_read_b64_tr_b16 v[128:129], v244 offset:64\n\t" \
    "ds_read_b64_tr_b16 v[130:131], v244 offset:1600\n\t" \
    "v_add_u32 v244, 0x3000, v243\n\t" \
    "s_waitcnt vmcnt(12) lgkmcnt(0)\n\t" \
    "s_barrier \n\t" \
    "s_add_u32 m0, %[wm], 0x0\n\t" \
    "s_mov_b32 s44, 0x540000\n\t" \
    "buffer_load_dwordx4 v240, %[wr], s44 offen offset:0 nt lds\n\t" \
    "buffer_load_dwordx4 v241, %[wr], s44 offen offset:1024 nt lds\n\t" \
    "buffer_load_dwordx4 v242, %[wr], s44 offen offset:2048 nt lds\n\t" \
    "ds_read_b64_tr_b16 v[228:229], v244 offset:0\n\t" \
    "ds_read_b64_tr_b16 v[230:231], v244 offset:1536\n\t" \
    "ds_read_b64_tr_b16 v[124:125], v244 offset:32\n\t" \
    "ds_read_b64_tr_b16 v[126:127], v244 offset:1568\n\t" \
    "ds_read_b64_tr_b16 v[132:133], v244 offset:64\n\t" \
    "ds_read_b64_tr_b16 v[134:135], v244 offset:1600\n\t" \
    "v_add_u32 v244, 0x6000, v243\n\t" \
    "s_waitcnt vmcnt(12) lgkmcnt(0)\n\t" \
    "s_barrier \n\t" \
    "s_add_u32 m0, %[wm], 0x3000\n\t" \
    "s_mov_b32 s44, 0x5a0000\n\t" \
    "buffer_load_dwordx4 v240, %[wr], s44 offen offset:0 nt lds\n\t" \
    "buffer_load_dwordx4 v241, %[wr], s44 offen offset:1024 nt lds\n\t" \
    "buffer_load_dwordx4 v242, %[wr], s44 offen offset:2048 nt lds\n\t" \
    "ds_read_b64_tr_b16 v[136:137], v244 offset:0\n\t" \
    "ds_read_b64_tr_b16 v[138:139], v244 offset:1536\n\t" \
    "ds_read_b64_tr_b16 v[144:145], v244 offset:32\n\t" \
    "ds_read_b64_tr_b16 v[146:147], v244 offset:1568\n\t" \
    "ds_read_b64_tr_b16 v[152:153], v244 offset:64\n\t" \
    "ds_read_b64_tr_b16 v[154:155], v244 offset:1600\n\t" \
    "v_add_u32 v244, 0x9000, v243\n\t" \
    "s_waitcnt vmcnt(12) lgkmcnt(0)\n\t" \
    "s_barrier \n\t" \
    "s_add_u32 m0, %[wm], 0x6000\n\t" \
    "s_mov_b32 s44, 0x600000\n\t" \
    "buffer_load_dwordx4 v240, %[wr], s44 offen offset:0 nt lds\n\t" \
    "buffer_load_dwordx4 v241, %[wr], s44 offen offset:1024 nt lds\n\t" \
    "buffer_load_dwordx4 v242, %[wr], s44 offen offset:2048 nt lds\n\t" \
    "ds_read_b64_tr_b16 v[140:141], v244 offset:0\n\t" \
    "ds_read_b64_tr_b16 v[142:143], v244 offset:1536\n\t" \
    "ds_read_b64_tr_b16 v[148:149], v244 offset:32\n\t" \
    "ds_read_b64_tr_b16 v[150:151], v244 offset:1568\n\t" \
    "ds_read_b64_tr_b16 v[156:157], v244 offset:64\n\t" \
    "ds_read_b64_tr_b16 v[158:159], v244 offset:1600\n\t" \
    "v_add_u32 v244, 0xc000, v243\n\t" \
    "s_waitcnt vmcnt(12) lgkmcnt(0)\n\t" \
    "s_barrier \n\t" \
    "s_add_u32 m0, %[wm], 0x9000\n\t" \
    "s_mov_b32 s44, 0x660000\n\t" \
    "buffer_load_dwordx4 v240, %[wr], s44 offen offset:0 nt lds\n\t" \
    "buffer_load_dwordx4 v241, %[wr], s44 offen offset:1024 nt lds\n\t" \
    "buffer_load_dwordx4 v242, %[wr], s44 offen offset:2048 nt lds\n\t" \
    "ds_read_b64_tr_b16 v[232:233], v244 offset:0\n\t" \
    "ds_read_b64_tr_b16 v[234:235], v244 offset:1536\n\t" \
    "ds_read_b64_tr_b16 v[160:161], v244 offset:32\n\t" \
    "ds_read_b64_tr_b16 v[162:163], v244 offset:1568\n\t" \
    "ds_read_b64_tr_b16 v[168:169], v244 offset:64\n\t" \
    "ds_read_b64_tr_b16 v[170:171], v244 offset:1600\n\t" \
    "v_add_u32 v244, 0xf000, v243\n\t" \
    "s_waitcnt vmcnt(12) lgkmcnt(0)\n\t" \
    "s_barrier \n\t" \
    ".Lv31_%=:\n\t" \
    "v_readfirstlane_b32 s43, v246\n\t" \
    "s_nop 0\n\t" \
    "s_cmp_lg_u32 s43, -1\n\t" \
    "s_cbranch_scc1 .Lv32_%=\n\t" \
    "s_cmp_eq_u32 s41, 0\n\t" \
    "s_cbranch_scc1 .Lv32_%=\n\t" \
    "s_sub_u32 s41, s41, 1\n\t" \
    "s_sleep 4\n\t" \
    "v_mov_b32 v246, 0\n\t" \
    "buffer_load_dword v246, v246, s[60:63], %[mo] offen sc1\n\t" \
    "s_waitcnt vmcnt(0)\n\t" \
    "s_branch .Lv31_%=\n\t" \
    ".Lv32_%=:\n\t" \
    "v_mov_b32 v246, 0x8000\n\t" \
    "s_mov_b64 exec, 1\n\t" \
    "buffer_store_dword v246, v246, s[60:63], %[mo] offen sc1\n\t" \
    "s_mov_b64 exec, -1\n\t" \
    "s_and_b32 s44, s43, 0x5000\n\t" \
    "s_xor_b32 %[mb], %[mb], s44\n\t" \
    "s_xor_b32 %[fl], %[fl], s44\n\t" \
    "s_and_b32 s43, s43, 0x2000\n\t" \
    "s_xor_b32 s43, s43, %[lx]\n\t" \
    "s_add_u32 s60, s60, s43\n\t" \
    "s_addc_u32 s61, s61, 0\n\t" \
    "v_and_b32 v252, 0xf0, v253\n\t" \
    "v_lshlrev_b32 v252, 3, v252\n\t" \
    "buffer_load_dword v252, v252, s[60:63], %[fl] offen sc1\n\t" \
    "s_add_u32 m0, %[wm], 0xc000\n\t" \
    "s_mov_b32 s44, 0x6c0000\n\t" \
    "buffer_load_dwordx4 v240, %[wr], s44 offen offset:0 nt lds\n\t" \
    "buffer_load_dwordx4 v241, %[wr], s44 offen offset:1024 nt lds\n\t" \
    "buffer_load_dwordx4 v242, %[wr], s44 offen offset:2048 nt lds\n\t" \
    "ds_read_b64_tr_b16 v[236:237], v244 offset:0\n\t" \
    "ds_read_b64_tr_b16 v[238:239], v244 offset:1536\n\t" \
    "ds_read_b64_tr_b16 v[164:165], v244 offset:32\n\t" \
    "ds_read_b64_tr_b16 v[166:167], v244 offset:1568\n\t" \
    "ds_read_b64_tr_b16 v[172:173], v244 offset:64\n\t" \
    "ds_read_b64_tr_b16 v[174:175], v244 offset:1600\n\t" \
    "v_add_u32 v244, 0x0, v243\n\t" \
    "s_waitcnt vmcnt(12) lgkmcnt(0)\n\t" \
    "s_barrier \n\t" \
    "s_add_u32 m0, %[wm], 0xf000\n\t" \
    "s_mov_b32 s44, 0x720000\n\t" \
    "buffer_load_dwordx4 v240, %[wr], s44 offen offset:0 nt lds\n\t" \
    "buffer_load_dwordx4 v241, %[wr], s44 offen offset:1024 nt lds\n\t" \
    "buffer_load_dwordx4 v242, %[wr], s44 offen offset:2048 nt lds\n\t" \
    "ds_read_b64_tr_b16 v[176:177], v244 offset:0\n\t" \
    "ds_read_b64_tr_b16 v[178:179], v244 offset:1536\n\t" \
    "ds_read_b64_tr_b16 v[184:185], v244 offset:32\n\t" \
    "ds_read_b64_tr_b16 v[186:187], v244 offset:1568\n\t" \
    "ds_read_b64_tr_b16 v[192:193], v244 offset:64\n\t" \
    "ds_read_b64_tr_b16 v[194:195], v244 offset:1600\n\t" \
    "v_add_u32 v244, 0x3000, v243\n\t" \
    "s_waitcnt vmcnt(12) lgkmcnt(0)\n\t" \
    "s_barrier \n\t" \
    "ds_read_b64_tr_b16 v[180:181], v244 offset:0\n\t" \
    "ds_read_b64_tr_b16 v[182:183], v244 offset:1536\n\t" \
    "ds_read_b64_tr_b16 v[188:189], v244 offset:32\n\t" \
    "ds_read_b64_tr_b16 v[190:191], v244 offset:1568\n\t" \
    "ds_read_b64_tr_b16 v[196:197], v244 offset:64\n\t" \
    "ds_read_b64_tr_b16 v[198:199], v244 offset:1600\n\t" \
    "v_add_u32 v244, 0x6000, v243\n\t" \
    "s_waitcnt vmcnt(9) lgkmcnt(0)\n\t" \
    "s_barrier \n\t" \
    "ds_read_b64_tr_b16 v[200:201], v244 offset:0\n\t" \
    "ds_read_b64_tr_b16 v[202:203], v244 offset:1536\n\t" \
    "ds_read_b64_tr_b16 v[0:1], v244 offset:32\n\t" \
    "ds_read_b64_tr_b16 v[2:3], v244 offset:1568\n\t" \
    "ds_read_b64_tr_b16 v[8:9], v244 offset:64\n\t" \
    "ds_read_b64_tr_b16 v[10:11], v244 offset:1600\n\t" \
    "v_add_u32 v244, 0x9000, v243\n\t" \
    "s_waitcnt vmcnt(6) lgkmcnt(0)\n\t" \
    "s_barrier \n\t" \
    "ds_read_b64_tr_b16 v[204:205], v244 offset:0\n\t" \
    "ds_read_b64_tr_b16 v[206:207], v244 offset:1536\n\t" \
    "ds_read_b64_tr_b16 v[4:5], v244 offset:32\n\t" \
    "ds_read_b64_tr_b16 v[6:7], v244 offset:1568\n\t" \
    "ds_read_b64_tr_b16 v[12:13], v244 offset:64\n\t" \
    "ds_read_b64_tr_b16 v[14:15], v244 offset:1600\n\t" \
    "v_add_u32 v244, 0xc000, v243\n\t" \
    "s_waitcnt vmcnt(3) lgkmcnt(0)\n\t" \
    "s_barrier \n\t" \
    "ds_read_b64_tr_b16 v[16:17], v244 offset:0\n\t" \
    "ds_read_b64_tr_b16 v[18:19], v244 offset:1536\n\t" \
    "ds_read_b64_tr_b16 v[24:25], v244 offset:32\n\t" \
    "ds_read_b64_tr_b16 v[26:27], v244 offset:1568\n\t" \
    "ds_read_b64_tr_b16 v[32:33], v244 offset:64\n\t" \
    "ds_read_b64_tr_b16 v[34:35], v244 offset:1600\n\t" \
    "s_mov_b32 s43, 0\n\t" \
    "v_cmp_eq_u32 vcc, 0, v252\n\t" \
    "s_cbranch_vccnz .Lv33_%=\n\t" \
    "s_mov_b32 m0, 0x3000\n\t" \
    "s_add_u32 s49, s42, 0x0\n\t" \
    "buffer_load_dwordx4 v253, s[60:63], s49 offen offset:0 sc1 lds\n\t" \
    "buffer_load_dwordx4 v253, s[60:63], s49 offen offset:1024 sc1 lds\n\t" \
    "buffer_load_dwordx4 v253, s[60:63], s49 offen offset:2048 sc1 lds\n\t" \
    "buffer_load_dwordx4 v253, s[60:63], s49 offen offset:3072 sc1 lds\n\t" \
    "s_mov_b32 m0, 0x4c00\n\t" \
    "s_add_u32 s49, s42, 0x4c00\n\t" \
    "buffer_load_dwordx4 v253, s[60:63], s49 offen offset:0 sc1 lds\n\t" \
    "s_mov_b32 m0, 0x8000\n\t" \
    "s_add_u32 s49, s42, 0x1000\n\t" \
    "buffer_load_dwordx4 v253, s[60:63], s49 offen offset:0 sc1 lds\n\t" \
    "buffer_load_dwordx4 v253, s[60:63], s49 offen offset:1024 sc1 lds\n\t" \
    "buffer_load_dwordx4 v253, s[60:63], s49 offen offset:2048 sc1 lds\n\t" \
    "buffer_load_dwordx4 v253, s[60:63], s49 offen offset:3072 sc1 lds\n\t" \
    "s_mov_b32 m0, 0x9c00\n\t" \
    "s_add_u32 s49, s42, 0x5c00\n\t" \
    "buffer_load_dwordx4 v253, s[60:63], s49 offen offset:0 sc1 lds\n\t" \
    "s_mov_b32 s43, 1\n\t" \
    ".Lv33_%=:\n\t" \
    "v_add_u32 v244, 0xf000, v243\n\t" \
    "s_waitcnt vmcnt(0) lgkmcnt(0)\n\t" \
    "s_barrier \n\t" \
    "ds_read_b64_tr_b16 v[20:21], v244 offset:0\n\t" \
    "ds_read_b64_tr_b16 v[22:23], v244 offset:1536\n\t" \
    "ds_read_b64_tr_b16 v[28:29], v244 offset:32\n\t" \
    "ds_read_b64_tr_b16 v[30:31], v244 offset:1568\n\t" \
    "ds_read_b64_tr_b16 v[36:37], v244 offset:64\n\t" \
    "ds_read_b64_tr_b16 v[38:39], v244 offset:1600\n\t" \
    "s_waitcnt lgkmcnt(0)\n\t" \
    "s_barrier \n\t" \
    LST0 \
    LJIT \
    "ds_write_b128 v254, v[200:203] offset:55296\n\t" \
    "ds_write_b128 v254, v[204:207] offset:56320\n\t" \
    "ds_write_b128 v254, v[208:211] offset:57344\n\t" \
    "ds_write_b128 v254, v[212:215] offset:58368\n\t" \
    "ds_write_b128 v254, v[216:219] offset:59392\n\t" \
    "ds_write_b128 v254, v[220:223] offset:60416\n\t" \
    "ds_write_b128 v254, v[224:227] offset:61440\n\t" \
    "ds_write_b128 v254, v[228:231] offset:62464\n\t" \
    "ds_write_b128 v254, v[232:235] offset:63488\n\t" \
    "ds_write_b128 v254, v[236:239] offset:64512\n\t" \
    "s_cmp_lg_u32 s43, 0\n\t" \
    "s_cbranch_scc1 .Lv34_%=\n\t" \
    "s_mov_b32 s44, 0\n\t" \
    ".Lv35_%=:\n\t" \
    "v_cmp_ge_u32 vcc, s44, v252\n\t" \
    "s_cbranch_vccz .Lv36_%=\n\t" \
    "s_cmp_eq_u32 s41, 0\n\t" \
    "s_cbranch_scc1 .Lv36_%=\n\t" \
    "s_sub_u32 s41, s41, 1\n\t" \
    "s_sleep 8\n\t" \
    "v_and_b32 v252, 0xf0, v253\n\t" \
    "v_lshlrev_b32 v252, 3, v252\n\t" \
    "buffer_load_dword v252, v252, s[60:63], %[fl] offen sc1\n\t" \
    "s_waitcnt vmcnt(0)\n\t" \
    "s_branch .Lv35_%=\n\t" \
    ".Lv36_%=:\n\t" \
    "s_mov_b32 m0, 0x3000\n\t" \
    "s_add_u32 s49, s42, 0x0\n\t" \
    "buffer_load_dwordx4 v253, s[60:63], s49 offen offset:0 sc1 lds\n\t" \
    "buffer_load_dwordx4 v253, s[60:63], s49 offen offset:1024 sc1 lds\n\t" \
    "buffer_load_dwordx4 v253, s[60:63], s49 offen offset:2048 sc1 lds\n\t" \
    "buffer_load_dwordx4 v253, s[60:63], s49 offen offset:3072 sc1 lds\n\t" \
    "s_mov_b32 m0, 0x4c00\n\t" \
    "s_add_u32 s49, s42, 0x4c00\n\t" \
    "buffer_load_dwordx4 v253, s[60:63], s49 offen offset:0 sc1 lds\n\t" \
    "s_mov_b32 m0, 0x8000\n\t" \
    "s_add_u32 s49, s42, 0x1000\n\t" \
    "buffer_load_dwordx4 v253, s[60:63], s49 offen offset:0 sc1 lds\n\t" \
    "buffer_load_dwordx4 v253, s[60:63], s49 offen offset:1024 sc1 lds\n\t" \
    "buffer_load_dwordx4 v253, s[60:63], s49 offen offset:2048 sc1 lds\n\t" \
    "buffer_load_dwordx4 v253, s[60:63], s49 offen offset:3072 sc1 lds\n\t" \
    "s_mov_b32 m0, 0x9c00\n\t" \
    "s_add_u32 s49, s42, 0x5c00\n\t" \
    "buffer_load_dwordx4 v253, s[60:63], s49 offen offset:0 sc1 lds\n\t" \
    ".Lv34_%=:\n\t" \
    "s_waitcnt vmcnt(0) lgkmcnt(0)\n\t" \
    "s_barrier \n\t" \
    "ds_read_b128 v[224:227], v253 offset:16384\n\t" \
    "ds_read_b128 v[244:247], v254 offset:55296\n\t" \
    "ds_read_b128 v[228:231], v253 offset:17408\n\t" \
    "ds_read_b128 v[232:235], v253 offset:36864\n\t" \
    "ds_read_b128 v[236:239], v253 offset:37888\n\t" \
    "ds_read_b128 v[248:251], v254 offset:56320\n\t" \
    ".p2align 8\n\t" \
    ".Lv37_%=:\n\t" \
    LST7 \
    LJIT \
    "s_waitcnt lgkmcnt(5)\n\t" \
    "v_mfma_f32_16x16x32_bf16 v[204:207], v[0:3], v[224:227], 0\n\t" \
    "s_waitcnt lgkmcnt(3)\n\t" \
    "v_mfma_f32_16x16x32_bf16 v[204:207], v[4:7], v[228:231], v[204:207]\n\t" \
    "ds_read_b128 v[240:243], v253 offset:18432\n\t" \
    "v_mfma_f32_16x16x32_bf16 v[208:211], v[8:11], v[224:227], 0\n\t" \
    "v_mfma_f32_16x16x32_bf16 v[208:211], v[12:15], v[228:231], v[208:211]\n\t" \
    "v_mfma_f32_16x16x32_bf16 v[200:203], v[244:247], v[224:227], 0\n\t" \
    "s_waitcnt lgkmcnt(1)\n\t" \
    "v_mfma_f32_16x16x32_bf16 v[200:203], v[248:251], v[228:231], v[200:203]\n\t" \
    "v_mfma_f32_16x16x32_bf16 v[216:219], v[0:3], v[232:235], 0\n\t" \
    "ds_read_b128 v[224:227], v253 offset:19456\n\t" \
    "v_mfma_f32_16x16x32_bf16 v[216:219], v[4:7], v[236:239], v[216:219]\n\t" \
    "ds_read_b128 v[228:231], v253 offset:0\n\t" \
    "v_mfma_f32_16x16x32_bf16 v[220:223], v[8:11], v[232:235], 0\n\t" \
    "v_mfma_f32_16x16x32_bf16 v[220:223], v[12:15], v[236:239], v[220:223]\n\t" \
    "v_mfma_f32_16x16x32_bf16 v[212:215], v[244:247], v[232:235], 0\n\t" \
    "ds_read_b128 v[244:247], v254 offset:57344\n\t" \
    "v_mfma_f32_16x16x32_bf16 v[212:215], v[248:251], v[236:239], v[212:215]\n\t" \
    "ds_read_b128 v[248:251], v254 offset:58368\n\t" \
    "s_waitcnt lgkmcnt(4)\n\t" \
    "v_mfma_f32_16x16x32_bf16 v[204:207], v[24:27], v[240:243], v[204:207]\n\t" \
    "ds_read_b128 v[232:235], v253 offset:1024\n\t" \
    "s_waitcnt lgkmcnt(4)\n\t" \
    "v_mfma_f32_16x16x32_bf16 v[204:207], v[28:31], v[224:227], v[204:207]\n\t" \
    "ds_read_b128 v[236:239], v253 offset:20480\n\t" \
    "v_mfma_f32_16x16x32_bf16 v[208:211], v[32:35], v[240:243], v[208:211]\n\t" \
    "v_mfma_f32_16x16x32_bf16 v[208:211], v[36:39], v[224:227], v[208:211]\n\t" \
    "v_mfma_f32_16x16x32_bf16 v[200:203], v[16:19], v[240:243], v[200:203]\n\t" \
    "v_mfma_f32_16x16x32_bf16 v[200:203], v[20:23], v[224:227], v[200:203]\n\t" \
    "s_waitcnt lgkmcnt(4)\n\t" \
    "v_mfma_f32_16x16x32_bf16 v[204:207], v[40:43], v[228:231], v[204:207]\n\t" \
    "ds_read_b128 v[240:243], v253 offset:21504\n\t" \
    "s_waitcnt lgkmcnt(2)\n\t" \
    "v_mfma_f32_16x16x32_bf16 v[204:207], v[44:47], v[232:235], v[204:207]\n\t" \
    "ds_read_b128 v[224:227], v253 offset:2048\n\t" \
    "v_mfma_f32_16x16x32_bf16 v[208:211], v[48:51], v[228:231], v[208:211]\n\t" \
    "v_mfma_f32_16x16x32_bf16 v[208:211], v[52:55], v[232:235], v[208:211]\n\t" \
    "v_mfma_f32_16x16x32_bf16 v[200:203], v[244:247], v[228:231], v[200:203]\n\t" \
    "v_mfma_f32_16x16x32_bf16 v[200:203], v[248:251], v[232:235], v[200:203]\n\t" \
    "s_waitcnt lgkmcnt(2)\n\t" \
    "v_mfma_f32_16x16x32_bf16 v[216:219], v[40:43], v[236:239], v[216:219]\n\t" \
    "ds_read_b128 v[228:231], v253 offset:3072\n\t" \
    "s_add_u32 s44, s40, 1\n\t" \
    "s_waitcnt lgkmcnt(2)\n\t" \
    "v_mfma_f32_16x16x32_bf16 v[216:219], v[44:47], v[240:243], v[216:219]\n\t" \
    "ds_read_b128 v[232:235], v253 offset:4096\n\t" \
    "s_and_b32 s45, s44, 15\n\t" \
    "v_mfma_f32_16x16x32_bf16 v[220:223], v[48:51], v[236:239], v[220:223]\n\t" \
    "s_cmp_eq_u32 s45, 0\n\t" \
    "v_mfma_f32_16x16x32_bf16 v[220:223], v[52:55], v[240:243], v[220:223]\n\t" \
    "s_cselect_b32 s45, %[cj], %[cs]\n\t" \
    "v_mfma_f32_16x16x32_bf16 v[212:215], v[244:247], v[236:239], v[212:215]\n\t" \
    "ds_read_b128 v[244:247], v254 offset:59392\n\t" \
    "s_cmp_eq_u32 s44, 64\n\t" \
    "v_mfma_f32_16x16x32_bf16 v[212:215], v[248:251], v[240:243], v[212:215]\n\t" \
    "ds_read_b128 v[248:251], v254 offset:60416\n\t" \
    "s_cselect_b32 s45, 0, s45\n\t" \
    "s_waitcnt lgkmcnt(4)\n\t" \
    "v_mfma_f32_16x16x32_bf16 v[204:207], v[64:67], v[224:227], v[204:207]\n\t" \
    "ds_read_b128 v[236:239], v253 offset:5120\n\t" \
    "s_add_u32 s42, s42, s45\n\t" \
    "s_waitcnt lgkmcnt(4)\n\t" \
    "v_mfma_f32_16x16x32_bf16 v[204:207], v[68:71], v[228:231], v[204:207]\n\t" \
    "ds_read_b128 v[240:243], v253 offset:24576\n\t" \
    "v_mfma_f32_16x16x32_bf16 v[208:211], v[72:75], v[224:227], v[208:211]\n\t" \
    "v_mfma_f32_16x16x32_bf16 v[208:211], v[76:79], v[228:231], v[208:211]\n\t" \
    "v_mfma_f32_16x16x32_bf16 v[200:203], v[56:59], v[224:227], v[200:203]\n\t" \
    "v_mfma_f32_16x16x32_bf16 v[200:203], v[60:63], v[228:231], v[200:203]\n\t" \
    "s_waitcnt lgkmcnt(4)\n\t" \
    "v_mfma_f32_16x16x32_bf16 v[204:207], v[80:83], v[232:235], v[204:207]\n\t" \
    "ds_read_b128 v[224:227], v253 offset:25600\n\t" \
    "s_waitcnt lgkmcnt(2)\n\t" \
    "v_mfma_f32_16x16x32_bf16 v[204:207], v[84:87], v[236:239], v[204:207]\n\t" \
    "ds_read_b128 v[228:231], v253 offset:6144\n\t" \
    "v_mfma_f32_16x16x32_bf16 v[208:211], v[88:91], v[232:235], v[208:211]\n\t" \
    "v_mfma_f32_16x16x32_bf16 v[208:211], v[92:95], v[236:239], v[208:211]\n\t" \
    "v_mfma_f32_16x16x32_bf16 v[200:203], v[244:247], v[232:235], v[200:203]\n\t" \
    "v_mfma_f32_16x16x32_bf16 v[200:203], v[248:251], v[236:239], v[200:203]\n\t" \
    "s_waitcnt lgkmcnt(2)\n\t" \
    "v_mfma_f32_16x16x32_bf16 v[216:219], v[80:83], v[240:243], v[216:219]\n\t" \
    "ds_read_b128 v[232:235], v253 offset:7168\n\t" \
    "s_and_b32 s44, s40, 3\n\t" \
    "s_waitcnt lgkmcnt(2)\n\t" \
    "v_mfma_f32_16x16x32_bf16 v[216:219], v[84:87], v[224:227], v[216:219]\n\t" \
    "ds_read_b128 v[236:239], v253 offset:8192\n\t" \
    "s_lshl_b32 s44, s44, 22\n\t" \
    "v_mfma_f32_16x16x32_bf16 v[220:223], v[88:91], v[240:243], v[220:223]\n\t" \
    "s_add_u32 s44, s44, %[mb]\n\t" \
    "v_mfma_f32_16x16x32_bf16 v[220:223], v[92:95], v[224:227], v[220:223]\n\t" \
    "s_add_u32 s46, s44, 0x10000\n\t" \
    "v_mfma_f32_16x16x32_bf16 v[212:215], v[244:247], v[240:243], v[212:215]\n\t" \
    "ds_read_b128 v[244:247], v254 offset:61440\n\t" \
    "s_add_u32 s47, s44, 0x10400\n\t" \
    "v_mfma_f32_16x16x32_bf16 v[212:215], v[248:251], v[224:227], v[212:215]\n\t" \
    "ds_read_b128 v[248:251], v254 offset:62464\n\t" \
    "s_waitcnt lgkmcnt(4)\n\t" \
    "v_mfma_f32_16x16x32_bf16 v[204:207], v[104:107], v[228:231], v[204:207]\n\t" \
    "ds_read_b128 v[240:243], v253 offset:9216\n\t" \
    "s_waitcnt lgkmcnt(4)\n\t" \
    "v_mfma_f32_16x16x32_bf16 v[204:207], v[108:111], v[232:235], v[204:207]\n\t" \
    "ds_read_b128 v[224:227], v253 offset:28672\n\t" \
    "v_mfma_f32_16x16x32_bf16 v[208:211], v[112:115], v[228:231], v[208:211]\n\t" \
    "v_mfma_f32_16x16x32_bf16 v[208:211], v[116:119], v[232:235], v[208:211]\n\t" \
    "v_mfma_f32_16x16x32_bf16 v[200:203], v[96:99], v[228:231], v[200:203]\n\t" \
    "v_mfma_f32_16x16x32_bf16 v[200:203], v[100:103], v[232:235], v[200:203]\n\t" \
    "s_waitcnt lgkmcnt(4)\n\t" \
    "v_mfma_f32_16x16x32_bf16 v[204:207], v[120:123], v[236:239], v[204:207]\n\t" \
    "ds_read_b128 v[228:231], v253 offset:29696\n\t" \
    "s_waitcnt lgkmcnt(2)\n\t" \
    "v_mfma_f32_16x16x32_bf16 v[204:207], v[124:127], v[240:243], v[204:207]\n\t" \
    "ds_read_b128 v[232:235], v253 offset:10240\n\t" \
    "v_mfma_f32_16x16x32_bf16 v[208:211], v[128:131], v[236:239], v[208:211]\n\t" \
    "v_mfma_f32_16x16x32_bf16 v[208:211], v[132:135], v[240:243], v[208:211]\n\t" \
    "v_mfma_f32_16x16x32_bf16 v[200:203], v[244:247], v[236:239], v[200:203]\n\t" \
    "v_mfma_f32_16x16x32_bf16 v[200:203], v[248:251], v[240:243], v[200:203]\n\t" \
    "s_waitcnt lgkmcnt(2)\n\t" \
    "v_mfma_f32_16x16x32_bf16 v[216:219], v[120:123], v[224:227], v[216:219]\n\t" \
    "ds_read_b128 v[236:239], v253 offset:11264\n\t" \
    "s_waitcnt lgkmcnt(2)\n\t" \
    "v_mfma_f32_16x16x32_bf16 v[216:219], v[124:127], v[228:231], v[216:219]\n\t" \
    "ds_read_b128 v[240:243], v253 offset:12288\n\t" \
    "v_mfma_f32_16x16x32_bf16 v[220:223], v[128:131], v[224:227], v[220:223]\n\t" \
    "v_mfma_f32_16x16x32_bf16 v[220:223], v[132:135], v[228:231], v[220:223]\n\t" \
    "v_mfma_f32_16x16x32_bf16 v[212:215], v[244:247], v[224:227], v[212:215]\n\t" \
    "ds_read_b128 v[244:247], v254 offset:63488\n\t" \
    "v_mfma_f32_16x16x32_bf16 v[212:215], v[248:251], v[228:231], v[212:215]\n\t" \
    "ds_read_b128 v[248:251], v254 offset:64512\n\t" \
    "v_and_b32 v252, 0xf0, v253\n\t" \
    "v_lshlrev_b32 v252, 3, v252\n\t" \
    "buffer_load_dword v252, v252, s[60:63], %[fl] offen sc1\n\t" \
    "s_waitcnt lgkmcnt(4)\n\t" \
    "v_mfma_f32_16x16x32_bf16 v[204:207], v[144:147], v[232:235], v[204:207]\n\t" \
    "ds_read_b128 v[224:227], v253 offset:13312\n\t" \
    "s_waitcnt lgkmcnt(4)\n\t" \
    "v_mfma_f32_16x16x32_bf16 v[204:207], v[148:151], v[236:239], v[204:207]\n\t" \
    "ds_read_b128 v[228:231], v253 offset:32768\n\t" \
    "v_mfma_f32_16x16x32_bf16 v[208:211], v[152:155], v[232:235], v[208:211]\n\t" \
    "v_mfma_f32_16x16x32_bf16 v[208:211], v[156:159], v[236:239], v[208:211]\n\t" \
    "v_mfma_f32_16x16x32_bf16 v[200:203], v[136:139], v[232:235], v[200:203]\n\t" \
    "v_mfma_f32_16x16x32_bf16 v[200:203], v[140:143], v[236:239], v[200:203]\n\t" \
    "s_waitcnt lgkmcnt(4)\n\t" \
    "v_mfma_f32_16x16x32_bf16 v[204:207], v[160:163], v[240:243], v[204:207]\n\t" \
    "ds_read_b128 v[232:235], v253 offset:33792\n\t" \
    "s_waitcnt lgkmcnt(2)\n\t" \
    "v_mfma_f32_16x16x32_bf16 v[204:207], v[164:167], v[224:227], v[204:207]\n\t" \
    "ds_read_b128 v[236:239], v253 offset:14336\n\t" \
    "v_mfma_f32_16x16x32_bf16 v[208:211], v[168:171], v[240:243], v[208:211]\n\t" \
    "v_mfma_f32_16x16x32_bf16 v[208:211], v[172:175], v[224:227], v[208:211]\n\t" \
    "v_mfma_f32_16x16x32_bf16 v[200:203], v[244:247], v[240:243], v[200:203]\n\t" \
    "v_mfma_f32_16x16x32_bf16 v[200:203], v[248:251], v[224:227], v[200:203]\n\t" \
    "s_waitcnt lgkmcnt(2)\n\t" \
    "v_mfma_f32_16x16x32_bf16 v[216:219], v[160:163], v[228:231], v[216:219]\n\t" \
    "ds_read_b128 v[240:243], v253 offset:15360\n\t" \
    "s_waitcnt lgkmcnt(2)\n\t" \
    "v_mfma_f32_16x16x32_bf16 v[216:219], v[164:167], v[232:235], v[216:219]\n\t" \
    "ds_read_b128 v[224:227], v253 offset:38912\n\t" \
    "v_mfma_f32_16x16x32_bf16 v[220:223], v[168:171], v[228:231], v[220:223]\n\t" \
    "v_mfma_f32_16x16x32_bf16 v[220:223], v[172:175], v[232:235], v[220:223]\n\t" \
    "v_mfma_f32_16x16x32_bf16 v[212:215], v[244:247], v[228:231], v[212:215]\n\t" \
    "v_mfma_f32_16x16x32_bf16 v[212:215], v[248:251], v[232:235], v[212:215]\n\t" \
    "s_waitcnt lgkmcnt(2)\n\t" \
    "v_mfma_f32_16x16x32_bf16 v[204:207], v[184:187], v[236:239], v[204:207]\n\t" \
    "ds_read_b128 v[228:231], v253 offset:39936\n\t" \
    "s_waitcnt lgkmcnt(2)\n\t" \
    "v_mfma_f32_16x16x32_bf16 v[204:207], v[188:191], v[240:243], v[204:207]\n\t" \
    "ds_read_b128 v[232:235], v253 offset:22528\n\t" \
    "v_mfma_f32_16x16x32_bf16 v[208:211], v[192:195], v[236:239], v[208:211]\n\t" \
    "v_mfma_f32_16x16x32_bf16 v[208:211], v[196:199], v[240:243], v[208:211]\n\t" \
    "v_mfma_f32_16x16x32_bf16 v[200:203], v[176:179], v[236:239], v[200:203]\n\t" \
    "v_mfma_f32_16x16x32_bf16 v[200:203], v[180:183], v[240:243], v[200:203]\n\t" \
    "s_waitcnt lgkmcnt(2)\n\t" \
    "v_mfma_f32_16x16x32_bf16 v[216:219], v[24:27], v[224:227], v[216:219]\n\t" \
    "ds_read_b128 v[236:239], v253 offset:23552\n\t" \
    "s_waitcnt lgkmcnt(2)\n\t" \
    "v_mfma_f32_16x16x32_bf16 v[216:219], v[28:31], v[228:231], v[216:219]\n\t" \
    "ds_read_b128 v[240:243], v253 offset:26624\n\t" \
    "v_mfma_f32_16x16x32_bf16 v[220:223], v[32:35], v[224:227], v[220:223]\n\t" \
    "v_cvt_pk_bf16_f32 v204, v204, v205\n\t" \
    "v_mfma_f32_16x16x32_bf16 v[220:223], v[36:39], v[228:231], v[220:223]\n\t" \
    "v_cvt_pk_bf16_f32 v205, v206, v207\n\t" \
    "v_mfma_f32_16x16x32_bf16 v[212:215], v[16:19], v[224:227], v[212:215]\n\t" \
    "ds_write_b64 v254, v[204:205] offset:4104\n\t" \
    "v_mfma_f32_16x16x32_bf16 v[212:215], v[20:23], v[228:231], v[212:215]\n\t" \
    "v_cvt_pk_bf16_f32 v208, v208, v209\n\t" \
    "s_waitcnt lgkmcnt(3)\n\t" \
    "v_mfma_f32_16x16x32_bf16 v[216:219], v[64:67], v[232:235], v[216:219]\n\t" \
    "ds_read_b128 v[224:227], v253 offset:27648\n\t" \
    "v_cvt_pk_bf16_f32 v209, v210, v211\n\t" \
    "s_waitcnt lgkmcnt(3)\n\t" \
    "v_mfma_f32_16x16x32_bf16 v[216:219], v[68:71], v[236:239], v[216:219]\n\t" \
    "ds_read_b128 v[228:231], v253 offset:30720\n\t" \
    "ds_write_b64 v254, v[208:209] offset:5128\n\t" \
    "v_mfma_f32_16x16x32_bf16 v[220:223], v[72:75], v[232:235], v[220:223]\n\t" \
    "v_cvt_pk_bf16_f32 v200, v200, v201\n\t" \
    "v_mfma_f32_16x16x32_bf16 v[220:223], v[76:79], v[236:239], v[220:223]\n\t" \
    "v_cvt_pk_bf16_f32 v201, v202, v203\n\t" \
    "v_mfma_f32_16x16x32_bf16 v[212:215], v[56:59], v[232:235], v[212:215]\n\t" \
    "ds_write_b64 v254, v[200:201] offset:3080\n\t" \
    "v_mfma_f32_16x16x32_bf16 v[212:215], v[60:63], v[236:239], v[212:215]\n\t" \
    "s_waitcnt lgkmcnt(5)\n\t" \
    "v_mfma_f32_16x16x32_bf16 v[216:219], v[104:107], v[240:243], v[216:219]\n\t" \
    "ds_read_b128 v[232:235], v253 offset:31744\n\t" \
    "s_waitcnt lgkmcnt(4)\n\t" \
    "v_mfma_f32_16x16x32_bf16 v[216:219], v[108:111], v[224:227], v[216:219]\n\t" \
    "ds_read_b128 v[236:239], v253 offset:34816\n\t" \
    "v_mfma_f32_16x16x32_bf16 v[220:223], v[112:115], v[240:243], v[220:223]\n\t" \
    "v_mfma_f32_16x16x32_bf16 v[220:223], v[116:119], v[224:227], v[220:223]\n\t" \
    "v_mfma_f32_16x16x32_bf16 v[212:215], v[96:99], v[240:243], v[212:215]\n\t" \
    "v_mfma_f32_16x16x32_bf16 v[212:215], v[100:103], v[224:227], v[212:215]\n\t" \
    "ds_read_b128 v[240:243], v253 offset:35840\n\t" \
    "s_waitcnt lgkmcnt(3)\n\t" \
    "s_barrier \n\t" \
    "v_mfma_f32_16x16x32_bf16 v[216:219], v[144:147], v[228:231], v[216:219]\n\t" \
    "s_waitcnt lgkmcnt(2)\n\t" \
    "v_mfma_f32_16x16x32_bf16 v[216:219], v[148:151], v[232:235], v[216:219]\n\t" \
    "s_waitcnt lgkmcnt(1)\n\t" \
    "v_mfma_f32_16x16x32_bf16 v[216:219], v[184:187], v[236:239], v[216:219]\n\t" \
    "s_waitcnt lgkmcnt(0)\n\t" \
    "v_mfma_f32_16x16x32_bf16 v[216:219], v[188:191], v[240:243], v[216:219]\n\t" \
    "v_mfma_f32_16x16x32_bf16 v[220:223], v[152:155], v[228:231], v[220:223]\n\t" \
    "v_mfma_f32_16x16x32_bf16 v[220:223], v[156:159], v[232:235], v[220:223]\n\t" \
    "v_mfma_f32_16x16x32_bf16 v[220:223], v[192:195], v[236:239], v[220:223]\n\t" \
    "v_cvt_pk_bf16_f32 v216, v216, v217\n\t" \
    "v_cvt_pk_bf16_f32 v217, v218, v219\n\t" \
    "v_mfma_f32_16x16x32_bf16 v[220:223], v[196:199], v[240:243], v[220:223]\n\t" \
    "ds_write_b64 v254, v[216:217] offset:10248\n\t" \
    "v_mfma_f32_16x16x32_bf16 v[212:215], v[136:139], v[228:231], v[212:215]\n\t" \
    "v_mfma_f32_16x16x32_bf16 v[212:215], v[140:143], v[232:235], v[212:215]\n\t" \
    "v_mfma_f32_16x16x32_bf16 v[212:215], v[176:179], v[236:239], v[212:215]\n\t" \
    "v_cvt_pk_bf16_f32 v220, v220, v221\n\t" \
    "v_cvt_pk_bf16_f32 v221, v222, v223\n\t" \
    "v_mfma_f32_16x16x32_bf16 v[212:215], v[180:183], v[240:243], v[212:215]\n\t" \
    "ds_write_b64 v254, v[220:221] offset:11272\n\t" \
    "s_nop 6\n\t" \
    "v_cvt_pk_bf16_f32 v212, v212, v213\n\t" \
    "v_cvt_pk_bf16_f32 v213, v214, v215\n\t" \
    "ds_write_b64 v254, v[212:213] offset:9224\n\t" \
    LST6 \
    LJIT \
    "s_waitcnt vmcnt(0) lgkmcnt(0)\n\t" \
    "s_barrier \n\t" \
    "s_setprio 2\n\t" \
    "s_add_u32 s44, s40, 1\n\t" \
    "s_min_u32 s44, s44, 63\n\t" \
    ".Lv38_%=:\n\t" \
    "v_cmp_ge_u32 vcc, s44, v252\n\t" \
    "s_cbranch_vccz .Lv39_%=\n\t" \
    "s_cmp_eq_u32 s41, 0\n\t" \
    "s_cbranch_scc1 .Lv39_%=\n\t" \
    "s_sub_u32 s41, s41, 1\n\t" \
    "s_sleep 2\n\t" \
    "v_and_b32 v252, 0xf0, v253\n\t" \
    "v_lshlrev_b32 v252, 3, v252\n\t" \
    "buffer_load_dword v252, v252, s[60:63], %[fl] offen sc1\n\t" \
    "s_waitcnt vmcnt(0)\n\t" \
    "s_branch .Lv38_%=\n\t" \
    ".Lv39_%=:\n\t" \
    "s_mov_b32 m0, 0x3000\n\t" \
    "s_add_u32 s49, s42, 0x0\n\t" \
    "buffer_load_dwordx4 v253, s[60:63], s49 offen offset:0 sc1 lds\n\t" \
    "s_sleep 1\n\t" \
    "buffer_load_dwordx4 v253, s[60:63], s49 offen offset:1024 sc1 lds\n\t" \
    "s_sleep 1\n\t" \
    "buffer_load_dwordx4 v253, s[60:63], s49 offen offset:2048 sc1 lds\n\t" \
    "s_sleep 1\n\t" \
    "buffer_load_dwordx4 v253, s[60:63], s49 offen offset:3072 sc1 lds\n\t" \
    "s_sleep 1\n\t" \
    "s_mov_b32 m0, 0x4c00\n\t" \
    "s_add_u32 s49, s42, 0x4c00\n\t" \
    "buffer_load_dwordx4 v253, s[60:63], s49 offen offset:0 sc1 lds\n\t" \
    "s_sleep 1\n\t" \
    "s_mov_b32 m0, 0x8000\n\t" \
    "s_add_u32 s49, s42, 0x1000\n\t" \
    "buffer_load_dwordx4 v253, s[60:63], s49 offen offset:0 sc1 lds\n\t" \
    "s_sleep 1\n\t" \
    "buffer_load_dwordx4 v253, s[60:63], s49 offen offset:1024 sc1 lds\n\t" \
    "s_sleep 1\n\t" \
    "buffer_load_dwordx4 v253, s[60:63], s49 offen offset:2048 sc1 lds\n\t" \
    "s_sleep 1\n\t" \
    "buffer_load_dwordx4 v253, s[60:63], s49 offen offset:3072 sc1 lds\n\t" \
    "s_sleep 1\n\t" \
    "s_mov_b32 m0, 0x9c00\n\t" \
    "s_add_u32 s49, s42, 0x5c00\n\t" \
    "buffer_load_dwordx4 v253, s[60:63], s49 offen offset:0 sc1 lds\n\t" \
    "s_sleep 1\n\t" \
    "ds_read_b128 v[200:203], v254 offset:55296\n\t" \
    "ds_read_b128 v[204:207], v254 offset:56320\n\t" \
    "ds_read_b128 v[216:219], v254 offset:57344\n\t" \
    "ds_read_b128 v[220:223], v254 offset:58368\n\t" \
    "v_mov_b64 v[208:209], 0\n\t" \
    "v_mov_b64 v[210:211], 0\n\t" \
    "v_mov_b64 v[244:245], 0\n\t" \
    "v_mov_b64 v[246:247], 0\n\t" \
    "v_mov_b64 v[248:249], 0\n\t" \
    "v_mov_b64 v[250:251], 0\n\t" \
    LST1 \
    LJIT \
    "s_sleep 5\n\t" \
    "s_mov_b32 m0, 0xc000\n\t" \
    "s_nop 0\n\t" \
    "buffer_load_dwordx4 v253, s[60:63], s46 offen sc1 lds\n\t" \
    "s_mov_b32 m0, 0xc400\n\t" \
    "s_nop 0\n\t" \
    "buffer_load_dwordx4 v253, s[60:63], s47 offen sc1 lds\n\t" \
    "s_waitcnt lgkmcnt(0)\n\t" \
    LFLUSH212 \
    "v_bfe_i32 v212, v253, 5, 1\n\t" \
    "v_and_b32 v212, 0xaaaa, v212\n\t" \
    "v_or_b32 v212, 0x4444, v212\n\t" \
    "v_and_b32 v213, 0xffff, v255\n\t" \
    "v_lshrrev_b32 v214, 16, v255\n\t" \
    ".Lv310_%=:\n\t" \
    "s_waitcnt vmcnt(0)\n\t" \
    "ds_read_b128 v[224:227], v253 offset:49152\n\t" \
    "ds_read_b128 v[228:231], v253 offset:50176\n\t" \
    "s_waitcnt lgkmcnt(1)\n\t" \
    "v_max_u32 v215, v224, v225\n\t" \
    "v_max3_u32 v215, v226, v227, v215\n\t" \
    "s_waitcnt lgkmcnt(0)\n\t" \
    "v_max3_u32 v215, v228, v229, v215\n\t" \
    "v_max3_u32 v215, v230, v231, v215\n\t" \
    "v_cmp_eq_u32 vcc, -1, v215\n\t" \
    "s_cbranch_vccz .Lv311_%=\n\t" \
    "v_max_u32 v215, v224, v225\n\t" \
    "v_max3_u32 v215, v226, v227, v215\n\t" \
    "v_cmp_eq_u32 vcc, -1, v215\n\t" \
    "s_cbranch_vccz .Lv312_%=\n\t" \
    "s_mov_b32 m0, 0xc000\n\t" \
    "s_nop 0\n\t" \
    "buffer_load_dwordx4 v253, s[60:63], s46 offen sc1 lds\n\t" \
    ".Lv312_%=:\n\t" \
    "v_max_u32 v215, v228, v229\n\t" \
    "v_max3_u32 v215, v230, v231, v215\n\t" \
    "v_cmp_eq_u32 vcc, -1, v215\n\t" \
    "s_cbranch_vccz .Lv313_%=\n\t" \
    "s_mov_b32 m0, 0xc400\n\t" \
    "s_nop 0\n\t" \
    "buffer_load_dwordx4 v253, s[60:63], s47 offen sc1 lds\n\t" \
    ".Lv313_%=:\n\t" \
    "s_cmp_eq_u32 s41, 0\n\t" \
    "s_cbranch_scc1 .Lv311_%=\n\t" \
    "s_sub_u32 s41, s41, 1\n\t" \
    "s_branch .Lv310_%=\n\t" \
    ".Lv311_%=:\n\t" \
    LST2 \
    LJIT \
    "s_waitcnt lgkmcnt(0)\n\t" \
    "s_barrier \n\t" \
    LST3 \
    LJIT \
    "ds_read_b128 v[232:235], v253 offset:40960\n\t" \
    "ds_read_b128 v[236:239], v253 offset:41984\n\t" \
    "ds_read_b128 v[240:243], v253 offset:43008\n\t" \
    "v_smfmac_f32_16x16x64_bf16 v[244:247], v[224:227], v[0:7], v212\n\t" \
    "v_smfmac_f32_16x16x64_bf16 v[248:251], v[224:227], v[8:15], v212\n\t" \
    "v_smfmac_f32_16x16x64_bf16 v[208:211], v[224:227], v[200:207], v212\n\t" \
    "ds_read_b128 v[200:203], v254 offset:59392\n\t" \
    "ds_read_b128 v[204:207], v254 offset:60416\n\t" \
    "ds_read_b128 v[224:227], v253 offset:44032\n\t" \
    "v_smfmac_f32_16x16x64_bf16 v[208:211], v[228:231], v[16:23], v212\n\t" \
    "v_smfmac_f32_16x16x64_bf16 v[244:247], v[228:231], v[24:31], v212\n\t" \
    "v_smfmac_f32_16x16x64_bf16 v[248:251], v[228:231], v[32:39], v212\n\t" \
    "ds_read_b128 v[228:231], v253 offset:45056\n\t" \
    "s_waitcnt lgkmcnt(6)\n\t" \
    "v_smfmac_f32_16x16x64_bf16 v[208:211], v[232:235], v[216:223], v212\n\t" \
    "ds_read_b128 v[216:219], v254 offset:61440\n\t" \
    "ds_read_b128 v[220:223], v254 offset:62464\n\t" \
    "v_smfmac_f32_16x16x64_bf16 v[244:247], v[232:235], v[40:47], v212\n\t" \
    "v_smfmac_f32_16x16x64_bf16 v[248:251], v[232:235], v[48:55], v212\n\t" \
    "ds_read_b128 v[232:235], v253 offset:46080\n\t" \
    "s_waitcnt lgkmcnt(8)\n\t" \
    "v_smfmac_f32_16x16x64_bf16 v[208:211], v[236:239], v[56:63], v212\n\t" \
    "v_smfmac_f32_16x16x64_bf16 v[244:247], v[236:239], v[64:71], v212\n\t" \
    "v_smfmac_f32_16x16x64_bf16 v[248:251], v[236:239], v[72:79], v212\n\t" \
    "ds_read_b128 v[236:239], v253 offset:47104\n\t" \
    "s_waitcnt lgkmcnt(6)\n\t" \
    "v_smfmac_f32_16x16x64_bf16 v[208:211], v[240:243], v[200:207], v212\n\t" \
    "ds_read_b128 v[200:203], v254 offset:63488\n\t" \
    "ds_read_b128 v[204:207], v254 offset:64512\n\t" \
    "v_smfmac_f32_16x16x64_bf16 v[244:247], v[240:243], v[80:87], v212\n\t" \
    "v_smfmac_f32_16x16x64_bf16 v[248:251], v[240:243], v[88:95], v212\n\t" \
    "ds_read_b128 v[240:243], v253 offset:48128\n\t" \
    "s_waitcnt lgkmcnt(8)\n\t" \
    "v_smfmac_f32_16x16x64_bf16 v[208:211], v[224:227], v[96:103], v212\n\t" \
    "v_smfmac_f32_16x16x64_bf16 v[244:247], v[224:227], v[104:111], v212\n\t" \
    "v_smfmac_f32_16x16x64_bf16 v[248:251], v[224:227], v[112:119], v212\n\t" \
    "s_waitcnt lgkmcnt(7)\n\t" \
    "v_smfmac_f32_16x16x64_bf16 v[244:247], v[228:231], v[120:127], v212\n\t" \
    "s_waitcnt lgkmcnt(4)\n\t" \
    "v_smfmac_f32_16x16x64_bf16 v[244:247], v[232:235], v[144:151], v212\n\t" \
    "s_waitcnt lgkmcnt(3)\n\t" \
    "v_smfmac_f32_16x16x64_bf16 v[244:247], v[236:239], v[160:167], v212\n\t" \
    "s_waitcnt lgkmcnt(0)\n\t" \
    "v_smfmac_f32_16x16x64_bf16 v[244:247], v[240:243], v[184:191], v212\n\t" \
    "v_smfmac_f32_16x16x64_bf16 v[248:251], v[228:231], v[128:135], v212\n\t" \
    "v_smfmac_f32_16x16x64_bf16 v[248:251], v[232:235], v[152:159], v212\n\t" \
    "v_smfmac_f32_16x16x64_bf16 v[248:251], v[236:239], v[168:175], v212\n\t" \
    "v_add_f32 v244, v244, v246\n\t" \
    "v_add_f32 v245, v245, v247\n\t" \
    "v_cvt_pk_bf16_f32 v244, v244, v245\n\t" \
    "v_smfmac_f32_16x16x64_bf16 v[248:251], v[240:243], v[192:199], v212\n\t" \
    "ds_write_b16 v213, v244 offset:512\n\t" \
    "ds_write_b16_d16_hi v213, v244 offset:528\n\t" \
    "v_smfmac_f32_16x16x64_bf16 v[208:211], v[228:231], v[216:223], v212\n\t" \
    "v_smfmac_f32_16x16x64_bf16 v[208:211], v[232:235], v[136:143], v212\n\t" \
    "v_smfmac_f32_16x16x64_bf16 v[208:211], v[236:239], v[200:207], v212\n\t" \
    "v_add_f32 v248, v248, v250\n\t" \
    "v_add_f32 v249, v249, v251\n\t" \
    "v_cvt_pk_bf16_f32 v248, v248, v249\n\t" \
    "v_smfmac_f32_16x16x64_bf16 v[208:211], v[240:243], v[176:183], v212\n\t" \
    "ds_write_b16 v213, v248 offset:1024\n\t" \
    "ds_write_b16_d16_hi v213, v248 offset:1040\n\t" \
    "s_nop 5\n\t" \
    "v_add_f32 v208, v208, v210\n\t" \
    "v_add_f32 v209, v209, v211\n\t" \
    "v_cvt_pk_bf16_f32 v208, v208, v209\n\t" \
    "ds_write_b16 v213, v208 offset:0\n\t" \
    "ds_write_b16_d16_hi v213, v208 offset:16\n\t" \
    "s_setprio 0\n\t" \
    "s_waitcnt lgkmcnt(0)\n\t" \
    "s_barrier \n\t" \
    LST4 \
    LJIT \
    "ds_read_b128 v[224:227], v253 offset:16384\n\t" \
    "ds_read_b128 v[244:247], v254 offset:55296\n\t" \
    "ds_read_b128 v[228:231], v253 offset:17408\n\t" \
    "ds_read_b128 v[232:235], v253 offset:36864\n\t" \
    "ds_read_b128 v[236:239], v253 offset:37888\n\t" \
    "ds_read_b128 v[248:251], v254 offset:56320\n\t" \
    "s_add_u32 s40, s40, 1\n\t" \
    "s_cmp_lg_u32 s40, 64\n\t" \
    "s_cbranch_scc1 .Lv37_%=\n\t" \
    "s_branch .Lend%=\n\t" \
    ".Lv2_%=:\n\t" \
    "s_mov_b32 s42, %[xs]\n\t" \
    "v_add_u32 v244, 0xc000, v243\n\t" \
    "s_waitcnt vmcnt(12) lgkmcnt(0)\n\t" \
    "s_barrier \n\t" \
    "s_add_u32 m0, %[wm], 0x9000\n\t" \
    "s_mov_b32 s44, 0x1e0000\n\t" \
    "buffer_load_dwordx4 v240, %[wr], s44 offen offset:0 nt lds\n\t" \
    "buffer_load_dwordx4 v241, %[wr], s44 offen offset:1024 nt lds\n\t" \
    "buffer_load_dwordx4 v242, %[wr], s44 offen offset:2048 nt lds\n\t" \
    "ds_read_b64_tr_b16 v[208:209], v244 offset:0\n\t" \
    "ds_read_b64_tr_b16 v[210:211], v244 offset:1536\n\t" \
    "ds_read_b64_tr_b16 v[40:41], v244 offset:32\n\t" \
    "ds_read_b64_tr_b16 v[42:43], v244 offset:1568\n\t" \
    "ds_read_b64_tr_b16 v[48:49], v244 offset:64\n\t" \
    "ds_read_b64_tr_b16 v[50:51], v244 offset:1600\n\t" \
    "v_add_u32 v244, 0xf000, v243\n\t" \
    "s_waitcnt vmcnt(12) lgkmcnt(0)\n\t" \
    "s_barrier \n\t" \
    "s_add_u32 m0, %[wm], 0xc000\n\t" \
    "s_mov_b32 s44, 0x240000\n\t" \
    "buffer_load_dwordx4 v240, %[wr], s44 offen offset:0 nt lds\n\t" \
    "buffer_load_dwordx4 v241, %[wr], s44 offen offset:1024 nt lds\n\t" \
    "buffer_load_dwordx4 v242, %[wr], s44 offen offset:2048 nt lds\n\t" \
    "ds_read_b64_tr_b16 v[212:213], v244 offset:0\n\t" \
    "ds_read_b64_tr_b16 v[214:215], v244 offset:1536\n\t" \
    "ds_read_b64_tr_b16 v[44:45], v244 offset:32\n\t" \
    "ds_read_b64_tr_b16 v[46:47], v244 offset:1568\n\t" \
    "ds_read_b64_tr_b16 v[52:53], v244 offset:64\n\t" \
    "ds_read_b64_tr_b16 v[54:55], v244 offset:1600\n\t" \
    "v_add_u32 v244, 0x0, v243\n\t" \
    "s_waitcnt vmcnt(12) lgkmcnt(0)\n\t" \
    "s_barrier \n\t" \
    "s_add_u32 m0, %[wm], 0xf000\n\t" \
    "s_mov_b32 s44, 0x2a0000\n\t" \
    "buffer_load_dwordx4 v240, %[wr], s44 offen offset:0 nt lds\n\t" \
    "buffer_load_dwordx4 v241, %[wr], s44 offen offset:1024 nt lds\n\t" \
    "buffer_load_dwordx4 v242, %[wr], s44 offen offset:2048 nt lds\n\t" \
    "ds_read_b64_tr_b16 v[56:57], v244 offset:0\n\t" \
    "ds_read_b64_tr_b16 v[58:59], v244 offset:1536\n\t" \
    "ds_read_b64_tr_b16 v[64:65], v244 offset:32\n\t" \
    "ds_read_b64_tr_b16 v[66:67], v244 offset:1568\n\t" \
    "ds_read_b64_tr_b16 v[72:73], v244 offset:64\n\t" \
    "ds_read_b64_tr_b16 v[74:75], v244 offset:1600\n\t" \
    "v_add_u32 v244, 0x3000, v243\n\t" \
    "s_waitcnt vmcnt(12) lgkmcnt(0)\n\t" \
    "s_barrier \n\t" \
    "s_add_u32 m0, %[wm], 0x0\n\t" \
    "s_mov_b32 s44, 0x300000\n\t" \
    "buffer_load_dwordx4 v240, %[wr], s44 offen offset:0 nt lds\n\t" \
    "buffer_load_dwordx4 v241, %[wr], s44 offen offset:1024 nt lds\n\t" \
    "buffer_load_dwordx4 v242, %[wr], s44 offen offset:2048 nt lds\n\t" \
    "ds_read_b64_tr_b16 v[60:61], v244 offset:0\n\t" \
    "ds_read_b64_tr_b16 v[62:63], v244 offset:1536\n\t" \
    "ds_read_b64_tr_b16 v[68:69], v244 offset:32\n\t" \
    "ds_read_b64_tr_b16 v[70:71], v244 offset:1568\n\t" \
    "ds_read_b64_tr_b16 v[76:77], v244 offset:64\n\t" \
    "ds_read_b64_tr_b16 v[78:79], v244 offset:1600\n\t" \
    "v_add_u32 v244, 0x6000, v243\n\t" \
    "s_waitcnt vmcnt(12) lgkmcnt(0)\n\t" \
    "s_barrier \n\t" \
    "s_add_u32 m0, %[wm], 0x3000\n\t" \
    "s_mov_b32 s44, 0x360000\n\t" \
    "buffer_load_dwordx4 v240, %[wr], s44 offen offset:0 nt lds\n\t" \
    "buffer_load_dwordx4 v241, %[wr], s44 offen offset:1024 nt lds\n\t" \
    "buffer_load_dwordx4 v242, %[wr], s44 offen offset:2048 nt lds\n\t" \
    "ds_read_b64_tr_b16 v[216:217], v244 offset:0\n\t" \
    "ds_read_b64_tr_b16 v[218:219], v244 offset:1536\n\t" \
    "ds_read_b64_tr_b16 v[80:81], v244 offset:32\n\t" \
    "ds_read_b64_tr_b16 v[82:83], v244 offset:1568\n\t" \
    "ds_read_b64_tr_b16 v[88:89], v244 offset:64\n\t" \
    "ds_read_b64_tr_b16 v[90:91], v244 offset:1600\n\t" \
    "v_add_u32 v244, 0x9000, v243\n\t" \
    "s_waitcnt vmcnt(12) lgkmcnt(0)\n\t" \
    "s_barrier \n\t" \
    "s_add_u32 m0, %[wm], 0x6000\n\t" \
    "s_mov_b32 s44, 0x3c0000\n\t" \
    "buffer_load_dwordx4 v240, %[wr], s44 offen offset:0 nt lds\n\t" \
    "buffer_load_dwordx4 v241, %[wr], s44 offen offset:1024 nt lds\n\t" \
    "buffer_load_dwordx4 v242, %[wr], s44 offen offset:2048 nt lds\n\t" \
    "ds_read_b64_tr_b16 v[220:221], v244 offset:0\n\t" \
    "ds_read_b64_tr_b16 v[222:223], v244 offset:1536\n\t" \
    "ds_read_b64_tr_b16 v[84:85], v244 offset:32\n\t" \
    "ds_read_b64_tr_b16 v[86:87], v244 offset:1568\n\t" \
    "ds_read_b64_tr_b16 v[92:93], v244 offset:64\n\t" \
    "ds_read_b64_tr_b16 v[94:95], v244 offset:1600\n\t" \
    "v_add_u32 v244, 0xc000, v243\n\t" \
    "s_waitcnt vmcnt(12) lgkmcnt(0)\n\t" \
    "s_barrier \n\t" \
    "s_add_u32 m0, %[wm], 0x9000\n\t" \
    "s_mov_b32 s44, 0x420000\n\t" \
    "buffer_load_dwordx4 v240, %[wr], s44 offen offset:0 nt lds\n\t" \
    "buffer_load_dwordx4 v241, %[wr], s44 offen offset:1024 nt lds\n\t" \
    "buffer_load_dwordx4 v242, %[wr], s44 offen offset:2048 nt lds\n\t" \
    "ds_read_b64_tr_b16 v[96:97], v244 offset:0\n\t" \
    "ds_read_b64_tr_b16 v[98:99], v244 offset:1536\n\t" \
    "ds_read_b64_tr_b16 v[104:105], v244 offset:32\n\t" \
    "ds_read_b64_tr_b16 v[106:107], v244 offset:1568\n\t" \
    "ds_read_b64_tr_b16 v[112:113], v244 offset:64\n\t" \
    "ds_read_b64_tr_b16 v[114:115], v244 offset:1600\n\t" \
    "v_add_u32 v244, 0xf000, v243\n\t" \
    "s_waitcnt vmcnt(12) lgkmcnt(0)\n\t" \
    "s_barrier \n\t" \
    "s_add_u32 m0, %[wm], 0xc000\n\t" \
    "s_mov_b32 s44, 0x480000\n\t" \
    "buffer_load_dwordx4 v240, %[wr], s44 offen offset:0 nt lds\n\t" \
    "buffer_load_dwordx4 v241, %[wr], s44 offen offset:1024 nt lds\n\t" \
    "buffer_load_dwordx4 v242, %[wr], s44 offen offset:2048 nt lds\n\t" \
    "ds_read_b64_tr_b16 v[100:101], v244 offset:0\n\t" \
    "ds_read_b64_tr_b16 v[102:103], v244 offset:1536\n\t" \
    "ds_read_b64_tr_b16 v[108:109], v244 offset:32\n\t" \
    "ds_read_b64_tr_b16 v[110:111], v244 offset:1568\n\t" \
    "ds_read_b64_tr_b16 v[116:117], v244 offset:64\n\t" \
    "ds_read_b64_tr_b16 v[118:119], v244 offset:1600\n\t" \
    "v_add_u32 v244, 0x0, v243\n\t" \
    "s_waitcnt vmcnt(12) lgkmcnt(0)\n\t" \
    "s_barrier \n\t" \
    "v_mov_b32 v246, 0\n\t" \
    "buffer_load_dword v246, v246, s[60:63], %[mo] offen sc1\n\t" \
    "s_add_u32 m0, %[wm], 0xf000\n\t" \
    "s_mov_b32 s44, 0x4e0000\n\t" \
    "buffer_load_dwordx4 v240, %[wr], s44 offen offset:0 nt lds\n\t" \
    "buffer_load_dwordx4 v241, %[wr], s44 offen offset:1024 nt lds\n\t" \
    "buffer_load_dwordx4 v242, %[wr], s44 offen offset:2048 nt lds\n\t" \
    "ds_read_b64_tr_b16 v[200:201], v244 offset:0\n\t" \
    "ds_read_b64_tr_b16 v[202:203], v244 offset:1536\n\t" \
    "ds_read_b64_tr_b16 v[0:1], v244 offset:32\n\t" \
    "ds_read_b64_tr_b16 v[2:3], v244 offset:1568\n\t" \
    "ds_read_b64_tr_b16 v[8:9], v244 offset:64\n\t" \
    "ds_read_b64_tr_b16 v[10:11], v244 offset:1600\n\t" \
    "v_add_u32 v244, 0x3000, v243\n\t" \
    "s_waitcnt vmcnt(12) lgkmcnt(0)\n\t" \
    "s_barrier \n\t" \
    "s_add_u32 m0, %[wm], 0x0\n\t" \
    "s_mov_b32 s44, 0x540000\n\t" \
    "buffer_load_dwordx4 v240, %[wr], s44 offen offset:0 nt lds\n\t" \
    "buffer_load_dwordx4 v241, %[wr], s44 offen offset:1024 nt lds\n\t" \
    "buffer_load_dwordx4 v242, %[wr], s44 offen offset:2048 nt lds\n\t" \
    "ds_read_b64_tr_b16 v[204:205], v244 offset:0\n\t" \
    "ds_read_b64_tr_b16 v[206:207], v244 offset:1536\n\t" \
    "ds_read_b64_tr_b16 v[4:5], v244 offset:32\n\t" \
    "ds_read_b64_tr_b16 v[6:7], v244 offset:1568\n\t" \
    "ds_read_b64_tr_b16 v[12:13], v244 offset:64\n\t" \
    "ds_read_b64_tr_b16 v[14:15], v244 offset:1600\n\t" \
    "v_add_u32 v244, 0x6000, v243\n\t" \
    "s_waitcnt vmcnt(12) lgkmcnt(0)\n\t" \
    "s_barrier \n\t" \
    "s_add_u32 m0, %[wm], 0x3000\n\t" \
    "s_mov_b32 s44, 0x5a0000\n\t" \
    "buffer_load_dwordx4 v240, %[wr], s44 offen offset:0 nt lds\n\t" \
    "buffer_load_dwordx4 v241, %[wr], s44 offen offset:1024 nt lds\n\t" \
    "buffer_load_dwordx4 v242, %[wr], s44 offen offset:2048 nt lds\n\t" \
    "ds_read_b64_tr_b16 v[224:225], v244 offset:0\n\t" \
    "ds_read_b64_tr_b16 v[226:227], v244 offset:1536\n\t" \
    "ds_read_b64_tr_b16 v[120:121], v244 offset:32\n\t" \
    "ds_read_b64_tr_b16 v[122:123], v244 offset:1568\n\t" \
    "ds_read_b64_tr_b16 v[128:129], v244 offset:64\n\t" \
    "ds_read_b64_tr_b16 v[130:131], v244 offset:1600\n\t" \
    "v_add_u32 v244, 0x9000, v243\n\t" \
    "s_waitcnt vmcnt(12) lgkmcnt(0)\n\t" \
    "s_barrier \n\t" \
    "s_add_u32 m0, %[wm], 0x6000\n\t" \
    "s_mov_b32 s44, 0x600000\n\t" \
    "buffer_load_dwordx4 v240, %[wr], s44 offen offset:0 nt lds\n\t" \
    "buffer_load_dwordx4 v241, %[wr], s44 offen offset:1024 nt lds\n\t" \
    "buffer_load_dwordx4 v242, %[wr], s44 offen offset:2048 nt lds\n\t" \
    "ds_read_b64_tr_b16 v[228:229], v244 offset:0\n\t" \
    "ds_read_b64_tr_b16 v[230:231], v244 offset:1536\n\t" \
    "ds_read_b64_tr_b16 v[124:125], v244 offset:32\n\t" \
    "ds_read_b64_tr_b16 v[126:127], v244 offset:1568\n\t" \
    "ds_read_b64_tr_b16 v[132:133], v244 offset:64\n\t" \
    "ds_read_b64_tr_b16 v[134:135], v244 offset:1600\n\t" \
    "v_add_u32 v244, 0xc000, v243\n\t" \
    "s_waitcnt vmcnt(12) lgkmcnt(0)\n\t" \
    "s_barrier \n\t" \
    "s_add_u32 m0, %[wm], 0x9000\n\t" \
    "s_mov_b32 s44, 0x660000\n\t" \
    "buffer_load_dwordx4 v240, %[wr], s44 offen offset:0 nt lds\n\t" \
    "buffer_load_dwordx4 v241, %[wr], s44 offen offset:1024 nt lds\n\t" \
    "buffer_load_dwordx4 v242, %[wr], s44 offen offset:2048 nt lds\n\t" \
    "ds_read_b64_tr_b16 v[136:137], v244 offset:0\n\t" \
    "ds_read_b64_tr_b16 v[138:139], v244 offset:1536\n\t" \
    "ds_read_b64_tr_b16 v[144:145], v244 offset:32\n\t" \
    "ds_read_b64_tr_b16 v[146:147], v244 offset:1568\n\t" \
    "ds_read_b64_tr_b16 v[152:153], v244 offset:64\n\t" \
    "ds_read_b64_tr_b16 v[154:155], v244 offset:1600\n\t" \
    "v_add_u32 v244, 0xf000, v243\n\t" \
    "s_waitcnt vmcnt(12) lgkmcnt(0)\n\t" \
    "s_barrier \n\t" \
    ".Lv21_%=:\n\t" \
    "v_readfirstlane_b32 s43, v246\n\t" \
    "s_nop 0\n\t" \
    "s_cmp_lg_u32 s43, -1\n\t" \
    "s_cbranch_scc1 .Lv22_%=\n\t" \
    "s_cmp_eq_u32 s41, 0\n\t" \
    "s_cbranch_scc1 .Lv22_%=\n\t" \
    "s_sub_u32 s41, s41, 1\n\t" \
    "s_sleep 4\n\t" \
    "v_mov_b32 v246, 0\n\t" \
    "buffer_load_dword v246, v246, s[60:63], %[mo] offen sc1\n\t" \
    "s_waitcnt vmcnt(0)\n\t" \
    "s_branch .Lv21_%=\n\t" \
    ".Lv22_%=:\n\t" \
    "v_mov_b32 v246, 0x8000\n\t" \
    "s_mov_b64 exec, 1\n\t" \
    "buffer_store_dword v246, v246, s[60:63], %[mo] offen sc1\n\t" \
    "s_mov_b64 exec, -1\n\t" \
    "s_and_b32 s44, s43, 0x5000\n\t" \
    "s_xor_b32 %[mb], %[mb], s44\n\t" \
    "s_xor_b32 %[fl], %[fl], s44\n\t" \
    "s_and_b32 s43, s43, 0x2000\n\t" \
    "s_xor_b32 s43, s43, %[lx]\n\t" \
    "s_add_u32 s60, s60, s43\n\t" \
    "s_addc_u32 s61, s61, 0\n\t" \
    "v_and_b32 v252, 0xf0, v253\n\t" \
    "v_lshlrev_b32 v252, 3, v252\n\t" \
    "buffer_load_dword v252, v252, s[60:63], %[fl] offen sc1\n\t" \
    "s_add_u32 m0, %[wm], 0xc000\n\t" \
    "s_mov_b32 s44, 0x6c0000\n\t" \
    "buffer_load_dwordx4 v240, %[wr], s44 offen offset:0 nt lds\n\t" \
    "buffer_load_dwordx4 v241, %[wr], s44 offen offset:1024 nt lds\n\t" \
    "buffer_load_dwordx4 v242, %[wr], s44 offen offset:2048 nt lds\n\t" \
    "ds_read_b64_tr_b16 v[140:141], v244 offset:0\n\t" \
    "ds_read_b64_tr_b16 v[142:143], v244 offset:1536\n\t" \
    "ds_read_b64_tr_b16 v[148:149], v244 offset:32\n\t" \
    "ds_read_b64_tr_b16 v[150:151], v244 offset:1568\n\t" \
    "ds_read_b64_tr_b16 v[156:157], v244 offset:64\n\t" \
    "ds_read_b64_tr_b16 v[158:159], v244 offset:1600\n\t" \
    "v_add_u32 v244, 0x0, v243\n\t" \
    "s_waitcnt vmcnt(12) lgkmcnt(0)\n\t" \
    "s_barrier \n\t" \
    "s_add_u32 m0, %[wm], 0xf000\n\t" \
    "s_mov_b32 s44, 0x720000\n\t" \
    "buffer_load_dwordx4 v240, %[wr], s44 offen offset:0 nt lds\n\t" \
    "buffer_load_dwordx4 v241, %[wr], s44 offen offset:1024 nt lds\n\t" \
    "buffer_load_dwordx4 v242, %[wr], s44 offen offset:2048 nt lds\n\t" \
    "ds_read_b64_tr_b16 v[16:17], v244 offset:0\n\t" \
    "ds_read_b64_tr_b16 v[18:19], v244 offset:1536\n\t" \
    "ds_read_b64_tr_b16 v[24:25], v244 offset:32\n\t" \
    "ds_read_b64_tr_b16 v[26:27], v244 offset:1568\n\t" \
    "ds_read_b64_tr_b16 v[32:33], v244 offset:64\n\t" \
    "ds_read_b64_tr_b16 v[34:35], v244 offset:1600\n\t" \
    "v_add_u32 v244, 0x3000, v243\n\t" \
    "s_waitcnt vmcnt(12) lgkmcnt(0)\n\t" \
    "s_barrier \n\t" \
    "ds_read_b64_tr_b16 v[20:21], v244 offset:0\n\t" \
    "ds_read_b64_tr_b16 v[22:23], v244 offset:1536\n\t" \
    "ds_read_b64_tr_b16 v[28:29], v244 offset:32\n\t" \
    "ds_read_b64_tr_b16 v[30:31], v244 offset:1568\n\t" \
    "ds_read_b64_tr_b16 v[36:37], v244 offset:64\n\t" \
    "ds_read_b64_tr_b16 v[38:39], v244 offset:1600\n\t" \
    "v_add_u32 v244, 0x6000, v243\n\t" \
    "s_waitcnt vmcnt(9) lgkmcnt(0)\n\t" \
    "s_barrier \n\t" \
    "ds_read_b64_tr_b16 v[232:233], v244 offset:0\n\t" \
    "ds_read_b64_tr_b16 v[234:235], v244 offset:1536\n\t" \
    "ds_read_b64_tr_b16 v[160:161], v244 offset:32\n\t" \
    "ds_read_b64_tr_b16 v[162:163], v244 offset:1568\n\t" \
    "ds_read_b64_tr_b16 v[168:169], v244 offset:64\n\t" \
    "ds_read_b64_tr_b16 v[170:171], v244 offset:1600\n\t" \
    "v_add_u32 v244, 0x9000, v243\n\t" \
    "s_waitcnt vmcnt(6) lgkmcnt(0)\n\t" \
    "s_barrier \n\t" \
    "ds_read_b64_tr_b16 v[236:237], v244 offset:0\n\t" \
    "ds_read_b64_tr_b16 v[238:239], v244 offset:1536\n\t" \
    "ds_read_b64_tr_b16 v[164:165], v244 offset:32\n\t" \
    "ds_read_b64_tr_b16 v[166:167], v244 offset:1568\n\t" \
    "ds_read_b64_tr_b16 v[172:173], v244 offset:64\n\t" \
    "ds_read_b64_tr_b16 v[174:175], v244 offset:1600\n\t" \
    "v_add_u32 v244, 0xc000, v243\n\t" \
    "s_waitcnt vmcnt(3) lgkmcnt(0)\n\t" \
    "s_barrier \n\t" \
    "ds_read_b64_tr_b16 v[176:177], v244 offset:0\n\t" \
    "ds_read_b64_tr_b16 v[178:179], v244 offset:1536\n\t" \
    "ds_read_b64_tr_b16 v[184:185], v244 offset:32\n\t" \
    "ds_read_b64_tr_b16 v[186:187], v244 offset:1568\n\t" \
    "ds_read_b64_tr_b16 v[192:193], v244 offset:64\n\t" \
    "ds_read_b64_tr_b16 v[194:195], v244 offset:1600\n\t" \
    "s_mov_b32 s43, 0\n\t" \
    "v_cmp_eq_u32 vcc, 0, v252\n\t" \
    "s_cbranch_vccnz .Lv23_%=\n\t" \
    "s_mov_b32 m0, 0x2000\n\t" \
    "s_add_u32 s49, s42, 0x0\n\t" \
    "buffer_load_dwordx4 v253, s[60:63], s49 offen offset:0 sc1 lds\n\t" \
    "buffer_load_dwordx4 v253, s[60:63], s49 offen offset:1024 sc1 lds\n\t" \
    "buffer_load_dwordx4 v253, s[60:63], s49 offen offset:2048 sc1 lds\n\t" \
    "buffer_load_dwordx4 v253, s[60:63], s49 offen offset:3072 sc1 lds\n\t" \
    "s_mov_b32 m0, 0x4800\n\t" \
    "s_add_u32 s49, s42, 0x8800\n\t" \
    "buffer_load_dwordx4 v253, s[60:63], s49 offen offset:0 sc1 lds\n\t" \
    "s_mov_b32 m0, 0x7000\n\t" \
    "s_add_u32 s49, s42, 0x1000\n\t" \
    "buffer_load_dwordx4 v253, s[60:63], s49 offen offset:0 sc1 lds\n\t" \
    "buffer_load_dwordx4 v253, s[60:63], s49 offen offset:1024 sc1 lds\n\t" \
    "buffer_load_dwordx4 v253, s[60:63], s49 offen offset:2048 sc1 lds\n\t" \
    "buffer_load_dwordx4 v253, s[60:63], s49 offen offset:3072 sc1 lds\n\t" \
    "s_mov_b32 m0, 0x9800\n\t" \
    "s_add_u32 s49, s42, 0x9800\n\t" \
    "buffer_load_dwordx4 v253, s[60:63], s49 offen offset:0 sc1 lds\n\t" \
    "s_mov_b32 s43, 1\n\t" \
    ".Lv23_%=:\n\t" \
    "v_add_u32 v244, 0xf000, v243\n\t" \
    "s_waitcnt vmcnt(0) lgkmcnt(0)\n\t" \
    "s_barrier \n\t" \
    "ds_read_b64_tr_b16 v[180:181], v244 offset:0\n\t" \
    "ds_read_b64_tr_b16 v[182:183], v244 offset:1536\n\t" \
    "ds_read_b64_tr_b16 v[188:189], v244 offset:32\n\t" \
    "ds_read_b64_tr_b16 v[190:191], v244 offset:1568\n\t" \
    "ds_read_b64_tr_b16 v[196:197], v244 offset:64\n\t" \
    "ds_read_b64_tr_b16 v[198:199], v244 offset:1600\n\t" \
    "s_waitcnt lgkmcnt(0)\n\t" \
    "s_barrier \n\t" \
    LST0 \
    LJIT \
    "ds_write_b128 v254, v[200:203] offset:51200\n\t" \
    "ds_write_b128 v254, v[204:207] offset:52224\n\t" \
    "ds_write_b128 v254, v[208:211] offset:53248\n\t" \
    "ds_write_b128 v254, v[212:215] offset:54272\n\t" \
    "ds_write_b128 v254, v[216:219] offset:55296\n\t" \
    "ds_write_b128 v254, v[220:223] offset:56320\n\t" \
    "ds_write_b128 v254, v[224:227] offset:57344\n\t" \
    "ds_write_b128 v254, v[228:231] offset:58368\n\t" \
    "ds_write_b128 v254, v[232:235] offset:59392\n\t" \
    "ds_write_b128 v254, v[236:239] offset:60416\n\t" \
    "s_cmp_lg_u32 s43, 0\n\t" \
    "s_cbranch_scc1 .Lv24_%=\n\t" \
    "s_mov_b32 s44, 0\n\t" \
    ".Lv25_%=:\n\t" \
    "v_cmp_ge_u32 vcc, s44, v252\n\t" \
    "s_cbranch_vccz .Lv26_%=\n\t" \
    "s_cmp_eq_u32 s41, 0\n\t" \
    "s_cbranch_scc1 .Lv26_%=\n\t" \
    "s_sub_u32 s41, s41, 1\n\t" \
    "s_sleep 8\n\t" \
    "v_and_b32 v252, 0xf0, v253\n\t" \
    "v_lshlrev_b32 v252, 3, v252\n\t" \
    "buffer_load_dword v252, v252, s[60:63], %[fl] offen sc1\n\t" \
    "s_waitcnt vmcnt(0)\n\t" \
    "s_branch .Lv25_%=\n\t" \
    ".Lv26_%=:\n\t" \
    "s_mov_b32 m0, 0x2000\n\t" \
    "s_add_u32 s49, s42, 0x0\n\t" \
    "buffer_load_dwordx4 v253, s[60:63], s49 offen offset:0 sc1 lds\n\t" \
    "buffer_load_dwordx4 v253, s[60:63], s49 offen offset:1024 sc1 lds\n\t" \
    "buffer_load_dwordx4 v253, s[60:63], s49 offen offset:2048 sc1 lds\n\t" \
    "buffer_load_dwordx4 v253, s[60:63], s49 offen offset:3072 sc1 lds\n\t" \
    "s_mov_b32 m0, 0x4800\n\t" \
    "s_add_u32 s49, s42, 0x8800\n\t" \
    "buffer_load_dwordx4 v253, s[60:63], s49 offen offset:0 sc1 lds\n\t" \
    "s_mov_b32 m0, 0x7000\n\t" \
    "s_add_u32 s49, s42, 0x1000\n\t" \
    "buffer_load_dwordx4 v253, s[60:63], s49 offen offset:0 sc1 lds\n\t" \
    "buffer_load_dwordx4 v253, s[60:63], s49 offen offset:1024 sc1 lds\n\t" \
    "buffer_load_dwordx4 v253, s[60:63], s49 offen offset:2048 sc1 lds\n\t" \
    "buffer_load_dwordx4 v253, s[60:63], s49 offen offset:3072 sc1 lds\n\t" \
    "s_mov_b32 m0, 0x9800\n\t" \
    "s_add_u32 s49, s42, 0x9800\n\t" \
    "buffer_load_dwordx4 v253, s[60:63], s49 offen offset:0 sc1 lds\n\t" \
    ".Lv24_%=:\n\t" \
    "s_waitcnt vmcnt(0) lgkmcnt(0)\n\t" \
    "s_barrier \n\t" \
    "ds_read_b128 v[224:227], v253 offset:8192\n\t" \
    "ds_read_b128 v[244:247], v254 offset:51200\n\t" \
    "ds_read_b128 v[228:231], v253 offset:9216\n\t" \
    "ds_read_b128 v[232:235], v253 offset:28672\n\t" \
    "ds_read_b128 v[236:239], v253 offset:29696\n\t" \
    "ds_read_b128 v[248:251], v254 offset:52224\n\t" \
    ".p2align 8\n\t" \
    ".Lv27_%=:\n\t" \
    LST7 \
    LJIT \
    "s_waitcnt lgkmcnt(5)\n\t" \
    "v_mfma_f32_16x16x32_bf16 v[204:207], v[0:3], v[224:227], 0\n\t" \
    "s_waitcnt lgkmcnt(3)\n\t" \
    "v_mfma_f32_16x16x32_bf16 v[204:207], v[4:7], v[228:231], v[204:207]\n\t" \
    "ds_read_b128 v[240:243], v253 offset:14336\n\t" \
    "v_mfma_f32_16x16x32_bf16 v[208:211], v[8:11], v[224:227], 0\n\t" \
    "v_mfma_f32_16x16x32_bf16 v[208:211], v[12:15], v[228:231], v[208:211]\n\t" \
    "v_mfma_f32_16x16x32_bf16 v[200:203], v[244:247], v[224:227], 0\n\t" \
    "s_waitcnt lgkmcnt(1)\n\t" \
    "v_mfma_f32_16x16x32_bf16 v[200:203], v[248:251], v[228:231], v[200:203]\n\t" \
    "v_mfma_f32_16x16x32_bf16 v[216:219], v[0:3], v[232:235], 0\n\t" \
    "ds_read_b128 v[224:227], v253 offset:15360\n\t" \
    "v_mfma_f32_16x16x32_bf16 v[216:219], v[4:7], v[236:239], v[216:219]\n\t" \
    "ds_read_b128 v[228:231], v253 offset:0\n\t" \
    "v_mfma_f32_16x16x32_bf16 v[220:223], v[8:11], v[232:235], 0\n\t" \
    "v_mfma_f32_16x16x32_bf16 v[220:223], v[12:15], v[236:239], v[220:223]\n\t" \
    "v_mfma_f32_16x16x32_bf16 v[212:215], v[244:247], v[232:235], 0\n\t" \
    "ds_read_b128 v[244:247], v254 offset:53248\n\t" \
    "v_mfma_f32_16x16x32_bf16 v[212:215], v[248:251], v[236:239], v[212:215]\n\t" \
    "ds_read_b128 v[248:251], v254 offset:54272\n\t" \
    "s_waitcnt lgkmcnt(4)\n\t" \
    "v_mfma_f32_16x16x32_bf16 v[204:207], v[24:27], v[240:243], v[204:207]\n\t" \
    "ds_read_b128 v[232:235], v253 offset:1024\n\t" \
    "s_waitcnt lgkmcnt(4)\n\t" \
    "v_mfma_f32_16x16x32_bf16 v[204:207], v[28:31], v[224:227], v[204:207]\n\t" \
    "ds_read_b128 v[236:239], v253 offset:20480\n\t" \
    "v_mfma_f32_16x16x32_bf16 v[208:211], v[32:35], v[240:243], v[208:211]\n\t" \
    "v_mfma_f32_16x16x32_bf16 v[208:211], v[36:39], v[224:227], v[208:211]\n\t" \
    "v_mfma_f32_16x16x32_bf16 v[200:203], v[16:19], v[240:243], v[200:203]\n\t" \
    "v_mfma_f32_16x16x32_bf16 v[200:203], v[20:23], v[224:227], v[200:203]\n\t" \
    "s_waitcnt lgkmcnt(4)\n\t" \
    "v_mfma_f32_16x16x32_bf16 v[204:207], v[40:43], v[228:231], v[204:207]\n\t" \
    "ds_read_b128 v[240:243], v253 offset:21504\n\t" \
    "s_waitcnt lgkmcnt(2)\n\t" \
    "v_mfma_f32_16x16x32_bf16 v[204:207], v[44:47], v[232:235], v[204:207]\n\t" \
    "ds_read_b128 v[224:227], v253 offset:2048\n\t" \
    "v_mfma_f32_16x16x32_bf16 v[208:211], v[48:51], v[228:231], v[208:211]\n\t" \
    "v_mfma_f32_16x16x32_bf16 v[208:211], v[52:55], v[232:235], v[208:211]\n\t" \
    "v_mfma_f32_16x16x32_bf16 v[200:203], v[244:247], v[228:231], v[200:203]\n\t" \
    "v_mfma_f32_16x16x32_bf16 v[200:203], v[248:251], v[232:235], v[200:203]\n\t" \
    "s_waitcnt lgkmcnt(2)\n\t" \
    "v_mfma_f32_16x16x32_bf16 v[216:219], v[40:43], v[236:239], v[216:219]\n\t" \
    "ds_read_b128 v[228:231], v253 offset:3072\n\t" \
    "s_add_u32 s44, s40, 1\n\t" \
    "s_waitcnt lgkmcnt(2)\n\t" \
    "v_mfma_f32_16x16x32_bf16 v[216:219], v[44:47], v[240:243], v[216:219]\n\t" \
    "ds_read_b128 v[232:235], v253 offset:4096\n\t" \
    "s_and_b32 s45, s44, 15\n\t" \
    "v_mfma_f32_16x16x32_bf16 v[220:223], v[48:51], v[236:239], v[220:223]\n\t" \
    "s_cmp_eq_u32 s45, 0\n\t" \
    "v_mfma_f32_16x16x32_bf16 v[220:223], v[52:55], v[240:243], v[220:223]\n\t" \
    "s_cselect_b32 s45, %[cj], %[cs]\n\t" \
    "v_mfma_f32_16x16x32_bf16 v[212:215], v[244:247], v[236:239], v[212:215]\n\t" \
    "ds_read_b128 v[244:247], v254 offset:55296\n\t" \
    "s_cmp_eq_u32 s44, 64\n\t" \
    "v_mfma_f32_16x16x32_bf16 v[212:215], v[248:251], v[240:243], v[212:215]\n\t" \
    "ds_read_b128 v[248:251], v254 offset:56320\n\t" \
    "s_cselect_b32 s45, 0, s45\n\t" \
    "s_waitcnt lgkmcnt(4)\n\t" \
    "v_mfma_f32_16x16x32_bf16 v[204:207], v[64:67], v[224:227], v[204:207]\n\t" \
    "ds_read_b128 v[236:239], v253 offset:5120\n\t" \
    "s_add_u32 s42, s42, s45\n\t" \
    "s_waitcnt lgkmcnt(4)\n\t" \
    "v_mfma_f32_16x16x32_bf16 v[204:207], v[68:71], v[228:231], v[204:207]\n\t" \
    "ds_read_b128 v[240:243], v253 offset:24576\n\t" \
    "v_mfma_f32_16x16x32_bf16 v[208:211], v[72:75], v[224:227], v[208:211]\n\t" \
    "v_mfma_f32_16x16x32_bf16 v[208:211], v[76:79], v[228:231], v[208:211]\n\t" \
    "v_mfma_f32_16x16x32_bf16 v[200:203], v[56:59], v[224:227], v[200:203]\n\t" \
    "v_mfma_f32_16x16x32_bf16 v[200:203], v[60:63], v[228:231], v[200:203]\n\t" \
    "s_waitcnt lgkmcnt(4)\n\t" \
    "v_mfma_f32_16x16x32_bf16 v[204:207], v[80:83], v[232:235], v[204:207]\n\t" \
    "ds_read_b128 v[224:227], v253 offset:25600\n\t" \
    "s_waitcnt lgkmcnt(2)\n\t" \
    "v_mfma_f32_16x16x32_bf16 v[204:207], v[84:87], v[236:239], v[204:207]\n\t" \
    "ds_read_b128 v[228:231], v253 offset:6144\n\t" \
    "v_mfma_f32_16x16x32_bf16 v[208:211], v[88:91], v[232:235], v[208:211]\n\t" \
    "v_mfma_f32_16x16x32_bf16 v[208:211], v[92:95], v[236:239], v[208:211]\n\t" \
    "v_mfma_f32_16x16x32_bf16 v[200:203], v[244:247], v[232:235], v[200:203]\n\t" \
    "v_mfma_f32_16x16x32_bf16 v[200:203], v[248:251], v[236:239], v[200:203]\n\t" \
    "s_waitcnt lgkmcnt(2)\n\t" \
    "v_mfma_f32_16x16x32_bf16 v[216:219], v[80:83], v[240:243], v[216:219]\n\t" \
    "ds_read_b128 v[232:235], v253 offset:7168\n\t" \
    "s_and_b32 s44, s40, 3\n\t" \
    "s_waitcnt lgkmcnt(2)\n\t" \
    "v_mfma_f32_16x16x32_bf16 v[216:219], v[84:87], v[224:227], v[216:219]\n\t" \
    "ds_read_b128 v[236:239], v253 offset:10240\n\t" \
    "s_lshl_b32 s44, s44, 22\n\t" \
    "v_mfma_f32_16x16x32_bf16 v[220:223], v[88:91], v[240:243], v[220:223]\n\t" \
    "s_add_u32 s44, s44, %[mb]\n\t" \
    "v_mfma_f32_16x16x32_bf16 v[220:223], v[92:95], v[224:227], v[220:223]\n\t" \
    "s_add_u32 s46, s44, 0x8000\n\t" \
    "v_mfma_f32_16x16x32_bf16 v[212:215], v[244:247], v[240:243], v[212:215]\n\t" \
    "ds_read_b128 v[244:247], v254 offset:57344\n\t" \
    "s_add_u32 s47, s44, 0x8c00\n\t" \
    "v_mfma_f32_16x16x32_bf16 v[212:215], v[248:251], v[224:227], v[212:215]\n\t" \
    "ds_read_b128 v[248:251], v254 offset:58368\n\t" \
    "s_waitcnt lgkmcnt(4)\n\t" \
    "v_mfma_f32_16x16x32_bf16 v[204:207], v[104:107], v[228:231], v[204:207]\n\t" \
    "ds_read_b128 v[240:243], v253 offset:11264\n\t" \
    "s_waitcnt lgkmcnt(4)\n\t" \
    "v_mfma_f32_16x16x32_bf16 v[204:207], v[108:111], v[232:235], v[204:207]\n\t" \
    "ds_read_b128 v[224:227], v253 offset:30720\n\t" \
    "v_mfma_f32_16x16x32_bf16 v[208:211], v[112:115], v[228:231], v[208:211]\n\t" \
    "v_mfma_f32_16x16x32_bf16 v[208:211], v[116:119], v[232:235], v[208:211]\n\t" \
    "v_mfma_f32_16x16x32_bf16 v[200:203], v[96:99], v[228:231], v[200:203]\n\t" \
    "v_mfma_f32_16x16x32_bf16 v[200:203], v[100:103], v[232:235], v[200:203]\n\t" \
    "s_waitcnt lgkmcnt(4)\n\t" \
    "v_mfma_f32_16x16x32_bf16 v[204:207], v[120:123], v[236:239], v[204:207]\n\t" \
    "ds_read_b128 v[228:231], v253 offset:31744\n\t" \
    "s_waitcnt lgkmcnt(2)\n\t" \
    "v_mfma_f32_16x16x32_bf16 v[204:207], v[124:127], v[240:243], v[204:207]\n\t" \
    "ds_read_b128 v[232:235], v253 offset:12288\n\t" \
    "v_mfma_f32_16x16x32_bf16 v[208:211], v[128:131], v[236:239], v[208:211]\n\t" \
    "v_mfma_f32_16x16x32_bf16 v[208:211], v[132:135], v[240:243], v[208:211]\n\t" \
    "v_mfma_f32_16x16x32_bf16 v[200:203], v[244:247], v[236:239], v[200:203]\n\t" \
    "v_mfma_f32_16x16x32_bf16 v[200:203], v[248:251], v[240:243], v[200:203]\n\t" \
    "s_waitcnt lgkmcnt(2)\n\t" \
    "v_mfma_f32_16x16x32_bf16 v[216:219], v[120:123], v[224:227], v[216:219]\n\t" \
    "ds_read_b128 v[236:239], v253 offset:13312\n\t" \
    "s_waitcnt lgkmcnt(2)\n\t" \
    "v_mfma_f32_16x16x32_bf16 v[216:219], v[124:127], v[228:231], v[216:219]\n\t" \
    "ds_read_b128 v[240:243], v253 offset:16384\n\t" \
    "v_mfma_f32_16x16x32_bf16 v[220:223], v[128:131], v[224:227], v[220:223]\n\t" \
    "v_mfma_f32_16x16x32_bf16 v[220:223], v[132:135], v[228:231], v[220:223]\n\t" \
    "v_mfma_f32_16x16x32_bf16 v[212:215], v[244:247], v[224:227], v[212:215]\n\t" \
    "ds_read_b128 v[244:247], v254 offset:59392\n\t" \
    "v_mfma_f32_16x16x32_bf16 v[212:215], v[248:251], v[228:231], v[212:215]\n\t" \
    "ds_read_b128 v[248:251], v254 offset:60416\n\t" \
    "v_and_b32 v252, 0xf0, v253\n\t" \
    "v_lshlrev_b32 v252, 3, v252\n\t" \
    "buffer_load_dword v252, v252, s[60:63], %[fl] offen sc1\n\t" \
    "s_waitcnt lgkmcnt(4)\n\t" \
    "v_mfma_f32_16x16x32_bf16 v[204:207], v[144:147], v[232:235], v[204:207]\n\t" \
    "ds_read_b128 v[224:227], v253 offset:17408\n\t" \
    "s_waitcnt lgkmcnt(4)\n\t" \
    "v_mfma_f32_16x16x32_bf16 v[204:207], v[148:151], v[236:239], v[204:207]\n\t" \
    "ds_read_b128 v[228:231], v253 offset:36864\n\t" \
    "v_mfma_f32_16x16x32_bf16 v[208:211], v[152:155], v[232:235], v[208:211]\n\t" \
    "v_mfma_f32_16x16x32_bf16 v[208:211], v[156:159], v[236:239], v[208:211]\n\t" \
    "v_mfma_f32_16x16x32_bf16 v[200:203], v[136:139], v[232:235], v[200:203]\n\t" \
    "v_mfma_f32_16x16x32_bf16 v[200:203], v[140:143], v[236:239], v[200:203]\n\t" \
    "s_waitcnt lgkmcnt(4)\n\t" \
    "v_mfma_f32_16x16x32_bf16 v[204:207], v[160:163], v[240:243], v[204:207]\n\t" \
    "ds_read_b128 v[232:235], v253 offset:37888\n\t" \
    "s_waitcnt lgkmcnt(2)\n\t" \
    "v_mfma_f32_16x16x32_bf16 v[204:207], v[164:167], v[224:227], v[204:207]\n\t" \
    "ds_read_b128 v[236:239], v253 offset:18432\n\t" \
    "v_mfma_f32_16x16x32_bf16 v[208:211], v[168:171], v[240:243], v[208:211]\n\t" \
    "v_mfma_f32_16x16x32_bf16 v[208:211], v[172:175], v[224:227], v[208:211]\n\t" \
    "v_mfma_f32_16x16x32_bf16 v[200:203], v[244:247], v[240:243], v[200:203]\n\t" \
    "v_mfma_f32_16x16x32_bf16 v[200:203], v[248:251], v[224:227], v[200:203]\n\t" \
    "s_waitcnt lgkmcnt(2)\n\t" \
    "v_mfma_f32_16x16x32_bf16 v[216:219], v[160:163], v[228:231], v[216:219]\n\t" \
    "ds_read_b128 v[240:243], v253 offset:19456\n\t" \
    "s_waitcnt lgkmcnt(2)\n\t" \
    "v_mfma_f32_16x16x32_bf16 v[216:219], v[164:167], v[232:235], v[216:219]\n\t" \
    "ds_read_b128 v[224:227], v253 offset:34816\n\t" \
    "v_mfma_f32_16x16x32_bf16 v[220:223], v[168:171], v[228:231], v[220:223]\n\t" \
    "v_mfma_f32_16x16x32_bf16 v[220:223], v[172:175], v[232:235], v[220:223]\n\t" \
    "v_mfma_f32_16x16x32_bf16 v[212:215], v[244:247], v[228:231], v[212:215]\n\t" \
    "v_mfma_f32_16x16x32_bf16 v[212:215], v[248:251], v[232:235], v[212:215]\n\t" \
    "s_waitcnt lgkmcnt(2)\n\t" \
    "v_mfma_f32_16x16x32_bf16 v[204:207], v[184:187], v[236:239], v[204:207]\n\t" \
    "ds_read_b128 v[228:231], v253 offset:35840\n\t" \
    "s_waitcnt lgkmcnt(2)\n\t" \
    "v_mfma_f32_16x16x32_bf16 v[204:207], v[188:191], v[240:243], v[204:207]\n\t" \
    "ds_read_b128 v[232:235], v253 offset:22528\n\t" \
    "v_mfma_f32_16x16x32_bf16 v[208:211], v[192:195], v[236:239], v[208:211]\n\t" \
    "v_mfma_f32_16x16x32_bf16 v[208:211], v[196:199], v[240:243], v[208:211]\n\t" \
    "v_mfma_f32_16x16x32_bf16 v[200:203], v[176:179], v[236:239], v[200:203]\n\t" \
    "v_mfma_f32_16x16x32_bf16 v[200:203], v[180:183], v[240:243], v[200:203]\n\t" \
    "s_waitcnt lgkmcnt(2)\n\t" \
    "v_mfma_f32_16x16x32_bf16 v[216:219], v[24:27], v[224:227], v[216:219]\n\t" \
    "ds_read_b128 v[236:239], v253 offset:23552\n\t" \
    "s_waitcnt lgkmcnt(2)\n\t" \
    "v_mfma_f32_16x16x32_bf16 v[216:219], v[28:31], v[228:231], v[216:219]\n\t" \
    "ds_read_b128 v[240:243], v253 offset:26624\n\t" \
    "v_mfma_f32_16x16x32_bf16 v[220:223], v[32:35], v[224:227], v[220:223]\n\t" \
    "v_cvt_pk_bf16_f32 v204, v204, v205\n\t" \
    "v_mfma_f32_16x16x32_bf16 v[220:223], v[36:39], v[228:231], v[220:223]\n\t" \
    "v_cvt_pk_bf16_f32 v205, v206, v207\n\t" \
    "v_mfma_f32_16x16x32_bf16 v[212:215], v[16:19], v[224:227], v[212:215]\n\t" \
    "ds_write_b64 v254, v[204:205] offset:7176\n\t" \
    "v_mfma_f32_16x16x32_bf16 v[212:215], v[20:23], v[228:231], v[212:215]\n\t" \
    "v_cvt_pk_bf16_f32 v208, v208, v209\n\t" \
    "s_waitcnt lgkmcnt(3)\n\t" \
    "v_mfma_f32_16x16x32_bf16 v[216:219], v[64:67], v[232:235], v[216:219]\n\t" \
    "ds_read_b128 v[224:227], v253 offset:27648\n\t" \
    "v_cvt_pk_bf16_f32 v209, v210, v211\n\t" \
    "s_waitcnt lgkmcnt(3)\n\t" \
    "v_mfma_f32_16x16x32_bf16 v[216:219], v[68:71], v[236:239], v[216:219]\n\t" \
    "ds_read_b128 v[228:231], v253 offset:32768\n\t" \
    "ds_write_b64 v254, v[208:209] offset:8200\n\t" \
    "v_mfma_f32_16x16x32_bf16 v[220:223], v[72:75], v[232:235], v[220:223]\n\t" \
    "v_cvt_pk_bf16_f32 v200, v200, v201\n\t" \
    "v_mfma_f32_16x16x32_bf16 v[220:223], v[76:79], v[236:239], v[220:223]\n\t" \
    "v_cvt_pk_bf16_f32 v201, v202, v203\n\t" \
    "v_mfma_f32_16x16x32_bf16 v[212:215], v[56:59], v[232:235], v[212:215]\n\t" \
    "ds_write_b64 v254, v[200:201] offset:6152\n\t" \
    "v_mfma_f32_16x16x32_bf16 v[212:215], v[60:63], v[236:239], v[212:215]\n\t" \
    "s_waitcnt lgkmcnt(5)\n\t" \
    "v_mfma_f32_16x16x32_bf16 v[216:219], v[104:107], v[240:243], v[216:219]\n\t" \
    "ds_read_b128 v[232:235], v253 offset:33792\n\t" \
    "s_waitcnt lgkmcnt(4)\n\t" \
    "v_mfma_f32_16x16x32_bf16 v[216:219], v[108:111], v[224:227], v[216:219]\n\t" \
    "ds_read_b128 v[236:239], v253 offset:38912\n\t" \
    "v_mfma_f32_16x16x32_bf16 v[220:223], v[112:115], v[240:243], v[220:223]\n\t" \
    "v_mfma_f32_16x16x32_bf16 v[220:223], v[116:119], v[224:227], v[220:223]\n\t" \
    "v_mfma_f32_16x16x32_bf16 v[212:215], v[96:99], v[240:243], v[212:215]\n\t" \
    "v_mfma_f32_16x16x32_bf16 v[212:215], v[100:103], v[224:227], v[212:215]\n\t" \
    "ds_read_b128 v[240:243], v253 offset:39936\n\t" \
    "s_waitcnt lgkmcnt(3)\n\t" \
    "s_barrier \n\t" \
    "v_mfma_f32_16x16x32_bf16 v[216:219], v[144:147], v[228:231], v[216:219]\n\t" \
    "s_waitcnt lgkmcnt(2)\n\t" \
    "v_mfma_f32_16x16x32_bf16 v[216:219], v[148:151], v[232:235], v[216:219]\n\t" \
    "s_waitcnt lgkmcnt(1)\n\t" \
    "v_mfma_f32_16x16x32_bf16 v[216:219], v[184:187], v[236:239], v[216:219]\n\t" \
    "s_waitcnt lgkmcnt(0)\n\t" \
    "v_mfma_f32_16x16x32_bf16 v[216:219], v[188:191], v[240:243], v[216:219]\n\t" \
    "v_mfma_f32_16x16x32_bf16 v[220:223], v[152:155], v[228:231], v[220:223]\n\t" \
    "v_mfma_f32_16x16x32_bf16 v[220:223], v[156:159], v[232:235], v[220:223]\n\t" \
    "v_mfma_f32_16x16x32_bf16 v[220:223], v[192:195], v[236:239], v[220:223]\n\t" \
    "v_cvt_pk_bf16_f32 v216, v216, v217\n\t" \
    "v_cvt_pk_bf16_f32 v217, v218, v219\n\t" \
    "v_mfma_f32_16x16x32_bf16 v[220:223], v[196:199], v[240:243], v[220:223]\n\t" \
    "ds_write_b64 v254, v[216:217] offset:13320\n\t" \
    "v_mfma_f32_16x16x32_bf16 v[212:215], v[136:139], v[228:231], v[212:215]\n\t" \
    "v_mfma_f32_16x16x32_bf16 v[212:215], v[140:143], v[232:235], v[212:215]\n\t" \
    "v_mfma_f32_16x16x32_bf16 v[212:215], v[176:179], v[236:239], v[212:215]\n\t" \
    "v_cvt_pk_bf16_f32 v220, v220, v221\n\t" \
    "v_cvt_pk_bf16_f32 v221, v222, v223\n\t" \
    "v_mfma_f32_16x16x32_bf16 v[212:215], v[180:183], v[240:243], v[212:215]\n\t" \
    "ds_write_b64 v254, v[220:221] offset:14344\n\t" \
    "s_nop 6\n\t" \
    "v_cvt_pk_bf16_f32 v212, v212, v213\n\t" \
    "v_cvt_pk_bf16_f32 v213, v214, v215\n\t" \
    "ds_write_b64 v254, v[212:213] offset:12296\n\t" \
    LST6 \
    LJIT \
    "s_waitcnt vmcnt(0) lgkmcnt(0)\n\t" \
    "s_barrier \n\t" \
    "s_setprio 2\n\t" \
    "s_add_u32 s44, s40, 1\n\t" \
    "s_min_u32 s44, s44, 63\n\t" \
    ".Lv28_%=:\n\t" \
    "v_cmp_ge_u32 vcc, s44, v252\n\t" \
    "s_cbranch_vccz .Lv29_%=\n\t" \
    "s_cmp_eq_u32 s41, 0\n\t" \
    "s_cbranch_scc1 .Lv29_%=\n\t" \
    "s_sub_u32 s41, s41, 1\n\t" \
    "s_sleep 2\n\t" \
    "v_and_b32 v252, 0xf0, v253\n\t" \
    "v_lshlrev_b32 v252, 3, v252\n\t" \
    "buffer_load_dword v252, v252, s[60:63], %[fl] offen sc1\n\t" \
    "s_waitcnt vmcnt(0)\n\t" \
    "s_branch .Lv28_%=\n\t" \
    ".Lv29_%=:\n\t" \
    "s_mov_b32 m0, 0x2000\n\t" \
    "s_add_u32 s49, s42, 0x0\n\t" \
    "buffer_load_dwordx4 v253, s[60:63], s49 offen offset:0 sc1 lds\n\t" \
    "s_sleep 1\n\t" \
    "buffer_load_dwordx4 v253, s[60:63], s49 offen offset:1024 sc1 lds\n\t" \
    "s_sleep 1\n\t" \
    "buffer_load_dwordx4 v253, s[60:63], s49 offen offset:2048 sc1 lds\n\t" \
    "s_sleep 1\n\t" \
    "buffer_load_dwordx4 v253, s[60:63], s49 offen offset:3072 sc1 lds\n\t" \
    "s_sleep 1\n\t" \
    "s_mov_b32 m0, 0x4800\n\t" \
    "s_add_u32 s49, s42, 0x8800\n\t" \
    "buffer_load_dwordx4 v253, s[60:63], s49 offen offset:0 sc1 lds\n\t" \
    "s_sleep 1\n\t" \
    "s_mov_b32 m0, 0x7000\n\t" \
    "s_add_u32 s49, s42, 0x1000\n\t" \
    "buffer_load_dwordx4 v253, s[60:63], s49 offen offset:0 sc1 lds\n\t" \
    "s_sleep 1\n\t" \
    "buffer_load_dwordx4 v253, s[60:63], s49 offen offset:1024 sc1 lds\n\t" \
    "s_sleep 1\n\t" \
    "buffer_load_dwordx4 v253, s[60:63], s49 offen offset:2048 sc1 lds\n\t" \
    "s_sleep 1\n\t" \
    "buffer_load_dwordx4 v253, s[60:63], s49 offen offset:3072 sc1 lds\n\t" \
    "s_sleep 1\n\t" \
    "s_mov_b32 m0, 0x9800\n\t" \
    "s_add_u32 s49, s42, 0x9800\n\t" \
    "buffer_load_dwordx4 v253, s[60:63], s49 offen offset:0 sc1 lds\n\t" \
    "s_sleep 1\n\t" \
    "ds_read_b128 v[200:203], v254 offset:51200\n\t" \
    "ds_read_b128 v[204:207], v254 offset:52224\n\t" \
    "ds_read_b128 v[216:219], v254 offset:53248\n\t" \
    "ds_read_b128 v[220:223], v254 offset:54272\n\t" \
    "v_mov_b64 v[208:209], 0\n\t" \
    "v_mov_b64 v[210:211], 0\n\t" \
    "v_mov_b64 v[244:245], 0\n\t" \
    "v_mov_b64 v[246:247], 0\n\t" \
    "v_mov_b64 v[248:249], 0\n\t" \
    "v_mov_b64 v[250:251], 0\n\t" \
    LST1 \
    LJIT \
    "s_sleep 5\n\t" \
    "s_mov_b32 m0, 0xb000\n\t" \
    "s_nop 0\n\t" \
    "buffer_load_dwordx4 v253, s[60:63], s46 offen sc1 lds\n\t" \
    "s_mov_b32 m0, 0xbc00\n\t" \
    "s_nop 0\n\t" \
    "buffer_load_dwordx4 v253, s[60:63], s47 offen sc1 lds\n\t" \
    "s_waitcnt lgkmcnt(0)\n\t" \
    LFLUSH212 \
    "v_bfe_i32 v212, v253, 5, 1\n\t" \
    "v_and_b32 v212, 0xaaaa, v212\n\t" \
    "v_or_b32 v212, 0x4444, v212\n\t" \
    "v_and_b32 v213, 0xffff, v255\n\t" \
    "v_lshrrev_b32 v214, 16, v255\n\t" \
    ".Lv210_%=:\n\t" \
    "s_waitcnt vmcnt(0)\n\t" \
    "ds_read_b128 v[224:227], v253 offset:45056\n\t" \
    "ds_read_b128 v[228:231], v253 offset:48128\n\t" \
    "s_waitcnt lgkmcnt(1)\n\t" \
    "v_max_u32 v215, v224, v225\n\t" \
    "v_max3_u32 v215, v226, v227, v215\n\t" \
    "s_waitcnt lgkmcnt(0)\n\t" \
    "v_max3_u32 v215, v228, v229, v215\n\t" \
    "v_max3_u32 v215, v230, v231, v215\n\t" \
    "v_cmp_eq_u32 vcc, -1, v215\n\t" \
    "s_cbranch_vccz .Lv211_%=\n\t" \
    "v_max_u32 v215, v224, v225\n\t" \
    "v_max3_u32 v215, v226, v227, v215\n\t" \
    "v_cmp_eq_u32 vcc, -1, v215\n\t" \
    "s_cbranch_vccz .Lv212_%=\n\t" \
    "s_mov_b32 m0, 0xb000\n\t" \
    "s_nop 0\n\t" \
    "buffer_load_dwordx4 v253, s[60:63], s46 offen sc1 lds\n\t" \
    ".Lv212_%=:\n\t" \
    "v_max_u32 v215, v228, v229\n\t" \
    "v_max3_u32 v215, v230, v231, v215\n\t" \
    "v_cmp_eq_u32 vcc, -1, v215\n\t" \
    "s_cbranch_vccz .Lv213_%=\n\t" \
    "s_mov_b32 m0, 0xbc00\n\t" \
    "s_nop 0\n\t" \
    "buffer_load_dwordx4 v253, s[60:63], s47 offen sc1 lds\n\t" \
    ".Lv213_%=:\n\t" \
    "s_cmp_eq_u32 s41, 0\n\t" \
    "s_cbranch_scc1 .Lv211_%=\n\t" \
    "s_sub_u32 s41, s41, 1\n\t" \
    "s_branch .Lv210_%=\n\t" \
    ".Lv211_%=:\n\t" \
    LST2 \
    LJIT \
    "s_waitcnt lgkmcnt(0)\n\t" \
    "s_barrier \n\t" \
    LST3 \
    LJIT \
    "ds_read_b128 v[232:235], v253 offset:40960\n\t" \
    "ds_read_b128 v[236:239], v253 offset:41984\n\t" \
    "ds_read_b128 v[240:243], v253 offset:43008\n\t" \
    "v_smfmac_f32_16x16x64_bf16 v[244:247], v[224:227], v[0:7], v212\n\t" \
    "v_smfmac_f32_16x16x64_bf16 v[248:251], v[224:227], v[8:15], v212\n\t" \
    "v_smfmac_f32_16x16x64_bf16 v[208:211], v[224:227], v[200:207], v212\n\t" \
    "ds_read_b128 v[200:203], v254 offset:55296\n\t" \
    "ds_read_b128 v[204:207], v254 offset:56320\n\t" \
    "ds_read_b128 v[224:227], v253 offset:44032\n\t" \
    "v_smfmac_f32_16x16x64_bf16 v[208:211], v[228:231], v[16:23], v212\n\t" \
    "v_smfmac_f32_16x16x64_bf16 v[244:247], v[228:231], v[24:31], v212\n\t" \
    "v_smfmac_f32_16x16x64_bf16 v[248:251], v[228:231], v[32:39], v212\n\t" \
    "ds_read_b128 v[228:231], v253 offset:46080\n\t" \
    "s_waitcnt lgkmcnt(6)\n\t" \
    "v_smfmac_f32_16x16x64_bf16 v[208:211], v[232:235], v[216:223], v212\n\t" \
    "ds_read_b128 v[216:219], v254 offset:57344\n\t" \
    "ds_read_b128 v[220:223], v254 offset:58368\n\t" \
    "v_smfmac_f32_16x16x64_bf16 v[244:247], v[232:235], v[40:47], v212\n\t" \
    "v_smfmac_f32_16x16x64_bf16 v[248:251], v[232:235], v[48:55], v212\n\t" \
    "ds_read_b128 v[232:235], v253 offset:47104\n\t" \
    "s_waitcnt lgkmcnt(8)\n\t" \
    "v_smfmac_f32_16x16x64_bf16 v[208:211], v[236:239], v[56:63], v212\n\t" \
    "v_smfmac_f32_16x16x64_bf16 v[244:247], v[236:239], v[64:71], v212\n\t" \
    "v_smfmac_f32_16x16x64_bf16 v[248:251], v[236:239], v[72:79], v212\n\t" \
    "ds_read_b128 v[236:239], v253 offset:49152\n\t" \
    "s_waitcnt lgkmcnt(6)\n\t" \
    "v_smfmac_f32_16x16x64_bf16 v[208:211], v[240:243], v[200:207], v212\n\t" \
    "ds_read_b128 v[200:203], v254 offset:59392\n\t" \
    "ds_read_b128 v[204:207], v254 offset:60416\n\t" \
    "v_smfmac_f32_16x16x64_bf16 v[244:247], v[240:243], v[80:87], v212\n\t" \
    "v_smfmac_f32_16x16x64_bf16 v[248:251], v[240:243], v[88:95], v212\n\t" \
    "ds_read_b128 v[240:243], v253 offset:50176\n\t" \
    "s_waitcnt lgkmcnt(8)\n\t" \
    "v_smfmac_f32_16x16x64_bf16 v[208:211], v[224:227], v[96:103], v212\n\t" \
    "v_smfmac_f32_16x16x64_bf16 v[244:247], v[224:227], v[104:111], v212\n\t" \
    "v_smfmac_f32_16x16x64_bf16 v[248:251], v[224:227], v[112:119], v212\n\t" \
    "s_waitcnt lgkmcnt(7)\n\t" \
    "v_smfmac_f32_16x16x64_bf16 v[244:247], v[228:231], v[120:127], v212\n\t" \
    "s_waitcnt lgkmcnt(4)\n\t" \
    "v_smfmac_f32_16x16x64_bf16 v[244:247], v[232:235], v[144:151], v212\n\t" \
    "s_waitcnt lgkmcnt(3)\n\t" \
    "v_smfmac_f32_16x16x64_bf16 v[244:247], v[236:239], v[160:167], v212\n\t" \
    "s_waitcnt lgkmcnt(0)\n\t" \
    "v_smfmac_f32_16x16x64_bf16 v[244:247], v[240:243], v[184:191], v212\n\t" \
    "v_smfmac_f32_16x16x64_bf16 v[248:251], v[228:231], v[128:135], v212\n\t" \
    "v_smfmac_f32_16x16x64_bf16 v[248:251], v[232:235], v[152:159], v212\n\t" \
    "v_smfmac_f32_16x16x64_bf16 v[248:251], v[236:239], v[168:175], v212\n\t" \
    "v_add_f32 v244, v244, v246\n\t" \
    "v_add_f32 v245, v245, v247\n\t" \
    "v_cvt_pk_bf16_f32 v244, v244, v245\n\t" \
    "v_smfmac_f32_16x16x64_bf16 v[248:251], v[240:243], v[192:199], v212\n\t" \
    "ds_write_b16 v213, v244 offset:512\n\t" \
    "ds_write_b16_d16_hi v213, v244 offset:528\n\t" \
    "v_smfmac_f32_16x16x64_bf16 v[208:211], v[228:231], v[216:223], v212\n\t" \
    "v_smfmac_f32_16x16x64_bf16 v[208:211], v[232:235], v[136:143], v212\n\t" \
    "v_smfmac_f32_16x16x64_bf16 v[208:211], v[236:239], v[200:207], v212\n\t" \
    "v_add_f32 v248, v248, v250\n\t" \
    "v_add_f32 v249, v249, v251\n\t" \
    "v_cvt_pk_bf16_f32 v248, v248, v249\n\t" \
    "v_smfmac_f32_16x16x64_bf16 v[208:211], v[240:243], v[176:183], v212\n\t" \
    "ds_write_b16 v213, v248 offset:1024\n\t" \
    "ds_write_b16_d16_hi v213, v248 offset:1040\n\t" \
    "s_nop 5\n\t" \
    "v_add_f32 v208, v208, v210\n\t" \
    "v_add_f32 v209, v209, v211\n\t" \
    "v_cvt_pk_bf16_f32 v208, v208, v209\n\t" \
    "ds_write_b16 v213, v208 offset:0\n\t" \
    "ds_write_b16_d16_hi v213, v208 offset:16\n\t" \
    "s_setprio 0\n\t" \
    "s_waitcnt lgkmcnt(0)\n\t" \
    "s_barrier \n\t" \
    LST4 \
    LJIT \
    "ds_read_b128 v[224:227], v253 offset:8192\n\t" \
    "ds_read_b128 v[244:247], v254 offset:51200\n\t" \
    "ds_read_b128 v[228:231], v253 offset:9216\n\t" \
    "ds_read_b128 v[232:235], v253 offset:28672\n\t" \
    "ds_read_b128 v[236:239], v253 offset:29696\n\t" \
    "ds_read_b128 v[248:251], v254 offset:52224\n\t" \
    "s_add_u32 s40, s40, 1\n\t" \
    "s_cmp_lg_u32 s40, 64\n\t" \
    "s_cbranch_scc1 .Lv27_%=\n\t" \
    "s_branch .Lend%=\n\t" \
    ".Lv1_%=:\n\t" \
    "s_mov_b32 s42, %[xs]\n\t" \
    "v_add_u32 v244, 0xc000, v243\n\t" \
    "s_waitcnt vmcnt(12) lgkmcnt(0)\n\t" \
    "s_barrier \n\t" \
    "s_add_u32 m0, %[wm], 0x9000\n\t" \
    "s_mov_b32 s44, 0x1e0000\n\t" \
    "buffer_load_dwordx4 v240, %[wr], s44 offen offset:0 nt lds\n\t" \
    "buffer_load_dwordx4 v241, %[wr], s44 offen offset:1024 nt lds\n\t" \
    "buffer_load_dwordx4 v242, %[wr], s44 offen offset:2048 nt lds\n\t" \
    "ds_read_b64_tr_b16 v[56:57], v244 offset:0\n\t" \
    "ds_read_b64_tr_b16 v[58:59], v244 offset:1536\n\t" \
    "ds_read_b64_tr_b16 v[64:65], v244 offset:32\n\t" \
    "ds_read_b64_tr_b16 v[66:67], v244 offset:1568\n\t" \
    "ds_read_b64_tr_b16 v[72:73], v244 offset:64\n\t" \
    "ds_read_b64_tr_b16 v[74:75], v244 offset:1600\n\t" \
    "v_add_u32 v244, 0xf000, v243\n\t" \
    "s_waitcnt vmcnt(12) lgkmcnt(0)\n\t" \
    "s_barrier \n\t" \
    "s_add_u32 m0, %[wm], 0xc000\n\t" \
    "s_mov_b32 s44, 0x240000\n\t" \
    "buffer_load_dwordx4 v240, %[wr], s44 offen offset:0 nt lds\n\t" \
    "buffer_load_dwordx4 v241, %[wr], s44 offen offset:1024 nt lds\n\t" \
    "buffer_load_dwordx4 v242, %[wr], s44 offen offset:2048 nt lds\n\t" \
    "ds_read_b64_tr_b16 v[60:61], v244 offset:0\n\t" \
    "ds_read_b64_tr_b16 v[62:63], v244 offset:1536\n\t" \
    "ds_read_b64_tr_b16 v[68:69], v244 offset:32\n\t" \
    "ds_read_b64_tr_b16 v[70:71], v244 offset:1568\n\t" \
    "ds_read_b64_tr_b16 v[76:77], v244 offset:64\n\t" \
    "ds_read_b64_tr_b16 v[78:79], v244 offset:1600\n\t" \
    "v_add_u32 v244, 0x0, v243\n\t" \
    "s_waitcnt vmcnt(12) lgkmcnt(0)\n\t" \
    "s_barrier \n\t" \
    "s_add_u32 m0, %[wm], 0xf000\n\t" \
    "s_mov_b32 s44, 0x2a0000\n\t" \
    "buffer_load_dwordx4 v240, %[wr], s44 offen offset:0 nt lds\n\t" \
    "buffer_load_dwordx4 v241, %[wr], s44 offen offset:1024 nt lds\n\t" \
    "buffer_load_dwordx4 v242, %[wr], s44 offen offset:2048 nt lds\n\t" \
    "ds_read_b64_tr_b16 v[96:97], v244 offset:0\n\t" \
    "ds_read_b64_tr_b16 v[98:99], v244 offset:1536\n\t" \
    "ds_read_b64_tr_b16 v[104:105], v244 offset:32\n\t" \
    "ds_read_b64_tr_b16 v[106:107], v244 offset:1568\n\t" \
    "ds_read_b64_tr_b16 v[112:113], v244 offset:64\n\t" \
    "ds_read_b64_tr_b16 v[114:115], v244 offset:1600\n\t" \
    "v_add_u32 v244, 0x3000, v243\n\t" \
    "s_waitcnt vmcnt(12) lgkmcnt(0)\n\t" \
    "s_barrier \n\t" \
    "s_add_u32 m0, %[wm], 0x0\n\t" \
    "s_mov_b32 s44, 0x300000\n\t" \
    "buffer_load_dwordx4 v240, %[wr], s44 offen offset:0 nt lds\n\t" \
    "buffer_load_dwordx4 v241, %[wr], s44 offen offset:1024 nt lds\n\t" \
    "buffer_load_dwordx4 v242, %[wr], s44 offen offset:2048 nt lds\n\t" \
    "ds_read_b64_tr_b16 v[100:101], v244 offset:0\n\t" \
    "ds_read_b64_tr_b16 v[102:103], v244 offset:1536\n\t" \
    "ds_read_b64_tr_b16 v[108:109], v244 offset:32\n\t" \
    "ds_read_b64_tr_b16 v[110:111], v244 offset:1568\n\t" \
    "ds_read_b64_tr_b16 v[116:117], v244 offset:64\n\t" \
    "ds_read_b64_tr_b16 v[118:119], v244 offset:1600\n\t" \
    "v_add_u32 v244, 0x6000, v243\n\t" \
    "s_waitcnt vmcnt(12) lgkmcnt(0)\n\t" \
    "s_barrier \n\t" \
    "s_add_u32 m0, %[wm], 0x3000\n\t" \
    "s_mov_b32 s44, 0x360000\n\t" \
    "buffer_load_dwordx4 v240, %[wr], s44 offen offset:0 nt lds\n\t" \
    "buffer_load_dwordx4 v241, %[wr], s44 offen offset:1024 nt lds\n\t" \
    "buffer_load_dwordx4 v242, %[wr], s44 offen offset:2048 nt lds\n\t" \
    "ds_read_b64_tr_b16 v[200:201], v244 offset:0\n\t" \
    "ds_read_b64_tr_b16 v[202:203], v244 offset:1536\n\t" \
    "ds_read_b64_tr_b16 v[0:1], v244 offset:32\n\t" \
    "ds_read_b64_tr_b16 v[2:3], v244 offset:1568\n\t" \
    "ds_read_b64_tr_b16 v[8:9], v244 offset:64\n\t" \
    "ds_read_b64_tr_b16 v[10:11], v244 offset:1600\n\t" \
    "v_add_u32 v244, 0x9000, v243\n\t" \
    "s_waitcnt vmcnt(12) lgkmcnt(0)\n\t" \
    "s_barrier \n\t" \
    "s_add_u32 m0, %[wm], 0x6000\n\t" \
    "s_mov_b32 s44, 0x3c0000\n\t" \
    "buffer_load_dwordx4 v240, %[wr], s44 offen offset:0 nt lds\n\t" \
    "buffer_load_dwordx4 v241, %[wr], s44 offen offset:1024 nt lds\n\t" \
    "buffer_load_dwordx4 v242, %[wr], s44 offen offset:2048 nt lds\n\t" \
    "ds_read_b64_tr_b16 v[204:205], v244 offset:0\n\t" \
    "ds_read_b64_tr_b16 v[206:207], v244 offset:1536\n\t" \
    "ds_read_b64_tr_b16 v[4:5], v244 offset:32\n\t" \
    "ds_read_b64_tr_b16 v[6:7], v244 offset:1568\n\t" \
    "ds_read_b64_tr_b16 v[12:13], v244 offset:64\n\t" \
    "ds_read_b64_tr_b16 v[14:15], v244 offset:1600\n\t" \
    "v_add_u32 v244, 0xc000, v243\n\t" \
    "s_waitcnt vmcnt(12) lgkmcnt(0)\n\t" \
    "s_barrier \n\t" \
    "s_add_u32 m0, %[wm], 0x9000\n\t" \
    "s_mov_b32 s44, 0x420000\n\t" \
    "buffer_load_dwordx4 v240, %[wr], s44 offen offset:0 nt lds\n\t" \
    "buffer_load_dwordx4 v241, %[wr], s44 offen offset:1024 nt lds\n\t" \
    "buffer_load_dwordx4 v242, %[wr], s44 offen offset:2048 nt lds\n\t" \
    "ds_read_b64_tr_b16 v[208:209], v244 offset:0\n\t" \
    "ds_read_b64_tr_b16 v[210:211], v244 offset:1536\n\t" \
    "ds_read_b64_tr_b16 v[40:41], v244 offset:32\n\t" \
    "ds_read_b64_tr_b16 v[42:43], v244 offset:1568\n\t" \
    "ds_read_b64_tr_b16 v[48:49], v244 offset:64\n\t" \
    "ds_read_b64_tr_b16 v[50:51], v244 offset:1600\n\t" \
    "v_add_u32 v244, 0xf000, v243\n\t" \
    "s_waitcnt vmcnt(12) lgkmcnt(0)\n\t" \
    "s_barrier \n\t" \
    "s_add_u32 m0, %[wm], 0xc000\n\t" \
    "s_mov_b32 s44, 0x480000\n\t" \
    "buffer_load_dwordx4 v240, %[wr], s44 offen offset:0 nt lds\n\t" \
    "buffer_load_dwordx4 v241, %[wr], s44 offen offset:1024 nt lds\n\t" \
    "buffer_load_dwordx4 v242, %[wr], s44 offen offset:2048 nt lds\n\t" \
    "ds_read_b64_tr_b16 v[212:213], v244 offset:0\n\t" \
    "ds_read_b64_tr_b16 v[214:215], v244 offset:1536\n\t" \
    "ds_read_b64_tr_b16 v[44:45], v244 offset:32\n\t" \
    "ds_read_b64_tr_b16 v[46:47], v244 offset:1568\n\t" \
    "ds_read_b64_tr_b16 v[52:53], v244 offset:64\n\t" \
    "ds_read_b64_tr_b16 v[54:55], v244 offset:1600\n\t" \
    "v_add_u32 v244, 0x0, v243\n\t" \
    "s_waitcnt vmcnt(12) lgkmcnt(0)\n\t" \
    "s_barrier \n\t" \
    "v_mov_b32 v246, 0\n\t" \
    "buffer_load_dword v246, v246, s[60:63], %[mo] offen sc1\n\t" \
    "s_add_u32 m0, %[wm], 0xf000\n\t" \
    "s_mov_b32 s44, 0x4e0000\n\t" \
    "buffer_load_dwordx4 v240, %[wr], s44 offen offset:0 nt lds\n\t" \
    "buffer_load_dwordx4 v241, %[wr], s44 offen offset:1024 nt lds\n\t" \
    "buffer_load_dwordx4 v242, %[wr], s44 offen offset:2048 nt lds\n\t" \
    "ds_read_b64_tr_b16 v[136:137], v244 offset:0\n\t" \
    "ds_read_b64_tr_b16 v[138:139], v244 offset:1536\n\t" \
    "ds_read_b64_tr_b16 v[144:145], v244 offset:32\n\t" \
    "ds_read_b64_tr_b16 v[146:147], v244 offset:1568\n\t" \
    "ds_read_b64_tr_b16 v[152:153], v244 offset:64\n\t" \
    "ds_read_b64_tr_b16 v[154:155], v244 offset:1600\n\t" \
    "v_add_u32 v244, 0x3000, v243\n\t" \
    "s_waitcnt vmcnt(12) lgkmcnt(0)\n\t" \
    "s_barrier \n\t" \
    "s_add_u32 m0, %[wm], 0x0\n\t" \
    "s_mov_b32 s44, 0x540000\n\t" \
    "buffer_load_dwordx4 v240, %[wr], s44 offen offset:0 nt lds\n\t" \
    "buffer_load_dwordx4 v241, %[wr], s44 offen offset:1024 nt lds\n\t" \
    "buffer_load_dwordx4 v242, %[wr], s44 offen offset:2048 nt lds\n\t" \
    "ds_read_b64_tr_b16 v[140:141], v244 offset:0\n\t" \
    "ds_read_b64_tr_b16 v[142:143], v244 offset:1536\n\t" \
    "ds_read_b64_tr_b16 v[148:149], v244 offset:32\n\t" \
    "ds_read_b64_tr_b16 v[150:151], v244 offset:1568\n\t" \
    "ds_read_b64_tr_b16 v[156:157], v244 offset:64\n\t" \
    "ds_read_b64_tr_b16 v[158:159], v244 offset:1600\n\t" \
    "v_add_u32 v244, 0x6000, v243\n\t" \
    "s_waitcnt vmcnt(12) lgkmcnt(0)\n\t" \
    "s_barrier \n\t" \
    "s_add_u32 m0, %[wm], 0x3000\n\t" \
    "s_mov_b32 s44, 0x5a0000\n\t" \
    "buffer_load_dwordx4 v240, %[wr], s44 offen offset:0 nt lds\n\t" \
    "buffer_load_dwordx4 v241, %[wr], s44 offen offset:1024 nt lds\n\t" \
    "buffer_load_dwordx4 v242, %[wr], s44 offen offset:2048 nt lds\n\t" \
    "ds_read_b64_tr_b16 v[216:217], v244 offset:0\n\t" \
    "ds_read_b64_tr_b16 v[218:219], v244 offset:1536\n\t" \
    "ds_read_b64_tr_b16 v[80:81], v244 offset:32\n\t" \
    "ds_read_b64_tr_b16 v[82:83], v244 offset:1568\n\t" \
    "ds_read_b64_tr_b16 v[88:89], v244 offset:64\n\t" \
    "ds_read_b64_tr_b16 v[90:91], v244 offset:1600\n\t" \
    "v_add_u32 v244, 0x9000, v243\n\t" \
    "s_waitcnt vmcnt(12) lgkmcnt(0)\n\t" \
    "s_barrier \n\t" \
    "s_add_u32 m0, %[wm], 0x6000\n\t" \
    "s_mov_b32 s44, 0x600000\n\t" \
    "buffer_load_dwordx4 v240, %[wr], s44 offen offset:0 nt lds\n\t" \
    "buffer_load_dwordx4 v241, %[wr], s44 offen offset:1024 nt lds\n\t" \
    "buffer_load_dwordx4 v242, %[wr], s44 offen offset:2048 nt lds\n\t" \
    "ds_read_b64_tr_b16 v[220:221], v244 offset:0\n\t" \
    "ds_read_b64_tr_b16 v[222:223], v244 offset:1536\n\t" \
    "ds_read_b64_tr_b16 v[84:85], v244 offset:32\n\t" \
    "ds_read_b64_tr_b16 v[86:87], v244 offset:1568\n\t" \
    "ds_read_b64_tr_b16 v[92:93], v244 offset:64\n\t" \
    "ds_read_b64_tr_b16 v[94:95], v244 offset:1600\n\t" \
    "v_add_u32 v244, 0xc000, v243\n\t" \
    "s_waitcnt vmcnt(12) lgkmcnt(0)\n\t" \
    "s_barrier \n\t" \
    "s_add_u32 m0, %[wm], 0x9000\n\t" \
    "s_mov_b32 s44, 0x660000\n\t" \
    "buffer_load_dwordx4 v240, %[wr], s44 offen offset:0 nt lds\n\t" \
    "buffer_load_dwordx4 v241, %[wr], s44 offen offset:1024 nt lds\n\t" \
    "buffer_load_dwordx4 v242, %[wr], s44 offen offset:2048 nt lds\n\t" \
    "ds_read_b64_tr_b16 v[16:17], v244 offset:0\n\t" \
    "ds_read_b64_tr_b16 v[18:19], v244 offset:1536\n\t" \
    "ds_read_b64_tr_b16 v[24:25], v244 offset:32\n\t" \
    "ds_read_b64_tr_b16 v[26:27], v244 offset:1568\n\t" \
    "ds_read_b64_tr_b16 v[32:33], v244 offset:64\n\t" \
    "ds_read_b64_tr_b16 v[34:35], v244 offset:1600\n\t" \
    "v_add_u32 v244, 0xf000, v243\n\t" \
    "s_waitcnt vmcnt(12) lgkmcnt(0)\n\t" \
    "s_barrier \n\t" \
    ".Lv11_%=:\n\t" \
    "v_readfirstlane_b32 s43, v246\n\t" \
    "s_nop 0\n\t" \
    "s_cmp_lg_u32 s43, -1\n\t" \
    "s_cbranch_scc1 .Lv12_%=\n\t" \
    "s_cmp_eq_u32 s41, 0\n\t" \
    "s_cbranch_scc1 .Lv12_%=\n\t" \
    "s_sub_u32 s41, s41, 1\n\t" \
    "s_sleep 4\n\t" \
    "v_mov_b32 v246, 0\n\t" \
    "buffer_load_dword v246, v246, s[60:63], %[mo] offen sc1\n\t" \
    "s_waitcnt vmcnt(0)\n\t" \
    "s_branch .Lv11_%=\n\t" \
    ".Lv12_%=:\n\t" \
    "v_mov_b32 v246, 0x8000\n\t" \
    "s_mov_b64 exec, 1\n\t" \
    "buffer_store_dword v246, v246, s[60:63], %[mo] offen sc1\n\t" \
    "s_mov_b64 exec, -1\n\t" \
    "s_and_b32 s44, s43, 0x5000\n\t" \
    "s_xor_b32 %[mb], %[mb], s44\n\t" \
    "s_xor_b32 %[fl], %[fl], s44\n\t" \
    "s_and_b32 s43, s43, 0x2000\n\t" \
    "s_xor_b32 s43, s43, %[lx]\n\t" \
    "s_add_u32 s60, s60, s43\n\t" \
    "s_addc_u32 s61, s61, 0\n\t" \
    "v_and_b32 v252, 0xf0, v253\n\t" \
    "v_lshlrev_b32 v252, 3, v252\n\t" \
    "buffer_load_dword v252, v252, s[60:63], %[fl] offen sc1\n\t" \
    "s_add_u32 m0, %[wm], 0xc000\n\t" \
    "s_mov_b32 s44, 0x6c0000\n\t" \
    "buffer_load_dwordx4 v240, %[wr], s44 offen offset:0 nt lds\n\t" \
    "buffer_load_dwordx4 v241, %[wr], s44 offen offset:1024 nt lds\n\t" \
    "buffer_load_dwordx4 v242, %[wr], s44 offen offset:2048 nt lds\n\t" \
    "ds_read_b64_tr_b16 v[20:21], v244 offset:0\n\t" \
    "ds_read_b64_tr_b16 v[22:23], v244 offset:1536\n\t" \
    "ds_read_b64_tr_b16 v[28:29], v244 offset:32\n\t" \
    "ds_read_b64_tr_b16 v[30:31], v244 offset:1568\n\t" \
    "ds_read_b64_tr_b16 v[36:37], v244 offset:64\n\t" \
    "ds_read_b64_tr_b16 v[38:39], v244 offset:1600\n\t" \
    "v_add_u32 v244, 0x0, v243\n\t" \
    "s_waitcnt vmcnt(12) lgkmcnt(0)\n\t" \
    "s_barrier \n\t" \
    "s_add_u32 m0, %[wm], 0xf000\n\t" \
    "s_mov_b32 s44, 0x720000\n\t" \
    "buffer_load_dwordx4 v240, %[wr], s44 offen offset:0 nt lds\n\t" \
    "buffer_load_dwordx4 v241, %[wr], s44 offen offset:1024 nt lds\n\t" \
    "buffer_load_dwordx4 v242, %[wr], s44 offen offset:2048 nt lds\n\t" \
    "ds_read_b64_tr_b16 v[224:225], v244 offset:0\n\t" \
    "ds_read_b64_tr_b16 v[226:227], v244 offset:1536\n\t" \
    "ds_read_b64_tr_b16 v[120:121], v244 offset:32\n\t" \
    "ds_read_b64_tr_b16 v[122:123], v244 offset:1568\n\t" \
    "ds_read_b64_tr_b16 v[128:129], v244 offset:64\n\t" \
    "ds_read_b64_tr_b16 v[130:131], v244 offset:1600\n\t" \
    "v_add_u32 v244, 0x3000, v243\n\t" \
    "s_waitcnt vmcnt(12) lgkmcnt(0)\n\t" \
    "s_barrier \n\t" \
    "ds_read_b64_tr_b16 v[228:229], v244 offset:0\n\t" \
    "ds_read_b64_tr_b16 v[230:231], v244 offset:1536\n\t" \
    "ds_read_b64_tr_b16 v[124:125], v244 offset:32\n\t" \
    "ds_read_b64_tr_b16 v[126:127], v244 offset:1568\n\t" \
    "ds_read_b64_tr_b16 v[132:133], v244 offset:64\n\t" \
    "ds_read_b64_tr_b16 v[134:135], v244 offset:1600\n\t" \
    "v_add_u32 v244, 0x6000, v243\n\t" \
    "s_waitcnt vmcnt(9) lgkmcnt(0)\n\t" \
    "s_barrier \n\t" \
    "ds_read_b64_tr_b16 v[232:233], v244 offset:0\n\t" \
    "ds_read_b64_tr_b16 v[234:235], v244 offset:1536\n\t" \
    "ds_read_b64_tr_b16 v[160:161], v244 offset:32\n\t" \
    "ds_read_b64_tr_b16 v[162:163], v244 offset:1568\n\t" \
    "ds_read_b64_tr_b16 v[168:169], v244 offset:64\n\t" \
    "ds_read_b64_tr_b16 v[170:171], v244 offset:1600\n\t" \
    "v_add_u32 v244, 0x9000, v243\n\t" \
    "s_waitcnt vmcnt(6) lgkmcnt(0)\n\t" \
    "s_barrier \n\t" \
    "ds_read_b64_tr_b16 v[236:237], v244 offset:0\n\t" \
    "ds_read_b64_tr_b16 v[238:239], v244 offset:1536\n\t" \
    "ds_read_b64_tr_b16 v[164:165], v244 offset:32\n\t" \
    "ds_read_b64_tr_b16 v[166:167], v244 offset:1568\n\t" \
    "ds_read_b64_tr_b16 v[172:173], v244 offset:64\n\t" \
    "ds_read_b64_tr_b16 v[174:175], v244 offset:1600\n\t" \
    "v_add_u32 v244, 0xc000, v243\n\t" \
    "s_waitcnt vmcnt(3) lgkmcnt(0)\n\t" \
    "s_barrier \n\t" \
    "ds_read_b64_tr_b16 v[176:177], v244 offset:0\n\t" \
    "ds_read_b64_tr_b16 v[178:179], v244 offset:1536\n\t" \
    "ds_read_b64_tr_b16 v[184:185], v244 offset:32\n\t" \
    "ds_read_b64_tr_b16 v[186:187], v244 offset:1568\n\t" \
    "ds_read_b64_tr_b16 v[192:193], v244 offset:64\n\t" \
    "ds_read_b64_tr_b16 v[194:195], v244 offset:1600\n\t" \
    "s_mov_b32 s43, 0\n\t" \
    "v_cmp_eq_u32 vcc, 0, v252\n\t" \
    "s_cbranch_vccnz .Lv13_%=\n\t" \
    "s_mov_b32 m0, 0x1000\n\t" \
    "s_add_u32 s49, s42, 0x0\n\t" \
    "buffer_load_dwordx4 v253, s[60:63], s49 offen offset:0 sc1 lds\n\t" \
    "buffer_load_dwordx4 v253, s[60:63], s49 offen offset:1024 sc1 lds\n\t" \
    "buffer_load_dwordx4 v253, s[60:63], s49 offen offset:2048 sc1 lds\n\t" \
    "buffer_load_dwordx4 v253, s[60:63], s49 offen offset:3072 sc1 lds\n\t" \
    "s_mov_b32 m0, 0x4400\n\t" \
    "s_add_u32 s49, s42, 0xc400\n\t" \
    "buffer_load_dwordx4 v253, s[60:63], s49 offen offset:0 sc1 lds\n\t" \
    "s_mov_b32 m0, 0x6000\n\t" \
    "s_add_u32 s49, s42, 0x1000\n\t" \
    "buffer_load_dwordx4 v253, s[60:63], s49 offen offset:0 sc1 lds\n\t" \
    "buffer_load_dwordx4 v253, s[60:63], s49 offen offset:1024 sc1 lds\n\t" \
    "buffer_load_dwordx4 v253, s[60:63], s49 offen offset:2048 sc1 lds\n\t" \
    "buffer_load_dwordx4 v253, s[60:63], s49 offen offset:3072 sc1 lds\n\t" \
    "s_mov_b32 m0, 0x9400\n\t" \
    "s_add_u32 s49, s42, 0xd400\n\t" \
    "buffer_load_dwordx4 v253, s[60:63], s49 offen offset:0 sc1 lds\n\t" \
    "s_mov_b32 s43, 1\n\t" \
    ".Lv13_%=:\n\t" \
    "v_add_u32 v244, 0xf000, v243\n\t" \
    "s_waitcnt vmcnt(0) lgkmcnt(0)\n\t" \
    "s_barrier \n\t" \
    "ds_read_b64_tr_b16 v[180:181], v244 offset:0\n\t" \
    "ds_read_b64_tr_b16 v[182:183], v244 offset:1536\n\t" \
    "ds_read_b64_tr_b16 v[188:189], v244 offset:32\n\t" \
    "ds_read_b64_tr_b16 v[190:191], v244 offset:1568\n\t" \
    "ds_read_b64_tr_b16 v[196:197], v244 offset:64\n\t" \
    "ds_read_b64_tr_b16 v[198:199], v244 offset:1600\n\t" \
    "s_waitcnt lgkmcnt(0)\n\t" \
    "s_barrier \n\t" \
    LST0 \
    LJIT \
    "ds_write_b128 v254, v[200:203] offset:40960\n\t" \
    "ds_write_b128 v254, v[204:207] offset:41984\n\t" \
    "ds_write_b128 v254, v[208:211] offset:43008\n\t" \
    "ds_write_b128 v254, v[212:215] offset:44032\n\t" \
    "ds_write_b128 v254, v[216:219] offset:45056\n\t" \
    "ds_write_b128 v254, v[220:223] offset:46080\n\t" \
    "ds_write_b128 v254, v[224:227] offset:47104\n\t" \
    "ds_write_b128 v254, v[228:231] offset:48128\n\t" \
    "ds_write_b128 v254, v[232:235] offset:49152\n\t" \
    "ds_write_b128 v254, v[236:239] offset:50176\n\t" \
    "s_cmp_lg_u32 s43, 0\n\t" \
    "s_cbranch_scc1 .Lv14_%=\n\t" \
    "s_mov_b32 s44, 0\n\t" \
    ".Lv15_%=:\n\t" \
    "v_cmp_ge_u32 vcc, s44, v252\n\t" \
    "s_cbranch_vccz .Lv16_%=\n\t" \
    "s_cmp_eq_u32 s41, 0\n\t" \
    "s_cbranch_scc1 .Lv16_%=\n\t" \
    "s_sub_u32 s41, s41, 1\n\t" \
    "s_sleep 8\n\t" \
    "v_and_b32 v252, 0xf0, v253\n\t" \
    "v_lshlrev_b32 v252, 3, v252\n\t" \
    "buffer_load_dword v252, v252, s[60:63], %[fl] offen sc1\n\t" \
    "s_waitcnt vmcnt(0)\n\t" \
    "s_branch .Lv15_%=\n\t" \
    ".Lv16_%=:\n\t" \
    "s_mov_b32 m0, 0x1000\n\t" \
    "s_add_u32 s49, s42, 0x0\n\t" \
    "buffer_load_dwordx4 v253, s[60:63], s49 offen offset:0 sc1 lds\n\t" \
    "buffer_load_dwordx4 v253, s[60:63], s49 offen offset:1024 sc1 lds\n\t" \
    "buffer_load_dwordx4 v253, s[60:63], s49 offen offset:2048 sc1 lds\n\t" \
    "buffer_load_dwordx4 v253, s[60:63], s49 offen offset:3072 sc1 lds\n\t" \
    "s_mov_b32 m0, 0x4400\n\t" \
    "s_add_u32 s49, s42, 0xc400\n\t" \
    "buffer_load_dwordx4 v253, s[60:63], s49 offen offset:0 sc1 lds\n\t" \
    "s_mov_b32 m0, 0x6000\n\t" \
    "s_add_u32 s49, s42, 0x1000\n\t" \
    "buffer_load_dwordx4 v253, s[60:63], s49 offen offset:0 sc1 lds\n\t" \
    "buffer_load_dwordx4 v253, s[60:63], s49 offen offset:1024 sc1 lds\n\t" \
    "buffer_load_dwordx4 v253, s[60:63], s49 offen offset:2048 sc1 lds\n\t" \
    "buffer_load_dwordx4 v253, s[60:63], s49 offen offset:3072 sc1 lds\n\t" \
    "s_mov_b32 m0, 0x9400\n\t" \
    "s_add_u32 s49, s42, 0xd400\n\t" \
    "buffer_load_dwordx4 v253, s[60:63], s49 offen offset:0 sc1 lds\n\t" \
    ".Lv14_%=:\n\t" \
    "s_waitcnt vmcnt(0) lgkmcnt(0)\n\t" \
    "s_barrier \n\t" \
    "ds_read_b128 v[224:227], v253 offset:4096\n\t" \
    "ds_read_b128 v[244:247], v254 offset:40960\n\t" \
    "ds_read_b128 v[228:231], v253 offset:5120\n\t" \
    "ds_read_b128 v[232:235], v253 offset:24576\n\t" \
    "ds_read_b128 v[236:239], v253 offset:25600\n\t" \
    "ds_read_b128 v[248:251], v254 offset:41984\n\t" \
    ".p2align 8\n\t" \
    ".Lv17_%=:\n\t" \
    LST7 \
    LJIT \
    "s_waitcnt lgkmcnt(5)\n\t" \
    "v_mfma_f32_16x16x32_bf16 v[204:207], v[0:3], v[224:227], 0\n\t" \
    "s_waitcnt lgkmcnt(3)\n\t" \
    "v_mfma_f32_16x16x32_bf16 v[204:207], v[4:7], v[228:231], v[204:207]\n\t" \
    "ds_read_b128 v[240:243], v253 offset:12288\n\t" \
    "v_mfma_f32_16x16x32_bf16 v[208:211], v[8:11], v[224:227], 0\n\t" \
    "v_mfma_f32_16x16x32_bf16 v[208:211], v[12:15], v[228:231], v[208:211]\n\t" \
    "v_mfma_f32_16x16x32_bf16 v[200:203], v[244:247], v[224:227], 0\n\t" \
    "s_waitcnt lgkmcnt(1)\n\t" \
    "v_mfma_f32_16x16x32_bf16 v[200:203], v[248:251], v[228:231], v[200:203]\n\t" \
    "v_mfma_f32_16x16x32_bf16 v[216:219], v[0:3], v[232:235], 0\n\t" \
    "ds_read_b128 v[224:227], v253 offset:13312\n\t" \
    "v_mfma_f32_16x16x32_bf16 v[216:219], v[4:7], v[236:239], v[216:219]\n\t" \
    "ds_read_b128 v[228:231], v253 offset:6144\n\t" \
    "v_mfma_f32_16x16x32_bf16 v[220:223], v[8:11], v[232:235], 0\n\t" \
    "v_mfma_f32_16x16x32_bf16 v[220:223], v[12:15], v[236:239], v[220:223]\n\t" \
    "v_mfma_f32_16x16x32_bf16 v[212:215], v[244:247], v[232:235], 0\n\t" \
    "ds_read_b128 v[244:247], v254 offset:43008\n\t" \
    "v_mfma_f32_16x16x32_bf16 v[212:215], v[248:251], v[236:239], v[212:215]\n\t" \
    "ds_read_b128 v[248:251], v254 offset:44032\n\t" \
    "s_waitcnt lgkmcnt(4)\n\t" \
    "v_mfma_f32_16x16x32_bf16 v[204:207], v[24:27], v[240:243], v[204:207]\n\t" \
    "ds_read_b128 v[232:235], v253 offset:7168\n\t" \
    "s_waitcnt lgkmcnt(4)\n\t" \
    "v_mfma_f32_16x16x32_bf16 v[204:207], v[28:31], v[224:227], v[204:207]\n\t" \
    "ds_read_b128 v[236:239], v253 offset:26624\n\t" \
    "v_mfma_f32_16x16x32_bf16 v[208:211], v[32:35], v[240:243], v[208:211]\n\t" \
    "v_mfma_f32_16x16x32_bf16 v[208:211], v[36:39], v[224:227], v[208:211]\n\t" \
    "v_mfma_f32_16x16x32_bf16 v[200:203], v[16:19], v[240:243], v[200:203]\n\t" \
    "v_mfma_f32_16x16x32_bf16 v[200:203], v[20:23], v[224:227], v[200:203]\n\t" \
    "s_waitcnt lgkmcnt(4)\n\t" \
    "v_mfma_f32_16x16x32_bf16 v[204:207], v[40:43], v[228:231], v[204:207]\n\t" \
    "ds_read_b128 v[240:243], v253 offset:27648\n\t" \
    "s_waitcnt lgkmcnt(2)\n\t" \
    "v_mfma_f32_16x16x32_bf16 v[204:207], v[44:47], v[232:235], v[204:207]\n\t" \
    "ds_read_b128 v[224:227], v253 offset:0\n\t" \
    "v_mfma_f32_16x16x32_bf16 v[208:211], v[48:51], v[228:231], v[208:211]\n\t" \
    "v_mfma_f32_16x16x32_bf16 v[208:211], v[52:55], v[232:235], v[208:211]\n\t" \
    "v_mfma_f32_16x16x32_bf16 v[200:203], v[244:247], v[228:231], v[200:203]\n\t" \
    "v_mfma_f32_16x16x32_bf16 v[200:203], v[248:251], v[232:235], v[200:203]\n\t" \
    "s_waitcnt lgkmcnt(2)\n\t" \
    "v_mfma_f32_16x16x32_bf16 v[216:219], v[40:43], v[236:239], v[216:219]\n\t" \
    "ds_read_b128 v[228:231], v253 offset:1024\n\t" \
    "s_add_u32 s44, s40, 1\n\t" \
    "s_waitcnt lgkmcnt(2)\n\t" \
    "v_mfma_f32_16x16x32_bf16 v[216:219], v[44:47], v[240:243], v[216:219]\n\t" \
    "ds_read_b128 v[232:235], v253 offset:10240\n\t" \
    "s_and_b32 s45, s44, 15\n\t" \
    "v_mfma_f32_16x16x32_bf16 v[220:223], v[48:51], v[236:239], v[220:223]\n\t" \
    "s_cmp_eq_u32 s45, 0\n\t" \
    "v_mfma_f32_16x16x32_bf16 v[220:223], v[52:55], v[240:243], v[220:223]\n\t" \
    "s_cselect_b32 s45, %[cj], %[cs]\n\t" \
    "v_mfma_f32_16x16x32_bf16 v[212:215], v[244:247], v[236:239], v[212:215]\n\t" \
    "ds_read_b128 v[244:247], v254 offset:45056\n\t" \
    "s_cmp_eq_u32 s44, 64\n\t" \
    "v_mfma_f32_16x16x32_bf16 v[212:215], v[248:251], v[240:243], v[212:215]\n\t" \
    "ds_read_b128 v[248:251], v254 offset:46080\n\t" \
    "s_cselect_b32 s45, 0, s45\n\t" \
    "s_waitcnt lgkmcnt(4)\n\t" \
    "v_mfma_f32_16x16x32_bf16 v[204:207], v[64:67], v[224:227], v[204:207]\n\t" \
    "ds_read_b128 v[236:239], v253 offset:11264\n\t" \
    "s_add_u32 s42, s42, s45\n\t" \
    "s_waitcnt lgkmcnt(4)\n\t" \
    "v_mfma_f32_16x16x32_bf16 v[204:207], v[68:71], v[228:231], v[204:207]\n\t" \
    "ds_read_b128 v[240:243], v253 offset:30720\n\t" \
    "v_mfma_f32_16x16x32_bf16 v[208:211], v[72:75], v[224:227], v[208:211]\n\t" \
    "v_mfma_f32_16x16x32_bf16 v[208:211], v[76:79], v[228:231], v[208:211]\n\t" \
    "v_mfma_f32_16x16x32_bf16 v[200:203], v[56:59], v[224:227], v[200:203]\n\t" \
    "v_mfma_f32_16x16x32_bf16 v[200:203], v[60:63], v[228:231], v[200:203]\n\t" \
    "s_waitcnt lgkmcnt(4)\n\t" \
    "v_mfma_f32_16x16x32_bf16 v[204:207], v[80:83], v[232:235], v[204:207]\n\t" \
    "ds_read_b128 v[224:227], v253 offset:31744\n\t" \
    "s_waitcnt lgkmcnt(2)\n\t" \
    "v_mfma_f32_16x16x32_bf16 v[204:207], v[84:87], v[236:239], v[204:207]\n\t" \
    "ds_read_b128 v[228:231], v253 offset:2048\n\t" \
    "v_mfma_f32_16x16x32_bf16 v[208:211], v[88:91], v[232:235], v[208:211]\n\t" \
    "v_mfma_f32_16x16x32_bf16 v[208:211], v[92:95], v[236:239], v[208:211]\n\t" \
    "v_mfma_f32_16x16x32_bf16 v[200:203], v[244:247], v[232:235], v[200:203]\n\t" \
    "v_mfma_f32_16x16x32_bf16 v[200:203], v[248:251], v[236:239], v[200:203]\n\t" \
    "s_waitcnt lgkmcnt(2)\n\t" \
    "v_mfma_f32_16x16x32_bf16 v[216:219], v[80:83], v[240:243], v[216:219]\n\t" \
    "ds_read_b128 v[232:235], v253 offset:3072\n\t" \
    "s_and_b32 s44, s40, 3\n\t" \
    "s_waitcnt lgkmcnt(2)\n\t" \
    "v_mfma_f32_16x16x32_bf16 v[216:219], v[84:87], v[224:227], v[216:219]\n\t" \
    "ds_read_b128 v[236:239], v253 offset:14336\n\t" \
    "s_lshl_b32 s44, s44, 22\n\t" \
    "v_mfma_f32_16x16x32_bf16 v[220:223], v[88:91], v[240:243], v[220:223]\n\t" \
    "s_add_u32 s44, s44, %[mb]\n\t" \
    "v_mfma_f32_16x16x32_bf16 v[220:223], v[92:95], v[224:227], v[220:223]\n\t" \
    "s_add_u32 s46, s44, 0x800\n\t" \
    "v_mfma_f32_16x16x32_bf16 v[212:215], v[244:247], v[240:243], v[212:215]\n\t" \
    "ds_read_b128 v[244:247], v254 offset:47104\n\t" \
    "s_add_u32 s47, s44, 0x8800\n\t" \
    "v_mfma_f32_16x16x32_bf16 v[212:215], v[248:251], v[224:227], v[212:215]\n\t" \
    "ds_read_b128 v[248:251], v254 offset:48128\n\t" \
    "s_add_u32 s48, s44, 0xc00\n\t" \
    "s_waitcnt lgkmcnt(4)\n\t" \
    "v_mfma_f32_16x16x32_bf16 v[204:207], v[104:107], v[228:231], v[204:207]\n\t" \
    "ds_read_b128 v[240:243], v253 offset:15360\n\t" \
    "s_waitcnt lgkmcnt(4)\n\t" \
    "v_mfma_f32_16x16x32_bf16 v[204:207], v[108:111], v[232:235], v[204:207]\n\t" \
    "ds_read_b128 v[224:227], v253 offset:34816\n\t" \
    "v_mfma_f32_16x16x32_bf16 v[208:211], v[112:115], v[228:231], v[208:211]\n\t" \
    "v_mfma_f32_16x16x32_bf16 v[208:211], v[116:119], v[232:235], v[208:211]\n\t" \
    "v_mfma_f32_16x16x32_bf16 v[200:203], v[96:99], v[228:231], v[200:203]\n\t" \
    "v_mfma_f32_16x16x32_bf16 v[200:203], v[100:103], v[232:235], v[200:203]\n\t" \
    "s_waitcnt lgkmcnt(4)\n\t" \
    "v_mfma_f32_16x16x32_bf16 v[204:207], v[120:123], v[236:239], v[204:207]\n\t" \
    "ds_read_b128 v[228:231], v253 offset:35840\n\t" \
    "s_waitcnt lgkmcnt(2)\n\t" \
    "v_mfma_f32_16x16x32_bf16 v[204:207], v[124:127], v[240:243], v[204:207]\n\t" \
    "ds_read_b128 v[232:235], v253 offset:8192\n\t" \
    "v_mfma_f32_16x16x32_bf16 v[208:211], v[128:131], v[236:239], v[208:211]\n\t" \
    "v_mfma_f32_16x16x32_bf16 v[208:211], v[132:135], v[240:243], v[208:211]\n\t" \
    "v_mfma_f32_16x16x32_bf16 v[200:203], v[244:247], v[236:239], v[200:203]\n\t" \
    "v_mfma_f32_16x16x32_bf16 v[200:203], v[248:251], v[240:243], v[200:203]\n\t" \
    "s_waitcnt lgkmcnt(2)\n\t" \
    "v_mfma_f32_16x16x32_bf16 v[216:219], v[120:123], v[224:227], v[216:219]\n\t" \
    "ds_read_b128 v[236:239], v253 offset:9216\n\t" \
    "s_waitcnt lgkmcnt(2)\n\t" \
    "v_mfma_f32_16x16x32_bf16 v[216:219], v[124:127], v[228:231], v[216:219]\n\t" \
    "ds_read_b128 v[240:243], v253 offset:16384\n\t" \
    "v_mfma_f32_16x16x32_bf16 v[220:223], v[128:131], v[224:227], v[220:223]\n\t" \
    "v_mfma_f32_16x16x32_bf16 v[220:223], v[132:135], v[228:231], v[220:223]\n\t" \
    "v_mfma_f32_16x16x32_bf16 v[212:215], v[244:247], v[224:227], v[212:215]\n\t" \
    "ds_read_b128 v[244:247], v254 offset:49152\n\t" \
    "v_mfma_f32_16x16x32_bf16 v[212:215], v[248:251], v[228:231], v[212:215]\n\t" \
    "ds_read_b128 v[248:251], v254 offset:50176\n\t" \
    "v_and_b32 v252, 0xf0, v253\n\t" \
    "v_lshlrev_b32 v252, 3, v252\n\t" \
    "buffer_load_dword v252, v252, s[60:63], %[fl] offen sc1\n\t" \
    "s_waitcnt lgkmcnt(4)\n\t" \
    "v_mfma_f32_16x16x32_bf16 v[204:207], v[144:147], v[232:235], v[204:207]\n\t" \
    "ds_read_b128 v[224:227], v253 offset:17408\n\t" \
    "s_waitcnt lgkmcnt(4)\n\t" \
    "v_mfma_f32_16x16x32_bf16 v[204:207], v[148:151], v[236:239], v[204:207]\n\t" \
    "ds_read_b128 v[228:231], v253 offset:36864\n\t" \
    "v_mfma_f32_16x16x32_bf16 v[208:211], v[152:155], v[232:235], v[208:211]\n\t" \
    "v_mfma_f32_16x16x32_bf16 v[208:211], v[156:159], v[236:239], v[208:211]\n\t" \
    "v_mfma_f32_16x16x32_bf16 v[200:203], v[136:139], v[232:235], v[200:203]\n\t" \
    "v_mfma_f32_16x16x32_bf16 v[200:203], v[140:143], v[236:239], v[200:203]\n\t" \
    "s_waitcnt lgkmcnt(4)\n\t" \
    "v_mfma_f32_16x16x32_bf16 v[204:207], v[160:163], v[240:243], v[204:207]\n\t" \
    "ds_read_b128 v[232:235], v253 offset:37888\n\t" \
    "s_waitcnt lgkmcnt(2)\n\t" \
    "v_mfma_f32_16x16x32_bf16 v[204:207], v[164:167], v[224:227], v[204:207]\n\t" \
    "ds_read_b128 v[236:239], v253 offset:18432\n\t" \
    "v_mfma_f32_16x16x32_bf16 v[208:211], v[168:171], v[240:243], v[208:211]\n\t" \
    "v_mfma_f32_16x16x32_bf16 v[208:211], v[172:175], v[224:227], v[208:211]\n\t" \
    "v_mfma_f32_16x16x32_bf16 v[200:203], v[244:247], v[240:243], v[200:203]\n\t" \
    "v_mfma_f32_16x16x32_bf16 v[200:203], v[248:251], v[224:227], v[200:203]\n\t" \
    "s_waitcnt lgkmcnt(2)\n\t" \
    "v_mfma_f32_16x16x32_bf16 v[216:219], v[160:163], v[228:231], v[216:219]\n\t" \
    "ds_read_b128 v[240:243], v253 offset:19456\n\t" \
    "s_waitcnt lgkmcnt(2)\n\t" \
    "v_mfma_f32_16x16x32_bf16 v[216:219], v[164:167], v[232:235], v[216:219]\n\t" \
    "ds_read_b128 v[224:227], v253 offset:32768\n\t" \
    "v_mfma_f32_16x16x32_bf16 v[220:223], v[168:171], v[228:231], v[220:223]\n\t" \
    "v_mfma_f32_16x16x32_bf16 v[220:223], v[172:175], v[232:235], v[220:223]\n\t" \
    "v_mfma_f32_16x16x32_bf16 v[212:215], v[244:247], v[228:231], v[212:215]\n\t" \
    "v_mfma_f32_16x16x32_bf16 v[212:215], v[248:251], v[232:235], v[212:215]\n\t" \
    "s_waitcnt lgkmcnt(2)\n\t" \
    "v_mfma_f32_16x16x32_bf16 v[204:207], v[184:187], v[236:239], v[204:207]\n\t" \
    "ds_read_b128 v[228:231], v253 offset:33792\n\t" \
    "s_waitcnt lgkmcnt(2)\n\t" \
    "v_mfma_f32_16x16x32_bf16 v[204:207], v[188:191], v[240:243], v[204:207]\n\t" \
    "ds_read_b128 v[232:235], v253 offset:20480\n\t" \
    "v_mfma_f32_16x16x32_bf16 v[208:211], v[192:195], v[236:239], v[208:211]\n\t" \
    "v_mfma_f32_16x16x32_bf16 v[208:211], v[196:199], v[240:243], v[208:211]\n\t" \
    "v_mfma_f32_16x16x32_bf16 v[200:203], v[176:179], v[236:239], v[200:203]\n\t" \
    "v_mfma_f32_16x16x32_bf16 v[200:203], v[180:183], v[240:243], v[200:203]\n\t" \
    "s_waitcnt lgkmcnt(2)\n\t" \
    "v_mfma_f32_16x16x32_bf16 v[216:219], v[24:27], v[224:227], v[216:219]\n\t" \
    "ds_read_b128 v[236:239], v253 offset:21504\n\t" \
    "s_waitcnt lgkmcnt(2)\n\t" \
    "v_mfma_f32_16x16x32_bf16 v[216:219], v[28:31], v[228:231], v[216:219]\n\t" \
    "ds_read_b128 v[240:243], v253 offset:22528\n\t" \
    "v_mfma_f32_16x16x32_bf16 v[220:223], v[32:35], v[224:227], v[220:223]\n\t" \
    "v_cvt_pk_bf16_f32 v204, v204, v205\n\t" \
    "v_mfma_f32_16x16x32_bf16 v[220:223], v[36:39], v[228:231], v[220:223]\n\t" \
    "v_cvt_pk_bf16_f32 v205, v206, v207\n\t" \
    "v_mfma_f32_16x16x32_bf16 v[212:215], v[16:19], v[224:227], v[212:215]\n\t" \
    "ds_write_b64 v254, v[204:205] offset:10240\n\t" \
    "v_mfma_f32_16x16x32_bf16 v[212:215], v[20:23], v[228:231], v[212:215]\n\t" \
    "v_cvt_pk_bf16_f32 v208, v208, v209\n\t" \
    "s_waitcnt lgkmcnt(3)\n\t" \
    "v_mfma_f32_16x16x32_bf16 v[216:219], v[64:67], v[232:235], v[216:219]\n\t" \
    "ds_read_b128 v[224:227], v253 offset:23552\n\t" \
    "v_cvt_pk_bf16_f32 v209, v210, v211\n\t" \
    "s_waitcnt lgkmcnt(3)\n\t" \
    "v_mfma_f32_16x16x32_bf16 v[216:219], v[68:71], v[236:239], v[216:219]\n\t" \
    "ds_read_b128 v[228:231], v253 offset:28672\n\t" \
    "ds_write_b64 v254, v[208:209] offset:11264\n\t" \
    "v_mfma_f32_16x16x32_bf16 v[220:223], v[72:75], v[232:235], v[220:223]\n\t" \
    "v_cvt_pk_bf16_f32 v200, v200, v201\n\t" \
    "v_mfma_f32_16x16x32_bf16 v[220:223], v[76:79], v[236:239], v[220:223]\n\t" \
    "v_cvt_pk_bf16_f32 v201, v202, v203\n\t" \
    "v_mfma_f32_16x16x32_bf16 v[212:215], v[56:59], v[232:235], v[212:215]\n\t" \
    "ds_write_b64 v254, v[200:201] offset:9216\n\t" \
    "v_mfma_f32_16x16x32_bf16 v[212:215], v[60:63], v[236:239], v[212:215]\n\t" \
    "s_waitcnt lgkmcnt(5)\n\t" \
    "v_mfma_f32_16x16x32_bf16 v[216:219], v[104:107], v[240:243], v[216:219]\n\t" \
    "ds_read_b128 v[232:235], v253 offset:29696\n\t" \
    "s_waitcnt lgkmcnt(4)\n\t" \
    "v_mfma_f32_16x16x32_bf16 v[216:219], v[108:111], v[224:227], v[216:219]\n\t" \
    "ds_read_b128 v[236:239], v253 offset:38912\n\t" \
    "v_mfma_f32_16x16x32_bf16 v[220:223], v[112:115], v[240:243], v[220:223]\n\t" \
    "v_mfma_f32_16x16x32_bf16 v[220:223], v[116:119], v[224:227], v[220:223]\n\t" \
    "v_mfma_f32_16x16x32_bf16 v[212:215], v[96:99], v[240:243], v[212:215]\n\t" \
    "v_mfma_f32_16x16x32_bf16 v[212:215], v[100:103], v[224:227], v[212:215]\n\t" \
    "ds_read_b128 v[240:243], v253 offset:39936\n\t" \
    "s_waitcnt lgkmcnt(3)\n\t" \
    "s_barrier \n\t" \
    "v_mfma_f32_16x16x32_bf16 v[216:219], v[144:147], v[228:231], v[216:219]\n\t" \
    "s_waitcnt lgkmcnt(2)\n\t" \
    "v_mfma_f32_16x16x32_bf16 v[216:219], v[148:151], v[232:235], v[216:219]\n\t" \
    "s_waitcnt lgkmcnt(1)\n\t" \
    "v_mfma_f32_16x16x32_bf16 v[216:219], v[184:187], v[236:239], v[216:219]\n\t" \
    "s_waitcnt lgkmcnt(0)\n\t" \
    "v_mfma_f32_16x16x32_bf16 v[216:219], v[188:191], v[240:243], v[216:219]\n\t" \
    "v_mfma_f32_16x16x32_bf16 v[220:223], v[152:155], v[228:231], v[220:223]\n\t" \
    "v_mfma_f32_16x16x32_bf16 v[220:223], v[156:159], v[232:235], v[220:223]\n\t" \
    "v_mfma_f32_16x16x32_bf16 v[220:223], v[192:195], v[236:239], v[220:223]\n\t" \
    "v_cvt_pk_bf16_f32 v216, v216, v217\n\t" \
    "v_cvt_pk_bf16_f32 v217, v218, v219\n\t" \
    "v_mfma_f32_16x16x32_bf16 v[220:223], v[196:199], v[240:243], v[220:223]\n\t" \
    "ds_write_b64 v254, v[216:217] offset:16384\n\t" \
    "v_mfma_f32_16x16x32_bf16 v[212:215], v[136:139], v[228:231], v[212:215]\n\t" \
    "v_mfma_f32_16x16x32_bf16 v[212:215], v[140:143], v[232:235], v[212:215]\n\t" \
    "v_mfma_f32_16x16x32_bf16 v[212:215], v[176:179], v[236:239], v[212:215]\n\t" \
    "v_cvt_pk_bf16_f32 v220, v220, v221\n\t" \
    "v_cvt_pk_bf16_f32 v221, v222, v223\n\t" \
    "v_mfma_f32_16x16x32_bf16 v[212:215], v[180:183], v[240:243], v[212:215]\n\t" \
    "ds_write_b64 v254, v[220:221] offset:17408\n\t" \
    "s_nop 6\n\t" \
    "v_cvt_pk_bf16_f32 v212, v212, v213\n\t" \
    "v_cvt_pk_bf16_f32 v213, v214, v215\n\t" \
    "ds_write_b64 v254, v[212:213] offset:15360\n\t" \
    LST6 \
    LJIT \
    "s_waitcnt vmcnt(0) lgkmcnt(0)\n\t" \
    "s_barrier \n\t" \
    "s_setprio 2\n\t" \
    "s_add_u32 s44, s40, 1\n\t" \
    "s_min_u32 s44, s44, 63\n\t" \
    ".Lv18_%=:\n\t" \
    "v_cmp_ge_u32 vcc, s44, v252\n\t" \
    "s_cbranch_vccz .Lv19_%=\n\t" \
    "s_cmp_eq_u32 s41, 0\n\t" \
    "s_cbranch_scc1 .Lv19_%=\n\t" \
    "s_sub_u32 s41, s41, 1\n\t" \
    "s_sleep 2\n\t" \
    "v_and_b32 v252, 0xf0, v253\n\t" \
    "v_lshlrev_b32 v252, 3, v252\n\t" \
    "buffer_load_dword v252, v252, s[60:63], %[fl] offen sc1\n\t" \
    "s_waitcnt vmcnt(0)\n\t" \
    "s_branch .Lv18_%=\n\t" \
    ".Lv19_%=:\n\t" \
    "s_mov_b32 m0, 0x1000\n\t" \
    "s_add_u32 s49, s42, 0x0\n\t" \
    "buffer_load_dwordx4 v253, s[60:63], s49 offen offset:0 sc1 lds\n\t" \
    "s_sleep 1\n\t" \
    "buffer_load_dwordx4 v253, s[60:63], s49 offen offset:1024 sc1 lds\n\t" \
    "s_sleep 1\n\t" \
    "buffer_load_dwordx4 v253, s[60:63], s49 offen offset:2048 sc1 lds\n\t" \
    "s_sleep 1\n\t" \
    "buffer_load_dwordx4 v253, s[60:63], s49 offen offset:3072 sc1 lds\n\t" \
    "s_sleep 1\n\t" \
    "s_mov_b32 m0, 0x4400\n\t" \
    "s_add_u32 s49, s42, 0xc400\n\t" \
    "buffer_load_dwordx4 v253, s[60:63], s49 offen offset:0 sc1 lds\n\t" \
    "s_sleep 1\n\t" \
    "s_mov_b32 m0, 0x6000\n\t" \
    "s_add_u32 s49, s42, 0x1000\n\t" \
    "buffer_load_dwordx4 v253, s[60:63], s49 offen offset:0 sc1 lds\n\t" \
    "s_sleep 1\n\t" \
    "buffer_load_dwordx4 v253, s[60:63], s49 offen offset:1024 sc1 lds\n\t" \
    "s_sleep 1\n\t" \
    "buffer_load_dwordx4 v253, s[60:63], s49 offen offset:2048 sc1 lds\n\t" \
    "s_sleep 1\n\t" \
    "buffer_load_dwordx4 v253, s[60:63], s49 offen offset:3072 sc1 lds\n\t" \
    "s_sleep 1\n\t" \
    "s_mov_b32 m0, 0x9400\n\t" \
    "s_add_u32 s49, s42, 0xd400\n\t" \
    "buffer_load_dwordx4 v253, s[60:63], s49 offen offset:0 sc1 lds\n\t" \
    "s_sleep 1\n\t" \
    "ds_read_b128 v[200:203], v254 offset:40960\n\t" \
    "ds_read_b128 v[204:207], v254 offset:41984\n\t" \
    "ds_read_b128 v[216:219], v254 offset:43008\n\t" \
    "ds_read_b128 v[220:223], v254 offset:44032\n\t" \
    "v_mov_b64 v[208:209], 0\n\t" \
    "v_mov_b64 v[210:211], 0\n\t" \
    "v_mov_b64 v[244:245], 0\n\t" \
    "v_mov_b64 v[246:247], 0\n\t" \
    "v_mov_b64 v[248:249], 0\n\t" \
    "v_mov_b64 v[250:251], 0\n\t" \
    LST1 \
    LJIT \
    "s_sleep 5\n\t" \
    "s_mov_b32 m0, 0xa800\n\t" \
    "s_nop 0\n\t" \
    "buffer_load_dwordx4 v253, s[60:63], s46 offen sc1 lds\n\t" \
    "s_mov_b32 m0, 0xb800\n\t" \
    "s_nop 0\n\t" \
    "buffer_load_dwordx4 v253, s[60:63], s47 offen sc1 lds\n\t" \
    "s_mov_b32 m0, 0xac00\n\t" \
    "s_nop 0\n\t" \
    "buffer_load_dwordx4 v253, s[60:63], s48 offen sc1 lds\n\t" \
    "s_waitcnt lgkmcnt(0)\n\t" \
    LFLUSH212 \
    "v_bfe_i32 v212, v253, 5, 1\n\t" \
    "v_and_b32 v212, 0xaaaa, v212\n\t" \
    "v_or_b32 v212, 0x4444, v212\n\t" \
    "v_and_b32 v213, 0xffff, v255\n\t" \
    "v_lshrrev_b32 v214, 16, v255\n\t" \
    ".Lv110_%=:\n\t" \
    "s_waitcnt vmcnt(0)\n\t" \
    "ds_read_b128 v[224:227], v253 offset:43008\n\t" \
    "ds_read_b128 v[228:231], v253 offset:47104\n\t" \
    "ds_read_b128 v[232:235], v253 offset:44032\n\t" \
    "s_waitcnt lgkmcnt(2)\n\t" \
    "v_max_u32 v215, v224, v225\n\t" \
    "v_max3_u32 v215, v226, v227, v215\n\t" \
    "s_waitcnt lgkmcnt(1)\n\t" \
    "v_max3_u32 v215, v228, v229, v215\n\t" \
    "v_max3_u32 v215, v230, v231, v215\n\t" \
    "s_waitcnt lgkmcnt(0)\n\t" \
    "v_max3_u32 v215, v232, v233, v215\n\t" \
    "v_max3_u32 v215, v234, v235, v215\n\t" \
    "v_cmp_eq_u32 vcc, -1, v215\n\t" \
    "s_cbranch_vccz .Lv111_%=\n\t" \
    "v_max_u32 v215, v224, v225\n\t" \
    "v_max3_u32 v215, v226, v227, v215\n\t" \
    "v_cmp_eq_u32 vcc, -1, v215\n\t" \
    "s_cbranch_vccz .Lv112_%=\n\t" \
    "s_mov_b32 m0, 0xa800\n\t" \
    "s_nop 0\n\t" \
    "buffer_load_dwordx4 v253, s[60:63], s46 offen sc1 lds\n\t" \
    ".Lv112_%=:\n\t" \
    "v_max_u32 v215, v228, v229\n\t" \
    "v_max3_u32 v215, v230, v231, v215\n\t" \
    "v_cmp_eq_u32 vcc, -1, v215\n\t" \
    "s_cbranch_vccz .Lv113_%=\n\t" \
    "s_mov_b32 m0, 0xb800\n\t" \
    "s_nop 0\n\t" \
    "buffer_load_dwordx4 v253, s[60:63], s47 offen sc1 lds\n\t" \
    ".Lv113_%=:\n\t" \
    "v_max_u32 v215, v232, v233\n\t" \
    "v_max3_u32 v215, v234, v235, v215\n\t" \
    "v_cmp_eq_u32 vcc, -1, v215\n\t" \
    "s_cbranch_vccz .Lv114_%=\n\t" \
    "s_mov_b32 m0, 0xac00\n\t" \
    "s_nop 0\n\t" \
    "buffer_load_dwordx4 v253, s[60:63], s48 offen sc1 lds\n\t" \
    ".Lv114_%=:\n\t" \
    "s_cmp_eq_u32 s41, 0\n\t" \
    "s_cbranch_scc1 .Lv111_%=\n\t" \
    "s_sub_u32 s41, s41, 1\n\t" \
    "s_branch .Lv110_%=\n\t" \
    ".Lv111_%=:\n\t" \
    LST2 \
    LJIT \
    "s_waitcnt lgkmcnt(0)\n\t" \
    "s_barrier \n\t" \
    LST3 \
    LJIT \
    "ds_read_b128 v[236:239], v253 offset:40960\n\t" \
    "ds_read_b128 v[240:243], v253 offset:46080\n\t" \
    "v_smfmac_f32_16x16x64_bf16 v[244:247], v[224:227], v[0:7], v212\n\t" \
    "v_smfmac_f32_16x16x64_bf16 v[248:251], v[224:227], v[8:15], v212\n\t" \
    "v_smfmac_f32_16x16x64_bf16 v[208:211], v[224:227], v[200:207], v212\n\t" \
    "ds_read_b128 v[200:203], v254 offset:45056\n\t" \
    "ds_read_b128 v[204:207], v254 offset:46080\n\t" \
    "ds_read_b128 v[224:227], v253 offset:41984\n\t" \
    "v_smfmac_f32_16x16x64_bf16 v[208:211], v[228:231], v[16:23], v212\n\t" \
    "v_smfmac_f32_16x16x64_bf16 v[244:247], v[228:231], v[24:31], v212\n\t" \
    "v_smfmac_f32_16x16x64_bf16 v[248:251], v[228:231], v[32:39], v212\n\t" \
    "ds_read_b128 v[228:231], v253 offset:48128\n\t" \
    "v_smfmac_f32_16x16x64_bf16 v[208:211], v[232:235], v[216:223], v212\n\t" \
    "ds_read_b128 v[216:219], v254 offset:47104\n\t" \
    "ds_read_b128 v[220:223], v254 offset:48128\n\t" \
    "v_smfmac_f32_16x16x64_bf16 v[244:247], v[232:235], v[40:47], v212\n\t" \
    "v_smfmac_f32_16x16x64_bf16 v[248:251], v[232:235], v[48:55], v212\n\t" \
    "ds_read_b128 v[232:235], v253 offset:45056\n\t" \
    "s_waitcnt lgkmcnt(8)\n\t" \
    "v_smfmac_f32_16x16x64_bf16 v[208:211], v[236:239], v[56:63], v212\n\t" \
    "v_smfmac_f32_16x16x64_bf16 v[244:247], v[236:239], v[64:71], v212\n\t" \
    "v_smfmac_f32_16x16x64_bf16 v[248:251], v[236:239], v[72:79], v212\n\t" \
    "ds_read_b128 v[236:239], v253 offset:49152\n\t" \
    "s_waitcnt lgkmcnt(6)\n\t" \
    "v_smfmac_f32_16x16x64_bf16 v[208:211], v[240:243], v[200:207], v212\n\t" \
    "ds_read_b128 v[200:203], v254 offset:49152\n\t" \
    "ds_read_b128 v[204:207], v254 offset:50176\n\t" \
    "v_smfmac_f32_16x16x64_bf16 v[244:247], v[240:243], v[80:87], v212\n\t" \
    "v_smfmac_f32_16x16x64_bf16 v[248:251], v[240:243], v[88:95], v212\n\t" \
    "ds_read_b128 v[240:243], v253 offset:50176\n\t" \
    "s_waitcnt lgkmcnt(8)\n\t" \
    "v_smfmac_f32_16x16x64_bf16 v[208:211], v[224:227], v[96:103], v212\n\t" \
    "v_smfmac_f32_16x16x64_bf16 v[244:247], v[224:227], v[104:111], v212\n\t" \
    "v_smfmac_f32_16x16x64_bf16 v[248:251], v[224:227], v[112:119], v212\n\t" \
    "s_waitcnt lgkmcnt(7)\n\t" \
    "v_smfmac_f32_16x16x64_bf16 v[244:247], v[228:231], v[120:127], v212\n\t" \
    "s_waitcnt lgkmcnt(4)\n\t" \
    "v_smfmac_f32_16x16x64_bf16 v[244:247], v[232:235], v[144:151], v212\n\t" \
    "s_waitcnt lgkmcnt(3)\n\t" \
    "v_smfmac_f32_16x16x64_bf16 v[244:247], v[236:239], v[160:167], v212\n\t" \
    "s_waitcnt lgkmcnt(0)\n\t" \
    "v_smfmac_f32_16x16x64_bf16 v[244:247], v[240:243], v[184:191], v212\n\t" \
    "v_smfmac_f32_16x16x64_bf16 v[248:251], v[228:231], v[128:135], v212\n\t" \
    "v_smfmac_f32_16x16x64_bf16 v[248:251], v[232:235], v[152:159], v212\n\t" \
    "v_smfmac_f32_16x16x64_bf16 v[248:251], v[236:239], v[168:175], v212\n\t" \
    "v_add_f32 v244, v244, v246\n\t" \
    "v_add_f32 v245, v245, v247\n\t" \
    "v_cvt_pk_bf16_f32 v244, v244, v245\n\t" \
    "v_smfmac_f32_16x16x64_bf16 v[248:251], v[240:243], v[192:199], v212\n\t" \
    "ds_write_b16 v213, v244 offset:512\n\t" \
    "ds_write_b16_d16_hi v213, v244 offset:528\n\t" \
    "v_smfmac_f32_16x16x64_bf16 v[208:211], v[228:231], v[216:223], v212\n\t" \
    "v_smfmac_f32_16x16x64_bf16 v[208:211], v[232:235], v[136:143], v212\n\t" \
    "v_smfmac_f32_16x16x64_bf16 v[208:211], v[236:239], v[200:207], v212\n\t" \
    "v_add_f32 v248, v248, v250\n\t" \
    "v_add_f32 v249, v249, v251\n\t" \
    "v_cvt_pk_bf16_f32 v248, v248, v249\n\t" \
    "v_smfmac_f32_16x16x64_bf16 v[208:211], v[240:243], v[176:183], v212\n\t" \
    "ds_write_b16 v213, v248 offset:1024\n\t" \
    "ds_write_b16_d16_hi v213, v248 offset:1040\n\t" \
    "s_nop 5\n\t" \
    "v_add_f32 v208, v208, v210\n\t" \
    "v_add_f32 v209, v209, v211\n\t" \
    "v_cvt_pk_bf16_f32 v208, v208, v209\n\t" \
    "ds_write_b16 v213, v208 offset:0\n\t" \
    "ds_write_b16_d16_hi v213, v208 offset:16\n\t" \
    "s_setprio 0\n\t" \
    "s_waitcnt lgkmcnt(0)\n\t" \
    "s_barrier \n\t" \
    LST4 \
    LJIT \
    "ds_read_b128 v[224:227], v253 offset:4096\n\t" \
    "ds_read_b128 v[244:247], v254 offset:40960\n\t" \
    "ds_read_b128 v[228:231], v253 offset:5120\n\t" \
    "ds_read_b128 v[232:235], v253 offset:24576\n\t" \
    "ds_read_b128 v[236:239], v253 offset:25600\n\t" \
    "ds_read_b128 v[248:251], v254 offset:41984\n\t" \
    "s_add_u32 s40, s40, 1\n\t" \
    "s_cmp_lg_u32 s40, 64\n\t" \
    "s_cbranch_scc1 .Lv17_%=\n\t" \
    "s_branch .Lend%=\n\t" \
    ".Lv0_%=:\n\t" \
    "s_mov_b32 s42, %[xs]\n\t" \
    "v_add_u32 v244, 0xc000, v243\n\t" \
    "s_waitcnt vmcnt(12) lgkmcnt(0)\n\t" \
    "s_barrier \n\t" \
    "s_add_u32 m0, %[wm], 0x9000\n\t" \
    "s_mov_b32 s44, 0x1e0000\n\t" \
    "buffer_load_dwordx4 v240, %[wr], s44 offen offset:0 nt lds\n\t" \
    "buffer_load_dwordx4 v241, %[wr], s44 offen offset:1024 nt lds\n\t" \
    "buffer_load_dwordx4 v242, %[wr], s44 offen offset:2048 nt lds\n\t" \
    "ds_read_b64_tr_b16 v[200:201], v244 offset:0\n\t" \
    "ds_read_b64_tr_b16 v[202:203], v244 offset:1536\n\t" \
    "ds_read_b64_tr_b16 v[0:1], v244 offset:32\n\t" \
    "ds_read_b64_tr_b16 v[2:3], v244 offset:1568\n\t" \
    "ds_read_b64_tr_b16 v[8:9], v244 offset:64\n\t" \
    "ds_read_b64_tr_b16 v[10:11], v244 offset:1600\n\t" \
    "v_add_u32 v244, 0xf000, v243\n\t" \
    "s_waitcnt vmcnt(12) lgkmcnt(0)\n\t" \
    "s_barrier \n\t" \
    "s_add_u32 m0, %[wm], 0xc000\n\t" \
    "s_mov_b32 s44, 0x240000\n\t" \
    "buffer_load_dwordx4 v240, %[wr], s44 offen offset:0 nt lds\n\t" \
    "buffer_load_dwordx4 v241, %[wr], s44 offen offset:1024 nt lds\n\t" \
    "buffer_load_dwordx4 v242, %[wr], s44 offen offset:2048 nt lds\n\t" \
    "ds_read_b64_tr_b16 v[204:205], v244 offset:0\n\t" \
    "ds_read_b64_tr_b16 v[206:207], v244 offset:1536\n\t" \
    "ds_read_b64_tr_b16 v[4:5], v244 offset:32\n\t" \
    "ds_read_b64_tr_b16 v[6:7], v244 offset:1568\n\t" \
    "ds_read_b64_tr_b16 v[12:13], v244 offset:64\n\t" \
    "ds_read_b64_tr_b16 v[14:15], v244 offset:1600\n\t" \
    "v_add_u32 v244, 0x0, v243\n\t" \
    "s_waitcnt vmcnt(12) lgkmcnt(0)\n\t" \
    "s_barrier \n\t" \
    "s_add_u32 m0, %[wm], 0xf000\n\t" \
    "s_mov_b32 s44, 0x2a0000\n\t" \
    "buffer_load_dwordx4 v240, %[wr], s44 offen offset:0 nt lds\n\t" \
    "buffer_load_dwordx4 v241, %[wr], s44 offen offset:1024 nt lds\n\t" \
    "buffer_load_dwordx4 v242, %[wr], s44 offen offset:2048 nt lds\n\t" \
    "ds_read_b64_tr_b16 v[208:209], v244 offset:0\n\t" \
    "ds_read_b64_tr_b16 v[210:211], v244 offset:1536\n\t" \
    "ds_read_b64_tr_b16 v[40:41], v244 offset:32\n\t" \
    "ds_read_b64_tr_b16 v[42:43], v244 offset:1568\n\t" \
    "ds_read_b64_tr_b16 v[48:49], v244 offset:64\n\t" \
    "ds_read_b64_tr_b16 v[50:51], v244 offset:1600\n\t" \
    "v_add_u32 v244, 0x3000, v243\n\t" \
    "s_waitcnt vmcnt(12) lgkmcnt(0)\n\t" \
    "s_barrier \n\t" \
    "s_add_u32 m0, %[wm], 0x0\n\t" \
    "s_mov_b32 s44, 0x300000\n\t" \
    "buffer_load_dwordx4 v240, %[wr], s44 offen offset:0 nt lds\n\t" \
    "buffer_load_dwordx4 v241, %[wr], s44 offen offset:1024 nt lds\n\t" \
    "buffer_load_dwordx4 v242, %[wr], s44 offen offset:2048 nt lds\n\t" \
    "ds_read_b64_tr_b16 v[212:213], v244 offset:0\n\t" \
    "ds_read_b64_tr_b16 v[214:215], v244 offset:1536\n\t" \
    "ds_read_b64_tr_b16 v[44:45], v244 offset:32\n\t" \
    "ds_read_b64_tr_b16 v[46:47], v244 offset:1568\n\t" \
    "ds_read_b64_tr_b16 v[52:53], v244 offset:64\n\t" \
    "ds_read_b64_tr_b16 v[54:55], v244 offset:1600\n\t" \
    "v_add_u32 v244, 0x6000, v243\n\t" \
    "s_waitcnt vmcnt(12) lgkmcnt(0)\n\t" \
    "s_barrier \n\t" \
    "s_add_u32 m0, %[wm], 0x3000\n\t" \
    "s_mov_b32 s44, 0x360000\n\t" \
    "buffer_load_dwordx4 v240, %[wr], s44 offen offset:0 nt lds\n\t" \
    "buffer_load_dwordx4 v241, %[wr], s44 offen offset:1024 nt lds\n\t" \
    "buffer_load_dwordx4 v242, %[wr], s44 offen offset:2048 nt lds\n\t" \
    "ds_read_b64_tr_b16 v[216:217], v244 offset:0\n\t" \
    "ds_read_b64_tr_b16 v[218:219], v244 offset:1536\n\t" \
    "ds_read_b64_tr_b16 v[80:81], v244 offset:32\n\t" \
    "ds_read_b64_tr_b16 v[82:83], v244 offset:1568\n\t" \
    "ds_read_b64_tr_b16 v[88:89], v244 offset:64\n\t" \
    "ds_read_b64_tr_b16 v[90:91], v244 offset:1600\n\t" \
    "v_add_u32 v244, 0x9000, v243\n\t" \
    "s_waitcnt vmcnt(12) lgkmcnt(0)\n\t" \
    "s_barrier \n\t" \
    "s_add_u32 m0, %[wm], 0x6000\n\t" \
    "s_mov_b32 s44, 0x3c0000\n\t" \
    "buffer_load_dwordx4 v240, %[wr], s44 offen offset:0 nt lds\n\t" \
    "buffer_load_dwordx4 v241, %[wr], s44 offen offset:1024 nt lds\n\t" \
    "buffer_load_dwordx4 v242, %[wr], s44 offen offset:2048 nt lds\n\t" \
    "ds_read_b64_tr_b16 v[220:221], v244 offset:0\n\t" \
    "ds_read_b64_tr_b16 v[222:223], v244 offset:1536\n\t" \
    "ds_read_b64_tr_b16 v[84:85], v244 offset:32\n\t" \
    "ds_read_b64_tr_b16 v[86:87], v244 offset:1568\n\t" \
    "ds_read_b64_tr_b16 v[92:93], v244 offset:64\n\t" \
    "ds_read_b64_tr_b16 v[94:95], v244 offset:1600\n\t" \
    "v_add_u32 v244, 0xc000, v243\n\t" \
    "s_waitcnt vmcnt(12) lgkmcnt(0)\n\t" \
    "s_barrier \n\t" \
    "s_add_u32 m0, %[wm], 0x9000\n\t" \
    "s_mov_b32 s44, 0x420000\n\t" \
    "buffer_load_dwordx4 v240, %[wr], s44 offen offset:0 nt lds\n\t" \
    "buffer_load_dwordx4 v241, %[wr], s44 offen offset:1024 nt lds\n\t" \
    "buffer_load_dwordx4 v242, %[wr], s44 offen offset:2048 nt lds\n\t" \
    "ds_read_b64_tr_b16 v[224:225], v244 offset:0\n\t" \
    "ds_read_b64_tr_b16 v[226:227], v244 offset:1536\n\t" \
    "ds_read_b64_tr_b16 v[120:121], v244 offset:32\n\t" \
    "ds_read_b64_tr_b16 v[122:123], v244 offset:1568\n\t" \
    "ds_read_b64_tr_b16 v[128:129], v244 offset:64\n\t" \
    "ds_read_b64_tr_b16 v[130:131], v244 offset:1600\n\t" \
    "v_add_u32 v244, 0xf000, v243\n\t" \
    "s_waitcnt vmcnt(12) lgkmcnt(0)\n\t" \
    "s_barrier \n\t" \
    "s_add_u32 m0, %[wm], 0xc000\n\t" \
    "s_mov_b32 s44, 0x480000\n\t" \
    "buffer_load_dwordx4 v240, %[wr], s44 offen offset:0 nt lds\n\t" \
    "buffer_load_dwordx4 v241, %[wr], s44 offen offset:1024 nt lds\n\t" \
    "buffer_load_dwordx4 v242, %[wr], s44 offen offset:2048 nt lds\n\t" \
    "ds_read_b64_tr_b16 v[228:229], v244 offset:0\n\t" \
    "ds_read_b64_tr_b16 v[230:231], v244 offset:1536\n\t" \
    "ds_read_b64_tr_b16 v[124:125], v244 offset:32\n\t" \
    "ds_read_b64_tr_b16 v[126:127], v244 offset:1568\n\t" \
    "ds_read_b64_tr_b16 v[132:133], v244 offset:64\n\t" \
    "ds_read_b64_tr_b16 v[134:135], v244 offset:1600\n\t" \
    "v_add_u32 v244, 0x0, v243\n\t" \
    "s_waitcnt vmcnt(12) lgkmcnt(0)\n\t" \
    "s_barrier \n\t" \
    "v_mov_b32 v246, 0\n\t" \
    "buffer_load_dword v246, v246, s[60:63], %[mo] offen sc1\n\t" \
    "s_add_u32 m0, %[wm], 0xf000\n\t" \
    "s_mov_b32 s44, 0x4e0000\n\t" \
    "buffer_load_dwordx4 v240, %[wr], s44 offen offset:0 nt lds\n\t" \
    "buffer_load_dwordx4 v241, %[wr], s44 offen offset:1024 nt lds\n\t" \
    "buffer_load_dwordx4 v242, %[wr], s44 offen offset:2048 nt lds\n\t" \
    "ds_read_b64_tr_b16 v[232:233], v244 offset:0\n\t" \
    "ds_read_b64_tr_b16 v[234:235], v244 offset:1536\n\t" \
    "ds_read_b64_tr_b16 v[160:161], v244 offset:32\n\t" \
    "ds_read_b64_tr_b16 v[162:163], v244 offset:1568\n\t" \
    "ds_read_b64_tr_b16 v[168:169], v244 offset:64\n\t" \
    "ds_read_b64_tr_b16 v[170:171], v244 offset:1600\n\t" \
    "v_add_u32 v244, 0x3000, v243\n\t" \
    "s_waitcnt vmcnt(12) lgkmcnt(0)\n\t" \
    "s_barrier \n\t" \
    "s_add_u32 m0, %[wm], 0x0\n\t" \
    "s_mov_b32 s44, 0x540000\n\t" \
    "buffer_load_dwordx4 v240, %[wr], s44 offen offset:0 nt lds\n\t" \
    "buffer_load_dwordx4 v241, %[wr], s44 offen offset:1024 nt lds\n\t" \
    "buffer_load_dwordx4 v242, %[wr], s44 offen offset:2048 nt lds\n\t" \
    "ds_read_b64_tr_b16 v[236:237], v244 offset:0\n\t" \
    "ds_read_b64_tr_b16 v[238:239], v244 offset:1536\n\t" \
    "ds_read_b64_tr_b16 v[164:165], v244 offset:32\n\t" \
    "ds_read_b64_tr_b16 v[166:167], v244 offset:1568\n\t" \
    "ds_read_b64_tr_b16 v[172:173], v244 offset:64\n\t" \
    "ds_read_b64_tr_b16 v[174:175], v244 offset:1600\n\t" \
    "v_add_u32 v244, 0x6000, v243\n\t" \
    "s_waitcnt vmcnt(12) lgkmcnt(0)\n\t" \
    "s_barrier \n\t" \
    "s_add_u32 m0, %[wm], 0x3000\n\t" \
    "s_mov_b32 s44, 0x5a0000\n\t" \
    "buffer_load_dwordx4 v240, %[wr], s44 offen offset:0 nt lds\n\t" \
    "buffer_load_dwordx4 v241, %[wr], s44 offen offset:1024 nt lds\n\t" \
    "buffer_load_dwordx4 v242, %[wr], s44 offen offset:2048 nt lds\n\t" \
    "ds_read_b64_tr_b16 v[16:17], v244 offset:0\n\t" \
    "ds_read_b64_tr_b16 v[18:19], v244 offset:1536\n\t" \
    "ds_read_b64_tr_b16 v[24:25], v244 offset:32\n\t" \
    "ds_read_b64_tr_b16 v[26:27], v244 offset:1568\n\t" \
    "ds_read_b64_tr_b16 v[32:33], v244 offset:64\n\t" \
    "ds_read_b64_tr_b16 v[34:35], v244 offset:1600\n\t" \
    "v_add_u32 v244, 0x9000, v243\n\t" \
    "s_waitcnt vmcnt(12) lgkmcnt(0)\n\t" \
    "s_barrier \n\t" \
    "s_add_u32 m0, %[wm], 0x6000\n\t" \
    "s_mov_b32 s44, 0x600000\n\t" \
    "buffer_load_dwordx4 v240, %[wr], s44 offen offset:0 nt lds\n\t" \
    "buffer_load_dwordx4 v241, %[wr], s44 offen offset:1024 nt lds\n\t" \
    "buffer_load_dwordx4 v242, %[wr], s44 offen offset:2048 nt lds\n\t" \
    "ds_read_b64_tr_b16 v[20:21], v244 offset:0\n\t" \
    "ds_read_b64_tr_b16 v[22:23], v244 offset:1536\n\t" \
    "ds_read_b64_tr_b16 v[28:29], v244 offset:32\n\t" \
    "ds_read_b64_tr_b16 v[30:31], v244 offset:1568\n\t" \
    "ds_read_b64_tr_b16 v[36:37], v244 offset:64\n\t" \
    "ds_read_b64_tr_b16 v[38:39], v244 offset:1600\n\t" \
    "v_add_u32 v244, 0xc000, v243\n\t" \
    "s_waitcnt vmcnt(12) lgkmcnt(0)\n\t" \
    "s_barrier \n\t" \
    "s_add_u32 m0, %[wm], 0x9000\n\t" \
    "s_mov_b32 s44, 0x660000\n\t" \
    "buffer_load_dwordx4 v240, %[wr], s44 offen offset:0 nt lds\n\t" \
    "buffer_load_dwordx4 v241, %[wr], s44 offen offset:1024 nt lds\n\t" \
    "buffer_load_dwordx4 v242, %[wr], s44 offen offset:2048 nt lds\n\t" \
    "ds_read_b64_tr_b16 v[56:57], v244 offset:0\n\t" \
    "ds_read_b64_tr_b16 v[58:59], v244 offset:1536\n\t" \
    "ds_read_b64_tr_b16 v[64:65], v244 offset:32\n\t" \
    "ds_read_b64_tr_b16 v[66:67], v244 offset:1568\n\t" \
    "ds_read_b64_tr_b16 v[72:73], v244 offset:64\n\t" \
    "ds_read_b64_tr_b16 v[74:75], v244 offset:1600\n\t" \
    "v_add_u32 v244, 0xf000, v243\n\t" \
    "s_waitcnt vmcnt(12) lgkmcnt(0)\n\t" \
    "s_barrier \n\t" \
    ".Lv01_%=:\n\t" \
    "v_readfirstlane_b32 s43, v246\n\t" \
    "s_nop 0\n\t" \
    "s_cmp_lg_u32 s43, -1\n\t" \
    "s_cbranch_scc1 .Lv02_%=\n\t" \
    "s_cmp_eq_u32 s41, 0\n\t" \
    "s_cbranch_scc1 .Lv02_%=\n\t" \
    "s_sub_u32 s41, s41, 1\n\t" \
    "s_sleep 4\n\t" \
    "v_mov_b32 v246, 0\n\t" \
    "buffer_load_dword v246, v246, s[60:63], %[mo] offen sc1\n\t" \
    "s_waitcnt vmcnt(0)\n\t" \
    "s_branch .Lv01_%=\n\t" \
    ".Lv02_%=:\n\t" \
    "v_mov_b32 v246, 0x8000\n\t" \
    "s_mov_b64 exec, 1\n\t" \
    "buffer_store_dword v246, v246, s[60:63], %[mo] offen sc1\n\t" \
    "s_mov_b64 exec, -1\n\t" \
    "s_and_b32 s44, s43, 0x5000\n\t" \
    "s_xor_b32 %[mb], %[mb], s44\n\t" \
    "s_xor_b32 %[fl], %[fl], s44\n\t" \
    "s_and_b32 s43, s43, 0x2000\n\t" \
    "s_xor_b32 s43, s43, %[lx]\n\t" \
    "s_add_u32 s60, s60, s43\n\t" \
    "s_addc_u32 s61, s61, 0\n\t" \
    "v_and_b32 v252, 0xf0, v253\n\t" \
    "v_lshlrev_b32 v252, 3, v252\n\t" \
    "buffer_load_dword v252, v252, s[60:63], %[fl] offen sc1\n\t" \
    "s_add_u32 m0, %[wm], 0xc000\n\t" \
    "s_mov_b32 s44, 0x6c0000\n\t" \
    "buffer_load_dwordx4 v240, %[wr], s44 offen offset:0 nt lds\n\t" \
    "buffer_load_dwordx4 v241, %[wr], s44 offen offset:1024 nt lds\n\t" \
    "buffer_load_dwordx4 v242, %[wr], s44 offen offset:2048 nt lds\n\t" \
    "ds_read_b64_tr_b16 v[60:61], v244 offset:0\n\t" \
    "ds_read_b64_tr_b16 v[62:63], v244 offset:1536\n\t" \
    "ds_read_b64_tr_b16 v[68:69], v244 offset:32\n\t" \
    "ds_read_b64_tr_b16 v[70:71], v244 offset:1568\n\t" \
    "ds_read_b64_tr_b16 v[76:77], v244 offset:64\n\t" \
    "ds_read_b64_tr_b16 v[78:79], v244 offset:1600\n\t" \
    "v_add_u32 v244, 0x0, v243\n\t" \
    "s_waitcnt vmcnt(12) lgkmcnt(0)\n\t" \
    "s_barrier \n\t" \
    "s_add_u32 m0, %[wm], 0xf000\n\t" \
    "s_mov_b32 s44, 0x720000\n\t" \
    "buffer_load_dwordx4 v240, %[wr], s44 offen offset:0 nt lds\n\t" \
    "buffer_load_dwordx4 v241, %[wr], s44 offen offset:1024 nt lds\n\t" \
    "buffer_load_dwordx4 v242, %[wr], s44 offen offset:2048 nt lds\n\t" \
    "ds_read_b64_tr_b16 v[96:97], v244 offset:0\n\t" \
    "ds_read_b64_tr_b16 v[98:99], v244 offset:1536\n\t" \
    "ds_read_b64_tr_b16 v[104:105], v244 offset:32\n\t" \
    "ds_read_b64_tr_b16 v[106:107], v244 offset:1568\n\t" \
    "ds_read_b64_tr_b16 v[112:113], v244 offset:64\n\t" \
    "ds_read_b64_tr_b16 v[114:115], v244 offset:1600\n\t" \
    "v_add_u32 v244, 0x3000, v243\n\t" \
    "s_waitcnt vmcnt(12) lgkmcnt(0)\n\t" \
    "s_barrier \n\t" \
    "ds_read_b64_tr_b16 v[100:101], v244 offset:0\n\t" \
    "ds_read_b64_tr_b16 v[102:103], v244 offset:1536\n\t" \
    "ds_read_b64_tr_b16 v[108:109], v244 offset:32\n\t" \
    "ds_read_b64_tr_b16 v[110:111], v244 offset:1568\n\t" \
    "ds_read_b64_tr_b16 v[116:117], v244 offset:64\n\t" \
    "ds_read_b64_tr_b16 v[118:119], v244 offset:1600\n\t" \
    "v_add_u32 v244, 0x6000, v243\n\t" \
    "s_waitcnt vmcnt(9) lgkmcnt(0)\n\t" \
    "s_barrier \n\t" \
    "ds_read_b64_tr_b16 v[136:137], v244 offset:0\n\t" \
    "ds_read_b64_tr_b16 v[138:139], v244 offset:1536\n\t" \
    "ds_read_b64_tr_b16 v[144:145], v244 offset:32\n\t" \
    "ds_read_b64_tr_b16 v[146:147], v244 offset:1568\n\t" \
    "ds_read_b64_tr_b16 v[152:153], v244 offset:64\n\t" \
    "ds_read_b64_tr_b16 v[154:155], v244 offset:1600\n\t" \
    "v_add_u32 v244, 0x9000, v243\n\t" \
    "s_waitcnt vmcnt(6) lgkmcnt(0)\n\t" \
    "s_barrier \n\t" \
    "ds_read_b64_tr_b16 v[140:141], v244 offset:0\n\t" \
    "ds_read_b64_tr_b16 v[142:143], v244 offset:1536\n\t" \
    "ds_read_b64_tr_b16 v[148:149], v244 offset:32\n\t" \
    "ds_read_b64_tr_b16 v[150:151], v244 offset:1568\n\t" \
    "ds_read_b64_tr_b16 v[156:157], v244 offset:64\n\t" \
    "ds_read_b64_tr_b16 v[158:159], v244 offset:1600\n\t" \
    "v_add_u32 v244, 0xc000, v243\n\t" \
    "s_waitcnt vmcnt(3) lgkmcnt(0)\n\t" \
    "s_barrier \n\t" \
    "ds_read_b64_tr_b16 v[176:177], v244 offset:0\n\t" \
    "ds_read_b64_tr_b16 v[178:179], v244 offset:1536\n\t" \
    "ds_read_b64_tr_b16 v[184:185], v244 offset:32\n\t" \
    "ds_read_b64_tr_b16 v[186:187], v244 offset:1568\n\t" \
    "ds_read_b64_tr_b16 v[192:193], v244 offset:64\n\t" \
    "ds_read_b64_tr_b16 v[194:195], v244 offset:1600\n\t" \
    "s_mov_b32 s43, 0\n\t" \
    "v_cmp_eq_u32 vcc, 0, v252\n\t" \
    "s_cbranch_vccnz .Lv03_%=\n\t" \
    "s_mov_b32 m0, 0x0\n\t" \
    "s_add_u32 s49, s42, 0x0\n\t" \
    "buffer_load_dwordx4 v253, s[60:63], s49 offen offset:0 sc1 lds\n\t" \
    "buffer_load_dwordx4 v253, s[60:63], s49 offen offset:1024 sc1 lds\n\t" \
    "buffer_load_dwordx4 v253, s[60:63], s49 offen offset:2048 sc1 lds\n\t" \
    "buffer_load_dwordx4 v253, s[60:63], s49 offen offset:3072 sc1 lds\n\t" \
    "s_mov_b32 m0, 0x4000\n\t" \
    "s_add_u32 s49, s42, 0x10000\n\t" \
    "buffer_load_dwordx4 v253, s[60:63], s49 offen offset:0 sc1 lds\n\t" \
    "s_mov_b32 m0, 0x5000\n\t" \
    "s_add_u32 s49, s42, 0x1000\n\t" \
    "buffer_load_dwordx4 v253, s[60:63], s49 offen offset:0 sc1 lds\n\t" \
    "buffer_load_dwordx4 v253, s[60:63], s49 offen offset:1024 sc1 lds\n\t" \
    "buffer_load_dwordx4 v253, s[60:63], s49 offen offset:2048 sc1 lds\n\t" \
    "buffer_load_dwordx4 v253, s[60:63], s49 offen offset:3072 sc1 lds\n\t" \
    "s_mov_b32 m0, 0x9000\n\t" \
    "s_add_u32 s49, s42, 0x11000\n\t" \
    "buffer_load_dwordx4 v253, s[60:63], s49 offen offset:0 sc1 lds\n\t" \
    "s_mov_b32 s43, 1\n\t" \
    ".Lv03_%=:\n\t" \
    "v_add_u32 v244, 0xf000, v243\n\t" \
    "s_waitcnt vmcnt(0) lgkmcnt(0)\n\t" \
    "s_barrier \n\t" \
    "ds_read_b64_tr_b16 v[180:181], v244 offset:0\n\t" \
    "ds_read_b64_tr_b16 v[182:183], v244 offset:1536\n\t" \
    "ds_read_b64_tr_b16 v[188:189], v244 offset:32\n\t" \
    "ds_read_b64_tr_b16 v[190:191], v244 offset:1568\n\t" \
    "ds_read_b64_tr_b16 v[196:197], v244 offset:64\n\t" \
    "ds_read_b64_tr_b16 v[198:199], v244 offset:1600\n\t" \
    "s_waitcnt lgkmcnt(0)\n\t" \
    "s_barrier \n\t" \
    LST0 \
    LJIT \
    "ds_write_b128 v254, v[200:203] offset:30720\n\t" \
    "ds_write_b128 v254, v[204:207] offset:31744\n\t" \
    "ds_write_b128 v254, v[208:211] offset:32768\n\t" \
    "ds_write_b128 v254, v[212:215] offset:33792\n\t" \
    "ds_write_b128 v254, v[216:219] offset:34816\n\t" \
    "ds_write_b128 v254, v[220:223] offset:35840\n\t" \
    "ds_write_b128 v254, v[224:227] offset:36864\n\t" \
    "ds_write_b128 v254, v[228:231] offset:37888\n\t" \
    "ds_write_b128 v254, v[232:235] offset:38912\n\t" \
    "ds_write_b128 v254, v[236:239] offset:39936\n\t" \
    "s_cmp_lg_u32 s43, 0\n\t" \
    "s_cbranch_scc1 .Lv04_%=\n\t" \
    "s_mov_b32 s44, 0\n\t" \
    ".Lv05_%=:\n\t" \
    "v_cmp_ge_u32 vcc, s44, v252\n\t" \
    "s_cbranch_vccz .Lv06_%=\n\t" \
    "s_cmp_eq_u32 s41, 0\n\t" \
    "s_cbranch_scc1 .Lv06_%=\n\t" \
    "s_sub_u32 s41, s41, 1\n\t" \
    "s_sleep 8\n\t" \
    "v_and_b32 v252, 0xf0, v253\n\t" \
    "v_lshlrev_b32 v252, 3, v252\n\t" \
    "buffer_load_dword v252, v252, s[60:63], %[fl] offen sc1\n\t" \
    "s_waitcnt vmcnt(0)\n\t" \
    "s_branch .Lv05_%=\n\t" \
    ".Lv06_%=:\n\t" \
    "s_mov_b32 m0, 0x0\n\t" \
    "s_add_u32 s49, s42, 0x0\n\t" \
    "buffer_load_dwordx4 v253, s[60:63], s49 offen offset:0 sc1 lds\n\t" \
    "buffer_load_dwordx4 v253, s[60:63], s49 offen offset:1024 sc1 lds\n\t" \
    "buffer_load_dwordx4 v253, s[60:63], s49 offen offset:2048 sc1 lds\n\t" \
    "buffer_load_dwordx4 v253, s[60:63], s49 offen offset:3072 sc1 lds\n\t" \
    "s_mov_b32 m0, 0x4000\n\t" \
    "s_add_u32 s49, s42, 0x10000\n\t" \
    "buffer_load_dwordx4 v253, s[60:63], s49 offen offset:0 sc1 lds\n\t" \
    "s_mov_b32 m0, 0x5000\n\t" \
    "s_add_u32 s49, s42, 0x1000\n\t" \
    "buffer_load_dwordx4 v253, s[60:63], s49 offen offset:0 sc1 lds\n\t" \
    "buffer_load_dwordx4 v253, s[60:63], s49 offen offset:1024 sc1 lds\n\t" \
    "buffer_load_dwordx4 v253, s[60:63], s49 offen offset:2048 sc1 lds\n\t" \
    "buffer_load_dwordx4 v253, s[60:63], s49 offen offset:3072 sc1 lds\n\t" \
    "s_mov_b32 m0, 0x9000\n\t" \
    "s_add_u32 s49, s42, 0x11000\n\t" \
    "buffer_load_dwordx4 v253, s[60:63], s49 offen offset:0 sc1 lds\n\t" \
    ".Lv04_%=:\n\t" \
    "s_waitcnt vmcnt(0) lgkmcnt(0)\n\t" \
    "s_barrier \n\t" \
    "ds_read_b128 v[224:227], v253 offset:0\n\t" \
    "ds_read_b128 v[244:247], v254 offset:30720\n\t" \
    "ds_read_b128 v[228:231], v253 offset:1024\n\t" \
    "ds_read_b128 v[232:235], v253 offset:20480\n\t" \
    "ds_read_b128 v[236:239], v253 offset:21504\n\t" \
    "ds_read_b128 v[248:251], v254 offset:31744\n\t" \
    ".p2align 8\n\t" \
    ".Lv07_%=:\n\t" \
    LST7 \
    LJIT \
    "s_waitcnt lgkmcnt(5)\n\t" \
    "v_mfma_f32_16x16x32_bf16 v[204:207], v[0:3], v[224:227], 0\n\t" \
    "s_waitcnt lgkmcnt(3)\n\t" \
    "v_mfma_f32_16x16x32_bf16 v[204:207], v[4:7], v[228:231], v[204:207]\n\t" \
    "ds_read_b128 v[240:243], v253 offset:10240\n\t" \
    "v_mfma_f32_16x16x32_bf16 v[208:211], v[8:11], v[224:227], 0\n\t" \
    "v_mfma_f32_16x16x32_bf16 v[208:211], v[12:15], v[228:231], v[208:211]\n\t" \
    "v_mfma_f32_16x16x32_bf16 v[200:203], v[244:247], v[224:227], 0\n\t" \
    "s_waitcnt lgkmcnt(1)\n\t" \
    "v_mfma_f32_16x16x32_bf16 v[200:203], v[248:251], v[228:231], v[200:203]\n\t" \
    "v_mfma_f32_16x16x32_bf16 v[216:219], v[0:3], v[232:235], 0\n\t" \
    "ds_read_b128 v[224:227], v253 offset:11264\n\t" \
    "v_mfma_f32_16x16x32_bf16 v[216:219], v[4:7], v[236:239], v[216:219]\n\t" \
    "ds_read_b128 v[228:231], v253 offset:2048\n\t" \
    "v_mfma_f32_16x16x32_bf16 v[220:223], v[8:11], v[232:235], 0\n\t" \
    "v_mfma_f32_16x16x32_bf16 v[220:223], v[12:15], v[236:239], v[220:223]\n\t" \
    "v_mfma_f32_16x16x32_bf16 v[212:215], v[244:247], v[232:235], 0\n\t" \
    "ds_read_b128 v[244:247], v254 offset:32768\n\t" \
    "v_mfma_f32_16x16x32_bf16 v[212:215], v[248:251], v[236:239], v[212:215]\n\t" \
    "ds_read_b128 v[248:251], v254 offset:33792\n\t" \
    "s_waitcnt lgkmcnt(4)\n\t" \
    "v_mfma_f32_16x16x32_bf16 v[204:207], v[24:27], v[240:243], v[204:207]\n\t" \
    "ds_read_b128 v[232:235], v253 offset:3072\n\t" \
    "s_waitcnt lgkmcnt(4)\n\t" \
    "v_mfma_f32_16x16x32_bf16 v[204:207], v[28:31], v[224:227], v[204:207]\n\t" \
    "ds_read_b128 v[236:239], v253 offset:22528\n\t" \
    "v_mfma_f32_16x16x32_bf16 v[208:211], v[32:35], v[240:243], v[208:211]\n\t" \
    "v_mfma_f32_16x16x32_bf16 v[208:211], v[36:39], v[224:227], v[208:211]\n\t" \
    "v_mfma_f32_16x16x32_bf16 v[200:203], v[16:19], v[240:243], v[200:203]\n\t" \
    "v_mfma_f32_16x16x32_bf16 v[200:203], v[20:23], v[224:227], v[200:203]\n\t" \
    "s_waitcnt lgkmcnt(4)\n\t" \
    "v_mfma_f32_16x16x32_bf16 v[204:207], v[40:43], v[228:231], v[204:207]\n\t" \
    "ds_read_b128 v[240:243], v253 offset:23552\n\t" \
    "s_waitcnt lgkmcnt(2)\n\t" \
    "v_mfma_f32_16x16x32_bf16 v[204:207], v[44:47], v[232:235], v[204:207]\n\t" \
    "ds_read_b128 v[224:227], v253 offset:12288\n\t" \
    "v_mfma_f32_16x16x32_bf16 v[208:211], v[48:51], v[228:231], v[208:211]\n\t" \
    "v_mfma_f32_16x16x32_bf16 v[208:211], v[52:55], v[232:235], v[208:211]\n\t" \
    "v_mfma_f32_16x16x32_bf16 v[200:203], v[244:247], v[228:231], v[200:203]\n\t" \
    "v_mfma_f32_16x16x32_bf16 v[200:203], v[248:251], v[232:235], v[200:203]\n\t" \
    "s_waitcnt lgkmcnt(2)\n\t" \
    "v_mfma_f32_16x16x32_bf16 v[216:219], v[40:43], v[236:239], v[216:219]\n\t" \
    "ds_read_b128 v[228:231], v253 offset:13312\n\t" \
    "s_add_u32 s44, s40, 1\n\t" \
    "s_waitcnt lgkmcnt(2)\n\t" \
    "v_mfma_f32_16x16x32_bf16 v[216:219], v[44:47], v[240:243], v[216:219]\n\t" \
    "ds_read_b128 v[232:235], v253 offset:4096\n\t" \
    "s_and_b32 s45, s44, 15\n\t" \
    "v_mfma_f32_16x16x32_bf16 v[220:223], v[48:51], v[236:239], v[220:223]\n\t" \
    "s_cmp_eq_u32 s45, 0\n\t" \
    "v_mfma_f32_16x16x32_bf16 v[220:223], v[52:55], v[240:243], v[220:223]\n\t" \
    "s_cselect_b32 s45, %[cj], %[cs]\n\t" \
    "v_mfma_f32_16x16x32_bf16 v[212:215], v[244:247], v[236:239], v[212:215]\n\t" \
    "ds_read_b128 v[244:247], v254 offset:34816\n\t" \
    "s_cmp_eq_u32 s44, 64\n\t" \
    "v_mfma_f32_16x16x32_bf16 v[212:215], v[248:251], v[240:243], v[212:215]\n\t" \
    "ds_read_b128 v[248:251], v254 offset:35840\n\t" \
    "s_cselect_b32 s45, 0, s45\n\t" \
    "s_waitcnt lgkmcnt(4)\n\t" \
    "v_mfma_f32_16x16x32_bf16 v[204:207], v[64:67], v[224:227], v[204:207]\n\t" \
    "ds_read_b128 v[236:239], v253 offset:5120\n\t" \
    "s_add_u32 s42, s42, s45\n\t" \
    "s_waitcnt lgkmcnt(4)\n\t" \
    "v_mfma_f32_16x16x32_bf16 v[204:207], v[68:71], v[228:231], v[204:207]\n\t" \
    "ds_read_b128 v[240:243], v253 offset:24576\n\t" \
    "v_mfma_f32_16x16x32_bf16 v[208:211], v[72:75], v[224:227], v[208:211]\n\t" \
    "v_mfma_f32_16x16x32_bf16 v[208:211], v[76:79], v[228:231], v[208:211]\n\t" \
    "v_mfma_f32_16x16x32_bf16 v[200:203], v[56:59], v[224:227], v[200:203]\n\t" \
    "v_mfma_f32_16x16x32_bf16 v[200:203], v[60:63], v[228:231], v[200:203]\n\t" \
    "s_waitcnt lgkmcnt(4)\n\t" \
    "v_mfma_f32_16x16x32_bf16 v[204:207], v[80:83], v[232:235], v[204:207]\n\t" \
    "ds_read_b128 v[224:227], v253 offset:25600\n\t" \
    "s_waitcnt lgkmcnt(2)\n\t" \
    "v_mfma_f32_16x16x32_bf16 v[204:207], v[84:87], v[236:239], v[204:207]\n\t" \
    "ds_read_b128 v[228:231], v253 offset:14336\n\t" \
    "v_mfma_f32_16x16x32_bf16 v[208:211], v[88:91], v[232:235], v[208:211]\n\t" \
    "v_mfma_f32_16x16x32_bf16 v[208:211], v[92:95], v[236:239], v[208:211]\n\t" \
    "v_mfma_f32_16x16x32_bf16 v[200:203], v[244:247], v[232:235], v[200:203]\n\t" \
    "v_mfma_f32_16x16x32_bf16 v[200:203], v[248:251], v[236:239], v[200:203]\n\t" \
    "s_waitcnt lgkmcnt(2)\n\t" \
    "v_mfma_f32_16x16x32_bf16 v[216:219], v[80:83], v[240:243], v[216:219]\n\t" \
    "ds_read_b128 v[232:235], v253 offset:15360\n\t" \
    "s_and_b32 s44, s40, 3\n\t" \
    "s_waitcnt lgkmcnt(2)\n\t" \
    "v_mfma_f32_16x16x32_bf16 v[216:219], v[84:87], v[224:227], v[216:219]\n\t" \
    "ds_read_b128 v[236:239], v253 offset:6144\n\t" \
    "s_lshl_b32 s44, s44, 22\n\t" \
    "v_mfma_f32_16x16x32_bf16 v[220:223], v[88:91], v[240:243], v[220:223]\n\t" \
    "s_add_u32 s44, s44, %[mb]\n\t" \
    "v_mfma_f32_16x16x32_bf16 v[220:223], v[92:95], v[224:227], v[220:223]\n\t" \
    "s_add_u32 s46, s44, 0x0\n\t" \
    "v_mfma_f32_16x16x32_bf16 v[212:215], v[244:247], v[240:243], v[212:215]\n\t" \
    "ds_read_b128 v[244:247], v254 offset:36864\n\t" \
    "s_add_u32 s47, s44, 0x8400\n\t" \
    "v_mfma_f32_16x16x32_bf16 v[212:215], v[248:251], v[224:227], v[212:215]\n\t" \
    "ds_read_b128 v[248:251], v254 offset:37888\n\t" \
    "s_add_u32 s48, s44, 0x400\n\t" \
    "s_waitcnt lgkmcnt(4)\n\t" \
    "v_mfma_f32_16x16x32_bf16 v[204:207], v[104:107], v[228:231], v[204:207]\n\t" \
    "ds_read_b128 v[240:243], v253 offset:7168\n\t" \
    "s_waitcnt lgkmcnt(4)\n\t" \
    "v_mfma_f32_16x16x32_bf16 v[204:207], v[108:111], v[232:235], v[204:207]\n\t" \
    "ds_read_b128 v[224:227], v253 offset:26624\n\t" \
    "v_mfma_f32_16x16x32_bf16 v[208:211], v[112:115], v[228:231], v[208:211]\n\t" \
    "v_mfma_f32_16x16x32_bf16 v[208:211], v[116:119], v[232:235], v[208:211]\n\t" \
    "v_mfma_f32_16x16x32_bf16 v[200:203], v[96:99], v[228:231], v[200:203]\n\t" \
    "v_mfma_f32_16x16x32_bf16 v[200:203], v[100:103], v[232:235], v[200:203]\n\t" \
    "s_waitcnt lgkmcnt(4)\n\t" \
    "v_mfma_f32_16x16x32_bf16 v[204:207], v[120:123], v[236:239], v[204:207]\n\t" \
    "ds_read_b128 v[228:231], v253 offset:27648\n\t" \
    "s_waitcnt lgkmcnt(2)\n\t" \
    "v_mfma_f32_16x16x32_bf16 v[204:207], v[124:127], v[240:243], v[204:207]\n\t" \
    "ds_read_b128 v[232:235], v253 offset:16384\n\t" \
    "v_mfma_f32_16x16x32_bf16 v[208:211], v[128:131], v[236:239], v[208:211]\n\t" \
    "v_mfma_f32_16x16x32_bf16 v[208:211], v[132:135], v[240:243], v[208:211]\n\t" \
    "v_mfma_f32_16x16x32_bf16 v[200:203], v[244:247], v[236:239], v[200:203]\n\t" \
    "v_mfma_f32_16x16x32_bf16 v[200:203], v[248:251], v[240:243], v[200:203]\n\t" \
    "s_waitcnt lgkmcnt(2)\n\t" \
    "v_mfma_f32_16x16x32_bf16 v[216:219], v[120:123], v[224:227], v[216:219]\n\t" \
    "ds_read_b128 v[236:239], v253 offset:17408\n\t" \
    "s_waitcnt lgkmcnt(2)\n\t" \
    "v_mfma_f32_16x16x32_bf16 v[216:219], v[124:127], v[228:231], v[216:219]\n\t" \
    "ds_read_b128 v[240:243], v253 offset:8192\n\t" \
    "v_mfma_f32_16x16x32_bf16 v[220:223], v[128:131], v[224:227], v[220:223]\n\t" \
    "v_mfma_f32_16x16x32_bf16 v[220:223], v[132:135], v[228:231], v[220:223]\n\t" \
    "v_mfma_f32_16x16x32_bf16 v[212:215], v[244:247], v[224:227], v[212:215]\n\t" \
    "ds_read_b128 v[244:247], v254 offset:38912\n\t" \
    "v_mfma_f32_16x16x32_bf16 v[212:215], v[248:251], v[228:231], v[212:215]\n\t" \
    "ds_read_b128 v[248:251], v254 offset:39936\n\t" \
    "v_and_b32 v252, 0xf0, v253\n\t" \
    "v_lshlrev_b32 v252, 3, v252\n\t" \
    "buffer_load_dword v252, v252, s[60:63], %[fl] offen sc1\n\t" \
    "s_waitcnt lgkmcnt(4)\n\t" \
    "v_mfma_f32_16x16x32_bf16 v[204:207], v[144:147], v[232:235], v[204:207]\n\t" \
    "ds_read_b128 v[224:227], v253 offset:9216\n\t" \
    "s_waitcnt lgkmcnt(4)\n\t" \
    "v_mfma_f32_16x16x32_bf16 v[204:207], v[148:151], v[236:239], v[204:207]\n\t" \
    "ds_read_b128 v[228:231], v253 offset:28672\n\t" \
    "v_mfma_f32_16x16x32_bf16 v[208:211], v[152:155], v[232:235], v[208:211]\n\t" \
    "v_mfma_f32_16x16x32_bf16 v[208:211], v[156:159], v[236:239], v[208:211]\n\t" \
    "v_mfma_f32_16x16x32_bf16 v[200:203], v[136:139], v[232:235], v[200:203]\n\t" \
    "v_mfma_f32_16x16x32_bf16 v[200:203], v[140:143], v[236:239], v[200:203]\n\t" \
    "s_waitcnt lgkmcnt(4)\n\t" \
    "v_mfma_f32_16x16x32_bf16 v[204:207], v[160:163], v[240:243], v[204:207]\n\t" \
    "ds_read_b128 v[232:235], v253 offset:29696\n\t" \
    "s_waitcnt lgkmcnt(2)\n\t" \
    "v_mfma_f32_16x16x32_bf16 v[204:207], v[164:167], v[224:227], v[204:207]\n\t" \
    "ds_read_b128 v[236:239], v253 offset:18432\n\t" \
    "v_mfma_f32_16x16x32_bf16 v[208:211], v[168:171], v[240:243], v[208:211]\n\t" \
    "v_mfma_f32_16x16x32_bf16 v[208:211], v[172:175], v[224:227], v[208:211]\n\t" \
    "v_mfma_f32_16x16x32_bf16 v[200:203], v[244:247], v[240:243], v[200:203]\n\t" \
    "v_mfma_f32_16x16x32_bf16 v[200:203], v[248:251], v[224:227], v[200:203]\n\t" \
    "s_waitcnt lgkmcnt(2)\n\t" \
    "v_mfma_f32_16x16x32_bf16 v[216:219], v[160:163], v[228:231], v[216:219]\n\t" \
    "ds_read_b128 v[240:243], v253 offset:19456\n\t" \
    "s_waitcnt lgkmcnt(2)\n\t" \
    "v_mfma_f32_16x16x32_bf16 v[216:219], v[164:167], v[232:235], v[216:219]\n\t" \
    "ds_read_b128 v[224:227], v253 offset:30720\n\t" \
    "v_mfma_f32_16x16x32_bf16 v[220:223], v[168:171], v[228:231], v[220:223]\n\t" \
    "v_mfma_f32_16x16x32_bf16 v[220:223], v[172:175], v[232:235], v[220:223]\n\t" \
    "v_mfma_f32_16x16x32_bf16 v[212:215], v[244:247], v[228:231], v[212:215]\n\t" \
    "v_mfma_f32_16x16x32_bf16 v[212:215], v[248:251], v[232:235], v[212:215]\n\t" \
    "s_waitcnt lgkmcnt(2)\n\t" \
    "v_mfma_f32_16x16x32_bf16 v[204:207], v[184:187], v[236:239], v[204:207]\n\t" \
    "ds_read_b128 v[228:231], v253 offset:31744\n\t" \
    "s_waitcnt lgkmcnt(2)\n\t" \
    "v_mfma_f32_16x16x32_bf16 v[204:207], v[188:191], v[240:243], v[204:207]\n\t" \
    "ds_read_b128 v[232:235], v253 offset:32768\n\t" \
    "v_mfma_f32_16x16x32_bf16 v[208:211], v[192:195], v[236:239], v[208:211]\n\t" \
    "v_mfma_f32_16x16x32_bf16 v[208:211], v[196:199], v[240:243], v[208:211]\n\t" \
    "v_mfma_f32_16x16x32_bf16 v[200:203], v[176:179], v[236:239], v[200:203]\n\t" \
    "v_mfma_f32_16x16x32_bf16 v[200:203], v[180:183], v[240:243], v[200:203]\n\t" \
    "s_waitcnt lgkmcnt(2)\n\t" \
    "v_mfma_f32_16x16x32_bf16 v[216:219], v[24:27], v[224:227], v[216:219]\n\t" \
    "ds_read_b128 v[236:239], v253 offset:33792\n\t" \
    "s_waitcnt lgkmcnt(2)\n\t" \
    "v_mfma_f32_16x16x32_bf16 v[216:219], v[28:31], v[228:231], v[216:219]\n\t" \
    "ds_read_b128 v[240:243], v253 offset:34816\n\t" \
    "v_mfma_f32_16x16x32_bf16 v[220:223], v[32:35], v[224:227], v[220:223]\n\t" \
    "v_cvt_pk_bf16_f32 v204, v204, v205\n\t" \
    "v_mfma_f32_16x16x32_bf16 v[220:223], v[36:39], v[228:231], v[220:223]\n\t" \
    "v_cvt_pk_bf16_f32 v205, v206, v207\n\t" \
    "v_mfma_f32_16x16x32_bf16 v[212:215], v[16:19], v[224:227], v[212:215]\n\t" \
    "ds_write_b64 v254, v[204:205] offset:7168\n\t" \
    "v_mfma_f32_16x16x32_bf16 v[212:215], v[20:23], v[228:231], v[212:215]\n\t" \
    "v_cvt_pk_bf16_f32 v208, v208, v209\n\t" \
    "s_waitcnt lgkmcnt(3)\n\t" \
    "v_mfma_f32_16x16x32_bf16 v[216:219], v[64:67], v[232:235], v[216:219]\n\t" \
    "ds_read_b128 v[224:227], v253 offset:35840\n\t" \
    "v_cvt_pk_bf16_f32 v209, v210, v211\n\t" \
    "s_waitcnt lgkmcnt(3)\n\t" \
    "v_mfma_f32_16x16x32_bf16 v[216:219], v[68:71], v[236:239], v[216:219]\n\t" \
    "ds_read_b128 v[228:231], v253 offset:36864\n\t" \
    "ds_write_b64 v254, v[208:209] offset:8192\n\t" \
    "v_mfma_f32_16x16x32_bf16 v[220:223], v[72:75], v[232:235], v[220:223]\n\t" \
    "v_cvt_pk_bf16_f32 v200, v200, v201\n\t" \
    "v_mfma_f32_16x16x32_bf16 v[220:223], v[76:79], v[236:239], v[220:223]\n\t" \
    "v_cvt_pk_bf16_f32 v201, v202, v203\n\t" \
    "v_mfma_f32_16x16x32_bf16 v[212:215], v[56:59], v[232:235], v[212:215]\n\t" \
    "ds_write_b64 v254, v[200:201] offset:6144\n\t" \
    "v_mfma_f32_16x16x32_bf16 v[212:215], v[60:63], v[236:239], v[212:215]\n\t" \
    "s_waitcnt lgkmcnt(5)\n\t" \
    "v_mfma_f32_16x16x32_bf16 v[216:219], v[104:107], v[240:243], v[216:219]\n\t" \
    "ds_read_b128 v[232:235], v253 offset:37888\n\t" \
    "s_waitcnt lgkmcnt(4)\n\t" \
    "v_mfma_f32_16x16x32_bf16 v[216:219], v[108:111], v[224:227], v[216:219]\n\t" \
    "ds_read_b128 v[236:239], v253 offset:38912\n\t" \
    "v_mfma_f32_16x16x32_bf16 v[220:223], v[112:115], v[240:243], v[220:223]\n\t" \
    "v_mfma_f32_16x16x32_bf16 v[220:223], v[116:119], v[224:227], v[220:223]\n\t" \
    "v_mfma_f32_16x16x32_bf16 v[212:215], v[96:99], v[240:243], v[212:215]\n\t" \
    "v_mfma_f32_16x16x32_bf16 v[212:215], v[100:103], v[224:227], v[212:215]\n\t" \
    "ds_read_b128 v[240:243], v253 offset:39936\n\t" \
    "s_waitcnt lgkmcnt(3)\n\t" \
    "s_barrier \n\t" \
    "v_mfma_f32_16x16x32_bf16 v[216:219], v[144:147], v[228:231], v[216:219]\n\t" \
    "s_waitcnt lgkmcnt(2)\n\t" \
    "v_mfma_f32_16x16x32_bf16 v[216:219], v[148:151], v[232:235], v[216:219]\n\t" \
    "s_waitcnt lgkmcnt(1)\n\t" \
    "v_mfma_f32_16x16x32_bf16 v[216:219], v[184:187], v[236:239], v[216:219]\n\t" \
    "s_waitcnt lgkmcnt(0)\n\t" \
    "v_mfma_f32_16x16x32_bf16 v[216:219], v[188:191], v[240:243], v[216:219]\n\t" \
    "v_mfma_f32_16x16x32_bf16 v[220:223], v[152:155], v[228:231], v[220:223]\n\t" \
    "v_mfma_f32_16x16x32_bf16 v[220:223], v[156:159], v[232:235], v[220:223]\n\t" \
    "v_mfma_f32_16x16x32_bf16 v[220:223], v[192:195], v[236:239], v[220:223]\n\t" \
    "v_cvt_pk_bf16_f32 v216, v216, v217\n\t" \
    "v_cvt_pk_bf16_f32 v217, v218, v219\n\t" \
    "v_mfma_f32_16x16x32_bf16 v[220:223], v[196:199], v[240:243], v[220:223]\n\t" \
    "ds_write_b64 v254, v[216:217] offset:13312\n\t" \
    "v_mfma_f32_16x16x32_bf16 v[212:215], v[136:139], v[228:231], v[212:215]\n\t" \
    "v_mfma_f32_16x16x32_bf16 v[212:215], v[140:143], v[232:235], v[212:215]\n\t" \
    "v_mfma_f32_16x16x32_bf16 v[212:215], v[176:179], v[236:239], v[212:215]\n\t" \
    "v_cvt_pk_bf16_f32 v220, v220, v221\n\t" \
    "v_cvt_pk_bf16_f32 v221, v222, v223\n\t" \
    "v_mfma_f32_16x16x32_bf16 v[212:215], v[180:183], v[240:243], v[212:215]\n\t" \
    "ds_write_b64 v254, v[220:221] offset:14336\n\t" \
    "s_nop 6\n\t" \
    "v_cvt_pk_bf16_f32 v212, v212, v213\n\t" \
    "v_cvt_pk_bf16_f32 v213, v214, v215\n\t" \
    "ds_write_b64 v254, v[212:213] offset:12288\n\t" \
    LST6 \
    LJIT \
    "s_waitcnt vmcnt(0) lgkmcnt(0)\n\t" \
    "s_barrier \n\t" \
    "s_setprio 2\n\t" \
    "s_add_u32 s44, s40, 1\n\t" \
    "s_min_u32 s44, s44, 63\n\t" \
    ".Lv08_%=:\n\t" \
    "v_cmp_ge_u32 vcc, s44, v252\n\t" \
    "s_cbranch_vccz .Lv09_%=\n\t" \
    "s_cmp_eq_u32 s41, 0\n\t" \
    "s_cbranch_scc1 .Lv09_%=\n\t" \
    "s_sub_u32 s41, s41, 1\n\t" \
    "s_sleep 2\n\t" \
    "v_and_b32 v252, 0xf0, v253\n\t" \
    "v_lshlrev_b32 v252, 3, v252\n\t" \
    "buffer_load_dword v252, v252, s[60:63], %[fl] offen sc1\n\t" \
    "s_waitcnt vmcnt(0)\n\t" \
    "s_branch .Lv08_%=\n\t" \
    ".Lv09_%=:\n\t" \
    "s_mov_b32 m0, 0x0\n\t" \
    "s_add_u32 s49, s42, 0x0\n\t" \
    "buffer_load_dwordx4 v253, s[60:63], s49 offen offset:0 sc1 lds\n\t" \
    "s_sleep 1\n\t" \
    "buffer_load_dwordx4 v253, s[60:63], s49 offen offset:1024 sc1 lds\n\t" \
    "s_sleep 1\n\t" \
    "buffer_load_dwordx4 v253, s[60:63], s49 offen offset:2048 sc1 lds\n\t" \
    "s_sleep 1\n\t" \
    "buffer_load_dwordx4 v253, s[60:63], s49 offen offset:3072 sc1 lds\n\t" \
    "s_sleep 1\n\t" \
    "s_mov_b32 m0, 0x4000\n\t" \
    "s_add_u32 s49, s42, 0x10000\n\t" \
    "buffer_load_dwordx4 v253, s[60:63], s49 offen offset:0 sc1 lds\n\t" \
    "s_sleep 1\n\t" \
    "s_mov_b32 m0, 0x5000\n\t" \
    "s_add_u32 s49, s42, 0x1000\n\t" \
    "buffer_load_dwordx4 v253, s[60:63], s49 offen offset:0 sc1 lds\n\t" \
    "s_sleep 1\n\t" \
    "buffer_load_dwordx4 v253, s[60:63], s49 offen offset:1024 sc1 lds\n\t" \
    "s_sleep 1\n\t" \
    "buffer_load_dwordx4 v253, s[60:63], s49 offen offset:2048 sc1 lds\n\t" \
    "s_sleep 1\n\t" \
    "buffer_load_dwordx4 v253, s[60:63], s49 offen offset:3072 sc1 lds\n\t" \
    "s_sleep 1\n\t" \
    "s_mov_b32 m0, 0x9000\n\t" \
    "s_add_u32 s49, s42, 0x11000\n\t" \
    "buffer_load_dwordx4 v253, s[60:63], s49 offen offset:0 sc1 lds\n\t" \
    "s_sleep 1\n\t" \
    "ds_read_b128 v[200:203], v254 offset:30720\n\t" \
    "ds_read_b128 v[204:207], v254 offset:31744\n\t" \
    "ds_read_b128 v[216:219], v254 offset:32768\n\t" \
    "ds_read_b128 v[220:223], v254 offset:33792\n\t" \
    "v_mov_b64 v[208:209], 0\n\t" \
    "v_mov_b64 v[210:211], 0\n\t" \
    "v_mov_b64 v[244:245], 0\n\t" \
    "v_mov_b64 v[246:247], 0\n\t" \
    "v_mov_b64 v[248:249], 0\n\t" \
    "v_mov_b64 v[250:251], 0\n\t" \
    LST1 \
    LJIT \
    "s_sleep 5\n\t" \
    "s_mov_b32 m0, 0xa000\n\t" \
    "s_nop 0\n\t" \
    "buffer_load_dwordx4 v253, s[60:63], s46 offen sc1 lds\n\t" \
    "s_mov_b32 m0, 0xb400\n\t" \
    "s_nop 0\n\t" \
    "buffer_load_dwordx4 v253, s[60:63], s47 offen sc1 lds\n\t" \
    "s_mov_b32 m0, 0xa400\n\t" \
    "s_nop 0\n\t" \
    "buffer_load_dwordx4 v253, s[60:63], s48 offen sc1 lds\n\t" \
    "s_waitcnt lgkmcnt(0)\n\t" \
    LFLUSH212 \
    "v_bfe_i32 v212, v253, 5, 1\n\t" \
    "v_and_b32 v212, 0xaaaa, v212\n\t" \
    "v_or_b32 v212, 0x4444, v212\n\t" \
    "v_and_b32 v213, 0xffff, v255\n\t" \
    "v_lshrrev_b32 v214, 16, v255\n\t" \
    ".Lv010_%=:\n\t" \
    "s_waitcnt vmcnt(0)\n\t" \
    "ds_read_b128 v[224:227], v253 offset:40960\n\t" \
    "ds_read_b128 v[228:231], v253 offset:46080\n\t" \
    "ds_read_b128 v[232:235], v253 offset:41984\n\t" \
    "s_waitcnt lgkmcnt(2)\n\t" \
    "v_max_u32 v215, v224, v225\n\t" \
    "v_max3_u32 v215, v226, v227, v215\n\t" \
    "s_waitcnt lgkmcnt(1)\n\t" \
    "v_max3_u32 v215, v228, v229, v215\n\t" \
    "v_max3_u32 v215, v230, v231, v215\n\t" \
    "s_waitcnt lgkmcnt(0)\n\t" \
    "v_max3_u32 v215, v232, v233, v215\n\t" \
    "v_max3_u32 v215, v234, v235, v215\n\t" \
    "v_cmp_eq_u32 vcc, -1, v215\n\t" \
    "s_cbranch_vccz .Lv011_%=\n\t" \
    "v_max_u32 v215, v224, v225\n\t" \
    "v_max3_u32 v215, v226, v227, v215\n\t" \
    "v_cmp_eq_u32 vcc, -1, v215\n\t" \
    "s_cbranch_vccz .Lv012_%=\n\t" \
    "s_mov_b32 m0, 0xa000\n\t" \
    "s_nop 0\n\t" \
    "buffer_load_dwordx4 v253, s[60:63], s46 offen sc1 lds\n\t" \
    ".Lv012_%=:\n\t" \
    "v_max_u32 v215, v228, v229\n\t" \
    "v_max3_u32 v215, v230, v231, v215\n\t" \
    "v_cmp_eq_u32 vcc, -1, v215\n\t" \
    "s_cbranch_vccz .Lv013_%=\n\t" \
    "s_mov_b32 m0, 0xb400\n\t" \
    "s_nop 0\n\t" \
    "buffer_load_dwordx4 v253, s[60:63], s47 offen sc1 lds\n\t" \
    ".Lv013_%=:\n\t" \
    "v_max_u32 v215, v232, v233\n\t" \
    "v_max3_u32 v215, v234, v235, v215\n\t" \
    "v_cmp_eq_u32 vcc, -1, v215\n\t" \
    "s_cbranch_vccz .Lv014_%=\n\t" \
    "s_mov_b32 m0, 0xa400\n\t" \
    "s_nop 0\n\t" \
    "buffer_load_dwordx4 v253, s[60:63], s48 offen sc1 lds\n\t" \
    ".Lv014_%=:\n\t" \
    "s_cmp_eq_u32 s41, 0\n\t" \
    "s_cbranch_scc1 .Lv011_%=\n\t" \
    "s_sub_u32 s41, s41, 1\n\t" \
    "s_branch .Lv010_%=\n\t" \
    ".Lv011_%=:\n\t" \
    LST2 \
    LJIT \
    "s_waitcnt lgkmcnt(0)\n\t" \
    "s_barrier \n\t" \
    LST3 \
    LJIT \
    "ds_read_b128 v[236:239], v253 offset:47104\n\t" \
    "ds_read_b128 v[240:243], v253 offset:43008\n\t" \
    "v_smfmac_f32_16x16x64_bf16 v[244:247], v[224:227], v[0:7], v212\n\t" \
    "v_smfmac_f32_16x16x64_bf16 v[248:251], v[224:227], v[8:15], v212\n\t" \
    "v_smfmac_f32_16x16x64_bf16 v[208:211], v[224:227], v[200:207], v212\n\t" \
    "ds_read_b128 v[200:203], v254 offset:34816\n\t" \
    "ds_read_b128 v[204:207], v254 offset:35840\n\t" \
    "ds_read_b128 v[224:227], v253 offset:48128\n\t" \
    "v_smfmac_f32_16x16x64_bf16 v[208:211], v[228:231], v[16:23], v212\n\t" \
    "v_smfmac_f32_16x16x64_bf16 v[244:247], v[228:231], v[24:31], v212\n\t" \
    "v_smfmac_f32_16x16x64_bf16 v[248:251], v[228:231], v[32:39], v212\n\t" \
    "ds_read_b128 v[228:231], v253 offset:44032\n\t" \
    "v_smfmac_f32_16x16x64_bf16 v[208:211], v[232:235], v[216:223], v212\n\t" \
    "ds_read_b128 v[216:219], v254 offset:36864\n\t" \
    "ds_read_b128 v[220:223], v254 offset:37888\n\t" \
    "v_smfmac_f32_16x16x64_bf16 v[244:247], v[232:235], v[40:47], v212\n\t" \
    "v_smfmac_f32_16x16x64_bf16 v[248:251], v[232:235], v[48:55], v212\n\t" \
    "ds_read_b128 v[232:235], v253 offset:49152\n\t" \
    "s_waitcnt lgkmcnt(8)\n\t" \
    "v_smfmac_f32_16x16x64_bf16 v[208:211], v[236:239], v[56:63], v212\n\t" \
    "v_smfmac_f32_16x16x64_bf16 v[244:247], v[236:239], v[64:71], v212\n\t" \
    "v_smfmac_f32_16x16x64_bf16 v[248:251], v[236:239], v[72:79], v212\n\t" \
    "ds_read_b128 v[236:239], v253 offset:45056\n\t" \
    "s_waitcnt lgkmcnt(6)\n\t" \
    "v_smfmac_f32_16x16x64_bf16 v[208:211], v[240:243], v[200:207], v212\n\t" \
    "ds_read_b128 v[200:203], v254 offset:38912\n\t" \
    "ds_read_b128 v[204:207], v254 offset:39936\n\t" \
    "v_smfmac_f32_16x16x64_bf16 v[244:247], v[240:243], v[80:87], v212\n\t" \
    "v_smfmac_f32_16x16x64_bf16 v[248:251], v[240:243], v[88:95], v212\n\t" \
    "ds_read_b128 v[240:243], v253 offset:50176\n\t" \
    "s_waitcnt lgkmcnt(8)\n\t" \
    "v_smfmac_f32_16x16x64_bf16 v[208:211], v[224:227], v[96:103], v212\n\t" \
    "v_smfmac_f32_16x16x64_bf16 v[244:247], v[224:227], v[104:111], v212\n\t" \
    "v_smfmac_f32_16x16x64_bf16 v[248:251], v[224:227], v[112:119], v212\n\t" \
    "s_waitcnt lgkmcnt(7)\n\t" \
    "v_smfmac_f32_16x16x64_bf16 v[244:247], v[228:231], v[120:127], v212\n\t" \
    "s_waitcnt lgkmcnt(4)\n\t" \
    "v_smfmac_f32_16x16x64_bf16 v[244:247], v[232:235], v[144:151], v212\n\t" \
    "s_waitcnt lgkmcnt(3)\n\t" \
    "v_smfmac_f32_16x16x64_bf16 v[244:247], v[236:239], v[160:167], v212\n\t" \
    "s_waitcnt lgkmcnt(0)\n\t" \
    "v_smfmac_f32_16x16x64_bf16 v[244:247], v[240:243], v[184:191], v212\n\t" \
    "v_smfmac_f32_16x16x64_bf16 v[248:251], v[228:231], v[128:135], v212\n\t" \
    "v_smfmac_f32_16x16x64_bf16 v[248:251], v[232:235], v[152:159], v212\n\t" \
    "v_smfmac_f32_16x16x64_bf16 v[248:251], v[236:239], v[168:175], v212\n\t" \
    "v_add_f32 v244, v244, v246\n\t" \
    "v_add_f32 v245, v245, v247\n\t" \
    "v_cvt_pk_bf16_f32 v244, v244, v245\n\t" \
    "v_smfmac_f32_16x16x64_bf16 v[248:251], v[240:243], v[192:199], v212\n\t" \
    "ds_write_b16 v213, v244 offset:512\n\t" \
    "ds_write_b16_d16_hi v213, v244 offset:528\n\t" \
    "v_smfmac_f32_16x16x64_bf16 v[208:211], v[228:231], v[216:223], v212\n\t" \
    "v_smfmac_f32_16x16x64_bf16 v[208:211], v[232:235], v[136:143], v212\n\t" \
    "v_smfmac_f32_16x16x64_bf16 v[208:211], v[236:239], v[200:207], v212\n\t" \
    "v_add_f32 v248, v248, v250\n\t" \
    "v_add_f32 v249, v249, v251\n\t" \
    "v_cvt_pk_bf16_f32 v248, v248, v249\n\t" \
    "v_smfmac_f32_16x16x64_bf16 v[208:211], v[240:243], v[176:183], v212\n\t" \
    "ds_write_b16 v213, v248 offset:1024\n\t" \
    "ds_write_b16_d16_hi v213, v248 offset:1040\n\t" \
    "s_nop 5\n\t" \
    "v_add_f32 v208, v208, v210\n\t" \
    "v_add_f32 v209, v209, v211\n\t" \
    "v_cvt_pk_bf16_f32 v208, v208, v209\n\t" \
    "ds_write_b16 v213, v208 offset:0\n\t" \
    "ds_write_b16_d16_hi v213, v208 offset:16\n\t" \
    "s_setprio 0\n\t" \
    "s_waitcnt lgkmcnt(0)\n\t" \
    "s_barrier \n\t" \
    LST4 \
    LJIT \
    "ds_read_b128 v[224:227], v253 offset:0\n\t" \
    "ds_read_b128 v[244:247], v254 offset:30720\n\t" \
    "ds_read_b128 v[228:231], v253 offset:1024\n\t" \
    "ds_read_b128 v[232:235], v253 offset:20480\n\t" \
    "ds_read_b128 v[236:239], v253 offset:21504\n\t" \
    "ds_read_b128 v[248:251], v254 offset:31744\n\t" \
    "s_add_u32 s40, s40, 1\n\t" \
    "s_cmp_lg_u32 s40, 64\n\t" \
    "s_cbranch_scc1 .Lv07_%=\n\t" \
    "s_branch .Lend%=\n\t" \
    ".Lend%=:\n\t" \
    "s_waitcnt vmcnt(0) lgkmcnt(0)\n\t" \
    "s_mov_b32 %[bud], s41\n\t"
#define ASM_R_LOOP \
    "s_mov_b32 s40, 0\n\t" \
    "s_mov_b32 s41, 0x40000\n\t" \
    LSTINIT \
    "v_mov_b64 v[252:253], -1\n\t" \
    "v_mov_b64 v[254:255], -1\n\t" \
    "v_mov_b32 v248, 0\n\t" \
    "s_mov_b32 s53, 0x05040100\n\t" \
    ".Lr1_%=:\n\t" \
    "s_mov_b32 s43, s40\n\t" \
    "s_and_b32 s44, s40, 3\n\t" \
    "s_lshl_b32 s42, s44, 22\n\t" \
    "s_add_u32 s42, s42, %[vx]\n\t" \
    "s_lshl_b32 s46, s44, 22\n\t" \
    "s_mov_b32 s47, s46\n\t" \
    "s_lshl_b32 s49, s44, 17\n\t" \
    "s_add_u32 s49, s49, %[px]\n\t" \
    "s_add_u32 s50, s49, 0x1000\n\t" \
    "s_add_u32 s44, s40, 1\n\t" \
    "s_and_b32 s44, s44, 3\n\t" \
    "s_lshl_b32 s48, s44, 22\n\t" \
    "s_mul_i32 s52, s40, 0x14000\n\t" \
    "s_lshr_b32 s44, s40, 4\n\t" \
    "s_mul_i32 s44, s44, 0xc2000\n\t" \
    "s_add_u32 s52, s52, s44\n\t" \
    "s_add_u32 s52, s52, %[xr]\n\t" \
    "s_add_i32 s44, s40, %[ad]\n\t" \
    "s_and_b32 s45, s44, 3\n\t" \
    "s_mul_i32 s51, s45, %[as]\n\t" \
    "s_cmp_lt_i32 s44, 0\n\t" \
    "s_cselect_b32 s55, 0x40000000, 0\n\t" \
    "s_add_u32 s44, s40, 3\n\t" \
    "s_mul_i32 s54, s44, 0x14000\n\t" \
    "s_lshr_b32 s45, s44, 4\n\t" \
    "s_mul_i32 s45, s45, 0xc2000\n\t" \
    "s_add_u32 s54, s54, s45\n\t" \
    "s_cmp_eq_u32 %[wave], 7\n\t" \
    "s_cselect_b32 s51, s54, s51\n\t" \
    "s_cselect_b32 s55, 0, s55\n\t" \
    "s_cselect_b32 s44, 60, 62\n\t" \
    "s_cmp_gt_u32 s40, s44\n\t" \
    "s_cselect_b32 s55, 0x40000000, s55\n\t" \
    "v_or_b32 v197, s55, %[ae]\n\t" \
    "v_mov_b64 v[180:181], -1\n\t" \
    "v_mov_b64 v[182:183], -1\n\t" \
    "v_mov_b64 v[184:185], -1\n\t" \
    "v_mov_b64 v[186:187], -1\n\t" \
    "v_mov_b64 v[188:189], -1\n\t" \
    "v_mov_b64 v[190:191], -1\n\t" \
    "v_mov_b64 v[192:193], -1\n\t" \
    "v_mov_b64 v[194:195], -1\n\t" \
    "s_waitcnt vmcnt(0)\n\t" \
    ".Lr2_%=:\n\t" \
    "buffer_load_dword v248, %[fo], %[rs], %[fs] offen sc1\n\t" \
    "s_waitcnt vmcnt(0)\n\t" \
    "v_cmp_ge_u32 vcc, s43, v248\n\t" \
    "s_cbranch_vccz .Lr3_%=\n\t" \
    "s_sleep 2\n\t" \
    "s_cmp_eq_u32 s41, 0\n\t" \
    "s_cbranch_scc1 .Lr3_%=\n\t" \
    "s_sub_u32 s41, s41, 1\n\t" \
    "s_branch .Lr2_%=\n\t" \
    ".Lr3_%=:\n\t" \
    "buffer_load_dwordx4 v[160:163], %[l16], %[rs], s42 offen offset:0 sc1\n\t" \
    "buffer_load_dwordx4 v[164:167], %[l16], %[rs], s42 offen offset:1024 sc1\n\t" \
    "buffer_load_dwordx4 v[168:171], %[l16], %[rs], s42 offen offset:2048 sc1\n\t" \
    "buffer_load_dwordx4 v[172:175], %[l16], %[rs], s42 offen offset:3072 sc1\n\t" \
    LST0 \
    LJIT \
    "s_cmp_lg_u32 s40, 63\n\t" \
    "s_cbranch_scc1 .Lr6_%=\n\t" \
    "buffer_load_dwordx4 v[180:183], %[ov], %[rs], s46 offen sc1\n\t" \
    ".Lr7_%=:\n\t" \
    "s_waitcnt vmcnt(12)\n\t" \
    "v_max_u32 v196, v180, v181\n\t" \
    "v_max3_u32 v196, v182, v183, v196\n\t" \
    "v_cmp_eq_u32 vcc, -1, v196\n\t" \
    "s_cbranch_vccz .Lr8_%=\n\t" \
    "s_nop 7\n\t" \
    "v_max_u32 v196, v180, v181\n\t" \
    "v_max3_u32 v196, v182, v183, v196\n\t" \
    "v_cmp_eq_u32 vcc, -1, v196\n\t" \
    "s_cbranch_vccz .Lr8_%=\n\t" \
    "s_nop 7\n\t" \
    "v_max_u32 v196, v180, v181\n\t" \
    "v_max3_u32 v196, v182, v183, v196\n\t" \
    "v_cmp_eq_u32 vcc, -1, v196\n\t" \
    "s_cbranch_vccz .Lr8_%=\n\t" \
    "s_nop 7\n\t" \
    "v_max_u32 v196, v180, v181\n\t" \
    "v_max3_u32 v196, v182, v183, v196\n\t" \
    "v_cmp_eq_u32 vcc, -1, v196\n\t" \
    "s_cbranch_vccz .Lr8_%=\n\t" \
    "s_nop 7\n\t" \
    "s_cmp_eq_u32 s41, 0\n\t" \
    "s_cbranch_scc1 .Lr8_%=\n\t" \
    "s_sub_u32 s41, s41, 1\n\t" \
    "v_max_u32 v196, v180, v181\n\t" \
    "v_max3_u32 v196, v182, v183, v196\n\t" \
    "v_cmp_eq_u32 vcc, -1, v196\n\t" \
    "s_cbranch_vccz .Lr9_%=\n\t" \
    "buffer_load_dwordx4 v[180:183], %[ov], %[rs], s46 offen sc1\n\t" \
    ".Lr9_%=:\n\t" \
    "s_branch .Lr7_%=\n\t" \
    ".Lr8_%=:\n\t" \
    "s_branch .Lr5_%=\n\t" \
    ".Lr6_%=:\n\t" \
    "s_sleep 9\n\t" \
    LST7 \
    LJIT \
    "buffer_load_dwordx4 v[184:187], %[o0], %[rs], s47 offen sc1\n\t" \
    "buffer_load_dwordx4 v[188:191], %[o1], %[rs], s47 offen sc1\n\t" \
    "buffer_load_dwordx4 v[192:195], %[o2], %[rs], s47 offen sc1\n\t" \
    "buffer_load_dwordx4 v[180:183], %[ov], %[rs], s46 offen sc1\n\t" \
    ".Lr10_%=:\n\t" \
    "s_waitcnt vmcnt(12)\n\t" \
    "v_max_u32 v196, v184, v185\n\t" \
    "v_max3_u32 v196, v186, v187, v196\n\t" \
    "v_max3_u32 v196, v188, v189, v196\n\t" \
    "v_max3_u32 v196, v190, v191, v196\n\t" \
    "v_max3_u32 v196, v192, v193, v196\n\t" \
    "v_max3_u32 v196, v194, v195, v196\n\t" \
    "v_max3_u32 v196, v180, v181, v196\n\t" \
    "v_max3_u32 v196, v182, v183, v196\n\t" \
    "v_cmp_eq_u32 vcc, -1, v196\n\t" \
    "s_cbranch_vccz .Lr11_%=\n\t" \
    "s_nop 7\n\t" \
    "v_max_u32 v196, v184, v185\n\t" \
    "v_max3_u32 v196, v186, v187, v196\n\t" \
    "v_max3_u32 v196, v188, v189, v196\n\t" \
    "v_max3_u32 v196, v190, v191, v196\n\t" \
    "v_max3_u32 v196, v192, v193, v196\n\t" \
    "v_max3_u32 v196, v194, v195, v196\n\t" \
    "v_max3_u32 v196, v180, v181, v196\n\t" \
    "v_max3_u32 v196, v182, v183, v196\n\t" \
    "v_cmp_eq_u32 vcc, -1, v196\n\t" \
    "s_cbranch_vccz .Lr11_%=\n\t" \
    "s_nop 7\n\t" \
    "v_max_u32 v196, v184, v185\n\t" \
    "v_max3_u32 v196, v186, v187, v196\n\t" \
    "v_max3_u32 v196, v188, v189, v196\n\t" \
    "v_max3_u32 v196, v190, v191, v196\n\t" \
    "v_max3_u32 v196, v192, v193, v196\n\t" \
    "v_max3_u32 v196, v194, v195, v196\n\t" \
    "v_max3_u32 v196, v180, v181, v196\n\t" \
    "v_max3_u32 v196, v182, v183, v196\n\t" \
    "v_cmp_eq_u32 vcc, -1, v196\n\t" \
    "s_cbranch_vccz .Lr11_%=\n\t" \
    "s_nop 7\n\t" \
    "v_max_u32 v196, v184, v185\n\t" \
    "v_max3_u32 v196, v186, v187, v196\n\t" \
    "v_max3_u32 v196, v188, v189, v196\n\t" \
    "v_max3_u32 v196, v190, v191, v196\n\t" \
    "v_max3_u32 v196, v192, v193, v196\n\t" \
    "v_max3_u32 v196, v194, v195, v196\n\t" \
    "v_max3_u32 v196, v180, v181, v196\n\t" \
    "v_max3_u32 v196, v182, v183, v196\n\t" \
    "v_cmp_eq_u32 vcc, -1, v196\n\t" \
    "s_cbranch_vccz .Lr11_%=\n\t" \
    "s_nop 7\n\t" \
    "v_max_u32 v196, v184, v185\n\t" \
    "v_max3_u32 v196, v186, v187, v196\n\t" \
    "v_max3_u32 v196, v188, v189, v196\n\t" \
    "v_max3_u32 v196, v190, v191, v196\n\t" \
    "v_max3_u32 v196, v192, v193, v196\n\t" \
    "v_max3_u32 v196, v194, v195, v196\n\t" \
    "v_max3_u32 v196, v180, v181, v196\n\t" \
    "v_max3_u32 v196, v182, v183, v196\n\t" \
    "v_cmp_eq_u32 vcc, -1, v196\n\t" \
    "s_cbranch_vccz .Lr11_%=\n\t" \
    "s_nop 7\n\t" \
    "v_max_u32 v196, v184, v185\n\t" \
    "v_max3_u32 v196, v186, v187, v196\n\t" \
    "v_max3_u32 v196, v188, v189, v196\n\t" \
    "v_max3_u32 v196, v190, v191, v196\n\t" \
    "v_max3_u32 v196, v192, v193, v196\n\t" \
    "v_max3_u32 v196, v194, v195, v196\n\t" \
    "v_max3_u32 v196, v180, v181, v196\n\t" \
    "v_max3_u32 v196, v182, v183, v196\n\t" \
    "v_cmp_eq_u32 vcc, -1, v196\n\t" \
    "s_cbranch_vccz .Lr11_%=\n\t" \
    "s_nop 7\n\t" \
    "v_max_u32 v196, v184, v185\n\t" \
    "v_max3_u32 v196, v186, v187, v196\n\t" \
    "v_max3_u32 v196, v188, v189, v196\n\t" \
    "v_max3_u32 v196, v190, v191, v196\n\t" \
    "v_max3_u32 v196, v192, v193, v196\n\t" \
    "v_max3_u32 v196, v194, v195, v196\n\t" \
    "v_max3_u32 v196, v180, v181, v196\n\t" \
    "v_max3_u32 v196, v182, v183, v196\n\t" \
    "v_cmp_eq_u32 vcc, -1, v196\n\t" \
    "s_cbranch_vccz .Lr11_%=\n\t" \
    "s_nop 7\n\t" \
    "v_max_u32 v196, v184, v185\n\t" \
    "v_max3_u32 v196, v186, v187, v196\n\t" \
    "v_max3_u32 v196, v188, v189, v196\n\t" \
    "v_max3_u32 v196, v190, v191, v196\n\t" \
    "v_max3_u32 v196, v192, v193, v196\n\t" \
    "v_max3_u32 v196, v194, v195, v196\n\t" \
    "v_max3_u32 v196, v180, v181, v196\n\t" \
    "v_max3_u32 v196, v182, v183, v196\n\t" \
    "v_cmp_eq_u32 vcc, -1, v196\n\t" \
    "s_cbranch_vccz .Lr11_%=\n\t" \
    "s_nop 7\n\t" \
    "s_cmp_eq_u32 s41, 0\n\t" \
    "s_cbranch_scc1 .Lr11_%=\n\t" \
    "s_sub_u32 s41, s41, 1\n\t" \
    "v_max_u32 v196, v184, v185\n\t" \
    "v_max3_u32 v196, v186, v187, v196\n\t" \
    "v_cmp_eq_u32 vcc, -1, v196\n\t" \
    "s_cbranch_vccz .Lr12_%=\n\t" \
    "buffer_load_dwordx4 v[184:187], %[o0], %[rs], s47 offen sc1\n\t" \
    ".Lr12_%=:\n\t" \
    "v_max_u32 v196, v188, v189\n\t" \
    "v_max3_u32 v196, v190, v191, v196\n\t" \
    "v_cmp_eq_u32 vcc, -1, v196\n\t" \
    "s_cbranch_vccz .Lr13_%=\n\t" \
    "buffer_load_dwordx4 v[188:191], %[o1], %[rs], s47 offen sc1\n\t" \
    ".Lr13_%=:\n\t" \
    "v_max_u32 v196, v192, v193\n\t" \
    "v_max3_u32 v196, v194, v195, v196\n\t" \
    "v_cmp_eq_u32 vcc, -1, v196\n\t" \
    "s_cbranch_vccz .Lr14_%=\n\t" \
    "buffer_load_dwordx4 v[192:195], %[o2], %[rs], s47 offen sc1\n\t" \
    ".Lr14_%=:\n\t" \
    "v_max_u32 v196, v180, v181\n\t" \
    "v_max3_u32 v196, v182, v183, v196\n\t" \
    "v_cmp_eq_u32 vcc, -1, v196\n\t" \
    "s_cbranch_vccz .Lr15_%=\n\t" \
    "buffer_load_dwordx4 v[180:183], %[ov], %[rs], s46 offen sc1\n\t" \
    ".Lr15_%=:\n\t" \
    "s_branch .Lr10_%=\n\t" \
    ".Lr11_%=:\n\t" \
    LST1 \
    LJIT \
    "v_mfma_f32_16x16x32_bf16 v[176:179], v[192:195], v[180:183], 0\n\t" \
    "v_smfmac_f32_16x16x64_bf16 v[176:179], v[184:187], v[160:167], %[sidx]\n\t" \
    "v_smfmac_f32_16x16x64_bf16 v[176:179], v[188:191], v[168:175], %[sidx]\n\t" \
    "s_cmp_gt_u32 %[wave], 3\n\t" \
    "s_nop 7\n\t" \
    "v_add_f32 v176, v176, v178\n\t" \
    "v_add_f32 v177, v177, v179\n\t" \
    "ds_write2_b32 %[rw], v176, v177 offset1:80\n\t" \
    "s_waitcnt lgkmcnt(0)\n\t" \
    "s_barrier \n\t" \
    LST2 \
    LJIT \
    "s_cbranch_scc1 .Lr4_%=\n\t" \
    "ds_read_b128 v[128:131], %[rd] offset:0\n\t" \
    "ds_read_b128 v[132:135], %[rd] offset:16\n\t" \
    "ds_read_b128 v[136:139], %[rd] offset:32\n\t" \
    "ds_read_b128 v[140:143], %[rd] offset:48\n\t" \
    "s_waitcnt lgkmcnt(2)\n\t" \
    "v_pk_add_f32 v[128:129], v[128:129], v[130:131]\n\t" \
    "v_pk_add_f32 v[132:133], v[132:133], v[134:135]\n\t" \
    "s_waitcnt lgkmcnt(0)\n\t" \
    "v_pk_add_f32 v[136:137], v[136:137], v[138:139]\n\t" \
    "v_pk_add_f32 v[140:141], v[140:141], v[142:143]\n\t" \
    "v_pk_add_f32 v[128:129], v[128:129], v[132:133]\n\t" \
    "v_pk_add_f32 v[136:137], v[136:137], v[140:141]\n\t" \
    "v_pk_add_f32 v[128:129], v[128:129], v[136:137]\n\t" \
    "v_cvt_pk_bf16_f32 v128, v128, v129\n\t" \
    "v_min_u32 v128, -2, v128\n\t" \
    "buffer_store_dword v128, %[oo], %[rs], s48 offen sc1\n\t" \
    ".Lr4_%=:\n\t" \
    LST3 \
    LJIT \
    "s_cmp_lt_u32 %[wave], 4\n\t" \
    "s_cbranch_scc1 .Lr17_%=\n\t" \
    "s_sleep 4\n\t" \
    ".Lr17_%=:\n\t" \
    "s_sleep 2\n\t" \
    ".Lr5_%=:\n\t" \
    "s_cmp_lt_u32 %[wave], 6\n\t" \
    "s_cbranch_scc1 .Lr16_%=\n\t" \
    "buffer_load_dwordx2 v[250:251], %[xo], %[rs], s52 offen sc1\n\t" \
    ".Lr16_%=:\n\t" \
    "buffer_load_dwordx4 v[220:223], %[l16], %[rs], s50 offen offset:1024 sc1\n\t" \
    "buffer_load_dwordx4 v[224:227], %[l16], %[rs], s50 offen offset:2048 sc1\n\t" \
    "buffer_load_dwordx4 v[228:231], %[l16], %[rs], s50 offen offset:3072 sc1\n\t" \
    "v_perm_b32 v232, v161, v160, s53\n\t" \
    "v_perm_b32 v233, v163, v162, s53\n\t" \
    "v_perm_b32 v234, v165, v164, s53\n\t" \
    "v_perm_b32 v235, v167, v166, s53\n\t" \
    "v_perm_b32 v236, v169, v168, s53\n\t" \
    "v_perm_b32 v237, v171, v170, s53\n\t" \
    "v_perm_b32 v238, v173, v172, s53\n\t" \
    "v_perm_b32 v239, v175, v174, s53\n\t" \
    "s_nop 1\n\t" \
    "v_permlane16_swap_b32 v232, v234\n\t" \
    "v_permlane16_swap_b32 v233, v235\n\t" \
    "v_permlane16_swap_b32 v236, v238\n\t" \
    "v_permlane16_swap_b32 v237, v239\n\t" \
    "s_waitcnt vmcnt(0)\n\t" \
    ".Lr18_%=:\n\t" \
    "v_max_u32 v196, v220, v221\n\t" \
    "v_max3_u32 v196, v222, v223, v196\n\t" \
    "v_max3_u32 v196, v224, v225, v196\n\t" \
    "v_max3_u32 v196, v226, v227, v196\n\t" \
    "v_max3_u32 v196, v228, v229, v196\n\t" \
    "v_max3_u32 v196, v230, v231, v196\n\t" \
    "v_cmp_eq_u32 vcc, -1, v196\n\t" \
    "s_cbranch_vccz .Lr19_%=\n\t" \
    "s_cmp_eq_u32 s41, 0\n\t" \
    "s_cbranch_scc1 .Lr19_%=\n\t" \
    "s_sub_u32 s41, s41, 1\n\t" \
    "v_max_u32 v196, v220, v221\n\t" \
    "v_max3_u32 v196, v222, v223, v196\n\t" \
    "v_cmp_eq_u32 vcc, -1, v196\n\t" \
    "s_cbranch_vccz .Lr20_%=\n\t" \
    "buffer_load_dwordx4 v[220:223], %[l16], %[rs], s50 offen offset:1024 sc1\n\t" \
    ".Lr20_%=:\n\t" \
    "v_max_u32 v196, v224, v225\n\t" \
    "v_max3_u32 v196, v226, v227, v196\n\t" \
    "v_cmp_eq_u32 vcc, -1, v196\n\t" \
    "s_cbranch_vccz .Lr21_%=\n\t" \
    "buffer_load_dwordx4 v[224:227], %[l16], %[rs], s50 offen offset:2048 sc1\n\t" \
    ".Lr21_%=:\n\t" \
    "v_max_u32 v196, v228, v229\n\t" \
    "v_max3_u32 v196, v230, v231, v196\n\t" \
    "v_cmp_eq_u32 vcc, -1, v196\n\t" \
    "s_cbranch_vccz .Lr22_%=\n\t" \
    "buffer_load_dwordx4 v[228:231], %[l16], %[rs], s50 offen offset:3072 sc1\n\t" \
    ".Lr22_%=:\n\t" \
    "s_waitcnt vmcnt(0)\n\t" \
    "s_branch .Lr18_%=\n\t" \
    ".Lr19_%=:\n\t" \
    "v_mfma_f32_16x16x32_bf16 v[240:243], v[232:235], v[220:223], 0\n\t" \
    "v_mfma_f32_16x16x32_bf16 v[240:243], v[236:239], v[224:227], v[240:243]\n\t" \
    "v_mfma_f32_16x16x32_bf16 v[240:243], v[180:183], v[228:231], v[240:243]\n\t" \
    "s_nop 7\n\t" \
    "ds_write_b128 %[pw], v[240:243]\n\t" \
    "s_waitcnt lgkmcnt(0)\n\t" \
    "s_barrier \n\t" \
    LST5 \
    LJIT \
    "buffer_load_dwordx4 v[200:203], %[l16], %[rs], s49 offen offset:0 sc1\n\t" \
    "buffer_load_dwordx4 v[204:207], %[l16], %[rs], s49 offen offset:1024 sc1\n\t" \
    "buffer_load_dwordx4 v[208:211], %[l16], %[rs], s49 offen offset:2048 sc1\n\t" \
    "buffer_load_dwordx4 v[212:215], %[l16], %[rs], s49 offen offset:3072 sc1\n\t" \
    "buffer_load_dwordx4 v[216:219], %[l16], %[rs], s50 offen offset:0 sc1\n\t" \
    "s_cmp_lg_u32 %[wave], 6\n\t" \
    "s_cbranch_scc1 .Lr23_%=\n\t" \
    "ds_read_b128 v[128:131], %[pr] offset:0\n\t" \
    "ds_read_b128 v[132:135], %[pr] offset:1024\n\t" \
    "ds_read_b128 v[136:139], %[pr] offset:2048\n\t" \
    "ds_read_b128 v[140:143], %[pr] offset:3072\n\t" \
    "ds_read_b128 v[144:147], %[pr] offset:4096\n\t" \
    "ds_read_b128 v[148:151], %[pr] offset:5120\n\t" \
    "ds_read_b128 v[152:155], %[pr] offset:6144\n\t" \
    "ds_read_b128 v[156:159], %[pr] offset:7168\n\t" \
    "v_lshlrev_b32 v176, 16, v250\n\t" \
    "v_and_b32 v177, 0xffff0000, v250\n\t" \
    "v_lshlrev_b32 v178, 16, v251\n\t" \
    "v_and_b32 v179, 0xffff0000, v251\n\t" \
    "s_waitcnt lgkmcnt(6)\n\t" \
    "v_pk_add_f32 v[128:129], v[128:129], v[132:133]\n\t" \
    "v_pk_add_f32 v[130:131], v[130:131], v[134:135]\n\t" \
    "s_waitcnt lgkmcnt(4)\n\t" \
    "v_pk_add_f32 v[136:137], v[136:137], v[140:141]\n\t" \
    "v_pk_add_f32 v[138:139], v[138:139], v[142:143]\n\t" \
    "v_pk_add_f32 v[128:129], v[128:129], v[136:137]\n\t" \
    "v_pk_add_f32 v[130:131], v[130:131], v[138:139]\n\t" \
    "s_waitcnt lgkmcnt(2)\n\t" \
    "v_pk_add_f32 v[144:145], v[144:145], v[148:149]\n\t" \
    "v_pk_add_f32 v[146:147], v[146:147], v[150:151]\n\t" \
    "s_waitcnt lgkmcnt(0)\n\t" \
    "v_pk_add_f32 v[152:153], v[152:153], v[156:157]\n\t" \
    "v_pk_add_f32 v[154:155], v[154:155], v[158:159]\n\t" \
    "v_pk_add_f32 v[144:145], v[144:145], v[152:153]\n\t" \
    "v_pk_add_f32 v[146:147], v[146:147], v[154:155]\n\t" \
    "v_pk_add_f32 v[128:129], v[128:129], v[144:145]\n\t" \
    "v_pk_add_f32 v[130:131], v[130:131], v[146:147]\n\t" \
    "v_pk_add_f32 v[128:129], v[128:129], v[176:177]\n\t" \
    "v_pk_add_f32 v[130:131], v[130:131], v[178:179]\n\t" \
    "v_cvt_pk_bf16_f32 v128, v128, v129\n\t" \
    "v_cvt_pk_bf16_f32 v129, v130, v131\n\t" \
    "v_min_u32 v128, -2, v128\n\t" \
    "v_min_u32 v129, -2, v129\n\t" \
    "s_add_u32 s44, s52, 0x1000000\n\t" \
    "buffer_store_dwordx2 v[128:129], %[xo], %[rs], s44 offen sc1\n\t" \
    "s_waitcnt vmcnt(1)\n\t" \
    "s_branch .Lr24_%=\n\t" \
    ".Lr23_%=:\n\t" \
    "s_waitcnt vmcnt(0)\n\t" \
    ".Lr24_%=:\n\t" \
    ".Lr25_%=:\n\t" \
    "v_max_u32 v196, v200, v201\n\t" \
    "v_max3_u32 v196, v202, v203, v196\n\t" \
    "v_max3_u32 v196, v204, v205, v196\n\t" \
    "v_max3_u32 v196, v206, v207, v196\n\t" \
    "v_max3_u32 v196, v208, v209, v196\n\t" \
    "v_max3_u32 v196, v210, v211, v196\n\t" \
    "v_max3_u32 v196, v212, v213, v196\n\t" \
    "v_max3_u32 v196, v214, v215, v196\n\t" \
    "v_max3_u32 v196, v216, v217, v196\n\t" \
    "v_max3_u32 v196, v218, v219, v196\n\t" \
    "v_cmp_eq_u32 vcc, -1, v196\n\t" \
    "s_cbranch_vccz .Lr26_%=\n\t" \
    "s_cmp_eq_u32 s41, 0\n\t" \
    "s_cbranch_scc1 .Lr26_%=\n\t" \
    "s_sub_u32 s41, s41, 1\n\t" \
    "v_max_u32 v196, v200, v201\n\t" \
    "v_max3_u32 v196, v202, v203, v196\n\t" \
    "v_cmp_eq_u32 vcc, -1, v196\n\t" \
    "s_cbranch_vccz .Lr27_%=\n\t" \
    "buffer_load_dwordx4 v[200:203], %[l16], %[rs], s49 offen offset:0 sc1\n\t" \
    ".Lr27_%=:\n\t" \
    "v_max_u32 v196, v204, v205\n\t" \
    "v_max3_u32 v196, v206, v207, v196\n\t" \
    "v_cmp_eq_u32 vcc, -1, v196\n\t" \
    "s_cbranch_vccz .Lr28_%=\n\t" \
    "buffer_load_dwordx4 v[204:207], %[l16], %[rs], s49 offen offset:1024 sc1\n\t" \
    ".Lr28_%=:\n\t" \
    "v_max_u32 v196, v208, v209\n\t" \
    "v_max3_u32 v196, v210, v211, v196\n\t" \
    "v_cmp_eq_u32 vcc, -1, v196\n\t" \
    "s_cbranch_vccz .Lr29_%=\n\t" \
    "buffer_load_dwordx4 v[208:211], %[l16], %[rs], s49 offen offset:2048 sc1\n\t" \
    ".Lr29_%=:\n\t" \
    "v_max_u32 v196, v212, v213\n\t" \
    "v_max3_u32 v196, v214, v215, v196\n\t" \
    "v_cmp_eq_u32 vcc, -1, v196\n\t" \
    "s_cbranch_vccz .Lr30_%=\n\t" \
    "buffer_load_dwordx4 v[212:215], %[l16], %[rs], s49 offen offset:3072 sc1\n\t" \
    ".Lr30_%=:\n\t" \
    "v_max_u32 v196, v216, v217\n\t" \
    "v_max3_u32 v196, v218, v219, v196\n\t" \
    "v_cmp_eq_u32 vcc, -1, v196\n\t" \
    "s_cbranch_vccz .Lr31_%=\n\t" \
    "buffer_load_dwordx4 v[216:219], %[l16], %[rs], s50 offen offset:0 sc1\n\t" \
    ".Lr31_%=:\n\t" \
    "s_waitcnt vmcnt(0)\n\t" \
    "s_branch .Lr25_%=\n\t" \
    ".Lr26_%=:\n\t" \
    "v_mfma_f32_16x16x32_bf16 v[244:247], v[160:163], v[200:203], 0\n\t" \
    "v_mfma_f32_16x16x32_bf16 v[244:247], v[164:167], v[204:207], v[244:247]\n\t" \
    "v_mfma_f32_16x16x32_bf16 v[244:247], v[168:171], v[208:211], v[244:247]\n\t" \
    "v_mfma_f32_16x16x32_bf16 v[244:247], v[172:175], v[212:215], v[244:247]\n\t" \
    "v_mfma_f32_16x16x32_bf16 v[244:247], v[180:183], v[216:219], v[244:247]\n\t" \
    "s_nop 7\n\t" \
    "ds_write_b128 %[pw], v[244:247] offset:8192\n\t" \
    "s_waitcnt lgkmcnt(0)\n\t" \
    "s_barrier \n\t" \
    LST4 \
    LJIT \
    "s_cmp_lg_u32 %[wave], 7\n\t" \
    "s_cbranch_scc1 .Lr32_%=\n\t" \
    "ds_read_b128 v[128:131], %[pr] offset:0\n\t" \
    "ds_read_b128 v[132:135], %[pr] offset:1024\n\t" \
    "ds_read_b128 v[136:139], %[pr] offset:2048\n\t" \
    "ds_read_b128 v[140:143], %[pr] offset:3072\n\t" \
    "ds_read_b128 v[144:147], %[pr] offset:4096\n\t" \
    "ds_read_b128 v[148:151], %[pr] offset:5120\n\t" \
    "ds_read_b128 v[152:155], %[pr] offset:6144\n\t" \
    "ds_read_b128 v[156:159], %[pr] offset:7168\n\t" \
    "v_lshlrev_b32 v176, 16, v250\n\t" \
    "v_and_b32 v177, 0xffff0000, v250\n\t" \
    "v_lshlrev_b32 v178, 16, v251\n\t" \
    "v_and_b32 v179, 0xffff0000, v251\n\t" \
    "s_waitcnt lgkmcnt(6)\n\t" \
    "v_pk_add_f32 v[128:129], v[128:129], v[132:133]\n\t" \
    "v_pk_add_f32 v[130:131], v[130:131], v[134:135]\n\t" \
    "s_waitcnt lgkmcnt(4)\n\t" \
    "v_pk_add_f32 v[136:137], v[136:137], v[140:141]\n\t" \
    "v_pk_add_f32 v[138:139], v[138:139], v[142:143]\n\t" \
    "v_pk_add_f32 v[128:129], v[128:129], v[136:137]\n\t" \
    "v_pk_add_f32 v[130:131], v[130:131], v[138:139]\n\t" \
    "s_waitcnt lgkmcnt(2)\n\t" \
    "v_pk_add_f32 v[144:145], v[144:145], v[148:149]\n\t" \
    "v_pk_add_f32 v[146:147], v[146:147], v[150:151]\n\t" \
    "s_waitcnt lgkmcnt(0)\n\t" \
    "v_pk_add_f32 v[152:153], v[152:153], v[156:157]\n\t" \
    "v_pk_add_f32 v[154:155], v[154:155], v[158:159]\n\t" \
    "v_pk_add_f32 v[144:145], v[144:145], v[152:153]\n\t" \
    "v_pk_add_f32 v[146:147], v[146:147], v[154:155]\n\t" \
    "v_pk_add_f32 v[128:129], v[128:129], v[144:145]\n\t" \
    "v_pk_add_f32 v[130:131], v[130:131], v[146:147]\n\t" \
    "v_pk_add_f32 v[128:129], v[128:129], v[176:177]\n\t" \
    "v_pk_add_f32 v[130:131], v[130:131], v[178:179]\n\t" \
    "v_cvt_pk_bf16_f32 v128, v128, v129\n\t" \
    "v_cvt_pk_bf16_f32 v129, v130, v131\n\t" \
    "v_min_u32 v128, -2, v128\n\t" \
    "v_min_u32 v129, -2, v129\n\t" \
    "s_add_u32 s44, s52, 0x1000000\n\t" \
    "buffer_store_dwordx2 v[128:129], %[xo], %[rs], s44 offen sc1\n\t" \
    ".Lr32_%=:\n\t" \
    "buffer_store_dwordx4 v[252:255], %[ov], %[rs], s46 offen sc1\n\t" \
    "buffer_store_dwordx4 v[252:255], v197, %[rs], s51 offen sc1\n\t" \
    LFLUSHR \
    "s_add_u32 s40, s40, 1\n\t" \
    "s_cmp_lg_u32 s40, 64\n\t" \
    "s_cbranch_scc1 .Lr1_%=\n\t" \
    "s_branch .Lend%=\n\t" \
    ".Lend%=:\n\t" \
    "s_waitcnt vmcnt(0) lgkmcnt(0)\n\t" \
    "s_mov_b32 %[bud], s41\n\t"
#define ASM_W4_MUL \
    "s_mov_b32 s40, 0\n\t" \
    "s_mov_b32 s41, 0x40000\n\t" \
    LSTINIT \
    "s_mov_b32 s43, 0\n\t" \
    "s_mov_b32 s46, %[hb]\n\t" \
    "s_add_u32 s47, s46, 0x4000\n\t" \
    "v_mov_b64 v[188:189], -1\n\t" \
    "v_mov_b32 v246, 0x1e000\n\t" \
    ".Lm1_%=:\n\t" \
    LST0 \
    LJIT \
    ".Lm2_%=:\n\t" \
    "ds_read_b32 v245, v246 offset:24\n\t" \
    "s_sub_i32 s44, s40, 3\n\t" \
    "s_waitcnt lgkmcnt(0)\n\t" \
    "v_cmp_gt_i32 vcc, s44, v245\n\t" \
    "s_cbranch_vccnz .Lm2_%=\n\t" \
    "s_add_u32 s44, s40, 1\n\t" \
    "s_min_u32 s44, s44, 127\n\t" \
    "s_lshr_b32 s45, s44, 1\n\t" \
    "s_and_b32 s44, s44, 1\n\t" \
    "s_lshl_b32 s44, s44, 12\n\t" \
    "s_mul_i32 s48, s45, 0x30000\n\t" \
    "s_add_u32 s48, s48, s44\n\t" \
    "s_lshr_b32 s45, s45, 3\n\t" \
    "s_mul_i32 s45, s45, 0x82000\n\t" \
    "s_add_u32 s48, s48, s45\n\t" \
    "s_add_u32 s48, s48, %[hb]\n\t" \
    "s_add_u32 s49, s48, 0x4000\n\t" \
    "s_cmp_lg_u32 s43, 0\n\t" \
    "s_cbranch_scc1 .Lm5_%=\n\t" \
    ".Lm3_%=:\n\t" \
    "v_max_u32 v244, v188, v189\n\t" \
    "v_max3_u32 v244, v190, v191, v244\n\t" \
    "v_cmp_eq_u32 vcc, -1, v244\n\t" \
    "s_cbranch_vccz .Lm4_%=\n\t" \
    "s_cmp_eq_u32 s41, 0\n\t" \
    "s_cbranch_scc1 .Lm4_%=\n\t" \
    "s_sub_u32 s41, s41, 1\n\t" \
    "s_sleep 3\n\t" \
    "buffer_load_dwordx4 v[188:191], %[l16], %[rs], s47 offen offset:3072 sc1\n\t" \
    "s_waitcnt vmcnt(0)\n\t" \
    "s_branch .Lm3_%=\n\t" \
    ".Lm4_%=:\n\t" \
    "buffer_load_dwordx4 v[184:187], %[l16], %[rs], s47 offen offset:2048 sc1\n\t" \
    "buffer_load_dwordx4 v[180:183], %[l16], %[rs], s47 offen offset:1024 sc1\n\t" \
    "buffer_load_dwordx4 v[176:179], %[l16], %[rs], s47 offen offset:0 sc1\n\t" \
    "buffer_load_dwordx4 v[172:175], %[l16], %[rs], s46 offen offset:3072 sc1\n\t" \
    "buffer_load_dwordx4 v[168:171], %[l16], %[rs], s46 offen offset:2048 sc1\n\t" \
    "buffer_load_dwordx4 v[164:167], %[l16], %[rs], s46 offen offset:1024 sc1\n\t" \
    "buffer_load_dwordx4 v[160:163], %[l16], %[rs], s46 offen offset:0 sc1\n\t" \
    ".Lm5_%=:\n\t" \
    LST1 \
    LJIT \
    "v_mfma_f32_16x16x32_bf16 v[224:227], v[28:31], v[188:191], 0\n\t" \
    "v_mfma_f32_16x16x32_bf16 v[228:231], v[60:63], v[188:191], 0\n\t" \
    "v_mfma_f32_16x16x32_bf16 v[232:235], v[92:95], v[188:191], 0\n\t" \
    "v_mfma_f32_16x16x32_bf16 v[236:239], v[124:127], v[188:191], 0\n\t" \
    "v_mfma_f32_16x16x32_bf16 v[240:243], v[156:159], v[188:191], 0\n\t" \
    "s_waitcnt vmcnt(6)\n\t" \
    ".Lm6_%=:\n\t" \
    "v_max_u32 v244, v184, v185\n\t" \
    "v_max3_u32 v244, v186, v187, v244\n\t" \
    "v_cmp_eq_u32 vcc, -1, v244\n\t" \
    "s_cbranch_vccnz .Lm7_%=\n\t" \
    ".Lm8_%=:\n\t" \
    "v_mfma_f32_16x16x32_bf16 v[224:227], v[24:27], v[184:187], v[224:227]\n\t" \
    "v_mfma_f32_16x16x32_bf16 v[228:231], v[56:59], v[184:187], v[228:231]\n\t" \
    "v_mfma_f32_16x16x32_bf16 v[232:235], v[88:91], v[184:187], v[232:235]\n\t" \
    "v_mfma_f32_16x16x32_bf16 v[236:239], v[120:123], v[184:187], v[236:239]\n\t" \
    "v_mfma_f32_16x16x32_bf16 v[240:243], v[152:155], v[184:187], v[240:243]\n\t" \
    "s_waitcnt vmcnt(5)\n\t" \
    ".Lm9_%=:\n\t" \
    "v_max_u32 v244, v180, v181\n\t" \
    "v_max3_u32 v244, v182, v183, v244\n\t" \
    "v_cmp_eq_u32 vcc, -1, v244\n\t" \
    "s_cbranch_vccnz .Lm10_%=\n\t" \
    ".Lm11_%=:\n\t" \
    "v_mfma_f32_16x16x32_bf16 v[224:227], v[20:23], v[180:183], v[224:227]\n\t" \
    "v_mfma_f32_16x16x32_bf16 v[228:231], v[52:55], v[180:183], v[228:231]\n\t" \
    "v_mfma_f32_16x16x32_bf16 v[232:235], v[84:87], v[180:183], v[232:235]\n\t" \
    "v_mfma_f32_16x16x32_bf16 v[236:239], v[116:119], v[180:183], v[236:239]\n\t" \
    "v_mfma_f32_16x16x32_bf16 v[240:243], v[148:151], v[180:183], v[240:243]\n\t" \
    "s_waitcnt vmcnt(4)\n\t" \
    ".Lm12_%=:\n\t" \
    "v_max_u32 v244, v176, v177\n\t" \
    "v_max3_u32 v244, v178, v179, v244\n\t" \
    "v_cmp_eq_u32 vcc, -1, v244\n\t" \
    "s_cbranch_vccnz .Lm13_%=\n\t" \
    ".Lm14_%=:\n\t" \
    "v_mfma_f32_16x16x32_bf16 v[224:227], v[16:19], v[176:179], v[224:227]\n\t" \
    "v_mfma_f32_16x16x32_bf16 v[228:231], v[48:51], v[176:179], v[228:231]\n\t" \
    "v_mfma_f32_16x16x32_bf16 v[232:235], v[80:83], v[176:179], v[232:235]\n\t" \
    "v_mfma_f32_16x16x32_bf16 v[236:239], v[112:115], v[176:179], v[236:239]\n\t" \
    "v_mfma_f32_16x16x32_bf16 v[240:243], v[144:147], v[176:179], v[240:243]\n\t" \
    "s_waitcnt vmcnt(3)\n\t" \
    ".Lm15_%=:\n\t" \
    "v_max_u32 v244, v172, v173\n\t" \
    "v_max3_u32 v244, v174, v175, v244\n\t" \
    "v_cmp_eq_u32 vcc, -1, v244\n\t" \
    "s_cbranch_vccnz .Lm16_%=\n\t" \
    ".Lm17_%=:\n\t" \
    "v_mfma_f32_16x16x32_bf16 v[224:227], v[12:15], v[172:175], v[224:227]\n\t" \
    "v_mfma_f32_16x16x32_bf16 v[228:231], v[44:47], v[172:175], v[228:231]\n\t" \
    "v_mfma_f32_16x16x32_bf16 v[232:235], v[76:79], v[172:175], v[232:235]\n\t" \
    "v_mfma_f32_16x16x32_bf16 v[236:239], v[108:111], v[172:175], v[236:239]\n\t" \
    "v_mfma_f32_16x16x32_bf16 v[240:243], v[140:143], v[172:175], v[240:243]\n\t" \
    "s_waitcnt vmcnt(2)\n\t" \
    ".Lm18_%=:\n\t" \
    "v_max_u32 v244, v168, v169\n\t" \
    "v_max3_u32 v244, v170, v171, v244\n\t" \
    "v_cmp_eq_u32 vcc, -1, v244\n\t" \
    "s_cbranch_vccnz .Lm19_%=\n\t" \
    ".Lm20_%=:\n\t" \
    "buffer_load_dwordx4 v[220:223], %[l16], %[rs], s49 offen offset:3072 sc1\n\t" \
    "v_mfma_f32_16x16x32_bf16 v[224:227], v[8:11], v[168:171], v[224:227]\n\t" \
    "v_mfma_f32_16x16x32_bf16 v[228:231], v[40:43], v[168:171], v[228:231]\n\t" \
    "v_mfma_f32_16x16x32_bf16 v[232:235], v[72:75], v[168:171], v[232:235]\n\t" \
    "v_mfma_f32_16x16x32_bf16 v[236:239], v[104:107], v[168:171], v[236:239]\n\t" \
    "v_mfma_f32_16x16x32_bf16 v[240:243], v[136:139], v[168:171], v[240:243]\n\t" \
    "s_waitcnt vmcnt(2)\n\t" \
    ".Lm21_%=:\n\t" \
    "v_max_u32 v244, v164, v165\n\t" \
    "v_max3_u32 v244, v166, v167, v244\n\t" \
    "v_cmp_eq_u32 vcc, -1, v244\n\t" \
    "s_cbranch_vccnz .Lm22_%=\n\t" \
    ".Lm23_%=:\n\t" \
    "v_mfma_f32_16x16x32_bf16 v[224:227], v[4:7], v[164:167], v[224:227]\n\t" \
    "v_mfma_f32_16x16x32_bf16 v[228:231], v[36:39], v[164:167], v[228:231]\n\t" \
    "v_mfma_f32_16x16x32_bf16 v[232:235], v[68:71], v[164:167], v[232:235]\n\t" \
    "v_mfma_f32_16x16x32_bf16 v[236:239], v[100:103], v[164:167], v[236:239]\n\t" \
    "v_mfma_f32_16x16x32_bf16 v[240:243], v[132:135], v[164:167], v[240:243]\n\t" \
    "s_waitcnt vmcnt(1)\n\t" \
    ".Lm24_%=:\n\t" \
    "v_max_u32 v244, v160, v161\n\t" \
    "v_max3_u32 v244, v162, v163, v244\n\t" \
    "v_cmp_eq_u32 vcc, -1, v244\n\t" \
    "s_cbranch_vccnz .Lm25_%=\n\t" \
    ".Lm26_%=:\n\t" \
    "v_mfma_f32_16x16x32_bf16 v[224:227], v[0:3], v[160:163], v[224:227]\n\t" \
    "v_mfma_f32_16x16x32_bf16 v[228:231], v[32:35], v[160:163], v[228:231]\n\t" \
    "v_mfma_f32_16x16x32_bf16 v[232:235], v[64:67], v[160:163], v[232:235]\n\t" \
    "v_mfma_f32_16x16x32_bf16 v[236:239], v[96:99], v[160:163], v[236:239]\n\t" \
    "v_mfma_f32_16x16x32_bf16 v[240:243], v[128:131], v[160:163], v[240:243]\n\t" \
    "s_add_u32 s40, s40, 1\n\t" \
    "v_mov_b32 v245, s40\n\t" \
    "ds_write_b128 %[ld], v[224:227] offset:0\n\t" \
    "ds_write_b128 %[ld], v[228:231] offset:6144\n\t" \
    "ds_write_b128 %[ld], v[232:235] offset:12288\n\t" \
    "s_nop 0\n\t" \
    "ds_write_b128 %[ld], v[236:239] offset:18432\n\t" \
    "s_nop 1\n\t" \
    "ds_write_b128 %[ld], v[240:243] offset:24576\n\t" \
    "ds_write_b32 %[fa], v245\n\t" \
    LST2 \
    LJIT \
    "s_mov_b32 s43, 0\n\t" \
    "s_waitcnt vmcnt(0)\n\t" \
    "v_max_u32 v244, v220, v221\n\t" \
    "v_max3_u32 v244, v222, v223, v244\n\t" \
    "v_cmp_eq_u32 vcc, -1, v244\n\t" \
    "s_cbranch_vccnz .Lm27_%=\n\t" \
    "buffer_load_dwordx4 v[216:219], %[l16], %[rs], s49 offen offset:2048 sc1\n\t" \
    "buffer_load_dwordx4 v[212:215], %[l16], %[rs], s49 offen offset:1024 sc1\n\t" \
    "buffer_load_dwordx4 v[208:211], %[l16], %[rs], s49 offen offset:0 sc1\n\t" \
    "buffer_load_dwordx4 v[204:207], %[l16], %[rs], s48 offen offset:3072 sc1\n\t" \
    "buffer_load_dwordx4 v[200:203], %[l16], %[rs], s48 offen offset:2048 sc1\n\t" \
    "buffer_load_dwordx4 v[196:199], %[l16], %[rs], s48 offen offset:1024 sc1\n\t" \
    "buffer_load_dwordx4 v[192:195], %[l16], %[rs], s48 offen offset:0 sc1\n\t" \
    "s_mov_b32 s43, 1\n\t" \
    ".Lm27_%=:\n\t" \
    LST0 \
    LJIT \
    ".Lm28_%=:\n\t" \
    "ds_read_b32 v245, v246 offset:28\n\t" \
    "s_sub_i32 s44, s40, 3\n\t" \
    "s_waitcnt lgkmcnt(0)\n\t" \
    "v_cmp_gt_i32 vcc, s44, v245\n\t" \
    "s_cbranch_vccnz .Lm28_%=\n\t" \
    "s_add_u32 s44, s40, 1\n\t" \
    "s_min_u32 s44, s44, 127\n\t" \
    "s_lshr_b32 s45, s44, 1\n\t" \
    "s_and_b32 s44, s44, 1\n\t" \
    "s_lshl_b32 s44, s44, 12\n\t" \
    "s_mul_i32 s46, s45, 0x30000\n\t" \
    "s_add_u32 s46, s46, s44\n\t" \
    "s_lshr_b32 s45, s45, 3\n\t" \
    "s_mul_i32 s45, s45, 0x82000\n\t" \
    "s_add_u32 s46, s46, s45\n\t" \
    "s_add_u32 s46, s46, %[hb]\n\t" \
    "s_add_u32 s47, s46, 0x4000\n\t" \
    "s_cmp_lg_u32 s43, 0\n\t" \
    "s_cbranch_scc1 .Lm31_%=\n\t" \
    ".Lm29_%=:\n\t" \
    "v_max_u32 v244, v220, v221\n\t" \
    "v_max3_u32 v244, v222, v223, v244\n\t" \
    "v_cmp_eq_u32 vcc, -1, v244\n\t" \
    "s_cbranch_vccz .Lm30_%=\n\t" \
    "s_cmp_eq_u32 s41, 0\n\t" \
    "s_cbranch_scc1 .Lm30_%=\n\t" \
    "s_sub_u32 s41, s41, 1\n\t" \
    "s_sleep 3\n\t" \
    "buffer_load_dwordx4 v[220:223], %[l16], %[rs], s49 offen offset:3072 sc1\n\t" \
    "s_waitcnt vmcnt(0)\n\t" \
    "s_branch .Lm29_%=\n\t" \
    ".Lm30_%=:\n\t" \
    "buffer_load_dwordx4 v[216:219], %[l16], %[rs], s49 offen offset:2048 sc1\n\t" \
    "buffer_load_dwordx4 v[212:215], %[l16], %[rs], s49 offen offset:1024 sc1\n\t" \
    "buffer_load_dwordx4 v[208:211], %[l16], %[rs], s49 offen offset:0 sc1\n\t" \
    "buffer_load_dwordx4 v[204:207], %[l16], %[rs], s48 offen offset:3072 sc1\n\t" \
    "buffer_load_dwordx4 v[200:203], %[l16], %[rs], s48 offen offset:2048 sc1\n\t" \
    "buffer_load_dwordx4 v[196:199], %[l16], %[rs], s48 offen offset:1024 sc1\n\t" \
    "buffer_load_dwordx4 v[192:195], %[l16], %[rs], s48 offen offset:0 sc1\n\t" \
    ".Lm31_%=:\n\t" \
    LST1 \
    LJIT \
    "v_mfma_f32_16x16x32_bf16 v[224:227], v[28:31], v[220:223], 0\n\t" \
    "v_mfma_f32_16x16x32_bf16 v[228:231], v[60:63], v[220:223], 0\n\t" \
    "v_mfma_f32_16x16x32_bf16 v[232:235], v[92:95], v[220:223], 0\n\t" \
    "v_mfma_f32_16x16x32_bf16 v[236:239], v[124:127], v[220:223], 0\n\t" \
    "v_mfma_f32_16x16x32_bf16 v[240:243], v[156:159], v[220:223], 0\n\t" \
    "s_waitcnt vmcnt(6)\n\t" \
    ".Lm32_%=:\n\t" \
    "v_max_u32 v244, v216, v217\n\t" \
    "v_max3_u32 v244, v218, v219, v244\n\t" \
    "v_cmp_eq_u32 vcc, -1, v244\n\t" \
    "s_cbranch_vccnz .Lm33_%=\n\t" \
    ".Lm34_%=:\n\t" \
    "v_mfma_f32_16x16x32_bf16 v[224:227], v[24:27], v[216:219], v[224:227]\n\t" \
    "v_mfma_f32_16x16x32_bf16 v[228:231], v[56:59], v[216:219], v[228:231]\n\t" \
    "v_mfma_f32_16x16x32_bf16 v[232:235], v[88:91], v[216:219], v[232:235]\n\t" \
    "v_mfma_f32_16x16x32_bf16 v[236:239], v[120:123], v[216:219], v[236:239]\n\t" \
    "v_mfma_f32_16x16x32_bf16 v[240:243], v[152:155], v[216:219], v[240:243]\n\t" \
    "s_waitcnt vmcnt(5)\n\t" \
    ".Lm35_%=:\n\t" \
    "v_max_u32 v244, v212, v213\n\t" \
    "v_max3_u32 v244, v214, v215, v244\n\t" \
    "v_cmp_eq_u32 vcc, -1, v244\n\t" \
    "s_cbranch_vccnz .Lm36_%=\n\t" \
    ".Lm37_%=:\n\t" \
    "v_mfma_f32_16x16x32_bf16 v[224:227], v[20:23], v[212:215], v[224:227]\n\t" \
    "v_mfma_f32_16x16x32_bf16 v[228:231], v[52:55], v[212:215], v[228:231]\n\t" \
    "v_mfma_f32_16x16x32_bf16 v[232:235], v[84:87], v[212:215], v[232:235]\n\t" \
    "v_mfma_f32_16x16x32_bf16 v[236:239], v[116:119], v[212:215], v[236:239]\n\t" \
    "v_mfma_f32_16x16x32_bf16 v[240:243], v[148:151], v[212:215], v[240:243]\n\t" \
    "s_waitcnt vmcnt(4)\n\t" \
    ".Lm38_%=:\n\t" \
    "v_max_u32 v244, v208, v209\n\t" \
    "v_max3_u32 v244, v210, v211, v244\n\t" \
    "v_cmp_eq_u32 vcc, -1, v244\n\t" \
    "s_cbranch_vccnz .Lm39_%=\n\t" \
    ".Lm40_%=:\n\t" \
    "v_mfma_f32_16x16x32_bf16 v[224:227], v[16:19], v[208:211], v[224:227]\n\t" \
    "v_mfma_f32_16x16x32_bf16 v[228:231], v[48:51], v[208:211], v[228:231]\n\t" \
    "v_mfma_f32_16x16x32_bf16 v[232:235], v[80:83], v[208:211], v[232:235]\n\t" \
    "v_mfma_f32_16x16x32_bf16 v[236:239], v[112:115], v[208:211], v[236:239]\n\t" \
    "v_mfma_f32_16x16x32_bf16 v[240:243], v[144:147], v[208:211], v[240:243]\n\t" \
    "s_waitcnt vmcnt(3)\n\t" \
    ".Lm41_%=:\n\t" \
    "v_max_u32 v244, v204, v205\n\t" \
    "v_max3_u32 v244, v206, v207, v244\n\t" \
    "v_cmp_eq_u32 vcc, -1, v244\n\t" \
    "s_cbranch_vccnz .Lm42_%=\n\t" \
    ".Lm43_%=:\n\t" \
    "v_mfma_f32_16x16x32_bf16 v[224:227], v[12:15], v[204:207], v[224:227]\n\t" \
    "v_mfma_f32_16x16x32_bf16 v[228:231], v[44:47], v[204:207], v[228:231]\n\t" \
    "v_mfma_f32_16x16x32_bf16 v[232:235], v[76:79], v[204:207], v[232:235]\n\t" \
    "v_mfma_f32_16x16x32_bf16 v[236:239], v[108:111], v[204:207], v[236:239]\n\t" \
    "v_mfma_f32_16x16x32_bf16 v[240:243], v[140:143], v[204:207], v[240:243]\n\t" \
    "s_waitcnt vmcnt(2)\n\t" \
    ".Lm44_%=:\n\t" \
    "v_max_u32 v244, v200, v201\n\t" \
    "v_max3_u32 v244, v202, v203, v244\n\t" \
    "v_cmp_eq_u32 vcc, -1, v244\n\t" \
    "s_cbranch_vccnz .Lm45_%=\n\t" \
    ".Lm46_%=:\n\t" \
    "buffer_load_dwordx4 v[188:191], %[l16], %[rs], s47 offen offset:3072 sc1\n\t" \
    "v_mfma_f32_16x16x32_bf16 v[224:227], v[8:11], v[200:203], v[224:227]\n\t" \
    "v_mfma_f32_16x16x32_bf16 v[228:231], v[40:43], v[200:203], v[228:231]\n\t" \
    "v_mfma_f32_16x16x32_bf16 v[232:235], v[72:75], v[200:203], v[232:235]\n\t" \
    "v_mfma_f32_16x16x32_bf16 v[236:239], v[104:107], v[200:203], v[236:239]\n\t" \
    "v_mfma_f32_16x16x32_bf16 v[240:243], v[136:139], v[200:203], v[240:243]\n\t" \
    "s_waitcnt vmcnt(2)\n\t" \
    ".Lm47_%=:\n\t" \
    "v_max_u32 v244, v196, v197\n\t" \
    "v_max3_u32 v244, v198, v199, v244\n\t" \
    "v_cmp_eq_u32 vcc, -1, v244\n\t" \
    "s_cbranch_vccnz .Lm48_%=\n\t" \
    ".Lm49_%=:\n\t" \
    "v_mfma_f32_16x16x32_bf16 v[224:227], v[4:7], v[196:199], v[224:227]\n\t" \
    "v_mfma_f32_16x16x32_bf16 v[228:231], v[36:39], v[196:199], v[228:231]\n\t" \
    "v_mfma_f32_16x16x32_bf16 v[232:235], v[68:71], v[196:199], v[232:235]\n\t" \
    "v_mfma_f32_16x16x32_bf16 v[236:239], v[100:103], v[196:199], v[236:239]\n\t" \
    "v_mfma_f32_16x16x32_bf16 v[240:243], v[132:135], v[196:199], v[240:243]\n\t" \
    "s_waitcnt vmcnt(1)\n\t" \
    ".Lm50_%=:\n\t" \
    "v_max_u32 v244, v192, v193\n\t" \
    "v_max3_u32 v244, v194, v195, v244\n\t" \
    "v_cmp_eq_u32 vcc, -1, v244\n\t" \
    "s_cbranch_vccnz .Lm51_%=\n\t" \
    ".Lm52_%=:\n\t" \
    "v_mfma_f32_16x16x32_bf16 v[224:227], v[0:3], v[192:195], v[224:227]\n\t" \
    "v_mfma_f32_16x16x32_bf16 v[228:231], v[32:35], v[192:195], v[228:231]\n\t" \
    "v_mfma_f32_16x16x32_bf16 v[232:235], v[64:67], v[192:195], v[232:235]\n\t" \
    "v_mfma_f32_16x16x32_bf16 v[236:239], v[96:99], v[192:195], v[236:239]\n\t" \
    "v_mfma_f32_16x16x32_bf16 v[240:243], v[128:131], v[192:195], v[240:243]\n\t" \
    "s_add_u32 s40, s40, 1\n\t" \
    "v_mov_b32 v245, s40\n\t" \
    "ds_write_b128 %[ld], v[224:227] offset:30720\n\t" \
    "ds_write_b128 %[ld], v[228:231] offset:36864\n\t" \
    "ds_write_b128 %[ld], v[232:235] offset:43008\n\t" \
    "s_nop 0\n\t" \
    "ds_write_b128 %[ld], v[236:239] offset:49152\n\t" \
    "s_nop 1\n\t" \
    "ds_write_b128 %[ld], v[240:243] offset:55296\n\t" \
    "ds_write_b32 %[fa], v245\n\t" \
    LST2 \
    LJIT \
    "s_mov_b32 s43, 0\n\t" \
    "s_cmp_eq_u32 s40, 128\n\t" \
    "s_cbranch_scc1 .Lm53_%=\n\t" \
    "s_waitcnt vmcnt(0)\n\t" \
    "v_max_u32 v244, v188, v189\n\t" \
    "v_max3_u32 v244, v190, v191, v244\n\t" \
    "v_cmp_eq_u32 vcc, -1, v244\n\t" \
    "s_cbranch_vccnz .Lm53_%=\n\t" \
    "buffer_load_dwordx4 v[184:187], %[l16], %[rs], s47 offen offset:2048 sc1\n\t" \
    "buffer_load_dwordx4 v[180:183], %[l16], %[rs], s47 offen offset:1024 sc1\n\t" \
    "buffer_load_dwordx4 v[176:179], %[l16], %[rs], s47 offen offset:0 sc1\n\t" \
    "buffer_load_dwordx4 v[172:175], %[l16], %[rs], s46 offen offset:3072 sc1\n\t" \
    "buffer_load_dwordx4 v[168:171], %[l16], %[rs], s46 offen offset:2048 sc1\n\t" \
    "buffer_load_dwordx4 v[164:167], %[l16], %[rs], s46 offen offset:1024 sc1\n\t" \
    "buffer_load_dwordx4 v[160:163], %[l16], %[rs], s46 offen offset:0 sc1\n\t" \
    "s_mov_b32 s43, 1\n\t" \
    ".Lm53_%=:\n\t" \
    "v_sub_u32 %[ld], %[l2], %[ld]\n\t" \
    LFLUSHM \
    "s_nop 0\n\t" \
    "s_cmp_lg_u32 s40, 128\n\t" \
    "s_cbranch_scc1 .Lm1_%=\n\t" \
    "s_branch .Lend%=\n\t" \
    ".Lm7_%=:\n\t" \
    "s_cmp_eq_u32 s41, 0\n\t" \
    "s_cbranch_scc1 .Lm8_%=\n\t" \
    "s_sub_u32 s41, s41, 1\n\t" \
    "buffer_load_dwordx4 v[184:187], %[l16], %[rs], s47 offen offset:2048 sc1\n\t" \
    "s_waitcnt vmcnt(0)\n\t" \
    "s_branch .Lm6_%=\n\t" \
    ".Lm10_%=:\n\t" \
    "s_cmp_eq_u32 s41, 0\n\t" \
    "s_cbranch_scc1 .Lm11_%=\n\t" \
    "s_sub_u32 s41, s41, 1\n\t" \
    "buffer_load_dwordx4 v[180:183], %[l16], %[rs], s47 offen offset:1024 sc1\n\t" \
    "s_waitcnt vmcnt(0)\n\t" \
    "s_branch .Lm9_%=\n\t" \
    ".Lm13_%=:\n\t" \
    "s_cmp_eq_u32 s41, 0\n\t" \
    "s_cbranch_scc1 .Lm14_%=\n\t" \
    "s_sub_u32 s41, s41, 1\n\t" \
    "buffer_load_dwordx4 v[176:179], %[l16], %[rs], s47 offen offset:0 sc1\n\t" \
    "s_waitcnt vmcnt(0)\n\t" \
    "s_branch .Lm12_%=\n\t" \
    ".Lm16_%=:\n\t" \
    "s_cmp_eq_u32 s41, 0\n\t" \
    "s_cbranch_scc1 .Lm17_%=\n\t" \
    "s_sub_u32 s41, s41, 1\n\t" \
    "buffer_load_dwordx4 v[172:175], %[l16], %[rs], s46 offen offset:3072 sc1\n\t" \
    "s_waitcnt vmcnt(0)\n\t" \
    "s_branch .Lm15_%=\n\t" \
    ".Lm19_%=:\n\t" \
    "s_cmp_eq_u32 s41, 0\n\t" \
    "s_cbranch_scc1 .Lm20_%=\n\t" \
    "s_sub_u32 s41, s41, 1\n\t" \
    "buffer_load_dwordx4 v[168:171], %[l16], %[rs], s46 offen offset:2048 sc1\n\t" \
    "s_waitcnt vmcnt(0)\n\t" \
    "s_branch .Lm18_%=\n\t" \
    ".Lm22_%=:\n\t" \
    "s_cmp_eq_u32 s41, 0\n\t" \
    "s_cbranch_scc1 .Lm23_%=\n\t" \
    "s_sub_u32 s41, s41, 1\n\t" \
    "buffer_load_dwordx4 v[164:167], %[l16], %[rs], s46 offen offset:1024 sc1\n\t" \
    "s_waitcnt vmcnt(0)\n\t" \
    "s_branch .Lm21_%=\n\t" \
    ".Lm25_%=:\n\t" \
    "s_cmp_eq_u32 s41, 0\n\t" \
    "s_cbranch_scc1 .Lm26_%=\n\t" \
    "s_sub_u32 s41, s41, 1\n\t" \
    "buffer_load_dwordx4 v[160:163], %[l16], %[rs], s46 offen offset:0 sc1\n\t" \
    "s_waitcnt vmcnt(0)\n\t" \
    "s_branch .Lm24_%=\n\t" \
    ".Lm33_%=:\n\t" \
    "s_cmp_eq_u32 s41, 0\n\t" \
    "s_cbranch_scc1 .Lm34_%=\n\t" \
    "s_sub_u32 s41, s41, 1\n\t" \
    "buffer_load_dwordx4 v[216:219], %[l16], %[rs], s49 offen offset:2048 sc1\n\t" \
    "s_waitcnt vmcnt(0)\n\t" \
    "s_branch .Lm32_%=\n\t" \
    ".Lm36_%=:\n\t" \
    "s_cmp_eq_u32 s41, 0\n\t" \
    "s_cbranch_scc1 .Lm37_%=\n\t" \
    "s_sub_u32 s41, s41, 1\n\t" \
    "buffer_load_dwordx4 v[212:215], %[l16], %[rs], s49 offen offset:1024 sc1\n\t" \
    "s_waitcnt vmcnt(0)\n\t" \
    "s_branch .Lm35_%=\n\t" \
    ".Lm39_%=:\n\t" \
    "s_cmp_eq_u32 s41, 0\n\t" \
    "s_cbranch_scc1 .Lm40_%=\n\t" \
    "s_sub_u32 s41, s41, 1\n\t" \
    "buffer_load_dwordx4 v[208:211], %[l16], %[rs], s49 offen offset:0 sc1\n\t" \
    "s_waitcnt vmcnt(0)\n\t" \
    "s_branch .Lm38_%=\n\t" \
    ".Lm42_%=:\n\t" \
    "s_cmp_eq_u32 s41, 0\n\t" \
    "s_cbranch_scc1 .Lm43_%=\n\t" \
    "s_sub_u32 s41, s41, 1\n\t" \
    "buffer_load_dwordx4 v[204:207], %[l16], %[rs], s48 offen offset:3072 sc1\n\t" \
    "s_waitcnt vmcnt(0)\n\t" \
    "s_branch .Lm41_%=\n\t" \
    ".Lm45_%=:\n\t" \
    "s_cmp_eq_u32 s41, 0\n\t" \
    "s_cbranch_scc1 .Lm46_%=\n\t" \
    "s_sub_u32 s41, s41, 1\n\t" \
    "buffer_load_dwordx4 v[200:203], %[l16], %[rs], s48 offen offset:2048 sc1\n\t" \
    "s_waitcnt vmcnt(0)\n\t" \
    "s_branch .Lm44_%=\n\t" \
    ".Lm48_%=:\n\t" \
    "s_cmp_eq_u32 s41, 0\n\t" \
    "s_cbranch_scc1 .Lm49_%=\n\t" \
    "s_sub_u32 s41, s41, 1\n\t" \
    "buffer_load_dwordx4 v[196:199], %[l16], %[rs], s48 offen offset:1024 sc1\n\t" \
    "s_waitcnt vmcnt(0)\n\t" \
    "s_branch .Lm47_%=\n\t" \
    ".Lm51_%=:\n\t" \
    "s_cmp_eq_u32 s41, 0\n\t" \
    "s_cbranch_scc1 .Lm52_%=\n\t" \
    "s_sub_u32 s41, s41, 1\n\t" \
    "buffer_load_dwordx4 v[192:195], %[l16], %[rs], s48 offen offset:0 sc1\n\t" \
    "s_waitcnt vmcnt(0)\n\t" \
    "s_branch .Lm50_%=\n\t" \
    ".Lend%=:\n\t" \
    "s_waitcnt vmcnt(0) lgkmcnt(0)\n\t" \
    "s_mov_b32 %[bud], s41\n\t"
#define ASM_W4_FIN0 \
    "s_mov_b32 s40, %[mt]\n\t" \
    "s_mov_b32 s41, 0x40000\n\t" \
    LSTINIT \
    "s_mov_b32 s43, 0\n\t" \
    "v_mov_b32 v99, 0x3f803f80\n\t" \
    "s_setprio 3\n\t" \
    "s_add_u32 s46, s43, 0x2c00000\n\t" \
    "s_add_u32 s47, s43, 0x1c00000\n\t" \
    "buffer_load_dwordx2 v[120:121], %[t0], %[rs], s46 offen sc1\n\t" \
    "buffer_load_dwordx2 v[122:123], %[t0], %[rs], s47 offen sc1\n\t" \
    "buffer_load_dwordx2 v[124:125], %[t1], %[rs], s46 offen sc1\n\t" \
    "buffer_load_dwordx2 v[126:127], %[t1], %[rs], s47 offen sc1\n\t" \
    "buffer_load_dwordx2 v[128:129], %[t2], %[rs], s46 offen sc1\n\t" \
    "buffer_load_dwordx2 v[130:131], %[t2], %[rs], s47 offen sc1\n\t" \
    "buffer_load_dwordx2 v[132:133], %[t3], %[rs], s46 offen sc1\n\t" \
    "buffer_load_dwordx2 v[134:135], %[t3], %[rs], s47 offen sc1\n\t" \
    "buffer_load_dwordx2 v[136:137], %[t4], %[rs], s46 offen sc1\n\t" \
    "buffer_load_dwordx2 v[138:139], %[t4], %[rs], s47 offen sc1\n\t" \
    "s_waitcnt vmcnt(0)\n\t" \
    ".Lf01_%=:\n\t" \
    LST0 \
    LJIT \
    ".Lf02_%=:\n\t" \
    "s_waitcnt vmcnt(1)\n\t" \
    "v_max_u32 v80, v120, v121\n\t" \
    "v_max3_u32 v80, v122, v123, v80\n\t" \
    "v_max3_u32 v80, v124, v125, v80\n\t" \
    "v_max3_u32 v80, v126, v127, v80\n\t" \
    "v_max3_u32 v80, v128, v129, v80\n\t" \
    "v_max3_u32 v80, v130, v131, v80\n\t" \
    "v_max3_u32 v80, v132, v133, v80\n\t" \
    "v_max3_u32 v80, v134, v135, v80\n\t" \
    "v_max3_u32 v80, v136, v137, v80\n\t" \
    "v_max3_u32 v80, v138, v139, v80\n\t" \
    "v_cmp_eq_u32 vcc, -1, v80\n\t" \
    "s_cbranch_vccz .Lf03_%=\n\t" \
    "s_cmp_eq_u32 s41, 0\n\t" \
    "s_cbranch_scc1 .Lf03_%=\n\t" \
    "s_sub_u32 s41, s41, 1\n\t" \
    "s_sleep 8\n\t" \
    "s_add_u32 s46, s43, 0x2c00000\n\t" \
    "s_add_u32 s47, s43, 0x1c00000\n\t" \
    "buffer_load_dwordx2 v[120:121], %[t0], %[rs], s46 offen sc1\n\t" \
    "buffer_load_dwordx2 v[122:123], %[t0], %[rs], s47 offen sc1\n\t" \
    "buffer_load_dwordx2 v[124:125], %[t1], %[rs], s46 offen sc1\n\t" \
    "buffer_load_dwordx2 v[126:127], %[t1], %[rs], s47 offen sc1\n\t" \
    "buffer_load_dwordx2 v[128:129], %[t2], %[rs], s46 offen sc1\n\t" \
    "buffer_load_dwordx2 v[130:131], %[t2], %[rs], s47 offen sc1\n\t" \
    "buffer_load_dwordx2 v[132:133], %[t3], %[rs], s46 offen sc1\n\t" \
    "buffer_load_dwordx2 v[134:135], %[t3], %[rs], s47 offen sc1\n\t" \
    "buffer_load_dwordx2 v[136:137], %[t4], %[rs], s46 offen sc1\n\t" \
    "buffer_load_dwordx2 v[138:139], %[t4], %[rs], s47 offen sc1\n\t" \
    "s_waitcnt vmcnt(0)\n\t" \
    "s_branch .Lf02_%=\n\t" \
    ".Lf03_%=:\n\t" \
    "v_lshlrev_b32 v140, 16, v120\n\t" \
    "v_and_b32 v141, 0xffff0000, v120\n\t" \
    "v_lshlrev_b32 v142, 16, v121\n\t" \
    "v_and_b32 v143, 0xffff0000, v121\n\t" \
    "v_lshlrev_b32 v80, 16, v122\n\t" \
    "v_and_b32 v81, 0xffff0000, v122\n\t" \
    "v_lshlrev_b32 v82, 16, v123\n\t" \
    "v_and_b32 v83, 0xffff0000, v123\n\t" \
    "v_add_f32 v140, v140, v80\n\t" \
    "v_add_f32 v141, v141, v81\n\t" \
    "v_add_f32 v142, v142, v82\n\t" \
    "v_add_f32 v143, v143, v83\n\t" \
    "v_lshlrev_b32 v144, 16, v124\n\t" \
    "v_and_b32 v145, 0xffff0000, v124\n\t" \
    "v_lshlrev_b32 v146, 16, v125\n\t" \
    "v_and_b32 v147, 0xffff0000, v125\n\t" \
    "v_lshlrev_b32 v80, 16, v126\n\t" \
    "v_and_b32 v81, 0xffff0000, v126\n\t" \
    "v_lshlrev_b32 v82, 16, v127\n\t" \
    "v_and_b32 v83, 0xffff0000, v127\n\t" \
    "v_add_f32 v144, v144, v80\n\t" \
    "v_add_f32 v145, v145, v81\n\t" \
    "v_add_f32 v146, v146, v82\n\t" \
    "v_add_f32 v147, v147, v83\n\t" \
    "v_lshlrev_b32 v148, 16, v128\n\t" \
    "v_and_b32 v149, 0xffff0000, v128\n\t" \
    "v_lshlrev_b32 v150, 16, v129\n\t" \
    "v_and_b32 v151, 0xffff0000, v129\n\t" \
    "v_lshlrev_b32 v80, 16, v130\n\t" \
    "v_and_b32 v81, 0xffff0000, v130\n\t" \
    "v_lshlrev_b32 v82, 16, v131\n\t" \
    "v_and_b32 v83, 0xffff0000, v131\n\t" \
    "v_add_f32 v148, v148, v80\n\t" \
    "v_add_f32 v149, v149, v81\n\t" \
    "v_add_f32 v150, v150, v82\n\t" \
    "v_add_f32 v151, v151, v83\n\t" \
    "v_lshlrev_b32 v152, 16, v132\n\t" \
    "v_and_b32 v153, 0xffff0000, v132\n\t" \
    "v_lshlrev_b32 v154, 16, v133\n\t" \
    "v_and_b32 v155, 0xffff0000, v133\n\t" \
    "v_lshlrev_b32 v80, 16, v134\n\t" \
    "v_and_b32 v81, 0xffff0000, v134\n\t" \
    "v_lshlrev_b32 v82, 16, v135\n\t" \
    "v_and_b32 v83, 0xffff0000, v135\n\t" \
    "v_add_f32 v152, v152, v80\n\t" \
    "v_add_f32 v153, v153, v81\n\t" \
    "v_add_f32 v154, v154, v82\n\t" \
    "v_add_f32 v155, v155, v83\n\t" \
    "v_lshlrev_b32 v156, 16, v136\n\t" \
    "v_and_b32 v157, 0xffff0000, v136\n\t" \
    "v_lshlrev_b32 v158, 16, v137\n\t" \
    "v_and_b32 v159, 0xffff0000, v137\n\t" \
    "v_lshlrev_b32 v80, 16, v138\n\t" \
    "v_and_b32 v81, 0xffff0000, v138\n\t" \
    "v_lshlrev_b32 v82, 16, v139\n\t" \
    "v_and_b32 v83, 0xffff0000, v139\n\t" \
    "v_add_f32 v156, v156, v80\n\t" \
    "v_add_f32 v157, v157, v81\n\t" \
    "v_add_f32 v158, v158, v82\n\t" \
    "v_add_f32 v159, v159, v83\n\t" \
    "s_mov_b32 s42, s43\n\t" \
    "s_lshr_b32 s48, s40, 1\n\t" \
    "s_add_u32 s48, s48, 1\n\t" \
    "s_mul_i32 s43, s48, 0x14000\n\t" \
    "s_lshr_b32 s44, s48, 4\n\t" \
    "s_mul_i32 s44, s44, 0xc2000\n\t" \
    "s_add_u32 s43, s43, s44\n\t" \
    "s_lshr_b32 s44, s40, 3\n\t" \
    "s_lshl_b32 s44, s44, 14\n\t" \
    "s_and_b32 s45, s40, 7\n\t" \
    "s_lshl_b32 s45, s45, 10\n\t" \
    "s_add_u32 s49, s44, s45\n\t" \
    "s_add_u32 s49, s49, 0x1c0000\n\t" \
    "s_add_u32 s50, s49, %[co]\n\t" \
    "s_add_u32 s51, s42, 0x1400000\n\t" \
    "s_add_u32 s52, s42, 0xc00000\n\t" \
    "s_mov_b32 s44, s48\n\t" \
    "s_mov_b32 s45, s48\n\t" \
    "v_mov_b64 v[76:77], s[44:45]\n\t" \
    "v_mov_b64 v[78:79], s[44:45]\n\t" \
    "s_add_u32 s44, s40, 1\n\t" \
    "v_mov_b32 v98, s44\n\t" \
    LST1 \
    LJIT \
    ".Lf04_%=:\n\t" \
    "s_nop 7\n\t" \
    "ds_read_b32 v80, %[fa]\n\t" \
    "s_waitcnt lgkmcnt(0)\n\t" \
    "v_cmp_ge_u32 vcc, s40, v80\n\t" \
    "s_cbranch_vccnz .Lf04_%=\n\t" \
    LST2 \
    LJIT \
    "ds_read_b128 v[160:163], %[la] offset:0\n\t" \
    "ds_read_b128 v[164:167], %[la] offset:6144\n\t" \
    "ds_read_b128 v[168:171], %[la] offset:12288\n\t" \
    "ds_read_b128 v[172:175], %[la] offset:18432\n\t" \
    "ds_read_b128 v[176:179], %[la] offset:24576\n\t" \
    "ds_read_b128 v[180:183], %[la] offset:1024\n\t" \
    "ds_read_b128 v[184:187], %[la] offset:7168\n\t" \
    "ds_read_b128 v[188:191], %[la] offset:13312\n\t" \
    "ds_read_b128 v[192:195], %[la] offset:19456\n\t" \
    "ds_read_b128 v[196:199], %[la] offset:25600\n\t" \
    "ds_read_b128 v[200:203], %[la] offset:2048\n\t" \
    "ds_read_b128 v[204:207], %[la] offset:8192\n\t" \
    "ds_read_b128 v[208:211], %[la] offset:14336\n\t" \
    "ds_read_b128 v[212:215], %[la] offset:20480\n\t" \
    "ds_read_b128 v[216:219], %[la] offset:26624\n\t" \
    "s_waitcnt lgkmcnt(14)\n\t" \
    "v_pk_add_f32 v[100:101], v[140:141], v[160:161]\n\t" \
    "v_pk_add_f32 v[102:103], v[142:143], v[162:163]\n\t" \
    "ds_read_b128 v[220:223], %[la] offset:3072\n\t" \
    "s_waitcnt lgkmcnt(14)\n\t" \
    "v_pk_add_f32 v[104:105], v[144:145], v[164:165]\n\t" \
    "v_pk_add_f32 v[106:107], v[146:147], v[166:167]\n\t" \
    "ds_read_b128 v[224:227], %[la] offset:9216\n\t" \
    "s_waitcnt lgkmcnt(14)\n\t" \
    "v_pk_add_f32 v[108:109], v[148:149], v[168:169]\n\t" \
    "v_pk_add_f32 v[110:111], v[150:151], v[170:171]\n\t" \
    "ds_read_b128 v[228:231], %[la] offset:15360\n\t" \
    "s_waitcnt lgkmcnt(14)\n\t" \
    "v_pk_add_f32 v[112:113], v[152:153], v[172:173]\n\t" \
    "v_pk_add_f32 v[114:115], v[154:155], v[174:175]\n\t" \
    "ds_read_b128 v[232:235], %[la] offset:21504\n\t" \
    "s_waitcnt lgkmcnt(14)\n\t" \
    "v_pk_add_f32 v[116:117], v[156:157], v[176:177]\n\t" \
    "v_pk_add_f32 v[118:119], v[158:159], v[178:179]\n\t" \
    "ds_read_b128 v[236:239], %[la] offset:27648\n\t" \
    "s_waitcnt lgkmcnt(14)\n\t" \
    "v_pk_add_f32 v[100:101], v[100:101], v[180:181]\n\t" \
    "v_pk_add_f32 v[102:103], v[102:103], v[182:183]\n\t" \
    "ds_read_b128 v[160:163], %[la] offset:4096\n\t" \
    "s_waitcnt lgkmcnt(14)\n\t" \
    "v_pk_add_f32 v[104:105], v[104:105], v[184:185]\n\t" \
    "v_pk_add_f32 v[106:107], v[106:107], v[186:187]\n\t" \
    "ds_read_b128 v[164:167], %[la] offset:10240\n\t" \
    "s_waitcnt lgkmcnt(14)\n\t" \
    "v_pk_add_f32 v[108:109], v[108:109], v[188:189]\n\t" \
    "v_pk_add_f32 v[110:111], v[110:111], v[190:191]\n\t" \
    "ds_read_b128 v[168:171], %[la] offset:16384\n\t" \
    "s_waitcnt lgkmcnt(14)\n\t" \
    "v_pk_add_f32 v[112:113], v[112:113], v[192:193]\n\t" \
    "v_pk_add_f32 v[114:115], v[114:115], v[194:195]\n\t" \
    "ds_read_b128 v[172:175], %[la] offset:22528\n\t" \
    "s_waitcnt lgkmcnt(14)\n\t" \
    "v_pk_add_f32 v[116:117], v[116:117], v[196:197]\n\t" \
    "v_pk_add_f32 v[118:119], v[118:119], v[198:199]\n\t" \
    "ds_read_b128 v[176:179], %[la] offset:28672\n\t" \
    "s_waitcnt lgkmcnt(14)\n\t" \
    "v_pk_add_f32 v[100:101], v[100:101], v[200:201]\n\t" \
    "v_pk_add_f32 v[102:103], v[102:103], v[202:203]\n\t" \
    "ds_read_b128 v[180:183], %[la] offset:5120\n\t" \
    "s_waitcnt lgkmcnt(14)\n\t" \
    "v_pk_add_f32 v[104:105], v[104:105], v[204:205]\n\t" \
    "v_pk_add_f32 v[106:107], v[106:107], v[206:207]\n\t" \
    "ds_read_b128 v[184:187], %[la] offset:11264\n\t" \
    "s_waitcnt lgkmcnt(14)\n\t" \
    "v_pk_add_f32 v[108:109], v[108:109], v[208:209]\n\t" \
    "v_pk_add_f32 v[110:111], v[110:111], v[210:211]\n\t" \
    "ds_read_b128 v[188:191], %[la] offset:17408\n\t" \
    "s_waitcnt lgkmcnt(14)\n\t" \
    "v_pk_add_f32 v[112:113], v[112:113], v[212:213]\n\t" \
    "v_pk_add_f32 v[114:115], v[114:115], v[214:215]\n\t" \
    "ds_read_b128 v[192:195], %[la] offset:23552\n\t" \
    "s_waitcnt lgkmcnt(14)\n\t" \
    "v_pk_add_f32 v[116:117], v[116:117], v[216:217]\n\t" \
    "v_pk_add_f32 v[118:119], v[118:119], v[218:219]\n\t" \
    "ds_read_b128 v[196:199], %[la] offset:29696\n\t" \
    "s_waitcnt lgkmcnt(14)\n\t" \
    "v_pk_add_f32 v[100:101], v[100:101], v[220:221]\n\t" \
    "v_pk_add_f32 v[102:103], v[102:103], v[222:223]\n\t" \
    "s_waitcnt lgkmcnt(13)\n\t" \
    "v_pk_add_f32 v[104:105], v[104:105], v[224:225]\n\t" \
    "v_pk_add_f32 v[106:107], v[106:107], v[226:227]\n\t" \
    "s_waitcnt lgkmcnt(12)\n\t" \
    "v_pk_add_f32 v[108:109], v[108:109], v[228:229]\n\t" \
    "v_pk_add_f32 v[110:111], v[110:111], v[230:231]\n\t" \
    "s_waitcnt lgkmcnt(11)\n\t" \
    "v_pk_add_f32 v[112:113], v[112:113], v[232:233]\n\t" \
    "v_pk_add_f32 v[114:115], v[114:115], v[234:235]\n\t" \
    "s_waitcnt lgkmcnt(10)\n\t" \
    "v_pk_add_f32 v[116:117], v[116:117], v[236:237]\n\t" \
    "v_pk_add_f32 v[118:119], v[118:119], v[238:239]\n\t" \
    "s_waitcnt lgkmcnt(9)\n\t" \
    "v_pk_add_f32 v[100:101], v[100:101], v[160:161]\n\t" \
    "v_pk_add_f32 v[102:103], v[102:103], v[162:163]\n\t" \
    "s_waitcnt lgkmcnt(8)\n\t" \
    "v_pk_add_f32 v[104:105], v[104:105], v[164:165]\n\t" \
    "v_pk_add_f32 v[106:107], v[106:107], v[166:167]\n\t" \
    "s_waitcnt lgkmcnt(7)\n\t" \
    "v_pk_add_f32 v[108:109], v[108:109], v[168:169]\n\t" \
    "v_pk_add_f32 v[110:111], v[110:111], v[170:171]\n\t" \
    "s_waitcnt lgkmcnt(6)\n\t" \
    "v_pk_add_f32 v[112:113], v[112:113], v[172:173]\n\t" \
    "v_pk_add_f32 v[114:115], v[114:115], v[174:175]\n\t" \
    "s_waitcnt lgkmcnt(5)\n\t" \
    "v_pk_add_f32 v[116:117], v[116:117], v[176:177]\n\t" \
    "v_pk_add_f32 v[118:119], v[118:119], v[178:179]\n\t" \
    "s_waitcnt lgkmcnt(4)\n\t" \
    "v_pk_add_f32 v[100:101], v[100:101], v[180:181]\n\t" \
    "v_pk_add_f32 v[102:103], v[102:103], v[182:183]\n\t" \
    "s_waitcnt lgkmcnt(3)\n\t" \
    "v_pk_add_f32 v[104:105], v[104:105], v[184:185]\n\t" \
    "v_pk_add_f32 v[106:107], v[106:107], v[186:187]\n\t" \
    "s_waitcnt lgkmcnt(2)\n\t" \
    "v_pk_add_f32 v[108:109], v[108:109], v[188:189]\n\t" \
    "v_pk_add_f32 v[110:111], v[110:111], v[190:191]\n\t" \
    "s_waitcnt lgkmcnt(1)\n\t" \
    "v_pk_add_f32 v[112:113], v[112:113], v[192:193]\n\t" \
    "v_pk_add_f32 v[114:115], v[114:115], v[194:195]\n\t" \
    "s_waitcnt lgkmcnt(0)\n\t" \
    "v_pk_add_f32 v[116:117], v[116:117], v[196:197]\n\t" \
    "v_pk_add_f32 v[118:119], v[118:119], v[198:199]\n\t" \
    "ds_write_b32 %[fc], v98\n\t" \
    "v_sub_u32 %[la], %[l2], %[la]\n\t" \
    "v_cvt_pk_bf16_f32 v84, v100, v101\n\t" \
    "v_cvt_pk_bf16_f32 v85, v102, v103\n\t" \
    "v_cvt_pk_bf16_f32 v86, v104, v105\n\t" \
    "v_cvt_pk_bf16_f32 v87, v106, v107\n\t" \
    "v_cvt_pk_bf16_f32 v88, v108, v109\n\t" \
    "v_cvt_pk_bf16_f32 v89, v110, v111\n\t" \
    "v_cvt_pk_bf16_f32 v90, v112, v113\n\t" \
    "v_cvt_pk_bf16_f32 v91, v114, v115\n\t" \
    "v_cvt_pk_bf16_f32 v92, v116, v117\n\t" \
    "v_cvt_pk_bf16_f32 v93, v118, v119\n\t" \
    "v_mov_b32 v80, 0\n\t" \
    "v_mov_b32 v82, 0\n\t" \
    "v_dot2c_f32_bf16 v80, v84, v99\n\t" \
    "v_dot2c_f32_bf16 v82, v84, v84\n\t" \
    "v_dot2c_f32_bf16 v80, v85, v99\n\t" \
    "v_dot2c_f32_bf16 v82, v85, v85\n\t" \
    "v_dot2c_f32_bf16 v80, v86, v99\n\t" \
    "v_dot2c_f32_bf16 v82, v86, v86\n\t" \
    "v_dot2c_f32_bf16 v80, v87, v99\n\t" \
    "v_dot2c_f32_bf16 v82, v87, v87\n\t" \
    "v_dot2c_f32_bf16 v80, v88, v99\n\t" \
    "v_dot2c_f32_bf16 v82, v88, v88\n\t" \
    "v_dot2c_f32_bf16 v80, v89, v99\n\t" \
    "v_dot2c_f32_bf16 v82, v89, v89\n\t" \
    "v_dot2c_f32_bf16 v80, v90, v99\n\t" \
    "v_dot2c_f32_bf16 v82, v90, v90\n\t" \
    "v_dot2c_f32_bf16 v80, v91, v99\n\t" \
    "v_dot2c_f32_bf16 v82, v91, v91\n\t" \
    "v_dot2c_f32_bf16 v80, v92, v99\n\t" \
    "v_dot2c_f32_bf16 v82, v92, v92\n\t" \
    "v_dot2c_f32_bf16 v80, v93, v99\n\t" \
    "v_dot2c_f32_bf16 v82, v93, v93\n\t" \
    "s_nop 2\n\t" \
    "v_mov_b32 v81, v80\n\t" \
    "v_mov_b32 v83, v82\n\t" \
    "s_nop 1\n\t" \
    "v_permlane16_swap_b32 v80, v81\n\t" \
    "v_permlane16_swap_b32 v82, v83\n\t" \
    "v_add_f32 v80, v80, v81\n\t" \
    "v_add_f32 v82, v82, v83\n\t" \
    "v_mov_b32 v81, v80\n\t" \
    "v_mov_b32 v83, v82\n\t" \
    "s_nop 1\n\t" \
    "v_permlane32_swap_b32 v80, v81\n\t" \
    "v_permlane32_swap_b32 v82, v83\n\t" \
    "v_add_f32 v80, v80, v81\n\t" \
    "v_add_f32 v82, v82, v83\n\t" \
    "v_min_u32 v80, -2, v80\n\t" \
    "v_min_u32 v81, -2, v82\n\t" \
    "s_mov_b64 exec, 0xffff\n\t" \
    "buffer_store_dwordx2 v[80:81], %[l8], %[rs], s50 offen sc1\n\t" \
    "s_mov_b64 exec, -1\n\t" \
    LST3 \
    LJIT \
    "s_add_u32 s46, s43, 0x2c00000\n\t" \
    "s_add_u32 s47, s43, 0x1c00000\n\t" \
    "buffer_load_dwordx2 v[120:121], %[t0], %[rs], s46 offen sc1\n\t" \
    "buffer_load_dwordx2 v[122:123], %[t0], %[rs], s47 offen sc1\n\t" \
    "buffer_load_dwordx2 v[124:125], %[t1], %[rs], s46 offen sc1\n\t" \
    "buffer_load_dwordx2 v[126:127], %[t1], %[rs], s47 offen sc1\n\t" \
    "buffer_load_dwordx2 v[128:129], %[t2], %[rs], s46 offen sc1\n\t" \
    "buffer_load_dwordx2 v[130:131], %[t2], %[rs], s47 offen sc1\n\t" \
    "buffer_load_dwordx2 v[132:133], %[t3], %[rs], s46 offen sc1\n\t" \
    "buffer_load_dwordx2 v[134:135], %[t3], %[rs], s47 offen sc1\n\t" \
    "buffer_load_dwordx2 v[136:137], %[t4], %[rs], s46 offen sc1\n\t" \
    "buffer_load_dwordx2 v[138:139], %[t4], %[rs], s47 offen sc1\n\t" \
    "s_nop 0\n\t" \
    "v_permlane16_swap_b32 v84, v86\n\t" \
    "v_permlane16_swap_b32 v85, v87\n\t" \
    "v_permlane16_swap_b32 v88, v90\n\t" \
    "v_permlane16_swap_b32 v89, v91\n\t" \
    "s_sleep 4\n\t" \
    ".Lf05_%=:\n\t" \
    "buffer_load_dwordx2 v[94:95], %[l8], %[rs], s49 offen sc1\n\t" \
    "buffer_load_dwordx2 v[96:97], %[l8], %[rs], s49 offen offset:512 sc1\n\t" \
    "s_waitcnt vmcnt(0)\n\t" \
    "v_max_u32 v80, v94, v95\n\t" \
    "v_max3_u32 v80, v96, v97, v80\n\t" \
    "v_cmp_eq_u32 vcc, -1, v80\n\t" \
    "s_cbranch_vccz .Lf06_%=\n\t" \
    "s_cmp_eq_u32 s41, 0\n\t" \
    "s_cbranch_scc1 .Lf06_%=\n\t" \
    "s_sub_u32 s41, s41, 1\n\t" \
    "s_branch .Lf05_%=\n\t" \
    ".Lf06_%=:\n\t" \
    LST4 \
    LJIT \
    "v_add_f32 v80, v94, v96\n\t" \
    "v_add_f32 v82, v95, v97\n\t" \
    "v_mov_b32 v81, v80\n\t" \
    "v_mov_b32 v83, v82\n\t" \
    "s_nop 1\n\t" \
    "v_permlane16_swap_b32 v80, v81\n\t" \
    "v_permlane16_swap_b32 v82, v83\n\t" \
    "v_add_f32 v80, v80, v81\n\t" \
    "v_add_f32 v82, v82, v83\n\t" \
    "v_mov_b32 v81, v80\n\t" \
    "v_mov_b32 v83, v82\n\t" \
    "s_nop 1\n\t" \
    "v_permlane32_swap_b32 v80, v81\n\t" \
    "v_permlane32_swap_b32 v82, v83\n\t" \
    "v_add_f32 v80, v80, v81\n\t" \
    "v_add_f32 v82, v82, v83\n\t" \
    "v_mul_f32 v80, 0x3acccccd, v80\n\t" \
    "v_mul_f32 v82, 0x3acccccd, v82\n\t" \
    "v_fma_f32 v82, -v80, v80, v82\n\t" \
    "v_cvt_pk_bf16_f32 v80, v80, v80\n\t" \
    "v_and_b32 v80, 0xffff0000, v80\n\t" \
    "v_max_f32 v82, 0, v82\n\t" \
    "v_cvt_pk_bf16_f32 v82, v82, v82\n\t" \
    "v_and_b32 v82, 0xffff0000, v82\n\t" \
    "v_add_f32 v82, 0x3727c5ac, v82\n\t" \
    "v_cvt_pk_bf16_f32 v82, v82, v82\n\t" \
    "v_and_b32 v82, 0xffff0000, v82\n\t" \
    "v_sqrt_f32 v82, v82\n\t" \
    "s_nop 0\n\t" \
    "v_cvt_pk_bf16_f32 v82, v82, v82\n\t" \
    "v_and_b32 v82, 0xffff0000, v82\n\t" \
    "v_rcp_f32 v82, v82\n\t" \
    "s_nop 0\n\t" \
    "v_mul_f32 v80, -v80, v82\n\t" \
    "v_fma_f32 v100, v100, v82, v80\n\t" \
    "v_fma_f32 v101, v101, v82, v80\n\t" \
    "v_fma_f32 v102, v102, v82, v80\n\t" \
    "v_fma_f32 v103, v103, v82, v80\n\t" \
    "v_fma_f32 v104, v104, v82, v80\n\t" \
    "v_fma_f32 v105, v105, v82, v80\n\t" \
    "v_fma_f32 v106, v106, v82, v80\n\t" \
    "v_fma_f32 v107, v107, v82, v80\n\t" \
    "v_fma_f32 v108, v108, v82, v80\n\t" \
    "v_fma_f32 v109, v109, v82, v80\n\t" \
    "v_fma_f32 v110, v110, v82, v80\n\t" \
    "v_fma_f32 v111, v111, v82, v80\n\t" \
    "v_fma_f32 v112, v112, v82, v80\n\t" \
    "v_fma_f32 v113, v113, v82, v80\n\t" \
    "v_fma_f32 v114, v114, v82, v80\n\t" \
    "v_fma_f32 v115, v115, v82, v80\n\t" \
    "v_fma_f32 v116, v116, v82, v80\n\t" \
    "v_fma_f32 v117, v117, v82, v80\n\t" \
    "v_fma_f32 v118, v118, v82, v80\n\t" \
    "v_fma_f32 v119, v119, v82, v80\n\t" \
    "v_cvt_pk_bf16_f32 v64, v100, v101\n\t" \
    "v_cvt_pk_bf16_f32 v65, v102, v103\n\t" \
    "v_cvt_pk_bf16_f32 v66, v104, v105\n\t" \
    "v_cvt_pk_bf16_f32 v67, v106, v107\n\t" \
    "v_cvt_pk_bf16_f32 v68, v108, v109\n\t" \
    "v_cvt_pk_bf16_f32 v69, v110, v111\n\t" \
    "v_cvt_pk_bf16_f32 v70, v112, v113\n\t" \
    "v_cvt_pk_bf16_f32 v71, v114, v115\n\t" \
    "v_cvt_pk_bf16_f32 v72, v116, v117\n\t" \
    "v_cvt_pk_bf16_f32 v73, v118, v119\n\t" \
    "s_nop 0\n\t" \
    "v_permlane16_swap_b32 v64, v66\n\t" \
    "v_permlane16_swap_b32 v65, v67\n\t" \
    "v_permlane16_swap_b32 v68, v70\n\t" \
    "v_permlane16_swap_b32 v69, v71\n\t" \
    "buffer_store_dwordx4 v[64:67], %[o16], %[rn], s51 offen sc1\n\t" \
    "buffer_store_dwordx4 v[68:71], %[o17], %[rn], s51 offen sc1\n\t" \
    "buffer_store_dwordx2 v[72:73], %[t4], %[rn], s51 offen sc1\n\t" \
    "buffer_store_dwordx4 v[84:87], %[o16], %[rn], s52 offen sc1\n\t" \
    "buffer_store_dwordx4 v[88:91], %[o17], %[rn], s52 offen sc1\n\t" \
    "buffer_store_dwordx2 v[92:93], %[t4], %[rn], s52 offen sc1\n\t" \
    "s_waitcnt vmcnt(0)\n\t" \
    "buffer_store_dwordx4 v[76:79], %[fo], %[rn], %[fs] offen sc1\n\t" \
    LST5 \
    LJIT \
    LFLUSHF \
    "s_add_u32 s40, s40, 2\n\t" \
    "s_cmp_lt_u32 s40, 128\n\t" \
    "s_cbranch_scc1 .Lf01_%=\n\t" \
    "s_branch .Lend%=\n\t" \
    ".Lend%=:\n\t" \
    "s_waitcnt vmcnt(0) lgkmcnt(0)\n\t" \
    "s_mov_b32 %[bud], s41\n\t"
#define ASM_W4_FIN1 \
    "s_mov_b32 s40, %[mt]\n\t" \
    "s_mov_b32 s41, 0x40000\n\t" \
    LSTINIT \
    "s_mov_b32 s43, 0\n\t" \
    "v_mov_b32 v99, 0x3f803f80\n\t" \
    "s_setprio 3\n\t" \
    "s_add_u32 s46, s43, 0x2c00000\n\t" \
    "s_add_u32 s47, s43, 0x1c00000\n\t" \
    "buffer_load_dwordx2 v[120:121], %[t0], %[rs], s46 offen sc1\n\t" \
    "buffer_load_dwordx2 v[122:123], %[t0], %[rs], s47 offen sc1\n\t" \
    "buffer_load_dwordx2 v[124:125], %[t1], %[rs], s46 offen sc1\n\t" \
    "buffer_load_dwordx2 v[126:127], %[t1], %[rs], s47 offen sc1\n\t" \
    "buffer_load_dwordx2 v[128:129], %[t2], %[rs], s46 offen sc1\n\t" \
    "buffer_load_dwordx2 v[130:131], %[t2], %[rs], s47 offen sc1\n\t" \
    "buffer_load_dwordx2 v[132:133], %[t3], %[rs], s46 offen sc1\n\t" \
    "buffer_load_dwordx2 v[134:135], %[t3], %[rs], s47 offen sc1\n\t" \
    "buffer_load_dwordx2 v[136:137], %[t4], %[rs], s46 offen sc1\n\t" \
    "buffer_load_dwordx2 v[138:139], %[t4], %[rs], s47 offen sc1\n\t" \
    ".Lf11_%=:\n\t" \
    LST0 \
    LJIT \
    ".Lf12_%=:\n\t" \
    "s_waitcnt vmcnt(0)\n\t" \
    "v_max_u32 v80, v120, v121\n\t" \
    "v_max3_u32 v80, v122, v123, v80\n\t" \
    "v_max3_u32 v80, v124, v125, v80\n\t" \
    "v_max3_u32 v80, v126, v127, v80\n\t" \
    "v_max3_u32 v80, v128, v129, v80\n\t" \
    "v_max3_u32 v80, v130, v131, v80\n\t" \
    "v_max3_u32 v80, v132, v133, v80\n\t" \
    "v_max3_u32 v80, v134, v135, v80\n\t" \
    "v_max3_u32 v80, v136, v137, v80\n\t" \
    "v_max3_u32 v80, v138, v139, v80\n\t" \
    "v_cmp_eq_u32 vcc, -1, v80\n\t" \
    "s_cbranch_vccz .Lf13_%=\n\t" \
    "s_cmp_eq_u32 s41, 0\n\t" \
    "s_cbranch_scc1 .Lf13_%=\n\t" \
    "s_sub_u32 s41, s41, 1\n\t" \
    "s_sleep 8\n\t" \
    "s_add_u32 s46, s43, 0x2c00000\n\t" \
    "s_add_u32 s47, s43, 0x1c00000\n\t" \
    "buffer_load_dwordx2 v[120:121], %[t0], %[rs], s46 offen sc1\n\t" \
    "buffer_load_dwordx2 v[122:123], %[t0], %[rs], s47 offen sc1\n\t" \
    "buffer_load_dwordx2 v[124:125], %[t1], %[rs], s46 offen sc1\n\t" \
    "buffer_load_dwordx2 v[126:127], %[t1], %[rs], s47 offen sc1\n\t" \
    "buffer_load_dwordx2 v[128:129], %[t2], %[rs], s46 offen sc1\n\t" \
    "buffer_load_dwordx2 v[130:131], %[t2], %[rs], s47 offen sc1\n\t" \
    "buffer_load_dwordx2 v[132:133], %[t3], %[rs], s46 offen sc1\n\t" \
    "buffer_load_dwordx2 v[134:135], %[t3], %[rs], s47 offen sc1\n\t" \
    "buffer_load_dwordx2 v[136:137], %[t4], %[rs], s46 offen sc1\n\t" \
    "buffer_load_dwordx2 v[138:139], %[t4], %[rs], s47 offen sc1\n\t" \
    "s_waitcnt vmcnt(0)\n\t" \
    "s_branch .Lf12_%=\n\t" \
    ".Lf13_%=:\n\t" \
    "v_lshlrev_b32 v140, 16, v120\n\t" \
    "v_and_b32 v141, 0xffff0000, v120\n\t" \
    "v_lshlrev_b32 v142, 16, v121\n\t" \
    "v_and_b32 v143, 0xffff0000, v121\n\t" \
    "v_lshlrev_b32 v80, 16, v122\n\t" \
    "v_and_b32 v81, 0xffff0000, v122\n\t" \
    "v_lshlrev_b32 v82, 16, v123\n\t" \
    "v_and_b32 v83, 0xffff0000, v123\n\t" \
    "v_add_f32 v140, v140, v80\n\t" \
    "v_add_f32 v141, v141, v81\n\t" \
    "v_add_f32 v142, v142, v82\n\t" \
    "v_add_f32 v143, v143, v83\n\t" \
    "v_lshlrev_b32 v144, 16, v124\n\t" \
    "v_and_b32 v145, 0xffff0000, v124\n\t" \
    "v_lshlrev_b32 v146, 16, v125\n\t" \
    "v_and_b32 v147, 0xffff0000, v125\n\t" \
    "v_lshlrev_b32 v80, 16, v126\n\t" \
    "v_and_b32 v81, 0xffff0000, v126\n\t" \
    "v_lshlrev_b32 v82, 16, v127\n\t" \
    "v_and_b32 v83, 0xffff0000, v127\n\t" \
    "v_add_f32 v144, v144, v80\n\t" \
    "v_add_f32 v145, v145, v81\n\t" \
    "v_add_f32 v146, v146, v82\n\t" \
    "v_add_f32 v147, v147, v83\n\t" \
    "v_lshlrev_b32 v148, 16, v128\n\t" \
    "v_and_b32 v149, 0xffff0000, v128\n\t" \
    "v_lshlrev_b32 v150, 16, v129\n\t" \
    "v_and_b32 v151, 0xffff0000, v129\n\t" \
    "v_lshlrev_b32 v80, 16, v130\n\t" \
    "v_and_b32 v81, 0xffff0000, v130\n\t" \
    "v_lshlrev_b32 v82, 16, v131\n\t" \
    "v_and_b32 v83, 0xffff0000, v131\n\t" \
    "v_add_f32 v148, v148, v80\n\t" \
    "v_add_f32 v149, v149, v81\n\t" \
    "v_add_f32 v150, v150, v82\n\t" \
    "v_add_f32 v151, v151, v83\n\t" \
    "v_lshlrev_b32 v152, 16, v132\n\t" \
    "v_and_b32 v153, 0xffff0000, v132\n\t" \
    "v_lshlrev_b32 v154, 16, v133\n\t" \
    "v_and_b32 v155, 0xffff0000, v133\n\t" \
    "v_lshlrev_b32 v80, 16, v134\n\t" \
    "v_and_b32 v81, 0xffff0000, v134\n\t" \
    "v_lshlrev_b32 v82, 16, v135\n\t" \
    "v_and_b32 v83, 0xffff0000, v135\n\t" \
    "v_add_f32 v152, v152, v80\n\t" \
    "v_add_f32 v153, v153, v81\n\t" \
    "v_add_f32 v154, v154, v82\n\t" \
    "v_add_f32 v155, v155, v83\n\t" \
    "v_lshlrev_b32 v156, 16, v136\n\t" \
    "v_and_b32 v157, 0xffff0000, v136\n\t" \
    "v_lshlrev_b32 v158, 16, v137\n\t" \
    "v_and_b32 v159, 0xffff0000, v137\n\t" \
    "v_lshlrev_b32 v80, 16, v138\n\t" \
    "v_and_b32 v81, 0xffff0000, v138\n\t" \
    "v_lshlrev_b32 v82, 16, v139\n\t" \
    "v_and_b32 v83, 0xffff0000, v139\n\t" \
    "v_add_f32 v156, v156, v80\n\t" \
    "v_add_f32 v157, v157, v81\n\t" \
    "v_add_f32 v158, v158, v82\n\t" \
    "v_add_f32 v159, v159, v83\n\t" \
    "s_mov_b32 s42, s43\n\t" \
    "s_lshr_b32 s48, s40, 1\n\t" \
    "s_add_u32 s48, s48, 1\n\t" \
    "s_mul_i32 s43, s48, 0x14000\n\t" \
    "s_lshr_b32 s44, s48, 4\n\t" \
    "s_mul_i32 s44, s44, 0xc2000\n\t" \
    "s_add_u32 s43, s43, s44\n\t" \
    "s_mul_i32 s49, s40, 20480\n\t" \
    "s_add_u32 s49, s49, %[co]\n\t" \
    "s_add_u32 s44, s40, 1\n\t" \
    "v_mov_b32 v98, s44\n\t" \
    LST1 \
    LJIT \
    ".Lf14_%=:\n\t" \
    "s_nop 7\n\t" \
    "ds_read_b32 v80, %[fa]\n\t" \
    "s_waitcnt lgkmcnt(0)\n\t" \
    "v_cmp_ge_u32 vcc, s40, v80\n\t" \
    "s_cbranch_vccnz .Lf14_%=\n\t" \
    LST2 \
    LJIT \
    "ds_read_b128 v[160:163], %[la] offset:0\n\t" \
    "ds_read_b128 v[164:167], %[la] offset:6144\n\t" \
    "ds_read_b128 v[168:171], %[la] offset:12288\n\t" \
    "ds_read_b128 v[172:175], %[la] offset:18432\n\t" \
    "ds_read_b128 v[176:179], %[la] offset:24576\n\t" \
    "ds_read_b128 v[180:183], %[la] offset:1024\n\t" \
    "ds_read_b128 v[184:187], %[la] offset:7168\n\t" \
    "ds_read_b128 v[188:191], %[la] offset:13312\n\t" \
    "ds_read_b128 v[192:195], %[la] offset:19456\n\t" \
    "ds_read_b128 v[196:199], %[la] offset:25600\n\t" \
    "ds_read_b128 v[200:203], %[la] offset:2048\n\t" \
    "ds_read_b128 v[204:207], %[la] offset:8192\n\t" \
    "ds_read_b128 v[208:211], %[la] offset:14336\n\t" \
    "ds_read_b128 v[212:215], %[la] offset:20480\n\t" \
    "ds_read_b128 v[216:219], %[la] offset:26624\n\t" \
    "s_waitcnt lgkmcnt(14)\n\t" \
    "v_pk_add_f32 v[100:101], v[140:141], v[160:161]\n\t" \
    "v_pk_add_f32 v[102:103], v[142:143], v[162:163]\n\t" \
    "ds_read_b128 v[220:223], %[la] offset:3072\n\t" \
    "s_waitcnt lgkmcnt(14)\n\t" \
    "v_pk_add_f32 v[104:105], v[144:145], v[164:165]\n\t" \
    "v_pk_add_f32 v[106:107], v[146:147], v[166:167]\n\t" \
    "ds_read_b128 v[224:227], %[la] offset:9216\n\t" \
    "s_waitcnt lgkmcnt(14)\n\t" \
    "v_pk_add_f32 v[108:109], v[148:149], v[168:169]\n\t" \
    "v_pk_add_f32 v[110:111], v[150:151], v[170:171]\n\t" \
    "ds_read_b128 v[228:231], %[la] offset:15360\n\t" \
    "s_waitcnt lgkmcnt(14)\n\t" \
    "v_pk_add_f32 v[112:113], v[152:153], v[172:173]\n\t" \
    "v_pk_add_f32 v[114:115], v[154:155], v[174:175]\n\t" \
    "ds_read_b128 v[232:235], %[la] offset:21504\n\t" \
    "s_waitcnt lgkmcnt(14)\n\t" \
    "v_pk_add_f32 v[116:117], v[156:157], v[176:177]\n\t" \
    "v_pk_add_f32 v[118:119], v[158:159], v[178:179]\n\t" \
    "ds_read_b128 v[236:239], %[la] offset:27648\n\t" \
    "s_waitcnt lgkmcnt(14)\n\t" \
    "v_pk_add_f32 v[100:101], v[100:101], v[180:181]\n\t" \
    "v_pk_add_f32 v[102:103], v[102:103], v[182:183]\n\t" \
    "ds_read_b128 v[160:163], %[la] offset:4096\n\t" \
    "s_waitcnt lgkmcnt(14)\n\t" \
    "v_pk_add_f32 v[104:105], v[104:105], v[184:185]\n\t" \
    "v_pk_add_f32 v[106:107], v[106:107], v[186:187]\n\t" \
    "ds_read_b128 v[164:167], %[la] offset:10240\n\t" \
    "s_waitcnt lgkmcnt(14)\n\t" \
    "v_pk_add_f32 v[108:109], v[108:109], v[188:189]\n\t" \
    "v_pk_add_f32 v[110:111], v[110:111], v[190:191]\n\t" \
    "ds_read_b128 v[168:171], %[la] offset:16384\n\t" \
    "s_waitcnt lgkmcnt(14)\n\t" \
    "v_pk_add_f32 v[112:113], v[112:113], v[192:193]\n\t" \
    "v_pk_add_f32 v[114:115], v[114:115], v[194:195]\n\t" \
    "ds_read_b128 v[172:175], %[la] offset:22528\n\t" \
    "s_waitcnt lgkmcnt(14)\n\t" \
    "v_pk_add_f32 v[116:117], v[116:117], v[196:197]\n\t" \
    "v_pk_add_f32 v[118:119], v[118:119], v[198:199]\n\t" \
    "ds_read_b128 v[176:179], %[la] offset:28672\n\t" \
    "s_waitcnt lgkmcnt(14)\n\t" \
    "v_pk_add_f32 v[100:101], v[100:101], v[200:201]\n\t" \
    "v_pk_add_f32 v[102:103], v[102:103], v[202:203]\n\t" \
    "ds_read_b128 v[180:183], %[la] offset:5120\n\t" \
    "s_waitcnt lgkmcnt(14)\n\t" \
    "v_pk_add_f32 v[104:105], v[104:105], v[204:205]\n\t" \
    "v_pk_add_f32 v[106:107], v[106:107], v[206:207]\n\t" \
    "ds_read_b128 v[184:187], %[la] offset:11264\n\t" \
    "s_waitcnt lgkmcnt(14)\n\t" \
    "v_pk_add_f32 v[108:109], v[108:109], v[208:209]\n\t" \
    "v_pk_add_f32 v[110:111], v[110:111], v[210:211]\n\t" \
    "ds_read_b128 v[188:191], %[la] offset:17408\n\t" \
    "s_waitcnt lgkmcnt(14)\n\t" \
    "v_pk_add_f32 v[112:113], v[112:113], v[212:213]\n\t" \
    "v_pk_add_f32 v[114:115], v[114:115], v[214:215]\n\t" \
    "ds_read_b128 v[192:195], %[la] offset:23552\n\t" \
    "s_waitcnt lgkmcnt(14)\n\t" \
    "v_pk_add_f32 v[116:117], v[116:117], v[216:217]\n\t" \
    "v_pk_add_f32 v[118:119], v[118:119], v[218:219]\n\t" \
    "ds_read_b128 v[196:199], %[la] offset:29696\n\t" \
    "s_waitcnt lgkmcnt(14)\n\t" \
    "v_pk_add_f32 v[100:101], v[100:101], v[220:221]\n\t" \
    "v_pk_add_f32 v[102:103], v[102:103], v[222:223]\n\t" \
    "s_waitcnt lgkmcnt(13)\n\t" \
    "v_pk_add_f32 v[104:105], v[104:105], v[224:225]\n\t" \
    "v_pk_add_f32 v[106:107], v[106:107], v[226:227]\n\t" \
    "s_waitcnt lgkmcnt(12)\n\t" \
    "v_pk_add_f32 v[108:109], v[108:109], v[228:229]\n\t" \
    "v_pk_add_f32 v[110:111], v[110:111], v[230:231]\n\t" \
    "s_waitcnt lgkmcnt(11)\n\t" \
    "v_pk_add_f32 v[112:113], v[112:113], v[232:233]\n\t" \
    "v_pk_add_f32 v[114:115], v[114:115], v[234:235]\n\t" \
    "s_waitcnt lgkmcnt(10)\n\t" \
    "v_pk_add_f32 v[116:117], v[116:117], v[236:237]\n\t" \
    "v_pk_add_f32 v[118:119], v[118:119], v[238:239]\n\t" \
    "s_waitcnt lgkmcnt(9)\n\t" \
    "v_pk_add_f32 v[100:101], v[100:101], v[160:161]\n\t" \
    "v_pk_add_f32 v[102:103], v[102:103], v[162:163]\n\t" \
    "s_waitcnt lgkmcnt(8)\n\t" \
    "v_pk_add_f32 v[104:105], v[104:105], v[164:165]\n\t" \
    "v_pk_add_f32 v[106:107], v[106:107], v[166:167]\n\t" \
    "s_waitcnt lgkmcnt(7)\n\t" \
    "v_pk_add_f32 v[108:109], v[108:109], v[168:169]\n\t" \
    "v_pk_add_f32 v[110:111], v[110:111], v[170:171]\n\t" \
    "s_waitcnt lgkmcnt(6)\n\t" \
    "v_pk_add_f32 v[112:113], v[112:113], v[172:173]\n\t" \
    "v_pk_add_f32 v[114:115], v[114:115], v[174:175]\n\t" \
    "s_waitcnt lgkmcnt(5)\n\t" \
    "v_pk_add_f32 v[116:117], v[116:117], v[176:177]\n\t" \
    "v_pk_add_f32 v[118:119], v[118:119], v[178:179]\n\t" \
    "s_waitcnt lgkmcnt(4)\n\t" \
    "v_pk_add_f32 v[100:101], v[100:101], v[180:181]\n\t" \
    "v_pk_add_f32 v[102:103], v[102:103], v[182:183]\n\t" \
    "s_waitcnt lgkmcnt(3)\n\t" \
    "v_pk_add_f32 v[104:105], v[104:105], v[184:185]\n\t" \
    "v_pk_add_f32 v[106:107], v[106:107], v[186:187]\n\t" \
    "s_waitcnt lgkmcnt(2)\n\t" \
    "v_pk_add_f32 v[108:109], v[108:109], v[188:189]\n\t" \
    "v_pk_add_f32 v[110:111], v[110:111], v[190:191]\n\t" \
    "s_waitcnt lgkmcnt(1)\n\t" \
    "v_pk_add_f32 v[112:113], v[112:113], v[192:193]\n\t" \
    "v_pk_add_f32 v[114:115], v[114:115], v[194:195]\n\t" \
    "s_waitcnt lgkmcnt(0)\n\t" \
    "v_pk_add_f32 v[116:117], v[116:117], v[196:197]\n\t" \
    "v_pk_add_f32 v[118:119], v[118:119], v[198:199]\n\t" \
    "ds_write_b32 %[fc], v98\n\t" \
    "v_sub_u32 %[la], %[l2], %[la]\n\t" \
    "v_cvt_pk_bf16_f32 v84, v100, v101\n\t" \
    "v_cvt_pk_bf16_f32 v85, v102, v103\n\t" \
    "v_cvt_pk_bf16_f32 v86, v104, v105\n\t" \
    "v_cvt_pk_bf16_f32 v87, v106, v107\n\t" \
    "v_cvt_pk_bf16_f32 v88, v108, v109\n\t" \
    "v_cvt_pk_bf16_f32 v89, v110, v111\n\t" \
    "v_cvt_pk_bf16_f32 v90, v112, v113\n\t" \
    "v_cvt_pk_bf16_f32 v91, v114, v115\n\t" \
    "v_cvt_pk_bf16_f32 v92, v116, v117\n\t" \
    "v_cvt_pk_bf16_f32 v93, v118, v119\n\t" \
    "s_nop 0\n\t" \
    "v_permlane16_swap_b32 v84, v86\n\t" \
    "v_permlane16_swap_b32 v85, v87\n\t" \
    "v_permlane16_swap_b32 v88, v90\n\t" \
    "v_permlane16_swap_b32 v89, v91\n\t" \
    "buffer_store_dwordx4 v[84:87], %[q16], %[ro], s49 offen nt\n\t" \
    "buffer_store_dwordx4 v[88:91], %[q16], %[ro], s49 offen offset:64 nt\n\t" \
    "buffer_store_dwordx2 v[92:93], %[q8], %[ro], s49 offen offset:128 nt\n\t" \
    "s_cmp_gt_u32 s40, 125\n\t" \
    "s_cbranch_scc1 .Lf15_%=\n\t" \
    "s_add_u32 s46, s43, 0x2c00000\n\t" \
    "s_add_u32 s47, s43, 0x1c00000\n\t" \
    "buffer_load_dwordx2 v[120:121], %[t0], %[rs], s46 offen sc1\n\t" \
    "buffer_load_dwordx2 v[122:123], %[t0], %[rs], s47 offen sc1\n\t" \
    "buffer_load_dwordx2 v[124:125], %[t1], %[rs], s46 offen sc1\n\t" \
    "buffer_load_dwordx2 v[126:127], %[t1], %[rs], s47 offen sc1\n\t" \
    "buffer_load_dwordx2 v[128:129], %[t2], %[rs], s46 offen sc1\n\t" \
    "buffer_load_dwordx2 v[130:131], %[t2], %[rs], s47 offen sc1\n\t" \
    "buffer_load_dwordx2 v[132:133], %[t3], %[rs], s46 offen sc1\n\t" \
    "buffer_load_dwordx2 v[134:135], %[t3], %[rs], s47 offen sc1\n\t" \
    "buffer_load_dwordx2 v[136:137], %[t4], %[rs], s46 offen sc1\n\t" \
    "buffer_load_dwordx2 v[138:139], %[t4], %[rs], s47 offen sc1\n\t" \
    ".Lf15_%=:\n\t" \
    LST5 \
    LJIT \
    LFLUSHF \
    "s_add_u32 s40, s40, 2\n\t" \
    "s_cmp_lt_u32 s40, 128\n\t" \
    "s_cbranch_scc1 .Lf11_%=\n\t" \
    "s_branch .Lend%=\n\t" \
    ".Lend%=:\n\t" \
    "s_waitcnt vmcnt(0) lgkmcnt(0)\n\t" \
    "s_mov_b32 %[bud], s41\n\t"
#define ASM_W1_LOOP \
    "s_mov_b32 s40, 0\n\t" \
    "s_mov_b32 s41, 0x20000\n\t" \
    LSTINIT \
    "s_add_u32 m0, %[wm], 0x0\n\t" \
    "s_add_u32 s44, %[wo], 0x0\n\t" \
    "buffer_load_dwordx4 v176, %[wr], s44 offen nt lds\n\t" \
    "s_add_u32 m0, %[wm], 0x2000\n\t" \
    "s_add_u32 s44, %[wo], 0xa000\n\t" \
    "buffer_load_dwordx4 v176, %[wr], s44 offen nt lds\n\t" \
    "s_add_u32 m0, %[wm], 0x4000\n\t" \
    "s_add_u32 s44, %[wo], 0x14000\n\t" \
    "buffer_load_dwordx4 v176, %[wr], s44 offen nt lds\n\t" \
    "s_add_u32 m0, %[wm], 0x6000\n\t" \
    "s_add_u32 s44, %[wo], 0x1e000\n\t" \
    "buffer_load_dwordx4 v176, %[wr], s44 offen nt lds\n\t" \
    "s_add_u32 m0, %[wm], 0x8000\n\t" \
    "s_add_u32 s44, %[wo], 0x28000\n\t" \
    "buffer_load_dwordx4 v176, %[wr], s44 offen nt lds\n\t" \
    "s_add_u32 m0, %[wm], 0xa000\n\t" \
    "s_add_u32 s44, %[wo], 0x32000\n\t" \
    "buffer_load_dwordx4 v176, %[wr], s44 offen nt lds\n\t" \
    "s_add_u32 m0, %[wm], 0xc000\n\t" \
    "s_add_u32 s44, %[wo], 0x3c000\n\t" \
    "buffer_load_dwordx4 v176, %[wr], s44 offen nt lds\n\t" \
    "s_add_u32 m0, %[wm], 0xe000\n\t" \
    "s_add_u32 s44, %[wo], 0x46000\n\t" \
    "buffer_load_dwordx4 v176, %[wr], s44 offen nt lds\n\t" \
    "s_add_u32 m0, %[wm], 0x10000\n\t" \
    "s_add_u32 s44, %[wo], 0x50000\n\t" \
    "buffer_load_dwordx4 v176, %[wr], s44 offen nt lds\n\t" \
    "s_add_u32 m0, %[wm], 0x12000\n\t" \
    "s_add_u32 s44, %[wo], 0x5a000\n\t" \
    "buffer_load_dwordx4 v176, %[wr], s44 offen nt lds\n\t" \
    "s_add_u32 m0, %[wm], 0x14000\n\t" \
    "s_add_u32 s44, %[wo], 0x64000\n\t" \
    "buffer_load_dwordx4 v176, %[wr], s44 offen nt lds\n\t" \
    "s_add_u32 m0, %[wm], 0x16000\n\t" \
    "s_add_u32 s44, %[wo], 0x6e000\n\t" \
    "buffer_load_dwordx4 v176, %[wr], s44 offen nt lds\n\t" \
    "s_add_u32 m0, %[wm], 0x18000\n\t" \
    "s_add_u32 s44, %[wo], 0x78000\n\t" \
    "buffer_load_dwordx4 v176, %[wr], s44 offen nt lds\n\t" \
    "s_add_u32 m0, %[wm], 0x1a000\n\t" \
    "s_add_u32 s44, %[wo], 0x82000\n\t" \
    "buffer_load_dwordx4 v176, %[wr], s44 offen nt lds\n\t" \
    "s_add_u32 m0, %[wm], 0x1c000\n\t" \
    "s_add_u32 s44, %[wo], 0x8c000\n\t" \
    "buffer_load_dwordx4 v176, %[wr], s44 offen nt lds\n\t" \
    "s_add_u32 m0, %[wm], 0x1e000\n\t" \
    "s_add_u32 s44, %[wo], 0x96000\n\t" \
    "buffer_load_dwordx4 v176, %[wr], s44 offen nt lds\n\t" \
    "s_add_u32 m0, %[wm], 0x20000\n\t" \
    "s_add_u32 s44, %[wo], 0xa0000\n\t" \
    "buffer_load_dwordx4 v176, %[wr], s44 offen nt lds\n\t" \
    "s_add_u32 m0, %[wm], 0x22000\n\t" \
    "s_add_u32 s44, %[wo], 0xaa000\n\t" \
    "buffer_load_dwordx4 v176, %[wr], s44 offen nt lds\n\t" \
    "s_add_u32 m0, %[wm], 0x24000\n\t" \
    "s_add_u32 s44, %[wo], 0xb4000\n\t" \
    "buffer_load_dwordx4 v176, %[wr], s44 offen nt lds\n\t" \
    "s_add_u32 m0, %[wm], 0x26000\n\t" \
    "s_add_u32 s44, %[wo], 0xbe000\n\t" \
    "buffer_load_dwordx4 v176, %[wr], s44 offen nt lds\n\t" \
    "s_waitcnt vmcnt(0) lgkmcnt(0)\n\t" \
    "s_barrier \n\t" \
    "v_add_u32 v178, 0x0, v177\n\t" \
    "ds_read_b64_tr_b16 v[0:1], v178 offset:0\n\t" \
    "ds_read_b64_tr_b16 v[2:3], v178 offset:1024\n\t" \
    "ds_read_b64_tr_b16 v[4:5], v178 offset:8192\n\t" \
    "ds_read_b64_tr_b16 v[6:7], v178 offset:9216\n\t" \
    "ds_read_b64_tr_b16 v[8:9], v178 offset:16384\n\t" \
    "ds_read_b64_tr_b16 v[10:11], v178 offset:17408\n\t" \
    "ds_read_b64_tr_b16 v[12:13], v178 offset:24576\n\t" \
    "ds_read_b64_tr_b16 v[14:15], v178 offset:25600\n\t" \
    "v_add_u32 v178, 0x8000, v177\n\t" \
    "ds_read_b64_tr_b16 v[16:17], v178 offset:0\n\t" \
    "ds_read_b64_tr_b16 v[18:19], v178 offset:1024\n\t" \
    "ds_read_b64_tr_b16 v[20:21], v178 offset:8192\n\t" \
    "ds_read_b64_tr_b16 v[22:23], v178 offset:9216\n\t" \
    "ds_read_b64_tr_b16 v[24:25], v178 offset:16384\n\t" \
    "ds_read_b64_tr_b16 v[26:27], v178 offset:17408\n\t" \
    "ds_read_b64_tr_b16 v[28:29], v178 offset:24576\n\t" \
    "ds_read_b64_tr_b16 v[30:31], v178 offset:25600\n\t" \
    "v_add_u32 v178, 0x10000, v177\n\t" \
    "ds_read_b64_tr_b16 v[32:33], v178 offset:0\n\t" \
    "ds_read_b64_tr_b16 v[34:35], v178 offset:1024\n\t" \
    "ds_read_b64_tr_b16 v[36:37], v178 offset:8192\n\t" \
    "ds_read_b64_tr_b16 v[38:39], v178 offset:9216\n\t" \
    "ds_read_b64_tr_b16 v[40:41], v178 offset:16384\n\t" \
    "ds_read_b64_tr_b16 v[42:43], v178 offset:17408\n\t" \
    "ds_read_b64_tr_b16 v[44:45], v178 offset:24576\n\t" \
    "ds_read_b64_tr_b16 v[46:47], v178 offset:25600\n\t" \
    "v_add_u32 v178, 0x18000, v177\n\t" \
    "ds_read_b64_tr_b16 v[48:49], v178 offset:0\n\t" \
    "ds_read_b64_tr_b16 v[50:51], v178 offset:1024\n\t" \
    "ds_read_b64_tr_b16 v[52:53], v178 offset:8192\n\t" \
    "ds_read_b64_tr_b16 v[54:55], v178 offset:9216\n\t" \
    "ds_read_b64_tr_b16 v[56:57], v178 offset:16384\n\t" \
    "ds_read_b64_tr_b16 v[58:59], v178 offset:17408\n\t" \
    "ds_read_b64_tr_b16 v[60:61], v178 offset:24576\n\t" \
    "ds_read_b64_tr_b16 v[62:63], v178 offset:25600\n\t" \
    "v_add_u32 v178, 0x20000, v177\n\t" \
    "ds_read_b64_tr_b16 v[64:65], v178 offset:0\n\t" \
    "ds_read_b64_tr_b16 v[66:67], v178 offset:1024\n\t" \
    "ds_read_b64_tr_b16 v[68:69], v178 offset:8192\n\t" \
    "ds_read_b64_tr_b16 v[70:71], v178 offset:9216\n\t" \
    "ds_read_b64_tr_b16 v[72:73], v178 offset:16384\n\t" \
    "ds_read_b64_tr_b16 v[74:75], v178 offset:17408\n\t" \
    "ds_read_b64_tr_b16 v[76:77], v178 offset:24576\n\t" \
    "ds_read_b64_tr_b16 v[78:79], v178 offset:25600\n\t" \
    "v_mov_b32 v228, 0x3f803f80\n\t" \
    "v_mov_b32 v229, 0x3f803f80\n\t" \
    "v_mov_b32 v230, 0x3f803f80\n\t" \
    "v_mov_b32 v231, 0x3f803f80\n\t" \
    "v_mov_b64 v[216:217], 0\n\t" \
    "v_mov_b32 v219, 0\n\t" \
    "v_bfe_u32 v83, v253, 2, 5\n\t" \
    "v_add_u32 v83, 0x7000, v83\n\t" \
    "v_mov_b32 v218, 0\n\t" \
    "s_waitcnt lgkmcnt(0)\n\t" \
    "s_barrier \n\t" \
    "ds_write_b32 v251, v228 offset:72\n\t" \
    "ds_write_b32 v251, v228 offset:2120\n\t" \
    "v_mfma_f32_16x16x32_bf16 v[160:163], v[0:3], v[228:231], 0\n\t" \
    "v_mfma_f32_16x16x32_bf16 v[160:163], v[4:7], v[228:231], v[160:163]\n\t" \
    "v_mfma_f32_16x16x32_bf16 v[160:163], v[8:11], v[228:231], v[160:163]\n\t" \
    "v_mfma_f32_16x16x32_bf16 v[160:163], v[12:15], v[228:231], v[160:163]\n\t" \
    "v_mfma_f32_16x16x32_bf16 v[160:163], v[16:19], v[228:231], v[160:163]\n\t" \
    "v_mfma_f32_16x16x32_bf16 v[160:163], v[20:23], v[228:231], v[160:163]\n\t" \
    "v_mfma_f32_16x16x32_bf16 v[160:163], v[24:27], v[228:231], v[160:163]\n\t" \
    "v_mfma_f32_16x16x32_bf16 v[160:163], v[28:31], v[228:231], v[160:163]\n\t" \
    "v_mfma_f32_16x16x32_bf16 v[160:163], v[32:35], v[228:231], v[160:163]\n\t" \
    "v_mfma_f32_16x16x32_bf16 v[160:163], v[36:39], v[228:231], v[160:163]\n\t" \
    "v_mfma_f32_16x16x32_bf16 v[160:163], v[40:43], v[228:231], v[160:163]\n\t" \
    "v_mfma_f32_16x16x32_bf16 v[160:163], v[44:47], v[228:231], v[160:163]\n\t" \
    "v_mfma_f32_16x16x32_bf16 v[160:163], v[48:51], v[228:231], v[160:163]\n\t" \
    "v_mfma_f32_16x16x32_bf16 v[160:163], v[52:55], v[228:231], v[160:163]\n\t" \
    "v_mfma_f32_16x16x32_bf16 v[160:163], v[56:59], v[228:231], v[160:163]\n\t" \
    "v_mfma_f32_16x16x32_bf16 v[160:163], v[60:63], v[228:231], v[160:163]\n\t" \
    "v_mfma_f32_16x16x32_bf16 v[160:163], v[64:67], v[228:231], v[160:163]\n\t" \
    "v_mfma_f32_16x16x32_bf16 v[160:163], v[68:71], v[228:231], v[160:163]\n\t" \
    "v_mfma_f32_16x16x32_bf16 v[160:163], v[72:75], v[228:231], v[160:163]\n\t" \
    "v_mfma_f32_16x16x32_bf16 v[160:163], v[76:79], v[228:231], v[160:163]\n\t" \
    "s_mov_b32 s52, 0x22222222\n\t" \
    "s_mov_b32 s53, 0x22222222\n\t" \
    "s_mov_b32 s54, 0x44444444\n\t" \
    "s_mov_b32 s55, 0x44444444\n\t" \
    "s_mov_b32 s56, 0x88888888\n\t" \
    "s_mov_b32 s57, 0x88888888\n\t" \
    "ds_write_b32 v83, v218\n\t" \
    "s_waitcnt lgkmcnt(0)\n\t" \
    "s_barrier \n\t" \
    ".Lw11_%=:\n\t" \
    "v_mov_b32 v248, 0\n\t" \
    "buffer_load_dword v248, v248, %[rs], %[go] offen sc1\n\t" \
    "s_waitcnt vmcnt(0)\n\t" \
    "v_cmp_ne_u32 vcc, 0, v248\n\t" \
    "s_cbranch_vccnz .Lw12_%=\n\t" \
    "s_cmp_eq_u32 s41, 0\n\t" \
    "s_cbranch_scc1 .Lw12_%=\n\t" \
    "s_sub_u32 s41, s41, 1\n\t" \
    "s_sleep 16\n\t" \
    "s_branch .Lw11_%=\n\t" \
    ".Lw12_%=:\n\t" \
    "s_sub_i32 s60, 0, %[ng]\n\t" \
    "s_sub_i32 s61, 1, %[ng]\n\t" \
    "s_add_i32 s62, s60, 6\n\t" \
    "s_add_i32 s63, s61, 6\n\t" \
    "s_mul_i32 s42, s61, 127\n\t" \
    "s_mul_i32 s44, %[ng], 0x733000\n\t" \
    "s_add_u32 s44, s44, 0x2400000\n\t" \
    "v_cmp_ne_u32 s[58:59], %[ng], v255\n\t" \
    "s_nop 1\n\t" \
    "s_mov_b64 exec, s[58:59]\n\t" \
    "buffer_store_dwordx2 v[216:217], v254, %[rs], s44 offen sc1\n\t" \
    "s_mov_b64 exec, -1\n\t" \
    "s_lshr_b32 s48, %[wave], 2\n\t" \
    "s_mul_i32 s48, s48, 0x3000\n\t" \
    "s_add_u32 s48, s48, %[pi]\n\t" \
    "s_mov_b32 s43, 0\n\t" \
    "s_add_u32 s46, s43, s48\n\t" \
    "s_add_u32 s47, s46, 0x0\n\t" \
    "buffer_load_dwordx4 v[208:211], v253, %[rs], s47 offen sc1\n\t" \
    "s_add_u32 s47, s46, 0x8000\n\t" \
    "buffer_load_dwordx4 v[212:215], v253, %[rs], s47 offen sc1\n\t" \
    "s_cmp_gt_u32 %[wave], 3\n\t" \
    "s_cbranch_scc1 .Lw13_%=\n\t" \
    "s_add_u32 s47, s46, 0x10000\n\t" \
    "buffer_load_dwordx4 v[216:219], v253, %[rs], s47 offen sc1\n\t" \
    ".Lw13_%=:\n\t" \
    "s_waitcnt vmcnt(0)\n\t" \
    "v_lshrrev_b32 v82, 1, v253\n\t" \
    ".Lw14_%=:\n\t" \
    "s_add_i32 s44, s40, s60\n\t" \
    "s_max_i32 s44, s44, 0\n\t" \
    "s_lshr_b32 s45, s44, 1\n\t" \
    "s_and_b32 s44, s44, 1\n\t" \
    "s_lshl_b32 s44, s44, 12\n\t" \
    "s_mul_i32 s49, s45, 0x14000\n\t" \
    "s_add_u32 s49, s49, s44\n\t" \
    "s_lshr_b32 s45, s45, 4\n\t" \
    "s_mul_i32 s45, s45, 0xc2000\n\t" \
    "s_add_u32 s49, s49, s45\n\t" \
    "s_add_i32 s44, s40, s61\n\t" \
    "s_max_i32 s44, s44, 0\n\t" \
    "s_lshr_b32 s45, s44, 1\n\t" \
    "s_and_b32 s44, s44, 1\n\t" \
    "s_lshl_b32 s44, s44, 12\n\t" \
    "s_mul_i32 s50, s45, 0x14000\n\t" \
    "s_add_u32 s50, s50, s44\n\t" \
    "s_lshr_b32 s45, s45, 4\n\t" \
    "s_mul_i32 s45, s45, 0xc2000\n\t" \
    "s_add_u32 s50, s50, s45\n\t" \
    "s_sub_u32 s50, s50, s49\n\t" \
    "s_add_u32 s49, s49, 0x2400000\n\t" \
    "v_mad_u32_u24 v80, v255, s50, v254\n\t" \
    "s_lshr_b32 s44, s40, 6\n\t" \
    "s_lshl_b32 s44, s44, 14\n\t" \
    "s_and_b32 s45, s40, 63\n\t" \
    "s_lshl_b32 s45, s45, 7\n\t" \
    "s_add_u32 s44, s44, s45\n\t" \
    "s_add_u32 s50, s44, 0x4d0000\n\t" \
    "s_add_i32 s44, s40, 1\n\t" \
    "s_max_i32 s44, s44, 0\n\t" \
    "s_lshr_b32 s45, s44, 1\n\t" \
    "s_and_b32 s44, s44, 1\n\t" \
    "s_lshl_b32 s44, s44, 12\n\t" \
    "s_mul_i32 s43, s45, 0x14000\n\t" \
    "s_add_u32 s43, s43, s44\n\t" \
    "s_lshr_b32 s45, s45, 4\n\t" \
    "s_mul_i32 s45, s45, 0xc2000\n\t" \
    "s_add_u32 s43, s43, s45\n\t" \
    LST0 \
    LJIT \
    "s_setprio 2\n\t" \
    "v_max_u32 v248, v208, v209\n\t" \
    "v_max3_u32 v248, v210, v211, v248\n\t" \
    "v_max3_u32 v248, v212, v213, v248\n\t" \
    "v_max3_u32 v248, v214, v215, v248\n\t" \
    "v_max3_u32 v248, v216, v217, v248\n\t" \
    "v_max3_u32 v248, v218, v219, v248\n\t" \
    "v_cmp_eq_u32 vcc, -1, v248\n\t" \
    "s_cbranch_vccz .Lw16_%=\n\t" \
    "s_and_b32 s44, s40, 1\n\t" \
    "s_cmp_eq_u32 s44, 1\n\t" \
    "s_cbranch_scc1 .Lw15_%=\n\t" \
    "s_cmp_gt_u32 s40, 123\n\t" \
    "s_cbranch_scc1 .Lw15_%=\n\t" \
    "s_sleep 10\n\t" \
    ".Lw15_%=:\n\t" \
    "v_max_u32 v248, v208, v209\n\t" \
    "v_max3_u32 v248, v210, v211, v248\n\t" \
    "v_cmp_eq_u32 vcc, -1, v248\n\t" \
    "s_cbranch_vccz .Lw18_%=\n\t" \
    "s_add_u32 s47, s46, 0x0\n\t" \
    "buffer_load_dwordx4 v[208:211], v253, %[rs], s47 offen sc1\n\t" \
    ".Lw18_%=:\n\t" \
    "v_max_u32 v248, v212, v213\n\t" \
    "v_max3_u32 v248, v214, v215, v248\n\t" \
    "v_cmp_eq_u32 vcc, -1, v248\n\t" \
    "s_cbranch_vccz .Lw19_%=\n\t" \
    "s_add_u32 s47, s46, 0x8000\n\t" \
    "buffer_load_dwordx4 v[212:215], v253, %[rs], s47 offen sc1\n\t" \
    ".Lw19_%=:\n\t" \
    "v_max_u32 v248, v216, v217\n\t" \
    "v_max3_u32 v248, v218, v219, v248\n\t" \
    "v_cmp_eq_u32 vcc, -1, v248\n\t" \
    "s_cbranch_vccz .Lw110_%=\n\t" \
    "s_add_u32 s47, s46, 0x10000\n\t" \
    "buffer_load_dwordx4 v[216:219], v253, %[rs], s47 offen sc1\n\t" \
    ".Lw110_%=:\n\t" \
    "s_waitcnt vmcnt(0)\n\t" \
    "v_max_u32 v248, v208, v209\n\t" \
    "v_max3_u32 v248, v210, v211, v248\n\t" \
    "v_max3_u32 v248, v212, v213, v248\n\t" \
    "v_max3_u32 v248, v214, v215, v248\n\t" \
    "v_max3_u32 v248, v216, v217, v248\n\t" \
    "v_max3_u32 v248, v218, v219, v248\n\t" \
    "v_cmp_eq_u32 vcc, -1, v248\n\t" \
    "s_cbranch_vccz .Lw16_%=\n\t" \
    "s_cmp_eq_u32 s41, 0\n\t" \
    "s_cbranch_scc1 .Lw16_%=\n\t" \
    "s_sub_u32 s41, s41, 1\n\t" \
    "s_branch .Lw15_%=\n\t" \
    ".Lw16_%=:\n\t" \
    LST1 \
    LJIT \
    "ds_write_b128 v250, v[208:211] offset:0\n\t" \
    "v_mfma_f32_16x16x32_bf16 v[236:239], v[208:211], v[208:211], 0\n\t" \
    "v_mfma_f32_16x16x32_bf16 v[232:235], v[228:231], v[208:211], 0\n\t" \
    "ds_write_b128 v250, v[212:215] offset:8192\n\t" \
    "v_mfma_f32_16x16x32_bf16 v[236:239], v[212:215], v[212:215], v[236:239]\n\t" \
    "v_mfma_f32_16x16x32_bf16 v[232:235], v[228:231], v[212:215], v[232:235]\n\t" \
    "s_cmp_gt_u32 %[wave], 3\n\t" \
    "s_cbranch_scc1 .Lw111_%=\n\t" \
    "ds_write_b128 v250, v[216:219] offset:16384\n\t" \
    ".Lw111_%=:\n\t" \
    "v_mfma_f32_16x16x32_bf16 v[236:239], v[216:219], v[216:219], v[236:239]\n\t" \
    "v_mfma_f32_16x16x32_bf16 v[232:235], v[228:231], v[216:219], v[232:235]\n\t" \
    "s_add_u32 s46, s43, s48\n\t" \
    "s_nop 4\n\t" \
    "v_cndmask_b32 v236, v236, v237, s[52:53]\n\t" \
    "v_cndmask_b32 v236, v236, v238, s[54:55]\n\t" \
    "v_cndmask_b32 v236, v236, v239, s[56:57]\n\t" \
    "s_mov_b32 exec_lo, 0xf0000f\n\t" \
    "s_mov_b32 exec_hi, 0xf0000f00\n\t" \
    "ds_write2_b32 v252, v232, v236 offset1:1\n\t" \
    "s_mov_b64 exec, -1\n\t" \
    "s_waitcnt lgkmcnt(0)\n\t" \
    "s_barrier \n\t" \
    "s_setprio 0\n\t" \
    LST2 \
    LJIT \
    "s_cmp_lg_u32 %[wave], 4\n\t" \
    "s_cbranch_scc1 .Lw112_%=\n\t" \
    "s_setprio 1\n\t" \
    "ds_read_b128 v[232:235], v251 offset:0\n\t" \
    "ds_read_b128 v[236:239], v251 offset:16\n\t" \
    "ds_read_b128 v[240:243], v251 offset:32\n\t" \
    "ds_read_b128 v[244:247], v251 offset:48\n\t" \
    "s_waitcnt lgkmcnt(0)\n\t" \
    "v_add_f32 v232, v232, v240\n\t" \
    "v_add_f32 v233, v233, v241\n\t" \
    "v_add_f32 v234, v234, v242\n\t" \
    "v_add_f32 v235, v235, v243\n\t" \
    "v_add_f32 v236, v236, v244\n\t" \
    "v_add_f32 v237, v237, v245\n\t" \
    "v_add_f32 v238, v238, v246\n\t" \
    "v_add_f32 v239, v239, v247\n\t" \
    "v_add_f32 v232, v232, v236\n\t" \
    "v_add_f32 v233, v233, v237\n\t" \
    "v_add_f32 v234, v234, v238\n\t" \
    "v_add_f32 v235, v235, v239\n\t" \
    "v_add_f32 v232, v232, v234\n\t" \
    "v_add_f32 v233, v233, v235\n\t" \
    "v_mul_f32 v232, 0x3acccccd, v232\n\t" \
    "v_mul_f32 v233, 0x3acccccd, v233\n\t" \
    "v_fma_f32 v233, -v232, v232, v233\n\t" \
    "v_mov_b32 v234, v232\n\t" \
    "v_max_f32 v233, 0, v233\n\t" \
    "v_cvt_pk_bf16_f32 v234, v234, v234\n\t" \
    "v_and_b32 v234, 0xffff0000, v234\n\t" \
    "v_mov_b32 v236, v233\n\t" \
    "v_cvt_pk_bf16_f32 v236, v236, v236\n\t" \
    "v_and_b32 v236, 0xffff0000, v236\n\t" \
    "v_add_f32 v236, 0x3727c5ac, v236\n\t" \
    "v_cvt_pk_bf16_f32 v236, v236, v236\n\t" \
    "v_and_b32 v236, 0xffff0000, v236\n\t" \
    "v_sqrt_f32 v236, v236\n\t" \
    "s_nop 0\n\t" \
    "v_cvt_pk_bf16_f32 v236, v236, v236\n\t" \
    "v_and_b32 v236, 0xffff0000, v236\n\t" \
    "v_rcp_f32 v235, v236\n\t" \
    "s_nop 0\n\t" \
    "v_sub_f32 v232, v232, v234\n\t" \
    "v_mul_f32 v233, v233, v235\n\t" \
    "v_mul_f32 v232, v232, v235\n\t" \
    "v_mul_f32 v233, v233, v235\n\t" \
    "v_cvt_pk_bf16_f32 v232, v232, v232\n\t" \
    "v_and_b32 v232, 0xffff0000, v232\n\t" \
    "v_cvt_pk_bf16_f32 v233, v233, v233\n\t" \
    "v_and_b32 v233, 0xffff0000, v233\n\t" \
    "v_add_f32 v233, 0x3727c5ac, v233\n\t" \
    "v_cvt_pk_bf16_f32 v233, v233, v233\n\t" \
    "v_and_b32 v233, 0xffff0000, v233\n\t" \
    "v_sqrt_f32 v233, v233\n\t" \
    "s_nop 0\n\t" \
    "v_cvt_pk_bf16_f32 v233, v233, v233\n\t" \
    "v_and_b32 v233, 0xffff0000, v233\n\t" \
    "v_rcp_f32 v233, v233\n\t" \
    "v_fma_f32 v232, v232, v236, v234\n\t" \
    "v_mul_f32 v237, v235, v233\n\t" \
    "v_mul_f32 v238, -v232, v237\n\t" \
    "v_min_u32 v234, -2, v234\n\t" \
    "v_min_u32 v235, -2, v235\n\t" \
    "v_mov_b32 v239, s40\n\t" \
    "ds_write2_b32 v251, v237, v238 offset0:16 offset1:17\n\t" \
    "ds_write_b32 v251, v239 offset:72\n\t" \
    "s_setprio 0\n\t" \
    "s_cmp_eq_u32 %[ss], 0\n\t" \
    "s_cbranch_scc1 .Lw113_%=\n\t" \
    "s_mov_b64 exec, 0xffff\n\t" \
    "buffer_store_dwordx2 v[234:235], v82, %[rs], s50 offen sc1\n\t" \
    "s_mov_b64 exec, -1\n\t" \
    ".Lw113_%=:\n\t" \
    ".Lw112_%=:\n\t" \
    "ds_read_b128 v[176:179], v249 offset:0\n\t" \
    "ds_read_b128 v[180:183], v249 offset:1024\n\t" \
    "ds_read_b128 v[184:187], v249 offset:2048\n\t" \
    "ds_read_b128 v[188:191], v249 offset:3072\n\t" \
    "ds_read_b128 v[192:195], v249 offset:4096\n\t" \
    "ds_read_b128 v[196:199], v249 offset:5120\n\t" \
    "ds_read_b128 v[200:203], v249 offset:6144\n\t" \
    "s_waitcnt lgkmcnt(6)\n\t" \
    "v_mfma_f32_16x16x32_bf16 v[168:171], v[0:3], v[176:179], 0\n\t" \
    "ds_read_b128 v[204:207], v249 offset:7168\n\t" \
    "s_waitcnt lgkmcnt(6)\n\t" \
    "v_mfma_f32_16x16x32_bf16 v[168:171], v[4:7], v[180:183], v[168:171]\n\t" \
    "ds_read_b128 v[176:179], v249 offset:8192\n\t" \
    "s_waitcnt lgkmcnt(6)\n\t" \
    "v_mfma_f32_16x16x32_bf16 v[168:171], v[8:11], v[184:187], v[168:171]\n\t" \
    "ds_read_b128 v[180:183], v249 offset:9216\n\t" \
    "s_waitcnt lgkmcnt(6)\n\t" \
    "v_mfma_f32_16x16x32_bf16 v[168:171], v[12:15], v[188:191], v[168:171]\n\t" \
    "ds_read_b128 v[184:187], v249 offset:10240\n\t" \
    "s_add_u32 s47, s46, 0x0\n\t" \
    "buffer_load_dwordx4 v[208:211], v253, %[rs], s47 offen sc1\n\t" \
    "s_add_u32 s47, s46, 0x8000\n\t" \
    "buffer_load_dwordx4 v[212:215], v253, %[rs], s47 offen sc1\n\t" \
    "s_cmp_gt_u32 %[wave], 3\n\t" \
    "s_cbranch_scc1 .Lw114_%=\n\t" \
    "s_add_u32 s47, s46, 0x10000\n\t" \
    "buffer_load_dwordx4 v[216:219], v253, %[rs], s47 offen sc1\n\t" \
    ".Lw114_%=:\n\t" \
    "s_waitcnt lgkmcnt(6)\n\t" \
    "v_mfma_f32_16x16x32_bf16 v[168:171], v[16:19], v[192:195], v[168:171]\n\t" \
    "ds_read_b128 v[188:191], v249 offset:11264\n\t" \
    "s_waitcnt lgkmcnt(6)\n\t" \
    "v_mfma_f32_16x16x32_bf16 v[168:171], v[20:23], v[196:199], v[168:171]\n\t" \
    "ds_read_b128 v[192:195], v249 offset:12288\n\t" \
    "s_waitcnt lgkmcnt(6)\n\t" \
    "v_mfma_f32_16x16x32_bf16 v[168:171], v[24:27], v[200:203], v[168:171]\n\t" \
    "ds_read_b128 v[196:199], v249 offset:13312\n\t" \
    "s_waitcnt lgkmcnt(6)\n\t" \
    "v_mfma_f32_16x16x32_bf16 v[168:171], v[28:31], v[204:207], v[168:171]\n\t" \
    "ds_read_b128 v[200:203], v249 offset:14336\n\t" \
    "s_waitcnt lgkmcnt(6)\n\t" \
    "v_mfma_f32_16x16x32_bf16 v[168:171], v[32:35], v[176:179], v[168:171]\n\t" \
    "ds_read_b128 v[204:207], v249 offset:15360\n\t" \
    "s_waitcnt lgkmcnt(6)\n\t" \
    "v_mfma_f32_16x16x32_bf16 v[168:171], v[36:39], v[180:183], v[168:171]\n\t" \
    "ds_read_b128 v[176:179], v249 offset:16384\n\t" \
    "s_waitcnt lgkmcnt(6)\n\t" \
    "v_mfma_f32_16x16x32_bf16 v[168:171], v[40:43], v[184:187], v[168:171]\n\t" \
    "ds_read_b128 v[180:183], v249 offset:17408\n\t" \
    "s_waitcnt lgkmcnt(6)\n\t" \
    "v_mfma_f32_16x16x32_bf16 v[168:171], v[44:47], v[188:191], v[168:171]\n\t" \
    "ds_read_b128 v[184:187], v249 offset:18432\n\t" \
    "s_waitcnt lgkmcnt(6)\n\t" \
    "v_mfma_f32_16x16x32_bf16 v[168:171], v[48:51], v[192:195], v[168:171]\n\t" \
    "ds_read_b128 v[188:191], v249 offset:19456\n\t" \
    "s_waitcnt lgkmcnt(6)\n\t" \
    "v_mfma_f32_16x16x32_bf16 v[168:171], v[52:55], v[196:199], v[168:171]\n\t" \
    "ds_read_b32 v236, v251 offset:72\n\t" \
    "ds_read_b64 v[238:239], v251 offset:64\n\t" \
    "s_waitcnt lgkmcnt(7)\n\t" \
    "v_mfma_f32_16x16x32_bf16 v[168:171], v[56:59], v[200:203], v[168:171]\n\t" \
    "s_waitcnt lgkmcnt(6)\n\t" \
    "v_mfma_f32_16x16x32_bf16 v[168:171], v[60:63], v[204:207], v[168:171]\n\t" \
    "s_waitcnt lgkmcnt(5)\n\t" \
    "v_mfma_f32_16x16x32_bf16 v[168:171], v[64:67], v[176:179], v[168:171]\n\t" \
    "s_waitcnt lgkmcnt(4)\n\t" \
    "v_mfma_f32_16x16x32_bf16 v[168:171], v[68:71], v[180:183], v[168:171]\n\t" \
    ".Lw115_%=:\n\t" \
    "s_waitcnt lgkmcnt(0)\n\t" \
    "v_cmp_ne_u32 vcc, s40, v236\n\t" \
    "s_cbranch_vccz .Lw116_%=\n\t" \
    "ds_read_b32 v236, v251 offset:72\n\t" \
    "ds_read_b64 v[238:239], v251 offset:64\n\t" \
    "s_branch .Lw115_%=\n\t" \
    ".Lw116_%=:\n\t" \
    "s_waitcnt lgkmcnt(0)\n\t" \
    "v_mul_f32 v240, v160, v239\n\t" \
    "v_mul_f32 v241, v161, v239\n\t" \
    "v_mfma_f32_16x16x32_bf16 v[168:171], v[72:75], v[184:187], v[168:171]\n\t" \
    "v_mul_f32 v242, v162, v239\n\t" \
    "v_mul_f32 v243, v163, v239\n\t" \
    "v_mfma_f32_16x16x32_bf16 v[168:171], v[76:79], v[188:191], v[168:171]\n\t" \
    "v_mov_b32 v237, 0xc0135761\n\t" \
    LST3 \
    LJIT \
    "s_nop 6\n\t" \
    "v_fma_f32 v168, v168, v238, v240\n\t" \
    "v_fma_f32 v169, v169, v238, v241\n\t" \
    "v_fma_f32 v170, v170, v238, v242\n\t" \
    "v_fma_f32 v171, v171, v238, v243\n\t" \
    "v_mul_f32 v176, v168, v168\n\t" \
    "v_mul_f32 v177, v169, v169\n\t" \
    "v_mul_f32 v178, v170, v170\n\t" \
    "v_mul_f32 v179, v171, v171\n\t" \
    "v_fmamk_f32 v176, v176, 0xbdd2d3e7, v237\n\t" \
    "v_fmamk_f32 v177, v177, 0xbdd2d3e7, v237\n\t" \
    "v_fmamk_f32 v178, v178, 0xbdd2d3e7, v237\n\t" \
    "v_fmamk_f32 v179, v179, 0xbdd2d3e7, v237\n\t" \
    "v_mul_f32 v176, v168, v176\n\t" \
    "v_mul_f32 v177, v169, v177\n\t" \
    "v_mul_f32 v178, v170, v178\n\t" \
    "v_mul_f32 v179, v171, v179\n\t" \
    "v_exp_f32 v176, v176\n\t" \
    "v_exp_f32 v177, v177\n\t" \
    "v_exp_f32 v178, v178\n\t" \
    "v_exp_f32 v179, v179\n\t" \
    "v_add_f32 v176, 1.0, v176\n\t" \
    "v_add_f32 v177, 1.0, v177\n\t" \
    "v_add_f32 v178, 1.0, v178\n\t" \
    "v_add_f32 v179, 1.0, v179\n\t" \
    "v_rcp_f32 v176, v176\n\t" \
    "v_rcp_f32 v177, v177\n\t" \
    "v_rcp_f32 v178, v178\n\t" \
    "v_rcp_f32 v179, v179\n\t" \
    "v_mul_f32 v168, v168, v176\n\t" \
    "v_mul_f32 v169, v169, v177\n\t" \
    "v_mul_f32 v170, v170, v178\n\t" \
    "v_mul_f32 v171, v171, v179\n\t" \
    "s_waitcnt vmcnt(0)\n\t" \
    "v_cvt_pk_bf16_f32 v184, v168, v169\n\t" \
    "v_cvt_pk_bf16_f32 v185, v170, v171\n\t" \
    "v_min_u32 v184, -2, v184\n\t" \
    "v_min_u32 v185, -2, v185\n\t" \
    "s_cmp_eq_u32 s40, s42\n\t" \
    "s_cselect_b64 s[44:45], s[58:59], 0\n\t" \
    "s_andn2_b64 exec, -1, s[44:45]\n\t" \
    "buffer_store_dwordx2 v[184:185], v80, %[rs], s49 offen sc1\n\t" \
    "s_mov_b64 exec, -1\n\t" \
    LST4 \
    LJIT \
    "s_cmp_ge_u32 s40, 122\n\t" \
    "s_cbranch_scc1 .Lw117_%=\n\t" \
    "s_add_i32 s44, s40, s62\n\t" \
    "s_max_i32 s44, s44, 0\n\t" \
    "s_lshr_b32 s45, s44, 1\n\t" \
    "s_and_b32 s44, s44, 1\n\t" \
    "s_lshl_b32 s44, s44, 12\n\t" \
    "s_mul_i32 s51, s45, 0x14000\n\t" \
    "s_add_u32 s51, s51, s44\n\t" \
    "s_lshr_b32 s45, s45, 4\n\t" \
    "s_mul_i32 s45, s45, 0xc2000\n\t" \
    "s_add_u32 s51, s51, s45\n\t" \
    "s_add_i32 s44, s40, s63\n\t" \
    "s_max_i32 s44, s44, 0\n\t" \
    "s_lshr_b32 s45, s44, 1\n\t" \
    "s_and_b32 s44, s44, 1\n\t" \
    "s_lshl_b32 s44, s44, 12\n\t" \
    "s_mul_i32 s50, s45, 0x14000\n\t" \
    "s_add_u32 s50, s50, s44\n\t" \
    "s_lshr_b32 s45, s45, 4\n\t" \
    "s_mul_i32 s45, s45, 0xc2000\n\t" \
    "s_add_u32 s50, s50, s45\n\t" \
    "s_sub_u32 s50, s50, s51\n\t" \
    "s_add_u32 s51, s51, 0x2400000\n\t" \
    "v_mad_u32_u24 v81, v255, s50, v254\n\t" \
    "v_mov_b64 v[188:189], -1\n\t" \
    "s_add_u32 s44, s40, 6\n\t" \
    "s_cmp_eq_u32 s44, s42\n\t" \
    "s_cselect_b64 s[44:45], s[58:59], 0\n\t" \
    "s_andn2_b64 exec, -1, s[44:45]\n\t" \
    "buffer_store_dwordx2 v[188:189], v81, %[rs], s51 offen sc1\n\t" \
    "s_mov_b64 exec, -1\n\t" \
    ".Lw117_%=:\n\t" \
    "v_xor_b32 v249, 0x8000, v249\n\t" \
    "v_xor_b32 v250, 0x8000, v250\n\t" \
    "v_xor_b32 v251, 0x800, v251\n\t" \
    "v_xor_b32 v252, 0x800, v252\n\t" \
    LFLUSHW \
    "s_add_u32 s40, s40, 1\n\t" \
    "s_cmp_lg_u32 s40, 128\n\t" \
    "s_cbranch_scc1 .Lw14_%=\n\t" \
    "s_branch .Lend%=\n\t" \
    ".Lend%=:\n\t" \
    "s_waitcnt vmcnt(0) lgkmcnt(0)\n\t" \
    "s_mov_b32 %[bud], s41\n\t"
#define ASM_W2_LOOP \
    "s_mov_b32 s40, 0\n\t" \
    "s_mov_b32 s41, 0x20000\n\t" \
    LSTINIT \
    "s_add_u32 m0, %[wm], 0x0\n\t" \
    "s_add_u32 s44, %[wo], 0x0\n\t" \
    "buffer_load_dwordx4 v176, %[wr], s44 offen nt lds\n\t" \
    "s_add_u32 m0, %[wm], 0x2000\n\t" \
    "s_add_u32 s44, %[wo], 0xa000\n\t" \
    "buffer_load_dwordx4 v176, %[wr], s44 offen nt lds\n\t" \
    "s_add_u32 m0, %[wm], 0x4000\n\t" \
    "s_add_u32 s44, %[wo], 0x14000\n\t" \
    "buffer_load_dwordx4 v176, %[wr], s44 offen nt lds\n\t" \
    "s_add_u32 m0, %[wm], 0x6000\n\t" \
    "s_add_u32 s44, %[wo], 0x1e000\n\t" \
    "buffer_load_dwordx4 v176, %[wr], s44 offen nt lds\n\t" \
    "s_add_u32 m0, %[wm], 0x8000\n\t" \
    "s_add_u32 s44, %[wo], 0x28000\n\t" \
    "buffer_load_dwordx4 v176, %[wr], s44 offen nt lds\n\t" \
    "s_add_u32 m0, %[wm], 0xa000\n\t" \
    "s_add_u32 s44, %[wo], 0x32000\n\t" \
    "buffer_load_dwordx4 v176, %[wr], s44 offen nt lds\n\t" \
    "s_add_u32 m0, %[wm], 0xc000\n\t" \
    "s_add_u32 s44, %[wo], 0x3c000\n\t" \
    "buffer_load_dwordx4 v176, %[wr], s44 offen nt lds\n\t" \
    "s_add_u32 m0, %[wm], 0xe000\n\t" \
    "s_add_u32 s44, %[wo], 0x46000\n\t" \
    "buffer_load_dwordx4 v176, %[wr], s44 offen nt lds\n\t" \
    "s_add_u32 m0, %[wm], 0x10000\n\t" \
    "s_add_u32 s44, %[wo], 0x50000\n\t" \
    "buffer_load_dwordx4 v176, %[wr], s44 offen nt lds\n\t" \
    "s_add_u32 m0, %[wm], 0x12000\n\t" \
    "s_add_u32 s44, %[wo], 0x5a000\n\t" \
    "buffer_load_dwordx4 v176, %[wr], s44 offen nt lds\n\t" \
    "s_add_u32 m0, %[wm], 0x14000\n\t" \
    "s_add_u32 s44, %[wo], 0x64000\n\t" \
    "buffer_load_dwordx4 v176, %[wr], s44 offen nt lds\n\t" \
    "s_add_u32 m0, %[wm], 0x16000\n\t" \
    "s_add_u32 s44, %[wo], 0x6e000\n\t" \
    "buffer_load_dwordx4 v176, %[wr], s44 offen nt lds\n\t" \
    "s_add_u32 m0, %[wm], 0x18000\n\t" \
    "s_add_u32 s44, %[wo], 0x78000\n\t" \
    "buffer_load_dwordx4 v176, %[wr], s44 offen nt lds\n\t" \
    "s_add_u32 m0, %[wm], 0x1a000\n\t" \
    "s_add_u32 s44, %[wo], 0x82000\n\t" \
    "buffer_load_dwordx4 v176, %[wr], s44 offen nt lds\n\t" \
    "s_add_u32 m0, %[wm], 0x1c000\n\t" \
    "s_add_u32 s44, %[wo], 0x8c000\n\t" \
    "buffer_load_dwordx4 v176, %[wr], s44 offen nt lds\n\t" \
    "s_add_u32 m0, %[wm], 0x1e000\n\t" \
    "s_add_u32 s44, %[wo], 0x96000\n\t" \
    "buffer_load_dwordx4 v176, %[wr], s44 offen nt lds\n\t" \
    "s_add_u32 m0, %[wm], 0x20000\n\t" \
    "s_add_u32 s44, %[wo], 0xa0000\n\t" \
    "buffer_load_dwordx4 v176, %[wr], s44 offen nt lds\n\t" \
    "s_add_u32 m0, %[wm], 0x22000\n\t" \
    "s_add_u32 s44, %[wo], 0xaa000\n\t" \
    "buffer_load_dwordx4 v176, %[wr], s44 offen nt lds\n\t" \
    "s_add_u32 m0, %[wm], 0x24000\n\t" \
    "s_add_u32 s44, %[wo], 0xb4000\n\t" \
    "buffer_load_dwordx4 v176, %[wr], s44 offen nt lds\n\t" \
    "s_add_u32 m0, %[wm], 0x26000\n\t" \
    "s_add_u32 s44, %[wo], 0xbe000\n\t" \
    "buffer_load_dwordx4 v176, %[wr], s44 offen nt lds\n\t" \
    "s_waitcnt vmcnt(0) lgkmcnt(0)\n\t" \
    "s_barrier \n\t" \
    "v_add_u32 v178, 0x0, v177\n\t" \
    "ds_read_b64_tr_b16 v[0:1], v178 offset:0\n\t" \
    "ds_read_b64_tr_b16 v[2:3], v178 offset:1024\n\t" \
    "ds_read_b64_tr_b16 v[4:5], v178 offset:8192\n\t" \
    "ds_read_b64_tr_b16 v[6:7], v178 offset:9216\n\t" \
    "ds_read_b64_tr_b16 v[8:9], v178 offset:16384\n\t" \
    "ds_read_b64_tr_b16 v[10:11], v178 offset:17408\n\t" \
    "ds_read_b64_tr_b16 v[12:13], v178 offset:24576\n\t" \
    "ds_read_b64_tr_b16 v[14:15], v178 offset:25600\n\t" \
    "v_add_u32 v178, 0x8000, v177\n\t" \
    "ds_read_b64_tr_b16 v[16:17], v178 offset:0\n\t" \
    "ds_read_b64_tr_b16 v[18:19], v178 offset:1024\n\t" \
    "ds_read_b64_tr_b16 v[20:21], v178 offset:8192\n\t" \
    "ds_read_b64_tr_b16 v[22:23], v178 offset:9216\n\t" \
    "ds_read_b64_tr_b16 v[24:25], v178 offset:16384\n\t" \
    "ds_read_b64_tr_b16 v[26:27], v178 offset:17408\n\t" \
    "ds_read_b64_tr_b16 v[28:29], v178 offset:24576\n\t" \
    "ds_read_b64_tr_b16 v[30:31], v178 offset:25600\n\t" \
    "v_add_u32 v178, 0x10000, v177\n\t" \
    "ds_read_b64_tr_b16 v[32:33], v178 offset:0\n\t" \
    "ds_read_b64_tr_b16 v[34:35], v178 offset:1024\n\t" \
    "ds_read_b64_tr_b16 v[36:37], v178 offset:8192\n\t" \
    "ds_read_b64_tr_b16 v[38:39], v178 offset:9216\n\t" \
    "ds_read_b64_tr_b16 v[40:41], v178 offset:16384\n\t" \
    "ds_read_b64_tr_b16 v[42:43], v178 offset:17408\n\t" \
    "ds_read_b64_tr_b16 v[44:45], v178 offset:24576\n\t" \
    "ds_read_b64_tr_b16 v[46:47], v178 offset:25600\n\t" \
    "v_add_u32 v178, 0x18000, v177\n\t" \
    "ds_read_b64_tr_b16 v[48:49], v178 offset:0\n\t" \
    "ds_read_b64_tr_b16 v[50:51], v178 offset:1024\n\t" \
    "ds_read_b64_tr_b16 v[52:53], v178 offset:8192\n\t" \
    "ds_read_b64_tr_b16 v[54:55], v178 offset:9216\n\t" \
    "ds_read_b64_tr_b16 v[56:57], v178 offset:16384\n\t" \
    "ds_read_b64_tr_b16 v[58:59], v178 offset:17408\n\t" \
    "ds_read_b64_tr_b16 v[60:61], v178 offset:24576\n\t" \
    "ds_read_b64_tr_b16 v[62:63], v178 offset:25600\n\t" \
    "v_add_u32 v178, 0x20000, v177\n\t" \
    "ds_read_b64_tr_b16 v[64:65], v178 offset:0\n\t" \
    "ds_read_b64_tr_b16 v[66:67], v178 offset:1024\n\t" \
    "ds_read_b64_tr_b16 v[68:69], v178 offset:8192\n\t" \
    "ds_read_b64_tr_b16 v[70:71], v178 offset:9216\n\t" \
    "ds_read_b64_tr_b16 v[72:73], v178 offset:16384\n\t" \
    "ds_read_b64_tr_b16 v[74:75], v178 offset:17408\n\t" \
    "ds_read_b64_tr_b16 v[76:77], v178 offset:24576\n\t" \
    "ds_read_b64_tr_b16 v[78:79], v178 offset:25600\n\t" \
    "v_mov_b32 v228, 0x3f803f80\n\t" \
    "v_mov_b32 v229, 0x3f803f80\n\t" \
    "v_mov_b32 v230, 0x3f803f80\n\t" \
    "v_mov_b32 v231, 0x3f803f80\n\t" \
    "v_mov_b64 v[216:217], 0\n\t" \
    "v_mov_b32 v219, 0\n\t" \
    "v_bfe_u32 v255, v253, 2, 5\n\t" \
    "v_add_u32 v255, 0x7000, v255\n\t" \
    "v_mov_b32 v218, 0\n\t" \
    "s_waitcnt lgkmcnt(0)\n\t" \
    "s_barrier \n\t" \
    "ds_write_b32 v255, v218\n\t" \
    "s_waitcnt lgkmcnt(0)\n\t" \
    "s_barrier \n\t" \
    ".Lw21_%=:\n\t" \
    "v_mov_b32 v248, 0\n\t" \
    "buffer_load_dword v248, v248, %[rs], %[go] offen sc1\n\t" \
    "s_waitcnt vmcnt(0)\n\t" \
    "v_cmp_ne_u32 vcc, 0, v248\n\t" \
    "s_cbranch_vccnz .Lw22_%=\n\t" \
    "s_cmp_eq_u32 s41, 0\n\t" \
    "s_cbranch_scc1 .Lw22_%=\n\t" \
    "s_sub_u32 s41, s41, 1\n\t" \
    "s_sleep 16\n\t" \
    "s_branch .Lw21_%=\n\t" \
    ".Lw22_%=:\n\t" \
    "s_lshr_b32 s48, %[wave], 2\n\t" \
    "s_mul_i32 s48, s48, 0x3000\n\t" \
    "s_add_u32 s48, s48, %[pi]\n\t" \
    "s_mov_b32 s43, 0\n\t" \
    "s_add_u32 s46, s43, s48\n\t" \
    "s_add_u32 s47, s46, 0x0\n\t" \
    "buffer_load_dwordx4 v[208:211], v253, %[rs], s47 offen sc1\n\t" \
    "s_add_u32 s47, s46, 0x8000\n\t" \
    "buffer_load_dwordx4 v[212:215], v253, %[rs], s47 offen sc1\n\t" \
    "s_cmp_gt_u32 %[wave], 3\n\t" \
    "s_cbranch_scc1 .Lw23_%=\n\t" \
    "s_add_u32 s47, s46, 0x10000\n\t" \
    "buffer_load_dwordx4 v[216:219], v253, %[rs], s47 offen sc1\n\t" \
    ".Lw23_%=:\n\t" \
    "s_waitcnt vmcnt(0)\n\t" \
    ".Lw24_%=:\n\t" \
    "s_add_u32 s49, s43, 0x2c00000\n\t" \
    "s_add_u32 s50, s43, 0x1c00000\n\t" \
    "s_lshr_b32 s44, s40, 6\n\t" \
    "s_lshl_b32 s44, s44, 14\n\t" \
    "s_and_b32 s45, s40, 63\n\t" \
    "s_lshl_b32 s45, s45, 7\n\t" \
    "s_add_u32 s44, s44, s45\n\t" \
    "s_add_u32 s51, s44, 0x4d0000\n\t" \
    "s_add_i32 s44, s40, 1\n\t" \
    "s_max_i32 s44, s44, 0\n\t" \
    "s_lshr_b32 s45, s44, 1\n\t" \
    "s_and_b32 s44, s44, 1\n\t" \
    "s_lshl_b32 s44, s44, 12\n\t" \
    "s_mul_i32 s43, s45, 0x14000\n\t" \
    "s_add_u32 s43, s43, s44\n\t" \
    "s_lshr_b32 s45, s45, 4\n\t" \
    "s_mul_i32 s45, s45, 0xc2000\n\t" \
    "s_add_u32 s43, s43, s45\n\t" \
    LST0 \
    LJIT \
    "s_setprio 2\n\t" \
    "v_max_u32 v248, v208, v209\n\t" \
    "v_max3_u32 v248, v210, v211, v248\n\t" \
    "v_max3_u32 v248, v212, v213, v248\n\t" \
    "v_max3_u32 v248, v214, v215, v248\n\t" \
    "v_max3_u32 v248, v216, v217, v248\n\t" \
    "v_max3_u32 v248, v218, v219, v248\n\t" \
    "v_cmp_eq_u32 vcc, -1, v248\n\t" \
    "s_cbranch_vccz .Lw26_%=\n\t" \
    "s_and_b32 s44, s40, 1\n\t" \
    "s_cmp_eq_u32 s44, 0\n\t" \
    "s_cbranch_scc1 .Lw25_%=\n\t" \
    "s_cmp_gt_u32 s40, 123\n\t" \
    "s_cbranch_scc1 .Lw25_%=\n\t" \
    "s_sleep 6\n\t" \
    ".Lw25_%=:\n\t" \
    "v_max_u32 v248, v208, v209\n\t" \
    "v_max3_u32 v248, v210, v211, v248\n\t" \
    "v_cmp_eq_u32 vcc, -1, v248\n\t" \
    "s_cbranch_vccz .Lw28_%=\n\t" \
    "s_add_u32 s47, s46, 0x0\n\t" \
    "buffer_load_dwordx4 v[208:211], v253, %[rs], s47 offen sc1\n\t" \
    ".Lw28_%=:\n\t" \
    "v_max_u32 v248, v212, v213\n\t" \
    "v_max3_u32 v248, v214, v215, v248\n\t" \
    "v_cmp_eq_u32 vcc, -1, v248\n\t" \
    "s_cbranch_vccz .Lw29_%=\n\t" \
    "s_add_u32 s47, s46, 0x8000\n\t" \
    "buffer_load_dwordx4 v[212:215], v253, %[rs], s47 offen sc1\n\t" \
    ".Lw29_%=:\n\t" \
    "v_max_u32 v248, v216, v217\n\t" \
    "v_max3_u32 v248, v218, v219, v248\n\t" \
    "v_cmp_eq_u32 vcc, -1, v248\n\t" \
    "s_cbranch_vccz .Lw210_%=\n\t" \
    "s_add_u32 s47, s46, 0x10000\n\t" \
    "buffer_load_dwordx4 v[216:219], v253, %[rs], s47 offen sc1\n\t" \
    ".Lw210_%=:\n\t" \
    "s_waitcnt vmcnt(0)\n\t" \
    "v_max_u32 v248, v208, v209\n\t" \
    "v_max3_u32 v248, v210, v211, v248\n\t" \
    "v_max3_u32 v248, v212, v213, v248\n\t" \
    "v_max3_u32 v248, v214, v215, v248\n\t" \
    "v_max3_u32 v248, v216, v217, v248\n\t" \
    "v_max3_u32 v248, v218, v219, v248\n\t" \
    "v_cmp_eq_u32 vcc, -1, v248\n\t" \
    "s_cbranch_vccz .Lw26_%=\n\t" \
    "s_cmp_eq_u32 s41, 0\n\t" \
    "s_cbranch_scc1 .Lw26_%=\n\t" \
    "s_sub_u32 s41, s41, 1\n\t" \
    "s_branch .Lw25_%=\n\t" \
    ".Lw26_%=:\n\t" \
    LST1 \
    LJIT \
    "ds_write_b128 v250, v[208:211] offset:0\n\t" \
    "ds_write_b128 v250, v[212:215] offset:8192\n\t" \
    "s_cmp_gt_u32 %[wave], 3\n\t" \
    "s_cbranch_scc1 .Lw211_%=\n\t" \
    "ds_write_b128 v250, v[216:219] offset:16384\n\t" \
    ".Lw211_%=:\n\t" \
    "s_add_u32 s46, s43, s48\n\t" \
    "s_waitcnt lgkmcnt(0)\n\t" \
    "s_barrier \n\t" \
    "s_setprio 0\n\t" \
    LST2 \
    LJIT \
    "ds_read_b128 v[176:179], v249 offset:0\n\t" \
    "buffer_load_dwordx2 v[232:233], v254, %[rs], s50 offen sc1\n\t" \
    "buffer_load_dwordx2 v[234:235], v251, %[rs], s51 offen sc1\n\t" \
    "ds_read_b128 v[180:183], v249 offset:1024\n\t" \
    "ds_read_b128 v[184:187], v249 offset:2048\n\t" \
    "ds_read_b128 v[188:191], v249 offset:3072\n\t" \
    "ds_read_b128 v[192:195], v249 offset:4096\n\t" \
    "ds_read_b128 v[196:199], v249 offset:5120\n\t" \
    "ds_read_b128 v[200:203], v249 offset:6144\n\t" \
    "s_waitcnt lgkmcnt(6)\n\t" \
    "v_mfma_f32_16x16x32_bf16 v[168:171], v[0:3], v[176:179], 0\n\t" \
    "ds_read_b128 v[204:207], v249 offset:7168\n\t" \
    "s_waitcnt lgkmcnt(6)\n\t" \
    "v_mfma_f32_16x16x32_bf16 v[168:171], v[4:7], v[180:183], v[168:171]\n\t" \
    "ds_read_b128 v[176:179], v249 offset:8192\n\t" \
    "s_waitcnt lgkmcnt(6)\n\t" \
    "v_mfma_f32_16x16x32_bf16 v[168:171], v[8:11], v[184:187], v[168:171]\n\t" \
    "ds_read_b128 v[180:183], v249 offset:9216\n\t" \
    "s_waitcnt lgkmcnt(6)\n\t" \
    "v_mfma_f32_16x16x32_bf16 v[168:171], v[12:15], v[188:191], v[168:171]\n\t" \
    "ds_read_b128 v[184:187], v249 offset:10240\n\t" \
    "s_add_u32 s47, s46, 0x0\n\t" \
    "buffer_load_dwordx4 v[208:211], v253, %[rs], s47 offen sc1\n\t" \
    "s_add_u32 s47, s46, 0x8000\n\t" \
    "buffer_load_dwordx4 v[212:215], v253, %[rs], s47 offen sc1\n\t" \
    "s_cmp_gt_u32 %[wave], 3\n\t" \
    "s_cbranch_scc1 .Lw212_%=\n\t" \
    "s_add_u32 s47, s46, 0x10000\n\t" \
    "buffer_load_dwordx4 v[216:219], v253, %[rs], s47 offen sc1\n\t" \
    ".Lw212_%=:\n\t" \
    "s_waitcnt lgkmcnt(6)\n\t" \
    "v_mfma_f32_16x16x32_bf16 v[168:171], v[16:19], v[192:195], v[168:171]\n\t" \
    "ds_read_b128 v[188:191], v249 offset:11264\n\t" \
    "s_waitcnt lgkmcnt(6)\n\t" \
    "v_mfma_f32_16x16x32_bf16 v[168:171], v[20:23], v[196:199], v[168:171]\n\t" \
    "ds_read_b128 v[192:195], v249 offset:12288\n\t" \
    "s_waitcnt lgkmcnt(6)\n\t" \
    "v_mfma_f32_16x16x32_bf16 v[168:171], v[24:27], v[200:203], v[168:171]\n\t" \
    "ds_read_b128 v[196:199], v249 offset:13312\n\t" \
    "s_waitcnt lgkmcnt(6)\n\t" \
    "v_mfma_f32_16x16x32_bf16 v[168:171], v[28:31], v[204:207], v[168:171]\n\t" \
    "ds_read_b128 v[200:203], v249 offset:14336\n\t" \
    "s_waitcnt lgkmcnt(6)\n\t" \
    "v_mfma_f32_16x16x32_bf16 v[168:171], v[32:35], v[176:179], v[168:171]\n\t" \
    "ds_read_b128 v[204:207], v249 offset:15360\n\t" \
    "s_waitcnt lgkmcnt(6)\n\t" \
    "v_mfma_f32_16x16x32_bf16 v[168:171], v[36:39], v[180:183], v[168:171]\n\t" \
    "ds_read_b128 v[176:179], v249 offset:16384\n\t" \
    "s_waitcnt lgkmcnt(6)\n\t" \
    "v_mfma_f32_16x16x32_bf16 v[168:171], v[40:43], v[184:187], v[168:171]\n\t" \
    "ds_read_b128 v[180:183], v249 offset:17408\n\t" \
    "s_waitcnt lgkmcnt(6)\n\t" \
    "v_mfma_f32_16x16x32_bf16 v[168:171], v[44:47], v[188:191], v[168:171]\n\t" \
    "ds_read_b128 v[184:187], v249 offset:18432\n\t" \
    "s_waitcnt lgkmcnt(6)\n\t" \
    "v_mfma_f32_16x16x32_bf16 v[168:171], v[48:51], v[192:195], v[168:171]\n\t" \
    "ds_read_b128 v[188:191], v249 offset:19456\n\t" \
    "s_waitcnt lgkmcnt(6)\n\t" \
    "v_mfma_f32_16x16x32_bf16 v[168:171], v[52:55], v[196:199], v[168:171]\n\t" \
    "s_waitcnt lgkmcnt(5)\n\t" \
    "v_mfma_f32_16x16x32_bf16 v[168:171], v[56:59], v[200:203], v[168:171]\n\t" \
    "s_waitcnt lgkmcnt(4)\n\t" \
    "v_mfma_f32_16x16x32_bf16 v[168:171], v[60:63], v[204:207], v[168:171]\n\t" \
    "s_waitcnt lgkmcnt(3)\n\t" \
    "v_mfma_f32_16x16x32_bf16 v[168:171], v[64:67], v[176:179], v[168:171]\n\t" \
    "s_waitcnt lgkmcnt(2)\n\t" \
    "v_mfma_f32_16x16x32_bf16 v[168:171], v[68:71], v[180:183], v[168:171]\n\t" \
    "s_waitcnt lgkmcnt(1)\n\t" \
    "v_mfma_f32_16x16x32_bf16 v[168:171], v[72:75], v[184:187], v[168:171]\n\t" \
    "s_waitcnt lgkmcnt(0)\n\t" \
    "v_mfma_f32_16x16x32_bf16 v[168:171], v[76:79], v[188:191], v[168:171]\n\t" \
    LST3 \
    LJIT \
    "s_waitcnt vmcnt(0)\n\t" \
    ".Lw213_%=:\n\t" \
    "v_max_u32 v248, v234, v235\n\t" \
    "v_cmp_eq_u32 vcc, -1, v248\n\t" \
    "s_cbranch_vccz .Lw214_%=\n\t" \
    "s_cmp_eq_u32 s41, 0\n\t" \
    "s_cbranch_scc1 .Lw214_%=\n\t" \
    "s_sub_u32 s41, s41, 1\n\t" \
    "buffer_load_dwordx2 v[234:235], v251, %[rs], s51 offen sc1\n\t" \
    "s_waitcnt vmcnt(0)\n\t" \
    "s_branch .Lw213_%=\n\t" \
    ".Lw214_%=:\n\t" \
    "v_lshlrev_b32 v176, 16, v232\n\t" \
    "v_and_b32 v177, 0xffff0000, v232\n\t" \
    "v_lshlrev_b32 v178, 16, v233\n\t" \
    "v_and_b32 v179, 0xffff0000, v233\n\t" \
    "v_sub_f32 v176, v176, v234\n\t" \
    "v_sub_f32 v177, v177, v234\n\t" \
    "v_sub_f32 v178, v178, v234\n\t" \
    "v_sub_f32 v179, v179, v234\n\t" \
    "v_mul_f32 v176, v176, v235\n\t" \
    "v_mul_f32 v177, v177, v235\n\t" \
    "v_mul_f32 v178, v178, v235\n\t" \
    "v_mul_f32 v179, v179, v235\n\t" \
    "v_cvt_pk_bf16_f32 v180, v176, v177\n\t" \
    "v_cvt_pk_bf16_f32 v181, v178, v179\n\t" \
    "v_lshlrev_b32 v176, 16, v180\n\t" \
    "v_and_b32 v177, 0xffff0000, v180\n\t" \
    "v_lshlrev_b32 v178, 16, v181\n\t" \
    "v_and_b32 v179, 0xffff0000, v181\n\t" \
    "v_add_f32 v168, v176, v168\n\t" \
    "v_add_f32 v169, v177, v169\n\t" \
    "v_add_f32 v170, v178, v170\n\t" \
    "v_add_f32 v171, v179, v171\n\t" \
    "v_cvt_pk_bf16_f32 v184, v168, v169\n\t" \
    "v_cvt_pk_bf16_f32 v185, v170, v171\n\t" \
    "v_min_u32 v184, -2, v184\n\t" \
    "v_min_u32 v185, -2, v185\n\t" \
    "buffer_store_dwordx2 v[184:185], v254, %[rs], s49 offen sc1\n\t" \
    LST4 \
    LJIT \
    "s_cmp_ge_u32 s40, 122\n\t" \
    "s_cbranch_scc1 .Lw215_%=\n\t" \
    "s_add_i32 s44, s40, 6\n\t" \
    "s_max_i32 s44, s44, 0\n\t" \
    "s_lshr_b32 s45, s44, 1\n\t" \
    "s_and_b32 s44, s44, 1\n\t" \
    "s_lshl_b32 s44, s44, 12\n\t" \
    "s_mul_i32 s50, s45, 0x14000\n\t" \
    "s_add_u32 s50, s50, s44\n\t" \
    "s_lshr_b32 s45, s45, 4\n\t" \
    "s_mul_i32 s45, s45, 0xc2000\n\t" \
    "s_add_u32 s50, s50, s45\n\t" \
    "s_add_u32 s50, s50, 0x2c00000\n\t" \
    "v_mov_b64 v[188:189], -1\n\t" \
    "buffer_store_dwordx2 v[188:189], v254, %[rs], s50 offen sc1\n\t" \
    ".Lw215_%=:\n\t" \
    "v_xor_b32 v249, 0x8000, v249\n\t" \
    "v_xor_b32 v250, 0x8000, v250\n\t" \
    LFLUSHW \
    "s_add_u32 s40, s40, 1\n\t" \
    "s_cmp_lg_u32 s40, 128\n\t" \
    "s_cbranch_scc1 .Lw24_%=\n\t" \
    "s_branch .Lend%=\n\t" \
    ".Lend%=:\n\t" \
    "s_waitcnt vmcnt(0) lgkmcnt(0)\n\t" \
    "s_mov_b32 %[bud], s41\n\t"
#define ASM_W3_LOOP \
    "s_mov_b32 s40, 0\n\t" \
    "s_mov_b32 s41, 0x20000\n\t" \
    LSTINIT \
    "s_add_u32 m0, %[wm], 0x0\n\t" \
    "s_add_u32 s44, %[wo], 0x0\n\t" \
    "buffer_load_dwordx4 v176, %[wr], s44 offen nt lds\n\t" \
    "s_add_u32 m0, %[wm], 0x2000\n\t" \
    "s_add_u32 s44, %[wo], 0xc000\n\t" \
    "buffer_load_dwordx4 v176, %[wr], s44 offen nt lds\n\t" \
    "s_add_u32 m0, %[wm], 0x4000\n\t" \
    "s_add_u32 s44, %[wo], 0x18000\n\t" \
    "buffer_load_dwordx4 v176, %[wr], s44 offen nt lds\n\t" \
    "s_add_u32 m0, %[wm], 0x6000\n\t" \
    "s_add_u32 s44, %[wo], 0x24000\n\t" \
    "buffer_load_dwordx4 v176, %[wr], s44 offen nt lds\n\t" \
    "s_add_u32 m0, %[wm], 0x8000\n\t" \
    "s_add_u32 s44, %[wo], 0x30000\n\t" \
    "buffer_load_dwordx4 v176, %[wr], s44 offen nt lds\n\t" \
    "s_add_u32 m0, %[wm], 0xa000\n\t" \
    "s_add_u32 s44, %[wo], 0x3c000\n\t" \
    "buffer_load_dwordx4 v176, %[wr], s44 offen nt lds\n\t" \
    "s_add_u32 m0, %[wm], 0xc000\n\t" \
    "s_add_u32 s44, %[wo], 0x48000\n\t" \
    "buffer_load_dwordx4 v176, %[wr], s44 offen nt lds\n\t" \
    "s_add_u32 m0, %[wm], 0xe000\n\t" \
    "s_add_u32 s44, %[wo], 0x54000\n\t" \
    "buffer_load_dwordx4 v176, %[wr], s44 offen nt lds\n\t" \
    "s_add_u32 m0, %[wm], 0x10000\n\t" \
    "s_add_u32 s44, %[wo], 0x60000\n\t" \
    "buffer_load_dwordx4 v176, %[wr], s44 offen nt lds\n\t" \
    "s_add_u32 m0, %[wm], 0x12000\n\t" \
    "s_add_u32 s44, %[wo], 0x6c000\n\t" \
    "buffer_load_dwordx4 v176, %[wr], s44 offen nt lds\n\t" \
    "s_add_u32 m0, %[wm], 0x14000\n\t" \
    "s_add_u32 s44, %[wo], 0x78000\n\t" \
    "buffer_load_dwordx4 v176, %[wr], s44 offen nt lds\n\t" \
    "s_add_u32 m0, %[wm], 0x16000\n\t" \
    "s_add_u32 s44, %[wo], 0x84000\n\t" \
    "buffer_load_dwordx4 v176, %[wr], s44 offen nt lds\n\t" \
    "s_add_u32 m0, %[wm], 0x18000\n\t" \
    "s_add_u32 s44, %[wo], 0x90000\n\t" \
    "buffer_load_dwordx4 v176, %[wr], s44 offen nt lds\n\t" \
    "s_add_u32 m0, %[wm], 0x1a000\n\t" \
    "s_add_u32 s44, %[wo], 0x9c000\n\t" \
    "buffer_load_dwordx4 v176, %[wr], s44 offen nt lds\n\t" \
    "s_add_u32 m0, %[wm], 0x1c000\n\t" \
    "s_add_u32 s44, %[wo], 0xa8000\n\t" \
    "buffer_load_dwordx4 v176, %[wr], s44 offen nt lds\n\t" \
    "s_add_u32 m0, %[wm], 0x1e000\n\t" \
    "s_add_u32 s44, %[wo], 0xb4000\n\t" \
    "buffer_load_dwordx4 v176, %[wr], s44 offen nt lds\n\t" \
    "s_add_u32 m0, %[wm], 0x20000\n\t" \
    "s_add_u32 s44, %[wo], 0xc0000\n\t" \
    "buffer_load_dwordx4 v176, %[wr], s44 offen nt lds\n\t" \
    "s_add_u32 m0, %[wm], 0x22000\n\t" \
    "s_add_u32 s44, %[wo], 0xcc000\n\t" \
    "buffer_load_dwordx4 v176, %[wr], s44 offen nt lds\n\t" \
    "s_add_u32 m0, %[wm], 0x24000\n\t" \
    "s_add_u32 s44, %[wo], 0xd8000\n\t" \
    "buffer_load_dwordx4 v176, %[wr], s44 offen nt lds\n\t" \
    "s_add_u32 m0, %[wm], 0x26000\n\t" \
    "s_add_u32 s44, %[wo], 0xe4000\n\t" \
    "buffer_load_dwordx4 v176, %[wr], s44 offen nt lds\n\t" \
    "s_waitcnt vmcnt(0) lgkmcnt(0)\n\t" \
    "s_barrier \n\t" \
    "v_add_u32 v178, 0x0, v177\n\t" \
    "v_xor_b32 v179, 32, v178\n\t" \
    "ds_read_b64_tr_b16 v[0:1], v178 offset:0\n\t" \
    "ds_read_b64_tr_b16 v[2:3], v178 offset:2048\n\t" \
    "ds_read_b64_tr_b16 v[80:81], v179 offset:0\n\t" \
    "ds_read_b64_tr_b16 v[82:83], v179 offset:2048\n\t" \
    "ds_read_b64_tr_b16 v[4:5], v178 offset:16384\n\t" \
    "ds_read_b64_tr_b16 v[6:7], v178 offset:18432\n\t" \
    "ds_read_b64_tr_b16 v[84:85], v179 offset:16384\n\t" \
    "ds_read_b64_tr_b16 v[86:87], v179 offset:18432\n\t" \
    "v_add_u32 v178, 0x8000, v177\n\t" \
    "v_xor_b32 v179, 32, v178\n\t" \
    "ds_read_b64_tr_b16 v[8:9], v178 offset:0\n\t" \
    "ds_read_b64_tr_b16 v[10:11], v178 offset:2048\n\t" \
    "ds_read_b64_tr_b16 v[88:89], v179 offset:0\n\t" \
    "ds_read_b64_tr_b16 v[90:91], v179 offset:2048\n\t" \
    "ds_read_b64_tr_b16 v[12:13], v178 offset:16384\n\t" \
    "ds_read_b64_tr_b16 v[14:15], v178 offset:18432\n\t" \
    "ds_read_b64_tr_b16 v[92:93], v179 offset:16384\n\t" \
    "ds_read_b64_tr_b16 v[94:95], v179 offset:18432\n\t" \
    "v_add_u32 v178, 0x10000, v177\n\t" \
    "v_xor_b32 v179, 32, v178\n\t" \
    "ds_read_b64_tr_b16 v[16:17], v178 offset:0\n\t" \
    "ds_read_b64_tr_b16 v[18:19], v178 offset:2048\n\t" \
    "ds_read_b64_tr_b16 v[96:97], v179 offset:0\n\t" \
    "ds_read_b64_tr_b16 v[98:99], v179 offset:2048\n\t" \
    "ds_read_b64_tr_b16 v[20:21], v178 offset:16384\n\t" \
    "ds_read_b64_tr_b16 v[22:23], v178 offset:18432\n\t" \
    "ds_read_b64_tr_b16 v[100:101], v179 offset:16384\n\t" \
    "ds_read_b64_tr_b16 v[102:103], v179 offset:18432\n\t" \
    "v_add_u32 v178, 0x18000, v177\n\t" \
    "v_xor_b32 v179, 32, v178\n\t" \
    "ds_read_b64_tr_b16 v[24:25], v178 offset:0\n\t" \
    "ds_read_b64_tr_b16 v[26:27], v178 offset:2048\n\t" \
    "ds_read_b64_tr_b16 v[104:105], v179 offset:0\n\t" \
    "ds_read_b64_tr_b16 v[106:107], v179 offset:2048\n\t" \
    "ds_read_b64_tr_b16 v[28:29], v178 offset:16384\n\t" \
    "ds_read_b64_tr_b16 v[30:31], v178 offset:18432\n\t" \
    "ds_read_b64_tr_b16 v[108:109], v179 offset:16384\n\t" \
    "ds_read_b64_tr_b16 v[110:111], v179 offset:18432\n\t" \
    "v_add_u32 v178, 0x20000, v177\n\t" \
    "v_xor_b32 v179, 32, v178\n\t" \
    "ds_read_b64_tr_b16 v[32:33], v178 offset:0\n\t" \
    "ds_read_b64_tr_b16 v[34:35], v178 offset:2048\n\t" \
    "ds_read_b64_tr_b16 v[112:113], v179 offset:0\n\t" \
    "ds_read_b64_tr_b16 v[114:115], v179 offset:2048\n\t" \
    "ds_read_b64_tr_b16 v[36:37], v178 offset:16384\n\t" \
    "ds_read_b64_tr_b16 v[38:39], v178 offset:18432\n\t" \
    "ds_read_b64_tr_b16 v[116:117], v179 offset:16384\n\t" \
    "ds_read_b64_tr_b16 v[118:119], v179 offset:18432\n\t" \
    "s_waitcnt lgkmcnt(0)\n\t" \
    "s_barrier \n\t" \
    "s_add_u32 m0, %[wm], 0x0\n\t" \
    "s_add_u32 s44, %[wo], 0xf0000\n\t" \
    "buffer_load_dwordx4 v176, %[wr], s44 offen nt lds\n\t" \
    "s_add_u32 m0, %[wm], 0x2000\n\t" \
    "s_add_u32 s44, %[wo], 0xfc000\n\t" \
    "buffer_load_dwordx4 v176, %[wr], s44 offen nt lds\n\t" \
    "s_add_u32 m0, %[wm], 0x4000\n\t" \
    "s_add_u32 s44, %[wo], 0x108000\n\t" \
    "buffer_load_dwordx4 v176, %[wr], s44 offen nt lds\n\t" \
    "s_add_u32 m0, %[wm], 0x6000\n\t" \
    "s_add_u32 s44, %[wo], 0x114000\n\t" \
    "buffer_load_dwordx4 v176, %[wr], s44 offen nt lds\n\t" \
    "s_add_u32 m0, %[wm], 0x8000\n\t" \
    "s_add_u32 s44, %[wo], 0x120000\n\t" \
    "buffer_load_dwordx4 v176, %[wr], s44 offen nt lds\n\t" \
    "s_add_u32 m0, %[wm], 0xa000\n\t" \
    "s_add_u32 s44, %[wo], 0x12c000\n\t" \
    "buffer_load_dwordx4 v176, %[wr], s44 offen nt lds\n\t" \
    "s_add_u32 m0, %[wm], 0xc000\n\t" \
    "s_add_u32 s44, %[wo], 0x138000\n\t" \
    "buffer_load_dwordx4 v176, %[wr], s44 offen nt lds\n\t" \
    "s_add_u32 m0, %[wm], 0xe000\n\t" \
    "s_add_u32 s44, %[wo], 0x144000\n\t" \
    "buffer_load_dwordx4 v176, %[wr], s44 offen nt lds\n\t" \
    "s_add_u32 m0, %[wm], 0x10000\n\t" \
    "s_add_u32 s44, %[wo], 0x150000\n\t" \
    "buffer_load_dwordx4 v176, %[wr], s44 offen nt lds\n\t" \
    "s_add_u32 m0, %[wm], 0x12000\n\t" \
    "s_add_u32 s44, %[wo], 0x15c000\n\t" \
    "buffer_load_dwordx4 v176, %[wr], s44 offen nt lds\n\t" \
    "s_add_u32 m0, %[wm], 0x14000\n\t" \
    "s_add_u32 s44, %[wo], 0x168000\n\t" \
    "buffer_load_dwordx4 v176, %[wr], s44 offen nt lds\n\t" \
    "s_add_u32 m0, %[wm], 0x16000\n\t" \
    "s_add_u32 s44, %[wo], 0x174000\n\t" \
    "buffer_load_dwordx4 v176, %[wr], s44 offen nt lds\n\t" \
    "s_add_u32 m0, %[wm], 0x18000\n\t" \
    "s_add_u32 s44, %[wo], 0x180000\n\t" \
    "buffer_load_dwordx4 v176, %[wr], s44 offen nt lds\n\t" \
    "s_add_u32 m0, %[wm], 0x1a000\n\t" \
    "s_add_u32 s44, %[wo], 0x18c000\n\t" \
    "buffer_load_dwordx4 v176, %[wr], s44 offen nt lds\n\t" \
    "s_add_u32 m0, %[wm], 0x1c000\n\t" \
    "s_add_u32 s44, %[wo], 0x198000\n\t" \
    "buffer_load_dwordx4 v176, %[wr], s44 offen nt lds\n\t" \
    "s_add_u32 m0, %[wm], 0x1e000\n\t" \
    "s_add_u32 s44, %[wo], 0x1a4000\n\t" \
    "buffer_load_dwordx4 v176, %[wr], s44 offen nt lds\n\t" \
    "s_add_u32 m0, %[wm], 0x20000\n\t" \
    "s_add_u32 s44, %[wo], 0x1b0000\n\t" \
    "buffer_load_dwordx4 v176, %[wr], s44 offen nt lds\n\t" \
    "s_add_u32 m0, %[wm], 0x22000\n\t" \
    "s_add_u32 s44, %[wo], 0x1bc000\n\t" \
    "buffer_load_dwordx4 v176, %[wr], s44 offen nt lds\n\t" \
    "s_add_u32 m0, %[wm], 0x24000\n\t" \
    "s_add_u32 s44, %[wo], 0x1c8000\n\t" \
    "buffer_load_dwordx4 v176, %[wr], s44 offen nt lds\n\t" \
    "s_add_u32 m0, %[wm], 0x26000\n\t" \
    "s_add_u32 s44, %[wo], 0x1d4000\n\t" \
    "buffer_load_dwordx4 v176, %[wr], s44 offen nt lds\n\t" \
    "s_waitcnt vmcnt(0) lgkmcnt(0)\n\t" \
    "s_barrier \n\t" \
    "v_add_u32 v178, 0x0, v177\n\t" \
    "v_xor_b32 v179, 32, v178\n\t" \
    "ds_read_b64_tr_b16 v[40:41], v178 offset:0\n\t" \
    "ds_read_b64_tr_b16 v[42:43], v178 offset:2048\n\t" \
    "ds_read_b64_tr_b16 v[120:121], v179 offset:0\n\t" \
    "ds_read_b64_tr_b16 v[122:123], v179 offset:2048\n\t" \
    "ds_read_b64_tr_b16 v[44:45], v178 offset:16384\n\t" \
    "ds_read_b64_tr_b16 v[46:47], v178 offset:18432\n\t" \
    "ds_read_b64_tr_b16 v[124:125], v179 offset:16384\n\t" \
    "ds_read_b64_tr_b16 v[126:127], v179 offset:18432\n\t" \
    "v_add_u32 v178, 0x8000, v177\n\t" \
    "v_xor_b32 v179, 32, v178\n\t" \
    "ds_read_b64_tr_b16 v[48:49], v178 offset:0\n\t" \
    "ds_read_b64_tr_b16 v[50:51], v178 offset:2048\n\t" \
    "ds_read_b64_tr_b16 v[128:129], v179 offset:0\n\t" \
    "ds_read_b64_tr_b16 v[130:131], v179 offset:2048\n\t" \
    "ds_read_b64_tr_b16 v[52:53], v178 offset:16384\n\t" \
    "ds_read_b64_tr_b16 v[54:55], v178 offset:18432\n\t" \
    "ds_read_b64_tr_b16 v[132:133], v179 offset:16384\n\t" \
    "ds_read_b64_tr_b16 v[134:135], v179 offset:18432\n\t" \
    "v_add_u32 v178, 0x10000, v177\n\t" \
    "v_xor_b32 v179, 32, v178\n\t" \
    "ds_read_b64_tr_b16 v[56:57], v178 offset:0\n\t" \
    "ds_read_b64_tr_b16 v[58:59], v178 offset:2048\n\t" \
    "ds_read_b64_tr_b16 v[136:137], v179 offset:0\n\t" \
    "ds_read_b64_tr_b16 v[138:139], v179 offset:2048\n\t" \
    "ds_read_b64_tr_b16 v[60:61], v178 offset:16384\n\t" \
    "ds_read_b64_tr_b16 v[62:63], v178 offset:18432\n\t" \
    "ds_read_b64_tr_b16 v[140:141], v179 offset:16384\n\t" \
    "ds_read_b64_tr_b16 v[142:143], v179 offset:18432\n\t" \
    "v_add_u32 v178, 0x18000, v177\n\t" \
    "v_xor_b32 v179, 32, v178\n\t" \
    "ds_read_b64_tr_b16 v[64:65], v178 offset:0\n\t" \
    "ds_read_b64_tr_b16 v[66:67], v178 offset:2048\n\t" \
    "ds_read_b64_tr_b16 v[144:145], v179 offset:0\n\t" \
    "ds_read_b64_tr_b16 v[146:147], v179 offset:2048\n\t" \
    "ds_read_b64_tr_b16 v[68:69], v178 offset:16384\n\t" \
    "ds_read_b64_tr_b16 v[70:71], v178 offset:18432\n\t" \
    "ds_read_b64_tr_b16 v[148:149], v179 offset:16384\n\t" \
    "ds_read_b64_tr_b16 v[150:151], v179 offset:18432\n\t" \
    "v_add_u32 v178, 0x20000, v177\n\t" \
    "v_xor_b32 v179, 32, v178\n\t" \
    "ds_read_b64_tr_b16 v[72:73], v178 offset:0\n\t" \
    "ds_read_b64_tr_b16 v[74:75], v178 offset:2048\n\t" \
    "ds_read_b64_tr_b16 v[152:153], v179 offset:0\n\t" \
    "ds_read_b64_tr_b16 v[154:155], v179 offset:2048\n\t" \
    "ds_read_b64_tr_b16 v[76:77], v178 offset:16384\n\t" \
    "ds_read_b64_tr_b16 v[78:79], v178 offset:18432\n\t" \
    "ds_read_b64_tr_b16 v[156:157], v179 offset:16384\n\t" \
    "ds_read_b64_tr_b16 v[158:159], v179 offset:18432\n\t" \
    "v_mov_b32 v228, 0x3f803f80\n\t" \
    "v_mov_b32 v229, 0x3f803f80\n\t" \
    "v_mov_b32 v230, 0x3f803f80\n\t" \
    "v_mov_b32 v231, 0x3f803f80\n\t" \
    "v_mov_b64 v[216:217], 0\n\t" \
    "v_mov_b32 v219, 0\n\t" \
    "v_bfe_u32 v255, v253, 2, 5\n\t" \
    "v_add_u32 v255, 0x7000, v255\n\t" \
    "v_bfe_u32 v218, v253, 6, 1\n\t" \
    "s_waitcnt lgkmcnt(0)\n\t" \
    "s_barrier \n\t" \
    "ds_write_b32 v251, v228 offset:72\n\t" \
    "ds_write_b32 v251, v228 offset:2120\n\t" \
    "v_mfma_f32_16x16x32_bf16 v[160:163], v[0:3], v[228:231], 0\n\t" \
    "v_mfma_f32_16x16x32_bf16 v[164:167], v[80:83], v[228:231], 0\n\t" \
    "v_mfma_f32_16x16x32_bf16 v[160:163], v[4:7], v[228:231], v[160:163]\n\t" \
    "v_mfma_f32_16x16x32_bf16 v[164:167], v[84:87], v[228:231], v[164:167]\n\t" \
    "v_mfma_f32_16x16x32_bf16 v[160:163], v[8:11], v[228:231], v[160:163]\n\t" \
    "v_mfma_f32_16x16x32_bf16 v[164:167], v[88:91], v[228:231], v[164:167]\n\t" \
    "v_mfma_f32_16x16x32_bf16 v[160:163], v[12:15], v[228:231], v[160:163]\n\t" \
    "v_mfma_f32_16x16x32_bf16 v[164:167], v[92:95], v[228:231], v[164:167]\n\t" \
    "v_mfma_f32_16x16x32_bf16 v[160:163], v[16:19], v[228:231], v[160:163]\n\t" \
    "v_mfma_f32_16x16x32_bf16 v[164:167], v[96:99], v[228:231], v[164:167]\n\t" \
    "v_mfma_f32_16x16x32_bf16 v[160:163], v[20:23], v[228:231], v[160:163]\n\t" \
    "v_mfma_f32_16x16x32_bf16 v[164:167], v[100:103], v[228:231], v[164:167]\n\t" \
    "v_mfma_f32_16x16x32_bf16 v[160:163], v[24:27], v[228:231], v[160:163]\n\t" \
    "v_mfma_f32_16x16x32_bf16 v[164:167], v[104:107], v[228:231], v[164:167]\n\t" \
    "v_mfma_f32_16x16x32_bf16 v[160:163], v[28:31], v[228:231], v[160:163]\n\t" \
    "v_mfma_f32_16x16x32_bf16 v[164:167], v[108:111], v[228:231], v[164:167]\n\t" \
    "v_mfma_f32_16x16x32_bf16 v[160:163], v[32:35], v[228:231], v[160:163]\n\t" \
    "v_mfma_f32_16x16x32_bf16 v[164:167], v[112:115], v[228:231], v[164:167]\n\t" \
    "v_mfma_f32_16x16x32_bf16 v[160:163], v[36:39], v[228:231], v[160:163]\n\t" \
    "v_mfma_f32_16x16x32_bf16 v[164:167], v[116:119], v[228:231], v[164:167]\n\t" \
    "v_mfma_f32_16x16x32_bf16 v[160:163], v[40:43], v[228:231], v[160:163]\n\t" \
    "v_mfma_f32_16x16x32_bf16 v[164:167], v[120:123], v[228:231], v[164:167]\n\t" \
    "v_mfma_f32_16x16x32_bf16 v[160:163], v[44:47], v[228:231], v[160:163]\n\t" \
    "v_mfma_f32_16x16x32_bf16 v[164:167], v[124:127], v[228:231], v[164:167]\n\t" \
    "v_mfma_f32_16x16x32_bf16 v[160:163], v[48:51], v[228:231], v[160:163]\n\t" \
    "v_mfma_f32_16x16x32_bf16 v[164:167], v[128:131], v[228:231], v[164:167]\n\t" \
    "v_mfma_f32_16x16x32_bf16 v[160:163], v[52:55], v[228:231], v[160:163]\n\t" \
    "v_mfma_f32_16x16x32_bf16 v[164:167], v[132:135], v[228:231], v[164:167]\n\t" \
    "v_mfma_f32_16x16x32_bf16 v[160:163], v[56:59], v[228:231], v[160:163]\n\t" \
    "v_mfma_f32_16x16x32_bf16 v[164:167], v[136:139], v[228:231], v[164:167]\n\t" \
    "v_mfma_f32_16x16x32_bf16 v[160:163], v[60:63], v[228:231], v[160:163]\n\t" \
    "v_mfma_f32_16x16x32_bf16 v[164:167], v[140:143], v[228:231], v[164:167]\n\t" \
    "v_mfma_f32_16x16x32_bf16 v[160:163], v[64:67], v[228:231], v[160:163]\n\t" \
    "v_mfma_f32_16x16x32_bf16 v[164:167], v[144:147], v[228:231], v[164:167]\n\t" \
    "v_mfma_f32_16x16x32_bf16 v[160:163], v[68:71], v[228:231], v[160:163]\n\t" \
    "v_mfma_f32_16x16x32_bf16 v[164:167], v[148:151], v[228:231], v[164:167]\n\t" \
    "v_mfma_f32_16x16x32_bf16 v[160:163], v[72:75], v[228:231], v[160:163]\n\t" \
    "v_mfma_f32_16x16x32_bf16 v[164:167], v[152:155], v[228:231], v[164:167]\n\t" \
    "v_mfma_f32_16x16x32_bf16 v[160:163], v[76:79], v[228:231], v[160:163]\n\t" \
    "v_mfma_f32_16x16x32_bf16 v[164:167], v[156:159], v[228:231], v[164:167]\n\t" \
    "s_mov_b32 s52, 0x22222222\n\t" \
    "s_mov_b32 s53, 0x22222222\n\t" \
    "s_mov_b32 s54, 0x44444444\n\t" \
    "s_mov_b32 s55, 0x44444444\n\t" \
    "s_mov_b32 s56, 0x88888888\n\t" \
    "s_mov_b32 s57, 0x88888888\n\t" \
    "ds_write_b32 v255, v218\n\t" \
    "s_waitcnt lgkmcnt(0)\n\t" \
    "s_barrier \n\t" \
    ".Lw31_%=:\n\t" \
    "v_mov_b32 v248, 0\n\t" \
    "buffer_load_dword v248, v248, %[rs], %[go] offen sc1\n\t" \
    "s_waitcnt vmcnt(0)\n\t" \
    "v_cmp_ne_u32 vcc, 0, v248\n\t" \
    "s_cbranch_vccnz .Lw32_%=\n\t" \
    "s_cmp_eq_u32 s41, 0\n\t" \
    "s_cbranch_scc1 .Lw32_%=\n\t" \
    "s_sub_u32 s41, s41, 1\n\t" \
    "s_sleep 16\n\t" \
    "s_branch .Lw31_%=\n\t" \
    ".Lw32_%=:\n\t" \
    "s_mov_b32 s43, 0\n\t" \
    "s_add_u32 s46, s43, %[pi]\n\t" \
    "s_cmp_gt_u32 %[wave], 3\n\t" \
    "s_cbranch_scc1 .Lw33_%=\n\t" \
    "s_add_u32 s47, s46, 0x0\n\t" \
    "buffer_load_dwordx4 v[208:211], v253, %[rs], s47 offen sc1\n\t" \
    "s_add_u32 s47, s46, 0x4000\n\t" \
    "buffer_load_dwordx4 v[212:215], v253, %[rs], s47 offen sc1\n\t" \
    "s_add_u32 s47, s46, 0x8000\n\t" \
    "buffer_load_dwordx4 v[216:219], v253, %[rs], s47 offen sc1\n\t" \
    "s_add_u32 s47, s46, 0xc000\n\t" \
    "buffer_load_dwordx4 v[220:223], v253, %[rs], s47 offen sc1\n\t" \
    "s_add_u32 s47, s46, 0x10000\n\t" \
    "buffer_load_dwordx4 v[224:227], v253, %[rs], s47 offen sc1\n\t" \
    ".Lw33_%=:\n\t" \
    "s_waitcnt vmcnt(0)\n\t" \
    ".Lw34_%=:\n\t" \
    "s_add_i32 s44, s40, 0\n\t" \
    "s_max_i32 s44, s44, 0\n\t" \
    "s_lshr_b32 s45, s44, 1\n\t" \
    "s_and_b32 s44, s44, 1\n\t" \
    "s_lshl_b32 s44, s44, 12\n\t" \
    "s_mul_i32 s49, s45, 0x30000\n\t" \
    "s_add_u32 s49, s49, s44\n\t" \
    "s_lshr_b32 s45, s45, 3\n\t" \
    "s_mul_i32 s45, s45, 0x82000\n\t" \
    "s_add_u32 s49, s49, s45\n\t" \
    "s_add_u32 s49, s49, %[ng]\n\t" \
    "s_add_i32 s44, s40, 1\n\t" \
    "s_max_i32 s44, s44, 0\n\t" \
    "s_lshr_b32 s45, s44, 1\n\t" \
    "s_and_b32 s44, s44, 1\n\t" \
    "s_lshl_b32 s44, s44, 12\n\t" \
    "s_mul_i32 s43, s45, 0x14000\n\t" \
    "s_add_u32 s43, s43, s44\n\t" \
    "s_lshr_b32 s45, s45, 4\n\t" \
    "s_mul_i32 s45, s45, 0xc2000\n\t" \
    "s_add_u32 s43, s43, s45\n\t" \
    LST0 \
    LJIT \
    "s_cmp_gt_u32 %[wave], 3\n\t" \
    "s_cbranch_scc1 .Lw37_%=\n\t" \
    "s_setprio 2\n\t" \
    "v_max_u32 v248, v208, v209\n\t" \
    "v_max3_u32 v248, v210, v211, v248\n\t" \
    "v_max3_u32 v248, v212, v213, v248\n\t" \
    "v_max3_u32 v248, v214, v215, v248\n\t" \
    "v_max3_u32 v248, v216, v217, v248\n\t" \
    "v_max3_u32 v248, v218, v219, v248\n\t" \
    "v_max3_u32 v248, v220, v221, v248\n\t" \
    "v_max3_u32 v248, v222, v223, v248\n\t" \
    "v_max3_u32 v248, v224, v225, v248\n\t" \
    "v_max3_u32 v248, v226, v227, v248\n\t" \
    "v_cmp_eq_u32 vcc, -1, v248\n\t" \
    "s_cbranch_vccz .Lw36_%=\n\t" \
    ".Lw35_%=:\n\t" \
    "v_max_u32 v248, v208, v209\n\t" \
    "v_max3_u32 v248, v210, v211, v248\n\t" \
    "v_cmp_eq_u32 vcc, -1, v248\n\t" \
    "s_cbranch_vccz .Lw38_%=\n\t" \
    "s_add_u32 s47, s46, 0x0\n\t" \
    "buffer_load_dwordx4 v[208:211], v253, %[rs], s47 offen sc1\n\t" \
    ".Lw38_%=:\n\t" \
    "v_max_u32 v248, v212, v213\n\t" \
    "v_max3_u32 v248, v214, v215, v248\n\t" \
    "v_cmp_eq_u32 vcc, -1, v248\n\t" \
    "s_cbranch_vccz .Lw39_%=\n\t" \
    "s_add_u32 s47, s46, 0x4000\n\t" \
    "buffer_load_dwordx4 v[212:215], v253, %[rs], s47 offen sc1\n\t" \
    ".Lw39_%=:\n\t" \
    "v_max_u32 v248, v216, v217\n\t" \
    "v_max3_u32 v248, v218, v219, v248\n\t" \
    "v_cmp_eq_u32 vcc, -1, v248\n\t" \
    "s_cbranch_vccz .Lw310_%=\n\t" \
    "s_add_u32 s47, s46, 0x8000\n\t" \
    "buffer_load_dwordx4 v[216:219], v253, %[rs], s47 offen sc1\n\t" \
    ".Lw310_%=:\n\t" \
    "v_max_u32 v248, v220, v221\n\t" \
    "v_max3_u32 v248, v222, v223, v248\n\t" \
    "v_cmp_eq_u32 vcc, -1, v248\n\t" \
    "s_cbranch_vccz .Lw311_%=\n\t" \
    "s_add_u32 s47, s46, 0xc000\n\t" \
    "buffer_load_dwordx4 v[220:223], v253, %[rs], s47 offen sc1\n\t" \
    ".Lw311_%=:\n\t" \
    "v_max_u32 v248, v224, v225\n\t" \
    "v_max3_u32 v248, v226, v227, v248\n\t" \
    "v_cmp_eq_u32 vcc, -1, v248\n\t" \
    "s_cbranch_vccz .Lw312_%=\n\t" \
    "s_add_u32 s47, s46, 0x10000\n\t" \
    "buffer_load_dwordx4 v[224:227], v253, %[rs], s47 offen sc1\n\t" \
    ".Lw312_%=:\n\t" \
    "s_waitcnt vmcnt(0)\n\t" \
    "v_max_u32 v248, v208, v209\n\t" \
    "v_max3_u32 v248, v210, v211, v248\n\t" \
    "v_max3_u32 v248, v212, v213, v248\n\t" \
    "v_max3_u32 v248, v214, v215, v248\n\t" \
    "v_max3_u32 v248, v216, v217, v248\n\t" \
    "v_max3_u32 v248, v218, v219, v248\n\t" \
    "v_max3_u32 v248, v220, v221, v248\n\t" \
    "v_max3_u32 v248, v222, v223, v248\n\t" \
    "v_max3_u32 v248, v224, v225, v248\n\t" \
    "v_max3_u32 v248, v226, v227, v248\n\t" \
    "v_cmp_eq_u32 vcc, -1, v248\n\t" \
    "s_cbranch_vccz .Lw36_%=\n\t" \
    "s_cmp_eq_u32 s41, 0\n\t" \
    "s_cbranch_scc1 .Lw36_%=\n\t" \
    "s_sub_u32 s41, s41, 1\n\t" \
    "s_branch .Lw35_%=\n\t" \
    ".Lw36_%=:\n\t" \
    LST1 \
    LJIT \
    "ds_write_b128 v250, v[208:211] offset:0\n\t" \
    "v_mfma_f32_16x16x32_bf16 v[236:239], v[208:211], v[208:211], 0\n\t" \
    "v_mfma_f32_16x16x32_bf16 v[232:235], v[228:231], v[208:211], 0\n\t" \
    "ds_write_b128 v250, v[212:215] offset:4096\n\t" \
    "v_mfma_f32_16x16x32_bf16 v[236:239], v[212:215], v[212:215], v[236:239]\n\t" \
    "v_mfma_f32_16x16x32_bf16 v[232:235], v[228:231], v[212:215], v[232:235]\n\t" \
    "ds_write_b128 v250, v[216:219] offset:8192\n\t" \
    "v_mfma_f32_16x16x32_bf16 v[236:239], v[216:219], v[216:219], v[236:239]\n\t" \
    "v_mfma_f32_16x16x32_bf16 v[232:235], v[228:231], v[216:219], v[232:235]\n\t" \
    "ds_write_b128 v250, v[220:223] offset:12288\n\t" \
    "v_mfma_f32_16x16x32_bf16 v[236:239], v[220:223], v[220:223], v[236:239]\n\t" \
    "v_mfma_f32_16x16x32_bf16 v[232:235], v[228:231], v[220:223], v[232:235]\n\t" \
    "ds_write_b128 v250, v[224:227] offset:16384\n\t" \
    "v_mfma_f32_16x16x32_bf16 v[236:239], v[224:227], v[224:227], v[236:239]\n\t" \
    "v_mfma_f32_16x16x32_bf16 v[232:235], v[228:231], v[224:227], v[232:235]\n\t" \
    "s_add_u32 s46, s43, %[pi]\n\t" \
    "s_nop 4\n\t" \
    "v_cndmask_b32 v236, v236, v237, s[52:53]\n\t" \
    "v_cndmask_b32 v236, v236, v238, s[54:55]\n\t" \
    "v_cndmask_b32 v236, v236, v239, s[56:57]\n\t" \
    "s_mov_b32 exec_lo, 0xf0000f\n\t" \
    "s_mov_b32 exec_hi, 0xf0000f00\n\t" \
    "ds_write2_b32 v252, v232, v236 offset1:1\n\t" \
    "s_mov_b64 exec, -1\n\t" \
    "s_add_u32 s44, s40, 1\n\t" \
    "v_mov_b32 v248, s44\n\t" \
    "s_lshl_b64 exec, 1, %[wave]\n\t" \
    "ds_write_b32 v255, v248\n\t" \
    "s_mov_b64 exec, -1\n\t" \
    ".Lw37_%=:\n\t" \
    "ds_read_b32 v248, v255\n\t" \
    "s_waitcnt lgkmcnt(0)\n\t" \
    "v_cmp_ge_u32 vcc, s40, v248\n\t" \
    "s_cbranch_vccz .Lw313_%=\n\t" \
    "s_cmp_lt_u32 %[wave], 4\n\t" \
    "s_cbranch_scc1 .Lw37_%=\n\t" \
    "s_sleep 1\n\t" \
    "s_branch .Lw37_%=\n\t" \
    ".Lw313_%=:\n\t" \
    "s_setprio 0\n\t" \
    LST2 \
    LJIT \
    "s_cmp_lg_u32 %[wave], 4\n\t" \
    "s_cbranch_scc1 .Lw314_%=\n\t" \
    "s_setprio 1\n\t" \
    "ds_read_b128 v[232:235], v251 offset:0\n\t" \
    "ds_read_b128 v[236:239], v251 offset:16\n\t" \
    "s_waitcnt lgkmcnt(0)\n\t" \
    "v_add_f32 v232, v232, v236\n\t" \
    "v_add_f32 v233, v233, v237\n\t" \
    "v_add_f32 v234, v234, v238\n\t" \
    "v_add_f32 v235, v235, v239\n\t" \
    "v_add_f32 v232, v232, v234\n\t" \
    "v_add_f32 v233, v233, v235\n\t" \
    "v_mul_f32 v232, 0x3acccccd, v232\n\t" \
    "v_mul_f32 v233, 0x3acccccd, v233\n\t" \
    "v_fma_f32 v233, -v232, v232, v233\n\t" \
    "v_mov_b32 v234, v232\n\t" \
    "v_max_f32 v233, 0, v233\n\t" \
    "v_cvt_pk_bf16_f32 v234, v234, v234\n\t" \
    "v_and_b32 v234, 0xffff0000, v234\n\t" \
    "v_mov_b32 v236, v233\n\t" \
    "v_cvt_pk_bf16_f32 v236, v236, v236\n\t" \
    "v_and_b32 v236, 0xffff0000, v236\n\t" \
    "v_add_f32 v236, 0x3727c5ac, v236\n\t" \
    "v_cvt_pk_bf16_f32 v236, v236, v236\n\t" \
    "v_and_b32 v236, 0xffff0000, v236\n\t" \
    "v_sqrt_f32 v236, v236\n\t" \
    "s_nop 0\n\t" \
    "v_cvt_pk_bf16_f32 v236, v236, v236\n\t" \
    "v_and_b32 v236, 0xffff0000, v236\n\t" \
    "v_rcp_f32 v235, v236\n\t" \
    "s_nop 0\n\t" \
    "v_mov_b32 v237, v235\n\t" \
    "v_mul_f32 v238, -v234, v235\n\t" \
    "v_mov_b32 v239, s40\n\t" \
    "ds_write2_b32 v251, v237, v238 offset0:16 offset1:17\n\t" \
    "ds_write_b32 v251, v239 offset:72\n\t" \
    "s_setprio 0\n\t" \
    ".Lw314_%=:\n\t" \
    "ds_read_b128 v[176:179], v249 offset:0\n\t" \
    "ds_read_b128 v[180:183], v249 offset:1024\n\t" \
    "ds_read_b128 v[184:187], v249 offset:2048\n\t" \
    "ds_read_b128 v[188:191], v249 offset:3072\n\t" \
    "ds_read_b128 v[192:195], v249 offset:4096\n\t" \
    "ds_read_b128 v[196:199], v249 offset:5120\n\t" \
    "ds_read_b128 v[200:203], v249 offset:6144\n\t" \
    "s_waitcnt lgkmcnt(6)\n\t" \
    "v_mfma_f32_16x16x32_bf16 v[168:171], v[0:3], v[176:179], 0\n\t" \
    "ds_read_b128 v[204:207], v249 offset:7168\n\t" \
    "v_mfma_f32_16x16x32_bf16 v[172:175], v[80:83], v[176:179], 0\n\t" \
    "s_waitcnt lgkmcnt(6)\n\t" \
    "v_mfma_f32_16x16x32_bf16 v[168:171], v[4:7], v[180:183], v[168:171]\n\t" \
    "ds_read_b128 v[176:179], v249 offset:8192\n\t" \
    "v_mfma_f32_16x16x32_bf16 v[172:175], v[84:87], v[180:183], v[172:175]\n\t" \
    "s_waitcnt lgkmcnt(6)\n\t" \
    "v_mfma_f32_16x16x32_bf16 v[168:171], v[8:11], v[184:187], v[168:171]\n\t" \
    "ds_read_b128 v[180:183], v249 offset:9216\n\t" \
    "v_mfma_f32_16x16x32_bf16 v[172:175], v[88:91], v[184:187], v[172:175]\n\t" \
    "s_waitcnt lgkmcnt(6)\n\t" \
    "v_mfma_f32_16x16x32_bf16 v[168:171], v[12:15], v[188:191], v[168:171]\n\t" \
    "ds_read_b128 v[184:187], v249 offset:10240\n\t" \
    "v_mfma_f32_16x16x32_bf16 v[172:175], v[92:95], v[188:191], v[172:175]\n\t" \
    "s_waitcnt lgkmcnt(6)\n\t" \
    "v_mfma_f32_16x16x32_bf16 v[168:171], v[16:19], v[192:195], v[168:171]\n\t" \
    "ds_read_b128 v[188:191], v249 offset:11264\n\t" \
    "v_mfma_f32_16x16x32_bf16 v[172:175], v[96:99], v[192:195], v[172:175]\n\t" \
    "s_waitcnt lgkmcnt(6)\n\t" \
    "v_mfma_f32_16x16x32_bf16 v[168:171], v[20:23], v[196:199], v[168:171]\n\t" \
    "ds_read_b128 v[192:195], v249 offset:12288\n\t" \
    "v_mfma_f32_16x16x32_bf16 v[172:175], v[100:103], v[196:199], v[172:175]\n\t" \
    "s_waitcnt lgkmcnt(6)\n\t" \
    "v_mfma_f32_16x16x32_bf16 v[168:171], v[24:27], v[200:203], v[168:171]\n\t" \
    "ds_read_b128 v[196:199], v249 offset:13312\n\t" \
    "v_mfma_f32_16x16x32_bf16 v[172:175], v[104:107], v[200:203], v[172:175]\n\t" \
    "s_waitcnt lgkmcnt(6)\n\t" \
    "v_mfma_f32_16x16x32_bf16 v[168:171], v[28:31], v[204:207], v[168:171]\n\t" \
    "ds_read_b128 v[200:203], v249 offset:14336\n\t" \
    "v_mfma_f32_16x16x32_bf16 v[172:175], v[108:111], v[204:207], v[172:175]\n\t" \
    "s_waitcnt lgkmcnt(6)\n\t" \
    "v_mfma_f32_16x16x32_bf16 v[168:171], v[32:35], v[176:179], v[168:171]\n\t" \
    "ds_read_b128 v[204:207], v249 offset:15360\n\t" \
    "v_mfma_f32_16x16x32_bf16 v[172:175], v[112:115], v[176:179], v[172:175]\n\t" \
    "s_waitcnt lgkmcnt(6)\n\t" \
    "v_mfma_f32_16x16x32_bf16 v[168:171], v[36:39], v[180:183], v[168:171]\n\t" \
    "ds_read_b128 v[176:179], v249 offset:16384\n\t" \
    "v_mfma_f32_16x16x32_bf16 v[172:175], v[116:119], v[180:183], v[172:175]\n\t" \
    "s_waitcnt lgkmcnt(6)\n\t" \
    "v_mfma_f32_16x16x32_bf16 v[168:171], v[40:43], v[184:187], v[168:171]\n\t" \
    "ds_read_b128 v[180:183], v249 offset:17408\n\t" \
    "v_mfma_f32_16x16x32_bf16 v[172:175], v[120:123], v[184:187], v[172:175]\n\t" \
    "s_waitcnt lgkmcnt(6)\n\t" \
    "v_mfma_f32_16x16x32_bf16 v[168:171], v[44:47], v[188:191], v[168:171]\n\t" \
    "ds_read_b128 v[184:187], v249 offset:18432\n\t" \
    "v_mfma_f32_16x16x32_bf16 v[172:175], v[124:127], v[188:191], v[172:175]\n\t" \
    "s_waitcnt lgkmcnt(6)\n\t" \
    "v_mfma_f32_16x16x32_bf16 v[168:171], v[48:51], v[192:195], v[168:171]\n\t" \
    "ds_read_b128 v[188:191], v249 offset:19456\n\t" \
    "v_mfma_f32_16x16x32_bf16 v[172:175], v[128:131], v[192:195], v[172:175]\n\t" \
    "s_waitcnt lgkmcnt(6)\n\t" \
    "v_mfma_f32_16x16x32_bf16 v[168:171], v[52:55], v[196:199], v[168:171]\n\t" \
    "v_mfma_f32_16x16x32_bf16 v[172:175], v[132:135], v[196:199], v[172:175]\n\t" \
    "s_waitcnt lgkmcnt(5)\n\t" \
    "v_mfma_f32_16x16x32_bf16 v[168:171], v[56:59], v[200:203], v[168:171]\n\t" \
    "v_mfma_f32_16x16x32_bf16 v[172:175], v[136:139], v[200:203], v[172:175]\n\t" \
    "s_cmp_gt_u32 %[wave], 3\n\t" \
    "s_cbranch_scc1 .Lw316_%=\n\t" \
    "s_add_u32 s47, s46, 0x0\n\t" \
    "buffer_load_dwordx4 v[208:211], v253, %[rs], s47 offen sc1\n\t" \
    "s_add_u32 s47, s46, 0x4000\n\t" \
    "buffer_load_dwordx4 v[212:215], v253, %[rs], s47 offen sc1\n\t" \
    "s_add_u32 s47, s46, 0x8000\n\t" \
    "buffer_load_dwordx4 v[216:219], v253, %[rs], s47 offen sc1\n\t" \
    "s_add_u32 s47, s46, 0xc000\n\t" \
    "buffer_load_dwordx4 v[220:223], v253, %[rs], s47 offen sc1\n\t" \
    "s_add_u32 s47, s46, 0x10000\n\t" \
    "buffer_load_dwordx4 v[224:227], v253, %[rs], s47 offen sc1\n\t" \
    ".Lw316_%=:\n\t" \
    "s_waitcnt lgkmcnt(4)\n\t" \
    "v_mfma_f32_16x16x32_bf16 v[168:171], v[60:63], v[204:207], v[168:171]\n\t" \
    "v_mfma_f32_16x16x32_bf16 v[172:175], v[140:143], v[204:207], v[172:175]\n\t" \
    "ds_read_b32 v236, v251 offset:72\n\t" \
    "ds_read_b64 v[238:239], v251 offset:64\n\t" \
    "s_waitcnt lgkmcnt(5)\n\t" \
    "v_mfma_f32_16x16x32_bf16 v[168:171], v[64:67], v[176:179], v[168:171]\n\t" \
    "v_mfma_f32_16x16x32_bf16 v[172:175], v[144:147], v[176:179], v[172:175]\n\t" \
    "s_waitcnt lgkmcnt(4)\n\t" \
    "v_mfma_f32_16x16x32_bf16 v[168:171], v[68:71], v[180:183], v[168:171]\n\t" \
    "v_mfma_f32_16x16x32_bf16 v[172:175], v[148:151], v[180:183], v[172:175]\n\t" \
    ".Lw317_%=:\n\t" \
    "s_waitcnt lgkmcnt(0)\n\t" \
    "v_cmp_ne_u32 vcc, s40, v236\n\t" \
    "s_cbranch_vccz .Lw318_%=\n\t" \
    "ds_read_b32 v236, v251 offset:72\n\t" \
    "ds_read_b64 v[238:239], v251 offset:64\n\t" \
    "s_branch .Lw317_%=\n\t" \
    ".Lw318_%=:\n\t" \
    "s_waitcnt lgkmcnt(0)\n\t" \
    "v_mul_f32 v240, v160, v239\n\t" \
    "v_mul_f32 v241, v161, v239\n\t" \
    "v_mfma_f32_16x16x32_bf16 v[168:171], v[72:75], v[184:187], v[168:171]\n\t" \
    "v_mul_f32 v242, v162, v239\n\t" \
    "v_mul_f32 v243, v163, v239\n\t" \
    "v_mfma_f32_16x16x32_bf16 v[172:175], v[152:155], v[184:187], v[172:175]\n\t" \
    "v_mul_f32 v244, v164, v239\n\t" \
    "v_mul_f32 v245, v165, v239\n\t" \
    "v_mfma_f32_16x16x32_bf16 v[168:171], v[76:79], v[188:191], v[168:171]\n\t" \
    "v_mul_f32 v246, v166, v239\n\t" \
    "v_mul_f32 v247, v167, v239\n\t" \
    "v_mfma_f32_16x16x32_bf16 v[172:175], v[156:159], v[188:191], v[172:175]\n\t" \
    "v_mov_b32 v237, 0xc0135761\n\t" \
    "s_cmp_lt_u32 %[wave], 4\n\t" \
    "s_cbranch_scc1 .Lw319_%=\n\t" \
    "s_add_u32 s44, s40, 2\n\t" \
    "v_mov_b32 v248, s44\n\t" \
    "s_lshl_b64 exec, 1, %[wave]\n\t" \
    "ds_write_b32 v255, v248\n\t" \
    "s_mov_b64 exec, -1\n\t" \
    ".Lw319_%=:\n\t" \
    "s_nop 1\n\t" \
    LST3 \
    LJIT \
    "s_nop 2\n\t" \
    "v_fma_f32 v168, v168, v238, v240\n\t" \
    "v_fma_f32 v169, v169, v238, v241\n\t" \
    "v_fma_f32 v170, v170, v238, v242\n\t" \
    "v_fma_f32 v171, v171, v238, v243\n\t" \
    "v_fma_f32 v172, v172, v238, v244\n\t" \
    "v_fma_f32 v173, v173, v238, v245\n\t" \
    "v_fma_f32 v174, v174, v238, v246\n\t" \
    "v_fma_f32 v175, v175, v238, v247\n\t" \
    "v_mul_f32 v176, v168, v168\n\t" \
    "v_mul_f32 v177, v169, v169\n\t" \
    "v_mul_f32 v178, v170, v170\n\t" \
    "v_mul_f32 v179, v171, v171\n\t" \
    "v_mul_f32 v180, v172, v172\n\t" \
    "v_mul_f32 v181, v173, v173\n\t" \
    "v_mul_f32 v182, v174, v174\n\t" \
    "v_mul_f32 v183, v175, v175\n\t" \
    "v_fmamk_f32 v176, v176, 0xbdd2d3e7, v237\n\t" \
    "v_fmamk_f32 v177, v177, 0xbdd2d3e7, v237\n\t" \
    "v_fmamk_f32 v178, v178, 0xbdd2d3e7, v237\n\t" \
    "v_fmamk_f32 v179, v179, 0xbdd2d3e7, v237\n\t" \
    "v_fmamk_f32 v180, v180, 0xbdd2d3e7, v237\n\t" \
    "v_fmamk_f32 v181, v181, 0xbdd2d3e7, v237\n\t" \
    "v_fmamk_f32 v182, v182, 0xbdd2d3e7, v237\n\t" \
    "v_fmamk_f32 v183, v183, 0xbdd2d3e7, v237\n\t" \
    "v_mul_f32 v176, v168, v176\n\t" \
    "v_mul_f32 v177, v169, v177\n\t" \
    "v_mul_f32 v178, v170, v178\n\t" \
    "v_mul_f32 v179, v171, v179\n\t" \
    "v_mul_f32 v180, v172, v180\n\t" \
    "v_mul_f32 v181, v173, v181\n\t" \
    "v_mul_f32 v182, v174, v182\n\t" \
    "v_mul_f32 v183, v175, v183\n\t" \
    "v_exp_f32 v176, v176\n\t" \
    "v_exp_f32 v177, v177\n\t" \
    "v_exp_f32 v178, v178\n\t" \
    "v_exp_f32 v179, v179\n\t" \
    "v_exp_f32 v180, v180\n\t" \
    "v_exp_f32 v181, v181\n\t" \
    "v_exp_f32 v182, v182\n\t" \
    "v_exp_f32 v183, v183\n\t" \
    "v_add_f32 v176, 1.0, v176\n\t" \
    "v_add_f32 v177, 1.0, v177\n\t" \
    "v_add_f32 v178, 1.0, v178\n\t" \
    "v_add_f32 v179, 1.0, v179\n\t" \
    "v_add_f32 v180, 1.0, v180\n\t" \
    "v_add_f32 v181, 1.0, v181\n\t" \
    "v_add_f32 v182, 1.0, v182\n\t" \
    "v_add_f32 v183, 1.0, v183\n\t" \
    "v_rcp_f32 v176, v176\n\t" \
    "v_rcp_f32 v177, v177\n\t" \
    "v_rcp_f32 v178, v178\n\t" \
    "v_rcp_f32 v179, v179\n\t" \
    "v_rcp_f32 v180, v180\n\t" \
    "v_rcp_f32 v181, v181\n\t" \
    "v_rcp_f32 v182, v182\n\t" \
    "v_rcp_f32 v183, v183\n\t" \
    "v_mul_f32 v168, v168, v176\n\t" \
    "v_mul_f32 v169, v169, v177\n\t" \
    "v_mul_f32 v170, v170, v178\n\t" \
    "v_mul_f32 v171, v171, v179\n\t" \
    "v_mul_f32 v172, v172, v180\n\t" \
    "v_mul_f32 v173, v173, v181\n\t" \
    "v_mul_f32 v174, v174, v182\n\t" \
    "v_mul_f32 v175, v175, v183\n\t" \
    "s_waitcnt vmcnt(0)\n\t" \
    "v_cvt_pk_bf16_f32 v184, v168, v169\n\t" \
    "v_cvt_pk_bf16_f32 v185, v170, v171\n\t" \
    "v_cvt_pk_bf16_f32 v186, v172, v173\n\t" \
    "v_cvt_pk_bf16_f32 v187, v174, v175\n\t" \
    "v_min_u32 v184, -2, v184\n\t" \
    "v_min_u32 v185, -2, v185\n\t" \
    "v_min_u32 v186, -2, v186\n\t" \
    "v_min_u32 v187, -2, v187\n\t" \
    "s_nop 0\n\t" \
    "v_permlane16_swap_b32 v184, v186\n\t" \
    "v_permlane16_swap_b32 v185, v187\n\t" \
    "buffer_store_dwordx4 v[184:187], v254, %[rs], s49 offen sc1\n\t" \
    LST4 \
    LJIT \
    "s_cmp_ge_u32 s40, 122\n\t" \
    "s_cbranch_scc1 .Lw320_%=\n\t" \
    "s_add_i32 s44, s40, 6\n\t" \
    "s_max_i32 s44, s44, 0\n\t" \
    "s_lshr_b32 s45, s44, 1\n\t" \
    "s_and_b32 s44, s44, 1\n\t" \
    "s_lshl_b32 s44, s44, 12\n\t" \
    "s_mul_i32 s50, s45, 0x30000\n\t" \
    "s_add_u32 s50, s50, s44\n\t" \
    "s_lshr_b32 s45, s45, 3\n\t" \
    "s_mul_i32 s45, s45, 0x82000\n\t" \
    "s_add_u32 s50, s50, s45\n\t" \
    "s_add_u32 s50, s50, %[ng]\n\t" \
    "v_mov_b32 v188, -1\n\t" \
    "v_mov_b32 v189, -1\n\t" \
    "v_mov_b32 v190, -1\n\t" \
    "v_mov_b32 v191, -1\n\t" \
    "buffer_store_dwordx4 v[188:191], v253, %[rs], s50 offen sc1\n\t" \
    ".Lw320_%=:\n\t" \
    "v_xor_b32 v249, 0x8000, v249\n\t" \
    "v_xor_b32 v250, 0x8000, v250\n\t" \
    "v_xor_b32 v251, 0x800, v251\n\t" \
    "v_xor_b32 v252, 0x800, v252\n\t" \
    LFLUSHW \
    "s_add_u32 s40, s40, 1\n\t" \
    "s_cmp_lg_u32 s40, 128\n\t" \
    "s_cbranch_scc1 .Lw34_%=\n\t" \
    "s_branch .Lend%=\n\t" \
    ".Lend%=:\n\t" \
    "s_waitcnt vmcnt(0) lgkmcnt(0)\n\t" \
    "s_mov_b32 %[bud], s41\n\t"
using u32x8 = __attribute__((ext_vector_type(8))) unsigned;
using u32x16 = __attribute__((ext_vector_type(16))) unsigned;
using u32x32 = __attribute__((ext_vector_type(32))) unsigned;
__device__ __forceinline__ u32x32 cat32(u32x8 a, u32x8 b, u32x8 c, u32x8 d) {
    const u32x16 lo = __builtin_shufflevector(a, b, 0, 1, 2, 3, 4, 5, 6, 7, 8, 9, 10, 11, 12, 13, 14, 15);
    const u32x16 hi = __builtin_shufflevector(c, d, 0, 1, 2, 3, 4, 5, 6, 7, 8, 9, 10, 11, 12, 13, 14, 15);
    return __builtin_shufflevector(lo, hi, 0, 1, 2, 3, 4, 5, 6, 7, 8, 9, 10, 11, 12, 13, 14, 15, 16, 17, 18, 19, 20, 21, 22, 23, 24, 25, 26, 27, 28, 29, 30, 31);
}
__device__ __forceinline__ unsigned c1_dst(int wave, int kind) {
    const int ln = lane_id(), c = ln & 15, g = ln >> 4, hf = (wave >> 1) & 1;
    if (kind == 2) {
        const unsigned b = L_KM + (c >> 2) * 256 + (wave & 1) * 128 + g * 32;
        return (b + hf * 8 + (c & 3) * 2) | (b + 1024 + hf * 1024 + (c & 3) * 4) << 16;
    }
    const unsigned b = L_Q + (c >> 3) * 256 + g * 64 + ((c >> 2) & 1) * 8;
    return (b + (wave & 1) * (kind ? 512 : 1536) + ((c >> 1) & 1) * 32 + hf * 4 + (c & 1) * 2) | (b + 1024 + (wave & 3) * 512 + (c & 1) * 32 + ((c >> 1) & 1) * 4) << 16;
}
__device__ __forceinline__ void dma1(rsrc_t rs, unsigned vo, unsigned so, unsigned dst) {
    asm volatile("s_mov_b32 m0, %3\n\ts_nop 0\n\tbuffer_load_dwordx4 %0, %1, %2 offen nt lds" :: "v"(vo), "s"(rs), "s"(so), "s"(dst) : "memory");
}
using s16x4 = __attribute__((ext_vector_type(4))) short;
__device__ __forceinline__ u32x4 tr_frag(unsigned a, unsigned step) {
    const u32x2 x = __builtin_bit_cast(u32x2, __builtin_amdgcn_ds_read_tr16_b64_v4i16((LAS s16x4*)(size_t)a)), y = __builtin_bit_cast(u32x2, __builtin_amdgcn_ds_read_tr16_b64_v4i16((LAS s16x4*)(size_t)(a + step)));
    return (u32x4){x[0], x[1], y[0], y[1]};
}
#define LOOP_CLOB "memory", "vcc", "scc", "v0", "v1", "v2", "v3", "v4", "v5", "v6", "v7", "v8", "v9", "v10", "v11", "v12", "v13", "v14", "v15", "v16", "v17", "v18", "v19", "v20", "v21", "v22", "v23", "v24", "v25", "v26", "v27", "v28", "v29", "v30", "v31", "v32", "v33", "v34", "v35", "v36", "v37", "v38", "v39", "v40", "v41", "v42", "v43", "v44", "v45", "v46", "v47", "v48", "v49", "v50", "v51", "v52", "v53", "v54", "v55", "v56", "v57", "v58", "v59", "v60", "v61", "v62", "v63", "v64", "v65", "v66", "v67", "v68", "v69", "v70", "v71", "v72", "v73", "v74", "v75", "v76", "v77", "v78", "v79", "v80", "v81", "v82", "v83", "v84", "v85", "v86", "v87", "v88", "v89", "v90", "v91", "v92", "v93", "v94", "v95", "v96", "v97", "v98", "v99", "v100", "v101", "v102", "v103", "v104", "v105", "v106", "v107", "v108", "v109", "v110", "v111", "v112", "v113", "v114", "v115", "v116", "v117", "v118", "v119", "v120", "v121", "v122", "v123", "v124", "v125", "v126", "v127", "v128", "v129", "v130", "v131", "v132", "v133", "v134", "v135", "v136", "v137", "v138", "v139", "v140", "v141", "v142", "v143", "v144", "v145", "v146", "v147", "v148", "v149", "v150", "v151", "v152", "v153", "v154", "v155", "v156", "v157", "v158", "v159", "v160", "v161", "v162", "v163", "v164", "v165", "v166", "v167", "v168", "v169", "v170", "v171", "v172", "v173", "v174", "v175", "v176", "v177", "v178", "v179", "v180", "v181", "v182", "v183", "v184", "v185", "v186", "v187", "v188", "v189", "v190", "v191", "v192", "v193", "v194", "v195", "v196", "v197", "v198", "v199", "v200", "v201", "v202", "v203", "v204", "v205", "v206", "v207", "v208", "v209", "v210", "v211", "v212", "v213", "v214", "v215", "v216", "v217", "v218", "v219", "v220", "v221", "v222", "v223", "v224", "v225", "v226", "v227", "v228", "v229", "v230", "v231", "v232", "v233", "v234", "v235", "v236", "v237", "v238", "v239", "v246", "v247", "v248", "v249", "v250", "v251", "v252", "s40", "s41", "s42", "s43", "s44", "s45", "s46", "s47", "s48", "s49", "s50", "s51", "s52", "s53", "s54", "s55", "s56", "s57", "s58", "s59", "s60", "s61", "s62", "s63", "s64", "s65", "s66", "s67", "s68", "s69", "s70", "s71", "s72", "s73", "s74", "s75", "s76", "s77", "s78", "s79"
__device__ __forceinline__ unsigned UNI(unsigned x) {
    unsigned r;
    asm volatile("s_nop 0\n\tv_readfirstlane_b32 %0, %1\n\ts_nop 4" : "=s"(r) : "v"(x));
    return r;
}
__device__ __forceinline__ void note_timeout(const Params& P, unsigned bud) {
    if (bud == 0) __builtin_amdgcn_raw_buffer_store_b32(1u, mkrsrc(P.ws + FL0, 512), 0x100, 0, 16);
}

#define LOOP_IO [bud] "=s"(bud), [mb] "+s"(mb), [pb] "+s"(pb), [fl] "+s"(fl), "+{v253}"(l16), "+{v240}"(W.a[0]), "+{v241}"(W.a[1]), "+{v242}"(W.a[2]), "+{v243}"(W.a[3]), "+{v244}"(W.a[4]), "+{v245}"(W.a[5])
#define LOOP_IN "{v254}"(b2), "{v255}"(dsto), [wave] "s"(wave), [ws] "s"(P.ws), [mo] "s"(FL_MB + blockIdx.x * 128 + pw * 4), [sz] "s"(RG_L), [lx] "s"(L << 13), [wr] "s"(wdesc(P, wave, isv, h)), [wm] "s"(UNI(wstage(pw, isv))), \
                [cj] "s"(REGS - 75 * WIN), [cs] "s"(5 * WIN)
struct WLoad { unsigned a[6]; };
__device__ __forceinline__ rsrc_t wdesc(const Params& P, int wave, bool isv, int h) {
    const unsigned wo = (isv && wave >= 4) ? h * DH * DM + (wave & 3) * 160 : h * DH;
    typedef const __attribute__((address_space(4))) unsigned long long* kptr;
    static_assert(__builtin_offsetof(Params, wq) == 24 && __builtin_offsetof(Params, wo) == 48, "argument order");
    const unsigned long long w = ((kptr)__builtin_amdgcn_kernarg_segment_ptr())[3 + (isv ? 2 : 0) + (wave >> 2)];
    return mkrsrc((const bf16_t*)w + wo, (DM * HD - wo) * 2);
}
__device__ __forceinline__ unsigned wstage(int wave, bool isv) { return (isv && wave >= 4) ? L_W + (wave & 3) * 20480 : wave * 3072; }
template <bool START>
__device__ __forceinline__ void wload_start(WLoad& W, const Params& P, int wave, bool isv, int h) {
    const int ln = lane_id(), w4 = wave & 3;
    const rsrc_t wr = wdesc(P, wave, isv, h);
    const unsigned wm = UNI(wstage(wave, isv));
    if (isv && wave >= 4) {
#pragma unroll
        for (int k = 0; k < 5; ++k) { const unsigned u = k * 64 + ln; W.a[k] = (u / 20) * (DM * 2) + (u % 20) * 16 - (k & 3) * 1024; }
#pragma unroll 1
        for (int k = 0; k < (START ? 4 : 0); ++k)
            asm volatile("s_mov_b32 m0, %7\n\ts_nop 0\n\tbuffer_load_dwordx4 %0, %5, %6 offen nt lds\n\tbuffer_load_dwordx4 %1, %5, %6 offen offset:1024 nt lds\n\tbuffer_load_dwordx4 %2, %5, %6 offen offset:2048 nt lds\n\t"
                         "buffer_load_dwordx4 %3, %5, %6 offen offset:3072 nt lds\n\ts_mov_b32 m0, %8\n\ts_nop 0\n\tbuffer_load_dwordx4 %4, %5, %6 offen nt lds"
                         :: "v"(W.a[0]), "v"(W.a[1]), "v"(W.a[2]), "v"(W.a[3]), "v"(W.a[4]), "s"(wr), "s"(k * 20480), "s"(wm + k * 5120), "s"(wm + k * 5120 + 4096) : "memory");
        W.a[5] = L_W + w4 * 20480 + (ln >> 2) * 320 + (ln & 3) * 8;
    } else {
        const unsigned sl = isv ? 12288 : 24576;
#pragma unroll
        for (int k = 0; k < 3; ++k) { const unsigned u = (w4 * 3 + k) * 64 + ln; W.a[k] = (u / 24) * (HD * 2) + (u % 24) * 16 - k * 1024; }
#pragma unroll 1
        for (int k = 0; k < (START ? 5 : 0); ++k)
            asm volatile("s_mov_b32 m0, %5\n\ts_nop 0\n\tbuffer_load_dwordx4 %0, %3, %4 offen nt lds\n\tbuffer_load_dwordx4 %1, %3, %4 offen offset:1024 nt lds\n\tbuffer_load_dwordx4 %2, %3, %4 offen offset:2048 nt lds"
                         :: "v"(W.a[0]), "v"(W.a[1]), "v"(W.a[2]), "s"(wr), "s"(k * 32 * HD * 2), "s"(wm + (k < 2 ? k + 4 : k - 2) * sl) : "memory");
        W.a[3] = (wave >> 2) * 12288 + ((ln >> 4) * 8 + ((ln >> 2) & 3)) * 384 + (ln & 3) * 8 + w4 * 96;
        W.a[4] = 0;
        W.a[5] = 0;
    }
}
#define PERM 0x78
#define PERMV 0x87
__device__ __forceinline__ int deal(int pw, int perm, unsigned a) {
    unsigned hw;
    asm volatile("s_getreg_b32 %0, hwreg(HW_REG_HW_ID, 4, 2)" : "=s"(hw));
    lds_vst(a + pw * 4, 1u << (hw + (pw & 4)));
    bar();
    const unsigned cov = lds_vld(a) | lds_vld(a + 4) | lds_vld(a + 8) | lds_vld(a + 12) | lds_vld(a + 16) | lds_vld(a + 20) | lds_vld(a + 24) | lds_vld(a + 28);
    return UNI(cov == 0xff && perm >= 0 ? (pw & 4) + ((perm >> (2 * hw)) & 3) : pw);
}
__device__ __forceinline__ void s_role(const Params& P, int pw, int L, int h, int cpy) {
    const int ln = lane_id(), wave = deal(pw, PERM, MEGA_LDS - 32);
    unsigned l16 = ln * 16;
    constexpr bool isv = false;
    WLoad W;
    wload_start<false>(W, P, pw, isv, h);
    W.a[3] += (wave - pw) * 96;
    const int mt = (wave >> 1) & 1, hf = wave & 1;
    const unsigned xs = D_XN + mt * 4096 + hf * WIN, x1 = (2 - hf) * WIN + hf * 2048, m0 = L_X + mt * 20480 + hf * 4096, m1 = L_X + mt * 20480 + 8192 + hf * 2048;
    const float SCL = 0.07216878364870322f * 1.4426950408889634f;
    const unsigned px = RG_PX + (h >> 2) * WIN;
    unsigned mb = UNI(blk(10, cpy)), fl = UNI(blk(15, cpy)), pb = UNI(wave == 0 ? blk(32 + (h >> 2), 0) + (h & 3) * 640 : px + (wave == 2 ? (h & 3) * 1024 : 5120 + ((h >> 1) & 1) * 1024 + (h & 1) * 256));
    unsigned b2 = l16 + (wave < 3 ? L_Q : wave == 3 ? L_QX : L_W + wave * 10240), dsto = c1_dst(wave, 1 + (wave >> 2)), bud;
    asm volatile(ASM_S_LOOP : LOOP_IO
                 : LOOP_IN, [xs] "s"(xs), [x1] "s"(x1), [m0] "s"(m0), [m1] "s"(m1), [qs] "s"(UNI(L_KX + hf * 1024 + mt * 8)), [q2] "s"(UNI(L_KX + 2048 + (wave & 3) * 1024)),
                   [pm] "s"(UNI(px + (wave == 2 ? 4096 : 7168) + (h & 3) * 256)), [scl] "s"(__float_as_uint(SCL)) LST_IN
                 : LOOP_CLOB);
    note_timeout(P, bud);
}

__device__ __forceinline__ void v_role(const Params& P, int pw, int L, int h, int cpy) {
    const int ln = lane_id(), dw = deal(pw, PERMV, L_W - 32), wave = pw < 4 ? dw : pw, w4 = wave & 3;
    unsigned l16 = ln * 16;
    constexpr bool isv = true;
    WLoad W;
    wload_start<false>(W, P, pw, isv, h);
    W.a[3] += (wave - pw) * 96;
    unsigned b2 = l16 + (wave < 3 ? L_Q : wave == 3 ? L_QX : L_W + wave * 10240), dsto = wave < 4 ? c1_dst(wave, 0) : (l16 >> 8) * GRP + (l16 & 255), bud;
    unsigned mb = UNI(wave < 4 ? blk(10, cpy) : blk(h >> 2, w4) + (h & 3) * 1024), fl = UNI(wave < 4 ? blk(15, cpy) : blk(14, w4) + h * 128), pb = UNI(blk(h & 16, w4) + (h & 15) * 256);
    if (wave >= 4) l16 += L_Q;
    asm volatile(ASM_V_LOOP : LOOP_IO
                 : LOOP_IN, [xs] "s"(D_XN + w4 * WIN) LST_IN
                 : LOOP_CLOB);
    note_timeout(P, bud);
}

#define D_W0 4
#define D_A1 8
#define D_R1 9
#define D_W1 12
__device__ __forceinline__ void delay(int n) { for (int i = 0; i < n; ++i) __builtin_amdgcn_s_sleep(64); }
__device__ __forceinline__ unsigned handshake(rsrc_t rw, unsigned mo) {
    wait_vm<0>();
    unsigned v = SENT;
    for (int spins = 0; spins < SPIN_CAP && v == SENT; ++spins) {
        asm volatile("s_sleep 4" ::: "memory");
        v = __builtin_amdgcn_readfirstlane(__builtin_amdgcn_raw_buffer_load_b32(rw, 0, mo, 16));
    }
    __builtin_amdgcn_raw_buffer_store_b32(1u, rw, lane_id() ? OOB : 32768u, mo, 16);
    return v & 0x7000;
}
template <class F>
__device__ __forceinline__ void x_tile(const Params& P, int tile, F target) {
    const int ln = lane_id(), l15 = ln & 15, l4 = ln >> 4;
    u32x4 v[20];
    float s = 0.f, q = 0.f;
#pragma unroll
    for (int c = 0; c < 20; ++c) v[c] = *(const u32x4*)(P.x + (size_t)(tile * 16 + l15) * DM + c * 32 + l4 * 8);
#pragma unroll
    for (int c = 0; c < 20; ++c)
#pragma unroll
        for (int e = 0; e < 4; ++e) s += lo2f(v[c][e]) + hi2f(v[c][e]);
    sum2_rows(s, q);
#pragma unroll
    for (int c = 0; c < 20; ++c) asm volatile("" : "+v"(v[c]));
    const float mean = s * (1.0f / DM);
#pragma unroll
    for (int c = 0; c < 20; ++c)
#pragma unroll
        for (int e = 0; e < 4; ++e) { const float a = lo2f(v[c][e]) - mean, b = hi2f(v[c][e]) - mean; q += a * a + b * b; }
    sum2_rows(s, q);
#pragma unroll
    for (int c = 0; c < 20; ++c) asm volatile("" : "+v"(v[c]));
    const float m1 = rbf(mean), r = 1.0f / ln_den(q * (1.0f / DM));
    const unsigned sb = segb(tile >> 1) + (tile & 1) * 4096;
    u32x4 n[20];
#pragma unroll
    for (int c = 0; c < 20; ++c) {
#pragma unroll
        for (int e = 0; e < 4; ++e) { const unsigned d = pack2(lo2f(v[c][e]) - m1, hi2f(v[c][e]) - m1); n[c][e] = pack2(lo2f(d) * r, hi2f(d) * r); }
        asm volatile("" : "+v"(n[c]));
    }
    const rsrc_t rr = target();
#pragma unroll
    for (int c = 0; c < 20; ++c) {
        st128(n[c], rr, ln * 16 + (c & 3) * 1024, D_XN + sb + (c >> 2) * WIN);
        st128(v[c], rr, ln * 16 + (c & 3) * 1024, D_XR + sb + (c >> 2) * WIN);
    }
    wait_vm<0>();
}
__device__ __forceinline__ void wait_go(const Params& P, rsrc_t rd, int k) {
    for (int spins = 0;; ++spins) {
        if (__builtin_amdgcn_readfirstlane(__builtin_amdgcn_raw_buffer_load_b32(rd, 0, FL_VO + (k & 31) * 128, 16))) break;
        asm volatile("s_sleep 16" ::: "memory");
        if (spin_fail(spins * 4 + 3, P)) break;
    }
    asm volatile("" ::: "memory");
}

constexpr unsigned R_RED = 0;
constexpr unsigned R_PART = 8192;
__device__ __forceinline__ void r_role(const Params& P, int wave, int L, int j, int cpy) {
    const int lane = lane_id(), l15 = lane & 15, l4 = lane >> 4;
    const unsigned l16 = lane * 16;
    const rsrc_t rr = mkrsrc(lbase(P, L), RG_L);
    const int sidx = (lane & 2) ? 0xEEEE : 0x4444;
    unsigned offp[3];
    const unsigned sel = P.par & 0x5000;
    auto hq = [&](int q) { return blk(32 + q, cpy) ^ sel; };
#define ROT 1
    const int hg = (wave + ROT * j) & 7;
    offp[0] = hq(hg) + (lane >> 5) * 640 + 128 + (lane & 31) * 16;
    offp[1] = offp[0] + 1280;
    offp[2] = (lane & 2) ? OOB : hq(hg) + l4 * 640 + ((l15 >> 2) * 2 + (l15 & 1)) * 16;
    const int jq = j / 10, j10 = j % 10;
    const unsigned ae = wave < 3 ? ((j10 < 8 && wave * 1024 + l16 < 2560) ? (blk(32 + j10, jq) ^ sel) + wave * 1024 + l16 : OOB)
                      : wave == 3 ? (blk(10 + (j10 >> 2), jq) ^ sel) + (j10 & 3) * 1024 + l16
                      : wave < 6 ? (j < 32 ? RG_PX + (j >> 2) * WIN + (j & 3) * 2048 + (wave & 1) * 1024 + l16 : OOB)
                      : wave == 7 ? D_XA + (j >> 3) * WIN + (j & 7) * 512 + (lane >> 5) * 4096 + (lane & 31) * 16 : OOB;
    const unsigned as = wave < 4 ? CH_SLOT : PX_SLOT;
    static_assert(AH == 3, "generated loop");
    const unsigned offv = (blk((hg >> 2) * 16 + j10, jq) ^ sel) + (hg & 3) * 1024 + l16;
    const unsigned offo = (wave < (int)NCP) ? (blk(10 + (j >> 4), wave) ^ sel) + (j & 15) * 256 + lane * 4 : OOB;
    {
        const u32x4 s4 = sent4();
#pragma unroll
        for (int k = 0; k < (int)NSLOT; ++k) {
            st128(s4, rr, offv, k * CH_SLOT);
            if (wave < 6 && (k || wave != 3)) st128(s4, rr, ae, k * as);
        }
        for (int u = j; u < 126; u += 40) st128(s4, rr, wave * 1024 + l16, u < 108 ? D_XA + (u < 60 ? u / 20 : 3) * 4 * REG + (u < 60 ? u % 20 : u - 60) * WIN : u < 124 ? RG_ST + (u - 108) * WIN : D_SS + (u - 124) * WIN);
        wait_vm<0>();
        bar();
        const int row = l4 * 2 + ((lane >> 2) & 1), col = 16 * j + 4 * (lane & 3) + 2 * ((lane >> 3) & 1);
        __builtin_amdgcn_raw_buffer_store_b32(nosent(*(const unsigned*)(P.mem0 + row * DM + col)), rr, offo, 0, 16);
    }
    auto red_addr = [](int fl) { return R_RED + fl * 80 + (fl >> 4) * 16; };
    const unsigned rw0 = red_addr(l4 * 16 + ((l15 >> 1) & 1) * 8 + (l15 >> 2)) + (l15 & 1) * 4 + (hg ^ (l15 >> 3)) * 8;
    const unsigned rrd = red_addr(lane);
    unsigned bud;
    asm volatile(ASM_R_LOOP : [bud] "=s"(bud)
                 : [wave] "s"(wave), [rs] "s"(rr), [l16] "v"(l16), [ov] "v"(offv), [o0] "v"(offp[0]), [o1] "v"(offp[1]), [o2] "v"(offp[2]), [oo] "v"(offo), [sidx] "v"(sidx), [rw] "v"(rw0), [rd] "v"(rrd),
                   [xo] "v"((l4 >> 1) * 256 + l15 * 16 + (l4 & 1) * 8), [pw] "v"(R_PART + hg * 1024 + l16), [pr] "v"(R_PART + (wave & 1) * 8192 + l16), [fo] "v"((lane & 3) * 128), [ae] "v"(ae),
                   [fs] "s"(UNI((blk(14, jq) ^ sel) + hg * 512)), [vx] "s"(UNI(vxb(j10, hg, jq) ^ sel)), [px] "s"(UNI(RG_PX + hg * WIN)),
                   [xr] "s"(UNI(D_XR + (j >> 3) * WIN + (wave & 1) * 4096 + (j & 7) * 512)), [ad] "s"(UNI(wave < 3 ? -1 : wave == 3 ? 0 : -2)), [as] "s"(UNI(as)) LST_IN
                 : "memory", "vcc", "scc", "v128", "v129", "v130", "v131", "v132", "v133", "v134", "v135", "v136", "v137", "v138", "v139", "v140", "v141", "v142", "v143", "v144", "v145", "v146", "v147", "v148", "v149", "v150", "v151", "v152", "v153", "v154", "v155", "v156", "v157", "v158", "v159", "v160", "v161", "v162", "v163", "v164", "v165", "v166", "v167", "v168", "v169", "v170", "v171", "v172", "v173", "v174", "v175", "v176", "v177", "v178", "v179", "v180", "v181", "v182", "v183", "v184", "v185", "v186", "v187", "v188", "v189", "v190", "v191", "v192", "v193", "v194", "v195", "v196", "v197", "v198", "v199", "v200", "v201", "v202", "v203", "v204", "v205", "v206", "v207", "v208", "v209", "v210", "v211", "v212", "v213", "v214", "v215", "v216", "v217", "v218", "v219", "v220", "v221", "v222", "v223", "v224", "v225", "v226", "v227", "v228", "v229", "v230", "v231", "v232", "v233", "v234", "v235", "v236", "v237", "v238", "v239", "v240", "v241", "v242", "v243", "v244", "v245", "v246", "v247", "v248", "v249", "v250", "v251", "v252", "v253", "v254", "v255", "s40", "s41", "s42", "s43", "s44", "s45", "s46", "s47", "s48", "s49", "s50", "s51", "s52", "s53", "s54", "s55", "s64", "s65", "s66", "s67", "s68", "s69", "s70", "s71", "s72", "s73", "s74", "s75", "s76", "s77", "s78", "s79");
    note_timeout(P, bud);
}

template <int WHICH>
__device__ __forceinline__ void w_role(const Params& P, int wave, int L, int cu) {
    constexpr int NT = (WHICH == 3) ? 2 : 1, N = (WHICH == 3) ? FF : DM, RB = NT * 256, RPI = 1024 / RB;
    const int ln = lane_id(), l15 = ln & 15, l4 = ln >> 4;
    const rsrc_t rd = mkrsrc(lbase(P, L), RG_L);
    const int nt0 = (cu * 8 + wave) * NT;
    const rsrc_t wr = mkrsrc((WHICH == 1 ? P.w1 : WHICH == 2 ? P.w2 : P.w3) + cu * (RB / 2), DM * N * 2 - cu * RB);
    auto swz = [](int row) { return ((row & 3) | ((row >> 1) & 4)) * 32; };
    const int dr = ln * 16 / RB, tr = l4 * 8 + ((ln >> 2) & 3);
    unsigned vo = dr * (N * 2) + ((ln * 16 % RB) ^ swz(wave * RPI + dr)), ta = tr * RB + ((wave * NT * 32 + (ln & 3) * 8) ^ swz(tr));
    const int grp = nt0 / 10, neg = grp & 1, sh = (WHICH == 1) ? (neg ? -1 : 1) * ((grp >> 1) + 1) : 0;
    unsigned ra = ln * 16, wa = ln * 16 + wave * 1024, sr = WHICH == 2 ? l15 * 8 : 24576 + l15 * 80, sw = sr + wave * 8, bud;
    const unsigned oo = WHICH == 3 ? (l4 & 1) * 512 + (l4 >> 1) * 256 + l15 * 16 : (nt0 >> 3) * WIN + (nt0 & 7) * 512 + (l4 >> 1) * 256 + ((l15 + sh) & 15) * 16 + (l4 & 1) * 8;
    unsigned up = ((unsigned)(l15 + sh) > 15u) ^ neg;
#define W_ARGS : [bud] "=s"(bud), "+{v176}"(vo), "+{v177}"(ta), "+{v249}"(ra), "+{v250}"(wa), "+{v251}"(sr), "+{v252}"(sw) \
               , "+{v255}"(up) : "{v253}"(ln * 16), "{v254}"(oo), [wave] "s"(wave), [rs] "s"(rd), [wr] "s"(wr), [wm] "s"(UNI(wave * 1024)), [wo] "s"(UNI(wave * RPI * N * 2)), [go] "s"(FL_VO + (WHICH * 8 + cu) * 128), \
                 [pi] "s"(UNI((WHICH == 1 ? D_XA : WHICH == 2 ? D_HS : D_X1) + wave * 1024)), \
                 [ng] "s"(UNI(WHICH == 3 ? D_H2 + (nt0 >> 3) * WIN + ((nt0 >> 1) & 3) * 1024 : neg)), [ss] "s"(UNI(cu == 0)) LST_IN LST_INW \
               : "memory", "vcc", "scc", "v0", "v1", "v2", "v3", "v4", "v5", "v6", "v7", "v8", "v9", "v10", "v11", "v12", "v13", "v14", "v15", "v16", "v17", "v18", "v19", "v20", "v21", "v22", "v23", "v24", "v25", "v26", "v27", "v28", "v29", "v30", "v31", "v32", "v33", "v34", "v35", "v36", "v37", "v38", "v39", "v40", "v41", "v42", "v43", "v44", "v45", "v46", "v47", "v48", "v49", "v50", "v51", "v52", "v53", "v54", "v55", "v56", "v57", "v58", "v59", "v60", "v61", "v62", "v63", "v64", "v65", "v66", "v67", "v68", "v69", "v70", "v71", "v72", "v73", "v74", "v75", "v76", "v77", "v78", "v79", "v80", "v81", "v82", "v83", "v84", "v85", "v86", "v87", "v88", "v89", "v90", "v91", "v92", "v93", "v94", "v95", "v96", "v97", "v98", "v99", "v100", "v101", "v102", "v103", "v104", "v105", "v106", "v107", "v108", "v109", "v110", "v111", "v112", "v113", "v114", "v115", "v116", "v117", "v118", "v119", "v120", "v121", "v122", "v123", "v124", "v125", "v126", "v127", "v128", "v129", "v130", "v131", "v132", "v133", "v134", "v135", "v136", "v137", "v138", "v139", "v140", "v141", "v142", "v143", "v144", "v145", "v146", "v147", "v148", "v149", "v150", "v151", "v152", "v153", "v154", "v155", "v156", "v157", "v158", "v159", "v160", "v161", "v162", "v163", "v164", "v165", "v166", "v167", "v168", "v169", "v170", "v171", "v172", "v173", "v174", "v175", "v178", "v179", "v180", "v181", "v182", "v183", "v184", "v185", "v186", "v187", "v188", "v189", "v190", "v191", "v192", "v193", "v194", "v195", "v196", "v197", "v198", "v199", "v200", "v201", "v202", "v203", "v204", "v205", "v206", "v207", "v208", "v209", "v210", "v211", "v212", "v213", "v214", "v215", "v216", "v217", "v218", "v219", "v220", "v221", "v222", "v223", "v224", "v225", "v226", "v227", "v228", "v229", "v230", "v231", "v232", "v233", "v234", "v235", "v236", "v237", "v238", "v239", "v240", "v241", "v242", "v243", "v244", "v245", "v246", "v247", "v248", "s40", "s41", "s42", "s43", "s44", "s45", "s46", "s47", "s48", "s49", "s50", "s51", "s52", "s53", "s54", "s55", "s56", "s57", "s58", "s59", "s60", "s61", "s62", "s63", "s64", "s65", "s66", "s67", "s68", "s69", "s70", "s71", "s72", "s73", "s74", "s75", "s76", "s77", "s78", "s79"
    if (WHICH == 1) asm volatile(ASM_W1_LOOP W_ARGS);
    if (WHICH == 2) asm volatile(ASM_W2_LOOP W_ARGS);
    if (WHICH == 3) asm volatile(ASM_W3_LOOP W_ARGS);
    note_timeout(P, bud);
}

constexpr unsigned W4_FL = 122880;
__device__ __forceinline__ void w4_role(const Params& P, int wave, int L, int cu) {
    const int ln = lane_id(), l15 = ln & 15, l4 = ln >> 4;
    const unsigned l16 = ln * 16;
    const rsrc_t rd = mkrsrc(lbase(P, L), RG_L);
    unsigned bud;
    if (wave >= 6) wait_go(P, rd, 32 + cu);
    if (wave < 6) {
        u32x32 w[5];
        {
            const rsrc_t wr = mkrsrc(P.w4 + wave * 256 * DM + cu * 80, (FF * DM - wave * 256 * DM - cu * 80) * 2);
            const unsigned ta = wave * 20480 + (l4 * 8 + ((ln >> 2) & 3)) * 160 + (ln & 3) * 8;
            u32x4 f[5][8];
#pragma unroll
            for (int r = 0; r < 2; ++r) {
#pragma unroll
                for (int i = 0; i < 5; ++i) {
                    const unsigned u = i * 64 + ln, vo = (u / 10) * (DM * 2) + (u % 10) * 16;
#pragma unroll
                    for (int k = 0; k < 4; ++k) dma1(wr, vo, (r * 128 + k * 32) * (DM * 2), wave * 20480 + k * 5120 + i * 1024);
                }
                wait_vm<0>();
#pragma unroll
                for (int k = 0; k < 4; ++k)
#pragma unroll
                    for (int n = 0; n < 5; ++n) f[n][r * 4 + k] = tr_frag(ta + k * 5120 + n * 32, 640);
                asm volatile("s_waitcnt lgkmcnt(0)" ::: "memory");
                SB();
            }
#pragma unroll
            for (int n = 0; n < 5; ++n) {
                u32x8 g[4];
#pragma unroll
                for (int i = 0; i < 4; ++i) g[i] = __builtin_shufflevector(f[n][2 * i], f[n][2 * i + 1], 0, 1, 2, 3, 4, 5, 6, 7);
                w[n] = cat32(g[0], g[1], g[2], g[3]);
            }
            asm volatile("" : "+{v[0:31]}"(w[0]), "+{v[32:63]}"(w[1]), "+{v[64:95]}"(w[2]), "+{v[96:127]}"(w[3]), "+{v[128:159]}"(w[4]));
        }
        wait_go(P, rd, 32 + cu);
        bar();
        unsigned ld = l16 + wave * 1024;
        asm volatile(ASM_W4_MUL : [bud] "=s"(bud), [ld] "+v"(ld)
                     : "{v[0:31]}"(w[0]), "{v[32:63]}"(w[1]), "{v[64:95]}"(w[2]), "{v[96:127]}"(w[3]), "{v[128:159]}"(w[4]), [l2] "v"(2 * ld + 61440), [l16] "v"(l16), [fa] "v"(W4_FL + wave * 4), [rs] "s"(rd),
                       [hb] "s"(UNI(D_H2 + wave * 2 * WIN)), [wave] "s"(wave), [ro] "s"(UNI(wave * 5120)), [fp] "v"(W4_FL + (ln < 6 ? ln : 0) * 4) LST_IN
                     : "memory", "vcc", "scc", "v160", "v161", "v162", "v163", "v164", "v165", "v166", "v167", "v168", "v169", "v170", "v171", "v172", "v173", "v174", "v175", "v176", "v177", "v178", "v179", "v180", "v181", "v182", "v183", "v184", "v185", "v186", "v187", "v188", "v189", "v190", "v191", "v192", "v193", "v194", "v195", "v196", "v197", "v198", "v199", "v200", "v201", "v202", "v203", "v204", "v205", "v206", "v207", "v208", "v209", "v210", "v211", "v212", "v213", "v214", "v215", "v216", "v217", "v218", "v219", "v220", "v221", "v222", "v223", "v224", "v225", "v226", "v227", "v228", "v229", "v230", "v231", "v232", "v233", "v234", "v235", "v236", "v237", "v238", "v239", "v240", "v241", "v242", "v243", "v244", "v245", "v246", "v247", "s40", "s41", "s42", "s43", "s44", "s45", "s46", "s47", "s48", "s49", "s50", "s51", "s52", "s53", "s54", "s55", "s64", "s65", "s66", "s67", "s68", "s69", "s70", "s71", "s72", "s73", "s74", "s75", "s76", "s77", "s78", "s79");
    } else {
        const int mt = wave - 6;
        if (ln < 16) lds_vst(W4_FL + ln * 4, 0u);
        bar();
        auto tl = [&](int n) { return (unsigned)((cu * 5 + n) >> 3) * WIN + ((cu * 5 + n) & 7) * 512 + mt * 4096; };
        const unsigned o8 = (l4 >> 1) * 256 + l15 * 16 + (l4 & 1) * 8;
        unsigned la = mt * 30720 + l16;
        static_assert(RG_ST == 112 * WIN && D_XN == 10 * REG && D_XR == 6 * REG, "generated loop");
#define F_IN [l2] "v"(2 * la + 61440), [fa] "v"(W4_FL + (ln < 6 ? ln : 0) * 4), [fb] "v"(W4_FL + 32 + (ln < 5 ? ln : 0) * 4), [fc] "v"(W4_FL + 24 + mt * 4), [rs] "s"(rd), [mt] "s"(mt), \
             [t0] "v"(o8 + tl(0)), [t1] "v"(o8 + tl(1)), [t2] "v"(o8 + tl(2)), [t3] "v"(o8 + tl(3)), [t4] "v"(o8 + tl(4))
#define F_CLOB "memory", "vcc", "scc", "v64", "v65", "v66", "v67", "v68", "v69", "v70", "v71", "v72", "v73", "v74", "v75", "v76", "v77", "v78", "v79", "v80", "v81", "v82", "v83", "v84", "v85", "v86", "v87", "v88", "v89", "v90", "v91", "v92", "v93", "v94", "v95", "v96", "v97", "v98", "v99", "v100", "v101", "v102", "v103", "v104", "v105", "v106", "v107", "v108", "v109", "v110", "v111", "v112", "v113", "v114", "v115", "v116", "v117", "v118", "v119", "v120", "v121", "v122", "v123", "v124", "v125", "v126", "v127", "v128", "v129", "v130", "v131", "v132", "v133", "v134", "v135", "v136", "v137", "v138", "v139", "v140", "v141", "v142", "v143", "v144", "v145", "v146", "v147", "v148", "v149", "v150", "v151", "v152", "v153", "v154", "v155", "v156", "v157", "v158", "v159", "v160", "v161", "v162", "v163", "v164", "v165", "v166", "v167", "v168", "v169", "v170", "v171", "v172", "v173", "v174", "v175", "v176", "v177", "v178", "v179", "v180", "v181", "v182", "v183", "v184", "v185", "v186", "v187", "v188", "v189", "v190", "v191", "v192", "v193", "v194", "v195", "v196", "v197", "v198", "v199", "v200", "v201", "v202", "v203", "v204", "v205", "v206", "v207", "v208", "v209", "v210", "v211", "v212", "v213", "v214", "v215", "v216", "v217", "v218", "v219", "v220", "v221", "v222", "v223", "v224", "v225", "v226", "v227", "v228", "v229", "v230", "v231", "v232", "v233", "v234", "v235", "v236", "v237", "v238", "v239", "s40", "s41", "s42", "s43", "s44", "s45", "s46", "s47", "s48", "s49", "s50", "s51", "s52", "s53", "s54", "s55", "s64", "s65", "s66", "s67", "s68", "s69", "s70", "s71", "s72", "s73", "s74", "s75", "s76", "s77", "s78", "s79"
        if (L) asm volatile(ASM_W4_FIN1 : [bud] "=s"(bud), [la] "+v"(la)
                            : F_IN, [ro] "s"(mkrsrc(P.out, ACT)), [co] "s"(cu * 160), [q8] "v"(l15 * (DM * 2) + l4 * 8), [q16] "v"(l15 * (DM * 2) + (l4 & 1) * 32 + (l4 >> 1) * 16) LST_IN : F_CLOB);
        else asm volatile(ASM_W4_FIN0 : [bud] "=s"(bud), [la] "+v"(la)
                          : F_IN, [rn] "s"(mkrsrc(lbase(P, 1), RG_L)), [co] "s"(cu * 128), [l8] "v"(ln * 8), [o16] "v"(tl(l4 & 1) + (l4 >> 1) * 256 + l15 * 16), [o17] "v"(tl(2 + (l4 & 1)) + (l4 >> 1) * 256 + l15 * 16),
                            [fo] "v"(ln < 32 ? blk(0, ln >> 3) - CH0 + (ln & 7) * 16 : OOB), [fs] "s"(FL_XN + (cu * 2 + mt) * 128) LST_IN : F_CLOB);
    }
    note_timeout(P, bud);
}

constexpr int LEADER = 252;
__device__ __forceinline__ unsigned leader(const Params& P, rsrc_t rw, int wave) {
    const int ln = lane_id();
    const u32x4 s4 = sent4(), z4 = s4 ^ s4;
    const unsigned mo = (wave * 32 + (ln >> 1)) * 128 + (ln & 1) * 16;
    __builtin_amdgcn_raw_buffer_store_b32(0u, rw, ln < 2 ? ln * 2048 : OOB, CAL0 + wave * 4096, 16);
    for (int k = 0; k < 6; ++k) st128(z4, rw, ln * 16, FL_VO + (k >> 2) * GRP + wave * 4096 + (k & 3) * 1024);
    st128(z4, rw, mo, FL_MB + 32768);
    if (wave == 0) st128(z4, rw, ln ? OOB : 256u, FL0);
    wait_vm<0>();
    bar();
    if (wave == 0) {
        const unsigned o = ln ? OOB : 0u;
        auto trip = [&](unsigned a) {
            unsigned long long t0, t1;
            asm volatile("s_memtime %0\n\ts_waitcnt lgkmcnt(0)\n\tbuffer_store_dword %2, %2, %3, %4 offen sc1\n\ts_waitcnt vmcnt(0)\n\ts_memtime %1\n\ts_waitcnt lgkmcnt(0)"
                         : "=&s"(t0), "=&s"(t1) : "v"(o), "s"(rw), "s"(CAL0 + a) : "memory");
            return (unsigned)(t1 - t0);
        };
        auto mn = [](unsigned a, unsigned b) { return a < b ? a : b; };
        const unsigned a0 = trip(0), b0 = trip(0x2000), a1 = trip(0x800), b1 = trip(0x2800);
        const unsigned h = mn(a0, a1) < mn(b0, b1) ? 0u : 0x2000u, t0 = h ? b0 + b1 : a0 + a1;
        const unsigned t14 = trip(h + 0x4000) + trip(h + 0x4800), t12 = trip(h + 0x1000) + trip(h + 0x1800);
        lds_vst(0, (h ^ 0x2000u) | (t14 < t0 ? 0x4000u : 0u) | (t12 < t0 ? 0x1000u : 0u));
    }
    bar();
    const unsigned par = __builtin_amdgcn_readfirstlane(lds_vld(0));
    for (int k = 0; k < 10; ++k) st128(s4, rw, ln * 16, CH0 + (10 + (k >> 2)) * GRP + wave * 4096 + (k & 3) * 1024);
    wait_vm<0>();
    bar();
    bool open = wave * 32 + (ln >> 1) != LEADER;
    for (int spins = 0; spins < SPIN_CAP; ++spins) {
        st128(s4 & par, rw, open ? mo : OOB, FL_MB);
        __builtin_amdgcn_s_sleep(20);
        const u32x4 d = LD128(rw, mo, FL_MB + 32768);
        open = open && !(d[0] && d[1] && d[2] && d[3]);
        if (!__any(open)) break;
    }
    bar();
    return par;
}
__global__ __launch_bounds__(512, 2) void mega_kernel(Params P) {
    const int wg = blockIdx.x;
    const int wave = __builtin_amdgcn_readfirstlane(threadIdx.x >> 6);
    const int L = (wg >> 2) & 1, x = wg & 3, r = wg >> 3, cpy = x;
    if (__builtin_expect(r < 16, 1)) {
        WLoad W;
        if (L) delay(D_A1);
        wload_start<true>(W, P, wave, r >= 8, x * 8 + (r & 7));
    }
    constexpr int nr = 10;
    const int j = x * 10 + r - 16, wk = r - 26;
    const int i = x == 0 ? 16 + wk : x == 2 ? 10 + wk : wk < 5 ? (x == 1 ? 5 : 0) + wk : 22 + (x >> 1);
    const rsrc_t rw = mkrsrc(P.ws, RG_L);
    const bool lead = wg == LEADER;
    CJIT(30);
    if (!lead) __builtin_amdgcn_raw_buffer_store_b32(SENT, rw, lane_id() ? OOB : 0u, FL_MB + wg * 128 + wave * 4, 16);
#define FIN() __builtin_amdgcn_endpgm()
    if (r < 8) { s_role(P, wave, L, x * 8 + r, cpy); FIN(); }
    if (r < 16) { v_role(P, wave, L, x * 8 + r - 8, cpy); FIN(); }
    if (lead) P.par = leader(P, rw, wave);
    else {
        const unsigned mo = FL_MB + wg * 128 + wave * 4;
        const bool ln = r - 16 < nr && j < 16, first = ln && !L && wave == 7;
        if (!first) { P.par = handshake(rw, mo); if (L || r - 16 >= nr) delay(!L ? D_W0 : r - 16 < nr ? D_R1 : D_W1); }
        if (first || (ln && L && wave)) x_tile(P, (L ? wave * 16 : 0) + j, [&]() { if (first) P.par = handshake(rw, mo); return mkrsrc(lbase(P, 0), RG_L); });
        if (ln && L) bar();
        if (first || (ln && L && !wave)) {
            const rsrc_t r0 = mkrsrc(lbase(P, 0), RG_L);
            const int lane = lane_id();
            const unsigned fo = lane < 32 ? blk(0, lane >> 3) - CH0 + (lane & 7) * 16 : OOB;
            if (L) for (int spins = 0; spins < SPIN_CAP && __any(lane < 32 && LD128(r0, fo, FL_XN + j * 128)[0] == 0u); ++spins) asm volatile("s_sleep 8" ::: "memory");
            st128(sent4() & (L ? 64u : 8u), r0, fo, FL_XN + j * 128);
        }
    }
    CJIT(30);
    if (r - 16 < nr) { r_role(P, wave, L, j, cpy); FIN(); }
    if (i < 5) { w_role<1>(P, wave, L, i); FIN(); }
    if (i < 10) { w_role<2>(P, wave, L, i - 5); FIN(); }
    if (i < 16) { w_role<3>(P, wave, L, i - 10); FIN(); }
    w4_role(P, wave, L, i - 16);
    FIN();
}

extern "C" void kernel_launch(void* const* d_in, const int* in_sizes, int n_in, void* d_out, int out_size, void* d_ws,
                              size_t ws_size, hipStream_t stream) {
    char* ws = (char*)d_ws + (-(size_t)d_ws & (REG - 1));
    Params P;
    P.par = 0; P.pad = 0; P.ws = ws; P.out = (bf16_t*)d_out;
    P.x = (const bf16_t*)d_in[0]; P.mem0 = (const bf16_t*)d_in[1]; P.wq = (const bf16_t*)d_in[2]; P.wk = (const bf16_t*)d_in[3]; P.wv = (const bf16_t*)d_in[4]; P.wo = (const bf16_t*)d_in[5];
    P.w1 = (const bf16_t*)d_in[8]; P.w2 = (const bf16_t*)d_in[9]; P.w3 = (const bf16_t*)d_in[10]; P.w4 = (const bf16_t*)d_in[11];
    (void)hipFuncSetAttribute((const void*)mega_kernel, hipFuncAttributeMaxDynamicSharedMemorySize, MEGA_LDS);

    mega_kernel<<<256, 512, MEGA_LDS, stream>>>(P);
}
```

```cpp
#include <hip/hip_runtime.h>
#include <stdint.h>

typedef unsigned short bf16_t;
using bf16x2 = __attribute__((ext_vector_type(2))) __bf16;
using f32x2 = __attribute__((ext_vector_type(2))) float;
using u32x2 = __attribute__((ext_vector_type(2))) unsigned;
using u32x4 = __attribute__((ext_vector_type(4))) unsigned;
typedef __amdgpu_buffer_rsrc_t rsrc_t;

__device__ __forceinline__ unsigned pack2(float a, float b) {
    bf16x2 r = __builtin_convertvector((f32x2){a, b}, bf16x2);
    return __builtin_bit_cast(unsigned, r);
}
__device__ __forceinline__ float rbf(float f) { return __uint_as_float(pack2(f, 0.f) << 16); }
__device__ __forceinline__ float lo2f(unsigned u) { return __uint_as_float(u << 16); }
__device__ __forceinline__ float hi2f(unsigned u) { return __uint_as_float(u & 0xFFFF0000u); }
__device__ __forceinline__ rsrc_t mkrsrc(const void* p, unsigned bytes) {
    return __builtin_amdgcn_make_buffer_rsrc((void*)p, 0, bytes, 0x00020000);
}

constexpr int S_ = 2048, DM = 640, NH = 32, DH = 192, HD = NH * DH  , SEG = 32, NSEG = 64;
constexpr int FF = 1536;

constexpr unsigned NSLOT = 4, NCP = 4;
constexpr unsigned WIN = 16384, REG = 2u << 20, REGS = REG + 8192;
constexpr unsigned GRP = 32768, CH0 = 48 * REG, CH_SLOT = 2 * REG;
__host__ __device__ constexpr unsigned blk(unsigned grp, unsigned x) { return CH0 + grp * GRP + (((x ^ (grp >> 4)) & 1) << 14) + ((((x >> 1) ^ (grp >> 5)) & 1) << 12); }
constexpr unsigned PX_SLOT = 8 * WIN;
constexpr unsigned RG_PX = 80 * WIN, RG_ST = RG_PX + NSLOT * PX_SLOT;
static_assert(RG_ST + 16 * WIN <= REG, "ring region");
constexpr unsigned FL0 = REG, FL_PAR = REG + 4096, FL_MB = REG + 65536;
constexpr unsigned FL_VO = blk(14, 0), FL_XN = blk(15, 0), D_SS = 2 * REG + 52 * WIN, CAL0 = blk(13, 0);
constexpr unsigned ACT = S_ * DM * 2;
constexpr unsigned D_XR = 6 * REG, D_XN = 10 * REG, D_XA = 14 * REG, D_HS = 18 * REG, D_X1 = 22 * REG, D_H2 = 26 * REG;
__host__ __device__ constexpr unsigned segb(unsigned seg) { return seg * (5 * WIN) + (seg >> 4) * (REGS - 80 * WIN); }
__host__ __device__ constexpr unsigned segh(unsigned seg) { return seg * (12 * WIN) + (seg >> 3) * (REGS - 96 * WIN); }
__host__ __device__ constexpr unsigned vxb(unsigned tile, unsigned hq, unsigned x) { return tile < 8 ? blk(tile * 8 + hq, x) + NSLOT * CH_SLOT : blk(tile * 8 - 16 + hq, x); }
constexpr int AH = 3;
constexpr unsigned RG_L = CH0 + 2 * NSLOT * CH_SLOT;

#define SB() __builtin_amdgcn_sched_barrier(0)
#define LAS __attribute__((address_space(3)))
__device__ __forceinline__ unsigned lds_vld(unsigned a) { asm volatile("" ::: "memory"); const unsigned v = *(volatile LAS unsigned*)(size_t)a; asm volatile("" ::: "memory"); return v; }
__device__ __forceinline__ void lds_vst(unsigned a, unsigned v) { asm volatile("" ::: "memory"); *(volatile LAS unsigned*)(size_t)a = v; asm volatile("" ::: "memory"); }
constexpr unsigned OOB = 0x7FFFFF00u;
template <int N> __device__ __forceinline__ void wait_vm() { asm volatile("s_waitcnt vmcnt(%0)" :: "i"(N) : "memory"); }
__device__ __forceinline__ void bar() {
    asm volatile("s_waitcnt lgkmcnt(0)" ::: "memory");
    __builtin_amdgcn_s_barrier();
    asm volatile("" ::: "memory");
}
__device__ __forceinline__ int lane_id() {
    int ln;
    asm volatile("v_mbcnt_lo_u32_b32 %0, -1, 0\n\tv_mbcnt_hi_u32_b32 %0, -1, %0" : "=v"(ln));
    return ln;
}
__device__ __forceinline__ void sum2_rows(float& s, float& q) {
    unsigned a = __float_as_uint(s), b = a, c = __float_as_uint(q), d = c;
    asm volatile("s_nop 2\n\tv_permlane16_swap_b32 %0, %1\n\tv_permlane16_swap_b32 %2, %3\n\tv_add_f32 %0, %0, %1\n\tv_add_f32 %2, %2, %3\n\tv_mov_b32 %1, %0\n\tv_mov_b32 %3, %2\n\t"
                 "s_nop 1\n\tv_permlane32_swap_b32 %0, %1\n\tv_permlane32_swap_b32 %2, %3\n\tv_add_f32 %0, %0, %1\n\tv_add_f32 %2, %2, %3" : "+v"(a), "+v"(b), "+v"(c), "+v"(d));
    s = __uint_as_float(a); q = __uint_as_float(c);
}
template <int AUXV>
__device__ __forceinline__ void st128_(u32x4 v, rsrc_t r, unsigned voff, unsigned soff) {
    __builtin_amdgcn_raw_buffer_store_b128(v, r, voff, soff, AUXV);
    asm volatile("s_nop 1" :: "v"(v) : "memory");
}
#define st128(V, R, VO_, SO_) st128_<16>(V, R, VO_, SO_)
#define LD128(R, VO_, SO_) __builtin_amdgcn_raw_buffer_load_b128(R, VO_, SO_, 16)
__device__ __forceinline__ float ln_den(float var) { return rbf(__builtin_amdgcn_sqrtf(rbf(rbf(var) + 1e-5f))); }

struct Params {
    char* ws;
    const bf16_t *x, *mem0, *wq, *wk, *wv, *wo, *w1, *w2, *w3, *w4;
    bf16_t* out;
    unsigned par, pad;
};

#define SENT 0xFFFFFFFFu
constexpr int SPIN_CAP = 400000;
__device__ __forceinline__ bool spin_fail(int spins, const Params& P) {
    if ((spins & 1023) == 1023) {
        const rsrc_t rt = mkrsrc(P.ws + FL0, 512);
        unsigned o;
        asm volatile("v_mov_b32 %0, 0x100" : "=v"(o));
        if (__builtin_amdgcn_readfirstlane(__builtin_amdgcn_raw_buffer_load_b32(rt, o, 0, 16)) != 0u) return true;
        if (spins > SPIN_CAP) { __builtin_amdgcn_raw_buffer_store_b32(o, rt, o, 0, 16); return true; }
    }
    return false;
}
__device__ __forceinline__ unsigned nosent(unsigned d) { return d < 0xFFFFFFFEu ? d : 0xFFFFFFFEu; }
__device__ __forceinline__ u32x4 sent4() { unsigned sv; asm volatile("v_mov_b32 %0, -1" : "=v"(sv)); return (u32x4){sv, sv, sv, sv}; }
__device__ __forceinline__ char* lbase(const Params& P, int L) { return P.ws + ((P.par ^ (L << 13)) & 0x2000); }

constexpr unsigned L_X = 0;
constexpr unsigned L_MEM = 40960;
constexpr unsigned L_Q = 51200;
constexpr unsigned L_KM = 54272;
constexpr unsigned L_QX = 57344;
constexpr unsigned L_KX = 69632;
constexpr unsigned L_W = 81920;
constexpr int MEGA_LDS = 163840;

#define ASM_S_LOOP \
    "s_mov_b64 s[60:61], %[ws]\n\t" \
    "s_mov_b32 s62, %[sz]\n\t" \
    "s_mov_b32 s63, 0x20000\n\t" \
    "s_mov_b32 s40, 0\n\t" \
    "s_mov_b32 s41, 0x20000\n\t" \
    "s_cmp_eq_u32 %[wave], 0\n\t" \
    "s_cbranch_scc1 .Ls0_%=\n\t" \
    "s_cmp_eq_u32 %[wave], 1\n\t" \
    "s_cbranch_scc1 .Ls1_%=\n\t" \
    "s_cmp_eq_u32 %[wave], 2\n\t" \
    "s_cbranch_scc1 .Ls2_%=\n\t" \
    "s_cmp_eq_u32 %[wave], 3\n\t" \
    "s_cbranch_scc1 .Ls3_%=\n\t" \
    ".Ls4_%=:\n\t" \
    "s_mov_b32 s42, %[xs]\n\t" \
    "v_add_u32 v244, 0x18000, v243\n\t" \
    "s_waitcnt vmcnt(12) lgkmcnt(0)\n\t" \
    "s_barrier \n\t" \
    "s_add_u32 m0, %[wm], 0x12000\n\t" \
    "s_mov_b32 s44, 0x1e0000\n\t" \
    "buffer_load_dwordx4 v240, %[wr], s44 offen offset:0 nt lds\n\t" \
    "buffer_load_dwordx4 v241, %[wr], s44 offen offset:1024 nt lds\n\t" \
    "buffer_load_dwordx4 v242, %[wr], s44 offen offset:2048 nt lds\n\t" \
    "ds_read_b64_tr_b16 v[200:201], v244 offset:0\n\t" \
    "ds_read_b64_tr_b16 v[202:203], v244 offset:1536\n\t" \
    "ds_read_b64_tr_b16 v[0:1], v244 offset:32\n\t" \
    "ds_read_b64_tr_b16 v[2:3], v244 offset:1568\n\t" \
    "ds_read_b64_tr_b16 v[8:9], v244 offset:64\n\t" \
    "ds_read_b64_tr_b16 v[10:11], v244 offset:1600\n\t" \
    "v_add_u32 v244, 0x1e000, v243\n\t" \
    "s_waitcnt vmcnt(12) lgkmcnt(0)\n\t" \
    "s_barrier \n\t" \
    "s_add_u32 m0, %[wm], 0x18000\n\t" \
    "s_mov_b32 s44, 0x240000\n\t" \
    "buffer_load_dwordx4 v240, %[wr], s44 offen offset:0 nt lds\n\t" \
    "buffer_load_dwordx4 v241, %[wr], s44 offen offset:1024 nt lds\n\t" \
    "buffer_load_dwordx4 v242, %[wr], s44 offen offset:2048 nt lds\n\t" \
    "ds_read_b64_tr_b16 v[204:205], v244 offset:0\n\t" \
    "ds_read_b64_tr_b16 v[206:207], v244 offset:1536\n\t" \
    "ds_read_b64_tr_b16 v[4:5], v244 offset:32\n\t" \
    "ds_read_b64_tr_b16 v[6:7], v244 offset:1568\n\t" \
    "ds_read_b64_tr_b16 v[12:13], v244 offset:64\n\t" \
    "ds_read_b64_tr_b16 v[14:15], v244 offset:1600\n\t" \
    "v_add_u32 v244, 0x0, v243\n\t" \
    "s_waitcnt vmcnt(12) lgkmcnt(0)\n\t" \
    "s_barrier \n\t" \
    "s_add_u32 m0, %[wm], 0x1e000\n\t" \
    "s_mov_b32 s44, 0x2a0000\n\t" \
    "buffer_load_dwordx4 v240, %[wr], s44 offen offset:0 nt lds\n\t" \
    "buffer_load_dwordx4 v241, %[wr], s44 offen offset:1024 nt lds\n\t" \
    "buffer_load_dwordx4 v242, %[wr], s44 offen offset:2048 nt lds\n\t" \
    "ds_read_b64_tr_b16 v[16:17], v244 offset:0\n\t" \
    "ds_read_b64_tr_b16 v[18:19], v244 offset:1536\n\t" \
    "ds_read_b64_tr_b16 v[24:25], v244 offset:32\n\t" \
    "ds_read_b64_tr_b16 v[26:27], v244 offset:1568\n\t" \
    "ds_read_b64_tr_b16 v[32:33], v244 offset:64\n\t" \
    "ds_read_b64_tr_b16 v[34:35], v244 offset:1600\n\t" \
    "v_add_u32 v244, 0x6000, v243\n\t" \
    "s_waitcnt vmcnt(12) lgkmcnt(0)\n\t" \
    "s_barrier \n\t" \
    "s_add_u32 m0, %[wm], 0x0\n\t" \
    "s_mov_b32 s44, 0x300000\n\t" \
    "buffer_load_dwordx4 v240, %[wr], s44 offen offset:0 nt lds\n\t" \
    "buffer_load_dwordx4 v241, %[wr], s44 offen offset:1024 nt lds\n\t" \
    "buffer_load_dwordx4 v242, %[wr], s44 offen offset:2048 nt lds\n\t" \
    "ds_read_b64_tr_b16 v[20:21], v244 offset:0\n\t" \
    "ds_read_b64_tr_b16 v[22:23], v244 offset:1536\n\t" \
    "ds_read_b64_tr_b16 v[28:29], v244 offset:32\n\t" \
    "ds_read_b64_tr_b16 v[30:31], v244 offset:1568\n\t" \
    "ds_read_b64_tr_b16 v[36:37], v244 offset:64\n\t" \
    "ds_read_b64_tr_b16 v[38:39], v244 offset:1600\n\t" \
    "v_add_u32 v244, 0xc000, v243\n\t" \
    "s_waitcnt vmcnt(12) lgkmcnt(0)\n\t" \
    "s_barrier \n\t" \
    "s_add_u32 m0, %[wm], 0x6000\n\t" \
    "s_mov_b32 s44, 0x360000\n\t" \
    "buffer_load_dwordx4 v240, %[wr], s44 offen offset:0 nt lds\n\t" \
    "buffer_load_dwordx4 v241, %[wr], s44 offen offset:1024 nt lds\n\t" \
    "buffer_load_dwordx4 v242, %[wr], s44 offen offset:2048 nt lds\n\t" \
    "ds_read_b64_tr_b16 v[208:209], v244 offset:0\n\t" \
    "ds_read_b64_tr_b16 v[210:211], v244 offset:1536\n\t" \
    "ds_read_b64_tr_b16 v[40:41], v244 offset:32\n\t" \
    "ds_read_b64_tr_b16 v[42:43], v244 offset:1568\n\t" \
    "ds_read_b64_tr_b16 v[48:49], v244 offset:64\n\t" \
    "ds_read_b64_tr_b16 v[50:51], v244 offset:1600\n\t" \
    "v_add_u32 v244, 0x12000, v243\n\t" \
    "s_waitcnt vmcnt(12) lgkmcnt(0)\n\t" \
    "s_barrier \n\t" \
    "s_add_u32 m0, %[wm], 0xc000\n\t" \
    "s_mov_b32 s44, 0x3c0000\n\t" \
    "buffer_load_dwordx4 v240, %[wr], s44 offen offset:0 nt lds\n\t" \
    "buffer_load_dwordx4 v241, %[wr], s44 offen offset:1024 nt lds\n\t" \
    "buffer_load_dwordx4 v242, %[wr], s44 offen offset:2048 nt lds\n\t" \
    "ds_read_b64_tr_b16 v[212:213], v244 offset:0\n\t" \
    "ds_read_b64_tr_b16 v[214:215], v244 offset:1536\n\t" \
    "ds_read_b64_tr_b16 v[44:45], v244 offset:32\n\t" \
    "ds_read_b64_tr_b16 v[46:47], v244 offset:1568\n\t" \
    "ds_read_b64_tr_b16 v[52:53], v244 offset:64\n\t" \
    "ds_read_b64_tr_b16 v[54:55], v244 offset:1600\n\t" \
    "v_add_u32 v244, 0x18000, v243\n\t" \
    "s_waitcnt vmcnt(12) lgkmcnt(0)\n\t" \
    "s_barrier \n\t" \
    "s_add_u32 m0, %[wm], 0x12000\n\t" \
    "s_mov_b32 s44, 0x420000\n\t" \
    "buffer_load_dwordx4 v240, %[wr], s44 offen offset:0 nt lds\n\t" \
    "buffer_load_dwordx4 v241, %[wr], s44 offen offset:1024 nt lds\n\t" \
    "buffer_load_dwordx4 v242, %[wr], s44 offen offset:2048 nt lds\n\t" \
    "ds_read_b64_tr_b16 v[56:57], v244 offset:0\n\t" \
    "ds_read_b64_tr_b16 v[58:59], v244 offset:1536\n\t" \
    "ds_read_b64_tr_b16 v[64:65], v244 offset:32\n\t" \
    "ds_read_b64_tr_b16 v[66:67], v244 offset:1568\n\t" \
    "ds_read_b64_tr_b16 v[72:73], v244 offset:64\n\t" \
    "ds_read_b64_tr_b16 v[74:75], v244 offset:1600\n\t" \
    "v_add_u32 v244, 0x1e000, v243\n\t" \
    "s_waitcnt vmcnt(12) lgkmcnt(0)\n\t" \
    "s_barrier \n\t" \
    "s_add_u32 m0, %[wm], 0x18000\n\t" \
    "s_mov_b32 s44, 0x480000\n\t" \
    "buffer_load_dwordx4 v240, %[wr], s44 offen offset:0 nt lds\n\t" \
    "buffer_load_dwordx4 v241, %[wr], s44 offen offset:1024 nt lds\n\t" \
    "buffer_load_dwordx4 v242, %[wr], s44 offen offset:2048 nt lds\n\t" \
    "ds_read_b64_tr_b16 v[60:61], v244 offset:0\n\t" \
    "ds_read_b64_tr_b16 v[62:63], v244 offset:1536\n\t" \
    "ds_read_b64_tr_b16 v[68:69], v244 offset:32\n\t" \
    "ds_read_b64_tr_b16 v[70:71], v244 offset:1568\n\t" \
    "ds_read_b64_tr_b16 v[76:77], v244 offset:64\n\t" \
    "ds_read_b64_tr_b16 v[78:79], v244 offset:1600\n\t" \
    "v_add_u32 v244, 0x0, v243\n\t" \
    "s_waitcnt vmcnt(12) lgkmcnt(0)\n\t" \
    "s_barrier \n\t" \
    "v_mov_b32 v246, 0\n\t" \
    "buffer_load_dword v246, v246, s[60:63], %[mo] offen sc1\n\t" \
    "s_add_u32 m0, %[wm], 0x1e000\n\t" \
    "s_mov_b32 s44, 0x4e0000\n\t" \
    "buffer_load_dwordx4 v240, %[wr], s44 offen offset:0 nt lds\n\t" \
    "buffer_load_dwordx4 v241, %[wr], s44 offen offset:1024 nt lds\n\t" \
    "buffer_load_dwordx4 v242, %[wr], s44 offen offset:2048 nt lds\n\t" \
    "ds_read_b64_tr_b16 v[216:217], v244 offset:0\n\t" \
    "ds_read_b64_tr_b16 v[218:219], v244 offset:1536\n\t" \
    "ds_read_b64_tr_b16 v[80:81], v244 offset:32\n\t" \
    "ds_read_b64_tr_b16 v[82:83], v244 offset:1568\n\t" \
    "ds_read_b64_tr_b16 v[88:89], v244 offset:64\n\t" \
    "ds_read_b64_tr_b16 v[90:91], v244 offset:1600\n\t" \
    "v_add_u32 v244, 0x6000, v243\n\t" \
    "s_waitcnt vmcnt(12) lgkmcnt(0)\n\t" \
    "s_barrier \n\t" \
    "s_add_u32 m0, %[wm], 0x0\n\t" \
    "s_mov_b32 s44, 0x540000\n\t" \
    "buffer_load_dwordx4 v240, %[wr], s44 offen offset:0 nt lds\n\t" \
    "buffer_load_dwordx4 v241, %[wr], s44 offen offset:1024 nt lds\n\t" \
    "buffer_load_dwordx4 v242, %[wr], s44 offen offset:2048 nt lds\n\t" \
    "ds_read_b64_tr_b16 v[220:221], v244 offset:0\n\t" \
    "ds_read_b64_tr_b16 v[222:223], v244 offset:1536\n\t" \
    "ds_read_b64_tr_b16 v[84:85], v244 offset:32\n\t" \
    "ds_read_b64_tr_b16 v[86:87], v244 offset:1568\n\t" \
    "ds_read_b64_tr_b16 v[92:93], v244 offset:64\n\t" \
    "ds_read_b64_tr_b16 v[94:95], v244 offset:1600\n\t" \
    "v_add_u32 v244, 0xc000, v243\n\t" \
    "s_waitcnt vmcnt(12) lgkmcnt(0)\n\t" \
    "s_barrier \n\t" \
    "s_add_u32 m0, %[wm], 0x6000\n\t" \
    "s_mov_b32 s44, 0x5a0000\n\t" \
    "buffer_load_dwordx4 v240, %[wr], s44 offen offset:0 nt lds\n\t" \
    "buffer_load_dwordx4 v241, %[wr], s44 offen offset:1024 nt lds\n\t" \
    "buffer_load_dwordx4 v242, %[wr], s44 offen offset:2048 nt lds\n\t" \
    "ds_read_b64_tr_b16 v[96:97], v244 offset:0\n\t" \
    "ds_read_b64_tr_b16 v[98:99], v244 offset:1536\n\t" \
    "ds_read_b64_tr_b16 v[104:105], v244 offset:32\n\t" \
    "ds_read_b64_tr_b16 v[106:107], v244 offset:1568\n\t" \
    "ds_read_b64_tr_b16 v[112:113], v244 offset:64\n\t" \
    "ds_read_b64_tr_b16 v[114:115], v244 offset:1600\n\t" \
    "v_add_u32 v244, 0x12000, v243\n\t" \
    "s_waitcnt vmcnt(12) lgkmcnt(0)\n\t" \
    "s_barrier \n\t" \
    "s_add_u32 m0, %[wm], 0xc000\n\t" \
    "s_mov_b32 s44, 0x600000\n\t" \
    "buffer_load_dwordx4 v240, %[wr], s44 offen offset:0 nt lds\n\t" \
    "buffer_load_dwordx4 v241, %[wr], s44 offen offset:1024 nt lds\n\t" \
    "buffer_load_dwordx4 v242, %[wr], s44 offen offset:2048 nt lds\n\t" \
    "ds_read_b64_tr_b16 v[100:101], v244 offset:0\n\t" \
    "ds_read_b64_tr_b16 v[102:103], v244 offset:1536\n\t" \
    "ds_read_b64_tr_b16 v[108:109], v244 offset:32\n\t" \
    "ds_read_b64_tr_b16 v[110:111], v244 offset:1568\n\t" \
    "ds_read_b64_tr_b16 v[116:117], v244 offset:64\n\t" \
    "ds_read_b64_tr_b16 v[118:119], v244 offset:1600\n\t" \
    "v_add_u32 v244, 0x18000, v243\n\t" \
    "s_waitcnt vmcnt(12) lgkmcnt(0)\n\t" \
    "s_barrier \n\t" \
    "s_add_u32 m0, %[wm], 0x12000\n\t" \
    "s_mov_b32 s44, 0x660000\n\t" \
    "buffer_load_dwordx4 v240, %[wr], s44 offen offset:0 nt lds\n\t" \
    "buffer_load_dwordx4 v241, %[wr], s44 offen offset:1024 nt lds\n\t" \
    "buffer_load_dwordx4 v242, %[wr], s44 offen offset:2048 nt lds\n\t" \
    "ds_read_b64_tr_b16 v[224:225], v244 offset:0\n\t" \
    "ds_read_b64_tr_b16 v[226:227], v244 offset:1536\n\t" \
    "ds_read_b64_tr_b16 v[120:121], v244 offset:32\n\t" \
    "ds_read_b64_tr_b16 v[122:123], v244 offset:1568\n\t" \
    "ds_read_b64_tr_b16 v[128:129], v244 offset:64\n\t" \
    "ds_read_b64_tr_b16 v[130:131], v244 offset:1600\n\t" \
    "v_add_u32 v244, 0x1e000, v243\n\t" \
    "s_waitcnt vmcnt(12) lgkmcnt(0)\n\t" \
    "s_barrier \n\t" \
    ".Ls41_%=:\n\t" \
    "v_readfirstlane_b32 s43, v246\n\t" \
    "s_nop 0\n\t" \
    "s_cmp_lg_u32 s43, -1\n\t" \
    "s_cbranch_scc1 .Ls42_%=\n\t" \
    "s_cmp_eq_u32 s41, 0\n\t" \
    "s_cbranch_scc1 .Ls42_%=\n\t" \
    "s_sub_u32 s41, s41, 1\n\t" \
    "s_sleep 4\n\t" \
    "v_mov_b32 v246, 0\n\t" \
    "buffer_load_dword v246, v246, s[60:63], %[mo] offen sc1\n\t" \
    "s_waitcnt vmcnt(0)\n\t" \
    "s_branch .Ls41_%=\n\t" \
    ".Ls42_%=:\n\t" \
    "v_mov_b32 v246, 0x8000\n\t" \
    "s_mov_b64 exec, 1\n\t" \
    "buffer_store_dword v246, v246, s[60:63], %[mo] offen sc1\n\t" \
    "s_mov_b64 exec, -1\n\t" \
    "s_and_b32 s44, s43, 0x5000\n\t" \
    "s_xor_b32 %[mb], %[mb], s44\n\t" \
    "s_xor_b32 %[fl], %[fl], s44\n\t" \
    "s_and_b32 s43, s43, 0x2000\n\t" \
    "s_xor_b32 s43, s43, %[lx]\n\t" \
    "s_add_u32 s60, s60, s43\n\t" \
    "s_addc_u32 s61, s61, 0\n\t" \
    "v_and_b32 v252, 0xf0, v253\n\t" \
    "v_lshlrev_b32 v252, 3, v252\n\t" \
    "buffer_load_dword v252, v252, s[60:63], %[fl] offen sc1\n\t" \
    "s_add_u32 m0, %[wm], 0x18000\n\t" \
    "s_mov_b32 s44, 0x6c0000\n\t" \
    "buffer_load_dwordx4 v240, %[wr], s44 offen offset:0 nt lds\n\t" \
    "buffer_load_dwordx4 v241, %[wr], s44 offen offset:1024 nt lds\n\t" \
    "buffer_load_dwordx4 v242, %[wr], s44 offen offset:2048 nt lds\n\t" \
    "ds_read_b64_tr_b16 v[228:229], v244 offset:0\n\t" \
    "ds_read_b64_tr_b16 v[230:231], v244 offset:1536\n\t" \
    "ds_read_b64_tr_b16 v[124:125], v244 offset:32\n\t" \
    "ds_read_b64_tr_b16 v[126:127], v244 offset:1568\n\t" \
    "ds_read_b64_tr_b16 v[132:133], v244 offset:64\n\t" \
    "ds_read_b64_tr_b16 v[134:135], v244 offset:1600\n\t" \
    "v_add_u32 v244, 0x0, v243\n\t" \
    "s_waitcnt vmcnt(12) lgkmcnt(0)\n\t" \
    "s_barrier \n\t" \
    "s_add_u32 m0, %[wm], 0x1e000\n\t" \
    "s_mov_b32 s44, 0x720000\n\t" \
    "buffer_load_dwordx4 v240, %[wr], s44 offen offset:0 nt lds\n\t" \
    "buffer_load_dwordx4 v241, %[wr], s44 offen offset:1024 nt lds\n\t" \
    "buffer_load_dwordx4 v242, %[wr], s44 offen offset:2048 nt lds\n\t" \
    "ds_read_b64_tr_b16 v[136:137], v244 offset:0\n\t" \
    "ds_read_b64_tr_b16 v[138:139], v244 offset:1536\n\t" \
    "ds_read_b64_tr_b16 v[144:145], v244 offset:32\n\t" \
    "ds_read_b64_tr_b16 v[146:147], v244 offset:1568\n\t" \
    "ds_read_b64_tr_b16 v[152:153], v244 offset:64\n\t" \
    "ds_read_b64_tr_b16 v[154:155], v244 offset:1600\n\t" \
    "v_add_u32 v244, 0x6000, v243\n\t" \
    "s_waitcnt vmcnt(12) lgkmcnt(0)\n\t" \
    "s_barrier \n\t" \
    "ds_read_b64_tr_b16 v[140:141], v244 offset:0\n\t" \
    "ds_read_b64_tr_b16 v[142:143], v244 offset:1536\n\t" \
    "ds_read_b64_tr_b16 v[148:149], v244 offset:32\n\t" \
    "ds_read_b64_tr_b16 v[150:151], v244 offset:1568\n\t" \
    "ds_read_b64_tr_b16 v[156:157], v244 offset:64\n\t" \
    "ds_read_b64_tr_b16 v[158:159], v244 offset:1600\n\t" \
    "v_add_u32 v244, 0xc000, v243\n\t" \
    "s_waitcnt vmcnt(9) lgkmcnt(0)\n\t" \
    "s_barrier \n\t" \
    "ds_read_b64_tr_b16 v[232:233], v244 offset:0\n\t" \
    "ds_read_b64_tr_b16 v[234:235], v244 offset:1536\n\t" \
    "ds_read_b64_tr_b16 v[160:161], v244 offset:32\n\t" \
    "ds_read_b64_tr_b16 v[162:163], v244 offset:1568\n\t" \
    "ds_read_b64_tr_b16 v[168:169], v244 offset:64\n\t" \
    "ds_read_b64_tr_b16 v[170:171], v244 offset:1600\n\t" \
    "v_add_u32 v244, 0x12000, v243\n\t" \
    "s_waitcnt vmcnt(6) lgkmcnt(0)\n\t" \
    "s_barrier \n\t" \
    "ds_read_b64_tr_b16 v[236:237], v244 offset:0\n\t" \
    "ds_read_b64_tr_b16 v[238:239], v244 offset:1536\n\t" \
    "ds_read_b64_tr_b16 v[164:165], v244 offset:32\n\t" \
    "ds_read_b64_tr_b16 v[166:167], v244 offset:1568\n\t" \
    "ds_read_b64_tr_b16 v[172:173], v244 offset:64\n\t" \
    "ds_read_b64_tr_b16 v[174:175], v244 offset:1600\n\t" \
    "v_add_u32 v244, 0x18000, v243\n\t" \
    "s_waitcnt vmcnt(3) lgkmcnt(0)\n\t" \
    "s_barrier \n\t" \
    "ds_read_b64_tr_b16 v[176:177], v244 offset:0\n\t" \
    "ds_read_b64_tr_b16 v[178:179], v244 offset:1536\n\t" \
    "ds_read_b64_tr_b16 v[184:185], v244 offset:32\n\t" \
    "ds_read_b64_tr_b16 v[186:187], v244 offset:1568\n\t" \
    "ds_read_b64_tr_b16 v[192:193], v244 offset:64\n\t" \
    "ds_read_b64_tr_b16 v[194:195], v244 offset:1600\n\t" \
    "s_mov_b32 s43, 0\n\t" \
    "v_cmp_eq_u32 vcc, 0, v252\n\t" \
    "s_cbranch_vccnz .Ls43_%=\n\t" \
    "s_mov_b32 m0, %[m0]\n\t" \
    "s_add_u32 s45, s42, %[x1]\n\t" \
    "buffer_load_dwordx4 v253, s[60:63], s42 offen offset:0 sc1 lds\n\t" \
    "buffer_load_dwordx4 v253, s[60:63], s42 offen offset:1024 sc1 lds\n\t" \
    "buffer_load_dwordx4 v253, s[60:63], s42 offen offset:2048 sc1 lds\n\t" \
    "buffer_load_dwordx4 v253, s[60:63], s42 offen offset:3072 sc1 lds\n\t" \
    "s_mov_b32 m0, %[m1]\n\t" \
    "s_nop 0\n\t" \
    "buffer_load_dwordx4 v253, s[60:63], s45 offen offset:0 sc1 lds\n\t" \
    "buffer_load_dwordx4 v253, s[60:63], s45 offen offset:1024 sc1 lds\n\t" \
    "s_mov_b32 s43, 1\n\t" \
    ".Ls43_%=:\n\t" \
    "v_add_u32 v244, 0x1e000, v243\n\t" \
    "s_waitcnt vmcnt(0) lgkmcnt(0)\n\t" \
    "s_barrier \n\t" \
    "ds_read_b64_tr_b16 v[180:181], v244 offset:0\n\t" \
    "ds_read_b64_tr_b16 v[182:183], v244 offset:1536\n\t" \
    "ds_read_b64_tr_b16 v[188:189], v244 offset:32\n\t" \
    "ds_read_b64_tr_b16 v[190:191], v244 offset:1568\n\t" \
    "ds_read_b64_tr_b16 v[196:197], v244 offset:64\n\t" \
    "ds_read_b64_tr_b16 v[198:199], v244 offset:1600\n\t" \
    "s_waitcnt lgkmcnt(0)\n\t" \
    "s_barrier \n\t" \
    "ds_write_b128 v254, v[200:203] offset:0\n\t" \
    "ds_write_b128 v254, v[204:207] offset:1024\n\t" \
    "ds_write_b128 v254, v[208:211] offset:2048\n\t" \
    "ds_write_b128 v254, v[212:215] offset:3072\n\t" \
    "ds_write_b128 v254, v[216:219] offset:4096\n\t" \
    "ds_write_b128 v254, v[220:223] offset:5120\n\t" \
    "ds_write_b128 v254, v[224:227] offset:6144\n\t" \
    "ds_write_b128 v254, v[228:231] offset:7168\n\t" \
    "ds_write_b128 v254, v[232:235] offset:8192\n\t" \
    "ds_write_b128 v254, v[236:239] offset:9216\n\t" \
    "s_cmp_lg_u32 s43, 0\n\t" \
    "s_cbranch_scc1 .Ls44_%=\n\t" \
    "s_mov_b32 s44, 0\n\t" \
    ".Ls45_%=:\n\t" \
    "v_cmp_ge_u32 vcc, s44, v252\n\t" \
    "s_cbranch_vccz .Ls46_%=\n\t" \
    "s_cmp_eq_u32 s41, 0\n\t" \
    "s_cbranch_scc1 .Ls46_%=\n\t" \
    "s_sub_u32 s41, s41, 1\n\t" \
    "s_sleep 8\n\t" \
    "v_and_b32 v252, 0xf0, v253\n\t" \
    "v_lshlrev_b32 v252, 3, v252\n\t" \
    "buffer_load_dword v252, v252, s[60:63], %[fl] offen sc1\n\t" \
    "s_waitcnt vmcnt(0)\n\t" \
    "s_branch .Ls45_%=\n\t" \
    ".Ls46_%=:\n\t" \
    "s_mov_b32 m0, %[m0]\n\t" \
    "s_add_u32 s45, s42, %[x1]\n\t" \
    "buffer_load_dwordx4 v253, s[60:63], s42 offen offset:0 sc1 lds\n\t" \
    "buffer_load_dwordx4 v253, s[60:63], s42 offen offset:1024 sc1 lds\n\t" \
    "buffer_load_dwordx4 v253, s[60:63], s42 offen offset:2048 sc1 lds\n\t" \
    "buffer_load_dwordx4 v253, s[60:63], s42 offen offset:3072 sc1 lds\n\t" \
    "s_mov_b32 m0, %[m1]\n\t" \
    "s_nop 0\n\t" \
    "buffer_load_dwordx4 v253, s[60:63], s45 offen offset:0 sc1 lds\n\t" \
    "buffer_load_dwordx4 v253, s[60:63], s45 offen offset:1024 sc1 lds\n\t" \
    ".Ls44_%=:\n\t" \
    "s_waitcnt vmcnt(0) lgkmcnt(0)\n\t" \
    "s_barrier \n\t" \
    "ds_read_b128 v[224:227], v253 offset:0\n\t" \
    "ds_read_b128 v[228:231], v253 offset:20480\n\t" \
    "ds_read_b128 v[232:235], v253 offset:1024\n\t" \
    "ds_read_b128 v[236:239], v253 offset:21504\n\t" \
    "ds_read_b128 v[244:247], v254 offset:0\n\t" \
    "ds_read_b128 v[248:251], v254 offset:1024\n\t" \
    ".p2align 8\n\t" \
    ".Ls47_%=:\n\t" \
    "s_cmp_lg_u32 %[wave], 4\n\t" \
    "s_cbranch_scc1 .Ls48_%=\n\t" \
    "s_sleep 6\n\t" \
    ".Ls48_%=:\n\t" \
    "s_waitcnt lgkmcnt(5)\n\t" \
    "v_mfma_f32_16x16x32_bf16 v[204:207], v[0:3], v[224:227], 0\n\t" \
    "ds_read_b128 v[240:243], v253 offset:2048\n\t" \
    "v_mfma_f32_16x16x32_bf16 v[208:211], v[8:11], v[224:227], 0\n\t" \
    "s_waitcnt lgkmcnt(2)\n\t" \
    "v_mfma_f32_16x16x32_bf16 v[200:203], v[244:247], v[224:227], 0\n\t" \
    "v_mfma_f32_16x16x32_bf16 v[216:219], v[0:3], v[228:231], 0\n\t" \
    "ds_read_b128 v[224:227], v253 offset:22528\n\t" \
    "v_mfma_f32_16x16x32_bf16 v[220:223], v[8:11], v[228:231], 0\n\t" \
    "v_mfma_f32_16x16x32_bf16 v[212:215], v[244:247], v[228:231], 0\n\t" \
    "ds_read_b128 v[244:247], v254 offset:2048\n\t" \
    "v_mfma_f32_16x16x32_bf16 v[204:207], v[4:7], v[232:235], v[204:207]\n\t" \
    "ds_read_b128 v[228:231], v253 offset:3072\n\t" \
    "s_add_u32 m0, %[m0], 0x3000\n\t" \
    "v_mfma_f32_16x16x32_bf16 v[208:211], v[12:15], v[232:235], v[208:211]\n\t" \
    "s_add_u32 s45, s42, 0xc000\n\t" \
    "s_waitcnt lgkmcnt(4)\n\t" \
    "v_mfma_f32_16x16x32_bf16 v[200:203], v[248:251], v[232:235], v[200:203]\n\t" \
    "buffer_load_dwordx4 v253, s[60:63], s45 offen offset:0 sc1 lds\n\t" \
    "v_mfma_f32_16x16x32_bf16 v[216:219], v[4:7], v[236:239], v[216:219]\n\t" \
    "ds_read_b128 v[232:235], v253 offset:23552\n\t" \
    "v_mfma_f32_16x16x32_bf16 v[220:223], v[12:15], v[236:239], v[220:223]\n\t" \
    "v_mfma_f32_16x16x32_bf16 v[212:215], v[248:251], v[236:239], v[212:215]\n\t" \
    "ds_read_b128 v[248:251], v254 offset:3072\n\t" \
    "s_waitcnt lgkmcnt(5)\n\t" \
    "v_mfma_f32_16x16x32_bf16 v[204:207], v[24:27], v[240:243], v[204:207]\n\t" \
    "ds_read_b128 v[236:239], v253 offset:4096\n\t" \
    "buffer_load_dwordx4 v253, s[60:63], s45 offen offset:1024 sc1 lds\n\t" \
    "v_mfma_f32_16x16x32_bf16 v[208:211], v[32:35], v[240:243], v[208:211]\n\t" \
    "v_mfma_f32_16x16x32_bf16 v[200:203], v[16:19], v[240:243], v[200:203]\n\t" \
    "s_waitcnt lgkmcnt(5)\n\t" \
    "v_mfma_f32_16x16x32_bf16 v[216:219], v[24:27], v[224:227], v[216:219]\n\t" \
    "ds_read_b128 v[240:243], v253 offset:24576\n\t" \
    "v_mfma_f32_16x16x32_bf16 v[220:223], v[32:35], v[224:227], v[220:223]\n\t" \
    "buffer_load_dwordx4 v253, s[60:63], s45 offen offset:2048 sc1 lds\n\t" \
    "v_mfma_f32_16x16x32_bf16 v[212:215], v[16:19], v[224:227], v[212:215]\n\t" \
    "s_waitcnt lgkmcnt(4)\n\t" \
    "v_mfma_f32_16x16x32_bf16 v[204:207], v[28:31], v[228:231], v[204:207]\n\t" \
    "ds_read_b128 v[224:227], v253 offset:5120\n\t" \
    "v_mfma_f32_16x16x32_bf16 v[208:211], v[36:39], v[228:231], v[208:211]\n\t" \
    "v_mfma_f32_16x16x32_bf16 v[200:203], v[20:23], v[228:231], v[200:203]\n\t" \
    "buffer_load_dwordx4 v253, s[60:63], s45 offen offset:3072 sc1 lds\n\t" \
    "s_waitcnt lgkmcnt(4)\n\t" \
    "v_mfma_f32_16x16x32_bf16 v[216:219], v[28:31], v[232:235], v[216:219]\n\t" \
    "ds_read_b128 v[228:231], v253 offset:25600\n\t" \
    "v_mfma_f32_16x16x32_bf16 v[220:223], v[36:39], v[232:235], v[220:223]\n\t" \
    "v_mfma_f32_16x16x32_bf16 v[212:215], v[20:23], v[232:235], v[212:215]\n\t" \
    "s_waitcnt lgkmcnt(3)\n\t" \
    "v_mfma_f32_16x16x32_bf16 v[204:207], v[40:43], v[236:239], v[204:207]\n\t" \
    "ds_read_b128 v[232:235], v253 offset:6144\n\t" \
    "v_mfma_f32_16x16x32_bf16 v[208:211], v[48:51], v[236:239], v[208:211]\n\t" \
    "v_mfma_f32_16x16x32_bf16 v[200:203], v[244:247], v[236:239], v[200:203]\n\t" \
    "s_waitcnt lgkmcnt(3)\n\t" \
    "v_mfma_f32_16x16x32_bf16 v[216:219], v[40:43], v[240:243], v[216:219]\n\t" \
    "ds_read_b128 v[236:239], v253 offset:26624\n\t" \
    "v_mfma_f32_16x16x32_bf16 v[220:223], v[48:51], v[240:243], v[220:223]\n\t" \
    "v_mfma_f32_16x16x32_bf16 v[212:215], v[244:247], v[240:243], v[212:215]\n\t" \
    "ds_read_b128 v[244:247], v254 offset:4096\n\t" \
    "s_waitcnt lgkmcnt(4)\n\t" \
    "v_mfma_f32_16x16x32_bf16 v[204:207], v[44:47], v[224:227], v[204:207]\n\t" \
    "ds_read_b128 v[240:243], v253 offset:7168\n\t" \
    "v_mfma_f32_16x16x32_bf16 v[208:211], v[52:55], v[224:227], v[208:211]\n\t" \
    "v_mfma_f32_16x16x32_bf16 v[200:203], v[248:251], v[224:227], v[200:203]\n\t" \
    "s_waitcnt lgkmcnt(4)\n\t" \
    "v_mfma_f32_16x16x32_bf16 v[216:219], v[44:47], v[228:231], v[216:219]\n\t" \
    "ds_read_b128 v[224:227], v253 offset:27648\n\t" \
    "v_mfma_f32_16x16x32_bf16 v[220:223], v[52:55], v[228:231], v[220:223]\n\t" \
    "v_mfma_f32_16x16x32_bf16 v[212:215], v[248:251], v[228:231], v[212:215]\n\t" \
    "ds_read_b128 v[248:251], v254 offset:5120\n\t" \
    "s_waitcnt lgkmcnt(5)\n\t" \
    "v_mfma_f32_16x16x32_bf16 v[204:207], v[64:67], v[232:235], v[204:207]\n\t" \
    "ds_read_b128 v[228:231], v253 offset:8192\n\t" \
    "v_mfma_f32_16x16x32_bf16 v[208:211], v[72:75], v[232:235], v[208:211]\n\t" \
    "v_mfma_f32_16x16x32_bf16 v[200:203], v[56:59], v[232:235], v[200:203]\n\t" \
    "s_waitcnt lgkmcnt(5)\n\t" \
    "v_mfma_f32_16x16x32_bf16 v[216:219], v[64:67], v[236:239], v[216:219]\n\t" \
    "ds_read_b128 v[232:235], v253 offset:28672\n\t" \
    "v_mfma_f32_16x16x32_bf16 v[220:223], v[72:75], v[236:239], v[220:223]\n\t" \
    "v_mfma_f32_16x16x32_bf16 v[212:215], v[56:59], v[236:239], v[212:215]\n\t" \
    "s_waitcnt lgkmcnt(4)\n\t" \
    "v_mfma_f32_16x16x32_bf16 v[204:207], v[68:71], v[240:243], v[204:207]\n\t" \
    "ds_read_b128 v[236:239], v253 offset:9216\n\t" \
    "v_mfma_f32_16x16x32_bf16 v[208:211], v[76:79], v[240:243], v[208:211]\n\t" \
    "v_mfma_f32_16x16x32_bf16 v[200:203], v[60:63], v[240:243], v[200:203]\n\t" \
    "v_and_b32 v252, 0xf0, v253\n\t" \
    "v_lshlrev_b32 v252, 3, v252\n\t" \
    "buffer_load_dword v252, v252, s[60:63], %[fl] offen sc1\n\t" \
    "s_waitcnt lgkmcnt(4)\n\t" \
    "v_mfma_f32_16x16x32_bf16 v[216:219], v[68:71], v[224:227], v[216:219]\n\t" \
    "ds_read_b128 v[240:243], v253 offset:29696\n\t" \
    "v_mfma_f32_16x16x32_bf16 v[220:223], v[76:79], v[224:227], v[220:223]\n\t" \
    "v_mfma_f32_16x16x32_bf16 v[212:215], v[60:63], v[224:227], v[212:215]\n\t" \
    "s_waitcnt lgkmcnt(3)\n\t" \
    "v_mfma_f32_16x16x32_bf16 v[204:207], v[80:83], v[228:231], v[204:207]\n\t" \
    "ds_read_b128 v[224:227], v253 offset:10240\n\t" \
    "v_mfma_f32_16x16x32_bf16 v[208:211], v[88:91], v[228:231], v[208:211]\n\t" \
    "v_mfma_f32_16x16x32_bf16 v[200:203], v[244:247], v[228:231], v[200:203]\n\t" \
    "s_waitcnt lgkmcnt(3)\n\t" \
    "v_mfma_f32_16x16x32_bf16 v[216:219], v[80:83], v[232:235], v[216:219]\n\t" \
    "ds_read_b128 v[228:231], v253 offset:30720\n\t" \
    "s_add_u32 s44, s40, 1\n\t" \
    "v_mfma_f32_16x16x32_bf16 v[220:223], v[88:91], v[232:235], v[220:223]\n\t" \
    "s_and_b32 s45, s44, 15\n\t" \
    "v_mfma_f32_16x16x32_bf16 v[212:215], v[244:247], v[232:235], v[212:215]\n\t" \
    "ds_read_b128 v[244:247], v254 offset:6144\n\t" \
    "s_cmp_eq_u32 s45, 0\n\t" \
    "s_waitcnt lgkmcnt(4)\n\t" \
    "v_mfma_f32_16x16x32_bf16 v[204:207], v[84:87], v[236:239], v[204:207]\n\t" \
    "ds_read_b128 v[232:235], v253 offset:11264\n\t" \
    "s_cselect_b32 s45, %[cj], %[cs]\n\t" \
    "v_mfma_f32_16x16x32_bf16 v[208:211], v[92:95], v[236:239], v[208:211]\n\t" \
    "s_cmp_eq_u32 s44, 64\n\t" \
    "v_mfma_f32_16x16x32_bf16 v[200:203], v[248:251], v[236:239], v[200:203]\n\t" \
    "s_cselect_b32 s45, 0, s45\n\t" \
    "s_waitcnt lgkmcnt(4)\n\t" \
    "v_mfma_f32_16x16x32_bf16 v[216:219], v[84:87], v[240:243], v[216:219]\n\t" \
    "ds_read_b128 v[236:239], v253 offset:31744\n\t" \
    "s_add_u32 s42, s42, s45\n\t" \
    "v_mfma_f32_16x16x32_bf16 v[220:223], v[92:95], v[240:243], v[220:223]\n\t" \
    "v_mfma_f32_16x16x32_bf16 v[212:215], v[248:251], v[240:243], v[212:215]\n\t" \
    "ds_read_b128 v[248:251], v254 offset:7168\n\t" \
    "s_setprio 1\n\t" \
    "s_waitcnt vmcnt(0) lgkmcnt(0)\n\t" \
    "s_barrier \n\t" \
    "s_add_u32 s44, s40, 1\n\t" \
    "s_min_u32 s44, s44, 63\n\t" \
    ".Ls49_%=:\n\t" \
    "v_cmp_ge_u32 vcc, s44, v252\n\t" \
    "s_cbranch_vccz .Ls410_%=\n\t" \
    "s_cmp_eq_u32 s41, 0\n\t" \
    "s_cbranch_scc1 .Ls410_%=\n\t" \
    "s_sub_u32 s41, s41, 1\n\t" \
    "s_sleep 2\n\t" \
    "v_and_b32 v252, 0xf0, v253\n\t" \
    "v_lshlrev_b32 v252, 3, v252\n\t" \
    "buffer_load_dword v252, v252, s[60:63], %[fl] offen sc1\n\t" \
    "s_waitcnt vmcnt(0)\n\t" \
    "s_branch .Ls49_%=\n\t" \
    ".Ls410_%=:\n\t" \
    "s_mov_b32 m0, %[m0]\n\t" \
    "s_add_u32 s45, s42, %[x1]\n\t" \
    "buffer_load_dwordx4 v253, s[60:63], s42 offen offset:0 sc1 lds\n\t" \
    "buffer_load_dwordx4 v253, s[60:63], s42 offen offset:1024 sc1 lds\n\t" \
    "buffer_load_dwordx4 v253, s[60:63], s42 offen offset:2048 sc1 lds\n\t" \
    "buffer_load_dwordx4 v253, s[60:63], s42 offen offset:3072 sc1 lds\n\t" \
    "s_mov_b32 m0, %[m1]\n\t" \
    "s_nop 0\n\t" \
    "buffer_load_dwordx4 v253, s[60:63], s45 offen offset:0 sc1 lds\n\t" \
    "buffer_load_dwordx4 v253, s[60:63], s45 offen offset:1024 sc1 lds\n\t" \
    "v_mfma_f32_16x16x32_bf16 v[204:207], v[104:107], v[224:227], v[204:207]\n\t" \
    "ds_read_b128 v[240:243], v253 offset:12288\n\t" \
    "v_mfma_f32_16x16x32_bf16 v[208:211], v[112:115], v[224:227], v[208:211]\n\t" \
    "v_mfma_f32_16x16x32_bf16 v[200:203], v[96:99], v[224:227], v[200:203]\n\t" \
    "v_mfma_f32_16x16x32_bf16 v[216:219], v[104:107], v[228:231], v[216:219]\n\t" \
    "ds_read_b128 v[224:227], v253 offset:32768\n\t" \
    "v_mfma_f32_16x16x32_bf16 v[220:223], v[112:115], v[228:231], v[220:223]\n\t" \
    "v_mfma_f32_16x16x32_bf16 v[212:215], v[96:99], v[228:231], v[212:215]\n\t" \
    "v_mfma_f32_16x16x32_bf16 v[204:207], v[108:111], v[232:235], v[204:207]\n\t" \
    "ds_read_b128 v[228:231], v253 offset:13312\n\t" \
    "v_mfma_f32_16x16x32_bf16 v[208:211], v[116:119], v[232:235], v[208:211]\n\t" \
    "v_mfma_f32_16x16x32_bf16 v[200:203], v[100:103], v[232:235], v[200:203]\n\t" \
    "v_mfma_f32_16x16x32_bf16 v[216:219], v[108:111], v[236:239], v[216:219]\n\t" \
    "ds_read_b128 v[232:235], v253 offset:33792\n\t" \
    "v_mfma_f32_16x16x32_bf16 v[220:223], v[116:119], v[236:239], v[220:223]\n\t" \
    "v_mfma_f32_16x16x32_bf16 v[212:215], v[100:103], v[236:239], v[212:215]\n\t" \
    "s_waitcnt lgkmcnt(3)\n\t" \
    "v_mfma_f32_16x16x32_bf16 v[204:207], v[120:123], v[240:243], v[204:207]\n\t" \
    "ds_read_b128 v[236:239], v253 offset:16384\n\t" \
    "v_mfma_f32_16x16x32_bf16 v[208:211], v[128:131], v[240:243], v[208:211]\n\t" \
    "v_mfma_f32_16x16x32_bf16 v[200:203], v[244:247], v[240:243], v[200:203]\n\t" \
    "s_waitcnt lgkmcnt(3)\n\t" \
    "v_mfma_f32_16x16x32_bf16 v[216:219], v[120:123], v[224:227], v[216:219]\n\t" \
    "ds_read_b128 v[240:243], v253 offset:36864\n\t" \
    "v_mfma_f32_16x16x32_bf16 v[220:223], v[128:131], v[224:227], v[220:223]\n\t" \
    "v_mfma_f32_16x16x32_bf16 v[212:215], v[244:247], v[224:227], v[212:215]\n\t" \
    "ds_read_b128 v[244:247], v254 offset:8192\n\t" \
    "s_waitcnt lgkmcnt(4)\n\t" \
    "v_mfma_f32_16x16x32_bf16 v[204:207], v[124:127], v[228:231], v[204:207]\n\t" \
    "ds_read_b128 v[224:227], v253 offset:17408\n\t" \
    "v_mfma_f32_16x16x32_bf16 v[208:211], v[132:135], v[228:231], v[208:211]\n\t" \
    "v_mfma_f32_16x16x32_bf16 v[200:203], v[248:251], v[228:231], v[200:203]\n\t" \
    "s_waitcnt lgkmcnt(4)\n\t" \
    "v_mfma_f32_16x16x32_bf16 v[216:219], v[124:127], v[232:235], v[216:219]\n\t" \
    "ds_read_b128 v[228:231], v253 offset:37888\n\t" \
    "v_mfma_f32_16x16x32_bf16 v[220:223], v[132:135], v[232:235], v[220:223]\n\t" \
    "v_mfma_f32_16x16x32_bf16 v[212:215], v[248:251], v[232:235], v[212:215]\n\t" \
    "ds_read_b128 v[248:251], v254 offset:9216\n\t" \
    "s_waitcnt lgkmcnt(5)\n\t" \
    "v_mfma_f32_16x16x32_bf16 v[204:207], v[160:163], v[236:239], v[204:207]\n\t" \
    "ds_read_b128 v[232:235], v253 offset:14336\n\t" \
    "v_mfma_f32_16x16x32_bf16 v[208:211], v[168:171], v[236:239], v[208:211]\n\t" \
    "s_waitcnt lgkmcnt(4)\n\t" \
    "v_mfma_f32_16x16x32_bf16 v[200:203], v[244:247], v[236:239], v[200:203]\n\t" \
    "v_mfma_f32_16x16x32_bf16 v[216:219], v[160:163], v[240:243], v[216:219]\n\t" \
    "ds_read_b128 v[236:239], v253 offset:15360\n\t" \
    "v_mfma_f32_16x16x32_bf16 v[220:223], v[168:171], v[240:243], v[220:223]\n\t" \
    "v_mfma_f32_16x16x32_bf16 v[212:215], v[244:247], v[240:243], v[212:215]\n\t" \
    "s_waitcnt lgkmcnt(4)\n\t" \
    "v_mfma_f32_16x16x32_bf16 v[204:207], v[164:167], v[224:227], v[204:207]\n\t" \
    "ds_read_b128 v[240:243], v253 offset:18432\n\t" \
    "v_add_u32 v252, %[q2], v253\n\t" \
    "v_mfma_f32_16x16x32_bf16 v[208:211], v[172:175], v[224:227], v[208:211]\n\t" \
    "s_waitcnt lgkmcnt(3)\n\t" \
    "v_mfma_f32_16x16x32_bf16 v[200:203], v[248:251], v[224:227], v[200:203]\n\t" \
    "v_mfma_f32_16x16x32_bf16 v[216:219], v[164:167], v[228:231], v[216:219]\n\t" \
    "ds_read_b128 v[224:227], v253 offset:19456\n\t" \
    "v_mfma_f32_16x16x32_bf16 v[220:223], v[172:175], v[228:231], v[220:223]\n\t" \
    "v_mfma_f32_16x16x32_bf16 v[212:215], v[248:251], v[228:231], v[212:215]\n\t" \
    "s_waitcnt lgkmcnt(3)\n\t" \
    "v_mfma_f32_16x16x32_bf16 v[204:207], v[144:147], v[232:235], v[204:207]\n\t" \
    "ds_read_b128 v[228:231], v253 offset:34816\n\t" \
    "v_mov_b64 v[244:245], 0\n\t" \
    "v_mfma_f32_16x16x32_bf16 v[208:211], v[152:155], v[232:235], v[208:211]\n\t" \
    "v_mov_b64 v[246:247], 0\n\t" \
    "v_mfma_f32_16x16x32_bf16 v[200:203], v[136:139], v[232:235], v[200:203]\n\t" \
    "v_mov_b64 v[248:249], 0\n\t" \
    "s_waitcnt lgkmcnt(3)\n\t" \
    "v_mfma_f32_16x16x32_bf16 v[204:207], v[148:151], v[236:239], v[204:207]\n\t" \
    "ds_read_b128 v[232:235], v253 offset:35840\n\t" \
    "v_mov_b64 v[250:251], 0\n\t" \
    "v_mfma_f32_16x16x32_bf16 v[208:211], v[156:159], v[236:239], v[208:211]\n\t" \
    "v_mfma_f32_16x16x32_bf16 v[200:203], v[140:143], v[236:239], v[200:203]\n\t" \
    "s_waitcnt lgkmcnt(3)\n\t" \
    "v_mfma_f32_16x16x32_bf16 v[204:207], v[184:187], v[240:243], v[204:207]\n\t" \
    "ds_read_b128 v[236:239], v253 offset:38912\n\t" \
    "v_mfma_f32_16x16x32_bf16 v[208:211], v[192:195], v[240:243], v[208:211]\n\t" \
    "v_mfma_f32_16x16x32_bf16 v[200:203], v[176:179], v[240:243], v[200:203]\n\t" \
    "s_waitcnt lgkmcnt(3)\n\t" \
    "v_mfma_f32_16x16x32_bf16 v[204:207], v[188:191], v[224:227], v[204:207]\n\t" \
    "ds_read_b128 v[240:243], v253 offset:39936\n\t" \
    "v_mfma_f32_16x16x32_bf16 v[208:211], v[196:199], v[224:227], v[208:211]\n\t" \
    "v_mfma_f32_16x16x32_bf16 v[200:203], v[180:183], v[224:227], v[200:203]\n\t" \
    "s_waitcnt lgkmcnt(3)\n\t" \
    "v_mfma_f32_16x16x32_bf16 v[216:219], v[144:147], v[228:231], v[216:219]\n\t" \
    "s_waitcnt lgkmcnt(2)\n\t" \
    "v_mfma_f32_16x16x32_bf16 v[216:219], v[148:151], v[232:235], v[216:219]\n\t" \
    "v_cvt_pk_bf16_f32 v204, v204, v208\n\t" \
    "v_cvt_pk_bf16_f32 v205, v205, v209\n\t" \
    "s_waitcnt lgkmcnt(1)\n\t" \
    "v_mfma_f32_16x16x32_bf16 v[216:219], v[184:187], v[236:239], v[216:219]\n\t" \
    "v_cvt_pk_bf16_f32 v206, v206, v210\n\t" \
    "v_cvt_pk_bf16_f32 v207, v207, v211\n\t" \
    "s_waitcnt lgkmcnt(0)\n\t" \
    "v_mfma_f32_16x16x32_bf16 v[216:219], v[188:191], v[240:243], v[216:219]\n\t" \
    "ds_write_b128 v252, v[204:207] offset:0\n\t" \
    "v_mfma_f32_16x16x32_bf16 v[220:223], v[152:155], v[228:231], v[220:223]\n\t" \
    "v_mfma_f32_16x16x32_bf16 v[220:223], v[156:159], v[232:235], v[220:223]\n\t" \
    "v_add_u32 v252, %[qs], v253\n\t" \
    "v_mfma_f32_16x16x32_bf16 v[220:223], v[192:195], v[236:239], v[220:223]\n\t" \
    "v_cvt_pk_bf16_f32 v200, v200, v201\n\t" \
    "v_cvt_pk_bf16_f32 v201, v202, v203\n\t" \
    "v_mfma_f32_16x16x32_bf16 v[220:223], v[196:199], v[240:243], v[220:223]\n\t" \
    "ds_write_b64 v252, v[200:201] offset:0\n\t" \
    "v_mfma_f32_16x16x32_bf16 v[212:215], v[136:139], v[228:231], v[212:215]\n\t" \
    "v_mfma_f32_16x16x32_bf16 v[212:215], v[140:143], v[232:235], v[212:215]\n\t" \
    "v_mov_b64 v[208:209], 0\n\t" \
    "v_mfma_f32_16x16x32_bf16 v[212:215], v[176:179], v[236:239], v[212:215]\n\t" \
    "v_mov_b64 v[210:211], 0\n\t" \
    "v_add_u32 v252, %[q2], v253\n\t" \
    "v_mfma_f32_16x16x32_bf16 v[212:215], v[180:183], v[240:243], v[212:215]\n\t" \
    "v_cvt_pk_bf16_f32 v216, v216, v220\n\t" \
    "v_cvt_pk_bf16_f32 v217, v217, v221\n\t" \
    "v_cvt_pk_bf16_f32 v218, v218, v222\n\t" \
    "v_cvt_pk_bf16_f32 v219, v219, v223\n\t" \
    "ds_write_b128 v252, v[216:219] offset:6144\n\t" \
    "v_add_u32 v252, %[qs], v253\n\t" \
    "s_nop 1\n\t" \
    "v_cvt_pk_bf16_f32 v212, v212, v213\n\t" \
    "v_cvt_pk_bf16_f32 v213, v214, v215\n\t" \
    "ds_write_b64 v252, v[212:213] offset:6144\n\t" \
    "s_setprio 2\n\t" \
    "s_waitcnt vmcnt(0) lgkmcnt(0)\n\t" \
    "s_barrier \n\t" \
    "ds_read_b128 v[224:227], v253 offset:40960\n\t" \
    "ds_read_b128 v[228:231], v253 offset:41984\n\t" \
    "ds_read_b128 v[232:235], v253 offset:43008\n\t" \
    "ds_read_b128 v[236:239], v253 offset:44032\n\t" \
    "ds_read_b128 v[240:243], v253 offset:45056\n\t" \
    "ds_read_b128 v[200:203], v254 offset:0\n\t" \
    "ds_read_b128 v[204:207], v254 offset:1024\n\t" \
    "ds_read_b128 v[216:219], v254 offset:2048\n\t" \
    "ds_read_b128 v[220:223], v254 offset:3072\n\t" \
    "v_bfe_i32 v212, v253, 5, 1\n\t" \
    "v_and_b32 v212, 0xaaaa, v212\n\t" \
    "v_or_b32 v212, 0x4444, v212\n\t" \
    "v_and_b32 v213, 0xffff, v255\n\t" \
    "v_lshrrev_b32 v214, 16, v255\n\t" \
    "s_waitcnt lgkmcnt(8)\n\t" \
    "v_smfmac_f32_16x16x64_bf16 v[244:247], v[224:227], v[0:7], v212\n\t" \
    "v_smfmac_f32_16x16x64_bf16 v[248:251], v[224:227], v[8:15], v212\n\t" \
    "s_waitcnt lgkmcnt(2)\n\t" \
    "v_smfmac_f32_16x16x64_bf16 v[208:211], v[224:227], v[200:207], v212\n\t" \
    "ds_read_b128 v[200:203], v254 offset:4096\n\t" \
    "ds_read_b128 v[204:207], v254 offset:5120\n\t" \
    "ds_read_b128 v[224:227], v253 offset:46080\n\t" \
    "v_smfmac_f32_16x16x64_bf16 v[208:211], v[228:231], v[16:23], v212\n\t" \
    "v_smfmac_f32_16x16x64_bf16 v[244:247], v[228:231], v[24:31], v212\n\t" \
    "v_smfmac_f32_16x16x64_bf16 v[248:251], v[228:231], v[32:39], v212\n\t" \
    "ds_read_b128 v[228:231], v253 offset:47104\n\t" \
    "s_waitcnt lgkmcnt(4)\n\t" \
    "v_smfmac_f32_16x16x64_bf16 v[208:211], v[232:235], v[216:223], v212\n\t" \
    "ds_read_b128 v[216:219], v254 offset:6144\n\t" \
    "ds_read_b128 v[220:223], v254 offset:7168\n\t" \
    "v_smfmac_f32_16x16x64_bf16 v[244:247], v[232:235], v[40:47], v212\n\t" \
    "v_smfmac_f32_16x16x64_bf16 v[248:251], v[232:235], v[48:55], v212\n\t" \
    "ds_read_b128 v[232:235], v253 offset:48128\n\t" \
    "v_smfmac_f32_16x16x64_bf16 v[208:211], v[236:239], v[56:63], v212\n\t" \
    "v_smfmac_f32_16x16x64_bf16 v[244:247], v[236:239], v[64:71], v212\n\t" \
    "v_smfmac_f32_16x16x64_bf16 v[248:251], v[236:239], v[72:79], v212\n\t" \
    "ds_read_b128 v[236:239], v253 offset:49152\n\t" \
    "s_waitcnt lgkmcnt(6)\n\t" \
    "v_smfmac_f32_16x16x64_bf16 v[208:211], v[240:243], v[200:207], v212\n\t" \
    "ds_read_b128 v[200:203], v254 offset:8192\n\t" \
    "ds_read_b128 v[204:207], v254 offset:9216\n\t" \
    "v_smfmac_f32_16x16x64_bf16 v[244:247], v[240:243], v[80:87], v212\n\t" \
    "v_smfmac_f32_16x16x64_bf16 v[248:251], v[240:243], v[88:95], v212\n\t" \
    "ds_read_b128 v[240:243], v253 offset:50176\n\t" \
    "s_waitcnt lgkmcnt(8)\n\t" \
    "v_smfmac_f32_16x16x64_bf16 v[208:211], v[224:227], v[96:103], v212\n\t" \
    "v_smfmac_f32_16x16x64_bf16 v[244:247], v[224:227], v[104:111], v212\n\t" \
    "v_smfmac_f32_16x16x64_bf16 v[248:251], v[224:227], v[112:119], v212\n\t" \
    "s_waitcnt lgkmcnt(5)\n\t" \
    "v_smfmac_f32_16x16x64_bf16 v[208:211], v[228:231], v[216:223], v212\n\t" \
    "s_waitcnt lgkmcnt(4)\n\t" \
    "v_smfmac_f32_16x16x64_bf16 v[208:211], v[232:235], v[136:143], v212\n\t" \
    "s_waitcnt lgkmcnt(1)\n\t" \
    "v_smfmac_f32_16x16x64_bf16 v[208:211], v[236:239], v[200:207], v212\n\t" \
    "s_waitcnt lgkmcnt(0)\n\t" \
    "v_smfmac_f32_16x16x64_bf16 v[208:211], v[240:243], v[176:183], v212\n\t" \
    "v_smfmac_f32_16x16x64_bf16 v[244:247], v[228:231], v[120:127], v212\n\t" \
    "v_smfmac_f32_16x16x64_bf16 v[244:247], v[232:235], v[144:151], v212\n\t" \
    "v_smfmac_f32_16x16x64_bf16 v[244:247], v[236:239], v[160:167], v212\n\t" \
    "v_add_f32 v208, v208, v210\n\t" \
    "v_add_f32 v209, v209, v211\n\t" \
    "v_cvt_pk_bf16_f32 v208, v208, v209\n\t" \
    "v_smfmac_f32_16x16x64_bf16 v[244:247], v[240:243], v[184:191], v212\n\t" \
    "ds_write_b16 v213, v208 offset:0\n\t" \
    "ds_write_b16_d16_hi v213, v208 offset:16\n\t" \
    "v_smfmac_f32_16x16x64_bf16 v[248:251], v[228:231], v[128:135], v212\n\t" \
    "v_smfmac_f32_16x16x64_bf16 v[248:251], v[232:235], v[152:159], v212\n\t" \
    "v_smfmac_f32_16x16x64_bf16 v[248:251], v[236:239], v[168:175], v212\n\t" \
    "v_add_f32 v244, v244, v246\n\t" \
    "v_add_f32 v245, v245, v247\n\t" \
    "v_smfmac_f32_16x16x64_bf16 v[248:251], v[240:243], v[192:199], v212\n\t" \
    "ds_read_b128 v[224:227], v253 offset:0\n\t" \
    "ds_read_b128 v[228:231], v253 offset:20480\n\t" \
    "s_nop 5\n\t" \
    "v_add_f32 v248, v248, v250\n\t" \
    "v_add_f32 v249, v249, v251\n\t" \
    "v_cvt_pk_bf16_f32 v244, v244, v248\n\t" \
    "v_cvt_pk_bf16_f32 v245, v245, v249\n\t" \
    "ds_write2_b32 v214, v244, v245 offset1:4\n\t" \
    "s_setprio 0\n\t" \
    "s_add_u32 s40, s40, 1\n\t" \
    "s_cmp_lg_u32 s40, 64\n\t" \
    "s_waitcnt lgkmcnt(0)\n\t" \
    "s_barrier \n\t" \
    "ds_read_b128 v[232:235], v253 offset:1024\n\t" \
    "ds_read_b128 v[236:239], v253 offset:21504\n\t" \
    "ds_read_b128 v[244:247], v254 offset:0\n\t" \
    "ds_read_b128 v[248:251], v254 offset:1024\n\t" \
    "s_setprio 1\n\t" \
    "s_nop 0\n\t" \
    "s_nop 0\n\t" \
    "s_cbranch_scc1 .Ls47_%=\n\t" \
    "s_branch .Lend%=\n\t" \
    ".Ls3_%=:\n\t" \
    "v_add_u32 v244, 0x18000, v243\n\t" \
    "s_waitcnt vmcnt(12) lgkmcnt(0)\n\t" \
    "s_barrier \n\t" \
    "s_add_u32 m0, %[wm], 0x12000\n\t" \
    "s_mov_b32 s44, 0x1e0000\n\t" \
    "buffer_load_dwordx4 v240, %[wr], s44 offen offset:0 nt lds\n\t" \
    "buffer_load_dwordx4 v241, %[wr], s44 offen offset:1024 nt lds\n\t" \
    "buffer_load_dwordx4 v242, %[wr], s44 offen offset:2048 nt lds\n\t" \
    "ds_read_b64_tr_b16 v[208:209], v244 offset:0\n\t" \
    "ds_read_b64_tr_b16 v[210:211], v244 offset:1536\n\t" \
    "ds_read_b64_tr_b16 v[40:41], v244 offset:32\n\t" \
    "ds_read_b64_tr_b16 v[42:43], v244 offset:1568\n\t" \
    "ds_read_b64_tr_b16 v[48:49], v244 offset:64\n\t" \
    "ds_read_b64_tr_b16 v[50:51], v244 offset:1600\n\t" \
    "v_add_u32 v244, 0x1e000, v243\n\t" \
    "s_waitcnt vmcnt(12) lgkmcnt(0)\n\t" \
    "s_barrier \n\t" \
    "s_add_u32 m0, %[wm], 0x18000\n\t" \
    "s_mov_b32 s44, 0x240000\n\t" \
    "buffer_load_dwordx4 v240, %[wr], s44 offen offset:0 nt lds\n\t" \
    "buffer_load_dwordx4 v241, %[wr], s44 offen offset:1024 nt lds\n\t" \
    "buffer_load_dwordx4 v242, %[wr], s44 offen offset:2048 nt lds\n\t" \
    "ds_read_b64_tr_b16 v[212:213], v244 offset:0\n\t" \
    "ds_read_b64_tr_b16 v[214:215], v244 offset:1536\n\t" \
    "ds_read_b64_tr_b16 v[44:45], v244 offset:32\n\t" \
    "ds_read_b64_tr_b16 v[46:47], v244 offset:1568\n\t" \
    "ds_read_b64_tr_b16 v[52:53], v244 offset:64\n\t" \
    "ds_read_b64_tr_b16 v[54:55], v244 offset:1600\n\t" \
    "v_add_u32 v244, 0x0, v243\n\t" \
    "s_waitcnt vmcnt(12) lgkmcnt(0)\n\t" \
    "s_barrier \n\t" \
    "s_add_u32 m0, %[wm], 0x1e000\n\t" \
    "s_mov_b32 s44, 0x2a0000\n\t" \
    "buffer_load_dwordx4 v240, %[wr], s44 offen offset:0 nt lds\n\t" \
    "buffer_load_dwordx4 v241, %[wr], s44 offen offset:1024 nt lds\n\t" \
    "buffer_load_dwordx4 v242, %[wr], s44 offen offset:2048 nt lds\n\t" \
    "ds_read_b64_tr_b16 v[56:57], v244 offset:0\n\t" \
    "ds_read_b64_tr_b16 v[58:59], v244 offset:1536\n\t" \
    "ds_read_b64_tr_b16 v[64:65], v244 offset:32\n\t" \
    "ds_read_b64_tr_b16 v[66:67], v244 offset:1568\n\t" \
    "ds_read_b64_tr_b16 v[72:73], v244 offset:64\n\t" \
    "ds_read_b64_tr_b16 v[74:75], v244 offset:1600\n\t" \
    "v_add_u32 v244, 0x6000, v243\n\t" \
    "s_waitcnt vmcnt(12) lgkmcnt(0)\n\t" \
    "s_barrier \n\t" \
    "s_add_u32 m0, %[wm], 0x0\n\t" \
    "s_mov_b32 s44, 0x300000\n\t" \
    "buffer_load_dwordx4 v240, %[wr], s44 offen offset:0 nt lds\n\t" \
    "buffer_load_dwordx4 v241, %[wr], s44 offen offset:1024 nt lds\n\t" \
    "buffer_load_dwordx4 v242, %[wr], s44 offen offset:2048 nt lds\n\t" \
    "ds_read_b64_tr_b16 v[60:61], v244 offset:0\n\t" \
    "ds_read_b64_tr_b16 v[62:63], v244 offset:1536\n\t" \
    "ds_read_b64_tr_b16 v[68:69], v244 offset:32\n\t" \
    "ds_read_b64_tr_b16 v[70:71], v244 offset:1568\n\t" \
    "ds_read_b64_tr_b16 v[76:77], v244 offset:64\n\t" \
    "ds_read_b64_tr_b16 v[78:79], v244 offset:1600\n\t" \
    "v_add_u32 v244, 0xc000, v243\n\t" \
    "s_waitcnt vmcnt(12) lgkmcnt(0)\n\t" \
    "s_barrier \n\t" \
    "s_add_u32 m0, %[wm], 0x6000\n\t" \
    "s_mov_b32 s44, 0x360000\n\t" \
    "buffer_load_dwordx4 v240, %[wr], s44 offen offset:0 nt lds\n\t" \
    "buffer_load_dwordx4 v241, %[wr], s44 offen offset:1024 nt lds\n\t" \
    "buffer_load_dwordx4 v242, %[wr], s44 offen offset:2048 nt lds\n\t" \
    "ds_read_b64_tr_b16 v[216:217], v244 offset:0\n\t" \
    "ds_read_b64_tr_b16 v[218:219], v244 offset:1536\n\t" \
    "ds_read_b64_tr_b16 v[80:81], v244 offset:32\n\t" \
    "ds_read_b64_tr_b16 v[82:83], v244 offset:1568\n\t" \
    "ds_read_b64_tr_b16 v[88:89], v244 offset:64\n\t" \
    "ds_read_b64_tr_b16 v[90:91], v244 offset:1600\n\t" \
    "v_add_u32 v244, 0x12000, v243\n\t" \
    "s_waitcnt vmcnt(12) lgkmcnt(0)\n\t" \
    "s_barrier \n\t" \
    "s_add_u32 m0, %[wm], 0xc000\n\t" \
    "s_mov_b32 s44, 0x3c0000\n\t" \
    "buffer_load_dwordx4 v240, %[wr], s44 offen offset:0 nt lds\n\t" \
    "buffer_load_dwordx4 v241, %[wr], s44 offen offset:1024 nt lds\n\t" \
    "buffer_load_dwordx4 v242, %[wr], s44 offen offset:2048 nt lds\n\t" \
    "ds_read_b64_tr_b16 v[220:221], v244 offset:0\n\t" \
    "ds_read_b64_tr_b16 v[222:223], v244 offset:1536\n\t" \
    "ds_read_b64_tr_b16 v[84:85], v244 offset:32\n\t" \
    "ds_read_b64_tr_b16 v[86:87], v244 offset:1568\n\t" \
    "ds_read_b64_tr_b16 v[92:93], v244 offset:64\n\t" \
    "ds_read_b64_tr_b16 v[94:95], v244 offset:1600\n\t" \
    "v_add_u32 v244, 0x18000, v243\n\t" \
    "s_waitcnt vmcnt(12) lgkmcnt(0)\n\t" \
    "s_barrier \n\t" \
    "s_add_u32 m0, %[wm], 0x12000\n\t" \
    "s_mov_b32 s44, 0x420000\n\t" \
    "buffer_load_dwordx4 v240, %[wr], s44 offen offset:0 nt lds\n\t" \
    "buffer_load_dwordx4 v241, %[wr], s44 offen offset:1024 nt lds\n\t" \
    "buffer_load_dwordx4 v242, %[wr], s44 offen offset:2048 nt lds\n\t" \
    "ds_read_b64_tr_b16 v[96:97], v244 offset:0\n\t" \
    "ds_read_b64_tr_b16 v[98:99], v244 offset:1536\n\t" \
    "ds_read_b64_tr_b16 v[104:105], v244 offset:32\n\t" \
    "ds_read_b64_tr_b16 v[106:107], v244 offset:1568\n\t" \
    "ds_read_b64_tr_b16 v[112:113], v244 offset:64\n\t" \
    "ds_read_b64_tr_b16 v[114:115], v244 offset:1600\n\t" \
    "v_add_u32 v244, 0x1e000, v243\n\t" \
    "s_waitcnt vmcnt(12) lgkmcnt(0)\n\t" \
    "s_barrier \n\t" \
    "s_add_u32 m0, %[wm], 0x18000\n\t" \
    "s_mov_b32 s44, 0x480000\n\t" \
    "buffer_load_dwordx4 v240, %[wr], s44 offen offset:0 nt lds\n\t" \
    "buffer_load_dwordx4 v241, %[wr], s44 offen offset:1024 nt lds\n\t" \
    "buffer_load_dwordx4 v242, %[wr], s44 offen offset:2048 nt lds\n\t" \
    "ds_read_b64_tr_b16 v[100:101], v244 offset:0\n\t" \
    "ds_read_b64_tr_b16 v[102:103], v244 offset:1536\n\t" \
    "ds_read_b64_tr_b16 v[108:109], v244 offset:32\n\t" \
    "ds_read_b64_tr_b16 v[110:111], v244 offset:1568\n\t" \
    "ds_read_b64_tr_b16 v[116:117], v244 offset:64\n\t" \
    "ds_read_b64_tr_b16 v[118:119], v244 offset:1600\n\t" \
    "v_add_u32 v244, 0x0, v243\n\t" \
    "s_waitcnt vmcnt(12) lgkmcnt(0)\n\t" \
    "s_barrier \n\t" \
    "v_mov_b32 v246, 0\n\t" \
    "buffer_load_dword v246, v246, s[60:63], %[mo] offen sc1\n\t" \
    "s_add_u32 m0, %[wm], 0x1e000\n\t" \
    "s_mov_b32 s44, 0x4e0000\n\t" \
    "buffer_load_dwordx4 v240, %[wr], s44 offen offset:0 nt lds\n\t" \
    "buffer_load_dwordx4 v241, %[wr], s44 offen offset:1024 nt lds\n\t" \
    "buffer_load_dwordx4 v242, %[wr], s44 offen offset:2048 nt lds\n\t" \
    "ds_read_b64_tr_b16 v[224:225], v244 offset:0\n\t" \
    "ds_read_b64_tr_b16 v[226:227], v244 offset:1536\n\t" \
    "ds_read_b64_tr_b16 v[120:121], v244 offset:32\n\t" \
    "ds_read_b64_tr_b16 v[122:123], v244 offset:1568\n\t" \
    "ds_read_b64_tr_b16 v[128:129], v244 offset:64\n\t" \
    "ds_read_b64_tr_b16 v[130:131], v244 offset:1600\n\t" \
    "v_add_u32 v244, 0x6000, v243\n\t" \
    "s_waitcnt vmcnt(12) lgkmcnt(0)\n\t" \
    "s_barrier \n\t" \
    "s_add_u32 m0, %[wm], 0x0\n\t" \
    "s_mov_b32 s44, 0x540000\n\t" \
    "buffer_load_dwordx4 v240, %[wr], s44 offen offset:0 nt lds\n\t" \
    "buffer_load_dwordx4 v241, %[wr], s44 offen offset:1024 nt lds\n\t" \
    "buffer_load_dwordx4 v242, %[wr], s44 offen offset:2048 nt lds\n\t" \
    "ds_read_b64_tr_b16 v[228:229], v244 offset:0\n\t" \
    "ds_read_b64_tr_b16 v[230:231], v244 offset:1536\n\t" \
    "ds_read_b64_tr_b16 v[124:125], v244 offset:32\n\t" \
    "ds_read_b64_tr_b16 v[126:127], v244 offset:1568\n\t" \
    "ds_read_b64_tr_b16 v[132:133], v244 offset:64\n\t" \
    "ds_read_b64_tr_b16 v[134:135], v244 offset:1600\n\t" \
    "v_add_u32 v244, 0xc000, v243\n\t" \
    "s_waitcnt vmcnt(12) lgkmcnt(0)\n\t" \
    "s_barrier \n\t" \
    "s_add_u32 m0, %[wm], 0x6000\n\t" \
    "s_mov_b32 s44, 0x5a0000\n\t" \
    "buffer_load_dwordx4 v240, %[wr], s44 offen offset:0 nt lds\n\t" \
    "buffer_load_dwordx4 v241, %[wr], s44 offen offset:1024 nt lds\n\t" \
    "buffer_load_dwordx4 v242, %[wr], s44 offen offset:2048 nt lds\n\t" \
    "ds_read_b64_tr_b16 v[136:137], v244 offset:0\n\t" \
    "ds_read_b64_tr_b16 v[138:139], v244 offset:1536\n\t" \
    "ds_read_b64_tr_b16 v[144:145], v244 offset:32\n\t" \
    "ds_read_b64_tr_b16 v[146:147], v244 offset:1568\n\t" \
    "ds_read_b64_tr_b16 v[152:153], v244 offset:64\n\t" \
    "ds_read_b64_tr_b16 v[154:155], v244 offset:1600\n\t" \
    "v_add_u32 v244, 0x12000, v243\n\t" \
    "s_waitcnt vmcnt(12) lgkmcnt(0)\n\t" \
    "s_barrier \n\t" \
    "s_add_u32 m0, %[wm], 0xc000\n\t" \
    "s_mov_b32 s44, 0x600000\n\t" \
    "buffer_load_dwordx4 v240, %[wr], s44 offen offset:0 nt lds\n\t" \
    "buffer_load_dwordx4 v241, %[wr], s44 offen offset:1024 nt lds\n\t" \
    "buffer_load_dwordx4 v242, %[wr], s44 offen offset:2048 nt lds\n\t" \
    "ds_read_b64_tr_b16 v[140:141], v244 offset:0\n\t" \
    "ds_read_b64_tr_b16 v[142:143], v244 offset:1536\n\t" \
    "ds_read_b64_tr_b16 v[148:149], v244 offset:32\n\t" \
    "ds_read_b64_tr_b16 v[150:151], v244 offset:1568\n\t" \
    "ds_read_b64_tr_b16 v[156:157], v244 offset:64\n\t" \
    "ds_read_b64_tr_b16 v[158:159], v244 offset:1600\n\t" \
    "v_add_u32 v244, 0x18000, v243\n\t" \
    "s_waitcnt vmcnt(12) lgkmcnt(0)\n\t" \
    "s_barrier \n\t" \
    "s_add_u32 m0, %[wm], 0x12000\n\t" \
    "s_mov_b32 s44, 0x660000\n\t" \
    "buffer_load_dwordx4 v240, %[wr], s44 offen offset:0 nt lds\n\t" \
    "buffer_load_dwordx4 v241, %[wr], s44 offen offset:1024 nt lds\n\t" \
    "buffer_load_dwordx4 v242, %[wr], s44 offen offset:2048 nt lds\n\t" \
    "ds_read_b64_tr_b16 v[232:233], v244 offset:0\n\t" \
    "ds_read_b64_tr_b16 v[234:235], v244 offset:1536\n\t" \
    "ds_read_b64_tr_b16 v[160:161], v244 offset:32\n\t" \
    "ds_read_b64_tr_b16 v[162:163], v244 offset:1568\n\t" \
    "ds_read_b64_tr_b16 v[168:169], v244 offset:64\n\t" \
    "ds_read_b64_tr_b16 v[170:171], v244 offset:1600\n\t" \
    "v_add_u32 v244, 0x1e000, v243\n\t" \
    "s_waitcnt vmcnt(12) lgkmcnt(0)\n\t" \
    "s_barrier \n\t" \
    ".Ls31_%=:\n\t" \
    "v_readfirstlane_b32 s43, v246\n\t" \
    "s_nop 0\n\t" \
    "s_cmp_lg_u32 s43, -1\n\t" \
    "s_cbranch_scc1 .Ls32_%=\n\t" \
    "s_cmp_eq_u32 s41, 0\n\t" \
    "s_cbranch_scc1 .Ls32_%=\n\t" \
    "s_sub_u32 s41, s41, 1\n\t" \
    "s_sleep 4\n\t" \
    "v_mov_b32 v246, 0\n\t" \
    "buffer_load_dword v246, v246, s[60:63], %[mo] offen sc1\n\t" \
    "s_waitcnt vmcnt(0)\n\t" \
    "s_branch .Ls31_%=\n\t" \
    ".Ls32_%=:\n\t" \
    "v_mov_b32 v246, 0x8000\n\t" \
    "s_mov_b64 exec, 1\n\t" \
    "buffer_store_dword v246, v246, s[60:63], %[mo] offen sc1\n\t" \
    "s_mov_b64 exec, -1\n\t" \
    "s_and_b32 s44, s43, 0x5000\n\t" \
    "s_xor_b32 %[mb], %[mb], s44\n\t" \
    "s_xor_b32 %[fl], %[fl], s44\n\t" \
    "s_and_b32 s43, s43, 0x2000\n\t" \
    "s_xor_b32 s43, s43, %[lx]\n\t" \
    "s_add_u32 s60, s60, s43\n\t" \
    "s_addc_u32 s61, s61, 0\n\t" \
    "s_add_u32 m0, %[wm], 0x18000\n\t" \
    "s_mov_b32 s44, 0x6c0000\n\t" \
    "buffer_load_dwordx4 v240, %[wr], s44 offen offset:0 nt lds\n\t" \
    "buffer_load_dwordx4 v241, %[wr], s44 offen offset:1024 nt lds\n\t" \
    "buffer_load_dwordx4 v242, %[wr], s44 offen offset:2048 nt lds\n\t" \
    "ds_read_b64_tr_b16 v[236:237], v244 offset:0\n\t" \
    "ds_read_b64_tr_b16 v[238:239], v244 offset:1536\n\t" \
    "ds_read_b64_tr_b16 v[164:165], v244 offset:32\n\t" \
    "ds_read_b64_tr_b16 v[166:167], v244 offset:1568\n\t" \
    "ds_read_b64_tr_b16 v[172:173], v244 offset:64\n\t" \
    "ds_read_b64_tr_b16 v[174:175], v244 offset:1600\n\t" \
    "v_add_u32 v244, 0x0, v243\n\t" \
    "s_waitcnt vmcnt(12) lgkmcnt(0)\n\t" \
    "s_barrier \n\t" \
    "s_add_u32 m0, %[wm], 0x1e000\n\t" \
    "s_mov_b32 s44, 0x720000\n\t" \
    "buffer_load_dwordx4 v240, %[wr], s44 offen offset:0 nt lds\n\t" \
    "buffer_load_dwordx4 v241, %[wr], s44 offen offset:1024 nt lds\n\t" \
    "buffer_load_dwordx4 v242, %[wr], s44 offen offset:2048 nt lds\n\t" \
    "ds_read_b64_tr_b16 v[176:177], v244 offset:0\n\t" \
    "ds_read_b64_tr_b16 v[178:179], v244 offset:1536\n\t" \
    "ds_read_b64_tr_b16 v[184:185], v244 offset:32\n\t" \
    "ds_read_b64_tr_b16 v[186:187], v244 offset:1568\n\t" \
    "ds_read_b64_tr_b16 v[192:193], v244 offset:64\n\t" \
    "ds_read_b64_tr_b16 v[194:195], v244 offset:1600\n\t" \
    "v_add_u32 v244, 0x6000, v243\n\t" \
    "s_waitcnt vmcnt(12) lgkmcnt(0)\n\t" \
    "s_barrier \n\t" \
    "ds_read_b64_tr_b16 v[180:181], v244 offset:0\n\t" \
    "ds_read_b64_tr_b16 v[182:183], v244 offset:1536\n\t" \
    "ds_read_b64_tr_b16 v[188:189], v244 offset:32\n\t" \
    "ds_read_b64_tr_b16 v[190:191], v244 offset:1568\n\t" \
    "ds_read_b64_tr_b16 v[196:197], v244 offset:64\n\t" \
    "ds_read_b64_tr_b16 v[198:199], v244 offset:1600\n\t" \
    "v_add_u32 v244, 0xc000, v243\n\t" \
    "s_waitcnt vmcnt(9) lgkmcnt(0)\n\t" \
    "s_barrier \n\t" \
    "ds_read_b64_tr_b16 v[200:201], v244 offset:0\n\t" \
    "ds_read_b64_tr_b16 v[202:203], v244 offset:1536\n\t" \
    "ds_read_b64_tr_b16 v[0:1], v244 offset:32\n\t" \
    "ds_read_b64_tr_b16 v[2:3], v244 offset:1568\n\t" \
    "ds_read_b64_tr_b16 v[8:9], v244 offset:64\n\t" \
    "ds_read_b64_tr_b16 v[10:11], v244 offset:1600\n\t" \
    "v_add_u32 v244, 0x12000, v243\n\t" \
    "s_waitcnt vmcnt(6) lgkmcnt(0)\n\t" \
    "s_barrier \n\t" \
    "ds_read_b64_tr_b16 v[204:205], v244 offset:0\n\t" \
    "ds_read_b64_tr_b16 v[206:207], v244 offset:1536\n\t" \
    "ds_read_b64_tr_b16 v[4:5], v244 offset:32\n\t" \
    "ds_read_b64_tr_b16 v[6:7], v244 offset:1568\n\t" \
    "ds_read_b64_tr_b16 v[12:13], v244 offset:64\n\t" \
    "ds_read_b64_tr_b16 v[14:15], v244 offset:1600\n\t" \
    "v_add_u32 v244, 0x18000, v243\n\t" \
    "s_waitcnt vmcnt(3) lgkmcnt(0)\n\t" \
    "s_barrier \n\t" \
    "ds_read_b64_tr_b16 v[16:17], v244 offset:0\n\t" \
    "ds_read_b64_tr_b16 v[18:19], v244 offset:1536\n\t" \
    "ds_read_b64_tr_b16 v[24:25], v244 offset:32\n\t" \
    "ds_read_b64_tr_b16 v[26:27], v244 offset:1568\n\t" \
    "ds_read_b64_tr_b16 v[32:33], v244 offset:64\n\t" \
    "ds_read_b64_tr_b16 v[34:35], v244 offset:1600\n\t" \
    "v_add_u32 v244, 0x1e000, v243\n\t" \
    "s_waitcnt vmcnt(0) lgkmcnt(0)\n\t" \
    "s_barrier \n\t" \
    "ds_read_b64_tr_b16 v[20:21], v244 offset:0\n\t" \
    "ds_read_b64_tr_b16 v[22:23], v244 offset:1536\n\t" \
    "ds_read_b64_tr_b16 v[28:29], v244 offset:32\n\t" \
    "ds_read_b64_tr_b16 v[30:31], v244 offset:1568\n\t" \
    "ds_read_b64_tr_b16 v[36:37], v244 offset:64\n\t" \
    "ds_read_b64_tr_b16 v[38:39], v244 offset:1600\n\t" \
    "s_waitcnt lgkmcnt(0)\n\t" \
    "s_barrier \n\t" \
    "ds_write_b128 v254, v[200:203] offset:55296\n\t" \
    "ds_write_b128 v254, v[204:207] offset:56320\n\t" \
    "ds_write_b128 v254, v[208:211] offset:57344\n\t" \
    "ds_write_b128 v254, v[212:215] offset:58368\n\t" \
    "ds_write_b128 v254, v[216:219] offset:59392\n\t" \
    "ds_write_b128 v254, v[220:223] offset:60416\n\t" \
    "ds_write_b128 v254, v[224:227] offset:61440\n\t" \
    "ds_write_b128 v254, v[228:231] offset:62464\n\t" \
    "ds_write_b128 v254, v[232:235] offset:63488\n\t" \
    "ds_write_b128 v254, v[236:239] offset:64512\n\t" \
    "s_waitcnt vmcnt(0) lgkmcnt(0)\n\t" \
    "s_barrier \n\t" \
    "ds_read_b128 v[208:211], v254 offset:24576\n\t" \
    "ds_read_b128 v[224:227], v253 offset:0\n\t" \
    "ds_read_b128 v[228:231], v253 offset:20480\n\t" \
    "ds_read_b128 v[212:215], v254 offset:25600\n\t" \
    "ds_read_b128 v[232:235], v253 offset:1024\n\t" \
    "ds_read_b128 v[236:239], v253 offset:21504\n\t" \
    "ds_read_b128 v[216:219], v254 offset:26624\n\t" \
    "ds_read_b128 v[240:243], v253 offset:2048\n\t" \
    "ds_read_b128 v[244:247], v253 offset:22528\n\t" \
    "ds_read_b128 v[220:223], v254 offset:27648\n\t" \
    "ds_read_b128 v[248:251], v253 offset:3072\n\t" \
    "s_waitcnt lgkmcnt(9)\n\t" \
    "v_mfma_f32_16x16x32_bf16 v[200:203], v[208:211], v[224:227], 0\n\t" \
    "ds_read_b128 v[224:227], v253 offset:23552\n\t" \
    "s_waitcnt lgkmcnt(9)\n\t" \
    "v_mfma_f32_16x16x32_bf16 v[204:207], v[208:211], v[228:231], 0\n\t" \
    "ds_read_b128 v[228:231], v253 offset:4096\n\t" \
    "ds_read_b128 v[208:211], v254 offset:28672\n\t" \
    "s_waitcnt lgkmcnt(9)\n\t" \
    "v_mfma_f32_16x16x32_bf16 v[200:203], v[212:215], v[232:235], v[200:203]\n\t" \
    "ds_read_b128 v[232:235], v253 offset:24576\n\t" \
    "s_waitcnt lgkmcnt(9)\n\t" \
    "v_mfma_f32_16x16x32_bf16 v[204:207], v[212:215], v[236:239], v[204:207]\n\t" \
    "ds_read_b128 v[236:239], v253 offset:5120\n\t" \
    "ds_read_b128 v[212:215], v254 offset:29696\n\t" \
    "s_waitcnt lgkmcnt(9)\n\t" \
    "v_mfma_f32_16x16x32_bf16 v[200:203], v[216:219], v[240:243], v[200:203]\n\t" \
    "ds_read_b128 v[240:243], v253 offset:25600\n\t" \
    "s_waitcnt lgkmcnt(9)\n\t" \
    "v_mfma_f32_16x16x32_bf16 v[204:207], v[216:219], v[244:247], v[204:207]\n\t" \
    "ds_read_b128 v[244:247], v253 offset:6144\n\t" \
    "ds_read_b128 v[216:219], v254 offset:30720\n\t" \
    "s_waitcnt lgkmcnt(9)\n\t" \
    "v_mfma_f32_16x16x32_bf16 v[200:203], v[220:223], v[248:251], v[200:203]\n\t" \
    "ds_read_b128 v[248:251], v253 offset:26624\n\t" \
    ".p2align 8\n\t" \
    ".Ls33_%=:\n\t" \
    "s_waitcnt lgkmcnt(9)\n\t" \
    "v_mfma_f32_16x16x32_bf16 v[204:207], v[220:223], v[224:227], v[204:207]\n\t" \
    "ds_read_b128 v[224:227], v253 offset:7168\n\t" \
    "ds_read_b128 v[220:223], v254 offset:31744\n\t" \
    "s_waitcnt lgkmcnt(9)\n\t" \
    "v_mfma_f32_16x16x32_bf16 v[200:203], v[208:211], v[228:231], v[200:203]\n\t" \
    "ds_read_b128 v[228:231], v253 offset:27648\n\t" \
    "s_waitcnt lgkmcnt(9)\n\t" \
    "v_mfma_f32_16x16x32_bf16 v[204:207], v[208:211], v[232:235], v[204:207]\n\t" \
    "ds_read_b128 v[232:235], v253 offset:8192\n\t" \
    "ds_read_b128 v[208:211], v254 offset:32768\n\t" \
    "s_waitcnt lgkmcnt(9)\n\t" \
    "v_mfma_f32_16x16x32_bf16 v[200:203], v[212:215], v[236:239], v[200:203]\n\t" \
    "ds_read_b128 v[236:239], v253 offset:28672\n\t" \
    "s_waitcnt lgkmcnt(9)\n\t" \
    "v_mfma_f32_16x16x32_bf16 v[204:207], v[212:215], v[240:243], v[204:207]\n\t" \
    "ds_read_b128 v[240:243], v253 offset:9216\n\t" \
    "ds_read_b128 v[212:215], v254 offset:33792\n\t" \
    "s_waitcnt lgkmcnt(9)\n\t" \
    "v_mfma_f32_16x16x32_bf16 v[200:203], v[216:219], v[244:247], v[200:203]\n\t" \
    "ds_read_b128 v[244:247], v253 offset:29696\n\t" \
    "s_waitcnt lgkmcnt(9)\n\t" \
    "v_mfma_f32_16x16x32_bf16 v[204:207], v[216:219], v[248:251], v[204:207]\n\t" \
    "s_waitcnt lgkmcnt(7)\n\t" \
    "v_mfma_f32_16x16x32_bf16 v[200:203], v[220:223], v[224:227], v[200:203]\n\t" \
    "s_waitcnt lgkmcnt(6)\n\t" \
    "v_mfma_f32_16x16x32_bf16 v[204:207], v[220:223], v[228:231], v[204:207]\n\t" \
    "s_waitcnt lgkmcnt(4)\n\t" \
    "v_mfma_f32_16x16x32_bf16 v[200:203], v[208:211], v[232:235], v[200:203]\n\t" \
    "s_waitcnt lgkmcnt(3)\n\t" \
    "v_mfma_f32_16x16x32_bf16 v[204:207], v[208:211], v[236:239], v[204:207]\n\t" \
    "s_waitcnt lgkmcnt(1)\n\t" \
    "v_mfma_f32_16x16x32_bf16 v[200:203], v[212:215], v[240:243], v[200:203]\n\t" \
    "s_waitcnt lgkmcnt(0)\n\t" \
    "v_mfma_f32_16x16x32_bf16 v[204:207], v[212:215], v[244:247], v[204:207]\n\t" \
    "ds_read_b128 v[224:227], v253 offset:0\n\t" \
    "ds_read_b128 v[244:247], v254 offset:57344\n\t" \
    "ds_read_b128 v[228:231], v253 offset:20480\n\t" \
    "ds_read_b128 v[232:235], v253 offset:1024\n\t" \
    "ds_read_b128 v[236:239], v253 offset:21504\n\t" \
    "ds_read_b128 v[248:251], v254 offset:58368\n\t" \
    "ds_write_b128 v253, v[200:203] offset:40960\n\t" \
    "s_nop 0\n\t" \
    "ds_write_b128 v253, v[204:207] offset:41984\n\t" \
    "s_nop 1\n\t" \
    "s_waitcnt lgkmcnt(7)\n\t" \
    "v_mfma_f32_16x16x32_bf16 v[204:207], v[40:43], v[224:227], 0\n\t" \
    "ds_read_b128 v[240:243], v253 offset:2048\n\t" \
    "v_mfma_f32_16x16x32_bf16 v[208:211], v[48:51], v[224:227], 0\n\t" \
    "s_waitcnt lgkmcnt(7)\n\t" \
    "v_mfma_f32_16x16x32_bf16 v[200:203], v[244:247], v[224:227], 0\n\t" \
    "s_waitcnt lgkmcnt(6)\n\t" \
    "v_mfma_f32_16x16x32_bf16 v[216:219], v[40:43], v[228:231], 0\n\t" \
    "ds_read_b128 v[224:227], v253 offset:22528\n\t" \
    "v_mfma_f32_16x16x32_bf16 v[220:223], v[48:51], v[228:231], 0\n\t" \
    "v_mfma_f32_16x16x32_bf16 v[212:215], v[244:247], v[228:231], 0\n\t" \
    "ds_read_b128 v[244:247], v254 offset:59392\n\t" \
    "s_waitcnt lgkmcnt(7)\n\t" \
    "v_mfma_f32_16x16x32_bf16 v[204:207], v[44:47], v[232:235], v[204:207]\n\t" \
    "ds_read_b128 v[228:231], v253 offset:3072\n\t" \
    "v_mfma_f32_16x16x32_bf16 v[208:211], v[52:55], v[232:235], v[208:211]\n\t" \
    "s_waitcnt lgkmcnt(6)\n\t" \
    "v_mfma_f32_16x16x32_bf16 v[200:203], v[248:251], v[232:235], v[200:203]\n\t" \
    "v_mfma_f32_16x16x32_bf16 v[216:219], v[44:47], v[236:239], v[216:219]\n\t" \
    "ds_read_b128 v[232:235], v253 offset:23552\n\t" \
    "v_mfma_f32_16x16x32_bf16 v[220:223], v[52:55], v[236:239], v[220:223]\n\t" \
    "v_mfma_f32_16x16x32_bf16 v[212:215], v[248:251], v[236:239], v[212:215]\n\t" \
    "ds_read_b128 v[248:251], v254 offset:60416\n\t" \
    "s_waitcnt lgkmcnt(5)\n\t" \
    "v_mfma_f32_16x16x32_bf16 v[204:207], v[64:67], v[240:243], v[204:207]\n\t" \
    "ds_read_b128 v[236:239], v253 offset:4096\n\t" \
    "v_mfma_f32_16x16x32_bf16 v[208:211], v[72:75], v[240:243], v[208:211]\n\t" \
    "v_mfma_f32_16x16x32_bf16 v[200:203], v[56:59], v[240:243], v[200:203]\n\t" \
    "s_waitcnt lgkmcnt(5)\n\t" \
    "v_mfma_f32_16x16x32_bf16 v[216:219], v[64:67], v[224:227], v[216:219]\n\t" \
    "ds_read_b128 v[240:243], v253 offset:24576\n\t" \
    "v_mfma_f32_16x16x32_bf16 v[220:223], v[72:75], v[224:227], v[220:223]\n\t" \
    "v_mfma_f32_16x16x32_bf16 v[212:215], v[56:59], v[224:227], v[212:215]\n\t" \
    "s_waitcnt lgkmcnt(4)\n\t" \
    "v_mfma_f32_16x16x32_bf16 v[204:207], v[68:71], v[228:231], v[204:207]\n\t" \
    "ds_read_b128 v[224:227], v253 offset:5120\n\t" \
    "v_mfma_f32_16x16x32_bf16 v[208:211], v[76:79], v[228:231], v[208:211]\n\t" \
    "v_mfma_f32_16x16x32_bf16 v[200:203], v[60:63], v[228:231], v[200:203]\n\t" \
    "s_waitcnt lgkmcnt(4)\n\t" \
    "v_mfma_f32_16x16x32_bf16 v[216:219], v[68:71], v[232:235], v[216:219]\n\t" \
    "ds_read_b128 v[228:231], v253 offset:25600\n\t" \
    "v_mfma_f32_16x16x32_bf16 v[220:223], v[76:79], v[232:235], v[220:223]\n\t" \
    "v_mfma_f32_16x16x32_bf16 v[212:215], v[60:63], v[232:235], v[212:215]\n\t" \
    "s_waitcnt lgkmcnt(3)\n\t" \
    "v_mfma_f32_16x16x32_bf16 v[204:207], v[80:83], v[236:239], v[204:207]\n\t" \
    "ds_read_b128 v[232:235], v253 offset:6144\n\t" \
    "v_mfma_f32_16x16x32_bf16 v[208:211], v[88:91], v[236:239], v[208:211]\n\t" \
    "v_mfma_f32_16x16x32_bf16 v[200:203], v[244:247], v[236:239], v[200:203]\n\t" \
    "s_waitcnt lgkmcnt(3)\n\t" \
    "v_mfma_f32_16x16x32_bf16 v[216:219], v[80:83], v[240:243], v[216:219]\n\t" \
    "ds_read_b128 v[236:239], v253 offset:26624\n\t" \
    "v_mfma_f32_16x16x32_bf16 v[220:223], v[88:91], v[240:243], v[220:223]\n\t" \
    "v_mfma_f32_16x16x32_bf16 v[212:215], v[244:247], v[240:243], v[212:215]\n\t" \
    "ds_read_b128 v[244:247], v254 offset:61440\n\t" \
    "s_waitcnt lgkmcnt(4)\n\t" \
    "v_mfma_f32_16x16x32_bf16 v[204:207], v[84:87], v[224:227], v[204:207]\n\t" \
    "ds_read_b128 v[240:243], v253 offset:7168\n\t" \
    "v_mfma_f32_16x16x32_bf16 v[208:211], v[92:95], v[224:227], v[208:211]\n\t" \
    "v_mfma_f32_16x16x32_bf16 v[200:203], v[248:251], v[224:227], v[200:203]\n\t" \
    "s_waitcnt lgkmcnt(4)\n\t" \
    "v_mfma_f32_16x16x32_bf16 v[216:219], v[84:87], v[228:231], v[216:219]\n\t" \
    "ds_read_b128 v[224:227], v253 offset:27648\n\t" \
    "v_mfma_f32_16x16x32_bf16 v[220:223], v[92:95], v[228:231], v[220:223]\n\t" \
    "v_mfma_f32_16x16x32_bf16 v[212:215], v[248:251], v[228:231], v[212:215]\n\t" \
    "ds_read_b128 v[248:251], v254 offset:62464\n\t" \
    "s_waitcnt lgkmcnt(5)\n\t" \
    "v_mfma_f32_16x16x32_bf16 v[204:207], v[104:107], v[232:235], v[204:207]\n\t" \
    "ds_read_b128 v[228:231], v253 offset:8192\n\t" \
    "v_mfma_f32_16x16x32_bf16 v[208:211], v[112:115], v[232:235], v[208:211]\n\t" \
    "v_mfma_f32_16x16x32_bf16 v[200:203], v[96:99], v[232:235], v[200:203]\n\t" \
    "s_waitcnt lgkmcnt(5)\n\t" \
    "v_mfma_f32_16x16x32_bf16 v[216:219], v[104:107], v[236:239], v[216:219]\n\t" \
    "ds_read_b128 v[232:235], v253 offset:28672\n\t" \
    "v_mfma_f32_16x16x32_bf16 v[220:223], v[112:115], v[236:239], v[220:223]\n\t" \
    "v_mfma_f32_16x16x32_bf16 v[212:215], v[96:99], v[236:239], v[212:215]\n\t" \
    "s_waitcnt lgkmcnt(4)\n\t" \
    "v_mfma_f32_16x16x32_bf16 v[204:207], v[108:111], v[240:243], v[204:207]\n\t" \
    "ds_read_b128 v[236:239], v253 offset:9216\n\t" \
    "v_mfma_f32_16x16x32_bf16 v[208:211], v[116:119], v[240:243], v[208:211]\n\t" \
    "v_mfma_f32_16x16x32_bf16 v[200:203], v[100:103], v[240:243], v[200:203]\n\t" \
    "s_waitcnt lgkmcnt(4)\n\t" \
    "v_mfma_f32_16x16x32_bf16 v[216:219], v[108:111], v[224:227], v[216:219]\n\t" \
    "ds_read_b128 v[240:243], v253 offset:29696\n\t" \
    "v_mfma_f32_16x16x32_bf16 v[220:223], v[116:119], v[224:227], v[220:223]\n\t" \
    "v_mfma_f32_16x16x32_bf16 v[212:215], v[100:103], v[224:227], v[212:215]\n\t" \
    "s_waitcnt lgkmcnt(3)\n\t" \
    "v_mfma_f32_16x16x32_bf16 v[204:207], v[120:123], v[228:231], v[204:207]\n\t" \
    "ds_read_b128 v[224:227], v253 offset:10240\n\t" \
    "v_mfma_f32_16x16x32_bf16 v[208:211], v[128:131], v[228:231], v[208:211]\n\t" \
    "v_mfma_f32_16x16x32_bf16 v[200:203], v[244:247], v[228:231], v[200:203]\n\t" \
    "s_waitcnt lgkmcnt(3)\n\t" \
    "v_mfma_f32_16x16x32_bf16 v[216:219], v[120:123], v[232:235], v[216:219]\n\t" \
    "ds_read_b128 v[228:231], v253 offset:30720\n\t" \
    "v_mfma_f32_16x16x32_bf16 v[220:223], v[128:131], v[232:235], v[220:223]\n\t" \
    "v_mfma_f32_16x16x32_bf16 v[212:215], v[244:247], v[232:235], v[212:215]\n\t" \
    "ds_read_b128 v[244:247], v254 offset:63488\n\t" \
    "s_waitcnt lgkmcnt(4)\n\t" \
    "v_mfma_f32_16x16x32_bf16 v[204:207], v[124:127], v[236:239], v[204:207]\n\t" \
    "ds_read_b128 v[232:235], v253 offset:11264\n\t" \
    "v_mfma_f32_16x16x32_bf16 v[208:211], v[132:135], v[236:239], v[208:211]\n\t" \
    "v_mfma_f32_16x16x32_bf16 v[200:203], v[248:251], v[236:239], v[200:203]\n\t" \
    "s_waitcnt lgkmcnt(4)\n\t" \
    "v_mfma_f32_16x16x32_bf16 v[216:219], v[124:127], v[240:243], v[216:219]\n\t" \
    "ds_read_b128 v[236:239], v253 offset:31744\n\t" \
    "v_mfma_f32_16x16x32_bf16 v[220:223], v[132:135], v[240:243], v[220:223]\n\t" \
    "v_mfma_f32_16x16x32_bf16 v[212:215], v[248:251], v[240:243], v[212:215]\n\t" \
    "ds_read_b128 v[248:251], v254 offset:64512\n\t" \
    "s_setprio 2\n\t" \
    "s_waitcnt lgkmcnt(0)\n\t" \
    "s_barrier \n\t" \
    "v_mfma_f32_16x16x32_bf16 v[204:207], v[144:147], v[224:227], v[204:207]\n\t" \
    "ds_read_b128 v[240:243], v253 offset:12288\n\t" \
    "v_mfma_f32_16x16x32_bf16 v[208:211], v[152:155], v[224:227], v[208:211]\n\t" \
    "v_mfma_f32_16x16x32_bf16 v[200:203], v[136:139], v[224:227], v[200:203]\n\t" \
    "v_mfma_f32_16x16x32_bf16 v[216:219], v[144:147], v[228:231], v[216:219]\n\t" \
    "ds_read_b128 v[224:227], v253 offset:32768\n\t" \
    "s_and_b32 s44, s40, 3\n\t" \
    "v_mfma_f32_16x16x32_bf16 v[220:223], v[152:155], v[228:231], v[220:223]\n\t" \
    "s_lshl_b32 s44, s44, 22\n\t" \
    "v_mfma_f32_16x16x32_bf16 v[212:215], v[136:139], v[228:231], v[212:215]\n\t" \
    "s_add_u32 s44, s44, %[mb]\n\t" \
    "v_mfma_f32_16x16x32_bf16 v[204:207], v[148:151], v[232:235], v[204:207]\n\t" \
    "ds_read_b128 v[228:231], v253 offset:13312\n\t" \
    "s_add_u32 s46, s44, 0x10000\n\t" \
    "v_mfma_f32_16x16x32_bf16 v[208:211], v[156:159], v[232:235], v[208:211]\n\t" \
    "s_add_u32 s47, s44, 0x10400\n\t" \
    "v_mfma_f32_16x16x32_bf16 v[200:203], v[140:143], v[232:235], v[200:203]\n\t" \
    "v_mfma_f32_16x16x32_bf16 v[216:219], v[148:151], v[236:239], v[216:219]\n\t" \
    "ds_read_b128 v[232:235], v253 offset:33792\n\t" \
    "v_mfma_f32_16x16x32_bf16 v[220:223], v[156:159], v[236:239], v[220:223]\n\t" \
    "v_mfma_f32_16x16x32_bf16 v[212:215], v[140:143], v[236:239], v[212:215]\n\t" \
    "s_waitcnt lgkmcnt(3)\n\t" \
    "v_mfma_f32_16x16x32_bf16 v[204:207], v[160:163], v[240:243], v[204:207]\n\t" \
    "ds_read_b128 v[236:239], v253 offset:16384\n\t" \
    "v_mfma_f32_16x16x32_bf16 v[208:211], v[168:171], v[240:243], v[208:211]\n\t" \
    "v_mfma_f32_16x16x32_bf16 v[200:203], v[244:247], v[240:243], v[200:203]\n\t" \
    "s_waitcnt lgkmcnt(3)\n\t" \
    "v_mfma_f32_16x16x32_bf16 v[216:219], v[160:163], v[224:227], v[216:219]\n\t" \
    "ds_read_b128 v[240:243], v253 offset:36864\n\t" \
    "v_mfma_f32_16x16x32_bf16 v[220:223], v[168:171], v[224:227], v[220:223]\n\t" \
    "v_mfma_f32_16x16x32_bf16 v[212:215], v[244:247], v[224:227], v[212:215]\n\t" \
    "ds_read_b128 v[244:247], v254 offset:55296\n\t" \
    "s_waitcnt lgkmcnt(4)\n\t" \
    "v_mfma_f32_16x16x32_bf16 v[204:207], v[164:167], v[228:231], v[204:207]\n\t" \
    "ds_read_b128 v[224:227], v253 offset:17408\n\t" \
    "v_mfma_f32_16x16x32_bf16 v[208:211], v[172:175], v[228:231], v[208:211]\n\t" \
    "v_mfma_f32_16x16x32_bf16 v[200:203], v[248:251], v[228:231], v[200:203]\n\t" \
    "s_waitcnt lgkmcnt(4)\n\t" \
    "v_mfma_f32_16x16x32_bf16 v[216:219], v[164:167], v[232:235], v[216:219]\n\t" \
    "ds_read_b128 v[228:231], v253 offset:37888\n\t" \
    "v_mfma_f32_16x16x32_bf16 v[220:223], v[172:175], v[232:235], v[220:223]\n\t" \
    "v_mfma_f32_16x16x32_bf16 v[212:215], v[248:251], v[232:235], v[212:215]\n\t" \
    "ds_read_b128 v[248:251], v254 offset:56320\n\t" \
    "s_waitcnt lgkmcnt(5)\n\t" \
    "v_mfma_f32_16x16x32_bf16 v[204:207], v[0:3], v[236:239], v[204:207]\n\t" \
    "ds_read_b128 v[232:235], v253 offset:14336\n\t" \
    "v_mfma_f32_16x16x32_bf16 v[208:211], v[8:11], v[236:239], v[208:211]\n\t" \
    "s_waitcnt lgkmcnt(4)\n\t" \
    "v_mfma_f32_16x16x32_bf16 v[200:203], v[244:247], v[236:239], v[200:203]\n\t" \
    "v_mfma_f32_16x16x32_bf16 v[216:219], v[0:3], v[240:243], v[216:219]\n\t" \
    "ds_read_b128 v[236:239], v253 offset:15360\n\t" \
    "v_mfma_f32_16x16x32_bf16 v[220:223], v[8:11], v[240:243], v[220:223]\n\t" \
    "v_mfma_f32_16x16x32_bf16 v[212:215], v[244:247], v[240:243], v[212:215]\n\t" \
    "s_waitcnt lgkmcnt(4)\n\t" \
    "v_mfma_f32_16x16x32_bf16 v[204:207], v[4:7], v[224:227], v[204:207]\n\t" \
    "ds_read_b128 v[240:243], v253 offset:18432\n\t" \
    "v_mfma_f32_16x16x32_bf16 v[208:211], v[12:15], v[224:227], v[208:211]\n\t" \
    "s_waitcnt lgkmcnt(3)\n\t" \
    "v_mfma_f32_16x16x32_bf16 v[200:203], v[248:251], v[224:227], v[200:203]\n\t" \
    "v_mfma_f32_16x16x32_bf16 v[216:219], v[4:7], v[228:231], v[216:219]\n\t" \
    "ds_read_b128 v[224:227], v253 offset:19456\n\t" \
    "v_mfma_f32_16x16x32_bf16 v[220:223], v[12:15], v[228:231], v[220:223]\n\t" \
    "v_mfma_f32_16x16x32_bf16 v[212:215], v[248:251], v[228:231], v[212:215]\n\t" \
    "s_waitcnt lgkmcnt(3)\n\t" \
    "v_mfma_f32_16x16x32_bf16 v[204:207], v[184:187], v[232:235], v[204:207]\n\t" \
    "ds_read_b128 v[228:231], v253 offset:34816\n\t" \
    "v_mov_b64 v[244:245], 0\n\t" \
    "v_mfma_f32_16x16x32_bf16 v[208:211], v[192:195], v[232:235], v[208:211]\n\t" \
    "v_mov_b64 v[246:247], 0\n\t" \
    "v_mfma_f32_16x16x32_bf16 v[200:203], v[176:179], v[232:235], v[200:203]\n\t" \
    "v_mov_b64 v[248:249], 0\n\t" \
    "s_waitcnt lgkmcnt(3)\n\t" \
    "v_mfma_f32_16x16x32_bf16 v[204:207], v[188:191], v[236:239], v[204:207]\n\t" \
    "ds_read_b128 v[232:235], v253 offset:35840\n\t" \
    "v_mov_b64 v[250:251], 0\n\t" \
    "v_mfma_f32_16x16x32_bf16 v[208:211], v[196:199], v[236:239], v[208:211]\n\t" \
    "v_mfma_f32_16x16x32_bf16 v[200:203], v[180:183], v[236:239], v[200:203]\n\t" \
    "s_waitcnt lgkmcnt(3)\n\t" \
    "v_mfma_f32_16x16x32_bf16 v[204:207], v[24:27], v[240:243], v[204:207]\n\t" \
    "ds_read_b128 v[236:239], v253 offset:38912\n\t" \
    "v_mfma_f32_16x16x32_bf16 v[208:211], v[32:35], v[240:243], v[208:211]\n\t" \
    "v_mfma_f32_16x16x32_bf16 v[200:203], v[16:19], v[240:243], v[200:203]\n\t" \
    "s_waitcnt lgkmcnt(3)\n\t" \
    "v_mfma_f32_16x16x32_bf16 v[204:207], v[28:31], v[224:227], v[204:207]\n\t" \
    "ds_read_b128 v[240:243], v253 offset:39936\n\t" \
    "v_mfma_f32_16x16x32_bf16 v[208:211], v[36:39], v[224:227], v[208:211]\n\t" \
    "v_mfma_f32_16x16x32_bf16 v[200:203], v[20:23], v[224:227], v[200:203]\n\t" \
    "s_waitcnt lgkmcnt(3)\n\t" \
    "v_mfma_f32_16x16x32_bf16 v[216:219], v[184:187], v[228:231], v[216:219]\n\t" \
    "s_waitcnt lgkmcnt(2)\n\t" \
    "v_mfma_f32_16x16x32_bf16 v[216:219], v[188:191], v[232:235], v[216:219]\n\t" \
    "v_cvt_pk_bf16_f32 v204, v204, v208\n\t" \
    "v_cvt_pk_bf16_f32 v205, v205, v209\n\t" \
    "s_waitcnt lgkmcnt(1)\n\t" \
    "v_mfma_f32_16x16x32_bf16 v[216:219], v[24:27], v[236:239], v[216:219]\n\t" \
    "v_cvt_pk_bf16_f32 v206, v206, v210\n\t" \
    "v_cvt_pk_bf16_f32 v207, v207, v211\n\t" \
    "s_waitcnt lgkmcnt(0)\n\t" \
    "v_mfma_f32_16x16x32_bf16 v[216:219], v[28:31], v[240:243], v[216:219]\n\t" \
    "ds_write_b128 v254, v[204:207] offset:5120\n\t" \
    "v_mfma_f32_16x16x32_bf16 v[220:223], v[192:195], v[228:231], v[220:223]\n\t" \
    "v_mfma_f32_16x16x32_bf16 v[220:223], v[196:199], v[232:235], v[220:223]\n\t" \
    "v_cvt_pk_bf16_f32 v200, v200, v201\n\t" \
    "v_mfma_f32_16x16x32_bf16 v[220:223], v[32:35], v[236:239], v[220:223]\n\t" \
    "v_cvt_pk_bf16_f32 v201, v202, v203\n\t" \
    "ds_write_b64 v254, v[200:201] offset:1032\n\t" \
    "v_mfma_f32_16x16x32_bf16 v[220:223], v[36:39], v[240:243], v[220:223]\n\t" \
    "v_mfma_f32_16x16x32_bf16 v[212:215], v[176:179], v[228:231], v[212:215]\n\t" \
    "v_mfma_f32_16x16x32_bf16 v[212:215], v[180:183], v[232:235], v[212:215]\n\t" \
    "ds_read_b128 v[200:203], v254 offset:55296\n\t" \
    "ds_read_b128 v[204:207], v254 offset:56320\n\t" \
    "v_mfma_f32_16x16x32_bf16 v[212:215], v[16:19], v[236:239], v[212:215]\n\t" \
    "v_mov_b64 v[208:209], 0\n\t" \
    "v_mov_b64 v[210:211], 0\n\t" \
    "v_mfma_f32_16x16x32_bf16 v[212:215], v[20:23], v[240:243], v[212:215]\n\t" \
    "v_cvt_pk_bf16_f32 v216, v216, v220\n\t" \
    "v_cvt_pk_bf16_f32 v217, v217, v221\n\t" \
    "s_setprio 0\n\t" \
    "v_cvt_pk_bf16_f32 v218, v218, v222\n\t" \
    "v_cvt_pk_bf16_f32 v219, v219, v223\n\t" \
    "ds_write_b128 v254, v[216:219] offset:11264\n\t" \
    "ds_read_b128 v[216:219], v254 offset:57344\n\t" \
    "ds_read_b128 v[220:223], v254 offset:58368\n\t" \
    "s_nop 0\n\t" \
    "v_cvt_pk_bf16_f32 v212, v212, v213\n\t" \
    "v_cvt_pk_bf16_f32 v213, v214, v215\n\t" \
    "ds_write_b64 v254, v[212:213] offset:7176\n\t" \
    "s_sleep 3\n\t" \
    "s_mov_b32 m0, 0xc000\n\t" \
    "s_nop 0\n\t" \
    "buffer_load_dwordx4 v253, s[60:63], s46 offen sc1 lds\n\t" \
    "s_mov_b32 m0, 0xc400\n\t" \
    "s_nop 0\n\t" \
    "buffer_load_dwordx4 v253, s[60:63], s47 offen sc1 lds\n\t" \
    "s_waitcnt lgkmcnt(0)\n\t" \
    "v_bfe_i32 v212, v253, 5, 1\n\t" \
    "v_and_b32 v212, 0xaaaa, v212\n\t" \
    "v_or_b32 v212, 0x4444, v212\n\t" \
    "v_and_b32 v213, 0xffff, v255\n\t" \
    "v_lshrrev_b32 v214, 16, v255\n\t" \
    ".Ls34_%=:\n\t" \
    "s_waitcnt vmcnt(0)\n\t" \
    "ds_read_b128 v[224:227], v253 offset:49152\n\t" \
    "ds_read_b128 v[228:231], v253 offset:50176\n\t" \
    "s_waitcnt lgkmcnt(1)\n\t" \
    "v_max_u32 v215, v224, v225\n\t" \
    "v_max3_u32 v215, v226, v227, v215\n\t" \
    "s_waitcnt lgkmcnt(0)\n\t" \
    "v_max3_u32 v215, v228, v229, v215\n\t" \
    "v_max3_u32 v215, v230, v231, v215\n\t" \
    "v_cmp_eq_u32 vcc, -1, v215\n\t" \
    "s_cbranch_vccz .Ls35_%=\n\t" \
    "v_max_u32 v215, v224, v225\n\t" \
    "v_max3_u32 v215, v226, v227, v215\n\t" \
    "v_cmp_eq_u32 vcc, -1, v215\n\t" \
    "s_cbranch_vccz .Ls36_%=\n\t" \
    "s_mov_b32 m0, 0xc000\n\t" \
    "s_nop 0\n\t" \
    "buffer_load_dwordx4 v253, s[60:63], s46 offen sc1 lds\n\t" \
    ".Ls36_%=:\n\t" \
    "v_max_u32 v215, v228, v229\n\t" \
    "v_max3_u32 v215, v230, v231, v215\n\t" \
    "v_cmp_eq_u32 vcc, -1, v215\n\t" \
    "s_cbranch_vccz .Ls37_%=\n\t" \
    "s_mov_b32 m0, 0xc400\n\t" \
    "s_nop 0\n\t" \
    "buffer_load_dwordx4 v253, s[60:63], s47 offen sc1 lds\n\t" \
    ".Ls37_%=:\n\t" \
    "s_cmp_eq_u32 s41, 0\n\t" \
    "s_cbranch_scc1 .Ls35_%=\n\t" \
    "s_sub_u32 s41, s41, 1\n\t" \
    "s_branch .Ls34_%=\n\t" \
    ".Ls35_%=:\n\t" \
    "s_setprio 3\n\t" \
    "s_waitcnt lgkmcnt(0)\n\t" \
    "s_barrier \n\t" \
    "ds_read_b128 v[232:235], v253 offset:40960\n\t" \
    "ds_read_b128 v[236:239], v253 offset:41984\n\t" \
    "ds_read_b128 v[240:243], v253 offset:43008\n\t" \
    "v_smfmac_f32_16x16x64_bf16 v[244:247], v[224:227], v[0:7], v212\n\t" \
    "v_smfmac_f32_16x16x64_bf16 v[248:251], v[224:227], v[8:15], v212\n\t" \
    "v_smfmac_f32_16x16x64_bf16 v[208:211], v[224:227], v[200:207], v212\n\t" \
    "ds_read_b128 v[200:203], v254 offset:59392\n\t" \
    "ds_read_b128 v[204:207], v254 offset:60416\n\t" \
    "ds_read_b128 v[224:227], v253 offset:44032\n\t" \
    "v_smfmac_f32_16x16x64_bf16 v[208:211], v[228:231], v[16:23], v212\n\t" \
    "v_smfmac_f32_16x16x64_bf16 v[244:247], v[228:231], v[24:31], v212\n\t" \
    "v_smfmac_f32_16x16x64_bf16 v[248:251], v[228:231], v[32:39], v212\n\t" \
    "ds_read_b128 v[228:231], v253 offset:45056\n\t" \
    "s_waitcnt lgkmcnt(6)\n\t" \
    "v_smfmac_f32_16x16x64_bf16 v[208:211], v[232:235], v[216:223], v212\n\t" \
    "ds_read_b128 v[216:219], v254 offset:61440\n\t" \
    "ds_read_b128 v[220:223], v254 offset:62464\n\t" \
    "v_smfmac_f32_16x16x64_bf16 v[244:247], v[232:235], v[40:47], v212\n\t" \
    "v_smfmac_f32_16x16x64_bf16 v[248:251], v[232:235], v[48:55], v212\n\t" \
    "ds_read_b128 v[232:235], v253 offset:46080\n\t" \
    "s_waitcnt lgkmcnt(8)\n\t" \
    "v_smfmac_f32_16x16x64_bf16 v[208:211], v[236:239], v[56:63], v212\n\t" \
    "v_smfmac_f32_16x16x64_bf16 v[244:247], v[236:239], v[64:71], v212\n\t" \
    "v_smfmac_f32_16x16x64_bf16 v[248:251], v[236:239], v[72:79], v212\n\t" \
    "ds_read_b128 v[236:239], v253 offset:47104\n\t" \
    "s_waitcnt lgkmcnt(6)\n\t" \
    "v_smfmac_f32_16x16x64_bf16 v[208:211], v[240:243], v[200:207], v212\n\t" \
    "ds_read_b128 v[200:203], v254 offset:63488\n\t" \
    "ds_read_b128 v[204:207], v254 offset:64512\n\t" \
    "v_smfmac_f32_16x16x64_bf16 v[244:247], v[240:243], v[80:87], v212\n\t" \
    "v_smfmac_f32_16x16x64_bf16 v[248:251], v[240:243], v[88:95], v212\n\t" \
    "ds_read_b128 v[240:243], v253 offset:48128\n\t" \
    "s_waitcnt lgkmcnt(8)\n\t" \
    "v_smfmac_f32_16x16x64_bf16 v[208:211], v[224:227], v[96:103], v212\n\t" \
    "v_smfmac_f32_16x16x64_bf16 v[244:247], v[224:227], v[104:111], v212\n\t" \
    "v_smfmac_f32_16x16x64_bf16 v[248:251], v[224:227], v[112:119], v212\n\t" \
    "s_waitcnt lgkmcnt(5)\n\t" \
    "v_smfmac_f32_16x16x64_bf16 v[208:211], v[228:231], v[216:223], v212\n\t" \
    "s_waitcnt lgkmcnt(4)\n\t" \
    "v_smfmac_f32_16x16x64_bf16 v[208:211], v[232:235], v[136:143], v212\n\t" \
    "s_waitcnt lgkmcnt(1)\n\t" \
    "v_smfmac_f32_16x16x64_bf16 v[208:211], v[236:239], v[200:207], v212\n\t" \
    "s_waitcnt lgkmcnt(0)\n\t" \
    "v_smfmac_f32_16x16x64_bf16 v[208:211], v[240:243], v[176:183], v212\n\t" \
    "v_smfmac_f32_16x16x64_bf16 v[244:247], v[228:231], v[120:127], v212\n\t" \
    "v_smfmac_f32_16x16x64_bf16 v[244:247], v[232:235], v[144:151], v212\n\t" \
    "v_smfmac_f32_16x16x64_bf16 v[244:247], v[236:239], v[160:167], v212\n\t" \
    "v_add_f32 v208, v208, v210\n\t" \
    "v_add_f32 v209, v209, v211\n\t" \
    "v_cvt_pk_bf16_f32 v208, v208, v209\n\t" \
    "v_smfmac_f32_16x16x64_bf16 v[244:247], v[240:243], v[184:191], v212\n\t" \
    "ds_write_b16 v213, v208 offset:0\n\t" \
    "ds_write_b16_d16_hi v213, v208 offset:16\n\t" \
    "v_smfmac_f32_16x16x64_bf16 v[248:251], v[228:231], v[128:135], v212\n\t" \
    "v_smfmac_f32_16x16x64_bf16 v[248:251], v[232:235], v[152:159], v212\n\t" \
    "v_smfmac_f32_16x16x64_bf16 v[248:251], v[236:239], v[168:175], v212\n\t" \
    "v_add_f32 v244, v244, v246\n\t" \
    "v_add_f32 v245, v245, v247\n\t" \
    "v_smfmac_f32_16x16x64_bf16 v[248:251], v[240:243], v[192:199], v212\n\t" \
    "s_nop 7\n\t" \
    "v_add_f32 v248, v248, v250\n\t" \
    "v_add_f32 v249, v249, v251\n\t" \
    "v_cvt_pk_bf16_f32 v244, v244, v248\n\t" \
    "v_cvt_pk_bf16_f32 v245, v245, v249\n\t" \
    "ds_write2_b32 v214, v244, v245 offset1:4\n\t" \
    "s_setprio 0\n\t" \
    "s_sleep 1\n\t" \
    "ds_read_b128 v[208:211], v254 offset:24576\n\t" \
    "ds_read_b128 v[224:227], v253 offset:0\n\t" \
    "ds_read_b128 v[228:231], v253 offset:20480\n\t" \
    "ds_read_b128 v[212:215], v254 offset:25600\n\t" \
    "ds_read_b128 v[232:235], v253 offset:1024\n\t" \
    "ds_read_b128 v[236:239], v253 offset:21504\n\t" \
    "ds_read_b128 v[216:219], v254 offset:26624\n\t" \
    "ds_read_b128 v[240:243], v253 offset:2048\n\t" \
    "ds_read_b128 v[244:247], v253 offset:22528\n\t" \
    "ds_read_b128 v[220:223], v254 offset:27648\n\t" \
    "ds_read_b128 v[248:251], v253 offset:3072\n\t" \
    "s_waitcnt lgkmcnt(9)\n\t" \
    "v_mfma_f32_16x16x32_bf16 v[200:203], v[208:211], v[224:227], 0\n\t" \
    "ds_read_b128 v[224:227], v253 offset:23552\n\t" \
    "s_waitcnt lgkmcnt(9)\n\t" \
    "v_mfma_f32_16x16x32_bf16 v[204:207], v[208:211], v[228:231], 0\n\t" \
    "ds_read_b128 v[228:231], v253 offset:4096\n\t" \
    "ds_read_b128 v[208:211], v254 offset:28672\n\t" \
    "s_waitcnt lgkmcnt(9)\n\t" \
    "v_mfma_f32_16x16x32_bf16 v[200:203], v[212:215], v[232:235], v[200:203]\n\t" \
    "ds_read_b128 v[232:235], v253 offset:24576\n\t" \
    "s_waitcnt lgkmcnt(9)\n\t" \
    "v_mfma_f32_16x16x32_bf16 v[204:207], v[212:215], v[236:239], v[204:207]\n\t" \
    "ds_read_b128 v[236:239], v253 offset:5120\n\t" \
    "ds_read_b128 v[212:215], v254 offset:29696\n\t" \
    "s_waitcnt lgkmcnt(9)\n\t" \
    "v_mfma_f32_16x16x32_bf16 v[200:203], v[216:219], v[240:243], v[200:203]\n\t" \
    "ds_read_b128 v[240:243], v253 offset:25600\n\t" \
    "s_waitcnt lgkmcnt(9)\n\t" \
    "v_mfma_f32_16x16x32_bf16 v[204:207], v[216:219], v[244:247], v[204:207]\n\t" \
    "ds_read_b128 v[244:247], v253 offset:6144\n\t" \
    "ds_read_b128 v[216:219], v254 offset:30720\n\t" \
    "s_waitcnt lgkmcnt(9)\n\t" \
    "v_mfma_f32_16x16x32_bf16 v[200:203], v[220:223], v[248:251], v[200:203]\n\t" \
    "ds_read_b128 v[248:251], v253 offset:26624\n\t" \
    "s_waitcnt lgkmcnt(0)\n\t" \
    "s_barrier \n\t" \
    "s_add_u32 s40, s40, 1\n\t" \
    "s_cmp_lg_u32 s40, 64\n\t" \
    "s_cbranch_scc1 .Ls33_%=\n\t" \
    "s_branch .Lend%=\n\t" \
    ".Ls2_%=:\n\t" \
    "v_add_u32 v244, 0x18000, v243\n\t" \
    "s_waitcnt vmcnt(12) lgkmcnt(0)\n\t" \
    "s_barrier \n\t" \
    "s_add_u32 m0, %[wm], 0x12000\n\t" \
    "s_mov_b32 s44, 0x1e0000\n\t" \
    "buffer_load_dwordx4 v240, %[wr], s44 offen offset:0 nt lds\n\t" \
    "buffer_load_dwordx4 v241, %[wr], s44 offen offset:1024 nt lds\n\t" \
    "buffer_load_dwordx4 v242, %[wr], s44 offen offset:2048 nt lds\n\t" \
    "ds_read_b64_tr_b16 v[208:209], v244 offset:0\n\t" \
    "ds_read_b64_tr_b16 v[210:211], v244 offset:1536\n\t" \
    "ds_read_b64_tr_b16 v[40:41], v244 offset:32\n\t" \
    "ds_read_b64_tr_b16 v[42:43], v244 offset:1568\n\t" \
    "ds_read_b64_tr_b16 v[48:49], v244 offset:64\n\t" \
    "ds_read_b64_tr_b16 v[50:51], v244 offset:1600\n\t" \
    "v_add_u32 v244, 0x1e000, v243\n\t" \
    "s_waitcnt vmcnt(12) lgkmcnt(0)\n\t" \
    "s_barrier \n\t" \
    "s_add_u32 m0, %[wm], 0x18000\n\t" \
    "s_mov_b32 s44, 0x240000\n\t" \
    "buffer_load_dwordx4 v240, %[wr], s44 offen offset:0 nt lds\n\t" \
    "buffer_load_dwordx4 v241, %[wr], s44 offen offset:1024 nt lds\n\t" \
    "buffer_load_dwordx4 v242, %[wr], s44 offen offset:2048 nt lds\n\t" \
    "ds_read_b64_tr_b16 v[212:213], v244 offset:0\n\t" \
    "ds_read_b64_tr_b16 v[214:215], v244 offset:1536\n\t" \
    "ds_read_b64_tr_b16 v[44:45], v244 offset:32\n\t" \
    "ds_read_b64_tr_b16 v[46:47], v244 offset:1568\n\t" \
    "ds_read_b64_tr_b16 v[52:53], v244 offset:64\n\t" \
    "ds_read_b64_tr_b16 v[54:55], v244 offset:1600\n\t" \
    "v_add_u32 v244, 0x0, v243\n\t" \
    "s_waitcnt vmcnt(12) lgkmcnt(0)\n\t" \
    "s_barrier \n\t" \
    "s_add_u32 m0, %[wm], 0x1e000\n\t" \
    "s_mov_b32 s44, 0x2a0000\n\t" \
    "buffer_load_dwordx4 v240, %[wr], s44 offen offset:0 nt lds\n\t" \
    "buffer_load_dwordx4 v241, %[wr], s44 offen offset:1024 nt lds\n\t" \
    "buffer_load_dwordx4 v242, %[wr], s44 offen offset:2048 nt lds\n\t" \
    "ds_read_b64_tr_b16 v[56:57], v244 offset:0\n\t" \
    "ds_read_b64_tr_b16 v[58:59], v244 offset:1536\n\t" \
    "ds_read_b64_tr_b16 v[64:65], v244 offset:32\n\t" \
    "ds_read_b64_tr_b16 v[66:67], v244 offset:1568\n\t" \
    "ds_read_b64_tr_b16 v[72:73], v244 offset:64\n\t" \
    "ds_read_b64_tr_b16 v[74:75], v244 offset:1600\n\t" \
    "v_add_u32 v244, 0x6000, v243\n\t" \
    "s_waitcnt vmcnt(12) lgkmcnt(0)\n\t" \
    "s_barrier \n\t" \
    "s_add_u32 m0, %[wm], 0x0\n\t" \
    "s_mov_b32 s44, 0x300000\n\t" \
    "buffer_load_dwordx4 v240, %[wr], s44 offen offset:0 nt lds\n\t" \
    "buffer_load_dwordx4 v241, %[wr], s44 offen offset:1024 nt lds\n\t" \
    "buffer_load_dwordx4 v242, %[wr], s44 offen offset:2048 nt lds\n\t" \
    "ds_read_b64_tr_b16 v[60:61], v244 offset:0\n\t" \
    "ds_read_b64_tr_b16 v[62:63], v244 offset:1536\n\t" \
    "ds_read_b64_tr_b16 v[68:69], v244 offset:32\n\t" \
    "ds_read_b64_tr_b16 v[70:71], v244 offset:1568\n\t" \
    "ds_read_b64_tr_b16 v[76:77], v244 offset:64\n\t" \
    "ds_read_b64_tr_b16 v[78:79], v244 offset:1600\n\t" \
    "v_add_u32 v244, 0xc000, v243\n\t" \
    "s_waitcnt vmcnt(12) lgkmcnt(0)\n\t" \
    "s_barrier \n\t" \
    "s_add_u32 m0, %[wm], 0x6000\n\t" \
    "s_mov_b32 s44, 0x360000\n\t" \
    "buffer_load_dwordx4 v240, %[wr], s44 offen offset:0 nt lds\n\t" \
    "buffer_load_dwordx4 v241, %[wr], s44 offen offset:1024 nt lds\n\t" \
    "buffer_load_dwordx4 v242, %[wr], s44 offen offset:2048 nt lds\n\t" \
    "ds_read_b64_tr_b16 v[216:217], v244 offset:0\n\t" \
    "ds_read_b64_tr_b16 v[218:219], v244 offset:1536\n\t" \
    "ds_read_b64_tr_b16 v[80:81], v244 offset:32\n\t" \
    "ds_read_b64_tr_b16 v[82:83], v244 offset:1568\n\t" \
    "ds_read_b64_tr_b16 v[88:89], v244 offset:64\n\t" \
    "ds_read_b64_tr_b16 v[90:91], v244 offset:1600\n\t" \
    "v_add_u32 v244, 0x12000, v243\n\t" \
    "s_waitcnt vmcnt(12) lgkmcnt(0)\n\t" \
    "s_barrier \n\t" \
    "s_add_u32 m0, %[wm], 0xc000\n\t" \
    "s_mov_b32 s44, 0x3c0000\n\t" \
    "buffer_load_dwordx4 v240, %[wr], s44 offen offset:0 nt lds\n\t" \
    "buffer_load_dwordx4 v241, %[wr], s44 offen offset:1024 nt lds\n\t" \
    "buffer_load_dwordx4 v242, %[wr], s44 offen offset:2048 nt lds\n\t" \
    "ds_read_b64_tr_b16 v[220:221], v244 offset:0\n\t" \
    "ds_read_b64_tr_b16 v[222:223], v244 offset:1536\n\t" \
    "ds_read_b64_tr_b16 v[84:85], v244 offset:32\n\t" \
    "ds_read_b64_tr_b16 v[86:87], v244 offset:1568\n\t" \
    "ds_read_b64_tr_b16 v[92:93], v244 offset:64\n\t" \
    "ds_read_b64_tr_b16 v[94:95], v244 offset:1600\n\t" \
    "v_add_u32 v244, 0x18000, v243\n\t" \
    "s_waitcnt vmcnt(12) lgkmcnt(0)\n\t" \
    "s_barrier \n\t" \
    "s_add_u32 m0, %[wm], 0x12000\n\t" \
    "s_mov_b32 s44, 0x420000\n\t" \
    "buffer_load_dwordx4 v240, %[wr], s44 offen offset:0 nt lds\n\t" \
    "buffer_load_dwordx4 v241, %[wr], s44 offen offset:1024 nt lds\n\t" \
    "buffer_load_dwordx4 v242, %[wr], s44 offen offset:2048 nt lds\n\t" \
    "ds_read_b64_tr_b16 v[96:97], v244 offset:0\n\t" \
    "ds_read_b64_tr_b16 v[98:99], v244 offset:1536\n\t" \
    "ds_read_b64_tr_b16 v[104:105], v244 offset:32\n\t" \
    "ds_read_b64_tr_b16 v[106:107], v244 offset:1568\n\t" \
    "ds_read_b64_tr_b16 v[112:113], v244 offset:64\n\t" \
    "ds_read_b64_tr_b16 v[114:115], v244 offset:1600\n\t" \
    "v_add_u32 v244, 0x1e000, v243\n\t" \
    "s_waitcnt vmcnt(12) lgkmcnt(0)\n\t" \
    "s_barrier \n\t" \
    "s_add_u32 m0, %[wm], 0x18000\n\t" \
    "s_mov_b32 s44, 0x480000\n\t" \
    "buffer_load_dwordx4 v240, %[wr], s44 offen offset:0 nt lds\n\t" \
    "buffer_load_dwordx4 v241, %[wr], s44 offen offset:1024 nt lds\n\t" \
    "buffer_load_dwordx4 v242, %[wr], s44 offen offset:2048 nt lds\n\t" \
    "ds_read_b64_tr_b16 v[100:101], v244 offset:0\n\t" \
    "ds_read_b64_tr_b16 v[102:103], v244 offset:1536\n\t" \
    "ds_read_b64_tr_b16 v[108:109], v244 offset:32\n\t" \
    "ds_read_b64_tr_b16 v[110:111], v244 offset:1568\n\t" \
    "ds_read_b64_tr_b16 v[116:117], v244 offset:64\n\t" \
    "ds_read_b64_tr_b16 v[118:119], v244 offset:1600\n\t" \
    "v_add_u32 v244, 0x0, v243\n\t" \
    "s_waitcnt vmcnt(12) lgkmcnt(0)\n\t" \
    "s_barrier \n\t" \
    "v_mov_b32 v246, 0\n\t" \
    "buffer_load_dword v246, v246, s[60:63], %[mo] offen sc1\n\t" \
    "s_add_u32 m0, %[wm], 0x1e000\n\t" \
    "s_mov_b32 s44, 0x4e0000\n\t" \
    "buffer_load_dwordx4 v240, %[wr], s44 offen offset:0 nt lds\n\t" \
    "buffer_load_dwordx4 v241, %[wr], s44 offen offset:1024 nt lds\n\t" \
    "buffer_load_dwordx4 v242, %[wr], s44 offen offset:2048 nt lds\n\t" \
    "ds_read_b64_tr_b16 v[200:201], v244 offset:0\n\t" \
    "ds_read_b64_tr_b16 v[202:203], v244 offset:1536\n\t" \
    "ds_read_b64_tr_b16 v[0:1], v244 offset:32\n\t" \
    "ds_read_b64_tr_b16 v[2:3], v244 offset:1568\n\t" \
    "ds_read_b64_tr_b16 v[8:9], v244 offset:64\n\t" \
    "ds_read_b64_tr_b16 v[10:11], v244 offset:1600\n\t" \
    "v_add_u32 v244, 0x6000, v243\n\t" \
    "s_waitcnt vmcnt(12) lgkmcnt(0)\n\t" \
    "s_barrier \n\t" \
    "s_add_u32 m0, %[wm], 0x0\n\t" \
    "s_mov_b32 s44, 0x540000\n\t" \
    "buffer_load_dwordx4 v240, %[wr], s44 offen offset:0 nt lds\n\t" \
    "buffer_load_dwordx4 v241, %[wr], s44 offen offset:1024 nt lds\n\t" \
    "buffer_load_dwordx4 v242, %[wr], s44 offen offset:2048 nt lds\n\t" \
    "ds_read_b64_tr_b16 v[204:205], v244 offset:0\n\t" \
    "ds_read_b64_tr_b16 v[206:207], v244 offset:1536\n\t" \
    "ds_read_b64_tr_b16 v[4:5], v244 offset:32\n\t" \
    "ds_read_b64_tr_b16 v[6:7], v244 offset:1568\n\t" \
    "ds_read_b64_tr_b16 v[12:13], v244 offset:64\n\t" \
    "ds_read_b64_tr_b16 v[14:15], v244 offset:1600\n\t" \
    "v_add_u32 v244, 0xc000, v243\n\t" \
    "s_waitcnt vmcnt(12) lgkmcnt(0)\n\t" \
    "s_barrier \n\t" \
    "s_add_u32 m0, %[wm], 0x6000\n\t" \
    "s_mov_b32 s44, 0x5a0000\n\t" \
    "buffer_load_dwordx4 v240, %[wr], s44 offen offset:0 nt lds\n\t" \
    "buffer_load_dwordx4 v241, %[wr], s44 offen offset:1024 nt lds\n\t" \
    "buffer_load_dwordx4 v242, %[wr], s44 offen offset:2048 nt lds\n\t" \
    "ds_read_b64_tr_b16 v[224:225], v244 offset:0\n\t" \
    "ds_read_b64_tr_b16 v[226:227], v244 offset:1536\n\t" \
    "ds_read_b64_tr_b16 v[120:121], v244 offset:32\n\t" \
    "ds_read_b64_tr_b16 v[122:123], v244 offset:1568\n\t" \
    "ds_read_b64_tr_b16 v[128:129], v244 offset:64\n\t" \
    "ds_read_b64_tr_b16 v[130:131], v244 offset:1600\n\t" \
    "v_add_u32 v244, 0x12000, v243\n\t" \
    "s_waitcnt vmcnt(12) lgkmcnt(0)\n\t" \
    "s_barrier \n\t" \
    "s_add_u32 m0, %[wm], 0xc000\n\t" \
    "s_mov_b32 s44, 0x600000\n\t" \
    "buffer_load_dwordx4 v240, %[wr], s44 offen offset:0 nt lds\n\t" \
    "buffer_load_dwordx4 v241, %[wr], s44 offen offset:1024 nt lds\n\t" \
    "buffer_load_dwordx4 v242, %[wr], s44 offen offset:2048 nt lds\n\t" \
    "ds_read_b64_tr_b16 v[228:229], v244 offset:0\n\t" \
    "ds_read_b64_tr_b16 v[230:231], v244 offset:1536\n\t" \
    "ds_read_b64_tr_b16 v[124:125], v244 offset:32\n\t" \
    "ds_read_b64_tr_b16 v[126:127], v244 offset:1568\n\t" \
    "ds_read_b64_tr_b16 v[132:133], v244 offset:64\n\t" \
    "ds_read_b64_tr_b16 v[134:135], v244 offset:1600\n\t" \
    "v_add_u32 v244, 0x18000, v243\n\t" \
    "s_waitcnt vmcnt(12) lgkmcnt(0)\n\t" \
    "s_barrier \n\t" \
    "s_add_u32 m0, %[wm], 0x12000\n\t" \
    "s_mov_b32 s44, 0x660000\n\t" \
    "buffer_load_dwordx4 v240, %[wr], s44 offen offset:0 nt lds\n\t" \
    "buffer_load_dwordx4 v241, %[wr], s44 offen offset:1024 nt lds\n\t" \
    "buffer_load_dwordx4 v242, %[wr], s44 offen offset:2048 nt lds\n\t" \
    "ds_read_b64_tr_b16 v[136:137], v244 offset:0\n\t" \
    "ds_read_b64_tr_b16 v[138:139], v244 offset:1536\n\t" \
    "ds_read_b64_tr_b16 v[144:145], v244 offset:32\n\t" \
    "ds_read_b64_tr_b16 v[146:147], v244 offset:1568\n\t" \
    "ds_read_b64_tr_b16 v[152:153], v244 offset:64\n\t" \
    "ds_read_b64_tr_b16 v[154:155], v244 offset:1600\n\t" \
    "v_add_u32 v244, 0x1e000, v243\n\t" \
    "s_waitcnt vmcnt(12) lgkmcnt(0)\n\t" \
    "s_barrier \n\t" \
    ".Ls21_%=:\n\t" \
    "v_readfirstlane_b32 s43, v246\n\t" \
    "s_nop 0\n\t" \
    "s_cmp_lg_u32 s43, -1\n\t" \
    "s_cbranch_scc1 .Ls22_%=\n\t" \
    "s_cmp_eq_u32 s41, 0\n\t" \
    "s_cbranch_scc1 .Ls22_%=\n\t" \
    "s_sub_u32 s41, s41, 1\n\t" \
    "s_sleep 4\n\t" \
    "v_mov_b32 v246, 0\n\t" \
    "buffer_load_dword v246, v246, s[60:63], %[mo] offen sc1\n\t" \
    "s_waitcnt vmcnt(0)\n\t" \
    "s_branch .Ls21_%=\n\t" \
    ".Ls22_%=:\n\t" \
    "v_mov_b32 v246, 0x8000\n\t" \
    "s_mov_b64 exec, 1\n\t" \
    "buffer_store_dword v246, v246, s[60:63], %[mo] offen sc1\n\t" \
    "s_mov_b64 exec, -1\n\t" \
    "s_and_b32 s44, s43, 0x5000\n\t" \
    "s_xor_b32 %[mb], %[mb], s44\n\t" \
    "s_xor_b32 %[fl], %[fl], s44\n\t" \
    "s_and_b32 s43, s43, 0x2000\n\t" \
    "s_xor_b32 s43, s43, %[lx]\n\t" \
    "s_add_u32 s60, s60, s43\n\t" \
    "s_addc_u32 s61, s61, 0\n\t" \
    "s_add_u32 m0, %[wm], 0x18000\n\t" \
    "s_mov_b32 s44, 0x6c0000\n\t" \
    "buffer_load_dwordx4 v240, %[wr], s44 offen offset:0 nt lds\n\t" \
    "buffer_load_dwordx4 v241, %[wr], s44 offen offset:1024 nt lds\n\t" \
    "buffer_load_dwordx4 v242, %[wr], s44 offen offset:2048 nt lds\n\t" \
    "ds_read_b64_tr_b16 v[140:141], v244 offset:0\n\t" \
    "ds_read_b64_tr_b16 v[142:143], v244 offset:1536\n\t" \
    "ds_read_b64_tr_b16 v[148:149], v244 offset:32\n\t" \
    "ds_read_b64_tr_b16 v[150:151], v244 offset:1568\n\t" \
    "ds_read_b64_tr_b16 v[156:157], v244 offset:64\n\t" \
    "ds_read_b64_tr_b16 v[158:159], v244 offset:1600\n\t" \
    "v_add_u32 v244, 0x0, v243\n\t" \
    "s_waitcnt vmcnt(12) lgkmcnt(0)\n\t" \
    "s_barrier \n\t" \
    "s_add_u32 m0, %[wm], 0x1e000\n\t" \
    "s_mov_b32 s44, 0x720000\n\t" \
    "buffer_load_dwordx4 v240, %[wr], s44 offen offset:0 nt lds\n\t" \
    "buffer_load_dwordx4 v241, %[wr], s44 offen offset:1024 nt lds\n\t" \
    "buffer_load_dwordx4 v242, %[wr], s44 offen offset:2048 nt lds\n\t" \
    "ds_read_b64_tr_b16 v[16:17], v244 offset:0\n\t" \
    "ds_read_b64_tr_b16 v[18:19], v244 offset:1536\n\t" \
    "ds_read_b64_tr_b16 v[24:25], v244 offset:32\n\t" \
    "ds_read_b64_tr_b16 v[26:27], v244 offset:1568\n\t" \
    "ds_read_b64_tr_b16 v[32:33], v244 offset:64\n\t" \
    "ds_read_b64_tr_b16 v[34:35], v244 offset:1600\n\t" \
    "v_add_u32 v244, 0x6000, v243\n\t" \
    "s_waitcnt vmcnt(12) lgkmcnt(0)\n\t" \
    "s_barrier \n\t" \
    "ds_read_b64_tr_b16 v[20:21], v244 offset:0\n\t" \
    "ds_read_b64_tr_b16 v[22:23], v244 offset:1536\n\t" \
    "ds_read_b64_tr_b16 v[28:29], v244 offset:32\n\t" \
    "ds_read_b64_tr_b16 v[30:31], v244 offset:1568\n\t" \
    "ds_read_b64_tr_b16 v[36:37], v244 offset:64\n\t" \
    "ds_read_b64_tr_b16 v[38:39], v244 offset:1600\n\t" \
    "v_add_u32 v244, 0xc000, v243\n\t" \
    "s_waitcnt vmcnt(9) lgkmcnt(0)\n\t" \
    "s_barrier \n\t" \
    "ds_read_b64_tr_b16 v[232:233], v244 offset:0\n\t" \
    "ds_read_b64_tr_b16 v[234:235], v244 offset:1536\n\t" \
    "ds_read_b64_tr_b16 v[160:161], v244 offset:32\n\t" \
    "ds_read_b64_tr_b16 v[162:163], v244 offset:1568\n\t" \
    "ds_read_b64_tr_b16 v[168:169], v244 offset:64\n\t" \
    "ds_read_b64_tr_b16 v[170:171], v244 offset:1600\n\t" \
    "v_add_u32 v244, 0x12000, v243\n\t" \
    "s_waitcnt vmcnt(6) lgkmcnt(0)\n\t" \
    "s_barrier \n\t" \
    "ds_read_b64_tr_b16 v[236:237], v244 offset:0\n\t" \
    "ds_read_b64_tr_b16 v[238:239], v244 offset:1536\n\t" \
    "ds_read_b64_tr_b16 v[164:165], v244 offset:32\n\t" \
    "ds_read_b64_tr_b16 v[166:167], v244 offset:1568\n\t" \
    "ds_read_b64_tr_b16 v[172:173], v244 offset:64\n\t" \
    "ds_read_b64_tr_b16 v[174:175], v244 offset:1600\n\t" \
    "v_add_u32 v244, 0x18000, v243\n\t" \
    "s_waitcnt vmcnt(3) lgkmcnt(0)\n\t" \
    "s_barrier \n\t" \
    "ds_read_b64_tr_b16 v[176:177], v244 offset:0\n\t" \
    "ds_read_b64_tr_b16 v[178:179], v244 offset:1536\n\t" \
    "ds_read_b64_tr_b16 v[184:185], v244 offset:32\n\t" \
    "ds_read_b64_tr_b16 v[186:187], v244 offset:1568\n\t" \
    "ds_read_b64_tr_b16 v[192:193], v244 offset:64\n\t" \
    "ds_read_b64_tr_b16 v[194:195], v244 offset:1600\n\t" \
    "v_add_u32 v244, 0x1e000, v243\n\t" \
    "s_waitcnt vmcnt(0) lgkmcnt(0)\n\t" \
    "s_barrier \n\t" \
    "ds_read_b64_tr_b16 v[180:181], v244 offset:0\n\t" \
    "ds_read_b64_tr_b16 v[182:183], v244 offset:1536\n\t" \
    "ds_read_b64_tr_b16 v[188:189], v244 offset:32\n\t" \
    "ds_read_b64_tr_b16 v[190:191], v244 offset:1568\n\t" \
    "ds_read_b64_tr_b16 v[196:197], v244 offset:64\n\t" \
    "ds_read_b64_tr_b16 v[198:199], v244 offset:1600\n\t" \
    "s_waitcnt lgkmcnt(0)\n\t" \
    "s_barrier \n\t" \
    "ds_write_b128 v254, v[200:203] offset:51200\n\t" \
    "ds_write_b128 v254, v[204:207] offset:52224\n\t" \
    "ds_write_b128 v254, v[208:211] offset:53248\n\t" \
    "ds_write_b128 v254, v[212:215] offset:54272\n\t" \
    "ds_write_b128 v254, v[216:219] offset:55296\n\t" \
    "ds_write_b128 v254, v[220:223] offset:56320\n\t" \
    "ds_write_b128 v254, v[224:227] offset:57344\n\t" \
    "ds_write_b128 v254, v[228:231] offset:58368\n\t" \
    "ds_write_b128 v254, v[232:235] offset:59392\n\t" \
    "ds_write_b128 v254, v[236:239] offset:60416\n\t" \
    "s_waitcnt vmcnt(0) lgkmcnt(0)\n\t" \
    "s_barrier \n\t" \
    "ds_read_b128 v[224:227], v253 offset:0\n\t" \
    "ds_read_b128 v[244:247], v254 offset:53248\n\t" \
    "ds_read_b128 v[228:231], v253 offset:20480\n\t" \
    "ds_read_b128 v[232:235], v253 offset:1024\n\t" \
    "ds_read_b128 v[236:239], v253 offset:21504\n\t" \
    "ds_read_b128 v[248:251], v254 offset:54272\n\t" \
    ".p2align 8\n\t" \
    ".Ls23_%=:\n\t" \
    "s_waitcnt lgkmcnt(5)\n\t" \
    "v_mfma_f32_16x16x32_bf16 v[204:207], v[40:43], v[224:227], 0\n\t" \
    "ds_read_b128 v[240:243], v253 offset:2048\n\t" \
    "v_mfma_f32_16x16x32_bf16 v[208:211], v[48:51], v[224:227], 0\n\t" \
    "s_waitcnt lgkmcnt(5)\n\t" \
    "v_mfma_f32_16x16x32_bf16 v[200:203], v[244:247], v[224:227], 0\n\t" \
    "s_waitcnt lgkmcnt(4)\n\t" \
    "v_mfma_f32_16x16x32_bf16 v[216:219], v[40:43], v[228:231], 0\n\t" \
    "ds_read_b128 v[224:227], v253 offset:22528\n\t" \
    "v_mfma_f32_16x16x32_bf16 v[220:223], v[48:51], v[228:231], 0\n\t" \
    "v_mfma_f32_16x16x32_bf16 v[212:215], v[244:247], v[228:231], 0\n\t" \
    "ds_read_b128 v[244:247], v254 offset:55296\n\t" \
    "s_waitcnt lgkmcnt(5)\n\t" \
    "v_mfma_f32_16x16x32_bf16 v[204:207], v[44:47], v[232:235], v[204:207]\n\t" \
    "ds_read_b128 v[228:231], v253 offset:3072\n\t" \
    "v_mfma_f32_16x16x32_bf16 v[208:211], v[52:55], v[232:235], v[208:211]\n\t" \
    "s_waitcnt lgkmcnt(4)\n\t" \
    "v_mfma_f32_16x16x32_bf16 v[200:203], v[248:251], v[232:235], v[200:203]\n\t" \
    "v_mfma_f32_16x16x32_bf16 v[216:219], v[44:47], v[236:239], v[216:219]\n\t" \
    "ds_read_b128 v[232:235], v253 offset:23552\n\t" \
    "v_mfma_f32_16x16x32_bf16 v[220:223], v[52:55], v[236:239], v[220:223]\n\t" \
    "v_mfma_f32_16x16x32_bf16 v[212:215], v[248:251], v[236:239], v[212:215]\n\t" \
    "ds_read_b128 v[248:251], v254 offset:56320\n\t" \
    "s_waitcnt lgkmcnt(5)\n\t" \
    "v_mfma_f32_16x16x32_bf16 v[204:207], v[64:67], v[240:243], v[204:207]\n\t" \
    "ds_read_b128 v[236:239], v253 offset:4096\n\t" \
    "v_mfma_f32_16x16x32_bf16 v[208:211], v[72:75], v[240:243], v[208:211]\n\t" \
    "v_mfma_f32_16x16x32_bf16 v[200:203], v[56:59], v[240:243], v[200:203]\n\t" \
    "s_waitcnt lgkmcnt(5)\n\t" \
    "v_mfma_f32_16x16x32_bf16 v[216:219], v[64:67], v[224:227], v[216:219]\n\t" \
    "ds_read_b128 v[240:243], v253 offset:24576\n\t" \
    "v_mfma_f32_16x16x32_bf16 v[220:223], v[72:75], v[224:227], v[220:223]\n\t" \
    "v_mfma_f32_16x16x32_bf16 v[212:215], v[56:59], v[224:227], v[212:215]\n\t" \
    "s_waitcnt lgkmcnt(4)\n\t" \
    "v_mfma_f32_16x16x32_bf16 v[204:207], v[68:71], v[228:231], v[204:207]\n\t" \
    "ds_read_b128 v[224:227], v253 offset:5120\n\t" \
    "v_mfma_f32_16x16x32_bf16 v[208:211], v[76:79], v[228:231], v[208:211]\n\t" \
    "v_mfma_f32_16x16x32_bf16 v[200:203], v[60:63], v[228:231], v[200:203]\n\t" \
    "s_waitcnt lgkmcnt(4)\n\t" \
    "v_mfma_f32_16x16x32_bf16 v[216:219], v[68:71], v[232:235], v[216:219]\n\t" \
    "ds_read_b128 v[228:231], v253 offset:25600\n\t" \
    "v_mfma_f32_16x16x32_bf16 v[220:223], v[76:79], v[232:235], v[220:223]\n\t" \
    "v_mfma_f32_16x16x32_bf16 v[212:215], v[60:63], v[232:235], v[212:215]\n\t" \
    "s_waitcnt lgkmcnt(3)\n\t" \
    "v_mfma_f32_16x16x32_bf16 v[204:207], v[80:83], v[236:239], v[204:207]\n\t" \
    "ds_read_b128 v[232:235], v253 offset:6144\n\t" \
    "v_mfma_f32_16x16x32_bf16 v[208:211], v[88:91], v[236:239], v[208:211]\n\t" \
    "v_mfma_f32_16x16x32_bf16 v[200:203], v[244:247], v[236:239], v[200:203]\n\t" \
    "s_waitcnt lgkmcnt(3)\n\t" \
    "v_mfma_f32_16x16x32_bf16 v[216:219], v[80:83], v[240:243], v[216:219]\n\t" \
    "ds_read_b128 v[236:239], v253 offset:26624\n\t" \
    "v_mfma_f32_16x16x32_bf16 v[220:223], v[88:91], v[240:243], v[220:223]\n\t" \
    "v_mfma_f32_16x16x32_bf16 v[212:215], v[244:247], v[240:243], v[212:215]\n\t" \
    "ds_read_b128 v[244:247], v254 offset:51200\n\t" \
    "s_waitcnt lgkmcnt(4)\n\t" \
    "v_mfma_f32_16x16x32_bf16 v[204:207], v[84:87], v[224:227], v[204:207]\n\t" \
    "ds_read_b128 v[240:243], v253 offset:7168\n\t" \
    "v_mfma_f32_16x16x32_bf16 v[208:211], v[92:95], v[224:227], v[208:211]\n\t" \
    "v_mfma_f32_16x16x32_bf16 v[200:203], v[248:251], v[224:227], v[200:203]\n\t" \
    "s_waitcnt lgkmcnt(4)\n\t" \
    "v_mfma_f32_16x16x32_bf16 v[216:219], v[84:87], v[228:231], v[216:219]\n\t" \
    "ds_read_b128 v[224:227], v253 offset:27648\n\t" \
    "v_mfma_f32_16x16x32_bf16 v[220:223], v[92:95], v[228:231], v[220:223]\n\t" \
    "v_mfma_f32_16x16x32_bf16 v[212:215], v[248:251], v[228:231], v[212:215]\n\t" \
    "ds_read_b128 v[248:251], v254 offset:52224\n\t" \
    "s_waitcnt lgkmcnt(5)\n\t" \
    "v_mfma_f32_16x16x32_bf16 v[204:207], v[104:107], v[232:235], v[204:207]\n\t" \
    "ds_read_b128 v[228:231], v253 offset:8192\n\t" \
    "v_mfma_f32_16x16x32_bf16 v[208:211], v[112:115], v[232:235], v[208:211]\n\t" \
    "v_mfma_f32_16x16x32_bf16 v[200:203], v[96:99], v[232:235], v[200:203]\n\t" \
    "s_waitcnt lgkmcnt(5)\n\t" \
    "v_mfma_f32_16x16x32_bf16 v[216:219], v[104:107], v[236:239], v[216:219]\n\t" \
    "ds_read_b128 v[232:235], v253 offset:28672\n\t" \
    "v_mfma_f32_16x16x32_bf16 v[220:223], v[112:115], v[236:239], v[220:223]\n\t" \
    "v_mfma_f32_16x16x32_bf16 v[212:215], v[96:99], v[236:239], v[212:215]\n\t" \
    "s_waitcnt lgkmcnt(4)\n\t" \
    "v_mfma_f32_16x16x32_bf16 v[204:207], v[108:111], v[240:243], v[204:207]\n\t" \
    "ds_read_b128 v[236:239], v253 offset:9216\n\t" \
    "v_mfma_f32_16x16x32_bf16 v[208:211], v[116:119], v[240:243], v[208:211]\n\t" \
    "v_mfma_f32_16x16x32_bf16 v[200:203], v[100:103], v[240:243], v[200:203]\n\t" \
    "s_waitcnt lgkmcnt(4)\n\t" \
    "v_mfma_f32_16x16x32_bf16 v[216:219], v[108:111], v[224:227], v[216:219]\n\t" \
    "ds_read_b128 v[240:243], v253 offset:29696\n\t" \
    "v_mfma_f32_16x16x32_bf16 v[220:223], v[116:119], v[224:227], v[220:223]\n\t" \
    "v_mfma_f32_16x16x32_bf16 v[212:215], v[100:103], v[224:227], v[212:215]\n\t" \
    "s_waitcnt lgkmcnt(3)\n\t" \
    "v_mfma_f32_16x16x32_bf16 v[204:207], v[0:3], v[228:231], v[204:207]\n\t" \
    "ds_read_b128 v[224:227], v253 offset:10240\n\t" \
    "v_mfma_f32_16x16x32_bf16 v[208:211], v[8:11], v[228:231], v[208:211]\n\t" \
    "v_mfma_f32_16x16x32_bf16 v[200:203], v[244:247], v[228:231], v[200:203]\n\t" \
    "s_waitcnt lgkmcnt(3)\n\t" \
    "v_mfma_f32_16x16x32_bf16 v[216:219], v[0:3], v[232:235], v[216:219]\n\t" \
    "ds_read_b128 v[228:231], v253 offset:30720\n\t" \
    "v_mfma_f32_16x16x32_bf16 v[220:223], v[8:11], v[232:235], v[220:223]\n\t" \
    "v_mfma_f32_16x16x32_bf16 v[212:215], v[244:247], v[232:235], v[212:215]\n\t" \
    "ds_read_b128 v[244:247], v254 offset:57344\n\t" \
    "s_waitcnt lgkmcnt(4)\n\t" \
    "v_mfma_f32_16x16x32_bf16 v[204:207], v[4:7], v[236:239], v[204:207]\n\t" \
    "ds_read_b128 v[232:235], v253 offset:11264\n\t" \
    "v_mfma_f32_16x16x32_bf16 v[208:211], v[12:15], v[236:239], v[208:211]\n\t" \
    "v_mfma_f32_16x16x32_bf16 v[200:203], v[248:251], v[236:239], v[200:203]\n\t" \
    "s_waitcnt lgkmcnt(4)\n\t" \
    "v_mfma_f32_16x16x32_bf16 v[216:219], v[4:7], v[240:243], v[216:219]\n\t" \
    "ds_read_b128 v[236:239], v253 offset:31744\n\t" \
    "v_mfma_f32_16x16x32_bf16 v[220:223], v[12:15], v[240:243], v[220:223]\n\t" \
    "v_mfma_f32_16x16x32_bf16 v[212:215], v[248:251], v[240:243], v[212:215]\n\t" \
    "ds_read_b128 v[248:251], v254 offset:58368\n\t" \
    "s_setprio 2\n\t" \
    "s_waitcnt lgkmcnt(0)\n\t" \
    "s_barrier \n\t" \
    "v_mfma_f32_16x16x32_bf16 v[204:207], v[120:123], v[224:227], v[204:207]\n\t" \
    "ds_read_b128 v[240:243], v253 offset:12288\n\t" \
    "v_mfma_f32_16x16x32_bf16 v[208:211], v[128:131], v[224:227], v[208:211]\n\t" \
    "v_mfma_f32_16x16x32_bf16 v[200:203], v[244:247], v[224:227], v[200:203]\n\t" \
    "v_mfma_f32_16x16x32_bf16 v[216:219], v[120:123], v[228:231], v[216:219]\n\t" \
    "ds_read_b128 v[224:227], v253 offset:32768\n\t" \
    "s_and_b32 s44, s40, 3\n\t" \
    "v_mfma_f32_16x16x32_bf16 v[220:223], v[128:131], v[228:231], v[220:223]\n\t" \
    "s_lshl_b32 s44, s44, 22\n\t" \
    "v_mfma_f32_16x16x32_bf16 v[212:215], v[244:247], v[228:231], v[212:215]\n\t" \
    "ds_read_b128 v[244:247], v254 offset:59392\n\t" \
    "s_add_u32 s44, s44, %[mb]\n\t" \
    "v_mfma_f32_16x16x32_bf16 v[204:207], v[124:127], v[232:235], v[204:207]\n\t" \
    "ds_read_b128 v[228:231], v253 offset:13312\n\t" \
    "s_add_u32 s46, s44, 0x8000\n\t" \
    "v_mfma_f32_16x16x32_bf16 v[208:211], v[132:135], v[232:235], v[208:211]\n\t" \
    "s_add_u32 s47, s44, 0x8c00\n\t" \
    "v_mfma_f32_16x16x32_bf16 v[200:203], v[248:251], v[232:235], v[200:203]\n\t" \
    "v_mfma_f32_16x16x32_bf16 v[216:219], v[124:127], v[236:239], v[216:219]\n\t" \
    "ds_read_b128 v[232:235], v253 offset:33792\n\t" \
    "v_mfma_f32_16x16x32_bf16 v[220:223], v[132:135], v[236:239], v[220:223]\n\t" \
    "v_mfma_f32_16x16x32_bf16 v[212:215], v[248:251], v[236:239], v[212:215]\n\t" \
    "ds_read_b128 v[248:251], v254 offset:60416\n\t" \
    "s_waitcnt lgkmcnt(5)\n\t" \
    "v_mfma_f32_16x16x32_bf16 v[204:207], v[144:147], v[240:243], v[204:207]\n\t" \
    "ds_read_b128 v[236:239], v253 offset:16384\n\t" \
    "v_mfma_f32_16x16x32_bf16 v[208:211], v[152:155], v[240:243], v[208:211]\n\t" \
    "v_mfma_f32_16x16x32_bf16 v[200:203], v[136:139], v[240:243], v[200:203]\n\t" \
    "s_waitcnt lgkmcnt(5)\n\t" \
    "v_mfma_f32_16x16x32_bf16 v[216:219], v[144:147], v[224:227], v[216:219]\n\t" \
    "ds_read_b128 v[240:243], v253 offset:36864\n\t" \
    "v_mfma_f32_16x16x32_bf16 v[220:223], v[152:155], v[224:227], v[220:223]\n\t" \
    "v_mfma_f32_16x16x32_bf16 v[212:215], v[136:139], v[224:227], v[212:215]\n\t" \
    "s_waitcnt lgkmcnt(4)\n\t" \
    "v_mfma_f32_16x16x32_bf16 v[204:207], v[148:151], v[228:231], v[204:207]\n\t" \
    "ds_read_b128 v[224:227], v253 offset:17408\n\t" \
    "v_mfma_f32_16x16x32_bf16 v[208:211], v[156:159], v[228:231], v[208:211]\n\t" \
    "v_mfma_f32_16x16x32_bf16 v[200:203], v[140:143], v[228:231], v[200:203]\n\t" \
    "s_waitcnt lgkmcnt(4)\n\t" \
    "v_mfma_f32_16x16x32_bf16 v[216:219], v[148:151], v[232:235], v[216:219]\n\t" \
    "ds_read_b128 v[228:231], v253 offset:37888\n\t" \
    "v_mfma_f32_16x16x32_bf16 v[220:223], v[156:159], v[232:235], v[220:223]\n\t" \
    "v_mfma_f32_16x16x32_bf16 v[212:215], v[140:143], v[232:235], v[212:215]\n\t" \
    "s_waitcnt lgkmcnt(3)\n\t" \
    "v_mfma_f32_16x16x32_bf16 v[204:207], v[160:163], v[236:239], v[204:207]\n\t" \
    "ds_read_b128 v[232:235], v253 offset:14336\n\t" \
    "v_mfma_f32_16x16x32_bf16 v[208:211], v[168:171], v[236:239], v[208:211]\n\t" \
    "v_mfma_f32_16x16x32_bf16 v[200:203], v[244:247], v[236:239], v[200:203]\n\t" \
    "s_waitcnt lgkmcnt(3)\n\t" \
    "v_mfma_f32_16x16x32_bf16 v[216:219], v[160:163], v[240:243], v[216:219]\n\t" \
    "ds_read_b128 v[236:239], v253 offset:15360\n\t" \
    "v_mfma_f32_16x16x32_bf16 v[220:223], v[168:171], v[240:243], v[220:223]\n\t" \
    "v_mfma_f32_16x16x32_bf16 v[212:215], v[244:247], v[240:243], v[212:215]\n\t" \
    "s_waitcnt lgkmcnt(3)\n\t" \
    "v_mfma_f32_16x16x32_bf16 v[204:207], v[164:167], v[224:227], v[204:207]\n\t" \
    "ds_read_b128 v[240:243], v253 offset:18432\n\t" \
    "v_mfma_f32_16x16x32_bf16 v[208:211], v[172:175], v[224:227], v[208:211]\n\t" \
    "v_mfma_f32_16x16x32_bf16 v[200:203], v[248:251], v[224:227], v[200:203]\n\t" \
    "s_waitcnt lgkmcnt(3)\n\t" \
    "v_mfma_f32_16x16x32_bf16 v[216:219], v[164:167], v[228:231], v[216:219]\n\t" \
    "ds_read_b128 v[224:227], v253 offset:19456\n\t" \
    "v_mfma_f32_16x16x32_bf16 v[220:223], v[172:175], v[228:231], v[220:223]\n\t" \
    "v_mfma_f32_16x16x32_bf16 v[212:215], v[248:251], v[228:231], v[212:215]\n\t" \
    "s_waitcnt lgkmcnt(3)\n\t" \
    "v_mfma_f32_16x16x32_bf16 v[204:207], v[24:27], v[232:235], v[204:207]\n\t" \
    "ds_read_b128 v[228:231], v253 offset:34816\n\t" \
    "v_mov_b64 v[244:245], 0\n\t" \
    "v_mfma_f32_16x16x32_bf16 v[208:211], v[32:35], v[232:235], v[208:211]\n\t" \
    "v_mov_b64 v[246:247], 0\n\t" \
    "v_mfma_f32_16x16x32_bf16 v[200:203], v[16:19], v[232:235], v[200:203]\n\t" \
    "v_mov_b64 v[248:249], 0\n\t" \
    "s_waitcnt lgkmcnt(3)\n\t" \
    "v_mfma_f32_16x16x32_bf16 v[204:207], v[28:31], v[236:239], v[204:207]\n\t" \
    "ds_read_b128 v[232:235], v253 offset:35840\n\t" \
    "v_mov_b64 v[250:251], 0\n\t" \
    "v_mfma_f32_16x16x32_bf16 v[208:211], v[36:39], v[236:239], v[208:211]\n\t" \
    "v_mfma_f32_16x16x32_bf16 v[200:203], v[20:23], v[236:239], v[200:203]\n\t" \
    "s_waitcnt lgkmcnt(3)\n\t" \
    "v_mfma_f32_16x16x32_bf16 v[204:207], v[184:187], v[240:243], v[204:207]\n\t" \
    "ds_read_b128 v[236:239], v253 offset:38912\n\t" \
    "v_mfma_f32_16x16x32_bf16 v[208:211], v[192:195], v[240:243], v[208:211]\n\t" \
    "v_mfma_f32_16x16x32_bf16 v[200:203], v[176:179], v[240:243], v[200:203]\n\t" \
    "s_waitcnt lgkmcnt(3)\n\t" \
    "v_mfma_f32_16x16x32_bf16 v[204:207], v[188:191], v[224:227], v[204:207]\n\t" \
    "ds_read_b128 v[240:243], v253 offset:39936\n\t" \
    "v_mfma_f32_16x16x32_bf16 v[208:211], v[196:199], v[224:227], v[208:211]\n\t" \
    "v_mfma_f32_16x16x32_bf16 v[200:203], v[180:183], v[224:227], v[200:203]\n\t" \
    "s_waitcnt lgkmcnt(3)\n\t" \
    "v_mfma_f32_16x16x32_bf16 v[216:219], v[24:27], v[228:231], v[216:219]\n\t" \
    "s_waitcnt lgkmcnt(2)\n\t" \
    "v_mfma_f32_16x16x32_bf16 v[216:219], v[28:31], v[232:235], v[216:219]\n\t" \
    "v_cvt_pk_bf16_f32 v204, v204, v208\n\t" \
    "v_cvt_pk_bf16_f32 v205, v205, v209\n\t" \
    "s_waitcnt lgkmcnt(1)\n\t" \
    "v_mfma_f32_16x16x32_bf16 v[216:219], v[184:187], v[236:239], v[216:219]\n\t" \
    "v_cvt_pk_bf16_f32 v206, v206, v210\n\t" \
    "v_cvt_pk_bf16_f32 v207, v207, v211\n\t" \
    "s_waitcnt lgkmcnt(0)\n\t" \
    "v_mfma_f32_16x16x32_bf16 v[216:219], v[188:191], v[240:243], v[216:219]\n\t" \
    "ds_write_b128 v254, v[204:207] offset:10240\n\t" \
    "v_mfma_f32_16x16x32_bf16 v[220:223], v[32:35], v[228:231], v[220:223]\n\t" \
    "v_mfma_f32_16x16x32_bf16 v[220:223], v[36:39], v[232:235], v[220:223]\n\t" \
    "v_cvt_pk_bf16_f32 v200, v200, v201\n\t" \
    "v_mfma_f32_16x16x32_bf16 v[220:223], v[192:195], v[236:239], v[220:223]\n\t" \
    "v_cvt_pk_bf16_f32 v201, v202, v203\n\t" \
    "ds_write_b64 v254, v[200:201] offset:6152\n\t" \
    "v_mfma_f32_16x16x32_bf16 v[220:223], v[196:199], v[240:243], v[220:223]\n\t" \
    "v_mfma_f32_16x16x32_bf16 v[212:215], v[16:19], v[228:231], v[212:215]\n\t" \
    "v_mfma_f32_16x16x32_bf16 v[212:215], v[20:23], v[232:235], v[212:215]\n\t" \
    "ds_read_b128 v[200:203], v254 offset:51200\n\t" \
    "ds_read_b128 v[204:207], v254 offset:52224\n\t" \
    "v_mfma_f32_16x16x32_bf16 v[212:215], v[176:179], v[236:239], v[212:215]\n\t" \
    "v_mov_b64 v[208:209], 0\n\t" \
    "v_mov_b64 v[210:211], 0\n\t" \
    "v_mfma_f32_16x16x32_bf16 v[212:215], v[180:183], v[240:243], v[212:215]\n\t" \
    "v_cvt_pk_bf16_f32 v216, v216, v220\n\t" \
    "v_cvt_pk_bf16_f32 v217, v217, v221\n\t" \
    "s_setprio 0\n\t" \
    "v_cvt_pk_bf16_f32 v218, v218, v222\n\t" \
    "v_cvt_pk_bf16_f32 v219, v219, v223\n\t" \
    "ds_write_b128 v254, v[216:219] offset:16384\n\t" \
    "ds_read_b128 v[216:219], v254 offset:53248\n\t" \
    "ds_read_b128 v[220:223], v254 offset:54272\n\t" \
    "s_nop 0\n\t" \
    "v_cvt_pk_bf16_f32 v212, v212, v213\n\t" \
    "v_cvt_pk_bf16_f32 v213, v214, v215\n\t" \
    "ds_write_b64 v254, v[212:213] offset:12296\n\t" \
    "s_sleep 3\n\t" \
    "s_mov_b32 m0, 0xb000\n\t" \
    "s_nop 0\n\t" \
    "buffer_load_dwordx4 v253, s[60:63], s46 offen sc1 lds\n\t" \
    "s_mov_b32 m0, 0xbc00\n\t" \
    "s_nop 0\n\t" \
    "buffer_load_dwordx4 v253, s[60:63], s47 offen sc1 lds\n\t" \
    "s_waitcnt lgkmcnt(0)\n\t" \
    "v_bfe_i32 v212, v253, 5, 1\n\t" \
    "v_and_b32 v212, 0xaaaa, v212\n\t" \
    "v_or_b32 v212, 0x4444, v212\n\t" \
    "v_and_b32 v213, 0xffff, v255\n\t" \
    "v_lshrrev_b32 v214, 16, v255\n\t" \
    ".Ls24_%=:\n\t" \
    "s_waitcnt vmcnt(0)\n\t" \
    "ds_read_b128 v[224:227], v253 offset:45056\n\t" \
    "ds_read_b128 v[228:231], v253 offset:48128\n\t" \
    "s_waitcnt lgkmcnt(1)\n\t" \
    "v_max_u32 v215, v224, v225\n\t" \
    "v_max3_u32 v215, v226, v227, v215\n\t" \
    "s_waitcnt lgkmcnt(0)\n\t" \
    "v_max3_u32 v215, v228, v229, v215\n\t" \
    "v_max3_u32 v215, v230, v231, v215\n\t" \
    "v_cmp_eq_u32 vcc, -1, v215\n\t" \
    "s_cbranch_vccz .Ls25_%=\n\t" \
    "v_max_u32 v215, v224, v225\n\t" \
    "v_max3_u32 v215, v226, v227, v215\n\t" \
    "v_cmp_eq_u32 vcc, -1, v215\n\t" \
    "s_cbranch_vccz .Ls26_%=\n\t" \
    "s_mov_b32 m0, 0xb000\n\t" \
    "s_nop 0\n\t" \
    "buffer_load_dwordx4 v253, s[60:63], s46 offen sc1 lds\n\t" \
    ".Ls26_%=:\n\t" \
    "v_max_u32 v215, v228, v229\n\t" \
    "v_max3_u32 v215, v230, v231, v215\n\t" \
    "v_cmp_eq_u32 vcc, -1, v215\n\t" \
    "s_cbranch_vccz .Ls27_%=\n\t" \
    "s_mov_b32 m0, 0xbc00\n\t" \
    "s_nop 0\n\t" \
    "buffer_load_dwordx4 v253, s[60:63], s47 offen sc1 lds\n\t" \
    ".Ls27_%=:\n\t" \
    "s_cmp_eq_u32 s41, 0\n\t" \
    "s_cbranch_scc1 .Ls25_%=\n\t" \
    "s_sub_u32 s41, s41, 1\n\t" \
    "s_branch .Ls24_%=\n\t" \
    ".Ls25_%=:\n\t" \
    "s_setprio 3\n\t" \
    "s_waitcnt lgkmcnt(0)\n\t" \
    "s_barrier \n\t" \
    "ds_read_b128 v[232:235], v253 offset:40960\n\t" \
    "ds_read_b128 v[236:239], v253 offset:41984\n\t" \
    "ds_read_b128 v[240:243], v253 offset:43008\n\t" \
    "v_smfmac_f32_16x16x64_bf16 v[244:247], v[224:227], v[0:7], v212\n\t" \
    "v_smfmac_f32_16x16x64_bf16 v[248:251], v[224:227], v[8:15], v212\n\t" \
    "v_smfmac_f32_16x16x64_bf16 v[208:211], v[224:227], v[200:207], v212\n\t" \
    "ds_read_b128 v[200:203], v254 offset:55296\n\t" \
    "ds_read_b128 v[204:207], v254 offset:56320\n\t" \
    "ds_read_b128 v[224:227], v253 offset:44032\n\t" \
    "v_smfmac_f32_16x16x64_bf16 v[208:211], v[228:231], v[16:23], v212\n\t" \
    "v_smfmac_f32_16x16x64_bf16 v[244:247], v[228:231], v[24:31], v212\n\t" \
    "v_smfmac_f32_16x16x64_bf16 v[248:251], v[228:231], v[32:39], v212\n\t" \
    "ds_read_b128 v[228:231], v253 offset:46080\n\t" \
    "s_waitcnt lgkmcnt(6)\n\t" \
    "v_smfmac_f32_16x16x64_bf16 v[208:211], v[232:235], v[216:223], v212\n\t" \
    "ds_read_b128 v[216:219], v254 offset:57344\n\t" \
    "ds_read_b128 v[220:223], v254 offset:58368\n\t" \
    "v_smfmac_f32_16x16x64_bf16 v[244:247], v[232:235], v[40:47], v212\n\t" \
    "v_smfmac_f32_16x16x64_bf16 v[248:251], v[232:235], v[48:55], v212\n\t" \
    "ds_read_b128 v[232:235], v253 offset:47104\n\t" \
    "s_waitcnt lgkmcnt(8)\n\t" \
    "v_smfmac_f32_16x16x64_bf16 v[208:211], v[236:239], v[56:63], v212\n\t" \
    "v_smfmac_f32_16x16x64_bf16 v[244:247], v[236:239], v[64:71], v212\n\t" \
    "v_smfmac_f32_16x16x64_bf16 v[248:251], v[236:239], v[72:79], v212\n\t" \
    "ds_read_b128 v[236:239], v253 offset:49152\n\t" \
    "s_waitcnt lgkmcnt(6)\n\t" \
    "v_smfmac_f32_16x16x64_bf16 v[208:211], v[240:243], v[200:207], v212\n\t" \
    "ds_read_b128 v[200:203], v254 offset:59392\n\t" \
    "ds_read_b128 v[204:207], v254 offset:60416\n\t" \
    "v_smfmac_f32_16x16x64_bf16 v[244:247], v[240:243], v[80:87], v212\n\t" \
    "v_smfmac_f32_16x16x64_bf16 v[248:251], v[240:243], v[88:95], v212\n\t" \
    "ds_read_b128 v[240:243], v253 offset:50176\n\t" \
    "s_waitcnt lgkmcnt(8)\n\t" \
    "v_smfmac_f32_16x16x64_bf16 v[208:211], v[224:227], v[96:103], v212\n\t" \
    "v_smfmac_f32_16x16x64_bf16 v[244:247], v[224:227], v[104:111], v212\n\t" \
    "v_smfmac_f32_16x16x64_bf16 v[248:251], v[224:227], v[112:119], v212\n\t" \
    "s_waitcnt lgkmcnt(5)\n\t" \
    "v_smfmac_f32_16x16x64_bf16 v[208:211], v[228:231], v[216:223], v212\n\t" \
    "s_waitcnt lgkmcnt(4)\n\t" \
    "v_smfmac_f32_16x16x64_bf16 v[208:211], v[232:235], v[136:143], v212\n\t" \
    "s_waitcnt lgkmcnt(1)\n\t" \
    "v_smfmac_f32_16x16x64_bf16 v[208:211], v[236:239], v[200:207], v212\n\t" \
    "s_waitcnt lgkmcnt(0)\n\t" \
    "v_smfmac_f32_16x16x64_bf16 v[208:211], v[240:243], v[176:183], v212\n\t" \
    "v_smfmac_f32_16x16x64_bf16 v[244:247], v[228:231], v[120:127], v212\n\t" \
    "v_smfmac_f32_16x16x64_bf16 v[244:247], v[232:235], v[144:151], v212\n\t" \
    "v_smfmac_f32_16x16x64_bf16 v[244:247], v[236:239], v[160:167], v212\n\t" \
    "v_add_f32 v208, v208, v210\n\t" \
    "v_add_f32 v209, v209, v211\n\t" \
    "v_cvt_pk_bf16_f32 v208, v208, v209\n\t" \
    "v_smfmac_f32_16x16x64_bf16 v[244:247], v[240:243], v[184:191], v212\n\t" \
    "ds_write_b16 v213, v208 offset:0\n\t" \
    "ds_write_b16_d16_hi v213, v208 offset:16\n\t" \
    "v_smfmac_f32_16x16x64_bf16 v[248:251], v[228:231], v[128:135], v212\n\t" \
    "v_smfmac_f32_16x16x64_bf16 v[248:251], v[232:235], v[152:159], v212\n\t" \
    "v_smfmac_f32_16x16x64_bf16 v[248:251], v[236:239], v[168:175], v212\n\t" \
    "v_add_f32 v244, v244, v246\n\t" \
    "v_add_f32 v245, v245, v247\n\t" \
    "v_smfmac_f32_16x16x64_bf16 v[248:251], v[240:243], v[192:199], v212\n\t" \
    "s_nop 7\n\t" \
    "v_add_f32 v248, v248, v250\n\t" \
    "v_add_f32 v249, v249, v251\n\t" \
    "v_cvt_pk_bf16_f32 v244, v244, v248\n\t" \
    "v_cvt_pk_bf16_f32 v245, v245, v249\n\t" \
    "ds_write2_b32 v214, v244, v245 offset1:4\n\t" \
    "s_setprio 0\n\t" \
    "s_sleep 1\n\t" \
    "ds_read_b128 v[200:203], v254 offset:12288\n\t" \
    "ds_read_b128 v[224:227], v254 offset:24576\n\t" \
    "ds_read_b128 v[204:207], v254 offset:13312\n\t" \
    "ds_read_b128 v[228:231], v254 offset:25600\n\t" \
    "ds_read_b128 v[208:211], v254 offset:14336\n\t" \
    "ds_read_b128 v[232:235], v254 offset:26624\n\t" \
    "ds_read_b128 v[212:215], v254 offset:15360\n\t" \
    "ds_read_b128 v[236:239], v254 offset:27648\n\t" \
    "ds_read_b128 v[216:219], v254 offset:16384\n\t" \
    "ds_read_b128 v[220:223], v254 offset:17408\n\t" \
    "s_and_b32 s44, s40, 3\n\t" \
    "s_lshl_b32 s44, s44, 17\n\t" \
    "s_add_u32 s50, s44, %[pb]\n\t" \
    "s_add_u32 s51, s44, %[pm]\n\t" \
    "s_mov_b32 s52, 0xf0000\n\t" \
    "s_mov_b32 s53, 0xfff00ff\n\t" \
    "s_mov_b32 s54, 0x1f0001\n\t" \
    "s_mov_b32 s55, 0x1fff01ff\n\t" \
    "s_mov_b32 s56, 0x3f0003\n\t" \
    "s_mov_b32 s57, 0x3fff03ff\n\t" \
    "s_mov_b32 s58, 0x7f0007\n\t" \
    "s_mov_b32 s59, 0x7fff07ff\n\t" \
    "s_waitcnt lgkmcnt(8)\n\t" \
    "v_mfma_f32_16x16x32_bf16 v[240:243], v[224:227], v[200:203], 0\n\t" \
    "ds_read_b128 v[224:227], v254 offset:28672\n\t" \
    "s_waitcnt lgkmcnt(7)\n\t" \
    "v_mfma_f32_16x16x32_bf16 v[240:243], v[228:231], v[204:207], v[240:243]\n\t" \
    "ds_read_b128 v[228:231], v254 offset:29696\n\t" \
    "s_waitcnt lgkmcnt(6)\n\t" \
    "v_mfma_f32_16x16x32_bf16 v[240:243], v[232:235], v[208:211], v[240:243]\n\t" \
    "ds_read_b128 v[232:235], v254 offset:18432\n\t" \
    "s_waitcnt lgkmcnt(5)\n\t" \
    "v_mfma_f32_16x16x32_bf16 v[240:243], v[236:239], v[212:215], v[240:243]\n\t" \
    "ds_read_b128 v[236:239], v254 offset:19456\n\t" \
    "s_waitcnt lgkmcnt(3)\n\t" \
    "v_mfma_f32_16x16x32_bf16 v[240:243], v[224:227], v[216:219], v[240:243]\n\t" \
    "ds_read_b128 v[224:227], v254 offset:20480\n\t" \
    "s_waitcnt lgkmcnt(3)\n\t" \
    "v_mfma_f32_16x16x32_bf16 v[240:243], v[228:231], v[220:223], v[240:243]\n\t" \
    "ds_read_b128 v[228:231], v254 offset:21504\n\t" \
    "s_waitcnt lgkmcnt(3)\n\t" \
    "v_mfma_f32_16x16x32_bf16 v[244:247], v[232:235], v[200:203], 0\n\t" \
    "ds_read_b128 v[232:235], v254 offset:22528\n\t" \
    "s_waitcnt lgkmcnt(3)\n\t" \
    "v_mfma_f32_16x16x32_bf16 v[244:247], v[236:239], v[204:207], v[244:247]\n\t" \
    "ds_read_b128 v[236:239], v254 offset:23552\n\t" \
    "v_and_b32 v252, 0xffffff7f, v254\n\t" \
    "s_waitcnt lgkmcnt(0)\n\t" \
    "s_barrier \n\t" \
    "v_mfma_f32_16x16x32_bf16 v[244:247], v[224:227], v[208:211], v[244:247]\n\t" \
    "ds_read_b128 v[224:227], v252 offset:3072\n\t" \
    "v_mfma_f32_16x16x32_bf16 v[244:247], v[228:231], v[212:215], v[244:247]\n\t" \
    "ds_read_b128 v[228:231], v252 offset:3200\n\t" \
    "v_mfma_f32_16x16x32_bf16 v[244:247], v[232:235], v[216:219], v[244:247]\n\t" \
    "ds_read_b128 v[232:235], v252 offset:4096\n\t" \
    "v_mfma_f32_16x16x32_bf16 v[244:247], v[236:239], v[220:223], v[244:247]\n\t" \
    "ds_read_b128 v[236:239], v252 offset:4224\n\t" \
    "s_waitcnt lgkmcnt(3)\n\t" \
    "v_mfma_f32_16x16x32_bf16 v[248:251], v[224:227], v[200:203], 0\n\t" \
    "ds_read_b128 v[224:227], v252 offset:5120\n\t" \
    "s_waitcnt lgkmcnt(3)\n\t" \
    "v_mfma_f32_16x16x32_bf16 v[248:251], v[228:231], v[204:207], v[248:251]\n\t" \
    "ds_read_b128 v[228:231], v252 offset:5248\n\t" \
    "s_waitcnt lgkmcnt(3)\n\t" \
    "v_mfma_f32_16x16x32_bf16 v[248:251], v[232:235], v[208:211], v[248:251]\n\t" \
    "s_waitcnt lgkmcnt(2)\n\t" \
    "v_mfma_f32_16x16x32_bf16 v[248:251], v[236:239], v[212:215], v[248:251]\n\t" \
    "s_waitcnt lgkmcnt(1)\n\t" \
    "v_mfma_f32_16x16x32_bf16 v[248:251], v[224:227], v[216:219], v[248:251]\n\t" \
    "s_waitcnt lgkmcnt(0)\n\t" \
    "v_mfma_f32_16x16x32_bf16 v[248:251], v[228:231], v[220:223], v[248:251]\n\t" \
    "v_mov_b32 v226, 0xff7fffff\n\t" \
    "v_cndmask_b32 v240, v240, v226, s[52:53]\n\t" \
    "v_cndmask_b32 v241, v241, v226, s[54:55]\n\t" \
    "v_cndmask_b32 v242, v242, v226, s[56:57]\n\t" \
    "v_cndmask_b32 v243, v243, v226, s[58:59]\n\t" \
    "v_max3_f32 v224, v240, v241, v242\n\t" \
    "v_max3_f32 v225, v244, v245, v246\n\t" \
    "v_max3_f32 v224, v224, v243, v247\n\t" \
    "v_lshrrev_b32 v200, 5, v253\n\t" \
    "v_and_b32 v201, 0xf0, v253\n\t" \
    "v_and_or_b32 v200, v200, 8, v201\n\t" \
    "v_max3_f32 v224, v224, v248, v249\n\t" \
    "v_max3_f32 v225, v225, v250, v251\n\t" \
    "v_max_f32 v224, v224, v225\n\t" \
    "v_mov_b32 v225, v224\n\t" \
    "s_nop 1\n\t" \
    "v_permlane16_swap_b32 v224, v225\n\t" \
    "v_max_f32 v224, v224, v225\n\t" \
    "v_mov_b32 v225, v224\n\t" \
    "s_nop 1\n\t" \
    "v_permlane32_swap_b32 v224, v225\n\t" \
    "v_max_f32 v224, v224, v225\n\t" \
    "v_mul_f32 v224, %[scl], v224\n\t" \
    "v_fma_f32 v240, v240, %[scl], -v224\n\t" \
    "v_fma_f32 v241, v241, %[scl], -v224\n\t" \
    "v_exp_f32 v240, v240\n\t" \
    "v_fma_f32 v242, v242, %[scl], -v224\n\t" \
    "v_exp_f32 v241, v241\n\t" \
    "v_fma_f32 v243, v243, %[scl], -v224\n\t" \
    "v_exp_f32 v242, v242\n\t" \
    "v_fma_f32 v244, v244, %[scl], -v224\n\t" \
    "v_exp_f32 v243, v243\n\t" \
    "v_fma_f32 v245, v245, %[scl], -v224\n\t" \
    "v_exp_f32 v244, v244\n\t" \
    "v_fma_f32 v246, v246, %[scl], -v224\n\t" \
    "v_exp_f32 v245, v245\n\t" \
    "v_fma_f32 v247, v247, %[scl], -v224\n\t" \
    "v_exp_f32 v246, v246\n\t" \
    "v_fma_f32 v248, v248, %[scl], -v224\n\t" \
    "v_exp_f32 v247, v247\n\t" \
    "v_fma_f32 v249, v249, %[scl], -v224\n\t" \
    "v_exp_f32 v248, v248\n\t" \
    "v_fma_f32 v250, v250, %[scl], -v224\n\t" \
    "v_exp_f32 v249, v249\n\t" \
    "v_fma_f32 v251, v251, %[scl], -v224\n\t" \
    "v_exp_f32 v250, v250\n\t" \
    "v_exp_f32 v251, v251\n\t" \
    "v_add_f32 v224, v240, v241\n\t" \
    "v_add_f32 v225, v242, v243\n\t" \
    "v_add_f32 v226, v244, v245\n\t" \
    "v_add_f32 v227, v246, v247\n\t" \
    "v_add_f32 v232, v248, v249\n\t" \
    "v_add_f32 v233, v250, v251\n\t" \
    "v_add_f32 v224, v224, v225\n\t" \
    "v_add_f32 v226, v226, v227\n\t" \
    "v_add_f32 v232, v232, v233\n\t" \
    "v_add_f32 v224, v224, v226\n\t" \
    "v_fma_f32 v224, v232, 0.5, v224\n\t" \
    "v_mov_b32 v225, v224\n\t" \
    "s_nop 1\n\t" \
    "v_permlane16_swap_b32 v224, v225\n\t" \
    "v_add_f32 v224, v224, v225\n\t" \
    "v_mov_b32 v225, v224\n\t" \
    "s_nop 1\n\t" \
    "v_permlane32_swap_b32 v224, v225\n\t" \
    "v_add_f32 v224, v224, v225\n\t" \
    "v_rcp_f32 v224, v224\n\t" \
    "s_nop 0\n\t" \
    "v_mul_f32 v240, v240, v224\n\t" \
    "v_mul_f32 v241, v241, v224\n\t" \
    "v_mul_f32 v242, v242, v224\n\t" \
    "v_mul_f32 v243, v243, v224\n\t" \
    "v_mul_f32 v244, v244, v224\n\t" \
    "v_mul_f32 v245, v245, v224\n\t" \
    "v_mul_f32 v246, v246, v224\n\t" \
    "v_mul_f32 v247, v247, v224\n\t" \
    "v_mul_f32 v248, v248, v224\n\t" \
    "v_mul_f32 v249, v249, v224\n\t" \
    "v_mul_f32 v250, v250, v224\n\t" \
    "v_mul_f32 v251, v251, v224\n\t" \
    "v_cvt_pk_bf16_f32 v204, |v244|, |v240|\n\t" \
    "v_cvt_pk_bf16_f32 v205, |v245|, |v241|\n\t" \
    "v_cvt_pk_bf16_f32 v206, |v246|, |v242|\n\t" \
    "v_cvt_pk_bf16_f32 v207, |v247|, |v243|\n\t" \
    "v_cvt_pk_bf16_f32 v208, |v248|, |v249|\n\t" \
    "v_cvt_pk_bf16_f32 v209, |v250|, |v251|\n\t" \
    "buffer_store_dwordx4 v[204:207], v253, s[60:63], s50 offen sc1\n\t" \
    "s_mov_b32 exec_lo, -1\n\t" \
    "s_mov_b32 exec_hi, 0\n\t" \
    "buffer_store_dwordx2 v[208:209], v200, s[60:63], s51 offen sc1\n\t" \
    "s_mov_b32 exec_hi, -1\n\t" \
    "ds_read_b128 v[224:227], v253 offset:0\n\t" \
    "ds_read_b128 v[244:247], v254 offset:53248\n\t" \
    "ds_read_b128 v[228:231], v253 offset:20480\n\t" \
    "ds_read_b128 v[232:235], v253 offset:1024\n\t" \
    "ds_read_b128 v[236:239], v253 offset:21504\n\t" \
    "ds_read_b128 v[248:251], v254 offset:54272\n\t" \
    "s_add_u32 s40, s40, 1\n\t" \
    "s_cmp_lg_u32 s40, 64\n\t" \
    "s_cbranch_scc1 .Ls23_%=\n\t" \
    "s_branch .Lend%=\n\t" \
    ".Ls1_%=:\n\t" \
    "v_add_u32 v244, 0x18000, v243\n\t" \
    "s_waitcnt vmcnt(12) lgkmcnt(0)\n\t" \
    "s_barrier \n\t" \
    "s_add_u32 m0, %[wm], 0x12000\n\t" \
    "s_mov_b32 s44, 0x1e0000\n\t" \
    "buffer_load_dwordx4 v240, %[wr], s44 offen offset:0 nt lds\n\t" \
    "buffer_load_dwordx4 v241, %[wr], s44 offen offset:1024 nt lds\n\t" \
    "buffer_load_dwordx4 v242, %[wr], s44 offen offset:2048 nt lds\n\t" \
    "ds_read_b64_tr_b16 v[56:57], v244 offset:0\n\t" \
    "ds_read_b64_tr_b16 v[58:59], v244 offset:1536\n\t" \
    "ds_read_b64_tr_b16 v[64:65], v244 offset:32\n\t" \
    "ds_read_b64_tr_b16 v[66:67], v244 offset:1568\n\t" \
    "ds_read_b64_tr_b16 v[72:73], v244 offset:64\n\t" \
    "ds_read_b64_tr_b16 v[74:75], v244 offset:1600\n\t" \
    "v_add_u32 v244, 0x1e000, v243\n\t" \
    "s_waitcnt vmcnt(12) lgkmcnt(0)\n\t" \
    "s_barrier \n\t" \
    "s_add_u32 m0, %[wm], 0x18000\n\t" \
    "s_mov_b32 s44, 0x240000\n\t" \
    "buffer_load_dwordx4 v240, %[wr], s44 offen offset:0 nt lds\n\t" \
    "buffer_load_dwordx4 v241, %[wr], s44 offen offset:1024 nt lds\n\t" \
    "buffer_load_dwordx4 v242, %[wr], s44 offen offset:2048 nt lds\n\t" \
    "ds_read_b64_tr_b16 v[60:61], v244 offset:0\n\t" \
    "ds_read_b64_tr_b16 v[62:63], v244 offset:1536\n\t" \
    "ds_read_b64_tr_b16 v[68:69], v244 offset:32\n\t" \
    "ds_read_b64_tr_b16 v[70:71], v244 offset:1568\n\t" \
    "ds_read_b64_tr_b16 v[76:77], v244 offset:64\n\t" \
    "ds_read_b64_tr_b16 v[78:79], v244 offset:1600\n\t" \
    "v_add_u32 v244, 0x0, v243\n\t" \
    "s_waitcnt vmcnt(12) lgkmcnt(0)\n\t" \
    "s_barrier \n\t" \
    "s_add_u32 m0, %[wm], 0x1e000\n\t" \
    "s_mov_b32 s44, 0x2a0000\n\t" \
    "buffer_load_dwordx4 v240, %[wr], s44 offen offset:0 nt lds\n\t" \
    "buffer_load_dwordx4 v241, %[wr], s44 offen offset:1024 nt lds\n\t" \
    "buffer_load_dwordx4 v242, %[wr], s44 offen offset:2048 nt lds\n\t" \
    "ds_read_b64_tr_b16 v[96:97], v244 offset:0\n\t" \
    "ds_read_b64_tr_b16 v[98:99], v244 offset:1536\n\t" \
    "ds_read_b64_tr_b16 v[104:105], v244 offset:32\n\t" \
    "ds_read_b64_tr_b16 v[106:107], v244 offset:1568\n\t" \
    "ds_read_b64_tr_b16 v[112:113], v244 offset:64\n\t" \
    "ds_read_b64_tr_b16 v[114:115], v244 offset:1600\n\t" \
    "v_add_u32 v244, 0x6000, v243\n\t" \
    "s_waitcnt vmcnt(12) lgkmcnt(0)\n\t" \
    "s_barrier \n\t" \
    "s_add_u32 m0, %[wm], 0x0\n\t" \
    "s_mov_b32 s44, 0x300000\n\t" \
    "buffer_load_dwordx4 v240, %[wr], s44 offen offset:0 nt lds\n\t" \
    "buffer_load_dwordx4 v241, %[wr], s44 offen offset:1024 nt lds\n\t" \
    "buffer_load_dwordx4 v242, %[wr], s44 offen offset:2048 nt lds\n\t" \
    "ds_read_b64_tr_b16 v[100:101], v244 offset:0\n\t" \
    "ds_read_b64_tr_b16 v[102:103], v244 offset:1536\n\t" \
    "ds_read_b64_tr_b16 v[108:109], v244 offset:32\n\t" \
    "ds_read_b64_tr_b16 v[110:111], v244 offset:1568\n\t" \
    "ds_read_b64_tr_b16 v[116:117], v244 offset:64\n\t" \
    "ds_read_b64_tr_b16 v[118:119], v244 offset:1600\n\t" \
    "v_add_u32 v244, 0xc000, v243\n\t" \
    "s_waitcnt vmcnt(12) lgkmcnt(0)\n\t" \
    "s_barrier \n\t" \
    "s_add_u32 m0, %[wm], 0x6000\n\t" \
    "s_mov_b32 s44, 0x360000\n\t" \
    "buffer_load_dwordx4 v240, %[wr], s44 offen offset:0 nt lds\n\t" \
    "buffer_load_dwordx4 v241, %[wr], s44 offen offset:1024 nt lds\n\t" \
    "buffer_load_dwordx4 v242, %[wr], s44 offen offset:2048 nt lds\n\t" \
    "ds_read_b64_tr_b16 v[200:201], v244 offset:0\n\t" \
    "ds_read_b64_tr_b16 v[202:203], v244 offset:1536\n\t" \
    "ds_read_b64_tr_b16 v[0:1], v244 offset:32\n\t" \
    "ds_read_b64_tr_b16 v[2:3], v244 offset:1568\n\t" \
    "ds_read_b64_tr_b16 v[8:9], v244 offset:64\n\t" \
    "ds_read_b64_tr_b16 v[10:11], v244 offset:1600\n\t" \
    "v_add_u32 v244, 0x12000, v243\n\t" \
    "s_waitcnt vmcnt(12) lgkmcnt(0)\n\t" \
    "s_barrier \n\t" \
    "s_add_u32 m0, %[wm], 0xc000\n\t" \
    "s_mov_b32 s44, 0x3c0000\n\t" \
    "buffer_load_dwordx4 v240, %[wr], s44 offen offset:0 nt lds\n\t" \
    "buffer_load_dwordx4 v241, %[wr], s44 offen offset:1024 nt lds\n\t" \
    "buffer_load_dwordx4 v242, %[wr], s44 offen offset:2048 nt lds\n\t" \
    "ds_read_b64_tr_b16 v[204:205], v244 offset:0\n\t" \
    "ds_read_b64_tr_b16 v[206:207], v244 offset:1536\n\t" \
    "ds_read_b64_tr_b16 v[4:5], v244 offset:32\n\t" \
    "ds_read_b64_tr_b16 v[6:7], v244 offset:1568\n\t" \
    "ds_read_b64_tr_b16 v[12:13], v244 offset:64\n\t" \
    "ds_read_b64_tr_b16 v[14:15], v244 offset:1600\n\t" \
    "v_add_u32 v244, 0x18000, v243\n\t" \
    "s_waitcnt vmcnt(12) lgkmcnt(0)\n\t" \
    "s_barrier \n\t" \
    "s_add_u32 m0, %[wm], 0x12000\n\t" \
    "s_mov_b32 s44, 0x420000\n\t" \
    "buffer_load_dwordx4 v240, %[wr], s44 offen offset:0 nt lds\n\t" \
    "buffer_load_dwordx4 v241, %[wr], s44 offen offset:1024 nt lds\n\t" \
    "buffer_load_dwordx4 v242, %[wr], s44 offen offset:2048 nt lds\n\t" \
    "ds_read_b64_tr_b16 v[208:209], v244 offset:0\n\t" \
    "ds_read_b64_tr_b16 v[210:211], v244 offset:1536\n\t" \
    "ds_read_b64_tr_b16 v[40:41], v244 offset:32\n\t" \
    "ds_read_b64_tr_b16 v[42:43], v244 offset:1568\n\t" \
    "ds_read_b64_tr_b16 v[48:49], v244 offset:64\n\t" \
    "ds_read_b64_tr_b16 v[50:51], v244 offset:1600\n\t" \
    "v_add_u32 v244, 0x1e000, v243\n\t" \
    "s_waitcnt vmcnt(12) lgkmcnt(0)\n\t" \
    "s_barrier \n\t" \
    "s_add_u32 m0, %[wm], 0x18000\n\t" \
    "s_mov_b32 s44, 0x480000\n\t" \
    "buffer_load_dwordx4 v240, %[wr], s44 offen offset:0 nt lds\n\t" \
    "buffer_load_dwordx4 v241, %[wr], s44 offen offset:1024 nt lds\n\t" \
    "buffer_load_dwordx4 v242, %[wr], s44 offen offset:2048 nt lds\n\t" \
    "ds_read_b64_tr_b16 v[212:213], v244 offset:0\n\t" \
    "ds_read_b64_tr_b16 v[214:215], v244 offset:1536\n\t" \
    "ds_read_b64_tr_b16 v[44:45], v244 offset:32\n\t" \
    "ds_read_b64_tr_b16 v[46:47], v244 offset:1568\n\t" \
    "ds_read_b64_tr_b16 v[52:53], v244 offset:64\n\t" \
    "ds_read_b64_tr_b16 v[54:55], v244 offset:1600\n\t" \
    "v_add_u32 v244, 0x0, v243\n\t" \
    "s_waitcnt vmcnt(12) lgkmcnt(0)\n\t" \
    "s_barrier \n\t" \
    "v_mov_b32 v246, 0\n\t" \
    "buffer_load_dword v246, v246, s[60:63], %[mo] offen sc1\n\t" \
    "s_add_u32 m0, %[wm], 0x1e000\n\t" \
    "s_mov_b32 s44, 0x4e0000\n\t" \
    "buffer_load_dwordx4 v240, %[wr], s44 offen offset:0 nt lds\n\t" \
    "buffer_load_dwordx4 v241, %[wr], s44 offen offset:1024 nt lds\n\t" \
    "buffer_load_dwordx4 v242, %[wr], s44 offen offset:2048 nt lds\n\t" \
    "ds_read_b64_tr_b16 v[136:137], v244 offset:0\n\t" \
    "ds_read_b64_tr_b16 v[138:139], v244 offset:1536\n\t" \
    "ds_read_b64_tr_b16 v[144:145], v244 offset:32\n\t" \
    "ds_read_b64_tr_b16 v[146:147], v244 offset:1568\n\t" \
    "ds_read_b64_tr_b16 v[152:153], v244 offset:64\n\t" \
    "ds_read_b64_tr_b16 v[154:155], v244 offset:1600\n\t" \
    "v_add_u32 v244, 0x6000, v243\n\t" \
    "s_waitcnt vmcnt(12) lgkmcnt(0)\n\t" \
    "s_barrier \n\t" \
    "s_add_u32 m0, %[wm], 0x0\n\t" \
    "s_mov_b32 s44, 0x540000\n\t" \
    "buffer_load_dwordx4 v240, %[wr], s44 offen offset:0 nt lds\n\t" \
    "buffer_load_dwordx4 v241, %[wr], s44 offen offset:1024 nt lds\n\t" \
    "buffer_load_dwordx4 v242, %[wr], s44 offen offset:2048 nt lds\n\t" \
    "ds_read_b64_tr_b16 v[140:141], v244 offset:0\n\t" \
    "ds_read_b64_tr_b16 v[142:143], v244 offset:1536\n\t" \
    "ds_read_b64_tr_b16 v[148:149], v244 offset:32\n\t" \
    "ds_read_b64_tr_b16 v[150:151], v244 offset:1568\n\t" \
    "ds_read_b64_tr_b16 v[156:157], v244 offset:64\n\t" \
    "ds_read_b64_tr_b16 v[158:159], v244 offset:1600\n\t" \
    "v_add_u32 v244, 0xc000, v243\n\t" \
    "s_waitcnt vmcnt(12) lgkmcnt(0)\n\t" \
    "s_barrier \n\t" \
    "s_add_u32 m0, %[wm], 0x6000\n\t" \
    "s_mov_b32 s44, 0x5a0000\n\t" \
    "buffer_load_dwordx4 v240, %[wr], s44 offen offset:0 nt lds\n\t" \
    "buffer_load_dwordx4 v241, %[wr], s44 offen offset:1024 nt lds\n\t" \
    "buffer_load_dwordx4 v242, %[wr], s44 offen offset:2048 nt lds\n\t" \
    "ds_read_b64_tr_b16 v[216:217], v244 offset:0\n\t" \
    "ds_read_b64_tr_b16 v[218:219], v244 offset:1536\n\t" \
    "ds_read_b64_tr_b16 v[80:81], v244 offset:32\n\t" \
    "ds_read_b64_tr_b16 v[82:83], v244 offset:1568\n\t" \
    "ds_read_b64_tr_b16 v[88:89], v244 offset:64\n\t" \
    "ds_read_b64_tr_b16 v[90:91], v244 offset:1600\n\t" \
    "v_add_u32 v244, 0x12000, v243\n\t" \
    "s_waitcnt vmcnt(12) lgkmcnt(0)\n\t" \
    "s_barrier \n\t" \
    "s_add_u32 m0, %[wm], 0xc000\n\t" \
    "s_mov_b32 s44, 0x600000\n\t" \
    "buffer_load_dwordx4 v240, %[wr], s44 offen offset:0 nt lds\n\t" \
    "buffer_load_dwordx4 v241, %[wr], s44 offen offset:1024 nt lds\n\t" \
    "buffer_load_dwordx4 v242, %[wr], s44 offen offset:2048 nt lds\n\t" \
    "ds_read_b64_tr_b16 v[220:221], v244 offset:0\n\t" \
    "ds_read_b64_tr_b16 v[222:223], v244 offset:1536\n\t" \
    "ds_read_b64_tr_b16 v[84:85], v244 offset:32\n\t" \
    "ds_read_b64_tr_b16 v[86:87], v244 offset:1568\n\t" \
    "ds_read_b64_tr_b16 v[92:93], v244 offset:64\n\t" \
    "ds_read_b64_tr_b16 v[94:95], v244 offset:1600\n\t" \
    "v_add_u32 v244, 0x18000, v243\n\t" \
    "s_waitcnt vmcnt(12) lgkmcnt(0)\n\t" \
    "s_barrier \n\t" \
    "s_add_u32 m0, %[wm], 0x12000\n\t" \
    "s_mov_b32 s44, 0x660000\n\t" \
    "buffer_load_dwordx4 v240, %[wr], s44 offen offset:0 nt lds\n\t" \
    "buffer_load_dwordx4 v241, %[wr], s44 offen offset:1024 nt lds\n\t" \
    "buffer_load_dwordx4 v242, %[wr], s44 offen offset:2048 nt lds\n\t" \
    "ds_read_b64_tr_b16 v[16:17], v244 offset:0\n\t" \
    "ds_read_b64_tr_b16 v[18:19], v244 offset:1536\n\t" \
    "ds_read_b64_tr_b16 v[24:25], v244 offset:32\n\t" \
    "ds_read_b64_tr_b16 v[26:27], v244 offset:1568\n\t" \
    "ds_read_b64_tr_b16 v[32:33], v244 offset:64\n\t" \
    "ds_read_b64_tr_b16 v[34:35], v244 offset:1600\n\t" \
    "v_add_u32 v244, 0x1e000, v243\n\t" \
    "s_waitcnt vmcnt(12) lgkmcnt(0)\n\t" \
    "s_barrier \n\t" \
    ".Ls11_%=:\n\t" \
    "v_readfirstlane_b32 s43, v246\n\t" \
    "s_nop 0\n\t" \
    "s_cmp_lg_u32 s43, -1\n\t" \
    "s_cbranch_scc1 .Ls12_%=\n\t" \
    "s_cmp_eq_u32 s41, 0\n\t" \
    "s_cbranch_scc1 .Ls12_%=\n\t" \
    "s_sub_u32 s41, s41, 1\n\t" \
    "s_sleep 4\n\t" \
    "v_mov_b32 v246, 0\n\t" \
    "buffer_load_dword v246, v246, s[60:63], %[mo] offen sc1\n\t" \
    "s_waitcnt vmcnt(0)\n\t" \
    "s_branch .Ls11_%=\n\t" \
    ".Ls12_%=:\n\t" \
    "v_mov_b32 v246, 0x8000\n\t" \
    "s_mov_b64 exec, 1\n\t" \
    "buffer_store_dword v246, v246, s[60:63], %[mo] offen sc1\n\t" \
    "s_mov_b64 exec, -1\n\t" \
    "s_and_b32 s44, s43, 0x5000\n\t" \
    "s_xor_b32 %[mb], %[mb], s44\n\t" \
    "s_xor_b32 %[fl], %[fl], s44\n\t" \
    "s_and_b32 s43, s43, 0x2000\n\t" \
    "s_xor_b32 s43, s43, %[lx]\n\t" \
    "s_add_u32 s60, s60, s43\n\t" \
    "s_addc_u32 s61, s61, 0\n\t" \
    "s_add_u32 m0, %[wm], 0x18000\n\t" \
    "s_mov_b32 s44, 0x6c0000\n\t" \
    "buffer_load_dwordx4 v240, %[wr], s44 offen offset:0 nt lds\n\t" \
    "buffer_load_dwordx4 v241, %[wr], s44 offen offset:1024 nt lds\n\t" \
    "buffer_load_dwordx4 v242, %[wr], s44 offen offset:2048 nt lds\n\t" \
    "ds_read_b64_tr_b16 v[20:21], v244 offset:0\n\t" \
    "ds_read_b64_tr_b16 v[22:23], v244 offset:1536\n\t" \
    "ds_read_b64_tr_b16 v[28:29], v244 offset:32\n\t" \
    "ds_read_b64_tr_b16 v[30:31], v244 offset:1568\n\t" \
    "ds_read_b64_tr_b16 v[36:37], v244 offset:64\n\t" \
    "ds_read_b64_tr_b16 v[38:39], v244 offset:1600\n\t" \
    "v_add_u32 v244, 0x0, v243\n\t" \
    "s_waitcnt vmcnt(12) lgkmcnt(0)\n\t" \
    "s_barrier \n\t" \
    "s_add_u32 m0, %[wm], 0x1e000\n\t" \
    "s_mov_b32 s44, 0x720000\n\t" \
    "buffer_load_dwordx4 v240, %[wr], s44 offen offset:0 nt lds\n\t" \
    "buffer_load_dwordx4 v241, %[wr], s44 offen offset:1024 nt lds\n\t" \
    "buffer_load_dwordx4 v242, %[wr], s44 offen offset:2048 nt lds\n\t" \
    "ds_read_b64_tr_b16 v[224:225], v244 offset:0\n\t" \
    "ds_read_b64_tr_b16 v[226:227], v244 offset:1536\n\t" \
    "ds_read_b64_tr_b16 v[120:121], v244 offset:32\n\t" \
    "ds_read_b64_tr_b16 v[122:123], v244 offset:1568\n\t" \
    "ds_read_b64_tr_b16 v[128:129], v244 offset:64\n\t" \
    "ds_read_b64_tr_b16 v[130:131], v244 offset:1600\n\t" \
    "v_add_u32 v244, 0x6000, v243\n\t" \
    "s_waitcnt vmcnt(12) lgkmcnt(0)\n\t" \
    "s_barrier \n\t" \
    "ds_read_b64_tr_b16 v[228:229], v244 offset:0\n\t" \
    "ds_read_b64_tr_b16 v[230:231], v244 offset:1536\n\t" \
    "ds_read_b64_tr_b16 v[124:125], v244 offset:32\n\t" \
    "ds_read_b64_tr_b16 v[126:127], v244 offset:1568\n\t" \
    "ds_read_b64_tr_b16 v[132:133], v244 offset:64\n\t" \
    "ds_read_b64_tr_b16 v[134:135], v244 offset:1600\n\t" \
    "v_add_u32 v244, 0xc000, v243\n\t" \
    "s_waitcnt vmcnt(9) lgkmcnt(0)\n\t" \
    "s_barrier \n\t" \
    "ds_read_b64_tr_b16 v[232:233], v244 offset:0\n\t" \
    "ds_read_b64_tr_b16 v[234:235], v244 offset:1536\n\t" \
    "ds_read_b64_tr_b16 v[160:161], v244 offset:32\n\t" \
    "ds_read_b64_tr_b16 v[162:163], v244 offset:1568\n\t" \
    "ds_read_b64_tr_b16 v[168:169], v244 offset:64\n\t" \
    "ds_read_b64_tr_b16 v[170:171], v244 offset:1600\n\t" \
    "v_add_u32 v244, 0x12000, v243\n\t" \
    "s_waitcnt vmcnt(6) lgkmcnt(0)\n\t" \
    "s_barrier \n\t" \
    "ds_read_b64_tr_b16 v[236:237], v244 offset:0\n\t" \
    "ds_read_b64_tr_b16 v[238:239], v244 offset:1536\n\t" \
    "ds_read_b64_tr_b16 v[164:165], v244 offset:32\n\t" \
    "ds_read_b64_tr_b16 v[166:167], v244 offset:1568\n\t" \
    "ds_read_b64_tr_b16 v[172:173], v244 offset:64\n\t" \
    "ds_read_b64_tr_b16 v[174:175], v244 offset:1600\n\t" \
    "v_add_u32 v244, 0x18000, v243\n\t" \
    "s_waitcnt vmcnt(3) lgkmcnt(0)\n\t" \
    "s_barrier \n\t" \
    "ds_read_b64_tr_b16 v[176:177], v244 offset:0\n\t" \
    "ds_read_b64_tr_b16 v[178:179], v244 offset:1536\n\t" \
    "ds_read_b64_tr_b16 v[184:185], v244 offset:32\n\t" \
    "ds_read_b64_tr_b16 v[186:187], v244 offset:1568\n\t" \
    "ds_read_b64_tr_b16 v[192:193], v244 offset:64\n\t" \
    "ds_read_b64_tr_b16 v[194:195], v244 offset:1600\n\t" \
    "v_add_u32 v244, 0x1e000, v243\n\t" \
    "s_waitcnt vmcnt(0) lgkmcnt(0)\n\t" \
    "s_barrier \n\t" \
    "ds_read_b64_tr_b16 v[180:181], v244 offset:0\n\t" \
    "ds_read_b64_tr_b16 v[182:183], v244 offset:1536\n\t" \
    "ds_read_b64_tr_b16 v[188:189], v244 offset:32\n\t" \
    "ds_read_b64_tr_b16 v[190:191], v244 offset:1568\n\t" \
    "ds_read_b64_tr_b16 v[196:197], v244 offset:64\n\t" \
    "ds_read_b64_tr_b16 v[198:199], v244 offset:1600\n\t" \
    "s_waitcnt lgkmcnt(0)\n\t" \
    "s_barrier \n\t" \
    "ds_write_b128 v254, v[200:203] offset:40960\n\t" \
    "ds_write_b128 v254, v[204:207] offset:41984\n\t" \
    "ds_write_b128 v254, v[208:211] offset:43008\n\t" \
    "ds_write_b128 v254, v[212:215] offset:44032\n\t" \
    "ds_write_b128 v254, v[216:219] offset:45056\n\t" \
    "ds_write_b128 v254, v[220:223] offset:46080\n\t" \
    "ds_write_b128 v254, v[224:227] offset:47104\n\t" \
    "ds_write_b128 v254, v[228:231] offset:48128\n\t" \
    "ds_write_b128 v254, v[232:235] offset:49152\n\t" \
    "ds_write_b128 v254, v[236:239] offset:50176\n\t" \
    "s_waitcnt vmcnt(0) lgkmcnt(0)\n\t" \
    "s_barrier \n\t" \
    "ds_read_b128 v[224:227], v253 offset:0\n\t" \
    "ds_read_b128 v[244:247], v254 offset:40960\n\t" \
    "ds_read_b128 v[228:231], v253 offset:20480\n\t" \
    "ds_read_b128 v[232:235], v253 offset:1024\n\t" \
    "ds_read_b128 v[236:239], v253 offset:21504\n\t" \
    "ds_read_b128 v[248:251], v254 offset:41984\n\t" \
    ".p2align 8\n\t" \
    ".Ls13_%=:\n\t" \
    "s_waitcnt lgkmcnt(5)\n\t" \
    "v_mfma_f32_16x16x32_bf16 v[204:207], v[64:67], v[224:227], 0\n\t" \
    "ds_read_b128 v[240:243], v253 offset:2048\n\t" \
    "v_mfma_f32_16x16x32_bf16 v[208:211], v[72:75], v[224:227], 0\n\t" \
    "v_mfma_f32_16x16x32_bf16 v[200:203], v[56:59], v[224:227], 0\n\t" \
    "s_waitcnt lgkmcnt(4)\n\t" \
    "v_mfma_f32_16x16x32_bf16 v[216:219], v[64:67], v[228:231], 0\n\t" \
    "ds_read_b128 v[224:227], v253 offset:22528\n\t" \
    "v_mfma_f32_16x16x32_bf16 v[220:223], v[72:75], v[228:231], 0\n\t" \
    "v_mfma_f32_16x16x32_bf16 v[212:215], v[56:59], v[228:231], 0\n\t" \
    "s_waitcnt lgkmcnt(4)\n\t" \
    "v_mfma_f32_16x16x32_bf16 v[204:207], v[68:71], v[232:235], v[204:207]\n\t" \
    "ds_read_b128 v[228:231], v253 offset:3072\n\t" \
    "v_mfma_f32_16x16x32_bf16 v[208:211], v[76:79], v[232:235], v[208:211]\n\t" \
    "v_mfma_f32_16x16x32_bf16 v[200:203], v[60:63], v[232:235], v[200:203]\n\t" \
    "s_waitcnt lgkmcnt(4)\n\t" \
    "v_mfma_f32_16x16x32_bf16 v[216:219], v[68:71], v[236:239], v[216:219]\n\t" \
    "ds_read_b128 v[232:235], v253 offset:23552\n\t" \
    "v_mfma_f32_16x16x32_bf16 v[220:223], v[76:79], v[236:239], v[220:223]\n\t" \
    "v_mfma_f32_16x16x32_bf16 v[212:215], v[60:63], v[236:239], v[212:215]\n\t" \
    "s_waitcnt lgkmcnt(3)\n\t" \
    "v_mfma_f32_16x16x32_bf16 v[204:207], v[104:107], v[240:243], v[204:207]\n\t" \
    "ds_read_b128 v[236:239], v253 offset:4096\n\t" \
    "v_mfma_f32_16x16x32_bf16 v[208:211], v[112:115], v[240:243], v[208:211]\n\t" \
    "v_mfma_f32_16x16x32_bf16 v[200:203], v[96:99], v[240:243], v[200:203]\n\t" \
    "s_waitcnt lgkmcnt(3)\n\t" \
    "v_mfma_f32_16x16x32_bf16 v[216:219], v[104:107], v[224:227], v[216:219]\n\t" \
    "ds_read_b128 v[240:243], v253 offset:24576\n\t" \
    "v_mfma_f32_16x16x32_bf16 v[220:223], v[112:115], v[224:227], v[220:223]\n\t" \
    "v_mfma_f32_16x16x32_bf16 v[212:215], v[96:99], v[224:227], v[212:215]\n\t" \
    "s_waitcnt lgkmcnt(3)\n\t" \
    "v_mfma_f32_16x16x32_bf16 v[204:207], v[108:111], v[228:231], v[204:207]\n\t" \
    "ds_read_b128 v[224:227], v253 offset:5120\n\t" \
    "v_mfma_f32_16x16x32_bf16 v[208:211], v[116:119], v[228:231], v[208:211]\n\t" \
    "v_mfma_f32_16x16x32_bf16 v[200:203], v[100:103], v[228:231], v[200:203]\n\t" \
    "s_waitcnt lgkmcnt(3)\n\t" \
    "v_mfma_f32_16x16x32_bf16 v[216:219], v[108:111], v[232:235], v[216:219]\n\t" \
    "ds_read_b128 v[228:231], v253 offset:25600\n\t" \
    "v_mfma_f32_16x16x32_bf16 v[220:223], v[116:119], v[232:235], v[220:223]\n\t" \
    "v_mfma_f32_16x16x32_bf16 v[212:215], v[100:103], v[232:235], v[212:215]\n\t" \
    "s_waitcnt lgkmcnt(3)\n\t" \
    "v_mfma_f32_16x16x32_bf16 v[204:207], v[0:3], v[236:239], v[204:207]\n\t" \
    "ds_read_b128 v[232:235], v253 offset:6144\n\t" \
    "v_mfma_f32_16x16x32_bf16 v[208:211], v[8:11], v[236:239], v[208:211]\n\t" \
    "v_mfma_f32_16x16x32_bf16 v[200:203], v[244:247], v[236:239], v[200:203]\n\t" \
    "s_waitcnt lgkmcnt(3)\n\t" \
    "v_mfma_f32_16x16x32_bf16 v[216:219], v[0:3], v[240:243], v[216:219]\n\t" \
    "ds_read_b128 v[236:239], v253 offset:26624\n\t" \
    "v_mfma_f32_16x16x32_bf16 v[220:223], v[8:11], v[240:243], v[220:223]\n\t" \
    "v_mfma_f32_16x16x32_bf16 v[212:215], v[244:247], v[240:243], v[212:215]\n\t" \
    "ds_read_b128 v[244:247], v254 offset:43008\n\t" \
    "s_waitcnt lgkmcnt(4)\n\t" \
    "v_mfma_f32_16x16x32_bf16 v[204:207], v[4:7], v[224:227], v[204:207]\n\t" \
    "ds_read_b128 v[240:243], v253 offset:7168\n\t" \
    "v_mfma_f32_16x16x32_bf16 v[208:211], v[12:15], v[224:227], v[208:211]\n\t" \
    "v_mfma_f32_16x16x32_bf16 v[200:203], v[248:251], v[224:227], v[200:203]\n\t" \
    "s_waitcnt lgkmcnt(4)\n\t" \
    "v_mfma_f32_16x16x32_bf16 v[216:219], v[4:7], v[228:231], v[216:219]\n\t" \
    "ds_read_b128 v[224:227], v253 offset:27648\n\t" \
    "v_mfma_f32_16x16x32_bf16 v[220:223], v[12:15], v[228:231], v[220:223]\n\t" \
    "v_mfma_f32_16x16x32_bf16 v[212:215], v[248:251], v[228:231], v[212:215]\n\t" \
    "ds_read_b128 v[248:251], v254 offset:44032\n\t" \
    "s_waitcnt lgkmcnt(5)\n\t" \
    "v_mfma_f32_16x16x32_bf16 v[204:207], v[40:43], v[232:235], v[204:207]\n\t" \
    "ds_read_b128 v[228:231], v253 offset:8192\n\t" \
    "v_mfma_f32_16x16x32_bf16 v[208:211], v[48:51], v[232:235], v[208:211]\n\t" \
    "s_waitcnt lgkmcnt(4)\n\t" \
    "v_mfma_f32_16x16x32_bf16 v[200:203], v[244:247], v[232:235], v[200:203]\n\t" \
    "v_mfma_f32_16x16x32_bf16 v[216:219], v[40:43], v[236:239], v[216:219]\n\t" \
    "ds_read_b128 v[232:235], v253 offset:28672\n\t" \
    "v_mfma_f32_16x16x32_bf16 v[220:223], v[48:51], v[236:239], v[220:223]\n\t" \
    "v_mfma_f32_16x16x32_bf16 v[212:215], v[244:247], v[236:239], v[212:215]\n\t" \
    "ds_read_b128 v[244:247], v254 offset:45056\n\t" \
    "s_waitcnt lgkmcnt(5)\n\t" \
    "v_mfma_f32_16x16x32_bf16 v[204:207], v[44:47], v[240:243], v[204:207]\n\t" \
    "ds_read_b128 v[236:239], v253 offset:9216\n\t" \
    "v_mfma_f32_16x16x32_bf16 v[208:211], v[52:55], v[240:243], v[208:211]\n\t" \
    "s_waitcnt lgkmcnt(4)\n\t" \
    "v_mfma_f32_16x16x32_bf16 v[200:203], v[248:251], v[240:243], v[200:203]\n\t" \
    "v_mfma_f32_16x16x32_bf16 v[216:219], v[44:47], v[224:227], v[216:219]\n\t" \
    "ds_read_b128 v[240:243], v253 offset:29696\n\t" \
    "v_mfma_f32_16x16x32_bf16 v[220:223], v[52:55], v[224:227], v[220:223]\n\t" \
    "v_mfma_f32_16x16x32_bf16 v[212:215], v[248:251], v[224:227], v[212:215]\n\t" \
    "ds_read_b128 v[248:251], v254 offset:46080\n\t" \
    "s_waitcnt lgkmcnt(5)\n\t" \
    "v_mfma_f32_16x16x32_bf16 v[204:207], v[144:147], v[228:231], v[204:207]\n\t" \
    "ds_read_b128 v[224:227], v253 offset:10240\n\t" \
    "v_mfma_f32_16x16x32_bf16 v[208:211], v[152:155], v[228:231], v[208:211]\n\t" \
    "v_mfma_f32_16x16x32_bf16 v[200:203], v[136:139], v[228:231], v[200:203]\n\t" \
    "s_waitcnt lgkmcnt(5)\n\t" \
    "v_mfma_f32_16x16x32_bf16 v[216:219], v[144:147], v[232:235], v[216:219]\n\t" \
    "ds_read_b128 v[228:231], v253 offset:30720\n\t" \
    "v_mfma_f32_16x16x32_bf16 v[220:223], v[152:155], v[232:235], v[220:223]\n\t" \
    "v_mfma_f32_16x16x32_bf16 v[212:215], v[136:139], v[232:235], v[212:215]\n\t" \
    "s_waitcnt lgkmcnt(4)\n\t" \
    "v_mfma_f32_16x16x32_bf16 v[204:207], v[148:151], v[236:239], v[204:207]\n\t" \
    "ds_read_b128 v[232:235], v253 offset:11264\n\t" \
    "v_mfma_f32_16x16x32_bf16 v[208:211], v[156:159], v[236:239], v[208:211]\n\t" \
    "v_mfma_f32_16x16x32_bf16 v[200:203], v[140:143], v[236:239], v[200:203]\n\t" \
    "s_waitcnt lgkmcnt(4)\n\t" \
    "v_mfma_f32_16x16x32_bf16 v[216:219], v[148:151], v[240:243], v[216:219]\n\t" \
    "ds_read_b128 v[236:239], v253 offset:31744\n\t" \
    "v_mfma_f32_16x16x32_bf16 v[220:223], v[156:159], v[240:243], v[220:223]\n\t" \
    "v_mfma_f32_16x16x32_bf16 v[212:215], v[140:143], v[240:243], v[212:215]\n\t" \
    "s_setprio 2\n\t" \
    "s_waitcnt lgkmcnt(0)\n\t" \
    "s_barrier \n\t" \
    "v_mfma_f32_16x16x32_bf16 v[204:207], v[80:83], v[224:227], v[204:207]\n\t" \
    "ds_read_b128 v[240:243], v253 offset:14336\n\t" \
    "v_mfma_f32_16x16x32_bf16 v[208:211], v[88:91], v[224:227], v[208:211]\n\t" \
    "v_mfma_f32_16x16x32_bf16 v[200:203], v[244:247], v[224:227], v[200:203]\n\t" \
    "v_mfma_f32_16x16x32_bf16 v[216:219], v[80:83], v[228:231], v[216:219]\n\t" \
    "ds_read_b128 v[224:227], v253 offset:34816\n\t" \
    "s_and_b32 s44, s40, 3\n\t" \
    "v_mfma_f32_16x16x32_bf16 v[220:223], v[88:91], v[228:231], v[220:223]\n\t" \
    "s_lshl_b32 s44, s44, 22\n\t" \
    "v_mfma_f32_16x16x32_bf16 v[212:215], v[244:247], v[228:231], v[212:215]\n\t" \
    "ds_read_b128 v[244:247], v254 offset:47104\n\t" \
    "s_add_u32 s44, s44, %[mb]\n\t" \
    "v_mfma_f32_16x16x32_bf16 v[204:207], v[84:87], v[232:235], v[204:207]\n\t" \
    "ds_read_b128 v[228:231], v253 offset:15360\n\t" \
    "s_add_u32 s46, s44, 0x800\n\t" \
    "v_mfma_f32_16x16x32_bf16 v[208:211], v[92:95], v[232:235], v[208:211]\n\t" \
    "s_add_u32 s47, s44, 0x8800\n\t" \
    "v_mfma_f32_16x16x32_bf16 v[200:203], v[248:251], v[232:235], v[200:203]\n\t" \
    "s_add_u32 s48, s44, 0xc00\n\t" \
    "v_mfma_f32_16x16x32_bf16 v[216:219], v[84:87], v[236:239], v[216:219]\n\t" \
    "ds_read_b128 v[232:235], v253 offset:35840\n\t" \
    "v_mfma_f32_16x16x32_bf16 v[220:223], v[92:95], v[236:239], v[220:223]\n\t" \
    "v_mfma_f32_16x16x32_bf16 v[212:215], v[248:251], v[236:239], v[212:215]\n\t" \
    "ds_read_b128 v[248:251], v254 offset:48128\n\t" \
    "s_waitcnt lgkmcnt(5)\n\t" \
    "v_mfma_f32_16x16x32_bf16 v[204:207], v[120:123], v[240:243], v[204:207]\n\t" \
    "ds_read_b128 v[236:239], v253 offset:16384\n\t" \
    "v_mfma_f32_16x16x32_bf16 v[208:211], v[128:131], v[240:243], v[208:211]\n\t" \
    "s_waitcnt lgkmcnt(4)\n\t" \
    "v_mfma_f32_16x16x32_bf16 v[200:203], v[244:247], v[240:243], v[200:203]\n\t" \
    "v_mfma_f32_16x16x32_bf16 v[216:219], v[120:123], v[224:227], v[216:219]\n\t" \
    "ds_read_b128 v[240:243], v253 offset:36864\n\t" \
    "v_mfma_f32_16x16x32_bf16 v[220:223], v[128:131], v[224:227], v[220:223]\n\t" \
    "v_mfma_f32_16x16x32_bf16 v[212:215], v[244:247], v[224:227], v[212:215]\n\t" \
    "ds_read_b128 v[244:247], v254 offset:49152\n\t" \
    "s_waitcnt lgkmcnt(5)\n\t" \
    "v_mfma_f32_16x16x32_bf16 v[204:207], v[124:127], v[228:231], v[204:207]\n\t" \
    "ds_read_b128 v[224:227], v253 offset:17408\n\t" \
    "v_mfma_f32_16x16x32_bf16 v[208:211], v[132:135], v[228:231], v[208:211]\n\t" \
    "s_waitcnt lgkmcnt(4)\n\t" \
    "v_mfma_f32_16x16x32_bf16 v[200:203], v[248:251], v[228:231], v[200:203]\n\t" \
    "v_mfma_f32_16x16x32_bf16 v[216:219], v[124:127], v[232:235], v[216:219]\n\t" \
    "ds_read_b128 v[228:231], v253 offset:37888\n\t" \
    "v_mfma_f32_16x16x32_bf16 v[220:223], v[132:135], v[232:235], v[220:223]\n\t" \
    "v_mfma_f32_16x16x32_bf16 v[212:215], v[248:251], v[232:235], v[212:215]\n\t" \
    "ds_read_b128 v[248:251], v254 offset:50176\n\t" \
    "s_waitcnt lgkmcnt(5)\n\t" \
    "v_mfma_f32_16x16x32_bf16 v[204:207], v[160:163], v[236:239], v[204:207]\n\t" \
    "ds_read_b128 v[232:235], v253 offset:12288\n\t" \
    "v_mfma_f32_16x16x32_bf16 v[208:211], v[168:171], v[236:239], v[208:211]\n\t" \
    "s_waitcnt lgkmcnt(4)\n\t" \
    "v_mfma_f32_16x16x32_bf16 v[200:203], v[244:247], v[236:239], v[200:203]\n\t" \
    "v_mfma_f32_16x16x32_bf16 v[216:219], v[160:163], v[240:243], v[216:219]\n\t" \
    "ds_read_b128 v[236:239], v253 offset:13312\n\t" \
    "v_mfma_f32_16x16x32_bf16 v[220:223], v[168:171], v[240:243], v[220:223]\n\t" \
    "v_mfma_f32_16x16x32_bf16 v[212:215], v[244:247], v[240:243], v[212:215]\n\t" \
    "s_waitcnt lgkmcnt(4)\n\t" \
    "v_mfma_f32_16x16x32_bf16 v[204:207], v[164:167], v[224:227], v[204:207]\n\t" \
    "ds_read_b128 v[240:243], v253 offset:18432\n\t" \
    "v_mfma_f32_16x16x32_bf16 v[208:211], v[172:175], v[224:227], v[208:211]\n\t" \
    "s_waitcnt lgkmcnt(3)\n\t" \
    "v_mfma_f32_16x16x32_bf16 v[200:203], v[248:251], v[224:227], v[200:203]\n\t" \
    "v_mfma_f32_16x16x32_bf16 v[216:219], v[164:167], v[228:231], v[216:219]\n\t" \
    "ds_read_b128 v[224:227], v253 offset:19456\n\t" \
    "v_mfma_f32_16x16x32_bf16 v[220:223], v[172:175], v[228:231], v[220:223]\n\t" \
    "v_mfma_f32_16x16x32_bf16 v[212:215], v[248:251], v[228:231], v[212:215]\n\t" \
    "s_waitcnt lgkmcnt(3)\n\t" \
    "v_mfma_f32_16x16x32_bf16 v[204:207], v[24:27], v[232:235], v[204:207]\n\t" \
    "ds_read_b128 v[228:231], v253 offset:32768\n\t" \
    "v_mov_b64 v[244:245], 0\n\t" \
    "v_mfma_f32_16x16x32_bf16 v[208:211], v[32:35], v[232:235], v[208:211]\n\t" \
    "v_mov_b64 v[246:247], 0\n\t" \
    "v_mfma_f32_16x16x32_bf16 v[200:203], v[16:19], v[232:235], v[200:203]\n\t" \
    "v_mov_b64 v[248:249], 0\n\t" \
    "s_waitcnt lgkmcnt(3)\n\t" \
    "v_mfma_f32_16x16x32_bf16 v[204:207], v[28:31], v[236:239], v[204:207]\n\t" \
    "ds_read_b128 v[232:235], v253 offset:33792\n\t" \
    "v_mov_b64 v[250:251], 0\n\t" \
    "v_mfma_f32_16x16x32_bf16 v[208:211], v[36:39], v[236:239], v[208:211]\n\t" \
    "v_mfma_f32_16x16x32_bf16 v[200:203], v[20:23], v[236:239], v[200:203]\n\t" \
    "s_waitcnt lgkmcnt(3)\n\t" \
    "v_mfma_f32_16x16x32_bf16 v[204:207], v[184:187], v[240:243], v[204:207]\n\t" \
    "ds_read_b128 v[236:239], v253 offset:38912\n\t" \
    "v_mfma_f32_16x16x32_bf16 v[208:211], v[192:195], v[240:243], v[208:211]\n\t" \
    "v_mfma_f32_16x16x32_bf16 v[200:203], v[176:179], v[240:243], v[200:203]\n\t" \
    "s_waitcnt lgkmcnt(3)\n\t" \
    "v_mfma_f32_16x16x32_bf16 v[204:207], v[188:191], v[224:227], v[204:207]\n\t" \
    "ds_read_b128 v[240:243], v253 offset:39936\n\t" \
    "v_mfma_f32_16x16x32_bf16 v[208:211], v[196:199], v[224:227], v[208:211]\n\t" \
    "v_mfma_f32_16x16x32_bf16 v[200:203], v[180:183], v[224:227], v[200:203]\n\t" \
    "s_waitcnt lgkmcnt(3)\n\t" \
    "v_mfma_f32_16x16x32_bf16 v[216:219], v[24:27], v[228:231], v[216:219]\n\t" \
    "s_waitcnt lgkmcnt(2)\n\t" \
    "v_mfma_f32_16x16x32_bf16 v[216:219], v[28:31], v[232:235], v[216:219]\n\t" \
    "v_cvt_pk_bf16_f32 v204, v204, v208\n\t" \
    "v_cvt_pk_bf16_f32 v205, v205, v209\n\t" \
    "s_waitcnt lgkmcnt(1)\n\t" \
    "v_mfma_f32_16x16x32_bf16 v[216:219], v[184:187], v[236:239], v[216:219]\n\t" \
    "v_cvt_pk_bf16_f32 v206, v206, v210\n\t" \
    "v_cvt_pk_bf16_f32 v207, v207, v211\n\t" \
    "s_waitcnt lgkmcnt(0)\n\t" \
    "v_mfma_f32_16x16x32_bf16 v[216:219], v[188:191], v[240:243], v[216:219]\n\t" \
    "ds_write_b128 v254, v[204:207] offset:9216\n\t" \
    "v_mfma_f32_16x16x32_bf16 v[220:223], v[32:35], v[228:231], v[220:223]\n\t" \
    "v_mfma_f32_16x16x32_bf16 v[220:223], v[36:39], v[232:235], v[220:223]\n\t" \
    "v_cvt_pk_bf16_f32 v200, v200, v201\n\t" \
    "v_mfma_f32_16x16x32_bf16 v[220:223], v[192:195], v[236:239], v[220:223]\n\t" \
    "v_cvt_pk_bf16_f32 v201, v202, v203\n\t" \
    "ds_write_b64 v254, v[200:201] offset:7168\n\t" \
    "v_mfma_f32_16x16x32_bf16 v[220:223], v[196:199], v[240:243], v[220:223]\n\t" \
    "v_mfma_f32_16x16x32_bf16 v[212:215], v[16:19], v[228:231], v[212:215]\n\t" \
    "v_mfma_f32_16x16x32_bf16 v[212:215], v[20:23], v[232:235], v[212:215]\n\t" \
    "ds_read_b128 v[200:203], v254 offset:40960\n\t" \
    "ds_read_b128 v[204:207], v254 offset:41984\n\t" \
    "v_mfma_f32_16x16x32_bf16 v[212:215], v[176:179], v[236:239], v[212:215]\n\t" \
    "v_mov_b64 v[208:209], 0\n\t" \
    "v_mov_b64 v[210:211], 0\n\t" \
    "v_mfma_f32_16x16x32_bf16 v[212:215], v[180:183], v[240:243], v[212:215]\n\t" \
    "v_cvt_pk_bf16_f32 v216, v216, v220\n\t" \
    "v_cvt_pk_bf16_f32 v217, v217, v221\n\t" \
    "s_setprio 0\n\t" \
    "v_cvt_pk_bf16_f32 v218, v218, v222\n\t" \
    "v_cvt_pk_bf16_f32 v219, v219, v223\n\t" \
    "ds_write_b128 v254, v[216:219] offset:15360\n\t" \
    "ds_read_b128 v[216:219], v254 offset:43008\n\t" \
    "ds_read_b128 v[220:223], v254 offset:44032\n\t" \
    "s_nop 0\n\t" \
    "v_cvt_pk_bf16_f32 v212, v212, v213\n\t" \
    "v_cvt_pk_bf16_f32 v213, v214, v215\n\t" \
    "ds_write_b64 v254, v[212:213] offset:13312\n\t" \
    "s_sleep 3\n\t" \
    "s_mov_b32 m0, 0xa800\n\t" \
    "s_nop 0\n\t" \
    "buffer_load_dwordx4 v253, s[60:63], s46 offen sc1 lds\n\t" \
    "s_mov_b32 m0, 0xb800\n\t" \
    "s_nop 0\n\t" \
    "buffer_load_dwordx4 v253, s[60:63], s47 offen sc1 lds\n\t" \
    "s_mov_b32 m0, 0xac00\n\t" \
    "s_nop 0\n\t" \
    "buffer_load_dwordx4 v253, s[60:63], s48 offen sc1 lds\n\t" \
    "s_waitcnt lgkmcnt(0)\n\t" \
    "v_bfe_i32 v212, v253, 5, 1\n\t" \
    "v_and_b32 v212, 0xaaaa, v212\n\t" \
    "v_or_b32 v212, 0x4444, v212\n\t" \
    "v_and_b32 v213, 0xffff, v255\n\t" \
    "v_lshrrev_b32 v214, 16, v255\n\t" \
    ".Ls14_%=:\n\t" \
    "s_waitcnt vmcnt(0)\n\t" \
    "ds_read_b128 v[224:227], v253 offset:43008\n\t" \
    "ds_read_b128 v[228:231], v253 offset:47104\n\t" \
    "ds_read_b128 v[232:235], v253 offset:44032\n\t" \
    "s_waitcnt lgkmcnt(2)\n\t" \
    "v_max_u32 v215, v224, v225\n\t" \
    "v_max3_u32 v215, v226, v227, v215\n\t" \
    "s_waitcnt lgkmcnt(1)\n\t" \
    "v_max3_u32 v215, v228, v229, v215\n\t" \
    "v_max3_u32 v215, v230, v231, v215\n\t" \
    "s_waitcnt lgkmcnt(0)\n\t" \
    "v_max3_u32 v215, v232, v233, v215\n\t" \
    "v_max3_u32 v215, v234, v235, v215\n\t" \
    "v_cmp_eq_u32 vcc, -1, v215\n\t" \
    "s_cbranch_vccz .Ls15_%=\n\t" \
    "v_max_u32 v215, v224, v225\n\t" \
    "v_max3_u32 v215, v226, v227, v215\n\t" \
    "v_cmp_eq_u32 vcc, -1, v215\n\t" \
    "s_cbranch_vccz .Ls16_%=\n\t" \
    "s_mov_b32 m0, 0xa800\n\t" \
    "s_nop 0\n\t" \
    "buffer_load_dwordx4 v253, s[60:63], s46 offen sc1 lds\n\t" \
    ".Ls16_%=:\n\t" \
    "v_max_u32 v215, v228, v229\n\t" \
    "v_max3_u32 v215, v230, v231, v215\n\t" \
    "v_cmp_eq_u32 vcc, -1, v215\n\t" \
    "s_cbranch_vccz .Ls17_%=\n\t" \
    "s_mov_b32 m0, 0xb800\n\t" \
    "s_nop 0\n\t" \
    "buffer_load_dwordx4 v253, s[60:63], s47 offen sc1 lds\n\t" \
    ".Ls17_%=:\n\t" \
    "v_max_u32 v215, v232, v233\n\t" \
    "v_max3_u32 v215, v234, v235, v215\n\t" \
    "v_cmp_eq_u32 vcc, -1, v215\n\t" \
    "s_cbranch_vccz .Ls18_%=\n\t" \
    "s_mov_b32 m0, 0xac00\n\t" \
    "s_nop 0\n\t" \
    "buffer_load_dwordx4 v253, s[60:63], s48 offen sc1 lds\n\t" \
    ".Ls18_%=:\n\t" \
    "s_cmp_eq_u32 s41, 0\n\t" \
    "s_cbranch_scc1 .Ls15_%=\n\t" \
    "s_sub_u32 s41, s41, 1\n\t" \
    "s_branch .Ls14_%=\n\t" \
    ".Ls15_%=:\n\t" \
    "s_setprio 3\n\t" \
    "s_waitcnt lgkmcnt(0)\n\t" \
    "s_barrier \n\t" \
    "ds_read_b128 v[236:239], v253 offset:40960\n\t" \
    "ds_read_b128 v[240:243], v253 offset:46080\n\t" \
    "v_smfmac_f32_16x16x64_bf16 v[244:247], v[224:227], v[0:7], v212\n\t" \
    "v_smfmac_f32_16x16x64_bf16 v[248:251], v[224:227], v[8:15], v212\n\t" \
    "v_smfmac_f32_16x16x64_bf16 v[208:211], v[224:227], v[200:207], v212\n\t" \
    "ds_read_b128 v[200:203], v254 offset:45056\n\t" \
    "ds_read_b128 v[204:207], v254 offset:46080\n\t" \
    "ds_read_b128 v[224:227], v253 offset:41984\n\t" \
    "v_smfmac_f32_16x16x64_bf16 v[208:211], v[228:231], v[16:23], v212\n\t" \
    "v_smfmac_f32_16x16x64_bf16 v[244:247], v[228:231], v[24:31], v212\n\t" \
    "v_smfmac_f32_16x16x64_bf16 v[248:251], v[228:231], v[32:39], v212\n\t" \
    "ds_read_b128 v[228:231], v253 offset:48128\n\t" \
    "v_smfmac_f32_16x16x64_bf16 v[208:211], v[232:235], v[216:223], v212\n\t" \
    "ds_read_b128 v[216:219], v254 offset:47104\n\t" \
    "ds_read_b128 v[220:223], v254 offset:48128\n\t" \
    "v_smfmac_f32_16x16x64_bf16 v[244:247], v[232:235], v[40:47], v212\n\t" \
    "v_smfmac_f32_16x16x64_bf16 v[248:251], v[232:235], v[48:55], v212\n\t" \
    "ds_read_b128 v[232:235], v253 offset:45056\n\t" \
    "s_waitcnt lgkmcnt(8)\n\t" \
    "v_smfmac_f32_16x16x64_bf16 v[208:211], v[236:239], v[56:63], v212\n\t" \
    "v_smfmac_f32_16x16x64_bf16 v[244:247], v[236:239], v[64:71], v212\n\t" \
    "v_smfmac_f32_16x16x64_bf16 v[248:251], v[236:239], v[72:79], v212\n\t" \
    "ds_read_b128 v[236:239], v253 offset:49152\n\t" \
    "s_waitcnt lgkmcnt(6)\n\t" \
    "v_smfmac_f32_16x16x64_bf16 v[208:211], v[240:243], v[200:207], v212\n\t" \
    "ds_read_b128 v[200:203], v254 offset:49152\n\t" \
    "ds_read_b128 v[204:207], v254 offset:50176\n\t" \
    "v_smfmac_f32_16x16x64_bf16 v[244:247], v[240:243], v[80:87], v212\n\t" \
    "v_smfmac_f32_16x16x64_bf16 v[248:251], v[240:243], v[88:95], v212\n\t" \
    "ds_read_b128 v[240:243], v253 offset:50176\n\t" \
    "s_waitcnt lgkmcnt(8)\n\t" \
    "v_smfmac_f32_16x16x64_bf16 v[208:211], v[224:227], v[96:103], v212\n\t" \
    "v_smfmac_f32_16x16x64_bf16 v[244:247], v[224:227], v[104:111], v212\n\t" \
    "v_smfmac_f32_16x16x64_bf16 v[248:251], v[224:227], v[112:119], v212\n\t" \
    "s_waitcnt lgkmcnt(5)\n\t" \
    "v_smfmac_f32_16x16x64_bf16 v[208:211], v[228:231], v[216:223], v212\n\t" \
    "s_waitcnt lgkmcnt(4)\n\t" \
    "v_smfmac_f32_16x16x64_bf16 v[208:211], v[232:235], v[136:143], v212\n\t" \
    "s_waitcnt lgkmcnt(1)\n\t" \
    "v_smfmac_f32_16x16x64_bf16 v[208:211], v[236:239], v[200:207], v212\n\t" \
    "s_waitcnt lgkmcnt(0)\n\t" \
    "v_smfmac_f32_16x16x64_bf16 v[208:211], v[240:243], v[176:183], v212\n\t" \
    "v_smfmac_f32_16x16x64_bf16 v[244:247], v[228:231], v[120:127], v212\n\t" \
    "v_smfmac_f32_16x16x64_bf16 v[244:247], v[232:235], v[144:151], v212\n\t" \
    "v_smfmac_f32_16x16x64_bf16 v[244:247], v[236:239], v[160:167], v212\n\t" \
    "v_add_f32 v208, v208, v210\n\t" \
    "v_add_f32 v209, v209, v211\n\t" \
    "v_cvt_pk_bf16_f32 v208, v208, v209\n\t" \
    "v_smfmac_f32_16x16x64_bf16 v[244:247], v[240:243], v[184:191], v212\n\t" \
    "ds_write_b16 v213, v208 offset:0\n\t" \
    "ds_write_b16_d16_hi v213, v208 offset:16\n\t" \
    "v_smfmac_f32_16x16x64_bf16 v[248:251], v[228:231], v[128:135], v212\n\t" \
    "v_smfmac_f32_16x16x64_bf16 v[248:251], v[232:235], v[152:159], v212\n\t" \
    "v_smfmac_f32_16x16x64_bf16 v[248:251], v[236:239], v[168:175], v212\n\t" \
    "v_add_f32 v244, v244, v246\n\t" \
    "v_add_f32 v245, v245, v247\n\t" \
    "v_smfmac_f32_16x16x64_bf16 v[248:251], v[240:243], v[192:199], v212\n\t" \
    "s_nop 7\n\t" \
    "v_add_f32 v248, v248, v250\n\t" \
    "v_add_f32 v249, v249, v251\n\t" \
    "v_cvt_pk_bf16_f32 v244, v244, v248\n\t" \
    "v_cvt_pk_bf16_f32 v245, v245, v249\n\t" \
    "ds_write2_b32 v214, v244, v245 offset1:4\n\t" \
    "s_setprio 0\n\t" \
    "s_sleep 1\n\t" \
    "ds_read_b128 v[200:203], v254 offset:6144\n\t" \
    "ds_read_b128 v[224:227], v254 offset:18432\n\t" \
    "ds_read_b128 v[204:207], v254 offset:7168\n\t" \
    "ds_read_b128 v[228:231], v254 offset:19456\n\t" \
    "ds_read_b128 v[208:211], v254 offset:8192\n\t" \
    "ds_read_b128 v[232:235], v254 offset:20480\n\t" \
    "ds_read_b128 v[212:215], v254 offset:9216\n\t" \
    "ds_read_b128 v[236:239], v254 offset:21504\n\t" \
    "ds_read_b128 v[216:219], v254 offset:10240\n\t" \
    "ds_read_b128 v[220:223], v254 offset:11264\n\t" \
    "s_and_b32 s44, s40, 3\n\t" \
    "s_lshl_b32 s44, s44, 17\n\t" \
    "s_add_u32 s50, s44, %[pb]\n\t" \
    "s_add_u32 s51, s44, %[pm]\n\t" \
    "s_mov_b32 s52, 0xf0000\n\t" \
    "s_mov_b32 s53, 0xfff00ff\n\t" \
    "s_mov_b32 s54, 0x1f0001\n\t" \
    "s_mov_b32 s55, 0x1fff01ff\n\t" \
    "s_mov_b32 s56, 0x3f0003\n\t" \
    "s_mov_b32 s57, 0x3fff03ff\n\t" \
    "s_mov_b32 s58, 0x7f0007\n\t" \
    "s_mov_b32 s59, 0x7fff07ff\n\t" \
    "s_waitcnt lgkmcnt(8)\n\t" \
    "v_mfma_f32_16x16x32_bf16 v[240:243], v[224:227], v[200:203], 0\n\t" \
    "ds_read_b128 v[224:227], v254 offset:22528\n\t" \
    "s_waitcnt lgkmcnt(7)\n\t" \
    "v_mfma_f32_16x16x32_bf16 v[240:243], v[228:231], v[204:207], v[240:243]\n\t" \
    "ds_read_b128 v[228:231], v254 offset:23552\n\t" \
    "v_and_b32 v252, 0xffffff7f, v254\n\t" \
    "s_waitcnt lgkmcnt(0)\n\t" \
    "s_barrier \n\t" \
    "v_mfma_f32_16x16x32_bf16 v[240:243], v[232:235], v[208:211], v[240:243]\n\t" \
    "ds_read_b128 v[232:235], v252 offset:3072\n\t" \
    "v_mfma_f32_16x16x32_bf16 v[240:243], v[236:239], v[212:215], v[240:243]\n\t" \
    "ds_read_b128 v[236:239], v252 offset:3200\n\t" \
    "v_mfma_f32_16x16x32_bf16 v[240:243], v[224:227], v[216:219], v[240:243]\n\t" \
    "ds_read_b128 v[224:227], v252 offset:4096\n\t" \
    "v_mfma_f32_16x16x32_bf16 v[240:243], v[228:231], v[220:223], v[240:243]\n\t" \
    "ds_read_b128 v[228:231], v252 offset:4224\n\t" \
    "s_waitcnt lgkmcnt(3)\n\t" \
    "v_mfma_f32_16x16x32_bf16 v[248:251], v[232:235], v[200:203], 0\n\t" \
    "ds_read_b128 v[232:235], v252 offset:5120\n\t" \
    "s_waitcnt lgkmcnt(3)\n\t" \
    "v_mfma_f32_16x16x32_bf16 v[248:251], v[236:239], v[204:207], v[248:251]\n\t" \
    "ds_read_b128 v[236:239], v252 offset:5248\n\t" \
    "s_waitcnt lgkmcnt(3)\n\t" \
    "v_mfma_f32_16x16x32_bf16 v[248:251], v[224:227], v[208:211], v[248:251]\n\t" \
    "s_waitcnt lgkmcnt(2)\n\t" \
    "v_mfma_f32_16x16x32_bf16 v[248:251], v[228:231], v[212:215], v[248:251]\n\t" \
    "s_waitcnt lgkmcnt(1)\n\t" \
    "v_mfma_f32_16x16x32_bf16 v[248:251], v[232:235], v[216:219], v[248:251]\n\t" \
    "s_waitcnt lgkmcnt(0)\n\t" \
    "v_mfma_f32_16x16x32_bf16 v[248:251], v[236:239], v[220:223], v[248:251]\n\t" \
    "v_mov_b32 v226, 0xff7fffff\n\t" \
    "v_cndmask_b32 v240, v240, v226, s[52:53]\n\t" \
    "v_cndmask_b32 v241, v241, v226, s[54:55]\n\t" \
    "v_cndmask_b32 v242, v242, v226, s[56:57]\n\t" \
    "v_cndmask_b32 v243, v243, v226, s[58:59]\n\t" \
    "v_max3_f32 v224, v240, v241, v242\n\t" \
    "v_mov_b32 v225, v243\n\t" \
    "v_lshrrev_b32 v200, 5, v253\n\t" \
    "v_and_b32 v201, 0xf0, v253\n\t" \
    "v_and_or_b32 v200, v200, 8, v201\n\t" \
    "v_max3_f32 v224, v224, v248, v249\n\t" \
    "v_max3_f32 v225, v225, v250, v251\n\t" \
    "v_max_f32 v224, v224, v225\n\t" \
    "v_mov_b32 v225, v224\n\t" \
    "s_nop 1\n\t" \
    "v_permlane16_swap_b32 v224, v225\n\t" \
    "v_max_f32 v224, v224, v225\n\t" \
    "v_mov_b32 v225, v224\n\t" \
    "s_nop 1\n\t" \
    "v_permlane32_swap_b32 v224, v225\n\t" \
    "v_max_f32 v224, v224, v225\n\t" \
    "v_mul_f32 v224, %[scl], v224\n\t" \
    "v_fma_f32 v240, v240, %[scl], -v224\n\t" \
    "v_fma_f32 v241, v241, %[scl], -v224\n\t" \
    "v_exp_f32 v240, v240\n\t" \
    "v_fma_f32 v242, v242, %[scl], -v224\n\t" \
    "v_exp_f32 v241, v241\n\t" \
    "v_fma_f32 v243, v243, %[scl], -v224\n\t" \
    "v_exp_f32 v242, v242\n\t" \
    "v_fma_f32 v248, v248, %[scl], -v224\n\t" \
    "v_exp_f32 v243, v243\n\t" \
    "v_fma_f32 v249, v249, %[scl], -v224\n\t" \
    "v_exp_f32 v248, v248\n\t" \
    "v_fma_f32 v250, v250, %[scl], -v224\n\t" \
    "v_exp_f32 v249, v249\n\t" \
    "v_fma_f32 v251, v251, %[scl], -v224\n\t" \
    "v_exp_f32 v250, v250\n\t" \
    "v_exp_f32 v251, v251\n\t" \
    "v_add_f32 v224, v240, v241\n\t" \
    "v_add_f32 v225, v242, v243\n\t" \
    "v_add_f32 v232, v248, v249\n\t" \
    "v_add_f32 v233, v250, v251\n\t" \
    "v_add_f32 v224, v224, v225\n\t" \
    "v_add_f32 v232, v232, v233\n\t" \
    "v_fma_f32 v224, v232, 0.5, v224\n\t" \
    "v_mov_b32 v225, v224\n\t" \
    "s_nop 1\n\t" \
    "v_permlane16_swap_b32 v224, v225\n\t" \
    "v_add_f32 v224, v224, v225\n\t" \
    "v_mov_b32 v225, v224\n\t" \
    "s_nop 1\n\t" \
    "v_permlane32_swap_b32 v224, v225\n\t" \
    "v_add_f32 v224, v224, v225\n\t" \
    "v_rcp_f32 v224, v224\n\t" \
    "s_nop 0\n\t" \
    "v_mul_f32 v240, v240, v224\n\t" \
    "v_mul_f32 v241, v241, v224\n\t" \
    "v_mul_f32 v242, v242, v224\n\t" \
    "v_mul_f32 v243, v243, v224\n\t" \
    "v_mul_f32 v248, v248, v224\n\t" \
    "v_mul_f32 v249, v249, v224\n\t" \
    "v_mul_f32 v250, v250, v224\n\t" \
    "v_mul_f32 v251, v251, v224\n\t" \
    "v_cvt_pk_bf16_f32 v204, |v240|, |v241|\n\t" \
    "v_cvt_pk_bf16_f32 v205, |v242|, |v243|\n\t" \
    "v_cvt_pk_bf16_f32 v208, |v248|, |v249|\n\t" \
    "v_cvt_pk_bf16_f32 v209, |v250|, |v251|\n\t" \
    "v_permlane16_swap_b32 v204, v206\n\t" \
    "v_permlane16_swap_b32 v205, v207\n\t" \
    "s_mov_b32 exec_lo, 0xffff\n\t" \
    "s_mov_b32 exec_hi, 0xffff\n\t" \
    "buffer_store_dwordx4 v[204:207], v253, s[60:63], s50 offen sc1\n\t" \
    "s_mov_b32 exec_lo, -1\n\t" \
    "s_mov_b32 exec_hi, 0\n\t" \
    "buffer_store_dwordx2 v[208:209], v200, s[60:63], s51 offen sc1\n\t" \
    "s_mov_b32 exec_hi, -1\n\t" \
    "ds_read_b128 v[224:227], v253 offset:0\n\t" \
    "ds_read_b128 v[244:247], v254 offset:40960\n\t" \
    "ds_read_b128 v[228:231], v253 offset:20480\n\t" \
    "ds_read_b128 v[232:235], v253 offset:1024\n\t" \
    "ds_read_b128 v[236:239], v253 offset:21504\n\t" \
    "ds_read_b128 v[248:251], v254 offset:41984\n\t" \
    "s_add_u32 s40, s40, 1\n\t" \
    "s_cmp_lg_u32 s40, 64\n\t" \
    "s_cbranch_scc1 .Ls13_%=\n\t" \
    "s_branch .Lend%=\n\t" \
    ".Ls0_%=:\n\t" \
    "v_add_u32 v244, 0x18000, v243\n\t" \
    "s_waitcnt vmcnt(12) lgkmcnt(0)\n\t" \
    "s_barrier \n\t" \
    "s_add_u32 m0, %[wm], 0x12000\n\t" \
    "s_mov_b32 s44, 0x1e0000\n\t" \
    "buffer_load_dwordx4 v240, %[wr], s44 offen offset:0 nt lds\n\t" \
    "buffer_load_dwordx4 v241, %[wr], s44 offen offset:1024 nt lds\n\t" \
    "buffer_load_dwordx4 v242, %[wr], s44 offen offset:2048 nt lds\n\t" \
    "ds_read_b64_tr_b16 v[200:201], v244 offset:0\n\t" \
    "ds_read_b64_tr_b16 v[202:203], v244 offset:1536\n\t" \
    "ds_read_b64_tr_b16 v[0:1], v244 offset:32\n\t" \
    "ds_read_b64_tr_b16 v[2:3], v244 offset:1568\n\t" \
    "ds_read_b64_tr_b16 v[8:9], v244 offset:64\n\t" \
    "ds_read_b64_tr_b16 v[10:11], v244 offset:1600\n\t" \
    "v_add_u32 v244, 0x1e000, v243\n\t" \
    "s_waitcnt vmcnt(12) lgkmcnt(0)\n\t" \
    "s_barrier \n\t" \
    "s_add_u32 m0, %[wm], 0x18000\n\t" \
    "s_mov_b32 s44, 0x240000\n\t" \
    "buffer_load_dwordx4 v240, %[wr], s44 offen offset:0 nt lds\n\t" \
    "buffer_load_dwordx4 v241, %[wr], s44 offen offset:1024 nt lds\n\t" \
    "buffer_load_dwordx4 v242, %[wr], s44 offen offset:2048 nt lds\n\t" \
    "ds_read_b64_tr_b16 v[204:205], v244 offset:0\n\t" \
    "ds_read_b64_tr_b16 v[206:207], v244 offset:1536\n\t" \
    "ds_read_b64_tr_b16 v[4:5], v244 offset:32\n\t" \
    "ds_read_b64_tr_b16 v[6:7], v244 offset:1568\n\t" \
    "ds_read_b64_tr_b16 v[12:13], v244 offset:64\n\t" \
    "ds_read_b64_tr_b16 v[14:15], v244 offset:1600\n\t" \
    "v_add_u32 v244, 0x0, v243\n\t" \
    "s_waitcnt vmcnt(12) lgkmcnt(0)\n\t" \
    "s_barrier \n\t" \
    "s_add_u32 m0, %[wm], 0x1e000\n\t" \
    "s_mov_b32 s44, 0x2a0000\n\t" \
    "buffer_load_dwordx4 v240, %[wr], s44 offen offset:0 nt lds\n\t" \
    "buffer_load_dwordx4 v241, %[wr], s44 offen offset:1024 nt lds\n\t" \
    "buffer_load_dwordx4 v242, %[wr], s44 offen offset:2048 nt lds\n\t" \
    "ds_read_b64_tr_b16 v[208:209], v244 offset:0\n\t" \
    "ds_read_b64_tr_b16 v[210:211], v244 offset:1536\n\t" \
    "ds_read_b64_tr_b16 v[40:41], v244 offset:32\n\t" \
    "ds_read_b64_tr_b16 v[42:43], v244 offset:1568\n\t" \
    "ds_read_b64_tr_b16 v[48:49], v244 offset:64\n\t" \
    "ds_read_b64_tr_b16 v[50:51], v244 offset:1600\n\t" \
    "v_add_u32 v244, 0x6000, v243\n\t" \
    "s_waitcnt vmcnt(12) lgkmcnt(0)\n\t" \
    "s_barrier \n\t" \
    "s_add_u32 m0, %[wm], 0x0\n\t" \
    "s_mov_b32 s44, 0x300000\n\t" \
    "buffer_load_dwordx4 v240, %[wr], s44 offen offset:0 nt lds\n\t" \
    "buffer_load_dwordx4 v241, %[wr], s44 offen offset:1024 nt lds\n\t" \
    "buffer_load_dwordx4 v242, %[wr], s44 offen offset:2048 nt lds\n\t" \
    "ds_read_b64_tr_b16 v[212:213], v244 offset:0\n\t" \
    "ds_read_b64_tr_b16 v[214:215], v244 offset:1536\n\t" \
    "ds_read_b64_tr_b16 v[44:45], v244 offset:32\n\t" \
    "ds_read_b64_tr_b16 v[46:47], v244 offset:1568\n\t" \
    "ds_read_b64_tr_b16 v[52:53], v244 offset:64\n\t" \
    "ds_read_b64_tr_b16 v[54:55], v244 offset:1600\n\t" \
    "v_add_u32 v244, 0xc000, v243\n\t" \
    "s_waitcnt vmcnt(12) lgkmcnt(0)\n\t" \
    "s_barrier \n\t" \
    "s_add_u32 m0, %[wm], 0x6000\n\t" \
    "s_mov_b32 s44, 0x360000\n\t" \
    "buffer_load_dwordx4 v240, %[wr], s44 offen offset:0 nt lds\n\t" \
    "buffer_load_dwordx4 v241, %[wr], s44 offen offset:1024 nt lds\n\t" \
    "buffer_load_dwordx4 v242, %[wr], s44 offen offset:2048 nt lds\n\t" \
    "ds_read_b64_tr_b16 v[216:217], v244 offset:0\n\t" \
    "ds_read_b64_tr_b16 v[218:219], v244 offset:1536\n\t" \
    "ds_read_b64_tr_b16 v[80:81], v244 offset:32\n\t" \
    "ds_read_b64_tr_b16 v[82:83], v244 offset:1568\n\t" \
    "ds_read_b64_tr_b16 v[88:89], v244 offset:64\n\t" \
    "ds_read_b64_tr_b16 v[90:91], v244 offset:1600\n\t" \
    "v_add_u32 v244, 0x12000, v243\n\t" \
    "s_waitcnt vmcnt(12) lgkmcnt(0)\n\t" \
    "s_barrier \n\t" \
    "s_add_u32 m0, %[wm], 0xc000\n\t" \
    "s_mov_b32 s44, 0x3c0000\n\t" \
    "buffer_load_dwordx4 v240, %[wr], s44 offen offset:0 nt lds\n\t" \
    "buffer_load_dwordx4 v241, %[wr], s44 offen offset:1024 nt lds\n\t" \
    "buffer_load_dwordx4 v242, %[wr], s44 offen offset:2048 nt lds\n\t" \
    "ds_read_b64_tr_b16 v[220:221], v244 offset:0\n\t" \
    "ds_read_b64_tr_b16 v[222:223], v244 offset:1536\n\t" \
    "ds_read_b64_tr_b16 v[84:85], v244 offset:32\n\t" \
    "ds_read_b64_tr_b16 v[86:87], v244 offset:1568\n\t" \
    "ds_read_b64_tr_b16 v[92:93], v244 offset:64\n\t" \
    "ds_read_b64_tr_b16 v[94:95], v244 offset:1600\n\t" \
    "v_add_u32 v244, 0x18000, v243\n\t" \
    "s_waitcnt vmcnt(12) lgkmcnt(0)\n\t" \
    "s_barrier \n\t" \
    "s_add_u32 m0, %[wm], 0x12000\n\t" \
    "s_mov_b32 s44, 0x420000\n\t" \
    "buffer_load_dwordx4 v240, %[wr], s44 offen offset:0 nt lds\n\t" \
    "buffer_load_dwordx4 v241, %[wr], s44 offen offset:1024 nt lds\n\t" \
    "buffer_load_dwordx4 v242, %[wr], s44 offen offset:2048 nt lds\n\t" \
    "ds_read_b64_tr_b16 v[224:225], v244 offset:0\n\t" \
    "ds_read_b64_tr_b16 v[226:227], v244 offset:1536\n\t" \
    "ds_read_b64_tr_b16 v[120:121], v244 offset:32\n\t" \
    "ds_read_b64_tr_b16 v[122:123], v244 offset:1568\n\t" \
    "ds_read_b64_tr_b16 v[128:129], v244 offset:64\n\t" \
    "ds_read_b64_tr_b16 v[130:131], v244 offset:1600\n\t" \
    "v_add_u32 v244, 0x1e000, v243\n\t" \
    "s_waitcnt vmcnt(12) lgkmcnt(0)\n\t" \
    "s_barrier \n\t" \
    "s_add_u32 m0, %[wm], 0x18000\n\t" \
    "s_mov_b32 s44, 0x480000\n\t" \
    "buffer_load_dwordx4 v240, %[wr], s44 offen offset:0 nt lds\n\t" \
    "buffer_load_dwordx4 v241, %[wr], s44 offen offset:1024 nt lds\n\t" \
    "buffer_load_dwordx4 v242, %[wr], s44 offen offset:2048 nt lds\n\t" \
    "ds_read_b64_tr_b16 v[228:229], v244 offset:0\n\t" \
    "ds_read_b64_tr_b16 v[230:231], v244 offset:1536\n\t" \
    "ds_read_b64_tr_b16 v[124:125], v244 offset:32\n\t" \
    "ds_read_b64_tr_b16 v[126:127], v244 offset:1568\n\t" \
    "ds_read_b64_tr_b16 v[132:133], v244 offset:64\n\t" \
    "ds_read_b64_tr_b16 v[134:135], v244 offset:1600\n\t" \
    "v_add_u32 v244, 0x0, v243\n\t" \
    "s_waitcnt vmcnt(12) lgkmcnt(0)\n\t" \
    "s_barrier \n\t" \
    "v_mov_b32 v246, 0\n\t" \
    "buffer_load_dword v246, v246, s[60:63], %[mo] offen sc1\n\t" \
    "s_add_u32 m0, %[wm], 0x1e000\n\t" \
    "s_mov_b32 s44, 0x4e0000\n\t" \
    "buffer_load_dwordx4 v240, %[wr], s44 offen offset:0 nt lds\n\t" \
    "buffer_load_dwordx4 v241, %[wr], s44 offen offset:1024 nt lds\n\t" \
    "buffer_load_dwordx4 v242, %[wr], s44 offen offset:2048 nt lds\n\t" \
    "ds_read_b64_tr_b16 v[232:233], v244 offset:0\n\t" \
    "ds_read_b64_tr_b16 v[234:235], v244 offset:1536\n\t" \
    "ds_read_b64_tr_b16 v[160:161], v244 offset:32\n\t" \
    "ds_read_b64_tr_b16 v[162:163], v244 offset:1568\n\t" \
    "ds_read_b64_tr_b16 v[168:169], v244 offset:64\n\t" \
    "ds_read_b64_tr_b16 v[170:171], v244 offset:1600\n\t" \
    "v_add_u32 v244, 0x6000, v243\n\t" \
    "s_waitcnt vmcnt(12) lgkmcnt(0)\n\t" \
    "s_barrier \n\t" \
    "s_add_u32 m0, %[wm], 0x0\n\t" \
    "s_mov_b32 s44, 0x540000\n\t" \
    "buffer_load_dwordx4 v240, %[wr], s44 offen offset:0 nt lds\n\t" \
    "buffer_load_dwordx4 v241, %[wr], s44 offen offset:1024 nt lds\n\t" \
    "buffer_load_dwordx4 v242, %[wr], s44 offen offset:2048 nt lds\n\t" \
    "ds_read_b64_tr_b16 v[236:237], v244 offset:0\n\t" \
    "ds_read_b64_tr_b16 v[238:239], v244 offset:1536\n\t" \
    "ds_read_b64_tr_b16 v[164:165], v244 offset:32\n\t" \
    "ds_read_b64_tr_b16 v[166:167], v244 offset:1568\n\t" \
    "ds_read_b64_tr_b16 v[172:173], v244 offset:64\n\t" \
    "ds_read_b64_tr_b16 v[174:175], v244 offset:1600\n\t" \
    "v_add_u32 v244, 0xc000, v243\n\t" \
    "s_waitcnt vmcnt(12) lgkmcnt(0)\n\t" \
    "s_barrier \n\t" \
    "s_add_u32 m0, %[wm], 0x6000\n\t" \
    "s_mov_b32 s44, 0x5a0000\n\t" \
    "buffer_load_dwordx4 v240, %[wr], s44 offen offset:0 nt lds\n\t" \
    "buffer_load_dwordx4 v241, %[wr], s44 offen offset:1024 nt lds\n\t" \
    "buffer_load_dwordx4 v242, %[wr], s44 offen offset:2048 nt lds\n\t" \
    "ds_read_b64_tr_b16 v[16:17], v244 offset:0\n\t" \
    "ds_read_b64_tr_b16 v[18:19], v244 offset:1536\n\t" \
    "ds_read_b64_tr_b16 v[24:25], v244 offset:32\n\t" \
    "ds_read_b64_tr_b16 v[26:27], v244 offset:1568\n\t" \
    "ds_read_b64_tr_b16 v[32:33], v244 offset:64\n\t" \
    "ds_read_b64_tr_b16 v[34:35], v244 offset:1600\n\t" \
    "v_add_u32 v244, 0x12000, v243\n\t" \
    "s_waitcnt vmcnt(12) lgkmcnt(0)\n\t" \
    "s_barrier \n\t" \
    "s_add_u32 m0, %[wm], 0xc000\n\t" \
    "s_mov_b32 s44, 0x600000\n\t" \
    "buffer_load_dwordx4 v240, %[wr], s44 offen offset:0 nt lds\n\t" \
    "buffer_load_dwordx4 v241, %[wr], s44 offen offset:1024 nt lds\n\t" \
    "buffer_load_dwordx4 v242, %[wr], s44 offen offset:2048 nt lds\n\t" \
    "ds_read_b64_tr_b16 v[20:21], v244 offset:0\n\t" \
    "ds_read_b64_tr_b16 v[22:23], v244 offset:1536\n\t" \
    "ds_read_b64_tr_b16 v[28:29], v244 offset:32\n\t" \
    "ds_read_b64_tr_b16 v[30:31], v244 offset:1568\n\t" \
    "ds_read_b64_tr_b16 v[36:37], v244 offset:64\n\t" \
    "ds_read_b64_tr_b16 v[38:39], v244 offset:1600\n\t" \
    "v_add_u32 v244, 0x18000, v243\n\t" \
    "s_waitcnt vmcnt(12) lgkmcnt(0)\n\t" \
    "s_barrier \n\t" \
    "s_add_u32 m0, %[wm], 0x12000\n\t" \
    "s_mov_b32 s44, 0x660000\n\t" \
    "buffer_load_dwordx4 v240, %[wr], s44 offen offset:0 nt lds\n\t" \
    "buffer_load_dwordx4 v241, %[wr], s44 offen offset:1024 nt lds\n\t" \
    "buffer_load_dwordx4 v242, %[wr], s44 offen offset:2048 nt lds\n\t" \
    "ds_read_b64_tr_b16 v[56:57], v244 offset:0\n\t" \
    "ds_read_b64_tr_b16 v[58:59], v244 offset:1536\n\t" \
    "ds_read_b64_tr_b16 v[64:65], v244 offset:32\n\t" \
    "ds_read_b64_tr_b16 v[66:67], v244 offset:1568\n\t" \
    "ds_read_b64_tr_b16 v[72:73], v244 offset:64\n\t" \
    "ds_read_b64_tr_b16 v[74:75], v244 offset:1600\n\t" \
    "v_add_u32 v244, 0x1e000, v243\n\t" \
    "s_waitcnt vmcnt(12) lgkmcnt(0)\n\t" \
    "s_barrier \n\t" \
    ".Ls01_%=:\n\t" \
    "v_readfirstlane_b32 s43, v246\n\t" \
    "s_nop 0\n\t" \
    "s_cmp_lg_u32 s43, -1\n\t" \
    "s_cbranch_scc1 .Ls02_%=\n\t" \
    "s_cmp_eq_u32 s41, 0\n\t" \
    "s_cbranch_scc1 .Ls02_%=\n\t" \
    "s_sub_u32 s41, s41, 1\n\t" \
    "s_sleep 4\n\t" \
    "v_mov_b32 v246, 0\n\t" \
    "buffer_load_dword v246, v246, s[60:63], %[mo] offen sc1\n\t" \
    "s_waitcnt vmcnt(0)\n\t" \
    "s_branch .Ls01_%=\n\t" \
    ".Ls02_%=:\n\t" \
    "v_mov_b32 v246, 0x8000\n\t" \
    "s_mov_b64 exec, 1\n\t" \
    "buffer_store_dword v246, v246, s[60:63], %[mo] offen sc1\n\t" \
    "s_mov_b64 exec, -1\n\t" \
    "s_and_b32 s44, s43, 0x5000\n\t" \
    "s_xor_b32 %[mb], %[mb], s44\n\t" \
    "s_xor_b32 %[fl], %[fl], s44\n\t" \
    "s_xor_b32 %[pb], %[pb], s44\n\t" \
    "s_and_b32 s43, s43, 0x2000\n\t" \
    "s_xor_b32 s43, s43, %[lx]\n\t" \
    "s_add_u32 s60, s60, s43\n\t" \
    "s_addc_u32 s61, s61, 0\n\t" \
    "s_add_u32 m0, %[wm], 0x18000\n\t" \
    "s_mov_b32 s44, 0x6c0000\n\t" \
    "buffer_load_dwordx4 v240, %[wr], s44 offen offset:0 nt lds\n\t" \
    "buffer_load_dwordx4 v241, %[wr], s44 offen offset:1024 nt lds\n\t" \
    "buffer_load_dwordx4 v242, %[wr], s44 offen offset:2048 nt lds\n\t" \
    "ds_read_b64_tr_b16 v[60:61], v244 offset:0\n\t" \
    "ds_read_b64_tr_b16 v[62:63], v244 offset:1536\n\t" \
    "ds_read_b64_tr_b16 v[68:69], v244 offset:32\n\t" \
    "ds_read_b64_tr_b16 v[70:71], v244 offset:1568\n\t" \
    "ds_read_b64_tr_b16 v[76:77], v244 offset:64\n\t" \
    "ds_read_b64_tr_b16 v[78:79], v244 offset:1600\n\t" \
    "v_add_u32 v244, 0x0, v243\n\t" \
    "s_waitcnt vmcnt(12) lgkmcnt(0)\n\t" \
    "s_barrier \n\t" \
    "s_add_u32 m0, %[wm], 0x1e000\n\t" \
    "s_mov_b32 s44, 0x720000\n\t" \
    "buffer_load_dwordx4 v240, %[wr], s44 offen offset:0 nt lds\n\t" \
    "buffer_load_dwordx4 v241, %[wr], s44 offen offset:1024 nt lds\n\t" \
    "buffer_load_dwordx4 v242, %[wr], s44 offen offset:2048 nt lds\n\t" \
    "ds_read_b64_tr_b16 v[96:97], v244 offset:0\n\t" \
    "ds_read_b64_tr_b16 v[98:99], v244 offset:1536\n\t" \
    "ds_read_b64_tr_b16 v[104:105], v244 offset:32\n\t" \
    "ds_read_b64_tr_b16 v[106:107], v244 offset:1568\n\t" \
    "ds_read_b64_tr_b16 v[112:113], v244 offset:64\n\t" \
    "ds_read_b64_tr_b16 v[114:115], v244 offset:1600\n\t" \
    "v_add_u32 v244, 0x6000, v243\n\t" \
    "s_waitcnt vmcnt(12) lgkmcnt(0)\n\t" \
    "s_barrier \n\t" \
    "ds_read_b64_tr_b16 v[100:101], v244 offset:0\n\t" \
    "ds_read_b64_tr_b16 v[102:103], v244 offset:1536\n\t" \
    "ds_read_b64_tr_b16 v[108:109], v244 offset:32\n\t" \
    "ds_read_b64_tr_b16 v[110:111], v244 offset:1568\n\t" \
    "ds_read_b64_tr_b16 v[116:117], v244 offset:64\n\t" \
    "ds_read_b64_tr_b16 v[118:119], v244 offset:1600\n\t" \
    "v_add_u32 v244, 0xc000, v243\n\t" \
    "s_waitcnt vmcnt(9) lgkmcnt(0)\n\t" \
    "s_barrier \n\t" \
    "ds_read_b64_tr_b16 v[136:137], v244 offset:0\n\t" \
    "ds_read_b64_tr_b16 v[138:139], v244 offset:1536\n\t" \
    "ds_read_b64_tr_b16 v[144:145], v244 offset:32\n\t" \
    "ds_read_b64_tr_b16 v[146:147], v244 offset:1568\n\t" \
    "ds_read_b64_tr_b16 v[152:153], v244 offset:64\n\t" \
    "ds_read_b64_tr_b16 v[154:155], v244 offset:1600\n\t" \
    "v_add_u32 v244, 0x12000, v243\n\t" \
    "s_waitcnt vmcnt(6) lgkmcnt(0)\n\t" \
    "s_barrier \n\t" \
    "ds_read_b64_tr_b16 v[140:141], v244 offset:0\n\t" \
    "ds_read_b64_tr_b16 v[142:143], v244 offset:1536\n\t" \
    "ds_read_b64_tr_b16 v[148:149], v244 offset:32\n\t" \
    "ds_read_b64_tr_b16 v[150:151], v244 offset:1568\n\t" \
    "ds_read_b64_tr_b16 v[156:157], v244 offset:64\n\t" \
    "ds_read_b64_tr_b16 v[158:159], v244 offset:1600\n\t" \
    "v_add_u32 v244, 0x18000, v243\n\t" \
    "s_waitcnt vmcnt(3) lgkmcnt(0)\n\t" \
    "s_barrier \n\t" \
    "ds_read_b64_tr_b16 v[176:177], v244 offset:0\n\t" \
    "ds_read_b64_tr_b16 v[178:179], v244 offset:1536\n\t" \
    "ds_read_b64_tr_b16 v[184:185], v244 offset:32\n\t" \
    "ds_read_b64_tr_b16 v[186:187], v244 offset:1568\n\t" \
    "ds_read_b64_tr_b16 v[192:193], v244 offset:64\n\t" \
    "ds_read_b64_tr_b16 v[194:195], v244 offset:1600\n\t" \
    "v_add_u32 v244, 0x1e000, v243\n\t" \
    "s_waitcnt vmcnt(0) lgkmcnt(0)\n\t" \
    "s_barrier \n\t" \
    "ds_read_b64_tr_b16 v[180:181], v244 offset:0\n\t" \
    "ds_read_b64_tr_b16 v[182:183], v244 offset:1536\n\t" \
    "ds_read_b64_tr_b16 v[188:189], v244 offset:32\n\t" \
    "ds_read_b64_tr_b16 v[190:191], v244 offset:1568\n\t" \
    "ds_read_b64_tr_b16 v[196:197], v244 offset:64\n\t" \
    "ds_read_b64_tr_b16 v[198:199], v244 offset:1600\n\t" \
    "s_waitcnt lgkmcnt(0)\n\t" \
    "s_barrier \n\t" \
    "ds_write_b128 v254, v[200:203] offset:30720\n\t" \
    "ds_write_b128 v254, v[204:207] offset:31744\n\t" \
    "ds_write_b128 v254, v[208:211] offset:32768\n\t" \
    "ds_write_b128 v254, v[212:215] offset:33792\n\t" \
    "ds_write_b128 v254, v[216:219] offset:34816\n\t" \
    "ds_write_b128 v254, v[220:223] offset:35840\n\t" \
    "ds_write_b128 v254, v[224:227] offset:36864\n\t" \
    "ds_write_b128 v254, v[228:231] offset:37888\n\t" \
    "ds_write_b128 v254, v[232:235] offset:38912\n\t" \
    "ds_write_b128 v254, v[236:239] offset:39936\n\t" \
    "s_waitcnt vmcnt(0) lgkmcnt(0)\n\t" \
    "s_barrier \n\t" \
    "ds_read_b128 v[224:227], v253 offset:0\n\t" \
    "ds_read_b128 v[228:231], v253 offset:20480\n\t" \
    "ds_read_b128 v[232:235], v253 offset:1024\n\t" \
    "ds_read_b128 v[236:239], v253 offset:21504\n\t" \
    ".p2align 8\n\t" \
    ".Ls03_%=:\n\t" \
    "s_waitcnt lgkmcnt(3)\n\t" \
    "v_mfma_f32_16x16x32_bf16 v[204:207], v[0:3], v[224:227], 0\n\t" \
    "ds_read_b128 v[240:243], v253 offset:2048\n\t" \
    "v_mfma_f32_16x16x32_bf16 v[208:211], v[8:11], v[224:227], 0\n\t" \
    "s_waitcnt lgkmcnt(3)\n\t" \
    "v_mfma_f32_16x16x32_bf16 v[216:219], v[0:3], v[228:231], 0\n\t" \
    "ds_read_b128 v[224:227], v253 offset:22528\n\t" \
    "v_mfma_f32_16x16x32_bf16 v[220:223], v[8:11], v[228:231], 0\n\t" \
    "s_waitcnt lgkmcnt(3)\n\t" \
    "v_mfma_f32_16x16x32_bf16 v[204:207], v[4:7], v[232:235], v[204:207]\n\t" \
    "ds_read_b128 v[228:231], v253 offset:3072\n\t" \
    "v_mfma_f32_16x16x32_bf16 v[208:211], v[12:15], v[232:235], v[208:211]\n\t" \
    "s_waitcnt lgkmcnt(3)\n\t" \
    "v_mfma_f32_16x16x32_bf16 v[216:219], v[4:7], v[236:239], v[216:219]\n\t" \
    "ds_read_b128 v[232:235], v253 offset:23552\n\t" \
    "v_mfma_f32_16x16x32_bf16 v[220:223], v[12:15], v[236:239], v[220:223]\n\t" \
    "s_waitcnt lgkmcnt(3)\n\t" \
    "v_mfma_f32_16x16x32_bf16 v[204:207], v[40:43], v[240:243], v[204:207]\n\t" \
    "ds_read_b128 v[236:239], v253 offset:4096\n\t" \
    "v_mfma_f32_16x16x32_bf16 v[208:211], v[48:51], v[240:243], v[208:211]\n\t" \
    "s_waitcnt lgkmcnt(3)\n\t" \
    "v_mfma_f32_16x16x32_bf16 v[216:219], v[40:43], v[224:227], v[216:219]\n\t" \
    "ds_read_b128 v[240:243], v253 offset:24576\n\t" \
    "v_mfma_f32_16x16x32_bf16 v[220:223], v[48:51], v[224:227], v[220:223]\n\t" \
    "s_waitcnt lgkmcnt(3)\n\t" \
    "v_mfma_f32_16x16x32_bf16 v[204:207], v[44:47], v[228:231], v[204:207]\n\t" \
    "ds_read_b128 v[224:227], v253 offset:5120\n\t" \
    "v_mfma_f32_16x16x32_bf16 v[208:211], v[52:55], v[228:231], v[208:211]\n\t" \
    "s_waitcnt lgkmcnt(3)\n\t" \
    "v_mfma_f32_16x16x32_bf16 v[216:219], v[44:47], v[232:235], v[216:219]\n\t" \
    "ds_read_b128 v[228:231], v253 offset:25600\n\t" \
    "v_mfma_f32_16x16x32_bf16 v[220:223], v[52:55], v[232:235], v[220:223]\n\t" \
    "s_waitcnt lgkmcnt(3)\n\t" \
    "v_mfma_f32_16x16x32_bf16 v[204:207], v[80:83], v[236:239], v[204:207]\n\t" \
    "ds_read_b128 v[232:235], v253 offset:6144\n\t" \
    "v_mfma_f32_16x16x32_bf16 v[208:211], v[88:91], v[236:239], v[208:211]\n\t" \
    "s_waitcnt lgkmcnt(3)\n\t" \
    "v_mfma_f32_16x16x32_bf16 v[216:219], v[80:83], v[240:243], v[216:219]\n\t" \
    "ds_read_b128 v[236:239], v253 offset:26624\n\t" \
    "v_mfma_f32_16x16x32_bf16 v[220:223], v[88:91], v[240:243], v[220:223]\n\t" \
    "s_waitcnt lgkmcnt(3)\n\t" \
    "v_mfma_f32_16x16x32_bf16 v[204:207], v[84:87], v[224:227], v[204:207]\n\t" \
    "ds_read_b128 v[240:243], v253 offset:7168\n\t" \
    "v_mfma_f32_16x16x32_bf16 v[208:211], v[92:95], v[224:227], v[208:211]\n\t" \
    "s_waitcnt lgkmcnt(3)\n\t" \
    "v_mfma_f32_16x16x32_bf16 v[216:219], v[84:87], v[228:231], v[216:219]\n\t" \
    "ds_read_b128 v[224:227], v253 offset:27648\n\t" \
    "v_mfma_f32_16x16x32_bf16 v[220:223], v[92:95], v[228:231], v[220:223]\n\t" \
    "s_waitcnt lgkmcnt(3)\n\t" \
    "v_mfma_f32_16x16x32_bf16 v[204:207], v[120:123], v[232:235], v[204:207]\n\t" \
    "ds_read_b128 v[228:231], v253 offset:8192\n\t" \
    "v_mfma_f32_16x16x32_bf16 v[208:211], v[128:131], v[232:235], v[208:211]\n\t" \
    "s_waitcnt lgkmcnt(3)\n\t" \
    "v_mfma_f32_16x16x32_bf16 v[216:219], v[120:123], v[236:239], v[216:219]\n\t" \
    "ds_read_b128 v[232:235], v253 offset:28672\n\t" \
    "v_mfma_f32_16x16x32_bf16 v[220:223], v[128:131], v[236:239], v[220:223]\n\t" \
    "s_waitcnt lgkmcnt(3)\n\t" \
    "v_mfma_f32_16x16x32_bf16 v[204:207], v[124:127], v[240:243], v[204:207]\n\t" \
    "ds_read_b128 v[236:239], v253 offset:9216\n\t" \
    "v_mfma_f32_16x16x32_bf16 v[208:211], v[132:135], v[240:243], v[208:211]\n\t" \
    "s_waitcnt lgkmcnt(3)\n\t" \
    "v_mfma_f32_16x16x32_bf16 v[216:219], v[124:127], v[224:227], v[216:219]\n\t" \
    "ds_read_b128 v[240:243], v253 offset:29696\n\t" \
    "v_mfma_f32_16x16x32_bf16 v[220:223], v[132:135], v[224:227], v[220:223]\n\t" \
    "s_waitcnt lgkmcnt(3)\n\t" \
    "v_mfma_f32_16x16x32_bf16 v[204:207], v[160:163], v[228:231], v[204:207]\n\t" \
    "ds_read_b128 v[224:227], v253 offset:10240\n\t" \
    "v_mfma_f32_16x16x32_bf16 v[208:211], v[168:171], v[228:231], v[208:211]\n\t" \
    "s_waitcnt lgkmcnt(3)\n\t" \
    "v_mfma_f32_16x16x32_bf16 v[216:219], v[160:163], v[232:235], v[216:219]\n\t" \
    "ds_read_b128 v[228:231], v253 offset:30720\n\t" \
    "v_mfma_f32_16x16x32_bf16 v[220:223], v[168:171], v[232:235], v[220:223]\n\t" \
    "s_waitcnt lgkmcnt(3)\n\t" \
    "v_mfma_f32_16x16x32_bf16 v[204:207], v[164:167], v[236:239], v[204:207]\n\t" \
    "ds_read_b128 v[232:235], v253 offset:11264\n\t" \
    "v_mfma_f32_16x16x32_bf16 v[208:211], v[172:175], v[236:239], v[208:211]\n\t" \
    "s_waitcnt lgkmcnt(3)\n\t" \
    "v_mfma_f32_16x16x32_bf16 v[216:219], v[164:167], v[240:243], v[216:219]\n\t" \
    "ds_read_b128 v[236:239], v253 offset:31744\n\t" \
    "v_mfma_f32_16x16x32_bf16 v[220:223], v[172:175], v[240:243], v[220:223]\n\t" \
    "s_setprio 2\n\t" \
    "s_waitcnt lgkmcnt(0)\n\t" \
    "s_barrier \n\t" \
    "ds_read_b128 v[200:203], v253 offset:40960\n\t" \
    "ds_read_b128 v[212:215], v253 offset:41984\n\t" \
    "v_mfma_f32_16x16x32_bf16 v[204:207], v[24:27], v[224:227], v[204:207]\n\t" \
    "ds_read_b128 v[240:243], v253 offset:12288\n\t" \
    "v_mfma_f32_16x16x32_bf16 v[208:211], v[32:35], v[224:227], v[208:211]\n\t" \
    "s_waitcnt lgkmcnt(2)\n\t" \
    "v_mfma_f32_16x16x32_bf16 v[200:203], v[16:19], v[224:227], v[200:203]\n\t" \
    "v_mfma_f32_16x16x32_bf16 v[216:219], v[24:27], v[228:231], v[216:219]\n\t" \
    "ds_read_b128 v[224:227], v253 offset:32768\n\t" \
    "s_and_b32 s44, s40, 3\n\t" \
    "v_mfma_f32_16x16x32_bf16 v[220:223], v[32:35], v[228:231], v[220:223]\n\t" \
    "s_lshl_b32 s44, s44, 22\n\t" \
    "s_waitcnt lgkmcnt(2)\n\t" \
    "v_mfma_f32_16x16x32_bf16 v[212:215], v[16:19], v[228:231], v[212:215]\n\t" \
    "s_add_u32 s44, s44, %[mb]\n\t" \
    "v_mfma_f32_16x16x32_bf16 v[204:207], v[28:31], v[232:235], v[204:207]\n\t" \
    "ds_read_b128 v[228:231], v253 offset:13312\n\t" \
    "s_add_u32 s46, s44, 0x0\n\t" \
    "v_mfma_f32_16x16x32_bf16 v[208:211], v[36:39], v[232:235], v[208:211]\n\t" \
    "s_add_u32 s47, s44, 0x8400\n\t" \
    "v_mfma_f32_16x16x32_bf16 v[200:203], v[20:23], v[232:235], v[200:203]\n\t" \
    "s_add_u32 s48, s44, 0x400\n\t" \
    "v_mfma_f32_16x16x32_bf16 v[216:219], v[28:31], v[236:239], v[216:219]\n\t" \
    "ds_read_b128 v[232:235], v253 offset:33792\n\t" \
    "v_mfma_f32_16x16x32_bf16 v[220:223], v[36:39], v[236:239], v[220:223]\n\t" \
    "v_mfma_f32_16x16x32_bf16 v[212:215], v[20:23], v[236:239], v[212:215]\n\t" \
    "s_waitcnt lgkmcnt(3)\n\t" \
    "v_mfma_f32_16x16x32_bf16 v[204:207], v[64:67], v[240:243], v[204:207]\n\t" \
    "ds_read_b128 v[236:239], v253 offset:14336\n\t" \
    "v_mfma_f32_16x16x32_bf16 v[208:211], v[72:75], v[240:243], v[208:211]\n\t" \
    "v_mfma_f32_16x16x32_bf16 v[200:203], v[56:59], v[240:243], v[200:203]\n\t" \
    "s_waitcnt lgkmcnt(3)\n\t" \
    "v_mfma_f32_16x16x32_bf16 v[216:219], v[64:67], v[224:227], v[216:219]\n\t" \
    "ds_read_b128 v[240:243], v253 offset:34816\n\t" \
    "v_mfma_f32_16x16x32_bf16 v[220:223], v[72:75], v[224:227], v[220:223]\n\t" \
    "v_mfma_f32_16x16x32_bf16 v[212:215], v[56:59], v[224:227], v[212:215]\n\t" \
    "s_waitcnt lgkmcnt(3)\n\t" \
    "v_mfma_f32_16x16x32_bf16 v[204:207], v[68:71], v[228:231], v[204:207]\n\t" \
    "ds_read_b128 v[224:227], v253 offset:15360\n\t" \
    "v_mfma_f32_16x16x32_bf16 v[208:211], v[76:79], v[228:231], v[208:211]\n\t" \
    "v_mfma_f32_16x16x32_bf16 v[200:203], v[60:63], v[228:231], v[200:203]\n\t" \
    "s_waitcnt lgkmcnt(3)\n\t" \
    "v_mfma_f32_16x16x32_bf16 v[216:219], v[68:71], v[232:235], v[216:219]\n\t" \
    "ds_read_b128 v[228:231], v253 offset:35840\n\t" \
    "v_mfma_f32_16x16x32_bf16 v[220:223], v[76:79], v[232:235], v[220:223]\n\t" \
    "v_mfma_f32_16x16x32_bf16 v[212:215], v[60:63], v[232:235], v[212:215]\n\t" \
    "s_waitcnt lgkmcnt(3)\n\t" \
    "v_mfma_f32_16x16x32_bf16 v[204:207], v[104:107], v[236:239], v[204:207]\n\t" \
    "ds_read_b128 v[232:235], v253 offset:16384\n\t" \
    "v_mfma_f32_16x16x32_bf16 v[208:211], v[112:115], v[236:239], v[208:211]\n\t" \
    "v_mfma_f32_16x16x32_bf16 v[200:203], v[96:99], v[236:239], v[200:203]\n\t" \
    "s_waitcnt lgkmcnt(3)\n\t" \
    "v_mfma_f32_16x16x32_bf16 v[216:219], v[104:107], v[240:243], v[216:219]\n\t" \
    "ds_read_b128 v[236:239], v253 offset:17408\n\t" \
    "v_mfma_f32_16x16x32_bf16 v[220:223], v[112:115], v[240:243], v[220:223]\n\t" \
    "v_mfma_f32_16x16x32_bf16 v[212:215], v[96:99], v[240:243], v[212:215]\n\t" \
    "s_waitcnt lgkmcnt(3)\n\t" \
    "v_mfma_f32_16x16x32_bf16 v[204:207], v[108:111], v[224:227], v[204:207]\n\t" \
    "ds_read_b128 v[240:243], v253 offset:18432\n\t" \
    "v_mfma_f32_16x16x32_bf16 v[208:211], v[116:119], v[224:227], v[208:211]\n\t" \
    "v_mfma_f32_16x16x32_bf16 v[200:203], v[100:103], v[224:227], v[200:203]\n\t" \
    "s_waitcnt lgkmcnt(3)\n\t" \
    "v_mfma_f32_16x16x32_bf16 v[216:219], v[108:111], v[228:231], v[216:219]\n\t" \
    "ds_read_b128 v[224:227], v253 offset:19456\n\t" \
    "v_mfma_f32_16x16x32_bf16 v[220:223], v[116:119], v[228:231], v[220:223]\n\t" \
    "v_mfma_f32_16x16x32_bf16 v[212:215], v[100:103], v[228:231], v[212:215]\n\t" \
    "s_waitcnt lgkmcnt(3)\n\t" \
    "v_mfma_f32_16x16x32_bf16 v[204:207], v[144:147], v[232:235], v[204:207]\n\t" \
    "ds_read_b128 v[228:231], v253 offset:36864\n\t" \
    "v_mov_b64 v[244:245], 0\n\t" \
    "v_mfma_f32_16x16x32_bf16 v[208:211], v[152:155], v[232:235], v[208:211]\n\t" \
    "v_mov_b64 v[246:247], 0\n\t" \
    "v_mfma_f32_16x16x32_bf16 v[200:203], v[136:139], v[232:235], v[200:203]\n\t" \
    "v_mov_b64 v[248:249], 0\n\t" \
    "s_waitcnt lgkmcnt(3)\n\t" \
    "v_mfma_f32_16x16x32_bf16 v[204:207], v[148:151], v[236:239], v[204:207]\n\t" \
    "ds_read_b128 v[232:235], v253 offset:37888\n\t" \
    "v_mov_b64 v[250:251], 0\n\t" \
    "v_mfma_f32_16x16x32_bf16 v[208:211], v[156:159], v[236:239], v[208:211]\n\t" \
    "v_mfma_f32_16x16x32_bf16 v[200:203], v[140:143], v[236:239], v[200:203]\n\t" \
    "s_waitcnt lgkmcnt(3)\n\t" \
    "v_mfma_f32_16x16x32_bf16 v[204:207], v[184:187], v[240:243], v[204:207]\n\t" \
    "ds_read_b128 v[236:239], v253 offset:38912\n\t" \
    "v_mfma_f32_16x16x32_bf16 v[208:211], v[192:195], v[240:243], v[208:211]\n\t" \
    "v_mfma_f32_16x16x32_bf16 v[200:203], v[176:179], v[240:243], v[200:203]\n\t" \
    "s_waitcnt lgkmcnt(3)\n\t" \
    "v_mfma_f32_16x16x32_bf16 v[204:207], v[188:191], v[224:227], v[204:207]\n\t" \
    "ds_read_b128 v[240:243], v253 offset:39936\n\t" \
    "v_mfma_f32_16x16x32_bf16 v[208:211], v[196:199], v[224:227], v[208:211]\n\t" \
    "v_mfma_f32_16x16x32_bf16 v[200:203], v[180:183], v[224:227], v[200:203]\n\t" \
    "s_waitcnt lgkmcnt(3)\n\t" \
    "v_mfma_f32_16x16x32_bf16 v[216:219], v[144:147], v[228:231], v[216:219]\n\t" \
    "s_waitcnt lgkmcnt(2)\n\t" \
    "v_mfma_f32_16x16x32_bf16 v[216:219], v[148:151], v[232:235], v[216:219]\n\t" \
    "v_cvt_pk_bf16_f32 v204, v204, v208\n\t" \
    "v_cvt_pk_bf16_f32 v205, v205, v209\n\t" \
    "s_waitcnt lgkmcnt(1)\n\t" \
    "v_mfma_f32_16x16x32_bf16 v[216:219], v[184:187], v[236:239], v[216:219]\n\t" \
    "v_cvt_pk_bf16_f32 v206, v206, v210\n\t" \
    "v_cvt_pk_bf16_f32 v207, v207, v211\n\t" \
    "s_waitcnt lgkmcnt(0)\n\t" \
    "v_mfma_f32_16x16x32_bf16 v[216:219], v[188:191], v[240:243], v[216:219]\n\t" \
    "ds_write_b128 v254, v[204:207] offset:8192\n\t" \
    "v_mfma_f32_16x16x32_bf16 v[220:223], v[152:155], v[228:231], v[220:223]\n\t" \
    "v_mfma_f32_16x16x32_bf16 v[220:223], v[156:159], v[232:235], v[220:223]\n\t" \
    "v_cvt_pk_bf16_f32 v200, v200, v201\n\t" \
    "v_mfma_f32_16x16x32_bf16 v[220:223], v[192:195], v[236:239], v[220:223]\n\t" \
    "v_cvt_pk_bf16_f32 v201, v202, v203\n\t" \
    "ds_write_b64 v254, v[200:201] offset:6144\n\t" \
    "v_mfma_f32_16x16x32_bf16 v[220:223], v[196:199], v[240:243], v[220:223]\n\t" \
    "v_mfma_f32_16x16x32_bf16 v[212:215], v[136:139], v[228:231], v[212:215]\n\t" \
    "v_mfma_f32_16x16x32_bf16 v[212:215], v[140:143], v[232:235], v[212:215]\n\t" \
    "ds_read_b128 v[200:203], v254 offset:30720\n\t" \
    "ds_read_b128 v[204:207], v254 offset:31744\n\t" \
    "v_mfma_f32_16x16x32_bf16 v[212:215], v[176:179], v[236:239], v[212:215]\n\t" \
    "v_mov_b64 v[208:209], 0\n\t" \
    "v_mov_b64 v[210:211], 0\n\t" \
    "v_mfma_f32_16x16x32_bf16 v[212:215], v[180:183], v[240:243], v[212:215]\n\t" \
    "v_cvt_pk_bf16_f32 v216, v216, v220\n\t" \
    "v_cvt_pk_bf16_f32 v217, v217, v221\n\t" \
    "s_setprio 0\n\t" \
    "v_cvt_pk_bf16_f32 v218, v218, v222\n\t" \
    "v_cvt_pk_bf16_f32 v219, v219, v223\n\t" \
    "ds_write_b128 v254, v[216:219] offset:14336\n\t" \
    "ds_read_b128 v[216:219], v254 offset:32768\n\t" \
    "ds_read_b128 v[220:223], v254 offset:33792\n\t" \
    "s_nop 0\n\t" \
    "v_cvt_pk_bf16_f32 v212, v212, v213\n\t" \
    "v_cvt_pk_bf16_f32 v213, v214, v215\n\t" \
    "ds_write_b64 v254, v[212:213] offset:12288\n\t" \
    "s_sleep 3\n\t" \
    "s_mov_b32 m0, 0xa000\n\t" \
    "s_nop 0\n\t" \
    "buffer_load_dwordx4 v253, s[60:63], s46 offen sc1 lds\n\t" \
    "s_mov_b32 m0, 0xb400\n\t" \
    "s_nop 0\n\t" \
    "buffer_load_dwordx4 v253, s[60:63], s47 offen sc1 lds\n\t" \
    "s_mov_b32 m0, 0xa400\n\t" \
    "s_nop 0\n\t" \
    "buffer_load_dwordx4 v253, s[60:63], s48 offen sc1 lds\n\t" \
    "s_waitcnt lgkmcnt(0)\n\t" \
    "v_bfe_i32 v212, v253, 5, 1\n\t" \
    "v_and_b32 v212, 0xaaaa, v212\n\t" \
    "v_or_b32 v212, 0x4444, v212\n\t" \
    "v_and_b32 v213, 0xffff, v255\n\t" \
    "v_lshrrev_b32 v214, 16, v255\n\t" \
    ".Ls04_%=:\n\t" \
    "s_waitcnt vmcnt(0)\n\t" \
    "ds_read_b128 v[224:227], v253 offset:40960\n\t" \
    "ds_read_b128 v[228:231], v253 offset:46080\n\t" \
    "ds_read_b128 v[232:235], v253 offset:41984\n\t" \
    "s_waitcnt lgkmcnt(2)\n\t" \
    "v_max_u32 v215, v224, v225\n\t" \
    "v_max3_u32 v215, v226, v227, v215\n\t" \
    "s_waitcnt lgkmcnt(1)\n\t" \
    "v_max3_u32 v215, v228, v229, v215\n\t" \
    "v_max3_u32 v215, v230, v231, v215\n\t" \
    "s_waitcnt lgkmcnt(0)\n\t" \
    "v_max3_u32 v215, v232, v233, v215\n\t" \
    "v_max3_u32 v215, v234, v235, v215\n\t" \
    "v_cmp_eq_u32 vcc, -1, v215\n\t" \
    "s_cbranch_vccz .Ls05_%=\n\t" \
    "v_max_u32 v215, v224, v225\n\t" \
    "v_max3_u32 v215, v226, v227, v215\n\t" \
    "v_cmp_eq_u32 vcc, -1, v215\n\t" \
    "s_cbranch_vccz .Ls06_%=\n\t" \
    "s_mov_b32 m0, 0xa000\n\t" \
    "s_nop 0\n\t" \
    "buffer_load_dwordx4 v253, s[60:63], s46 offen sc1 lds\n\t" \
    ".Ls06_%=:\n\t" \
    "v_max_u32 v215, v228, v229\n\t" \
    "v_max3_u32 v215, v230, v231, v215\n\t" \
    "v_cmp_eq_u32 vcc, -1, v215\n\t" \
    "s_cbranch_vccz .Ls07_%=\n\t" \
    "s_mov_b32 m0, 0xb400\n\t" \
    "s_nop 0\n\t" \
    "buffer_load_dwordx4 v253, s[60:63], s47 offen sc1 lds\n\t" \
    ".Ls07_%=:\n\t" \
    "v_max_u32 v215, v232, v233\n\t" \
    "v_max3_u32 v215, v234, v235, v215\n\t" \
    "v_cmp_eq_u32 vcc, -1, v215\n\t" \
    "s_cbranch_vccz .Ls08_%=\n\t" \
    "s_mov_b32 m0, 0xa400\n\t" \
    "s_nop 0\n\t" \
    "buffer_load_dwordx4 v253, s[60:63], s48 offen sc1 lds\n\t" \
    ".Ls08_%=:\n\t" \
    "s_cmp_eq_u32 s41, 0\n\t" \
    "s_cbranch_scc1 .Ls05_%=\n\t" \
    "s_sub_u32 s41, s41, 1\n\t" \
    "s_branch .Ls04_%=\n\t" \
    ".Ls05_%=:\n\t" \
    "s_setprio 3\n\t" \
    "s_waitcnt lgkmcnt(0)\n\t" \
    "s_barrier \n\t" \
    "ds_read_b128 v[236:239], v253 offset:47104\n\t" \
    "ds_read_b128 v[240:243], v253 offset:43008\n\t" \
    "v_smfmac_f32_16x16x64_bf16 v[244:247], v[224:227], v[0:7], v212\n\t" \
    "v_smfmac_f32_16x16x64_bf16 v[248:251], v[224:227], v[8:15], v212\n\t" \
    "v_smfmac_f32_16x16x64_bf16 v[208:211], v[224:227], v[200:207], v212\n\t" \
    "ds_read_b128 v[200:203], v254 offset:34816\n\t" \
    "ds_read_b128 v[204:207], v254 offset:35840\n\t" \
    "ds_read_b128 v[224:227], v253 offset:48128\n\t" \
    "v_smfmac_f32_16x16x64_bf16 v[208:211], v[228:231], v[16:23], v212\n\t" \
    "v_smfmac_f32_16x16x64_bf16 v[244:247], v[228:231], v[24:31], v212\n\t" \
    "v_smfmac_f32_16x16x64_bf16 v[248:251], v[228:231], v[32:39], v212\n\t" \
    "ds_read_b128 v[228:231], v253 offset:44032\n\t" \
    "v_smfmac_f32_16x16x64_bf16 v[208:211], v[232:235], v[216:223], v212\n\t" \
    "ds_read_b128 v[216:219], v254 offset:36864\n\t" \
    "ds_read_b128 v[220:223], v254 offset:37888\n\t" \
    "v_smfmac_f32_16x16x64_bf16 v[244:247], v[232:235], v[40:47], v212\n\t" \
    "v_smfmac_f32_16x16x64_bf16 v[248:251], v[232:235], v[48:55], v212\n\t" \
    "ds_read_b128 v[232:235], v253 offset:49152\n\t" \
    "s_waitcnt lgkmcnt(8)\n\t" \
    "v_smfmac_f32_16x16x64_bf16 v[208:211], v[236:239], v[56:63], v212\n\t" \
    "v_smfmac_f32_16x16x64_bf16 v[244:247], v[236:239], v[64:71], v212\n\t" \
    "v_smfmac_f32_16x16x64_bf16 v[248:251], v[236:239], v[72:79], v212\n\t" \
    "ds_read_b128 v[236:239], v253 offset:45056\n\t" \
    "s_waitcnt lgkmcnt(6)\n\t" \
    "v_smfmac_f32_16x16x64_bf16 v[208:211], v[240:243], v[200:207], v212\n\t" \
    "ds_read_b128 v[200:203], v254 offset:38912\n\t" \
    "ds_read_b128 v[204:207], v254 offset:39936\n\t" \
    "v_smfmac_f32_16x16x64_bf16 v[244:247], v[240:243], v[80:87], v212\n\t" \
    "v_smfmac_f32_16x16x64_bf16 v[248:251], v[240:243], v[88:95], v212\n\t" \
    "ds_read_b128 v[240:243], v253 offset:50176\n\t" \
    "s_waitcnt lgkmcnt(8)\n\t" \
    "v_smfmac_f32_16x16x64_bf16 v[208:211], v[224:227], v[96:103], v212\n\t" \
    "v_smfmac_f32_16x16x64_bf16 v[244:247], v[224:227], v[104:111], v212\n\t" \
    "v_smfmac_f32_16x16x64_bf16 v[248:251], v[224:227], v[112:119], v212\n\t" \
    "s_waitcnt lgkmcnt(5)\n\t" \
    "v_smfmac_f32_16x16x64_bf16 v[208:211], v[228:231], v[216:223], v212\n\t" \
    "s_waitcnt lgkmcnt(4)\n\t" \
    "v_smfmac_f32_16x16x64_bf16 v[208:211], v[232:235], v[136:143], v212\n\t" \
    "s_waitcnt lgkmcnt(1)\n\t" \
    "v_smfmac_f32_16x16x64_bf16 v[208:211], v[236:239], v[200:207], v212\n\t" \
    "s_waitcnt lgkmcnt(0)\n\t" \
    "v_smfmac_f32_16x16x64_bf16 v[208:211], v[240:243], v[176:183], v212\n\t" \
    "v_smfmac_f32_16x16x64_bf16 v[244:247], v[228:231], v[120:127], v212\n\t" \
    "v_smfmac_f32_16x16x64_bf16 v[244:247], v[232:235], v[144:151], v212\n\t" \
    "v_smfmac_f32_16x16x64_bf16 v[244:247], v[236:239], v[160:167], v212\n\t" \
    "v_add_f32 v208, v208, v210\n\t" \
    "v_add_f32 v209, v209, v211\n\t" \
    "v_cvt_pk_bf16_f32 v208, v208, v209\n\t" \
    "v_smfmac_f32_16x16x64_bf16 v[244:247], v[240:243], v[184:191], v212\n\t" \
    "ds_write_b16 v213, v208 offset:0\n\t" \
    "ds_write_b16_d16_hi v213, v208 offset:16\n\t" \
    "v_smfmac_f32_16x16x64_bf16 v[248:251], v[228:231], v[128:135], v212\n\t" \
    "v_smfmac_f32_16x16x64_bf16 v[248:251], v[232:235], v[152:159], v212\n\t" \
    "v_smfmac_f32_16x16x64_bf16 v[248:251], v[236:239], v[168:175], v212\n\t" \
    "v_add_f32 v244, v244, v246\n\t" \
    "v_add_f32 v245, v245, v247\n\t" \
    "v_smfmac_f32_16x16x64_bf16 v[248:251], v[240:243], v[192:199], v212\n\t" \
    "s_nop 7\n\t" \
    "v_add_f32 v248, v248, v250\n\t" \
    "v_add_f32 v249, v249, v251\n\t" \
    "v_cvt_pk_bf16_f32 v244, v244, v248\n\t" \
    "v_cvt_pk_bf16_f32 v245, v245, v249\n\t" \
    "ds_write2_b32 v214, v244, v245 offset1:4\n\t" \
    "s_setprio 0\n\t" \
    "s_sleep 3\n\t" \
    "ds_read_b128 v[200:203], v254 offset:18432\n\t" \
    "ds_read_b128 v[204:207], v254 offset:19456\n\t" \
    "ds_read_b128 v[214:217], v254 offset:24576\n\t" \
    "ds_read_b128 v[218:221], v254 offset:25600\n\t" \
    "ds_read_b128 v[222:225], v254 offset:20480\n\t" \
    "ds_read_b128 v[226:229], v254 offset:21504\n\t" \
    "ds_read_b128 v[230:233], v254 offset:26624\n\t" \
    "ds_read_b128 v[234:237], v254 offset:27648\n\t" \
    "v_and_b32 v213, 0xffffff7f, v254\n\t" \
    "v_mov_b64 v[242:243], 0\n\t" \
    "v_mov_b64 v[244:245], 0\n\t" \
    "v_mov_b64 v[246:247], 0\n\t" \
    "v_mov_b64 v[248:249], 0\n\t" \
    "v_mov_b64 v[250:251], 0\n\t" \
    "v_mov_b64 v[252:253], 0\n\t" \
    "s_and_b32 s44, s40, 3\n\t" \
    "s_lshl_b32 s44, s44, 22\n\t" \
    "s_add_u32 s50, s44, %[pb]\n\t" \
    "s_xor_b32 s51, s50, 0x4000\n\t" \
    "s_xor_b32 s52, s50, 0x1000\n\t" \
    "s_xor_b32 s53, s50, 0x5000\n\t" \
    "s_setprio 3\n\t" \
    "s_waitcnt lgkmcnt(0)\n\t" \
    "s_barrier \n\t" \
    "ds_read_b128 v[208:211], v254 offset:0\n\t" \
    "ds_read_b128 v[238:241], v254 offset:1024\n\t" \
    "s_waitcnt lgkmcnt(1)\n\t" \
    "v_smfmac_f32_16x16x64_bf16 v[242:245], v[208:211], v[200:207], v212\n\t" \
    "ds_read_b128 v[200:203], v213 offset:3072\n\t" \
    "ds_read_b128 v[204:207], v213 offset:3200\n\t" \
    "v_smfmac_f32_16x16x64_bf16 v[246:249], v[208:211], v[214:221], v212\n\t" \
    "ds_read_b128 v[214:217], v213 offset:4096\n\t" \
    "ds_read_b128 v[218:221], v213 offset:4224\n\t" \
    "s_waitcnt lgkmcnt(4)\n\t" \
    "v_smfmac_f32_16x16x64_bf16 v[242:245], v[238:241], v[222:229], v212\n\t" \
    "ds_read_b128 v[222:225], v254 offset:22528\n\t" \
    "ds_read_b128 v[226:229], v254 offset:23552\n\t" \
    "v_smfmac_f32_16x16x64_bf16 v[246:249], v[238:241], v[230:237], v212\n\t" \
    "ds_read_b128 v[230:233], v254 offset:28672\n\t" \
    "ds_read_b128 v[234:237], v254 offset:29696\n\t" \
    "s_waitcnt lgkmcnt(6)\n\t" \
    "v_smfmac_f32_16x16x64_bf16 v[250:253], v[208:211], v[200:207], v212\n\t" \
    "ds_read_b128 v[208:211], v254 offset:2048\n\t" \
    "ds_read_b128 v[200:203], v213 offset:5120\n\t" \
    "ds_read_b128 v[204:207], v213 offset:5248\n\t" \
    "s_waitcnt lgkmcnt(7)\n\t" \
    "v_smfmac_f32_16x16x64_bf16 v[250:253], v[238:241], v[214:221], v212\n\t" \
    "v_lshrrev_b32 v238, 3, v254\n\t" \
    "v_and_b32 v238, 0x6e, v238\n\t" \
    "v_lshrrev_b32 v239, 16, v255\n\t" \
    "s_waitcnt lgkmcnt(2)\n\t" \
    "v_smfmac_f32_16x16x64_bf16 v[242:245], v[208:211], v[222:229], v212\n\t" \
    "v_smfmac_f32_16x16x64_bf16 v[246:249], v[208:211], v[230:237], v212\n\t" \
    "s_waitcnt lgkmcnt(0)\n\t" \
    "v_smfmac_f32_16x16x64_bf16 v[250:253], v[208:211], v[200:207], v212\n\t" \
    "s_nop 1\n\t" \
    "v_add_f32 v242, v242, v244\n\t" \
    "v_add_f32 v243, v243, v245\n\t" \
    "s_nop 0\n\t" \
    "v_add_f32 v246, v246, v248\n\t" \
    "v_add_f32 v247, v247, v249\n\t" \
    "s_nop 0\n\t" \
    "v_add_f32 v250, v250, v252\n\t" \
    "v_add_f32 v251, v251, v253\n\t" \
    "v_max3_f32 v244, v242, v246, v250\n\t" \
    "v_max3_f32 v245, v243, v247, v251\n\t" \
    "s_nop 0\n\t" \
    "v_max_f32_dpp v244, v244, v244 quad_perm:[1,0,3,2] row_mask:0xf bank_mask:0xf\n\t" \
    "v_max_f32_dpp v245, v245, v245 quad_perm:[1,0,3,2] row_mask:0xf bank_mask:0xf\n\t" \
    "s_nop 0\n\t" \
    "v_max_f32_dpp v244, v244, v244 quad_perm:[2,3,0,1] row_mask:0xf bank_mask:0xf\n\t" \
    "v_max_f32_dpp v245, v245, v245 quad_perm:[2,3,0,1] row_mask:0xf bank_mask:0xf\n\t" \
    "s_nop 0\n\t" \
    "v_max_f32_dpp v244, v244, v244 row_half_mirror row_mask:0xf bank_mask:0xf\n\t" \
    "v_max_f32_dpp v245, v245, v245 row_half_mirror row_mask:0xf bank_mask:0xf\n\t" \
    "s_nop 0\n\t" \
    "v_max_f32_dpp v244, v244, v244 row_mirror row_mask:0xf bank_mask:0xf\n\t" \
    "v_max_f32_dpp v245, v245, v245 row_mirror row_mask:0xf bank_mask:0xf\n\t" \
    "v_mul_f32 v244, %[scl], v244\n\t" \
    "v_mul_f32 v245, %[scl], v245\n\t" \
    "v_fma_f32 v242, v242, %[scl], -v244\n\t" \
    "v_fma_f32 v243, v243, %[scl], -v245\n\t" \
    "v_fma_f32 v246, v246, %[scl], -v244\n\t" \
    "v_fma_f32 v247, v247, %[scl], -v245\n\t" \
    "v_exp_f32 v242, v242\n\t" \
    "v_exp_f32 v243, v243\n\t" \
    "v_fma_f32 v250, v250, %[scl], -v244\n\t" \
    "v_fma_f32 v251, v251, %[scl], -v245\n\t" \
    "v_exp_f32 v246, v246\n\t" \
    "v_exp_f32 v247, v247\n\t" \
    "v_exp_f32 v250, v250\n\t" \
    "v_exp_f32 v251, v251\n\t" \
    "s_wakeup\n\t" \
    "v_add_f32 v244, v242, v246\n\t" \
    "v_add_f32 v245, v243, v247\n\t" \
    "v_fma_f32 v244, v250, 0.5, v244\n\t" \
    "v_fma_f32 v245, v251, 0.5, v245\n\t" \
    "s_nop 0\n\t" \
    "v_add_f32_dpp v244, v244, v244 quad_perm:[1,0,3,2] row_mask:0xf bank_mask:0xf\n\t" \
    "v_add_f32_dpp v245, v245, v245 quad_perm:[1,0,3,2] row_mask:0xf bank_mask:0xf\n\t" \
    "s_nop 0\n\t" \
    "v_add_f32_dpp v244, v244, v244 quad_perm:[2,3,0,1] row_mask:0xf bank_mask:0xf\n\t" \
    "v_add_f32_dpp v245, v245, v245 quad_perm:[2,3,0,1] row_mask:0xf bank_mask:0xf\n\t" \
    "s_nop 0\n\t" \
    "v_add_f32_dpp v244, v244, v244 row_half_mirror row_mask:0xf bank_mask:0xf\n\t" \
    "v_add_f32_dpp v245, v245, v245 row_half_mirror row_mask:0xf bank_mask:0xf\n\t" \
    "s_nop 0\n\t" \
    "v_add_f32_dpp v244, v244, v244 row_mirror row_mask:0xf bank_mask:0xf\n\t" \
    "v_add_f32_dpp v245, v245, v245 row_mirror row_mask:0xf bank_mask:0xf\n\t" \
    "v_rcp_f32 v244, v244\n\t" \
    "v_rcp_f32 v245, v245\n\t" \
    "v_mul_f32 v250, v250, v244\n\t" \
    "v_mul_f32 v251, v251, v245\n\t" \
    "v_mul_f32 v242, v242, v244\n\t" \
    "v_mul_f32 v243, v243, v245\n\t" \
    "v_mul_f32 v246, v246, v244\n\t" \
    "v_mul_f32 v247, v247, v245\n\t" \
    "v_cvt_pk_bf16_f32 v250, |v250|, |v251|\n\t" \
    "ds_write_b16 v238, v250 offset:52096\n\t" \
    "ds_write_b16_d16_hi v238, v250 offset:52112\n\t" \
    "v_cvt_pk_bf16_f32 v242, |v242|, |v246|\n\t" \
    "v_cvt_pk_bf16_f32 v243, |v243|, |v247|\n\t" \
    "ds_write2_b32 v239, v242, v243 offset1:4\n\t" \
    "ds_read_b128 v[200:203], v254 offset:896\n\t" \
    "v_and_b32 v253, 0x3ff, v254\n\t" \
    "s_mov_b32 exec_hi, 0xff\n\t" \
    "s_waitcnt lgkmcnt(0)\n\t" \
    "buffer_store_dwordx4 v[200:203], v253, s[60:63], s50 offen sc1\n\t" \
    "buffer_store_dwordx4 v[200:203], v253, s[60:63], s51 offen sc1\n\t" \
    "buffer_store_dwordx4 v[200:203], v253, s[60:63], s52 offen sc1\n\t" \
    "buffer_store_dwordx4 v[200:203], v253, s[60:63], s53 offen sc1\n\t" \
    "s_mov_b32 exec_hi, -1\n\t" \
    "s_setprio 0\n\t" \
    "ds_read_b128 v[224:227], v253 offset:0\n\t" \
    "ds_read_b128 v[228:231], v253 offset:20480\n\t" \
    "ds_read_b128 v[232:235], v253 offset:1024\n\t" \
    "ds_read_b128 v[236:239], v253 offset:21504\n\t" \
    "s_add_u32 s40, s40, 1\n\t" \
    "s_cmp_lg_u32 s40, 64\n\t" \
    "s_cbranch_scc1 .Ls03_%=\n\t" \
    "s_branch .Lend%=\n\t" \
    ".Lend%=:\n\t" \
    "s_waitcnt vmcnt(0) lgkmcnt(0)\n\t" \
    "s_mov_b32 %[bud], s41\n\t"
#define ASM_V_LOOP \
    "s_mov_b64 s[60:61], %[ws]\n\t" \
    "s_mov_b32 s62, %[sz]\n\t" \
    "s_mov_b32 s63, 0x20000\n\t" \
    "s_mov_b32 s40, 0\n\t" \
    "s_mov_b32 s41, 0x20000\n\t" \
    "s_cmp_eq_u32 %[wave], 0\n\t" \
    "s_cbranch_scc1 .Lv0_%=\n\t" \
    "s_cmp_eq_u32 %[wave], 1\n\t" \
    "s_cbranch_scc1 .Lv1_%=\n\t" \
    "s_cmp_eq_u32 %[wave], 2\n\t" \
    "s_cbranch_scc1 .Lv2_%=\n\t" \
    "s_cmp_eq_u32 %[wave], 3\n\t" \
    "s_cbranch_scc1 .Lv3_%=\n\t" \
    ".Lv4_%=:\n\t" \
    "s_barrier \n\t" \
    "s_waitcnt vmcnt(15)\n\t" \
    "ds_read_b64_tr_b16 v[200:201], v245 offset:0\n\t" \
    "ds_read_b64_tr_b16 v[16:17], v245 offset:32\n\t" \
    "ds_read_b64_tr_b16 v[208:209], v245 offset:64\n\t" \
    "ds_read_b64_tr_b16 v[56:57], v245 offset:96\n\t" \
    "ds_read_b64_tr_b16 v[216:217], v245 offset:128\n\t" \
    "ds_read_b64_tr_b16 v[96:97], v245 offset:160\n\t" \
    "ds_read_b64_tr_b16 v[224:225], v245 offset:192\n\t" \
    "ds_read_b64_tr_b16 v[136:137], v245 offset:224\n\t" \
    "ds_read_b64_tr_b16 v[232:233], v245 offset:256\n\t" \
    "ds_read_b64_tr_b16 v[176:177], v245 offset:288\n\t" \
    "s_waitcnt lgkmcnt(0)\n\t" \
    "s_add_u32 m0, %[wm], 0x0\n\t" \
    "s_mov_b32 s44, 0x14000\n\t" \
    "buffer_load_dwordx4 v240, %[wr], s44 offen offset:0 nt lds\n\t" \
    "buffer_load_dwordx4 v241, %[wr], s44 offen offset:1024 nt lds\n\t" \
    "buffer_load_dwordx4 v242, %[wr], s44 offen offset:2048 nt lds\n\t" \
    "buffer_load_dwordx4 v243, %[wr], s44 offen offset:3072 nt lds\n\t" \
    "s_add_u32 m0, %[wm], 0x1000\n\t" \
    "s_nop 0\n\t" \
    "buffer_load_dwordx4 v244, %[wr], s44 offen nt lds\n\t" \
    "s_barrier \n\t" \
    "s_barrier \n\t" \
    "s_waitcnt vmcnt(15)\n\t" \
    "ds_read_b64_tr_b16 v[204:205], v245 offset:5120\n\t" \
    "ds_read_b64_tr_b16 v[20:21], v245 offset:5152\n\t" \
    "ds_read_b64_tr_b16 v[212:213], v245 offset:5184\n\t" \
    "ds_read_b64_tr_b16 v[60:61], v245 offset:5216\n\t" \
    "ds_read_b64_tr_b16 v[220:221], v245 offset:5248\n\t" \
    "ds_read_b64_tr_b16 v[100:101], v245 offset:5280\n\t" \
    "ds_read_b64_tr_b16 v[228:229], v245 offset:5312\n\t" \
    "ds_read_b64_tr_b16 v[140:141], v245 offset:5344\n\t" \
    "ds_read_b64_tr_b16 v[236:237], v245 offset:5376\n\t" \
    "ds_read_b64_tr_b16 v[180:181], v245 offset:5408\n\t" \
    "s_waitcnt lgkmcnt(0)\n\t" \
    "s_add_u32 m0, %[wm], 0x1400\n\t" \
    "s_mov_b32 s44, 0x19000\n\t" \
    "buffer_load_dwordx4 v240, %[wr], s44 offen offset:0 nt lds\n\t" \
    "buffer_load_dwordx4 v241, %[wr], s44 offen offset:1024 nt lds\n\t" \
    "buffer_load_dwordx4 v242, %[wr], s44 offen offset:2048 nt lds\n\t" \
    "buffer_load_dwordx4 v243, %[wr], s44 offen offset:3072 nt lds\n\t" \
    "s_add_u32 m0, %[wm], 0x2400\n\t" \
    "s_nop 0\n\t" \
    "buffer_load_dwordx4 v244, %[wr], s44 offen nt lds\n\t" \
    "s_barrier \n\t" \
    "s_barrier \n\t" \
    "s_waitcnt vmcnt(15)\n\t" \
    "ds_read_b64_tr_b16 v[0:1], v245 offset:10240\n\t" \
    "ds_read_b64_tr_b16 v[24:25], v245 offset:10272\n\t" \
    "ds_read_b64_tr_b16 v[40:41], v245 offset:10304\n\t" \
    "ds_read_b64_tr_b16 v[64:65], v245 offset:10336\n\t" \
    "ds_read_b64_tr_b16 v[80:81], v245 offset:10368\n\t" \
    "ds_read_b64_tr_b16 v[104:105], v245 offset:10400\n\t" \
    "ds_read_b64_tr_b16 v[120:121], v245 offset:10432\n\t" \
    "ds_read_b64_tr_b16 v[144:145], v245 offset:10464\n\t" \
    "ds_read_b64_tr_b16 v[160:161], v245 offset:10496\n\t" \
    "ds_read_b64_tr_b16 v[184:185], v245 offset:10528\n\t" \
    "s_waitcnt lgkmcnt(0)\n\t" \
    "s_add_u32 m0, %[wm], 0x2800\n\t" \
    "s_mov_b32 s44, 0x1e000\n\t" \
    "buffer_load_dwordx4 v240, %[wr], s44 offen offset:0 nt lds\n\t" \
    "buffer_load_dwordx4 v241, %[wr], s44 offen offset:1024 nt lds\n\t" \
    "buffer_load_dwordx4 v242, %[wr], s44 offen offset:2048 nt lds\n\t" \
    "buffer_load_dwordx4 v243, %[wr], s44 offen offset:3072 nt lds\n\t" \
    "s_add_u32 m0, %[wm], 0x3800\n\t" \
    "s_nop 0\n\t" \
    "buffer_load_dwordx4 v244, %[wr], s44 offen nt lds\n\t" \
    "s_barrier \n\t" \
    "s_waitcnt vmcnt(15)\n\t" \
    "ds_read_b64_tr_b16 v[4:5], v245 offset:15360\n\t" \
    "ds_read_b64_tr_b16 v[28:29], v245 offset:15392\n\t" \
    "ds_read_b64_tr_b16 v[44:45], v245 offset:15424\n\t" \
    "ds_read_b64_tr_b16 v[68:69], v245 offset:15456\n\t" \
    "ds_read_b64_tr_b16 v[84:85], v245 offset:15488\n\t" \
    "ds_read_b64_tr_b16 v[108:109], v245 offset:15520\n\t" \
    "ds_read_b64_tr_b16 v[124:125], v245 offset:15552\n\t" \
    "ds_read_b64_tr_b16 v[148:149], v245 offset:15584\n\t" \
    "ds_read_b64_tr_b16 v[164:165], v245 offset:15616\n\t" \
    "ds_read_b64_tr_b16 v[188:189], v245 offset:15648\n\t" \
    "s_waitcnt lgkmcnt(0)\n\t" \
    "s_add_u32 m0, %[wm], 0x3c00\n\t" \
    "s_mov_b32 s44, 0x23000\n\t" \
    "buffer_load_dwordx4 v240, %[wr], s44 offen offset:0 nt lds\n\t" \
    "buffer_load_dwordx4 v241, %[wr], s44 offen offset:1024 nt lds\n\t" \
    "buffer_load_dwordx4 v242, %[wr], s44 offen offset:2048 nt lds\n\t" \
    "buffer_load_dwordx4 v243, %[wr], s44 offen offset:3072 nt lds\n\t" \
    "s_add_u32 m0, %[wm], 0x4c00\n\t" \
    "s_nop 0\n\t" \
    "buffer_load_dwordx4 v244, %[wr], s44 offen nt lds\n\t" \
    "s_barrier \n\t" \
    "s_barrier \n\t" \
    "s_waitcnt vmcnt(15)\n\t" \
    "ds_read_b64_tr_b16 v[8:9], v245 offset:0\n\t" \
    "ds_read_b64_tr_b16 v[32:33], v245 offset:32\n\t" \
    "ds_read_b64_tr_b16 v[48:49], v245 offset:64\n\t" \
    "ds_read_b64_tr_b16 v[72:73], v245 offset:96\n\t" \
    "ds_read_b64_tr_b16 v[88:89], v245 offset:128\n\t" \
    "ds_read_b64_tr_b16 v[112:113], v245 offset:160\n\t" \
    "ds_read_b64_tr_b16 v[128:129], v245 offset:192\n\t" \
    "ds_read_b64_tr_b16 v[152:153], v245 offset:224\n\t" \
    "ds_read_b64_tr_b16 v[168:169], v245 offset:256\n\t" \
    "ds_read_b64_tr_b16 v[192:193], v245 offset:288\n\t" \
    "s_waitcnt lgkmcnt(0)\n\t" \
    "s_add_u32 m0, %[wm], 0x0\n\t" \
    "s_mov_b32 s44, 0x28000\n\t" \
    "buffer_load_dwordx4 v240, %[wr], s44 offen offset:0 nt lds\n\t" \
    "buffer_load_dwordx4 v241, %[wr], s44 offen offset:1024 nt lds\n\t" \
    "buffer_load_dwordx4 v242, %[wr], s44 offen offset:2048 nt lds\n\t" \
    "buffer_load_dwordx4 v243, %[wr], s44 offen offset:3072 nt lds\n\t" \
    "s_add_u32 m0, %[wm], 0x1000\n\t" \
    "s_nop 0\n\t" \
    "buffer_load_dwordx4 v244, %[wr], s44 offen nt lds\n\t" \
    "s_barrier \n\t" \
    "s_barrier \n\t" \
    "s_waitcnt vmcnt(15)\n\t" \
    "ds_read_b64_tr_b16 v[12:13], v245 offset:5120\n\t" \
    "ds_read_b64_tr_b16 v[36:37], v245 offset:5152\n\t" \
    "ds_read_b64_tr_b16 v[52:53], v245 offset:5184\n\t" \
    "ds_read_b64_tr_b16 v[76:77], v245 offset:5216\n\t" \
    "ds_read_b64_tr_b16 v[92:93], v245 offset:5248\n\t" \
    "ds_read_b64_tr_b16 v[116:117], v245 offset:5280\n\t" \
    "ds_read_b64_tr_b16 v[132:133], v245 offset:5312\n\t" \
    "ds_read_b64_tr_b16 v[156:157], v245 offset:5344\n\t" \
    "ds_read_b64_tr_b16 v[172:173], v245 offset:5376\n\t" \
    "ds_read_b64_tr_b16 v[196:197], v245 offset:5408\n\t" \
    "s_waitcnt lgkmcnt(0)\n\t" \
    "s_add_u32 m0, %[wm], 0x1400\n\t" \
    "s_mov_b32 s44, 0x2d000\n\t" \
    "buffer_load_dwordx4 v240, %[wr], s44 offen offset:0 nt lds\n\t" \
    "buffer_load_dwordx4 v241, %[wr], s44 offen offset:1024 nt lds\n\t" \
    "buffer_load_dwordx4 v242, %[wr], s44 offen offset:2048 nt lds\n\t" \
    "buffer_load_dwordx4 v243, %[wr], s44 offen offset:3072 nt lds\n\t" \
    "s_add_u32 m0, %[wm], 0x2400\n\t" \
    "s_nop 0\n\t" \
    "buffer_load_dwordx4 v244, %[wr], s44 offen nt lds\n\t" \
    "s_barrier \n\t" \
    "s_waitcnt vmcnt(15)\n\t" \
    "ds_read_b64_tr_b16 v[202:203], v245 offset:10240\n\t" \
    "ds_read_b64_tr_b16 v[18:19], v245 offset:10272\n\t" \
    "ds_read_b64_tr_b16 v[210:211], v245 offset:10304\n\t" \
    "ds_read_b64_tr_b16 v[58:59], v245 offset:10336\n\t" \
    "ds_read_b64_tr_b16 v[218:219], v245 offset:10368\n\t" \
    "ds_read_b64_tr_b16 v[98:99], v245 offset:10400\n\t" \
    "ds_read_b64_tr_b16 v[226:227], v245 offset:10432\n\t" \
    "ds_read_b64_tr_b16 v[138:139], v245 offset:10464\n\t" \
    "ds_read_b64_tr_b16 v[234:235], v245 offset:10496\n\t" \
    "ds_read_b64_tr_b16 v[178:179], v245 offset:10528\n\t" \
    "v_mov_b32 v246, 0\n\t" \
    "buffer_load_dword v246, v246, s[60:63], %[mo] offen sc1\n\t" \
    "s_waitcnt lgkmcnt(0)\n\t" \
    "s_add_u32 m0, %[wm], 0x2800\n\t" \
    "s_mov_b32 s44, 0x32000\n\t" \
    "buffer_load_dwordx4 v240, %[wr], s44 offen offset:0 nt lds\n\t" \
    "buffer_load_dwordx4 v241, %[wr], s44 offen offset:1024 nt lds\n\t" \
    "buffer_load_dwordx4 v242, %[wr], s44 offen offset:2048 nt lds\n\t" \
    "buffer_load_dwordx4 v243, %[wr], s44 offen offset:3072 nt lds\n\t" \
    "s_add_u32 m0, %[wm], 0x3800\n\t" \
    "s_nop 0\n\t" \
    "buffer_load_dwordx4 v244, %[wr], s44 offen nt lds\n\t" \
    "s_barrier \n\t" \
    "s_barrier \n\t" \
    "s_waitcnt vmcnt(15)\n\t" \
    "ds_read_b64_tr_b16 v[206:207], v245 offset:15360\n\t" \
    "ds_read_b64_tr_b16 v[22:23], v245 offset:15392\n\t" \
    "ds_read_b64_tr_b16 v[214:215], v245 offset:15424\n\t" \
    "ds_read_b64_tr_b16 v[62:63], v245 offset:15456\n\t" \
    "ds_read_b64_tr_b16 v[222:223], v245 offset:15488\n\t" \
    "ds_read_b64_tr_b16 v[102:103], v245 offset:15520\n\t" \
    "ds_read_b64_tr_b16 v[230:231], v245 offset:15552\n\t" \
    "ds_read_b64_tr_b16 v[142:143], v245 offset:15584\n\t" \
    "ds_read_b64_tr_b16 v[238:239], v245 offset:15616\n\t" \
    "ds_read_b64_tr_b16 v[182:183], v245 offset:15648\n\t" \
    "s_waitcnt lgkmcnt(0)\n\t" \
    "s_add_u32 m0, %[wm], 0x3c00\n\t" \
    "s_mov_b32 s44, 0x37000\n\t" \
    "buffer_load_dwordx4 v240, %[wr], s44 offen offset:0 nt lds\n\t" \
    "buffer_load_dwordx4 v241, %[wr], s44 offen offset:1024 nt lds\n\t" \
    "buffer_load_dwordx4 v242, %[wr], s44 offen offset:2048 nt lds\n\t" \
    "buffer_load_dwordx4 v243, %[wr], s44 offen offset:3072 nt lds\n\t" \
    "s_add_u32 m0, %[wm], 0x4c00\n\t" \
    "s_nop 0\n\t" \
    "buffer_load_dwordx4 v244, %[wr], s44 offen nt lds\n\t" \
    "s_barrier \n\t" \
    "s_barrier \n\t" \
    "s_waitcnt vmcnt(15)\n\t" \
    "ds_read_b64_tr_b16 v[2:3], v245 offset:0\n\t" \
    "ds_read_b64_tr_b16 v[26:27], v245 offset:32\n\t" \
    "ds_read_b64_tr_b16 v[42:43], v245 offset:64\n\t" \
    "ds_read_b64_tr_b16 v[66:67], v245 offset:96\n\t" \
    "ds_read_b64_tr_b16 v[82:83], v245 offset:128\n\t" \
    "ds_read_b64_tr_b16 v[106:107], v245 offset:160\n\t" \
    "ds_read_b64_tr_b16 v[122:123], v245 offset:192\n\t" \
    "ds_read_b64_tr_b16 v[146:147], v245 offset:224\n\t" \
    "ds_read_b64_tr_b16 v[162:163], v245 offset:256\n\t" \
    "ds_read_b64_tr_b16 v[186:187], v245 offset:288\n\t" \
    "s_barrier \n\t" \
    "s_waitcnt vmcnt(10)\n\t" \
    "ds_read_b64_tr_b16 v[6:7], v245 offset:5120\n\t" \
    "ds_read_b64_tr_b16 v[30:31], v245 offset:5152\n\t" \
    "ds_read_b64_tr_b16 v[46:47], v245 offset:5184\n\t" \
    "ds_read_b64_tr_b16 v[70:71], v245 offset:5216\n\t" \
    "ds_read_b64_tr_b16 v[86:87], v245 offset:5248\n\t" \
    "ds_read_b64_tr_b16 v[110:111], v245 offset:5280\n\t" \
    "ds_read_b64_tr_b16 v[126:127], v245 offset:5312\n\t" \
    "ds_read_b64_tr_b16 v[150:151], v245 offset:5344\n\t" \
    "ds_read_b64_tr_b16 v[166:167], v245 offset:5376\n\t" \
    "ds_read_b64_tr_b16 v[190:191], v245 offset:5408\n\t" \
    "s_barrier \n\t" \
    "s_barrier \n\t" \
    "s_waitcnt vmcnt(5)\n\t" \
    "ds_read_b64_tr_b16 v[10:11], v245 offset:10240\n\t" \
    "ds_read_b64_tr_b16 v[34:35], v245 offset:10272\n\t" \
    "ds_read_b64_tr_b16 v[50:51], v245 offset:10304\n\t" \
    "ds_read_b64_tr_b16 v[74:75], v245 offset:10336\n\t" \
    "ds_read_b64_tr_b16 v[90:91], v245 offset:10368\n\t" \
    "ds_read_b64_tr_b16 v[114:115], v245 offset:10400\n\t" \
    "ds_read_b64_tr_b16 v[130:131], v245 offset:10432\n\t" \
    "ds_read_b64_tr_b16 v[154:155], v245 offset:10464\n\t" \
    "ds_read_b64_tr_b16 v[170:171], v245 offset:10496\n\t" \
    "ds_read_b64_tr_b16 v[194:195], v245 offset:10528\n\t" \
    ".Lv41_%=:\n\t" \
    "v_readfirstlane_b32 s43, v246\n\t" \
    "s_nop 0\n\t" \
    "s_cmp_lg_u32 s43, -1\n\t" \
    "s_cbranch_scc1 .Lv42_%=\n\t" \
    "s_cmp_eq_u32 s41, 0\n\t" \
    "s_cbranch_scc1 .Lv42_%=\n\t" \
    "s_sub_u32 s41, s41, 1\n\t" \
    "s_sleep 4\n\t" \
    "v_mov_b32 v246, 0\n\t" \
    "buffer_load_dword v246, v246, s[60:63], %[mo] offen sc1\n\t" \
    "s_waitcnt vmcnt(0)\n\t" \
    "s_branch .Lv41_%=\n\t" \
    ".Lv42_%=:\n\t" \
    "v_mov_b32 v246, 0x8000\n\t" \
    "s_mov_b64 exec, 1\n\t" \
    "buffer_store_dword v246, v246, s[60:63], %[mo] offen sc1\n\t" \
    "s_mov_b64 exec, -1\n\t" \
    "s_and_b32 s44, s43, 0x5000\n\t" \
    "s_xor_b32 %[mb], %[mb], s44\n\t" \
    "s_xor_b32 %[fl], %[fl], s44\n\t" \
    "s_sub_u32 %[fl], %[fl], 0xc800\n\t" \
    "s_xor_b32 %[pb], %[pb], s44\n\t" \
    "s_and_b32 s43, s43, 0x2000\n\t" \
    "s_xor_b32 s43, s43, %[lx]\n\t" \
    "s_add_u32 s60, s60, s43\n\t" \
    "s_addc_u32 s61, s61, 0\n\t" \
    "s_barrier \n\t" \
    "s_barrier \n\t" \
    "s_waitcnt vmcnt(0)\n\t" \
    "ds_read_b64_tr_b16 v[14:15], v245 offset:15360\n\t" \
    "ds_read_b64_tr_b16 v[38:39], v245 offset:15392\n\t" \
    "ds_read_b64_tr_b16 v[54:55], v245 offset:15424\n\t" \
    "ds_read_b64_tr_b16 v[78:79], v245 offset:15456\n\t" \
    "ds_read_b64_tr_b16 v[94:95], v245 offset:15488\n\t" \
    "ds_read_b64_tr_b16 v[118:119], v245 offset:15520\n\t" \
    "ds_read_b64_tr_b16 v[134:135], v245 offset:15552\n\t" \
    "ds_read_b64_tr_b16 v[158:159], v245 offset:15584\n\t" \
    "ds_read_b64_tr_b16 v[174:175], v245 offset:15616\n\t" \
    "ds_read_b64_tr_b16 v[198:199], v245 offset:15648\n\t" \
    "s_waitcnt lgkmcnt(0)\n\t" \
    "s_barrier \n\t" \
    "ds_write_b128 v254, v[200:203] offset:0\n\t" \
    "ds_write_b128 v254, v[204:207] offset:1024\n\t" \
    "ds_write_b128 v254, v[208:211] offset:2048\n\t" \
    "ds_write_b128 v254, v[212:215] offset:3072\n\t" \
    "ds_write_b128 v254, v[216:219] offset:4096\n\t" \
    "ds_write_b128 v254, v[220:223] offset:5120\n\t" \
    "ds_write_b128 v254, v[224:227] offset:6144\n\t" \
    "ds_write_b128 v254, v[228:231] offset:7168\n\t" \
    "ds_write_b128 v254, v[232:235] offset:8192\n\t" \
    "ds_write_b128 v254, v[236:239] offset:9216\n\t" \
    "s_waitcnt vmcnt(0) lgkmcnt(0)\n\t" \
    "s_barrier \n\t" \
    "v_mov_b64 v[200:201], 0\n\t" \
    "v_mov_b64 v[202:203], 0\n\t" \
    "ds_write_b128 v253, v[200:203] offset:3072\n\t" \
    "ds_write_b128 v253, v[200:203] offset:4096\n\t" \
    "v_bfe_i32 v252, v253, 5, 1\n\t" \
    "v_and_b32 v252, 0xaaaa, v252\n\t" \
    "v_or_b32 v252, 0x4444, v252\n\t" \
    ".p2align 8\n\t" \
    ".Lv43_%=:\n\t" \
    "ds_read_b128 v[248:251], v254 offset:0\n\t" \
    "s_and_b32 s44, s40, 3\n\t" \
    "s_lshl_b32 s44, s44, 22\n\t" \
    "s_add_u32 s54, s44, %[mb]\n\t" \
    "s_xor_b32 s55, s54, 0x4000\n\t" \
    "s_xor_b32 s56, s54, 0x1000\n\t" \
    "s_xor_b32 s57, s54, 0x5000\n\t" \
    "s_waitcnt lgkmcnt(0)\n\t" \
    "s_barrier \n\t" \
    "ds_read_b128 v[224:227], v253 offset:6144\n\t" \
    "ds_read_b128 v[232:235], v253 offset:8192\n\t" \
    "ds_read_b128 v[236:239], v253 offset:9216\n\t" \
    "ds_read_b128 v[240:243], v253 offset:10240\n\t" \
    "ds_read_b128 v[244:247], v253 offset:11264\n\t" \
    "ds_read_b128 v[228:231], v253 offset:7168\n\t" \
    "s_waitcnt lgkmcnt(5)\n\t" \
    "v_mfma_f32_16x16x32_bf16 v[200:203], v[224:227], v[16:19], 0\n\t" \
    "s_waitcnt lgkmcnt(4)\n\t" \
    "v_mfma_f32_16x16x32_bf16 v[200:203], v[232:235], v[24:27], v[200:203]\n\t" \
    "s_waitcnt lgkmcnt(3)\n\t" \
    "v_mfma_f32_16x16x32_bf16 v[200:203], v[236:239], v[28:31], v[200:203]\n\t" \
    "s_waitcnt lgkmcnt(2)\n\t" \
    "v_mfma_f32_16x16x32_bf16 v[200:203], v[240:243], v[32:35], v[200:203]\n\t" \
    "s_waitcnt lgkmcnt(1)\n\t" \
    "v_mfma_f32_16x16x32_bf16 v[200:203], v[244:247], v[36:39], v[200:203]\n\t" \
    "s_waitcnt lgkmcnt(0)\n\t" \
    "v_mfma_f32_16x16x32_bf16 v[200:203], v[228:231], v[20:23], v[200:203]\n\t" \
    "v_mfma_f32_16x16x32_bf16 v[204:207], v[224:227], v[248:251], 0\n\t" \
    "ds_read_b128 v[248:251], v254 offset:1024\n\t" \
    "v_mfma_f32_16x16x32_bf16 v[204:207], v[232:235], v[0:3], v[204:207]\n\t" \
    "v_mfma_f32_16x16x32_bf16 v[204:207], v[236:239], v[4:7], v[204:207]\n\t" \
    "v_mfma_f32_16x16x32_bf16 v[204:207], v[240:243], v[8:11], v[204:207]\n\t" \
    "v_mfma_f32_16x16x32_bf16 v[204:207], v[244:247], v[12:15], v[204:207]\n\t" \
    "s_waitcnt lgkmcnt(0)\n\t" \
    "v_mfma_f32_16x16x32_bf16 v[204:207], v[228:231], v[248:251], v[204:207]\n\t" \
    "ds_read_b128 v[248:251], v254 offset:2048\n\t" \
    "s_barrier \n\t" \
    "v_mfma_f32_16x16x32_bf16 v[208:211], v[224:227], v[56:59], 0\n\t" \
    "v_mfma_f32_16x16x32_bf16 v[208:211], v[232:235], v[64:67], v[208:211]\n\t" \
    "v_mfma_f32_16x16x32_bf16 v[208:211], v[236:239], v[68:71], v[208:211]\n\t" \
    "v_mfma_f32_16x16x32_bf16 v[208:211], v[240:243], v[72:75], v[208:211]\n\t" \
    "v_mfma_f32_16x16x32_bf16 v[208:211], v[244:247], v[76:79], v[208:211]\n\t" \
    "v_mfma_f32_16x16x32_bf16 v[208:211], v[228:231], v[60:63], v[208:211]\n\t" \
    "s_waitcnt lgkmcnt(0)\n\t" \
    "v_mfma_f32_16x16x32_bf16 v[212:215], v[224:227], v[248:251], 0\n\t" \
    "ds_read_b128 v[248:251], v254 offset:3072\n\t" \
    "ds_read_b128 v[224:227], v253 offset:12288\n\t" \
    "v_mfma_f32_16x16x32_bf16 v[212:215], v[232:235], v[40:43], v[212:215]\n\t" \
    "ds_read_b128 v[232:235], v253 offset:14336\n\t" \
    "v_mfma_f32_16x16x32_bf16 v[212:215], v[236:239], v[44:47], v[212:215]\n\t" \
    "ds_read_b128 v[236:239], v253 offset:15360\n\t" \
    "v_mfma_f32_16x16x32_bf16 v[212:215], v[240:243], v[48:51], v[212:215]\n\t" \
    "ds_read_b128 v[240:243], v253 offset:16384\n\t" \
    "v_mfma_f32_16x16x32_bf16 v[212:215], v[244:247], v[52:55], v[212:215]\n\t" \
    "ds_read_b128 v[244:247], v253 offset:17408\n\t" \
    "s_waitcnt lgkmcnt(5)\n\t" \
    "v_mfma_f32_16x16x32_bf16 v[212:215], v[228:231], v[248:251], v[212:215]\n\t" \
    "ds_read_b128 v[248:251], v254 offset:4096\n\t" \
    "ds_read_b128 v[228:231], v253 offset:13312\n\t" \
    "s_waitcnt lgkmcnt(6)\n\t" \
    "v_mfma_f32_16x16x32_bf16 v[216:219], v[224:227], v[16:19], 0\n\t" \
    "s_waitcnt lgkmcnt(5)\n\t" \
    "v_mfma_f32_16x16x32_bf16 v[216:219], v[232:235], v[24:27], v[216:219]\n\t" \
    "s_waitcnt lgkmcnt(4)\n\t" \
    "v_mfma_f32_16x16x32_bf16 v[216:219], v[236:239], v[28:31], v[216:219]\n\t" \
    "s_waitcnt lgkmcnt(3)\n\t" \
    "v_mfma_f32_16x16x32_bf16 v[216:219], v[240:243], v[32:35], v[216:219]\n\t" \
    "s_waitcnt lgkmcnt(2)\n\t" \
    "v_mfma_f32_16x16x32_bf16 v[216:219], v[244:247], v[36:39], v[216:219]\n\t" \
    "s_waitcnt lgkmcnt(0)\n\t" \
    "v_mfma_f32_16x16x32_bf16 v[216:219], v[228:231], v[20:23], v[216:219]\n\t" \
    "v_mfma_f32_16x16x32_bf16 v[220:223], v[224:227], v[248:251], 0\n\t" \
    "ds_read_b128 v[248:251], v254 offset:5120\n\t" \
    "v_mfma_f32_16x16x32_bf16 v[220:223], v[232:235], v[80:83], v[220:223]\n\t" \
    "v_mfma_f32_16x16x32_bf16 v[220:223], v[236:239], v[84:87], v[220:223]\n\t" \
    "v_mfma_f32_16x16x32_bf16 v[220:223], v[240:243], v[88:91], v[220:223]\n\t" \
    "v_cvt_pk_bf16_f32 v200, v200, v216\n\t" \
    "v_cvt_pk_bf16_f32 v201, v201, v217\n\t" \
    "v_mfma_f32_16x16x32_bf16 v[220:223], v[244:247], v[92:95], v[220:223]\n\t" \
    "v_cvt_pk_bf16_f32 v202, v202, v218\n\t" \
    "v_cvt_pk_bf16_f32 v203, v203, v219\n\t" \
    "s_waitcnt lgkmcnt(0)\n\t" \
    "v_mfma_f32_16x16x32_bf16 v[220:223], v[228:231], v[248:251], v[220:223]\n\t" \
    "ds_read_b128 v[248:251], v254 offset:0\n\t" \
    "s_add_u32 s44, s54, 0x1033800\n\t" \
    "v_mfma_f32_16x16x32_bf16 v[216:219], v[224:227], v[56:59], 0\n\t" \
    "buffer_store_dwordx4 v[200:203], v253, s[60:63], s44 offen sc1\n\t" \
    "v_mfma_f32_16x16x32_bf16 v[216:219], v[232:235], v[64:67], v[216:219]\n\t" \
    "v_mfma_f32_16x16x32_bf16 v[216:219], v[236:239], v[68:71], v[216:219]\n\t" \
    "v_mfma_f32_16x16x32_bf16 v[216:219], v[240:243], v[72:75], v[216:219]\n\t" \
    "v_mfma_f32_16x16x32_bf16 v[216:219], v[244:247], v[76:79], v[216:219]\n\t" \
    "v_mfma_f32_16x16x32_bf16 v[216:219], v[228:231], v[60:63], v[216:219]\n\t" \
    "s_waitcnt lgkmcnt(0)\n\t" \
    "v_mfma_f32_16x16x32_bf16 v[200:203], v[224:227], v[248:251], 0\n\t" \
    "ds_read_b128 v[248:251], v254 offset:1024\n\t" \
    "v_mfma_f32_16x16x32_bf16 v[200:203], v[232:235], v[0:3], v[200:203]\n\t" \
    "v_mfma_f32_16x16x32_bf16 v[200:203], v[236:239], v[4:7], v[200:203]\n\t" \
    "v_mfma_f32_16x16x32_bf16 v[200:203], v[240:243], v[8:11], v[200:203]\n\t" \
    "v_cvt_pk_bf16_f32 v208, v208, v216\n\t" \
    "v_cvt_pk_bf16_f32 v209, v209, v217\n\t" \
    "v_mfma_f32_16x16x32_bf16 v[200:203], v[244:247], v[12:15], v[200:203]\n\t" \
    "v_cvt_pk_bf16_f32 v210, v210, v218\n\t" \
    "v_cvt_pk_bf16_f32 v211, v211, v219\n\t" \
    "s_waitcnt lgkmcnt(0)\n\t" \
    "v_mfma_f32_16x16x32_bf16 v[200:203], v[228:231], v[248:251], v[200:203]\n\t" \
    "ds_read_b128 v[248:251], v254 offset:2048\n\t" \
    "s_add_u32 s44, s55, 0x10b3800\n\t" \
    "v_mfma_f32_16x16x32_bf16 v[216:219], v[224:227], v[96:99], 0\n\t" \
    "buffer_store_dwordx4 v[208:211], v253, s[60:63], s44 offen sc1\n\t" \
    "v_mfma_f32_16x16x32_bf16 v[216:219], v[232:235], v[104:107], v[216:219]\n\t" \
    "v_mfma_f32_16x16x32_bf16 v[216:219], v[236:239], v[108:111], v[216:219]\n\t" \
    "v_mfma_f32_16x16x32_bf16 v[216:219], v[240:243], v[112:115], v[216:219]\n\t" \
    "v_mfma_f32_16x16x32_bf16 v[216:219], v[244:247], v[116:119], v[216:219]\n\t" \
    "v_cvt_pk_bf16_f32 v204, v204, v200\n\t" \
    "v_mfma_f32_16x16x32_bf16 v[216:219], v[228:231], v[100:103], v[216:219]\n\t" \
    "v_cvt_pk_bf16_f32 v205, v205, v201\n\t" \
    "v_cvt_pk_bf16_f32 v206, v206, v202\n\t" \
    "s_waitcnt lgkmcnt(0)\n\t" \
    "v_mfma_f32_16x16x32_bf16 v[208:211], v[224:227], v[248:251], 0\n\t" \
    "ds_read_b128 v[248:251], v254 offset:3072\n\t" \
    "v_cvt_pk_bf16_f32 v207, v207, v203\n\t" \
    "v_mfma_f32_16x16x32_bf16 v[208:211], v[232:235], v[40:43], v[208:211]\n\t" \
    "v_mfma_f32_16x16x32_bf16 v[208:211], v[236:239], v[44:47], v[208:211]\n\t" \
    "s_add_u32 s44, s54, 0xff3800\n\t" \
    "v_mfma_f32_16x16x32_bf16 v[208:211], v[240:243], v[48:51], v[208:211]\n\t" \
    "buffer_store_dwordx4 v[204:207], v253, s[60:63], s44 offen sc1\n\t" \
    "v_mfma_f32_16x16x32_bf16 v[208:211], v[244:247], v[52:55], v[208:211]\n\t" \
    "s_waitcnt lgkmcnt(0)\n\t" \
    "v_mfma_f32_16x16x32_bf16 v[208:211], v[228:231], v[248:251], v[208:211]\n\t" \
    "ds_read_b128 v[248:251], v254 offset:6144\n\t" \
    "v_mfma_f32_16x16x32_bf16 v[200:203], v[224:227], v[136:139], 0\n\t" \
    "v_mfma_f32_16x16x32_bf16 v[200:203], v[232:235], v[144:147], v[200:203]\n\t" \
    "v_mfma_f32_16x16x32_bf16 v[200:203], v[236:239], v[148:151], v[200:203]\n\t" \
    "v_mfma_f32_16x16x32_bf16 v[200:203], v[240:243], v[152:155], v[200:203]\n\t" \
    "v_cvt_pk_bf16_f32 v212, v212, v208\n\t" \
    "v_cvt_pk_bf16_f32 v213, v213, v209\n\t" \
    "v_mfma_f32_16x16x32_bf16 v[200:203], v[244:247], v[156:159], v[200:203]\n\t" \
    "v_cvt_pk_bf16_f32 v214, v214, v210\n\t" \
    "v_cvt_pk_bf16_f32 v215, v215, v211\n\t" \
    "v_mfma_f32_16x16x32_bf16 v[200:203], v[228:231], v[140:143], v[200:203]\n\t" \
    "s_add_u32 s44, s55, 0x1073800\n\t" \
    "s_waitcnt lgkmcnt(0)\n\t" \
    "v_mfma_f32_16x16x32_bf16 v[204:207], v[224:227], v[248:251], 0\n\t" \
    "ds_read_b128 v[248:251], v254 offset:7168\n\t" \
    "ds_read_b128 v[224:227], v253 offset:6144\n\t" \
    "buffer_store_dwordx4 v[212:215], v253, s[60:63], s44 offen sc1\n\t" \
    "v_mfma_f32_16x16x32_bf16 v[204:207], v[232:235], v[120:123], v[204:207]\n\t" \
    "ds_read_b128 v[232:235], v253 offset:8192\n\t" \
    "v_mfma_f32_16x16x32_bf16 v[204:207], v[236:239], v[124:127], v[204:207]\n\t" \
    "ds_read_b128 v[236:239], v253 offset:9216\n\t" \
    "v_mfma_f32_16x16x32_bf16 v[204:207], v[240:243], v[128:131], v[204:207]\n\t" \
    "ds_read_b128 v[240:243], v253 offset:10240\n\t" \
    "v_mfma_f32_16x16x32_bf16 v[204:207], v[244:247], v[132:135], v[204:207]\n\t" \
    "ds_read_b128 v[244:247], v253 offset:11264\n\t" \
    "s_waitcnt lgkmcnt(5)\n\t" \
    "v_mfma_f32_16x16x32_bf16 v[204:207], v[228:231], v[248:251], v[204:207]\n\t" \
    "ds_read_b128 v[248:251], v254 offset:4096\n\t" \
    "ds_read_b128 v[228:231], v253 offset:7168\n\t" \
    "s_waitcnt lgkmcnt(6)\n\t" \
    "v_mfma_f32_16x16x32_bf16 v[208:211], v[224:227], v[96:99], 0\n\t" \
    "s_waitcnt lgkmcnt(5)\n\t" \
    "v_mfma_f32_16x16x32_bf16 v[208:211], v[232:235], v[104:107], v[208:211]\n\t" \
    "s_waitcnt lgkmcnt(4)\n\t" \
    "v_mfma_f32_16x16x32_bf16 v[208:211], v[236:239], v[108:111], v[208:211]\n\t" \
    "s_waitcnt lgkmcnt(3)\n\t" \
    "v_mfma_f32_16x16x32_bf16 v[208:211], v[240:243], v[112:115], v[208:211]\n\t" \
    "s_waitcnt lgkmcnt(2)\n\t" \
    "v_mfma_f32_16x16x32_bf16 v[208:211], v[244:247], v[116:119], v[208:211]\n\t" \
    "s_waitcnt lgkmcnt(0)\n\t" \
    "v_mfma_f32_16x16x32_bf16 v[208:211], v[228:231], v[100:103], v[208:211]\n\t" \
    "v_mfma_f32_16x16x32_bf16 v[212:215], v[224:227], v[248:251], 0\n\t" \
    "ds_read_b128 v[248:251], v254 offset:5120\n\t" \
    "v_mfma_f32_16x16x32_bf16 v[212:215], v[232:235], v[80:83], v[212:215]\n\t" \
    "v_mfma_f32_16x16x32_bf16 v[212:215], v[236:239], v[84:87], v[212:215]\n\t" \
    "v_mfma_f32_16x16x32_bf16 v[212:215], v[240:243], v[88:91], v[212:215]\n\t" \
    "v_cvt_pk_bf16_f32 v216, v208, v216\n\t" \
    "v_cvt_pk_bf16_f32 v217, v209, v217\n\t" \
    "v_mfma_f32_16x16x32_bf16 v[212:215], v[244:247], v[92:95], v[212:215]\n\t" \
    "v_cvt_pk_bf16_f32 v218, v210, v218\n\t" \
    "v_cvt_pk_bf16_f32 v219, v211, v219\n\t" \
    "s_waitcnt lgkmcnt(0)\n\t" \
    "v_mfma_f32_16x16x32_bf16 v[212:215], v[228:231], v[248:251], v[212:215]\n\t" \
    "ds_read_b128 v[248:251], v254 offset:6144\n\t" \
    "s_add_u32 s44, s56, 0x1133800\n\t" \
    "v_mfma_f32_16x16x32_bf16 v[208:211], v[224:227], v[176:179], 0\n\t" \
    "buffer_store_dwordx4 v[216:219], v253, s[60:63], s44 offen sc1\n\t" \
    "v_mfma_f32_16x16x32_bf16 v[208:211], v[232:235], v[184:187], v[208:211]\n\t" \
    "v_mfma_f32_16x16x32_bf16 v[208:211], v[236:239], v[188:191], v[208:211]\n\t" \
    "v_mfma_f32_16x16x32_bf16 v[208:211], v[240:243], v[192:195], v[208:211]\n\t" \
    "v_mfma_f32_16x16x32_bf16 v[208:211], v[244:247], v[196:199], v[208:211]\n\t" \
    "v_cvt_pk_bf16_f32 v220, v212, v220\n\t" \
    "v_mfma_f32_16x16x32_bf16 v[208:211], v[228:231], v[180:183], v[208:211]\n\t" \
    "v_cvt_pk_bf16_f32 v221, v213, v221\n\t" \
    "v_cvt_pk_bf16_f32 v222, v214, v222\n\t" \
    "s_waitcnt lgkmcnt(0)\n\t" \
    "v_mfma_f32_16x16x32_bf16 v[216:219], v[224:227], v[248:251], 0\n\t" \
    "ds_read_b128 v[248:251], v254 offset:7168\n\t" \
    "v_cvt_pk_bf16_f32 v223, v215, v223\n\t" \
    "v_mfma_f32_16x16x32_bf16 v[216:219], v[232:235], v[120:123], v[216:219]\n\t" \
    "v_mfma_f32_16x16x32_bf16 v[216:219], v[236:239], v[124:127], v[216:219]\n\t" \
    "s_add_u32 s44, s56, 0x10f3800\n\t" \
    "v_mfma_f32_16x16x32_bf16 v[216:219], v[240:243], v[128:131], v[216:219]\n\t" \
    "buffer_store_dwordx4 v[220:223], v253, s[60:63], s44 offen sc1\n\t" \
    "v_mfma_f32_16x16x32_bf16 v[216:219], v[244:247], v[132:135], v[216:219]\n\t" \
    "s_waitcnt lgkmcnt(0)\n\t" \
    "v_mfma_f32_16x16x32_bf16 v[216:219], v[228:231], v[248:251], v[216:219]\n\t" \
    "ds_read_b128 v[248:251], v254 offset:8192\n\t" \
    "v_mfma_f32_16x16x32_bf16 v[212:215], v[224:227], v[136:139], 0\n\t" \
    "v_mfma_f32_16x16x32_bf16 v[212:215], v[232:235], v[144:147], v[212:215]\n\t" \
    "v_mfma_f32_16x16x32_bf16 v[212:215], v[236:239], v[148:151], v[212:215]\n\t" \
    "v_mfma_f32_16x16x32_bf16 v[212:215], v[240:243], v[152:155], v[212:215]\n\t" \
    "v_cvt_pk_bf16_f32 v204, v216, v204\n\t" \
    "v_cvt_pk_bf16_f32 v205, v217, v205\n\t" \
    "v_mfma_f32_16x16x32_bf16 v[212:215], v[244:247], v[156:159], v[212:215]\n\t" \
    "v_cvt_pk_bf16_f32 v206, v218, v206\n\t" \
    "v_cvt_pk_bf16_f32 v207, v219, v207\n\t" \
    "v_mfma_f32_16x16x32_bf16 v[212:215], v[228:231], v[140:143], v[212:215]\n\t" \
    "s_add_u32 s44, s57, 0x1173800\n\t" \
    "s_waitcnt lgkmcnt(0)\n\t" \
    "v_mfma_f32_16x16x32_bf16 v[220:223], v[224:227], v[248:251], 0\n\t" \
    "ds_read_b128 v[248:251], v254 offset:9216\n\t" \
    "ds_read_b128 v[224:227], v253 offset:12288\n\t" \
    "buffer_store_dwordx4 v[204:207], v253, s[60:63], s44 offen sc1\n\t" \
    "v_mfma_f32_16x16x32_bf16 v[220:223], v[232:235], v[160:163], v[220:223]\n\t" \
    "ds_read_b128 v[232:235], v253 offset:14336\n\t" \
    "v_mfma_f32_16x16x32_bf16 v[220:223], v[236:239], v[164:167], v[220:223]\n\t" \
    "ds_read_b128 v[236:239], v253 offset:15360\n\t" \
    "v_mfma_f32_16x16x32_bf16 v[220:223], v[240:243], v[168:171], v[220:223]\n\t" \
    "ds_read_b128 v[240:243], v253 offset:16384\n\t" \
    "v_mfma_f32_16x16x32_bf16 v[220:223], v[244:247], v[172:175], v[220:223]\n\t" \
    "ds_read_b128 v[244:247], v253 offset:17408\n\t" \
    "v_cvt_pk_bf16_f32 v200, v212, v200\n\t" \
    "s_waitcnt lgkmcnt(5)\n\t" \
    "v_mfma_f32_16x16x32_bf16 v[220:223], v[228:231], v[248:251], v[220:223]\n\t" \
    "ds_read_b128 v[248:251], v254 offset:8192\n\t" \
    "ds_read_b128 v[228:231], v253 offset:13312\n\t" \
    "v_cvt_pk_bf16_f32 v201, v213, v201\n\t" \
    "v_cvt_pk_bf16_f32 v202, v214, v202\n\t" \
    "s_waitcnt lgkmcnt(6)\n\t" \
    "v_mfma_f32_16x16x32_bf16 v[216:219], v[224:227], v[176:179], 0\n\t" \
    "v_cvt_pk_bf16_f32 v203, v215, v203\n\t" \
    "s_waitcnt lgkmcnt(5)\n\t" \
    "v_mfma_f32_16x16x32_bf16 v[216:219], v[232:235], v[184:187], v[216:219]\n\t" \
    "s_waitcnt lgkmcnt(4)\n\t" \
    "v_mfma_f32_16x16x32_bf16 v[216:219], v[236:239], v[188:191], v[216:219]\n\t" \
    "s_add_u32 s44, s57, 0x11b3800\n\t" \
    "s_waitcnt lgkmcnt(3)\n\t" \
    "v_mfma_f32_16x16x32_bf16 v[216:219], v[240:243], v[192:195], v[216:219]\n\t" \
    "buffer_store_dwordx4 v[200:203], v253, s[60:63], s44 offen sc1\n\t" \
    "s_waitcnt lgkmcnt(2)\n\t" \
    "v_mfma_f32_16x16x32_bf16 v[216:219], v[244:247], v[196:199], v[216:219]\n\t" \
    "s_waitcnt lgkmcnt(0)\n\t" \
    "v_mfma_f32_16x16x32_bf16 v[216:219], v[228:231], v[180:183], v[216:219]\n\t" \
    "v_mfma_f32_16x16x32_bf16 v[204:207], v[224:227], v[248:251], 0\n\t" \
    "ds_read_b128 v[248:251], v254 offset:9216\n\t" \
    "v_mfma_f32_16x16x32_bf16 v[204:207], v[232:235], v[160:163], v[204:207]\n\t" \
    "v_mfma_f32_16x16x32_bf16 v[204:207], v[236:239], v[164:167], v[204:207]\n\t" \
    "v_mfma_f32_16x16x32_bf16 v[204:207], v[240:243], v[168:171], v[204:207]\n\t" \
    "v_cvt_pk_bf16_f32 v208, v208, v216\n\t" \
    "v_cvt_pk_bf16_f32 v209, v209, v217\n\t" \
    "v_mfma_f32_16x16x32_bf16 v[204:207], v[244:247], v[172:175], v[204:207]\n\t" \
    "v_cvt_pk_bf16_f32 v210, v210, v218\n\t" \
    "v_cvt_pk_bf16_f32 v211, v211, v219\n\t" \
    "s_waitcnt lgkmcnt(0)\n\t" \
    "v_mfma_f32_16x16x32_bf16 v[204:207], v[228:231], v[248:251], v[204:207]\n\t" \
    "s_add_u32 s44, s57, 0x1b3800\n\t" \
    "buffer_store_dwordx4 v[208:211], v253, s[60:63], s44 offen sc1\n\t" \
    "s_nop 6\n\t" \
    "v_cvt_pk_bf16_f32 v220, v220, v204\n\t" \
    "v_cvt_pk_bf16_f32 v221, v221, v205\n\t" \
    "v_cvt_pk_bf16_f32 v222, v222, v206\n\t" \
    "v_cvt_pk_bf16_f32 v223, v223, v207\n\t" \
    "s_add_u32 s44, s57, 0x173800\n\t" \
    "buffer_store_dwordx4 v[220:223], v253, s[60:63], s44 offen sc1\n\t" \
    "s_add_u32 s44, s40, 1\n\t" \
    "s_mov_b32 s45, s44\n\t" \
    "v_mov_b64 v[200:201], s[44:45]\n\t" \
    "v_mov_b64 v[202:203], s[44:45]\n\t" \
    "s_waitcnt lgkmcnt(0)\n\t" \
    "s_barrier \n\t" \
    "ds_read_b128 v[220:223], v253 offset:3072\n\t" \
    "ds_read_b128 v[224:227], v253 offset:3072\n\t" \
    "ds_read_b128 v[228:231], v253 offset:3072\n\t" \
    "ds_read_b128 v[232:235], v253 offset:3072\n\t" \
    "ds_read_b128 v[236:239], v253 offset:3072\n\t" \
    "ds_read_b128 v[240:243], v253 offset:3072\n\t" \
    "ds_read_b128 v[244:247], v253 offset:3072\n\t" \
    "ds_read_b128 v[248:251], v253 offset:3072\n\t" \
    "ds_read_b128 v[212:215], v254 offset:0\n\t" \
    "ds_read_b128 v[216:219], v254 offset:1024\n\t" \
    "s_and_b32 s44, s40, 3\n\t" \
    "s_lshl_b32 s44, s44, 22\n\t" \
    "s_add_u32 s42, s44, %[pb]\n\t" \
    "s_mov_b64 exec, 0xff\n\t" \
    "s_waitcnt vmcnt(0)\n\t" \
    "buffer_store_dwordx4 v[200:203], v253, s[60:63], %[fl] offen sc1\n\t" \
    "s_mov_b64 exec, -1\n\t" \
    "s_setprio 3\n\t" \
    "s_waitcnt lgkmcnt(0)\n\t" \
    "s_barrier \n\t" \
    "ds_read_b128 v[204:207], v253 offset:1024\n\t" \
    "ds_read_b128 v[208:211], v253 offset:2048\n\t" \
    "ds_read_b128 v[200:203], v253 offset:0\n\t" \
    "s_waitcnt lgkmcnt(2)\n\t" \
    "v_smfmac_f32_16x16x64_bf16 v[224:227], v[204:207], v[24:31], v252\n\t" \
    "v_smfmac_f32_16x16x64_bf16 v[220:223], v[204:207], v[0:7], v252\n\t" \
    "s_waitcnt lgkmcnt(1)\n\t" \
    "v_smfmac_f32_16x16x64_bf16 v[224:227], v[208:211], v[32:39], v252\n\t" \
    "v_smfmac_f32_16x16x64_bf16 v[220:223], v[208:211], v[8:15], v252\n\t" \
    "s_waitcnt lgkmcnt(0)\n\t" \
    "v_smfmac_f32_16x16x64_bf16 v[224:227], v[200:203], v[16:23], v252\n\t" \
    "v_smfmac_f32_16x16x64_bf16 v[220:223], v[200:203], v[212:219], v252\n\t" \
    "ds_read_b128 v[212:215], v254 offset:2048\n\t" \
    "ds_read_b128 v[216:219], v254 offset:3072\n\t" \
    "v_smfmac_f32_16x16x64_bf16 v[232:235], v[204:207], v[64:71], v252\n\t" \
    "v_smfmac_f32_16x16x64_bf16 v[228:231], v[204:207], v[40:47], v252\n\t" \
    "v_smfmac_f32_16x16x64_bf16 v[232:235], v[208:211], v[72:79], v252\n\t" \
    "v_add_f32 v220, v220, v222\n\t" \
    "v_add_f32 v221, v221, v223\n\t" \
    "v_cvt_pk_bf16_f32 v220, v220, v221\n\t" \
    "v_smfmac_f32_16x16x64_bf16 v[228:231], v[208:211], v[48:55], v252\n\t" \
    "v_min_u32 v220, -2, v220\n\t" \
    "v_add_f32 v224, v224, v226\n\t" \
    "v_add_f32 v225, v225, v227\n\t" \
    "v_smfmac_f32_16x16x64_bf16 v[232:235], v[200:203], v[56:63], v252\n\t" \
    "v_cvt_pk_bf16_f32 v221, v224, v225\n\t" \
    "v_min_u32 v221, -2, v221\n\t" \
    "s_waitcnt lgkmcnt(0)\n\t" \
    "v_smfmac_f32_16x16x64_bf16 v[228:231], v[200:203], v[212:219], v252\n\t" \
    "ds_read_b128 v[212:215], v254 offset:4096\n\t" \
    "ds_read_b128 v[216:219], v254 offset:5120\n\t" \
    "v_smfmac_f32_16x16x64_bf16 v[240:243], v[204:207], v[104:111], v252\n\t" \
    "v_smfmac_f32_16x16x64_bf16 v[236:239], v[204:207], v[80:87], v252\n\t" \
    "v_smfmac_f32_16x16x64_bf16 v[240:243], v[208:211], v[112:119], v252\n\t" \
    "v_add_f32 v228, v228, v230\n\t" \
    "v_add_f32 v229, v229, v231\n\t" \
    "v_cvt_pk_bf16_f32 v222, v228, v229\n\t" \
    "v_smfmac_f32_16x16x64_bf16 v[236:239], v[208:211], v[88:95], v252\n\t" \
    "v_min_u32 v222, -2, v222\n\t" \
    "v_add_f32 v232, v232, v234\n\t" \
    "v_add_f32 v233, v233, v235\n\t" \
    "v_smfmac_f32_16x16x64_bf16 v[240:243], v[200:203], v[96:103], v252\n\t" \
    "v_cvt_pk_bf16_f32 v223, v232, v233\n\t" \
    "v_min_u32 v223, -2, v223\n\t" \
    "v_permlane32_swap_b32 v220, v222\n\t" \
    "s_waitcnt lgkmcnt(0)\n\t" \
    "v_smfmac_f32_16x16x64_bf16 v[236:239], v[200:203], v[212:219], v252\n\t" \
    "ds_read_b128 v[212:215], v254 offset:6144\n\t" \
    "ds_read_b128 v[216:219], v254 offset:7168\n\t" \
    "s_nop 0\n\t" \
    "v_permlane32_swap_b32 v221, v223\n\t" \
    "s_nop 1\n\t" \
    "v_permlane16_swap_b32 v220, v221\n\t" \
    "v_permlane16_swap_b32 v222, v223\n\t" \
    "buffer_store_dwordx4 v[220:223], v255, s[60:63], s42 offen sc1\n\t" \
    "s_add_u32 s42, s42, 0x20000\n\t" \
    "ds_read_b128 v[224:227], v253 offset:3072\n\t" \
    "ds_read_b128 v[228:231], v253 offset:3072\n\t" \
    "v_smfmac_f32_16x16x64_bf16 v[248:251], v[204:207], v[144:151], v252\n\t" \
    "v_smfmac_f32_16x16x64_bf16 v[244:247], v[204:207], v[120:127], v252\n\t" \
    "v_smfmac_f32_16x16x64_bf16 v[248:251], v[208:211], v[152:159], v252\n\t" \
    "v_add_f32 v236, v236, v238\n\t" \
    "v_add_f32 v237, v237, v239\n\t" \
    "v_cvt_pk_bf16_f32 v236, v236, v237\n\t" \
    "v_smfmac_f32_16x16x64_bf16 v[244:247], v[208:211], v[128:135], v252\n\t" \
    "v_min_u32 v236, -2, v236\n\t" \
    "v_add_f32 v240, v240, v242\n\t" \
    "v_add_f32 v241, v241, v243\n\t" \
    "v_smfmac_f32_16x16x64_bf16 v[248:251], v[200:203], v[136:143], v252\n\t" \
    "v_cvt_pk_bf16_f32 v237, v240, v241\n\t" \
    "v_min_u32 v237, -2, v237\n\t" \
    "s_waitcnt lgkmcnt(2)\n\t" \
    "v_smfmac_f32_16x16x64_bf16 v[244:247], v[200:203], v[212:219], v252\n\t" \
    "ds_read_b128 v[212:215], v254 offset:8192\n\t" \
    "ds_read_b128 v[216:219], v254 offset:9216\n\t" \
    "s_waitcnt lgkmcnt(2)\n\t" \
    "v_smfmac_f32_16x16x64_bf16 v[228:231], v[204:207], v[184:191], v252\n\t" \
    "v_smfmac_f32_16x16x64_bf16 v[224:227], v[204:207], v[160:167], v252\n\t" \
    "v_smfmac_f32_16x16x64_bf16 v[228:231], v[208:211], v[192:199], v252\n\t" \
    "v_and_b32 v232, 0xffff, v255\n\t" \
    "v_lshrrev_b32 v233, 16, v255\n\t" \
    "v_lshl_add_u32 v232, v233, 3, v232\n\t" \
    "v_smfmac_f32_16x16x64_bf16 v[224:227], v[208:211], v[168:175], v252\n\t" \
    "v_add_f32 v244, v244, v246\n\t" \
    "v_add_f32 v245, v245, v247\n\t" \
    "v_cvt_pk_bf16_f32 v238, v244, v245\n\t" \
    "v_smfmac_f32_16x16x64_bf16 v[228:231], v[200:203], v[176:183], v252\n\t" \
    "v_min_u32 v238, -2, v238\n\t" \
    "v_add_f32 v248, v248, v250\n\t" \
    "v_add_f32 v249, v249, v251\n\t" \
    "s_waitcnt lgkmcnt(0)\n\t" \
    "v_smfmac_f32_16x16x64_bf16 v[224:227], v[200:203], v[212:219], v252\n\t" \
    "v_cvt_pk_bf16_f32 v239, v248, v249\n\t" \
    "v_min_u32 v239, -2, v239\n\t" \
    "v_permlane32_swap_b32 v236, v238\n\t" \
    "s_nop 0\n\t" \
    "v_permlane32_swap_b32 v237, v239\n\t" \
    "s_nop 1\n\t" \
    "v_permlane16_swap_b32 v236, v237\n\t" \
    "v_permlane16_swap_b32 v238, v239\n\t" \
    "buffer_store_dwordx4 v[236:239], v255, s[60:63], s42 offen sc1\n\t" \
    "s_add_u32 s42, s42, 0x20000\n\t" \
    "v_add_f32 v228, v228, v230\n\t" \
    "v_add_f32 v229, v229, v231\n\t" \
    "v_add_f32 v224, v224, v226\n\t" \
    "v_add_f32 v225, v225, v227\n\t" \
    "v_cvt_pk_bf16_f32 v229, v228, v229\n\t" \
    "v_cvt_pk_bf16_f32 v228, v224, v225\n\t" \
    "v_min_u32 v229, -2, v229\n\t" \
    "v_min_u32 v228, -2, v228\n\t" \
    "s_nop 1\n\t" \
    "v_permlane16_swap_b32 v228, v229\n\t" \
    "buffer_store_dwordx2 v[228:229], v232, s[60:63], s42 offen sc1\n\t" \
    "s_setprio 0\n\t" \
    "s_add_u32 s40, s40, 1\n\t" \
    "s_cmp_lg_u32 s40, 64\n\t" \
    "s_cbranch_scc1 .Lv43_%=\n\t" \
    "s_branch .Lend%=\n\t" \
    ".Lv3_%=:\n\t" \
    "s_mov_b32 s42, %[xs]\n\t" \
    "v_add_u32 v244, 0xc000, v243\n\t" \
    "s_waitcnt vmcnt(12) lgkmcnt(0)\n\t" \
    "s_barrier \n\t" \
    "s_add_u32 m0, %[wm], 0x9000\n\t" \
    "s_mov_b32 s44, 0x1e0000\n\t" \
    "buffer_load_dwordx4 v240, %[wr], s44 offen offset:0 nt lds\n\t" \
    "buffer_load_dwordx4 v241, %[wr], s44 offen offset:1024 nt lds\n\t" \
    "buffer_load_dwordx4 v242, %[wr], s44 offen offset:2048 nt lds\n\t" \
    "ds_read_b64_tr_b16 v[208:209], v244 offset:0\n\t" \
    "ds_read_b64_tr_b16 v[210:211], v244 offset:1536\n\t" \
    "ds_read_b64_tr_b16 v[40:41], v244 offset:32\n\t" \
    "ds_read_b64_tr_b16 v[42:43], v244 offset:1568\n\t" \
    "ds_read_b64_tr_b16 v[48:49], v244 offset:64\n\t" \
    "ds_read_b64_tr_b16 v[50:51], v244 offset:1600\n\t" \
    "v_add_u32 v244, 0xf000, v243\n\t" \
    "s_waitcnt vmcnt(12) lgkmcnt(0)\n\t" \
    "s_barrier \n\t" \
    "s_add_u32 m0, %[wm], 0xc000\n\t" \
    "s_mov_b32 s44, 0x240000\n\t" \
    "buffer_load_dwordx4 v240, %[wr], s44 offen offset:0 nt lds\n\t" \
    "buffer_load_dwordx4 v241, %[wr], s44 offen offset:1024 nt lds\n\t" \
    "buffer_load_dwordx4 v242, %[wr], s44 offen offset:2048 nt lds\n\t" \
    "ds_read_b64_tr_b16 v[212:213], v244 offset:0\n\t" \
    "ds_read_b64_tr_b16 v[214:215], v244 offset:1536\n\t" \
    "ds_read_b64_tr_b16 v[44:45], v244 offset:32\n\t" \
    "ds_read_b64_tr_b16 v[46:47], v244 offset:1568\n\t" \
    "ds_read_b64_tr_b16 v[52:53], v244 offset:64\n\t" \
    "ds_read_b64_tr_b16 v[54:55], v244 offset:1600\n\t" \
    "v_add_u32 v244, 0x0, v243\n\t" \
    "s_waitcnt vmcnt(12) lgkmcnt(0)\n\t" \
    "s_barrier \n\t" \
    "s_add_u32 m0, %[wm], 0xf000\n\t" \
    "s_mov_b32 s44, 0x2a0000\n\t" \
    "buffer_load_dwordx4 v240, %[wr], s44 offen offset:0 nt lds\n\t" \
    "buffer_load_dwordx4 v241, %[wr], s44 offen offset:1024 nt lds\n\t" \
    "buffer_load_dwordx4 v242, %[wr], s44 offen offset:2048 nt lds\n\t" \
    "ds_read_b64_tr_b16 v[56:57], v244 offset:0\n\t" \
    "ds_read_b64_tr_b16 v[58:59], v244 offset:1536\n\t" \
    "ds_read_b64_tr_b16 v[64:65], v244 offset:32\n\t" \
    "ds_read_b64_tr_b16 v[66:67], v244 offset:1568\n\t" \
    "ds_read_b64_tr_b16 v[72:73], v244 offset:64\n\t" \
    "ds_read_b64_tr_b16 v[74:75], v244 offset:1600\n\t" \
    "v_add_u32 v244, 0x3000, v243\n\t" \
    "s_waitcnt vmcnt(12) lgkmcnt(0)\n\t" \
    "s_barrier \n\t" \
    "s_add_u32 m0, %[wm], 0x0\n\t" \
    "s_mov_b32 s44, 0x300000\n\t" \
    "buffer_load_dwordx4 v240, %[wr], s44 offen offset:0 nt lds\n\t" \
    "buffer_load_dwordx4 v241, %[wr], s44 offen offset:1024 nt lds\n\t" \
    "buffer_load_dwordx4 v242, %[wr], s44 offen offset:2048 nt lds\n\t" \
    "ds_read_b64_tr_b16 v[60:61], v244 offset:0\n\t" \
    "ds_read_b64_tr_b16 v[62:63], v244 offset:1536\n\t" \
    "ds_read_b64_tr_b16 v[68:69], v244 offset:32\n\t" \
    "ds_read_b64_tr_b16 v[70:71], v244 offset:1568\n\t" \
    "ds_read_b64_tr_b16 v[76:77], v244 offset:64\n\t" \
    "ds_read_b64_tr_b16 v[78:79], v244 offset:1600\n\t" \
    "v_add_u32 v244, 0x6000, v243\n\t" \
    "s_waitcnt vmcnt(12) lgkmcnt(0)\n\t" \
    "s_barrier \n\t" \
    "s_add_u32 m0, %[wm], 0x3000\n\t" \
    "s_mov_b32 s44, 0x360000\n\t" \
    "buffer_load_dwordx4 v240, %[wr], s44 offen offset:0 nt lds\n\t" \
    "buffer_load_dwordx4 v241, %[wr], s44 offen offset:1024 nt lds\n\t" \
    "buffer_load_dwordx4 v242, %[wr], s44 offen offset:2048 nt lds\n\t" \
    "ds_read_b64_tr_b16 v[216:217], v244 offset:0\n\t" \
    "ds_read_b64_tr_b16 v[218:219], v244 offset:1536\n\t" \
    "ds_read_b64_tr_b16 v[80:81], v244 offset:32\n\t" \
    "ds_read_b64_tr_b16 v[82:83], v244 offset:1568\n\t" \
    "ds_read_b64_tr_b16 v[88:89], v244 offset:64\n\t" \
    "ds_read_b64_tr_b16 v[90:91], v244 offset:1600\n\t" \
    "v_add_u32 v244, 0x9000, v243\n\t" \
    "s_waitcnt vmcnt(12) lgkmcnt(0)\n\t" \
    "s_barrier \n\t" \
    "s_add_u32 m0, %[wm], 0x6000\n\t" \
    "s_mov_b32 s44, 0x3c0000\n\t" \
    "buffer_load_dwordx4 v240, %[wr], s44 offen offset:0 nt lds\n\t" \
    "buffer_load_dwordx4 v241, %[wr], s44 offen offset:1024 nt lds\n\t" \
    "buffer_load_dwordx4 v242, %[wr], s44 offen offset:2048 nt lds\n\t" \
    "ds_read_b64_tr_b16 v[220:221], v244 offset:0\n\t" \
    "ds_read_b64_tr_b16 v[222:223], v244 offset:1536\n\t" \
    "ds_read_b64_tr_b16 v[84:85], v244 offset:32\n\t" \
    "ds_read_b64_tr_b16 v[86:87], v244 offset:1568\n\t" \
    "ds_read_b64_tr_b16 v[92:93], v244 offset:64\n\t" \
    "ds_read_b64_tr_b16 v[94:95], v244 offset:1600\n\t" \
    "v_add_u32 v244, 0xc000, v243\n\t" \
    "s_waitcnt vmcnt(12) lgkmcnt(0)\n\t" \
    "s_barrier \n\t" \
    "s_add_u32 m0, %[wm], 0x9000\n\t" \
    "s_mov_b32 s44, 0x420000\n\t" \
    "buffer_load_dwordx4 v240, %[wr], s44 offen offset:0 nt lds\n\t" \
    "buffer_load_dwordx4 v241, %[wr], s44 offen offset:1024 nt lds\n\t" \
    "buffer_load_dwordx4 v242, %[wr], s44 offen offset:2048 nt lds\n\t" \
    "ds_read_b64_tr_b16 v[96:97], v244 offset:0\n\t" \
    "ds_read_b64_tr_b16 v[98:99], v244 offset:1536\n\t" \
    "ds_read_b64_tr_b16 v[104:105], v244 offset:32\n\t" \
    "ds_read_b64_tr_b16 v[106:107], v244 offset:1568\n\t" \
    "ds_read_b64_tr_b16 v[112:113], v244 offset:64\n\t" \
    "ds_read_b64_tr_b16 v[114:115], v244 offset:1600\n\t" \
    "v_add_u32 v244, 0xf000, v243\n\t" \
    "s_waitcnt vmcnt(12) lgkmcnt(0)\n\t" \
    "s_barrier \n\t" \
    "s_add_u32 m0, %[wm], 0xc000\n\t" \
    "s_mov_b32 s44, 0x480000\n\t" \
    "buffer_load_dwordx4 v240, %[wr], s44 offen offset:0 nt lds\n\t" \
    "buffer_load_dwordx4 v241, %[wr], s44 offen offset:1024 nt lds\n\t" \
    "buffer_load_dwordx4 v242, %[wr], s44 offen offset:2048 nt lds\n\t" \
    "ds_read_b64_tr_b16 v[100:101], v244 offset:0\n\t" \
    "ds_read_b64_tr_b16 v[102:103], v244 offset:1536\n\t" \
    "ds_read_b64_tr_b16 v[108:109], v244 offset:32\n\t" \
    "ds_read_b64_tr_b16 v[110:111], v244 offset:1568\n\t" \
    "ds_read_b64_tr_b16 v[116:117], v244 offset:64\n\t" \
    "ds_read_b64_tr_b16 v[118:119], v244 offset:1600\n\t" \
    "v_add_u32 v244, 0x0, v243\n\t" \
    "s_waitcnt vmcnt(12) lgkmcnt(0)\n\t" \
    "s_barrier \n\t" \
    "v_mov_b32 v246, 0\n\t" \
    "buffer_load_dword v246, v246, s[60:63], %[mo] offen sc1\n\t" \
    "s_add_u32 m0, %[wm], 0xf000\n\t" \
    "s_mov_b32 s44, 0x4e0000\n\t" \
    "buffer_load_dwordx4 v240, %[wr], s44 offen offset:0 nt lds\n\t" \
    "buffer_load_dwordx4 v241, %[wr], s44 offen offset:1024 nt lds\n\t" \
    "buffer_load_dwordx4 v242, %[wr], s44 offen offset:2048 nt lds\n\t" \
    "ds_read_b64_tr_b16 v[224:225], v244 offset:0\n\t" \
    "ds_read_b64_tr_b16 v[226:227], v244 offset:1536\n\t" \
    "ds_read_b64_tr_b16 v[120:121], v244 offset:32\n\t" \
    "ds_read_b64_tr_b16 v[122:123], v244 offset:1568\n\t" \
    "ds_read_b64_tr_b16 v[128:129], v244 offset:64\n\t" \
    "ds_read_b64_tr_b16 v[130:131], v244 offset:1600\n\t" \
    "v_add_u32 v244, 0x3000, v243\n\t" \
    "s_waitcnt vmcnt(12) lgkmcnt(0)\n\t" \
    "s_barrier \n\t" \
    "s_add_u32 m0, %[wm], 0x0\n\t" \
    "s_mov_b32 s44, 0x540000\n\t" \
    "buffer_load_dwordx4 v240, %[wr], s44 offen offset:0 nt lds\n\t" \
    "buffer_load_dwordx4 v241, %[wr], s44 offen offset:1024 nt lds\n\t" \
    "buffer_load_dwordx4 v242, %[wr], s44 offen offset:2048 nt lds\n\t" \
    "ds_read_b64_tr_b16 v[228:229], v244 offset:0\n\t" \
    "ds_read_b64_tr_b16 v[230:231], v244 offset:1536\n\t" \
    "ds_read_b64_tr_b16 v[124:125], v244 offset:32\n\t" \
    "ds_read_b64_tr_b16 v[126:127], v244 offset:1568\n\t" \
    "ds_read_b64_tr_b16 v[132:133], v244 offset:64\n\t" \
    "ds_read_b64_tr_b16 v[134:135], v244 offset:1600\n\t" \
    "v_add_u32 v244, 0x6000, v243\n\t" \
    "s_waitcnt vmcnt(12) lgkmcnt(0)\n\t" \
    "s_barrier \n\t" \
    "s_add_u32 m0, %[wm], 0x3000\n\t" \
    "s_mov_b32 s44, 0x5a0000\n\t" \
    "buffer_load_dwordx4 v240, %[wr], s44 offen offset:0 nt lds\n\t" \
    "buffer_load_dwordx4 v241, %[wr], s44 offen offset:1024 nt lds\n\t" \
    "buffer_load_dwordx4 v242, %[wr], s44 offen offset:2048 nt lds\n\t" \
    "ds_read_b64_tr_b16 v[136:137], v244 offset:0\n\t" \
    "ds_read_b64_tr_b16 v[138:139], v244 offset:1536\n\t" \
    "ds_read_b64_tr_b16 v[144:145], v244 offset:32\n\t" \
    "ds_read_b64_tr_b16 v[146:147], v244 offset:1568\n\t" \
    "ds_read_b64_tr_b16 v[152:153], v244 offset:64\n\t" \
    "ds_read_b64_tr_b16 v[154:155], v244 offset:1600\n\t" \
    "v_add_u32 v244, 0x9000, v243\n\t" \
    "s_waitcnt vmcnt(12) lgkmcnt(0)\n\t" \
    "s_barrier \n\t" \
    "s_add_u32 m0, %[wm], 0x6000\n\t" \
    "s_mov_b32 s44, 0x600000\n\t" \
    "buffer_load_dwordx4 v240, %[wr], s44 offen offset:0 nt lds\n\t" \
    "buffer_load_dwordx4 v241, %[wr], s44 offen offset:1024 nt lds\n\t" \
    "buffer_load_dwordx4 v242, %[wr], s44 offen offset:2048 nt lds\n\t" \
    "ds_read_b64_tr_b16 v[140:141], v244 offset:0\n\t" \
    "ds_read_b64_tr_b16 v[142:143], v244 offset:1536\n\t" \
    "ds_read_b64_tr_b16 v[148:149], v244 offset:32\n\t" \
    "ds_read_b64_tr_b16 v[150:151], v244 offset:1568\n\t" \
    "ds_read_b64_tr_b16 v[156:157], v244 offset:64\n\t" \
    "ds_read_b64_tr_b16 v[158:159], v244 offset:1600\n\t" \
    "v_add_u32 v244, 0xc000, v243\n\t" \
    "s_waitcnt vmcnt(12) lgkmcnt(0)\n\t" \
    "s_barrier \n\t" \
    "s_add_u32 m0, %[wm], 0x9000\n\t" \
    "s_mov_b32 s44, 0x660000\n\t" \
    "buffer_load_dwordx4 v240, %[wr], s44 offen offset:0 nt lds\n\t" \
    "buffer_load_dwordx4 v241, %[wr], s44 offen offset:1024 nt lds\n\t" \
    "buffer_load_dwordx4 v242, %[wr], s44 offen offset:2048 nt lds\n\t" \
    "ds_read_b64_tr_b16 v[232:233], v244 offset:0\n\t" \
    "ds_read_b64_tr_b16 v[234:235], v244 offset:1536\n\t" \
    "ds_read_b64_tr_b16 v[160:161], v244 offset:32\n\t" \
    "ds_read_b64_tr_b16 v[162:163], v244 offset:1568\n\t" \
    "ds_read_b64_tr_b16 v[168:169], v244 offset:64\n\t" \
    "ds_read_b64_tr_b16 v[170:171], v244 offset:1600\n\t" \
    "v_add_u32 v244, 0xf000, v243\n\t" \
    "s_waitcnt vmcnt(12) lgkmcnt(0)\n\t" \
    "s_barrier \n\t" \
    ".Lv31_%=:\n\t" \
    "v_readfirstlane_b32 s43, v246\n\t" \
    "s_nop 0\n\t" \
    "s_cmp_lg_u32 s43, -1\n\t" \
    "s_cbranch_scc1 .Lv32_%=\n\t" \
    "s_cmp_eq_u32 s41, 0\n\t" \
    "s_cbranch_scc1 .Lv32_%=\n\t" \
    "s_sub_u32 s41, s41, 1\n\t" \
    "s_sleep 4\n\t" \
    "v_mov_b32 v246, 0\n\t" \
    "buffer_load_dword v246, v246, s[60:63], %[mo] offen sc1\n\t" \
    "s_waitcnt vmcnt(0)\n\t" \
    "s_branch .Lv31_%=\n\t" \
    ".Lv32_%=:\n\t" \
    "v_mov_b32 v246, 0x8000\n\t" \
    "s_mov_b64 exec, 1\n\t" \
    "buffer_store_dword v246, v246, s[60:63], %[mo] offen sc1\n\t" \
    "s_mov_b64 exec, -1\n\t" \
    "s_and_b32 s44, s43, 0x5000\n\t" \
    "s_xor_b32 %[mb], %[mb], s44\n\t" \
    "s_xor_b32 %[fl], %[fl], s44\n\t" \
    "s_and_b32 s43, s43, 0x2000\n\t" \
    "s_xor_b32 s43, s43, %[lx]\n\t" \
    "s_add_u32 s60, s60, s43\n\t" \
    "s_addc_u32 s61, s61, 0\n\t" \
    "v_and_b32 v252, 0xf0, v253\n\t" \
    "v_lshlrev_b32 v252, 3, v252\n\t" \
    "buffer_load_dword v252, v252, s[60:63], %[fl] offen sc1\n\t" \
    "s_add_u32 m0, %[wm], 0xc000\n\t" \
    "s_mov_b32 s44, 0x6c0000\n\t" \
    "buffer_load_dwordx4 v240, %[wr], s44 offen offset:0 nt lds\n\t" \
    "buffer_load_dwordx4 v241, %[wr], s44 offen offset:1024 nt lds\n\t" \
    "buffer_load_dwordx4 v242, %[wr], s44 offen offset:2048 nt lds\n\t" \
    "ds_read_b64_tr_b16 v[236:237], v244 offset:0\n\t" \
    "ds_read_b64_tr_b16 v[238:239], v244 offset:1536\n\t" \
    "ds_read_b64_tr_b16 v[164:165], v244 offset:32\n\t" \
    "ds_read_b64_tr_b16 v[166:167], v244 offset:1568\n\t" \
    "ds_read_b64_tr_b16 v[172:173], v244 offset:64\n\t" \
    "ds_read_b64_tr_b16 v[174:175], v244 offset:1600\n\t" \
    "v_add_u32 v244, 0x0, v243\n\t" \
    "s_waitcnt vmcnt(12) lgkmcnt(0)\n\t" \
    "s_barrier \n\t" \
    "s_add_u32 m0, %[wm], 0xf000\n\t" \
    "s_mov_b32 s44, 0x720000\n\t" \
    "buffer_load_dwordx4 v240, %[wr], s44 offen offset:0 nt lds\n\t" \
    "buffer_load_dwordx4 v241, %[wr], s44 offen offset:1024 nt lds\n\t" \
    "buffer_load_dwordx4 v242, %[wr], s44 offen offset:2048 nt lds\n\t" \
    "ds_read_b64_tr_b16 v[176:177], v244 offset:0\n\t" \
    "ds_read_b64_tr_b16 v[178:179], v244 offset:1536\n\t" \
    "ds_read_b64_tr_b16 v[184:185], v244 offset:32\n\t" \
    "ds_read_b64_tr_b16 v[186:187], v244 offset:1568\n\t" \
    "ds_read_b64_tr_b16 v[192:193], v244 offset:64\n\t" \
    "ds_read_b64_tr_b16 v[194:195], v244 offset:1600\n\t" \
    "v_add_u32 v244, 0x3000, v243\n\t" \
    "s_waitcnt vmcnt(12) lgkmcnt(0)\n\t" \
    "s_barrier \n\t" \
    "ds_read_b64_tr_b16 v[180:181], v244 offset:0\n\t" \
    "ds_read_b64_tr_b16 v[182:183], v244 offset:1536\n\t" \
    "ds_read_b64_tr_b16 v[188:189], v244 offset:32\n\t" \
    "ds_read_b64_tr_b16 v[190:191], v244 offset:1568\n\t" \
    "ds_read_b64_tr_b16 v[196:197], v244 offset:64\n\t" \
    "ds_read_b64_tr_b16 v[198:199], v244 offset:1600\n\t" \
    "v_add_u32 v244, 0x6000, v243\n\t" \
    "s_waitcnt vmcnt(9) lgkmcnt(0)\n\t" \
    "s_barrier \n\t" \
    "ds_read_b64_tr_b16 v[200:201], v244 offset:0\n\t" \
    "ds_read_b64_tr_b16 v[202:203], v244 offset:1536\n\t" \
    "ds_read_b64_tr_b16 v[0:1], v244 offset:32\n\t" \
    "ds_read_b64_tr_b16 v[2:3], v244 offset:1568\n\t" \
    "ds_read_b64_tr_b16 v[8:9], v244 offset:64\n\t" \
    "ds_read_b64_tr_b16 v[10:11], v244 offset:1600\n\t" \
    "v_add_u32 v244, 0x9000, v243\n\t" \
    "s_waitcnt vmcnt(6) lgkmcnt(0)\n\t" \
    "s_barrier \n\t" \
    "ds_read_b64_tr_b16 v[204:205], v244 offset:0\n\t" \
    "ds_read_b64_tr_b16 v[206:207], v244 offset:1536\n\t" \
    "ds_read_b64_tr_b16 v[4:5], v244 offset:32\n\t" \
    "ds_read_b64_tr_b16 v[6:7], v244 offset:1568\n\t" \
    "ds_read_b64_tr_b16 v[12:13], v244 offset:64\n\t" \
    "ds_read_b64_tr_b16 v[14:15], v244 offset:1600\n\t" \
    "v_add_u32 v244, 0xc000, v243\n\t" \
    "s_waitcnt vmcnt(3) lgkmcnt(0)\n\t" \
    "s_barrier \n\t" \
    "ds_read_b64_tr_b16 v[16:17], v244 offset:0\n\t" \
    "ds_read_b64_tr_b16 v[18:19], v244 offset:1536\n\t" \
    "ds_read_b64_tr_b16 v[24:25], v244 offset:32\n\t" \
    "ds_read_b64_tr_b16 v[26:27], v244 offset:1568\n\t" \
    "ds_read_b64_tr_b16 v[32:33], v244 offset:64\n\t" \
    "ds_read_b64_tr_b16 v[34:35], v244 offset:1600\n\t" \
    "s_mov_b32 s43, 0\n\t" \
    "v_cmp_eq_u32 vcc, 0, v252\n\t" \
    "s_cbranch_vccnz .Lv33_%=\n\t" \
    "s_mov_b32 m0, 0x3000\n\t" \
    "s_add_u32 s49, s42, 0x0\n\t" \
    "buffer_load_dwordx4 v253, s[60:63], s49 offen offset:0 sc1 lds\n\t" \
    "buffer_load_dwordx4 v253, s[60:63], s49 offen offset:1024 sc1 lds\n\t" \
    "buffer_load_dwordx4 v253, s[60:63], s49 offen offset:2048 sc1 lds\n\t" \
    "buffer_load_dwordx4 v253, s[60:63], s49 offen offset:3072 sc1 lds\n\t" \
    "s_mov_b32 m0, 0x4c00\n\t" \
    "s_add_u32 s49, s42, 0x4c00\n\t" \
    "buffer_load_dwordx4 v253, s[60:63], s49 offen offset:0 sc1 lds\n\t" \
    "s_mov_b32 m0, 0x8000\n\t" \
    "s_add_u32 s49, s42, 0x1000\n\t" \
    "buffer_load_dwordx4 v253, s[60:63], s49 offen offset:0 sc1 lds\n\t" \
    "buffer_load_dwordx4 v253, s[60:63], s49 offen offset:1024 sc1 lds\n\t" \
    "buffer_load_dwordx4 v253, s[60:63], s49 offen offset:2048 sc1 lds\n\t" \
    "buffer_load_dwordx4 v253, s[60:63], s49 offen offset:3072 sc1 lds\n\t" \
    "s_mov_b32 m0, 0x9c00\n\t" \
    "s_add_u32 s49, s42, 0x5c00\n\t" \
    "buffer_load_dwordx4 v253, s[60:63], s49 offen offset:0 sc1 lds\n\t" \
    "s_mov_b32 s43, 1\n\t" \
    ".Lv33_%=:\n\t" \
    "v_add_u32 v244, 0xf000, v243\n\t" \
    "s_waitcnt vmcnt(0) lgkmcnt(0)\n\t" \
    "s_barrier \n\t" \
    "ds_read_b64_tr_b16 v[20:21], v244 offset:0\n\t" \
    "ds_read_b64_tr_b16 v[22:23], v244 offset:1536\n\t" \
    "ds_read_b64_tr_b16 v[28:29], v244 offset:32\n\t" \
    "ds_read_b64_tr_b16 v[30:31], v244 offset:1568\n\t" \
    "ds_read_b64_tr_b16 v[36:37], v244 offset:64\n\t" \
    "ds_read_b64_tr_b16 v[38:39], v244 offset:1600\n\t" \
    "s_waitcnt lgkmcnt(0)\n\t" \
    "s_barrier \n\t" \
    "ds_write_b128 v254, v[200:203] offset:55296\n\t" \
    "ds_write_b128 v254, v[204:207] offset:56320\n\t" \
    "ds_write_b128 v254, v[208:211] offset:57344\n\t" \
    "ds_write_b128 v254, v[212:215] offset:58368\n\t" \
    "ds_write_b128 v254, v[216:219] offset:59392\n\t" \
    "ds_write_b128 v254, v[220:223] offset:60416\n\t" \
    "ds_write_b128 v254, v[224:227] offset:61440\n\t" \
    "ds_write_b128 v254, v[228:231] offset:62464\n\t" \
    "ds_write_b128 v254, v[232:235] offset:63488\n\t" \
    "ds_write_b128 v254, v[236:239] offset:64512\n\t" \
    "s_cmp_lg_u32 s43, 0\n\t" \
    "s_cbranch_scc1 .Lv34_%=\n\t" \
    "s_mov_b32 s44, 0\n\t" \
    ".Lv35_%=:\n\t" \
    "v_cmp_ge_u32 vcc, s44, v252\n\t" \
    "s_cbranch_vccz .Lv36_%=\n\t" \
    "s_cmp_eq_u32 s41, 0\n\t" \
    "s_cbranch_scc1 .Lv36_%=\n\t" \
    "s_sub_u32 s41, s41, 1\n\t" \
    "s_sleep 8\n\t" \
    "v_and_b32 v252, 0xf0, v253\n\t" \
    "v_lshlrev_b32 v252, 3, v252\n\t" \
    "buffer_load_dword v252, v252, s[60:63], %[fl] offen sc1\n\t" \
    "s_waitcnt vmcnt(0)\n\t" \
    "s_branch .Lv35_%=\n\t" \
    ".Lv36_%=:\n\t" \
    "s_mov_b32 m0, 0x3000\n\t" \
    "s_add_u32 s49, s42, 0x0\n\t" \
    "buffer_load_dwordx4 v253, s[60:63], s49 offen offset:0 sc1 lds\n\t" \
    "buffer_load_dwordx4 v253, s[60:63], s49 offen offset:1024 sc1 lds\n\t" \
    "buffer_load_dwordx4 v253, s[60:63], s49 offen offset:2048 sc1 lds\n\t" \
    "buffer_load_dwordx4 v253, s[60:63], s49 offen offset:3072 sc1 lds\n\t" \
    "s_mov_b32 m0, 0x4c00\n\t" \
    "s_add_u32 s49, s42, 0x4c00\n\t" \
    "buffer_load_dwordx4 v253, s[60:63], s49 offen offset:0 sc1 lds\n\t" \
    "s_mov_b32 m0, 0x8000\n\t" \
    "s_add_u32 s49, s42, 0x1000\n\t" \
    "buffer_load_dwordx4 v253, s[60:63], s49 offen offset:0 sc1 lds\n\t" \
    "buffer_load_dwordx4 v253, s[60:63], s49 offen offset:1024 sc1 lds\n\t" \
    "buffer_load_dwordx4 v253, s[60:63], s49 offen offset:2048 sc1 lds\n\t" \
    "buffer_load_dwordx4 v253, s[60:63], s49 offen offset:3072 sc1 lds\n\t" \
    "s_mov_b32 m0, 0x9c00\n\t" \
    "s_add_u32 s49, s42, 0x5c00\n\t" \
    "buffer_load_dwordx4 v253, s[60:63], s49 offen offset:0 sc1 lds\n\t" \
    ".Lv34_%=:\n\t" \
    "s_waitcnt vmcnt(0) lgkmcnt(0)\n\t" \
    "s_barrier \n\t" \
    "ds_read_b128 v[224:227], v253 offset:16384\n\t" \
    "ds_read_b128 v[244:247], v254 offset:55296\n\t" \
    "ds_read_b128 v[228:231], v253 offset:17408\n\t" \
    "ds_read_b128 v[232:235], v253 offset:36864\n\t" \
    "ds_read_b128 v[236:239], v253 offset:37888\n\t" \
    "ds_read_b128 v[248:251], v254 offset:56320\n\t" \
    ".p2align 8\n\t" \
    ".Lv37_%=:\n\t" \
    "s_waitcnt lgkmcnt(5)\n\t" \
    "v_mfma_f32_16x16x32_bf16 v[204:207], v[0:3], v[224:227], 0\n\t" \
    "s_waitcnt lgkmcnt(3)\n\t" \
    "v_mfma_f32_16x16x32_bf16 v[204:207], v[4:7], v[228:231], v[204:207]\n\t" \
    "ds_read_b128 v[240:243], v253 offset:18432\n\t" \
    "v_mfma_f32_16x16x32_bf16 v[208:211], v[8:11], v[224:227], 0\n\t" \
    "v_mfma_f32_16x16x32_bf16 v[208:211], v[12:15], v[228:231], v[208:211]\n\t" \
    "v_mfma_f32_16x16x32_bf16 v[200:203], v[244:247], v[224:227], 0\n\t" \
    "s_waitcnt lgkmcnt(1)\n\t" \
    "v_mfma_f32_16x16x32_bf16 v[200:203], v[248:251], v[228:231], v[200:203]\n\t" \
    "v_mfma_f32_16x16x32_bf16 v[216:219], v[0:3], v[232:235], 0\n\t" \
    "ds_read_b128 v[224:227], v253 offset:19456\n\t" \
    "v_mfma_f32_16x16x32_bf16 v[216:219], v[4:7], v[236:239], v[216:219]\n\t" \
    "ds_read_b128 v[228:231], v253 offset:0\n\t" \
    "v_mfma_f32_16x16x32_bf16 v[220:223], v[8:11], v[232:235], 0\n\t" \
    "v_mfma_f32_16x16x32_bf16 v[220:223], v[12:15], v[236:239], v[220:223]\n\t" \
    "v_mfma_f32_16x16x32_bf16 v[212:215], v[244:247], v[232:235], 0\n\t" \
    "ds_read_b128 v[244:247], v254 offset:57344\n\t" \
    "v_mfma_f32_16x16x32_bf16 v[212:215], v[248:251], v[236:239], v[212:215]\n\t" \
    "ds_read_b128 v[248:251], v254 offset:58368\n\t" \
    "s_waitcnt lgkmcnt(4)\n\t" \
    "v_mfma_f32_16x16x32_bf16 v[204:207], v[24:27], v[240:243], v[204:207]\n\t" \
    "ds_read_b128 v[232:235], v253 offset:1024\n\t" \
    "s_waitcnt lgkmcnt(4)\n\t" \
    "v_mfma_f32_16x16x32_bf16 v[204:207], v[28:31], v[224:227], v[204:207]\n\t" \
    "ds_read_b128 v[236:239], v253 offset:20480\n\t" \
    "v_mfma_f32_16x16x32_bf16 v[208:211], v[32:35], v[240:243], v[208:211]\n\t" \
    "v_mfma_f32_16x16x32_bf16 v[208:211], v[36:39], v[224:227], v[208:211]\n\t" \
    "v_mfma_f32_16x16x32_bf16 v[200:203], v[16:19], v[240:243], v[200:203]\n\t" \
    "v_mfma_f32_16x16x32_bf16 v[200:203], v[20:23], v[224:227], v[200:203]\n\t" \
    "s_waitcnt lgkmcnt(4)\n\t" \
    "v_mfma_f32_16x16x32_bf16 v[204:207], v[40:43], v[228:231], v[204:207]\n\t" \
    "ds_read_b128 v[240:243], v253 offset:21504\n\t" \
    "s_waitcnt lgkmcnt(2)\n\t" \
    "v_mfma_f32_16x16x32_bf16 v[204:207], v[44:47], v[232:235], v[204:207]\n\t" \
    "ds_read_b128 v[224:227], v253 offset:2048\n\t" \
    "v_mfma_f32_16x16x32_bf16 v[208:211], v[48:51], v[228:231], v[208:211]\n\t" \
    "v_mfma_f32_16x16x32_bf16 v[208:211], v[52:55], v[232:235], v[208:211]\n\t" \
    "v_mfma_f32_16x16x32_bf16 v[200:203], v[244:247], v[228:231], v[200:203]\n\t" \
    "v_mfma_f32_16x16x32_bf16 v[200:203], v[248:251], v[232:235], v[200:203]\n\t" \
    "s_waitcnt lgkmcnt(2)\n\t" \
    "v_mfma_f32_16x16x32_bf16 v[216:219], v[40:43], v[236:239], v[216:219]\n\t" \
    "ds_read_b128 v[228:231], v253 offset:3072\n\t" \
    "s_add_u32 s44, s40, 1\n\t" \
    "s_waitcnt lgkmcnt(2)\n\t" \
    "v_mfma_f32_16x16x32_bf16 v[216:219], v[44:47], v[240:243], v[216:219]\n\t" \
    "ds_read_b128 v[232:235], v253 offset:4096\n\t" \
    "s_and_b32 s45, s44, 15\n\t" \
    "v_mfma_f32_16x16x32_bf16 v[220:223], v[48:51], v[236:239], v[220:223]\n\t" \
    "s_cmp_eq_u32 s45, 0\n\t" \
    "v_mfma_f32_16x16x32_bf16 v[220:223], v[52:55], v[240:243], v[220:223]\n\t" \
    "s_cselect_b32 s45, %[cj], %[cs]\n\t" \
    "v_mfma_f32_16x16x32_bf16 v[212:215], v[244:247], v[236:239], v[212:215]\n\t" \
    "ds_read_b128 v[244:247], v254 offset:59392\n\t" \
    "s_cmp_eq_u32 s44, 64\n\t" \
    "v_mfma_f32_16x16x32_bf16 v[212:215], v[248:251], v[240:243], v[212:215]\n\t" \
    "ds_read_b128 v[248:251], v254 offset:60416\n\t" \
    "s_cselect_b32 s45, 0, s45\n\t" \
    "s_waitcnt lgkmcnt(4)\n\t" \
    "v_mfma_f32_16x16x32_bf16 v[204:207], v[64:67], v[224:227], v[204:207]\n\t" \
    "ds_read_b128 v[236:239], v253 offset:5120\n\t" \
    "s_add_u32 s42, s42, s45\n\t" \
    "s_waitcnt lgkmcnt(4)\n\t" \
    "v_mfma_f32_16x16x32_bf16 v[204:207], v[68:71], v[228:231], v[204:207]\n\t" \
    "ds_read_b128 v[240:243], v253 offset:24576\n\t" \
    "v_mfma_f32_16x16x32_bf16 v[208:211], v[72:75], v[224:227], v[208:211]\n\t" \
    "v_mfma_f32_16x16x32_bf16 v[208:211], v[76:79], v[228:231], v[208:211]\n\t" \
    "v_mfma_f32_16x16x32_bf16 v[200:203], v[56:59], v[224:227], v[200:203]\n\t" \
    "v_mfma_f32_16x16x32_bf16 v[200:203], v[60:63], v[228:231], v[200:203]\n\t" \
    "s_waitcnt lgkmcnt(4)\n\t" \
    "v_mfma_f32_16x16x32_bf16 v[204:207], v[80:83], v[232:235], v[204:207]\n\t" \
    "ds_read_b128 v[224:227], v253 offset:25600\n\t" \
    "s_waitcnt lgkmcnt(2)\n\t" \
    "v_mfma_f32_16x16x32_bf16 v[204:207], v[84:87], v[236:239], v[204:207]\n\t" \
    "ds_read_b128 v[228:231], v253 offset:6144\n\t" \
    "v_mfma_f32_16x16x32_bf16 v[208:211], v[88:91], v[232:235], v[208:211]\n\t" \
    "v_mfma_f32_16x16x32_bf16 v[208:211], v[92:95], v[236:239], v[208:211]\n\t" \
    "v_mfma_f32_16x16x32_bf16 v[200:203], v[244:247], v[232:235], v[200:203]\n\t" \
    "v_mfma_f32_16x16x32_bf16 v[200:203], v[248:251], v[236:239], v[200:203]\n\t" \
    "s_waitcnt lgkmcnt(2)\n\t" \
    "v_mfma_f32_16x16x32_bf16 v[216:219], v[80:83], v[240:243], v[216:219]\n\t" \
    "ds_read_b128 v[232:235], v253 offset:7168\n\t" \
    "s_and_b32 s44, s40, 3\n\t" \
    "s_waitcnt lgkmcnt(2)\n\t" \
    "v_mfma_f32_16x16x32_bf16 v[216:219], v[84:87], v[224:227], v[216:219]\n\t" \
    "ds_read_b128 v[236:239], v253 offset:8192\n\t" \
    "s_lshl_b32 s44, s44, 22\n\t" \
    "v_mfma_f32_16x16x32_bf16 v[220:223], v[88:91], v[240:243], v[220:223]\n\t" \
    "s_add_u32 s44, s44, %[mb]\n\t" \
    "v_mfma_f32_16x16x32_bf16 v[220:223], v[92:95], v[224:227], v[220:223]\n\t" \
    "s_add_u32 s46, s44, 0x10000\n\t" \
    "v_mfma_f32_16x16x32_bf16 v[212:215], v[244:247], v[240:243], v[212:215]\n\t" \
    "ds_read_b128 v[244:247], v254 offset:61440\n\t" \
    "s_add_u32 s47, s44, 0x10400\n\t" \
    "v_mfma_f32_16x16x32_bf16 v[212:215], v[248:251], v[224:227], v[212:215]\n\t" \
    "ds_read_b128 v[248:251], v254 offset:62464\n\t" \
    "s_waitcnt lgkmcnt(4)\n\t" \
    "v_mfma_f32_16x16x32_bf16 v[204:207], v[104:107], v[228:231], v[204:207]\n\t" \
    "ds_read_b128 v[240:243], v253 offset:9216\n\t" \
    "s_waitcnt lgkmcnt(4)\n\t" \
    "v_mfma_f32_16x16x32_bf16 v[204:207], v[108:111], v[232:235], v[204:207]\n\t" \
    "ds_read_b128 v[224:227], v253 offset:28672\n\t" \
    "v_mfma_f32_16x16x32_bf16 v[208:211], v[112:115], v[228:231], v[208:211]\n\t" \
    "v_mfma_f32_16x16x32_bf16 v[208:211], v[116:119], v[232:235], v[208:211]\n\t" \
    "v_mfma_f32_16x16x32_bf16 v[200:203], v[96:99], v[228:231], v[200:203]\n\t" \
    "v_mfma_f32_16x16x32_bf16 v[200:203], v[100:103], v[232:235], v[200:203]\n\t" \
    "s_waitcnt lgkmcnt(4)\n\t" \
    "v_mfma_f32_16x16x32_bf16 v[204:207], v[120:123], v[236:239], v[204:207]\n\t" \
    "ds_read_b128 v[228:231], v253 offset:29696\n\t" \
    "s_waitcnt lgkmcnt(2)\n\t" \
    "v_mfma_f32_16x16x32_bf16 v[204:207], v[124:127], v[240:243], v[204:207]\n\t" \
    "ds_read_b128 v[232:235], v253 offset:10240\n\t" \
    "v_mfma_f32_16x16x32_bf16 v[208:211], v[128:131], v[236:239], v[208:211]\n\t" \
    "v_mfma_f32_16x16x32_bf16 v[208:211], v[132:135], v[240:243], v[208:211]\n\t" \
    "v_mfma_f32_16x16x32_bf16 v[200:203], v[244:247], v[236:239], v[200:203]\n\t" \
    "v_mfma_f32_16x16x32_bf16 v[200:203], v[248:251], v[240:243], v[200:203]\n\t" \
    "s_waitcnt lgkmcnt(2)\n\t" \
    "v_mfma_f32_16x16x32_bf16 v[216:219], v[120:123], v[224:227], v[216:219]\n\t" \
    "ds_read_b128 v[236:239], v253 offset:11264\n\t" \
    "s_waitcnt lgkmcnt(2)\n\t" \
    "v_mfma_f32_16x16x32_bf16 v[216:219], v[124:127], v[228:231], v[216:219]\n\t" \
    "ds_read_b128 v[240:243], v253 offset:12288\n\t" \
    "v_mfma_f32_16x16x32_bf16 v[220:223], v[128:131], v[224:227], v[220:223]\n\t" \
    "v_mfma_f32_16x16x32_bf16 v[220:223], v[132:135], v[228:231], v[220:223]\n\t" \
    "v_mfma_f32_16x16x32_bf16 v[212:215], v[244:247], v[224:227], v[212:215]\n\t" \
    "ds_read_b128 v[244:247], v254 offset:63488\n\t" \
    "v_mfma_f32_16x16x32_bf16 v[212:215], v[248:251], v[228:231], v[212:215]\n\t" \
    "ds_read_b128 v[248:251], v254 offset:64512\n\t" \
    "v_and_b32 v252, 0xf0, v253\n\t" \
    "v_lshlrev_b32 v252, 3, v252\n\t" \
    "buffer_load_dword v252, v252, s[60:63], %[fl] offen sc1\n\t" \
    "s_waitcnt lgkmcnt(4)\n\t" \
    "v_mfma_f32_16x16x32_bf16 v[204:207], v[144:147], v[232:235], v[204:207]\n\t" \
    "ds_read_b128 v[224:227], v253 offset:13312\n\t" \
    "s_waitcnt lgkmcnt(4)\n\t" \
    "v_mfma_f32_16x16x32_bf16 v[204:207], v[148:151], v[236:239], v[204:207]\n\t" \
    "ds_read_b128 v[228:231], v253 offset:32768\n\t" \
    "v_mfma_f32_16x16x32_bf16 v[208:211], v[152:155], v[232:235], v[208:211]\n\t" \
    "v_mfma_f32_16x16x32_bf16 v[208:211], v[156:159], v[236:239], v[208:211]\n\t" \
    "v_mfma_f32_16x16x32_bf16 v[200:203], v[136:139], v[232:235], v[200:203]\n\t" \
    "v_mfma_f32_16x16x32_bf16 v[200:203], v[140:143], v[236:239], v[200:203]\n\t" \
    "s_waitcnt lgkmcnt(4)\n\t" \
    "v_mfma_f32_16x16x32_bf16 v[204:207], v[160:163], v[240:243], v[204:207]\n\t" \
    "ds_read_b128 v[232:235], v253 offset:33792\n\t" \
    "s_waitcnt lgkmcnt(2)\n\t" \
    "v_mfma_f32_16x16x32_bf16 v[204:207], v[164:167], v[224:227], v[204:207]\n\t" \
    "ds_read_b128 v[236:239], v253 offset:14336\n\t" \
    "v_mfma_f32_16x16x32_bf16 v[208:211], v[168:171], v[240:243], v[208:211]\n\t" \
    "v_mfma_f32_16x16x32_bf16 v[208:211], v[172:175], v[224:227], v[208:211]\n\t" \
    "v_mfma_f32_16x16x32_bf16 v[200:203], v[244:247], v[240:243], v[200:203]\n\t" \
    "v_mfma_f32_16x16x32_bf16 v[200:203], v[248:251], v[224:227], v[200:203]\n\t" \
    "s_waitcnt lgkmcnt(2)\n\t" \
    "v_mfma_f32_16x16x32_bf16 v[216:219], v[160:163], v[228:231], v[216:219]\n\t" \
    "ds_read_b128 v[240:243], v253 offset:15360\n\t" \
    "s_waitcnt lgkmcnt(2)\n\t" \
    "v_mfma_f32_16x16x32_bf16 v[216:219], v[164:167], v[232:235], v[216:219]\n\t" \
    "ds_read_b128 v[224:227], v253 offset:38912\n\t" \
    "v_mfma_f32_16x16x32_bf16 v[220:223], v[168:171], v[228:231], v[220:223]\n\t" \
    "v_mfma_f32_16x16x32_bf16 v[220:223], v[172:175], v[232:235], v[220:223]\n\t" \
    "v_mfma_f32_16x16x32_bf16 v[212:215], v[244:247], v[228:231], v[212:215]\n\t" \
    "v_mfma_f32_16x16x32_bf16 v[212:215], v[248:251], v[232:235], v[212:215]\n\t" \
    "s_waitcnt lgkmcnt(2)\n\t" \
    "v_mfma_f32_16x16x32_bf16 v[204:207], v[184:187], v[236:239], v[204:207]\n\t" \
    "ds_read_b128 v[228:231], v253 offset:39936\n\t" \
    "s_waitcnt lgkmcnt(2)\n\t" \
    "v_mfma_f32_16x16x32_bf16 v[204:207], v[188:191], v[240:243], v[204:207]\n\t" \
    "ds_read_b128 v[232:235], v253 offset:22528\n\t" \
    "v_mfma_f32_16x16x32_bf16 v[208:211], v[192:195], v[236:239], v[208:211]\n\t" \
    "v_mfma_f32_16x16x32_bf16 v[208:211], v[196:199], v[240:243], v[208:211]\n\t" \
    "v_mfma_f32_16x16x32_bf16 v[200:203], v[176:179], v[236:239], v[200:203]\n\t" \
    "v_mfma_f32_16x16x32_bf16 v[200:203], v[180:183], v[240:243], v[200:203]\n\t" \
    "s_waitcnt lgkmcnt(2)\n\t" \
    "v_mfma_f32_16x16x32_bf16 v[216:219], v[24:27], v[224:227], v[216:219]\n\t" \
    "ds_read_b128 v[236:239], v253 offset:23552\n\t" \
    "s_waitcnt lgkmcnt(2)\n\t" \
    "v_mfma_f32_16x16x32_bf16 v[216:219], v[28:31], v[228:231], v[216:219]\n\t" \
    "ds_read_b128 v[240:243], v253 offset:26624\n\t" \
    "v_mfma_f32_16x16x32_bf16 v[220:223], v[32:35], v[224:227], v[220:223]\n\t" \
    "v_cvt_pk_bf16_f32 v204, v204, v205\n\t" \
    "v_mfma_f32_16x16x32_bf16 v[220:223], v[36:39], v[228:231], v[220:223]\n\t" \
    "v_cvt_pk_bf16_f32 v205, v206, v207\n\t" \
    "v_mfma_f32_16x16x32_bf16 v[212:215], v[16:19], v[224:227], v[212:215]\n\t" \
    "ds_write_b64 v254, v[204:205] offset:4104\n\t" \
    "v_mfma_f32_16x16x32_bf16 v[212:215], v[20:23], v[228:231], v[212:215]\n\t" \
    "v_cvt_pk_bf16_f32 v208, v208, v209\n\t" \
    "s_waitcnt lgkmcnt(3)\n\t" \
    "v_mfma_f32_16x16x32_bf16 v[216:219], v[64:67], v[232:235], v[216:219]\n\t" \
    "ds_read_b128 v[224:227], v253 offset:27648\n\t" \
    "v_cvt_pk_bf16_f32 v209, v210, v211\n\t" \
    "s_waitcnt lgkmcnt(3)\n\t" \
    "v_mfma_f32_16x16x32_bf16 v[216:219], v[68:71], v[236:239], v[216:219]\n\t" \
    "ds_read_b128 v[228:231], v253 offset:30720\n\t" \
    "ds_write_b64 v254, v[208:209] offset:5128\n\t" \
    "v_mfma_f32_16x16x32_bf16 v[220:223], v[72:75], v[232:235], v[220:223]\n\t" \
    "v_cvt_pk_bf16_f32 v200, v200, v201\n\t" \
    "v_mfma_f32_16x16x32_bf16 v[220:223], v[76:79], v[236:239], v[220:223]\n\t" \
    "v_cvt_pk_bf16_f32 v201, v202, v203\n\t" \
    "v_mfma_f32_16x16x32_bf16 v[212:215], v[56:59], v[232:235], v[212:215]\n\t" \
    "ds_write_b64 v254, v[200:201] offset:3080\n\t" \
    "v_mfma_f32_16x16x32_bf16 v[212:215], v[60:63], v[236:239], v[212:215]\n\t" \
    "s_waitcnt lgkmcnt(5)\n\t" \
    "v_mfma_f32_16x16x32_bf16 v[216:219], v[104:107], v[240:243], v[216:219]\n\t" \
    "ds_read_b128 v[232:235], v253 offset:31744\n\t" \
    "s_waitcnt lgkmcnt(4)\n\t" \
    "v_mfma_f32_16x16x32_bf16 v[216:219], v[108:111], v[224:227], v[216:219]\n\t" \
    "ds_read_b128 v[236:239], v253 offset:34816\n\t" \
    "v_mfma_f32_16x16x32_bf16 v[220:223], v[112:115], v[240:243], v[220:223]\n\t" \
    "v_mfma_f32_16x16x32_bf16 v[220:223], v[116:119], v[224:227], v[220:223]\n\t" \
    "v_mfma_f32_16x16x32_bf16 v[212:215], v[96:99], v[240:243], v[212:215]\n\t" \
    "v_mfma_f32_16x16x32_bf16 v[212:215], v[100:103], v[224:227], v[212:215]\n\t" \
    "ds_read_b128 v[240:243], v253 offset:35840\n\t" \
    "s_waitcnt lgkmcnt(3)\n\t" \
    "s_barrier \n\t" \
    "v_mfma_f32_16x16x32_bf16 v[216:219], v[144:147], v[228:231], v[216:219]\n\t" \
    "s_waitcnt lgkmcnt(2)\n\t" \
    "v_mfma_f32_16x16x32_bf16 v[216:219], v[148:151], v[232:235], v[216:219]\n\t" \
    "s_waitcnt lgkmcnt(1)\n\t" \
    "v_mfma_f32_16x16x32_bf16 v[216:219], v[184:187], v[236:239], v[216:219]\n\t" \
    "s_waitcnt lgkmcnt(0)\n\t" \
    "v_mfma_f32_16x16x32_bf16 v[216:219], v[188:191], v[240:243], v[216:219]\n\t" \
    "v_mfma_f32_16x16x32_bf16 v[220:223], v[152:155], v[228:231], v[220:223]\n\t" \
    "v_mfma_f32_16x16x32_bf16 v[220:223], v[156:159], v[232:235], v[220:223]\n\t" \
    "v_mfma_f32_16x16x32_bf16 v[220:223], v[192:195], v[236:239], v[220:223]\n\t" \
    "v_cvt_pk_bf16_f32 v216, v216, v217\n\t" \
    "v_cvt_pk_bf16_f32 v217, v218, v219\n\t" \
    "v_mfma_f32_16x16x32_bf16 v[220:223], v[196:199], v[240:243], v[220:223]\n\t" \
    "ds_write_b64 v254, v[216:217] offset:10248\n\t" \
    "v_mfma_f32_16x16x32_bf16 v[212:215], v[136:139], v[228:231], v[212:215]\n\t" \
    "v_mfma_f32_16x16x32_bf16 v[212:215], v[140:143], v[232:235], v[212:215]\n\t" \
    "v_mfma_f32_16x16x32_bf16 v[212:215], v[176:179], v[236:239], v[212:215]\n\t" \
    "v_cvt_pk_bf16_f32 v220, v220, v221\n\t" \
    "v_cvt_pk_bf16_f32 v221, v222, v223\n\t" \
    "v_mfma_f32_16x16x32_bf16 v[212:215], v[180:183], v[240:243], v[212:215]\n\t" \
    "ds_write_b64 v254, v[220:221] offset:11272\n\t" \
    "s_nop 6\n\t" \
    "v_cvt_pk_bf16_f32 v212, v212, v213\n\t" \
    "v_cvt_pk_bf16_f32 v213, v214, v215\n\t" \
    "ds_write_b64 v254, v[212:213] offset:9224\n\t" \
    "s_waitcnt vmcnt(0) lgkmcnt(0)\n\t" \
    "s_barrier \n\t" \
    "s_setprio 2\n\t" \
    "s_add_u32 s44, s40, 1\n\t" \
    "s_min_u32 s44, s44, 63\n\t" \
    ".Lv38_%=:\n\t" \
    "v_cmp_ge_u32 vcc, s44, v252\n\t" \
    "s_cbranch_vccz .Lv39_%=\n\t" \
    "s_cmp_eq_u32 s41, 0\n\t" \
    "s_cbranch_scc1 .Lv39_%=\n\t" \
    "s_sub_u32 s41, s41, 1\n\t" \
    "s_sleep 2\n\t" \
    "v_and_b32 v252, 0xf0, v253\n\t" \
    "v_lshlrev_b32 v252, 3, v252\n\t" \
    "buffer_load_dword v252, v252, s[60:63], %[fl] offen sc1\n\t" \
    "s_waitcnt vmcnt(0)\n\t" \
    "s_branch .Lv38_%=\n\t" \
    ".Lv39_%=:\n\t" \
    "s_mov_b32 m0, 0x3000\n\t" \
    "s_add_u32 s49, s42, 0x0\n\t" \
    "buffer_load_dwordx4 v253, s[60:63], s49 offen offset:0 sc1 lds\n\t" \
    "s_sleep 1\n\t" \
    "buffer_load_dwordx4 v253, s[60:63], s49 offen offset:1024 sc1 lds\n\t" \
    "s_sleep 1\n\t" \
    "buffer_load_dwordx4 v253, s[60:63], s49 offen offset:2048 sc1 lds\n\t" \
    "s_sleep 1\n\t" \
    "buffer_load_dwordx4 v253, s[60:63], s49 offen offset:3072 sc1 lds\n\t" \
    "s_sleep 1\n\t" \
    "s_mov_b32 m0, 0x4c00\n\t" \
    "s_add_u32 s49, s42, 0x4c00\n\t" \
    "buffer_load_dwordx4 v253, s[60:63], s49 offen offset:0 sc1 lds\n\t" \
    "s_sleep 1\n\t" \
    "s_mov_b32 m0, 0x8000\n\t" \
    "s_add_u32 s49, s42, 0x1000\n\t" \
    "buffer_load_dwordx4 v253, s[60:63], s49 offen offset:0 sc1 lds\n\t" \
    "s_sleep 1\n\t" \
    "buffer_load_dwordx4 v253, s[60:63], s49 offen offset:1024 sc1 lds\n\t" \
    "s_sleep 1\n\t" \
    "buffer_load_dwordx4 v253, s[60:63], s49 offen offset:2048 sc1 lds\n\t" \
    "s_sleep 1\n\t" \
    "buffer_load_dwordx4 v253, s[60:63], s49 offen offset:3072 sc1 lds\n\t" \
    "s_sleep 1\n\t" \
    "s_mov_b32 m0, 0x9c00\n\t" \
    "s_add_u32 s49, s42, 0x5c00\n\t" \
    "buffer_load_dwordx4 v253, s[60:63], s49 offen offset:0 sc1 lds\n\t" \
    "s_sleep 1\n\t" \
    "ds_read_b128 v[200:203], v254 offset:55296\n\t" \
    "ds_read_b128 v[204:207], v254 offset:56320\n\t" \
    "ds_read_b128 v[216:219], v254 offset:57344\n\t" \
    "ds_read_b128 v[220:223], v254 offset:58368\n\t" \
    "v_mov_b64 v[208:209], 0\n\t" \
    "v_mov_b64 v[210:211], 0\n\t" \
    "v_mov_b64 v[244:245], 0\n\t" \
    "v_mov_b64 v[246:247], 0\n\t" \
    "v_mov_b64 v[248:249], 0\n\t" \
    "v_mov_b64 v[250:251], 0\n\t" \
    "s_sleep 5\n\t" \
    "s_mov_b32 m0, 0xc000\n\t" \
    "s_nop 0\n\t" \
    "buffer_load_dwordx4 v253, s[60:63], s46 offen sc1 lds\n\t" \
    "s_mov_b32 m0, 0xc400\n\t" \
    "s_nop 0\n\t" \
    "buffer_load_dwordx4 v253, s[60:63], s47 offen sc1 lds\n\t" \
    "s_waitcnt lgkmcnt(0)\n\t" \
    "v_bfe_i32 v212, v253, 5, 1\n\t" \
    "v_and_b32 v212, 0xaaaa, v212\n\t" \
    "v_or_b32 v212, 0x4444, v212\n\t" \
    "v_and_b32 v213, 0xffff, v255\n\t" \
    "v_lshrrev_b32 v214, 16, v255\n\t" \
    ".Lv310_%=:\n\t" \
    "s_waitcnt vmcnt(0)\n\t" \
    "ds_read_b128 v[224:227], v253 offset:49152\n\t" \
    "ds_read_b128 v[228:231], v253 offset:50176\n\t" \
    "s_waitcnt lgkmcnt(1)\n\t" \
    "v_max_u32 v215, v224, v225\n\t" \
    "v_max3_u32 v215, v226, v227, v215\n\t" \
    "s_waitcnt lgkmcnt(0)\n\t" \
    "v_max3_u32 v215, v228, v229, v215\n\t" \
    "v_max3_u32 v215, v230, v231, v215\n\t" \
    "v_cmp_eq_u32 vcc, -1, v215\n\t" \
    "s_cbranch_vccz .Lv311_%=\n\t" \
    "v_max_u32 v215, v224, v225\n\t" \
    "v_max3_u32 v215, v226, v227, v215\n\t" \
    "v_cmp_eq_u32 vcc, -1, v215\n\t" \
    "s_cbranch_vccz .Lv312_%=\n\t" \
    "s_mov_b32 m0, 0xc000\n\t" \
    "s_nop 0\n\t" \
    "buffer_load_dwordx4 v253, s[60:63], s46 offen sc1 lds\n\t" \
    ".Lv312_%=:\n\t" \
    "v_max_u32 v215, v228, v229\n\t" \
    "v_max3_u32 v215, v230, v231, v215\n\t" \
    "v_cmp_eq_u32 vcc, -1, v215\n\t" \
    "s_cbranch_vccz .Lv313_%=\n\t" \
    "s_mov_b32 m0, 0xc400\n\t" \
    "s_nop 0\n\t" \
    "buffer_load_dwordx4 v253, s[60:63], s47 offen sc1 lds\n\t" \
    ".Lv313_%=:\n\t" \
    "s_cmp_eq_u32 s41, 0\n\t" \
    "s_cbranch_scc1 .Lv311_%=\n\t" \
    "s_sub_u32 s41, s41, 1\n\t" \
    "s_branch .Lv310_%=\n\t" \
    ".Lv311_%=:\n\t" \
    "s_waitcnt lgkmcnt(0)\n\t" \
    "s_barrier \n\t" \
    "ds_read_b128 v[232:235], v253 offset:40960\n\t" \
    "ds_read_b128 v[236:239], v253 offset:41984\n\t" \
    "ds_read_b128 v[240:243], v253 offset:43008\n\t" \
    "v_smfmac_f32_16x16x64_bf16 v[244:247], v[224:227], v[0:7], v212\n\t" \
    "v_smfmac_f32_16x16x64_bf16 v[248:251], v[224:227], v[8:15], v212\n\t" \
    "v_smfmac_f32_16x16x64_bf16 v[208:211], v[224:227], v[200:207], v212\n\t" \
    "ds_read_b128 v[200:203], v254 offset:59392\n\t" \
    "ds_read_b128 v[204:207], v254 offset:60416\n\t" \
    "ds_read_b128 v[224:227], v253 offset:44032\n\t" \
    "v_smfmac_f32_16x16x64_bf16 v[208:211], v[228:231], v[16:23], v212\n\t" \
    "v_smfmac_f32_16x16x64_bf16 v[244:247], v[228:231], v[24:31], v212\n\t" \
    "v_smfmac_f32_16x16x64_bf16 v[248:251], v[228:231], v[32:39], v212\n\t" \
    "ds_read_b128 v[228:231], v253 offset:45056\n\t" \
    "s_waitcnt lgkmcnt(6)\n\t" \
    "v_smfmac_f32_16x16x64_bf16 v[208:211], v[232:235], v[216:223], v212\n\t" \
    "ds_read_b128 v[216:219], v254 offset:61440\n\t" \
    "ds_read_b128 v[220:223], v254 offset:62464\n\t" \
    "v_smfmac_f32_16x16x64_bf16 v[244:247], v[232:235], v[40:47], v212\n\t" \
    "v_smfmac_f32_16x16x64_bf16 v[248:251], v[232:235], v[48:55], v212\n\t" \
    "ds_read_b128 v[232:235], v253 offset:46080\n\t" \
    "s_waitcnt lgkmcnt(8)\n\t" \
    "v_smfmac_f32_16x16x64_bf16 v[208:211], v[236:239], v[56:63], v212\n\t" \
    "v_smfmac_f32_16x16x64_bf16 v[244:247], v[236:239], v[64:71], v212\n\t" \
    "v_smfmac_f32_16x16x64_bf16 v[248:251], v[236:239], v[72:79], v212\n\t" \
    "ds_read_b128 v[236:239], v253 offset:47104\n\t" \
    "s_waitcnt lgkmcnt(6)\n\t" \
    "v_smfmac_f32_16x16x64_bf16 v[208:211], v[240:243], v[200:207], v212\n\t" \
    "ds_read_b128 v[200:203], v254 offset:63488\n\t" \
    "ds_read_b128 v[204:207], v254 offset:64512\n\t" \
    "v_smfmac_f32_16x16x64_bf16 v[244:247], v[240:243], v[80:87], v212\n\t" \
    "v_smfmac_f32_16x16x64_bf16 v[248:251], v[240:243], v[88:95], v212\n\t" \
    "ds_read_b128 v[240:243], v253 offset:48128\n\t" \
    "s_waitcnt lgkmcnt(8)\n\t" \
    "v_smfmac_f32_16x16x64_bf16 v[208:211], v[224:227], v[96:103], v212\n\t" \
    "v_smfmac_f32_16x16x64_bf16 v[244:247], v[224:227], v[104:111], v212\n\t" \
    "v_smfmac_f32_16x16x64_bf16 v[248:251], v[224:227], v[112:119], v212\n\t" \
    "s_waitcnt lgkmcnt(7)\n\t" \
    "v_smfmac_f32_16x16x64_bf16 v[244:247], v[228:231], v[120:127], v212\n\t" \
    "s_waitcnt lgkmcnt(4)\n\t" \
    "v_smfmac_f32_16x16x64_bf16 v[244:247], v[232:235], v[144:151], v212\n\t" \
    "s_waitcnt lgkmcnt(3)\n\t" \
    "v_smfmac_f32_16x16x64_bf16 v[244:247], v[236:239], v[160:167], v212\n\t" \
    "s_waitcnt lgkmcnt(0)\n\t" \
    "v_smfmac_f32_16x16x64_bf16 v[244:247], v[240:243], v[184:191], v212\n\t" \
    "v_smfmac_f32_16x16x64_bf16 v[248:251], v[228:231], v[128:135], v212\n\t" \
    "v_smfmac_f32_16x16x64_bf16 v[248:251], v[232:235], v[152:159], v212\n\t" \
    "v_smfmac_f32_16x16x64_bf16 v[248:251], v[236:239], v[168:175], v212\n\t" \
    "v_add_f32 v244, v244, v246\n\t" \
    "v_add_f32 v245, v245, v247\n\t" \
    "v_cvt_pk_bf16_f32 v244, v244, v245\n\t" \
    "v_smfmac_f32_16x16x64_bf16 v[248:251], v[240:243], v[192:199], v212\n\t" \
    "ds_write_b16 v213, v244 offset:512\n\t" \
    "ds_write_b16_d16_hi v213, v244 offset:528\n\t" \
    "v_smfmac_f32_16x16x64_bf16 v[208:211], v[228:231], v[216:223], v212\n\t" \
    "v_smfmac_f32_16x16x64_bf16 v[208:211], v[232:235], v[136:143], v212\n\t" \
    "v_smfmac_f32_16x16x64_bf16 v[208:211], v[236:239], v[200:207], v212\n\t" \
    "v_add_f32 v248, v248, v250\n\t" \
    "v_add_f32 v249, v249, v251\n\t" \
    "v_cvt_pk_bf16_f32 v248, v248, v249\n\t" \
    "v_smfmac_f32_16x16x64_bf16 v[208:211], v[240:243], v[176:183], v212\n\t" \
    "ds_write_b16 v213, v248 offset:1024\n\t" \
    "ds_write_b16_d16_hi v213, v248 offset:1040\n\t" \
    "s_nop 5\n\t" \
    "v_add_f32 v208, v208, v210\n\t" \
    "v_add_f32 v209, v209, v211\n\t" \
    "v_cvt_pk_bf16_f32 v208, v208, v209\n\t" \
    "ds_write_b16 v213, v208 offset:0\n\t" \
    "ds_write_b16_d16_hi v213, v208 offset:16\n\t" \
    "s_setprio 0\n\t" \
    "s_waitcnt lgkmcnt(0)\n\t" \
    "s_barrier \n\t" \
    "ds_read_b128 v[224:227], v253 offset:16384\n\t" \
    "ds_read_b128 v[244:247], v254 offset:55296\n\t" \
    "ds_read_b128 v[228:231], v253 offset:17408\n\t" \
    "ds_read_b128 v[232:235], v253 offset:36864\n\t" \
    "ds_read_b128 v[236:239], v253 offset:37888\n\t" \
    "ds_read_b128 v[248:251], v254 offset:56320\n\t" \
    "s_add_u32 s40, s40, 1\n\t" \
    "s_cmp_lg_u32 s40, 64\n\t" \
    "s_cbranch_scc1 .Lv37_%=\n\t" \
    "s_branch .Lend%=\n\t" \
    ".Lv2_%=:\n\t" \
    "s_mov_b32 s42, %[xs]\n\t" \
    "v_add_u32 v244, 0xc000, v243\n\t" \
    "s_waitcnt vmcnt(12) lgkmcnt(0)\n\t" \
    "s_barrier \n\t" \
    "s_add_u32 m0, %[wm], 0x9000\n\t" \
    "s_mov_b32 s44, 0x1e0000\n\t" \
    "buffer_load_dwordx4 v240, %[wr], s44 offen offset:0 nt lds\n\t" \
    "buffer_load_dwordx4 v241, %[wr], s44 offen offset:1024 nt lds\n\t" \
    "buffer_load_dwordx4 v242, %[wr], s44 offen offset:2048 nt lds\n\t" \
    "ds_read_b64_tr_b16 v[208:209], v244 offset:0\n\t" \
    "ds_read_b64_tr_b16 v[210:211], v244 offset:1536\n\t" \
    "ds_read_b64_tr_b16 v[40:41], v244 offset:32\n\t" \
    "ds_read_b64_tr_b16 v[42:43], v244 offset:1568\n\t" \
    "ds_read_b64_tr_b16 v[48:49], v244 offset:64\n\t" \
    "ds_read_b64_tr_b16 v[50:51], v244 offset:1600\n\t" \
    "v_add_u32 v244, 0xf000, v243\n\t" \
    "s_waitcnt vmcnt(12) lgkmcnt(0)\n\t" \
    "s_barrier \n\t" \
    "s_add_u32 m0, %[wm], 0xc000\n\t" \
    "s_mov_b32 s44, 0x240000\n\t" \
    "buffer_load_dwordx4 v240, %[wr], s44 offen offset:0 nt lds\n\t" \
    "buffer_load_dwordx4 v241, %[wr], s44 offen offset:1024 nt lds\n\t" \
    "buffer_load_dwordx4 v242, %[wr], s44 offen offset:2048 nt lds\n\t" \
    "ds_read_b64_tr_b16 v[212:213], v244 offset:0\n\t" \
    "ds_read_b64_tr_b16 v[214:215], v244 offset:1536\n\t" \
    "ds_read_b64_tr_b16 v[44:45], v244 offset:32\n\t" \
    "ds_read_b64_tr_b16 v[46:47], v244 offset:1568\n\t" \
    "ds_read_b64_tr_b16 v[52:53], v244 offset:64\n\t" \
    "ds_read_b64_tr_b16 v[54:55], v244 offset:1600\n\t" \
    "v_add_u32 v244, 0x0, v243\n\t" \
    "s_waitcnt vmcnt(12) lgkmcnt(0)\n\t" \
    "s_barrier \n\t" \
    "s_add_u32 m0, %[wm], 0xf000\n\t" \
    "s_mov_b32 s44, 0x2a0000\n\t" \
    "buffer_load_dwordx4 v240, %[wr], s44 offen offset:0 nt lds\n\t" \
    "buffer_load_dwordx4 v241, %[wr], s44 offen offset:1024 nt lds\n\t" \
    "buffer_load_dwordx4 v242, %[wr], s44 offen offset:2048 nt lds\n\t" \
    "ds_read_b64_tr_b16 v[56:57], v244 offset:0\n\t" \
    "ds_read_b64_tr_b16 v[58:59], v244 offset:1536\n\t" \
    "ds_read_b64_tr_b16 v[64:65], v244 offset:32\n\t" \
    "ds_read_b64_tr_b16 v[66:67], v244 offset:1568\n\t" \
    "ds_read_b64_tr_b16 v[72:73], v244 offset:64\n\t" \
    "ds_read_b64_tr_b16 v[74:75], v244 offset:1600\n\t" \
    "v_add_u32 v244, 0x3000, v243\n\t" \
    "s_waitcnt vmcnt(12) lgkmcnt(0)\n\t" \
    "s_barrier \n\t" \
    "s_add_u32 m0, %[wm], 0x0\n\t" \
    "s_mov_b32 s44, 0x300000\n\t" \
    "buffer_load_dwordx4 v240, %[wr], s44 offen offset:0 nt lds\n\t" \
    "buffer_load_dwordx4 v241, %[wr], s44 offen offset:1024 nt lds\n\t" \
    "buffer_load_dwordx4 v242, %[wr], s44 offen offset:2048 nt lds\n\t" \
    "ds_read_b64_tr_b16 v[60:61], v244 offset:0\n\t" \
    "ds_read_b64_tr_b16 v[62:63], v244 offset:1536\n\t" \
    "ds_read_b64_tr_b16 v[68:69], v244 offset:32\n\t" \
    "ds_read_b64_tr_b16 v[70:71], v244 offset:1568\n\t" \
    "ds_read_b64_tr_b16 v[76:77], v244 offset:64\n\t" \
    "ds_read_b64_tr_b16 v[78:79], v244 offset:1600\n\t" \
    "v_add_u32 v244, 0x6000, v243\n\t" \
    "s_waitcnt vmcnt(12) lgkmcnt(0)\n\t" \
    "s_barrier \n\t" \
    "s_add_u32 m0, %[wm], 0x3000\n\t" \
    "s_mov_b32 s44, 0x360000\n\t" \
    "buffer_load_dwordx4 v240, %[wr], s44 offen offset:0 nt lds\n\t" \
    "buffer_load_dwordx4 v241, %[wr], s44 offen offset:1024 nt lds\n\t" \
    "buffer_load_dwordx4 v242, %[wr], s44 offen offset:2048 nt lds\n\t" \
    "ds_read_b64_tr_b16 v[216:217], v244 offset:0\n\t" \
    "ds_read_b64_tr_b16 v[218:219], v244 offset:1536\n\t" \
    "ds_read_b64_tr_b16 v[80:81], v244 offset:32\n\t" \
    "ds_read_b64_tr_b16 v[82:83], v244 offset:1568\n\t" \
    "ds_read_b64_tr_b16 v[88:89], v244 offset:64\n\t" \
    "ds_read_b64_tr_b16 v[90:91], v244 offset:1600\n\t" \
    "v_add_u32 v244, 0x9000, v243\n\t" \
    "s_waitcnt vmcnt(12) lgkmcnt(0)\n\t" \
    "s_barrier \n\t" \
    "s_add_u32 m0, %[wm], 0x6000\n\t" \
    "s_mov_b32 s44, 0x3c0000\n\t" \
    "buffer_load_dwordx4 v240, %[wr], s44 offen offset:0 nt lds\n\t" \
    "buffer_load_dwordx4 v241, %[wr], s44 offen offset:1024 nt lds\n\t" \
    "buffer_load_dwordx4 v242, %[wr], s44 offen offset:2048 nt lds\n\t" \
    "ds_read_b64_tr_b16 v[220:221], v244 offset:0\n\t" \
    "ds_read_b64_tr_b16 v[222:223], v244 offset:1536\n\t" \
    "ds_read_b64_tr_b16 v[84:85], v244 offset:32\n\t" \
    "ds_read_b64_tr_b16 v[86:87], v244 offset:1568\n\t" \
    "ds_read_b64_tr_b16 v[92:93], v244 offset:64\n\t" \
    "ds_read_b64_tr_b16 v[94:95], v244 offset:1600\n\t" \
    "v_add_u32 v244, 0xc000, v243\n\t" \
    "s_waitcnt vmcnt(12) lgkmcnt(0)\n\t" \
    "s_barrier \n\t" \
    "s_add_u32 m0, %[wm], 0x9000\n\t" \
    "s_mov_b32 s44, 0x420000\n\t" \
    "buffer_load_dwordx4 v240, %[wr], s44 offen offset:0 nt lds\n\t" \
    "buffer_load_dwordx4 v241, %[wr], s44 offen offset:1024 nt lds\n\t" \
    "buffer_load_dwordx4 v242, %[wr], s44 offen offset:2048 nt lds\n\t" \
    "ds_read_b64_tr_b16 v[96:97], v244 offset:0\n\t" \
    "ds_read_b64_tr_b16 v[98:99], v244 offset:1536\n\t" \
    "ds_read_b64_tr_b16 v[104:105], v244 offset:32\n\t" \
    "ds_read_b64_tr_b16 v[106:107], v244 offset:1568\n\t" \
    "ds_read_b64_tr_b16 v[112:113], v244 offset:64\n\t" \
    "ds_read_b64_tr_b16 v[114:115], v244 offset:1600\n\t" \
    "v_add_u32 v244, 0xf000, v243\n\t" \
    "s_waitcnt vmcnt(12) lgkmcnt(0)\n\t" \
    "s_barrier \n\t" \
    "s_add_u32 m0, %[wm], 0xc000\n\t" \
    "s_mov_b32 s44, 0x480000\n\t" \
    "buffer_load_dwordx4 v240, %[wr], s44 offen offset:0 nt lds\n\t" \
    "buffer_load_dwordx4 v241, %[wr], s44 offen offset:1024 nt lds\n\t" \
    "buffer_load_dwordx4 v242, %[wr], s44 offen offset:2048 nt lds\n\t" \
    "ds_read_b64_tr_b16 v[100:101], v244 offset:0\n\t" \
    "ds_read_b64_tr_b16 v[102:103], v244 offset:1536\n\t" \
    "ds_read_b64_tr_b16 v[108:109], v244 offset:32\n\t" \
    "ds_read_b64_tr_b16 v[110:111], v244 offset:1568\n\t" \
    "ds_read_b64_tr_b16 v[116:117], v244 offset:64\n\t" \
    "ds_read_b64_tr_b16 v[118:119], v244 offset:1600\n\t" \
    "v_add_u32 v244, 0x0, v243\n\t" \
    "s_waitcnt vmcnt(12) lgkmcnt(0)\n\t" \
    "s_barrier \n\t" \
    "v_mov_b32 v246, 0\n\t" \
    "buffer_load_dword v246, v246, s[60:63], %[mo] offen sc1\n\t" \
    "s_add_u32 m0, %[wm], 0xf000\n\t" \
    "s_mov_b32 s44, 0x4e0000\n\t" \
    "buffer_load_dwordx4 v240, %[wr], s44 offen offset:0 nt lds\n\t" \
    "buffer_load_dwordx4 v241, %[wr], s44 offen offset:1024 nt lds\n\t" \
    "buffer_load_dwordx4 v242, %[wr], s44 offen offset:2048 nt lds\n\t" \
    "ds_read_b64_tr_b16 v[200:201], v244 offset:0\n\t" \
    "ds_read_b64_tr_b16 v[202:203], v244 offset:1536\n\t" \
    "ds_read_b64_tr_b16 v[0:1], v244 offset:32\n\t" \
    "ds_read_b64_tr_b16 v[2:3], v244 offset:1568\n\t" \
    "ds_read_b64_tr_b16 v[8:9], v244 offset:64\n\t" \
    "ds_read_b64_tr_b16 v[10:11], v244 offset:1600\n\t" \
    "v_add_u32 v244, 0x3000, v243\n\t" \
    "s_waitcnt vmcnt(12) lgkmcnt(0)\n\t" \
    "s_barrier \n\t" \
    "s_add_u32 m0, %[wm], 0x0\n\t" \
    "s_mov_b32 s44, 0x540000\n\t" \
    "buffer_load_dwordx4 v240, %[wr], s44 offen offset:0 nt lds\n\t" \
    "buffer_load_dwordx4 v241, %[wr], s44 offen offset:1024 nt lds\n\t" \
    "buffer_load_dwordx4 v242, %[wr], s44 offen offset:2048 nt lds\n\t" \
    "ds_read_b64_tr_b16 v[204:205], v244 offset:0\n\t" \
    "ds_read_b64_tr_b16 v[206:207], v244 offset:1536\n\t" \
    "ds_read_b64_tr_b16 v[4:5], v244 offset:32\n\t" \
    "ds_read_b64_tr_b16 v[6:7], v244 offset:1568\n\t" \
    "ds_read_b64_tr_b16 v[12:13], v244 offset:64\n\t" \
    "ds_read_b64_tr_b16 v[14:15], v244 offset:1600\n\t" \
    "v_add_u32 v244, 0x6000, v243\n\t" \
    "s_waitcnt vmcnt(12) lgkmcnt(0)\n\t" \
    "s_barrier \n\t" \
    "s_add_u32 m0, %[wm], 0x3000\n\t" \
    "s_mov_b32 s44, 0x5a0000\n\t" \
    "buffer_load_dwordx4 v240, %[wr], s44 offen offset:0 nt lds\n\t" \
    "buffer_load_dwordx4 v241, %[wr], s44 offen offset:1024 nt lds\n\t" \
    "buffer_load_dwordx4 v242, %[wr], s44 offen offset:2048 nt lds\n\t" \
    "ds_read_b64_tr_b16 v[224:225], v244 offset:0\n\t" \
    "ds_read_b64_tr_b16 v[226:227], v244 offset:1536\n\t" \
    "ds_read_b64_tr_b16 v[120:121], v244 offset:32\n\t" \
    "ds_read_b64_tr_b16 v[122:123], v244 offset:1568\n\t" \
    "ds_read_b64_tr_b16 v[128:129], v244 offset:64\n\t" \
    "ds_read_b64_tr_b16 v[130:131], v244 offset:1600\n\t" \
    "v_add_u32 v244, 0x9000, v243\n\t" \
    "s_waitcnt vmcnt(12) lgkmcnt(0)\n\t" \
    "s_barrier \n\t" \
    "s_add_u32 m0, %[wm], 0x6000\n\t" \
    "s_mov_b32 s44, 0x600000\n\t" \
    "buffer_load_dwordx4 v240, %[wr], s44 offen offset:0 nt lds\n\t" \
    "buffer_load_dwordx4 v241, %[wr], s44 offen offset:1024 nt lds\n\t" \
    "buffer_load_dwordx4 v242, %[wr], s44 offen offset:2048 nt lds\n\t" \
    "ds_read_b64_tr_b16 v[228:229], v244 offset:0\n\t" \
    "ds_read_b64_tr_b16 v[230:231], v244 offset:1536\n\t" \
    "ds_read_b64_tr_b16 v[124:125], v244 offset:32\n\t" \
    "ds_read_b64_tr_b16 v[126:127], v244 offset:1568\n\t" \
    "ds_read_b64_tr_b16 v[132:133], v244 offset:64\n\t" \
    "ds_read_b64_tr_b16 v[134:135], v244 offset:1600\n\t" \
    "v_add_u32 v244, 0xc000, v243\n\t" \
    "s_waitcnt vmcnt(12) lgkmcnt(0)\n\t" \
    "s_barrier \n\t" \
    "s_add_u32 m0, %[wm], 0x9000\n\t" \
    "s_mov_b32 s44, 0x660000\n\t" \
    "buffer_load_dwordx4 v240, %[wr], s44 offen offset:0 nt lds\n\t" \
    "buffer_load_dwordx4 v241, %[wr], s44 offen offset:1024 nt lds\n\t" \
    "buffer_load_dwordx4 v242, %[wr], s44 offen offset:2048 nt lds\n\t" \
    "ds_read_b64_tr_b16 v[136:137], v244 offset:0\n\t" \
    "ds_read_b64_tr_b16 v[138:139], v244 offset:1536\n\t" \
    "ds_read_b64_tr_b16 v[144:145], v244 offset:32\n\t" \
    "ds_read_b64_tr_b16 v[146:147], v244 offset:1568\n\t" \
    "ds_read_b64_tr_b16 v[152:153], v244 offset:64\n\t" \
    "ds_read_b64_tr_b16 v[154:155], v244 offset:1600\n\t" \
    "v_add_u32 v244, 0xf000, v243\n\t" \
    "s_waitcnt vmcnt(12) lgkmcnt(0)\n\t" \
    "s_barrier \n\t" \
    ".Lv21_%=:\n\t" \
    "v_readfirstlane_b32 s43, v246\n\t" \
    "s_nop 0\n\t" \
    "s_cmp_lg_u32 s43, -1\n\t" \
    "s_cbranch_scc1 .Lv22_%=\n\t" \
    "s_cmp_eq_u32 s41, 0\n\t" \
    "s_cbranch_scc1 .Lv22_%=\n\t" \
    "s_sub_u32 s41, s41, 1\n\t" \
    "s_sleep 4\n\t" \
    "v_mov_b32 v246, 0\n\t" \
    "buffer_load_dword v246, v246, s[60:63], %[mo] offen sc1\n\t" \
    "s_waitcnt vmcnt(0)\n\t" \
    "s_branch .Lv21_%=\n\t" \
    ".Lv22_%=:\n\t" \
    "v_mov_b32 v246, 0x8000\n\t" \
    "s_mov_b64 exec, 1\n\t" \
    "buffer_store_dword v246, v246, s[60:63], %[mo] offen sc1\n\t" \
    "s_mov_b64 exec, -1\n\t" \
    "s_and_b32 s44, s43, 0x5000\n\t" \
    "s_xor_b32 %[mb], %[mb], s44\n\t" \
    "s_xor_b32 %[fl], %[fl], s44\n\t" \
    "s_and_b32 s43, s43, 0x2000\n\t" \
    "s_xor_b32 s43, s43, %[lx]\n\t" \
    "s_add_u32 s60, s60, s43\n\t" \
    "s_addc_u32 s61, s61, 0\n\t" \
    "v_and_b32 v252, 0xf0, v253\n\t" \
    "v_lshlrev_b32 v252, 3, v252\n\t" \
    "buffer_load_dword v252, v252, s[60:63], %[fl] offen sc1\n\t" \
    "s_add_u32 m0, %[wm], 0xc000\n\t" \
    "s_mov_b32 s44, 0x6c0000\n\t" \
    "buffer_load_dwordx4 v240, %[wr], s44 offen offset:0 nt lds\n\t" \
    "buffer_load_dwordx4 v241, %[wr], s44 offen offset:1024 nt lds\n\t" \
    "buffer_load_dwordx4 v242, %[wr], s44 offen offset:2048 nt lds\n\t" \
    "ds_read_b64_tr_b16 v[140:141], v244 offset:0\n\t" \
    "ds_read_b64_tr_b16 v[142:143], v244 offset:1536\n\t" \
    "ds_read_b64_tr_b16 v[148:149], v244 offset:32\n\t" \
    "ds_read_b64_tr_b16 v[150:151], v244 offset:1568\n\t" \
    "ds_read_b64_tr_b16 v[156:157], v244 offset:64\n\t" \
    "ds_read_b64_tr_b16 v[158:159], v244 offset:1600\n\t" \
    "v_add_u32 v244, 0x0, v243\n\t" \
    "s_waitcnt vmcnt(12) lgkmcnt(0)\n\t" \
    "s_barrier \n\t" \
    "s_add_u32 m0, %[wm], 0xf000\n\t" \
    "s_mov_b32 s44, 0x720000\n\t" \
    "buffer_load_dwordx4 v240, %[wr], s44 offen offset:0 nt lds\n\t" \
    "buffer_load_dwordx4 v241, %[wr], s44 offen offset:1024 nt lds\n\t" \
    "buffer_load_dwordx4 v242, %[wr], s44 offen offset:2048 nt lds\n\t" \
    "ds_read_b64_tr_b16 v[16:17], v244 offset:0\n\t" \
    "ds_read_b64_tr_b16 v[18:19], v244 offset:1536\n\t" \
    "ds_read_b64_tr_b16 v[24:25], v244 offset:32\n\t" \
    "ds_read_b64_tr_b16 v[26:27], v244 offset:1568\n\t" \
    "ds_read_b64_tr_b16 v[32:33], v244 offset:64\n\t" \
    "ds_read_b64_tr_b16 v[34:35], v244 offset:1600\n\t" \
    "v_add_u32 v244, 0x3000, v243\n\t" \
    "s_waitcnt vmcnt(12) lgkmcnt(0)\n\t" \
    "s_barrier \n\t" \
    "ds_read_b64_tr_b16 v[20:21], v244 offset:0\n\t" \
    "ds_read_b64_tr_b16 v[22:23], v244 offset:1536\n\t" \
    "ds_read_b64_tr_b16 v[28:29], v244 offset:32\n\t" \
    "ds_read_b64_tr_b16 v[30:31], v244 offset:1568\n\t" \
    "ds_read_b64_tr_b16 v[36:37], v244 offset:64\n\t" \
    "ds_read_b64_tr_b16 v[38:39], v244 offset:1600\n\t" \
    "v_add_u32 v244, 0x6000, v243\n\t" \
    "s_waitcnt vmcnt(9) lgkmcnt(0)\n\t" \
    "s_barrier \n\t" \
    "ds_read_b64_tr_b16 v[232:233], v244 offset:0\n\t" \
    "ds_read_b64_tr_b16 v[234:235], v244 offset:1536\n\t" \
    "ds_read_b64_tr_b16 v[160:161], v244 offset:32\n\t" \
    "ds_read_b64_tr_b16 v[162:163], v244 offset:1568\n\t" \
    "ds_read_b64_tr_b16 v[168:169], v244 offset:64\n\t" \
    "ds_read_b64_tr_b16 v[170:171], v244 offset:1600\n\t" \
    "v_add_u32 v244, 0x9000, v243\n\t" \
    "s_waitcnt vmcnt(6) lgkmcnt(0)\n\t" \
    "s_barrier \n\t" \
    "ds_read_b64_tr_b16 v[236:237], v244 offset:0\n\t" \
    "ds_read_b64_tr_b16 v[238:239], v244 offset:1536\n\t" \
    "ds_read_b64_tr_b16 v[164:165], v244 offset:32\n\t" \
    "ds_read_b64_tr_b16 v[166:167], v244 offset:1568\n\t" \
    "ds_read_b64_tr_b16 v[172:173], v244 offset:64\n\t" \
    "ds_read_b64_tr_b16 v[174:175], v244 offset:1600\n\t" \
    "v_add_u32 v244, 0xc000, v243\n\t" \
    "s_waitcnt vmcnt(3) lgkmcnt(0)\n\t" \
    "s_barrier \n\t" \
    "ds_read_b64_tr_b16 v[176:177], v244 offset:0\n\t" \
    "ds_read_b64_tr_b16 v[178:179], v244 offset:1536\n\t" \
    "ds_read_b64_tr_b16 v[184:185], v244 offset:32\n\t" \
    "ds_read_b64_tr_b16 v[186:187], v244 offset:1568\n\t" \
    "ds_read_b64_tr_b16 v[192:193], v244 offset:64\n\t" \
    "ds_read_b64_tr_b16 v[194:195], v244 offset:1600\n\t" \
    "s_mov_b32 s43, 0\n\t" \
    "v_cmp_eq_u32 vcc, 0, v252\n\t" \
    "s_cbranch_vccnz .Lv23_%=\n\t" \
    "s_mov_b32 m0, 0x2000\n\t" \
    "s_add_u32 s49, s42, 0x0\n\t" \
    "buffer_load_dwordx4 v253, s[60:63], s49 offen offset:0 sc1 lds\n\t" \
    "buffer_load_dwordx4 v253, s[60:63], s49 offen offset:1024 sc1 lds\n\t" \
    "buffer_load_dwordx4 v253, s[60:63], s49 offen offset:2048 sc1 lds\n\t" \
    "buffer_load_dwordx4 v253, s[60:63], s49 offen offset:3072 sc1 lds\n\t" \
    "s_mov_b32 m0, 0x4800\n\t" \
    "s_add_u32 s49, s42, 0x8800\n\t" \
    "buffer_load_dwordx4 v253, s[60:63], s49 offen offset:0 sc1 lds\n\t" \
    "s_mov_b32 m0, 0x7000\n\t" \
    "s_add_u32 s49, s42, 0x1000\n\t" \
    "buffer_load_dwordx4 v253, s[60:63], s49 offen offset:0 sc1 lds\n\t" \
    "buffer_load_dwordx4 v253, s[60:63], s49 offen offset:1024 sc1 lds\n\t" \
    "buffer_load_dwordx4 v253, s[60:63], s49 offen offset:2048 sc1 lds\n\t" \
    "buffer_load_dwordx4 v253, s[60:63], s49 offen offset:3072 sc1 lds\n\t" \
    "s_mov_b32 m0, 0x9800\n\t" \
    "s_add_u32 s49, s42, 0x9800\n\t" \
    "buffer_load_dwordx4 v253, s[60:63], s49 offen offset:0 sc1 lds\n\t" \
    "s_mov_b32 s43, 1\n\t" \
    ".Lv23_%=:\n\t" \
    "v_add_u32 v244, 0xf000, v243\n\t" \
    "s_waitcnt vmcnt(0) lgkmcnt(0)\n\t" \
    "s_barrier \n\t" \
    "ds_read_b64_tr_b16 v[180:181], v244 offset:0\n\t" \
    "ds_read_b64_tr_b16 v[182:183], v244 offset:1536\n\t" \
    "ds_read_b64_tr_b16 v[188:189], v244 offset:32\n\t" \
    "ds_read_b64_tr_b16 v[190:191], v244 offset:1568\n\t" \
    "ds_read_b64_tr_b16 v[196:197], v244 offset:64\n\t" \
    "ds_read_b64_tr_b16 v[198:199], v244 offset:1600\n\t" \
    "s_waitcnt lgkmcnt(0)\n\t" \
    "s_barrier \n\t" \
    "ds_write_b128 v254, v[200:203] offset:51200\n\t" \
    "ds_write_b128 v254, v[204:207] offset:52224\n\t" \
    "ds_write_b128 v254, v[208:211] offset:53248\n\t" \
    "ds_write_b128 v254, v[212:215] offset:54272\n\t" \
    "ds_write_b128 v254, v[216:219] offset:55296\n\t" \
    "ds_write_b128 v254, v[220:223] offset:56320\n\t" \
    "ds_write_b128 v254, v[224:227] offset:57344\n\t" \
    "ds_write_b128 v254, v[228:231] offset:58368\n\t" \
    "ds_write_b128 v254, v[232:235] offset:59392\n\t" \
    "ds_write_b128 v254, v[236:239] offset:60416\n\t" \
    "s_cmp_lg_u32 s43, 0\n\t" \
    "s_cbranch_scc1 .Lv24_%=\n\t" \
    "s_mov_b32 s44, 0\n\t" \
    ".Lv25_%=:\n\t" \
    "v_cmp_ge_u32 vcc, s44, v252\n\t" \
    "s_cbranch_vccz .Lv26_%=\n\t" \
    "s_cmp_eq_u32 s41, 0\n\t" \
    "s_cbranch_scc1 .Lv26_%=\n\t" \
    "s_sub_u32 s41, s41, 1\n\t" \
    "s_sleep 8\n\t" \
    "v_and_b32 v252, 0xf0, v253\n\t" \
    "v_lshlrev_b32 v252, 3, v252\n\t" \
    "buffer_load_dword v252, v252, s[60:63], %[fl] offen sc1\n\t" \
    "s_waitcnt vmcnt(0)\n\t" \
    "s_branch .Lv25_%=\n\t" \
    ".Lv26_%=:\n\t" \
    "s_mov_b32 m0, 0x2000\n\t" \
    "s_add_u32 s49, s42, 0x0\n\t" \
    "buffer_load_dwordx4 v253, s[60:63], s49 offen offset:0 sc1 lds\n\t" \
    "buffer_load_dwordx4 v253, s[60:63], s49 offen offset:1024 sc1 lds\n\t" \
    "buffer_load_dwordx4 v253, s[60:63], s49 offen offset:2048 sc1 lds\n\t" \
    "buffer_load_dwordx4 v253, s[60:63], s49 offen offset:3072 sc1 lds\n\t" \
    "s_mov_b32 m0, 0x4800\n\t" \
    "s_add_u32 s49, s42, 0x8800\n\t" \
    "buffer_load_dwordx4 v253, s[60:63], s49 offen offset:0 sc1 lds\n\t" \
    "s_mov_b32 m0, 0x7000\n\t" \
    "s_add_u32 s49, s42, 0x1000\n\t" \
    "buffer_load_dwordx4 v253, s[60:63], s49 offen offset:0 sc1 lds\n\t" \
    "buffer_load_dwordx4 v253, s[60:63], s49 offen offset:1024 sc1 lds\n\t" \
    "buffer_load_dwordx4 v253, s[60:63], s49 offen offset:2048 sc1 lds\n\t" \
    "buffer_load_dwordx4 v253, s[60:63], s49 offen offset:3072 sc1 lds\n\t" \
    "s_mov_b32 m0, 0x9800\n\t" \
    "s_add_u32 s49, s42, 0x9800\n\t" \
    "buffer_load_dwordx4 v253, s[60:63], s49 offen offset:0 sc1 lds\n\t" \
    ".Lv24_%=:\n\t" \
    "s_waitcnt vmcnt(0) lgkmcnt(0)\n\t" \
    "s_barrier \n\t" \
    "ds_read_b128 v[224:227], v253 offset:8192\n\t" \
    "ds_read_b128 v[244:247], v254 offset:51200\n\t" \
    "ds_read_b128 v[228:231], v253 offset:9216\n\t" \
    "ds_read_b128 v[232:235], v253 offset:28672\n\t" \
    "ds_read_b128 v[236:239], v253 offset:29696\n\t" \
    "ds_read_b128 v[248:251], v254 offset:52224\n\t" \
    ".p2align 8\n\t" \
    ".Lv27_%=:\n\t" \
    "s_waitcnt lgkmcnt(5)\n\t" \
    "v_mfma_f32_16x16x32_bf16 v[204:207], v[0:3], v[224:227], 0\n\t" \
    "s_waitcnt lgkmcnt(3)\n\t" \
    "v_mfma_f32_16x16x32_bf16 v[204:207], v[4:7], v[228:231], v[204:207]\n\t" \
    "ds_read_b128 v[240:243], v253 offset:14336\n\t" \
    "v_mfma_f32_16x16x32_bf16 v[208:211], v[8:11], v[224:227], 0\n\t" \
    "v_mfma_f32_16x16x32_bf16 v[208:211], v[12:15], v[228:231], v[208:211]\n\t" \
    "v_mfma_f32_16x16x32_bf16 v[200:203], v[244:247], v[224:227], 0\n\t" \
    "s_waitcnt lgkmcnt(1)\n\t" \
    "v_mfma_f32_16x16x32_bf16 v[200:203], v[248:251], v[228:231], v[200:203]\n\t" \
    "v_mfma_f32_16x16x32_bf16 v[216:219], v[0:3], v[232:235], 0\n\t" \
    "ds_read_b128 v[224:227], v253 offset:15360\n\t" \
    "v_mfma_f32_16x16x32_bf16 v[216:219], v[4:7], v[236:239], v[216:219]\n\t" \
    "ds_read_b128 v[228:231], v253 offset:0\n\t" \
    "v_mfma_f32_16x16x32_bf16 v[220:223], v[8:11], v[232:235], 0\n\t" \
    "v_mfma_f32_16x16x32_bf16 v[220:223], v[12:15], v[236:239], v[220:223]\n\t" \
    "v_mfma_f32_16x16x32_bf16 v[212:215], v[244:247], v[232:235], 0\n\t" \
    "ds_read_b128 v[244:247], v254 offset:53248\n\t" \
    "v_mfma_f32_16x16x32_bf16 v[212:215], v[248:251], v[236:239], v[212:215]\n\t" \
    "ds_read_b128 v[248:251], v254 offset:54272\n\t" \
    "s_waitcnt lgkmcnt(4)\n\t" \
    "v_mfma_f32_16x16x32_bf16 v[204:207], v[24:27], v[240:243], v[204:207]\n\t" \
    "ds_read_b128 v[232:235], v253 offset:1024\n\t" \
    "s_waitcnt lgkmcnt(4)\n\t" \
    "v_mfma_f32_16x16x32_bf16 v[204:207], v[28:31], v[224:227], v[204:207]\n\t" \
    "ds_read_b128 v[236:239], v253 offset:20480\n\t" \
    "v_mfma_f32_16x16x32_bf16 v[208:211], v[32:35], v[240:243], v[208:211]\n\t" \
    "v_mfma_f32_16x16x32_bf16 v[208:211], v[36:39], v[224:227], v[208:211]\n\t" \
    "v_mfma_f32_16x16x32_bf16 v[200:203], v[16:19], v[240:243], v[200:203]\n\t" \
    "v_mfma_f32_16x16x32_bf16 v[200:203], v[20:23], v[224:227], v[200:203]\n\t" \
    "s_waitcnt lgkmcnt(4)\n\t" \
    "v_mfma_f32_16x16x32_bf16 v[204:207], v[40:43], v[228:231], v[204:207]\n\t" \
    "ds_read_b128 v[240:243], v253 offset:21504\n\t" \
    "s_waitcnt lgkmcnt(2)\n\t" \
    "v_mfma_f32_16x16x32_bf16 v[204:207], v[44:47], v[232:235], v[204:207]\n\t" \
    "ds_read_b128 v[224:227], v253 offset:2048\n\t" \
    "v_mfma_f32_16x16x32_bf16 v[208:211], v[48:51], v[228:231], v[208:211]\n\t" \
    "v_mfma_f32_16x16x32_bf16 v[208:211], v[52:55], v[232:235], v[208:211]\n\t" \
    "v_mfma_f32_16x16x32_bf16 v[200:203], v[244:247], v[228:231], v[200:203]\n\t" \
    "v_mfma_f32_16x16x32_bf16 v[200:203], v[248:251], v[232:235], v[200:203]\n\t" \
    "s_waitcnt lgkmcnt(2)\n\t" \
    "v_mfma_f32_16x16x32_bf16 v[216:219], v[40:43], v[236:239], v[216:219]\n\t" \
    "ds_read_b128 v[228:231], v253 offset:3072\n\t" \
    "s_add_u32 s44, s40, 1\n\t" \
    "s_waitcnt lgkmcnt(2)\n\t" \
    "v_mfma_f32_16x16x32_bf16 v[216:219], v[44:47], v[240:243], v[216:219]\n\t" \
    "ds_read_b128 v[232:235], v253 offset:4096\n\t" \
    "s_and_b32 s45, s44, 15\n\t" \
    "v_mfma_f32_16x16x32_bf16 v[220:223], v[48:51], v[236:239], v[220:223]\n\t" \
    "s_cmp_eq_u32 s45, 0\n\t" \
    "v_mfma_f32_16x16x32_bf16 v[220:223], v[52:55], v[240:243], v[220:223]\n\t" \
    "s_cselect_b32 s45, %[cj], %[cs]\n\t" \
    "v_mfma_f32_16x16x32_bf16 v[212:215], v[244:247], v[236:239], v[212:215]\n\t" \
    "ds_read_b128 v[244:247], v254 offset:55296\n\t" \
    "s_cmp_eq_u32 s44, 64\n\t" \
    "v_mfma_f32_16x16x32_bf16 v[212:215], v[248:251], v[240:243], v[212:215]\n\t" \
    "ds_read_b128 v[248:251], v254 offset:56320\n\t" \
    "s_cselect_b32 s45, 0, s45\n\t" \
    "s_waitcnt lgkmcnt(4)\n\t" \
    "v_mfma_f32_16x16x32_bf16 v[204:207], v[64:67], v[224:227], v[204:207]\n\t" \
    "ds_read_b128 v[236:239], v253 offset:5120\n\t" \
    "s_add_u32 s42, s42, s45\n\t" \
    "s_waitcnt lgkmcnt(4)\n\t" \
    "v_mfma_f32_16x16x32_bf16 v[204:207], v[68:71], v[228:231], v[204:207]\n\t" \
    "ds_read_b128 v[240:243], v253 offset:24576\n\t" \
    "v_mfma_f32_16x16x32_bf16 v[208:211], v[72:75], v[224:227], v[208:211]\n\t" \
    "v_mfma_f32_16x16x32_bf16 v[208:211], v[76:79], v[228:231], v[208:211]\n\t" \
    "v_mfma_f32_16x16x32_bf16 v[200:203], v[56:59], v[224:227], v[200:203]\n\t" \
    "v_mfma_f32_16x16x32_bf16 v[200:203], v[60:63], v[228:231], v[200:203]\n\t" \
    "s_waitcnt lgkmcnt(4)\n\t" \
    "v_mfma_f32_16x16x32_bf16 v[204:207], v[80:83], v[232:235], v[204:207]\n\t" \
    "ds_read_b128 v[224:227], v253 offset:25600\n\t" \
    "s_waitcnt lgkmcnt(2)\n\t" \
    "v_mfma_f32_16x16x32_bf16 v[204:207], v[84:87], v[236:239], v[204:207]\n\t" \
    "ds_read_b128 v[228:231], v253 offset:6144\n\t" \
    "v_mfma_f32_16x16x32_bf16 v[208:211], v[88:91], v[232:235], v[208:211]\n\t" \
    "v_mfma_f32_16x16x32_bf16 v[208:211], v[92:95], v[236:239], v[208:211]\n\t" \
    "v_mfma_f32_16x16x32_bf16 v[200:203], v[244:247], v[232:235], v[200:203]\n\t" \
    "v_mfma_f32_16x16x32_bf16 v[200:203], v[248:251], v[236:239], v[200:203]\n\t" \
    "s_waitcnt lgkmcnt(2)\n\t" \
    "v_mfma_f32_16x16x32_bf16 v[216:219], v[80:83], v[240:243], v[216:219]\n\t" \
    "ds_read_b128 v[232:235], v253 offset:7168\n\t" \
    "s_and_b32 s44, s40, 3\n\t" \
    "s_waitcnt lgkmcnt(2)\n\t" \
    "v_mfma_f32_16x16x32_bf16 v[216:219], v[84:87], v[224:227], v[216:219]\n\t" \
    "ds_read_b128 v[236:239], v253 offset:10240\n\t" \
    "s_lshl_b32 s44, s44, 22\n\t" \
    "v_mfma_f32_16x16x32_bf16 v[220:223], v[88:91], v[240:243], v[220:223]\n\t" \
    "s_add_u32 s44, s44, %[mb]\n\t" \
    "v_mfma_f32_16x16x32_bf16 v[220:223], v[92:95], v[224:227], v[220:223]\n\t" \
    "s_add_u32 s46, s44, 0x8000\n\t" \
    "v_mfma_f32_16x16x32_bf16 v[212:215], v[244:247], v[240:243], v[212:215]\n\t" \
    "ds_read_b128 v[244:247], v254 offset:57344\n\t" \
    "s_add_u32 s47, s44, 0x8c00\n\t" \
    "v_mfma_f32_16x16x32_bf16 v[212:215], v[248:251], v[224:227], v[212:215]\n\t" \
    "ds_read_b128 v[248:251], v254 offset:58368\n\t" \
    "s_waitcnt lgkmcnt(4)\n\t" \
    "v_mfma_f32_16x16x32_bf16 v[204:207], v[104:107], v[228:231], v[204:207]\n\t" \
    "ds_read_b128 v[240:243], v253 offset:11264\n\t" \
    "s_waitcnt lgkmcnt(4)\n\t" \
    "v_mfma_f32_16x16x32_bf16 v[204:207], v[108:111], v[232:235], v[204:207]\n\t" \
    "ds_read_b128 v[224:227], v253 offset:30720\n\t" \
    "v_mfma_f32_16x16x32_bf16 v[208:211], v[112:115], v[228:231], v[208:211]\n\t" \
    "v_mfma_f32_16x16x32_bf16 v[208:211], v[116:119], v[232:235], v[208:211]\n\t" \
    "v_mfma_f32_16x16x32_bf16 v[200:203], v[96:99], v[228:231], v[200:203]\n\t" \
    "v_mfma_f32_16x16x32_bf16 v[200:203], v[100:103], v[232:235], v[200:203]\n\t" \
    "s_waitcnt lgkmcnt(4)\n\t" \
    "v_mfma_f32_16x16x32_bf16 v[204:207], v[120:123], v[236:239], v[204:207]\n\t" \
    "ds_read_b128 v[228:231], v253 offset:31744\n\t" \
    "s_waitcnt lgkmcnt(2)\n\t" \
    "v_mfma_f32_16x16x32_bf16 v[204:207], v[124:127], v[240:243], v[204:207]\n\t" \
    "ds_read_b128 v[232:235], v253 offset:12288\n\t" \
    "v_mfma_f32_16x16x32_bf16 v[208:211], v[128:131], v[236:239], v[208:211]\n\t" \
    "v_mfma_f32_16x16x32_bf16 v[208:211], v[132:135], v[240:243], v[208:211]\n\t" \
    "v_mfma_f32_16x16x32_bf16 v[200:203], v[244:247], v[236:239], v[200:203]\n\t" \
    "v_mfma_f32_16x16x32_bf16 v[200:203], v[248:251], v[240:243], v[200:203]\n\t" \
    "s_waitcnt lgkmcnt(2)\n\t" \
    "v_mfma_f32_16x16x32_bf16 v[216:219], v[120:123], v[224:227], v[216:219]\n\t" \
    "ds_read_b128 v[236:239], v253 offset:13312\n\t" \
    "s_waitcnt lgkmcnt(2)\n\t" \
    "v_mfma_f32_16x16x32_bf16 v[216:219], v[124:127], v[228:231], v[216:219]\n\t" \
    "ds_read_b128 v[240:243], v253 offset:16384\n\t" \
    "v_mfma_f32_16x16x32_bf16 v[220:223], v[128:131], v[224:227], v[220:223]\n\t" \
    "v_mfma_f32_16x16x32_bf16 v[220:223], v[132:135], v[228:231], v[220:223]\n\t" \
    "v_mfma_f32_16x16x32_bf16 v[212:215], v[244:247], v[224:227], v[212:215]\n\t" \
    "ds_read_b128 v[244:247], v254 offset:59392\n\t" \
    "v_mfma_f32_16x16x32_bf16 v[212:215], v[248:251], v[228:231], v[212:215]\n\t" \
    "ds_read_b128 v[248:251], v254 offset:60416\n\t" \
    "v_and_b32 v252, 0xf0, v253\n\t" \
    "v_lshlrev_b32 v252, 3, v252\n\t" \
    "buffer_load_dword v252, v252, s[60:63], %[fl] offen sc1\n\t" \
    "s_waitcnt lgkmcnt(4)\n\t" \
    "v_mfma_f32_16x16x32_bf16 v[204:207], v[144:147], v[232:235], v[204:207]\n\t" \
    "ds_read_b128 v[224:227], v253 offset:17408\n\t" \
    "s_waitcnt lgkmcnt(4)\n\t" \
    "v_mfma_f32_16x16x32_bf16 v[204:207], v[148:151], v[236:239], v[204:207]\n\t" \
    "ds_read_b128 v[228:231], v253 offset:36864\n\t" \
    "v_mfma_f32_16x16x32_bf16 v[208:211], v[152:155], v[232:235], v[208:211]\n\t" \
    "v_mfma_f32_16x16x32_bf16 v[208:211], v[156:159], v[236:239], v[208:211]\n\t" \
    "v_mfma_f32_16x16x32_bf16 v[200:203], v[136:139], v[232:235], v[200:203]\n\t" \
    "v_mfma_f32_16x16x32_bf16 v[200:203], v[140:143], v[236:239], v[200:203]\n\t" \
    "s_waitcnt lgkmcnt(4)\n\t" \
    "v_mfma_f32_16x16x32_bf16 v[204:207], v[160:163], v[240:243], v[204:207]\n\t" \
    "ds_read_b128 v[232:235], v253 offset:37888\n\t" \
    "s_waitcnt lgkmcnt(2)\n\t" \
    "v_mfma_f32_16x16x32_bf16 v[204:207], v[164:167], v[224:227], v[204:207]\n\t" \
    "ds_read_b128 v[236:239], v253 offset:18432\n\t" \
    "v_mfma_f32_16x16x32_bf16 v[208:211], v[168:171], v[240:243], v[208:211]\n\t" \
    "v_mfma_f32_16x16x32_bf16 v[208:211], v[172:175], v[224:227], v[208:211]\n\t" \
    "v_mfma_f32_16x16x32_bf16 v[200:203], v[244:247], v[240:243], v[200:203]\n\t" \
    "v_mfma_f32_16x16x32_bf16 v[200:203], v[248:251], v[224:227], v[200:203]\n\t" \
    "s_waitcnt lgkmcnt(2)\n\t" \
    "v_mfma_f32_16x16x32_bf16 v[216:219], v[160:163], v[228:231], v[216:219]\n\t" \
    "ds_read_b128 v[240:243], v253 offset:19456\n\t" \
    "s_waitcnt lgkmcnt(2)\n\t" \
    "v_mfma_f32_16x16x32_bf16 v[216:219], v[164:167], v[232:235], v[216:219]\n\t" \
    "ds_read_b128 v[224:227], v253 offset:34816\n\t" \
    "v_mfma_f32_16x16x32_bf16 v[220:223], v[168:171], v[228:231], v[220:223]\n\t" \
    "v_mfma_f32_16x16x32_bf16 v[220:223], v[172:175], v[232:235], v[220:223]\n\t" \
    "v_mfma_f32_16x16x32_bf16 v[212:215], v[244:247], v[228:231], v[212:215]\n\t" \
    "v_mfma_f32_16x16x32_bf16 v[212:215], v[248:251], v[232:235], v[212:215]\n\t" \
    "s_waitcnt lgkmcnt(2)\n\t" \
    "v_mfma_f32_16x16x32_bf16 v[204:207], v[184:187], v[236:239], v[204:207]\n\t" \
    "ds_read_b128 v[228:231], v253 offset:35840\n\t" \
    "s_waitcnt lgkmcnt(2)\n\t" \
    "v_mfma_f32_16x16x32_bf16 v[204:207], v[188:191], v[240:243], v[204:207]\n\t" \
    "ds_read_b128 v[232:235], v253 offset:22528\n\t" \
    "v_mfma_f32_16x16x32_bf16 v[208:211], v[192:195], v[236:239], v[208:211]\n\t" \
    "v_mfma_f32_16x16x32_bf16 v[208:211], v[196:199], v[240:243], v[208:211]\n\t" \
    "v_mfma_f32_16x16x32_bf16 v[200:203], v[176:179], v[236:239], v[200:203]\n\t" \
    "v_mfma_f32_16x16x32_bf16 v[200:203], v[180:183], v[240:243], v[200:203]\n\t" \
    "s_waitcnt lgkmcnt(2)\n\t" \
    "v_mfma_f32_16x16x32_bf16 v[216:219], v[24:27], v[224:227], v[216:219]\n\t" \
    "ds_read_b128 v[236:239], v253 offset:23552\n\t" \
    "s_waitcnt lgkmcnt(2)\n\t" \
    "v_mfma_f32_16x16x32_bf16 v[216:219], v[28:31], v[228:231], v[216:219]\n\t" \
    "ds_read_b128 v[240:243], v253 offset:26624\n\t" \
    "v_mfma_f32_16x16x32_bf16 v[220:223], v[32:35], v[224:227], v[220:223]\n\t" \
    "v_cvt_pk_bf16_f32 v204, v204, v205\n\t" \
    "v_mfma_f32_16x16x32_bf16 v[220:223], v[36:39], v[228:231], v[220:223]\n\t" \
    "v_cvt_pk_bf16_f32 v205, v206, v207\n\t" \
    "v_mfma_f32_16x16x32_bf16 v[212:215], v[16:19], v[224:227], v[212:215]\n\t" \
    "ds_write_b64 v254, v[204:205] offset:7176\n\t" \
    "v_mfma_f32_16x16x32_bf16 v[212:215], v[20:23], v[228:231], v[212:215]\n\t" \
    "v_cvt_pk_bf16_f32 v208, v208, v209\n\t" \
    "s_waitcnt lgkmcnt(3)\n\t" \
    "v_mfma_f32_16x16x32_bf16 v[216:219], v[64:67], v[232:235], v[216:219]\n\t" \
    "ds_read_b128 v[224:227], v253 offset:27648\n\t" \
    "v_cvt_pk_bf16_f32 v209, v210, v211\n\t" \
    "s_waitcnt lgkmcnt(3)\n\t" \
    "v_mfma_f32_16x16x32_bf16 v[216:219], v[68:71], v[236:239], v[216:219]\n\t" \
    "ds_read_b128 v[228:231], v253 offset:32768\n\t" \
    "ds_write_b64 v254, v[208:209] offset:8200\n\t" \
    "v_mfma_f32_16x16x32_bf16 v[220:223], v[72:75], v[232:235], v[220:223]\n\t" \
    "v_cvt_pk_bf16_f32 v200, v200, v201\n\t" \
    "v_mfma_f32_16x16x32_bf16 v[220:223], v[76:79], v[236:239], v[220:223]\n\t" \
    "v_cvt_pk_bf16_f32 v201, v202, v203\n\t" \
    "v_mfma_f32_16x16x32_bf16 v[212:215], v[56:59], v[232:235], v[212:215]\n\t" \
    "ds_write_b64 v254, v[200:201] offset:6152\n\t" \
    "v_mfma_f32_16x16x32_bf16 v[212:215], v[60:63], v[236:239], v[212:215]\n\t" \
    "s_waitcnt lgkmcnt(5)\n\t" \
    "v_mfma_f32_16x16x32_bf16 v[216:219], v[104:107], v[240:243], v[216:219]\n\t" \
    "ds_read_b128 v[232:235], v253 offset:33792\n\t" \
    "s_waitcnt lgkmcnt(4)\n\t" \
    "v_mfma_f32_16x16x32_bf16 v[216:219], v[108:111], v[224:227], v[216:219]\n\t" \
    "ds_read_b128 v[236:239], v253 offset:38912\n\t" \
    "v_mfma_f32_16x16x32_bf16 v[220:223], v[112:115], v[240:243], v[220:223]\n\t" \
    "v_mfma_f32_16x16x32_bf16 v[220:223], v[116:119], v[224:227], v[220:223]\n\t" \
    "v_mfma_f32_16x16x32_bf16 v[212:215], v[96:99], v[240:243], v[212:215]\n\t" \
    "v_mfma_f32_16x16x32_bf16 v[212:215], v[100:103], v[224:227], v[212:215]\n\t" \
    "ds_read_b128 v[240:243], v253 offset:39936\n\t" \
    "s_waitcnt lgkmcnt(3)\n\t" \
    "s_barrier \n\t" \
    "v_mfma_f32_16x16x32_bf16 v[216:219], v[144:147], v[228:231], v[216:219]\n\t" \
    "s_waitcnt lgkmcnt(2)\n\t" \
    "v_mfma_f32_16x16x32_bf16 v[216:219], v[148:151], v[232:235], v[216:219]\n\t" \
    "s_waitcnt lgkmcnt(1)\n\t" \
    "v_mfma_f32_16x16x32_bf16 v[216:219], v[184:187], v[236:239], v[216:219]\n\t" \
    "s_waitcnt lgkmcnt(0)\n\t" \
    "v_mfma_f32_16x16x32_bf16 v[216:219], v[188:191], v[240:243], v[216:219]\n\t" \
    "v_mfma_f32_16x16x32_bf16 v[220:223], v[152:155], v[228:231], v[220:223]\n\t" \
    "v_mfma_f32_16x16x32_bf16 v[220:223], v[156:159], v[232:235], v[220:223]\n\t" \
    "v_mfma_f32_16x16x32_bf16 v[220:223], v[192:195], v[236:239], v[220:223]\n\t" \
    "v_cvt_pk_bf16_f32 v216, v216, v217\n\t" \
    "v_cvt_pk_bf16_f32 v217, v218, v219\n\t" \
    "v_mfma_f32_16x16x32_bf16 v[220:223], v[196:199], v[240:243], v[220:223]\n\t" \
    "ds_write_b64 v254, v[216:217] offset:13320\n\t" \
    "v_mfma_f32_16x16x32_bf16 v[212:215], v[136:139], v[228:231], v[212:215]\n\t" \
    "v_mfma_f32_16x16x32_bf16 v[212:215], v[140:143], v[232:235], v[212:215]\n\t" \
    "v_mfma_f32_16x16x32_bf16 v[212:215], v[176:179], v[236:239], v[212:215]\n\t" \
    "v_cvt_pk_bf16_f32 v220, v220, v221\n\t" \
    "v_cvt_pk_bf16_f32 v221, v222, v223\n\t" \
    "v_mfma_f32_16x16x32_bf16 v[212:215], v[180:183], v[240:243], v[212:215]\n\t" \
    "ds_write_b64 v254, v[220:221] offset:14344\n\t" \
    "s_nop 6\n\t" \
    "v_cvt_pk_bf16_f32 v212, v212, v213\n\t" \
    "v_cvt_pk_bf16_f32 v213, v214, v215\n\t" \
    "ds_write_b64 v254, v[212:213] offset:12296\n\t" \
    "s_waitcnt vmcnt(0) lgkmcnt(0)\n\t" \
    "s_barrier \n\t" \
    "s_setprio 2\n\t" \
    "s_add_u32 s44, s40, 1\n\t" \
    "s_min_u32 s44, s44, 63\n\t" \
    ".Lv28_%=:\n\t" \
    "v_cmp_ge_u32 vcc, s44, v252\n\t" \
    "s_cbranch_vccz .Lv29_%=\n\t" \
    "s_cmp_eq_u32 s41, 0\n\t" \
    "s_cbranch_scc1 .Lv29_%=\n\t" \
    "s_sub_u32 s41, s41, 1\n\t" \
    "s_sleep 2\n\t" \
    "v_and_b32 v252, 0xf0, v253\n\t" \
    "v_lshlrev_b32 v252, 3, v252\n\t" \
    "buffer_load_dword v252, v252, s[60:63], %[fl] offen sc1\n\t" \
    "s_waitcnt vmcnt(0)\n\t" \
    "s_branch .Lv28_%=\n\t" \
    ".Lv29_%=:\n\t" \
    "s_mov_b32 m0, 0x2000\n\t" \
    "s_add_u32 s49, s42, 0x0\n\t" \
    "buffer_load_dwordx4 v253, s[60:63], s49 offen offset:0 sc1 lds\n\t" \
    "s_sleep 1\n\t" \
    "buffer_load_dwordx4 v253, s[60:63], s49 offen offset:1024 sc1 lds\n\t" \
    "s_sleep 1\n\t" \
    "buffer_load_dwordx4 v253, s[60:63], s49 offen offset:2048 sc1 lds\n\t" \
    "s_sleep 1\n\t" \
    "buffer_load_dwordx4 v253, s[60:63], s49 offen offset:3072 sc1 lds\n\t" \
    "s_sleep 1\n\t" \
    "s_mov_b32 m0, 0x4800\n\t" \
    "s_add_u32 s49, s42, 0x8800\n\t" \
    "buffer_load_dwordx4 v253, s[60:63], s49 offen offset:0 sc1 lds\n\t" \
    "s_sleep 1\n\t" \
    "s_mov_b32 m0, 0x7000\n\t" \
    "s_add_u32 s49, s42, 0x1000\n\t" \
    "buffer_load_dwordx4 v253, s[60:63], s49 offen offset:0 sc1 lds\n\t" \
    "s_sleep 1\n\t" \
    "buffer_load_dwordx4 v253, s[60:63], s49 offen offset:1024 sc1 lds\n\t" \
    "s_sleep 1\n\t" \
    "buffer_load_dwordx4 v253, s[60:63], s49 offen offset:2048 sc1 lds\n\t" \
    "s_sleep 1\n\t" \
    "buffer_load_dwordx4 v253, s[60:63], s49 offen offset:3072 sc1 lds\n\t" \
    "s_sleep 1\n\t" \
    "s_mov_b32 m0, 0x9800\n\t" \
    "s_add_u32 s49, s42, 0x9800\n\t" \
    "buffer_load_dwordx4 v253, s[60:63], s49 offen offset:0 sc1 lds\n\t" \
    "s_sleep 1\n\t" \
    "ds_read_b128 v[200:203], v254 offset:51200\n\t" \
    "ds_read_b128 v[204:207], v254 offset:52224\n\t" \
    "ds_read_b128 v[216:219], v254 offset:53248\n\t" \
    "ds_read_b128 v[220:223], v254 offset:54272\n\t" \
    "v_mov_b64 v[208:209], 0\n\t" \
    "v_mov_b64 v[210:211], 0\n\t" \
    "v_mov_b64 v[244:245], 0\n\t" \
    "v_mov_b64 v[246:247], 0\n\t" \
    "v_mov_b64 v[248:249], 0\n\t" \
    "v_mov_b64 v[250:251], 0\n\t" \
    "s_sleep 5\n\t" \
    "s_mov_b32 m0, 0xb000\n\t" \
    "s_nop 0\n\t" \
    "buffer_load_dwordx4 v253, s[60:63], s46 offen sc1 lds\n\t" \
    "s_mov_b32 m0, 0xbc00\n\t" \
    "s_nop 0\n\t" \
    "buffer_load_dwordx4 v253, s[60:63], s47 offen sc1 lds\n\t" \
    "s_waitcnt lgkmcnt(0)\n\t" \
    "v_bfe_i32 v212, v253, 5, 1\n\t" \
    "v_and_b32 v212, 0xaaaa, v212\n\t" \
    "v_or_b32 v212, 0x4444, v212\n\t" \
    "v_and_b32 v213, 0xffff, v255\n\t" \
    "v_lshrrev_b32 v214, 16, v255\n\t" \
    ".Lv210_%=:\n\t" \
    "s_waitcnt vmcnt(0)\n\t" \
    "ds_read_b128 v[224:227], v253 offset:45056\n\t" \
    "ds_read_b128 v[228:231], v253 offset:48128\n\t" \
    "s_waitcnt lgkmcnt(1)\n\t" \
    "v_max_u32 v215, v224, v225\n\t" \
    "v_max3_u32 v215, v226, v227, v215\n\t" \
    "s_waitcnt lgkmcnt(0)\n\t" \
    "v_max3_u32 v215, v228, v229, v215\n\t" \
    "v_max3_u32 v215, v230, v231, v215\n\t" \
    "v_cmp_eq_u32 vcc, -1, v215\n\t" \
    "s_cbranch_vccz .Lv211_%=\n\t" \
    "v_max_u32 v215, v224, v225\n\t" \
    "v_max3_u32 v215, v226, v227, v215\n\t" \
    "v_cmp_eq_u32 vcc, -1, v215\n\t" \
    "s_cbranch_vccz .Lv212_%=\n\t" \
    "s_mov_b32 m0, 0xb000\n\t" \
    "s_nop 0\n\t" \
    "buffer_load_dwordx4 v253, s[60:63], s46 offen sc1 lds\n\t" \
    ".Lv212_%=:\n\t" \
    "v_max_u32 v215, v228, v229\n\t" \
    "v_max3_u32 v215, v230, v231, v215\n\t" \
    "v_cmp_eq_u32 vcc, -1, v215\n\t" \
    "s_cbranch_vccz .Lv213_%=\n\t" \
    "s_mov_b32 m0, 0xbc00\n\t" \
    "s_nop 0\n\t" \
    "buffer_load_dwordx4 v253, s[60:63], s47 offen sc1 lds\n\t" \
    ".Lv213_%=:\n\t" \
    "s_cmp_eq_u32 s41, 0\n\t" \
    "s_cbranch_scc1 .Lv211_%=\n\t" \
    "s_sub_u32 s41, s41, 1\n\t" \
    "s_branch .Lv210_%=\n\t" \
    ".Lv211_%=:\n\t" \
    "s_waitcnt lgkmcnt(0)\n\t" \
    "s_barrier \n\t" \
    "ds_read_b128 v[232:235], v253 offset:40960\n\t" \
    "ds_read_b128 v[236:239], v253 offset:41984\n\t" \
    "ds_read_b128 v[240:243], v253 offset:43008\n\t" \
    "v_smfmac_f32_16x16x64_bf16 v[244:247], v[224:227], v[0:7], v212\n\t" \
    "v_smfmac_f32_16x16x64_bf16 v[248:251], v[224:227], v[8:15], v212\n\t" \
    "v_smfmac_f32_16x16x64_bf16 v[208:211], v[224:227], v[200:207], v212\n\t" \
    "ds_read_b128 v[200:203], v254 offset:55296\n\t" \
    "ds_read_b128 v[204:207], v254 offset:56320\n\t" \
    "ds_read_b128 v[224:227], v253 offset:44032\n\t" \
    "v_smfmac_f32_16x16x64_bf16 v[208:211], v[228:231], v[16:23], v212\n\t" \
    "v_smfmac_f32_16x16x64_bf16 v[244:247], v[228:231], v[24:31], v212\n\t" \
    "v_smfmac_f32_16x16x64_bf16 v[248:251], v[228:231], v[32:39], v212\n\t" \
    "ds_read_b128 v[228:231], v253 offset:46080\n\t" \
    "s_waitcnt lgkmcnt(6)\n\t" \
    "v_smfmac_f32_16x16x64_bf16 v[208:211], v[232:235], v[216:223], v212\n\t" \
    "ds_read_b128 v[216:219], v254 offset:57344\n\t" \
    "ds_read_b128 v[220:223], v254 offset:58368\n\t" \
    "v_smfmac_f32_16x16x64_bf16 v[244:247], v[232:235], v[40:47], v212\n\t" \
    "v_smfmac_f32_16x16x64_bf16 v[248:251], v[232:235], v[48:55], v212\n\t" \
    "ds_read_b128 v[232:235], v253 offset:47104\n\t" \
    "s_waitcnt lgkmcnt(8)\n\t" \
    "v_smfmac_f32_16x16x64_bf16 v[208:211], v[236:239], v[56:63], v212\n\t" \
    "v_smfmac_f32_16x16x64_bf16 v[244:247], v[236:239], v[64:71], v212\n\t" \
    "v_smfmac_f32_16x16x64_bf16 v[248:251], v[236:239], v[72:79], v212\n\t" \
    "ds_read_b128 v[236:239], v253 offset:49152\n\t" \
    "s_waitcnt lgkmcnt(6)\n\t" \
    "v_smfmac_f32_16x16x64_bf16 v[208:211], v[240:243], v[200:207], v212\n\t" \
    "ds_read_b128 v[200:203], v254 offset:59392\n\t" \
    "ds_read_b128 v[204:207], v254 offset:60416\n\t" \
    "v_smfmac_f32_16x16x64_bf16 v[244:247], v[240:243], v[80:87], v212\n\t" \
    "v_smfmac_f32_16x16x64_bf16 v[248:251], v[240:243], v[88:95], v212\n\t" \
    "ds_read_b128 v[240:243], v253 offset:50176\n\t" \
    "s_waitcnt lgkmcnt(8)\n\t" \
    "v_smfmac_f32_16x16x64_bf16 v[208:211], v[224:227], v[96:103], v212\n\t" \
    "v_smfmac_f32_16x16x64_bf16 v[244:247], v[224:227], v[104:111], v212\n\t" \
    "v_smfmac_f32_16x16x64_bf16 v[248:251], v[224:227], v[112:119], v212\n\t" \
    "s_waitcnt lgkmcnt(7)\n\t" \
    "v_smfmac_f32_16x16x64_bf16 v[244:247], v[228:231], v[120:127], v212\n\t" \
    "s_waitcnt lgkmcnt(4)\n\t" \
    "v_smfmac_f32_16x16x64_bf16 v[244:247], v[232:235], v[144:151], v212\n\t" \
    "s_waitcnt lgkmcnt(3)\n\t" \
    "v_smfmac_f32_16x16x64_bf16 v[244:247], v[236:239], v[160:167], v212\n\t" \
    "s_waitcnt lgkmcnt(0)\n\t" \
    "v_smfmac_f32_16x16x64_bf16 v[244:247], v[240:243], v[184:191], v212\n\t" \
    "v_smfmac_f32_16x16x64_bf16 v[248:251], v[228:231], v[128:135], v212\n\t" \
    "v_smfmac_f32_16x16x64_bf16 v[248:251], v[232:235], v[152:159], v212\n\t" \
    "v_smfmac_f32_16x16x64_bf16 v[248:251], v[236:239], v[168:175], v212\n\t" \
    "v_add_f32 v244, v244, v246\n\t" \
    "v_add_f32 v245, v245, v247\n\t" \
    "v_cvt_pk_bf16_f32 v244, v244, v245\n\t" \
    "v_smfmac_f32_16x16x64_bf16 v[248:251], v[240:243], v[192:199], v212\n\t" \
    "ds_write_b16 v213, v244 offset:512\n\t" \
    "ds_write_b16_d16_hi v213, v244 offset:528\n\t" \
    "v_smfmac_f32_16x16x64_bf16 v[208:211], v[228:231], v[216:223], v212\n\t" \
    "v_smfmac_f32_16x16x64_bf16 v[208:211], v[232:235], v[136:143], v212\n\t" \
    "v_smfmac_f32_16x16x64_bf16 v[208:211], v[236:239], v[200:207], v212\n\t" \
    "v_add_f32 v248, v248, v250\n\t" \
    "v_add_f32 v249, v249, v251\n\t" \
    "v_cvt_pk_bf16_f32 v248, v248, v249\n\t" \
    "v_smfmac_f32_16x16x64_bf16 v[208:211], v[240:243], v[176:183], v212\n\t" \
    "ds_write_b16 v213, v248 offset:1024\n\t" \
    "ds_write_b16_d16_hi v213, v248 offset:1040\n\t" \
    "s_nop 5\n\t" \
    "v_add_f32 v208, v208, v210\n\t" \
    "v_add_f32 v209, v209, v211\n\t" \
    "v_cvt_pk_bf16_f32 v208, v208, v209\n\t" \
    "ds_write_b16 v213, v208 offset:0\n\t" \
    "ds_write_b16_d16_hi v213, v208 offset:16\n\t" \
    "s_setprio 0\n\t" \
    "s_waitcnt lgkmcnt(0)\n\t" \
    "s_barrier \n\t" \
    "ds_read_b128 v[224:227], v253 offset:8192\n\t" \
    "ds_read_b128 v[244:247], v254 offset:51200\n\t" \
    "ds_read_b128 v[228:231], v253 offset:9216\n\t" \
    "ds_read_b128 v[232:235], v253 offset:28672\n\t" \
    "ds_read_b128 v[236:239], v253 offset:29696\n\t" \
    "ds_read_b128 v[248:251], v254 offset:52224\n\t" \
    "s_add_u32 s40, s40, 1\n\t" \
    "s_cmp_lg_u32 s40, 64\n\t" \
    "s_cbranch_scc1 .Lv27_%=\n\t" \
    "s_branch .Lend%=\n\t" \
    ".Lv1_%=:\n\t" \
    "s_mov_b32 s42, %[xs]\n\t" \
    "v_add_u32 v244, 0xc000, v243\n\t" \
    "s_waitcnt vmcnt(12) lgkmcnt(0)\n\t" \
    "s_barrier \n\t" \
    "s_add_u32 m0, %[wm], 0x9000\n\t" \
    "s_mov_b32 s44, 0x1e0000\n\t" \
    "buffer_load_dwordx4 v240, %[wr], s44 offen offset:0 nt lds\n\t" \
    "buffer_load_dwordx4 v241, %[wr], s44 offen offset:1024 nt lds\n\t" \
    "buffer_load_dwordx4 v242, %[wr], s44 offen offset:2048 nt lds\n\t" \
    "ds_read_b64_tr_b16 v[56:57], v244 offset:0\n\t" \
    "ds_read_b64_tr_b16 v[58:59], v244 offset:1536\n\t" \
    "ds_read_b64_tr_b16 v[64:65], v244 offset:32\n\t" \
    "ds_read_b64_tr_b16 v[66:67], v244 offset:1568\n\t" \
    "ds_read_b64_tr_b16 v[72:73], v244 offset:64\n\t" \
    "ds_read_b64_tr_b16 v[74:75], v244 offset:1600\n\t" \
    "v_add_u32 v244, 0xf000, v243\n\t" \
    "s_waitcnt vmcnt(12) lgkmcnt(0)\n\t" \
    "s_barrier \n\t" \
    "s_add_u32 m0, %[wm], 0xc000\n\t" \
    "s_mov_b32 s44, 0x240000\n\t" \
    "buffer_load_dwordx4 v240, %[wr], s44 offen offset:0 nt lds\n\t" \
    "buffer_load_dwordx4 v241, %[wr], s44 offen offset:1024 nt lds\n\t" \
    "buffer_load_dwordx4 v242, %[wr], s44 offen offset:2048 nt lds\n\t" \
    "ds_read_b64_tr_b16 v[60:61], v244 offset:0\n\t" \
    "ds_read_b64_tr_b16 v[62:63], v244 offset:1536\n\t" \
    "ds_read_b64_tr_b16 v[68:69], v244 offset:32\n\t" \
    "ds_read_b64_tr_b16 v[70:71], v244 offset:1568\n\t" \
    "ds_read_b64_tr_b16 v[76:77], v244 offset:64\n\t" \
    "ds_read_b64_tr_b16 v[78:79], v244 offset:1600\n\t" \
    "v_add_u32 v244, 0x0, v243\n\t" \
    "s_waitcnt vmcnt(12) lgkmcnt(0)\n\t" \
    "s_barrier \n\t" \
    "s_add_u32 m0, %[wm], 0xf000\n\t" \
    "s_mov_b32 s44, 0x2a0000\n\t" \
    "buffer_load_dwordx4 v240, %[wr], s44 offen offset:0 nt lds\n\t" \
    "buffer_load_dwordx4 v241, %[wr], s44 offen offset:1024 nt lds\n\t" \
    "buffer_load_dwordx4 v242, %[wr], s44 offen offset:2048 nt lds\n\t" \
    "ds_read_b64_tr_b16 v[96:97], v244 offset:0\n\t" \
    "ds_read_b64_tr_b16 v[98:99], v244 offset:1536\n\t" \
    "ds_read_b64_tr_b16 v[104:105], v244 offset:32\n\t" \
    "ds_read_b64_tr_b16 v[106:107], v244 offset:1568\n\t" \
    "ds_read_b64_tr_b16 v[112:113], v244 offset:64\n\t" \
    "ds_read_b64_tr_b16 v[114:115], v244 offset:1600\n\t" \
    "v_add_u32 v244, 0x3000, v243\n\t" \
    "s_waitcnt vmcnt(12) lgkmcnt(0)\n\t" \
    "s_barrier \n\t" \
    "s_add_u32 m0, %[wm], 0x0\n\t" \
    "s_mov_b32 s44, 0x300000\n\t" \
    "buffer_load_dwordx4 v240, %[wr], s44 offen offset:0 nt lds\n\t" \
    "buffer_load_dwordx4 v241, %[wr], s44 offen offset:1024 nt lds\n\t" \
    "buffer_load_dwordx4 v242, %[wr], s44 offen offset:2048 nt lds\n\t" \
    "ds_read_b64_tr_b16 v[100:101], v244 offset:0\n\t" \
    "ds_read_b64_tr_b16 v[102:103], v244 offset:1536\n\t" \
    "ds_read_b64_tr_b16 v[108:109], v244 offset:32\n\t" \
    "ds_read_b64_tr_b16 v[110:111], v244 offset:1568\n\t" \
    "ds_read_b64_tr_b16 v[116:117], v244 offset:64\n\t" \
    "ds_read_b64_tr_b16 v[118:119], v244 offset:1600\n\t" \
    "v_add_u32 v244, 0x6000, v243\n\t" \
    "s_waitcnt vmcnt(12) lgkmcnt(0)\n\t" \
    "s_barrier \n\t" \
    "s_add_u32 m0, %[wm], 0x3000\n\t" \
    "s_mov_b32 s44, 0x360000\n\t" \
    "buffer_load_dwordx4 v240, %[wr], s44 offen offset:0 nt lds\n\t" \
    "buffer_load_dwordx4 v241, %[wr], s44 offen offset:1024 nt lds\n\t" \
    "buffer_load_dwordx4 v242, %[wr], s44 offen offset:2048 nt lds\n\t" \
    "ds_read_b64_tr_b16 v[200:201], v244 offset:0\n\t" \
    "ds_read_b64_tr_b16 v[202:203], v244 offset:1536\n\t" \
    "ds_read_b64_tr_b16 v[0:1], v244 offset:32\n\t" \
    "ds_read_b64_tr_b16 v[2:3], v244 offset:1568\n\t" \
    "ds_read_b64_tr_b16 v[8:9], v244 offset:64\n\t" \
    "ds_read_b64_tr_b16 v[10:11], v244 offset:1600\n\t" \
    "v_add_u32 v244, 0x9000, v243\n\t" \
    "s_waitcnt vmcnt(12) lgkmcnt(0)\n\t" \
    "s_barrier \n\t" \
    "s_add_u32 m0, %[wm], 0x6000\n\t" \
    "s_mov_b32 s44, 0x3c0000\n\t" \
    "buffer_load_dwordx4 v240, %[wr], s44 offen offset:0 nt lds\n\t" \
    "buffer_load_dwordx4 v241, %[wr], s44 offen offset:1024 nt lds\n\t" \
    "buffer_load_dwordx4 v242, %[wr], s44 offen offset:2048 nt lds\n\t" \
    "ds_read_b64_tr_b16 v[204:205], v244 offset:0\n\t" \
    "ds_read_b64_tr_b16 v[206:207], v244 offset:1536\n\t" \
    "ds_read_b64_tr_b16 v[4:5], v244 offset:32\n\t" \
    "ds_read_b64_tr_b16 v[6:7], v244 offset:1568\n\t" \
    "ds_read_b64_tr_b16 v[12:13], v244 offset:64\n\t" \
    "ds_read_b64_tr_b16 v[14:15], v244 offset:1600\n\t" \
    "v_add_u32 v244, 0xc000, v243\n\t" \
    "s_waitcnt vmcnt(12) lgkmcnt(0)\n\t" \
    "s_barrier \n\t" \
    "s_add_u32 m0, %[wm], 0x9000\n\t" \
    "s_mov_b32 s44, 0x420000\n\t" \
    "buffer_load_dwordx4 v240, %[wr], s44 offen offset:0 nt lds\n\t" \
    "buffer_load_dwordx4 v241, %[wr], s44 offen offset:1024 nt lds\n\t" \
    "buffer_load_dwordx4 v242, %[wr], s44 offen offset:2048 nt lds\n\t" \
    "ds_read_b64_tr_b16 v[208:209], v244 offset:0\n\t" \
    "ds_read_b64_tr_b16 v[210:211], v244 offset:1536\n\t" \
    "ds_read_b64_tr_b16 v[40:41], v244 offset:32\n\t" \
    "ds_read_b64_tr_b16 v[42:43], v244 offset:1568\n\t" \
    "ds_read_b64_tr_b16 v[48:49], v244 offset:64\n\t" \
    "ds_read_b64_tr_b16 v[50:51], v244 offset:1600\n\t" \
    "v_add_u32 v244, 0xf000, v243\n\t" \
    "s_waitcnt vmcnt(12) lgkmcnt(0)\n\t" \
    "s_barrier \n\t" \
    "s_add_u32 m0, %[wm], 0xc000\n\t" \
    "s_mov_b32 s44, 0x480000\n\t" \
    "buffer_load_dwordx4 v240, %[wr], s44 offen offset:0 nt lds\n\t" \
    "buffer_load_dwordx4 v241, %[wr], s44 offen offset:1024 nt lds\n\t" \
    "buffer_load_dwordx4 v242, %[wr], s44 offen offset:2048 nt lds\n\t" \
    "ds_read_b64_tr_b16 v[212:213], v244 offset:0\n\t" \
    "ds_read_b64_tr_b16 v[214:215], v244 offset:1536\n\t" \
    "ds_read_b64_tr_b16 v[44:45], v244 offset:32\n\t" \
    "ds_read_b64_tr_b16 v[46:47], v244 offset:1568\n\t" \
    "ds_read_b64_tr_b16 v[52:53], v244 offset:64\n\t" \
    "ds_read_b64_tr_b16 v[54:55], v244 offset:1600\n\t" \
    "v_add_u32 v244, 0x0, v243\n\t" \
    "s_waitcnt vmcnt(12) lgkmcnt(0)\n\t" \
    "s_barrier \n\t" \
    "v_mov_b32 v246, 0\n\t" \
    "buffer_load_dword v246, v246, s[60:63], %[mo] offen sc1\n\t" \
    "s_add_u32 m0, %[wm], 0xf000\n\t" \
    "s_mov_b32 s44, 0x4e0000\n\t" \
    "buffer_load_dwordx4 v240, %[wr], s44 offen offset:0 nt lds\n\t" \
    "buffer_load_dwordx4 v241, %[wr], s44 offen offset:1024 nt lds\n\t" \
    "buffer_load_dwordx4 v242, %[wr], s44 offen offset:2048 nt lds\n\t" \
    "ds_read_b64_tr_b16 v[136:137], v244 offset:0\n\t" \
    "ds_read_b64_tr_b16 v[138:139], v244 offset:1536\n\t" \
    "ds_read_b64_tr_b16 v[144:145], v244 offset:32\n\t" \
    "ds_read_b64_tr_b16 v[146:147], v244 offset:1568\n\t" \
    "ds_read_b64_tr_b16 v[152:153], v244 offset:64\n\t" \
    "ds_read_b64_tr_b16 v[154:155], v244 offset:1600\n\t" \
    "v_add_u32 v244, 0x3000, v243\n\t" \
    "s_waitcnt vmcnt(12) lgkmcnt(0)\n\t" \
    "s_barrier \n\t" \
    "s_add_u32 m0, %[wm], 0x0\n\t" \
    "s_mov_b32 s44, 0x540000\n\t" \
    "buffer_load_dwordx4 v240, %[wr], s44 offen offset:0 nt lds\n\t" \
    "buffer_load_dwordx4 v241, %[wr], s44 offen offset:1024 nt lds\n\t" \
    "buffer_load_dwordx4 v242, %[wr], s44 offen offset:2048 nt lds\n\t" \
    "ds_read_b64_tr_b16 v[140:141], v244 offset:0\n\t" \
    "ds_read_b64_tr_b16 v[142:143], v244 offset:1536\n\t" \
    "ds_read_b64_tr_b16 v[148:149], v244 offset:32\n\t" \
    "ds_read_b64_tr_b16 v[150:151], v244 offset:1568\n\t" \
    "ds_read_b64_tr_b16 v[156:157], v244 offset:64\n\t" \
    "ds_read_b64_tr_b16 v[158:159], v244 offset:1600\n\t" \
    "v_add_u32 v244, 0x6000, v243\n\t" \
    "s_waitcnt vmcnt(12) lgkmcnt(0)\n\t" \
    "s_barrier \n\t" \
    "s_add_u32 m0, %[wm], 0x3000\n\t" \
    "s_mov_b32 s44, 0x5a0000\n\t" \
    "buffer_load_dwordx4 v240, %[wr], s44 offen offset:0 nt lds\n\t" \
    "buffer_load_dwordx4 v241, %[wr], s44 offen offset:1024 nt lds\n\t" \
    "buffer_load_dwordx4 v242, %[wr], s44 offen offset:2048 nt lds\n\t" \
    "ds_read_b64_tr_b16 v[216:217], v244 offset:0\n\t" \
    "ds_read_b64_tr_b16 v[218:219], v244 offset:1536\n\t" \
    "ds_read_b64_tr_b16 v[80:81], v244 offset:32\n\t" \
    "ds_read_b64_tr_b16 v[82:83], v244 offset:1568\n\t" \
    "ds_read_b64_tr_b16 v[88:89], v244 offset:64\n\t" \
    "ds_read_b64_tr_b16 v[90:91], v244 offset:1600\n\t" \
    "v_add_u32 v244, 0x9000, v243\n\t" \
    "s_waitcnt vmcnt(12) lgkmcnt(0)\n\t" \
    "s_barrier \n\t" \
    "s_add_u32 m0, %[wm], 0x6000\n\t" \
    "s_mov_b32 s44, 0x600000\n\t" \
    "buffer_load_dwordx4 v240, %[wr], s44 offen offset:0 nt lds\n\t" \
    "buffer_load_dwordx4 v241, %[wr], s44 offen offset:1024 nt lds\n\t" \
    "buffer_load_dwordx4 v242, %[wr], s44 offen offset:2048 nt lds\n\t" \
    "ds_read_b64_tr_b16 v[220:221], v244 offset:0\n\t" \
    "ds_read_b64_tr_b16 v[222:223], v244 offset:1536\n\t" \
    "ds_read_b64_tr_b16 v[84:85], v244 offset:32\n\t" \
    "ds_read_b64_tr_b16 v[86:87], v244 offset:1568\n\t" \
    "ds_read_b64_tr_b16 v[92:93], v244 offset:64\n\t" \
    "ds_read_b64_tr_b16 v[94:95], v244 offset:1600\n\t" \
    "v_add_u32 v244, 0xc000, v243\n\t" \
    "s_waitcnt vmcnt(12) lgkmcnt(0)\n\t" \
    "s_barrier \n\t" \
    "s_add_u32 m0, %[wm], 0x9000\n\t" \
    "s_mov_b32 s44, 0x660000\n\t" \
    "buffer_load_dwordx4 v240, %[wr], s44 offen offset:0 nt lds\n\t" \
    "buffer_load_dwordx4 v241, %[wr], s44 offen offset:1024 nt lds\n\t" \
    "buffer_load_dwordx4 v242, %[wr], s44 offen offset:2048 nt lds\n\t" \
    "ds_read_b64_tr_b16 v[16:17], v244 offset:0\n\t" \
    "ds_read_b64_tr_b16 v[18:19], v244 offset:1536\n\t" \
    "ds_read_b64_tr_b16 v[24:25], v244 offset:32\n\t" \
    "ds_read_b64_tr_b16 v[26:27], v244 offset:1568\n\t" \
    "ds_read_b64_tr_b16 v[32:33], v244 offset:64\n\t" \
    "ds_read_b64_tr_b16 v[34:35], v244 offset:1600\n\t" \
    "v_add_u32 v244, 0xf000, v243\n\t" \
    "s_waitcnt vmcnt(12) lgkmcnt(0)\n\t" \
    "s_barrier \n\t" \
    ".Lv11_%=:\n\t" \
    "v_readfirstlane_b32 s43, v246\n\t" \
    "s_nop 0\n\t" \
    "s_cmp_lg_u32 s43, -1\n\t" \
    "s_cbranch_scc1 .Lv12_%=\n\t" \
    "s_cmp_eq_u32 s41, 0\n\t" \
    "s_cbranch_scc1 .Lv12_%=\n\t" \
    "s_sub_u32 s41, s41, 1\n\t" \
    "s_sleep 4\n\t" \
    "v_mov_b32 v246, 0\n\t" \
    "buffer_load_dword v246, v246, s[60:63], %[mo] offen sc1\n\t" \
    "s_waitcnt vmcnt(0)\n\t" \
    "s_branch .Lv11_%=\n\t" \
    ".Lv12_%=:\n\t" \
    "v_mov_b32 v246, 0x8000\n\t" \
    "s_mov_b64 exec, 1\n\t" \
    "buffer_store_dword v246, v246, s[60:63], %[mo] offen sc1\n\t" \
    "s_mov_b64 exec, -1\n\t" \
    "s_and_b32 s44, s43, 0x5000\n\t" \
    "s_xor_b32 %[mb], %[mb], s44\n\t" \
    "s_xor_b32 %[fl], %[fl], s44\n\t" \
    "s_and_b32 s43, s43, 0x2000\n\t" \
    "s_xor_b32 s43, s43, %[lx]\n\t" \
    "s_add_u32 s60, s60, s43\n\t" \
    "s_addc_u32 s61, s61, 0\n\t" \
    "v_and_b32 v252, 0xf0, v253\n\t" \
    "v_lshlrev_b32 v252, 3, v252\n\t" \
    "buffer_load_dword v252, v252, s[60:63], %[fl] offen sc1\n\t" \
    "s_add_u32 m0, %[wm], 0xc000\n\t" \
    "s_mov_b32 s44, 0x6c0000\n\t" \
    "buffer_load_dwordx4 v240, %[wr], s44 offen offset:0 nt lds\n\t" \
    "buffer_load_dwordx4 v241, %[wr], s44 offen offset:1024 nt lds\n\t" \
    "buffer_load_dwordx4 v242, %[wr], s44 offen offset:2048 nt lds\n\t" \
    "ds_read_b64_tr_b16 v[20:21], v244 offset:0\n\t" \
    "ds_read_b64_tr_b16 v[22:23], v244 offset:1536\n\t" \
    "ds_read_b64_tr_b16 v[28:29], v244 offset:32\n\t" \
    "ds_read_b64_tr_b16 v[30:31], v244 offset:1568\n\t" \
    "ds_read_b64_tr_b16 v[36:37], v244 offset:64\n\t" \
    "ds_read_b64_tr_b16 v[38:39], v244 offset:1600\n\t" \
    "v_add_u32 v244, 0x0, v243\n\t" \
    "s_waitcnt vmcnt(12) lgkmcnt(0)\n\t" \
    "s_barrier \n\t" \
    "s_add_u32 m0, %[wm], 0xf000\n\t" \
    "s_mov_b32 s44, 0x720000\n\t" \
    "buffer_load_dwordx4 v240, %[wr], s44 offen offset:0 nt lds\n\t" \
    "buffer_load_dwordx4 v241, %[wr], s44 offen offset:1024 nt lds\n\t" \
    "buffer_load_dwordx4 v242, %[wr], s44 offen offset:2048 nt lds\n\t" \
    "ds_read_b64_tr_b16 v[224:225], v244 offset:0\n\t" \
    "ds_read_b64_tr_b16 v[226:227], v244 offset:1536\n\t" \
    "ds_read_b64_tr_b16 v[120:121], v244 offset:32\n\t" \
    "ds_read_b64_tr_b16 v[122:123], v244 offset:1568\n\t" \
    "ds_read_b64_tr_b16 v[128:129], v244 offset:64\n\t" \
    "ds_read_b64_tr_b16 v[130:131], v244 offset:1600\n\t" \
    "v_add_u32 v244, 0x3000, v243\n\t" \
    "s_waitcnt vmcnt(12) lgkmcnt(0)\n\t" \
    "s_barrier \n\t" \
    "ds_read_b64_tr_b16 v[228:229], v244 offset:0\n\t" \
    "ds_read_b64_tr_b16 v[230:231], v244 offset:1536\n\t" \
    "ds_read_b64_tr_b16 v[124:125], v244 offset:32\n\t" \
    "ds_read_b64_tr_b16 v[126:127], v244 offset:1568\n\t" \
    "ds_read_b64_tr_b16 v[132:133], v244 offset:64\n\t" \
    "ds_read_b64_tr_b16 v[134:135], v244 offset:1600\n\t" \
    "v_add_u32 v244, 0x6000, v243\n\t" \
    "s_waitcnt vmcnt(9) lgkmcnt(0)\n\t" \
    "s_barrier \n\t" \
    "ds_read_b64_tr_b16 v[232:233], v244 offset:0\n\t" \
    "ds_read_b64_tr_b16 v[234:235], v244 offset:1536\n\t" \
    "ds_read_b64_tr_b16 v[160:161], v244 offset:32\n\t" \
    "ds_read_b64_tr_b16 v[162:163], v244 offset:1568\n\t" \
    "ds_read_b64_tr_b16 v[168:169], v244 offset:64\n\t" \
    "ds_read_b64_tr_b16 v[170:171], v244 offset:1600\n\t" \
    "v_add_u32 v244, 0x9000, v243\n\t" \
    "s_waitcnt vmcnt(6) lgkmcnt(0)\n\t" \
    "s_barrier \n\t" \
    "ds_read_b64_tr_b16 v[236:237], v244 offset:0\n\t" \
    "ds_read_b64_tr_b16 v[238:239], v244 offset:1536\n\t" \
    "ds_read_b64_tr_b16 v[164:165], v244 offset:32\n\t" \
    "ds_read_b64_tr_b16 v[166:167], v244 offset:1568\n\t" \
    "ds_read_b64_tr_b16 v[172:173], v244 offset:64\n\t" \
    "ds_read_b64_tr_b16 v[174:175], v244 offset:1600\n\t" \
    "v_add_u32 v244, 0xc000, v243\n\t" \
    "s_waitcnt vmcnt(3) lgkmcnt(0)\n\t" \
    "s_barrier \n\t" \
    "ds_read_b64_tr_b16 v[176:177], v244 offset:0\n\t" \
    "ds_read_b64_tr_b16 v[178:179], v244 offset:1536\n\t" \
    "ds_read_b64_tr_b16 v[184:185], v244 offset:32\n\t" \
    "ds_read_b64_tr_b16 v[186:187], v244 offset:1568\n\t" \
    "ds_read_b64_tr_b16 v[192:193], v244 offset:64\n\t" \
    "ds_read_b64_tr_b16 v[194:195], v244 offset:1600\n\t" \
    "s_mov_b32 s43, 0\n\t" \
    "v_cmp_eq_u32 vcc, 0, v252\n\t" \
    "s_cbranch_vccnz .Lv13_%=\n\t" \
    "s_mov_b32 m0, 0x1000\n\t" \
    "s_add_u32 s49, s42, 0x0\n\t" \
    "buffer_load_dwordx4 v253, s[60:63], s49 offen offset:0 sc1 lds\n\t" \
    "buffer_load_dwordx4 v253, s[60:63], s49 offen offset:1024 sc1 lds\n\t" \
    "buffer_load_dwordx4 v253, s[60:63], s49 offen offset:2048 sc1 lds\n\t" \
    "buffer_load_dwordx4 v253, s[60:63], s49 offen offset:3072 sc1 lds\n\t" \
    "s_mov_b32 m0, 0x4400\n\t" \
    "s_add_u32 s49, s42, 0xc400\n\t" \
    "buffer_load_dwordx4 v253, s[60:63], s49 offen offset:0 sc1 lds\n\t" \
    "s_mov_b32 m0, 0x6000\n\t" \
    "s_add_u32 s49, s42, 0x1000\n\t" \
    "buffer_load_dwordx4 v253, s[60:63], s49 offen offset:0 sc1 lds\n\t" \
    "buffer_load_dwordx4 v253, s[60:63], s49 offen offset:1024 sc1 lds\n\t" \
    "buffer_load_dwordx4 v253, s[60:63], s49 offen offset:2048 sc1 lds\n\t" \
    "buffer_load_dwordx4 v253, s[60:63], s49 offen offset:3072 sc1 lds\n\t" \
    "s_mov_b32 m0, 0x9400\n\t" \
    "s_add_u32 s49, s42, 0xd400\n\t" \
    "buffer_load_dwordx4 v253, s[60:63], s49 offen offset:0 sc1 lds\n\t" \
    "s_mov_b32 s43, 1\n\t" \
    ".Lv13_%=:\n\t" \
    "v_add_u32 v244, 0xf000, v243\n\t" \
    "s_waitcnt vmcnt(0) lgkmcnt(0)\n\t" \
    "s_barrier \n\t" \
    "ds_read_b64_tr_b16 v[180:181], v244 offset:0\n\t" \
    "ds_read_b64_tr_b16 v[182:183], v244 offset:1536\n\t" \
    "ds_read_b64_tr_b16 v[188:189], v244 offset:32\n\t" \
    "ds_read_b64_tr_b16 v[190:191], v244 offset:1568\n\t" \
    "ds_read_b64_tr_b16 v[196:197], v244 offset:64\n\t" \
    "ds_read_b64_tr_b16 v[198:199], v244 offset:1600\n\t" \
    "s_waitcnt lgkmcnt(0)\n\t" \
    "s_barrier \n\t" \
    "ds_write_b128 v254, v[200:203] offset:40960\n\t" \
    "ds_write_b128 v254, v[204:207] offset:41984\n\t" \
    "ds_write_b128 v254, v[208:211] offset:43008\n\t" \
    "ds_write_b128 v254, v[212:215] offset:44032\n\t" \
    "ds_write_b128 v254, v[216:219] offset:45056\n\t" \
    "ds_write_b128 v254, v[220:223] offset:46080\n\t" \
    "ds_write_b128 v254, v[224:227] offset:47104\n\t" \
    "ds_write_b128 v254, v[228:231] offset:48128\n\t" \
    "ds_write_b128 v254, v[232:235] offset:49152\n\t" \
    "ds_write_b128 v254, v[236:239] offset:50176\n\t" \
    "s_cmp_lg_u32 s43, 0\n\t" \
    "s_cbranch_scc1 .Lv14_%=\n\t" \
    "s_mov_b32 s44, 0\n\t" \
    ".Lv15_%=:\n\t" \
    "v_cmp_ge_u32 vcc, s44, v252\n\t" \
    "s_cbranch_vccz .Lv16_%=\n\t" \
    "s_cmp_eq_u32 s41, 0\n\t" \
    "s_cbranch_scc1 .Lv16_%=\n\t" \
    "s_sub_u32 s41, s41, 1\n\t" \
    "s_sleep 8\n\t" \
    "v_and_b32 v252, 0xf0, v253\n\t" \
    "v_lshlrev_b32 v252, 3, v252\n\t" \
    "buffer_load_dword v252, v252, s[60:63], %[fl] offen sc1\n\t" \
    "s_waitcnt vmcnt(0)\n\t" \
    "s_branch .Lv15_%=\n\t" \
    ".Lv16_%=:\n\t" \
    "s_mov_b32 m0, 0x1000\n\t" \
    "s_add_u32 s49, s42, 0x0\n\t" \
    "buffer_load_dwordx4 v253, s[60:63], s49 offen offset:0 sc1 lds\n\t" \
    "buffer_load_dwordx4 v253, s[60:63], s49 offen offset:1024 sc1 lds\n\t" \
    "buffer_load_dwordx4 v253, s[60:63], s49 offen offset:2048 sc1 lds\n\t" \
    "buffer_load_dwordx4 v253, s[60:63], s49 offen offset:3072 sc1 lds\n\t" \
    "s_mov_b32 m0, 0x4400\n\t" \
    "s_add_u32 s49, s42, 0xc400\n\t" \
    "buffer_load_dwordx4 v253, s[60:63], s49 offen offset:0 sc1 lds\n\t" \
    "s_mov_b32 m0, 0x6000\n\t" \
    "s_add_u32 s49, s42, 0x1000\n\t" \
    "buffer_load_dwordx4 v253, s[60:63], s49 offen offset:0 sc1 lds\n\t" \
    "buffer_load_dwordx4 v253, s[60:63], s49 offen offset:1024 sc1 lds\n\t" \
    "buffer_load_dwordx4 v253, s[60:63], s49 offen offset:2048 sc1 lds\n\t" \
    "buffer_load_dwordx4 v253, s[60:63], s49 offen offset:3072 sc1 lds\n\t" \
    "s_mov_b32 m0, 0x9400\n\t" \
    "s_add_u32 s49, s42, 0xd400\n\t" \
    "buffer_load_dwordx4 v253, s[60:63], s49 offen offset:0 sc1 lds\n\t" \
    ".Lv14_%=:\n\t" \
    "s_waitcnt vmcnt(0) lgkmcnt(0)\n\t" \
    "s_barrier \n\t" \
    "ds_read_b128 v[224:227], v253 offset:4096\n\t" \
    "ds_read_b128 v[244:247], v254 offset:40960\n\t" \
    "ds_read_b128 v[228:231], v253 offset:5120\n\t" \
    "ds_read_b128 v[232:235], v253 offset:24576\n\t" \
    "ds_read_b128 v[236:239], v253 offset:25600\n\t" \
    "ds_read_b128 v[248:251], v254 offset:41984\n\t" \
    ".p2align 8\n\t" \
    ".Lv17_%=:\n\t" \
    "s_waitcnt lgkmcnt(5)\n\t" \
    "v_mfma_f32_16x16x32_bf16 v[204:207], v[0:3], v[224:227], 0\n\t" \
    "s_waitcnt lgkmcnt(3)\n\t" \
    "v_mfma_f32_16x16x32_bf16 v[204:207], v[4:7], v[228:231], v[204:207]\n\t" \
    "ds_read_b128 v[240:243], v253 offset:12288\n\t" \
    "v_mfma_f32_16x16x32_bf16 v[208:211], v[8:11], v[224:227], 0\n\t" \
    "v_mfma_f32_16x16x32_bf16 v[208:211], v[12:15], v[228:231], v[208:211]\n\t" \
    "v_mfma_f32_16x16x32_bf16 v[200:203], v[244:247], v[224:227], 0\n\t" \
    "s_waitcnt lgkmcnt(1)\n\t" \
    "v_mfma_f32_16x16x32_bf16 v[200:203], v[248:251], v[228:231], v[200:203]\n\t" \
    "v_mfma_f32_16x16x32_bf16 v[216:219], v[0:3], v[232:235], 0\n\t" \
    "ds_read_b128 v[224:227], v253 offset:13312\n\t" \
    "v_mfma_f32_16x16x32_bf16 v[216:219], v[4:7], v[236:239], v[216:219]\n\t" \
    "ds_read_b128 v[228:231], v253 offset:6144\n\t" \
    "v_mfma_f32_16x16x32_bf16 v[220:223], v[8:11], v[232:235], 0\n\t" \
    "v_mfma_f32_16x16x32_bf16 v[220:223], v[12:15], v[236:239], v[220:223]\n\t" \
    "v_mfma_f32_16x16x32_bf16 v[212:215], v[244:247], v[232:235], 0\n\t" \
    "ds_read_b128 v[244:247], v254 offset:43008\n\t" \
    "v_mfma_f32_16x16x32_bf16 v[212:215], v[248:251], v[236:239], v[212:215]\n\t" \
    "ds_read_b128 v[248:251], v254 offset:44032\n\t" \
    "s_waitcnt lgkmcnt(4)\n\t" \
    "v_mfma_f32_16x16x32_bf16 v[204:207], v[24:27], v[240:243], v[204:207]\n\t" \
    "ds_read_b128 v[232:235], v253 offset:7168\n\t" \
    "s_waitcnt lgkmcnt(4)\n\t" \
    "v_mfma_f32_16x16x32_bf16 v[204:207], v[28:31], v[224:227], v[204:207]\n\t" \
    "ds_read_b128 v[236:239], v253 offset:26624\n\t" \
    "v_mfma_f32_16x16x32_bf16 v[208:211], v[32:35], v[240:243], v[208:211]\n\t" \
    "v_mfma_f32_16x16x32_bf16 v[208:211], v[36:39], v[224:227], v[208:211]\n\t" \
    "v_mfma_f32_16x16x32_bf16 v[200:203], v[16:19], v[240:243], v[200:203]\n\t" \
    "v_mfma_f32_16x16x32_bf16 v[200:203], v[20:23], v[224:227], v[200:203]\n\t" \
    "s_waitcnt lgkmcnt(4)\n\t" \
    "v_mfma_f32_16x16x32_bf16 v[204:207], v[40:43], v[228:231], v[204:207]\n\t" \
    "ds_read_b128 v[240:243], v253 offset:27648\n\t" \
    "s_waitcnt lgkmcnt(2)\n\t" \
    "v_mfma_f32_16x16x32_bf16 v[204:207], v[44:47], v[232:235], v[204:207]\n\t" \
    "ds_read_b128 v[224:227], v253 offset:0\n\t" \
    "v_mfma_f32_16x16x32_bf16 v[208:211], v[48:51], v[228:231], v[208:211]\n\t" \
    "v_mfma_f32_16x16x32_bf16 v[208:211], v[52:55], v[232:235], v[208:211]\n\t" \
    "v_mfma_f32_16x16x32_bf16 v[200:203], v[244:247], v[228:231], v[200:203]\n\t" \
    "v_mfma_f32_16x16x32_bf16 v[200:203], v[248:251], v[232:235], v[200:203]\n\t" \
    "s_waitcnt lgkmcnt(2)\n\t" \
    "v_mfma_f32_16x16x32_bf16 v[216:219], v[40:43], v[236:239], v[216:219]\n\t" \
    "ds_read_b128 v[228:231], v253 offset:1024\n\t" \
    "s_add_u32 s44, s40, 1\n\t" \
    "s_waitcnt lgkmcnt(2)\n\t" \
    "v_mfma_f32_16x16x32_bf16 v[216:219], v[44:47], v[240:243], v[216:219]\n\t" \
    "ds_read_b128 v[232:235], v253 offset:10240\n\t" \
    "s_and_b32 s45, s44, 15\n\t" \
    "v_mfma_f32_16x16x32_bf16 v[220:223], v[48:51], v[236:239], v[220:223]\n\t" \
    "s_cmp_eq_u32 s45, 0\n\t" \
    "v_mfma_f32_16x16x32_bf16 v[220:223], v[52:55], v[240:243], v[220:223]\n\t" \
    "s_cselect_b32 s45, %[cj], %[cs]\n\t" \
    "v_mfma_f32_16x16x32_bf16 v[212:215], v[244:247], v[236:239], v[212:215]\n\t" \
    "ds_read_b128 v[244:247], v254 offset:45056\n\t" \
    "s_cmp_eq_u32 s44, 64\n\t" \
    "v_mfma_f32_16x16x32_bf16 v[212:215], v[248:251], v[240:243], v[212:215]\n\t" \
    "ds_read_b128 v[248:251], v254 offset:46080\n\t" \
    "s_cselect_b32 s45, 0, s45\n\t" \
    "s_waitcnt lgkmcnt(4)\n\t" \
    "v_mfma_f32_16x16x32_bf16 v[204:207], v[64:67], v[224:227], v[204:207]\n\t" \
    "ds_read_b128 v[236:239], v253 offset:11264\n\t" \
    "s_add_u32 s42, s42, s45\n\t" \
    "s_waitcnt lgkmcnt(4)\n\t" \
    "v_mfma_f32_16x16x32_bf16 v[204:207], v[68:71], v[228:231], v[204:207]\n\t" \
    "ds_read_b128 v[240:243], v253 offset:30720\n\t" \
    "v_mfma_f32_16x16x32_bf16 v[208:211], v[72:75], v[224:227], v[208:211]\n\t" \
    "v_mfma_f32_16x16x32_bf16 v[208:211], v[76:79], v[228:231], v[208:211]\n\t" \
    "v_mfma_f32_16x16x32_bf16 v[200:203], v[56:59], v[224:227], v[200:203]\n\t" \
    "v_mfma_f32_16x16x32_bf16 v[200:203], v[60:63], v[228:231], v[200:203]\n\t" \
    "s_waitcnt lgkmcnt(4)\n\t" \
    "v_mfma_f32_16x16x32_bf16 v[204:207], v[80:83], v[232:235], v[204:207]\n\t" \
    "ds_read_b128 v[224:227], v253 offset:31744\n\t" \
    "s_waitcnt lgkmcnt(2)\n\t" \
    "v_mfma_f32_16x16x32_bf16 v[204:207], v[84:87], v[236:239], v[204:207]\n\t" \
    "ds_read_b128 v[228:231], v253 offset:2048\n\t" \
    "v_mfma_f32_16x16x32_bf16 v[208:211], v[88:91], v[232:235], v[208:211]\n\t" \
    "v_mfma_f32_16x16x32_bf16 v[208:211], v[92:95], v[236:239], v[208:211]\n\t" \
    "v_mfma_f32_16x16x32_bf16 v[200:203], v[244:247], v[232:235], v[200:203]\n\t" \
    "v_mfma_f32_16x16x32_bf16 v[200:203], v[248:251], v[236:239], v[200:203]\n\t" \
    "s_waitcnt lgkmcnt(2)\n\t" \
    "v_mfma_f32_16x16x32_bf16 v[216:219], v[80:83], v[240:243], v[216:219]\n\t" \
    "ds_read_b128 v[232:235], v253 offset:3072\n\t" \
    "s_and_b32 s44, s40, 3\n\t" \
    "s_waitcnt lgkmcnt(2)\n\t" \
    "v_mfma_f32_16x16x32_bf16 v[216:219], v[84:87], v[224:227], v[216:219]\n\t" \
    "ds_read_b128 v[236:239], v253 offset:14336\n\t" \
    "s_lshl_b32 s44, s44, 22\n\t" \
    "v_mfma_f32_16x16x32_bf16 v[220:223], v[88:91], v[240:243], v[220:223]\n\t" \
    "s_add_u32 s44, s44, %[mb]\n\t" \
    "v_mfma_f32_16x16x32_bf16 v[220:223], v[92:95], v[224:227], v[220:223]\n\t" \
    "s_add_u32 s46, s44, 0x800\n\t" \
    "v_mfma_f32_16x16x32_bf16 v[212:215], v[244:247], v[240:243], v[212:215]\n\t" \
    "ds_read_b128 v[244:247], v254 offset:47104\n\t" \
    "s_add_u32 s47, s44, 0x8800\n\t" \
    "v_mfma_f32_16x16x32_bf16 v[212:215], v[248:251], v[224:227], v[212:215]\n\t" \
    "ds_read_b128 v[248:251], v254 offset:48128\n\t" \
    "s_add_u32 s48, s44, 0xc00\n\t" \
    "s_waitcnt lgkmcnt(4)\n\t" \
    "v_mfma_f32_16x16x32_bf16 v[204:207], v[104:107], v[228:231], v[204:207]\n\t" \
    "ds_read_b128 v[240:243], v253 offset:15360\n\t" \
    "s_waitcnt lgkmcnt(4)\n\t" \
    "v_mfma_f32_16x16x32_bf16 v[204:207], v[108:111], v[232:235], v[204:207]\n\t" \
    "ds_read_b128 v[224:227], v253 offset:34816\n\t" \
    "v_mfma_f32_16x16x32_bf16 v[208:211], v[112:115], v[228:231], v[208:211]\n\t" \
    "v_mfma_f32_16x16x32_bf16 v[208:211], v[116:119], v[232:235], v[208:211]\n\t" \
    "v_mfma_f32_16x16x32_bf16 v[200:203], v[96:99], v[228:231], v[200:203]\n\t" \
    "v_mfma_f32_16x16x32_bf16 v[200:203], v[100:103], v[232:235], v[200:203]\n\t" \
    "s_waitcnt lgkmcnt(4)\n\t" \
    "v_mfma_f32_16x16x32_bf16 v[204:207], v[120:123], v[236:239], v[204:207]\n\t" \
    "ds_read_b128 v[228:231], v253 offset:35840\n\t" \
    "s_waitcnt lgkmcnt(2)\n\t" \
    "v_mfma_f32_16x16x32_bf16 v[204:207], v[124:127], v[240:243], v[204:207]\n\t" \
    "ds_read_b128 v[232:235], v253 offset:8192\n\t" \
    "v_mfma_f32_16x16x32_bf16 v[208:211], v[128:131], v[236:239], v[208:211]\n\t" \
    "v_mfma_f32_16x16x32_bf16 v[208:211], v[132:135], v[240:243], v[208:211]\n\t" \
    "v_mfma_f32_16x16x32_bf16 v[200:203], v[244:247], v[236:239], v[200:203]\n\t" \
    "v_mfma_f32_16x16x32_bf16 v[200:203], v[248:251], v[240:243], v[200:203]\n\t" \
    "s_waitcnt lgkmcnt(2)\n\t" \
    "v_mfma_f32_16x16x32_bf16 v[216:219], v[120:123], v[224:227], v[216:219]\n\t" \
    "ds_read_b128 v[236:239], v253 offset:9216\n\t" \
    "s_waitcnt lgkmcnt(2)\n\t" \
    "v_mfma_f32_16x16x32_bf16 v[216:219], v[124:127], v[228:231], v[216:219]\n\t" \
    "ds_read_b128 v[240:243], v253 offset:16384\n\t" \
    "v_mfma_f32_16x16x32_bf16 v[220:223], v[128:131], v[224:227], v[220:223]\n\t" \
    "v_mfma_f32_16x16x32_bf16 v[220:223], v[132:135], v[228:231], v[220:223]\n\t" \
    "v_mfma_f32_16x16x32_bf16 v[212:215], v[244:247], v[224:227], v[212:215]\n\t" \
    "ds_read_b128 v[244:247], v254 offset:49152\n\t" \
    "v_mfma_f32_16x16x32_bf16 v[212:215], v[248:251], v[228:231], v[212:215]\n\t" \
    "ds_read_b128 v[248:251], v254 offset:50176\n\t" \
    "v_and_b32 v252, 0xf0, v253\n\t" \
    "v_lshlrev_b32 v252, 3, v252\n\t" \
    "buffer_load_dword v252, v252, s[60:63], %[fl] offen sc1\n\t" \
    "s_waitcnt lgkmcnt(4)\n\t" \
    "v_mfma_f32_16x16x32_bf16 v[204:207], v[144:147], v[232:235], v[204:207]\n\t" \
    "ds_read_b128 v[224:227], v253 offset:17408\n\t" \
    "s_waitcnt lgkmcnt(4)\n\t" \
    "v_mfma_f32_16x16x32_bf16 v[204:207], v[148:151], v[236:239], v[204:207]\n\t" \
    "ds_read_b128 v[228:231], v253 offset:36864\n\t" \
    "v_mfma_f32_16x16x32_bf16 v[208:211], v[152:155], v[232:235], v[208:211]\n\t" \
    "v_mfma_f32_16x16x32_bf16 v[208:211], v[156:159], v[236:239], v[208:211]\n\t" \
    "v_mfma_f32_16x16x32_bf16 v[200:203], v[136:139], v[232:235], v[200:203]\n\t" \
    "v_mfma_f32_16x16x32_bf16 v[200:203], v[140:143], v[236:239], v[200:203]\n\t" \
    "s_waitcnt lgkmcnt(4)\n\t" \
    "v_mfma_f32_16x16x32_bf16 v[204:207], v[160:163], v[240:243], v[204:207]\n\t" \
    "ds_read_b128 v[232:235], v253 offset:37888\n\t" \
    "s_waitcnt lgkmcnt(2)\n\t" \
    "v_mfma_f32_16x16x32_bf16 v[204:207], v[164:167], v[224:227], v[204:207]\n\t" \
    "ds_read_b128 v[236:239], v253 offset:18432\n\t" \
    "v_mfma_f32_16x16x32_bf16 v[208:211], v[168:171], v[240:243], v[208:211]\n\t" \
    "v_mfma_f32_16x16x32_bf16 v[208:211], v[172:175], v[224:227], v[208:211]\n\t" \
    "v_mfma_f32_16x16x32_bf16 v[200:203], v[244:247], v[240:243], v[200:203]\n\t" \
    "v_mfma_f32_16x16x32_bf16 v[200:203], v[248:251], v[224:227], v[200:203]\n\t" \
    "s_waitcnt lgkmcnt(2)\n\t" \
    "v_mfma_f32_16x16x32_bf16 v[216:219], v[160:163], v[228:231], v[216:219]\n\t" \
    "ds_read_b128 v[240:243], v253 offset:19456\n\t" \
    "s_waitcnt lgkmcnt(2)\n\t" \
    "v_mfma_f32_16x16x32_bf16 v[216:219], v[164:167], v[232:235], v[216:219]\n\t" \
    "ds_read_b128 v[224:227], v253 offset:32768\n\t" \
    "v_mfma_f32_16x16x32_bf16 v[220:223], v[168:171], v[228:231], v[220:223]\n\t" \
    "v_mfma_f32_16x16x32_bf16 v[220:223], v[172:175], v[232:235], v[220:223]\n\t" \
    "v_mfma_f32_16x16x32_bf16 v[212:215], v[244:247], v[228:231], v[212:215]\n\t" \
    "v_mfma_f32_16x16x32_bf16 v[212:215], v[248:251], v[232:235], v[212:215]\n\t" \
    "s_waitcnt lgkmcnt(2)\n\t" \
    "v_mfma_f32_16x16x32_bf16 v[204:207], v[184:187], v[236:239], v[204:207]\n\t" \
    "ds_read_b128 v[228:231], v253 offset:33792\n\t" \
    "s_waitcnt lgkmcnt(2)\n\t" \
    "v_mfma_f32_16x16x32_bf16 v[204:207], v[188:191], v[240:243], v[204:207]\n\t" \
    "ds_read_b128 v[232:235], v253 offset:20480\n\t" \
    "v_mfma_f32_16x16x32_bf16 v[208:211], v[192:195], v[236:239], v[208:211]\n\t" \
    "v_mfma_f32_16x16x32_bf16 v[208:211], v[196:199], v[240:243], v[208:211]\n\t" \
    "v_mfma_f32_16x16x32_bf16 v[200:203], v[176:179], v[236:239], v[200:203]\n\t" \
    "v_mfma_f32_16x16x32_bf16 v[200:203], v[180:183], v[240:243], v[200:203]\n\t" \
    "s_waitcnt lgkmcnt(2)\n\t" \
    "v_mfma_f32_16x16x32_bf16 v[216:219], v[24:27], v[224:227], v[216:219]\n\t" \
    "ds_read_b128 v[236:239], v253 offset:21504\n\t" \
    "s_waitcnt lgkmcnt(2)\n\t" \
    "v_mfma_f32_16x16x32_bf16 v[216:219], v[28:31], v[228:231], v[216:219]\n\t" \
    "ds_read_b128 v[240:243], v253 offset:22528\n\t" \
    "v_mfma_f32_16x16x32_bf16 v[220:223], v[32:35], v[224:227], v[220:223]\n\t" \
    "v_cvt_pk_bf16_f32 v204, v204, v205\n\t" \
    "v_mfma_f32_16x16x32_bf16 v[220:223], v[36:39], v[228:231], v[220:223]\n\t" \
    "v_cvt_pk_bf16_f32 v205, v206, v207\n\t" \
    "v_mfma_f32_16x16x32_bf16 v[212:215], v[16:19], v[224:227], v[212:215]\n\t" \
    "ds_write_b64 v254, v[204:205] offset:10240\n\t" \
    "v_mfma_f32_16x16x32_bf16 v[212:215], v[20:23], v[228:231], v[212:215]\n\t" \
    "v_cvt_pk_bf16_f32 v208, v208, v209\n\t" \
    "s_waitcnt lgkmcnt(3)\n\t" \
    "v_mfma_f32_16x16x32_bf16 v[216:219], v[64:67], v[232:235], v[216:219]\n\t" \
    "ds_read_b128 v[224:227], v253 offset:23552\n\t" \
    "v_cvt_pk_bf16_f32 v209, v210, v211\n\t" \
    "s_waitcnt lgkmcnt(3)\n\t" \
    "v_mfma_f32_16x16x32_bf16 v[216:219], v[68:71], v[236:239], v[216:219]\n\t" \
    "ds_read_b128 v[228:231], v253 offset:28672\n\t" \
    "ds_write_b64 v254, v[208:209] offset:11264\n\t" \
    "v_mfma_f32_16x16x32_bf16 v[220:223], v[72:75], v[232:235], v[220:223]\n\t" \
    "v_cvt_pk_bf16_f32 v200, v200, v201\n\t" \
    "v_mfma_f32_16x16x32_bf16 v[220:223], v[76:79], v[236:239], v[220:223]\n\t" \
    "v_cvt_pk_bf16_f32 v201, v202, v203\n\t" \
    "v_mfma_f32_16x16x32_bf16 v[212:215], v[56:59], v[232:235], v[212:215]\n\t" \
    "ds_write_b64 v254, v[200:201] offset:9216\n\t" \
    "v_mfma_f32_16x16x32_bf16 v[212:215], v[60:63], v[236:239], v[212:215]\n\t" \
    "s_waitcnt lgkmcnt(5)\n\t" \
    "v_mfma_f32_16x16x32_bf16 v[216:219], v[104:107], v[240:243], v[216:219]\n\t" \
    "ds_read_b128 v[232:235], v253 offset:29696\n\t" \
    "s_waitcnt lgkmcnt(4)\n\t" \
    "v_mfma_f32_16x16x32_bf16 v[216:219], v[108:111], v[224:227], v[216:219]\n\t" \
    "ds_read_b128 v[236:239], v253 offset:38912\n\t" \
    "v_mfma_f32_16x16x32_bf16 v[220:223], v[112:115], v[240:243], v[220:223]\n\t" \
    "v_mfma_f32_16x16x32_bf16 v[220:223], v[116:119], v[224:227], v[220:223]\n\t" \
    "v_mfma_f32_16x16x32_bf16 v[212:215], v[96:99], v[240:243], v[212:215]\n\t" \
    "v_mfma_f32_16x16x32_bf16 v[212:215], v[100:103], v[224:227], v[212:215]\n\t" \
    "ds_read_b128 v[240:243], v253 offset:39936\n\t" \
    "s_waitcnt lgkmcnt(3)\n\t" \
    "s_barrier \n\t" \
    "v_mfma_f32_16x16x32_bf16 v[216:219], v[144:147], v[228:231], v[216:219]\n\t" \
    "s_waitcnt lgkmcnt(2)\n\t" \
    "v_mfma_f32_16x16x32_bf16 v[216:219], v[148:151], v[232:235], v[216:219]\n\t" \
    "s_waitcnt lgkmcnt(1)\n\t" \
    "v_mfma_f32_16x16x32_bf16 v[216:219], v[184:187], v[236:239], v[216:219]\n\t" \
    "s_waitcnt lgkmcnt(0)\n\t" \
    "v_mfma_f32_16x16x32_bf16 v[216:219], v[188:191], v[240:243], v[216:219]\n\t" \
    "v_mfma_f32_16x16x32_bf16 v[220:223], v[152:155], v[228:231], v[220:223]\n\t" \
    "v_mfma_f32_16x16x32_bf16 v[220:223], v[156:159], v[232:235], v[220:223]\n\t" \
    "v_mfma_f32_16x16x32_bf16 v[220:223], v[192:195], v[236:239], v[220:223]\n\t" \
    "v_cvt_pk_bf16_f32 v216, v216, v217\n\t" \
    "v_cvt_pk_bf16_f32 v217, v218, v219\n\t" \
    "v_mfma_f32_16x16x32_bf16 v[220:223], v[196:199], v[240:243], v[220:223]\n\t" \
    "ds_write_b64 v254, v[216:217] offset:16384\n\t" \
    "v_mfma_f32_16x16x32_bf16 v[212:215], v[136:139], v[228:231], v[212:215]\n\t" \
    "v_mfma_f32_16x16x32_bf16 v[212:215], v[140:143], v[232:235], v[212:215]\n\t" \
    "v_mfma_f32_16x16x32_bf16 v[212:215], v[176:179], v[236:239], v[212:215]\n\t" \
    "v_cvt_pk_bf16_f32 v220, v220, v221\n\t" \
    "v_cvt_pk_bf16_f32 v221, v222, v223\n\t" \
    "v_mfma_f32_16x16x32_bf16 v[212:215], v[180:183], v[240:243], v[212:215]\n\t" \
    "ds_write_b64 v254, v[220:221] offset:17408\n\t" \
    "s_nop 6\n\t" \
    "v_cvt_pk_bf16_f32 v212, v212, v213\n\t" \
    "v_cvt_pk_bf16_f32 v213, v214, v215\n\t" \
    "ds_write_b64 v254, v[212:213] offset:15360\n\t" \
    "s_waitcnt vmcnt(0) lgkmcnt(0)\n\t" \
    "s_barrier \n\t" \
    "s_setprio 2\n\t" \
    "s_add_u32 s44, s40, 1\n\t" \
    "s_min_u32 s44, s44, 63\n\t" \
    ".Lv18_%=:\n\t" \
    "v_cmp_ge_u32 vcc, s44, v252\n\t" \
    "s_cbranch_vccz .Lv19_%=\n\t" \
    "s_cmp_eq_u32 s41, 0\n\t" \
    "s_cbranch_scc1 .Lv19_%=\n\t" \
    "s_sub_u32 s41, s41, 1\n\t" \
    "s_sleep 2\n\t" \
    "v_and_b32 v252, 0xf0, v253\n\t" \
    "v_lshlrev_b32 v252, 3, v252\n\t" \
    "buffer_load_dword v252, v252, s[60:63], %[fl] offen sc1\n\t" \
    "s_waitcnt vmcnt(0)\n\t" \
    "s_branch .Lv18_%=\n\t" \
    ".Lv19_%=:\n\t" \
    "s_mov_b32 m0, 0x1000\n\t" \
    "s_add_u32 s49, s42, 0x0\n\t" \
    "buffer_load_dwordx4 v253, s[60:63], s49 offen offset:0 sc1 lds\n\t" \
    "s_sleep 1\n\t" \
    "buffer_load_dwordx4 v253, s[60:63], s49 offen offset:1024 sc1 lds\n\t" \
    "s_sleep 1\n\t" \
    "buffer_load_dwordx4 v253, s[60:63], s49 offen offset:2048 sc1 lds\n\t" \
    "s_sleep 1\n\t" \
    "buffer_load_dwordx4 v253, s[60:63], s49 offen offset:3072 sc1 lds\n\t" \
    "s_sleep 1\n\t" \
    "s_mov_b32 m0, 0x4400\n\t" \
    "s_add_u32 s49, s42, 0xc400\n\t" \
    "buffer_load_dwordx4 v253, s[60:63], s49 offen offset:0 sc1 lds\n\t" \
    "s_sleep 1\n\t" \
    "s_mov_b32 m0, 0x6000\n\t" \
    "s_add_u32 s49, s42, 0x1000\n\t" \
    "buffer_load_dwordx4 v253, s[60:63], s49 offen offset:0 sc1 lds\n\t" \
    "s_sleep 1\n\t" \
    "buffer_load_dwordx4 v253, s[60:63], s49 offen offset:1024 sc1 lds\n\t" \
    "s_sleep 1\n\t" \
    "buffer_load_dwordx4 v253, s[60:63], s49 offen offset:2048 sc1 lds\n\t" \
    "s_sleep 1\n\t" \
    "buffer_load_dwordx4 v253, s[60:63], s49 offen offset:3072 sc1 lds\n\t" \
    "s_sleep 1\n\t" \
    "s_mov_b32 m0, 0x9400\n\t" \
    "s_add_u32 s49, s42, 0xd400\n\t" \
    "buffer_load_dwordx4 v253, s[60:63], s49 offen offset:0 sc1 lds\n\t" \
    "s_sleep 1\n\t" \
    "ds_read_b128 v[200:203], v254 offset:40960\n\t" \
    "ds_read_b128 v[204:207], v254 offset:41984\n\t" \
    "ds_read_b128 v[216:219], v254 offset:43008\n\t" \
    "ds_read_b128 v[220:223], v254 offset:44032\n\t" \
    "v_mov_b64 v[208:209], 0\n\t" \
    "v_mov_b64 v[210:211], 0\n\t" \
    "v_mov_b64 v[244:245], 0\n\t" \
    "v_mov_b64 v[246:247], 0\n\t" \
    "v_mov_b64 v[248:249], 0\n\t" \
    "v_mov_b64 v[250:251], 0\n\t" \
    "s_sleep 5\n\t" \
    "s_mov_b32 m0, 0xa800\n\t" \
    "s_nop 0\n\t" \
    "buffer_load_dwordx4 v253, s[60:63], s46 offen sc1 lds\n\t" \
    "s_mov_b32 m0, 0xb800\n\t" \
    "s_nop 0\n\t" \
    "buffer_load_dwordx4 v253, s[60:63], s47 offen sc1 lds\n\t" \
    "s_mov_b32 m0, 0xac00\n\t" \
    "s_nop 0\n\t" \
    "buffer_load_dwordx4 v253, s[60:63], s48 offen sc1 lds\n\t" \
    "s_waitcnt lgkmcnt(0)\n\t" \
    "v_bfe_i32 v212, v253, 5, 1\n\t" \
    "v_and_b32 v212, 0xaaaa, v212\n\t" \
    "v_or_b32 v212, 0x4444, v212\n\t" \
    "v_and_b32 v213, 0xffff, v255\n\t" \
    "v_lshrrev_b32 v214, 16, v255\n\t" \
    ".Lv110_%=:\n\t" \
    "s_waitcnt vmcnt(0)\n\t" \
    "ds_read_b128 v[224:227], v253 offset:43008\n\t" \
    "ds_read_b128 v[228:231], v253 offset:47104\n\t" \
    "ds_read_b128 v[232:235], v253 offset:44032\n\t" \
    "s_waitcnt lgkmcnt(2)\n\t" \
    "v_max_u32 v215, v224, v225\n\t" \
    "v_max3_u32 v215, v226, v227, v215\n\t" \
    "s_waitcnt lgkmcnt(1)\n\t" \
    "v_max3_u32 v215, v228, v229, v215\n\t" \
    "v_max3_u32 v215, v230, v231, v215\n\t" \
    "s_waitcnt lgkmcnt(0)\n\t" \
    "v_max3_u32 v215, v232, v233, v215\n\t" \
    "v_max3_u32 v215, v234, v235, v215\n\t" \
    "v_cmp_eq_u32 vcc, -1, v215\n\t" \
    "s_cbranch_vccz .Lv111_%=\n\t" \
    "v_max_u32 v215, v224, v225\n\t" \
    "v_max3_u32 v215, v226, v227, v215\n\t" \
    "v_cmp_eq_u32 vcc, -1, v215\n\t" \
    "s_cbranch_vccz .Lv112_%=\n\t" \
    "s_mov_b32 m0, 0xa800\n\t" \
    "s_nop 0\n\t" \
    "buffer_load_dwordx4 v253, s[60:63], s46 offen sc1 lds\n\t" \
    ".Lv112_%=:\n\t" \
    "v_max_u32 v215, v228, v229\n\t" \
    "v_max3_u32 v215, v230, v231, v215\n\t" \
    "v_cmp_eq_u32 vcc, -1, v215\n\t" \
    "s_cbranch_vccz .Lv113_%=\n\t" \
    "s_mov_b32 m0, 0xb800\n\t" \
    "s_nop 0\n\t" \
    "buffer_load_dwordx4 v253, s[60:63], s47 offen sc1 lds\n\t" \
    ".Lv113_%=:\n\t" \
    "v_max_u32 v215, v232, v233\n\t" \
    "v_max3_u32 v215, v234, v235, v215\n\t" \
    "v_cmp_eq_u32 vcc, -1, v215\n\t" \
    "s_cbranch_vccz .Lv114_%=\n\t" \
    "s_mov_b32 m0, 0xac00\n\t" \
    "s_nop 0\n\t" \
    "buffer_load_dwordx4 v253, s[60:63], s48 offen sc1 lds\n\t" \
    ".Lv114_%=:\n\t" \
    "s_cmp_eq_u32 s41, 0\n\t" \
    "s_cbranch_scc1 .Lv111_%=\n\t" \
    "s_sub_u32 s41, s41, 1\n\t" \
    "s_branch .Lv110_%=\n\t" \
    ".Lv111_%=:\n\t" \
    "s_waitcnt lgkmcnt(0)\n\t" \
    "s_barrier \n\t" \
    "ds_read_b128 v[236:239], v253 offset:40960\n\t" \
    "ds_read_b128 v[240:243], v253 offset:46080\n\t" \
    "v_smfmac_f32_16x16x64_bf16 v[244:247], v[224:227], v[0:7], v212\n\t" \
    "v_smfmac_f32_16x16x64_bf16 v[248:251], v[224:227], v[8:15], v212\n\t" \
    "v_smfmac_f32_16x16x64_bf16 v[208:211], v[224:227], v[200:207], v212\n\t" \
    "ds_read_b128 v[200:203], v254 offset:45056\n\t" \
    "ds_read_b128 v[204:207], v254 offset:46080\n\t" \
    "ds_read_b128 v[224:227], v253 offset:41984\n\t" \
    "v_smfmac_f32_16x16x64_bf16 v[208:211], v[228:231], v[16:23], v212\n\t" \
    "v_smfmac_f32_16x16x64_bf16 v[244:247], v[228:231], v[24:31], v212\n\t" \
    "v_smfmac_f32_16x16x64_bf16 v[248:251], v[228:231], v[32:39], v212\n\t" \
    "ds_read_b128 v[228:231], v253 offset:48128\n\t" \
    "v_smfmac_f32_16x16x64_bf16 v[208:211], v[232:235], v[216:223], v212\n\t" \
    "ds_read_b128 v[216:219], v254 offset:47104\n\t" \
    "ds_read_b128 v[220:223], v254 offset:48128\n\t" \
    "v_smfmac_f32_16x16x64_bf16 v[244:247], v[232:235], v[40:47], v212\n\t" \
    "v_smfmac_f32_16x16x64_bf16 v[248:251], v[232:235], v[48:55], v212\n\t" \
    "ds_read_b128 v[232:235], v253 offset:45056\n\t" \
    "s_waitcnt lgkmcnt(8)\n\t" \
    "v_smfmac_f32_16x16x64_bf16 v[208:211], v[236:239], v[56:63], v212\n\t" \
    "v_smfmac_f32_16x16x64_bf16 v[244:247], v[236:239], v[64:71], v212\n\t" \
    "v_smfmac_f32_16x16x64_bf16 v[248:251], v[236:239], v[72:79], v212\n\t" \
    "ds_read_b128 v[236:239], v253 offset:49152\n\t" \
    "s_waitcnt lgkmcnt(6)\n\t" \
    "v_smfmac_f32_16x16x64_bf16 v[208:211], v[240:243], v[200:207], v212\n\t" \
    "ds_read_b128 v[200:203], v254 offset:49152\n\t" \
    "ds_read_b128 v[204:207], v254 offset:50176\n\t" \
    "v_smfmac_f32_16x16x64_bf16 v[244:247], v[240:243], v[80:87], v212\n\t" \
    "v_smfmac_f32_16x16x64_bf16 v[248:251], v[240:243], v[88:95], v212\n\t" \
    "ds_read_b128 v[240:243], v253 offset:50176\n\t" \
    "s_waitcnt lgkmcnt(8)\n\t" \
    "v_smfmac_f32_16x16x64_bf16 v[208:211], v[224:227], v[96:103], v212\n\t" \
    "v_smfmac_f32_16x16x64_bf16 v[244:247], v[224:227], v[104:111], v212\n\t" \
    "v_smfmac_f32_16x16x64_bf16 v[248:251], v[224:227], v[112:119], v212\n\t" \
    "s_waitcnt lgkmcnt(7)\n\t" \
    "v_smfmac_f32_16x16x64_bf16 v[244:247], v[228:231], v[120:127], v212\n\t" \
    "s_waitcnt lgkmcnt(4)\n\t" \
    "v_smfmac_f32_16x16x64_bf16 v[244:247], v[232:235], v[144:151], v212\n\t" \
    "s_waitcnt lgkmcnt(3)\n\t" \
    "v_smfmac_f32_16x16x64_bf16 v[244:247], v[236:239], v[160:167], v212\n\t" \
    "s_waitcnt lgkmcnt(0)\n\t" \
    "v_smfmac_f32_16x16x64_bf16 v[244:247], v[240:243], v[184:191], v212\n\t" \
    "v_smfmac_f32_16x16x64_bf16 v[248:251], v[228:231], v[128:135], v212\n\t" \
    "v_smfmac_f32_16x16x64_bf16 v[248:251], v[232:235], v[152:159], v212\n\t" \
    "v_smfmac_f32_16x16x64_bf16 v[248:251], v[236:239], v[168:175], v212\n\t" \
    "v_add_f32 v244, v244, v246\n\t" \
    "v_add_f32 v245, v245, v247\n\t" \
    "v_cvt_pk_bf16_f32 v244, v244, v245\n\t" \
    "v_smfmac_f32_16x16x64_bf16 v[248:251], v[240:243], v[192:199], v212\n\t" \
    "ds_write_b16 v213, v244 offset:512\n\t" \
    "ds_write_b16_d16_hi v213, v244 offset:528\n\t" \
    "v_smfmac_f32_16x16x64_bf16 v[208:211], v[228:231], v[216:223], v212\n\t" \
    "v_smfmac_f32_16x16x64_bf16 v[208:211], v[232:235], v[136:143], v212\n\t" \
    "v_smfmac_f32_16x16x64_bf16 v[208:211], v[236:239], v[200:207], v212\n\t" \
    "v_add_f32 v248, v248, v250\n\t" \
    "v_add_f32 v249, v249, v251\n\t" \
    "v_cvt_pk_bf16_f32 v248, v248, v249\n\t" \
    "v_smfmac_f32_16x16x64_bf16 v[208:211], v[240:243], v[176:183], v212\n\t" \
    "ds_write_b16 v213, v248 offset:1024\n\t" \
    "ds_write_b16_d16_hi v213, v248 offset:1040\n\t" \
    "s_nop 5\n\t" \
    "v_add_f32 v208, v208, v210\n\t" \
    "v_add_f32 v209, v209, v211\n\t" \
    "v_cvt_pk_bf16_f32 v208, v208, v209\n\t" \
    "ds_write_b16 v213, v208 offset:0\n\t" \
    "ds_write_b16_d16_hi v213, v208 offset:16\n\t" \
    "s_setprio 0\n\t" \
    "s_waitcnt lgkmcnt(0)\n\t" \
    "s_barrier \n\t" \
    "ds_read_b128 v[224:227], v253 offset:4096\n\t" \
    "ds_read_b128 v[244:247], v254 offset:40960\n\t" \
    "ds_read_b128 v[228:231], v253 offset:5120\n\t" \
    "ds_read_b128 v[232:235], v253 offset:24576\n\t" \
    "ds_read_b128 v[236:239], v253 offset:25600\n\t" \
    "ds_read_b128 v[248:251], v254 offset:41984\n\t" \
    "s_add_u32 s40, s40, 1\n\t" \
    "s_cmp_lg_u32 s40, 64\n\t" \
    "s_cbranch_scc1 .Lv17_%=\n\t" \
    "s_branch .Lend%=\n\t" \
    ".Lv0_%=:\n\t" \
    "s_mov_b32 s42, %[xs]\n\t" \
    "v_add_u32 v244, 0xc000, v243\n\t" \
    "s_waitcnt vmcnt(12) lgkmcnt(0)\n\t" \
    "s_barrier \n\t" \
    "s_add_u32 m0, %[wm], 0x9000\n\t" \
    "s_mov_b32 s44, 0x1e0000\n\t" \
    "buffer_load_dwordx4 v240, %[wr], s44 offen offset:0 nt lds\n\t" \
    "buffer_load_dwordx4 v241, %[wr], s44 offen offset:1024 nt lds\n\t" \
    "buffer_load_dwordx4 v242, %[wr], s44 offen offset:2048 nt lds\n\t" \
    "ds_read_b64_tr_b16 v[200:201], v244 offset:0\n\t" \
    "ds_read_b64_tr_b16 v[202:203], v244 offset:1536\n\t" \
    "ds_read_b64_tr_b16 v[0:1], v244 offset:32\n\t" \
    "ds_read_b64_tr_b16 v[2:3], v244 offset:1568\n\t" \
    "ds_read_b64_tr_b16 v[8:9], v244 offset:64\n\t" \
    "ds_read_b64_tr_b16 v[10:11], v244 offset:1600\n\t" \
    "v_add_u32 v244, 0xf000, v243\n\t" \
    "s_waitcnt vmcnt(12) lgkmcnt(0)\n\t" \
    "s_barrier \n\t" \
    "s_add_u32 m0, %[wm], 0xc000\n\t" \
    "s_mov_b32 s44, 0x240000\n\t" \
    "buffer_load_dwordx4 v240, %[wr], s44 offen offset:0 nt lds\n\t" \
    "buffer_load_dwordx4 v241, %[wr], s44 offen offset:1024 nt lds\n\t" \
    "buffer_load_dwordx4 v242, %[wr], s44 offen offset:2048 nt lds\n\t" \
    "ds_read_b64_tr_b16 v[204:205], v244 offset:0\n\t" \
    "ds_read_b64_tr_b16 v[206:207], v244 offset:1536\n\t" \
    "ds_read_b64_tr_b16 v[4:5], v244 offset:32\n\t" \
    "ds_read_b64_tr_b16 v[6:7], v244 offset:1568\n\t" \
    "ds_read_b64_tr_b16 v[12:13], v244 offset:64\n\t" \
    "ds_read_b64_tr_b16 v[14:15], v244 offset:1600\n\t" \
    "v_add_u32 v244, 0x0, v243\n\t" \
    "s_waitcnt vmcnt(12) lgkmcnt(0)\n\t" \
    "s_barrier \n\t" \
    "s_add_u32 m0, %[wm], 0xf000\n\t" \
    "s_mov_b32 s44, 0x2a0000\n\t" \
    "buffer_load_dwordx4 v240, %[wr], s44 offen offset:0 nt lds\n\t" \
    "buffer_load_dwordx4 v241, %[wr], s44 offen offset:1024 nt lds\n\t" \
    "buffer_load_dwordx4 v242, %[wr], s44 offen offset:2048 nt lds\n\t" \
    "ds_read_b64_tr_b16 v[208:209], v244 offset:0\n\t" \
    "ds_read_b64_tr_b16 v[210:211], v244 offset:1536\n\t" \
    "ds_read_b64_tr_b16 v[40:41], v244 offset:32\n\t" \
    "ds_read_b64_tr_b16 v[42:43], v244 offset:1568\n\t" \
    "ds_read_b64_tr_b16 v[48:49], v244 offset:64\n\t" \
    "ds_read_b64_tr_b16 v[50:51], v244 offset:1600\n\t" \
    "v_add_u32 v244, 0x3000, v243\n\t" \
    "s_waitcnt vmcnt(12) lgkmcnt(0)\n\t" \
    "s_barrier \n\t" \
    "s_add_u32 m0, %[wm], 0x0\n\t" \
    "s_mov_b32 s44, 0x300000\n\t" \
    "buffer_load_dwordx4 v240, %[wr], s44 offen offset:0 nt lds\n\t" \
    "buffer_load_dwordx4 v241, %[wr], s44 offen offset:1024 nt lds\n\t" \
    "buffer_load_dwordx4 v242, %[wr], s44 offen offset:2048 nt lds\n\t" \
    "ds_read_b64_tr_b16 v[212:213], v244 offset:0\n\t" \
    "ds_read_b64_tr_b16 v[214:215], v244 offset:1536\n\t" \
    "ds_read_b64_tr_b16 v[44:45], v244 offset:32\n\t" \
    "ds_read_b64_tr_b16 v[46:47], v244 offset:1568\n\t" \
    "ds_read_b64_tr_b16 v[52:53], v244 offset:64\n\t" \
    "ds_read_b64_tr_b16 v[54:55], v244 offset:1600\n\t" \
    "v_add_u32 v244, 0x6000, v243\n\t" \
    "s_waitcnt vmcnt(12) lgkmcnt(0)\n\t" \
    "s_barrier \n\t" \
    "s_add_u32 m0, %[wm], 0x3000\n\t" \
    "s_mov_b32 s44, 0x360000\n\t" \
    "buffer_load_dwordx4 v240, %[wr], s44 offen offset:0 nt lds\n\t" \
    "buffer_load_dwordx4 v241, %[wr], s44 offen offset:1024 nt lds\n\t" \
    "buffer_load_dwordx4 v242, %[wr], s44 offen offset:2048 nt lds\n\t" \
    "ds_read_b64_tr_b16 v[216:217], v244 offset:0\n\t" \
    "ds_read_b64_tr_b16 v[218:219], v244 offset:1536\n\t" \
    "ds_read_b64_tr_b16 v[80:81], v244 offset:32\n\t" \
    "ds_read_b64_tr_b16 v[82:83], v244 offset:1568\n\t" \
    "ds_read_b64_tr_b16 v[88:89], v244 offset:64\n\t" \
    "ds_read_b64_tr_b16 v[90:91], v244 offset:1600\n\t" \
    "v_add_u32 v244, 0x9000, v243\n\t" \
    "s_waitcnt vmcnt(12) lgkmcnt(0)\n\t" \
    "s_barrier \n\t" \
    "s_add_u32 m0, %[wm], 0x6000\n\t" \
    "s_mov_b32 s44, 0x3c0000\n\t" \
    "buffer_load_dwordx4 v240, %[wr], s44 offen offset:0 nt lds\n\t" \
    "buffer_load_dwordx4 v241, %[wr], s44 offen offset:1024 nt lds\n\t" \
    "buffer_load_dwordx4 v242, %[wr], s44 offen offset:2048 nt lds\n\t" \
    "ds_read_b64_tr_b16 v[220:221], v244 offset:0\n\t" \
    "ds_read_b64_tr_b16 v[222:223], v244 offset:1536\n\t" \
    "ds_read_b64_tr_b16 v[84:85], v244 offset:32\n\t" \
    "ds_read_b64_tr_b16 v[86:87], v244 offset:1568\n\t" \
    "ds_read_b64_tr_b16 v[92:93], v244 offset:64\n\t" \
    "ds_read_b64_tr_b16 v[94:95], v244 offset:1600\n\t" \
    "v_add_u32 v244, 0xc000, v243\n\t" \
    "s_waitcnt vmcnt(12) lgkmcnt(0)\n\t" \
    "s_barrier \n\t" \
    "s_add_u32 m0, %[wm], 0x9000\n\t" \
    "s_mov_b32 s44, 0x420000\n\t" \
    "buffer_load_dwordx4 v240, %[wr], s44 offen offset:0 nt lds\n\t" \
    "buffer_load_dwordx4 v241, %[wr], s44 offen offset:1024 nt lds\n\t" \
    "buffer_load_dwordx4 v242, %[wr], s44 offen offset:2048 nt lds\n\t" \
    "ds_read_b64_tr_b16 v[224:225], v244 offset:0\n\t" \
    "ds_read_b64_tr_b16 v[226:227], v244 offset:1536\n\t" \
    "ds_read_b64_tr_b16 v[120:121], v244 offset:32\n\t" \
    "ds_read_b64_tr_b16 v[122:123], v244 offset:1568\n\t" \
    "ds_read_b64_tr_b16 v[128:129], v244 offset:64\n\t" \
    "ds_read_b64_tr_b16 v[130:131], v244 offset:1600\n\t" \
    "v_add_u32 v244, 0xf000, v243\n\t" \
    "s_waitcnt vmcnt(12) lgkmcnt(0)\n\t" \
    "s_barrier \n\t" \
    "s_add_u32 m0, %[wm], 0xc000\n\t" \
    "s_mov_b32 s44, 0x480000\n\t" \
    "buffer_load_dwordx4 v240, %[wr], s44 offen offset:0 nt lds\n\t" \
    "buffer_load_dwordx4 v241, %[wr], s44 offen offset:1024 nt lds\n\t" \
    "buffer_load_dwordx4 v242, %[wr], s44 offen offset:2048 nt lds\n\t" \
    "ds_read_b64_tr_b16 v[228:229], v244 offset:0\n\t" \
    "ds_read_b64_tr_b16 v[230:231], v244 offset:1536\n\t" \
    "ds_read_b64_tr_b16 v[124:125], v244 offset:32\n\t" \
    "ds_read_b64_tr_b16 v[126:127], v244 offset:1568\n\t" \
    "ds_read_b64_tr_b16 v[132:133], v244 offset:64\n\t" \
    "ds_read_b64_tr_b16 v[134:135], v244 offset:1600\n\t" \
    "v_add_u32 v244, 0x0, v243\n\t" \
    "s_waitcnt vmcnt(12) lgkmcnt(0)\n\t" \
    "s_barrier \n\t" \
    "v_mov_b32 v246, 0\n\t" \
    "buffer_load_dword v246, v246, s[60:63], %[mo] offen sc1\n\t" \
    "s_add_u32 m0, %[wm], 0xf000\n\t" \
    "s_mov_b32 s44, 0x4e0000\n\t" \
    "buffer_load_dwordx4 v240, %[wr], s44 offen offset:0 nt lds\n\t" \
    "buffer_load_dwordx4 v241, %[wr], s44 offen offset:1024 nt lds\n\t" \
    "buffer_load_dwordx4 v242, %[wr], s44 offen offset:2048 nt lds\n\t" \
    "ds_read_b64_tr_b16 v[232:233], v244 offset:0\n\t" \
    "ds_read_b64_tr_b16 v[234:235], v244 offset:1536\n\t" \
    "ds_read_b64_tr_b16 v[160:161], v244 offset:32\n\t" \
    "ds_read_b64_tr_b16 v[162:163], v244 offset:1568\n\t" \
    "ds_read_b64_tr_b16 v[168:169], v244 offset:64\n\t" \
    "ds_read_b64_tr_b16 v[170:171], v244 offset:1600\n\t" \
    "v_add_u32 v244, 0x3000, v243\n\t" \
    "s_waitcnt vmcnt(12) lgkmcnt(0)\n\t" \
    "s_barrier \n\t" \
    "s_add_u32 m0, %[wm], 0x0\n\t" \
    "s_mov_b32 s44, 0x540000\n\t" \
    "buffer_load_dwordx4 v240, %[wr], s44 offen offset:0 nt lds\n\t" \
    "buffer_load_dwordx4 v241, %[wr], s44 offen offset:1024 nt lds\n\t" \
    "buffer_load_dwordx4 v242, %[wr], s44 offen offset:2048 nt lds\n\t" \
    "ds_read_b64_tr_b16 v[236:237], v244 offset:0\n\t" \
    "ds_read_b64_tr_b16 v[238:239], v244 offset:1536\n\t" \
    "ds_read_b64_tr_b16 v[164:165], v244 offset:32\n\t" \
    "ds_read_b64_tr_b16 v[166:167], v244 offset:1568\n\t" \
    "ds_read_b64_tr_b16 v[172:173], v244 offset:64\n\t" \
    "ds_read_b64_tr_b16 v[174:175], v244 offset:1600\n\t" \
    "v_add_u32 v244, 0x6000, v243\n\t" \
    "s_waitcnt vmcnt(12) lgkmcnt(0)\n\t" \
    "s_barrier \n\t" \
    "s_add_u32 m0, %[wm], 0x3000\n\t" \
    "s_mov_b32 s44, 0x5a0000\n\t" \
    "buffer_load_dwordx4 v240, %[wr], s44 offen offset:0 nt lds\n\t" \
    "buffer_load_dwordx4 v241, %[wr], s44 offen offset:1024 nt lds\n\t" \
    "buffer_load_dwordx4 v242, %[wr], s44 offen offset:2048 nt lds\n\t" \
    "ds_read_b64_tr_b16 v[16:17], v244 offset:0\n\t" \
    "ds_read_b64_tr_b16 v[18:19], v244 offset:1536\n\t" \
    "ds_read_b64_tr_b16 v[24:25], v244 offset:32\n\t" \
    "ds_read_b64_tr_b16 v[26:27], v244 offset:1568\n\t" \
    "ds_read_b64_tr_b16 v[32:33], v244 offset:64\n\t" \
    "ds_read_b64_tr_b16 v[34:35], v244 offset:1600\n\t" \
    "v_add_u32 v244, 0x9000, v243\n\t" \
    "s_waitcnt vmcnt(12) lgkmcnt(0)\n\t" \
    "s_barrier \n\t" \
    "s_add_u32 m0, %[wm], 0x6000\n\t" \
    "s_mov_b32 s44, 0x600000\n\t" \
    "buffer_load_dwordx4 v240, %[wr], s44 offen offset:0 nt lds\n\t" \
    "buffer_load_dwordx4 v241, %[wr], s44 offen offset:1024 nt lds\n\t" \
    "buffer_load_dwordx4 v242, %[wr], s44 offen offset:2048 nt lds\n\t" \
    "ds_read_b64_tr_b16 v[20:21], v244 offset:0\n\t" \
    "ds_read_b64_tr_b16 v[22:23], v244 offset:1536\n\t" \
    "ds_read_b64_tr_b16 v[28:29], v244 offset:32\n\t" \
    "ds_read_b64_tr_b16 v[30:31], v244 offset:1568\n\t" \
    "ds_read_b64_tr_b16 v[36:37], v244 offset:64\n\t" \
    "ds_read_b64_tr_b16 v[38:39], v244 offset:1600\n\t" \
    "v_add_u32 v244, 0xc000, v243\n\t" \
    "s_waitcnt vmcnt(12) lgkmcnt(0)\n\t" \
    "s_barrier \n\t" \
    "s_add_u32 m0, %[wm], 0x9000\n\t" \
    "s_mov_b32 s44, 0x660000\n\t" \
    "buffer_load_dwordx4 v240, %[wr], s44 offen offset:0 nt lds\n\t" \
    "buffer_load_dwordx4 v241, %[wr], s44 offen offset:1024 nt lds\n\t" \
    "buffer_load_dwordx4 v242, %[wr], s44 offen offset:2048 nt lds\n\t" \
    "ds_read_b64_tr_b16 v[56:57], v244 offset:0\n\t" \
    "ds_read_b64_tr_b16 v[58:59], v244 offset:1536\n\t" \
    "ds_read_b64_tr_b16 v[64:65], v244 offset:32\n\t" \
    "ds_read_b64_tr_b16 v[66:67], v244 offset:1568\n\t" \
    "ds_read_b64_tr_b16 v[72:73], v244 offset:64\n\t" \
    "ds_read_b64_tr_b16 v[74:75], v244 offset:1600\n\t" \
    "v_add_u32 v244, 0xf000, v243\n\t" \
    "s_waitcnt vmcnt(12) lgkmcnt(0)\n\t" \
    "s_barrier \n\t" \
    ".Lv01_%=:\n\t" \
    "v_readfirstlane_b32 s43, v246\n\t" \
    "s_nop 0\n\t" \
    "s_cmp_lg_u32 s43, -1\n\t" \
    "s_cbranch_scc1 .Lv02_%=\n\t" \
    "s_cmp_eq_u32 s41, 0\n\t" \
    "s_cbranch_scc1 .Lv02_%=\n\t" \
    "s_sub_u32 s41, s41, 1\n\t" \
    "s_sleep 4\n\t" \
    "v_mov_b32 v246, 0\n\t" \
    "buffer_load_dword v246, v246, s[60:63], %[mo] offen sc1\n\t" \
    "s_waitcnt vmcnt(0)\n\t" \
    "s_branch .Lv01_%=\n\t" \
    ".Lv02_%=:\n\t" \
    "v_mov_b32 v246, 0x8000\n\t" \
    "s_mov_b64 exec, 1\n\t" \
    "buffer_store_dword v246, v246, s[60:63], %[mo] offen sc1\n\t" \
    "s_mov_b64 exec, -1\n\t" \
    "s_and_b32 s44, s43, 0x5000\n\t" \
    "s_xor_b32 %[mb], %[mb], s44\n\t" \
    "s_xor_b32 %[fl], %[fl], s44\n\t" \
    "s_and_b32 s43, s43, 0x2000\n\t" \
    "s_xor_b32 s43, s43, %[lx]\n\t" \
    "s_add_u32 s60, s60, s43\n\t" \
    "s_addc_u32 s61, s61, 0\n\t" \
    "v_and_b32 v252, 0xf0, v253\n\t" \
    "v_lshlrev_b32 v252, 3, v252\n\t" \
    "buffer_load_dword v252, v252, s[60:63], %[fl] offen sc1\n\t" \
    "s_add_u32 m0, %[wm], 0xc000\n\t" \
    "s_mov_b32 s44, 0x6c0000\n\t" \
    "buffer_load_dwordx4 v240, %[wr], s44 offen offset:0 nt lds\n\t" \
    "buffer_load_dwordx4 v241, %[wr], s44 offen offset:1024 nt lds\n\t" \
    "buffer_load_dwordx4 v242, %[wr], s44 offen offset:2048 nt lds\n\t" \
    "ds_read_b64_tr_b16 v[60:61], v244 offset:0\n\t" \
    "ds_read_b64_tr_b16 v[62:63], v244 offset:1536\n\t" \
    "ds_read_b64_tr_b16 v[68:69], v244 offset:32\n\t" \
    "ds_read_b64_tr_b16 v[70:71], v244 offset:1568\n\t" \
    "ds_read_b64_tr_b16 v[76:77], v244 offset:64\n\t" \
    "ds_read_b64_tr_b16 v[78:79], v244 offset:1600\n\t" \
    "v_add_u32 v244, 0x0, v243\n\t" \
    "s_waitcnt vmcnt(12) lgkmcnt(0)\n\t" \
    "s_barrier \n\t" \
    "s_add_u32 m0, %[wm], 0xf000\n\t" \
    "s_mov_b32 s44, 0x720000\n\t" \
    "buffer_load_dwordx4 v240, %[wr], s44 offen offset:0 nt lds\n\t" \
    "buffer_load_dwordx4 v241, %[wr], s44 offen offset:1024 nt lds\n\t" \
    "buffer_load_dwordx4 v242, %[wr], s44 offen offset:2048 nt lds\n\t" \
    "ds_read_b64_tr_b16 v[96:97], v244 offset:0\n\t" \
    "ds_read_b64_tr_b16 v[98:99], v244 offset:1536\n\t" \
    "ds_read_b64_tr_b16 v[104:105], v244 offset:32\n\t" \
    "ds_read_b64_tr_b16 v[106:107], v244 offset:1568\n\t" \
    "ds_read_b64_tr_b16 v[112:113], v244 offset:64\n\t" \
    "ds_read_b64_tr_b16 v[114:115], v244 offset:1600\n\t" \
    "v_add_u32 v244, 0x3000, v243\n\t" \
    "s_waitcnt vmcnt(12) lgkmcnt(0)\n\t" \
    "s_barrier \n\t" \
    "ds_read_b64_tr_b16 v[100:101], v244 offset:0\n\t" \
    "ds_read_b64_tr_b16 v[102:103], v244 offset:1536\n\t" \
    "ds_read_b64_tr_b16 v[108:109], v244 offset:32\n\t" \
    "ds_read_b64_tr_b16 v[110:111], v244 offset:1568\n\t" \
    "ds_read_b64_tr_b16 v[116:117], v244 offset:64\n\t" \
    "ds_read_b64_tr_b16 v[118:119], v244 offset:1600\n\t" \
    "v_add_u32 v244, 0x6000, v243\n\t" \
    "s_waitcnt vmcnt(9) lgkmcnt(0)\n\t" \
    "s_barrier \n\t" \
    "ds_read_b64_tr_b16 v[136:137], v244 offset:0\n\t" \
    "ds_read_b64_tr_b16 v[138:139], v244 offset:1536\n\t" \
    "ds_read_b64_tr_b16 v[144:145], v244 offset:32\n\t" \
    "ds_read_b64_tr_b16 v[146:147], v244 offset:1568\n\t" \
    "ds_read_b64_tr_b16 v[152:153], v244 offset:64\n\t" \
    "ds_read_b64_tr_b16 v[154:155], v244 offset:1600\n\t" \
    "v_add_u32 v244, 0x9000, v243\n\t" \
    "s_waitcnt vmcnt(6) lgkmcnt(0)\n\t" \
    "s_barrier \n\t" \
    "ds_read_b64_tr_b16 v[140:141], v244 offset:0\n\t" \
    "ds_read_b64_tr_b16 v[142:143], v244 offset:1536\n\t" \
    "ds_read_b64_tr_b16 v[148:149], v244 offset:32\n\t" \
    "ds_read_b64_tr_b16 v[150:151], v244 offset:1568\n\t" \
    "ds_read_b64_tr_b16 v[156:157], v244 offset:64\n\t" \
    "ds_read_b64_tr_b16 v[158:159], v244 offset:1600\n\t" \
    "v_add_u32 v244, 0xc000, v243\n\t" \
    "s_waitcnt vmcnt(3) lgkmcnt(0)\n\t" \
    "s_barrier \n\t" \
    "ds_read_b64_tr_b16 v[176:177], v244 offset:0\n\t" \
    "ds_read_b64_tr_b16 v[178:179], v244 offset:1536\n\t" \
    "ds_read_b64_tr_b16 v[184:185], v244 offset:32\n\t" \
    "ds_read_b64_tr_b16 v[186:187], v244 offset:1568\n\t" \
    "ds_read_b64_tr_b16 v[192:193], v244 offset:64\n\t" \
    "ds_read_b64_tr_b16 v[194:195], v244 offset:1600\n\t" \
    "s_mov_b32 s43, 0\n\t" \
    "v_cmp_eq_u32 vcc, 0, v252\n\t" \
    "s_cbranch_vccnz .Lv03_%=\n\t" \
    "s_mov_b32 m0, 0x0\n\t" \
    "s_add_u32 s49, s42, 0x0\n\t" \
    "buffer_load_dwordx4 v253, s[60:63], s49 offen offset:0 sc1 lds\n\t" \
    "buffer_load_dwordx4 v253, s[60:63], s49 offen offset:1024 sc1 lds\n\t" \
    "buffer_load_dwordx4 v253, s[60:63], s49 offen offset:2048 sc1 lds\n\t" \
    "buffer_load_dwordx4 v253, s[60:63], s49 offen offset:3072 sc1 lds\n\t" \
    "s_mov_b32 m0, 0x4000\n\t" \
    "s_add_u32 s49, s42, 0x10000\n\t" \
    "buffer_load_dwordx4 v253, s[60:63], s49 offen offset:0 sc1 lds\n\t" \
    "s_mov_b32 m0, 0x5000\n\t" \
    "s_add_u32 s49, s42, 0x1000\n\t" \
    "buffer_load_dwordx4 v253, s[60:63], s49 offen offset:0 sc1 lds\n\t" \
    "buffer_load_dwordx4 v253, s[60:63], s49 offen offset:1024 sc1 lds\n\t" \
    "buffer_load_dwordx4 v253, s[60:63], s49 offen offset:2048 sc1 lds\n\t" \
    "buffer_load_dwordx4 v253, s[60:63], s49 offen offset:3072 sc1 lds\n\t" \
    "s_mov_b32 m0, 0x9000\n\t" \
    "s_add_u32 s49, s42, 0x11000\n\t" \
    "buffer_load_dwordx4 v253, s[60:63], s49 offen offset:0 sc1 lds\n\t" \
    "s_mov_b32 s43, 1\n\t" \
    ".Lv03_%=:\n\t" \
    "v_add_u32 v244, 0xf000, v243\n\t" \
    "s_waitcnt vmcnt(0) lgkmcnt(0)\n\t" \
    "s_barrier \n\t" \
    "ds_read_b64_tr_b16 v[180:181], v244 offset:0\n\t" \
    "ds_read_b64_tr_b16 v[182:183], v244 offset:1536\n\t" \
    "ds_read_b64_tr_b16 v[188:189], v244 offset:32\n\t" \
    "ds_read_b64_tr_b16 v[190:191], v244 offset:1568\n\t" \
    "ds_read_b64_tr_b16 v[196:197], v244 offset:64\n\t" \
    "ds_read_b64_tr_b16 v[198:199], v244 offset:1600\n\t" \
    "s_waitcnt lgkmcnt(0)\n\t" \
    "s_barrier \n\t" \
    "ds_write_b128 v254, v[200:203] offset:30720\n\t" \
    "ds_write_b128 v254, v[204:207] offset:31744\n\t" \
    "ds_write_b128 v254, v[208:211] offset:32768\n\t" \
    "ds_write_b128 v254, v[212:215] offset:33792\n\t" \
    "ds_write_b128 v254, v[216:219] offset:34816\n\t" \
    "ds_write_b128 v254, v[220:223] offset:35840\n\t" \
    "ds_write_b128 v254, v[224:227] offset:36864\n\t" \
    "ds_write_b128 v254, v[228:231] offset:37888\n\t" \
    "ds_write_b128 v254, v[232:235] offset:38912\n\t" \
    "ds_write_b128 v254, v[236:239] offset:39936\n\t" \
    "s_cmp_lg_u32 s43, 0\n\t" \
    "s_cbranch_scc1 .Lv04_%=\n\t" \
    "s_mov_b32 s44, 0\n\t" \
    ".Lv05_%=:\n\t" \
    "v_cmp_ge_u32 vcc, s44, v252\n\t" \
    "s_cbranch_vccz .Lv06_%=\n\t" \
    "s_cmp_eq_u32 s41, 0\n\t" \
    "s_cbranch_scc1 .Lv06_%=\n\t" \
    "s_sub_u32 s41, s41, 1\n\t" \
    "s_sleep 8\n\t" \
    "v_and_b32 v252, 0xf0, v253\n\t" \
    "v_lshlrev_b32 v252, 3, v252\n\t" \
    "buffer_load_dword v252, v252, s[60:63], %[fl] offen sc1\n\t" \
    "s_waitcnt vmcnt(0)\n\t" \
    "s_branch .Lv05_%=\n\t" \
    ".Lv06_%=:\n\t" \
    "s_mov_b32 m0, 0x0\n\t" \
    "s_add_u32 s49, s42, 0x0\n\t" \
    "buffer_load_dwordx4 v253, s[60:63], s49 offen offset:0 sc1 lds\n\t" \
    "buffer_load_dwordx4 v253, s[60:63], s49 offen offset:1024 sc1 lds\n\t" \
    "buffer_load_dwordx4 v253, s[60:63], s49 offen offset:2048 sc1 lds\n\t" \
    "buffer_load_dwordx4 v253, s[60:63], s49 offen offset:3072 sc1 lds\n\t" \
    "s_mov_b32 m0, 0x4000\n\t" \
    "s_add_u32 s49, s42, 0x10000\n\t" \
    "buffer_load_dwordx4 v253, s[60:63], s49 offen offset:0 sc1 lds\n\t" \
    "s_mov_b32 m0, 0x5000\n\t" \
    "s_add_u32 s49, s42, 0x1000\n\t" \
    "buffer_load_dwordx4 v253, s[60:63], s49 offen offset:0 sc1 lds\n\t" \
    "buffer_load_dwordx4 v253, s[60:63], s49 offen offset:1024 sc1 lds\n\t" \
    "buffer_load_dwordx4 v253, s[60:63], s49 offen offset:2048 sc1 lds\n\t" \
    "buffer_load_dwordx4 v253, s[60:63], s49 offen offset:3072 sc1 lds\n\t" \
    "s_mov_b32 m0, 0x9000\n\t" \
    "s_add_u32 s49, s42, 0x11000\n\t" \
    "buffer_load_dwordx4 v253, s[60:63], s49 offen offset:0 sc1 lds\n\t" \
    ".Lv04_%=:\n\t" \
    "s_waitcnt vmcnt(0) lgkmcnt(0)\n\t" \
    "s_barrier \n\t" \
    "ds_read_b128 v[224:227], v253 offset:0\n\t" \
    "ds_read_b128 v[244:247], v254 offset:30720\n\t" \
    "ds_read_b128 v[228:231], v253 offset:1024\n\t" \
    "ds_read_b128 v[232:235], v253 offset:20480\n\t" \
    "ds_read_b128 v[236:239], v253 offset:21504\n\t" \
    "ds_read_b128 v[248:251], v254 offset:31744\n\t" \
    ".p2align 8\n\t" \
    ".Lv07_%=:\n\t" \
    "s_waitcnt lgkmcnt(5)\n\t" \
    "v_mfma_f32_16x16x32_bf16 v[204:207], v[0:3], v[224:227], 0\n\t" \
    "s_waitcnt lgkmcnt(3)\n\t" \
    "v_mfma_f32_16x16x32_bf16 v[204:207], v[4:7], v[228:231], v[204:207]\n\t" \
    "ds_read_b128 v[240:243], v253 offset:10240\n\t" \
    "v_mfma_f32_16x16x32_bf16 v[208:211], v[8:11], v[224:227], 0\n\t" \
    "v_mfma_f32_16x16x32_bf16 v[208:211], v[12:15], v[228:231], v[208:211]\n\t" \
    "v_mfma_f32_16x16x32_bf16 v[200:203], v[244:247], v[224:227], 0\n\t" \
    "s_waitcnt lgkmcnt(1)\n\t" \
    "v_mfma_f32_16x16x32_bf16 v[200:203], v[248:251], v[228:231], v[200:203]\n\t" \
    "v_mfma_f32_16x16x32_bf16 v[216:219], v[0:3], v[232:235], 0\n\t" \
    "ds_read_b128 v[224:227], v253 offset:11264\n\t" \
    "v_mfma_f32_16x16x32_bf16 v[216:219], v[4:7], v[236:239], v[216:219]\n\t" \
    "ds_read_b128 v[228:231], v253 offset:2048\n\t" \
    "v_mfma_f32_16x16x32_bf16 v[220:223], v[8:11], v[232:235], 0\n\t" \
    "v_mfma_f32_16x16x32_bf16 v[220:223], v[12:15], v[236:239], v[220:223]\n\t" \
    "v_mfma_f32_16x16x32_bf16 v[212:215], v[244:247], v[232:235], 0\n\t" \
    "ds_read_b128 v[244:247], v254 offset:32768\n\t" \
    "v_mfma_f32_16x16x32_bf16 v[212:215], v[248:251], v[236:239], v[212:215]\n\t" \
    "ds_read_b128 v[248:251], v254 offset:33792\n\t" \
    "s_waitcnt lgkmcnt(4)\n\t" \
    "v_mfma_f32_16x16x32_bf16 v[204:207], v[24:27], v[240:243], v[204:207]\n\t" \
    "ds_read_b128 v[232:235], v253 offset:3072\n\t" \
    "s_waitcnt lgkmcnt(4)\n\t" \
    "v_mfma_f32_16x16x32_bf16 v[204:207], v[28:31], v[224:227], v[204:207]\n\t" \
    "ds_read_b128 v[236:239], v253 offset:22528\n\t" \
    "v_mfma_f32_16x16x32_bf16 v[208:211], v[32:35], v[240:243], v[208:211]\n\t" \
    "v_mfma_f32_16x16x32_bf16 v[208:211], v[36:39], v[224:227], v[208:211]\n\t" \
    "v_mfma_f32_16x16x32_bf16 v[200:203], v[16:19], v[240:243], v[200:203]\n\t" \
    "v_mfma_f32_16x16x32_bf16 v[200:203], v[20:23], v[224:227], v[200:203]\n\t" \
    "s_waitcnt lgkmcnt(4)\n\t" \
    "v_mfma_f32_16x16x32_bf16 v[204:207], v[40:43], v[228:231], v[204:207]\n\t" \
    "ds_read_b128 v[240:243], v253 offset:23552\n\t" \
    "s_waitcnt lgkmcnt(2)\n\t" \
    "v_mfma_f32_16x16x32_bf16 v[204:207], v[44:47], v[232:235], v[204:207]\n\t" \
    "ds_read_b128 v[224:227], v253 offset:12288\n\t" \
    "v_mfma_f32_16x16x32_bf16 v[208:211], v[48:51], v[228:231], v[208:211]\n\t" \
    "v_mfma_f32_16x16x32_bf16 v[208:211], v[52:55], v[232:235], v[208:211]\n\t" \
    "v_mfma_f32_16x16x32_bf16 v[200:203], v[244:247], v[228:231], v[200:203]\n\t" \
    "v_mfma_f32_16x16x32_bf16 v[200:203], v[248:251], v[232:235], v[200:203]\n\t" \
    "s_waitcnt lgkmcnt(2)\n\t" \
    "v_mfma_f32_16x16x32_bf16 v[216:219], v[40:43], v[236:239], v[216:219]\n\t" \
    "ds_read_b128 v[228:231], v253 offset:13312\n\t" \
    "s_add_u32 s44, s40, 1\n\t" \
    "s_waitcnt lgkmcnt(2)\n\t" \
    "v_mfma_f32_16x16x32_bf16 v[216:219], v[44:47], v[240:243], v[216:219]\n\t" \
    "ds_read_b128 v[232:235], v253 offset:4096\n\t" \
    "s_and_b32 s45, s44, 15\n\t" \
    "v_mfma_f32_16x16x32_bf16 v[220:223], v[48:51], v[236:239], v[220:223]\n\t" \
    "s_cmp_eq_u32 s45, 0\n\t" \
    "v_mfma_f32_16x16x32_bf16 v[220:223], v[52:55], v[240:243], v[220:223]\n\t" \
    "s_cselect_b32 s45, %[cj], %[cs]\n\t" \
    "v_mfma_f32_16x16x32_bf16 v[212:215], v[244:247], v[236:239], v[212:215]\n\t" \
    "ds_read_b128 v[244:247], v254 offset:34816\n\t" \
    "s_cmp_eq_u32 s44, 64\n\t" \
    "v_mfma_f32_16x16x32_bf16 v[212:215], v[248:251], v[240:243], v[212:215]\n\t" \
    "ds_read_b128 v[248:251], v254 offset:35840\n\t" \
    "s_cselect_b32 s45, 0, s45\n\t" \
    "s_waitcnt lgkmcnt(4)\n\t" \
    "v_mfma_f32_16x16x32_bf16 v[204:207], v[64:67], v[224:227], v[204:207]\n\t" \
    "ds_read_b128 v[236:239], v253 offset:5120\n\t" \
    "s_add_u32 s42, s42, s45\n\t" \
    "s_waitcnt lgkmcnt(4)\n\t" \
    "v_mfma_f32_16x16x32_bf16 v[204:207], v[68:71], v[228:231], v[204:207]\n\t" \
    "ds_read_b128 v[240:243], v253 offset:24576\n\t" \
    "v_mfma_f32_16x16x32_bf16 v[208:211], v[72:75], v[224:227], v[208:211]\n\t" \
    "v_mfma_f32_16x16x32_bf16 v[208:211], v[76:79], v[228:231], v[208:211]\n\t" \
    "v_mfma_f32_16x16x32_bf16 v[200:203], v[56:59], v[224:227], v[200:203]\n\t" \
    "v_mfma_f32_16x16x32_bf16 v[200:203], v[60:63], v[228:231], v[200:203]\n\t" \
    "s_waitcnt lgkmcnt(4)\n\t" \
    "v_mfma_f32_16x16x32_bf16 v[204:207], v[80:83], v[232:235], v[204:207]\n\t" \
    "ds_read_b128 v[224:227], v253 offset:25600\n\t" \
    "s_waitcnt lgkmcnt(2)\n\t" \
    "v_mfma_f32_16x16x32_bf16 v[204:207], v[84:87], v[236:239], v[204:207]\n\t" \
    "ds_read_b128 v[228:231], v253 offset:14336\n\t" \
    "v_mfma_f32_16x16x32_bf16 v[208:211], v[88:91], v[232:235], v[208:211]\n\t" \
    "v_mfma_f32_16x16x32_bf16 v[208:211], v[92:95], v[236:239], v[208:211]\n\t" \
    "v_mfma_f32_16x16x32_bf16 v[200:203], v[244:247], v[232:235], v[200:203]\n\t" \
    "v_mfma_f32_16x16x32_bf16 v[200:203], v[248:251], v[236:239], v[200:203]\n\t" \
    "s_waitcnt lgkmcnt(2)\n\t" \
    "v_mfma_f32_16x16x32_bf16 v[216:219], v[80:83], v[240:243], v[216:219]\n\t" \
    "ds_read_b128 v[232:235], v253 offset:15360\n\t" \
    "s_and_b32 s44, s40, 3\n\t" \
    "s_waitcnt lgkmcnt(2)\n\t" \
    "v_mfma_f32_16x16x32_bf16 v[216:219], v[84:87], v[224:227], v[216:219]\n\t" \
    "ds_read_b128 v[236:239], v253 offset:6144\n\t" \
    "s_lshl_b32 s44, s44, 22\n\t" \
    "v_mfma_f32_16x16x32_bf16 v[220:223], v[88:91], v[240:243], v[220:223]\n\t" \
    "s_add_u32 s44, s44, %[mb]\n\t" \
    "v_mfma_f32_16x16x32_bf16 v[220:223], v[92:95], v[224:227], v[220:223]\n\t" \
    "s_add_u32 s46, s44, 0x0\n\t" \
    "v_mfma_f32_16x16x32_bf16 v[212:215], v[244:247], v[240:243], v[212:215]\n\t" \
    "ds_read_b128 v[244:247], v254 offset:36864\n\t" \
    "s_add_u32 s47, s44, 0x8400\n\t" \
    "v_mfma_f32_16x16x32_bf16 v[212:215], v[248:251], v[224:227], v[212:215]\n\t" \
    "ds_read_b128 v[248:251], v254 offset:37888\n\t" \
    "s_add_u32 s48, s44, 0x400\n\t" \
    "s_waitcnt lgkmcnt(4)\n\t" \
    "v_mfma_f32_16x16x32_bf16 v[204:207], v[104:107], v[228:231], v[204:207]\n\t" \
    "ds_read_b128 v[240:243], v253 offset:7168\n\t" \
    "s_waitcnt lgkmcnt(4)\n\t" \
    "v_mfma_f32_16x16x32_bf16 v[204:207], v[108:111], v[232:235], v[204:207]\n\t" \
    "ds_read_b128 v[224:227], v253 offset:26624\n\t" \
    "v_mfma_f32_16x16x32_bf16 v[208:211], v[112:115], v[228:231], v[208:211]\n\t" \
    "v_mfma_f32_16x16x32_bf16 v[208:211], v[116:119], v[232:235], v[208:211]\n\t" \
    "v_mfma_f32_16x16x32_bf16 v[200:203], v[96:99], v[228:231], v[200:203]\n\t" \
    "v_mfma_f32_16x16x32_bf16 v[200:203], v[100:103], v[232:235], v[200:203]\n\t" \
    "s_waitcnt lgkmcnt(4)\n\t" \
    "v_mfma_f32_16x16x32_bf16 v[204:207], v[120:123], v[236:239], v[204:207]\n\t" \
    "ds_read_b128 v[228:231], v253 offset:27648\n\t" \
    "s_waitcnt lgkmcnt(2)\n\t" \
    "v_mfma_f32_16x16x32_bf16 v[204:207], v[124:127], v[240:243], v[204:207]\n\t" \
    "ds_read_b128 v[232:235], v253 offset:16384\n\t" \
    "v_mfma_f32_16x16x32_bf16 v[208:211], v[128:131], v[236:239], v[208:211]\n\t" \
    "v_mfma_f32_16x16x32_bf16 v[208:211], v[132:135], v[240:243], v[208:211]\n\t" \
    "v_mfma_f32_16x16x32_bf16 v[200:203], v[244:247], v[236:239], v[200:203]\n\t" \
    "v_mfma_f32_16x16x32_bf16 v[200:203], v[248:251], v[240:243], v[200:203]\n\t" \
    "s_waitcnt lgkmcnt(2)\n\t" \
    "v_mfma_f32_16x16x32_bf16 v[216:219], v[120:123], v[224:227], v[216:219]\n\t" \
    "ds_read_b128 v[236:239], v253 offset:17408\n\t" \
    "s_waitcnt lgkmcnt(2)\n\t" \
    "v_mfma_f32_16x16x32_bf16 v[216:219], v[124:127], v[228:231], v[216:219]\n\t" \
    "ds_read_b128 v[240:243], v253 offset:8192\n\t" \
    "v_mfma_f32_16x16x32_bf16 v[220:223], v[128:131], v[224:227], v[220:223]\n\t" \
    "v_mfma_f32_16x16x32_bf16 v[220:223], v[132:135], v[228:231], v[220:223]\n\t" \
    "v_mfma_f32_16x16x32_bf16 v[212:215], v[244:247], v[224:227], v[212:215]\n\t" \
    "ds_read_b128 v[244:247], v254 offset:38912\n\t" \
    "v_mfma_f32_16x16x32_bf16 v[212:215], v[248:251], v[228:231], v[212:215]\n\t" \
    "ds_read_b128 v[248:251], v254 offset:39936\n\t" \
    "v_and_b32 v252, 0xf0, v253\n\t" \
    "v_lshlrev_b32 v252, 3, v252\n\t" \
    "buffer_load_dword v252, v252, s[60:63], %[fl] offen sc1\n\t" \
    "s_waitcnt lgkmcnt(4)\n\t" \
    "v_mfma_f32_16x16x32_bf16 v[204:207], v[144:147], v[232:235], v[204:207]\n\t" \
    "ds_read_b128 v[224:227], v253 offset:9216\n\t" \
    "s_waitcnt lgkmcnt(4)\n\t" \
    "v_mfma_f32_16x16x32_bf16 v[204:207], v[148:151], v[236:239], v[204:207]\n\t" \
    "ds_read_b128 v[228:231], v253 offset:28672\n\t" \
    "v_mfma_f32_16x16x32_bf16 v[208:211], v[152:155], v[232:235], v[208:211]\n\t" \
    "v_mfma_f32_16x16x32_bf16 v[208:211], v[156:159], v[236:239], v[208:211]\n\t" \
    "v_mfma_f32_16x16x32_bf16 v[200:203], v[136:139], v[232:235], v[200:203]\n\t" \
    "v_mfma_f32_16x16x32_bf16 v[200:203], v[140:143], v[236:239], v[200:203]\n\t" \
    "s_waitcnt lgkmcnt(4)\n\t" \
    "v_mfma_f32_16x16x32_bf16 v[204:207], v[160:163], v[240:243], v[204:207]\n\t" \
    "ds_read_b128 v[232:235], v253 offset:29696\n\t" \
    "s_waitcnt lgkmcnt(2)\n\t" \
    "v_mfma_f32_16x16x32_bf16 v[204:207], v[164:167], v[224:227], v[204:207]\n\t" \
    "ds_read_b128 v[236:239], v253 offset:18432\n\t" \
    "v_mfma_f32_16x16x32_bf16 v[208:211], v[168:171], v[240:243], v[208:211]\n\t" \
    "v_mfma_f32_16x16x32_bf16 v[208:211], v[172:175], v[224:227], v[208:211]\n\t" \
    "v_mfma_f32_16x16x32_bf16 v[200:203], v[244:247], v[240:243], v[200:203]\n\t" \
    "v_mfma_f32_16x16x32_bf16 v[200:203], v[248:251], v[224:227], v[200:203]\n\t" \
    "s_waitcnt lgkmcnt(2)\n\t" \
    "v_mfma_f32_16x16x32_bf16 v[216:219], v[160:163], v[228:231], v[216:219]\n\t" \
    "ds_read_b128 v[240:243], v253 offset:19456\n\t" \
    "s_waitcnt lgkmcnt(2)\n\t" \
    "v_mfma_f32_16x16x32_bf16 v[216:219], v[164:167], v[232:235], v[216:219]\n\t" \
    "ds_read_b128 v[224:227], v253 offset:30720\n\t" \
    "v_mfma_f32_16x16x32_bf16 v[220:223], v[168:171], v[228:231], v[220:223]\n\t" \
    "v_mfma_f32_16x16x32_bf16 v[220:223], v[172:175], v[232:235], v[220:223]\n\t" \
    "v_mfma_f32_16x16x32_bf16 v[212:215], v[244:247], v[228:231], v[212:215]\n\t" \
    "v_mfma_f32_16x16x32_bf16 v[212:215], v[248:251], v[232:235], v[212:215]\n\t" \
    "s_waitcnt lgkmcnt(2)\n\t" \
    "v_mfma_f32_16x16x32_bf16 v[204:207], v[184:187], v[236:239], v[204:207]\n\t" \
    "ds_read_b128 v[228:231], v253 offset:31744\n\t" \
    "s_waitcnt lgkmcnt(2)\n\t" \
    "v_mfma_f32_16x16x32_bf16 v[204:207], v[188:191], v[240:243], v[204:207]\n\t" \
    "ds_read_b128 v[232:235], v253 offset:32768\n\t" \
    "v_mfma_f32_16x16x32_bf16 v[208:211], v[192:195], v[236:239], v[208:211]\n\t" \
    "v_mfma_f32_16x16x32_bf16 v[208:211], v[196:199], v[240:243], v[208:211]\n\t" \
    "v_mfma_f32_16x16x32_bf16 v[200:203], v[176:179], v[236:239], v[200:203]\n\t" \
    "v_mfma_f32_16x16x32_bf16 v[200:203], v[180:183], v[240:243], v[200:203]\n\t" \
    "s_waitcnt lgkmcnt(2)\n\t" \
    "v_mfma_f32_16x16x32_bf16 v[216:219], v[24:27], v[224:227], v[216:219]\n\t" \
    "ds_read_b128 v[236:239], v253 offset:33792\n\t" \
    "s_waitcnt lgkmcnt(2)\n\t" \
    "v_mfma_f32_16x16x32_bf16 v[216:219], v[28:31], v[228:231], v[216:219]\n\t" \
    "ds_read_b128 v[240:243], v253 offset:34816\n\t" \
    "v_mfma_f32_16x16x32_bf16 v[220:223], v[32:35], v[224:227], v[220:223]\n\t" \
    "v_cvt_pk_bf16_f32 v204, v204, v205\n\t" \
    "v_mfma_f32_16x16x32_bf16 v[220:223], v[36:39], v[228:231], v[220:223]\n\t" \
    "v_cvt_pk_bf16_f32 v205, v206, v207\n\t" \
    "v_mfma_f32_16x16x32_bf16 v[212:215], v[16:19], v[224:227], v[212:215]\n\t" \
    "ds_write_b64 v254, v[204:205] offset:7168\n\t" \
    "v_mfma_f32_16x16x32_bf16 v[212:215], v[20:23], v[228:231], v[212:215]\n\t" \
    "v_cvt_pk_bf16_f32 v208, v208, v209\n\t" \
    "s_waitcnt lgkmcnt(3)\n\t" \
    "v_mfma_f32_16x16x32_bf16 v[216:219], v[64:67], v[232:235], v[216:219]\n\t" \
    "ds_read_b128 v[224:227], v253 offset:35840\n\t" \
    "v_cvt_pk_bf16_f32 v209, v210, v211\n\t" \
    "s_waitcnt lgkmcnt(3)\n\t" \
    "v_mfma_f32_16x16x32_bf16 v[216:219], v[68:71], v[236:239], v[216:219]\n\t" \
    "ds_read_b128 v[228:231], v253 offset:36864\n\t" \
    "ds_write_b64 v254, v[208:209] offset:8192\n\t" \
    "v_mfma_f32_16x16x32_bf16 v[220:223], v[72:75], v[232:235], v[220:223]\n\t" \
    "v_cvt_pk_bf16_f32 v200, v200, v201\n\t" \
    "v_mfma_f32_16x16x32_bf16 v[220:223], v[76:79], v[236:239], v[220:223]\n\t" \
    "v_cvt_pk_bf16_f32 v201, v202, v203\n\t" \
    "v_mfma_f32_16x16x32_bf16 v[212:215], v[56:59], v[232:235], v[212:215]\n\t" \
    "ds_write_b64 v254, v[200:201] offset:6144\n\t" \
    "v_mfma_f32_16x16x32_bf16 v[212:215], v[60:63], v[236:239], v[212:215]\n\t" \
    "s_waitcnt lgkmcnt(5)\n\t" \
    "v_mfma_f32_16x16x32_bf16 v[216:219], v[104:107], v[240:243], v[216:219]\n\t" \
    "ds_read_b128 v[232:235], v253 offset:37888\n\t" \
    "s_waitcnt lgkmcnt(4)\n\t" \
    "v_mfma_f32_16x16x32_bf16 v[216:219], v[108:111], v[224:227], v[216:219]\n\t" \
    "ds_read_b128 v[236:239], v253 offset:38912\n\t" \
    "v_mfma_f32_16x16x32_bf16 v[220:223], v[112:115], v[240:243], v[220:223]\n\t" \
    "v_mfma_f32_16x16x32_bf16 v[220:223], v[116:119], v[224:227], v[220:223]\n\t" \
    "v_mfma_f32_16x16x32_bf16 v[212:215], v[96:99], v[240:243], v[212:215]\n\t" \
    "v_mfma_f32_16x16x32_bf16 v[212:215], v[100:103], v[224:227], v[212:215]\n\t" \
    "ds_read_b128 v[240:243], v253 offset:39936\n\t" \
    "s_waitcnt lgkmcnt(3)\n\t" \
    "s_barrier \n\t" \
    "v_mfma_f32_16x16x32_bf16 v[216:219], v[144:147], v[228:231], v[216:219]\n\t" \
    "s_waitcnt lgkmcnt(2)\n\t" \
    "v_mfma_f32_16x16x32_bf16 v[216:219], v[148:151], v[232:235], v[216:219]\n\t" \
    "s_waitcnt lgkmcnt(1)\n\t" \
    "v_mfma_f32_16x16x32_bf16 v[216:219], v[184:187], v[236:239], v[216:219]\n\t" \
    "s_waitcnt lgkmcnt(0)\n\t" \
    "v_mfma_f32_16x16x32_bf16 v[216:219], v[188:191], v[240:243], v[216:219]\n\t" \
    "v_mfma_f32_16x16x32_bf16 v[220:223], v[152:155], v[228:231], v[220:223]\n\t" \
    "v_mfma_f32_16x16x32_bf16 v[220:223], v[156:159], v[232:235], v[220:223]\n\t" \
    "v_mfma_f32_16x16x32_bf16 v[220:223], v[192:195], v[236:239], v[220:223]\n\t" \
    "v_cvt_pk_bf16_f32 v216, v216, v217\n\t" \
    "v_cvt_pk_bf16_f32 v217, v218, v219\n\t" \
    "v_mfma_f32_16x16x32_bf16 v[220:223], v[196:199], v[240:243], v[220:223]\n\t" \
    "ds_write_b64 v254, v[216:217] offset:13312\n\t" \
    "v_mfma_f32_16x16x32_bf16 v[212:215], v[136:139], v[228:231], v[212:215]\n\t" \
    "v_mfma_f32_16x16x32_bf16 v[212:215], v[140:143], v[232:235], v[212:215]\n\t" \
    "v_mfma_f32_16x16x32_bf16 v[212:215], v[176:179], v[236:239], v[212:215]\n\t" \
    "v_cvt_pk_bf16_f32 v220, v220, v221\n\t" \
    "v_cvt_pk_bf16_f32 v221, v222, v223\n\t" \
    "v_mfma_f32_16x16x32_bf16 v[212:215], v[180:183], v[240:243], v[212:215]\n\t" \
    "ds_write_b64 v254, v[220:221] offset:14336\n\t" \
    "s_nop 6\n\t" \
    "v_cvt_pk_bf16_f32 v212, v212, v213\n\t" \
    "v_cvt_pk_bf16_f32 v213, v214, v215\n\t" \
    "ds_write_b64 v254, v[212:213] offset:12288\n\t" \
    "s_waitcnt vmcnt(0) lgkmcnt(0)\n\t" \
    "s_barrier \n\t" \
    "s_setprio 2\n\t" \
    "s_add_u32 s44, s40, 1\n\t" \
    "s_min_u32 s44, s44, 63\n\t" \
    ".Lv08_%=:\n\t" \
    "v_cmp_ge_u32 vcc, s44, v252\n\t" \
    "s_cbranch_vccz .Lv09_%=\n\t" \
    "s_cmp_eq_u32 s41, 0\n\t" \
    "s_cbranch_scc1 .Lv09_%=\n\t" \
    "s_sub_u32 s41, s41, 1\n\t" \
    "s_sleep 2\n\t" \
    "v_and_b32 v252, 0xf0, v253\n\t" \
    "v_lshlrev_b32 v252, 3, v252\n\t" \
    "buffer_load_dword v252, v252, s[60:63], %[fl] offen sc1\n\t" \
    "s_waitcnt vmcnt(0)\n\t" \
    "s_branch .Lv08_%=\n\t" \
    ".Lv09_%=:\n\t" \
    "s_mov_b32 m0, 0x0\n\t" \
    "s_add_u32 s49, s42, 0x0\n\t" \
    "buffer_load_dwordx4 v253, s[60:63], s49 offen offset:0 sc1 lds\n\t" \
    "s_sleep 1\n\t" \
    "buffer_load_dwordx4 v253, s[60:63], s49 offen offset:1024 sc1 lds\n\t" \
    "s_sleep 1\n\t" \
    "buffer_load_dwordx4 v253, s[60:63], s49 offen offset:2048 sc1 lds\n\t" \
    "s_sleep 1\n\t" \
    "buffer_load_dwordx4 v253, s[60:63], s49 offen offset:3072 sc1 lds\n\t" \
    "s_sleep 1\n\t" \
    "s_mov_b32 m0, 0x4000\n\t" \
    "s_add_u32 s49, s42, 0x10000\n\t" \
    "buffer_load_dwordx4 v253, s[60:63], s49 offen offset:0 sc1 lds\n\t" \
    "s_sleep 1\n\t" \
    "s_mov_b32 m0, 0x5000\n\t" \
    "s_add_u32 s49, s42, 0x1000\n\t" \
    "buffer_load_dwordx4 v253, s[60:63], s49 offen offset:0 sc1 lds\n\t" \
    "s_sleep 1\n\t" \
    "buffer_load_dwordx4 v253, s[60:63], s49 offen offset:1024 sc1 lds\n\t" \
    "s_sleep 1\n\t" \
    "buffer_load_dwordx4 v253, s[60:63], s49 offen offset:2048 sc1 lds\n\t" \
    "s_sleep 1\n\t" \
    "buffer_load_dwordx4 v253, s[60:63], s49 offen offset:3072 sc1 lds\n\t" \
    "s_sleep 1\n\t" \
    "s_mov_b32 m0, 0x9000\n\t" \
    "s_add_u32 s49, s42, 0x11000\n\t" \
    "buffer_load_dwordx4 v253, s[60:63], s49 offen offset:0 sc1 lds\n\t" \
    "s_sleep 1\n\t" \
    "ds_read_b128 v[200:203], v254 offset:30720\n\t" \
    "ds_read_b128 v[204:207], v254 offset:31744\n\t" \
    "ds_read_b128 v[216:219], v254 offset:32768\n\t" \
    "ds_read_b128 v[220:223], v254 offset:33792\n\t" \
    "v_mov_b64 v[208:209], 0\n\t" \
    "v_mov_b64 v[210:211], 0\n\t" \
    "v_mov_b64 v[244:245], 0\n\t" \
    "v_mov_b64 v[246:247], 0\n\t" \
    "v_mov_b64 v[248:249], 0\n\t" \
    "v_mov_b64 v[250:251], 0\n\t" \
    "s_sleep 5\n\t" \
    "s_mov_b32 m0, 0xa000\n\t" \
    "s_nop 0\n\t" \
    "buffer_load_dwordx4 v253, s[60:63], s46 offen sc1 lds\n\t" \
    "s_mov_b32 m0, 0xb400\n\t" \
    "s_nop 0\n\t" \
    "buffer_load_dwordx4 v253, s[60:63], s47 offen sc1 lds\n\t" \
    "s_mov_b32 m0, 0xa400\n\t" \
    "s_nop 0\n\t" \
    "buffer_load_dwordx4 v253, s[60:63], s48 offen sc1 lds\n\t" \
    "s_waitcnt lgkmcnt(0)\n\t" \
    "v_bfe_i32 v212, v253, 5, 1\n\t" \
    "v_and_b32 v212, 0xaaaa, v212\n\t" \
    "v_or_b32 v212, 0x4444, v212\n\t" \
    "v_and_b32 v213, 0xffff, v255\n\t" \
    "v_lshrrev_b32 v214, 16, v255\n\t" \
    ".Lv010_%=:\n\t" \
    "s_waitcnt vmcnt(0)\n\t" \
    "ds_read_b128 v[224:227], v253 offset:40960\n\t" \
    "ds_read_b128 v[228:231], v253 offset:46080\n\t" \
    "ds_read_b128 v[232:235], v253 offset:41984\n\t" \
    "s_waitcnt lgkmcnt(2)\n\t" \
    "v_max_u32 v215, v224, v225\n\t" \
    "v_max3_u32 v215, v226, v227, v215\n\t" \
    "s_waitcnt lgkmcnt(1)\n\t" \
    "v_max3_u32 v215, v228, v229, v215\n\t" \
    "v_max3_u32 v215, v230, v231, v215\n\t" \
    "s_waitcnt lgkmcnt(0)\n\t" \
    "v_max3_u32 v215, v232, v233, v215\n\t" \
    "v_max3_u32 v215, v234, v235, v215\n\t" \
    "v_cmp_eq_u32 vcc, -1, v215\n\t" \
    "s_cbranch_vccz .Lv011_%=\n\t" \
    "v_max_u32 v215, v224, v225\n\t" \
    "v_max3_u32 v215, v226, v227, v215\n\t" \
    "v_cmp_eq_u32 vcc, -1, v215\n\t" \
    "s_cbranch_vccz .Lv012_%=\n\t" \
    "s_mov_b32 m0, 0xa000\n\t" \
    "s_nop 0\n\t" \
    "buffer_load_dwordx4 v253, s[60:63], s46 offen sc1 lds\n\t" \
    ".Lv012_%=:\n\t" \
    "v_max_u32 v215, v228, v229\n\t" \
    "v_max3_u32 v215, v230, v231, v215\n\t" \
    "v_cmp_eq_u32 vcc, -1, v215\n\t" \
    "s_cbranch_vccz .Lv013_%=\n\t" \
    "s_mov_b32 m0, 0xb400\n\t" \
    "s_nop 0\n\t" \
    "buffer_load_dwordx4 v253, s[60:63], s47 offen sc1 lds\n\t" \
    ".Lv013_%=:\n\t" \
    "v_max_u32 v215, v232, v233\n\t" \
    "v_max3_u32 v215, v234, v235, v215\n\t" \
    "v_cmp_eq_u32 vcc, -1, v215\n\t" \
    "s_cbranch_vccz .Lv014_%=\n\t" \
    "s_mov_b32 m0, 0xa400\n\t" \
    "s_nop 0\n\t" \
    "buffer_load_dwordx4 v253, s[60:63], s48 offen sc1 lds\n\t" \
    ".Lv014_%=:\n\t" \
    "s_cmp_eq_u32 s41, 0\n\t" \
    "s_cbranch_scc1 .Lv011_%=\n\t" \
    "s_sub_u32 s41, s41, 1\n\t" \
    "s_branch .Lv010_%=\n\t" \
    ".Lv011_%=:\n\t" \
    "s_waitcnt lgkmcnt(0)\n\t" \
    "s_barrier \n\t" \
    "ds_read_b128 v[236:239], v253 offset:47104\n\t" \
    "ds_read_b128 v[240:243], v253 offset:43008\n\t" \
    "v_smfmac_f32_16x16x64_bf16 v[244:247], v[224:227], v[0:7], v212\n\t" \
    "v_smfmac_f32_16x16x64_bf16 v[248:251], v[224:227], v[8:15], v212\n\t" \
    "v_smfmac_f32_16x16x64_bf16 v[208:211], v[224:227], v[200:207], v212\n\t" \
    "ds_read_b128 v[200:203], v254 offset:34816\n\t" \
    "ds_read_b128 v[204:207], v254 offset:35840\n\t" \
    "ds_read_b128 v[224:227], v253 offset:48128\n\t" \
    "v_smfmac_f32_16x16x64_bf16 v[208:211], v[228:231], v[16:23], v212\n\t" \
    "v_smfmac_f32_16x16x64_bf16 v[244:247], v[228:231], v[24:31], v212\n\t" \
    "v_smfmac_f32_16x16x64_bf16 v[248:251], v[228:231], v[32:39], v212\n\t" \
    "ds_read_b128 v[228:231], v253 offset:44032\n\t" \
    "v_smfmac_f32_16x16x64_bf16 v[208:211], v[232:235], v[216:223], v212\n\t" \
    "ds_read_b128 v[216:219], v254 offset:36864\n\t" \
    "ds_read_b128 v[220:223], v254 offset:37888\n\t" \
    "v_smfmac_f32_16x16x64_bf16 v[244:247], v[232:235], v[40:47], v212\n\t" \
    "v_smfmac_f32_16x16x64_bf16 v[248:251], v[232:235], v[48:55], v212\n\t" \
    "ds_read_b128 v[232:235], v253 offset:49152\n\t" \
    "s_waitcnt lgkmcnt(8)\n\t" \
    "v_smfmac_f32_16x16x64_bf16 v[208:211], v[236:239], v[56:63], v212\n\t" \
    "v_smfmac_f32_16x16x64_bf16 v[244:247], v[236:239], v[64:71], v212\n\t" \
    "v_smfmac_f32_16x16x64_bf16 v[248:251], v[236:239], v[72:79], v212\n\t" \
    "ds_read_b128 v[236:239], v253 offset:45056\n\t" \
    "s_waitcnt lgkmcnt(6)\n\t" \
    "v_smfmac_f32_16x16x64_bf16 v[208:211], v[240:243], v[200:207], v212\n\t" \
    "ds_read_b128 v[200:203], v254 offset:38912\n\t" \
    "ds_read_b128 v[204:207], v254 offset:39936\n\t" \
    "v_smfmac_f32_16x16x64_bf16 v[244:247], v[240:243], v[80:87], v212\n\t" \
    "v_smfmac_f32_16x16x64_bf16 v[248:251], v[240:243], v[88:95], v212\n\t" \
    "ds_read_b128 v[240:243], v253 offset:50176\n\t" \
    "s_waitcnt lgkmcnt(8)\n\t" \
    "v_smfmac_f32_16x16x64_bf16 v[208:211], v[224:227], v[96:103], v212\n\t" \
    "v_smfmac_f32_16x16x64_bf16 v[244:247], v[224:227], v[104:111], v212\n\t" \
    "v_smfmac_f32_16x16x64_bf16 v[248:251], v[224:227], v[112:119], v212\n\t" \
    "s_waitcnt lgkmcnt(7)\n\t" \
    "v_smfmac_f32_16x16x64_bf16 v[244:247], v[228:231], v[120:127], v212\n\t" \
    "s_waitcnt lgkmcnt(4)\n\t" \
    "v_smfmac_f32_16x16x64_bf16 v[244:247], v[232:235], v[144:151], v212\n\t" \
    "s_waitcnt lgkmcnt(3)\n\t" \
    "v_smfmac_f32_16x16x64_bf16 v[244:247], v[236:239], v[160:167], v212\n\t" \
    "s_waitcnt lgkmcnt(0)\n\t" \
    "v_smfmac_f32_16x16x64_bf16 v[244:247], v[240:243], v[184:191], v212\n\t" \
    "v_smfmac_f32_16x16x64_bf16 v[248:251], v[228:231], v[128:135], v212\n\t" \
    "v_smfmac_f32_16x16x64_bf16 v[248:251], v[232:235], v[152:159], v212\n\t" \
    "v_smfmac_f32_16x16x64_bf16 v[248:251], v[236:239], v[168:175], v212\n\t" \
    "v_add_f32 v244, v244, v246\n\t" \
    "v_add_f32 v245, v245, v247\n\t" \
    "v_cvt_pk_bf16_f32 v244, v244, v245\n\t" \
    "v_smfmac_f32_16x16x64_bf16 v[248:251], v[240:243], v[192:199], v212\n\t" \
    "ds_write_b16 v213, v244 offset:512\n\t" \
    "ds_write_b16_d16_hi v213, v244 offset:528\n\t" \
    "v_smfmac_f32_16x16x64_bf16 v[208:211], v[228:231], v[216:223], v212\n\t" \
    "v_smfmac_f32_16x16x64_bf16 v[208:211], v[232:235], v[136:143], v212\n\t" \
    "v_smfmac_f32_16x16x64_bf16 v[208:211], v[236:239], v[200:207], v212\n\t" \
    "v_add_f32 v248, v248, v250\n\t" \
    "v_add_f32 v249, v249, v251\n\t" \
    "v_cvt_pk_bf16_f32 v248, v248, v249\n\t" \
    "v_smfmac_f32_16x16x64_bf16 v[208:211], v[240:243], v[176:183], v212\n\t" \
    "ds_write_b16 v213, v248 offset:1024\n\t" \
    "ds_write_b16_d16_hi v213, v248 offset:1040\n\t" \
    "s_nop 5\n\t" \
    "v_add_f32 v208, v208, v210\n\t" \
    "v_add_f32 v209, v209, v211\n\t" \
    "v_cvt_pk_bf16_f32 v208, v208, v209\n\t" \
    "ds_write_b16 v213, v208 offset:0\n\t" \
    "ds_write_b16_d16_hi v213, v208 offset:16\n\t" \
    "s_setprio 0\n\t" \
    "s_waitcnt lgkmcnt(0)\n\t" \
    "s_barrier \n\t" \
    "ds_read_b128 v[224:227], v253 offset:0\n\t" \
    "ds_read_b128 v[244:247], v254 offset:30720\n\t" \
    "ds_read_b128 v[228:231], v253 offset:1024\n\t" \
    "ds_read_b128 v[232:235], v253 offset:20480\n\t" \
    "ds_read_b128 v[236:239], v253 offset:21504\n\t" \
    "ds_read_b128 v[248:251], v254 offset:31744\n\t" \
    "s_add_u32 s40, s40, 1\n\t" \
    "s_cmp_lg_u32 s40, 64\n\t" \
    "s_cbranch_scc1 .Lv07_%=\n\t" \
    "s_branch .Lend%=\n\t" \
    ".Lend%=:\n\t" \
    "s_waitcnt vmcnt(0) lgkmcnt(0)\n\t" \
    "s_mov_b32 %[bud], s41\n\t"
#define ASM_R_LOOP \
    "s_mov_b32 s40, 0\n\t" \
    "s_mov_b32 s41, 0x40000\n\t" \
    "v_mov_b64 v[252:253], -1\n\t" \
    "v_mov_b64 v[254:255], -1\n\t" \
    "v_mov_b32 v248, 0\n\t" \
    "s_mov_b32 s53, 0x05040100\n\t" \
    ".Lr1_%=:\n\t" \
    "s_mov_b32 s43, s40\n\t" \
    "s_and_b32 s44, s40, 3\n\t" \
    "s_lshl_b32 s42, s44, 22\n\t" \
    "s_add_u32 s42, s42, %[vx]\n\t" \
    "s_lshl_b32 s46, s44, 22\n\t" \
    "s_mov_b32 s47, s46\n\t" \
    "s_lshl_b32 s49, s44, 17\n\t" \
    "s_add_u32 s49, s49, %[px]\n\t" \
    "s_add_u32 s50, s49, 0x1000\n\t" \
    "s_add_u32 s44, s40, 1\n\t" \
    "s_and_b32 s44, s44, 3\n\t" \
    "s_lshl_b32 s48, s44, 22\n\t" \
    "s_mul_i32 s52, s40, 0x14000\n\t" \
    "s_lshr_b32 s44, s40, 4\n\t" \
    "s_mul_i32 s44, s44, 0xc2000\n\t" \
    "s_add_u32 s52, s52, s44\n\t" \
    "s_add_u32 s52, s52, %[xr]\n\t" \
    "s_add_i32 s44, s40, %[ad]\n\t" \
    "s_and_b32 s45, s44, 3\n\t" \
    "s_mul_i32 s51, s45, %[as]\n\t" \
    "s_cmp_lt_i32 s44, 0\n\t" \
    "s_cselect_b32 s55, 0x40000000, 0\n\t" \
    "s_add_u32 s44, s40, 3\n\t" \
    "s_mul_i32 s54, s44, 0x14000\n\t" \
    "s_lshr_b32 s45, s44, 4\n\t" \
    "s_mul_i32 s45, s45, 0xc2000\n\t" \
    "s_add_u32 s54, s54, s45\n\t" \
    "s_cmp_eq_u32 %[wave], 7\n\t" \
    "s_cselect_b32 s51, s54, s51\n\t" \
    "s_cselect_b32 s55, 0, s55\n\t" \
    "s_cselect_b32 s44, 60, 62\n\t" \
    "s_cmp_gt_u32 s40, s44\n\t" \
    "s_cselect_b32 s55, 0x40000000, s55\n\t" \
    "v_or_b32 v197, s55, %[ae]\n\t" \
    "v_mov_b64 v[180:181], -1\n\t" \
    "v_mov_b64 v[182:183], -1\n\t" \
    "v_mov_b64 v[184:185], -1\n\t" \
    "v_mov_b64 v[186:187], -1\n\t" \
    "v_mov_b64 v[188:189], -1\n\t" \
    "v_mov_b64 v[190:191], -1\n\t" \
    "v_mov_b64 v[192:193], -1\n\t" \
    "v_mov_b64 v[194:195], -1\n\t" \
    "s_waitcnt vmcnt(0)\n\t" \
    ".Lr2_%=:\n\t" \
    "buffer_load_dword v248, %[fo], %[rs], %[fs] offen sc1\n\t" \
    "s_waitcnt vmcnt(0)\n\t" \
    "v_cmp_ge_u32 vcc, s43, v248\n\t" \
    "s_cbranch_vccz .Lr3_%=\n\t" \
    "s_sleep 2\n\t" \
    "s_cmp_eq_u32 s41, 0\n\t" \
    "s_cbranch_scc1 .Lr3_%=\n\t" \
    "s_sub_u32 s41, s41, 1\n\t" \
    "s_branch .Lr2_%=\n\t" \
    ".Lr3_%=:\n\t" \
    "buffer_load_dwordx4 v[160:163], %[l16], %[rs], s42 offen offset:0 sc1\n\t" \
    "buffer_load_dwordx4 v[164:167], %[l16], %[rs], s42 offen offset:1024 sc1\n\t" \
    "buffer_load_dwordx4 v[168:171], %[l16], %[rs], s42 offen offset:2048 sc1\n\t" \
    "buffer_load_dwordx4 v[172:175], %[l16], %[rs], s42 offen offset:3072 sc1\n\t" \
    "s_cmp_lg_u32 s40, 63\n\t" \
    "s_cbranch_scc1 .Lr6_%=\n\t" \
    "buffer_load_dwordx4 v[180:183], %[ov], %[rs], s46 offen sc1\n\t" \
    ".Lr7_%=:\n\t" \
    "s_waitcnt vmcnt(12)\n\t" \
    "v_max_u32 v196, v180, v181\n\t" \
    "v_max3_u32 v196, v182, v183, v196\n\t" \
    "v_cmp_eq_u32 vcc, -1, v196\n\t" \
    "s_cbranch_vccz .Lr8_%=\n\t" \
    "s_nop 7\n\t" \
    "v_max_u32 v196, v180, v181\n\t" \
    "v_max3_u32 v196, v182, v183, v196\n\t" \
    "v_cmp_eq_u32 vcc, -1, v196\n\t" \
    "s_cbranch_vccz .Lr8_%=\n\t" \
    "s_nop 7\n\t" \
    "v_max_u32 v196, v180, v181\n\t" \
    "v_max3_u32 v196, v182, v183, v196\n\t" \
    "v_cmp_eq_u32 vcc, -1, v196\n\t" \
    "s_cbranch_vccz .Lr8_%=\n\t" \
    "s_nop 7\n\t" \
    "v_max_u32 v196, v180, v181\n\t" \
    "v_max3_u32 v196, v182, v183, v196\n\t" \
    "v_cmp_eq_u32 vcc, -1, v196\n\t" \
    "s_cbranch_vccz .Lr8_%=\n\t" \
    "s_nop 7\n\t" \
    "s_cmp_eq_u32 s41, 0\n\t" \
    "s_cbranch_scc1 .Lr8_%=\n\t" \
    "s_sub_u32 s41, s41, 1\n\t" \
    "v_max_u32 v196, v180, v181\n\t" \
    "v_max3_u32 v196, v182, v183, v196\n\t" \
    "v_cmp_eq_u32 vcc, -1, v196\n\t" \
    "s_cbranch_vccz .Lr9_%=\n\t" \
    "buffer_load_dwordx4 v[180:183], %[ov], %[rs], s46 offen sc1\n\t" \
    ".Lr9_%=:\n\t" \
    "s_branch .Lr7_%=\n\t" \
    ".Lr8_%=:\n\t" \
    "s_branch .Lr5_%=\n\t" \
    ".Lr6_%=:\n\t" \
    "s_sleep 9\n\t" \
    "buffer_load_dwordx4 v[184:187], %[o0], %[rs], s47 offen sc1\n\t" \
    "buffer_load_dwordx4 v[188:191], %[o1], %[rs], s47 offen sc1\n\t" \
    "buffer_load_dwordx4 v[192:195], %[o2], %[rs], s47 offen sc1\n\t" \
    "buffer_load_dwordx4 v[180:183], %[ov], %[rs], s46 offen sc1\n\t" \
    ".Lr10_%=:\n\t" \
    "s_waitcnt vmcnt(12)\n\t" \
    "v_max_u32 v196, v184, v185\n\t" \
    "v_max3_u32 v196, v186, v187, v196\n\t" \
    "v_max3_u32 v196, v188, v189, v196\n\t" \
    "v_max3_u32 v196, v190, v191, v196\n\t" \
    "v_max3_u32 v196, v192, v193, v196\n\t" \
    "v_max3_u32 v196, v194, v195, v196\n\t" \
    "v_max3_u32 v196, v180, v181, v196\n\t" \
    "v_max3_u32 v196, v182, v183, v196\n\t" \
    "v_cmp_eq_u32 vcc, -1, v196\n\t" \
    "s_cbranch_vccz .Lr11_%=\n\t" \
    "s_nop 7\n\t" \
    "v_max_u32 v196, v184, v185\n\t" \
    "v_max3_u32 v196, v186, v187, v196\n\t" \
    "v_max3_u32 v196, v188, v189, v196\n\t" \
    "v_max3_u32 v196, v190, v191, v196\n\t" \
    "v_max3_u32 v196, v192, v193, v196\n\t" \
    "v_max3_u32 v196, v194, v195, v196\n\t" \
    "v_max3_u32 v196, v180, v181, v196\n\t" \
    "v_max3_u32 v196, v182, v183, v196\n\t" \
    "v_cmp_eq_u32 vcc, -1, v196\n\t" \
    "s_cbranch_vccz .Lr11_%=\n\t" \
    "s_nop 7\n\t" \
    "v_max_u32 v196, v184, v185\n\t" \
    "v_max3_u32 v196, v186, v187, v196\n\t" \
    "v_max3_u32 v196, v188, v189, v196\n\t" \
    "v_max3_u32 v196, v190, v191, v196\n\t" \
    "v_max3_u32 v196, v192, v193, v196\n\t" \
    "v_max3_u32 v196, v194, v195, v196\n\t" \
    "v_max3_u32 v196, v180, v181, v196\n\t" \
    "v_max3_u32 v196, v182, v183, v196\n\t" \
    "v_cmp_eq_u32 vcc, -1, v196\n\t" \
    "s_cbranch_vccz .Lr11_%=\n\t" \
    "s_nop 7\n\t" \
    "v_max_u32 v196, v184, v185\n\t" \
    "v_max3_u32 v196, v186, v187, v196\n\t" \
    "v_max3_u32 v196, v188, v189, v196\n\t" \
    "v_max3_u32 v196, v190, v191, v196\n\t" \
    "v_max3_u32 v196, v192, v193, v196\n\t" \
    "v_max3_u32 v196, v194, v195, v196\n\t" \
    "v_max3_u32 v196, v180, v181, v196\n\t" \
    "v_max3_u32 v196, v182, v183, v196\n\t" \
    "v_cmp_eq_u32 vcc, -1, v196\n\t" \
    "s_cbranch_vccz .Lr11_%=\n\t" \
    "s_nop 7\n\t" \
    "v_max_u32 v196, v184, v185\n\t" \
    "v_max3_u32 v196, v186, v187, v196\n\t" \
    "v_max3_u32 v196, v188, v189, v196\n\t" \
    "v_max3_u32 v196, v190, v191, v196\n\t" \
    "v_max3_u32 v196, v192, v193, v196\n\t" \
    "v_max3_u32 v196, v194, v195, v196\n\t" \
    "v_max3_u32 v196, v180, v181, v196\n\t" \
    "v_max3_u32 v196, v182, v183, v196\n\t" \
    "v_cmp_eq_u32 vcc, -1, v196\n\t" \
    "s_cbranch_vccz .Lr11_%=\n\t" \
    "s_nop 7\n\t" \
    "v_max_u32 v196, v184, v185\n\t" \
    "v_max3_u32 v196, v186, v187, v196\n\t" \
    "v_max3_u32 v196, v188, v189, v196\n\t" \
    "v_max3_u32 v196, v190, v191, v196\n\t" \
    "v_max3_u32 v196, v192, v193, v196\n\t" \
    "v_max3_u32 v196, v194, v195, v196\n\t" \
    "v_max3_u32 v196, v180, v181, v196\n\t" \
    "v_max3_u32 v196, v182, v183, v196\n\t" \
    "v_cmp_eq_u32 vcc, -1, v196\n\t" \
    "s_cbranch_vccz .Lr11_%=\n\t" \
    "s_nop 7\n\t" \
    "v_max_u32 v196, v184, v185\n\t" \
    "v_max3_u32 v196, v186, v187, v196\n\t" \
    "v_max3_u32 v196, v188, v189, v196\n\t" \
    "v_max3_u32 v196, v190, v191, v196\n\t" \
    "v_max3_u32 v196, v192, v193, v196\n\t" \
    "v_max3_u32 v196, v194, v195, v196\n\t" \
    "v_max3_u32 v196, v180, v181, v196\n\t" \
    "v_max3_u32 v196, v182, v183, v196\n\t" \
    "v_cmp_eq_u32 vcc, -1, v196\n\t" \
    "s_cbranch_vccz .Lr11_%=\n\t" \
    "s_nop 7\n\t" \
    "v_max_u32 v196, v184, v185\n\t" \
    "v_max3_u32 v196, v186, v187, v196\n\t" \
    "v_max3_u32 v196, v188, v189, v196\n\t" \
    "v_max3_u32 v196, v190, v191, v196\n\t" \
    "v_max3_u32 v196, v192, v193, v196\n\t" \
    "v_max3_u32 v196, v194, v195, v196\n\t" \
    "v_max3_u32 v196, v180, v181, v196\n\t" \
    "v_max3_u32 v196, v182, v183, v196\n\t" \
    "v_cmp_eq_u32 vcc, -1, v196\n\t" \
    "s_cbranch_vccz .Lr11_%=\n\t" \
    "s_nop 7\n\t" \
    "s_cmp_eq_u32 s41, 0\n\t" \
    "s_cbranch_scc1 .Lr11_%=\n\t" \
    "s_sub_u32 s41, s41, 1\n\t" \
    "v_max_u32 v196, v184, v185\n\t" \
    "v_max3_u32 v196, v186, v187, v196\n\t" \
    "v_cmp_eq_u32 vcc, -1, v196\n\t" \
    "s_cbranch_vccz .Lr12_%=\n\t" \
    "buffer_load_dwordx4 v[184:187], %[o0], %[rs], s47 offen sc1\n\t" \
    ".Lr12_%=:\n\t" \
    "v_max_u32 v196, v188, v189\n\t" \
    "v_max3_u32 v196, v190, v191, v196\n\t" \
    "v_cmp_eq_u32 vcc, -1, v196\n\t" \
    "s_cbranch_vccz .Lr13_%=\n\t" \
    "buffer_load_dwordx4 v[188:191], %[o1], %[rs], s47 offen sc1\n\t" \
    ".Lr13_%=:\n\t" \
    "v_max_u32 v196, v192, v193\n\t" \
    "v_max3_u32 v196, v194, v195, v196\n\t" \
    "v_cmp_eq_u32 vcc, -1, v196\n\t" \
    "s_cbranch_vccz .Lr14_%=\n\t" \
    "buffer_load_dwordx4 v[192:195], %[o2], %[rs], s47 offen sc1\n\t" \
    ".Lr14_%=:\n\t" \
    "v_max_u32 v196, v180, v181\n\t" \
    "v_max3_u32 v196, v182, v183, v196\n\t" \
    "v_cmp_eq_u32 vcc, -1, v196\n\t" \
    "s_cbranch_vccz .Lr15_%=\n\t" \
    "buffer_load_dwordx4 v[180:183], %[ov], %[rs], s46 offen sc1\n\t" \
    ".Lr15_%=:\n\t" \
    "s_branch .Lr10_%=\n\t" \
    ".Lr11_%=:\n\t" \
    "v_mfma_f32_16x16x32_bf16 v[176:179], v[192:195], v[180:183], 0\n\t" \
    "v_smfmac_f32_16x16x64_bf16 v[176:179], v[184:187], v[160:167], %[sidx]\n\t" \
    "v_smfmac_f32_16x16x64_bf16 v[176:179], v[188:191], v[168:175], %[sidx]\n\t" \
    "s_cmp_gt_u32 %[wave], 3\n\t" \
    "s_nop 7\n\t" \
    "v_add_f32 v176, v176, v178\n\t" \
    "v_add_f32 v177, v177, v179\n\t" \
    "ds_write2_b32 %[rw], v176, v177 offset1:80\n\t" \
    "s_waitcnt lgkmcnt(0)\n\t" \
    "s_barrier \n\t" \
    "s_cbranch_scc1 .Lr4_%=\n\t" \
    "ds_read_b128 v[128:131], %[rd] offset:0\n\t" \
    "ds_read_b128 v[132:135], %[rd] offset:16\n\t" \
    "ds_read_b128 v[136:139], %[rd] offset:32\n\t" \
    "ds_read_b128 v[140:143], %[rd] offset:48\n\t" \
    "s_waitcnt lgkmcnt(2)\n\t" \
    "v_pk_add_f32 v[128:129], v[128:129], v[130:131]\n\t" \
    "v_pk_add_f32 v[132:133], v[132:133], v[134:135]\n\t" \
    "s_waitcnt lgkmcnt(0)\n\t" \
    "v_pk_add_f32 v[136:137], v[136:137], v[138:139]\n\t" \
    "v_pk_add_f32 v[140:141], v[140:141], v[142:143]\n\t" \
    "v_pk_add_f32 v[128:129], v[128:129], v[132:133]\n\t" \
    "v_pk_add_f32 v[136:137], v[136:137], v[140:141]\n\t" \
    "v_pk_add_f32 v[128:129], v[128:129], v[136:137]\n\t" \
    "v_cvt_pk_bf16_f32 v128, v128, v129\n\t" \
    "v_min_u32 v128, -2, v128\n\t" \
    "buffer_store_dword v128, %[oo], %[rs], s48 offen sc1\n\t" \
    ".Lr4_%=:\n\t" \
    "s_cmp_lt_u32 %[wave], 4\n\t" \
    "s_cbranch_scc1 .Lr17_%=\n\t" \
    "s_sleep 4\n\t" \
    ".Lr17_%=:\n\t" \
    "s_sleep 2\n\t" \
    ".Lr5_%=:\n\t" \
    "s_cmp_lt_u32 %[wave], 6\n\t" \
    "s_cbranch_scc1 .Lr16_%=\n\t" \
    "buffer_load_dwordx2 v[250:251], %[xo], %[rs], s52 offen sc1\n\t" \
    ".Lr16_%=:\n\t" \
    "buffer_load_dwordx4 v[220:223], %[l16], %[rs], s50 offen offset:1024 sc1\n\t" \
    "buffer_load_dwordx4 v[224:227], %[l16], %[rs], s50 offen offset:2048 sc1\n\t" \
    "buffer_load_dwordx4 v[228:231], %[l16], %[rs], s50 offen offset:3072 sc1\n\t" \
    "v_perm_b32 v232, v161, v160, s53\n\t" \
    "v_perm_b32 v233, v163, v162, s53\n\t" \
    "v_perm_b32 v234, v165, v164, s53\n\t" \
    "v_perm_b32 v235, v167, v166, s53\n\t" \
    "v_perm_b32 v236, v169, v168, s53\n\t" \
    "v_perm_b32 v237, v171, v170, s53\n\t" \
    "v_perm_b32 v238, v173, v172, s53\n\t" \
    "v_perm_b32 v239, v175, v174, s53\n\t" \
    "s_nop 1\n\t" \
    "v_permlane16_swap_b32 v232, v234\n\t" \
    "v_permlane16_swap_b32 v233, v235\n\t" \
    "v_permlane16_swap_b32 v236, v238\n\t" \
    "v_permlane16_swap_b32 v237, v239\n\t" \
    "s_waitcnt vmcnt(0)\n\t" \
    ".Lr18_%=:\n\t" \
    "v_max_u32 v196, v220, v221\n\t" \
    "v_max3_u32 v196, v222, v223, v196\n\t" \
    "v_max3_u32 v196, v224, v225, v196\n\t" \
    "v_max3_u32 v196, v226, v227, v196\n\t" \
    "v_max3_u32 v196, v228, v229, v196\n\t" \
    "v_max3_u32 v196, v230, v231, v196\n\t" \
    "v_cmp_eq_u32 vcc, -1, v196\n\t" \
    "s_cbranch_vccz .Lr19_%=\n\t" \
    "s_cmp_eq_u32 s41, 0\n\t" \
    "s_cbranch_scc1 .Lr19_%=\n\t" \
    "s_sub_u32 s41, s41, 1\n\t" \
    "v_max_u32 v196, v220, v221\n\t" \
    "v_max3_u32 v196, v222, v223, v196\n\t" \
    "v_cmp_eq_u32 vcc, -1, v196\n\t" \
    "s_cbranch_vccz .Lr20_%=\n\t" \
    "buffer_load_dwordx4 v[220:223], %[l16], %[rs], s50 offen offset:1024 sc1\n\t" \
    ".Lr20_%=:\n\t" \
    "v_max_u32 v196, v224, v225\n\t" \
    "v_max3_u32 v196, v226, v227, v196\n\t" \
    "v_cmp_eq_u32 vcc, -1, v196\n\t" \
    "s_cbranch_vccz .Lr21_%=\n\t" \
    "buffer_load_dwordx4 v[224:227], %[l16], %[rs], s50 offen offset:2048 sc1\n\t" \
    ".Lr21_%=:\n\t" \
    "v_max_u32 v196, v228, v229\n\t" \
    "v_max3_u32 v196, v230, v231, v196\n\t" \
    "v_cmp_eq_u32 vcc, -1, v196\n\t" \
    "s_cbranch_vccz .Lr22_%=\n\t" \
    "buffer_load_dwordx4 v[228:231], %[l16], %[rs], s50 offen offset:3072 sc1\n\t" \
    ".Lr22_%=:\n\t" \
    "s_waitcnt vmcnt(0)\n\t" \
    "s_branch .Lr18_%=\n\t" \
    ".Lr19_%=:\n\t" \
    "v_mfma_f32_16x16x32_bf16 v[240:243], v[232:235], v[220:223], 0\n\t" \
    "v_mfma_f32_16x16x32_bf16 v[240:243], v[236:239], v[224:227], v[240:243]\n\t" \
    "v_mfma_f32_16x16x32_bf16 v[240:243], v[180:183], v[228:231], v[240:243]\n\t" \
    "s_nop 7\n\t" \
    "ds_write_b128 %[pw], v[240:243]\n\t" \
    "s_waitcnt lgkmcnt(0)\n\t" \
    "s_barrier \n\t" \
    "buffer_load_dwordx4 v[200:203], %[l16], %[rs], s49 offen offset:0 sc1\n\t" \
    "buffer_load_dwordx4 v[204:207], %[l16], %[rs], s49 offen offset:1024 sc1\n\t" \
    "buffer_load_dwordx4 v[208:211], %[l16], %[rs], s49 offen offset:2048 sc1\n\t" \
    "buffer_load_dwordx4 v[212:215], %[l16], %[rs], s49 offen offset:3072 sc1\n\t" \
    "buffer_load_dwordx4 v[216:219], %[l16], %[rs], s50 offen offset:0 sc1\n\t" \
    "s_cmp_lg_u32 %[wave], 6\n\t" \
    "s_cbranch_scc1 .Lr23_%=\n\t" \
    "ds_read_b128 v[128:131], %[pr] offset:0\n\t" \
    "ds_read_b128 v[132:135], %[pr] offset:1024\n\t" \
    "ds_read_b128 v[136:139], %[pr] offset:2048\n\t" \
    "ds_read_b128 v[140:143], %[pr] offset:3072\n\t" \
    "ds_read_b128 v[144:147], %[pr] offset:4096\n\t" \
    "ds_read_b128 v[148:151], %[pr] offset:5120\n\t" \
    "ds_read_b128 v[152:155], %[pr] offset:6144\n\t" \
    "ds_read_b128 v[156:159], %[pr] offset:7168\n\t" \
    "v_lshlrev_b32 v176, 16, v250\n\t" \
    "v_and_b32 v177, 0xffff0000, v250\n\t" \
    "v_lshlrev_b32 v178, 16, v251\n\t" \
    "v_and_b32 v179, 0xffff0000, v251\n\t" \
    "s_waitcnt lgkmcnt(6)\n\t" \
    "v_pk_add_f32 v[128:129], v[128:129], v[132:133]\n\t" \
    "v_pk_add_f32 v[130:131], v[130:131], v[134:135]\n\t" \
    "s_waitcnt lgkmcnt(4)\n\t" \
    "v_pk_add_f32 v[136:137], v[136:137], v[140:141]\n\t" \
    "v_pk_add_f32 v[138:139], v[138:139], v[142:143]\n\t" \
    "v_pk_add_f32 v[128:129], v[128:129], v[136:137]\n\t" \
    "v_pk_add_f32 v[130:131], v[130:131], v[138:139]\n\t" \
    "s_waitcnt lgkmcnt(2)\n\t" \
    "v_pk_add_f32 v[144:145], v[144:145], v[148:149]\n\t" \
    "v_pk_add_f32 v[146:147], v[146:147], v[150:151]\n\t" \
    "s_waitcnt lgkmcnt(0)\n\t" \
    "v_pk_add_f32 v[152:153], v[152:153], v[156:157]\n\t" \
    "v_pk_add_f32 v[154:155], v[154:155], v[158:159]\n\t" \
    "v_pk_add_f32 v[144:145], v[144:145], v[152:153]\n\t" \
    "v_pk_add_f32 v[146:147], v[146:147], v[154:155]\n\t" \
    "v_pk_add_f32 v[128:129], v[128:129], v[144:145]\n\t" \
    "v_pk_add_f32 v[130:131], v[130:131], v[146:147]\n\t" \
    "v_pk_add_f32 v[128:129], v[128:129], v[176:177]\n\t" \
    "v_pk_add_f32 v[130:131], v[130:131], v[178:179]\n\t" \
    "v_cvt_pk_bf16_f32 v128, v128, v129\n\t" \
    "v_cvt_pk_bf16_f32 v129, v130, v131\n\t" \
    "v_min_u32 v128, -2, v128\n\t" \
    "v_min_u32 v129, -2, v129\n\t" \
    "s_add_u32 s44, s52, 0x1000000\n\t" \
    "buffer_store_dwordx2 v[128:129], %[xo], %[rs], s44 offen sc1\n\t" \
    "s_waitcnt vmcnt(1)\n\t" \
    "s_branch .Lr24_%=\n\t" \
    ".Lr23_%=:\n\t" \
    "s_waitcnt vmcnt(0)\n\t" \
    ".Lr24_%=:\n\t" \
    ".Lr25_%=:\n\t" \
    "v_max_u32 v196, v200, v201\n\t" \
    "v_max3_u32 v196, v202, v203, v196\n\t" \
    "v_max3_u32 v196, v204, v205, v196\n\t" \
    "v_max3_u32 v196, v206, v207, v196\n\t" \
    "v_max3_u32 v196, v208, v209, v196\n\t" \
    "v_max3_u32 v196, v210, v211, v196\n\t" \
    "v_max3_u32 v196, v212, v213, v196\n\t" \
    "v_max3_u32 v196, v214, v215, v196\n\t" \
    "v_max3_u32 v196, v216, v217, v196\n\t" \
    "v_max3_u32 v196, v218, v219, v196\n\t" \
    "v_cmp_eq_u32 vcc, -1, v196\n\t" \
    "s_cbranch_vccz .Lr26_%=\n\t" \
    "s_cmp_eq_u32 s41, 0\n\t" \
    "s_cbranch_scc1 .Lr26_%=\n\t" \
    "s_sub_u32 s41, s41, 1\n\t" \
    "v_max_u32 v196, v200, v201\n\t" \
    "v_max3_u32 v196, v202, v203, v196\n\t" \
    "v_cmp_eq_u32 vcc, -1, v196\n\t" \
    "s_cbranch_vccz .Lr27_%=\n\t" \
    "buffer_load_dwordx4 v[200:203], %[l16], %[rs], s49 offen offset:0 sc1\n\t" \
    ".Lr27_%=:\n\t" \
    "v_max_u32 v196, v204, v205\n\t" \
    "v_max3_u32 v196, v206, v207, v196\n\t" \
    "v_cmp_eq_u32 vcc, -1, v196\n\t" \
    "s_cbranch_vccz .Lr28_%=\n\t" \
    "buffer_load_dwordx4 v[204:207], %[l16], %[rs], s49 offen offset:1024 sc1\n\t" \
    ".Lr28_%=:\n\t" \
    "v_max_u32 v196, v208, v209\n\t" \
    "v_max3_u32 v196, v210, v211, v196\n\t" \
    "v_cmp_eq_u32 vcc, -1, v196\n\t" \
    "s_cbranch_vccz .Lr29_%=\n\t" \
    "buffer_load_dwordx4 v[208:211], %[l16], %[rs], s49 offen offset:2048 sc1\n\t" \
    ".Lr29_%=:\n\t" \
    "v_max_u32 v196, v212, v213\n\t" \
    "v_max3_u32 v196, v214, v215, v196\n\t" \
    "v_cmp_eq_u32 vcc, -1, v196\n\t" \
    "s_cbranch_vccz .Lr30_%=\n\t" \
    "buffer_load_dwordx4 v[212:215], %[l16], %[rs], s49 offen offset:3072 sc1\n\t" \
    ".Lr30_%=:\n\t" \
    "v_max_u32 v196, v216, v217\n\t" \
    "v_max3_u32 v196, v218, v219, v196\n\t" \
    "v_cmp_eq_u32 vcc, -1, v196\n\t" \
    "s_cbranch_vccz .Lr31_%=\n\t" \
    "buffer_load_dwordx4 v[216:219], %[l16], %[rs], s50 offen offset:0 sc1\n\t" \
    ".Lr31_%=:\n\t" \
    "s_waitcnt vmcnt(0)\n\t" \
    "s_branch .Lr25_%=\n\t" \
    ".Lr26_%=:\n\t" \
    "v_mfma_f32_16x16x32_bf16 v[244:247], v[160:163], v[200:203], 0\n\t" \
    "v_mfma_f32_16x16x32_bf16 v[244:247], v[164:167], v[204:207], v[244:247]\n\t" \
    "v_mfma_f32_16x16x32_bf16 v[244:247], v[168:171], v[208:211], v[244:247]\n\t" \
    "v_mfma_f32_16x16x32_bf16 v[244:247], v[172:175], v[212:215], v[244:247]\n\t" \
    "v_mfma_f32_16x16x32_bf16 v[244:247], v[180:183], v[216:219], v[244:247]\n\t" \
    "s_nop 7\n\t" \
    "ds_write_b128 %[pw], v[244:247] offset:8192\n\t" \
    "s_waitcnt lgkmcnt(0)\n\t" \
    "s_barrier \n\t" \
    "s_cmp_lg_u32 %[wave], 7\n\t" \
    "s_cbranch_scc1 .Lr32_%=\n\t" \
    "ds_read_b128 v[128:131], %[pr] offset:0\n\t" \
    "ds_read_b128 v[132:135], %[pr] offset:1024\n\t" \
    "ds_read_b128 v[136:139], %[pr] offset:2048\n\t" \
    "ds_read_b128 v[140:143], %[pr] offset:3072\n\t" \
    "ds_read_b128 v[144:147], %[pr] offset:4096\n\t" \
    "ds_read_b128 v[148:151], %[pr] offset:5120\n\t" \
    "ds_read_b128 v[152:155], %[pr] offset:6144\n\t" \
    "ds_read_b128 v[156:159], %[pr] offset:7168\n\t" \
    "v_lshlrev_b32 v176, 16, v250\n\t" \
    "v_and_b32 v177, 0xffff0000, v250\n\t" \
    "v_lshlrev_b32 v178, 16, v251\n\t" \
    "v_and_b32 v179, 0xffff0000, v251\n\t" \
    "s_waitcnt lgkmcnt(6)\n\t" \
    "v_pk_add_f32 v[128:129], v[128:129], v[132:133]\n\t" \
    "v_pk_add_f32 v[130:131], v[130:131], v[134:135]\n\t" \
    "s_waitcnt lgkmcnt(4)\n\t" \
    "v_pk_add_f32 v[136:137], v[136:137], v[140:141]\n\t" \
    "v_pk_add_f32 v[138:139], v[138:139], v[142:143]\n\t" \
    "v_pk_add_f32 v[128:129], v[128:129], v[136:137]\n\t" \
    "v_pk_add_f32 v[130:131], v[130:131], v[138:139]\n\t" \
    "s_waitcnt lgkmcnt(2)\n\t" \
    "v_pk_add_f32 v[144:145], v[144:145], v[148:149]\n\t" \
    "v_pk_add_f32 v[146:147], v[146:147], v[150:151]\n\t" \
    "s_waitcnt lgkmcnt(0)\n\t" \
    "v_pk_add_f32 v[152:153], v[152:153], v[156:157]\n\t" \
    "v_pk_add_f32 v[154:155], v[154:155], v[158:159]\n\t" \
    "v_pk_add_f32 v[144:145], v[144:145], v[152:153]\n\t" \
    "v_pk_add_f32 v[146:147], v[146:147], v[154:155]\n\t" \
    "v_pk_add_f32 v[128:129], v[128:129], v[144:145]\n\t" \
    "v_pk_add_f32 v[130:131], v[130:131], v[146:147]\n\t" \
    "v_pk_add_f32 v[128:129], v[128:129], v[176:177]\n\t" \
    "v_pk_add_f32 v[130:131], v[130:131], v[178:179]\n\t" \
    "v_cvt_pk_bf16_f32 v128, v128, v129\n\t" \
    "v_cvt_pk_bf16_f32 v129, v130, v131\n\t" \
    "v_min_u32 v128, -2, v128\n\t" \
    "v_min_u32 v129, -2, v129\n\t" \
    "s_add_u32 s44, s52, 0x1000000\n\t" \
    "buffer_store_dwordx2 v[128:129], %[xo], %[rs], s44 offen sc1\n\t" \
    ".Lr32_%=:\n\t" \
    "buffer_store_dwordx4 v[252:255], %[ov], %[rs], s46 offen sc1\n\t" \
    "buffer_store_dwordx4 v[252:255], v197, %[rs], s51 offen sc1\n\t" \
    "s_add_u32 s40, s40, 1\n\t" \
    "s_cmp_lg_u32 s40, 64\n\t" \
    "s_cbranch_scc1 .Lr1_%=\n\t" \
    "s_branch .Lend%=\n\t" \
    ".Lend%=:\n\t" \
    "s_waitcnt vmcnt(0) lgkmcnt(0)\n\t" \
    "s_mov_b32 %[bud], s41\n\t"
#define ASM_W4_MUL \
    "s_mov_b32 s40, 0\n\t" \
    "s_mov_b32 s41, 0x40000\n\t" \
    "s_mov_b32 s43, 0\n\t" \
    "s_mov_b32 s46, %[hb]\n\t" \
    "s_add_u32 s47, s46, 0x4000\n\t" \
    "v_mov_b64 v[188:189], -1\n\t" \
    "v_mov_b32 v246, 0x1e000\n\t" \
    ".Lm1_%=:\n\t" \
    ".Lm2_%=:\n\t" \
    "ds_read_b32 v245, v246 offset:24\n\t" \
    "s_sub_i32 s44, s40, 3\n\t" \
    "s_waitcnt lgkmcnt(0)\n\t" \
    "v_cmp_gt_i32 vcc, s44, v245\n\t" \
    "s_cbranch_vccnz .Lm2_%=\n\t" \
    "s_add_u32 s44, s40, 1\n\t" \
    "s_min_u32 s44, s44, 127\n\t" \
    "s_lshr_b32 s45, s44, 1\n\t" \
    "s_and_b32 s44, s44, 1\n\t" \
    "s_lshl_b32 s44, s44, 12\n\t" \
    "s_mul_i32 s48, s45, 0x30000\n\t" \
    "s_add_u32 s48, s48, s44\n\t" \
    "s_lshr_b32 s45, s45, 3\n\t" \
    "s_mul_i32 s45, s45, 0x82000\n\t" \
    "s_add_u32 s48, s48, s45\n\t" \
    "s_add_u32 s48, s48, %[hb]\n\t" \
    "s_add_u32 s49, s48, 0x4000\n\t" \
    "s_cmp_lg_u32 s43, 0\n\t" \
    "s_cbranch_scc1 .Lm5_%=\n\t" \
    ".Lm3_%=:\n\t" \
    "v_max_u32 v244, v188, v189\n\t" \
    "v_max3_u32 v244, v190, v191, v244\n\t" \
    "v_cmp_eq_u32 vcc, -1, v244\n\t" \
    "s_cbranch_vccz .Lm4_%=\n\t" \
    "s_cmp_eq_u32 s41, 0\n\t" \
    "s_cbranch_scc1 .Lm4_%=\n\t" \
    "s_sub_u32 s41, s41, 1\n\t" \
    "s_sleep 3\n\t" \
    "buffer_load_dwordx4 v[188:191], %[l16], %[rs], s47 offen offset:3072 sc1\n\t" \
    "s_waitcnt vmcnt(0)\n\t" \
    "s_branch .Lm3_%=\n\t" \
    ".Lm4_%=:\n\t" \
    "buffer_load_dwordx4 v[184:187], %[l16], %[rs], s47 offen offset:2048 sc1\n\t" \
    "buffer_load_dwordx4 v[180:183], %[l16], %[rs], s47 offen offset:1024 sc1\n\t" \
    "buffer_load_dwordx4 v[176:179], %[l16], %[rs], s47 offen offset:0 sc1\n\t" \
    "buffer_load_dwordx4 v[172:175], %[l16], %[rs], s46 offen offset:3072 sc1\n\t" \
    "buffer_load_dwordx4 v[168:171], %[l16], %[rs], s46 offen offset:2048 sc1\n\t" \
    "buffer_load_dwordx4 v[164:167], %[l16], %[rs], s46 offen offset:1024 sc1\n\t" \
    "buffer_load_dwordx4 v[160:163], %[l16], %[rs], s46 offen offset:0 sc1\n\t" \
    ".Lm5_%=:\n\t" \
    "v_mfma_f32_16x16x32_bf16 v[224:227], v[28:31], v[188:191], 0\n\t" \
    "v_mfma_f32_16x16x32_bf16 v[228:231], v[60:63], v[188:191], 0\n\t" \
    "v_mfma_f32_16x16x32_bf16 v[232:235], v[92:95], v[188:191], 0\n\t" \
    "v_mfma_f32_16x16x32_bf16 v[236:239], v[124:127], v[188:191], 0\n\t" \
    "v_mfma_f32_16x16x32_bf16 v[240:243], v[156:159], v[188:191], 0\n\t" \
    "s_waitcnt vmcnt(6)\n\t" \
    ".Lm6_%=:\n\t" \
    "v_max_u32 v244, v184, v185\n\t" \
    "v_max3_u32 v244, v186, v187, v244\n\t" \
    "v_cmp_eq_u32 vcc, -1, v244\n\t" \
    "s_cbranch_vccnz .Lm7_%=\n\t" \
    ".Lm8_%=:\n\t" \
    "v_mfma_f32_16x16x32_bf16 v[224:227], v[24:27], v[184:187], v[224:227]\n\t" \
    "v_mfma_f32_16x16x32_bf16 v[228:231], v[56:59], v[184:187], v[228:231]\n\t" \
    "v_mfma_f32_16x16x32_bf16 v[232:235], v[88:91], v[184:187], v[232:235]\n\t" \
    "v_mfma_f32_16x16x32_bf16 v[236:239], v[120:123], v[184:187], v[236:239]\n\t" \
    "v_mfma_f32_16x16x32_bf16 v[240:243], v[152:155], v[184:187], v[240:243]\n\t" \
    "s_waitcnt vmcnt(5)\n\t" \
    ".Lm9_%=:\n\t" \
    "v_max_u32 v244, v180, v181\n\t" \
    "v_max3_u32 v244, v182, v183, v244\n\t" \
    "v_cmp_eq_u32 vcc, -1, v244\n\t" \
    "s_cbranch_vccnz .Lm10_%=\n\t" \
    ".Lm11_%=:\n\t" \
    "v_mfma_f32_16x16x32_bf16 v[224:227], v[20:23], v[180:183], v[224:227]\n\t" \
    "v_mfma_f32_16x16x32_bf16 v[228:231], v[52:55], v[180:183], v[228:231]\n\t" \
    "v_mfma_f32_16x16x32_bf16 v[232:235], v[84:87], v[180:183], v[232:235]\n\t" \
    "v_mfma_f32_16x16x32_bf16 v[236:239], v[116:119], v[180:183], v[236:239]\n\t" \
    "v_mfma_f32_16x16x32_bf16 v[240:243], v[148:151], v[180:183], v[240:243]\n\t" \
    "s_waitcnt vmcnt(4)\n\t" \
    ".Lm12_%=:\n\t" \
    "v_max_u32 v244, v176, v177\n\t" \
    "v_max3_u32 v244, v178, v179, v244\n\t" \
    "v_cmp_eq_u32 vcc, -1, v244\n\t" \
    "s_cbranch_vccnz .Lm13_%=\n\t" \
    ".Lm14_%=:\n\t" \
    "v_mfma_f32_16x16x32_bf16 v[224:227], v[16:19], v[176:179], v[224:227]\n\t" \
    "v_mfma_f32_16x16x32_bf16 v[228:231], v[48:51], v[176:179], v[228:231]\n\t" \
    "v_mfma_f32_16x16x32_bf16 v[232:235], v[80:83], v[176:179], v[232:235]\n\t" \
    "v_mfma_f32_16x16x32_bf16 v[236:239], v[112:115], v[176:179], v[236:239]\n\t" \
    "v_mfma_f32_16x16x32_bf16 v[240:243], v[144:147], v[176:179], v[240:243]\n\t" \
    "s_waitcnt vmcnt(3)\n\t" \
    ".Lm15_%=:\n\t" \
    "v_max_u32 v244, v172, v173\n\t" \
    "v_max3_u32 v244, v174, v175, v244\n\t" \
    "v_cmp_eq_u32 vcc, -1, v244\n\t" \
    "s_cbranch_vccnz .Lm16_%=\n\t" \
    ".Lm17_%=:\n\t" \
    "v_mfma_f32_16x16x32_bf16 v[224:227], v[12:15], v[172:175], v[224:227]\n\t" \
    "v_mfma_f32_16x16x32_bf16 v[228:231], v[44:47], v[172:175], v[228:231]\n\t" \
    "v_mfma_f32_16x16x32_bf16 v[232:235], v[76:79], v[172:175], v[232:235]\n\t" \
    "v_mfma_f32_16x16x32_bf16 v[236:239], v[108:111], v[172:175], v[236:239]\n\t" \
    "v_mfma_f32_16x16x32_bf16 v[240:243], v[140:143], v[172:175], v[240:243]\n\t" \
    "s_waitcnt vmcnt(2)\n\t" \
    ".Lm18_%=:\n\t" \
    "v_max_u32 v244, v168, v169\n\t" \
    "v_max3_u32 v244, v170, v171, v244\n\t" \
    "v_cmp_eq_u32 vcc, -1, v244\n\t" \
    "s_cbranch_vccnz .Lm19_%=\n\t" \
    ".Lm20_%=:\n\t" \
    "buffer_load_dwordx4 v[220:223], %[l16], %[rs], s49 offen offset:3072 sc1\n\t" \
    "v_mfma_f32_16x16x32_bf16 v[224:227], v[8:11], v[168:171], v[224:227]\n\t" \
    "v_mfma_f32_16x16x32_bf16 v[228:231], v[40:43], v[168:171], v[228:231]\n\t" \
    "v_mfma_f32_16x16x32_bf16 v[232:235], v[72:75], v[168:171], v[232:235]\n\t" \
    "v_mfma_f32_16x16x32_bf16 v[236:239], v[104:107], v[168:171], v[236:239]\n\t" \
    "v_mfma_f32_16x16x32_bf16 v[240:243], v[136:139], v[168:171], v[240:243]\n\t" \
    "s_waitcnt vmcnt(2)\n\t" \
    ".Lm21_%=:\n\t" \
    "v_max_u32 v244, v164, v165\n\t" \
    "v_max3_u32 v244, v166, v167, v244\n\t" \
    "v_cmp_eq_u32 vcc, -1, v244\n\t" \
    "s_cbranch_vccnz .Lm22_%=\n\t" \
    ".Lm23_%=:\n\t" \
    "v_mfma_f32_16x16x32_bf16 v[224:227], v[4:7], v[164:167], v[224:227]\n\t" \
    "v_mfma_f32_16x16x32_bf16 v[228:231], v[36:39], v[164:167], v[228:231]\n\t" \
    "v_mfma_f32_16x16x32_bf16 v[232:235], v[68:71], v[164:167], v[232:235]\n\t" \
    "v_mfma_f32_16x16x32_bf16 v[236:239], v[100:103], v[164:167], v[236:239]\n\t" \
    "v_mfma_f32_16x16x32_bf16 v[240:243], v[132:135], v[164:167], v[240:243]\n\t" \
    "s_waitcnt vmcnt(1)\n\t" \
    ".Lm24_%=:\n\t" \
    "v_max_u32 v244, v160, v161\n\t" \
    "v_max3_u32 v244, v162, v163, v244\n\t" \
    "v_cmp_eq_u32 vcc, -1, v244\n\t" \
    "s_cbranch_vccnz .Lm25_%=\n\t" \
    ".Lm26_%=:\n\t" \
    "v_mfma_f32_16x16x32_bf16 v[224:227], v[0:3], v[160:163], v[224:227]\n\t" \
    "v_mfma_f32_16x16x32_bf16 v[228:231], v[32:35], v[160:163], v[228:231]\n\t" \
    "v_mfma_f32_16x16x32_bf16 v[232:235], v[64:67], v[160:163], v[232:235]\n\t" \
    "v_mfma_f32_16x16x32_bf16 v[236:239], v[96:99], v[160:163], v[236:239]\n\t" \
    "v_mfma_f32_16x16x32_bf16 v[240:243], v[128:131], v[160:163], v[240:243]\n\t" \
    "s_add_u32 s40, s40, 1\n\t" \
    "v_mov_b32 v245, s40\n\t" \
    "ds_write_b128 %[ld], v[224:227] offset:0\n\t" \
    "ds_write_b128 %[ld], v[228:231] offset:6144\n\t" \
    "ds_write_b128 %[ld], v[232:235] offset:12288\n\t" \
    "s_nop 0\n\t" \
    "ds_write_b128 %[ld], v[236:239] offset:18432\n\t" \
    "s_nop 1\n\t" \
    "ds_write_b128 %[ld], v[240:243] offset:24576\n\t" \
    "ds_write_b32 %[fa], v245\n\t" \
    "s_mov_b32 s43, 0\n\t" \
    "s_waitcnt vmcnt(0)\n\t" \
    "v_max_u32 v244, v220, v221\n\t" \
    "v_max3_u32 v244, v222, v223, v244\n\t" \
    "v_cmp_eq_u32 vcc, -1, v244\n\t" \
    "s_cbranch_vccnz .Lm27_%=\n\t" \
    "buffer_load_dwordx4 v[216:219], %[l16], %[rs], s49 offen offset:2048 sc1\n\t" \
    "buffer_load_dwordx4 v[212:215], %[l16], %[rs], s49 offen offset:1024 sc1\n\t" \
    "buffer_load_dwordx4 v[208:211], %[l16], %[rs], s49 offen offset:0 sc1\n\t" \
    "buffer_load_dwordx4 v[204:207], %[l16], %[rs], s48 offen offset:3072 sc1\n\t" \
    "buffer_load_dwordx4 v[200:203], %[l16], %[rs], s48 offen offset:2048 sc1\n\t" \
    "buffer_load_dwordx4 v[196:199], %[l16], %[rs], s48 offen offset:1024 sc1\n\t" \
    "buffer_load_dwordx4 v[192:195], %[l16], %[rs], s48 offen offset:0 sc1\n\t" \
    "s_mov_b32 s43, 1\n\t" \
    ".Lm27_%=:\n\t" \
    ".Lm28_%=:\n\t" \
    "ds_read_b32 v245, v246 offset:28\n\t" \
    "s_sub_i32 s44, s40, 3\n\t" \
    "s_waitcnt lgkmcnt(0)\n\t" \
    "v_cmp_gt_i32 vcc, s44, v245\n\t" \
    "s_cbranch_vccnz .Lm28_%=\n\t" \
    "s_add_u32 s44, s40, 1\n\t" \
    "s_min_u32 s44, s44, 127\n\t" \
    "s_lshr_b32 s45, s44, 1\n\t" \
    "s_and_b32 s44, s44, 1\n\t" \
    "s_lshl_b32 s44, s44, 12\n\t" \
    "s_mul_i32 s46, s45, 0x30000\n\t" \
    "s_add_u32 s46, s46, s44\n\t" \
    "s_lshr_b32 s45, s45, 3\n\t" \
    "s_mul_i32 s45, s45, 0x82000\n\t" \
    "s_add_u32 s46, s46, s45\n\t" \
    "s_add_u32 s46, s46, %[hb]\n\t" \
    "s_add_u32 s47, s46, 0x4000\n\t" \
    "s_cmp_lg_u32 s43, 0\n\t" \
    "s_cbranch_scc1 .Lm31_%=\n\t" \
    ".Lm29_%=:\n\t" \
    "v_max_u32 v244, v220, v221\n\t" \
    "v_max3_u32 v244, v222, v223, v244\n\t" \
    "v_cmp_eq_u32 vcc, -1, v244\n\t" \
    "s_cbranch_vccz .Lm30_%=\n\t" \
    "s_cmp_eq_u32 s41, 0\n\t" \
    "s_cbranch_scc1 .Lm30_%=\n\t" \
    "s_sub_u32 s41, s41, 1\n\t" \
    "s_sleep 3\n\t" \
    "buffer_load_dwordx4 v[220:223], %[l16], %[rs], s49 offen offset:3072 sc1\n\t" \
    "s_waitcnt vmcnt(0)\n\t" \
    "s_branch .Lm29_%=\n\t" \
    ".Lm30_%=:\n\t" \
    "buffer_load_dwordx4 v[216:219], %[l16], %[rs], s49 offen offset:2048 sc1\n\t" \
    "buffer_load_dwordx4 v[212:215], %[l16], %[rs], s49 offen offset:1024 sc1\n\t" \
    "buffer_load_dwordx4 v[208:211], %[l16], %[rs], s49 offen offset:0 sc1\n\t" \
    "buffer_load_dwordx4 v[204:207], %[l16], %[rs], s48 offen offset:3072 sc1\n\t" \
    "buffer_load_dwordx4 v[200:203], %[l16], %[rs], s48 offen offset:2048 sc1\n\t" \
    "buffer_load_dwordx4 v[196:199], %[l16], %[rs], s48 offen offset:1024 sc1\n\t" \
    "buffer_load_dwordx4 v[192:195], %[l16], %[rs], s48 offen offset:0 sc1\n\t" \
    ".Lm31_%=:\n\t" \
    "v_mfma_f32_16x16x32_bf16 v[224:227], v[28:31], v[220:223], 0\n\t" \
    "v_mfma_f32_16x16x32_bf16 v[228:231], v[60:63], v[220:223], 0\n\t" \
    "v_mfma_f32_16x16x32_bf16 v[232:235], v[92:95], v[220:223], 0\n\t" \
    "v_mfma_f32_16x16x32_bf16 v[236:239], v[124:127], v[220:223], 0\n\t" \
    "v_mfma_f32_16x16x32_bf16 v[240:243], v[156:159], v[220:223], 0\n\t" \
    "s_waitcnt vmcnt(6)\n\t" \
    ".Lm32_%=:\n\t" \
    "v_max_u32 v244, v216, v217\n\t" \
    "v_max3_u32 v244, v218, v219, v244\n\t" \
    "v_cmp_eq_u32 vcc, -1, v244\n\t" \
    "s_cbranch_vccnz .Lm33_%=\n\t" \
    ".Lm34_%=:\n\t" \
    "v_mfma_f32_16x16x32_bf16 v[224:227], v[24:27], v[216:219], v[224:227]\n\t" \
    "v_mfma_f32_16x16x32_bf16 v[228:231], v[56:59], v[216:219], v[228:231]\n\t" \
    "v_mfma_f32_16x16x32_bf16 v[232:235], v[88:91], v[216:219], v[232:235]\n\t" \
    "v_mfma_f32_16x16x32_bf16 v[236:239], v[120:123], v[216:219], v[236:239]\n\t" \
    "v_mfma_f32_16x16x32_bf16 v[240:243], v[152:155], v[216:219], v[240:243]\n\t" \
    "s_waitcnt vmcnt(5)\n\t" \
    ".Lm35_%=:\n\t" \
    "v_max_u32 v244, v212, v213\n\t" \
    "v_max3_u32 v244, v214, v215, v244\n\t" \
    "v_cmp_eq_u32 vcc, -1, v244\n\t" \
    "s_cbranch_vccnz .Lm36_%=\n\t" \
    ".Lm37_%=:\n\t" \
    "v_mfma_f32_16x16x32_bf16 v[224:227], v[20:23], v[212:215], v[224:227]\n\t" \
    "v_mfma_f32_16x16x32_bf16 v[228:231], v[52:55], v[212:215], v[228:231]\n\t" \
    "v_mfma_f32_16x16x32_bf16 v[232:235], v[84:87], v[212:215], v[232:235]\n\t" \
    "v_mfma_f32_16x16x32_bf16 v[236:239], v[116:119], v[212:215], v[236:239]\n\t" \
    "v_mfma_f32_16x16x32_bf16 v[240:243], v[148:151], v[212:215], v[240:243]\n\t" \
    "s_waitcnt vmcnt(4)\n\t" \
    ".Lm38_%=:\n\t" \
    "v_max_u32 v244, v208, v209\n\t" \
    "v_max3_u32 v244, v210, v211, v244\n\t" \
    "v_cmp_eq_u32 vcc, -1, v244\n\t" \
    "s_cbranch_vccnz .Lm39_%=\n\t" \
    ".Lm40_%=:\n\t" \
    "v_mfma_f32_16x16x32_bf16 v[224:227], v[16:19], v[208:211], v[224:227]\n\t" \
    "v_mfma_f32_16x16x32_bf16 v[228:231], v[48:51], v[208:211], v[228:231]\n\t" \
    "v_mfma_f32_16x16x32_bf16 v[232:235], v[80:83], v[208:211], v[232:235]\n\t" \
    "v_mfma_f32_16x16x32_bf16 v[236:239], v[112:115], v[208:211], v[236:239]\n\t" \
    "v_mfma_f32_16x16x32_bf16 v[240:243], v[144:147], v[208:211], v[240:243]\n\t" \
    "s_waitcnt vmcnt(3)\n\t" \
    ".Lm41_%=:\n\t" \
    "v_max_u32 v244, v204, v205\n\t" \
    "v_max3_u32 v244, v206, v207, v244\n\t" \
    "v_cmp_eq_u32 vcc, -1, v244\n\t" \
    "s_cbranch_vccnz .Lm42_%=\n\t" \
    ".Lm43_%=:\n\t" \
    "v_mfma_f32_16x16x32_bf16 v[224:227], v[12:15], v[204:207], v[224:227]\n\t" \
    "v_mfma_f32_16x16x32_bf16 v[228:231], v[44:47], v[204:207], v[228:231]\n\t" \
    "v_mfma_f32_16x16x32_bf16 v[232:235], v[76:79], v[204:207], v[232:235]\n\t" \
    "v_mfma_f32_16x16x32_bf16 v[236:239], v[108:111], v[204:207], v[236:239]\n\t" \
    "v_mfma_f32_16x16x32_bf16 v[240:243], v[140:143], v[204:207], v[240:243]\n\t" \
    "s_waitcnt vmcnt(2)\n\t" \
    ".Lm44_%=:\n\t" \
    "v_max_u32 v244, v200, v201\n\t" \
    "v_max3_u32 v244, v202, v203, v244\n\t" \
    "v_cmp_eq_u32 vcc, -1, v244\n\t" \
    "s_cbranch_vccnz .Lm45_%=\n\t" \
    ".Lm46_%=:\n\t" \
    "buffer_load_dwordx4 v[188:191], %[l16], %[rs], s47 offen offset:3072 sc1\n\t" \
    "v_mfma_f32_16x16x32_bf16 v[224:227], v[8:11], v[200:203], v[224:227]\n\t" \
    "v_mfma_f32_16x16x32_bf16 v[228:231], v[40:43], v[200:203], v[228:231]\n\t" \
    "v_mfma_f32_16x16x32_bf16 v[232:235], v[72:75], v[200:203], v[232:235]\n\t" \
    "v_mfma_f32_16x16x32_bf16 v[236:239], v[104:107], v[200:203], v[236:239]\n\t" \
    "v_mfma_f32_16x16x32_bf16 v[240:243], v[136:139], v[200:203], v[240:243]\n\t" \
    "s_waitcnt vmcnt(2)\n\t" \
    ".Lm47_%=:\n\t" \
    "v_max_u32 v244, v196, v197\n\t" \
    "v_max3_u32 v244, v198, v199, v244\n\t" \
    "v_cmp_eq_u32 vcc, -1, v244\n\t" \
    "s_cbranch_vccnz .Lm48_%=\n\t" \
    ".Lm49_%=:\n\t" \
    "v_mfma_f32_16x16x32_bf16 v[224:227], v[4:7], v[196:199], v[224:227]\n\t" \
    "v_mfma_f32_16x16x32_bf16 v[228:231], v[36:39], v[196:199], v[228:231]\n\t" \
    "v_mfma_f32_16x16x32_bf16 v[232:235], v[68:71], v[196:199], v[232:235]\n\t" \
    "v_mfma_f32_16x16x32_bf16 v[236:239], v[100:103], v[196:199], v[236:239]\n\t" \
    "v_mfma_f32_16x16x32_bf16 v[240:243], v[132:135], v[196:199], v[240:243]\n\t" \
    "s_waitcnt vmcnt(1)\n\t" \
    ".Lm50_%=:\n\t" \
    "v_max_u32 v244, v192, v193\n\t" \
    "v_max3_u32 v244, v194, v195, v244\n\t" \
    "v_cmp_eq_u32 vcc, -1, v244\n\t" \
    "s_cbranch_vccnz .Lm51_%=\n\t" \
    ".Lm52_%=:\n\t" \
    "v_mfma_f32_16x16x32_bf16 v[224:227], v[0:3], v[192:195], v[224:227]\n\t" \
    "v_mfma_f32_16x16x32_bf16 v[228:231], v[32:35], v[192:195], v[228:231]\n\t" \
    "v_mfma_f32_16x16x32_bf16 v[232:235], v[64:67], v[192:195], v[232:235]\n\t" \
    "v_mfma_f32_16x16x32_bf16 v[236:239], v[96:99], v[192:195], v[236:239]\n\t" \
    "v_mfma_f32_16x16x32_bf16 v[240:243], v[128:131], v[192:195], v[240:243]\n\t" \
    "s_add_u32 s40, s40, 1\n\t" \
    "v_mov_b32 v245, s40\n\t" \
    "ds_write_b128 %[ld], v[224:227] offset:30720\n\t" \
    "ds_write_b128 %[ld], v[228:231] offset:36864\n\t" \
    "ds_write_b128 %[ld], v[232:235] offset:43008\n\t" \
    "s_nop 0\n\t" \
    "ds_write_b128 %[ld], v[236:239] offset:49152\n\t" \
    "s_nop 1\n\t" \
    "ds_write_b128 %[ld], v[240:243] offset:55296\n\t" \
    "ds_write_b32 %[fa], v245\n\t" \
    "s_mov_b32 s43, 0\n\t" \
    "s_cmp_eq_u32 s40, 128\n\t" \
    "s_cbranch_scc1 .Lm53_%=\n\t" \
    "s_waitcnt vmcnt(0)\n\t" \
    "v_max_u32 v244, v188, v189\n\t" \
    "v_max3_u32 v244, v190, v191, v244\n\t" \
    "v_cmp_eq_u32 vcc, -1, v244\n\t" \
    "s_cbranch_vccnz .Lm53_%=\n\t" \
    "buffer_load_dwordx4 v[184:187], %[l16], %[rs], s47 offen offset:2048 sc1\n\t" \
    "buffer_load_dwordx4 v[180:183], %[l16], %[rs], s47 offen offset:1024 sc1\n\t" \
    "buffer_load_dwordx4 v[176:179], %[l16], %[rs], s47 offen offset:0 sc1\n\t" \
    "buffer_load_dwordx4 v[172:175], %[l16], %[rs], s46 offen offset:3072 sc1\n\t" \
    "buffer_load_dwordx4 v[168:171], %[l16], %[rs], s46 offen offset:2048 sc1\n\t" \
    "buffer_load_dwordx4 v[164:167], %[l16], %[rs], s46 offen offset:1024 sc1\n\t" \
    "buffer_load_dwordx4 v[160:163], %[l16], %[rs], s46 offen offset:0 sc1\n\t" \
    "s_mov_b32 s43, 1\n\t" \
    ".Lm53_%=:\n\t" \
    "v_sub_u32 %[ld], %[l2], %[ld]\n\t" \
    "s_nop 0\n\t" \
    "s_cmp_lg_u32 s40, 128\n\t" \
    "s_cbranch_scc1 .Lm1_%=\n\t" \
    "s_branch .Lend%=\n\t" \
    ".Lm7_%=:\n\t" \
    "s_cmp_eq_u32 s41, 0\n\t" \
    "s_cbranch_scc1 .Lm8_%=\n\t" \
    "s_sub_u32 s41, s41, 1\n\t" \
    "buffer_load_dwordx4 v[184:187], %[l16], %[rs], s47 offen offset:2048 sc1\n\t" \
    "s_waitcnt vmcnt(0)\n\t" \
    "s_branch .Lm6_%=\n\t" \
    ".Lm10_%=:\n\t" \
    "s_cmp_eq_u32 s41, 0\n\t" \
    "s_cbranch_scc1 .Lm11_%=\n\t" \
    "s_sub_u32 s41, s41, 1\n\t" \
    "buffer_load_dwordx4 v[180:183], %[l16], %[rs], s47 offen offset:1024 sc1\n\t" \
    "s_waitcnt vmcnt(0)\n\t" \
    "s_branch .Lm9_%=\n\t" \
    ".Lm13_%=:\n\t" \
    "s_cmp_eq_u32 s41, 0\n\t" \
    "s_cbranch_scc1 .Lm14_%=\n\t" \
    "s_sub_u32 s41, s41, 1\n\t" \
    "buffer_load_dwordx4 v[176:179], %[l16], %[rs], s47 offen offset:0 sc1\n\t" \
    "s_waitcnt vmcnt(0)\n\t" \
    "s_branch .Lm12_%=\n\t" \
    ".Lm16_%=:\n\t" \
    "s_cmp_eq_u32 s41, 0\n\t" \
    "s_cbranch_scc1 .Lm17_%=\n\t" \
    "s_sub_u32 s41, s41, 1\n\t" \
    "buffer_load_dwordx4 v[172:175], %[l16], %[rs], s46 offen offset:3072 sc1\n\t" \
    "s_waitcnt vmcnt(0)\n\t" \
    "s_branch .Lm15_%=\n\t" \
    ".Lm19_%=:\n\t" \
    "s_cmp_eq_u32 s41, 0\n\t" \
    "s_cbranch_scc1 .Lm20_%=\n\t" \
    "s_sub_u32 s41, s41, 1\n\t" \
    "buffer_load_dwordx4 v[168:171], %[l16], %[rs], s46 offen offset:2048 sc1\n\t" \
    "s_waitcnt vmcnt(0)\n\t" \
    "s_branch .Lm18_%=\n\t" \
    ".Lm22_%=:\n\t" \
    "s_cmp_eq_u32 s41, 0\n\t" \
    "s_cbranch_scc1 .Lm23_%=\n\t" \
    "s_sub_u32 s41, s41, 1\n\t" \
    "buffer_load_dwordx4 v[164:167], %[l16], %[rs], s46 offen offset:1024 sc1\n\t" \
    "s_waitcnt vmcnt(0)\n\t" \
    "s_branch .Lm21_%=\n\t" \
    ".Lm25_%=:\n\t" \
    "s_cmp_eq_u32 s41, 0\n\t" \
    "s_cbranch_scc1 .Lm26_%=\n\t" \
    "s_sub_u32 s41, s41, 1\n\t" \
    "buffer_load_dwordx4 v[160:163], %[l16], %[rs], s46 offen offset:0 sc1\n\t" \
    "s_waitcnt vmcnt(0)\n\t" \
    "s_branch .Lm24_%=\n\t" \
    ".Lm33_%=:\n\t" \
    "s_cmp_eq_u32 s41, 0\n\t" \
    "s_cbranch_scc1 .Lm34_%=\n\t" \
    "s_sub_u32 s41, s41, 1\n\t" \
    "buffer_load_dwordx4 v[216:219], %[l16], %[rs], s49 offen offset:2048 sc1\n\t" \
    "s_waitcnt vmcnt(0)\n\t" \
    "s_branch .Lm32_%=\n\t" \
    ".Lm36_%=:\n\t" \
    "s_cmp_eq_u32 s41, 0\n\t" \
    "s_cbranch_scc1 .Lm37_%=\n\t" \
    "s_sub_u32 s41, s41, 1\n\t" \
    "buffer_load_dwordx4 v[212:215], %[l16], %[rs], s49 offen offset:1024 sc1\n\t" \
    "s_waitcnt vmcnt(0)\n\t" \
    "s_branch .Lm35_%=\n\t" \
    ".Lm39_%=:\n\t" \
    "s_cmp_eq_u32 s41, 0\n\t" \
    "s_cbranch_scc1 .Lm40_%=\n\t" \
    "s_sub_u32 s41, s41, 1\n\t" \
    "buffer_load_dwordx4 v[208:211], %[l16], %[rs], s49 offen offset:0 sc1\n\t" \
    "s_waitcnt vmcnt(0)\n\t" \
    "s_branch .Lm38_%=\n\t" \
    ".Lm42_%=:\n\t" \
    "s_cmp_eq_u32 s41, 0\n\t" \
    "s_cbranch_scc1 .Lm43_%=\n\t" \
    "s_sub_u32 s41, s41, 1\n\t" \
    "buffer_load_dwordx4 v[204:207], %[l16], %[rs], s48 offen offset:3072 sc1\n\t" \
    "s_waitcnt vmcnt(0)\n\t" \
    "s_branch .Lm41_%=\n\t" \
    ".Lm45_%=:\n\t" \
    "s_cmp_eq_u32 s41, 0\n\t" \
    "s_cbranch_scc1 .Lm46_%=\n\t" \
    "s_sub_u32 s41, s41, 1\n\t" \
    "buffer_load_dwordx4 v[200:203], %[l16], %[rs], s48 offen offset:2048 sc1\n\t" \
    "s_waitcnt vmcnt(0)\n\t" \
    "s_branch .Lm44_%=\n\t" \
    ".Lm48_%=:\n\t" \
    "s_cmp_eq_u32 s41, 0\n\t" \
    "s_cbranch_scc1 .Lm49_%=\n\t" \
    "s_sub_u32 s41, s41, 1\n\t" \
    "buffer_load_dwordx4 v[196:199], %[l16], %[rs], s48 offen offset:1024 sc1\n\t" \
    "s_waitcnt vmcnt(0)\n\t" \
    "s_branch .Lm47_%=\n\t" \
    ".Lm51_%=:\n\t" \
    "s_cmp_eq_u32 s41, 0\n\t" \
    "s_cbranch_scc1 .Lm52_%=\n\t" \
    "s_sub_u32 s41, s41, 1\n\t" \
    "buffer_load_dwordx4 v[192:195], %[l16], %[rs], s48 offen offset:0 sc1\n\t" \
    "s_waitcnt vmcnt(0)\n\t" \
    "s_branch .Lm50_%=\n\t" \
    ".Lend%=:\n\t" \
    "s_waitcnt vmcnt(0) lgkmcnt(0)\n\t" \
    "s_mov_b32 %[bud], s41\n\t"
#define ASM_W4_FIN0 \
    "s_mov_b32 s40, %[mt]\n\t" \
    "s_mov_b32 s41, 0x40000\n\t" \
    "s_mov_b32 s43, 0\n\t" \
    "v_mov_b32 v99, 0x3f803f80\n\t" \
    "s_setprio 3\n\t" \
    "s_add_u32 s46, s43, 0x2c00000\n\t" \
    "s_add_u32 s47, s43, 0x1c00000\n\t" \
    "buffer_load_dwordx2 v[120:121], %[t0], %[rs], s46 offen sc1\n\t" \
    "buffer_load_dwordx2 v[122:123], %[t0], %[rs], s47 offen sc1\n\t" \
    "buffer_load_dwordx2 v[124:125], %[t1], %[rs], s46 offen sc1\n\t" \
    "buffer_load_dwordx2 v[126:127], %[t1], %[rs], s47 offen sc1\n\t" \
    "buffer_load_dwordx2 v[128:129], %[t2], %[rs], s46 offen sc1\n\t" \
    "buffer_load_dwordx2 v[130:131], %[t2], %[rs], s47 offen sc1\n\t" \
    "buffer_load_dwordx2 v[132:133], %[t3], %[rs], s46 offen sc1\n\t" \
    "buffer_load_dwordx2 v[134:135], %[t3], %[rs], s47 offen sc1\n\t" \
    "buffer_load_dwordx2 v[136:137], %[t4], %[rs], s46 offen sc1\n\t" \
    "buffer_load_dwordx2 v[138:139], %[t4], %[rs], s47 offen sc1\n\t" \
    "s_waitcnt vmcnt(0)\n\t" \
    ".Lf01_%=:\n\t" \
    ".Lf02_%=:\n\t" \
    "s_waitcnt vmcnt(1)\n\t" \
    "v_max_u32 v80, v120, v121\n\t" \
    "v_max3_u32 v80, v122, v123, v80\n\t" \
    "v_max3_u32 v80, v124, v125, v80\n\t" \
    "v_max3_u32 v80, v126, v127, v80\n\t" \
    "v_max3_u32 v80, v128, v129, v80\n\t" \
    "v_max3_u32 v80, v130, v131, v80\n\t" \
    "v_max3_u32 v80, v132, v133, v80\n\t" \
    "v_max3_u32 v80, v134, v135, v80\n\t" \
    "v_max3_u32 v80, v136, v137, v80\n\t" \
    "v_max3_u32 v80, v138, v139, v80\n\t" \
    "v_cmp_eq_u32 vcc, -1, v80\n\t" \
    "s_cbranch_vccz .Lf03_%=\n\t" \
    "s_cmp_eq_u32 s41, 0\n\t" \
    "s_cbranch_scc1 .Lf03_%=\n\t" \
    "s_sub_u32 s41, s41, 1\n\t" \
    "s_sleep 8\n\t" \
    "s_add_u32 s46, s43, 0x2c00000\n\t" \
    "s_add_u32 s47, s43, 0x1c00000\n\t" \
    "buffer_load_dwordx2 v[120:121], %[t0], %[rs], s46 offen sc1\n\t" \
    "buffer_load_dwordx2 v[122:123], %[t0], %[rs], s47 offen sc1\n\t" \
    "buffer_load_dwordx2 v[124:125], %[t1], %[rs], s46 offen sc1\n\t" \
    "buffer_load_dwordx2 v[126:127], %[t1], %[rs], s47 offen sc1\n\t" \
    "buffer_load_dwordx2 v[128:129], %[t2], %[rs], s46 offen sc1\n\t" \
    "buffer_load_dwordx2 v[130:131], %[t2], %[rs], s47 offen sc1\n\t" \
    "buffer_load_dwordx2 v[132:133], %[t3], %[rs], s46 offen sc1\n\t" \
    "buffer_load_dwordx2 v[134:135], %[t3], %[rs], s47 offen sc1\n\t" \
    "buffer_load_dwordx2 v[136:137], %[t4], %[rs], s46 offen sc1\n\t" \
    "buffer_load_dwordx2 v[138:139], %[t4], %[rs], s47 offen sc1\n\t" \
    "s_waitcnt vmcnt(0)\n\t" \
    "s_branch .Lf02_%=\n\t" \
    ".Lf03_%=:\n\t" \
    "v_lshlrev_b32 v140, 16, v120\n\t" \
    "v_and_b32 v141, 0xffff0000, v120\n\t" \
    "v_lshlrev_b32 v142, 16, v121\n\t" \
    "v_and_b32 v143, 0xffff0000, v121\n\t" \
    "v_lshlrev_b32 v80, 16, v122\n\t" \
    "v_and_b32 v81, 0xffff0000, v122\n\t" \
    "v_lshlrev_b32 v82, 16, v123\n\t" \
    "v_and_b32 v83, 0xffff0000, v123\n\t" \
    "v_add_f32 v140, v140, v80\n\t" \
    "v_add_f32 v141, v141, v81\n\t" \
    "v_add_f32 v142, v142, v82\n\t" \
    "v_add_f32 v143, v143, v83\n\t" \
    "v_lshlrev_b32 v144, 16, v124\n\t" \
    "v_and_b32 v145, 0xffff0000, v124\n\t" \
    "v_lshlrev_b32 v146, 16, v125\n\t" \
    "v_and_b32 v147, 0xffff0000, v125\n\t" \
    "v_lshlrev_b32 v80, 16, v126\n\t" \
    "v_and_b32 v81, 0xffff0000, v126\n\t" \
    "v_lshlrev_b32 v82, 16, v127\n\t" \
    "v_and_b32 v83, 0xffff0000, v127\n\t" \
    "v_add_f32 v144, v144, v80\n\t" \
    "v_add_f32 v145, v145, v81\n\t" \
    "v_add_f32 v146, v146, v82\n\t" \
    "v_add_f32 v147, v147, v83\n\t" \
    "v_lshlrev_b32 v148, 16, v128\n\t" \
    "v_and_b32 v149, 0xffff0000, v128\n\t" \
    "v_lshlrev_b32 v150, 16, v129\n\t" \
    "v_and_b32 v151, 0xffff0000, v129\n\t" \
    "v_lshlrev_b32 v80, 16, v130\n\t" \
    "v_and_b32 v81, 0xffff0000, v130\n\t" \
    "v_lshlrev_b32 v82, 16, v131\n\t" \
    "v_and_b32 v83, 0xffff0000, v131\n\t" \
    "v_add_f32 v148, v148, v80\n\t" \
    "v_add_f32 v149, v149, v81\n\t" \
    "v_add_f32 v150, v150, v82\n\t" \
    "v_add_f32 v151, v151, v83\n\t" \
    "v_lshlrev_b32 v152, 16, v132\n\t" \
    "v_and_b32 v153, 0xffff0000, v132\n\t" \
    "v_lshlrev_b32 v154, 16, v133\n\t" \
    "v_and_b32 v155, 0xffff0000, v133\n\t" \
    "v_lshlrev_b32 v80, 16, v134\n\t" \
    "v_and_b32 v81, 0xffff0000, v134\n\t" \
    "v_lshlrev_b32 v82, 16, v135\n\t" \
    "v_and_b32 v83, 0xffff0000, v135\n\t" \
    "v_add_f32 v152, v152, v80\n\t" \
    "v_add_f32 v153, v153, v81\n\t" \
    "v_add_f32 v154, v154, v82\n\t" \
    "v_add_f32 v155, v155, v83\n\t" \
    "v_lshlrev_b32 v156, 16, v136\n\t" \
    "v_and_b32 v157, 0xffff0000, v136\n\t" \
    "v_lshlrev_b32 v158, 16, v137\n\t" \
    "v_and_b32 v159, 0xffff0000, v137\n\t" \
    "v_lshlrev_b32 v80, 16, v138\n\t" \
    "v_and_b32 v81, 0xffff0000, v138\n\t" \
    "v_lshlrev_b32 v82, 16, v139\n\t" \
    "v_and_b32 v83, 0xffff0000, v139\n\t" \
    "v_add_f32 v156, v156, v80\n\t" \
    "v_add_f32 v157, v157, v81\n\t" \
    "v_add_f32 v158, v158, v82\n\t" \
    "v_add_f32 v159, v159, v83\n\t" \
    "s_mov_b32 s42, s43\n\t" \
    "s_lshr_b32 s48, s40, 1\n\t" \
    "s_add_u32 s48, s48, 1\n\t" \
    "s_mul_i32 s43, s48, 0x14000\n\t" \
    "s_lshr_b32 s44, s48, 4\n\t" \
    "s_mul_i32 s44, s44, 0xc2000\n\t" \
    "s_add_u32 s43, s43, s44\n\t" \
    "s_lshr_b32 s44, s40, 3\n\t" \
    "s_lshl_b32 s44, s44, 14\n\t" \
    "s_and_b32 s45, s40, 7\n\t" \
    "s_lshl_b32 s45, s45, 10\n\t" \
    "s_add_u32 s49, s44, s45\n\t" \
    "s_add_u32 s49, s49, 0x1c0000\n\t" \
    "s_add_u32 s50, s49, %[co]\n\t" \
    "s_add_u32 s51, s42, 0x1400000\n\t" \
    "s_add_u32 s52, s42, 0xc00000\n\t" \
    "s_mov_b32 s44, s48\n\t" \
    "s_mov_b32 s45, s48\n\t" \
    "v_mov_b64 v[76:77], s[44:45]\n\t" \
    "v_mov_b64 v[78:79], s[44:45]\n\t" \
    "s_add_u32 s44, s40, 1\n\t" \
    "v_mov_b32 v98, s44\n\t" \
    ".Lf04_%=:\n\t" \
    "s_nop 7\n\t" \
    "ds_read_b32 v80, %[fa]\n\t" \
    "s_waitcnt lgkmcnt(0)\n\t" \
    "v_cmp_ge_u32 vcc, s40, v80\n\t" \
    "s_cbranch_vccnz .Lf04_%=\n\t" \
    "ds_read_b128 v[160:163], %[la] offset:0\n\t" \
    "ds_read_b128 v[164:167], %[la] offset:6144\n\t" \
    "ds_read_b128 v[168:171], %[la] offset:12288\n\t" \
    "ds_read_b128 v[172:175], %[la] offset:18432\n\t" \
    "ds_read_b128 v[176:179], %[la] offset:24576\n\t" \
    "ds_read_b128 v[180:183], %[la] offset:1024\n\t" \
    "ds_read_b128 v[184:187], %[la] offset:7168\n\t" \
    "ds_read_b128 v[188:191], %[la] offset:13312\n\t" \
    "ds_read_b128 v[192:195], %[la] offset:19456\n\t" \
    "ds_read_b128 v[196:199], %[la] offset:25600\n\t" \
    "ds_read_b128 v[200:203], %[la] offset:2048\n\t" \
    "ds_read_b128 v[204:207], %[la] offset:8192\n\t" \
    "ds_read_b128 v[208:211], %[la] offset:14336\n\t" \
    "ds_read_b128 v[212:215], %[la] offset:20480\n\t" \
    "ds_read_b128 v[216:219], %[la] offset:26624\n\t" \
    "s_waitcnt lgkmcnt(14)\n\t" \
    "v_pk_add_f32 v[100:101], v[140:141], v[160:161]\n\t" \
    "v_pk_add_f32 v[102:103], v[142:143], v[162:163]\n\t" \
    "ds_read_b128 v[220:223], %[la] offset:3072\n\t" \
    "s_waitcnt lgkmcnt(14)\n\t" \
    "v_pk_add_f32 v[104:105], v[144:145], v[164:165]\n\t" \
    "v_pk_add_f32 v[106:107], v[146:147], v[166:167]\n\t" \
    "ds_read_b128 v[224:227], %[la] offset:9216\n\t" \
    "s_waitcnt lgkmcnt(14)\n\t" \
    "v_pk_add_f32 v[108:109], v[148:149], v[168:169]\n\t" \
    "v_pk_add_f32 v[110:111], v[150:151], v[170:171]\n\t" \
    "ds_read_b128 v[228:231], %[la] offset:15360\n\t" \
    "s_waitcnt lgkmcnt(14)\n\t" \
    "v_pk_add_f32 v[112:113], v[152:153], v[172:173]\n\t" \
    "v_pk_add_f32 v[114:115], v[154:155], v[174:175]\n\t" \
    "ds_read_b128 v[232:235], %[la] offset:21504\n\t" \
    "s_waitcnt lgkmcnt(14)\n\t" \
    "v_pk_add_f32 v[116:117], v[156:157], v[176:177]\n\t" \
    "v_pk_add_f32 v[118:119], v[158:159], v[178:179]\n\t" \
    "ds_read_b128 v[236:239], %[la] offset:27648\n\t" \
    "s_waitcnt lgkmcnt(14)\n\t" \
    "v_pk_add_f32 v[100:101], v[100:101], v[180:181]\n\t" \
    "v_pk_add_f32 v[102:103], v[102:103], v[182:183]\n\t" \
    "ds_read_b128 v[160:163], %[la] offset:4096\n\t" \
    "s_waitcnt lgkmcnt(14)\n\t" \
    "v_pk_add_f32 v[104:105], v[104:105], v[184:185]\n\t" \
    "v_pk_add_f32 v[106:107], v[106:107], v[186:187]\n\t" \
    "ds_read_b128 v[164:167], %[la] offset:10240\n\t" \
    "s_waitcnt lgkmcnt(14)\n\t" \
    "v_pk_add_f32 v[108:109], v[108:109], v[188:189]\n\t" \
    "v_pk_add_f32 v[110:111], v[110:111], v[190:191]\n\t" \
    "ds_read_b128 v[168:171], %[la] offset:16384\n\t" \
    "s_waitcnt lgkmcnt(14)\n\t" \
    "v_pk_add_f32 v[112:113], v[112:113], v[192:193]\n\t" \
    "v_pk_add_f32 v[114:115], v[114:115], v[194:195]\n\t" \
    "ds_read_b128 v[172:175], %[la] offset:22528\n\t" \
    "s_waitcnt lgkmcnt(14)\n\t" \
    "v_pk_add_f32 v[116:117], v[116:117], v[196:197]\n\t" \
    "v_pk_add_f32 v[118:119], v[118:119], v[198:199]\n\t" \
    "ds_read_b128 v[176:179], %[la] offset:28672\n\t" \
    "s_waitcnt lgkmcnt(14)\n\t" \
    "v_pk_add_f32 v[100:101], v[100:101], v[200:201]\n\t" \
    "v_pk_add_f32 v[102:103], v[102:103], v[202:203]\n\t" \
    "ds_read_b128 v[180:183], %[la] offset:5120\n\t" \
    "s_waitcnt lgkmcnt(14)\n\t" \
    "v_pk_add_f32 v[104:105], v[104:105], v[204:205]\n\t" \
    "v_pk_add_f32 v[106:107], v[106:107], v[206:207]\n\t" \
    "ds_read_b128 v[184:187], %[la] offset:11264\n\t" \
    "s_waitcnt lgkmcnt(14)\n\t" \
    "v_pk_add_f32 v[108:109], v[108:109], v[208:209]\n\t" \
    "v_pk_add_f32 v[110:111], v[110:111], v[210:211]\n\t" \
    "ds_read_b128 v[188:191], %[la] offset:17408\n\t" \
    "s_waitcnt lgkmcnt(14)\n\t" \
    "v_pk_add_f32 v[112:113], v[112:113], v[212:213]\n\t" \
    "v_pk_add_f32 v[114:115], v[114:115], v[214:215]\n\t" \
    "ds_read_b128 v[192:195], %[la] offset:23552\n\t" \
    "s_waitcnt lgkmcnt(14)\n\t" \
    "v_pk_add_f32 v[116:117], v[116:117], v[216:217]\n\t" \
    "v_pk_add_f32 v[118:119], v[118:119], v[218:219]\n\t" \
    "ds_read_b128 v[196:199], %[la] offset:29696\n\t" \
    "s_waitcnt lgkmcnt(14)\n\t" \
    "v_pk_add_f32 v[100:101], v[100:101], v[220:221]\n\t" \
    "v_pk_add_f32 v[102:103], v[102:103], v[222:223]\n\t" \
    "s_waitcnt lgkmcnt(13)\n\t" \
    "v_pk_add_f32 v[104:105], v[104:105], v[224:225]\n\t" \
    "v_pk_add_f32 v[106:107], v[106:107], v[226:227]\n\t" \
    "s_waitcnt lgkmcnt(12)\n\t" \
    "v_pk_add_f32 v[108:109], v[108:109], v[228:229]\n\t" \
    "v_pk_add_f32 v[110:111], v[110:111], v[230:231]\n\t" \
    "s_waitcnt lgkmcnt(11)\n\t" \
    "v_pk_add_f32 v[112:113], v[112:113], v[232:233]\n\t" \
    "v_pk_add_f32 v[114:115], v[114:115], v[234:235]\n\t" \
    "s_waitcnt lgkmcnt(10)\n\t" \
    "v_pk_add_f32 v[116:117], v[116:117], v[236:237]\n\t" \
    "v_pk_add_f32 v[118:119], v[118:119], v[238:239]\n\t" \
    "s_waitcnt lgkmcnt(9)\n\t" \
    "v_pk_add_f32 v[100:101], v[100:101], v[160:161]\n\t" \
    "v_pk_add_f32 v[102:103], v[102:103], v[162:163]\n\t" \
    "s_waitcnt lgkmcnt(8)\n\t" \
    "v_pk_add_f32 v[104:105], v[104:105], v[164:165]\n\t" \
    "v_pk_add_f32 v[106:107], v[106:107], v[166:167]\n\t" \
    "s_waitcnt lgkmcnt(7)\n\t" \
    "v_pk_add_f32 v[108:109], v[108:109], v[168:169]\n\t" \
    "v_pk_add_f32 v[110:111], v[110:111], v[170:171]\n\t" \
    "s_waitcnt lgkmcnt(6)\n\t" \
    "v_pk_add_f32 v[112:113], v[112:113], v[172:173]\n\t" \
    "v_pk_add_f32 v[114:115], v[114:115], v[174:175]\n\t" \
    "s_waitcnt lgkmcnt(5)\n\t" \
    "v_pk_add_f32 v[116:117], v[116:117], v[176:177]\n\t" \
    "v_pk_add_f32 v[118:119], v[118:119], v[178:179]\n\t" \
    "s_waitcnt lgkmcnt(4)\n\t" \
    "v_pk_add_f32 v[100:101], v[100:101], v[180:181]\n\t" \
    "v_pk_add_f32 v[102:103], v[102:103], v[182:183]\n\t" \
    "s_waitcnt lgkmcnt(3)\n\t" \
    "v_pk_add_f32 v[104:105], v[104:105], v[184:185]\n\t" \
    "v_pk_add_f32 v[106:107], v[106:107], v[186:187]\n\t" \
    "s_waitcnt lgkmcnt(2)\n\t" \
    "v_pk_add_f32 v[108:109], v[108:109], v[188:189]\n\t" \
    "v_pk_add_f32 v[110:111], v[110:111], v[190:191]\n\t" \
    "s_waitcnt lgkmcnt(1)\n\t" \
    "v_pk_add_f32 v[112:113], v[112:113], v[192:193]\n\t" \
    "v_pk_add_f32 v[114:115], v[114:115], v[194:195]\n\t" \
    "s_waitcnt lgkmcnt(0)\n\t" \
    "v_pk_add_f32 v[116:117], v[116:117], v[196:197]\n\t" \
    "v_pk_add_f32 v[118:119], v[118:119], v[198:199]\n\t" \
    "ds_write_b32 %[fc], v98\n\t" \
    "v_sub_u32 %[la], %[l2], %[la]\n\t" \
    "v_cvt_pk_bf16_f32 v84, v100, v101\n\t" \
    "v_cvt_pk_bf16_f32 v85, v102, v103\n\t" \
    "v_cvt_pk_bf16_f32 v86, v104, v105\n\t" \
    "v_cvt_pk_bf16_f32 v87, v106, v107\n\t" \
    "v_cvt_pk_bf16_f32 v88, v108, v109\n\t" \
    "v_cvt_pk_bf16_f32 v89, v110, v111\n\t" \
    "v_cvt_pk_bf16_f32 v90, v112, v113\n\t" \
    "v_cvt_pk_bf16_f32 v91, v114, v115\n\t" \
    "v_cvt_pk_bf16_f32 v92, v116, v117\n\t" \
    "v_cvt_pk_bf16_f32 v93, v118, v119\n\t" \
    "v_mov_b32 v80, 0\n\t" \
    "v_mov_b32 v82, 0\n\t" \
    "v_dot2c_f32_bf16 v80, v84, v99\n\t" \
    "v_dot2c_f32_bf16 v82, v84, v84\n\t" \
    "v_dot2c_f32_bf16 v80, v85, v99\n\t" \
    "v_dot2c_f32_bf16 v82, v85, v85\n\t" \
    "v_dot2c_f32_bf16 v80, v86, v99\n\t" \
    "v_dot2c_f32_bf16 v82, v86, v86\n\t" \
    "v_dot2c_f32_bf16 v80, v87, v99\n\t" \
    "v_dot2c_f32_bf16 v82, v87, v87\n\t" \
    "v_dot2c_f32_bf16 v80, v88, v99\n\t" \
    "v_dot2c_f32_bf16 v82, v88, v88\n\t" \
    "v_dot2c_f32_bf16 v80, v89, v99\n\t" \
    "v_dot2c_f32_bf16 v82, v89, v89\n\t" \
    "v_dot2c_f32_bf16 v80, v90, v99\n\t" \
    "v_dot2c_f32_bf16 v82, v90, v90\n\t" \
    "v_dot2c_f32_bf16 v80, v91, v99\n\t" \
    "v_dot2c_f32_bf16 v82, v91, v91\n\t" \
    "v_dot2c_f32_bf16 v80, v92, v99\n\t" \
    "v_dot2c_f32_bf16 v82, v92, v92\n\t" \
    "v_dot2c_f32_bf16 v80, v93, v99\n\t" \
    "v_dot2c_f32_bf16 v82, v93, v93\n\t" \
    "s_nop 2\n\t" \
    "v_mov_b32 v81, v80\n\t" \
    "v_mov_b32 v83, v82\n\t" \
    "s_nop 1\n\t" \
    "v_permlane16_swap_b32 v80, v81\n\t" \
    "v_permlane16_swap_b32 v82, v83\n\t" \
    "v_add_f32 v80, v80, v81\n\t" \
    "v_add_f32 v82, v82, v83\n\t" \
    "v_mov_b32 v81, v80\n\t" \
    "v_mov_b32 v83, v82\n\t" \
    "s_nop 1\n\t" \
    "v_permlane32_swap_b32 v80, v81\n\t" \
    "v_permlane32_swap_b32 v82, v83\n\t" \
    "v_add_f32 v80, v80, v81\n\t" \
    "v_add_f32 v82, v82, v83\n\t" \
    "v_min_u32 v80, -2, v80\n\t" \
    "v_min_u32 v81, -2, v82\n\t" \
    "s_mov_b64 exec, 0xffff\n\t" \
    "buffer_store_dwordx2 v[80:81], %[l8], %[rs], s50 offen sc1\n\t" \
    "s_mov_b64 exec, -1\n\t" \
    "s_add_u32 s46, s43, 0x2c00000\n\t" \
    "s_add_u32 s47, s43, 0x1c00000\n\t" \
    "buffer_load_dwordx2 v[120:121], %[t0], %[rs], s46 offen sc1\n\t" \
    "buffer_load_dwordx2 v[122:123], %[t0], %[rs], s47 offen sc1\n\t" \
    "buffer_load_dwordx2 v[124:125], %[t1], %[rs], s46 offen sc1\n\t" \
    "buffer_load_dwordx2 v[126:127], %[t1], %[rs], s47 offen sc1\n\t" \
    "buffer_load_dwordx2 v[128:129], %[t2], %[rs], s46 offen sc1\n\t" \
    "buffer_load_dwordx2 v[130:131], %[t2], %[rs], s47 offen sc1\n\t" \
    "buffer_load_dwordx2 v[132:133], %[t3], %[rs], s46 offen sc1\n\t" \
    "buffer_load_dwordx2 v[134:135], %[t3], %[rs], s47 offen sc1\n\t" \
    "buffer_load_dwordx2 v[136:137], %[t4], %[rs], s46 offen sc1\n\t" \
    "buffer_load_dwordx2 v[138:139], %[t4], %[rs], s47 offen sc1\n\t" \
    "s_nop 0\n\t" \
    "v_permlane16_swap_b32 v84, v86\n\t" \
    "v_permlane16_swap_b32 v85, v87\n\t" \
    "v_permlane16_swap_b32 v88, v90\n\t" \
    "v_permlane16_swap_b32 v89, v91\n\t" \
    "s_sleep 4\n\t" \
    ".Lf05_%=:\n\t" \
    "buffer_load_dwordx2 v[94:95], %[l8], %[rs], s49 offen sc1\n\t" \
    "buffer_load_dwordx2 v[96:97], %[l8], %[rs], s49 offen offset:512 sc1\n\t" \
    "s_waitcnt vmcnt(0)\n\t" \
    "v_max_u32 v80, v94, v95\n\t" \
    "v_max3_u32 v80, v96, v97, v80\n\t" \
    "v_cmp_eq_u32 vcc, -1, v80\n\t" \
    "s_cbranch_vccz .Lf06_%=\n\t" \
    "s_cmp_eq_u32 s41, 0\n\t" \
    "s_cbranch_scc1 .Lf06_%=\n\t" \
    "s_sub_u32 s41, s41, 1\n\t" \
    "s_branch .Lf05_%=\n\t" \
    ".Lf06_%=:\n\t" \
    "v_add_f32 v80, v94, v96\n\t" \
    "v_add_f32 v82, v95, v97\n\t" \
    "v_mov_b32 v81, v80\n\t" \
    "v_mov_b32 v83, v82\n\t" \
    "s_nop 1\n\t" \
    "v_permlane16_swap_b32 v80, v81\n\t" \
    "v_permlane16_swap_b32 v82, v83\n\t" \
    "v_add_f32 v80, v80, v81\n\t" \
    "v_add_f32 v82, v82, v83\n\t" \
    "v_mov_b32 v81, v80\n\t" \
    "v_mov_b32 v83, v82\n\t" \
    "s_nop 1\n\t" \
    "v_permlane32_swap_b32 v80, v81\n\t" \
    "v_permlane32_swap_b32 v82, v83\n\t" \
    "v_add_f32 v80, v80, v81\n\t" \
    "v_add_f32 v82, v82, v83\n\t" \
    "v_mul_f32 v80, 0x3acccccd, v80\n\t" \
    "v_mul_f32 v82, 0x3acccccd, v82\n\t" \
    "v_fma_f32 v82, -v80, v80, v82\n\t" \
    "v_cvt_pk_bf16_f32 v80, v80, v80\n\t" \
    "v_and_b32 v80, 0xffff0000, v80\n\t" \
    "v_max_f32 v82, 0, v82\n\t" \
    "v_cvt_pk_bf16_f32 v82, v82, v82\n\t" \
    "v_and_b32 v82, 0xffff0000, v82\n\t" \
    "v_add_f32 v82, 0x3727c5ac, v82\n\t" \
    "v_cvt_pk_bf16_f32 v82, v82, v82\n\t" \
    "v_and_b32 v82, 0xffff0000, v82\n\t" \
    "v_sqrt_f32 v82, v82\n\t" \
    "s_nop 0\n\t" \
    "v_cvt_pk_bf16_f32 v82, v82, v82\n\t" \
    "v_and_b32 v82, 0xffff0000, v82\n\t" \
    "v_rcp_f32 v82, v82\n\t" \
    "s_nop 0\n\t" \
    "v_mul_f32 v80, -v80, v82\n\t" \
    "v_fma_f32 v100, v100, v82, v80\n\t" \
    "v_fma_f32 v101, v101, v82, v80\n\t" \
    "v_fma_f32 v102, v102, v82, v80\n\t" \
    "v_fma_f32 v103, v103, v82, v80\n\t" \
    "v_fma_f32 v104, v104, v82, v80\n\t" \
    "v_fma_f32 v105, v105, v82, v80\n\t" \
    "v_fma_f32 v106, v106, v82, v80\n\t" \
    "v_fma_f32 v107, v107, v82, v80\n\t" \
    "v_fma_f32 v108, v108, v82, v80\n\t" \
    "v_fma_f32 v109, v109, v82, v80\n\t" \
    "v_fma_f32 v110, v110, v82, v80\n\t" \
    "v_fma_f32 v111, v111, v82, v80\n\t" \
    "v_fma_f32 v112, v112, v82, v80\n\t" \
    "v_fma_f32 v113, v113, v82, v80\n\t" \
    "v_fma_f32 v114, v114, v82, v80\n\t" \
    "v_fma_f32 v115, v115, v82, v80\n\t" \
    "v_fma_f32 v116, v116, v82, v80\n\t" \
    "v_fma_f32 v117, v117, v82, v80\n\t" \
    "v_fma_f32 v118, v118, v82, v80\n\t" \
    "v_fma_f32 v119, v119, v82, v80\n\t" \
    "v_cvt_pk_bf16_f32 v64, v100, v101\n\t" \
    "v_cvt_pk_bf16_f32 v65, v102, v103\n\t" \
    "v_cvt_pk_bf16_f32 v66, v104, v105\n\t" \
    "v_cvt_pk_bf16_f32 v67, v106, v107\n\t" \
    "v_cvt_pk_bf16_f32 v68, v108, v109\n\t" \
    "v_cvt_pk_bf16_f32 v69, v110, v111\n\t" \
    "v_cvt_pk_bf16_f32 v70, v112, v113\n\t" \
    "v_cvt_pk_bf16_f32 v71, v114, v115\n\t" \
    "v_cvt_pk_bf16_f32 v72, v116, v117\n\t" \
    "v_cvt_pk_bf16_f32 v73, v118, v119\n\t" \
    "s_nop 0\n\t" \
    "v_permlane16_swap_b32 v64, v66\n\t" \
    "v_permlane16_swap_b32 v65, v67\n\t" \
    "v_permlane16_swap_b32 v68, v70\n\t" \
    "v_permlane16_swap_b32 v69, v71\n\t" \
    "buffer_store_dwordx4 v[64:67], %[o16], %[rn], s51 offen sc1\n\t" \
    "buffer_store_dwordx4 v[68:71], %[o17], %[rn], s51 offen sc1\n\t" \
    "buffer_store_dwordx2 v[72:73], %[t4], %[rn], s51 offen sc1\n\t" \
    "buffer_store_dwordx4 v[84:87], %[o16], %[rn], s52 offen sc1\n\t" \
    "buffer_store_dwordx4 v[88:91], %[o17], %[rn], s52 offen sc1\n\t" \
    "buffer_store_dwordx2 v[92:93], %[t4], %[rn], s52 offen sc1\n\t" \
    "s_waitcnt vmcnt(0)\n\t" \
    "buffer_store_dwordx4 v[76:79], %[fo], %[rn], %[fs] offen sc1\n\t" \
    "s_add_u32 s40, s40, 2\n\t" \
    "s_cmp_lt_u32 s40, 128\n\t" \
    "s_cbranch_scc1 .Lf01_%=\n\t" \
    "s_branch .Lend%=\n\t" \
    ".Lend%=:\n\t" \
    "s_waitcnt vmcnt(0) lgkmcnt(0)\n\t" \
    "s_mov_b32 %[bud], s41\n\t"
#define ASM_W4_FIN1 \
    "s_mov_b32 s40, %[mt]\n\t" \
    "s_mov_b32 s41, 0x40000\n\t" \
    "s_mov_b32 s43, 0\n\t" \
    "v_mov_b32 v99, 0x3f803f80\n\t" \
    "s_setprio 3\n\t" \
    "s_add_u32 s46, s43, 0x2c00000\n\t" \
    "s_add_u32 s47, s43, 0x1c00000\n\t" \
    "buffer_load_dwordx2 v[120:121], %[t0], %[rs], s46 offen sc1\n\t" \
    "buffer_load_dwordx2 v[122:123], %[t0], %[rs], s47 offen sc1\n\t" \
    "buffer_load_dwordx2 v[124:125], %[t1], %[rs], s46 offen sc1\n\t" \
    "buffer_load_dwordx2 v[126:127], %[t1], %[rs], s47 offen sc1\n\t" \
    "buffer_load_dwordx2 v[128:129], %[t2], %[rs], s46 offen sc1\n\t" \
    "buffer_load_dwordx2 v[130:131], %[t2], %[rs], s47 offen sc1\n\t" \
    "buffer_load_dwordx2 v[132:133], %[t3], %[rs], s46 offen sc1\n\t" \
    "buffer_load_dwordx2 v[134:135], %[t3], %[rs], s47 offen sc1\n\t" \
    "buffer_load_dwordx2 v[136:137], %[t4], %[rs], s46 offen sc1\n\t" \
    "buffer_load_dwordx2 v[138:139], %[t4], %[rs], s47 offen sc1\n\t" \
    ".Lf11_%=:\n\t" \
    ".Lf12_%=:\n\t" \
    "s_waitcnt vmcnt(0)\n\t" \
    "v_max_u32 v80, v120, v121\n\t" \
    "v_max3_u32 v80, v122, v123, v80\n\t" \
    "v_max3_u32 v80, v124, v125, v80\n\t" \
    "v_max3_u32 v80, v126, v127, v80\n\t" \
    "v_max3_u32 v80, v128, v129, v80\n\t" \
    "v_max3_u32 v80, v130, v131, v80\n\t" \
    "v_max3_u32 v80, v132, v133, v80\n\t" \
    "v_max3_u32 v80, v134, v135, v80\n\t" \
    "v_max3_u32 v80, v136, v137, v80\n\t" \
    "v_max3_u32 v80, v138, v139, v80\n\t" \
    "v_cmp_eq_u32 vcc, -1, v80\n\t" \
    "s_cbranch_vccz .Lf13_%=\n\t" \
    "s_cmp_eq_u32 s41, 0\n\t" \
    "s_cbranch_scc1 .Lf13_%=\n\t" \
    "s_sub_u32 s41, s41, 1\n\t" \
    "s_sleep 8\n\t" \
    "s_add_u32 s46, s43, 0x2c00000\n\t" \
    "s_add_u32 s47, s43, 0x1c00000\n\t" \
    "buffer_load_dwordx2 v[120:121], %[t0], %[rs], s46 offen sc1\n\t" \
    "buffer_load_dwordx2 v[122:123], %[t0], %[rs], s47 offen sc1\n\t" \
    "buffer_load_dwordx2 v[124:125], %[t1], %[rs], s46 offen sc1\n\t" \
    "buffer_load_dwordx2 v[126:127], %[t1], %[rs], s47 offen sc1\n\t" \
    "buffer_load_dwordx2 v[128:129], %[t2], %[rs], s46 offen sc1\n\t" \
    "buffer_load_dwordx2 v[130:131], %[t2], %[rs], s47 offen sc1\n\t" \
    "buffer_load_dwordx2 v[132:133], %[t3], %[rs], s46 offen sc1\n\t" \
    "buffer_load_dwordx2 v[134:135], %[t3], %[rs], s47 offen sc1\n\t" \
    "buffer_load_dwordx2 v[136:137], %[t4], %[rs], s46 offen sc1\n\t" \
    "buffer_load_dwordx2 v[138:139], %[t4], %[rs], s47 offen sc1\n\t" \
    "s_waitcnt vmcnt(0)\n\t" \
    "s_branch .Lf12_%=\n\t" \
    ".Lf13_%=:\n\t" \
    "v_lshlrev_b32 v140, 16, v120\n\t" \
    "v_and_b32 v141, 0xffff0000, v120\n\t" \
    "v_lshlrev_b32 v142, 16, v121\n\t" \
    "v_and_b32 v143, 0xffff0000, v121\n\t" \
    "v_lshlrev_b32 v80, 16, v122\n\t" \
    "v_and_b32 v81, 0xffff0000, v122\n\t" \
    "v_lshlrev_b32 v82, 16, v123\n\t" \
    "v_and_b32 v83, 0xffff0000, v123\n\t" \
    "v_add_f32 v140, v140, v80\n\t" \
    "v_add_f32 v141, v141, v81\n\t" \
    "v_add_f32 v142, v142, v82\n\t" \
    "v_add_f32 v143, v143, v83\n\t" \
    "v_lshlrev_b32 v144, 16, v124\n\t" \
    "v_and_b32 v145, 0xffff0000, v124\n\t" \
    "v_lshlrev_b32 v146, 16, v125\n\t" \
    "v_and_b32 v147, 0xffff0000, v125\n\t" \
    "v_lshlrev_b32 v80, 16, v126\n\t" \
    "v_and_b32 v81, 0xffff0000, v126\n\t" \
    "v_lshlrev_b32 v82, 16, v127\n\t" \
    "v_and_b32 v83, 0xffff0000, v127\n\t" \
    "v_add_f32 v144, v144, v80\n\t" \
    "v_add_f32 v145, v145, v81\n\t" \
    "v_add_f32 v146, v146, v82\n\t" \
    "v_add_f32 v147, v147, v83\n\t" \
    "v_lshlrev_b32 v148, 16, v128\n\t" \
    "v_and_b32 v149, 0xffff0000, v128\n\t" \
    "v_lshlrev_b32 v150, 16, v129\n\t" \
    "v_and_b32 v151, 0xffff0000, v129\n\t" \
    "v_lshlrev_b32 v80, 16, v130\n\t" \
    "v_and_b32 v81, 0xffff0000, v130\n\t" \
    "v_lshlrev_b32 v82, 16, v131\n\t" \
    "v_and_b32 v83, 0xffff0000, v131\n\t" \
    "v_add_f32 v148, v148, v80\n\t" \
    "v_add_f32 v149, v149, v81\n\t" \
    "v_add_f32 v150, v150, v82\n\t" \
    "v_add_f32 v151, v151, v83\n\t" \
    "v_lshlrev_b32 v152, 16, v132\n\t" \
    "v_and_b32 v153, 0xffff0000, v132\n\t" \
    "v_lshlrev_b32 v154, 16, v133\n\t" \
    "v_and_b32 v155, 0xffff0000, v133\n\t" \
    "v_lshlrev_b32 v80, 16, v134\n\t" \
    "v_and_b32 v81, 0xffff0000, v134\n\t" \
    "v_lshlrev_b32 v82, 16, v135\n\t" \
    "v_and_b32 v83, 0xffff0000, v135\n\t" \
    "v_add_f32 v152, v152, v80\n\t" \
    "v_add_f32 v153, v153, v81\n\t" \
    "v_add_f32 v154, v154, v82\n\t" \
    "v_add_f32 v155, v155, v83\n\t" \
    "v_lshlrev_b32 v156, 16, v136\n\t" \
    "v_and_b32 v157, 0xffff0000, v136\n\t" \
    "v_lshlrev_b32 v158, 16, v137\n\t" \
    "v_and_b32 v159, 0xffff0000, v137\n\t" \
    "v_lshlrev_b32 v80, 16, v138\n\t" \
    "v_and_b32 v81, 0xffff0000, v138\n\t" \
    "v_lshlrev_b32 v82, 16, v139\n\t" \
    "v_and_b32 v83, 0xffff0000, v139\n\t" \
    "v_add_f32 v156, v156, v80\n\t" \
    "v_add_f32 v157, v157, v81\n\t" \
    "v_add_f32 v158, v158, v82\n\t" \
    "v_add_f32 v159, v159, v83\n\t" \
    "s_mov_b32 s42, s43\n\t" \
    "s_lshr_b32 s48, s40, 1\n\t" \
    "s_add_u32 s48, s48, 1\n\t" \
    "s_mul_i32 s43, s48, 0x14000\n\t" \
    "s_lshr_b32 s44, s48, 4\n\t" \
    "s_mul_i32 s44, s44, 0xc2000\n\t" \
    "s_add_u32 s43, s43, s44\n\t" \
    "s_mul_i32 s49, s40, 20480\n\t" \
    "s_add_u32 s49, s49, %[co]\n\t" \
    "s_add_u32 s44, s40, 1\n\t" \
    "v_mov_b32 v98, s44\n\t" \
    ".Lf14_%=:\n\t" \
    "s_nop 7\n\t" \
    "ds_read_b32 v80, %[fa]\n\t" \
    "s_waitcnt lgkmcnt(0)\n\t" \
    "v_cmp_ge_u32 vcc, s40, v80\n\t" \
    "s_cbranch_vccnz .Lf14_%=\n\t" \
    "ds_read_b128 v[160:163], %[la] offset:0\n\t" \
    "ds_read_b128 v[164:167], %[la] offset:6144\n\t" \
    "ds_read_b128 v[168:171], %[la] offset:12288\n\t" \
    "ds_read_b128 v[172:175], %[la] offset:18432\n\t" \
    "ds_read_b128 v[176:179], %[la] offset:24576\n\t" \
    "ds_read_b128 v[180:183], %[la] offset:1024\n\t" \
    "ds_read_b128 v[184:187], %[la] offset:7168\n\t" \
    "ds_read_b128 v[188:191], %[la] offset:13312\n\t" \
    "ds_read_b128 v[192:195], %[la] offset:19456\n\t" \
    "ds_read_b128 v[196:199], %[la] offset:25600\n\t" \
    "ds_read_b128 v[200:203], %[la] offset:2048\n\t" \
    "ds_read_b128 v[204:207], %[la] offset:8192\n\t" \
    "ds_read_b128 v[208:211], %[la] offset:14336\n\t" \
    "ds_read_b128 v[212:215], %[la] offset:20480\n\t" \
    "ds_read_b128 v[216:219], %[la] offset:26624\n\t" \
    "s_waitcnt lgkmcnt(14)\n\t" \
    "v_pk_add_f32 v[100:101], v[140:141], v[160:161]\n\t" \
    "v_pk_add_f32 v[102:103], v[142:143], v[162:163]\n\t" \
    "ds_read_b128 v[220:223], %[la] offset:3072\n\t" \
    "s_waitcnt lgkmcnt(14)\n\t" \
    "v_pk_add_f32 v[104:105], v[144:145], v[164:165]\n\t" \
    "v_pk_add_f32 v[106:107], v[146:147], v[166:167]\n\t" \
    "ds_read_b128 v[224:227], %[la] offset:9216\n\t" \
    "s_waitcnt lgkmcnt(14)\n\t" \
    "v_pk_add_f32 v[108:109], v[148:149], v[168:169]\n\t" \
    "v_pk_add_f32 v[110:111], v[150:151], v[170:171]\n\t" \
    "ds_read_b128 v[228:231], %[la] offset:15360\n\t" \
    "s_waitcnt lgkmcnt(14)\n\t" \
    "v_pk_add_f32 v[112:113], v[152:153], v[172:173]\n\t" \
    "v_pk_add_f32 v[114:115], v[154:155], v[174:175]\n\t" \
    "ds_read_b128 v[232:235], %[la] offset:21504\n\t" \
    "s_waitcnt lgkmcnt(14)\n\t" \
    "v_pk_add_f32 v[116:117], v[156:157], v[176:177]\n\t" \
    "v_pk_add_f32 v[118:119], v[158:159], v[178:179]\n\t" \
    "ds_read_b128 v[236:239], %[la] offset:27648\n\t" \
    "s_waitcnt lgkmcnt(14)\n\t" \
    "v_pk_add_f32 v[100:101], v[100:101], v[180:181]\n\t" \
    "v_pk_add_f32 v[102:103], v[102:103], v[182:183]\n\t" \
    "ds_read_b128 v[160:163], %[la] offset:4096\n\t" \
    "s_waitcnt lgkmcnt(14)\n\t" \
    "v_pk_add_f32 v[104:105], v[104:105], v[184:185]\n\t" \
    "v_pk_add_f32 v[106:107], v[106:107], v[186:187]\n\t" \
    "ds_read_b128 v[164:167], %[la] offset:10240\n\t" \
    "s_waitcnt lgkmcnt(14)\n\t" \
    "v_pk_add_f32 v[108:109], v[108:109], v[188:189]\n\t" \
    "v_pk_add_f32 v[110:111], v[110:111], v[190:191]\n\t" \
    "ds_read_b128 v[168:171], %[la] offset:16384\n\t" \
    "s_waitcnt lgkmcnt(14)\n\t" \
    "v_pk_add_f32 v[112:113], v[112:113], v[192:193]\n\t" \
    "v_pk_add_f32 v[114:115], v[114:115], v[194:195]\n\t" \
    "ds_read_b128 v[172:175], %[la] offset:22528\n\t" \
    "s_waitcnt lgkmcnt(14)\n\t" \
    "v_pk_add_f32 v[116:117], v[116:117], v[196:197]\n\t" \
    "v_pk_add_f32 v[118:119], v[118:119], v[198:199]\n\t" \
    "ds_read_b128 v[176:179], %[la] offset:28672\n\t" \
    "s_waitcnt lgkmcnt(14)\n\t" \
    "v_pk_add_f32 v[100:101], v[100:101], v[200:201]\n\t" \
    "v_pk_add_f32 v[102:103], v[102:103], v[202:203]\n\t" \
    "ds_read_b128 v[180:183], %[la] offset:5120\n\t" \
    "s_waitcnt lgkmcnt(14)\n\t" \
    "v_pk_add_f32 v[104:105], v[104:105], v[204:205]\n\t" \
    "v_pk_add_f32 v[106:107], v[106:107], v[206:207]\n\t" \
    "ds_read_b128 v[184:187], %[la] offset:11264\n\t" \
    "s_waitcnt lgkmcnt(14)\n\t" \
    "v_pk_add_f32 v[108:109], v[108:109], v[208:209]\n\t" \
    "v_pk_add_f32 v[110:111], v[110:111], v[210:211]\n\t" \
    "ds_read_b128 v[188:191], %[la] offset:17408\n\t" \
    "s_waitcnt lgkmcnt(14)\n\t" \
    "v_pk_add_f32 v[112:113], v[112:113], v[212:213]\n\t" \
    "v_pk_add_f32 v[114:115], v[114:115], v[214:215]\n\t" \
    "ds_read_b128 v[192:195], %[la] offset:23552\n\t" \
    "s_waitcnt lgkmcnt(14)\n\t" \
    "v_pk_add_f32 v[116:117], v[116:117], v[216:217]\n\t" \
    "v_pk_add_f32 v[118:119], v[118:119], v[218:219]\n\t" \
    "ds_read_b128 v[196:199], %[la] offset:29696\n\t" \
    "s_waitcnt lgkmcnt(14)\n\t" \
    "v_pk_add_f32 v[100:101], v[100:101], v[220:221]\n\t" \
    "v_pk_add_f32 v[102:103], v[102:103], v[222:223]\n\t" \
    "s_waitcnt lgkmcnt(13)\n\t" \
    "v_pk_add_f32 v[104:105], v[104:105], v[224:225]\n\t" \
    "v_pk_add_f32 v[106:107], v[106:107], v[226:227]\n\t" \
    "s_waitcnt lgkmcnt(12)\n\t" \
    "v_pk_add_f32 v[108:109], v[108:109], v[228:229]\n\t" \
    "v_pk_add_f32 v[110:111], v[110:111], v[230:231]\n\t" \
    "s_waitcnt lgkmcnt(11)\n\t" \
    "v_pk_add_f32 v[112:113], v[112:113], v[232:233]\n\t" \
    "v_pk_add_f32 v[114:115], v[114:115], v[234:235]\n\t" \
    "s_waitcnt lgkmcnt(10)\n\t" \
    "v_pk_add_f32 v[116:117], v[116:117], v[236:237]\n\t" \
    "v_pk_add_f32 v[118:119], v[118:119], v[238:239]\n\t" \
    "s_waitcnt lgkmcnt(9)\n\t" \
    "v_pk_add_f32 v[100:101], v[100:101], v[160:161]\n\t" \
    "v_pk_add_f32 v[102:103], v[102:103], v[162:163]\n\t" \
    "s_waitcnt lgkmcnt(8)\n\t" \
    "v_pk_add_f32 v[104:105], v[104:105], v[164:165]\n\t" \
    "v_pk_add_f32 v[106:107], v[106:107], v[166:167]\n\t" \
    "s_waitcnt lgkmcnt(7)\n\t" \
    "v_pk_add_f32 v[108:109], v[108:109], v[168:169]\n\t" \
    "v_pk_add_f32 v[110:111], v[110:111], v[170:171]\n\t" \
    "s_waitcnt lgkmcnt(6)\n\t" \
    "v_pk_add_f32 v[112:113], v[112:113], v[172:173]\n\t" \
    "v_pk_add_f32 v[114:115], v[114:115], v[174:175]\n\t" \
    "s_waitcnt lgkmcnt(5)\n\t" \
    "v_pk_add_f32 v[116:117], v[116:117], v[176:177]\n\t" \
    "v_pk_add_f32 v[118:119], v[118:119], v[178:179]\n\t" \
    "s_waitcnt lgkmcnt(4)\n\t" \
    "v_pk_add_f32 v[100:101], v[100:101], v[180:181]\n\t" \
    "v_pk_add_f32 v[102:103], v[102:103], v[182:183]\n\t" \
    "s_waitcnt lgkmcnt(3)\n\t" \
    "v_pk_add_f32 v[104:105], v[104:105], v[184:185]\n\t" \
    "v_pk_add_f32 v[106:107], v[106:107], v[186:187]\n\t" \
    "s_waitcnt lgkmcnt(2)\n\t" \
    "v_pk_add_f32 v[108:109], v[108:109], v[188:189]\n\t" \
    "v_pk_add_f32 v[110:111], v[110:111], v[190:191]\n\t" \
    "s_waitcnt lgkmcnt(1)\n\t" \
    "v_pk_add_f32 v[112:113], v[112:113], v[192:193]\n\t" \
    "v_pk_add_f32 v[114:115], v[114:115], v[194:195]\n\t" \
    "s_waitcnt lgkmcnt(0)\n\t" \
    "v_pk_add_f32 v[116:117], v[116:117], v[196:197]\n\t" \
    "v_pk_add_f32 v[118:119], v[118:119], v[198:199]\n\t" \
    "ds_write_b32 %[fc], v98\n\t" \
    "v_sub_u32 %[la], %[l2], %[la]\n\t" \
    "v_cvt_pk_bf16_f32 v84, v100, v101\n\t" \
    "v_cvt_pk_bf16_f32 v85, v102, v103\n\t" \
    "v_cvt_pk_bf16_f32 v86, v104, v105\n\t" \
    "v_cvt_pk_bf16_f32 v87, v106, v107\n\t" \
    "v_cvt_pk_bf16_f32 v88, v108, v109\n\t" \
    "v_cvt_pk_bf16_f32 v89, v110, v111\n\t" \
    "v_cvt_pk_bf16_f32 v90, v112, v113\n\t" \
    "v_cvt_pk_bf16_f32 v91, v114, v115\n\t" \
    "v_cvt_pk_bf16_f32 v92, v116, v117\n\t" \
    "v_cvt_pk_bf16_f32 v93, v118, v119\n\t" \
    "s_nop 0\n\t" \
    "v_permlane16_swap_b32 v84, v86\n\t" \
    "v_permlane16_swap_b32 v85, v87\n\t" \
    "v_permlane16_swap_b32 v88, v90\n\t" \
    "v_permlane16_swap_b32 v89, v91\n\t" \
    "buffer_store_dwordx4 v[84:87], %[q16], %[ro], s49 offen nt\n\t" \
    "buffer_store_dwordx4 v[88:91], %[q16], %[ro], s49 offen offset:64 nt\n\t" \
    "buffer_store_dwordx2 v[92:93], %[q8], %[ro], s49 offen offset:128 nt\n\t" \
    "s_cmp_gt_u32 s40, 125\n\t" \
    "s_cbranch_scc1 .Lf15_%=\n\t" \
    "s_add_u32 s46, s43, 0x2c00000\n\t" \
    "s_add_u32 s47, s43, 0x1c00000\n\t" \
    "buffer_load_dwordx2 v[120:121], %[t0], %[rs], s46 offen sc1\n\t" \
    "buffer_load_dwordx2 v[122:123], %[t0], %[rs], s47 offen sc1\n\t" \
    "buffer_load_dwordx2 v[124:125], %[t1], %[rs], s46 offen sc1\n\t" \
    "buffer_load_dwordx2 v[126:127], %[t1], %[rs], s47 offen sc1\n\t" \
    "buffer_load_dwordx2 v[128:129], %[t2], %[rs], s46 offen sc1\n\t" \
    "buffer_load_dwordx2 v[130:131], %[t2], %[rs], s47 offen sc1\n\t" \
    "buffer_load_dwordx2 v[132:133], %[t3], %[rs], s46 offen sc1\n\t" \
    "buffer_load_dwordx2 v[134:135], %[t3], %[rs], s47 offen sc1\n\t" \
    "buffer_load_dwordx2 v[136:137], %[t4], %[rs], s46 offen sc1\n\t" \
    "buffer_load_dwordx2 v[138:139], %[t4], %[rs], s47 offen sc1\n\t" \
    ".Lf15_%=:\n\t" \
    "s_add_u32 s40, s40, 2\n\t" \
    "s_cmp_lt_u32 s40, 128\n\t" \
    "s_cbranch_scc1 .Lf11_%=\n\t" \
    "s_branch .Lend%=\n\t" \
    ".Lend%=:\n\t" \
    "s_waitcnt vmcnt(0) lgkmcnt(0)\n\t" \
    "s_mov_b32 %[bud], s41\n\t"
#define ASM_W1_LOOP \
    "s_mov_b32 s40, 0\n\t" \
    "s_mov_b32 s41, 0x20000\n\t" \
    "s_add_u32 m0, %[wm], 0x0\n\t" \
    "s_add_u32 s44, %[wo], 0x0\n\t" \
    "buffer_load_dwordx4 v176, %[wr], s44 offen nt lds\n\t" \
    "s_add_u32 m0, %[wm], 0x2000\n\t" \
    "s_add_u32 s44, %[wo], 0xa000\n\t" \
    "buffer_load_dwordx4 v176, %[wr], s44 offen nt lds\n\t" \
    "s_add_u32 m0, %[wm], 0x4000\n\t" \
    "s_add_u32 s44, %[wo], 0x14000\n\t" \
    "buffer_load_dwordx4 v176, %[wr], s44 offen nt lds\n\t" \
    "s_add_u32 m0, %[wm], 0x6000\n\t" \
    "s_add_u32 s44, %[wo], 0x1e000\n\t" \
    "buffer_load_dwordx4 v176, %[wr], s44 offen nt lds\n\t" \
    "s_add_u32 m0, %[wm], 0x8000\n\t" \
    "s_add_u32 s44, %[wo], 0x28000\n\t" \
    "buffer_load_dwordx4 v176, %[wr], s44 offen nt lds\n\t" \
    "s_add_u32 m0, %[wm], 0xa000\n\t" \
    "s_add_u32 s44, %[wo], 0x32000\n\t" \
    "buffer_load_dwordx4 v176, %[wr], s44 offen nt lds\n\t" \
    "s_add_u32 m0, %[wm], 0xc000\n\t" \
    "s_add_u32 s44, %[wo], 0x3c000\n\t" \
    "buffer_load_dwordx4 v176, %[wr], s44 offen nt lds\n\t" \
    "s_add_u32 m0, %[wm], 0xe000\n\t" \
    "s_add_u32 s44, %[wo], 0x46000\n\t" \
    "buffer_load_dwordx4 v176, %[wr], s44 offen nt lds\n\t" \
    "s_add_u32 m0, %[wm], 0x10000\n\t" \
    "s_add_u32 s44, %[wo], 0x50000\n\t" \
    "buffer_load_dwordx4 v176, %[wr], s44 offen nt lds\n\t" \
    "s_add_u32 m0, %[wm], 0x12000\n\t" \
    "s_add_u32 s44, %[wo], 0x5a000\n\t" \
    "buffer_load_dwordx4 v176, %[wr], s44 offen nt lds\n\t" \
    "s_add_u32 m0, %[wm], 0x14000\n\t" \
    "s_add_u32 s44, %[wo], 0x64000\n\t" \
    "buffer_load_dwordx4 v176, %[wr], s44 offen nt lds\n\t" \
    "s_add_u32 m0, %[wm], 0x16000\n\t" \
    "s_add_u32 s44, %[wo], 0x6e000\n\t" \
    "buffer_load_dwordx4 v176, %[wr], s44 offen nt lds\n\t" \
    "s_add_u32 m0, %[wm], 0x18000\n\t" \
    "s_add_u32 s44, %[wo], 0x78000\n\t" \
    "buffer_load_dwordx4 v176, %[wr], s44 offen nt lds\n\t" \
    "s_add_u32 m0, %[wm], 0x1a000\n\t" \
    "s_add_u32 s44, %[wo], 0x82000\n\t" \
    "buffer_load_dwordx4 v176, %[wr], s44 offen nt lds\n\t" \
    "s_add_u32 m0, %[wm], 0x1c000\n\t" \
    "s_add_u32 s44, %[wo], 0x8c000\n\t" \
    "buffer_load_dwordx4 v176, %[wr], s44 offen nt lds\n\t" \
    "s_add_u32 m0, %[wm], 0x1e000\n\t" \
    "s_add_u32 s44, %[wo], 0x96000\n\t" \
    "buffer_load_dwordx4 v176, %[wr], s44 offen nt lds\n\t" \
    "s_add_u32 m0, %[wm], 0x20000\n\t" \
    "s_add_u32 s44, %[wo], 0xa0000\n\t" \
    "buffer_load_dwordx4 v176, %[wr], s44 offen nt lds\n\t" \
    "s_add_u32 m0, %[wm], 0x22000\n\t" \
    "s_add_u32 s44, %[wo], 0xaa000\n\t" \
    "buffer_load_dwordx4 v176, %[wr], s44 offen nt lds\n\t" \
    "s_add_u32 m0, %[wm], 0x24000\n\t" \
    "s_add_u32 s44, %[wo], 0xb4000\n\t" \
    "buffer_load_dwordx4 v176, %[wr], s44 offen nt lds\n\t" \
    "s_add_u32 m0, %[wm], 0x26000\n\t" \
    "s_add_u32 s44, %[wo], 0xbe000\n\t" \
    "buffer_load_dwordx4 v176, %[wr], s44 offen nt lds\n\t" \
    "s_waitcnt vmcnt(0) lgkmcnt(0)\n\t" \
    "s_barrier \n\t" \
    "v_add_u32 v178, 0x0, v177\n\t" \
    "ds_read_b64_tr_b16 v[0:1], v178 offset:0\n\t" \
    "ds_read_b64_tr_b16 v[2:3], v178 offset:1024\n\t" \
    "ds_read_b64_tr_b16 v[4:5], v178 offset:8192\n\t" \
    "ds_read_b64_tr_b16 v[6:7], v178 offset:9216\n\t" \
    "ds_read_b64_tr_b16 v[8:9], v178 offset:16384\n\t" \
    "ds_read_b64_tr_b16 v[10:11], v178 offset:17408\n\t" \
    "ds_read_b64_tr_b16 v[12:13], v178 offset:24576\n\t" \
    "ds_read_b64_tr_b16 v[14:15], v178 offset:25600\n\t" \
    "v_add_u32 v178, 0x8000, v177\n\t" \
    "ds_read_b64_tr_b16 v[16:17], v178 offset:0\n\t" \
    "ds_read_b64_tr_b16 v[18:19], v178 offset:1024\n\t" \
    "ds_read_b64_tr_b16 v[20:21], v178 offset:8192\n\t" \
    "ds_read_b64_tr_b16 v[22:23], v178 offset:9216\n\t" \
    "ds_read_b64_tr_b16 v[24:25], v178 offset:16384\n\t" \
    "ds_read_b64_tr_b16 v[26:27], v178 offset:17408\n\t" \
    "ds_read_b64_tr_b16 v[28:29], v178 offset:24576\n\t" \
    "ds_read_b64_tr_b16 v[30:31], v178 offset:25600\n\t" \
    "v_add_u32 v178, 0x10000, v177\n\t" \
    "ds_read_b64_tr_b16 v[32:33], v178 offset:0\n\t" \
    "ds_read_b64_tr_b16 v[34:35], v178 offset:1024\n\t" \
    "ds_read_b64_tr_b16 v[36:37], v178 offset:8192\n\t" \
    "ds_read_b64_tr_b16 v[38:39], v178 offset:9216\n\t" \
    "ds_read_b64_tr_b16 v[40:41], v178 offset:16384\n\t" \
    "ds_read_b64_tr_b16 v[42:43], v178 offset:17408\n\t" \
    "ds_read_b64_tr_b16 v[44:45], v178 offset:24576\n\t" \
    "ds_read_b64_tr_b16 v[46:47], v178 offset:25600\n\t" \
    "v_add_u32 v178, 0x18000, v177\n\t" \
    "ds_read_b64_tr_b16 v[48:49], v178 offset:0\n\t" \
    "ds_read_b64_tr_b16 v[50:51], v178 offset:1024\n\t" \
    "ds_read_b64_tr_b16 v[52:53], v178 offset:8192\n\t" \
    "ds_read_b64_tr_b16 v[54:55], v178 offset:9216\n\t" \
    "ds_read_b64_tr_b16 v[56:57], v178 offset:16384\n\t" \
    "ds_read_b64_tr_b16 v[58:59], v178 offset:17408\n\t" \
    "ds_read_b64_tr_b16 v[60:61], v178 offset:24576\n\t" \
    "ds_read_b64_tr_b16 v[62:63], v178 offset:25600\n\t" \
    "v_add_u32 v178, 0x20000, v177\n\t" \
    "ds_read_b64_tr_b16 v[64:65], v178 offset:0\n\t" \
    "ds_read_b64_tr_b16 v[66:67], v178 offset:1024\n\t" \
    "ds_read_b64_tr_b16 v[68:69], v178 offset:8192\n\t" \
    "ds_read_b64_tr_b16 v[70:71], v178 offset:9216\n\t" \
    "ds_read_b64_tr_b16 v[72:73], v178 offset:16384\n\t" \
    "ds_read_b64_tr_b16 v[74:75], v178 offset:17408\n\t" \
    "ds_read_b64_tr_b16 v[76:77], v178 offset:24576\n\t" \
    "ds_read_b64_tr_b16 v[78:79], v178 offset:25600\n\t" \
    "v_mov_b32 v228, 0x3f803f80\n\t" \
    "v_mov_b32 v229, 0x3f803f80\n\t" \
    "v_mov_b32 v230, 0x3f803f80\n\t" \
    "v_mov_b32 v231, 0x3f803f80\n\t" \
    "v_mov_b64 v[216:217], 0\n\t" \
    "v_mov_b32 v219, 0\n\t" \
    "v_bfe_u32 v83, v253, 2, 5\n\t" \
    "v_add_u32 v83, 0x7000, v83\n\t" \
    "v_mov_b32 v218, 0\n\t" \
    "s_waitcnt lgkmcnt(0)\n\t" \
    "s_barrier \n\t" \
    "ds_write_b32 v251, v228 offset:72\n\t" \
    "ds_write_b32 v251, v228 offset:2120\n\t" \
    "v_mfma_f32_16x16x32_bf16 v[160:163], v[0:3], v[228:231], 0\n\t" \
    "v_mfma_f32_16x16x32_bf16 v[160:163], v[4:7], v[228:231], v[160:163]\n\t" \
    "v_mfma_f32_16x16x32_bf16 v[160:163], v[8:11], v[228:231], v[160:163]\n\t" \
    "v_mfma_f32_16x16x32_bf16 v[160:163], v[12:15], v[228:231], v[160:163]\n\t" \
    "v_mfma_f32_16x16x32_bf16 v[160:163], v[16:19], v[228:231], v[160:163]\n\t" \
    "v_mfma_f32_16x16x32_bf16 v[160:163], v[20:23], v[228:231], v[160:163]\n\t" \
    "v_mfma_f32_16x16x32_bf16 v[160:163], v[24:27], v[228:231], v[160:163]\n\t" \
    "v_mfma_f32_16x16x32_bf16 v[160:163], v[28:31], v[228:231], v[160:163]\n\t" \
    "v_mfma_f32_16x16x32_bf16 v[160:163], v[32:35], v[228:231], v[160:163]\n\t" \
    "v_mfma_f32_16x16x32_bf16 v[160:163], v[36:39], v[228:231], v[160:163]\n\t" \
    "v_mfma_f32_16x16x32_bf16 v[160:163], v[40:43], v[228:231], v[160:163]\n\t" \
    "v_mfma_f32_16x16x32_bf16 v[160:163], v[44:47], v[228:231], v[160:163]\n\t" \
    "v_mfma_f32_16x16x32_bf16 v[160:163], v[48:51], v[228:231], v[160:163]\n\t" \
    "v_mfma_f32_16x16x32_bf16 v[160:163], v[52:55], v[228:231], v[160:163]\n\t" \
    "v_mfma_f32_16x16x32_bf16 v[160:163], v[56:59], v[228:231], v[160:163]\n\t" \
    "v_mfma_f32_16x16x32_bf16 v[160:163], v[60:63], v[228:231], v[160:163]\n\t" \
    "v_mfma_f32_16x16x32_bf16 v[160:163], v[64:67], v[228:231], v[160:163]\n\t" \
    "v_mfma_f32_16x16x32_bf16 v[160:163], v[68:71], v[228:231], v[160:163]\n\t" \
    "v_mfma_f32_16x16x32_bf16 v[160:163], v[72:75], v[228:231], v[160:163]\n\t" \
    "v_mfma_f32_16x16x32_bf16 v[160:163], v[76:79], v[228:231], v[160:163]\n\t" \
    "s_mov_b32 s52, 0x22222222\n\t" \
    "s_mov_b32 s53, 0x22222222\n\t" \
    "s_mov_b32 s54, 0x44444444\n\t" \
    "s_mov_b32 s55, 0x44444444\n\t" \
    "s_mov_b32 s56, 0x88888888\n\t" \
    "s_mov_b32 s57, 0x88888888\n\t" \
    "ds_write_b32 v83, v218\n\t" \
    "s_waitcnt lgkmcnt(0)\n\t" \
    "s_barrier \n\t" \
    ".Lw11_%=:\n\t" \
    "v_mov_b32 v248, 0\n\t" \
    "buffer_load_dword v248, v248, %[rs], %[go] offen sc1\n\t" \
    "s_waitcnt vmcnt(0)\n\t" \
    "v_cmp_ne_u32 vcc, 0, v248\n\t" \
    "s_cbranch_vccnz .Lw12_%=\n\t" \
    "s_cmp_eq_u32 s41, 0\n\t" \
    "s_cbranch_scc1 .Lw12_%=\n\t" \
    "s_sub_u32 s41, s41, 1\n\t" \
    "s_sleep 16\n\t" \
    "s_branch .Lw11_%=\n\t" \
    ".Lw12_%=:\n\t" \
    "s_sub_i32 s60, 0, %[ng]\n\t" \
    "s_sub_i32 s61, 1, %[ng]\n\t" \
    "s_add_i32 s62, s60, 6\n\t" \
    "s_add_i32 s63, s61, 6\n\t" \
    "s_mul_i32 s42, s61, 127\n\t" \
    "s_mul_i32 s44, %[ng], 0x733000\n\t" \
    "s_add_u32 s44, s44, 0x2400000\n\t" \
    "v_cmp_ne_u32 s[58:59], %[ng], v255\n\t" \
    "s_nop 1\n\t" \
    "s_mov_b64 exec, s[58:59]\n\t" \
    "buffer_store_dwordx2 v[216:217], v254, %[rs], s44 offen sc1\n\t" \
    "s_mov_b64 exec, -1\n\t" \
    "s_lshr_b32 s48, %[wave], 2\n\t" \
    "s_mul_i32 s48, s48, 0x3000\n\t" \
    "s_add_u32 s48, s48, %[pi]\n\t" \
    "s_mov_b32 s43, 0\n\t" \
    "s_add_u32 s46, s43, s48\n\t" \
    "s_add_u32 s47, s46, 0x0\n\t" \
    "buffer_load_dwordx4 v[208:211], v253, %[rs], s47 offen sc1\n\t" \
    "s_add_u32 s47, s46, 0x8000\n\t" \
    "buffer_load_dwordx4 v[212:215], v253, %[rs], s47 offen sc1\n\t" \
    "s_cmp_gt_u32 %[wave], 3\n\t" \
    "s_cbranch_scc1 .Lw13_%=\n\t" \
    "s_add_u32 s47, s46, 0x10000\n\t" \
    "buffer_load_dwordx4 v[216:219], v253, %[rs], s47 offen sc1\n\t" \
    ".Lw13_%=:\n\t" \
    "s_waitcnt vmcnt(0)\n\t" \
    "v_lshrrev_b32 v82, 1, v253\n\t" \
    ".Lw14_%=:\n\t" \
    "s_add_i32 s44, s40, s60\n\t" \
    "s_max_i32 s44, s44, 0\n\t" \
    "s_lshr_b32 s45, s44, 1\n\t" \
    "s_and_b32 s44, s44, 1\n\t" \
    "s_lshl_b32 s44, s44, 12\n\t" \
    "s_mul_i32 s49, s45, 0x14000\n\t" \
    "s_add_u32 s49, s49, s44\n\t" \
    "s_lshr_b32 s45, s45, 4\n\t" \
    "s_mul_i32 s45, s45, 0xc2000\n\t" \
    "s_add_u32 s49, s49, s45\n\t" \
    "s_add_i32 s44, s40, s61\n\t" \
    "s_max_i32 s44, s44, 0\n\t" \
    "s_lshr_b32 s45, s44, 1\n\t" \
    "s_and_b32 s44, s44, 1\n\t" \
    "s_lshl_b32 s44, s44, 12\n\t" \
    "s_mul_i32 s50, s45, 0x14000\n\t" \
    "s_add_u32 s50, s50, s44\n\t" \
    "s_lshr_b32 s45, s45, 4\n\t" \
    "s_mul_i32 s45, s45, 0xc2000\n\t" \
    "s_add_u32 s50, s50, s45\n\t" \
    "s_sub_u32 s50, s50, s49\n\t" \
    "s_add_u32 s49, s49, 0x2400000\n\t" \
    "v_mad_u32_u24 v80, v255, s50, v254\n\t" \
    "s_lshr_b32 s44, s40, 6\n\t" \
    "s_lshl_b32 s44, s44, 14\n\t" \
    "s_and_b32 s45, s40, 63\n\t" \
    "s_lshl_b32 s45, s45, 7\n\t" \
    "s_add_u32 s44, s44, s45\n\t" \
    "s_add_u32 s50, s44, 0x4d0000\n\t" \
    "s_add_i32 s44, s40, 1\n\t" \
    "s_max_i32 s44, s44, 0\n\t" \
    "s_lshr_b32 s45, s44, 1\n\t" \
    "s_and_b32 s44, s44, 1\n\t" \
    "s_lshl_b32 s44, s44, 12\n\t" \
    "s_mul_i32 s43, s45, 0x14000\n\t" \
    "s_add_u32 s43, s43, s44\n\t" \
    "s_lshr_b32 s45, s45, 4\n\t" \
    "s_mul_i32 s45, s45, 0xc2000\n\t" \
    "s_add_u32 s43, s43, s45\n\t" \
    "s_setprio 2\n\t" \
    "v_max_u32 v248, v208, v209\n\t" \
    "v_max3_u32 v248, v210, v211, v248\n\t" \
    "v_max3_u32 v248, v212, v213, v248\n\t" \
    "v_max3_u32 v248, v214, v215, v248\n\t" \
    "v_max3_u32 v248, v216, v217, v248\n\t" \
    "v_max3_u32 v248, v218, v219, v248\n\t" \
    "v_cmp_eq_u32 vcc, -1, v248\n\t" \
    "s_cbranch_vccz .Lw16_%=\n\t" \
    "s_and_b32 s44, s40, 1\n\t" \
    "s_cmp_eq_u32 s44, 1\n\t" \
    "s_cbranch_scc1 .Lw15_%=\n\t" \
    "s_cmp_gt_u32 s40, 123\n\t" \
    "s_cbranch_scc1 .Lw15_%=\n\t" \
    "s_sleep 10\n\t" \
    ".Lw15_%=:\n\t" \
    "v_max_u32 v248, v208, v209\n\t" \
    "v_max3_u32 v248, v210, v211, v248\n\t" \
    "v_cmp_eq_u32 vcc, -1, v248\n\t" \
    "s_cbranch_vccz .Lw18_%=\n\t" \
    "s_add_u32 s47, s46, 0x0\n\t" \
    "buffer_load_dwordx4 v[208:211], v253, %[rs], s47 offen sc1\n\t" \
    ".Lw18_%=:\n\t" \
    "v_max_u32 v248, v212, v213\n\t" \
    "v_max3_u32 v248, v214, v215, v248\n\t" \
    "v_cmp_eq_u32 vcc, -1, v248\n\t" \
    "s_cbranch_vccz .Lw19_%=\n\t" \
    "s_add_u32 s47, s46, 0x8000\n\t" \
    "buffer_load_dwordx4 v[212:215], v253, %[rs], s47 offen sc1\n\t" \
    ".Lw19_%=:\n\t" \
    "v_max_u32 v248, v216, v217\n\t" \
    "v_max3_u32 v248, v218, v219, v248\n\t" \
    "v_cmp_eq_u32 vcc, -1, v248\n\t" \
    "s_cbranch_vccz .Lw110_%=\n\t" \
    "s_add_u32 s47, s46, 0x10000\n\t" \
    "buffer_load_dwordx4 v[216:219], v253, %[rs], s47 offen sc1\n\t" \
    ".Lw110_%=:\n\t" \
    "s_waitcnt vmcnt(0)\n\t" \
    "v_max_u32 v248, v208, v209\n\t" \
    "v_max3_u32 v248, v210, v211, v248\n\t" \
    "v_max3_u32 v248, v212, v213, v248\n\t" \
    "v_max3_u32 v248, v214, v215, v248\n\t" \
    "v_max3_u32 v248, v216, v217, v248\n\t" \
    "v_max3_u32 v248, v218, v219, v248\n\t" \
    "v_cmp_eq_u32 vcc, -1, v248\n\t" \
    "s_cbranch_vccz .Lw16_%=\n\t" \
    "s_cmp_eq_u32 s41, 0\n\t" \
    "s_cbranch_scc1 .Lw16_%=\n\t" \
    "s_sub_u32 s41, s41, 1\n\t" \
    "s_branch .Lw15_%=\n\t" \
    ".Lw16_%=:\n\t" \
    "ds_write_b128 v250, v[208:211] offset:0\n\t" \
    "v_mfma_f32_16x16x32_bf16 v[236:239], v[208:211], v[208:211], 0\n\t" \
    "v_mfma_f32_16x16x32_bf16 v[232:235], v[228:231], v[208:211], 0\n\t" \
    "ds_write_b128 v250, v[212:215] offset:8192\n\t" \
    "v_mfma_f32_16x16x32_bf16 v[236:239], v[212:215], v[212:215], v[236:239]\n\t" \
    "v_mfma_f32_16x16x32_bf16 v[232:235], v[228:231], v[212:215], v[232:235]\n\t" \
    "s_cmp_gt_u32 %[wave], 3\n\t" \
    "s_cbranch_scc1 .Lw111_%=\n\t" \
    "ds_write_b128 v250, v[216:219] offset:16384\n\t" \
    ".Lw111_%=:\n\t" \
    "v_mfma_f32_16x16x32_bf16 v[236:239], v[216:219], v[216:219], v[236:239]\n\t" \
    "v_mfma_f32_16x16x32_bf16 v[232:235], v[228:231], v[216:219], v[232:235]\n\t" \
    "s_add_u32 s46, s43, s48\n\t" \
    "s_nop 4\n\t" \
    "v_cndmask_b32 v236, v236, v237, s[52:53]\n\t" \
    "v_cndmask_b32 v236, v236, v238, s[54:55]\n\t" \
    "v_cndmask_b32 v236, v236, v239, s[56:57]\n\t" \
    "s_mov_b32 exec_lo, 0xf0000f\n\t" \
    "s_mov_b32 exec_hi, 0xf0000f00\n\t" \
    "ds_write2_b32 v252, v232, v236 offset1:1\n\t" \
    "s_mov_b64 exec, -1\n\t" \
    "s_waitcnt lgkmcnt(0)\n\t" \
    "s_barrier \n\t" \
    "s_setprio 0\n\t" \
    "s_cmp_lg_u32 %[wave], 4\n\t" \
    "s_cbranch_scc1 .Lw112_%=\n\t" \
    "s_setprio 1\n\t" \
    "ds_read_b128 v[232:235], v251 offset:0\n\t" \
    "ds_read_b128 v[236:239], v251 offset:16\n\t" \
    "ds_read_b128 v[240:243], v251 offset:32\n\t" \
    "ds_read_b128 v[244:247], v251 offset:48\n\t" \
    "s_waitcnt lgkmcnt(0)\n\t" \
    "v_add_f32 v232, v232, v240\n\t" \
    "v_add_f32 v233, v233, v241\n\t" \
    "v_add_f32 v234, v234, v242\n\t" \
    "v_add_f32 v235, v235, v243\n\t" \
    "v_add_f32 v236, v236, v244\n\t" \
    "v_add_f32 v237, v237, v245\n\t" \
    "v_add_f32 v238, v238, v246\n\t" \
    "v_add_f32 v239, v239, v247\n\t" \
    "v_add_f32 v232, v232, v236\n\t" \
    "v_add_f32 v233, v233, v237\n\t" \
    "v_add_f32 v234, v234, v238\n\t" \
    "v_add_f32 v235, v235, v239\n\t" \
    "v_add_f32 v232, v232, v234\n\t" \
    "v_add_f32 v233, v233, v235\n\t" \
    "v_mul_f32 v232, 0x3acccccd, v232\n\t" \
    "v_mul_f32 v233, 0x3acccccd, v233\n\t" \
    "v_fma_f32 v233, -v232, v232, v233\n\t" \
    "v_mov_b32 v234, v232\n\t" \
    "v_max_f32 v233, 0, v233\n\t" \
    "v_cvt_pk_bf16_f32 v234, v234, v234\n\t" \
    "v_and_b32 v234, 0xffff0000, v234\n\t" \
    "v_mov_b32 v236, v233\n\t" \
    "v_cvt_pk_bf16_f32 v236, v236, v236\n\t" \
    "v_and_b32 v236, 0xffff0000, v236\n\t" \
    "v_add_f32 v236, 0x3727c5ac, v236\n\t" \
    "v_cvt_pk_bf16_f32 v236, v236, v236\n\t" \
    "v_and_b32 v236, 0xffff0000, v236\n\t" \
    "v_sqrt_f32 v236, v236\n\t" \
    "s_nop 0\n\t" \
    "v_cvt_pk_bf16_f32 v236, v236, v236\n\t" \
    "v_and_b32 v236, 0xffff0000, v236\n\t" \
    "v_rcp_f32 v235, v236\n\t" \
    "s_nop 0\n\t" \
    "v_sub_f32 v232, v232, v234\n\t" \
    "v_mul_f32 v233, v233, v235\n\t" \
    "v_mul_f32 v232, v232, v235\n\t" \
    "v_mul_f32 v233, v233, v235\n\t" \
    "v_cvt_pk_bf16_f32 v232, v232, v232\n\t" \
    "v_and_b32 v232, 0xffff0000, v232\n\t" \
    "v_cvt_pk_bf16_f32 v233, v233, v233\n\t" \
    "v_and_b32 v233, 0xffff0000, v233\n\t" \
    "v_add_f32 v233, 0x3727c5ac, v233\n\t" \
    "v_cvt_pk_bf16_f32 v233, v233, v233\n\t" \
    "v_and_b32 v233, 0xffff0000, v233\n\t" \
    "v_sqrt_f32 v233, v233\n\t" \
    "s_nop 0\n\t" \
    "v_cvt_pk_bf16_f32 v233, v233, v233\n\t" \
    "v_and_b32 v233, 0xffff0000, v233\n\t" \
    "v_rcp_f32 v233, v233\n\t" \
    "v_fma_f32 v232, v232, v236, v234\n\t" \
    "v_mul_f32 v237, v235, v233\n\t" \
    "v_mul_f32 v238, -v232, v237\n\t" \
    "v_min_u32 v234, -2, v234\n\t" \
    "v_min_u32 v235, -2, v235\n\t" \
    "v_mov_b32 v239, s40\n\t" \
    "ds_write2_b32 v251, v237, v238 offset0:16 offset1:17\n\t" \
    "ds_write_b32 v251, v239 offset:72\n\t" \
    "s_setprio 0\n\t" \
    "s_cmp_eq_u32 %[ss], 0\n\t" \
    "s_cbranch_scc1 .Lw113_%=\n\t" \
    "s_mov_b64 exec, 0xffff\n\t" \
    "buffer_store_dwordx2 v[234:235], v82, %[rs], s50 offen sc1\n\t" \
    "s_mov_b64 exec, -1\n\t" \
    ".Lw113_%=:\n\t" \
    ".Lw112_%=:\n\t" \
    "ds_read_b128 v[176:179], v249 offset:0\n\t" \
    "ds_read_b128 v[180:183], v249 offset:1024\n\t" \
    "ds_read_b128 v[184:187], v249 offset:2048\n\t" \
    "ds_read_b128 v[188:191], v249 offset:3072\n\t" \
    "ds_read_b128 v[192:195], v249 offset:4096\n\t" \
    "ds_read_b128 v[196:199], v249 offset:5120\n\t" \
    "ds_read_b128 v[200:203], v249 offset:6144\n\t" \
    "s_waitcnt lgkmcnt(6)\n\t" \
    "v_mfma_f32_16x16x32_bf16 v[168:171], v[0:3], v[176:179], 0\n\t" \
    "ds_read_b128 v[204:207], v249 offset:7168\n\t" \
    "s_waitcnt lgkmcnt(6)\n\t" \
    "v_mfma_f32_16x16x32_bf16 v[168:171], v[4:7], v[180:183], v[168:171]\n\t" \
    "ds_read_b128 v[176:179], v249 offset:8192\n\t" \
    "s_waitcnt lgkmcnt(6)\n\t" \
    "v_mfma_f32_16x16x32_bf16 v[168:171], v[8:11], v[184:187], v[168:171]\n\t" \
    "ds_read_b128 v[180:183], v249 offset:9216\n\t" \
    "s_waitcnt lgkmcnt(6)\n\t" \
    "v_mfma_f32_16x16x32_bf16 v[168:171], v[12:15], v[188:191], v[168:171]\n\t" \
    "ds_read_b128 v[184:187], v249 offset:10240\n\t" \
    "s_add_u32 s47, s46, 0x0\n\t" \
    "buffer_load_dwordx4 v[208:211], v253, %[rs], s47 offen sc1\n\t" \
    "s_add_u32 s47, s46, 0x8000\n\t" \
    "buffer_load_dwordx4 v[212:215], v253, %[rs], s47 offen sc1\n\t" \
    "s_cmp_gt_u32 %[wave], 3\n\t" \
    "s_cbranch_scc1 .Lw114_%=\n\t" \
    "s_add_u32 s47, s46, 0x10000\n\t" \
    "buffer_load_dwordx4 v[216:219], v253, %[rs], s47 offen sc1\n\t" \
    ".Lw114_%=:\n\t" \
    "s_waitcnt lgkmcnt(6)\n\t" \
    "v_mfma_f32_16x16x32_bf16 v[168:171], v[16:19], v[192:195], v[168:171]\n\t" \
    "ds_read_b128 v[188:191], v249 offset:11264\n\t" \
    "s_waitcnt lgkmcnt(6)\n\t" \
    "v_mfma_f32_16x16x32_bf16 v[168:171], v[20:23], v[196:199], v[168:171]\n\t" \
    "ds_read_b128 v[192:195], v249 offset:12288\n\t" \
    "s_waitcnt lgkmcnt(6)\n\t" \
    "v_mfma_f32_16x16x32_bf16 v[168:171], v[24:27], v[200:203], v[168:171]\n\t" \
    "ds_read_b128 v[196:199], v249 offset:13312\n\t" \
    "s_waitcnt lgkmcnt(6)\n\t" \
    "v_mfma_f32_16x16x32_bf16 v[168:171], v[28:31], v[204:207], v[168:171]\n\t" \
    "ds_read_b128 v[200:203], v249 offset:14336\n\t" \
    "s_waitcnt lgkmcnt(6)\n\t" \
    "v_mfma_f32_16x16x32_bf16 v[168:171], v[32:35], v[176:179], v[168:171]\n\t" \
    "ds_read_b128 v[204:207], v249 offset:15360\n\t" \
    "s_waitcnt lgkmcnt(6)\n\t" \
    "v_mfma_f32_16x16x32_bf16 v[168:171], v[36:39], v[180:183], v[168:171]\n\t" \
    "ds_read_b128 v[176:179], v249 offset:16384\n\t" \
    "s_waitcnt lgkmcnt(6)\n\t" \
    "v_mfma_f32_16x16x32_bf16 v[168:171], v[40:43], v[184:187], v[168:171]\n\t" \
    "ds_read_b128 v[180:183], v249 offset:17408\n\t" \
    "s_waitcnt lgkmcnt(6)\n\t" \
    "v_mfma_f32_16x16x32_bf16 v[168:171], v[44:47], v[188:191], v[168:171]\n\t" \
    "ds_read_b128 v[184:187], v249 offset:18432\n\t" \
    "s_waitcnt lgkmcnt(6)\n\t" \
    "v_mfma_f32_16x16x32_bf16 v[168:171], v[48:51], v[192:195], v[168:171]\n\t" \
    "ds_read_b128 v[188:191], v249 offset:19456\n\t" \
    "s_waitcnt lgkmcnt(6)\n\t" \
    "v_mfma_f32_16x16x32_bf16 v[168:171], v[52:55], v[196:199], v[168:171]\n\t" \
    "ds_read_b32 v236, v251 offset:72\n\t" \
    "ds_read_b64 v[238:239], v251 offset:64\n\t" \
    "s_waitcnt lgkmcnt(7)\n\t" \
    "v_mfma_f32_16x16x32_bf16 v[168:171], v[56:59], v[200:203], v[168:171]\n\t" \
    "s_waitcnt lgkmcnt(6)\n\t" \
    "v_mfma_f32_16x16x32_bf16 v[168:171], v[60:63], v[204:207], v[168:171]\n\t" \
    "s_waitcnt lgkmcnt(5)\n\t" \
    "v_mfma_f32_16x16x32_bf16 v[168:171], v[64:67], v[176:179], v[168:171]\n\t" \
    "s_waitcnt lgkmcnt(4)\n\t" \
    "v_mfma_f32_16x16x32_bf16 v[168:171], v[68:71], v[180:183], v[168:171]\n\t" \
    ".Lw115_%=:\n\t" \
    "s_waitcnt lgkmcnt(0)\n\t" \
    "v_cmp_ne_u32 vcc, s40, v236\n\t" \
    "s_cbranch_vccz .Lw116_%=\n\t" \
    "ds_read_b32 v236, v251 offset:72\n\t" \
    "ds_read_b64 v[238:239], v251 offset:64\n\t" \
    "s_branch .Lw115_%=\n\t" \
    ".Lw116_%=:\n\t" \
    "s_waitcnt lgkmcnt(0)\n\t" \
    "v_mul_f32 v240, v160, v239\n\t" \
    "v_mul_f32 v241, v161, v239\n\t" \
    "v_mfma_f32_16x16x32_bf16 v[168:171], v[72:75], v[184:187], v[168:171]\n\t" \
    "v_mul_f32 v242, v162, v239\n\t" \
    "v_mul_f32 v243, v163, v239\n\t" \
    "v_mfma_f32_16x16x32_bf16 v[168:171], v[76:79], v[188:191], v[168:171]\n\t" \
    "v_mov_b32 v237, 0xc0135761\n\t" \
    "s_nop 6\n\t" \
    "v_fma_f32 v168, v168, v238, v240\n\t" \
    "v_fma_f32 v169, v169, v238, v241\n\t" \
    "v_fma_f32 v170, v170, v238, v242\n\t" \
    "v_fma_f32 v171, v171, v238, v243\n\t" \
    "v_mul_f32 v176, v168, v168\n\t" \
    "v_mul_f32 v177, v169, v169\n\t" \
    "v_mul_f32 v178, v170, v170\n\t" \
    "v_mul_f32 v179, v171, v171\n\t" \
    "v_fmamk_f32 v176, v176, 0xbdd2d3e7, v237\n\t" \
    "v_fmamk_f32 v177, v177, 0xbdd2d3e7, v237\n\t" \
    "v_fmamk_f32 v178, v178, 0xbdd2d3e7, v237\n\t" \
    "v_fmamk_f32 v179, v179, 0xbdd2d3e7, v237\n\t" \
    "v_mul_f32 v176, v168, v176\n\t" \
    "v_mul_f32 v177, v169, v177\n\t" \
    "v_mul_f32 v178, v170, v178\n\t" \
    "v_mul_f32 v179, v171, v179\n\t" \
    "v_exp_f32 v176, v176\n\t" \
    "v_exp_f32 v177, v177\n\t" \
    "v_exp_f32 v178, v178\n\t" \
    "v_exp_f32 v179, v179\n\t" \
    "v_add_f32 v176, 1.0, v176\n\t" \
    "v_add_f32 v177, 1.0, v177\n\t" \
    "v_add_f32 v178, 1.0, v178\n\t" \
    "v_add_f32 v179, 1.0, v179\n\t" \
    "v_rcp_f32 v176, v176\n\t" \
    "v_rcp_f32 v177, v177\n\t" \
    "v_rcp_f32 v178, v178\n\t" \
    "v_rcp_f32 v179, v179\n\t" \
    "v_mul_f32 v168, v168, v176\n\t" \
    "v_mul_f32 v169, v169, v177\n\t" \
    "v_mul_f32 v170, v170, v178\n\t" \
    "v_mul_f32 v171, v171, v179\n\t" \
    "s_waitcnt vmcnt(0)\n\t" \
    "v_cvt_pk_bf16_f32 v184, v168, v169\n\t" \
    "v_cvt_pk_bf16_f32 v185, v170, v171\n\t" \
    "v_min_u32 v184, -2, v184\n\t" \
    "v_min_u32 v185, -2, v185\n\t" \
    "s_cmp_eq_u32 s40, s42\n\t" \
    "s_cselect_b64 s[44:45], s[58:59], 0\n\t" \
    "s_andn2_b64 exec, -1, s[44:45]\n\t" \
    "buffer_store_dwordx2 v[184:185], v80, %[rs], s49 offen sc1\n\t" \
    "s_mov_b64 exec, -1\n\t" \
    "s_cmp_ge_u32 s40, 122\n\t" \
    "s_cbranch_scc1 .Lw117_%=\n\t" \
    "s_add_i32 s44, s40, s62\n\t" \
    "s_max_i32 s44, s44, 0\n\t" \
    "s_lshr_b32 s45, s44, 1\n\t" \
    "s_and_b32 s44, s44, 1\n\t" \
    "s_lshl_b32 s44, s44, 12\n\t" \
    "s_mul_i32 s51, s45, 0x14000\n\t" \
    "s_add_u32 s51, s51, s44\n\t" \
    "s_lshr_b32 s45, s45, 4\n\t" \
    "s_mul_i32 s45, s45, 0xc2000\n\t" \
    "s_add_u32 s51, s51, s45\n\t" \
    "s_add_i32 s44, s40, s63\n\t" \
    "s_max_i32 s44, s44, 0\n\t" \
    "s_lshr_b32 s45, s44, 1\n\t" \
    "s_and_b32 s44, s44, 1\n\t" \
    "s_lshl_b32 s44, s44, 12\n\t" \
    "s_mul_i32 s50, s45, 0x14000\n\t" \
    "s_add_u32 s50, s50, s44\n\t" \
    "s_lshr_b32 s45, s45, 4\n\t" \
    "s_mul_i32 s45, s45, 0xc2000\n\t" \
    "s_add_u32 s50, s50, s45\n\t" \
    "s_sub_u32 s50, s50, s51\n\t" \
    "s_add_u32 s51, s51, 0x2400000\n\t" \
    "v_mad_u32_u24 v81, v255, s50, v254\n\t" \
    "v_mov_b64 v[188:189], -1\n\t" \
    "s_add_u32 s44, s40, 6\n\t" \
    "s_cmp_eq_u32 s44, s42\n\t" \
    "s_cselect_b64 s[44:45], s[58:59], 0\n\t" \
    "s_andn2_b64 exec, -1, s[44:45]\n\t" \
    "buffer_store_dwordx2 v[188:189], v81, %[rs], s51 offen sc1\n\t" \
    "s_mov_b64 exec, -1\n\t" \
    ".Lw117_%=:\n\t" \
    "v_xor_b32 v249, 0x8000, v249\n\t" \
    "v_xor_b32 v250, 0x8000, v250\n\t" \
    "v_xor_b32 v251, 0x800, v251\n\t" \
    "v_xor_b32 v252, 0x800, v252\n\t" \
    "s_add_u32 s40, s40, 1\n\t" \
    "s_cmp_lg_u32 s40, 128\n\t" \
    "s_cbranch_scc1 .Lw14_%=\n\t" \
    "s_branch .Lend%=\n\t" \
    ".Lend%=:\n\t" \
    "s_waitcnt vmcnt(0) lgkmcnt(0)\n\t" \
    "s_mov_b32 %[bud], s41\n\t"
#define ASM_W2_LOOP \
    "s_mov_b32 s40, 0\n\t" \
    "s_mov_b32 s41, 0x20000\n\t" \
    "s_add_u32 m0, %[wm], 0x0\n\t" \
    "s_add_u32 s44, %[wo], 0x0\n\t" \
    "buffer_load_dwordx4 v176, %[wr], s44 offen nt lds\n\t" \
    "s_add_u32 m0, %[wm], 0x2000\n\t" \
    "s_add_u32 s44, %[wo], 0xa000\n\t" \
    "buffer_load_dwordx4 v176, %[wr], s44 offen nt lds\n\t" \
    "s_add_u32 m0, %[wm], 0x4000\n\t" \
    "s_add_u32 s44, %[wo], 0x14000\n\t" \
    "buffer_load_dwordx4 v176, %[wr], s44 offen nt lds\n\t" \
    "s_add_u32 m0, %[wm], 0x6000\n\t" \
    "s_add_u32 s44, %[wo], 0x1e000\n\t" \
    "buffer_load_dwordx4 v176, %[wr], s44 offen nt lds\n\t" \
    "s_add_u32 m0, %[wm], 0x8000\n\t" \
    "s_add_u32 s44, %[wo], 0x28000\n\t" \
    "buffer_load_dwordx4 v176, %[wr], s44 offen nt lds\n\t" \
    "s_add_u32 m0, %[wm], 0xa000\n\t" \
    "s_add_u32 s44, %[wo], 0x32000\n\t" \
    "buffer_load_dwordx4 v176, %[wr], s44 offen nt lds\n\t" \
    "s_add_u32 m0, %[wm], 0xc000\n\t" \
    "s_add_u32 s44, %[wo], 0x3c000\n\t" \
    "buffer_load_dwordx4 v176, %[wr], s44 offen nt lds\n\t" \
    "s_add_u32 m0, %[wm], 0xe000\n\t" \
    "s_add_u32 s44, %[wo], 0x46000\n\t" \
    "buffer_load_dwordx4 v176, %[wr], s44 offen nt lds\n\t" \
    "s_add_u32 m0, %[wm], 0x10000\n\t" \
    "s_add_u32 s44, %[wo], 0x50000\n\t" \
    "buffer_load_dwordx4 v176, %[wr], s44 offen nt lds\n\t" \
    "s_add_u32 m0, %[wm], 0x12000\n\t" \
    "s_add_u32 s44, %[wo], 0x5a000\n\t" \
    "buffer_load_dwordx4 v176, %[wr], s44 offen nt lds\n\t" \
    "s_add_u32 m0, %[wm], 0x14000\n\t" \
    "s_add_u32 s44, %[wo], 0x64000\n\t" \
    "buffer_load_dwordx4 v176, %[wr], s44 offen nt lds\n\t" \
    "s_add_u32 m0, %[wm], 0x16000\n\t" \
    "s_add_u32 s44, %[wo], 0x6e000\n\t" \
    "buffer_load_dwordx4 v176, %[wr], s44 offen nt lds\n\t" \
    "s_add_u32 m0, %[wm], 0x18000\n\t" \
    "s_add_u32 s44, %[wo], 0x78000\n\t" \
    "buffer_load_dwordx4 v176, %[wr], s44 offen nt lds\n\t" \
    "s_add_u32 m0, %[wm], 0x1a000\n\t" \
    "s_add_u32 s44, %[wo], 0x82000\n\t" \
    "buffer_load_dwordx4 v176, %[wr], s44 offen nt lds\n\t" \
    "s_add_u32 m0, %[wm], 0x1c000\n\t" \
    "s_add_u32 s44, %[wo], 0x8c000\n\t" \
    "buffer_load_dwordx4 v176, %[wr], s44 offen nt lds\n\t" \
    "s_add_u32 m0, %[wm], 0x1e000\n\t" \
    "s_add_u32 s44, %[wo], 0x96000\n\t" \
    "buffer_load_dwordx4 v176, %[wr], s44 offen nt lds\n\t" \
    "s_add_u32 m0, %[wm], 0x20000\n\t" \
    "s_add_u32 s44, %[wo], 0xa0000\n\t" \
    "buffer_load_dwordx4 v176, %[wr], s44 offen nt lds\n\t" \
    "s_add_u32 m0, %[wm], 0x22000\n\t" \
    "s_add_u32 s44, %[wo], 0xaa000\n\t" \
    "buffer_load_dwordx4 v176, %[wr], s44 offen nt lds\n\t" \
    "s_add_u32 m0, %[wm], 0x24000\n\t" \
    "s_add_u32 s44, %[wo], 0xb4000\n\t" \
    "buffer_load_dwordx4 v176, %[wr], s44 offen nt lds\n\t" \
    "s_add_u32 m0, %[wm], 0x26000\n\t" \
    "s_add_u32 s44, %[wo], 0xbe000\n\t" \
    "buffer_load_dwordx4 v176, %[wr], s44 offen nt lds\n\t" \
    "s_waitcnt vmcnt(0) lgkmcnt(0)\n\t" \
    "s_barrier \n\t" \
    "v_add_u32 v178, 0x0, v177\n\t" \
    "ds_read_b64_tr_b16 v[0:1], v178 offset:0\n\t" \
    "ds_read_b64_tr_b16 v[2:3], v178 offset:1024\n\t" \
    "ds_read_b64_tr_b16 v[4:5], v178 offset:8192\n\t" \
    "ds_read_b64_tr_b16 v[6:7], v178 offset:9216\n\t" \
    "ds_read_b64_tr_b16 v[8:9], v178 offset:16384\n\t" \
    "ds_read_b64_tr_b16 v[10:11], v178 offset:17408\n\t" \
    "ds_read_b64_tr_b16 v[12:13], v178 offset:24576\n\t" \
    "ds_read_b64_tr_b16 v[14:15], v178 offset:25600\n\t" \
    "v_add_u32 v178, 0x8000, v177\n\t" \
    "ds_read_b64_tr_b16 v[16:17], v178 offset:0\n\t" \
    "ds_read_b64_tr_b16 v[18:19], v178 offset:1024\n\t" \
    "ds_read_b64_tr_b16 v[20:21], v178 offset:8192\n\t" \
    "ds_read_b64_tr_b16 v[22:23], v178 offset:9216\n\t" \
    "ds_read_b64_tr_b16 v[24:25], v178 offset:16384\n\t" \
    "ds_read_b64_tr_b16 v[26:27], v178 offset:17408\n\t" \
    "ds_read_b64_tr_b16 v[28:29], v178 offset:24576\n\t" \
    "ds_read_b64_tr_b16 v[30:31], v178 offset:25600\n\t" \
    "v_add_u32 v178, 0x10000, v177\n\t" \
    "ds_read_b64_tr_b16 v[32:33], v178 offset:0\n\t" \
    "ds_read_b64_tr_b16 v[34:35], v178 offset:1024\n\t" \
    "ds_read_b64_tr_b16 v[36:37], v178 offset:8192\n\t" \
    "ds_read_b64_tr_b16 v[38:39], v178 offset:9216\n\t" \
    "ds_read_b64_tr_b16 v[40:41], v178 offset:16384\n\t" \
    "ds_read_b64_tr_b16 v[42:43], v178 offset:17408\n\t" \
    "ds_read_b64_tr_b16 v[44:45], v178 offset:24576\n\t" \
    "ds_read_b64_tr_b16 v[46:47], v178 offset:25600\n\t" \
    "v_add_u32 v178, 0x18000, v177\n\t" \
    "ds_read_b64_tr_b16 v[48:49], v178 offset:0\n\t" \
    "ds_read_b64_tr_b16 v[50:51], v178 offset:1024\n\t" \
    "ds_read_b64_tr_b16 v[52:53], v178 offset:8192\n\t" \
    "ds_read_b64_tr_b16 v[54:55], v178 offset:9216\n\t" \
    "ds_read_b64_tr_b16 v[56:57], v178 offset:16384\n\t" \
    "ds_read_b64_tr_b16 v[58:59], v178 offset:17408\n\t" \
    "ds_read_b64_tr_b16 v[60:61], v178 offset:24576\n\t" \
    "ds_read_b64_tr_b16 v[62:63], v178 offset:25600\n\t" \
    "v_add_u32 v178, 0x20000, v177\n\t" \
    "ds_read_b64_tr_b16 v[64:65], v178 offset:0\n\t" \
    "ds_read_b64_tr_b16 v[66:67], v178 offset:1024\n\t" \
    "ds_read_b64_tr_b16 v[68:69], v178 offset:8192\n\t" \
    "ds_read_b64_tr_b16 v[70:71], v178 offset:9216\n\t" \
    "ds_read_b64_tr_b16 v[72:73], v178 offset:16384\n\t" \
    "ds_read_b64_tr_b16 v[74:75], v178 offset:17408\n\t" \
    "ds_read_b64_tr_b16 v[76:77], v178 offset:24576\n\t" \
    "ds_read_b64_tr_b16 v[78:79], v178 offset:25600\n\t" \
    "v_mov_b32 v228, 0x3f803f80\n\t" \
    "v_mov_b32 v229, 0x3f803f80\n\t" \
    "v_mov_b32 v230, 0x3f803f80\n\t" \
    "v_mov_b32 v231, 0x3f803f80\n\t" \
    "v_mov_b64 v[216:217], 0\n\t" \
    "v_mov_b32 v219, 0\n\t" \
    "v_bfe_u32 v255, v253, 2, 5\n\t" \
    "v_add_u32 v255, 0x7000, v255\n\t" \
    "v_mov_b32 v218, 0\n\t" \
    "s_waitcnt lgkmcnt(0)\n\t" \
    "s_barrier \n\t" \
    "ds_write_b32 v255, v218\n\t" \
    "s_waitcnt lgkmcnt(0)\n\t" \
    "s_barrier \n\t" \
    ".Lw21_%=:\n\t" \
    "v_mov_b32 v248, 0\n\t" \
    "buffer_load_dword v248, v248, %[rs], %[go] offen sc1\n\t" \
    "s_waitcnt vmcnt(0)\n\t" \
    "v_cmp_ne_u32 vcc, 0, v248\n\t" \
    "s_cbranch_vccnz .Lw22_%=\n\t" \
    "s_cmp_eq_u32 s41, 0\n\t" \
    "s_cbranch_scc1 .Lw22_%=\n\t" \
    "s_sub_u32 s41, s41, 1\n\t" \
    "s_sleep 16\n\t" \
    "s_branch .Lw21_%=\n\t" \
    ".Lw22_%=:\n\t" \
    "s_lshr_b32 s48, %[wave], 2\n\t" \
    "s_mul_i32 s48, s48, 0x3000\n\t" \
    "s_add_u32 s48, s48, %[pi]\n\t" \
    "s_mov_b32 s43, 0\n\t" \
    "s_add_u32 s46, s43, s48\n\t" \
    "s_add_u32 s47, s46, 0x0\n\t" \
    "buffer_load_dwordx4 v[208:211], v253, %[rs], s47 offen sc1\n\t" \
    "s_add_u32 s47, s46, 0x8000\n\t" \
    "buffer_load_dwordx4 v[212:215], v253, %[rs], s47 offen sc1\n\t" \
    "s_cmp_gt_u32 %[wave], 3\n\t" \
    "s_cbranch_scc1 .Lw23_%=\n\t" \
    "s_add_u32 s47, s46, 0x10000\n\t" \
    "buffer_load_dwordx4 v[216:219], v253, %[rs], s47 offen sc1\n\t" \
    ".Lw23_%=:\n\t" \
    "s_waitcnt vmcnt(0)\n\t" \
    ".Lw24_%=:\n\t" \
    "s_add_u32 s49, s43, 0x2c00000\n\t" \
    "s_add_u32 s50, s43, 0x1c00000\n\t" \
    "s_lshr_b32 s44, s40, 6\n\t" \
    "s_lshl_b32 s44, s44, 14\n\t" \
    "s_and_b32 s45, s40, 63\n\t" \
    "s_lshl_b32 s45, s45, 7\n\t" \
    "s_add_u32 s44, s44, s45\n\t" \
    "s_add_u32 s51, s44, 0x4d0000\n\t" \
    "s_add_i32 s44, s40, 1\n\t" \
    "s_max_i32 s44, s44, 0\n\t" \
    "s_lshr_b32 s45, s44, 1\n\t" \
    "s_and_b32 s44, s44, 1\n\t" \
    "s_lshl_b32 s44, s44, 12\n\t" \
    "s_mul_i32 s43, s45, 0x14000\n\t" \
    "s_add_u32 s43, s43, s44\n\t" \
    "s_lshr_b32 s45, s45, 4\n\t" \
    "s_mul_i32 s45, s45, 0xc2000\n\t" \
    "s_add_u32 s43, s43, s45\n\t" \
    "s_setprio 2\n\t" \
    "v_max_u32 v248, v208, v209\n\t" \
    "v_max3_u32 v248, v210, v211, v248\n\t" \
    "v_max3_u32 v248, v212, v213, v248\n\t" \
    "v_max3_u32 v248, v214, v215, v248\n\t" \
    "v_max3_u32 v248, v216, v217, v248\n\t" \
    "v_max3_u32 v248, v218, v219, v248\n\t" \
    "v_cmp_eq_u32 vcc, -1, v248\n\t" \
    "s_cbranch_vccz .Lw26_%=\n\t" \
    "s_and_b32 s44, s40, 1\n\t" \
    "s_cmp_eq_u32 s44, 0\n\t" \
    "s_cbranch_scc1 .Lw25_%=\n\t" \
    "s_cmp_gt_u32 s40, 123\n\t" \
    "s_cbranch_scc1 .Lw25_%=\n\t" \
    "s_sleep 6\n\t" \
    ".Lw25_%=:\n\t" \
    "v_max_u32 v248, v208, v209\n\t" \
    "v_max3_u32 v248, v210, v211, v248\n\t" \
    "v_cmp_eq_u32 vcc, -1, v248\n\t" \
    "s_cbranch_vccz .Lw28_%=\n\t" \
    "s_add_u32 s47, s46, 0x0\n\t" \
    "buffer_load_dwordx4 v[208:211], v253, %[rs], s47 offen sc1\n\t" \
    ".Lw28_%=:\n\t" \
    "v_max_u32 v248, v212, v213\n\t" \
    "v_max3_u32 v248, v214, v215, v248\n\t" \
    "v_cmp_eq_u32 vcc, -1, v248\n\t" \
    "s_cbranch_vccz .Lw29_%=\n\t" \
    "s_add_u32 s47, s46, 0x8000\n\t" \
    "buffer_load_dwordx4 v[212:215], v253, %[rs], s47 offen sc1\n\t" \
    ".Lw29_%=:\n\t" \
    "v_max_u32 v248, v216, v217\n\t" \
    "v_max3_u32 v248, v218, v219, v248\n\t" \
    "v_cmp_eq_u32 vcc, -1, v248\n\t" \
    "s_cbranch_vccz .Lw210_%=\n\t" \
    "s_add_u32 s47, s46, 0x10000\n\t" \
    "buffer_load_dwordx4 v[216:219], v253, %[rs], s47 offen sc1\n\t" \
    ".Lw210_%=:\n\t" \
    "s_waitcnt vmcnt(0)\n\t" \
    "v_max_u32 v248, v208, v209\n\t" \
    "v_max3_u32 v248, v210, v211, v248\n\t" \
    "v_max3_u32 v248, v212, v213, v248\n\t" \
    "v_max3_u32 v248, v214, v215, v248\n\t" \
    "v_max3_u32 v248, v216, v217, v248\n\t" \
    "v_max3_u32 v248, v218, v219, v248\n\t" \
    "v_cmp_eq_u32 vcc, -1, v248\n\t" \
    "s_cbranch_vccz .Lw26_%=\n\t" \
    "s_cmp_eq_u32 s41, 0\n\t" \
    "s_cbranch_scc1 .Lw26_%=\n\t" \
    "s_sub_u32 s41, s41, 1\n\t" \
    "s_branch .Lw25_%=\n\t" \
    ".Lw26_%=:\n\t" \
    "ds_write_b128 v250, v[208:211] offset:0\n\t" \
    "ds_write_b128 v250, v[212:215] offset:8192\n\t" \
    "s_cmp_gt_u32 %[wave], 3\n\t" \
    "s_cbranch_scc1 .Lw211_%=\n\t" \
    "ds_write_b128 v250, v[216:219] offset:16384\n\t" \
    ".Lw211_%=:\n\t" \
    "s_add_u32 s46, s43, s48\n\t" \
    "s_waitcnt lgkmcnt(0)\n\t" \
    "s_barrier \n\t" \
    "s_setprio 0\n\t" \
    "ds_read_b128 v[176:179], v249 offset:0\n\t" \
    "buffer_load_dwordx2 v[232:233], v254, %[rs], s50 offen sc1\n\t" \
    "buffer_load_dwordx2 v[234:235], v251, %[rs], s51 offen sc1\n\t" \
    "ds_read_b128 v[180:183], v249 offset:1024\n\t" \
    "ds_read_b128 v[184:187], v249 offset:2048\n\t" \
    "ds_read_b128 v[188:191], v249 offset:3072\n\t" \
    "ds_read_b128 v[192:195], v249 offset:4096\n\t" \
    "ds_read_b128 v[196:199], v249 offset:5120\n\t" \
    "ds_read_b128 v[200:203], v249 offset:6144\n\t" \
    "s_waitcnt lgkmcnt(6)\n\t" \
    "v_mfma_f32_16x16x32_bf16 v[168:171], v[0:3], v[176:179], 0\n\t" \
    "ds_read_b128 v[204:207], v249 offset:7168\n\t" \
    "s_waitcnt lgkmcnt(6)\n\t" \
    "v_mfma_f32_16x16x32_bf16 v[168:171], v[4:7], v[180:183], v[168:171]\n\t" \
    "ds_read_b128 v[176:179], v249 offset:8192\n\t" \
    "s_waitcnt lgkmcnt(6)\n\t" \
    "v_mfma_f32_16x16x32_bf16 v[168:171], v[8:11], v[184:187], v[168:171]\n\t" \
    "ds_read_b128 v[180:183], v249 offset:9216\n\t" \
    "s_waitcnt lgkmcnt(6)\n\t" \
    "v_mfma_f32_16x16x32_bf16 v[168:171], v[12:15], v[188:191], v[168:171]\n\t" \
    "ds_read_b128 v[184:187], v249 offset:10240\n\t" \
    "s_add_u32 s47, s46, 0x0\n\t" \
    "buffer_load_dwordx4 v[208:211], v253, %[rs], s47 offen sc1\n\t" \
    "s_add_u32 s47, s46, 0x8000\n\t" \
    "buffer_load_dwordx4 v[212:215], v253, %[rs], s47 offen sc1\n\t" \
    "s_cmp_gt_u32 %[wave], 3\n\t" \
    "s_cbranch_scc1 .Lw212_%=\n\t" \
    "s_add_u32 s47, s46, 0x10000\n\t" \
    "buffer_load_dwordx4 v[216:219], v253, %[rs], s47 offen sc1\n\t" \
    ".Lw212_%=:\n\t" \
    "s_waitcnt lgkmcnt(6)\n\t" \
    "v_mfma_f32_16x16x32_bf16 v[168:171], v[16:19], v[192:195], v[168:171]\n\t" \
    "ds_read_b128 v[188:191], v249 offset:11264\n\t" \
    "s_waitcnt lgkmcnt(6)\n\t" \
    "v_mfma_f32_16x16x32_bf16 v[168:171], v[20:23], v[196:199], v[168:171]\n\t" \
    "ds_read_b128 v[192:195], v249 offset:12288\n\t" \
    "s_waitcnt lgkmcnt(6)\n\t" \
    "v_mfma_f32_16x16x32_bf16 v[168:171], v[24:27], v[200:203], v[168:171]\n\t" \
    "ds_read_b128 v[196:199], v249 offset:13312\n\t" \
    "s_waitcnt lgkmcnt(6)\n\t" \
    "v_mfma_f32_16x16x32_bf16 v[168:171], v[28:31], v[204:207], v[168:171]\n\t" \
    "ds_read_b128 v[200:203], v249 offset:14336\n\t" \
    "s_waitcnt lgkmcnt(6)\n\t" \
    "v_mfma_f32_16x16x32_bf16 v[168:171], v[32:35], v[176:179], v[168:171]\n\t" \
    "ds_read_b128 v[204:207], v249 offset:15360\n\t" \
    "s_waitcnt lgkmcnt(6)\n\t" \
    "v_mfma_f32_16x16x32_bf16 v[168:171], v[36:39], v[180:183], v[168:171]\n\t" \
    "ds_read_b128 v[176:179], v249 offset:16384\n\t" \
    "s_waitcnt lgkmcnt(6)\n\t" \
    "v_mfma_f32_16x16x32_bf16 v[168:171], v[40:43], v[184:187], v[168:171]\n\t" \
    "ds_read_b128 v[180:183], v249 offset:17408\n\t" \
    "s_waitcnt lgkmcnt(6)\n\t" \
    "v_mfma_f32_16x16x32_bf16 v[168:171], v[44:47], v[188:191], v[168:171]\n\t" \
    "ds_read_b128 v[184:187], v249 offset:18432\n\t" \
    "s_waitcnt lgkmcnt(6)\n\t" \
    "v_mfma_f32_16x16x32_bf16 v[168:171], v[48:51], v[192:195], v[168:171]\n\t" \
    "ds_read_b128 v[188:191], v249 offset:19456\n\t" \
    "s_waitcnt lgkmcnt(6)\n\t" \
    "v_mfma_f32_16x16x32_bf16 v[168:171], v[52:55], v[196:199], v[168:171]\n\t" \
    "s_waitcnt lgkmcnt(5)\n\t" \
    "v_mfma_f32_16x16x32_bf16 v[168:171], v[56:59], v[200:203], v[168:171]\n\t" \
    "s_waitcnt lgkmcnt(4)\n\t" \
    "v_mfma_f32_16x16x32_bf16 v[168:171], v[60:63], v[204:207], v[168:171]\n\t" \
    "s_waitcnt lgkmcnt(3)\n\t" \
    "v_mfma_f32_16x16x32_bf16 v[168:171], v[64:67], v[176:179], v[168:171]\n\t" \
    "s_waitcnt lgkmcnt(2)\n\t" \
    "v_mfma_f32_16x16x32_bf16 v[168:171], v[68:71], v[180:183], v[168:171]\n\t" \
    "s_waitcnt lgkmcnt(1)\n\t" \
    "v_mfma_f32_16x16x32_bf16 v[168:171], v[72:75], v[184:187], v[168:171]\n\t" \
    "s_waitcnt lgkmcnt(0)\n\t" \
    "v_mfma_f32_16x16x32_bf16 v[168:171], v[76:79], v[188:191], v[168:171]\n\t" \
    "s_waitcnt vmcnt(0)\n\t" \
    ".Lw213_%=:\n\t" \
    "v_max_u32 v248, v234, v235\n\t" \
    "v_cmp_eq_u32 vcc, -1, v248\n\t" \
    "s_cbranch_vccz .Lw214_%=\n\t" \
    "s_cmp_eq_u32 s41, 0\n\t" \
    "s_cbranch_scc1 .Lw214_%=\n\t" \
    "s_sub_u32 s41, s41, 1\n\t" \
    "buffer_load_dwordx2 v[234:235], v251, %[rs], s51 offen sc1\n\t" \
    "s_waitcnt vmcnt(0)\n\t" \
    "s_branch .Lw213_%=\n\t" \
    ".Lw214_%=:\n\t" \
    "v_lshlrev_b32 v176, 16, v232\n\t" \
    "v_and_b32 v177, 0xffff0000, v232\n\t" \
    "v_lshlrev_b32 v178, 16, v233\n\t" \
    "v_and_b32 v179, 0xffff0000, v233\n\t" \
    "v_sub_f32 v176, v176, v234\n\t" \
    "v_sub_f32 v177, v177, v234\n\t" \
    "v_sub_f32 v178, v178, v234\n\t" \
    "v_sub_f32 v179, v179, v234\n\t" \
    "v_mul_f32 v176, v176, v235\n\t" \
    "v_mul_f32 v177, v177, v235\n\t" \
    "v_mul_f32 v178, v178, v235\n\t" \
    "v_mul_f32 v179, v179, v235\n\t" \
    "v_cvt_pk_bf16_f32 v180, v176, v177\n\t" \
    "v_cvt_pk_bf16_f32 v181, v178, v179\n\t" \
    "v_lshlrev_b32 v176, 16, v180\n\t" \
    "v_and_b32 v177, 0xffff0000, v180\n\t" \
    "v_lshlrev_b32 v178, 16, v181\n\t" \
    "v_and_b32 v179, 0xffff0000, v181\n\t" \
    "v_add_f32 v168, v176, v168\n\t" \
    "v_add_f32 v169, v177, v169\n\t" \
    "v_add_f32 v170, v178, v170\n\t" \
    "v_add_f32 v171, v179, v171\n\t" \
    "v_cvt_pk_bf16_f32 v184, v168, v169\n\t" \
    "v_cvt_pk_bf16_f32 v185, v170, v171\n\t" \
    "v_min_u32 v184, -2, v184\n\t" \
    "v_min_u32 v185, -2, v185\n\t" \
    "buffer_store_dwordx2 v[184:185], v254, %[rs], s49 offen sc1\n\t" \
    "s_cmp_ge_u32 s40, 122\n\t" \
    "s_cbranch_scc1 .Lw215_%=\n\t" \
    "s_add_i32 s44, s40, 6\n\t" \
    "s_max_i32 s44, s44, 0\n\t" \
    "s_lshr_b32 s45, s44, 1\n\t" \
    "s_and_b32 s44, s44, 1\n\t" \
    "s_lshl_b32 s44, s44, 12\n\t" \
    "s_mul_i32 s50, s45, 0x14000\n\t" \
    "s_add_u32 s50, s50, s44\n\t" \
    "s_lshr_b32 s45, s45, 4\n\t" \
    "s_mul_i32 s45, s45, 0xc2000\n\t" \
    "s_add_u32 s50, s50, s45\n\t" \
    "s_add_u32 s50, s50, 0x2c00000\n\t" \
    "v_mov_b64 v[188:189], -1\n\t" \
    "buffer_store_dwordx2 v[188:189], v254, %[rs], s50 offen sc1\n\t" \
    ".Lw215_%=:\n\t" \
    "v_xor_b32 v249, 0x8000, v249\n\t" \
    "v_xor_b32 v250, 0x8000, v250\n\t" \
    "s_add_u32 s40, s40, 1\n\t" \
    "s_cmp_lg_u32 s40, 128\n\t" \
    "s_cbranch_scc1 .Lw24_%=\n\t" \
    "s_branch .Lend%=\n\t" \
    ".Lend%=:\n\t" \
    "s_waitcnt vmcnt(0) lgkmcnt(0)\n\t" \
    "s_mov_b32 %[bud], s41\n\t"
#define ASM_W3_LOOP \
    "s_mov_b32 s40, 0\n\t" \
    "s_mov_b32 s41, 0x20000\n\t" \
    "s_add_u32 m0, %[wm], 0x0\n\t" \
    "s_add_u32 s44, %[wo], 0x0\n\t" \
    "buffer_load_dwordx4 v176, %[wr], s44 offen nt lds\n\t" \
    "s_add_u32 m0, %[wm], 0x2000\n\t" \
    "s_add_u32 s44, %[wo], 0xc000\n\t" \
    "buffer_load_dwordx4 v176, %[wr], s44 offen nt lds\n\t" \
    "s_add_u32 m0, %[wm], 0x4000\n\t" \
    "s_add_u32 s44, %[wo], 0x18000\n\t" \
    "buffer_load_dwordx4 v176, %[wr], s44 offen nt lds\n\t" \
    "s_add_u32 m0, %[wm], 0x6000\n\t" \
    "s_add_u32 s44, %[wo], 0x24000\n\t" \
    "buffer_load_dwordx4 v176, %[wr], s44 offen nt lds\n\t" \
    "s_add_u32 m0, %[wm], 0x8000\n\t" \
    "s_add_u32 s44, %[wo], 0x30000\n\t" \
    "buffer_load_dwordx4 v176, %[wr], s44 offen nt lds\n\t" \
    "s_add_u32 m0, %[wm], 0xa000\n\t" \
    "s_add_u32 s44, %[wo], 0x3c000\n\t" \
    "buffer_load_dwordx4 v176, %[wr], s44 offen nt lds\n\t" \
    "s_add_u32 m0, %[wm], 0xc000\n\t" \
    "s_add_u32 s44, %[wo], 0x48000\n\t" \
    "buffer_load_dwordx4 v176, %[wr], s44 offen nt lds\n\t" \
    "s_add_u32 m0, %[wm], 0xe000\n\t" \
    "s_add_u32 s44, %[wo], 0x54000\n\t" \
    "buffer_load_dwordx4 v176, %[wr], s44 offen nt lds\n\t" \
    "s_add_u32 m0, %[wm], 0x10000\n\t" \
    "s_add_u32 s44, %[wo], 0x60000\n\t" \
    "buffer_load_dwordx4 v176, %[wr], s44 offen nt lds\n\t" \
    "s_add_u32 m0, %[wm], 0x12000\n\t" \
    "s_add_u32 s44, %[wo], 0x6c000\n\t" \
    "buffer_load_dwordx4 v176, %[wr], s44 offen nt lds\n\t" \
    "s_add_u32 m0, %[wm], 0x14000\n\t" \
    "s_add_u32 s44, %[wo], 0x78000\n\t" \
    "buffer_load_dwordx4 v176, %[wr], s44 offen nt lds\n\t" \
    "s_add_u32 m0, %[wm], 0x16000\n\t" \
    "s_add_u32 s44, %[wo], 0x84000\n\t" \
    "buffer_load_dwordx4 v176, %[wr], s44 offen nt lds\n\t" \
    "s_add_u32 m0, %[wm], 0x18000\n\t" \
    "s_add_u32 s44, %[wo], 0x90000\n\t" \
    "buffer_load_dwordx4 v176, %[wr], s44 offen nt lds\n\t" \
    "s_add_u32 m0, %[wm], 0x1a000\n\t" \
    "s_add_u32 s44, %[wo], 0x9c000\n\t" \
    "buffer_load_dwordx4 v176, %[wr], s44 offen nt lds\n\t" \
    "s_add_u32 m0, %[wm], 0x1c000\n\t" \
    "s_add_u32 s44, %[wo], 0xa8000\n\t" \
    "buffer_load_dwordx4 v176, %[wr], s44 offen nt lds\n\t" \
    "s_add_u32 m0, %[wm], 0x1e000\n\t" \
    "s_add_u32 s44, %[wo], 0xb4000\n\t" \
    "buffer_load_dwordx4 v176, %[wr], s44 offen nt lds\n\t" \
    "s_add_u32 m0, %[wm], 0x20000\n\t" \
    "s_add_u32 s44, %[wo], 0xc0000\n\t" \
    "buffer_load_dwordx4 v176, %[wr], s44 offen nt lds\n\t" \
    "s_add_u32 m0, %[wm], 0x22000\n\t" \
    "s_add_u32 s44, %[wo], 0xcc000\n\t" \
    "buffer_load_dwordx4 v176, %[wr], s44 offen nt lds\n\t" \
    "s_add_u32 m0, %[wm], 0x24000\n\t" \
    "s_add_u32 s44, %[wo], 0xd8000\n\t" \
    "buffer_load_dwordx4 v176, %[wr], s44 offen nt lds\n\t" \
    "s_add_u32 m0, %[wm], 0x26000\n\t" \
    "s_add_u32 s44, %[wo], 0xe4000\n\t" \
    "buffer_load_dwordx4 v176, %[wr], s44 offen nt lds\n\t" \
    "s_waitcnt vmcnt(0) lgkmcnt(0)\n\t" \
    "s_barrier \n\t" \
    "v_add_u32 v178, 0x0, v177\n\t" \
    "v_xor_b32 v179, 32, v178\n\t" \
    "ds_read_b64_tr_b16 v[0:1], v178 offset:0\n\t" \
    "ds_read_b64_tr_b16 v[2:3], v178 offset:2048\n\t" \
    "ds_read_b64_tr_b16 v[80:81], v179 offset:0\n\t" \
    "ds_read_b64_tr_b16 v[82:83], v179 offset:2048\n\t" \
    "ds_read_b64_tr_b16 v[4:5], v178 offset:16384\n\t" \
    "ds_read_b64_tr_b16 v[6:7], v178 offset:18432\n\t" \
    "ds_read_b64_tr_b16 v[84:85], v179 offset:16384\n\t" \
    "ds_read_b64_tr_b16 v[86:87], v179 offset:18432\n\t" \
    "v_add_u32 v178, 0x8000, v177\n\t" \
    "v_xor_b32 v179, 32, v178\n\t" \
    "ds_read_b64_tr_b16 v[8:9], v178 offset:0\n\t" \
    "ds_read_b64_tr_b16 v[10:11], v178 offset:2048\n\t" \
    "ds_read_b64_tr_b16 v[88:89], v179 offset:0\n\t" \
    "ds_read_b64_tr_b16 v[90:91], v179 offset:2048\n\t" \
    "ds_read_b64_tr_b16 v[12:13], v178 offset:16384\n\t" \
    "ds_read_b64_tr_b16 v[14:15], v178 offset:18432\n\t" \
    "ds_read_b64_tr_b16 v[92:93], v179 offset:16384\n\t" \
    "ds_read_b64_tr_b16 v[94:95], v179 offset:18432\n\t" \
    "v_add_u32 v178, 0x10000, v177\n\t" \
    "v_xor_b32 v179, 32, v178\n\t" \
    "ds_read_b64_tr_b16 v[16:17], v178 offset:0\n\t" \
    "ds_read_b64_tr_b16 v[18:19], v178 offset:2048\n\t" \
    "ds_read_b64_tr_b16 v[96:97], v179 offset:0\n\t" \
    "ds_read_b64_tr_b16 v[98:99], v179 offset:2048\n\t" \
    "ds_read_b64_tr_b16 v[20:21], v178 offset:16384\n\t" \
    "ds_read_b64_tr_b16 v[22:23], v178 offset:18432\n\t" \
    "ds_read_b64_tr_b16 v[100:101], v179 offset:16384\n\t" \
    "ds_read_b64_tr_b16 v[102:103], v179 offset:18432\n\t" \
    "v_add_u32 v178, 0x18000, v177\n\t" \
    "v_xor_b32 v179, 32, v178\n\t" \
    "ds_read_b64_tr_b16 v[24:25], v178 offset:0\n\t" \
    "ds_read_b64_tr_b16 v[26:27], v178 offset:2048\n\t" \
    "ds_read_b64_tr_b16 v[104:105], v179 offset:0\n\t" \
    "ds_read_b64_tr_b16 v[106:107], v179 offset:2048\n\t" \
    "ds_read_b64_tr_b16 v[28:29], v178 offset:16384\n\t" \
    "ds_read_b64_tr_b16 v[30:31], v178 offset:18432\n\t" \
    "ds_read_b64_tr_b16 v[108:109], v179 offset:16384\n\t" \
    "ds_read_b64_tr_b16 v[110:111], v179 offset:18432\n\t" \
    "v_add_u32 v178, 0x20000, v177\n\t" \
    "v_xor_b32 v179, 32, v178\n\t" \
    "ds_read_b64_tr_b16 v[32:33], v178 offset:0\n\t" \
    "ds_read_b64_tr_b16 v[34:35], v178 offset:2048\n\t" \
    "ds_read_b64_tr_b16 v[112:113], v179 offset:0\n\t" \
    "ds_read_b64_tr_b16 v[114:115], v179 offset:2048\n\t" \
    "ds_read_b64_tr_b16 v[36:37], v178 offset:16384\n\t" \
    "ds_read_b64_tr_b16 v[38:39], v178 offset:18432\n\t" \
    "ds_read_b64_tr_b16 v[116:117], v179 offset:16384\n\t" \
    "ds_read_b64_tr_b16 v[118:119], v179 offset:18432\n\t" \
    "s_waitcnt lgkmcnt(0)\n\t" \
    "s_barrier \n\t" \
    "s_add_u32 m0, %[wm], 0x0\n\t" \
    "s_add_u32 s44, %[wo], 0xf0000\n\t" \
    "buffer_load_dwordx4 v176, %[wr], s44 offen nt lds\n\t" \
    "s_add_u32 m0, %[wm], 0x2000\n\t" \
    "s_add_u32 s44, %[wo], 0xfc000\n\t" \
    "buffer_load_dwordx4 v176, %[wr], s44 offen nt lds\n\t" \
    "s_add_u32 m0, %[wm], 0x4000\n\t" \
    "s_add_u32 s44, %[wo], 0x108000\n\t" \
    "buffer_load_dwordx4 v176, %[wr], s44 offen nt lds\n\t" \
    "s_add_u32 m0, %[wm], 0x6000\n\t" \
    "s_add_u32 s44, %[wo], 0x114000\n\t" \
    "buffer_load_dwordx4 v176, %[wr], s44 offen nt lds\n\t" \
    "s_add_u32 m0, %[wm], 0x8000\n\t" \
    "s_add_u32 s44, %[wo], 0x120000\n\t" \
    "buffer_load_dwordx4 v176, %[wr], s44 offen nt lds\n\t" \
    "s_add_u32 m0, %[wm], 0xa000\n\t" \
    "s_add_u32 s44, %[wo], 0x12c000\n\t" \
    "buffer_load_dwordx4 v176, %[wr], s44 offen nt lds\n\t" \
    "s_add_u32 m0, %[wm], 0xc000\n\t" \
    "s_add_u32 s44, %[wo], 0x138000\n\t" \
    "buffer_load_dwordx4 v176, %[wr], s44 offen nt lds\n\t" \
    "s_add_u32 m0, %[wm], 0xe000\n\t" \
    "s_add_u32 s44, %[wo], 0x144000\n\t" \
    "buffer_load_dwordx4 v176, %[wr], s44 offen nt lds\n\t" \
    "s_add_u32 m0, %[wm], 0x10000\n\t" \
    "s_add_u32 s44, %[wo], 0x150000\n\t" \
    "buffer_load_dwordx4 v176, %[wr], s44 offen nt lds\n\t" \
    "s_add_u32 m0, %[wm], 0x12000\n\t" \
    "s_add_u32 s44, %[wo], 0x15c000\n\t" \
    "buffer_load_dwordx4 v176, %[wr], s44 offen nt lds\n\t" \
    "s_add_u32 m0, %[wm], 0x14000\n\t" \
    "s_add_u32 s44, %[wo], 0x168000\n\t" \
    "buffer_load_dwordx4 v176, %[wr], s44 offen nt lds\n\t" \
    "s_add_u32 m0, %[wm], 0x16000\n\t" \
    "s_add_u32 s44, %[wo], 0x174000\n\t" \
    "buffer_load_dwordx4 v176, %[wr], s44 offen nt lds\n\t" \
    "s_add_u32 m0, %[wm], 0x18000\n\t" \
    "s_add_u32 s44, %[wo], 0x180000\n\t" \
    "buffer_load_dwordx4 v176, %[wr], s44 offen nt lds\n\t" \
    "s_add_u32 m0, %[wm], 0x1a000\n\t" \
    "s_add_u32 s44, %[wo], 0x18c000\n\t" \
    "buffer_load_dwordx4 v176, %[wr], s44 offen nt lds\n\t" \
    "s_add_u32 m0, %[wm], 0x1c000\n\t" \
    "s_add_u32 s44, %[wo], 0x198000\n\t" \
    "buffer_load_dwordx4 v176, %[wr], s44 offen nt lds\n\t" \
    "s_add_u32 m0, %[wm], 0x1e000\n\t" \
    "s_add_u32 s44, %[wo], 0x1a4000\n\t" \
    "buffer_load_dwordx4 v176, %[wr], s44 offen nt lds\n\t" \
    "s_add_u32 m0, %[wm], 0x20000\n\t" \
    "s_add_u32 s44, %[wo], 0x1b0000\n\t" \
    "buffer_load_dwordx4 v176, %[wr], s44 offen nt lds\n\t" \
    "s_add_u32 m0, %[wm], 0x22000\n\t" \
    "s_add_u32 s44, %[wo], 0x1bc000\n\t" \
    "buffer_load_dwordx4 v176, %[wr], s44 offen nt lds\n\t" \
    "s_add_u32 m0, %[wm], 0x24000\n\t" \
    "s_add_u32 s44, %[wo], 0x1c8000\n\t" \
    "buffer_load_dwordx4 v176, %[wr], s44 offen nt lds\n\t" \
    "s_add_u32 m0, %[wm], 0x26000\n\t" \
    "s_add_u32 s44, %[wo], 0x1d4000\n\t" \
    "buffer_load_dwordx4 v176, %[wr], s44 offen nt lds\n\t" \
    "s_waitcnt vmcnt(0) lgkmcnt(0)\n\t" \
    "s_barrier \n\t" \
    "v_add_u32 v178, 0x0, v177\n\t" \
    "v_xor_b32 v179, 32, v178\n\t" \
    "ds_read_b64_tr_b16 v[40:41], v178 offset:0\n\t" \
    "ds_read_b64_tr_b16 v[42:43], v178 offset:2048\n\t" \
    "ds_read_b64_tr_b16 v[120:121], v179 offset:0\n\t" \
    "ds_read_b64_tr_b16 v[122:123], v179 offset:2048\n\t" \
    "ds_read_b64_tr_b16 v[44:45], v178 offset:16384\n\t" \
    "ds_read_b64_tr_b16 v[46:47], v178 offset:18432\n\t" \
    "ds_read_b64_tr_b16 v[124:125], v179 offset:16384\n\t" \
    "ds_read_b64_tr_b16 v[126:127], v179 offset:18432\n\t" \
    "v_add_u32 v178, 0x8000, v177\n\t" \
    "v_xor_b32 v179, 32, v178\n\t" \
    "ds_read_b64_tr_b16 v[48:49], v178 offset:0\n\t" \
    "ds_read_b64_tr_b16 v[50:51], v178 offset:2048\n\t" \
    "ds_read_b64_tr_b16 v[128:129], v179 offset:0\n\t" \
    "ds_read_b64_tr_b16 v[130:131], v179 offset:2048\n\t" \
    "ds_read_b64_tr_b16 v[52:53], v178 offset:16384\n\t" \
    "ds_read_b64_tr_b16 v[54:55], v178 offset:18432\n\t" \
    "ds_read_b64_tr_b16 v[132:133], v179 offset:16384\n\t" \
    "ds_read_b64_tr_b16 v[134:135], v179 offset:18432\n\t" \
    "v_add_u32 v178, 0x10000, v177\n\t" \
    "v_xor_b32 v179, 32, v178\n\t" \
    "ds_read_b64_tr_b16 v[56:57], v178 offset:0\n\t" \
    "ds_read_b64_tr_b16 v[58:59], v178 offset:2048\n\t" \
    "ds_read_b64_tr_b16 v[136:137], v179 offset:0\n\t" \
    "ds_read_b64_tr_b16 v[138:139], v179 offset:2048\n\t" \
    "ds_read_b64_tr_b16 v[60:61], v178 offset:16384\n\t" \
    "ds_read_b64_tr_b16 v[62:63], v178 offset:18432\n\t" \
    "ds_read_b64_tr_b16 v[140:141], v179 offset:16384\n\t" \
    "ds_read_b64_tr_b16 v[142:143], v179 offset:18432\n\t" \
    "v_add_u32 v178, 0x18000, v177\n\t" \
    "v_xor_b32 v179, 32, v178\n\t" \
    "ds_read_b64_tr_b16 v[64:65], v178 offset:0\n\t" \
    "ds_read_b64_tr_b16 v[66:67], v178 offset:2048\n\t" \
    "ds_read_b64_tr_b16 v[144:145], v179 offset:0\n\t" \
    "ds_read_b64_tr_b16 v[146:147], v179 offset:2048\n\t" \
    "ds_read_b64_tr_b16 v[68:69], v178 offset:16384\n\t" \
    "ds_read_b64_tr_b16 v[70:71], v178 offset:18432\n\t" \
    "ds_read_b64_tr_b16 v[148:149], v179 offset:16384\n\t" \
    "ds_read_b64_tr_b16 v[150:151], v179 offset:18432\n\t" \
    "v_add_u32 v178, 0x20000, v177\n\t" \
    "v_xor_b32 v179, 32, v178\n\t" \
    "ds_read_b64_tr_b16 v[72:73], v178 offset:0\n\t" \
    "ds_read_b64_tr_b16 v[74:75], v178 offset:2048\n\t" \
    "ds_read_b64_tr_b16 v[152:153], v179 offset:0\n\t" \
    "ds_read_b64_tr_b16 v[154:155], v179 offset:2048\n\t" \
    "ds_read_b64_tr_b16 v[76:77], v178 offset:16384\n\t" \
    "ds_read_b64_tr_b16 v[78:79], v178 offset:18432\n\t" \
    "ds_read_b64_tr_b16 v[156:157], v179 offset:16384\n\t" \
    "ds_read_b64_tr_b16 v[158:159], v179 offset:18432\n\t" \
    "v_mov_b32 v228, 0x3f803f80\n\t" \
    "v_mov_b32 v229, 0x3f803f80\n\t" \
    "v_mov_b32 v230, 0x3f803f80\n\t" \
    "v_mov_b32 v231, 0x3f803f80\n\t" \
    "v_mov_b64 v[216:217], 0\n\t" \
    "v_mov_b32 v219, 0\n\t" \
    "v_bfe_u32 v255, v253, 2, 5\n\t" \
    "v_add_u32 v255, 0x7000, v255\n\t" \
    "v_bfe_u32 v218, v253, 6, 1\n\t" \
    "s_waitcnt lgkmcnt(0)\n\t" \
    "s_barrier \n\t" \
    "ds_write_b32 v251, v228 offset:72\n\t" \
    "ds_write_b32 v251, v228 offset:2120\n\t" \
    "v_mfma_f32_16x16x32_bf16 v[160:163], v[0:3], v[228:231], 0\n\t" \
    "v_mfma_f32_16x16x32_bf16 v[164:167], v[80:83], v[228:231], 0\n\t" \
    "v_mfma_f32_16x16x32_bf16 v[160:163], v[4:7], v[228:231], v[160:163]\n\t" \
    "v_mfma_f32_16x16x32_bf16 v[164:167], v[84:87], v[228:231], v[164:167]\n\t" \
    "v_mfma_f32_16x16x32_bf16 v[160:163], v[8:11], v[228:231], v[160:163]\n\t" \
    "v_mfma_f32_16x16x32_bf16 v[164:167], v[88:91], v[228:231], v[164:167]\n\t" \
    "v_mfma_f32_16x16x32_bf16 v[160:163], v[12:15], v[228:231], v[160:163]\n\t" \
    "v_mfma_f32_16x16x32_bf16 v[164:167], v[92:95], v[228:231], v[164:167]\n\t" \
    "v_mfma_f32_16x16x32_bf16 v[160:163], v[16:19], v[228:231], v[160:163]\n\t" \
    "v_mfma_f32_16x16x32_bf16 v[164:167], v[96:99], v[228:231], v[164:167]\n\t" \
    "v_mfma_f32_16x16x32_bf16 v[160:163], v[20:23], v[228:231], v[160:163]\n\t" \
    "v_mfma_f32_16x16x32_bf16 v[164:167], v[100:103], v[228:231], v[164:167]\n\t" \
    "v_mfma_f32_16x16x32_bf16 v[160:163], v[24:27], v[228:231], v[160:163]\n\t" \
    "v_mfma_f32_16x16x32_bf16 v[164:167], v[104:107], v[228:231], v[164:167]\n\t" \
    "v_mfma_f32_16x16x32_bf16 v[160:163], v[28:31], v[228:231], v[160:163]\n\t" \
    "v_mfma_f32_16x16x32_bf16 v[164:167], v[108:111], v[228:231], v[164:167]\n\t" \
    "v_mfma_f32_16x16x32_bf16 v[160:163], v[32:35], v[228:231], v[160:163]\n\t" \
    "v_mfma_f32_16x16x32_bf16 v[164:167], v[112:115], v[228:231], v[164:167]\n\t" \
    "v_mfma_f32_16x16x32_bf16 v[160:163], v[36:39], v[228:231], v[160:163]\n\t" \
    "v_mfma_f32_16x16x32_bf16 v[164:167], v[116:119], v[228:231], v[164:167]\n\t" \
    "v_mfma_f32_16x16x32_bf16 v[160:163], v[40:43], v[228:231], v[160:163]\n\t" \
    "v_mfma_f32_16x16x32_bf16 v[164:167], v[120:123], v[228:231], v[164:167]\n\t" \
    "v_mfma_f32_16x16x32_bf16 v[160:163], v[44:47], v[228:231], v[160:163]\n\t" \
    "v_mfma_f32_16x16x32_bf16 v[164:167], v[124:127], v[228:231], v[164:167]\n\t" \
    "v_mfma_f32_16x16x32_bf16 v[160:163], v[48:51], v[228:231], v[160:163]\n\t" \
    "v_mfma_f32_16x16x32_bf16 v[164:167], v[128:131], v[228:231], v[164:167]\n\t" \
    "v_mfma_f32_16x16x32_bf16 v[160:163], v[52:55], v[228:231], v[160:163]\n\t" \
    "v_mfma_f32_16x16x32_bf16 v[164:167], v[132:135], v[228:231], v[164:167]\n\t" \
    "v_mfma_f32_16x16x32_bf16 v[160:163], v[56:59], v[228:231], v[160:163]\n\t" \
    "v_mfma_f32_16x16x32_bf16 v[164:167], v[136:139], v[228:231], v[164:167]\n\t" \
    "v_mfma_f32_16x16x32_bf16 v[160:163], v[60:63], v[228:231], v[160:163]\n\t" \
    "v_mfma_f32_16x16x32_bf16 v[164:167], v[140:143], v[228:231], v[164:167]\n\t" \
    "v_mfma_f32_16x16x32_bf16 v[160:163], v[64:67], v[228:231], v[160:163]\n\t" \
    "v_mfma_f32_16x16x32_bf16 v[164:167], v[144:147], v[228:231], v[164:167]\n\t" \
    "v_mfma_f32_16x16x32_bf16 v[160:163], v[68:71], v[228:231], v[160:163]\n\t" \
    "v_mfma_f32_16x16x32_bf16 v[164:167], v[148:151], v[228:231], v[164:167]\n\t" \
    "v_mfma_f32_16x16x32_bf16 v[160:163], v[72:75], v[228:231], v[160:163]\n\t" \
    "v_mfma_f32_16x16x32_bf16 v[164:167], v[152:155], v[228:231], v[164:167]\n\t" \
    "v_mfma_f32_16x16x32_bf16 v[160:163], v[76:79], v[228:231], v[160:163]\n\t" \
    "v_mfma_f32_16x16x32_bf16 v[164:167], v[156:159], v[228:231], v[164:167]\n\t" \
    "s_mov_b32 s52, 0x22222222\n\t" \
    "s_mov_b32 s53, 0x22222222\n\t" \
    "s_mov_b32 s54, 0x44444444\n\t" \
    "s_mov_b32 s55, 0x44444444\n\t" \
    "s_mov_b32 s56, 0x88888888\n\t" \
    "s_mov_b32 s57, 0x88888888\n\t" \
    "ds_write_b32 v255, v218\n\t" \
    "s_waitcnt lgkmcnt(0)\n\t" \
    "s_barrier \n\t" \
    ".Lw31_%=:\n\t" \
    "v_mov_b32 v248, 0\n\t" \
    "buffer_load_dword v248, v248, %[rs], %[go] offen sc1\n\t" \
    "s_waitcnt vmcnt(0)\n\t" \
    "v_cmp_ne_u32 vcc, 0, v248\n\t" \
    "s_cbranch_vccnz .Lw32_%=\n\t" \
    "s_cmp_eq_u32 s41, 0\n\t" \
    "s_cbranch_scc1 .Lw32_%=\n\t" \
    "s_sub_u32 s41, s41, 1\n\t" \
    "s_sleep 16\n\t" \
    "s_branch .Lw31_%=\n\t" \
    ".Lw32_%=:\n\t" \
    "s_mov_b32 s43, 0\n\t" \
    "s_add_u32 s46, s43, %[pi]\n\t" \
    "s_cmp_gt_u32 %[wave], 3\n\t" \
    "s_cbranch_scc1 .Lw33_%=\n\t" \
    "s_add_u32 s47, s46, 0x0\n\t" \
    "buffer_load_dwordx4 v[208:211], v253, %[rs], s47 offen sc1\n\t" \
    "s_add_u32 s47, s46, 0x4000\n\t" \
    "buffer_load_dwordx4 v[212:215], v253, %[rs], s47 offen sc1\n\t" \
    "s_add_u32 s47, s46, 0x8000\n\t" \
    "buffer_load_dwordx4 v[216:219], v253, %[rs], s47 offen sc1\n\t" \
    "s_add_u32 s47, s46, 0xc000\n\t" \
    "buffer_load_dwordx4 v[220:223], v253, %[rs], s47 offen sc1\n\t" \
    "s_add_u32 s47, s46, 0x10000\n\t" \
    "buffer_load_dwordx4 v[224:227], v253, %[rs], s47 offen sc1\n\t" \
    ".Lw33_%=:\n\t" \
    "s_waitcnt vmcnt(0)\n\t" \
    ".Lw34_%=:\n\t" \
    "s_add_i32 s44, s40, 0\n\t" \
    "s_max_i32 s44, s44, 0\n\t" \
    "s_lshr_b32 s45, s44, 1\n\t" \
    "s_and_b32 s44, s44, 1\n\t" \
    "s_lshl_b32 s44, s44, 12\n\t" \
    "s_mul_i32 s49, s45, 0x30000\n\t" \
    "s_add_u32 s49, s49, s44\n\t" \
    "s_lshr_b32 s45, s45, 3\n\t" \
    "s_mul_i32 s45, s45, 0x82000\n\t" \
    "s_add_u32 s49, s49, s45\n\t" \
    "s_add_u32 s49, s49, %[ng]\n\t" \
    "s_add_i32 s44, s40, 1\n\t" \
    "s_max_i32 s44, s44, 0\n\t" \
    "s_lshr_b32 s45, s44, 1\n\t" \
    "s_and_b32 s44, s44, 1\n\t" \
    "s_lshl_b32 s44, s44, 12\n\t" \
    "s_mul_i32 s43, s45, 0x14000\n\t" \
    "s_add_u32 s43, s43, s44\n\t" \
    "s_lshr_b32 s45, s45, 4\n\t" \
    "s_mul_i32 s45, s45, 0xc2000\n\t" \
    "s_add_u32 s43, s43, s45\n\t" \
    "s_cmp_gt_u32 %[wave], 3\n\t" \
    "s_cbranch_scc1 .Lw37_%=\n\t" \
    "s_setprio 2\n\t" \
    "v_max_u32 v248, v208, v209\n\t" \
    "v_max3_u32 v248, v210, v211, v248\n\t" \
    "v_max3_u32 v248, v212, v213, v248\n\t" \
    "v_max3_u32 v248, v214, v215, v248\n\t" \
    "v_max3_u32 v248, v216, v217, v248\n\t" \
    "v_max3_u32 v248, v218, v219, v248\n\t" \
    "v_max3_u32 v248, v220, v221, v248\n\t" \
    "v_max3_u32 v248, v222, v223, v248\n\t" \
    "v_max3_u32 v248, v224, v225, v248\n\t" \
    "v_max3_u32 v248, v226, v227, v248\n\t" \
    "v_cmp_eq_u32 vcc, -1, v248\n\t" \
    "s_cbranch_vccz .Lw36_%=\n\t" \
    ".Lw35_%=:\n\t" \
    "v_max_u32 v248, v208, v209\n\t" \
    "v_max3_u32 v248, v210, v211, v248\n\t" \
    "v_cmp_eq_u32 vcc, -1, v248\n\t" \
    "s_cbranch_vccz .Lw38_%=\n\t" \
    "s_add_u32 s47, s46, 0x0\n\t" \
    "buffer_load_dwordx4 v[208:211], v253, %[rs], s47 offen sc1\n\t" \
    ".Lw38_%=:\n\t" \
    "v_max_u32 v248, v212, v213\n\t" \
    "v_max3_u32 v248, v214, v215, v248\n\t" \
    "v_cmp_eq_u32 vcc, -1, v248\n\t" \
    "s_cbranch_vccz .Lw39_%=\n\t" \
    "s_add_u32 s47, s46, 0x4000\n\t" \
    "buffer_load_dwordx4 v[212:215], v253, %[rs], s47 offen sc1\n\t" \
    ".Lw39_%=:\n\t" \
    "v_max_u32 v248, v216, v217\n\t" \
    "v_max3_u32 v248, v218, v219, v248\n\t" \
    "v_cmp_eq_u32 vcc, -1, v248\n\t" \
    "s_cbranch_vccz .Lw310_%=\n\t" \
    "s_add_u32 s47, s46, 0x8000\n\t" \
    "buffer_load_dwordx4 v[216:219], v253, %[rs], s47 offen sc1\n\t" \
    ".Lw310_%=:\n\t" \
    "v_max_u32 v248, v220, v221\n\t" \
    "v_max3_u32 v248, v222, v223, v248\n\t" \
    "v_cmp_eq_u32 vcc, -1, v248\n\t" \
    "s_cbranch_vccz .Lw311_%=\n\t" \
    "s_add_u32 s47, s46, 0xc000\n\t" \
    "buffer_load_dwordx4 v[220:223], v253, %[rs], s47 offen sc1\n\t" \
    ".Lw311_%=:\n\t" \
    "v_max_u32 v248, v224, v225\n\t" \
    "v_max3_u32 v248, v226, v227, v248\n\t" \
    "v_cmp_eq_u32 vcc, -1, v248\n\t" \
    "s_cbranch_vccz .Lw312_%=\n\t" \
    "s_add_u32 s47, s46, 0x10000\n\t" \
    "buffer_load_dwordx4 v[224:227], v253, %[rs], s47 offen sc1\n\t" \
    ".Lw312_%=:\n\t" \
    "s_waitcnt vmcnt(0)\n\t" \
    "v_max_u32 v248, v208, v209\n\t" \
    "v_max3_u32 v248, v210, v211, v248\n\t" \
    "v_max3_u32 v248, v212, v213, v248\n\t" \
    "v_max3_u32 v248, v214, v215, v248\n\t" \
    "v_max3_u32 v248, v216, v217, v248\n\t" \
    "v_max3_u32 v248, v218, v219, v248\n\t" \
    "v_max3_u32 v248, v220, v221, v248\n\t" \
    "v_max3_u32 v248, v222, v223, v248\n\t" \
    "v_max3_u32 v248, v224, v225, v248\n\t" \
    "v_max3_u32 v248, v226, v227, v248\n\t" \
    "v_cmp_eq_u32 vcc, -1, v248\n\t" \
    "s_cbranch_vccz .Lw36_%=\n\t" \
    "s_cmp_eq_u32 s41, 0\n\t" \
    "s_cbranch_scc1 .Lw36_%=\n\t" \
    "s_sub_u32 s41, s41, 1\n\t" \
    "s_branch .Lw35_%=\n\t" \
    ".Lw36_%=:\n\t" \
    "ds_write_b128 v250, v[208:211] offset:0\n\t" \
    "v_mfma_f32_16x16x32_bf16 v[236:239], v[208:211], v[208:211], 0\n\t" \
    "v_mfma_f32_16x16x32_bf16 v[232:235], v[228:231], v[208:211], 0\n\t" \
    "ds_write_b128 v250, v[212:215] offset:4096\n\t" \
    "v_mfma_f32_16x16x32_bf16 v[236:239], v[212:215], v[212:215], v[236:239]\n\t" \
    "v_mfma_f32_16x16x32_bf16 v[232:235], v[228:231], v[212:215], v[232:235]\n\t" \
    "ds_write_b128 v250, v[216:219] offset:8192\n\t" \
    "v_mfma_f32_16x16x32_bf16 v[236:239], v[216:219], v[216:219], v[236:239]\n\t" \
    "v_mfma_f32_16x16x32_bf16 v[232:235], v[228:231], v[216:219], v[232:235]\n\t" \
    "ds_write_b128 v250, v[220:223] offset:12288\n\t" \
    "v_mfma_f32_16x16x32_bf16 v[236:239], v[220:223], v[220:223], v[236:239]\n\t" \
    "v_mfma_f32_16x16x32_bf16 v[232:235], v[228:231], v[220:223], v[232:235]\n\t" \
    "ds_write_b128 v250, v[224:227] offset:16384\n\t" \
    "v_mfma_f32_16x16x32_bf16 v[236:239], v[224:227], v[224:227], v[236:239]\n\t" \
    "v_mfma_f32_16x16x32_bf16 v[232:235], v[228:231], v[224:227], v[232:235]\n\t" \
    "s_add_u32 s46, s43, %[pi]\n\t" \
    "s_nop 4\n\t" \
    "v_cndmask_b32 v236, v236, v237, s[52:53]\n\t" \
    "v_cndmask_b32 v236, v236, v238, s[54:55]\n\t" \
    "v_cndmask_b32 v236, v236, v239, s[56:57]\n\t" \
    "s_mov_b32 exec_lo, 0xf0000f\n\t" \
    "s_mov_b32 exec_hi, 0xf0000f00\n\t" \
    "ds_write2_b32 v252, v232, v236 offset1:1\n\t" \
    "s_mov_b64 exec, -1\n\t" \
    "s_add_u32 s44, s40, 1\n\t" \
    "v_mov_b32 v248, s44\n\t" \
    "s_lshl_b64 exec, 1, %[wave]\n\t" \
    "ds_write_b32 v255, v248\n\t" \
    "s_mov_b64 exec, -1\n\t" \
    ".Lw37_%=:\n\t" \
    "ds_read_b32 v248, v255\n\t" \
    "s_waitcnt lgkmcnt(0)\n\t" \
    "v_cmp_ge_u32 vcc, s40, v248\n\t" \
    "s_cbranch_vccz .Lw313_%=\n\t" \
    "s_cmp_lt_u32 %[wave], 4\n\t" \
    "s_cbranch_scc1 .Lw37_%=\n\t" \
    "s_sleep 1\n\t" \
    "s_branch .Lw37_%=\n\t" \
    ".Lw313_%=:\n\t" \
    "s_setprio 0\n\t" \
    "s_cmp_lg_u32 %[wave], 4\n\t" \
    "s_cbranch_scc1 .Lw314_%=\n\t" \
    "s_setprio 1\n\t" \
    "ds_read_b128 v[232:235], v251 offset:0\n\t" \
    "ds_read_b128 v[236:239], v251 offset:16\n\t" \
    "s_waitcnt lgkmcnt(0)\n\t" \
    "v_add_f32 v232, v232, v236\n\t" \
    "v_add_f32 v233, v233, v237\n\t" \
    "v_add_f32 v234, v234, v238\n\t" \
    "v_add_f32 v235, v235, v239\n\t" \
    "v_add_f32 v232, v232, v234\n\t" \
    "v_add_f32 v233, v233, v235\n\t" \
    "v_mul_f32 v232, 0x3acccccd, v232\n\t" \
    "v_mul_f32 v233, 0x3acccccd, v233\n\t" \
    "v_fma_f32 v233, -v232, v232, v233\n\t" \
    "v_mov_b32 v234, v232\n\t" \
    "v_max_f32 v233, 0, v233\n\t" \
    "v_cvt_pk_bf16_f32 v234, v234, v234\n\t" \
    "v_and_b32 v234, 0xffff0000, v234\n\t" \
    "v_mov_b32 v236, v233\n\t" \
    "v_cvt_pk_bf16_f32 v236, v236, v236\n\t" \
    "v_and_b32 v236, 0xffff0000, v236\n\t" \
    "v_add_f32 v236, 0x3727c5ac, v236\n\t" \
    "v_cvt_pk_bf16_f32 v236, v236, v236\n\t" \
    "v_and_b32 v236, 0xffff0000, v236\n\t" \
    "v_sqrt_f32 v236, v236\n\t" \
    "s_nop 0\n\t" \
    "v_cvt_pk_bf16_f32 v236, v236, v236\n\t" \
    "v_and_b32 v236, 0xffff0000, v236\n\t" \
    "v_rcp_f32 v235, v236\n\t" \
    "s_nop 0\n\t" \
    "v_mov_b32 v237, v235\n\t" \
    "v_mul_f32 v238, -v234, v235\n\t" \
    "v_mov_b32 v239, s40\n\t" \
    "ds_write2_b32 v251, v237, v238 offset0:16 offset1:17\n\t" \
    "ds_write_b32 v251, v239 offset:72\n\t" \
    "s_setprio 0\n\t" \
    ".Lw314_%=:\n\t" \
    "ds_read_b128 v[176:179], v249 offset:0\n\t" \
    "ds_read_b128 v[180:183], v249 offset:1024\n\t" \
    "ds_read_b128 v[184:187], v249 offset:2048\n\t" \
    "ds_read_b128 v[188:191], v249 offset:3072\n\t" \
    "ds_read_b128 v[192:195], v249 offset:4096\n\t" \
    "ds_read_b128 v[196:199], v249 offset:5120\n\t" \
    "ds_read_b128 v[200:203], v249 offset:6144\n\t" \
    "s_waitcnt lgkmcnt(6)\n\t" \
    "v_mfma_f32_16x16x32_bf16 v[168:171], v[0:3], v[176:179], 0\n\t" \
    "ds_read_b128 v[204:207], v249 offset:7168\n\t" \
    "v_mfma_f32_16x16x32_bf16 v[172:175], v[80:83], v[176:179], 0\n\t" \
    "s_waitcnt lgkmcnt(6)\n\t" \
    "v_mfma_f32_16x16x32_bf16 v[168:171], v[4:7], v[180:183], v[168:171]\n\t" \
    "ds_read_b128 v[176:179], v249 offset:8192\n\t" \
    "v_mfma_f32_16x16x32_bf16 v[172:175], v[84:87], v[180:183], v[172:175]\n\t" \
    "s_waitcnt lgkmcnt(6)\n\t" \
    "v_mfma_f32_16x16x32_bf16 v[168:171], v[8:11], v[184:187], v[168:171]\n\t" \
    "ds_read_b128 v[180:183], v249 offset:9216\n\t" \
    "v_mfma_f32_16x16x32_bf16 v[172:175], v[88:91], v[184:187], v[172:175]\n\t" \
    "s_waitcnt lgkmcnt(6)\n\t" \
    "v_mfma_f32_16x16x32_bf16 v[168:171], v[12:15], v[188:191], v[168:171]\n\t" \
    "ds_read_b128 v[184:187], v249 offset:10240\n\t" \
    "v_mfma_f32_16x16x32_bf16 v[172:175], v[92:95], v[188:191], v[172:175]\n\t" \
    "s_waitcnt lgkmcnt(6)\n\t" \
    "v_mfma_f32_16x16x32_bf16 v[168:171], v[16:19], v[192:195], v[168:171]\n\t" \
    "ds_read_b128 v[188:191], v249 offset:11264\n\t" \
    "v_mfma_f32_16x16x32_bf16 v[172:175], v[96:99], v[192:195], v[172:175]\n\t" \
    "s_waitcnt lgkmcnt(6)\n\t" \
    "v_mfma_f32_16x16x32_bf16 v[168:171], v[20:23], v[196:199], v[168:171]\n\t" \
    "ds_read_b128 v[192:195], v249 offset:12288\n\t" \
    "v_mfma_f32_16x16x32_bf16 v[172:175], v[100:103], v[196:199], v[172:175]\n\t" \
    "s_waitcnt lgkmcnt(6)\n\t" \
    "v_mfma_f32_16x16x32_bf16 v[168:171], v[24:27], v[200:203], v[168:171]\n\t" \
    "ds_read_b128 v[196:199], v249 offset:13312\n\t" \
    "v_mfma_f32_16x16x32_bf16 v[172:175], v[104:107], v[200:203], v[172:175]\n\t" \
    "s_waitcnt lgkmcnt(6)\n\t" \
    "v_mfma_f32_16x16x32_bf16 v[168:171], v[28:31], v[204:207], v[168:171]\n\t" \
    "ds_read_b128 v[200:203], v249 offset:14336\n\t" \
    "v_mfma_f32_16x16x32_bf16 v[172:175], v[108:111], v[204:207], v[172:175]\n\t" \
    "s_waitcnt lgkmcnt(6)\n\t" \
    "v_mfma_f32_16x16x32_bf16 v[168:171], v[32:35], v[176:179], v[168:171]\n\t" \
    "ds_read_b128 v[204:207], v249 offset:15360\n\t" \
    "v_mfma_f32_16x16x32_bf16 v[172:175], v[112:115], v[176:179], v[172:175]\n\t" \
    "s_waitcnt lgkmcnt(6)\n\t" \
    "v_mfma_f32_16x16x32_bf16 v[168:171], v[36:39], v[180:183], v[168:171]\n\t" \
    "ds_read_b128 v[176:179], v249 offset:16384\n\t" \
    "v_mfma_f32_16x16x32_bf16 v[172:175], v[116:119], v[180:183], v[172:175]\n\t" \
    "s_waitcnt lgkmcnt(6)\n\t" \
    "v_mfma_f32_16x16x32_bf16 v[168:171], v[40:43], v[184:187], v[168:171]\n\t" \
    "ds_read_b128 v[180:183], v249 offset:17408\n\t" \
    "v_mfma_f32_16x16x32_bf16 v[172:175], v[120:123], v[184:187], v[172:175]\n\t" \
    "s_waitcnt lgkmcnt(6)\n\t" \
    "v_mfma_f32_16x16x32_bf16 v[168:171], v[44:47], v[188:191], v[168:171]\n\t" \
    "ds_read_b128 v[184:187], v249 offset:18432\n\t" \
    "v_mfma_f32_16x16x32_bf16 v[172:175], v[124:127], v[188:191], v[172:175]\n\t" \
    "s_waitcnt lgkmcnt(6)\n\t" \
    "v_mfma_f32_16x16x32_bf16 v[168:171], v[48:51], v[192:195], v[168:171]\n\t" \
    "ds_read_b128 v[188:191], v249 offset:19456\n\t" \
    "v_mfma_f32_16x16x32_bf16 v[172:175], v[128:131], v[192:195], v[172:175]\n\t" \
    "s_waitcnt lgkmcnt(6)\n\t" \
    "v_mfma_f32_16x16x32_bf16 v[168:171], v[52:55], v[196:199], v[168:171]\n\t" \
    "v_mfma_f32_16x16x32_bf16 v[172:175], v[132:135], v[196:199], v[172:175]\n\t" \
    "s_waitcnt lgkmcnt(5)\n\t" \
    "v_mfma_f32_16x16x32_bf16 v[168:171], v[56:59], v[200:203], v[168:171]\n\t" \
    "v_mfma_f32_16x16x32_bf16 v[172:175], v[136:139], v[200:203], v[172:175]\n\t" \
    "s_cmp_gt_u32 %[wave], 3\n\t" \
    "s_cbranch_scc1 .Lw316_%=\n\t" \
    "s_add_u32 s47, s46, 0x0\n\t" \
    "buffer_load_dwordx4 v[208:211], v253, %[rs], s47 offen sc1\n\t" \
    "s_add_u32 s47, s46, 0x4000\n\t" \
    "buffer_load_dwordx4 v[212:215], v253, %[rs], s47 offen sc1\n\t" \
    "s_add_u32 s47, s46, 0x8000\n\t" \
    "buffer_load_dwordx4 v[216:219], v253, %[rs], s47 offen sc1\n\t" \
    "s_add_u32 s47, s46, 0xc000\n\t" \
    "buffer_load_dwordx4 v[220:223], v253, %[rs], s47 offen sc1\n\t" \
    "s_add_u32 s47, s46, 0x10000\n\t" \
    "buffer_load_dwordx4 v[224:227], v253, %[rs], s47 offen sc1\n\t" \
    ".Lw316_%=:\n\t" \
    "s_waitcnt lgkmcnt(4)\n\t" \
    "v_mfma_f32_16x16x32_bf16 v[168:171], v[60:63], v[204:207], v[168:171]\n\t" \
    "v_mfma_f32_16x16x32_bf16 v[172:175], v[140:143], v[204:207], v[172:175]\n\t" \
    "ds_read_b32 v236, v251 offset:72\n\t" \
    "ds_read_b64 v[238:239], v251 offset:64\n\t" \
    "s_waitcnt lgkmcnt(5)\n\t" \
    "v_mfma_f32_16x16x32_bf16 v[168:171], v[64:67], v[176:179], v[168:171]\n\t" \
    "v_mfma_f32_16x16x32_bf16 v[172:175], v[144:147], v[176:179], v[172:175]\n\t" \
    "s_waitcnt lgkmcnt(4)\n\t" \
    "v_mfma_f32_16x16x32_bf16 v[168:171], v[68:71], v[180:183], v[168:171]\n\t" \
    "v_mfma_f32_16x16x32_bf16 v[172:175], v[148:151], v[180:183], v[172:175]\n\t" \
    ".Lw317_%=:\n\t" \
    "s_waitcnt lgkmcnt(0)\n\t" \
    "v_cmp_ne_u32 vcc, s40, v236\n\t" \
    "s_cbranch_vccz .Lw318_%=\n\t" \
    "ds_read_b32 v236, v251 offset:72\n\t" \
    "ds_read_b64 v[238:239], v251 offset:64\n\t" \
    "s_branch .Lw317_%=\n\t" \
    ".Lw318_%=:\n\t" \
    "s_waitcnt lgkmcnt(0)\n\t" \
    "v_mul_f32 v240, v160, v239\n\t" \
    "v_mul_f32 v241, v161, v239\n\t" \
    "v_mfma_f32_16x16x32_bf16 v[168:171], v[72:75], v[184:187], v[168:171]\n\t" \
    "v_mul_f32 v242, v162, v239\n\t" \
    "v_mul_f32 v243, v163, v239\n\t" \
    "v_mfma_f32_16x16x32_bf16 v[172:175], v[152:155], v[184:187], v[172:175]\n\t" \
    "v_mul_f32 v244, v164, v239\n\t" \
    "v_mul_f32 v245, v165, v239\n\t" \
    "v_mfma_f32_16x16x32_bf16 v[168:171], v[76:79], v[188:191], v[168:171]\n\t" \
    "v_mul_f32 v246, v166, v239\n\t" \
    "v_mul_f32 v247, v167, v239\n\t" \
    "v_mfma_f32_16x16x32_bf16 v[172:175], v[156:159], v[188:191], v[172:175]\n\t" \
    "v_mov_b32 v237, 0xc0135761\n\t" \
    "s_cmp_lt_u32 %[wave], 4\n\t" \
    "s_cbranch_scc1 .Lw319_%=\n\t" \
    "s_add_u32 s44, s40, 2\n\t" \
    "v_mov_b32 v248, s44\n\t" \
    "s_lshl_b64 exec, 1, %[wave]\n\t" \
    "ds_write_b32 v255, v248\n\t" \
    "s_mov_b64 exec, -1\n\t" \
    ".Lw319_%=:\n\t" \
    "s_nop 1\n\t" \
    "s_nop 2\n\t" \
    "v_fma_f32 v168, v168, v238, v240\n\t" \
    "v_fma_f32 v169, v169, v238, v241\n\t" \
    "v_fma_f32 v170, v170, v238, v242\n\t" \
    "v_fma_f32 v171, v171, v238, v243\n\t" \
    "v_fma_f32 v172, v172, v238, v244\n\t" \
    "v_fma_f32 v173, v173, v238, v245\n\t" \
    "v_fma_f32 v174, v174, v238, v246\n\t" \
    "v_fma_f32 v175, v175, v238, v247\n\t" \
    "v_mul_f32 v176, v168, v168\n\t" \
    "v_mul_f32 v177, v169, v169\n\t" \
    "v_mul_f32 v178, v170, v170\n\t" \
    "v_mul_f32 v179, v171, v171\n\t" \
    "v_mul_f32 v180, v172, v172\n\t" \
    "v_mul_f32 v181, v173, v173\n\t" \
    "v_mul_f32 v182, v174, v174\n\t" \
    "v_mul_f32 v183, v175, v175\n\t" \
    "v_fmamk_f32 v176, v176, 0xbdd2d3e7, v237\n\t" \
    "v_fmamk_f32 v177, v177, 0xbdd2d3e7, v237\n\t" \
    "v_fmamk_f32 v178, v178, 0xbdd2d3e7, v237\n\t" \
    "v_fmamk_f32 v179, v179, 0xbdd2d3e7, v237\n\t" \
    "v_fmamk_f32 v180, v180, 0xbdd2d3e7, v237\n\t" \
    "v_fmamk_f32 v181, v181, 0xbdd2d3e7, v237\n\t" \
    "v_fmamk_f32 v182, v182, 0xbdd2d3e7, v237\n\t" \
    "v_fmamk_f32 v183, v183, 0xbdd2d3e7, v237\n\t" \
    "v_mul_f32 v176, v168, v176\n\t" \
    "v_mul_f32 v177, v169, v177\n\t" \
    "v_mul_f32 v178, v170, v178\n\t" \
    "v_mul_f32 v179, v171, v179\n\t" \
    "v_mul_f32 v180, v172, v180\n\t" \
    "v_mul_f32 v181, v173, v181\n\t" \
    "v_mul_f32 v182, v174, v182\n\t" \
    "v_mul_f32 v183, v175, v183\n\t" \
    "v_exp_f32 v176, v176\n\t" \
    "v_exp_f32 v177, v177\n\t" \
    "v_exp_f32 v178, v178\n\t" \
    "v_exp_f32 v179, v179\n\t" \
    "v_exp_f32 v180, v180\n\t" \
    "v_exp_f32 v181, v181\n\t" \
    "v_exp_f32 v182, v182\n\t" \
    "v_exp_f32 v183, v183\n\t" \
    "v_add_f32 v176, 1.0, v176\n\t" \
    "v_add_f32 v177, 1.0, v177\n\t" \
    "v_add_f32 v178, 1.0, v178\n\t" \
    "v_add_f32 v179, 1.0, v179\n\t" \
    "v_add_f32 v180, 1.0, v180\n\t" \
    "v_add_f32 v181, 1.0, v181\n\t" \
    "v_add_f32 v182, 1.0, v182\n\t" \
    "v_add_f32 v183, 1.0, v183\n\t" \
    "v_rcp_f32 v176, v176\n\t" \
    "v_rcp_f32 v177, v177\n\t" \
    "v_rcp_f32 v178, v178\n\t" \
    "v_rcp_f32 v179, v179\n\t" \
    "v_rcp_f32 v180, v180\n\t" \
    "v_rcp_f32 v181, v181\n\t" \
    "v_rcp_f32 v182, v182\n\t" \
    "v_rcp_f32 v183, v183\n\t" \
    "v_mul_f32 v168, v168, v176\n\t" \
    "v_mul_f32 v169, v169, v177\n\t" \
    "v_mul_f32 v170, v170, v178\n\t" \
    "v_mul_f32 v171, v171, v179\n\t" \
    "v_mul_f32 v172, v172, v180\n\t" \
    "v_mul_f32 v173, v173, v181\n\t" \
    "v_mul_f32 v174, v174, v182\n\t" \
    "v_mul_f32 v175, v175, v183\n\t" \
    "s_waitcnt vmcnt(0)\n\t" \
    "v_cvt_pk_bf16_f32 v184, v168, v169\n\t" \
    "v_cvt_pk_bf16_f32 v185, v170, v171\n\t" \
    "v_cvt_pk_bf16_f32 v186, v172, v173\n\t" \
    "v_cvt_pk_bf16_f32 v187, v174, v175\n\t" \
    "v_min_u32 v184, -2, v184\n\t" \
    "v_min_u32 v185, -2, v185\n\t" \
    "v_min_u32 v186, -2, v186\n\t" \
    "v_min_u32 v187, -2, v187\n\t" \
    "s_nop 0\n\t" \
    "v_permlane16_swap_b32 v184, v186\n\t" \
    "v_permlane16_swap_b32 v185, v187\n\t" \
    "buffer_store_dwordx4 v[184:187], v254, %[rs], s49 offen sc1\n\t" \
    "s_cmp_ge_u32 s40, 122\n\t" \
    "s_cbranch_scc1 .Lw320_%=\n\t" \
    "s_add_i32 s44, s40, 6\n\t" \
    "s_max_i32 s44, s44, 0\n\t" \
    "s_lshr_b32 s45, s44, 1\n\t" \
    "s_and_b32 s44, s44, 1\n\t" \
    "s_lshl_b32 s44, s44, 12\n\t" \
    "s_mul_i32 s50, s45, 0x30000\n\t" \
    "s_add_u32 s50, s50, s44\n\t" \
    "s_lshr_b32 s45, s45, 3\n\t" \
    "s_mul_i32 s45, s45, 0x82000\n\t" \
    "s_add_u32 s50, s50, s45\n\t" \
    "s_add_u32 s50, s50, %[ng]\n\t" \
    "v_mov_b32 v188, -1\n\t" \
    "v_mov_b32 v189, -1\n\t" \
    "v_mov_b32 v190, -1\n\t" \
    "v_mov_b32 v191, -1\n\t" \
    "buffer_store_dwordx4 v[188:191], v253, %[rs], s50 offen sc1\n\t" \
    ".Lw320_%=:\n\t" \
    "v_xor_b32 v249, 0x8000, v249\n\t" \
    "v_xor_b32 v250, 0x8000, v250\n\t" \
    "v_xor_b32 v251, 0x800, v251\n\t" \
    "v_xor_b32 v252, 0x800, v252\n\t" \
    "s_add_u32 s40, s40, 1\n\t" \
    "s_cmp_lg_u32 s40, 128\n\t" \
    "s_cbranch_scc1 .Lw34_%=\n\t" \
    "s_branch .Lend%=\n\t" \
    ".Lend%=:\n\t" \
    "s_waitcnt vmcnt(0) lgkmcnt(0)\n\t" \
    "s_mov_b32 %[bud], s41\n\t"
using u32x8 = __attribute__((ext_vector_type(8))) unsigned;
using u32x16 = __attribute__((ext_vector_type(16))) unsigned;
using u32x32 = __attribute__((ext_vector_type(32))) unsigned;
__device__ __forceinline__ u32x32 cat32(u32x8 a, u32x8 b, u32x8 c, u32x8 d) {
    const u32x16 lo = __builtin_shufflevector(a, b, 0, 1, 2, 3, 4, 5, 6, 7, 8, 9, 10, 11, 12, 13, 14, 15);
    const u32x16 hi = __builtin_shufflevector(c, d, 0, 1, 2, 3, 4, 5, 6, 7, 8, 9, 10, 11, 12, 13, 14, 15);
    return __builtin_shufflevector(lo, hi, 0, 1, 2, 3, 4, 5, 6, 7, 8, 9, 10, 11, 12, 13, 14, 15, 16, 17, 18, 19, 20, 21, 22, 23, 24, 25, 26, 27, 28, 29, 30, 31);
}
__device__ __forceinline__ unsigned c1_dst(int wave, int kind) {
    const int ln = lane_id(), c = ln & 15, g = ln >> 4, hf = (wave >> 1) & 1;
    if (kind == 2) {
        const unsigned b = L_KM + (c >> 2) * 256 + (wave & 1) * 128 + g * 32;
        return (b + hf * 8 + (c & 3) * 2) | (b + 1024 + hf * 1024 + (c & 3) * 4) << 16;
    }
    const unsigned b = L_Q + (c >> 3) * 256 + g * 64 + ((c >> 2) & 1) * 8;
    return (b + (wave & 1) * (kind ? 512 : 1536) + ((c >> 1) & 1) * 32 + hf * 4 + (c & 1) * 2) | (b + 1024 + (wave & 3) * 512 + (c & 1) * 32 + ((c >> 1) & 1) * 4) << 16;
}
__device__ __forceinline__ void dma1(rsrc_t rs, unsigned vo, unsigned so, unsigned dst) {
    asm volatile("s_mov_b32 m0, %3\n\ts_nop 0\n\tbuffer_load_dwordx4 %0, %1, %2 offen nt lds" :: "v"(vo), "s"(rs), "s"(so), "s"(dst) : "memory");
}
using s16x4 = __attribute__((ext_vector_type(4))) short;
__device__ __forceinline__ u32x4 tr_frag(unsigned a, unsigned step) {
    const u32x2 x = __builtin_bit_cast(u32x2, __builtin_amdgcn_ds_read_tr16_b64_v4i16((LAS s16x4*)(size_t)a)), y = __builtin_bit_cast(u32x2, __builtin_amdgcn_ds_read_tr16_b64_v4i16((LAS s16x4*)(size_t)(a + step)));
    return (u32x4){x[0], x[1], y[0], y[1]};
}
#define LOOP_CLOB "memory", "vcc", "scc", "v0", "v1", "v2", "v3", "v4", "v5", "v6", "v7", "v8", "v9", "v10", "v11", "v12", "v13", "v14", "v15", "v16", "v17", "v18", "v19", "v20", "v21", "v22", "v23", "v24", "v25", "v26", "v27", "v28", "v29", "v30", "v31", "v32", "v33", "v34", "v35", "v36", "v37", "v38", "v39", "v40", "v41", "v42", "v43", "v44", "v45", "v46", "v47", "v48", "v49", "v50", "v51", "v52", "v53", "v54", "v55", "v56", "v57", "v58", "v59", "v60", "v61", "v62", "v63", "v64", "v65", "v66", "v67", "v68", "v69", "v70", "v71", "v72", "v73", "v74", "v75", "v76", "v77", "v78", "v79", "v80", "v81", "v82", "v83", "v84", "v85", "v86", "v87", "v88", "v89", "v90", "v91", "v92", "v93", "v94", "v95", "v96", "v97", "v98", "v99", "v100", "v101", "v102", "v103", "v104", "v105", "v106", "v107", "v108", "v109", "v110", "v111", "v112", "v113", "v114", "v115", "v116", "v117", "v118", "v119", "v120", "v121", "v122", "v123", "v124", "v125", "v126", "v127", "v128", "v129", "v130", "v131", "v132", "v133", "v134", "v135", "v136", "v137", "v138", "v139", "v140", "v141", "v142", "v143", "v144", "v145", "v146", "v147", "v148", "v149", "v150", "v151", "v152", "v153", "v154", "v155", "v156", "v157", "v158", "v159", "v160", "v161", "v162", "v163", "v164", "v165", "v166", "v167", "v168", "v169", "v170", "v171", "v172", "v173", "v174", "v175", "v176", "v177", "v178", "v179", "v180", "v181", "v182", "v183", "v184", "v185", "v186", "v187", "v188", "v189", "v190", "v191", "v192", "v193", "v194", "v195", "v196", "v197", "v198", "v199", "v200", "v201", "v202", "v203", "v204", "v205", "v206", "v207", "v208", "v209", "v210", "v211", "v212", "v213", "v214", "v215", "v216", "v217", "v218", "v219", "v220", "v221", "v222", "v223", "v224", "v225", "v226", "v227", "v228", "v229", "v230", "v231", "v232", "v233", "v234", "v235", "v236", "v237", "v238", "v239", "v246", "v247", "v248", "v249", "v250", "v251", "v252", "s40", "s41", "s42", "s43", "s44", "s45", "s46", "s47", "s48", "s49", "s50", "s51", "s52", "s53", "s54", "s55", "s56", "s57", "s58", "s59", "s60", "s61", "s62", "s63", "s64", "s65", "s66", "s67", "s68", "s69", "s70", "s71", "s72", "s73", "s74", "s75", "s76", "s77", "s78", "s79"
__device__ __forceinline__ unsigned UNI(unsigned x) {
    unsigned r;
    asm volatile("s_nop 0\n\tv_readfirstlane_b32 %0, %1\n\ts_nop 4" : "=s"(r) : "v"(x));
    return r;
}
__device__ __forceinline__ void note_timeout(const Params& P, unsigned bud) {
    if (bud == 0) __builtin_amdgcn_raw_buffer_store_b32(1u, mkrsrc(P.ws + FL0, 512), 0x100, 0, 16);
}

#define LOOP_IO [bud] "=s"(bud), [mb] "+s"(mb), [pb] "+s"(pb), [fl] "+s"(fl), "+{v253}"(l16), "+{v240}"(W.a[0]), "+{v241}"(W.a[1]), "+{v242}"(W.a[2]), "+{v243}"(W.a[3]), "+{v244}"(W.a[4]), "+{v245}"(W.a[5])
#define LOOP_IN "{v254}"(b2), "{v255}"(dsto), [wave] "s"(wave), [ws] "s"(P.ws), [mo] "s"(FL_MB + blockIdx.x * 128 + pw * 4), [sz] "s"(RG_L), [lx] "s"(L << 13), [wr] "s"(wdesc(P, wave, isv, h)), [wm] "s"(UNI(wstage(pw, isv))), \
                [cj] "s"(REGS - 75 * WIN), [cs] "s"(5 * WIN)
struct WLoad { unsigned a[6]; };
__device__ __forceinline__ rsrc_t wdesc(const Params& P, int wave, bool isv, int h) {
    const unsigned wo = (isv && wave >= 4) ? h * DH * DM + (wave & 3) * 160 : h * DH;
    typedef const __attribute__((address_space(4))) unsigned long long* kptr;
    static_assert(__builtin_offsetof(Params, wq) == 24 && __builtin_offsetof(Params, wo) == 48, "argument order");
    const unsigned long long w = ((kptr)__builtin_amdgcn_kernarg_segment_ptr())[3 + (isv ? 2 : 0) + (wave >> 2)];
    return mkrsrc((const bf16_t*)w + wo, (DM * HD - wo) * 2);
}
__device__ __forceinline__ unsigned wstage(int wave, bool isv) { return (isv && wave >= 4) ? L_W + (wave & 3) * 20480 : wave * 3072; }
template <bool START>
__device__ __forceinline__ void wload_start(WLoad& W, const Params& P, int wave, bool isv, int h) {
    const int ln = lane_id(), w4 = wave & 3;
    const rsrc_t wr = wdesc(P, wave, isv, h);
    const unsigned wm = UNI(wstage(wave, isv));
    if (isv && wave >= 4) {
#pragma unroll
        for (int k = 0; k < 5; ++k) { const unsigned u = k * 64 + ln; W.a[k] = (u / 20) * (DM * 2) + (u % 20) * 16 - (k & 3) * 1024; }
#pragma unroll 1
        for (int k = 0; k < (START ? 4 : 0); ++k)
            asm volatile("s_mov_b32 m0, %7\n\ts_nop 0\n\tbuffer_load_dwordx4 %0, %5, %6 offen nt lds\n\tbuffer_load_dwordx4 %1, %5, %6 offen offset:1024 nt lds\n\tbuffer_load_dwordx4 %2, %5, %6 offen offset:2048 nt lds\n\t"
                         "buffer_load_dwordx4 %3, %5, %6 offen offset:3072 nt lds\n\ts_mov_b32 m0, %8\n\ts_nop 0\n\tbuffer_load_dwordx4 %4, %5, %6 offen nt lds"
                         :: "v"(W.a[0]), "v"(W.a[1]), "v"(W.a[2]), "v"(W.a[3]), "v"(W.a[4]), "s"(wr), "s"(k * 20480), "s"(wm + k * 5120), "s"(wm + k * 5120 + 4096) : "memory");
        W.a[5] = L_W + w4 * 20480 + (ln >> 2) * 320 + (ln & 3) * 8;
    } else {
        const unsigned sl = isv ? 12288 : 24576;
#pragma unroll
        for (int k = 0; k < 3; ++k) { const unsigned u = (w4 * 3 + k) * 64 + ln; W.a[k] = (u / 24) * (HD * 2) + (u % 24) * 16 - k * 1024; }
#pragma unroll 1
        for (int k = 0; k < (START ? 5 : 0); ++k)
            asm volatile("s_mov_b32 m0, %5\n\ts_nop 0\n\tbuffer_load_dwordx4 %0, %3, %4 offen nt lds\n\tbuffer_load_dwordx4 %1, %3, %4 offen offset:1024 nt lds\n\tbuffer_load_dwordx4 %2, %3, %4 offen offset:2048 nt lds"
                         :: "v"(W.a[0]), "v"(W.a[1]), "v"(W.a[2]), "s"(wr), "s"(k * 32 * HD * 2), "s"(wm + (k < 2 ? k + 4 : k - 2) * sl) : "memory");
        W.a[3] = (wave >> 2) * 12288 + ((ln >> 4) * 8 + ((ln >> 2) & 3)) * 384 + (ln & 3) * 8 + w4 * 96;
        W.a[4] = 0;
        W.a[5] = 0;
    }
}
#define PERM 0x78
#define PERMV 0x87
__device__ __forceinline__ int deal(int pw, int perm, unsigned a) {
    unsigned hw;
    asm volatile("s_getreg_b32 %0, hwreg(HW_REG_HW_ID, 4, 2)" : "=s"(hw));
    lds_vst(a + pw * 4, 1u << (hw + (pw & 4)));
    bar();
    const unsigned cov = lds_vld(a) | lds_vld(a + 4) | lds_vld(a + 8) | lds_vld(a + 12) | lds_vld(a + 16) | lds_vld(a + 20) | lds_vld(a + 24) | lds_vld(a + 28);
    return UNI(cov == 0xff && perm >= 0 ? (pw & 4) + ((perm >> (2 * hw)) & 3) : pw);
}
__device__ __forceinline__ void s_role(const Params& P, int pw, int L, int h, int cpy) {
    const int ln = lane_id(), wave = deal(pw, PERM, MEGA_LDS - 32);
    unsigned l16 = ln * 16;
    constexpr bool isv = false;
    WLoad W;
    wload_start<false>(W, P, pw, isv, h);
    W.a[3] += (wave - pw) * 96;
    const int mt = (wave >> 1) & 1, hf = wave & 1;
    const unsigned xs = D_XN + mt * 4096 + hf * WIN, x1 = (2 - hf) * WIN + hf * 2048, m0 = L_X + mt * 20480 + hf * 4096, m1 = L_X + mt * 20480 + 8192 + hf * 2048;
    const float SCL = 0.07216878364870322f * 1.4426950408889634f;
    const unsigned px = RG_PX + (h >> 2) * WIN;
    unsigned mb = UNI(blk(10, cpy)), fl = UNI(blk(15, cpy)), pb = UNI(wave == 0 ? blk(32 + (h >> 2), 0) + (h & 3) * 640 : px + (wave == 2 ? (h & 3) * 1024 : 5120 + ((h >> 1) & 1) * 1024 + (h & 1) * 256));
    unsigned b2 = l16 + (wave < 3 ? L_Q : wave == 3 ? L_QX : L_W + wave * 10240), dsto = c1_dst(wave, 1 + (wave >> 2)), bud;
    asm volatile(ASM_S_LOOP ".Lcode_mark:" : LOOP_IO
                 : LOOP_IN, [xs] "s"(xs), [x1] "s"(x1), [m0] "s"(m0), [m1] "s"(m1), [qs] "s"(UNI(L_KX + hf * 1024 + mt * 8)), [q2] "s"(UNI(L_KX + 2048 + (wave & 3) * 1024)),
                   [pm] "s"(UNI(px + (wave == 2 ? 4096 : 7168) + (h & 3) * 256)), [scl] "s"(__float_as_uint(SCL))
                 : LOOP_CLOB);
    note_timeout(P, bud);
}

__device__ __forceinline__ void v_role(const Params& P, int pw, int L, int h, int cpy) {
    const int ln = lane_id(), dw = deal(pw, PERMV, L_W - 32), wave = pw < 4 ? dw : pw, w4 = wave & 3;
    unsigned l16 = ln * 16;
    constexpr bool isv = true;
    WLoad W;
    wload_start<false>(W, P, pw, isv, h);
    W.a[3] += (wave - pw) * 96;
    unsigned b2 = l16 + (wave < 3 ? L_Q : wave == 3 ? L_QX : L_W + wave * 10240), dsto = wave < 4 ? c1_dst(wave, 0) : (l16 >> 8) * GRP + (l16 & 255), bud;
    unsigned mb = UNI(wave < 4 ? blk(10, cpy) : blk(h >> 2, w4) + (h & 3) * 1024), fl = UNI(wave < 4 ? blk(15, cpy) : blk(14, w4) + h * 128), pb = UNI(blk(h & 16, w4) + (h & 15) * 256);
    if (wave >= 4) l16 += L_Q;
    asm volatile(ASM_V_LOOP : LOOP_IO
                 : LOOP_IN, [xs] "s"(D_XN + w4 * WIN)
                 : LOOP_CLOB);
    note_timeout(P, bud);
}

#define D_W0 4
#define D_A1 8
#define D_R1 9
#define D_W1 12
__device__ __forceinline__ void delay(int n) { for (int i = 0; i < n; ++i) __builtin_amdgcn_s_sleep(64); }
__device__ __forceinline__ unsigned handshake(rsrc_t rw, unsigned mo) {
    wait_vm<0>();
    unsigned v = SENT;
    for (int spins = 0; spins < SPIN_CAP && v == SENT; ++spins) {
        asm volatile("s_sleep 4" ::: "memory");
        v = __builtin_amdgcn_readfirstlane(__builtin_amdgcn_raw_buffer_load_b32(rw, 0, mo, 16));
    }
    __builtin_amdgcn_raw_buffer_store_b32(1u, rw, lane_id() ? OOB : 32768u, mo, 16);
    return v & 0x7000;
}
template <class F>
__device__ __forceinline__ void x_tile(const Params& P, int tile, F target) {
    const int ln = lane_id(), l15 = ln & 15, l4 = ln >> 4;
    u32x4 v[20];
    float s = 0.f, q = 0.f;
#pragma unroll
    for (int c = 0; c < 20; ++c) v[c] = *(const u32x4*)(P.x + (size_t)(tile * 16 + l15) * DM + c * 32 + l4 * 8);
#pragma unroll
    for (int c = 0; c < 20; ++c)
#pragma unroll
        for (int e = 0; e < 4; ++e) s += lo2f(v[c][e]) + hi2f(v[c][e]);
    sum2_rows(s, q);
#pragma unroll
    for (int c = 0; c < 20; ++c) asm volatile("" : "+v"(v[c]));
    const float mean = s * (1.0f / DM);
#pragma unroll
    for (int c = 0; c < 20; ++c)
#pragma unroll
        for (int e = 0; e < 4; ++e) { const float a = lo2f(v[c][e]) - mean, b = hi2f(v[c][e]) - mean; q += a * a + b * b; }
    sum2_rows(s, q);
#pragma unroll
    for (int c = 0; c < 20; ++c) asm volatile("" : "+v"(v[c]));
    const float m1 = rbf(mean), r = 1.0f / ln_den(q * (1.0f / DM));
    const unsigned sb = segb(tile >> 1) + (tile & 1) * 4096;
    u32x4 n[20];
#pragma unroll
    for (int c = 0; c < 20; ++c) {
#pragma unroll
        for (int e = 0; e < 4; ++e) { const unsigned d = pack2(lo2f(v[c][e]) - m1, hi2f(v[c][e]) - m1); n[c][e] = pack2(lo2f(d) * r, hi2f(d) * r); }
        asm volatile("" : "+v"(n[c]));
    }
    const rsrc_t rr = target();
#pragma unroll
    for (int c = 0; c < 20; ++c) {
        st128(n[c], rr, ln * 16 + (c & 3) * 1024, D_XN + sb + (c >> 2) * WIN);
        st128(v[c], rr, ln * 16 + (c & 3) * 1024, D_XR + sb + (c >> 2) * WIN);
    }
    wait_vm<0>();
}
__device__ __forceinline__ void wait_go(const Params& P, rsrc_t rd, int k) {
    for (int spins = 0;; ++spins) {
        if (__builtin_amdgcn_readfirstlane(__builtin_amdgcn_raw_buffer_load_b32(rd, 0, FL_VO + (k & 31) * 128, 16))) break;
        asm volatile("s_sleep 16" ::: "memory");
        if (spin_fail(spins * 4 + 3, P)) break;
    }
    asm volatile("" ::: "memory");
}

constexpr unsigned R_RED = 0;
constexpr unsigned R_PART = 8192;
__device__ __forceinline__ void r_role(const Params& P, int wave, int L, int j, int cpy) {
    const int lane = lane_id(), l15 = lane & 15, l4 = lane >> 4;
    const unsigned l16 = lane * 16;
    const rsrc_t rr = mkrsrc(lbase(P, L), RG_L);
    const int sidx = (lane & 2) ? 0xEEEE : 0x4444;
    unsigned offp[3];
    const unsigned sel = P.par & 0x5000;
    auto hq = [&](int q) { return blk(32 + q, cpy) ^ sel; };
#define ROT 1
    const int hg = (wave + ROT * j) & 7;
    offp[0] = hq(hg) + (lane >> 5) * 640 + 128 + (lane & 31) * 16;
    offp[1] = offp[0] + 1280;
    offp[2] = (lane & 2) ? OOB : hq(hg) + l4 * 640 + ((l15 >> 2) * 2 + (l15 & 1)) * 16;
    const int jq = j / 10, j10 = j % 10;
    const unsigned ae = wave < 3 ? ((j10 < 8 && wave * 1024 + l16 < 2560) ? (blk(32 + j10, jq) ^ sel) + wave * 1024 + l16 : OOB)
                      : wave == 3 ? (blk(10 + (j10 >> 2), jq) ^ sel) + (j10 & 3) * 1024 + l16
                      : wave < 6 ? (j < 32 ? RG_PX + (j >> 2) * WIN + (j & 3) * 2048 + (wave & 1) * 1024 + l16 : OOB)
                      : wave == 7 ? D_XA + (j >> 3) * WIN + (j & 7) * 512 + (lane >> 5) * 4096 + (lane & 31) * 16 : OOB;
    const unsigned as = wave < 4 ? CH_SLOT : PX_SLOT;
    static_assert(AH == 3, "generated loop");
    const unsigned offv = (blk((hg >> 2) * 16 + j10, jq) ^ sel) + (hg & 3) * 1024 + l16;
    const unsigned offo = (wave < (int)NCP) ? (blk(10 + (j >> 4), wave) ^ sel) + (j & 15) * 256 + lane * 4 : OOB;
    {
        const u32x4 s4 = sent4();
#pragma unroll
        for (int k = 0; k < (int)NSLOT; ++k) {
            st128(s4, rr, offv, k * CH_SLOT);
            if (wave < 6 && (k || wave != 3)) st128(s4, rr, ae, k * as);
        }
        for (int u = j; u < 126; u += 40) st128(s4, rr, wave * 1024 + l16, u < 108 ? D_XA + (u < 60 ? u / 20 : 3) * 4 * REG + (u < 60 ? u % 20 : u - 60) * WIN : u < 124 ? RG_ST + (u - 108) * WIN : D_SS + (u - 124) * WIN);
        wait_vm<0>();
        bar();
        const int row = l4 * 2 + ((lane >> 2) & 1), col = 16 * j + 4 * (lane & 3) + 2 * ((lane >> 3) & 1);
        __builtin_amdgcn_raw_buffer_store_b32(nosent(*(const unsigned*)(P.mem0 + row * DM + col)), rr, offo, 0, 16);
    }
    auto red_addr = [](int fl) { return R_RED + fl * 80 + (fl >> 4) * 16; };
    const unsigned rw0 = red_addr(l4 * 16 + ((l15 >> 1) & 1) * 8 + (l15 >> 2)) + (l15 & 1) * 4 + (hg ^ (l15 >> 3)) * 8;
    const unsigned rrd = red_addr(lane);
    unsigned bud;
    asm volatile(ASM_R_LOOP : [bud] "=s"(bud)
                 : [wave] "s"(wave), [rs] "s"(rr), [l16] "v"(l16), [ov] "v"(offv), [o0] "v"(offp[0]), [o1] "v"(offp[1]), [o2] "v"(offp[2]), [oo] "v"(offo), [sidx] "v"(sidx), [rw] "v"(rw0), [rd] "v"(rrd),
                   [xo] "v"((l4 >> 1) * 256 + l15 * 16 + (l4 & 1) * 8), [pw] "v"(R_PART + hg * 1024 + l16), [pr] "v"(R_PART + (wave & 1) * 8192 + l16), [fo] "v"((lane & 3) * 128), [ae] "v"(ae),
                   [fs] "s"(UNI((blk(14, jq) ^ sel) + hg * 512)), [vx] "s"(UNI(vxb(j10, hg, jq) ^ sel)), [px] "s"(UNI(RG_PX + hg * WIN)),
                   [xr] "s"(UNI(D_XR + (j >> 3) * WIN + (wave & 1) * 4096 + (j & 7) * 512)), [ad] "s"(UNI(wave < 3 ? -1 : wave == 3 ? 0 : -2)), [as] "s"(UNI(as))
                 : "memory", "vcc", "scc", "v128", "v129", "v130", "v131", "v132", "v133", "v134", "v135", "v136", "v137", "v138", "v139", "v140", "v141", "v142", "v143", "v144", "v145", "v146", "v147", "v148", "v149", "v150", "v151", "v152", "v153", "v154", "v155", "v156", "v157", "v158", "v159", "v160", "v161", "v162", "v163", "v164", "v165", "v166", "v167", "v168", "v169", "v170", "v171", "v172", "v173", "v174", "v175", "v176", "v177", "v178", "v179", "v180", "v181", "v182", "v183", "v184", "v185", "v186", "v187", "v188", "v189", "v190", "v191", "v192", "v193", "v194", "v195", "v196", "v197", "v198", "v199", "v200", "v201", "v202", "v203", "v204", "v205", "v206", "v207", "v208", "v209", "v210", "v211", "v212", "v213", "v214", "v215", "v216", "v217", "v218", "v219", "v220", "v221", "v222", "v223", "v224", "v225", "v226", "v227", "v228", "v229", "v230", "v231", "v232", "v233", "v234", "v235", "v236", "v237", "v238", "v239", "v240", "v241", "v242", "v243", "v244", "v245", "v246", "v247", "v248", "v249", "v250", "v251", "v252", "v253", "v254", "v255", "s40", "s41", "s42", "s43", "s44", "s45", "s46", "s47", "s48", "s49", "s50", "s51", "s52", "s53", "s54", "s55", "s64", "s65", "s66", "s67", "s68", "s69", "s70", "s71", "s72", "s73", "s74", "s75", "s76", "s77", "s78", "s79");
    note_timeout(P, bud);
}

template <int WHICH>
__device__ __forceinline__ void w_role(const Params& P, int wave, int L, int cu) {
    constexpr int NT = (WHICH == 3) ? 2 : 1, N = (WHICH == 3) ? FF : DM, RB = NT * 256, RPI = 1024 / RB;
    const int ln = lane_id(), l15 = ln & 15, l4 = ln >> 4;
    const rsrc_t rd = mkrsrc(lbase(P, L), RG_L);
    const int nt0 = (cu * 8 + wave) * NT;
    const rsrc_t wr = mkrsrc((WHICH == 1 ? P.w1 : WHICH == 2 ? P.w2 : P.w3) + cu * (RB / 2), DM * N * 2 - cu * RB);
    auto swz = [](int row) { return ((row & 3) | ((row >> 1) & 4)) * 32; };
    const int dr = ln * 16 / RB, tr = l4 * 8 + ((ln >> 2) & 3);
    unsigned vo = dr * (N * 2) + ((ln * 16 % RB) ^ swz(wave * RPI + dr)), ta = tr * RB + ((wave * NT * 32 + (ln & 3) * 8) ^ swz(tr));
    const int grp = nt0 / 10, neg = grp & 1, sh = (WHICH == 1) ? (neg ? -1 : 1) * ((grp >> 1) + 1) : 0;
    unsigned ra = ln * 16, wa = ln * 16 + wave * 1024, sr = WHICH == 2 ? l15 * 8 : 24576 + l15 * 80, sw = sr + wave * 8, bud;
    const unsigned oo = WHICH == 3 ? (l4 & 1) * 512 + (l4 >> 1) * 256 + l15 * 16 : (nt0 >> 3) * WIN + (nt0 & 7) * 512 + (l4 >> 1) * 256 + ((l15 + sh) & 15) * 16 + (l4 & 1) * 8;
    unsigned up = ((unsigned)(l15 + sh) > 15u) ^ neg;
#define W_ARGS : [bud] "=s"(bud), "+{v176}"(vo), "+{v177}"(ta), "+{v249}"(ra), "+{v250}"(wa), "+{v251}"(sr), "+{v252}"(sw) \
               , "+{v255}"(up) : "{v253}"(ln * 16), "{v254}"(oo), [wave] "s"(wave), [rs] "s"(rd), [wr] "s"(wr), [wm] "s"(UNI(wave * 1024)), [wo] "s"(UNI(wave * RPI * N * 2)), [go] "s"(FL_VO + (WHICH * 8 + cu) * 128), \
                 [pi] "s"(UNI((WHICH == 1 ? D_XA : WHICH == 2 ? D_HS : D_X1) + wave * 1024)), \
                 [ng] "s"(UNI(WHICH == 3 ? D_H2 + (nt0 >> 3) * WIN + ((nt0 >> 1) & 3) * 1024 : neg)), [ss] "s"(UNI(cu == 0)) \
               : "memory", "vcc", "scc", "v0", "v1", "v2", "v3", "v4", "v5", "v6", "v7", "v8", "v9", "v10", "v11", "v12", "v13", "v14", "v15", "v16", "v17", "v18", "v19", "v20", "v21", "v22", "v23", "v24", "v25", "v26", "v27", "v28", "v29", "v30", "v31", "v32", "v33", "v34", "v35", "v36", "v37", "v38", "v39", "v40", "v41", "v42", "v43", "v44", "v45", "v46", "v47", "v48", "v49", "v50", "v51", "v52", "v53", "v54", "v55", "v56", "v57", "v58", "v59", "v60", "v61", "v62", "v63", "v64", "v65", "v66", "v67", "v68", "v69", "v70", "v71", "v72", "v73", "v74", "v75", "v76", "v77", "v78", "v79", "v80", "v81", "v82", "v83", "v84", "v85", "v86", "v87", "v88", "v89", "v90", "v91", "v92", "v93", "v94", "v95", "v96", "v97", "v98", "v99", "v100", "v101", "v102", "v103", "v104", "v105", "v106", "v107", "v108", "v109", "v110", "v111", "v112", "v113", "v114", "v115", "v116", "v117", "v118", "v119", "v120", "v121", "v122", "v123", "v124", "v125", "v126", "v127", "v128", "v129", "v130", "v131", "v132", "v133", "v134", "v135", "v136", "v137", "v138", "v139", "v140", "v141", "v142", "v143", "v144", "v145", "v146", "v147", "v148", "v149", "v150", "v151", "v152", "v153", "v154", "v155", "v156", "v157", "v158", "v159", "v160", "v161", "v162", "v163", "v164", "v165", "v166", "v167", "v168", "v169", "v170", "v171", "v172", "v173", "v174", "v175", "v178", "v179", "v180", "v181", "v182", "v183", "v184", "v185", "v186", "v187", "v188", "v189", "v190", "v191", "v192", "v193", "v194", "v195", "v196", "v197", "v198", "v199", "v200", "v201", "v202", "v203", "v204", "v205", "v206", "v207", "v208", "v209", "v210", "v211", "v212", "v213", "v214", "v215", "v216", "v217", "v218", "v219", "v220", "v221", "v222", "v223", "v224", "v225", "v226", "v227", "v228", "v229", "v230", "v231", "v232", "v233", "v234", "v235", "v236", "v237", "v238", "v239", "v240", "v241", "v242", "v243", "v244", "v245", "v246", "v247", "v248", "s40", "s41", "s42", "s43", "s44", "s45", "s46", "s47", "s48", "s49", "s50", "s51", "s52", "s53", "s54", "s55", "s56", "s57", "s58", "s59", "s60", "s61", "s62", "s63", "s64", "s65", "s66", "s67", "s68", "s69", "s70", "s71", "s72", "s73", "s74", "s75", "s76", "s77", "s78", "s79"
    if (WHICH == 1) asm volatile(ASM_W1_LOOP W_ARGS);
    if (WHICH == 2) asm volatile(ASM_W2_LOOP W_ARGS);
    if (WHICH == 3) asm volatile(ASM_W3_LOOP W_ARGS);
    note_timeout(P, bud);
}

constexpr unsigned W4_FL = 122880;
__device__ __forceinline__ void w4_role(const Params& P, int wave, int L, int cu) {
    const int ln = lane_id(), l15 = ln & 15, l4 = ln >> 4;
    const unsigned l16 = ln * 16;
    const rsrc_t rd = mkrsrc(lbase(P, L), RG_L);
    unsigned bud;
    if (wave >= 6) wait_go(P, rd, 32 + cu);
    if (wave < 6) {
        u32x32 w[5];
        {
            const rsrc_t wr = mkrsrc(P.w4 + wave * 256 * DM + cu * 80, (FF * DM - wave * 256 * DM - cu * 80) * 2);
            const unsigned ta = wave * 20480 + (l4 * 8 + ((ln >> 2) & 3)) * 160 + (ln & 3) * 8;
            u32x4 f[5][8];
#pragma unroll
            for (int r = 0; r < 2; ++r) {
#pragma unroll
                for (int i = 0; i < 5; ++i) {
                    const unsigned u = i * 64 + ln, vo = (u / 10) * (DM * 2) + (u % 10) * 16;
#pragma unroll
                    for (int k = 0; k < 4; ++k) dma1(wr, vo, (r * 128 + k * 32) * (DM * 2), wave * 20480 + k * 5120 + i * 1024);
                }
                wait_vm<0>();
#pragma unroll
                for (int k = 0; k < 4; ++k)
#pragma unroll
                    for (int n = 0; n < 5; ++n) f[n][r * 4 + k] = tr_frag(ta + k * 5120 + n * 32, 640);
                asm volatile("s_waitcnt lgkmcnt(0)" ::: "memory");
                SB();
            }
#pragma unroll
            for (int n = 0; n < 5; ++n) {
                u32x8 g[4];
#pragma unroll
                for (int i = 0; i < 4; ++i) g[i] = __builtin_shufflevector(f[n][2 * i], f[n][2 * i + 1], 0, 1, 2, 3, 4, 5, 6, 7);
                w[n] = cat32(g[0], g[1], g[2], g[3]);
            }
            asm volatile("" : "+{v[0:31]}"(w[0]), "+{v[32:63]}"(w[1]), "+{v[64:95]}"(w[2]), "+{v[96:127]}"(w[3]), "+{v[128:159]}"(w[4]));
        }
        wait_go(P, rd, 32 + cu);
        bar();
        unsigned ld = l16 + wave * 1024;
        asm volatile(ASM_W4_MUL : [bud] "=s"(bud), [ld] "+v"(ld)
                     : "{v[0:31]}"(w[0]), "{v[32:63]}"(w[1]), "{v[64:95]}"(w[2]), "{v[96:127]}"(w[3]), "{v[128:159]}"(w[4]), [l2] "v"(2 * ld + 61440), [l16] "v"(l16), [fa] "v"(W4_FL + wave * 4), [rs] "s"(rd),
                       [hb] "s"(UNI(D_H2 + wave * 2 * WIN)), [wave] "s"(wave), [ro] "s"(UNI(wave * 5120)), [fp] "v"(W4_FL + (ln < 6 ? ln : 0) * 4)
                     : "memory", "vcc", "scc", "v160", "v161", "v162", "v163", "v164", "v165", "v166", "v167", "v168", "v169", "v170", "v171", "v172", "v173", "v174", "v175", "v176", "v177", "v178", "v179", "v180", "v181", "v182", "v183", "v184", "v185", "v186", "v187", "v188", "v189", "v190", "v191", "v192", "v193", "v194", "v195", "v196", "v197", "v198", "v199", "v200", "v201", "v202", "v203", "v204", "v205", "v206", "v207", "v208", "v209", "v210", "v211", "v212", "v213", "v214", "v215", "v216", "v217", "v218", "v219", "v220", "v221", "v222", "v223", "v224", "v225", "v226", "v227", "v228", "v229", "v230", "v231", "v232", "v233", "v234", "v235", "v236", "v237", "v238", "v239", "v240", "v241", "v242", "v243", "v244", "v245", "v246", "v247", "s40", "s41", "s42", "s43", "s44", "s45", "s46", "s47", "s48", "s49", "s50", "s51", "s52", "s53", "s54", "s55", "s64", "s65", "s66", "s67", "s68", "s69", "s70", "s71", "s72", "s73", "s74", "s75", "s76", "s77", "s78", "s79");
    } else {
        const int mt = wave - 6;
        if (ln < 16) lds_vst(W4_FL + ln * 4, 0u);
        bar();
        auto tl = [&](int n) { return (unsigned)((cu * 5 + n) >> 3) * WIN + ((cu * 5 + n) & 7) * 512 + mt * 4096; };
        const unsigned o8 = (l4 >> 1) * 256 + l15 * 16 + (l4 & 1) * 8;
        unsigned la = mt * 30720 + l16;
        static_assert(RG_ST == 112 * WIN && D_XN == 10 * REG && D_XR == 6 * REG, "generated loop");
#define F_IN [l2] "v"(2 * la + 61440), [fa] "v"(W4_FL + (ln < 6 ? ln : 0) * 4), [fb] "v"(W4_FL + 32 + (ln < 5 ? ln : 0) * 4), [fc] "v"(W4_FL + 24 + mt * 4), [rs] "s"(rd), [mt] "s"(mt), \
             [t0] "v"(o8 + tl(0)), [t1] "v"(o8 + tl(1)), [t2] "v"(o8 + tl(2)), [t3] "v"(o8 + tl(3)), [t4] "v"(o8 + tl(4))
#define F_CLOB "memory", "vcc", "scc", "v64", "v65", "v66", "v67", "v68", "v69", "v70", "v71", "v72", "v73", "v74", "v75", "v76", "v77", "v78", "v79", "v80", "v81", "v82", "v83", "v84", "v85", "v86", "v87", "v88", "v89", "v90", "v91", "v92", "v93", "v94", "v95", "v96", "v97", "v98", "v99", "v100", "v101", "v102", "v103", "v104", "v105", "v106", "v107", "v108", "v109", "v110", "v111", "v112", "v113", "v114", "v115", "v116", "v117", "v118", "v119", "v120", "v121", "v122", "v123", "v124", "v125", "v126", "v127", "v128", "v129", "v130", "v131", "v132", "v133", "v134", "v135", "v136", "v137", "v138", "v139", "v140", "v141", "v142", "v143", "v144", "v145", "v146", "v147", "v148", "v149", "v150", "v151", "v152", "v153", "v154", "v155", "v156", "v157", "v158", "v159", "v160", "v161", "v162", "v163", "v164", "v165", "v166", "v167", "v168", "v169", "v170", "v171", "v172", "v173", "v174", "v175", "v176", "v177", "v178", "v179", "v180", "v181", "v182", "v183", "v184", "v185", "v186", "v187", "v188", "v189", "v190", "v191", "v192", "v193", "v194", "v195", "v196", "v197", "v198", "v199", "v200", "v201", "v202", "v203", "v204", "v205", "v206", "v207", "v208", "v209", "v210", "v211", "v212", "v213", "v214", "v215", "v216", "v217", "v218", "v219", "v220", "v221", "v222", "v223", "v224", "v225", "v226", "v227", "v228", "v229", "v230", "v231", "v232", "v233", "v234", "v235", "v236", "v237", "v238", "v239", "s40", "s41", "s42", "s43", "s44", "s45", "s46", "s47", "s48", "s49", "s50", "s51", "s52", "s53", "s54", "s55", "s64", "s65", "s66", "s67", "s68", "s69", "s70", "s71", "s72", "s73", "s74", "s75", "s76", "s77", "s78", "s79"
        if (L) asm volatile(ASM_W4_FIN1 : [bud] "=s"(bud), [la] "+v"(la)
                            : F_IN, [ro] "s"(mkrsrc(P.out, ACT)), [co] "s"(cu * 160), [q8] "v"(l15 * (DM * 2) + l4 * 8), [q16] "v"(l15 * (DM * 2) + (l4 & 1) * 32 + (l4 >> 1) * 16) : F_CLOB);
        else asm volatile(ASM_W4_FIN0 : [bud] "=s"(bud), [la] "+v"(la)
                          : F_IN, [rn] "s"(mkrsrc(lbase(P, 1), RG_L)), [co] "s"(cu * 128), [l8] "v"(ln * 8), [o16] "v"(tl(l4 & 1) + (l4 >> 1) * 256 + l15 * 16), [o17] "v"(tl(2 + (l4 & 1)) + (l4 >> 1) * 256 + l15 * 16),
                            [fo] "v"(ln < 32 ? blk(0, ln >> 3) - CH0 + (ln & 7) * 16 : OOB), [fs] "s"(FL_XN + (cu * 2 + mt) * 128) : F_CLOB);
    }
    note_timeout(P, bud);
}

constexpr int LEADER = 252;
__device__ __forceinline__ unsigned leader(const Params& P, rsrc_t rw, int wave) {
    const int ln = lane_id();
    const u32x4 s4 = sent4(), z4 = s4 ^ s4;
    const unsigned mo = (wave * 32 + (ln >> 1)) * 128 + (ln & 1) * 16;
    __builtin_amdgcn_raw_buffer_store_b32(0u, rw, ln < 2 ? ln * 2048 : OOB, CAL0 + wave * 4096, 16);
    for (int k = 0; k < 6; ++k) st128(z4, rw, ln * 16, FL_VO + (k >> 2) * GRP + wave * 4096 + (k & 3) * 1024);
    st128(z4, rw, mo, FL_MB + 32768);
    if (wave == 0) st128(z4, rw, ln ? OOB : 256u, FL0);
    wait_vm<0>();
    bar();
    if (wave == 0) {
        const unsigned o = ln ? OOB : 0u;
        auto trip = [&](unsigned a) {
            unsigned long long t0, t1;
            asm volatile("s_memtime %0\n\ts_waitcnt lgkmcnt(0)\n\tbuffer_store_dword %2, %2, %3, %4 offen sc1\n\ts_waitcnt vmcnt(0)\n\ts_memtime %1\n\ts_waitcnt lgkmcnt(0)"
                         : "=&s"(t0), "=&s"(t1) : "v"(o), "s"(rw), "s"(CAL0 + a) : "memory");
            return (unsigned)(t1 - t0);
        };
        auto mn = [](unsigned a, unsigned b) { return a < b ? a : b; };
        const unsigned a0 = trip(0), b0 = trip(0x2000), a1 = trip(0x800), b1 = trip(0x2800);
        const unsigned h = mn(a0, a1) < mn(b0, b1) ? 0u : 0x2000u, t0 = h ? b0 + b1 : a0 + a1;
        const unsigned t14 = trip(h + 0x4000) + trip(h + 0x4800), t12 = trip(h + 0x1000) + trip(h + 0x1800);
        lds_vst(0, (h ^ 0x2000u) | (t14 < t0 ? 0x4000u : 0u) | (t12 < t0 ? 0x1000u : 0u));
    }
    bar();
    const unsigned par = __builtin_amdgcn_readfirstlane(lds_vld(0));
    for (int k = 0; k < 10; ++k) st128(s4, rw, ln * 16, CH0 + (10 + (k >> 2)) * GRP + wave * 4096 + (k & 3) * 1024);
    wait_vm<0>();
    bar();
    bool open = wave * 32 + (ln >> 1) != LEADER;
    for (int spins = 0; spins < SPIN_CAP; ++spins) {
        st128(s4 & par, rw, open ? mo : OOB, FL_MB);
        __builtin_amdgcn_s_sleep(20);
        const u32x4 d = LD128(rw, mo, FL_MB + 32768);
        open = open && !(d[0] && d[1] && d[2] && d[3]);
        if (!__any(open)) break;
    }
    bar();
    return par;
}
__global__ __launch_bounds__(512, 2) void mega_kernel(Params P) {
    const int wg = blockIdx.x;
    const int wave = __builtin_amdgcn_readfirstlane(threadIdx.x >> 6);
    const int L = (wg >> 2) & 1, x = wg & 3, r = wg >> 3, cpy = x;
    if (__builtin_expect(r < 16, 1)) {
        WLoad W;
        if (L) delay(D_A1);
        wload_start<true>(W, P, wave, r >= 8, x * 8 + (r & 7));
    }
    constexpr int nr = 10;
    const int j = x * 10 + r - 16, wk = r - 26;
    const int i = x == 0 ? 16 + wk : x == 2 ? 10 + wk : wk < 5 ? (x == 1 ? 5 : 0) + wk : 22 + (x >> 1);
    const rsrc_t rw = mkrsrc(P.ws, RG_L);
    const bool lead = wg == LEADER;
    if (!lead) __builtin_amdgcn_raw_buffer_store_b32(SENT, rw, lane_id() ? OOB : 0u, FL_MB + wg * 128 + wave * 4, 16);
#define FIN() __builtin_amdgcn_endpgm()
    if (r < 8) { s_role(P, wave, L, x * 8 + r, cpy); FIN(); }
    if (r < 16) { v_role(P, wave, L, x * 8 + r - 8, cpy); FIN(); }
    if (r == 30) {
        unsigned long long pc;
        unsigned len;
        asm volatile("s_getpc_b64 %0\n\t.Lcp%=:\n\ts_mov_b32 %1, .Lcode_mark-.Lcp%=" : "=s"(pc), "=s"(len));
        if ((int)len < 0) len = 0;
        const rsrc_t rc = mkrsrc((const void*)pc, len);
        for (unsigned o = wave * 1024; o < len; o += 8192) asm volatile("s_mov_b32 m0, %2\n\ts_nop 0\n\tbuffer_load_dwordx4 %0, %1, 0 offen lds" :: "v"(lane_id() * 16 + o), "s"(rc), "s"(65536) : "memory");
    }
    if (lead) P.par = leader(P, rw, wave);
    else {
        const unsigned mo = FL_MB + wg * 128 + wave * 4;
        const bool ln = r - 16 < nr && j < 16, first = ln && !L && wave == 7;
        if (!first) { P.par = handshake(rw, mo); if (L || r - 16 >= nr) delay(!L ? D_W0 : r - 16 < nr ? D_R1 : D_W1); }
        if (first || (ln && L && wave)) x_tile(P, (L ? wave * 16 : 0) + j, [&]() { if (first) P.par = handshake(rw, mo); return mkrsrc(lbase(P, 0), RG_L); });
        if (ln && L) bar();
        if (first || (ln && L && !wave)) {
            const rsrc_t r0 = mkrsrc(lbase(P, 0), RG_L);
            const int lane = lane_id();
            const unsigned fo = lane < 32 ? blk(0, lane >> 3) - CH0 + (lane & 7) * 16 : OOB;
            if (L) for (int spins = 0; spins < SPIN_CAP && __any(lane < 32 && LD128(r0, fo, FL_XN + j * 128)[0] == 0u); ++spins) asm volatile("s_sleep 8" ::: "memory");
            st128(sent4() & (L ? 64u : 8u), r0, fo, FL_XN + j * 128);
        }
    }
    if (r - 16 < nr) { r_role(P, wave, L, j, cpy); FIN(); }
    if (i < 5) { w_role<1>(P, wave, L, i); FIN(); }
    if (i < 10) { w_role<2>(P, wave, L, i - 5); FIN(); }
    if (i < 16) { w_role<3>(P, wave, L, i - 10); FIN(); }
    w4_role(P, wave, L, i - 16);
    FIN();
}

extern "C" void kernel_launch(void* const* d_in, const int* in_sizes, int n_in, void* d_out, int out_size, void* d_ws,
                              size_t ws_size, hipStream_t stream) {
    char* ws = (char*)d_ws + (-(size_t)d_ws & (REG - 1));
    Params P;
    P.par = 0; P.pad = 0; P.ws = ws; P.out = (bf16_t*)d_out;
    P.x = (const bf16_t*)d_in[0]; P.mem0 = (const bf16_t*)d_in[1]; P.wq = (const bf16_t*)d_in[2]; P.wk = (const bf16_t*)d_in[3]; P.wv = (const bf16_t*)d_in[4]; P.wo = (const bf16_t*)d_in[5];
    P.w1 = (const bf16_t*)d_in[8]; P.w2 = (const bf16_t*)d_in[9]; P.w3 = (const bf16_t*)d_in[10]; P.w4 = (const bf16_t*)d_in[11];
    (void)hipFuncSetAttribute((const void*)mega_kernel, hipFuncAttributeMaxDynamicSharedMemorySize, MEGA_LDS);

    mega_kernel<<<256, 512, MEGA_LDS, stream>>>(P);
}
```

```cpp
#include <hip/hip_runtime.h>
#include <stdint.h>

typedef unsigned short bf16_t;
using bf16x2 = __attribute__((ext_vector_type(2))) __bf16;
using f32x2 = __attribute__((ext_vector_type(2))) float;
using u32x2 = __attribute__((ext_vector_type(2))) unsigned;
using u32x4 = __attribute__((ext_vector_type(4))) unsigned;
typedef __amdgpu_buffer_rsrc_t rsrc_t;

__device__ __forceinline__ unsigned pack2(float a, float b) {
    bf16x2 r = __builtin_convertvector((f32x2){a, b}, bf16x2);
    return __builtin_bit_cast(unsigned, r);
}
__device__ __forceinline__ float rbf(float f) { return __uint_as_float(pack2(f, 0.f) << 16); }
__device__ __forceinline__ float lo2f(unsigned u) { return __uint_as_float(u << 16); }
__device__ __forceinline__ float hi2f(unsigned u) { return __uint_as_float(u & 0xFFFF0000u); }
__device__ __forceinline__ rsrc_t mkrsrc(const void* p, unsigned bytes) {
    return __builtin_amdgcn_make_buffer_rsrc((void*)p, 0, bytes, 0x00020000);
}

constexpr int S_ = 2048, DM = 640, NH = 32, DH = 192, HD = NH * DH  , SEG = 32, NSEG = 64;
constexpr int FF = 1536;

constexpr unsigned NSLOT = 4, NCP = 4;
constexpr unsigned WIN = 16384, REG = 2u << 20, REGS = REG + 8192;
constexpr unsigned GRP = 32768, CH0 = 48 * REG, CH_SLOT = 2 * REG;
__host__ __device__ constexpr unsigned blk(unsigned grp, unsigned x) { return CH0 + grp * GRP + (((x ^ (grp >> 4)) & 1) << 14) + ((((x >> 1) ^ (grp >> 5)) & 1) << 12); }
constexpr unsigned PX_SLOT = 8 * WIN;
constexpr unsigned RG_PX = 80 * WIN, RG_ST = RG_PX + NSLOT * PX_SLOT;
static_assert(RG_ST + 16 * WIN <= REG, "ring region");
constexpr unsigned FL0 = REG, FL_PAR = REG + 4096, FL_MB = REG + 65536;
constexpr unsigned FL_VO = blk(14, 0), FL_XN = blk(15, 0), D_SS = 2 * REG + 52 * WIN, CAL0 = blk(13, 0);
constexpr unsigned ACT = S_ * DM * 2;
constexpr unsigned D_XR = 6 * REG, D_XN = 10 * REG, D_XA = 14 * REG, D_HS = 18 * REG, D_X1 = 22 * REG, D_H2 = 26 * REG;
__host__ __device__ constexpr unsigned segb(unsigned seg) { return seg * (5 * WIN) + (seg >> 4) * (REGS - 80 * WIN); }
__host__ __device__ constexpr unsigned segh(unsigned seg) { return seg * (12 * WIN) + (seg >> 3) * (REGS - 96 * WIN); }
__host__ __device__ constexpr unsigned vxb(unsigned tile, unsigned hq, unsigned x) { return tile < 8 ? blk(tile * 8 + hq, x) + NSLOT * CH_SLOT : blk(tile * 8 - 16 + hq, x); }
constexpr int AH = 3;
constexpr unsigned RG_L = CH0 + 2 * NSLOT * CH_SLOT;

#define SB() __builtin_amdgcn_sched_barrier(0)
#define LAS __attribute__((address_space(3)))
__device__ __forceinline__ unsigned lds_vld(unsigned a) { asm volatile("" ::: "memory"); const unsigned v = *(volatile LAS unsigned*)(size_t)a; asm volatile("" ::: "memory"); return v; }
__device__ __forceinline__ void lds_vst(unsigned a, unsigned v) { asm volatile("" ::: "memory"); *(volatile LAS unsigned*)(size_t)a = v; asm volatile("" ::: "memory"); }
constexpr unsigned OOB = 0x7FFFFF00u;
template <int N> __device__ __forceinline__ void wait_vm() { asm volatile("s_waitcnt vmcnt(%0)" :: "i"(N) : "memory"); }
__device__ __forceinline__ void bar() {
    asm volatile("s_waitcnt lgkmcnt(0)" ::: "memory");
    __builtin_amdgcn_s_barrier();
    asm volatile("" ::: "memory");
}
__device__ __forceinline__ int lane_id() {
    int ln;
    asm volatile("v_mbcnt_lo_u32_b32 %0, -1, 0\n\tv_mbcnt_hi_u32_b32 %0, -1, %0" : "=v"(ln));
    return ln;
}
__device__ __forceinline__ void sum2_rows(float& s, float& q) {
    unsigned a = __float_as_uint(s), b = a, c = __float_as_uint(q), d = c;
    asm volatile("s_nop 2\n\tv_permlane16_swap_b32 %0, %1\n\tv_permlane16_swap_b32 %2, %3\n\tv_add_f32 %0, %0, %1\n\tv_add_f32 %2, %2, %3\n\tv_mov_b32 %1, %0\n\tv_mov_b32 %3, %2\n\t"
                 "s_nop 1\n\tv_permlane32_swap_b32 %0, %1\n\tv_permlane32_swap_b32 %2, %3\n\tv_add_f32 %0, %0, %1\n\tv_add_f32 %2, %2, %3" : "+v"(a), "+v"(b), "+v"(c), "+v"(d));
    s = __uint_as_float(a); q = __uint_as_float(c);
}
template <int AUXV>
__device__ __forceinline__ void st128_(u32x4 v, rsrc_t r, unsigned voff, unsigned soff) {
    __builtin_amdgcn_raw_buffer_store_b128(v, r, voff, soff, AUXV);
    asm volatile("s_nop 1" :: "v"(v) : "memory");
}
#define st128(V, R, VO_, SO_) st128_<16>(V, R, VO_, SO_)
#define LD128(R, VO_, SO_) __builtin_amdgcn_raw_buffer_load_b128(R, VO_, SO_, 16)
__device__ __forceinline__ float ln_den(float var) { return rbf(__builtin_amdgcn_sqrtf(rbf(rbf(var) + 1e-5f))); }

struct Params {
    char* ws;
    const bf16_t *x, *mem0, *wq, *wk, *wv, *wo, *w1, *w2, *w3, *w4;
    bf16_t* out;
    unsigned par, pad;
};

#define SENT 0xFFFFFFFFu
constexpr int SPIN_CAP = 400000;
__device__ __forceinline__ bool spin_fail(int spins, const Params& P) {
    if ((spins & 1023) == 1023) {
        const rsrc_t rt = mkrsrc(P.ws + FL0, 512);
        unsigned o;
        asm volatile("v_mov_b32 %0, 0x100" : "=v"(o));
        if (__builtin_amdgcn_readfirstlane(__builtin_amdgcn_raw_buffer_load_b32(rt, o, 0, 16)) != 0u) return true;
        if (spins > SPIN_CAP) { __builtin_amdgcn_raw_buffer_store_b32(o, rt, o, 0, 16); return true; }
    }
    return false;
}
__device__ __forceinline__ unsigned nosent(unsigned d) { return d < 0xFFFFFFFEu ? d : 0xFFFFFFFEu; }
__device__ __forceinline__ u32x4 sent4() { unsigned sv; asm volatile("v_mov_b32 %0, -1" : "=v"(sv)); return (u32x4){sv, sv, sv, sv}; }
__device__ __forceinline__ char* lbase(const Params& P, int L) { return P.ws + ((P.par ^ (L << 13)) & 0x2000); }

constexpr unsigned L_X = 0;
constexpr unsigned L_MEM = 40960;
constexpr unsigned L_Q = 51200;
constexpr unsigned L_KM = 54272;
constexpr unsigned L_QX = 57344;
constexpr unsigned L_KX = 69632;
constexpr unsigned L_W = 81920;
constexpr int MEGA_LDS = 163840;

#define ASM_S_LOOP \
    "s_mov_b64 s[60:61], %[ws]\n\t" \
    "s_mov_b32 s62, %[sz]\n\t" \
    "s_mov_b32 s63, 0x20000\n\t" \
    "s_mov_b32 s40, 0\n\t" \
    "s_mov_b32 s41, 0x20000\n\t" \
    "s_cmp_eq_u32 %[wave], 0\n\t" \
    "s_cbranch_scc1 .Ls0_%=\n\t" \
    "s_cmp_eq_u32 %[wave], 1\n\t" \
    "s_cbranch_scc1 .Ls1_%=\n\t" \
    "s_cmp_eq_u32 %[wave], 2\n\t" \
    "s_cbranch_scc1 .Ls2_%=\n\t" \
    "s_cmp_eq_u32 %[wave], 3\n\t" \
    "s_cbranch_scc1 .Ls3_%=\n\t" \
    ".Ls4_%=:\n\t" \
    "s_mov_b32 s42, %[xs]\n\t" \
    "v_add_u32 v244, 0x18000, v243\n\t" \
    "s_waitcnt vmcnt(12) lgkmcnt(0)\n\t" \
    "s_barrier \n\t" \
    "s_add_u32 m0, %[wm], 0x12000\n\t" \
    "s_mov_b32 s44, 0x1e0000\n\t" \
    "buffer_load_dwordx4 v240, %[wr], s44 offen offset:0 nt lds\n\t" \
    "buffer_load_dwordx4 v241, %[wr], s44 offen offset:1024 nt lds\n\t" \
    "buffer_load_dwordx4 v242, %[wr], s44 offen offset:2048 nt lds\n\t" \
    "ds_read_b64_tr_b16 v[200:201], v244 offset:0\n\t" \
    "ds_read_b64_tr_b16 v[202:203], v244 offset:1536\n\t" \
    "ds_read_b64_tr_b16 v[0:1], v244 offset:32\n\t" \
    "ds_read_b64_tr_b16 v[2:3], v244 offset:1568\n\t" \
    "ds_read_b64_tr_b16 v[8:9], v244 offset:64\n\t" \
    "ds_read_b64_tr_b16 v[10:11], v244 offset:1600\n\t" \
    "v_add_u32 v244, 0x1e000, v243\n\t" \
    "s_waitcnt vmcnt(12) lgkmcnt(0)\n\t" \
    "s_barrier \n\t" \
    "s_add_u32 m0, %[wm], 0x18000\n\t" \
    "s_mov_b32 s44, 0x240000\n\t" \
    "buffer_load_dwordx4 v240, %[wr], s44 offen offset:0 nt lds\n\t" \
    "buffer_load_dwordx4 v241, %[wr], s44 offen offset:1024 nt lds\n\t" \
    "buffer_load_dwordx4 v242, %[wr], s44 offen offset:2048 nt lds\n\t" \
    "ds_read_b64_tr_b16 v[204:205], v244 offset:0\n\t" \
    "ds_read_b64_tr_b16 v[206:207], v244 offset:1536\n\t" \
    "ds_read_b64_tr_b16 v[4:5], v244 offset:32\n\t" \
    "ds_read_b64_tr_b16 v[6:7], v244 offset:1568\n\t" \
    "ds_read_b64_tr_b16 v[12:13], v244 offset:64\n\t" \
    "ds_read_b64_tr_b16 v[14:15], v244 offset:1600\n\t" \
    "v_add_u32 v244, 0x0, v243\n\t" \
    "s_waitcnt vmcnt(12) lgkmcnt(0)\n\t" \
    "s_barrier \n\t" \
    "s_add_u32 m0, %[wm], 0x1e000\n\t" \
    "s_mov_b32 s44, 0x2a0000\n\t" \
    "buffer_load_dwordx4 v240, %[wr], s44 offen offset:0 nt lds\n\t" \
    "buffer_load_dwordx4 v241, %[wr], s44 offen offset:1024 nt lds\n\t" \
    "buffer_load_dwordx4 v242, %[wr], s44 offen offset:2048 nt lds\n\t" \
    "ds_read_b64_tr_b16 v[16:17], v244 offset:0\n\t" \
    "ds_read_b64_tr_b16 v[18:19], v244 offset:1536\n\t" \
    "ds_read_b64_tr_b16 v[24:25], v244 offset:32\n\t" \
    "ds_read_b64_tr_b16 v[26:27], v244 offset:1568\n\t" \
    "ds_read_b64_tr_b16 v[32:33], v244 offset:64\n\t" \
    "ds_read_b64_tr_b16 v[34:35], v244 offset:1600\n\t" \
    "v_add_u32 v244, 0x6000, v243\n\t" \
    "s_waitcnt vmcnt(12) lgkmcnt(0)\n\t" \
    "s_barrier \n\t" \
    "s_add_u32 m0, %[wm], 0x0\n\t" \
    "s_mov_b32 s44, 0x300000\n\t" \
    "buffer_load_dwordx4 v240, %[wr], s44 offen offset:0 nt lds\n\t" \
    "buffer_load_dwordx4 v241, %[wr], s44 offen offset:1024 nt lds\n\t" \
    "buffer_load_dwordx4 v242, %[wr], s44 offen offset:2048 nt lds\n\t" \
    "ds_read_b64_tr_b16 v[20:21], v244 offset:0\n\t" \
    "ds_read_b64_tr_b16 v[22:23], v244 offset:1536\n\t" \
    "ds_read_b64_tr_b16 v[28:29], v244 offset:32\n\t" \
    "ds_read_b64_tr_b16 v[30:31], v244 offset:1568\n\t" \
    "ds_read_b64_tr_b16 v[36:37], v244 offset:64\n\t" \
    "ds_read_b64_tr_b16 v[38:39], v244 offset:1600\n\t" \
    "v_add_u32 v244, 0xc000, v243\n\t" \
    "s_waitcnt vmcnt(12) lgkmcnt(0)\n\t" \
    "s_barrier \n\t" \
    "s_add_u32 m0, %[wm], 0x6000\n\t" \
    "s_mov_b32 s44, 0x360000\n\t" \
    "buffer_load_dwordx4 v240, %[wr], s44 offen offset:0 nt lds\n\t" \
    "buffer_load_dwordx4 v241, %[wr], s44 offen offset:1024 nt lds\n\t" \
    "buffer_load_dwordx4 v242, %[wr], s44 offen offset:2048 nt lds\n\t" \
    "ds_read_b64_tr_b16 v[208:209], v244 offset:0\n\t" \
    "ds_read_b64_tr_b16 v[210:211], v244 offset:1536\n\t" \
    "ds_read_b64_tr_b16 v[40:41], v244 offset:32\n\t" \
    "ds_read_b64_tr_b16 v[42:43], v244 offset:1568\n\t" \
    "ds_read_b64_tr_b16 v[48:49], v244 offset:64\n\t" \
    "ds_read_b64_tr_b16 v[50:51], v244 offset:1600\n\t" \
    "v_add_u32 v244, 0x12000, v243\n\t" \
    "s_waitcnt vmcnt(12) lgkmcnt(0)\n\t" \
    "s_barrier \n\t" \
    "s_add_u32 m0, %[wm], 0xc000\n\t" \
    "s_mov_b32 s44, 0x3c0000\n\t" \
    "buffer_load_dwordx4 v240, %[wr], s44 offen offset:0 nt lds\n\t" \
    "buffer_load_dwordx4 v241, %[wr], s44 offen offset:1024 nt lds\n\t" \
    "buffer_load_dwordx4 v242, %[wr], s44 offen offset:2048 nt lds\n\t" \
    "ds_read_b64_tr_b16 v[212:213], v244 offset:0\n\t" \
    "ds_read_b64_tr_b16 v[214:215], v244 offset:1536\n\t" \
    "ds_read_b64_tr_b16 v[44:45], v244 offset:32\n\t" \
    "ds_read_b64_tr_b16 v[46:47], v244 offset:1568\n\t" \
    "ds_read_b64_tr_b16 v[52:53], v244 offset:64\n\t" \
    "ds_read_b64_tr_b16 v[54:55], v244 offset:1600\n\t" \
    "v_add_u32 v244, 0x18000, v243\n\t" \
    "s_waitcnt vmcnt(12) lgkmcnt(0)\n\t" \
    "s_barrier \n\t" \
    "s_add_u32 m0, %[wm], 0x12000\n\t" \
    "s_mov_b32 s44, 0x420000\n\t" \
    "buffer_load_dwordx4 v240, %[wr], s44 offen offset:0 nt lds\n\t" \
    "buffer_load_dwordx4 v241, %[wr], s44 offen offset:1024 nt lds\n\t" \
    "buffer_load_dwordx4 v242, %[wr], s44 offen offset:2048 nt lds\n\t" \
    "ds_read_b64_tr_b16 v[56:57], v244 offset:0\n\t" \
    "ds_read_b64_tr_b16 v[58:59], v244 offset:1536\n\t" \
    "ds_read_b64_tr_b16 v[64:65], v244 offset:32\n\t" \
    "ds_read_b64_tr_b16 v[66:67], v244 offset:1568\n\t" \
    "ds_read_b64_tr_b16 v[72:73], v244 offset:64\n\t" \
    "ds_read_b64_tr_b16 v[74:75], v244 offset:1600\n\t" \
    "v_add_u32 v244, 0x1e000, v243\n\t" \
    "s_waitcnt vmcnt(12) lgkmcnt(0)\n\t" \
    "s_barrier \n\t" \
    "s_add_u32 m0, %[wm], 0x18000\n\t" \
    "s_mov_b32 s44, 0x480000\n\t" \
    "buffer_load_dwordx4 v240, %[wr], s44 offen offset:0 nt lds\n\t" \
    "buffer_load_dwordx4 v241, %[wr], s44 offen offset:1024 nt lds\n\t" \
    "buffer_load_dwordx4 v242, %[wr], s44 offen offset:2048 nt lds\n\t" \
    "ds_read_b64_tr_b16 v[60:61], v244 offset:0\n\t" \
    "ds_read_b64_tr_b16 v[62:63], v244 offset:1536\n\t" \
    "ds_read_b64_tr_b16 v[68:69], v244 offset:32\n\t" \
    "ds_read_b64_tr_b16 v[70:71], v244 offset:1568\n\t" \
    "ds_read_b64_tr_b16 v[76:77], v244 offset:64\n\t" \
    "ds_read_b64_tr_b16 v[78:79], v244 offset:1600\n\t" \
    "v_add_u32 v244, 0x0, v243\n\t" \
    "s_waitcnt vmcnt(12) lgkmcnt(0)\n\t" \
    "s_barrier \n\t" \
    "v_mov_b32 v246, 0\n\t" \
    "buffer_load_dword v246, v246, s[60:63], %[mo] offen sc1\n\t" \
    "s_add_u32 m0, %[wm], 0x1e000\n\t" \
    "s_mov_b32 s44, 0x4e0000\n\t" \
    "buffer_load_dwordx4 v240, %[wr], s44 offen offset:0 nt lds\n\t" \
    "buffer_load_dwordx4 v241, %[wr], s44 offen offset:1024 nt lds\n\t" \
    "buffer_load_dwordx4 v242, %[wr], s44 offen offset:2048 nt lds\n\t" \
    "ds_read_b64_tr_b16 v[216:217], v244 offset:0\n\t" \
    "ds_read_b64_tr_b16 v[218:219], v244 offset:1536\n\t" \
    "ds_read_b64_tr_b16 v[80:81], v244 offset:32\n\t" \
    "ds_read_b64_tr_b16 v[82:83], v244 offset:1568\n\t" \
    "ds_read_b64_tr_b16 v[88:89], v244 offset:64\n\t" \
    "ds_read_b64_tr_b16 v[90:91], v244 offset:1600\n\t" \
    "v_add_u32 v244, 0x6000, v243\n\t" \
    "s_waitcnt vmcnt(12) lgkmcnt(0)\n\t" \
    "s_barrier \n\t" \
    "s_add_u32 m0, %[wm], 0x0\n\t" \
    "s_mov_b32 s44, 0x540000\n\t" \
    "buffer_load_dwordx4 v240, %[wr], s44 offen offset:0 nt lds\n\t" \
    "buffer_load_dwordx4 v241, %[wr], s44 offen offset:1024 nt lds\n\t" \
    "buffer_load_dwordx4 v242, %[wr], s44 offen offset:2048 nt lds\n\t" \
    "ds_read_b64_tr_b16 v[220:221], v244 offset:0\n\t" \
    "ds_read_b64_tr_b16 v[222:223], v244 offset:1536\n\t" \
    "ds_read_b64_tr_b16 v[84:85], v244 offset:32\n\t" \
    "ds_read_b64_tr_b16 v[86:87], v244 offset:1568\n\t" \
    "ds_read_b64_tr_b16 v[92:93], v244 offset:64\n\t" \
    "ds_read_b64_tr_b16 v[94:95], v244 offset:1600\n\t" \
    "v_add_u32 v244, 0xc000, v243\n\t" \
    "s_waitcnt vmcnt(12) lgkmcnt(0)\n\t" \
    "s_barrier \n\t" \
    "s_add_u32 m0, %[wm], 0x6000\n\t" \
    "s_mov_b32 s44, 0x5a0000\n\t" \
    "buffer_load_dwordx4 v240, %[wr], s44 offen offset:0 nt lds\n\t" \
    "buffer_load_dwordx4 v241, %[wr], s44 offen offset:1024 nt lds\n\t" \
    "buffer_load_dwordx4 v242, %[wr], s44 offen offset:2048 nt lds\n\t" \
    "ds_read_b64_tr_b16 v[96:97], v244 offset:0\n\t" \
    "ds_read_b64_tr_b16 v[98:99], v244 offset:1536\n\t" \
    "ds_read_b64_tr_b16 v[104:105], v244 offset:32\n\t" \
    "ds_read_b64_tr_b16 v[106:107], v244 offset:1568\n\t" \
    "ds_read_b64_tr_b16 v[112:113], v244 offset:64\n\t" \
    "ds_read_b64_tr_b16 v[114:115], v244 offset:1600\n\t" \
    "v_add_u32 v244, 0x12000, v243\n\t" \
    "s_waitcnt vmcnt(12) lgkmcnt(0)\n\t" \
    "s_barrier \n\t" \
    "s_add_u32 m0, %[wm], 0xc000\n\t" \
    "s_mov_b32 s44, 0x600000\n\t" \
    "buffer_load_dwordx4 v240, %[wr], s44 offen offset:0 nt lds\n\t" \
    "buffer_load_dwordx4 v241, %[wr], s44 offen offset:1024 nt lds\n\t" \
    "buffer_load_dwordx4 v242, %[wr], s44 offen offset:2048 nt lds\n\t" \
    "ds_read_b64_tr_b16 v[100:101], v244 offset:0\n\t" \
    "ds_read_b64_tr_b16 v[102:103], v244 offset:1536\n\t" \
    "ds_read_b64_tr_b16 v[108:109], v244 offset:32\n\t" \
    "ds_read_b64_tr_b16 v[110:111], v244 offset:1568\n\t" \
    "ds_read_b64_tr_b16 v[116:117], v244 offset:64\n\t" \
    "ds_read_b64_tr_b16 v[118:119], v244 offset:1600\n\t" \
    "v_add_u32 v244, 0x18000, v243\n\t" \
    "s_waitcnt vmcnt(12) lgkmcnt(0)\n\t" \
    "s_barrier \n\t" \
    "s_add_u32 m0, %[wm], 0x12000\n\t" \
    "s_mov_b32 s44, 0x660000\n\t" \
    "buffer_load_dwordx4 v240, %[wr], s44 offen offset:0 nt lds\n\t" \
    "buffer_load_dwordx4 v241, %[wr], s44 offen offset:1024 nt lds\n\t" \
    "buffer_load_dwordx4 v242, %[wr], s44 offen offset:2048 nt lds\n\t" \
    "ds_read_b64_tr_b16 v[224:225], v244 offset:0\n\t" \
    "ds_read_b64_tr_b16 v[226:227], v244 offset:1536\n\t" \
    "ds_read_b64_tr_b16 v[120:121], v244 offset:32\n\t" \
    "ds_read_b64_tr_b16 v[122:123], v244 offset:1568\n\t" \
    "ds_read_b64_tr_b16 v[128:129], v244 offset:64\n\t" \
    "ds_read_b64_tr_b16 v[130:131], v244 offset:1600\n\t" \
    "v_add_u32 v244, 0x1e000, v243\n\t" \
    "s_waitcnt vmcnt(12) lgkmcnt(0)\n\t" \
    "s_barrier \n\t" \
    ".Ls41_%=:\n\t" \
    "v_readfirstlane_b32 s43, v246\n\t" \
    "s_nop 0\n\t" \
    "s_cmp_lg_u32 s43, -1\n\t" \
    "s_cbranch_scc1 .Ls42_%=\n\t" \
    "s_cmp_eq_u32 s41, 0\n\t" \
    "s_cbranch_scc1 .Ls42_%=\n\t" \
    "s_sub_u32 s41, s41, 1\n\t" \
    "s_sleep 4\n\t" \
    "v_mov_b32 v246, 0\n\t" \
    "buffer_load_dword v246, v246, s[60:63], %[mo] offen sc1\n\t" \
    "s_waitcnt vmcnt(0)\n\t" \
    "s_branch .Ls41_%=\n\t" \
    ".Ls42_%=:\n\t" \
    "v_mov_b32 v246, 0x8000\n\t" \
    "s_mov_b64 exec, 1\n\t" \
    "buffer_store_dword v246, v246, s[60:63], %[mo] offen sc1\n\t" \
    "s_mov_b64 exec, -1\n\t" \
    "s_and_b32 s44, s43, 0x5000\n\t" \
    "s_xor_b32 %[mb], %[mb], s44\n\t" \
    "s_xor_b32 %[fl], %[fl], s44\n\t" \
    "s_and_b32 s43, s43, 0x2000\n\t" \
    "s_xor_b32 s43, s43, %[lx]\n\t" \
    "s_add_u32 s60, s60, s43\n\t" \
    "s_addc_u32 s61, s61, 0\n\t" \
    "v_and_b32 v252, 0xf0, v253\n\t" \
    "v_lshlrev_b32 v252, 3, v252\n\t" \
    "buffer_load_dword v252, v252, s[60:63], %[fl] offen sc1\n\t" \
    "s_add_u32 m0, %[wm], 0x18000\n\t" \
    "s_mov_b32 s44, 0x6c0000\n\t" \
    "buffer_load_dwordx4 v240, %[wr], s44 offen offset:0 nt lds\n\t" \
    "buffer_load_dwordx4 v241, %[wr], s44 offen offset:1024 nt lds\n\t" \
    "buffer_load_dwordx4 v242, %[wr], s44 offen offset:2048 nt lds\n\t" \
    "ds_read_b64_tr_b16 v[228:229], v244 offset:0\n\t" \
    "ds_read_b64_tr_b16 v[230:231], v244 offset:1536\n\t" \
    "ds_read_b64_tr_b16 v[124:125], v244 offset:32\n\t" \
    "ds_read_b64_tr_b16 v[126:127], v244 offset:1568\n\t" \
    "ds_read_b64_tr_b16 v[132:133], v244 offset:64\n\t" \
    "ds_read_b64_tr_b16 v[134:135], v244 offset:1600\n\t" \
    "v_add_u32 v244, 0x0, v243\n\t" \
    "s_waitcnt vmcnt(12) lgkmcnt(0)\n\t" \
    "s_barrier \n\t" \
    "s_add_u32 m0, %[wm], 0x1e000\n\t" \
    "s_mov_b32 s44, 0x720000\n\t" \
    "buffer_load_dwordx4 v240, %[wr], s44 offen offset:0 nt lds\n\t" \
    "buffer_load_dwordx4 v241, %[wr], s44 offen offset:1024 nt lds\n\t" \
    "buffer_load_dwordx4 v242, %[wr], s44 offen offset:2048 nt lds\n\t" \
    "ds_read_b64_tr_b16 v[136:137], v244 offset:0\n\t" \
    "ds_read_b64_tr_b16 v[138:139], v244 offset:1536\n\t" \
    "ds_read_b64_tr_b16 v[144:145], v244 offset:32\n\t" \
    "ds_read_b64_tr_b16 v[146:147], v244 offset:1568\n\t" \
    "ds_read_b64_tr_b16 v[152:153], v244 offset:64\n\t" \
    "ds_read_b64_tr_b16 v[154:155], v244 offset:1600\n\t" \
    "v_add_u32 v244, 0x6000, v243\n\t" \
    "s_waitcnt vmcnt(12) lgkmcnt(0)\n\t" \
    "s_barrier \n\t" \
    "ds_read_b64_tr_b16 v[140:141], v244 offset:0\n\t" \
    "ds_read_b64_tr_b16 v[142:143], v244 offset:1536\n\t" \
    "ds_read_b64_tr_b16 v[148:149], v244 offset:32\n\t" \
    "ds_read_b64_tr_b16 v[150:151], v244 offset:1568\n\t" \
    "ds_read_b64_tr_b16 v[156:157], v244 offset:64\n\t" \
    "ds_read_b64_tr_b16 v[158:159], v244 offset:1600\n\t" \
    "v_add_u32 v244, 0xc000, v243\n\t" \
    "s_waitcnt vmcnt(9) lgkmcnt(0)\n\t" \
    "s_barrier \n\t" \
    "ds_read_b64_tr_b16 v[232:233], v244 offset:0\n\t" \
    "ds_read_b64_tr_b16 v[234:235], v244 offset:1536\n\t" \
    "ds_read_b64_tr_b16 v[160:161], v244 offset:32\n\t" \
    "ds_read_b64_tr_b16 v[162:163], v244 offset:1568\n\t" \
    "ds_read_b64_tr_b16 v[168:169], v244 offset:64\n\t" \
    "ds_read_b64_tr_b16 v[170:171], v244 offset:1600\n\t" \
    "v_add_u32 v244, 0x12000, v243\n\t" \
    "s_waitcnt vmcnt(6) lgkmcnt(0)\n\t" \
    "s_barrier \n\t" \
    "ds_read_b64_tr_b16 v[236:237], v244 offset:0\n\t" \
    "ds_read_b64_tr_b16 v[238:239], v244 offset:1536\n\t" \
    "ds_read_b64_tr_b16 v[164:165], v244 offset:32\n\t" \
    "ds_read_b64_tr_b16 v[166:167], v244 offset:1568\n\t" \
    "ds_read_b64_tr_b16 v[172:173], v244 offset:64\n\t" \
    "ds_read_b64_tr_b16 v[174:175], v244 offset:1600\n\t" \
    "v_add_u32 v244, 0x18000, v243\n\t" \
    "s_waitcnt vmcnt(3) lgkmcnt(0)\n\t" \
    "s_barrier \n\t" \
    "ds_read_b64_tr_b16 v[176:177], v244 offset:0\n\t" \
    "ds_read_b64_tr_b16 v[178:179], v244 offset:1536\n\t" \
    "ds_read_b64_tr_b16 v[184:185], v244 offset:32\n\t" \
    "ds_read_b64_tr_b16 v[186:187], v244 offset:1568\n\t" \
    "ds_read_b64_tr_b16 v[192:193], v244 offset:64\n\t" \
    "ds_read_b64_tr_b16 v[194:195], v244 offset:1600\n\t" \
    "s_mov_b32 s43, 0\n\t" \
    "v_cmp_eq_u32 vcc, 0, v252\n\t" \
    "s_cbranch_vccnz .Ls43_%=\n\t" \
    "s_mov_b32 m0, %[m0]\n\t" \
    "s_add_u32 s45, s42, %[x1]\n\t" \
    "buffer_load_dwordx4 v253, s[60:63], s42 offen offset:0 sc1 lds\n\t" \
    "buffer_load_dwordx4 v253, s[60:63], s42 offen offset:1024 sc1 lds\n\t" \
    "buffer_load_dwordx4 v253, s[60:63], s42 offen offset:2048 sc1 lds\n\t" \
    "buffer_load_dwordx4 v253, s[60:63], s42 offen offset:3072 sc1 lds\n\t" \
    "s_mov_b32 m0, %[m1]\n\t" \
    "s_nop 0\n\t" \
    "buffer_load_dwordx4 v253, s[60:63], s45 offen offset:0 sc1 lds\n\t" \
    "buffer_load_dwordx4 v253, s[60:63], s45 offen offset:1024 sc1 lds\n\t" \
    "s_mov_b32 s43, 1\n\t" \
    ".Ls43_%=:\n\t" \
    "v_add_u32 v244, 0x1e000, v243\n\t" \
    "s_waitcnt vmcnt(0) lgkmcnt(0)\n\t" \
    "s_barrier \n\t" \
    "ds_read_b64_tr_b16 v[180:181], v244 offset:0\n\t" \
    "ds_read_b64_tr_b16 v[182:183], v244 offset:1536\n\t" \
    "ds_read_b64_tr_b16 v[188:189], v244 offset:32\n\t" \
    "ds_read_b64_tr_b16 v[190:191], v244 offset:1568\n\t" \
    "ds_read_b64_tr_b16 v[196:197], v244 offset:64\n\t" \
    "ds_read_b64_tr_b16 v[198:199], v244 offset:1600\n\t" \
    "s_waitcnt lgkmcnt(0)\n\t" \
    "s_barrier \n\t" \
    "ds_write_b128 v254, v[200:203] offset:0\n\t" \
    "ds_write_b128 v254, v[204:207] offset:1024\n\t" \
    "ds_write_b128 v254, v[208:211] offset:2048\n\t" \
    "ds_write_b128 v254, v[212:215] offset:3072\n\t" \
    "ds_write_b128 v254, v[216:219] offset:4096\n\t" \
    "ds_write_b128 v254, v[220:223] offset:5120\n\t" \
    "ds_write_b128 v254, v[224:227] offset:6144\n\t" \
    "ds_write_b128 v254, v[228:231] offset:7168\n\t" \
    "ds_write_b128 v254, v[232:235] offset:8192\n\t" \
    "ds_write_b128 v254, v[236:239] offset:9216\n\t" \
    "s_cmp_lg_u32 s43, 0\n\t" \
    "s_cbranch_scc1 .Ls44_%=\n\t" \
    "s_mov_b32 s44, 0\n\t" \
    ".Ls45_%=:\n\t" \
    "v_cmp_ge_u32 vcc, s44, v252\n\t" \
    "s_cbranch_vccz .Ls46_%=\n\t" \
    "s_cmp_eq_u32 s41, 0\n\t" \
    "s_cbranch_scc1 .Ls46_%=\n\t" \
    "s_sub_u32 s41, s41, 1\n\t" \
    "s_sleep 8\n\t" \
    "v_and_b32 v252, 0xf0, v253\n\t" \
    "v_lshlrev_b32 v252, 3, v252\n\t" \
    "buffer_load_dword v252, v252, s[60:63], %[fl] offen sc1\n\t" \
    "s_waitcnt vmcnt(0)\n\t" \
    "s_branch .Ls45_%=\n\t" \
    ".Ls46_%=:\n\t" \
    "s_mov_b32 m0, %[m0]\n\t" \
    "s_add_u32 s45, s42, %[x1]\n\t" \
    "buffer_load_dwordx4 v253, s[60:63], s42 offen offset:0 sc1 lds\n\t" \
    "buffer_load_dwordx4 v253, s[60:63], s42 offen offset:1024 sc1 lds\n\t" \
    "buffer_load_dwordx4 v253, s[60:63], s42 offen offset:2048 sc1 lds\n\t" \
    "buffer_load_dwordx4 v253, s[60:63], s42 offen offset:3072 sc1 lds\n\t" \
    "s_mov_b32 m0, %[m1]\n\t" \
    "s_nop 0\n\t" \
    "buffer_load_dwordx4 v253, s[60:63], s45 offen offset:0 sc1 lds\n\t" \
    "buffer_load_dwordx4 v253, s[60:63], s45 offen offset:1024 sc1 lds\n\t" \
    ".Ls44_%=:\n\t" \
    "s_waitcnt vmcnt(0) lgkmcnt(0)\n\t" \
    "s_barrier \n\t" \
    "ds_read_b128 v[224:227], v253 offset:0\n\t" \
    "ds_read_b128 v[228:231], v253 offset:20480\n\t" \
    "ds_read_b128 v[232:235], v253 offset:1024\n\t" \
    "ds_read_b128 v[236:239], v253 offset:21504\n\t" \
    "ds_read_b128 v[244:247], v254 offset:0\n\t" \
    "ds_read_b128 v[248:251], v254 offset:1024\n\t" \
    ".p2align 8\n\t" \
    ".Ls47_%=:\n\t" \
    "s_cmp_lg_u32 %[wave], 4\n\t" \
    "s_cbranch_scc1 .Ls48_%=\n\t" \
    "s_sleep 6\n\t" \
    ".Ls48_%=:\n\t" \
    "s_waitcnt lgkmcnt(5)\n\t" \
    "v_mfma_f32_16x16x32_bf16 v[204:207], v[0:3], v[224:227], 0\n\t" \
    "ds_read_b128 v[240:243], v253 offset:2048\n\t" \
    "v_mfma_f32_16x16x32_bf16 v[208:211], v[8:11], v[224:227], 0\n\t" \
    "s_waitcnt lgkmcnt(2)\n\t" \
    "v_mfma_f32_16x16x32_bf16 v[200:203], v[244:247], v[224:227], 0\n\t" \
    "v_mfma_f32_16x16x32_bf16 v[216:219], v[0:3], v[228:231], 0\n\t" \
    "ds_read_b128 v[224:227], v253 offset:22528\n\t" \
    "v_mfma_f32_16x16x32_bf16 v[220:223], v[8:11], v[228:231], 0\n\t" \
    "v_mfma_f32_16x16x32_bf16 v[212:215], v[244:247], v[228:231], 0\n\t" \
    "ds_read_b128 v[244:247], v254 offset:2048\n\t" \
    "v_mfma_f32_16x16x32_bf16 v[204:207], v[4:7], v[232:235], v[204:207]\n\t" \
    "ds_read_b128 v[228:231], v253 offset:3072\n\t" \
    "s_add_u32 m0, %[m0], 0x3000\n\t" \
    "v_mfma_f32_16x16x32_bf16 v[208:211], v[12:15], v[232:235], v[208:211]\n\t" \
    "s_add_u32 s45, s42, 0xc000\n\t" \
    "s_waitcnt lgkmcnt(4)\n\t" \
    "v_mfma_f32_16x16x32_bf16 v[200:203], v[248:251], v[232:235], v[200:203]\n\t" \
    "buffer_load_dwordx4 v253, s[60:63], s45 offen offset:0 sc1 lds\n\t" \
    "v_mfma_f32_16x16x32_bf16 v[216:219], v[4:7], v[236:239], v[216:219]\n\t" \
    "ds_read_b128 v[232:235], v253 offset:23552\n\t" \
    "v_mfma_f32_16x16x32_bf16 v[220:223], v[12:15], v[236:239], v[220:223]\n\t" \
    "v_mfma_f32_16x16x32_bf16 v[212:215], v[248:251], v[236:239], v[212:215]\n\t" \
    "ds_read_b128 v[248:251], v254 offset:3072\n\t" \
    "s_waitcnt lgkmcnt(5)\n\t" \
    "v_mfma_f32_16x16x32_bf16 v[204:207], v[24:27], v[240:243], v[204:207]\n\t" \
    "ds_read_b128 v[236:239], v253 offset:4096\n\t" \
    "buffer_load_dwordx4 v253, s[60:63], s45 offen offset:1024 sc1 lds\n\t" \
    "v_mfma_f32_16x16x32_bf16 v[208:211], v[32:35], v[240:243], v[208:211]\n\t" \
    "v_mfma_f32_16x16x32_bf16 v[200:203], v[16:19], v[240:243], v[200:203]\n\t" \
    "s_waitcnt lgkmcnt(5)\n\t" \
    "v_mfma_f32_16x16x32_bf16 v[216:219], v[24:27], v[224:227], v[216:219]\n\t" \
    "ds_read_b128 v[240:243], v253 offset:24576\n\t" \
    "v_mfma_f32_16x16x32_bf16 v[220:223], v[32:35], v[224:227], v[220:223]\n\t" \
    "buffer_load_dwordx4 v253, s[60:63], s45 offen offset:2048 sc1 lds\n\t" \
    "v_mfma_f32_16x16x32_bf16 v[212:215], v[16:19], v[224:227], v[212:215]\n\t" \
    "s_waitcnt lgkmcnt(4)\n\t" \
    "v_mfma_f32_16x16x32_bf16 v[204:207], v[28:31], v[228:231], v[204:207]\n\t" \
    "ds_read_b128 v[224:227], v253 offset:5120\n\t" \
    "v_mfma_f32_16x16x32_bf16 v[208:211], v[36:39], v[228:231], v[208:211]\n\t" \
    "v_mfma_f32_16x16x32_bf16 v[200:203], v[20:23], v[228:231], v[200:203]\n\t" \
    "buffer_load_dwordx4 v253, s[60:63], s45 offen offset:3072 sc1 lds\n\t" \
    "s_waitcnt lgkmcnt(4)\n\t" \
    "v_mfma_f32_16x16x32_bf16 v[216:219], v[28:31], v[232:235], v[216:219]\n\t" \
    "ds_read_b128 v[228:231], v253 offset:25600\n\t" \
    "v_mfma_f32_16x16x32_bf16 v[220:223], v[36:39], v[232:235], v[220:223]\n\t" \
    "v_mfma_f32_16x16x32_bf16 v[212:215], v[20:23], v[232:235], v[212:215]\n\t" \
    "s_waitcnt lgkmcnt(3)\n\t" \
    "v_mfma_f32_16x16x32_bf16 v[204:207], v[40:43], v[236:239], v[204:207]\n\t" \
    "ds_read_b128 v[232:235], v253 offset:6144\n\t" \
    "v_mfma_f32_16x16x32_bf16 v[208:211], v[48:51], v[236:239], v[208:211]\n\t" \
    "v_mfma_f32_16x16x32_bf16 v[200:203], v[244:247], v[236:239], v[200:203]\n\t" \
    "s_waitcnt lgkmcnt(3)\n\t" \
    "v_mfma_f32_16x16x32_bf16 v[216:219], v[40:43], v[240:243], v[216:219]\n\t" \
    "ds_read_b128 v[236:239], v253 offset:26624\n\t" \
    "v_mfma_f32_16x16x32_bf16 v[220:223], v[48:51], v[240:243], v[220:223]\n\t" \
    "v_mfma_f32_16x16x32_bf16 v[212:215], v[244:247], v[240:243], v[212:215]\n\t" \
    "ds_read_b128 v[244:247], v254 offset:4096\n\t" \
    "s_waitcnt lgkmcnt(4)\n\t" \
    "v_mfma_f32_16x16x32_bf16 v[204:207], v[44:47], v[224:227], v[204:207]\n\t" \
    "ds_read_b128 v[240:243], v253 offset:7168\n\t" \
    "v_mfma_f32_16x16x32_bf16 v[208:211], v[52:55], v[224:227], v[208:211]\n\t" \
    "v_mfma_f32_16x16x32_bf16 v[200:203], v[248:251], v[224:227], v[200:203]\n\t" \
    "s_waitcnt lgkmcnt(4)\n\t" \
    "v_mfma_f32_16x16x32_bf16 v[216:219], v[44:47], v[228:231], v[216:219]\n\t" \
    "ds_read_b128 v[224:227], v253 offset:27648\n\t" \
    "v_mfma_f32_16x16x32_bf16 v[220:223], v[52:55], v[228:231], v[220:223]\n\t" \
    "v_mfma_f32_16x16x32_bf16 v[212:215], v[248:251], v[228:231], v[212:215]\n\t" \
    "ds_read_b128 v[248:251], v254 offset:5120\n\t" \
    "s_waitcnt lgkmcnt(5)\n\t" \
    "v_mfma_f32_16x16x32_bf16 v[204:207], v[64:67], v[232:235], v[204:207]\n\t" \
    "ds_read_b128 v[228:231], v253 offset:8192\n\t" \
    "v_mfma_f32_16x16x32_bf16 v[208:211], v[72:75], v[232:235], v[208:211]\n\t" \
    "v_mfma_f32_16x16x32_bf16 v[200:203], v[56:59], v[232:235], v[200:203]\n\t" \
    "s_waitcnt lgkmcnt(5)\n\t" \
    "v_mfma_f32_16x16x32_bf16 v[216:219], v[64:67], v[236:239], v[216:219]\n\t" \
    "ds_read_b128 v[232:235], v253 offset:28672\n\t" \
    "v_mfma_f32_16x16x32_bf16 v[220:223], v[72:75], v[236:239], v[220:223]\n\t" \
    "v_mfma_f32_16x16x32_bf16 v[212:215], v[56:59], v[236:239], v[212:215]\n\t" \
    "s_waitcnt lgkmcnt(4)\n\t" \
    "v_mfma_f32_16x16x32_bf16 v[204:207], v[68:71], v[240:243], v[204:207]\n\t" \
    "ds_read_b128 v[236:239], v253 offset:9216\n\t" \
    "v_mfma_f32_16x16x32_bf16 v[208:211], v[76:79], v[240:243], v[208:211]\n\t" \
    "v_mfma_f32_16x16x32_bf16 v[200:203], v[60:63], v[240:243], v[200:203]\n\t" \
    "v_and_b32 v252, 0xf0, v253\n\t" \
    "v_lshlrev_b32 v252, 3, v252\n\t" \
    "buffer_load_dword v252, v252, s[60:63], %[fl] offen sc1\n\t" \
    "s_waitcnt lgkmcnt(4)\n\t" \
    "v_mfma_f32_16x16x32_bf16 v[216:219], v[68:71], v[224:227], v[216:219]\n\t" \
    "ds_read_b128 v[240:243], v253 offset:29696\n\t" \
    "v_mfma_f32_16x16x32_bf16 v[220:223], v[76:79], v[224:227], v[220:223]\n\t" \
    "v_mfma_f32_16x16x32_bf16 v[212:215], v[60:63], v[224:227], v[212:215]\n\t" \
    "s_waitcnt lgkmcnt(3)\n\t" \
    "v_mfma_f32_16x16x32_bf16 v[204:207], v[80:83], v[228:231], v[204:207]\n\t" \
    "ds_read_b128 v[224:227], v253 offset:10240\n\t" \
    "v_mfma_f32_16x16x32_bf16 v[208:211], v[88:91], v[228:231], v[208:211]\n\t" \
    "v_mfma_f32_16x16x32_bf16 v[200:203], v[244:247], v[228:231], v[200:203]\n\t" \
    "s_waitcnt lgkmcnt(3)\n\t" \
    "v_mfma_f32_16x16x32_bf16 v[216:219], v[80:83], v[232:235], v[216:219]\n\t" \
    "ds_read_b128 v[228:231], v253 offset:30720\n\t" \
    "s_add_u32 s44, s40, 1\n\t" \
    "v_mfma_f32_16x16x32_bf16 v[220:223], v[88:91], v[232:235], v[220:223]\n\t" \
    "s_and_b32 s45, s44, 15\n\t" \
    "v_mfma_f32_16x16x32_bf16 v[212:215], v[244:247], v[232:235], v[212:215]\n\t" \
    "ds_read_b128 v[244:247], v254 offset:6144\n\t" \
    "s_cmp_eq_u32 s45, 0\n\t" \
    "s_waitcnt lgkmcnt(4)\n\t" \
    "v_mfma_f32_16x16x32_bf16 v[204:207], v[84:87], v[236:239], v[204:207]\n\t" \
    "ds_read_b128 v[232:235], v253 offset:11264\n\t" \
    "s_cselect_b32 s45, %[cj], %[cs]\n\t" \
    "v_mfma_f32_16x16x32_bf16 v[208:211], v[92:95], v[236:239], v[208:211]\n\t" \
    "s_cmp_eq_u32 s44, 64\n\t" \
    "v_mfma_f32_16x16x32_bf16 v[200:203], v[248:251], v[236:239], v[200:203]\n\t" \
    "s_cselect_b32 s45, 0, s45\n\t" \
    "s_waitcnt lgkmcnt(4)\n\t" \
    "v_mfma_f32_16x16x32_bf16 v[216:219], v[84:87], v[240:243], v[216:219]\n\t" \
    "ds_read_b128 v[236:239], v253 offset:31744\n\t" \
    "s_add_u32 s42, s42, s45\n\t" \
    "v_mfma_f32_16x16x32_bf16 v[220:223], v[92:95], v[240:243], v[220:223]\n\t" \
    "v_mfma_f32_16x16x32_bf16 v[212:215], v[248:251], v[240:243], v[212:215]\n\t" \
    "ds_read_b128 v[248:251], v254 offset:7168\n\t" \
    "s_setprio 1\n\t" \
    "s_waitcnt vmcnt(0) lgkmcnt(0)\n\t" \
    "s_barrier \n\t" \
    "s_add_u32 s44, s40, 1\n\t" \
    "s_min_u32 s44, s44, 63\n\t" \
    ".Ls49_%=:\n\t" \
    "v_cmp_ge_u32 vcc, s44, v252\n\t" \
    "s_cbranch_vccz .Ls410_%=\n\t" \
    "s_cmp_eq_u32 s41, 0\n\t" \
    "s_cbranch_scc1 .Ls410_%=\n\t" \
    "s_sub_u32 s41, s41, 1\n\t" \
    "s_sleep 2\n\t" \
    "v_and_b32 v252, 0xf0, v253\n\t" \
    "v_lshlrev_b32 v252, 3, v252\n\t" \
    "buffer_load_dword v252, v252, s[60:63], %[fl] offen sc1\n\t" \
    "s_waitcnt vmcnt(0)\n\t" \
    "s_branch .Ls49_%=\n\t" \
    ".Ls410_%=:\n\t" \
    "s_mov_b32 m0, %[m0]\n\t" \
    "s_add_u32 s45, s42, %[x1]\n\t" \
    "buffer_load_dwordx4 v253, s[60:63], s42 offen offset:0 sc1 lds\n\t" \
    "buffer_load_dwordx4 v253, s[60:63], s42 offen offset:1024 sc1 lds\n\t" \
    "buffer_load_dwordx4 v253, s[60:63], s42 offen offset:2048 sc1 lds\n\t" \
    "buffer_load_dwordx4 v253, s[60:63], s42 offen offset:3072 sc1 lds\n\t" \
    "s_mov_b32 m0, %[m1]\n\t" \
    "s_nop 0\n\t" \
    "buffer_load_dwordx4 v253, s[60:63], s45 offen offset:0 sc1 lds\n\t" \
    "buffer_load_dwordx4 v253, s[60:63], s45 offen offset:1024 sc1 lds\n\t" \
    "v_mfma_f32_16x16x32_bf16 v[204:207], v[104:107], v[224:227], v[204:207]\n\t" \
    "ds_read_b128 v[240:243], v253 offset:12288\n\t" \
    "v_mfma_f32_16x16x32_bf16 v[208:211], v[112:115], v[224:227], v[208:211]\n\t" \
    "v_mfma_f32_16x16x32_bf16 v[200:203], v[96:99], v[224:227], v[200:203]\n\t" \
    "v_mfma_f32_16x16x32_bf16 v[216:219], v[104:107], v[228:231], v[216:219]\n\t" \
    "ds_read_b128 v[224:227], v253 offset:32768\n\t" \
    "v_mfma_f32_16x16x32_bf16 v[220:223], v[112:115], v[228:231], v[220:223]\n\t" \
    "v_mfma_f32_16x16x32_bf16 v[212:215], v[96:99], v[228:231], v[212:215]\n\t" \
    "v_mfma_f32_16x16x32_bf16 v[204:207], v[108:111], v[232:235], v[204:207]\n\t" \
    "ds_read_b128 v[228:231], v253 offset:13312\n\t" \
    "v_mfma_f32_16x16x32_bf16 v[208:211], v[116:119], v[232:235], v[208:211]\n\t" \
    "v_mfma_f32_16x16x32_bf16 v[200:203], v[100:103], v[232:235], v[200:203]\n\t" \
    "v_mfma_f32_16x16x32_bf16 v[216:219], v[108:111], v[236:239], v[216:219]\n\t" \
    "ds_read_b128 v[232:235], v253 offset:33792\n\t" \
    "v_mfma_f32_16x16x32_bf16 v[220:223], v[116:119], v[236:239], v[220:223]\n\t" \
    "v_mfma_f32_16x16x32_bf16 v[212:215], v[100:103], v[236:239], v[212:215]\n\t" \
    "s_waitcnt lgkmcnt(3)\n\t" \
    "v_mfma_f32_16x16x32_bf16 v[204:207], v[120:123], v[240:243], v[204:207]\n\t" \
    "ds_read_b128 v[236:239], v253 offset:16384\n\t" \
    "v_mfma_f32_16x16x32_bf16 v[208:211], v[128:131], v[240:243], v[208:211]\n\t" \
    "v_mfma_f32_16x16x32_bf16 v[200:203], v[244:247], v[240:243], v[200:203]\n\t" \
    "s_waitcnt lgkmcnt(3)\n\t" \
    "v_mfma_f32_16x16x32_bf16 v[216:219], v[120:123], v[224:227], v[216:219]\n\t" \
    "ds_read_b128 v[240:243], v253 offset:36864\n\t" \
    "v_mfma_f32_16x16x32_bf16 v[220:223], v[128:131], v[224:227], v[220:223]\n\t" \
    "v_mfma_f32_16x16x32_bf16 v[212:215], v[244:247], v[224:227], v[212:215]\n\t" \
    "ds_read_b128 v[244:247], v254 offset:8192\n\t" \
    "s_waitcnt lgkmcnt(4)\n\t" \
    "v_mfma_f32_16x16x32_bf16 v[204:207], v[124:127], v[228:231], v[204:207]\n\t" \
    "ds_read_b128 v[224:227], v253 offset:17408\n\t" \
    "v_mfma_f32_16x16x32_bf16 v[208:211], v[132:135], v[228:231], v[208:211]\n\t" \
    "v_mfma_f32_16x16x32_bf16 v[200:203], v[248:251], v[228:231], v[200:203]\n\t" \
    "s_waitcnt lgkmcnt(4)\n\t" \
    "v_mfma_f32_16x16x32_bf16 v[216:219], v[124:127], v[232:235], v[216:219]\n\t" \
    "ds_read_b128 v[228:231], v253 offset:37888\n\t" \
    "v_mfma_f32_16x16x32_bf16 v[220:223], v[132:135], v[232:235], v[220:223]\n\t" \
    "v_mfma_f32_16x16x32_bf16 v[212:215], v[248:251], v[232:235], v[212:215]\n\t" \
    "ds_read_b128 v[248:251], v254 offset:9216\n\t" \
    "s_waitcnt lgkmcnt(5)\n\t" \
    "v_mfma_f32_16x16x32_bf16 v[204:207], v[160:163], v[236:239], v[204:207]\n\t" \
    "ds_read_b128 v[232:235], v253 offset:14336\n\t" \
    "v_mfma_f32_16x16x32_bf16 v[208:211], v[168:171], v[236:239], v[208:211]\n\t" \
    "s_waitcnt lgkmcnt(4)\n\t" \
    "v_mfma_f32_16x16x32_bf16 v[200:203], v[244:247], v[236:239], v[200:203]\n\t" \
    "v_mfma_f32_16x16x32_bf16 v[216:219], v[160:163], v[240:243], v[216:219]\n\t" \
    "ds_read_b128 v[236:239], v253 offset:15360\n\t" \
    "v_mfma_f32_16x16x32_bf16 v[220:223], v[168:171], v[240:243], v[220:223]\n\t" \
    "v_mfma_f32_16x16x32_bf16 v[212:215], v[244:247], v[240:243], v[212:215]\n\t" \
    "s_waitcnt lgkmcnt(4)\n\t" \
    "v_mfma_f32_16x16x32_bf16 v[204:207], v[164:167], v[224:227], v[204:207]\n\t" \
    "ds_read_b128 v[240:243], v253 offset:18432\n\t" \
    "v_add_u32 v252, %[q2], v253\n\t" \
    "v_mfma_f32_16x16x32_bf16 v[208:211], v[172:175], v[224:227], v[208:211]\n\t" \
    "s_waitcnt lgkmcnt(3)\n\t" \
    "v_mfma_f32_16x16x32_bf16 v[200:203], v[248:251], v[224:227], v[200:203]\n\t" \
    "v_mfma_f32_16x16x32_bf16 v[216:219], v[164:167], v[228:231], v[216:219]\n\t" \
    "ds_read_b128 v[224:227], v253 offset:19456\n\t" \
    "v_mfma_f32_16x16x32_bf16 v[220:223], v[172:175], v[228:231], v[220:223]\n\t" \
    "v_mfma_f32_16x16x32_bf16 v[212:215], v[248:251], v[228:231], v[212:215]\n\t" \
    "s_waitcnt lgkmcnt(3)\n\t" \
    "v_mfma_f32_16x16x32_bf16 v[204:207], v[144:147], v[232:235], v[204:207]\n\t" \
    "ds_read_b128 v[228:231], v253 offset:34816\n\t" \
    "v_mov_b64 v[244:245], 0\n\t" \
    "v_mfma_f32_16x16x32_bf16 v[208:211], v[152:155], v[232:235], v[208:211]\n\t" \
    "v_mov_b64 v[246:247], 0\n\t" \
    "v_mfma_f32_16x16x32_bf16 v[200:203], v[136:139], v[232:235], v[200:203]\n\t" \
    "v_mov_b64 v[248:249], 0\n\t" \
    "s_waitcnt lgkmcnt(3)\n\t" \
    "v_mfma_f32_16x16x32_bf16 v[204:207], v[148:151], v[236:239], v[204:207]\n\t" \
    "ds_read_b128 v[232:235], v253 offset:35840\n\t" \
    "v_mov_b64 v[250:251], 0\n\t" \
    "v_mfma_f32_16x16x32_bf16 v[208:211], v[156:159], v[236:239], v[208:211]\n\t" \
    "v_mfma_f32_16x16x32_bf16 v[200:203], v[140:143], v[236:239], v[200:203]\n\t" \
    "s_waitcnt lgkmcnt(3)\n\t" \
    "v_mfma_f32_16x16x32_bf16 v[204:207], v[184:187], v[240:243], v[204:207]\n\t" \
    "ds_read_b128 v[236:239], v253 offset:38912\n\t" \
    "v_mfma_f32_16x16x32_bf16 v[208:211], v[192:195], v[240:243], v[208:211]\n\t" \
    "v_mfma_f32_16x16x32_bf16 v[200:203], v[176:179], v[240:243], v[200:203]\n\t" \
    "s_waitcnt lgkmcnt(3)\n\t" \
    "v_mfma_f32_16x16x32_bf16 v[204:207], v[188:191], v[224:227], v[204:207]\n\t" \
    "ds_read_b128 v[240:243], v253 offset:39936\n\t" \
    "v_mfma_f32_16x16x32_bf16 v[208:211], v[196:199], v[224:227], v[208:211]\n\t" \
    "v_mfma_f32_16x16x32_bf16 v[200:203], v[180:183], v[224:227], v[200:203]\n\t" \
    "s_waitcnt lgkmcnt(3)\n\t" \
    "v_mfma_f32_16x16x32_bf16 v[216:219], v[144:147], v[228:231], v[216:219]\n\t" \
    "s_waitcnt lgkmcnt(2)\n\t" \
    "v_mfma_f32_16x16x32_bf16 v[216:219], v[148:151], v[232:235], v[216:219]\n\t" \
    "v_cvt_pk_bf16_f32 v204, v204, v208\n\t" \
    "v_cvt_pk_bf16_f32 v205, v205, v209\n\t" \
    "s_waitcnt lgkmcnt(1)\n\t" \
    "v_mfma_f32_16x16x32_bf16 v[216:219], v[184:187], v[236:239], v[216:219]\n\t" \
    "v_cvt_pk_bf16_f32 v206, v206, v210\n\t" \
    "v_cvt_pk_bf16_f32 v207, v207, v211\n\t" \
    "s_waitcnt lgkmcnt(0)\n\t" \
    "v_mfma_f32_16x16x32_bf16 v[216:219], v[188:191], v[240:243], v[216:219]\n\t" \
    "ds_write_b128 v252, v[204:207] offset:0\n\t" \
    "v_mfma_f32_16x16x32_bf16 v[220:223], v[152:155], v[228:231], v[220:223]\n\t" \
    "v_mfma_f32_16x16x32_bf16 v[220:223], v[156:159], v[232:235], v[220:223]\n\t" \
    "v_add_u32 v252, %[qs], v253\n\t" \
    "v_mfma_f32_16x16x32_bf16 v[220:223], v[192:195], v[236:239], v[220:223]\n\t" \
    "v_cvt_pk_bf16_f32 v200, v200, v201\n\t" \
    "v_cvt_pk_bf16_f32 v201, v202, v203\n\t" \
    "v_mfma_f32_16x16x32_bf16 v[220:223], v[196:199], v[240:243], v[220:223]\n\t" \
    "ds_write_b64 v252, v[200:201] offset:0\n\t" \
    "v_mfma_f32_16x16x32_bf16 v[212:215], v[136:139], v[228:231], v[212:215]\n\t" \
    "v_mfma_f32_16x16x32_bf16 v[212:215], v[140:143], v[232:235], v[212:215]\n\t" \
    "v_mov_b64 v[208:209], 0\n\t" \
    "v_mfma_f32_16x16x32_bf16 v[212:215], v[176:179], v[236:239], v[212:215]\n\t" \
    "v_mov_b64 v[210:211], 0\n\t" \
    "v_add_u32 v252, %[q2], v253\n\t" \
    "v_mfma_f32_16x16x32_bf16 v[212:215], v[180:183], v[240:243], v[212:215]\n\t" \
    "v_cvt_pk_bf16_f32 v216, v216, v220\n\t" \
    "v_cvt_pk_bf16_f32 v217, v217, v221\n\t" \
    "v_cvt_pk_bf16_f32 v218, v218, v222\n\t" \
    "v_cvt_pk_bf16_f32 v219, v219, v223\n\t" \
    "ds_write_b128 v252, v[216:219] offset:6144\n\t" \
    "v_add_u32 v252, %[qs], v253\n\t" \
    "s_nop 1\n\t" \
    "v_cvt_pk_bf16_f32 v212, v212, v213\n\t" \
    "v_cvt_pk_bf16_f32 v213, v214, v215\n\t" \
    "ds_write_b64 v252, v[212:213] offset:6144\n\t" \
    "s_setprio 2\n\t" \
    "s_waitcnt vmcnt(0) lgkmcnt(0)\n\t" \
    "s_barrier \n\t" \
    "ds_read_b128 v[224:227], v253 offset:40960\n\t" \
    "ds_read_b128 v[228:231], v253 offset:41984\n\t" \
    "ds_read_b128 v[232:235], v253 offset:43008\n\t" \
    "ds_read_b128 v[236:239], v253 offset:44032\n\t" \
    "ds_read_b128 v[240:243], v253 offset:45056\n\t" \
    "ds_read_b128 v[200:203], v254 offset:0\n\t" \
    "ds_read_b128 v[204:207], v254 offset:1024\n\t" \
    "ds_read_b128 v[216:219], v254 offset:2048\n\t" \
    "ds_read_b128 v[220:223], v254 offset:3072\n\t" \
    "v_bfe_i32 v212, v253, 5, 1\n\t" \
    "v_and_b32 v212, 0xaaaa, v212\n\t" \
    "v_or_b32 v212, 0x4444, v212\n\t" \
    "v_and_b32 v213, 0xffff, v255\n\t" \
    "v_lshrrev_b32 v214, 16, v255\n\t" \
    "s_waitcnt lgkmcnt(8)\n\t" \
    "v_smfmac_f32_16x16x64_bf16 v[244:247], v[224:227], v[0:7], v212\n\t" \
    "v_smfmac_f32_16x16x64_bf16 v[248:251], v[224:227], v[8:15], v212\n\t" \
    "s_waitcnt lgkmcnt(2)\n\t" \
    "v_smfmac_f32_16x16x64_bf16 v[208:211], v[224:227], v[200:207], v212\n\t" \
    "ds_read_b128 v[200:203], v254 offset:4096\n\t" \
    "ds_read_b128 v[204:207], v254 offset:5120\n\t" \
    "ds_read_b128 v[224:227], v253 offset:46080\n\t" \
    "v_smfmac_f32_16x16x64_bf16 v[208:211], v[228:231], v[16:23], v212\n\t" \
    "v_smfmac_f32_16x16x64_bf16 v[244:247], v[228:231], v[24:31], v212\n\t" \
    "v_smfmac_f32_16x16x64_bf16 v[248:251], v[228:231], v[32:39], v212\n\t" \
    "ds_read_b128 v[228:231], v253 offset:47104\n\t" \
    "s_waitcnt lgkmcnt(4)\n\t" \
    "v_smfmac_f32_16x16x64_bf16 v[208:211], v[232:235], v[216:223], v212\n\t" \
    "ds_read_b128 v[216:219], v254 offset:6144\n\t" \
    "ds_read_b128 v[220:223], v254 offset:7168\n\t" \
    "v_smfmac_f32_16x16x64_bf16 v[244:247], v[232:235], v[40:47], v212\n\t" \
    "v_smfmac_f32_16x16x64_bf16 v[248:251], v[232:235], v[48:55], v212\n\t" \
    "ds_read_b128 v[232:235], v253 offset:48128\n\t" \
    "v_smfmac_f32_16x16x64_bf16 v[208:211], v[236:239], v[56:63], v212\n\t" \
    "v_smfmac_f32_16x16x64_bf16 v[244:247], v[236:239], v[64:71], v212\n\t" \
    "v_smfmac_f32_16x16x64_bf16 v[248:251], v[236:239], v[72:79], v212\n\t" \
    "ds_read_b128 v[236:239], v253 offset:49152\n\t" \
    "s_waitcnt lgkmcnt(6)\n\t" \
    "v_smfmac_f32_16x16x64_bf16 v[208:211], v[240:243], v[200:207], v212\n\t" \
    "ds_read_b128 v[200:203], v254 offset:8192\n\t" \
    "ds_read_b128 v[204:207], v254 offset:9216\n\t" \
    "v_smfmac_f32_16x16x64_bf16 v[244:247], v[240:243], v[80:87], v212\n\t" \
    "v_smfmac_f32_16x16x64_bf16 v[248:251], v[240:243], v[88:95], v212\n\t" \
    "ds_read_b128 v[240:243], v253 offset:50176\n\t" \
    "s_waitcnt lgkmcnt(8)\n\t" \
    "v_smfmac_f32_16x16x64_bf16 v[208:211], v[224:227], v[96:103], v212\n\t" \
    "v_smfmac_f32_16x16x64_bf16 v[244:247], v[224:227], v[104:111], v212\n\t" \
    "v_smfmac_f32_16x16x64_bf16 v[248:251], v[224:227], v[112:119], v212\n\t" \
    "s_waitcnt lgkmcnt(5)\n\t" \
    "v_smfmac_f32_16x16x64_bf16 v[208:211], v[228:231], v[216:223], v212\n\t" \
    "s_waitcnt lgkmcnt(4)\n\t" \
    "v_smfmac_f32_16x16x64_bf16 v[208:211], v[232:235], v[136:143], v212\n\t" \
    "s_waitcnt lgkmcnt(1)\n\t" \
    "v_smfmac_f32_16x16x64_bf16 v[208:211], v[236:239], v[200:207], v212\n\t" \
    "s_waitcnt lgkmcnt(0)\n\t" \
    "v_smfmac_f32_16x16x64_bf16 v[208:211], v[240:243], v[176:183], v212\n\t" \
    "v_smfmac_f32_16x16x64_bf16 v[244:247], v[228:231], v[120:127], v212\n\t" \
    "v_smfmac_f32_16x16x64_bf16 v[244:247], v[232:235], v[144:151], v212\n\t" \
    "v_smfmac_f32_16x16x64_bf16 v[244:247], v[236:239], v[160:167], v212\n\t" \
    "v_add_f32 v208, v208, v210\n\t" \
    "v_add_f32 v209, v209, v211\n\t" \
    "v_cvt_pk_bf16_f32 v208, v208, v209\n\t" \
    "v_smfmac_f32_16x16x64_bf16 v[244:247], v[240:243], v[184:191], v212\n\t" \
    "ds_write_b16 v213, v208 offset:0\n\t" \
    "ds_write_b16_d16_hi v213, v208 offset:16\n\t" \
    "v_smfmac_f32_16x16x64_bf16 v[248:251], v[228:231], v[128:135], v212\n\t" \
    "v_smfmac_f32_16x16x64_bf16 v[248:251], v[232:235], v[152:159], v212\n\t" \
    "v_smfmac_f32_16x16x64_bf16 v[248:251], v[236:239], v[168:175], v212\n\t" \
    "v_add_f32 v244, v244, v246\n\t" \
    "v_add_f32 v245, v245, v247\n\t" \
    "v_smfmac_f32_16x16x64_bf16 v[248:251], v[240:243], v[192:199], v212\n\t" \
    "ds_read_b128 v[224:227], v253 offset:0\n\t" \
    "ds_read_b128 v[228:231], v253 offset:20480\n\t" \
    "s_nop 5\n\t" \
    "v_add_f32 v248, v248, v250\n\t" \
    "v_add_f32 v249, v249, v251\n\t" \
    "v_cvt_pk_bf16_f32 v244, v244, v248\n\t" \
    "v_cvt_pk_bf16_f32 v245, v245, v249\n\t" \
    "ds_write2_b32 v214, v244, v245 offset1:4\n\t" \
    "s_setprio 0\n\t" \
    "s_add_u32 s40, s40, 1\n\t" \
    "s_cmp_lg_u32 s40, 64\n\t" \
    "s_waitcnt lgkmcnt(0)\n\t" \
    "s_barrier \n\t" \
    "ds_read_b128 v[232:235], v253 offset:1024\n\t" \
    "ds_read_b128 v[236:239], v253 offset:21504\n\t" \
    "ds_read_b128 v[244:247], v254 offset:0\n\t" \
    "ds_read_b128 v[248:251], v254 offset:1024\n\t" \
    "s_setprio 1\n\t" \
    "s_nop 0\n\t" \
    "s_nop 0\n\t" \
    "s_cbranch_scc1 .Ls47_%=\n\t" \
    "s_branch .Lend%=\n\t" \
    ".Ls3_%=:\n\t" \
    "v_add_u32 v244, 0x18000, v243\n\t" \
    "s_waitcnt vmcnt(12) lgkmcnt(0)\n\t" \
    "s_barrier \n\t" \
    "s_add_u32 m0, %[wm], 0x12000\n\t" \
    "s_mov_b32 s44, 0x1e0000\n\t" \
    "buffer_load_dwordx4 v240, %[wr], s44 offen offset:0 nt lds\n\t" \
    "buffer_load_dwordx4 v241, %[wr], s44 offen offset:1024 nt lds\n\t" \
    "buffer_load_dwordx4 v242, %[wr], s44 offen offset:2048 nt lds\n\t" \
    "ds_read_b64_tr_b16 v[208:209], v244 offset:0\n\t" \
    "ds_read_b64_tr_b16 v[210:211], v244 offset:1536\n\t" \
    "ds_read_b64_tr_b16 v[40:41], v244 offset:32\n\t" \
    "ds_read_b64_tr_b16 v[42:43], v244 offset:1568\n\t" \
    "ds_read_b64_tr_b16 v[48:49], v244 offset:64\n\t" \
    "ds_read_b64_tr_b16 v[50:51], v244 offset:1600\n\t" \
    "v_add_u32 v244, 0x1e000, v243\n\t" \
    "s_waitcnt vmcnt(12) lgkmcnt(0)\n\t" \
    "s_barrier \n\t" \
    "s_add_u32 m0, %[wm], 0x18000\n\t" \
    "s_mov_b32 s44, 0x240000\n\t" \
    "buffer_load_dwordx4 v240, %[wr], s44 offen offset:0 nt lds\n\t" \
    "buffer_load_dwordx4 v241, %[wr], s44 offen offset:1024 nt lds\n\t" \
    "buffer_load_dwordx4 v242, %[wr], s44 offen offset:2048 nt lds\n\t" \
    "ds_read_b64_tr_b16 v[212:213], v244 offset:0\n\t" \
    "ds_read_b64_tr_b16 v[214:215], v244 offset:1536\n\t" \
    "ds_read_b64_tr_b16 v[44:45], v244 offset:32\n\t" \
    "ds_read_b64_tr_b16 v[46:47], v244 offset:1568\n\t" \
    "ds_read_b64_tr_b16 v[52:53], v244 offset:64\n\t" \
    "ds_read_b64_tr_b16 v[54:55], v244 offset:1600\n\t" \
    "v_add_u32 v244, 0x0, v243\n\t" \
    "s_waitcnt vmcnt(12) lgkmcnt(0)\n\t" \
    "s_barrier \n\t" \
    "s_add_u32 m0, %[wm], 0x1e000\n\t" \
    "s_mov_b32 s44, 0x2a0000\n\t" \
    "buffer_load_dwordx4 v240, %[wr], s44 offen offset:0 nt lds\n\t" \
    "buffer_load_dwordx4 v241, %[wr], s44 offen offset:1024 nt lds\n\t" \
    "buffer_load_dwordx4 v242, %[wr], s44 offen offset:2048 nt lds\n\t" \
    "ds_read_b64_tr_b16 v[56:57], v244 offset:0\n\t" \
    "ds_read_b64_tr_b16 v[58:59], v244 offset:1536\n\t" \
    "ds_read_b64_tr_b16 v[64:65], v244 offset:32\n\t" \
    "ds_read_b64_tr_b16 v[66:67], v244 offset:1568\n\t" \
    "ds_read_b64_tr_b16 v[72:73], v244 offset:64\n\t" \
    "ds_read_b64_tr_b16 v[74:75], v244 offset:1600\n\t" \
    "v_add_u32 v244, 0x6000, v243\n\t" \
    "s_waitcnt vmcnt(12) lgkmcnt(0)\n\t" \
    "s_barrier \n\t" \
    "s_add_u32 m0, %[wm], 0x0\n\t" \
    "s_mov_b32 s44, 0x300000\n\t" \
    "buffer_load_dwordx4 v240, %[wr], s44 offen offset:0 nt lds\n\t" \
    "buffer_load_dwordx4 v241, %[wr], s44 offen offset:1024 nt lds\n\t" \
    "buffer_load_dwordx4 v242, %[wr], s44 offen offset:2048 nt lds\n\t" \
    "ds_read_b64_tr_b16 v[60:61], v244 offset:0\n\t" \
    "ds_read_b64_tr_b16 v[62:63], v244 offset:1536\n\t" \
    "ds_read_b64_tr_b16 v[68:69], v244 offset:32\n\t" \
    "ds_read_b64_tr_b16 v[70:71], v244 offset:1568\n\t" \
    "ds_read_b64_tr_b16 v[76:77], v244 offset:64\n\t" \
    "ds_read_b64_tr_b16 v[78:79], v244 offset:1600\n\t" \
    "v_add_u32 v244, 0xc000, v243\n\t" \
    "s_waitcnt vmcnt(12) lgkmcnt(0)\n\t" \
    "s_barrier \n\t" \
    "s_add_u32 m0, %[wm], 0x6000\n\t" \
    "s_mov_b32 s44, 0x360000\n\t" \
    "buffer_load_dwordx4 v240, %[wr], s44 offen offset:0 nt lds\n\t" \
    "buffer_load_dwordx4 v241, %[wr], s44 offen offset:1024 nt lds\n\t" \
    "buffer_load_dwordx4 v242, %[wr], s44 offen offset:2048 nt lds\n\t" \
    "ds_read_b64_tr_b16 v[216:217], v244 offset:0\n\t" \
    "ds_read_b64_tr_b16 v[218:219], v244 offset:1536\n\t" \
    "ds_read_b64_tr_b16 v[80:81], v244 offset:32\n\t" \
    "ds_read_b64_tr_b16 v[82:83], v244 offset:1568\n\t" \
    "ds_read_b64_tr_b16 v[88:89], v244 offset:64\n\t" \
    "ds_read_b64_tr_b16 v[90:91], v244 offset:1600\n\t" \
    "v_add_u32 v244, 0x12000, v243\n\t" \
    "s_waitcnt vmcnt(12) lgkmcnt(0)\n\t" \
    "s_barrier \n\t" \
    "s_add_u32 m0, %[wm], 0xc000\n\t" \
    "s_mov_b32 s44, 0x3c0000\n\t" \
    "buffer_load_dwordx4 v240, %[wr], s44 offen offset:0 nt lds\n\t" \
    "buffer_load_dwordx4 v241, %[wr], s44 offen offset:1024 nt lds\n\t" \
    "buffer_load_dwordx4 v242, %[wr], s44 offen offset:2048 nt lds\n\t" \
    "ds_read_b64_tr_b16 v[220:221], v244 offset:0\n\t" \
    "ds_read_b64_tr_b16 v[222:223], v244 offset:1536\n\t" \
    "ds_read_b64_tr_b16 v[84:85], v244 offset:32\n\t" \
    "ds_read_b64_tr_b16 v[86:87], v244 offset:1568\n\t" \
    "ds_read_b64_tr_b16 v[92:93], v244 offset:64\n\t" \
    "ds_read_b64_tr_b16 v[94:95], v244 offset:1600\n\t" \
    "v_add_u32 v244, 0x18000, v243\n\t" \
    "s_waitcnt vmcnt(12) lgkmcnt(0)\n\t" \
    "s_barrier \n\t" \
    "s_add_u32 m0, %[wm], 0x12000\n\t" \
    "s_mov_b32 s44, 0x420000\n\t" \
    "buffer_load_dwordx4 v240, %[wr], s44 offen offset:0 nt lds\n\t" \
    "buffer_load_dwordx4 v241, %[wr], s44 offen offset:1024 nt lds\n\t" \
    "buffer_load_dwordx4 v242, %[wr], s44 offen offset:2048 nt lds\n\t" \
    "ds_read_b64_tr_b16 v[96:97], v244 offset:0\n\t" \
    "ds_read_b64_tr_b16 v[98:99], v244 offset:1536\n\t" \
    "ds_read_b64_tr_b16 v[104:105], v244 offset:32\n\t" \
    "ds_read_b64_tr_b16 v[106:107], v244 offset:1568\n\t" \
    "ds_read_b64_tr_b16 v[112:113], v244 offset:64\n\t" \
    "ds_read_b64_tr_b16 v[114:115], v244 offset:1600\n\t" \
    "v_add_u32 v244, 0x1e000, v243\n\t" \
    "s_waitcnt vmcnt(12) lgkmcnt(0)\n\t" \
    "s_barrier \n\t" \
    "s_add_u32 m0, %[wm], 0x18000\n\t" \
    "s_mov_b32 s44, 0x480000\n\t" \
    "buffer_load_dwordx4 v240, %[wr], s44 offen offset:0 nt lds\n\t" \
    "buffer_load_dwordx4 v241, %[wr], s44 offen offset:1024 nt lds\n\t" \
    "buffer_load_dwordx4 v242, %[wr], s44 offen offset:2048 nt lds\n\t" \
    "ds_read_b64_tr_b16 v[100:101], v244 offset:0\n\t" \
    "ds_read_b64_tr_b16 v[102:103], v244 offset:1536\n\t" \
    "ds_read_b64_tr_b16 v[108:109], v244 offset:32\n\t" \
    "ds_read_b64_tr_b16 v[110:111], v244 offset:1568\n\t" \
    "ds_read_b64_tr_b16 v[116:117], v244 offset:64\n\t" \
    "ds_read_b64_tr_b16 v[118:119], v244 offset:1600\n\t" \
    "v_add_u32 v244, 0x0, v243\n\t" \
    "s_waitcnt vmcnt(12) lgkmcnt(0)\n\t" \
    "s_barrier \n\t" \
    "v_mov_b32 v246, 0\n\t" \
    "buffer_load_dword v246, v246, s[60:63], %[mo] offen sc1\n\t" \
    "s_add_u32 m0, %[wm], 0x1e000\n\t" \
    "s_mov_b32 s44, 0x4e0000\n\t" \
    "buffer_load_dwordx4 v240, %[wr], s44 offen offset:0 nt lds\n\t" \
    "buffer_load_dwordx4 v241, %[wr], s44 offen offset:1024 nt lds\n\t" \
    "buffer_load_dwordx4 v242, %[wr], s44 offen offset:2048 nt lds\n\t" \
    "ds_read_b64_tr_b16 v[224:225], v244 offset:0\n\t" \
    "ds_read_b64_tr_b16 v[226:227], v244 offset:1536\n\t" \
    "ds_read_b64_tr_b16 v[120:121], v244 offset:32\n\t" \
    "ds_read_b64_tr_b16 v[122:123], v244 offset:1568\n\t" \
    "ds_read_b64_tr_b16 v[128:129], v244 offset:64\n\t" \
    "ds_read_b64_tr_b16 v[130:131], v244 offset:1600\n\t" \
    "v_add_u32 v244, 0x6000, v243\n\t" \
    "s_waitcnt vmcnt(12) lgkmcnt(0)\n\t" \
    "s_barrier \n\t" \
    "s_add_u32 m0, %[wm], 0x0\n\t" \
    "s_mov_b32 s44, 0x540000\n\t" \
    "buffer_load_dwordx4 v240, %[wr], s44 offen offset:0 nt lds\n\t" \
    "buffer_load_dwordx4 v241, %[wr], s44 offen offset:1024 nt lds\n\t" \
    "buffer_load_dwordx4 v242, %[wr], s44 offen offset:2048 nt lds\n\t" \
    "ds_read_b64_tr_b16 v[228:229], v244 offset:0\n\t" \
    "ds_read_b64_tr_b16 v[230:231], v244 offset:1536\n\t" \
    "ds_read_b64_tr_b16 v[124:125], v244 offset:32\n\t" \
    "ds_read_b64_tr_b16 v[126:127], v244 offset:1568\n\t" \
    "ds_read_b64_tr_b16 v[132:133], v244 offset:64\n\t" \
    "ds_read_b64_tr_b16 v[134:135], v244 offset:1600\n\t" \
    "v_add_u32 v244, 0xc000, v243\n\t" \
    "s_waitcnt vmcnt(12) lgkmcnt(0)\n\t" \
    "s_barrier \n\t" \
    "s_add_u32 m0, %[wm], 0x6000\n\t" \
    "s_mov_b32 s44, 0x5a0000\n\t" \
    "buffer_load_dwordx4 v240, %[wr], s44 offen offset:0 nt lds\n\t" \
    "buffer_load_dwordx4 v241, %[wr], s44 offen offset:1024 nt lds\n\t" \
    "buffer_load_dwordx4 v242, %[wr], s44 offen offset:2048 nt lds\n\t" \
    "ds_read_b64_tr_b16 v[136:137], v244 offset:0\n\t" \
    "ds_read_b64_tr_b16 v[138:139], v244 offset:1536\n\t" \
    "ds_read_b64_tr_b16 v[144:145], v244 offset:32\n\t" \
    "ds_read_b64_tr_b16 v[146:147], v244 offset:1568\n\t" \
    "ds_read_b64_tr_b16 v[152:153], v244 offset:64\n\t" \
    "ds_read_b64_tr_b16 v[154:155], v244 offset:1600\n\t" \
    "v_add_u32 v244, 0x12000, v243\n\t" \
    "s_waitcnt vmcnt(12) lgkmcnt(0)\n\t" \
    "s_barrier \n\t" \
    "s_add_u32 m0, %[wm], 0xc000\n\t" \
    "s_mov_b32 s44, 0x600000\n\t" \
    "buffer_load_dwordx4 v240, %[wr], s44 offen offset:0 nt lds\n\t" \
    "buffer_load_dwordx4 v241, %[wr], s44 offen offset:1024 nt lds\n\t" \
    "buffer_load_dwordx4 v242, %[wr], s44 offen offset:2048 nt lds\n\t" \
    "ds_read_b64_tr_b16 v[140:141], v244 offset:0\n\t" \
    "ds_read_b64_tr_b16 v[142:143], v244 offset:1536\n\t" \
    "ds_read_b64_tr_b16 v[148:149], v244 offset:32\n\t" \
    "ds_read_b64_tr_b16 v[150:151], v244 offset:1568\n\t" \
    "ds_read_b64_tr_b16 v[156:157], v244 offset:64\n\t" \
    "ds_read_b64_tr_b16 v[158:159], v244 offset:1600\n\t" \
    "v_add_u32 v244, 0x18000, v243\n\t" \
    "s_waitcnt vmcnt(12) lgkmcnt(0)\n\t" \
    "s_barrier \n\t" \
    "s_add_u32 m0, %[wm], 0x12000\n\t" \
    "s_mov_b32 s44, 0x660000\n\t" \
    "buffer_load_dwordx4 v240, %[wr], s44 offen offset:0 nt lds\n\t" \
    "buffer_load_dwordx4 v241, %[wr], s44 offen offset:1024 nt lds\n\t" \
    "buffer_load_dwordx4 v242, %[wr], s44 offen offset:2048 nt lds\n\t" \
    "ds_read_b64_tr_b16 v[232:233], v244 offset:0\n\t" \
    "ds_read_b64_tr_b16 v[234:235], v244 offset:1536\n\t" \
    "ds_read_b64_tr_b16 v[160:161], v244 offset:32\n\t" \
    "ds_read_b64_tr_b16 v[162:163], v244 offset:1568\n\t" \
    "ds_read_b64_tr_b16 v[168:169], v244 offset:64\n\t" \
    "ds_read_b64_tr_b16 v[170:171], v244 offset:1600\n\t" \
    "v_add_u32 v244, 0x1e000, v243\n\t" \
    "s_waitcnt vmcnt(12) lgkmcnt(0)\n\t" \
    "s_barrier \n\t" \
    ".Ls31_%=:\n\t" \
    "v_readfirstlane_b32 s43, v246\n\t" \
    "s_nop 0\n\t" \
    "s_cmp_lg_u32 s43, -1\n\t" \
    "s_cbranch_scc1 .Ls32_%=\n\t" \
    "s_cmp_eq_u32 s41, 0\n\t" \
    "s_cbranch_scc1 .Ls32_%=\n\t" \
    "s_sub_u32 s41, s41, 1\n\t" \
    "s_sleep 4\n\t" \
    "v_mov_b32 v246, 0\n\t" \
    "buffer_load_dword v246, v246, s[60:63], %[mo] offen sc1\n\t" \
    "s_waitcnt vmcnt(0)\n\t" \
    "s_branch .Ls31_%=\n\t" \
    ".Ls32_%=:\n\t" \
    "v_mov_b32 v246, 0x8000\n\t" \
    "s_mov_b64 exec, 1\n\t" \
    "buffer_store_dword v246, v246, s[60:63], %[mo] offen sc1\n\t" \
    "s_mov_b64 exec, -1\n\t" \
    "s_and_b32 s44, s43, 0x5000\n\t" \
    "s_xor_b32 %[mb], %[mb], s44\n\t" \
    "s_xor_b32 %[fl], %[fl], s44\n\t" \
    "s_and_b32 s43, s43, 0x2000\n\t" \
    "s_xor_b32 s43, s43, %[lx]\n\t" \
    "s_add_u32 s60, s60, s43\n\t" \
    "s_addc_u32 s61, s61, 0\n\t" \
    "s_add_u32 m0, %[wm], 0x18000\n\t" \
    "s_mov_b32 s44, 0x6c0000\n\t" \
    "buffer_load_dwordx4 v240, %[wr], s44 offen offset:0 nt lds\n\t" \
    "buffer_load_dwordx4 v241, %[wr], s44 offen offset:1024 nt lds\n\t" \
    "buffer_load_dwordx4 v242, %[wr], s44 offen offset:2048 nt lds\n\t" \
    "ds_read_b64_tr_b16 v[236:237], v244 offset:0\n\t" \
    "ds_read_b64_tr_b16 v[238:239], v244 offset:1536\n\t" \
    "ds_read_b64_tr_b16 v[164:165], v244 offset:32\n\t" \
    "ds_read_b64_tr_b16 v[166:167], v244 offset:1568\n\t" \
    "ds_read_b64_tr_b16 v[172:173], v244 offset:64\n\t" \
    "ds_read_b64_tr_b16 v[174:175], v244 offset:1600\n\t" \
    "v_add_u32 v244, 0x0, v243\n\t" \
    "s_waitcnt vmcnt(12) lgkmcnt(0)\n\t" \
    "s_barrier \n\t" \
    "s_add_u32 m0, %[wm], 0x1e000\n\t" \
    "s_mov_b32 s44, 0x720000\n\t" \
    "buffer_load_dwordx4 v240, %[wr], s44 offen offset:0 nt lds\n\t" \
    "buffer_load_dwordx4 v241, %[wr], s44 offen offset:1024 nt lds\n\t" \
    "buffer_load_dwordx4 v242, %[wr], s44 offen offset:2048 nt lds\n\t" \
    "ds_read_b64_tr_b16 v[176:177], v244 offset:0\n\t" \
    "ds_read_b64_tr_b16 v[178:179], v244 offset:1536\n\t" \
    "ds_read_b64_tr_b16 v[184:185], v244 offset:32\n\t" \
    "ds_read_b64_tr_b16 v[186:187], v244 offset:1568\n\t" \
    "ds_read_b64_tr_b16 v[192:193], v244 offset:64\n\t" \
    "ds_read_b64_tr_b16 v[194:195], v244 offset:1600\n\t" \
    "v_add_u32 v244, 0x6000, v243\n\t" \
    "s_waitcnt vmcnt(12) lgkmcnt(0)\n\t" \
    "s_barrier \n\t" \
    "ds_read_b64_tr_b16 v[180:181], v244 offset:0\n\t" \
    "ds_read_b64_tr_b16 v[182:183], v244 offset:1536\n\t" \
    "ds_read_b64_tr_b16 v[188:189], v244 offset:32\n\t" \
    "ds_read_b64_tr_b16 v[190:191], v244 offset:1568\n\t" \
    "ds_read_b64_tr_b16 v[196:197], v244 offset:64\n\t" \
    "ds_read_b64_tr_b16 v[198:199], v244 offset:1600\n\t" \
    "v_add_u32 v244, 0xc000, v243\n\t" \
    "s_waitcnt vmcnt(9) lgkmcnt(0)\n\t" \
    "s_barrier \n\t" \
    "ds_read_b64_tr_b16 v[200:201], v244 offset:0\n\t" \
    "ds_read_b64_tr_b16 v[202:203], v244 offset:1536\n\t" \
    "ds_read_b64_tr_b16 v[0:1], v244 offset:32\n\t" \
    "ds_read_b64_tr_b16 v[2:3], v244 offset:1568\n\t" \
    "ds_read_b64_tr_b16 v[8:9], v244 offset:64\n\t" \
    "ds_read_b64_tr_b16 v[10:11], v244 offset:1600\n\t" \
    "v_add_u32 v244, 0x12000, v243\n\t" \
    "s_waitcnt vmcnt(6) lgkmcnt(0)\n\t" \
    "s_barrier \n\t" \
    "ds_read_b64_tr_b16 v[204:205], v244 offset:0\n\t" \
    "ds_read_b64_tr_b16 v[206:207], v244 offset:1536\n\t" \
    "ds_read_b64_tr_b16 v[4:5], v244 offset:32\n\t" \
    "ds_read_b64_tr_b16 v[6:7], v244 offset:1568\n\t" \
    "ds_read_b64_tr_b16 v[12:13], v244 offset:64\n\t" \
    "ds_read_b64_tr_b16 v[14:15], v244 offset:1600\n\t" \
    "v_add_u32 v244, 0x18000, v243\n\t" \
    "s_waitcnt vmcnt(3) lgkmcnt(0)\n\t" \
    "s_barrier \n\t" \
    "ds_read_b64_tr_b16 v[16:17], v244 offset:0\n\t" \
    "ds_read_b64_tr_b16 v[18:19], v244 offset:1536\n\t" \
    "ds_read_b64_tr_b16 v[24:25], v244 offset:32\n\t" \
    "ds_read_b64_tr_b16 v[26:27], v244 offset:1568\n\t" \
    "ds_read_b64_tr_b16 v[32:33], v244 offset:64\n\t" \
    "ds_read_b64_tr_b16 v[34:35], v244 offset:1600\n\t" \
    "v_add_u32 v244, 0x1e000, v243\n\t" \
    "s_waitcnt vmcnt(0) lgkmcnt(0)\n\t" \
    "s_barrier \n\t" \
    "ds_read_b64_tr_b16 v[20:21], v244 offset:0\n\t" \
    "ds_read_b64_tr_b16 v[22:23], v244 offset:1536\n\t" \
    "ds_read_b64_tr_b16 v[28:29], v244 offset:32\n\t" \
    "ds_read_b64_tr_b16 v[30:31], v244 offset:1568\n\t" \
    "ds_read_b64_tr_b16 v[36:37], v244 offset:64\n\t" \
    "ds_read_b64_tr_b16 v[38:39], v244 offset:1600\n\t" \
    "s_waitcnt lgkmcnt(0)\n\t" \
    "s_barrier \n\t" \
    "ds_write_b128 v254, v[200:203] offset:55296\n\t" \
    "ds_write_b128 v254, v[204:207] offset:56320\n\t" \
    "ds_write_b128 v254, v[208:211] offset:57344\n\t" \
    "ds_write_b128 v254, v[212:215] offset:58368\n\t" \
    "ds_write_b128 v254, v[216:219] offset:59392\n\t" \
    "ds_write_b128 v254, v[220:223] offset:60416\n\t" \
    "ds_write_b128 v254, v[224:227] offset:61440\n\t" \
    "ds_write_b128 v254, v[228:231] offset:62464\n\t" \
    "ds_write_b128 v254, v[232:235] offset:63488\n\t" \
    "ds_write_b128 v254, v[236:239] offset:64512\n\t" \
    "s_waitcnt vmcnt(0) lgkmcnt(0)\n\t" \
    "s_barrier \n\t" \
    "ds_read_b128 v[208:211], v254 offset:24576\n\t" \
    "ds_read_b128 v[224:227], v253 offset:0\n\t" \
    "ds_read_b128 v[228:231], v253 offset:20480\n\t" \
    "ds_read_b128 v[212:215], v254 offset:25600\n\t" \
    "ds_read_b128 v[232:235], v253 offset:1024\n\t" \
    "ds_read_b128 v[236:239], v253 offset:21504\n\t" \
    "ds_read_b128 v[216:219], v254 offset:26624\n\t" \
    "ds_read_b128 v[240:243], v253 offset:2048\n\t" \
    "ds_read_b128 v[244:247], v253 offset:22528\n\t" \
    "ds_read_b128 v[220:223], v254 offset:27648\n\t" \
    "ds_read_b128 v[248:251], v253 offset:3072\n\t" \
    "s_waitcnt lgkmcnt(9)\n\t" \
    "v_mfma_f32_16x16x32_bf16 v[200:203], v[208:211], v[224:227], 0\n\t" \
    "ds_read_b128 v[224:227], v253 offset:23552\n\t" \
    "s_waitcnt lgkmcnt(9)\n\t" \
    "v_mfma_f32_16x16x32_bf16 v[204:207], v[208:211], v[228:231], 0\n\t" \
    "ds_read_b128 v[228:231], v253 offset:4096\n\t" \
    "ds_read_b128 v[208:211], v254 offset:28672\n\t" \
    "s_waitcnt lgkmcnt(9)\n\t" \
    "v_mfma_f32_16x16x32_bf16 v[200:203], v[212:215], v[232:235], v[200:203]\n\t" \
    "ds_read_b128 v[232:235], v253 offset:24576\n\t" \
    "s_waitcnt lgkmcnt(9)\n\t" \
    "v_mfma_f32_16x16x32_bf16 v[204:207], v[212:215], v[236:239], v[204:207]\n\t" \
    "ds_read_b128 v[236:239], v253 offset:5120\n\t" \
    "ds_read_b128 v[212:215], v254 offset:29696\n\t" \
    "s_waitcnt lgkmcnt(9)\n\t" \
    "v_mfma_f32_16x16x32_bf16 v[200:203], v[216:219], v[240:243], v[200:203]\n\t" \
    "ds_read_b128 v[240:243], v253 offset:25600\n\t" \
    "s_waitcnt lgkmcnt(9)\n\t" \
    "v_mfma_f32_16x16x32_bf16 v[204:207], v[216:219], v[244:247], v[204:207]\n\t" \
    "ds_read_b128 v[244:247], v253 offset:6144\n\t" \
    "ds_read_b128 v[216:219], v254 offset:30720\n\t" \
    "s_waitcnt lgkmcnt(9)\n\t" \
    "v_mfma_f32_16x16x32_bf16 v[200:203], v[220:223], v[248:251], v[200:203]\n\t" \
    "ds_read_b128 v[248:251], v253 offset:26624\n\t" \
    ".p2align 8\n\t" \
    ".Ls33_%=:\n\t" \
    "s_waitcnt lgkmcnt(9)\n\t" \
    "v_mfma_f32_16x16x32_bf16 v[204:207], v[220:223], v[224:227], v[204:207]\n\t" \
    "ds_read_b128 v[224:227], v253 offset:7168\n\t" \
    "ds_read_b128 v[220:223], v254 offset:31744\n\t" \
    "s_waitcnt lgkmcnt(9)\n\t" \
    "v_mfma_f32_16x16x32_bf16 v[200:203], v[208:211], v[228:231], v[200:203]\n\t" \
    "ds_read_b128 v[228:231], v253 offset:27648\n\t" \
    "s_waitcnt lgkmcnt(9)\n\t" \
    "v_mfma_f32_16x16x32_bf16 v[204:207], v[208:211], v[232:235], v[204:207]\n\t" \
    "ds_read_b128 v[232:235], v253 offset:8192\n\t" \
    "ds_read_b128 v[208:211], v254 offset:32768\n\t" \
    "s_waitcnt lgkmcnt(9)\n\t" \
    "v_mfma_f32_16x16x32_bf16 v[200:203], v[212:215], v[236:239], v[200:203]\n\t" \
    "ds_read_b128 v[236:239], v253 offset:28672\n\t" \
    "s_waitcnt lgkmcnt(9)\n\t" \
    "v_mfma_f32_16x16x32_bf16 v[204:207], v[212:215], v[240:243], v[204:207]\n\t" \
    "ds_read_b128 v[240:243], v253 offset:9216\n\t" \
    "ds_read_b128 v[212:215], v254 offset:33792\n\t" \
    "s_waitcnt lgkmcnt(9)\n\t" \
    "v_mfma_f32_16x16x32_bf16 v[200:203], v[216:219], v[244:247], v[200:203]\n\t" \
    "ds_read_b128 v[244:247], v253 offset:29696\n\t" \
    "s_waitcnt lgkmcnt(9)\n\t" \
    "v_mfma_f32_16x16x32_bf16 v[204:207], v[216:219], v[248:251], v[204:207]\n\t" \
    "s_waitcnt lgkmcnt(7)\n\t" \
    "v_mfma_f32_16x16x32_bf16 v[200:203], v[220:223], v[224:227], v[200:203]\n\t" \
    "s_waitcnt lgkmcnt(6)\n\t" \
    "v_mfma_f32_16x16x32_bf16 v[204:207], v[220:223], v[228:231], v[204:207]\n\t" \
    "s_waitcnt lgkmcnt(4)\n\t" \
    "v_mfma_f32_16x16x32_bf16 v[200:203], v[208:211], v[232:235], v[200:203]\n\t" \
    "s_waitcnt lgkmcnt(3)\n\t" \
    "v_mfma_f32_16x16x32_bf16 v[204:207], v[208:211], v[236:239], v[204:207]\n\t" \
    "s_waitcnt lgkmcnt(1)\n\t" \
    "v_mfma_f32_16x16x32_bf16 v[200:203], v[212:215], v[240:243], v[200:203]\n\t" \
    "s_waitcnt lgkmcnt(0)\n\t" \
    "v_mfma_f32_16x16x32_bf16 v[204:207], v[212:215], v[244:247], v[204:207]\n\t" \
    "ds_read_b128 v[224:227], v253 offset:0\n\t" \
    "ds_read_b128 v[244:247], v254 offset:57344\n\t" \
    "ds_read_b128 v[228:231], v253 offset:20480\n\t" \
    "ds_read_b128 v[232:235], v253 offset:1024\n\t" \
    "ds_read_b128 v[236:239], v253 offset:21504\n\t" \
    "ds_read_b128 v[248:251], v254 offset:58368\n\t" \
    "ds_write_b128 v253, v[200:203] offset:40960\n\t" \
    "s_nop 0\n\t" \
    "ds_write_b128 v253, v[204:207] offset:41984\n\t" \
    "s_nop 1\n\t" \
    "s_waitcnt lgkmcnt(7)\n\t" \
    "v_mfma_f32_16x16x32_bf16 v[204:207], v[40:43], v[224:227], 0\n\t" \
    "ds_read_b128 v[240:243], v253 offset:2048\n\t" \
    "v_mfma_f32_16x16x32_bf16 v[208:211], v[48:51], v[224:227], 0\n\t" \
    "s_waitcnt lgkmcnt(7)\n\t" \
    "v_mfma_f32_16x16x32_bf16 v[200:203], v[244:247], v[224:227], 0\n\t" \
    "s_waitcnt lgkmcnt(6)\n\t" \
    "v_mfma_f32_16x16x32_bf16 v[216:219], v[40:43], v[228:231], 0\n\t" \
    "ds_read_b128 v[224:227], v253 offset:22528\n\t" \
    "v_mfma_f32_16x16x32_bf16 v[220:223], v[48:51], v[228:231], 0\n\t" \
    "v_mfma_f32_16x16x32_bf16 v[212:215], v[244:247], v[228:231], 0\n\t" \
    "ds_read_b128 v[244:247], v254 offset:59392\n\t" \
    "s_waitcnt lgkmcnt(7)\n\t" \
    "v_mfma_f32_16x16x32_bf16 v[204:207], v[44:47], v[232:235], v[204:207]\n\t" \
    "ds_read_b128 v[228:231], v253 offset:3072\n\t" \
    "v_mfma_f32_16x16x32_bf16 v[208:211], v[52:55], v[232:235], v[208:211]\n\t" \
    "s_waitcnt lgkmcnt(6)\n\t" \
    "v_mfma_f32_16x16x32_bf16 v[200:203], v[248:251], v[232:235], v[200:203]\n\t" \
    "v_mfma_f32_16x16x32_bf16 v[216:219], v[44:47], v[236:239], v[216:219]\n\t" \
    "ds_read_b128 v[232:235], v253 offset:23552\n\t" \
    "v_mfma_f32_16x16x32_bf16 v[220:223], v[52:55], v[236:239], v[220:223]\n\t" \
    "v_mfma_f32_16x16x32_bf16 v[212:215], v[248:251], v[236:239], v[212:215]\n\t" \
    "ds_read_b128 v[248:251], v254 offset:60416\n\t" \
    "s_waitcnt lgkmcnt(5)\n\t" \
    "v_mfma_f32_16x16x32_bf16 v[204:207], v[64:67], v[240:243], v[204:207]\n\t" \
    "ds_read_b128 v[236:239], v253 offset:4096\n\t" \
    "v_mfma_f32_16x16x32_bf16 v[208:211], v[72:75], v[240:243], v[208:211]\n\t" \
    "v_mfma_f32_16x16x32_bf16 v[200:203], v[56:59], v[240:243], v[200:203]\n\t" \
    "s_waitcnt lgkmcnt(5)\n\t" \
    "v_mfma_f32_16x16x32_bf16 v[216:219], v[64:67], v[224:227], v[216:219]\n\t" \
    "ds_read_b128 v[240:243], v253 offset:24576\n\t" \
    "v_mfma_f32_16x16x32_bf16 v[220:223], v[72:75], v[224:227], v[220:223]\n\t" \
    "v_mfma_f32_16x16x32_bf16 v[212:215], v[56:59], v[224:227], v[212:215]\n\t" \
    "s_waitcnt lgkmcnt(4)\n\t" \
    "v_mfma_f32_16x16x32_bf16 v[204:207], v[68:71], v[228:231], v[204:207]\n\t" \
    "ds_read_b128 v[224:227], v253 offset:5120\n\t" \
    "v_mfma_f32_16x16x32_bf16 v[208:211], v[76:79], v[228:231], v[208:211]\n\t" \
    "v_mfma_f32_16x16x32_bf16 v[200:203], v[60:63], v[228:231], v[200:203]\n\t" \
    "s_waitcnt lgkmcnt(4)\n\t" \
    "v_mfma_f32_16x16x32_bf16 v[216:219], v[68:71], v[232:235], v[216:219]\n\t" \
    "ds_read_b128 v[228:231], v253 offset:25600\n\t" \
    "v_mfma_f32_16x16x32_bf16 v[220:223], v[76:79], v[232:235], v[220:223]\n\t" \
    "v_mfma_f32_16x16x32_bf16 v[212:215], v[60:63], v[232:235], v[212:215]\n\t" \
    "s_waitcnt lgkmcnt(3)\n\t" \
    "v_mfma_f32_16x16x32_bf16 v[204:207], v[80:83], v[236:239], v[204:207]\n\t" \
    "ds_read_b128 v[232:235], v253 offset:6144\n\t" \
    "v_mfma_f32_16x16x32_bf16 v[208:211], v[88:91], v[236:239], v[208:211]\n\t" \
    "v_mfma_f32_16x16x32_bf16 v[200:203], v[244:247], v[236:239], v[200:203]\n\t" \
    "s_waitcnt lgkmcnt(3)\n\t" \
    "v_mfma_f32_16x16x32_bf16 v[216:219], v[80:83], v[240:243], v[216:219]\n\t" \
    "ds_read_b128 v[236:239], v253 offset:26624\n\t" \
    "v_mfma_f32_16x16x32_bf16 v[220:223], v[88:91], v[240:243], v[220:223]\n\t" \
    "v_mfma_f32_16x16x32_bf16 v[212:215], v[244:247], v[240:243], v[212:215]\n\t" \
    "ds_read_b128 v[244:247], v254 offset:61440\n\t" \
    "s_waitcnt lgkmcnt(4)\n\t" \
    "v_mfma_f32_16x16x32_bf16 v[204:207], v[84:87], v[224:227], v[204:207]\n\t" \
    "ds_read_b128 v[240:243], v253 offset:7168\n\t" \
    "v_mfma_f32_16x16x32_bf16 v[208:211], v[92:95], v[224:227], v[208:211]\n\t" \
    "v_mfma_f32_16x16x32_bf16 v[200:203], v[248:251], v[224:227], v[200:203]\n\t" \
    "s_waitcnt lgkmcnt(4)\n\t" \
    "v_mfma_f32_16x16x32_bf16 v[216:219], v[84:87], v[228:231], v[216:219]\n\t" \
    "ds_read_b128 v[224:227], v253 offset:27648\n\t" \
    "v_mfma_f32_16x16x32_bf16 v[220:223], v[92:95], v[228:231], v[220:223]\n\t" \
    "v_mfma_f32_16x16x32_bf16 v[212:215], v[248:251], v[228:231], v[212:215]\n\t" \
    "ds_read_b128 v[248:251], v254 offset:62464\n\t" \
    "s_waitcnt lgkmcnt(5)\n\t" \
    "v_mfma_f32_16x16x32_bf16 v[204:207], v[104:107], v[232:235], v[204:207]\n\t" \
    "ds_read_b128 v[228:231], v253 offset:8192\n\t" \
    "v_mfma_f32_16x16x32_bf16 v[208:211], v[112:115], v[232:235], v[208:211]\n\t" \
    "v_mfma_f32_16x16x32_bf16 v[200:203], v[96:99], v[232:235], v[200:203]\n\t" \
    "s_waitcnt lgkmcnt(5)\n\t" \
    "v_mfma_f32_16x16x32_bf16 v[216:219], v[104:107], v[236:239], v[216:219]\n\t" \
    "ds_read_b128 v[232:235], v253 offset:28672\n\t" \
    "v_mfma_f32_16x16x32_bf16 v[220:223], v[112:115], v[236:239], v[220:223]\n\t" \
    "v_mfma_f32_16x16x32_bf16 v[212:215], v[96:99], v[236:239], v[212:215]\n\t" \
    "s_waitcnt lgkmcnt(4)\n\t" \
    "v_mfma_f32_16x16x32_bf16 v[204:207], v[108:111], v[240:243], v[204:207]\n\t" \
    "ds_read_b128 v[236:239], v253 offset:9216\n\t" \
    "v_mfma_f32_16x16x32_bf16 v[208:211], v[116:119], v[240:243], v[208:211]\n\t" \
    "v_mfma_f32_16x16x32_bf16 v[200:203], v[100:103], v[240:243], v[200:203]\n\t" \
    "s_waitcnt lgkmcnt(4)\n\t" \
    "v_mfma_f32_16x16x32_bf16 v[216:219], v[108:111], v[224:227], v[216:219]\n\t" \
    "ds_read_b128 v[240:243], v253 offset:29696\n\t" \
    "v_mfma_f32_16x16x32_bf16 v[220:223], v[116:119], v[224:227], v[220:223]\n\t" \
    "v_mfma_f32_16x16x32_bf16 v[212:215], v[100:103], v[224:227], v[212:215]\n\t" \
    "s_waitcnt lgkmcnt(3)\n\t" \
    "v_mfma_f32_16x16x32_bf16 v[204:207], v[120:123], v[228:231], v[204:207]\n\t" \
    "ds_read_b128 v[224:227], v253 offset:10240\n\t" \
    "v_mfma_f32_16x16x32_bf16 v[208:211], v[128:131], v[228:231], v[208:211]\n\t" \
    "v_mfma_f32_16x16x32_bf16 v[200:203], v[244:247], v[228:231], v[200:203]\n\t" \
    "s_waitcnt lgkmcnt(3)\n\t" \
    "v_mfma_f32_16x16x32_bf16 v[216:219], v[120:123], v[232:235], v[216:219]\n\t" \
    "ds_read_b128 v[228:231], v253 offset:30720\n\t" \
    "v_mfma_f32_16x16x32_bf16 v[220:223], v[128:131], v[232:235], v[220:223]\n\t" \
    "v_mfma_f32_16x16x32_bf16 v[212:215], v[244:247], v[232:235], v[212:215]\n\t" \
    "ds_read_b128 v[244:247], v254 offset:63488\n\t" \
    "s_waitcnt lgkmcnt(4)\n\t" \
    "v_mfma_f32_16x16x32_bf16 v[204:207], v[124:127], v[236:239], v[204:207]\n\t" \
    "ds_read_b128 v[232:235], v253 offset:11264\n\t" \
    "v_mfma_f32_16x16x32_bf16 v[208:211], v[132:135], v[236:239], v[208:211]\n\t" \
    "v_mfma_f32_16x16x32_bf16 v[200:203], v[248:251], v[236:239], v[200:203]\n\t" \
    "s_waitcnt lgkmcnt(4)\n\t" \
    "v_mfma_f32_16x16x32_bf16 v[216:219], v[124:127], v[240:243], v[216:219]\n\t" \
    "ds_read_b128 v[236:239], v253 offset:31744\n\t" \
    "v_mfma_f32_16x16x32_bf16 v[220:223], v[132:135], v[240:243], v[220:223]\n\t" \
    "v_mfma_f32_16x16x32_bf16 v[212:215], v[248:251], v[240:243], v[212:215]\n\t" \
    "ds_read_b128 v[248:251], v254 offset:64512\n\t" \
    "s_setprio 2\n\t" \
    "s_waitcnt lgkmcnt(0)\n\t" \
    "s_barrier \n\t" \
    "v_mfma_f32_16x16x32_bf16 v[204:207], v[144:147], v[224:227], v[204:207]\n\t" \
    "ds_read_b128 v[240:243], v253 offset:12288\n\t" \
    "v_mfma_f32_16x16x32_bf16 v[208:211], v[152:155], v[224:227], v[208:211]\n\t" \
    "v_mfma_f32_16x16x32_bf16 v[200:203], v[136:139], v[224:227], v[200:203]\n\t" \
    "v_mfma_f32_16x16x32_bf16 v[216:219], v[144:147], v[228:231], v[216:219]\n\t" \
    "ds_read_b128 v[224:227], v253 offset:32768\n\t" \
    "s_and_b32 s44, s40, 3\n\t" \
    "v_mfma_f32_16x16x32_bf16 v[220:223], v[152:155], v[228:231], v[220:223]\n\t" \
    "s_lshl_b32 s44, s44, 22\n\t" \
    "v_mfma_f32_16x16x32_bf16 v[212:215], v[136:139], v[228:231], v[212:215]\n\t" \
    "s_add_u32 s44, s44, %[mb]\n\t" \
    "v_mfma_f32_16x16x32_bf16 v[204:207], v[148:151], v[232:235], v[204:207]\n\t" \
    "ds_read_b128 v[228:231], v253 offset:13312\n\t" \
    "s_add_u32 s46, s44, 0x10000\n\t" \
    "v_mfma_f32_16x16x32_bf16 v[208:211], v[156:159], v[232:235], v[208:211]\n\t" \
    "s_add_u32 s47, s44, 0x10400\n\t" \
    "v_mfma_f32_16x16x32_bf16 v[200:203], v[140:143], v[232:235], v[200:203]\n\t" \
    "v_mfma_f32_16x16x32_bf16 v[216:219], v[148:151], v[236:239], v[216:219]\n\t" \
    "ds_read_b128 v[232:235], v253 offset:33792\n\t" \
    "v_mfma_f32_16x16x32_bf16 v[220:223], v[156:159], v[236:239], v[220:223]\n\t" \
    "v_mfma_f32_16x16x32_bf16 v[212:215], v[140:143], v[236:239], v[212:215]\n\t" \
    "s_waitcnt lgkmcnt(3)\n\t" \
    "v_mfma_f32_16x16x32_bf16 v[204:207], v[160:163], v[240:243], v[204:207]\n\t" \
    "ds_read_b128 v[236:239], v253 offset:16384\n\t" \
    "v_mfma_f32_16x16x32_bf16 v[208:211], v[168:171], v[240:243], v[208:211]\n\t" \
    "v_mfma_f32_16x16x32_bf16 v[200:203], v[244:247], v[240:243], v[200:203]\n\t" \
    "s_waitcnt lgkmcnt(3)\n\t" \
    "v_mfma_f32_16x16x32_bf16 v[216:219], v[160:163], v[224:227], v[216:219]\n\t" \
    "ds_read_b128 v[240:243], v253 offset:36864\n\t" \
    "v_mfma_f32_16x16x32_bf16 v[220:223], v[168:171], v[224:227], v[220:223]\n\t" \
    "v_mfma_f32_16x16x32_bf16 v[212:215], v[244:247], v[224:227], v[212:215]\n\t" \
    "ds_read_b128 v[244:247], v254 offset:55296\n\t" \
    "s_waitcnt lgkmcnt(4)\n\t" \
    "v_mfma_f32_16x16x32_bf16 v[204:207], v[164:167], v[228:231], v[204:207]\n\t" \
    "ds_read_b128 v[224:227], v253 offset:17408\n\t" \
    "v_mfma_f32_16x16x32_bf16 v[208:211], v[172:175], v[228:231], v[208:211]\n\t" \
    "v_mfma_f32_16x16x32_bf16 v[200:203], v[248:251], v[228:231], v[200:203]\n\t" \
    "s_waitcnt lgkmcnt(4)\n\t" \
    "v_mfma_f32_16x16x32_bf16 v[216:219], v[164:167], v[232:235], v[216:219]\n\t" \
    "ds_read_b128 v[228:231], v253 offset:37888\n\t" \
    "v_mfma_f32_16x16x32_bf16 v[220:223], v[172:175], v[232:235], v[220:223]\n\t" \
    "v_mfma_f32_16x16x32_bf16 v[212:215], v[248:251], v[232:235], v[212:215]\n\t" \
    "ds_read_b128 v[248:251], v254 offset:56320\n\t" \
    "s_waitcnt lgkmcnt(5)\n\t" \
    "v_mfma_f32_16x16x32_bf16 v[204:207], v[0:3], v[236:239], v[204:207]\n\t" \
    "ds_read_b128 v[232:235], v253 offset:14336\n\t" \
    "v_mfma_f32_16x16x32_bf16 v[208:211], v[8:11], v[236:239], v[208:211]\n\t" \
    "s_waitcnt lgkmcnt(4)\n\t" \
    "v_mfma_f32_16x16x32_bf16 v[200:203], v[244:247], v[236:239], v[200:203]\n\t" \
    "v_mfma_f32_16x16x32_bf16 v[216:219], v[0:3], v[240:243], v[216:219]\n\t" \
    "ds_read_b128 v[236:239], v253 offset:15360\n\t" \
    "v_mfma_f32_16x16x32_bf16 v[220:223], v[8:11], v[240:243], v[220:223]\n\t" \
    "v_mfma_f32_16x16x32_bf16 v[212:215], v[244:247], v[240:243], v[212:215]\n\t" \
    "s_waitcnt lgkmcnt(4)\n\t" \
    "v_mfma_f32_16x16x32_bf16 v[204:207], v[4:7], v[224:227], v[204:207]\n\t" \
    "ds_read_b128 v[240:243], v253 offset:18432\n\t" \
    "v_mfma_f32_16x16x32_bf16 v[208:211], v[12:15], v[224:227], v[208:211]\n\t" \
    "s_waitcnt lgkmcnt(3)\n\t" \
    "v_mfma_f32_16x16x32_bf16 v[200:203], v[248:251], v[224:227], v[200:203]\n\t" \
    "v_mfma_f32_16x16x32_bf16 v[216:219], v[4:7], v[228:231], v[216:219]\n\t" \
    "ds_read_b128 v[224:227], v253 offset:19456\n\t" \
    "v_mfma_f32_16x16x32_bf16 v[220:223], v[12:15], v[228:231], v[220:223]\n\t" \
    "v_mfma_f32_16x16x32_bf16 v[212:215], v[248:251], v[228:231], v[212:215]\n\t" \
    "s_waitcnt lgkmcnt(3)\n\t" \
    "v_mfma_f32_16x16x32_bf16 v[204:207], v[184:187], v[232:235], v[204:207]\n\t" \
    "ds_read_b128 v[228:231], v253 offset:34816\n\t" \
    "v_mov_b64 v[244:245], 0\n\t" \
    "v_mfma_f32_16x16x32_bf16 v[208:211], v[192:195], v[232:235], v[208:211]\n\t" \
    "v_mov_b64 v[246:247], 0\n\t" \
    "v_mfma_f32_16x16x32_bf16 v[200:203], v[176:179], v[232:235], v[200:203]\n\t" \
    "v_mov_b64 v[248:249], 0\n\t" \
    "s_waitcnt lgkmcnt(3)\n\t" \
    "v_mfma_f32_16x16x32_bf16 v[204:207], v[188:191], v[236:239], v[204:207]\n\t" \
    "ds_read_b128 v[232:235], v253 offset:35840\n\t" \
    "v_mov_b64 v[250:251], 0\n\t" \
    "v_mfma_f32_16x16x32_bf16 v[208:211], v[196:199], v[236:239], v[208:211]\n\t" \
    "v_mfma_f32_16x16x32_bf16 v[200:203], v[180:183], v[236:239], v[200:203]\n\t" \
    "s_waitcnt lgkmcnt(3)\n\t" \
    "v_mfma_f32_16x16x32_bf16 v[204:207], v[24:27], v[240:243], v[204:207]\n\t" \
    "ds_read_b128 v[236:239], v253 offset:38912\n\t" \
    "v_mfma_f32_16x16x32_bf16 v[208:211], v[32:35], v[240:243], v[208:211]\n\t" \
    "v_mfma_f32_16x16x32_bf16 v[200:203], v[16:19], v[240:243], v[200:203]\n\t" \
    "s_waitcnt lgkmcnt(3)\n\t" \
    "v_mfma_f32_16x16x32_bf16 v[204:207], v[28:31], v[224:227], v[204:207]\n\t" \
    "ds_read_b128 v[240:243], v253 offset:39936\n\t" \
    "v_mfma_f32_16x16x32_bf16 v[208:211], v[36:39], v[224:227], v[208:211]\n\t" \
    "v_mfma_f32_16x16x32_bf16 v[200:203], v[20:23], v[224:227], v[200:203]\n\t" \
    "s_waitcnt lgkmcnt(3)\n\t" \
    "v_mfma_f32_16x16x32_bf16 v[216:219], v[184:187], v[228:231], v[216:219]\n\t" \
    "s_waitcnt lgkmcnt(2)\n\t" \
    "v_mfma_f32_16x16x32_bf16 v[216:219], v[188:191], v[232:235], v[216:219]\n\t" \
    "v_cvt_pk_bf16_f32 v204, v204, v208\n\t" \
    "v_cvt_pk_bf16_f32 v205, v205, v209\n\t" \
    "s_waitcnt lgkmcnt(1)\n\t" \
    "v_mfma_f32_16x16x32_bf16 v[216:219], v[24:27], v[236:239], v[216:219]\n\t" \
    "v_cvt_pk_bf16_f32 v206, v206, v210\n\t" \
    "v_cvt_pk_bf16_f32 v207, v207, v211\n\t" \
    "s_waitcnt lgkmcnt(0)\n\t" \
    "v_mfma_f32_16x16x32_bf16 v[216:219], v[28:31], v[240:243], v[216:219]\n\t" \
    "ds_write_b128 v254, v[204:207] offset:5120\n\t" \
    "v_mfma_f32_16x16x32_bf16 v[220:223], v[192:195], v[228:231], v[220:223]\n\t" \
    "v_mfma_f32_16x16x32_bf16 v[220:223], v[196:199], v[232:235], v[220:223]\n\t" \
    "v_cvt_pk_bf16_f32 v200, v200, v201\n\t" \
    "v_mfma_f32_16x16x32_bf16 v[220:223], v[32:35], v[236:239], v[220:223]\n\t" \
    "v_cvt_pk_bf16_f32 v201, v202, v203\n\t" \
    "ds_write_b64 v254, v[200:201] offset:1032\n\t" \
    "v_mfma_f32_16x16x32_bf16 v[220:223], v[36:39], v[240:243], v[220:223]\n\t" \
    "v_mfma_f32_16x16x32_bf16 v[212:215], v[176:179], v[228:231], v[212:215]\n\t" \
    "v_mfma_f32_16x16x32_bf16 v[212:215], v[180:183], v[232:235], v[212:215]\n\t" \
    "ds_read_b128 v[200:203], v254 offset:55296\n\t" \
    "ds_read_b128 v[204:207], v254 offset:56320\n\t" \
    "v_mfma_f32_16x16x32_bf16 v[212:215], v[16:19], v[236:239], v[212:215]\n\t" \
    "v_mov_b64 v[208:209], 0\n\t" \
    "v_mov_b64 v[210:211], 0\n\t" \
    "v_mfma_f32_16x16x32_bf16 v[212:215], v[20:23], v[240:243], v[212:215]\n\t" \
    "v_cvt_pk_bf16_f32 v216, v216, v220\n\t" \
    "v_cvt_pk_bf16_f32 v217, v217, v221\n\t" \
    "s_setprio 0\n\t" \
    "v_cvt_pk_bf16_f32 v218, v218, v222\n\t" \
    "v_cvt_pk_bf16_f32 v219, v219, v223\n\t" \
    "ds_write_b128 v254, v[216:219] offset:11264\n\t" \
    "ds_read_b128 v[216:219], v254 offset:57344\n\t" \
    "ds_read_b128 v[220:223], v254 offset:58368\n\t" \
    "s_nop 0\n\t" \
    "v_cvt_pk_bf16_f32 v212, v212, v213\n\t" \
    "v_cvt_pk_bf16_f32 v213, v214, v215\n\t" \
    "ds_write_b64 v254, v[212:213] offset:7176\n\t" \
    "s_sleep 3\n\t" \
    "s_mov_b32 m0, 0xc000\n\t" \
    "s_nop 0\n\t" \
    "buffer_load_dwordx4 v253, s[60:63], s46 offen sc1 lds\n\t" \
    "s_mov_b32 m0, 0xc400\n\t" \
    "s_nop 0\n\t" \
    "buffer_load_dwordx4 v253, s[60:63], s47 offen sc1 lds\n\t" \
    "s_waitcnt lgkmcnt(0)\n\t" \
    "v_bfe_i32 v212, v253, 5, 1\n\t" \
    "v_and_b32 v212, 0xaaaa, v212\n\t" \
    "v_or_b32 v212, 0x4444, v212\n\t" \
    "v_and_b32 v213, 0xffff, v255\n\t" \
    "v_lshrrev_b32 v214, 16, v255\n\t" \
    ".Ls34_%=:\n\t" \
    "s_waitcnt vmcnt(0)\n\t" \
    "ds_read_b128 v[224:227], v253 offset:49152\n\t" \
    "ds_read_b128 v[228:231], v253 offset:50176\n\t" \
    "s_waitcnt lgkmcnt(1)\n\t" \
    "v_max_u32 v215, v224, v225\n\t" \
    "v_max3_u32 v215, v226, v227, v215\n\t" \
    "s_waitcnt lgkmcnt(0)\n\t" \
    "v_max3_u32 v215, v228, v229, v215\n\t" \
    "v_max3_u32 v215, v230, v231, v215\n\t" \
    "v_cmp_eq_u32 vcc, -1, v215\n\t" \
    "s_cbranch_vccz .Ls35_%=\n\t" \
    "v_max_u32 v215, v224, v225\n\t" \
    "v_max3_u32 v215, v226, v227, v215\n\t" \
    "v_cmp_eq_u32 vcc, -1, v215\n\t" \
    "s_cbranch_vccz .Ls36_%=\n\t" \
    "s_mov_b32 m0, 0xc000\n\t" \
    "s_nop 0\n\t" \
    "buffer_load_dwordx4 v253, s[60:63], s46 offen sc1 lds\n\t" \
    ".Ls36_%=:\n\t" \
    "v_max_u32 v215, v228, v229\n\t" \
    "v_max3_u32 v215, v230, v231, v215\n\t" \
    "v_cmp_eq_u32 vcc, -1, v215\n\t" \
    "s_cbranch_vccz .Ls37_%=\n\t" \
    "s_mov_b32 m0, 0xc400\n\t" \
    "s_nop 0\n\t" \
    "buffer_load_dwordx4 v253, s[60:63], s47 offen sc1 lds\n\t" \
    ".Ls37_%=:\n\t" \
    "s_cmp_eq_u32 s41, 0\n\t" \
    "s_cbranch_scc1 .Ls35_%=\n\t" \
    "s_sub_u32 s41, s41, 1\n\t" \
    "s_branch .Ls34_%=\n\t" \
    ".Ls35_%=:\n\t" \
    "s_setprio 3\n\t" \
    "s_waitcnt lgkmcnt(0)\n\t" \
    "s_barrier \n\t" \
    "ds_read_b128 v[232:235], v253 offset:40960\n\t" \
    "ds_read_b128 v[236:239], v253 offset:41984\n\t" \
    "ds_read_b128 v[240:243], v253 offset:43008\n\t" \
    "v_smfmac_f32_16x16x64_bf16 v[244:247], v[224:227], v[0:7], v212\n\t" \
    "v_smfmac_f32_16x16x64_bf16 v[248:251], v[224:227], v[8:15], v212\n\t" \
    "v_smfmac_f32_16x16x64_bf16 v[208:211], v[224:227], v[200:207], v212\n\t" \
    "ds_read_b128 v[200:203], v254 offset:59392\n\t" \
    "ds_read_b128 v[204:207], v254 offset:60416\n\t" \
    "ds_read_b128 v[224:227], v253 offset:44032\n\t" \
    "v_smfmac_f32_16x16x64_bf16 v[208:211], v[228:231], v[16:23], v212\n\t" \
    "v_smfmac_f32_16x16x64_bf16 v[244:247], v[228:231], v[24:31], v212\n\t" \
    "v_smfmac_f32_16x16x64_bf16 v[248:251], v[228:231], v[32:39], v212\n\t" \
    "ds_read_b128 v[228:231], v253 offset:45056\n\t" \
    "s_waitcnt lgkmcnt(6)\n\t" \
    "v_smfmac_f32_16x16x64_bf16 v[208:211], v[232:235], v[216:223], v212\n\t" \
    "ds_read_b128 v[216:219], v254 offset:61440\n\t" \
    "ds_read_b128 v[220:223], v254 offset:62464\n\t" \
    "v_smfmac_f32_16x16x64_bf16 v[244:247], v[232:235], v[40:47], v212\n\t" \
    "v_smfmac_f32_16x16x64_bf16 v[248:251], v[232:235], v[48:55], v212\n\t" \
    "ds_read_b128 v[232:235], v253 offset:46080\n\t" \
    "s_waitcnt lgkmcnt(8)\n\t" \
    "v_smfmac_f32_16x16x64_bf16 v[208:211], v[236:239], v[56:63], v212\n\t" \
    "v_smfmac_f32_16x16x64_bf16 v[244:247], v[236:239], v[64:71], v212\n\t" \
    "v_smfmac_f32_16x16x64_bf16 v[248:251], v[236:239], v[72:79], v212\n\t" \
    "ds_read_b128 v[236:239], v253 offset:47104\n\t" \
    "s_waitcnt lgkmcnt(6)\n\t" \
    "v_smfmac_f32_16x16x64_bf16 v[208:211], v[240:243], v[200:207], v212\n\t" \
    "ds_read_b128 v[200:203], v254 offset:63488\n\t" \
    "ds_read_b128 v[204:207], v254 offset:64512\n\t" \
    "v_smfmac_f32_16x16x64_bf16 v[244:247], v[240:243], v[80:87], v212\n\t" \
    "v_smfmac_f32_16x16x64_bf16 v[248:251], v[240:243], v[88:95], v212\n\t" \
    "ds_read_b128 v[240:243], v253 offset:48128\n\t" \
    "s_waitcnt lgkmcnt(8)\n\t" \
    "v_smfmac_f32_16x16x64_bf16 v[208:211], v[224:227], v[96:103], v212\n\t" \
    "v_smfmac_f32_16x16x64_bf16 v[244:247], v[224:227], v[104:111], v212\n\t" \
    "v_smfmac_f32_16x16x64_bf16 v[248:251], v[224:227], v[112:119], v212\n\t" \
    "s_waitcnt lgkmcnt(5)\n\t" \
    "v_smfmac_f32_16x16x64_bf16 v[208:211], v[228:231], v[216:223], v212\n\t" \
    "s_waitcnt lgkmcnt(4)\n\t" \
    "v_smfmac_f32_16x16x64_bf16 v[208:211], v[232:235], v[136:143], v212\n\t" \
    "s_waitcnt lgkmcnt(1)\n\t" \
    "v_smfmac_f32_16x16x64_bf16 v[208:211], v[236:239], v[200:207], v212\n\t" \
    "s_waitcnt lgkmcnt(0)\n\t" \
    "v_smfmac_f32_16x16x64_bf16 v[208:211], v[240:243], v[176:183], v212\n\t" \
    "v_smfmac_f32_16x16x64_bf16 v[244:247], v[228:231], v[120:127], v212\n\t" \
    "v_smfmac_f32_16x16x64_bf16 v[244:247], v[232:235], v[144:151], v212\n\t" \
    "v_smfmac_f32_16x16x64_bf16 v[244:247], v[236:239], v[160:167], v212\n\t" \
    "v_add_f32 v208, v208, v210\n\t" \
    "v_add_f32 v209, v209, v211\n\t" \
    "v_cvt_pk_bf16_f32 v208, v208, v209\n\t" \
    "v_smfmac_f32_16x16x64_bf16 v[244:247], v[240:243], v[184:191], v212\n\t" \
    "ds_write_b16 v213, v208 offset:0\n\t" \
    "ds_write_b16_d16_hi v213, v208 offset:16\n\t" \
    "v_smfmac_f32_16x16x64_bf16 v[248:251], v[228:231], v[128:135], v212\n\t" \
    "v_smfmac_f32_16x16x64_bf16 v[248:251], v[232:235], v[152:159], v212\n\t" \
    "v_smfmac_f32_16x16x64_bf16 v[248:251], v[236:239], v[168:175], v212\n\t" \
    "v_add_f32 v244, v244, v246\n\t" \
    "v_add_f32 v245, v245, v247\n\t" \
    "v_smfmac_f32_16x16x64_bf16 v[248:251], v[240:243], v[192:199], v212\n\t" \
    "s_nop 7\n\t" \
    "v_add_f32 v248, v248, v250\n\t" \
    "v_add_f32 v249, v249, v251\n\t" \
    "v_cvt_pk_bf16_f32 v244, v244, v248\n\t" \
    "v_cvt_pk_bf16_f32 v245, v245, v249\n\t" \
    "ds_write2_b32 v214, v244, v245 offset1:4\n\t" \
    "s_setprio 0\n\t" \
    "s_sleep 1\n\t" \
    "ds_read_b128 v[208:211], v254 offset:24576\n\t" \
    "ds_read_b128 v[224:227], v253 offset:0\n\t" \
    "ds_read_b128 v[228:231], v253 offset:20480\n\t" \
    "ds_read_b128 v[212:215], v254 offset:25600\n\t" \
    "ds_read_b128 v[232:235], v253 offset:1024\n\t" \
    "ds_read_b128 v[236:239], v253 offset:21504\n\t" \
    "ds_read_b128 v[216:219], v254 offset:26624\n\t" \
    "ds_read_b128 v[240:243], v253 offset:2048\n\t" \
    "ds_read_b128 v[244:247], v253 offset:22528\n\t" \
    "ds_read_b128 v[220:223], v254 offset:27648\n\t" \
    "ds_read_b128 v[248:251], v253 offset:3072\n\t" \
    "s_waitcnt lgkmcnt(9)\n\t" \
    "v_mfma_f32_16x16x32_bf16 v[200:203], v[208:211], v[224:227], 0\n\t" \
    "ds_read_b128 v[224:227], v253 offset:23552\n\t" \
    "s_waitcnt lgkmcnt(9)\n\t" \
    "v_mfma_f32_16x16x32_bf16 v[204:207], v[208:211], v[228:231], 0\n\t" \
    "ds_read_b128 v[228:231], v253 offset:4096\n\t" \
    "ds_read_b128 v[208:211], v254 offset:28672\n\t" \
    "s_waitcnt lgkmcnt(9)\n\t" \
    "v_mfma_f32_16x16x32_bf16 v[200:203], v[212:215], v[232:235], v[200:203]\n\t" \
    "ds_read_b128 v[232:235], v253 offset:24576\n\t" \
    "s_waitcnt lgkmcnt(9)\n\t" \
    "v_mfma_f32_16x16x32_bf16 v[204:207], v[212:215], v[236:239], v[204:207]\n\t" \
    "ds_read_b128 v[236:239], v253 offset:5120\n\t" \
    "ds_read_b128 v[212:215], v254 offset:29696\n\t" \
    "s_waitcnt lgkmcnt(9)\n\t" \
    "v_mfma_f32_16x16x32_bf16 v[200:203], v[216:219], v[240:243], v[200:203]\n\t" \
    "ds_read_b128 v[240:243], v253 offset:25600\n\t" \
    "s_waitcnt lgkmcnt(9)\n\t" \
    "v_mfma_f32_16x16x32_bf16 v[204:207], v[216:219], v[244:247], v[204:207]\n\t" \
    "ds_read_b128 v[244:247], v253 offset:6144\n\t" \
    "ds_read_b128 v[216:219], v254 offset:30720\n\t" \
    "s_waitcnt lgkmcnt(9)\n\t" \
    "v_mfma_f32_16x16x32_bf16 v[200:203], v[220:223], v[248:251], v[200:203]\n\t" \
    "ds_read_b128 v[248:251], v253 offset:26624\n\t" \
    "s_waitcnt lgkmcnt(0)\n\t" \
    "s_barrier \n\t" \
    "s_add_u32 s40, s40, 1\n\t" \
    "s_cmp_lg_u32 s40, 64\n\t" \
    "s_cbranch_scc1 .Ls33_%=\n\t" \
    "s_branch .Lend%=\n\t" \
    ".Ls2_%=:\n\t" \
    "v_add_u32 v244, 0x18000, v243\n\t" \
    "s_waitcnt vmcnt(12) lgkmcnt(0)\n\t" \
    "s_barrier \n\t" \
    "s_add_u32 m0, %[wm], 0x12000\n\t" \
    "s_mov_b32 s44, 0x1e0000\n\t" \
    "buffer_load_dwordx4 v240, %[wr], s44 offen offset:0 nt lds\n\t" \
    "buffer_load_dwordx4 v241, %[wr], s44 offen offset:1024 nt lds\n\t" \
    "buffer_load_dwordx4 v242, %[wr], s44 offen offset:2048 nt lds\n\t" \
    "ds_read_b64_tr_b16 v[208:209], v244 offset:0\n\t" \
    "ds_read_b64_tr_b16 v[210:211], v244 offset:1536\n\t" \
    "ds_read_b64_tr_b16 v[40:41], v244 offset:32\n\t" \
    "ds_read_b64_tr_b16 v[42:43], v244 offset:1568\n\t" \
    "ds_read_b64_tr_b16 v[48:49], v244 offset:64\n\t" \
    "ds_read_b64_tr_b16 v[50:51], v244 offset:1600\n\t" \
    "v_add_u32 v244, 0x1e000, v243\n\t" \
    "s_waitcnt vmcnt(12) lgkmcnt(0)\n\t" \
    "s_barrier \n\t" \
    "s_add_u32 m0, %[wm], 0x18000\n\t" \
    "s_mov_b32 s44, 0x240000\n\t" \
    "buffer_load_dwordx4 v240, %[wr], s44 offen offset:0 nt lds\n\t" \
    "buffer_load_dwordx4 v241, %[wr], s44 offen offset:1024 nt lds\n\t" \
    "buffer_load_dwordx4 v242, %[wr], s44 offen offset:2048 nt lds\n\t" \
    "ds_read_b64_tr_b16 v[212:213], v244 offset:0\n\t" \
    "ds_read_b64_tr_b16 v[214:215], v244 offset:1536\n\t" \
    "ds_read_b64_tr_b16 v[44:45], v244 offset:32\n\t" \
    "ds_read_b64_tr_b16 v[46:47], v244 offset:1568\n\t" \
    "ds_read_b64_tr_b16 v[52:53], v244 offset:64\n\t" \
    "ds_read_b64_tr_b16 v[54:55], v244 offset:1600\n\t" \
    "v_add_u32 v244, 0x0, v243\n\t" \
    "s_waitcnt vmcnt(12) lgkmcnt(0)\n\t" \
    "s_barrier \n\t" \
    "s_add_u32 m0, %[wm], 0x1e000\n\t" \
    "s_mov_b32 s44, 0x2a0000\n\t" \
    "buffer_load_dwordx4 v240, %[wr], s44 offen offset:0 nt lds\n\t" \
    "buffer_load_dwordx4 v241, %[wr], s44 offen offset:1024 nt lds\n\t" \
    "buffer_load_dwordx4 v242, %[wr], s44 offen offset:2048 nt lds\n\t" \
    "ds_read_b64_tr_b16 v[56:57], v244 offset:0\n\t" \
    "ds_read_b64_tr_b16 v[58:59], v244 offset:1536\n\t" \
    "ds_read_b64_tr_b16 v[64:65], v244 offset:32\n\t" \
    "ds_read_b64_tr_b16 v[66:67], v244 offset:1568\n\t" \
    "ds_read_b64_tr_b16 v[72:73], v244 offset:64\n\t" \
    "ds_read_b64_tr_b16 v[74:75], v244 offset:1600\n\t" \
    "v_add_u32 v244, 0x6000, v243\n\t" \
    "s_waitcnt vmcnt(12) lgkmcnt(0)\n\t" \
    "s_barrier \n\t" \
    "s_add_u32 m0, %[wm], 0x0\n\t" \
    "s_mov_b32 s44, 0x300000\n\t" \
    "buffer_load_dwordx4 v240, %[wr], s44 offen offset:0 nt lds\n\t" \
    "buffer_load_dwordx4 v241, %[wr], s44 offen offset:1024 nt lds\n\t" \
    "buffer_load_dwordx4 v242, %[wr], s44 offen offset:2048 nt lds\n\t" \
    "ds_read_b64_tr_b16 v[60:61], v244 offset:0\n\t" \
    "ds_read_b64_tr_b16 v[62:63], v244 offset:1536\n\t" \
    "ds_read_b64_tr_b16 v[68:69], v244 offset:32\n\t" \
    "ds_read_b64_tr_b16 v[70:71], v244 offset:1568\n\t" \
    "ds_read_b64_tr_b16 v[76:77], v244 offset:64\n\t" \
    "ds_read_b64_tr_b16 v[78:79], v244 offset:1600\n\t" \
    "v_add_u32 v244, 0xc000, v243\n\t" \
    "s_waitcnt vmcnt(12) lgkmcnt(0)\n\t" \
    "s_barrier \n\t" \
    "s_add_u32 m0, %[wm], 0x6000\n\t" \
    "s_mov_b32 s44, 0x360000\n\t" \
    "buffer_load_dwordx4 v240, %[wr], s44 offen offset:0 nt lds\n\t" \
    "buffer_load_dwordx4 v241, %[wr], s44 offen offset:1024 nt lds\n\t" \
    "buffer_load_dwordx4 v242, %[wr], s44 offen offset:2048 nt lds\n\t" \
    "ds_read_b64_tr_b16 v[216:217], v244 offset:0\n\t" \
    "ds_read_b64_tr_b16 v[218:219], v244 offset:1536\n\t" \
    "ds_read_b64_tr_b16 v[80:81], v244 offset:32\n\t" \
    "ds_read_b64_tr_b16 v[82:83], v244 offset:1568\n\t" \
    "ds_read_b64_tr_b16 v[88:89], v244 offset:64\n\t" \
    "ds_read_b64_tr_b16 v[90:91], v244 offset:1600\n\t" \
    "v_add_u32 v244, 0x12000, v243\n\t" \
    "s_waitcnt vmcnt(12) lgkmcnt(0)\n\t" \
    "s_barrier \n\t" \
    "s_add_u32 m0, %[wm], 0xc000\n\t" \
    "s_mov_b32 s44, 0x3c0000\n\t" \
    "buffer_load_dwordx4 v240, %[wr], s44 offen offset:0 nt lds\n\t" \
    "buffer_load_dwordx4 v241, %[wr], s44 offen offset:1024 nt lds\n\t" \
    "buffer_load_dwordx4 v242, %[wr], s44 offen offset:2048 nt lds\n\t" \
    "ds_read_b64_tr_b16 v[220:221], v244 offset:0\n\t" \
    "ds_read_b64_tr_b16 v[222:223], v244 offset:1536\n\t" \
    "ds_read_b64_tr_b16 v[84:85], v244 offset:32\n\t" \
    "ds_read_b64_tr_b16 v[86:87], v244 offset:1568\n\t" \
    "ds_read_b64_tr_b16 v[92:93], v244 offset:64\n\t" \
    "ds_read_b64_tr_b16 v[94:95], v244 offset:1600\n\t" \
    "v_add_u32 v244, 0x18000, v243\n\t" \
    "s_waitcnt vmcnt(12) lgkmcnt(0)\n\t" \
    "s_barrier \n\t" \
    "s_add_u32 m0, %[wm], 0x12000\n\t" \
    "s_mov_b32 s44, 0x420000\n\t" \
    "buffer_load_dwordx4 v240, %[wr], s44 offen offset:0 nt lds\n\t" \
    "buffer_load_dwordx4 v241, %[wr], s44 offen offset:1024 nt lds\n\t" \
    "buffer_load_dwordx4 v242, %[wr], s44 offen offset:2048 nt lds\n\t" \
    "ds_read_b64_tr_b16 v[96:97], v244 offset:0\n\t" \
    "ds_read_b64_tr_b16 v[98:99], v244 offset:1536\n\t" \
    "ds_read_b64_tr_b16 v[104:105], v244 offset:32\n\t" \
    "ds_read_b64_tr_b16 v[106:107], v244 offset:1568\n\t" \
    "ds_read_b64_tr_b16 v[112:113], v244 offset:64\n\t" \
    "ds_read_b64_tr_b16 v[114:115], v244 offset:1600\n\t" \
    "v_add_u32 v244, 0x1e000, v243\n\t" \
    "s_waitcnt vmcnt(12) lgkmcnt(0)\n\t" \
    "s_barrier \n\t" \
    "s_add_u32 m0, %[wm], 0x18000\n\t" \
    "s_mov_b32 s44, 0x480000\n\t" \
    "buffer_load_dwordx4 v240, %[wr], s44 offen offset:0 nt lds\n\t" \
    "buffer_load_dwordx4 v241, %[wr], s44 offen offset:1024 nt lds\n\t" \
    "buffer_load_dwordx4 v242, %[wr], s44 offen offset:2048 nt lds\n\t" \
    "ds_read_b64_tr_b16 v[100:101], v244 offset:0\n\t" \
    "ds_read_b64_tr_b16 v[102:103], v244 offset:1536\n\t" \
    "ds_read_b64_tr_b16 v[108:109], v244 offset:32\n\t" \
    "ds_read_b64_tr_b16 v[110:111], v244 offset:1568\n\t" \
    "ds_read_b64_tr_b16 v[116:117], v244 offset:64\n\t" \
    "ds_read_b64_tr_b16 v[118:119], v244 offset:1600\n\t" \
    "v_add_u32 v244, 0x0, v243\n\t" \
    "s_waitcnt vmcnt(12) lgkmcnt(0)\n\t" \
    "s_barrier \n\t" \
    "v_mov_b32 v246, 0\n\t" \
    "buffer_load_dword v246, v246, s[60:63], %[mo] offen sc1\n\t" \
    "s_add_u32 m0, %[wm], 0x1e000\n\t" \
    "s_mov_b32 s44, 0x4e0000\n\t" \
    "buffer_load_dwordx4 v240, %[wr], s44 offen offset:0 nt lds\n\t" \
    "buffer_load_dwordx4 v241, %[wr], s44 offen offset:1024 nt lds\n\t" \
    "buffer_load_dwordx4 v242, %[wr], s44 offen offset:2048 nt lds\n\t" \
    "ds_read_b64_tr_b16 v[200:201], v244 offset:0\n\t" \
    "ds_read_b64_tr_b16 v[202:203], v244 offset:1536\n\t" \
    "ds_read_b64_tr_b16 v[0:1], v244 offset:32\n\t" \
    "ds_read_b64_tr_b16 v[2:3], v244 offset:1568\n\t" \
    "ds_read_b64_tr_b16 v[8:9], v244 offset:64\n\t" \
    "ds_read_b64_tr_b16 v[10:11], v244 offset:1600\n\t" \
    "v_add_u32 v244, 0x6000, v243\n\t" \
    "s_waitcnt vmcnt(12) lgkmcnt(0)\n\t" \
    "s_barrier \n\t" \
    "s_add_u32 m0, %[wm], 0x0\n\t" \
    "s_mov_b32 s44, 0x540000\n\t" \
    "buffer_load_dwordx4 v240, %[wr], s44 offen offset:0 nt lds\n\t" \
    "buffer_load_dwordx4 v241, %[wr], s44 offen offset:1024 nt lds\n\t" \
    "buffer_load_dwordx4 v242, %[wr], s44 offen offset:2048 nt lds\n\t" \
    "ds_read_b64_tr_b16 v[204:205], v244 offset:0\n\t" \
    "ds_read_b64_tr_b16 v[206:207], v244 offset:1536\n\t" \
    "ds_read_b64_tr_b16 v[4:5], v244 offset:32\n\t" \
    "ds_read_b64_tr_b16 v[6:7], v244 offset:1568\n\t" \
    "ds_read_b64_tr_b16 v[12:13], v244 offset:64\n\t" \
    "ds_read_b64_tr_b16 v[14:15], v244 offset:1600\n\t" \
    "v_add_u32 v244, 0xc000, v243\n\t" \
    "s_waitcnt vmcnt(12) lgkmcnt(0)\n\t" \
    "s_barrier \n\t" \
    "s_add_u32 m0, %[wm], 0x6000\n\t" \
    "s_mov_b32 s44, 0x5a0000\n\t" \
    "buffer_load_dwordx4 v240, %[wr], s44 offen offset:0 nt lds\n\t" \
    "buffer_load_dwordx4 v241, %[wr], s44 offen offset:1024 nt lds\n\t" \
    "buffer_load_dwordx4 v242, %[wr], s44 offen offset:2048 nt lds\n\t" \
    "ds_read_b64_tr_b16 v[224:225], v244 offset:0\n\t" \
    "ds_read_b64_tr_b16 v[226:227], v244 offset:1536\n\t" \
    "ds_read_b64_tr_b16 v[120:121], v244 offset:32\n\t" \
    "ds_read_b64_tr_b16 v[122:123], v244 offset:1568\n\t" \
    "ds_read_b64_tr_b16 v[128:129], v244 offset:64\n\t" \
    "ds_read_b64_tr_b16 v[130:131], v244 offset:1600\n\t" \
    "v_add_u32 v244, 0x12000, v243\n\t" \
    "s_waitcnt vmcnt(12) lgkmcnt(0)\n\t" \
    "s_barrier \n\t" \
    "s_add_u32 m0, %[wm], 0xc000\n\t" \
    "s_mov_b32 s44, 0x600000\n\t" \
    "buffer_load_dwordx4 v240, %[wr], s44 offen offset:0 nt lds\n\t" \
    "buffer_load_dwordx4 v241, %[wr], s44 offen offset:1024 nt lds\n\t" \
    "buffer_load_dwordx4 v242, %[wr], s44 offen offset:2048 nt lds\n\t" \
    "ds_read_b64_tr_b16 v[228:229], v244 offset:0\n\t" \
    "ds_read_b64_tr_b16 v[230:231], v244 offset:1536\n\t" \
    "ds_read_b64_tr_b16 v[124:125], v244 offset:32\n\t" \
    "ds_read_b64_tr_b16 v[126:127], v244 offset:1568\n\t" \
    "ds_read_b64_tr_b16 v[132:133], v244 offset:64\n\t" \
    "ds_read_b64_tr_b16 v[134:135], v244 offset:1600\n\t" \
    "v_add_u32 v244, 0x18000, v243\n\t" \
    "s_waitcnt vmcnt(12) lgkmcnt(0)\n\t" \
    "s_barrier \n\t" \
    "s_add_u32 m0, %[wm], 0x12000\n\t" \
    "s_mov_b32 s44, 0x660000\n\t" \
    "buffer_load_dwordx4 v240, %[wr], s44 offen offset:0 nt lds\n\t" \
    "buffer_load_dwordx4 v241, %[wr], s44 offen offset:1024 nt lds\n\t" \
    "buffer_load_dwordx4 v242, %[wr], s44 offen offset:2048 nt lds\n\t" \
    "ds_read_b64_tr_b16 v[136:137], v244 offset:0\n\t" \
    "ds_read_b64_tr_b16 v[138:139], v244 offset:1536\n\t" \
    "ds_read_b64_tr_b16 v[144:145], v244 offset:32\n\t" \
    "ds_read_b64_tr_b16 v[146:147], v244 offset:1568\n\t" \
    "ds_read_b64_tr_b16 v[152:153], v244 offset:64\n\t" \
    "ds_read_b64_tr_b16 v[154:155], v244 offset:1600\n\t" \
    "v_add_u32 v244, 0x1e000, v243\n\t" \
    "s_waitcnt vmcnt(12) lgkmcnt(0)\n\t" \
    "s_barrier \n\t" \
    ".Ls21_%=:\n\t" \
    "v_readfirstlane_b32 s43, v246\n\t" \
    "s_nop 0\n\t" \
    "s_cmp_lg_u32 s43, -1\n\t" \
    "s_cbranch_scc1 .Ls22_%=\n\t" \
    "s_cmp_eq_u32 s41, 0\n\t" \
    "s_cbranch_scc1 .Ls22_%=\n\t" \
    "s_sub_u32 s41, s41, 1\n\t" \
    "s_sleep 4\n\t" \
    "v_mov_b32 v246, 0\n\t" \
    "buffer_load_dword v246, v246, s[60:63], %[mo] offen sc1\n\t" \
    "s_waitcnt vmcnt(0)\n\t" \
    "s_branch .Ls21_%=\n\t" \
    ".Ls22_%=:\n\t" \
    "v_mov_b32 v246, 0x8000\n\t" \
    "s_mov_b64 exec, 1\n\t" \
    "buffer_store_dword v246, v246, s[60:63], %[mo] offen sc1\n\t" \
    "s_mov_b64 exec, -1\n\t" \
    "s_and_b32 s44, s43, 0x5000\n\t" \
    "s_xor_b32 %[mb], %[mb], s44\n\t" \
    "s_xor_b32 %[fl], %[fl], s44\n\t" \
    "s_and_b32 s43, s43, 0x2000\n\t" \
    "s_xor_b32 s43, s43, %[lx]\n\t" \
    "s_add_u32 s60, s60, s43\n\t" \
    "s_addc_u32 s61, s61, 0\n\t" \
    "s_add_u32 m0, %[wm], 0x18000\n\t" \
    "s_mov_b32 s44, 0x6c0000\n\t" \
    "buffer_load_dwordx4 v240, %[wr], s44 offen offset:0 nt lds\n\t" \
    "buffer_load_dwordx4 v241, %[wr], s44 offen offset:1024 nt lds\n\t" \
    "buffer_load_dwordx4 v242, %[wr], s44 offen offset:2048 nt lds\n\t" \
    "ds_read_b64_tr_b16 v[140:141], v244 offset:0\n\t" \
    "ds_read_b64_tr_b16 v[142:143], v244 offset:1536\n\t" \
    "ds_read_b64_tr_b16 v[148:149], v244 offset:32\n\t" \
    "ds_read_b64_tr_b16 v[150:151], v244 offset:1568\n\t" \
    "ds_read_b64_tr_b16 v[156:157], v244 offset:64\n\t" \
    "ds_read_b64_tr_b16 v[158:159], v244 offset:1600\n\t" \
    "v_add_u32 v244, 0x0, v243\n\t" \
    "s_waitcnt vmcnt(12) lgkmcnt(0)\n\t" \
    "s_barrier \n\t" \
    "s_add_u32 m0, %[wm], 0x1e000\n\t" \
    "s_mov_b32 s44, 0x720000\n\t" \
    "buffer_load_dwordx4 v240, %[wr], s44 offen offset:0 nt lds\n\t" \
    "buffer_load_dwordx4 v241, %[wr], s44 offen offset:1024 nt lds\n\t" \
    "buffer_load_dwordx4 v242, %[wr], s44 offen offset:2048 nt lds\n\t" \
    "ds_read_b64_tr_b16 v[16:17], v244 offset:0\n\t" \
    "ds_read_b64_tr_b16 v[18:19], v244 offset:1536\n\t" \
    "ds_read_b64_tr_b16 v[24:25], v244 offset:32\n\t" \
    "ds_read_b64_tr_b16 v[26:27], v244 offset:1568\n\t" \
    "ds_read_b64_tr_b16 v[32:33], v244 offset:64\n\t" \
    "ds_read_b64_tr_b16 v[34:35], v244 offset:1600\n\t" \
    "v_add_u32 v244, 0x6000, v243\n\t" \
    "s_waitcnt vmcnt(12) lgkmcnt(0)\n\t" \
    "s_barrier \n\t" \
    "ds_read_b64_tr_b16 v[20:21], v244 offset:0\n\t" \
    "ds_read_b64_tr_b16 v[22:23], v244 offset:1536\n\t" \
    "ds_read_b64_tr_b16 v[28:29], v244 offset:32\n\t" \
    "ds_read_b64_tr_b16 v[30:31], v244 offset:1568\n\t" \
    "ds_read_b64_tr_b16 v[36:37], v244 offset:64\n\t" \
    "ds_read_b64_tr_b16 v[38:39], v244 offset:1600\n\t" \
    "v_add_u32 v244, 0xc000, v243\n\t" \
    "s_waitcnt vmcnt(9) lgkmcnt(0)\n\t" \
    "s_barrier \n\t" \
    "ds_read_b64_tr_b16 v[232:233], v244 offset:0\n\t" \
    "ds_read_b64_tr_b16 v[234:235], v244 offset:1536\n\t" \
    "ds_read_b64_tr_b16 v[160:161], v244 offset:32\n\t" \
    "ds_read_b64_tr_b16 v[162:163], v244 offset:1568\n\t" \
    "ds_read_b64_tr_b16 v[168:169], v244 offset:64\n\t" \
    "ds_read_b64_tr_b16 v[170:171], v244 offset:1600\n\t" \
    "v_add_u32 v244, 0x12000, v243\n\t" \
    "s_waitcnt vmcnt(6) lgkmcnt(0)\n\t" \
    "s_barrier \n\t" \
    "ds_read_b64_tr_b16 v[236:237], v244 offset:0\n\t" \
    "ds_read_b64_tr_b16 v[238:239], v244 offset:1536\n\t" \
    "ds_read_b64_tr_b16 v[164:165], v244 offset:32\n\t" \
    "ds_read_b64_tr_b16 v[166:167], v244 offset:1568\n\t" \
    "ds_read_b64_tr_b16 v[172:173], v244 offset:64\n\t" \
    "ds_read_b64_tr_b16 v[174:175], v244 offset:1600\n\t" \
    "v_add_u32 v244, 0x18000, v243\n\t" \
    "s_waitcnt vmcnt(3) lgkmcnt(0)\n\t" \
    "s_barrier \n\t" \
    "ds_read_b64_tr_b16 v[176:177], v244 offset:0\n\t" \
    "ds_read_b64_tr_b16 v[178:179], v244 offset:1536\n\t" \
    "ds_read_b64_tr_b16 v[184:185], v244 offset:32\n\t" \
    "ds_read_b64_tr_b16 v[186:187], v244 offset:1568\n\t" \
    "ds_read_b64_tr_b16 v[192:193], v244 offset:64\n\t" \
    "ds_read_b64_tr_b16 v[194:195], v244 offset:1600\n\t" \
    "v_add_u32 v244, 0x1e000, v243\n\t" \
    "s_waitcnt vmcnt(0) lgkmcnt(0)\n\t" \
    "s_barrier \n\t" \
    "ds_read_b64_tr_b16 v[180:181], v244 offset:0\n\t" \
    "ds_read_b64_tr_b16 v[182:183], v244 offset:1536\n\t" \
    "ds_read_b64_tr_b16 v[188:189], v244 offset:32\n\t" \
    "ds_read_b64_tr_b16 v[190:191], v244 offset:1568\n\t" \
    "ds_read_b64_tr_b16 v[196:197], v244 offset:64\n\t" \
    "ds_read_b64_tr_b16 v[198:199], v244 offset:1600\n\t" \
    "s_waitcnt lgkmcnt(0)\n\t" \
    "s_barrier \n\t" \
    "ds_write_b128 v254, v[200:203] offset:51200\n\t" \
    "ds_write_b128 v254, v[204:207] offset:52224\n\t" \
    "ds_write_b128 v254, v[208:211] offset:53248\n\t" \
    "ds_write_b128 v254, v[212:215] offset:54272\n\t" \
    "ds_write_b128 v254, v[216:219] offset:55296\n\t" \
    "ds_write_b128 v254, v[220:223] offset:56320\n\t" \
    "ds_write_b128 v254, v[224:227] offset:57344\n\t" \
    "ds_write_b128 v254, v[228:231] offset:58368\n\t" \
    "ds_write_b128 v254, v[232:235] offset:59392\n\t" \
    "ds_write_b128 v254, v[236:239] offset:60416\n\t" \
    "s_waitcnt vmcnt(0) lgkmcnt(0)\n\t" \
    "s_barrier \n\t" \
    "ds_read_b128 v[224:227], v253 offset:0\n\t" \
    "ds_read_b128 v[244:247], v254 offset:53248\n\t" \
    "ds_read_b128 v[228:231], v253 offset:20480\n\t" \
    "ds_read_b128 v[232:235], v253 offset:1024\n\t" \
    "ds_read_b128 v[236:239], v253 offset:21504\n\t" \
    "ds_read_b128 v[248:251], v254 offset:54272\n\t" \
    ".p2align 8\n\t" \
    ".Ls23_%=:\n\t" \
    "s_waitcnt lgkmcnt(5)\n\t" \
    "v_mfma_f32_16x16x32_bf16 v[204:207], v[40:43], v[224:227], 0\n\t" \
    "ds_read_b128 v[240:243], v253 offset:2048\n\t" \
    "v_mfma_f32_16x16x32_bf16 v[208:211], v[48:51], v[224:227], 0\n\t" \
    "s_waitcnt lgkmcnt(5)\n\t" \
    "v_mfma_f32_16x16x32_bf16 v[200:203], v[244:247], v[224:227], 0\n\t" \
    "s_waitcnt lgkmcnt(4)\n\t" \
    "v_mfma_f32_16x16x32_bf16 v[216:219], v[40:43], v[228:231], 0\n\t" \
    "ds_read_b128 v[224:227], v253 offset:22528\n\t" \
    "v_mfma_f32_16x16x32_bf16 v[220:223], v[48:51], v[228:231], 0\n\t" \
    "v_mfma_f32_16x16x32_bf16 v[212:215], v[244:247], v[228:231], 0\n\t" \
    "ds_read_b128 v[244:247], v254 offset:55296\n\t" \
    "s_waitcnt lgkmcnt(5)\n\t" \
    "v_mfma_f32_16x16x32_bf16 v[204:207], v[44:47], v[232:235], v[204:207]\n\t" \
    "ds_read_b128 v[228:231], v253 offset:3072\n\t" \
    "v_mfma_f32_16x16x32_bf16 v[208:211], v[52:55], v[232:235], v[208:211]\n\t" \
    "s_waitcnt lgkmcnt(4)\n\t" \
    "v_mfma_f32_16x16x32_bf16 v[200:203], v[248:251], v[232:235], v[200:203]\n\t" \
    "v_mfma_f32_16x16x32_bf16 v[216:219], v[44:47], v[236:239], v[216:219]\n\t" \
    "ds_read_b128 v[232:235], v253 offset:23552\n\t" \
    "v_mfma_f32_16x16x32_bf16 v[220:223], v[52:55], v[236:239], v[220:223]\n\t" \
    "v_mfma_f32_16x16x32_bf16 v[212:215], v[248:251], v[236:239], v[212:215]\n\t" \
    "ds_read_b128 v[248:251], v254 offset:56320\n\t" \
    "s_waitcnt lgkmcnt(5)\n\t" \
    "v_mfma_f32_16x16x32_bf16 v[204:207], v[64:67], v[240:243], v[204:207]\n\t" \
    "ds_read_b128 v[236:239], v253 offset:4096\n\t" \
    "v_mfma_f32_16x16x32_bf16 v[208:211], v[72:75], v[240:243], v[208:211]\n\t" \
    "v_mfma_f32_16x16x32_bf16 v[200:203], v[56:59], v[240:243], v[200:203]\n\t" \
    "s_waitcnt lgkmcnt(5)\n\t" \
    "v_mfma_f32_16x16x32_bf16 v[216:219], v[64:67], v[224:227], v[216:219]\n\t" \
    "ds_read_b128 v[240:243], v253 offset:24576\n\t" \
    "v_mfma_f32_16x16x32_bf16 v[220:223], v[72:75], v[224:227], v[220:223]\n\t" \
    "v_mfma_f32_16x16x32_bf16 v[212:215], v[56:59], v[224:227], v[212:215]\n\t" \
    "s_waitcnt lgkmcnt(4)\n\t" \
    "v_mfma_f32_16x16x32_bf16 v[204:207], v[68:71], v[228:231], v[204:207]\n\t" \
    "ds_read_b128 v[224:227], v253 offset:5120\n\t" \
    "v_mfma_f32_16x16x32_bf16 v[208:211], v[76:79], v[228:231], v[208:211]\n\t" \
    "v_mfma_f32_16x16x32_bf16 v[200:203], v[60:63], v[228:231], v[200:203]\n\t" \
    "s_waitcnt lgkmcnt(4)\n\t" \
    "v_mfma_f32_16x16x32_bf16 v[216:219], v[68:71], v[232:235], v[216:219]\n\t" \
    "ds_read_b128 v[228:231], v253 offset:25600\n\t" \
    "v_mfma_f32_16x16x32_bf16 v[220:223], v[76:79], v[232:235], v[220:223]\n\t" \
    "v_mfma_f32_16x16x32_bf16 v[212:215], v[60:63], v[232:235], v[212:215]\n\t" \
    "s_waitcnt lgkmcnt(3)\n\t" \
    "v_mfma_f32_16x16x32_bf16 v[204:207], v[80:83], v[236:239], v[204:207]\n\t" \
    "ds_read_b128 v[232:235], v253 offset:6144\n\t" \
    "v_mfma_f32_16x16x32_bf16 v[208:211], v[88:91], v[236:239], v[208:211]\n\t" \
    "v_mfma_f32_16x16x32_bf16 v[200:203], v[244:247], v[236:239], v[200:203]\n\t" \
    "s_waitcnt lgkmcnt(3)\n\t" \
    "v_mfma_f32_16x16x32_bf16 v[216:219], v[80:83], v[240:243], v[216:219]\n\t" \
    "ds_read_b128 v[236:239], v253 offset:26624\n\t" \
    "v_mfma_f32_16x16x32_bf16 v[220:223], v[88:91], v[240:243], v[220:223]\n\t" \
    "v_mfma_f32_16x16x32_bf16 v[212:215], v[244:247], v[240:243], v[212:215]\n\t" \
    "ds_read_b128 v[244:247], v254 offset:51200\n\t" \
    "s_waitcnt lgkmcnt(4)\n\t" \
    "v_mfma_f32_16x16x32_bf16 v[204:207], v[84:87], v[224:227], v[204:207]\n\t" \
    "ds_read_b128 v[240:243], v253 offset:7168\n\t" \
    "v_mfma_f32_16x16x32_bf16 v[208:211], v[92:95], v[224:227], v[208:211]\n\t" \
    "v_mfma_f32_16x16x32_bf16 v[200:203], v[248:251], v[224:227], v[200:203]\n\t" \
    "s_waitcnt lgkmcnt(4)\n\t" \
    "v_mfma_f32_16x16x32_bf16 v[216:219], v[84:87], v[228:231], v[216:219]\n\t" \
    "ds_read_b128 v[224:227], v253 offset:27648\n\t" \
    "v_mfma_f32_16x16x32_bf16 v[220:223], v[92:95], v[228:231], v[220:223]\n\t" \
    "v_mfma_f32_16x16x32_bf16 v[212:215], v[248:251], v[228:231], v[212:215]\n\t" \
    "ds_read_b128 v[248:251], v254 offset:52224\n\t" \
    "s_waitcnt lgkmcnt(5)\n\t" \
    "v_mfma_f32_16x16x32_bf16 v[204:207], v[104:107], v[232:235], v[204:207]\n\t" \
    "ds_read_b128 v[228:231], v253 offset:8192\n\t" \
    "v_mfma_f32_16x16x32_bf16 v[208:211], v[112:115], v[232:235], v[208:211]\n\t" \
    "v_mfma_f32_16x16x32_bf16 v[200:203], v[96:99], v[232:235], v[200:203]\n\t" \
    "s_waitcnt lgkmcnt(5)\n\t" \
    "v_mfma_f32_16x16x32_bf16 v[216:219], v[104:107], v[236:239], v[216:219]\n\t" \
    "ds_read_b128 v[232:235], v253 offset:28672\n\t" \
    "v_mfma_f32_16x16x32_bf16 v[220:223], v[112:115], v[236:239], v[220:223]\n\t" \
    "v_mfma_f32_16x16x32_bf16 v[212:215], v[96:99], v[236:239], v[212:215]\n\t" \
    "s_waitcnt lgkmcnt(4)\n\t" \
    "v_mfma_f32_16x16x32_bf16 v[204:207], v[108:111], v[240:243], v[204:207]\n\t" \
    "ds_read_b128 v[236:239], v253 offset:9216\n\t" \
    "v_mfma_f32_16x16x32_bf16 v[208:211], v[116:119], v[240:243], v[208:211]\n\t" \
    "v_mfma_f32_16x16x32_bf16 v[200:203], v[100:103], v[240:243], v[200:203]\n\t" \
    "s_waitcnt lgkmcnt(4)\n\t" \
    "v_mfma_f32_16x16x32_bf16 v[216:219], v[108:111], v[224:227], v[216:219]\n\t" \
    "ds_read_b128 v[240:243], v253 offset:29696\n\t" \
    "v_mfma_f32_16x16x32_bf16 v[220:223], v[116:119], v[224:227], v[220:223]\n\t" \
    "v_mfma_f32_16x16x32_bf16 v[212:215], v[100:103], v[224:227], v[212:215]\n\t" \
    "s_waitcnt lgkmcnt(3)\n\t" \
    "v_mfma_f32_16x16x32_bf16 v[204:207], v[0:3], v[228:231], v[204:207]\n\t" \
    "ds_read_b128 v[224:227], v253 offset:10240\n\t" \
    "v_mfma_f32_16x16x32_bf16 v[208:211], v[8:11], v[228:231], v[208:211]\n\t" \
    "v_mfma_f32_16x16x32_bf16 v[200:203], v[244:247], v[228:231], v[200:203]\n\t" \
    "s_waitcnt lgkmcnt(3)\n\t" \
    "v_mfma_f32_16x16x32_bf16 v[216:219], v[0:3], v[232:235], v[216:219]\n\t" \
    "ds_read_b128 v[228:231], v253 offset:30720\n\t" \
    "v_mfma_f32_16x16x32_bf16 v[220:223], v[8:11], v[232:235], v[220:223]\n\t" \
    "v_mfma_f32_16x16x32_bf16 v[212:215], v[244:247], v[232:235], v[212:215]\n\t" \
    "ds_read_b128 v[244:247], v254 offset:57344\n\t" \
    "s_waitcnt lgkmcnt(4)\n\t" \
    "v_mfma_f32_16x16x32_bf16 v[204:207], v[4:7], v[236:239], v[204:207]\n\t" \
    "ds_read_b128 v[232:235], v253 offset:11264\n\t" \
    "v_mfma_f32_16x16x32_bf16 v[208:211], v[12:15], v[236:239], v[208:211]\n\t" \
    "v_mfma_f32_16x16x32_bf16 v[200:203], v[248:251], v[236:239], v[200:203]\n\t" \
    "s_waitcnt lgkmcnt(4)\n\t" \
    "v_mfma_f32_16x16x32_bf16 v[216:219], v[4:7], v[240:243], v[216:219]\n\t" \
    "ds_read_b128 v[236:239], v253 offset:31744\n\t" \
    "v_mfma_f32_16x16x32_bf16 v[220:223], v[12:15], v[240:243], v[220:223]\n\t" \
    "v_mfma_f32_16x16x32_bf16 v[212:215], v[248:251], v[240:243], v[212:215]\n\t" \
    "ds_read_b128 v[248:251], v254 offset:58368\n\t" \
    "s_setprio 2\n\t" \
    "s_waitcnt lgkmcnt(0)\n\t" \
    "s_barrier \n\t" \
    "v_mfma_f32_16x16x32_bf16 v[204:207], v[120:123], v[224:227], v[204:207]\n\t" \
    "ds_read_b128 v[240:243], v253 offset:12288\n\t" \
    "v_mfma_f32_16x16x32_bf16 v[208:211], v[128:131], v[224:227], v[208:211]\n\t" \
    "v_mfma_f32_16x16x32_bf16 v[200:203], v[244:247], v[224:227], v[200:203]\n\t" \
    "v_mfma_f32_16x16x32_bf16 v[216:219], v[120:123], v[228:231], v[216:219]\n\t" \
    "ds_read_b128 v[224:227], v253 offset:32768\n\t" \
    "s_and_b32 s44, s40, 3\n\t" \
    "v_mfma_f32_16x16x32_bf16 v[220:223], v[128:131], v[228:231], v[220:223]\n\t" \
    "s_lshl_b32 s44, s44, 22\n\t" \
    "v_mfma_f32_16x16x32_bf16 v[212:215], v[244:247], v[228:231], v[212:215]\n\t" \
    "ds_read_b128 v[244:247], v254 offset:59392\n\t" \
    "s_add_u32 s44, s44, %[mb]\n\t" \
    "v_mfma_f32_16x16x32_bf16 v[204:207], v[124:127], v[232:235], v[204:207]\n\t" \
    "ds_read_b128 v[228:231], v253 offset:13312\n\t" \
    "s_add_u32 s46, s44, 0x8000\n\t" \
    "v_mfma_f32_16x16x32_bf16 v[208:211], v[132:135], v[232:235], v[208:211]\n\t" \
    "s_add_u32 s47, s44, 0x8c00\n\t" \
    "v_mfma_f32_16x16x32_bf16 v[200:203], v[248:251], v[232:235], v[200:203]\n\t" \
    "v_mfma_f32_16x16x32_bf16 v[216:219], v[124:127], v[236:239], v[216:219]\n\t" \
    "ds_read_b128 v[232:235], v253 offset:33792\n\t" \
    "v_mfma_f32_16x16x32_bf16 v[220:223], v[132:135], v[236:239], v[220:223]\n\t" \
    "v_mfma_f32_16x16x32_bf16 v[212:215], v[248:251], v[236:239], v[212:215]\n\t" \
    "ds_read_b128 v[248:251], v254 offset:60416\n\t" \
    "s_waitcnt lgkmcnt(5)\n\t" \
    "v_mfma_f32_16x16x32_bf16 v[204:207], v[144:147], v[240:243], v[204:207]\n\t" \
    "ds_read_b128 v[236:239], v253 offset:16384\n\t" \
    "v_mfma_f32_16x16x32_bf16 v[208:211], v[152:155], v[240:243], v[208:211]\n\t" \
    "v_mfma_f32_16x16x32_bf16 v[200:203], v[136:139], v[240:243], v[200:203]\n\t" \
    "s_waitcnt lgkmcnt(5)\n\t" \
    "v_mfma_f32_16x16x32_bf16 v[216:219], v[144:147], v[224:227], v[216:219]\n\t" \
    "ds_read_b128 v[240:243], v253 offset:36864\n\t" \
    "v_mfma_f32_16x16x32_bf16 v[220:223], v[152:155], v[224:227], v[220:223]\n\t" \
    "v_mfma_f32_16x16x32_bf16 v[212:215], v[136:139], v[224:227], v[212:215]\n\t" \
    "s_waitcnt lgkmcnt(4)\n\t" \
    "v_mfma_f32_16x16x32_bf16 v[204:207], v[148:151], v[228:231], v[204:207]\n\t" \
    "ds_read_b128 v[224:227], v253 offset:17408\n\t" \
    "v_mfma_f32_16x16x32_bf16 v[208:211], v[156:159], v[228:231], v[208:211]\n\t" \
    "v_mfma_f32_16x16x32_bf16 v[200:203], v[140:143], v[228:231], v[200:203]\n\t" \
    "s_waitcnt lgkmcnt(4)\n\t" \
    "v_mfma_f32_16x16x32_bf16 v[216:219], v[148:151], v[232:235], v[216:219]\n\t" \
    "ds_read_b128 v[228:231], v253 offset:37888\n\t" \
    "v_mfma_f32_16x16x32_bf16 v[220:223], v[156:159], v[232:235], v[220:223]\n\t" \
    "v_mfma_f32_16x16x32_bf16 v[212:215], v[140:143], v[232:235], v[212:215]\n\t" \
    "s_waitcnt lgkmcnt(3)\n\t" \
    "v_mfma_f32_16x16x32_bf16 v[204:207], v[160:163], v[236:239], v[204:207]\n\t" \
    "ds_read_b128 v[232:235], v253 offset:14336\n\t" \
    "v_mfma_f32_16x16x32_bf16 v[208:211], v[168:171], v[236:239], v[208:211]\n\t" \
    "v_mfma_f32_16x16x32_bf16 v[200:203], v[244:247], v[236:239], v[200:203]\n\t" \
    "s_waitcnt lgkmcnt(3)\n\t" \
    "v_mfma_f32_16x16x32_bf16 v[216:219], v[160:163], v[240:243], v[216:219]\n\t" \
    "ds_read_b128 v[236:239], v253 offset:15360\n\t" \
    "v_mfma_f32_16x16x32_bf16 v[220:223], v[168:171], v[240:243], v[220:223]\n\t" \
    "v_mfma_f32_16x16x32_bf16 v[212:215], v[244:247], v[240:243], v[212:215]\n\t" \
    "s_waitcnt lgkmcnt(3)\n\t" \
    "v_mfma_f32_16x16x32_bf16 v[204:207], v[164:167], v[224:227], v[204:207]\n\t" \
    "ds_read_b128 v[240:243], v253 offset:18432\n\t" \
    "v_mfma_f32_16x16x32_bf16 v[208:211], v[172:175], v[224:227], v[208:211]\n\t" \
    "v_mfma_f32_16x16x32_bf16 v[200:203], v[248:251], v[224:227], v[200:203]\n\t" \
    "s_waitcnt lgkmcnt(3)\n\t" \
    "v_mfma_f32_16x16x32_bf16 v[216:219], v[164:167], v[228:231], v[216:219]\n\t" \
    "ds_read_b128 v[224:227], v253 offset:19456\n\t" \
    "v_mfma_f32_16x16x32_bf16 v[220:223], v[172:175], v[228:231], v[220:223]\n\t" \
    "v_mfma_f32_16x16x32_bf16 v[212:215], v[248:251], v[228:231], v[212:215]\n\t" \
    "s_waitcnt lgkmcnt(3)\n\t" \
    "v_mfma_f32_16x16x32_bf16 v[204:207], v[24:27], v[232:235], v[204:207]\n\t" \
    "ds_read_b128 v[228:231], v253 offset:34816\n\t" \
    "v_mov_b64 v[244:245], 0\n\t" \
    "v_mfma_f32_16x16x32_bf16 v[208:211], v[32:35], v[232:235], v[208:211]\n\t" \
    "v_mov_b64 v[246:247], 0\n\t" \
    "v_mfma_f32_16x16x32_bf16 v[200:203], v[16:19], v[232:235], v[200:203]\n\t" \
    "v_mov_b64 v[248:249], 0\n\t" \
    "s_waitcnt lgkmcnt(3)\n\t" \
    "v_mfma_f32_16x16x32_bf16 v[204:207], v[28:31], v[236:239], v[204:207]\n\t" \
    "ds_read_b128 v[232:235], v253 offset:35840\n\t" \
    "v_mov_b64 v[250:251], 0\n\t" \
    "v_mfma_f32_16x16x32_bf16 v[208:211], v[36:39], v[236:239], v[208:211]\n\t" \
    "v_mfma_f32_16x16x32_bf16 v[200:203], v[20:23], v[236:239], v[200:203]\n\t" \
    "s_waitcnt lgkmcnt(3)\n\t" \
    "v_mfma_f32_16x16x32_bf16 v[204:207], v[184:187], v[240:243], v[204:207]\n\t" \
    "ds_read_b128 v[236:239], v253 offset:38912\n\t" \
    "v_mfma_f32_16x16x32_bf16 v[208:211], v[192:195], v[240:243], v[208:211]\n\t" \
    "v_mfma_f32_16x16x32_bf16 v[200:203], v[176:179], v[240:243], v[200:203]\n\t" \
    "s_waitcnt lgkmcnt(3)\n\t" \
    "v_mfma_f32_16x16x32_bf16 v[204:207], v[188:191], v[224:227], v[204:207]\n\t" \
    "ds_read_b128 v[240:243], v253 offset:39936\n\t" \
    "v_mfma_f32_16x16x32_bf16 v[208:211], v[196:199], v[224:227], v[208:211]\n\t" \
    "v_mfma_f32_16x16x32_bf16 v[200:203], v[180:183], v[224:227], v[200:203]\n\t" \
    "s_waitcnt lgkmcnt(3)\n\t" \
    "v_mfma_f32_16x16x32_bf16 v[216:219], v[24:27], v[228:231], v[216:219]\n\t" \
    "s_waitcnt lgkmcnt(2)\n\t" \
    "v_mfma_f32_16x16x32_bf16 v[216:219], v[28:31], v[232:235], v[216:219]\n\t" \
    "v_cvt_pk_bf16_f32 v204, v204, v208\n\t" \
    "v_cvt_pk_bf16_f32 v205, v205, v209\n\t" \
    "s_waitcnt lgkmcnt(1)\n\t" \
    "v_mfma_f32_16x16x32_bf16 v[216:219], v[184:187], v[236:239], v[216:219]\n\t" \
    "v_cvt_pk_bf16_f32 v206, v206, v210\n\t" \
    "v_cvt_pk_bf16_f32 v207, v207, v211\n\t" \
    "s_waitcnt lgkmcnt(0)\n\t" \
    "v_mfma_f32_16x16x32_bf16 v[216:219], v[188:191], v[240:243], v[216:219]\n\t" \
    "ds_write_b128 v254, v[204:207] offset:10240\n\t" \
    "v_mfma_f32_16x16x32_bf16 v[220:223], v[32:35], v[228:231], v[220:223]\n\t" \
    "v_mfma_f32_16x16x32_bf16 v[220:223], v[36:39], v[232:235], v[220:223]\n\t" \
    "v_cvt_pk_bf16_f32 v200, v200, v201\n\t" \
    "v_mfma_f32_16x16x32_bf16 v[220:223], v[192:195], v[236:239], v[220:223]\n\t" \
    "v_cvt_pk_bf16_f32 v201, v202, v203\n\t" \
    "ds_write_b64 v254, v[200:201] offset:6152\n\t" \
    "v_mfma_f32_16x16x32_bf16 v[220:223], v[196:199], v[240:243], v[220:223]\n\t" \
    "v_mfma_f32_16x16x32_bf16 v[212:215], v[16:19], v[228:231], v[212:215]\n\t" \
    "v_mfma_f32_16x16x32_bf16 v[212:215], v[20:23], v[232:235], v[212:215]\n\t" \
    "ds_read_b128 v[200:203], v254 offset:51200\n\t" \
    "ds_read_b128 v[204:207], v254 offset:52224\n\t" \
    "v_mfma_f32_16x16x32_bf16 v[212:215], v[176:179], v[236:239], v[212:215]\n\t" \
    "v_mov_b64 v[208:209], 0\n\t" \
    "v_mov_b64 v[210:211], 0\n\t" \
    "v_mfma_f32_16x16x32_bf16 v[212:215], v[180:183], v[240:243], v[212:215]\n\t" \
    "v_cvt_pk_bf16_f32 v216, v216, v220\n\t" \
    "v_cvt_pk_bf16_f32 v217, v217, v221\n\t" \
    "s_setprio 0\n\t" \
    "v_cvt_pk_bf16_f32 v218, v218, v222\n\t" \
    "v_cvt_pk_bf16_f32 v219, v219, v223\n\t" \
    "ds_write_b128 v254, v[216:219] offset:16384\n\t" \
    "ds_read_b128 v[216:219], v254 offset:53248\n\t" \
    "ds_read_b128 v[220:223], v254 offset:54272\n\t" \
    "s_nop 0\n\t" \
    "v_cvt_pk_bf16_f32 v212, v212, v213\n\t" \
    "v_cvt_pk_bf16_f32 v213, v214, v215\n\t" \
    "ds_write_b64 v254, v[212:213] offset:12296\n\t" \
    "s_sleep 3\n\t" \
    "s_mov_b32 m0, 0xb000\n\t" \
    "s_nop 0\n\t" \
    "buffer_load_dwordx4 v253, s[60:63], s46 offen sc1 lds\n\t" \
    "s_mov_b32 m0, 0xbc00\n\t" \
    "s_nop 0\n\t" \
    "buffer_load_dwordx4 v253, s[60:63], s47 offen sc1 lds\n\t" \
    "s_waitcnt lgkmcnt(0)\n\t" \
    "v_bfe_i32 v212, v253, 5, 1\n\t" \
    "v_and_b32 v212, 0xaaaa, v212\n\t" \
    "v_or_b32 v212, 0x4444, v212\n\t" \
    "v_and_b32 v213, 0xffff, v255\n\t" \
    "v_lshrrev_b32 v214, 16, v255\n\t" \
    ".Ls24_%=:\n\t" \
    "s_waitcnt vmcnt(0)\n\t" \
    "ds_read_b128 v[224:227], v253 offset:45056\n\t" \
    "ds_read_b128 v[228:231], v253 offset:48128\n\t" \
    "s_waitcnt lgkmcnt(1)\n\t" \
    "v_max_u32 v215, v224, v225\n\t" \
    "v_max3_u32 v215, v226, v227, v215\n\t" \
    "s_waitcnt lgkmcnt(0)\n\t" \
    "v_max3_u32 v215, v228, v229, v215\n\t" \
    "v_max3_u32 v215, v230, v231, v215\n\t" \
    "v_cmp_eq_u32 vcc, -1, v215\n\t" \
    "s_cbranch_vccz .Ls25_%=\n\t" \
    "v_max_u32 v215, v224, v225\n\t" \
    "v_max3_u32 v215, v226, v227, v215\n\t" \
    "v_cmp_eq_u32 vcc, -1, v215\n\t" \
    "s_cbranch_vccz .Ls26_%=\n\t" \
    "s_mov_b32 m0, 0xb000\n\t" \
    "s_nop 0\n\t" \
    "buffer_load_dwordx4 v253, s[60:63], s46 offen sc1 lds\n\t" \
    ".Ls26_%=:\n\t" \
    "v_max_u32 v215, v228, v229\n\t" \
    "v_max3_u32 v215, v230, v231, v215\n\t" \
    "v_cmp_eq_u32 vcc, -1, v215\n\t" \
    "s_cbranch_vccz .Ls27_%=\n\t" \
    "s_mov_b32 m0, 0xbc00\n\t" \
    "s_nop 0\n\t" \
    "buffer_load_dwordx4 v253, s[60:63], s47 offen sc1 lds\n\t" \
    ".Ls27_%=:\n\t" \
    "s_cmp_eq_u32 s41, 0\n\t" \
    "s_cbranch_scc1 .Ls25_%=\n\t" \
    "s_sub_u32 s41, s41, 1\n\t" \
    "s_branch .Ls24_%=\n\t" \
    ".Ls25_%=:\n\t" \
    "s_setprio 3\n\t" \
    "s_waitcnt lgkmcnt(0)\n\t" \
    "s_barrier \n\t" \
    "ds_read_b128 v[232:235], v253 offset:40960\n\t" \
    "ds_read_b128 v[236:239], v253 offset:41984\n\t" \
    "ds_read_b128 v[240:243], v253 offset:43008\n\t" \
    "v_smfmac_f32_16x16x64_bf16 v[244:247], v[224:227], v[0:7], v212\n\t" \
    "v_smfmac_f32_16x16x64_bf16 v[248:251], v[224:227], v[8:15], v212\n\t" \
    "v_smfmac_f32_16x16x64_bf16 v[208:211], v[224:227], v[200:207], v212\n\t" \
    "ds_read_b128 v[200:203], v254 offset:55296\n\t" \
    "ds_read_b128 v[204:207], v254 offset:56320\n\t" \
    "ds_read_b128 v[224:227], v253 offset:44032\n\t" \
    "v_smfmac_f32_16x16x64_bf16 v[208:211], v[228:231], v[16:23], v212\n\t" \
    "v_smfmac_f32_16x16x64_bf16 v[244:247], v[228:231], v[24:31], v212\n\t" \
    "v_smfmac_f32_16x16x64_bf16 v[248:251], v[228:231], v[32:39], v212\n\t" \
    "ds_read_b128 v[228:231], v253 offset:46080\n\t" \
    "s_waitcnt lgkmcnt(6)\n\t" \
    "v_smfmac_f32_16x16x64_bf16 v[208:211], v[232:235], v[216:223], v212\n\t" \
    "ds_read_b128 v[216:219], v254 offset:57344\n\t" \
    "ds_read_b128 v[220:223], v254 offset:58368\n\t" \
    "v_smfmac_f32_16x16x64_bf16 v[244:247], v[232:235], v[40:47], v212\n\t" \
    "v_smfmac_f32_16x16x64_bf16 v[248:251], v[232:235], v[48:55], v212\n\t" \
    "ds_read_b128 v[232:235], v253 offset:47104\n\t" \
    "s_waitcnt lgkmcnt(8)\n\t" \
    "v_smfmac_f32_16x16x64_bf16 v[208:211], v[236:239], v[56:63], v212\n\t" \
    "v_smfmac_f32_16x16x64_bf16 v[244:247], v[236:239], v[64:71], v212\n\t" \
    "v_smfmac_f32_16x16x64_bf16 v[248:251], v[236:239], v[72:79], v212\n\t" \
    "ds_read_b128 v[236:239], v253 offset:49152\n\t" \
    "s_waitcnt lgkmcnt(6)\n\t" \
    "v_smfmac_f32_16x16x64_bf16 v[208:211], v[240:243], v[200:207], v212\n\t" \
    "ds_read_b128 v[200:203], v254 offset:59392\n\t" \
    "ds_read_b128 v[204:207], v254 offset:60416\n\t" \
    "v_smfmac_f32_16x16x64_bf16 v[244:247], v[240:243], v[80:87], v212\n\t" \
    "v_smfmac_f32_16x16x64_bf16 v[248:251], v[240:243], v[88:95], v212\n\t" \
    "ds_read_b128 v[240:243], v253 offset:50176\n\t" \
    "s_waitcnt lgkmcnt(8)\n\t" \
    "v_smfmac_f32_16x16x64_bf16 v[208:211], v[224:227], v[96:103], v212\n\t" \
    "v_smfmac_f32_16x16x64_bf16 v[244:247], v[224:227], v[104:111], v212\n\t" \
    "v_smfmac_f32_16x16x64_bf16 v[248:251], v[224:227], v[112:119], v212\n\t" \
    "s_waitcnt lgkmcnt(5)\n\t" \
    "v_smfmac_f32_16x16x64_bf16 v[208:211], v[228:231], v[216:223], v212\n\t" \
    "s_waitcnt lgkmcnt(4)\n\t" \
    "v_smfmac_f32_16x16x64_bf16 v[208:211], v[232:235], v[136:143], v212\n\t" \
    "s_waitcnt lgkmcnt(1)\n\t" \
    "v_smfmac_f32_16x16x64_bf16 v[208:211], v[236:239], v[200:207], v212\n\t" \
    "s_waitcnt lgkmcnt(0)\n\t" \
    "v_smfmac_f32_16x16x64_bf16 v[208:211], v[240:243], v[176:183], v212\n\t" \
    "v_smfmac_f32_16x16x64_bf16 v[244:247], v[228:231], v[120:127], v212\n\t" \
    "v_smfmac_f32_16x16x64_bf16 v[244:247], v[232:235], v[144:151], v212\n\t" \
    "v_smfmac_f32_16x16x64_bf16 v[244:247], v[236:239], v[160:167], v212\n\t" \
    "v_add_f32 v208, v208, v210\n\t" \
    "v_add_f32 v209, v209, v211\n\t" \
    "v_cvt_pk_bf16_f32 v208, v208, v209\n\t" \
    "v_smfmac_f32_16x16x64_bf16 v[244:247], v[240:243], v[184:191], v212\n\t" \
    "ds_write_b16 v213, v208 offset:0\n\t" \
    "ds_write_b16_d16_hi v213, v208 offset:16\n\t" \
    "v_smfmac_f32_16x16x64_bf16 v[248:251], v[228:231], v[128:135], v212\n\t" \
    "v_smfmac_f32_16x16x64_bf16 v[248:251], v[232:235], v[152:159], v212\n\t" \
    "v_smfmac_f32_16x16x64_bf16 v[248:251], v[236:239], v[168:175], v212\n\t" \
    "v_add_f32 v244, v244, v246\n\t" \
    "v_add_f32 v245, v245, v247\n\t" \
    "v_smfmac_f32_16x16x64_bf16 v[248:251], v[240:243], v[192:199], v212\n\t" \
    "s_nop 7\n\t" \
    "v_add_f32 v248, v248, v250\n\t" \
    "v_add_f32 v249, v249, v251\n\t" \
    "v_cvt_pk_bf16_f32 v244, v244, v248\n\t" \
    "v_cvt_pk_bf16_f32 v245, v245, v249\n\t" \
    "ds_write2_b32 v214, v244, v245 offset1:4\n\t" \
    "s_setprio 0\n\t" \
    "s_sleep 1\n\t" \
    "ds_read_b128 v[200:203], v254 offset:12288\n\t" \
    "ds_read_b128 v[224:227], v254 offset:24576\n\t" \
    "ds_read_b128 v[204:207], v254 offset:13312\n\t" \
    "ds_read_b128 v[228:231], v254 offset:25600\n\t" \
    "ds_read_b128 v[208:211], v254 offset:14336\n\t" \
    "ds_read_b128 v[232:235], v254 offset:26624\n\t" \
    "ds_read_b128 v[212:215], v254 offset:15360\n\t" \
    "ds_read_b128 v[236:239], v254 offset:27648\n\t" \
    "ds_read_b128 v[216:219], v254 offset:16384\n\t" \
    "ds_read_b128 v[220:223], v254 offset:17408\n\t" \
    "s_and_b32 s44, s40, 3\n\t" \
    "s_lshl_b32 s44, s44, 17\n\t" \
    "s_add_u32 s50, s44, %[pb]\n\t" \
    "s_add_u32 s51, s44, %[pm]\n\t" \
    "s_mov_b32 s52, 0xf0000\n\t" \
    "s_mov_b32 s53, 0xfff00ff\n\t" \
    "s_mov_b32 s54, 0x1f0001\n\t" \
    "s_mov_b32 s55, 0x1fff01ff\n\t" \
    "s_mov_b32 s56, 0x3f0003\n\t" \
    "s_mov_b32 s57, 0x3fff03ff\n\t" \
    "s_mov_b32 s58, 0x7f0007\n\t" \
    "s_mov_b32 s59, 0x7fff07ff\n\t" \
    "s_waitcnt lgkmcnt(8)\n\t" \
    "v_mfma_f32_16x16x32_bf16 v[240:243], v[224:227], v[200:203], 0\n\t" \
    "ds_read_b128 v[224:227], v254 offset:28672\n\t" \
    "s_waitcnt lgkmcnt(7)\n\t" \
    "v_mfma_f32_16x16x32_bf16 v[240:243], v[228:231], v[204:207], v[240:243]\n\t" \
    "ds_read_b128 v[228:231], v254 offset:29696\n\t" \
    "s_waitcnt lgkmcnt(6)\n\t" \
    "v_mfma_f32_16x16x32_bf16 v[240:243], v[232:235], v[208:211], v[240:243]\n\t" \
    "ds_read_b128 v[232:235], v254 offset:18432\n\t" \
    "s_waitcnt lgkmcnt(5)\n\t" \
    "v_mfma_f32_16x16x32_bf16 v[240:243], v[236:239], v[212:215], v[240:243]\n\t" \
    "ds_read_b128 v[236:239], v254 offset:19456\n\t" \
    "s_waitcnt lgkmcnt(3)\n\t" \
    "v_mfma_f32_16x16x32_bf16 v[240:243], v[224:227], v[216:219], v[240:243]\n\t" \
    "ds_read_b128 v[224:227], v254 offset:20480\n\t" \
    "s_waitcnt lgkmcnt(3)\n\t" \
    "v_mfma_f32_16x16x32_bf16 v[240:243], v[228:231], v[220:223], v[240:243]\n\t" \
    "ds_read_b128 v[228:231], v254 offset:21504\n\t" \
    "s_waitcnt lgkmcnt(3)\n\t" \
    "v_mfma_f32_16x16x32_bf16 v[244:247], v[232:235], v[200:203], 0\n\t" \
    "ds_read_b128 v[232:235], v254 offset:22528\n\t" \
    "s_waitcnt lgkmcnt(3)\n\t" \
    "v_mfma_f32_16x16x32_bf16 v[244:247], v[236:239], v[204:207], v[244:247]\n\t" \
    "ds_read_b128 v[236:239], v254 offset:23552\n\t" \
    "v_and_b32 v252, 0xffffff7f, v254\n\t" \
    "s_waitcnt lgkmcnt(0)\n\t" \
    "s_barrier \n\t" \
    "v_mfma_f32_16x16x32_bf16 v[244:247], v[224:227], v[208:211], v[244:247]\n\t" \
    "ds_read_b128 v[224:227], v252 offset:3072\n\t" \
    "v_mfma_f32_16x16x32_bf16 v[244:247], v[228:231], v[212:215], v[244:247]\n\t" \
    "ds_read_b128 v[228:231], v252 offset:3200\n\t" \
    "v_mfma_f32_16x16x32_bf16 v[244:247], v[232:235], v[216:219], v[244:247]\n\t" \
    "ds_read_b128 v[232:235], v252 offset:4096\n\t" \
    "v_mfma_f32_16x16x32_bf16 v[244:247], v[236:239], v[220:223], v[244:247]\n\t" \
    "ds_read_b128 v[236:239], v252 offset:4224\n\t" \
    "s_waitcnt lgkmcnt(3)\n\t" \
    "v_mfma_f32_16x16x32_bf16 v[248:251], v[224:227], v[200:203], 0\n\t" \
    "ds_read_b128 v[224:227], v252 offset:5120\n\t" \
    "s_waitcnt lgkmcnt(3)\n\t" \
    "v_mfma_f32_16x16x32_bf16 v[248:251], v[228:231], v[204:207], v[248:251]\n\t" \
    "ds_read_b128 v[228:231], v252 offset:5248\n\t" \
    "s_waitcnt lgkmcnt(3)\n\t" \
    "v_mfma_f32_16x16x32_bf16 v[248:251], v[232:235], v[208:211], v[248:251]\n\t" \
    "s_waitcnt lgkmcnt(2)\n\t" \
    "v_mfma_f32_16x16x32_bf16 v[248:251], v[236:239], v[212:215], v[248:251]\n\t" \
    "s_waitcnt lgkmcnt(1)\n\t" \
    "v_mfma_f32_16x16x32_bf16 v[248:251], v[224:227], v[216:219], v[248:251]\n\t" \
    "s_waitcnt lgkmcnt(0)\n\t" \
    "v_mfma_f32_16x16x32_bf16 v[248:251], v[228:231], v[220:223], v[248:251]\n\t" \
    "v_mov_b32 v226, 0xff7fffff\n\t" \
    "v_cndmask_b32 v240, v240, v226, s[52:53]\n\t" \
    "v_cndmask_b32 v241, v241, v226, s[54:55]\n\t" \
    "v_cndmask_b32 v242, v242, v226, s[56:57]\n\t" \
    "v_cndmask_b32 v243, v243, v226, s[58:59]\n\t" \
    "v_max3_f32 v224, v240, v241, v242\n\t" \
    "v_max3_f32 v225, v244, v245, v246\n\t" \
    "v_max3_f32 v224, v224, v243, v247\n\t" \
    "v_lshrrev_b32 v200, 5, v253\n\t" \
    "v_and_b32 v201, 0xf0, v253\n\t" \
    "v_and_or_b32 v200, v200, 8, v201\n\t" \
    "v_max3_f32 v224, v224, v248, v249\n\t" \
    "v_max3_f32 v225, v225, v250, v251\n\t" \
    "v_max_f32 v224, v224, v225\n\t" \
    "v_mov_b32 v225, v224\n\t" \
    "s_nop 1\n\t" \
    "v_permlane16_swap_b32 v224, v225\n\t" \
    "v_max_f32 v224, v224, v225\n\t" \
    "v_mov_b32 v225, v224\n\t" \
    "s_nop 1\n\t" \
    "v_permlane32_swap_b32 v224, v225\n\t" \
    "v_max_f32 v224, v224, v225\n\t" \
    "v_mul_f32 v224, %[scl], v224\n\t" \
    "v_fma_f32 v240, v240, %[scl], -v224\n\t" \
    "v_fma_f32 v241, v241, %[scl], -v224\n\t" \
    "v_exp_f32 v240, v240\n\t" \
    "v_fma_f32 v242, v242, %[scl], -v224\n\t" \
    "v_exp_f32 v241, v241\n\t" \
    "v_fma_f32 v243, v243, %[scl], -v224\n\t" \
    "v_exp_f32 v242, v242\n\t" \
    "v_fma_f32 v244, v244, %[scl], -v224\n\t" \
    "v_exp_f32 v243, v243\n\t" \
    "v_fma_f32 v245, v245, %[scl], -v224\n\t" \
    "v_exp_f32 v244, v244\n\t" \
    "v_fma_f32 v246, v246, %[scl], -v224\n\t" \
    "v_exp_f32 v245, v245\n\t" \
    "v_fma_f32 v247, v247, %[scl], -v224\n\t" \
    "v_exp_f32 v246, v246\n\t" \
    "v_fma_f32 v248, v248, %[scl], -v224\n\t" \
    "v_exp_f32 v247, v247\n\t" \
    "v_fma_f32 v249, v249, %[scl], -v224\n\t" \
    "v_exp_f32 v248, v248\n\t" \
    "v_fma_f32 v250, v250, %[scl], -v224\n\t" \
    "v_exp_f32 v249, v249\n\t" \
    "v_fma_f32 v251, v251, %[scl], -v224\n\t" \
    "v_exp_f32 v250, v250\n\t" \
    "v_exp_f32 v251, v251\n\t" \
    "v_add_f32 v224, v240, v241\n\t" \
    "v_add_f32 v225, v242, v243\n\t" \
    "v_add_f32 v226, v244, v245\n\t" \
    "v_add_f32 v227, v246, v247\n\t" \
    "v_add_f32 v232, v248, v249\n\t" \
    "v_add_f32 v233, v250, v251\n\t" \
    "v_add_f32 v224, v224, v225\n\t" \
    "v_add_f32 v226, v226, v227\n\t" \
    "v_add_f32 v232, v232, v233\n\t" \
    "v_add_f32 v224, v224, v226\n\t" \
    "v_fma_f32 v224, v232, 0.5, v224\n\t" \
    "v_mov_b32 v225, v224\n\t" \
    "s_nop 1\n\t" \
    "v_permlane16_swap_b32 v224, v225\n\t" \
    "v_add_f32 v224, v224, v225\n\t" \
    "v_mov_b32 v225, v224\n\t" \
    "s_nop 1\n\t" \
    "v_permlane32_swap_b32 v224, v225\n\t" \
    "v_add_f32 v224, v224, v225\n\t" \
    "v_rcp_f32 v224, v224\n\t" \
    "s_nop 0\n\t" \
    "v_mul_f32 v240, v240, v224\n\t" \
    "v_mul_f32 v241, v241, v224\n\t" \
    "v_mul_f32 v242, v242, v224\n\t" \
    "v_mul_f32 v243, v243, v224\n\t" \
    "v_mul_f32 v244, v244, v224\n\t" \
    "v_mul_f32 v245, v245, v224\n\t" \
    "v_mul_f32 v246, v246, v224\n\t" \
    "v_mul_f32 v247, v247, v224\n\t" \
    "v_mul_f32 v248, v248, v224\n\t" \
    "v_mul_f32 v249, v249, v224\n\t" \
    "v_mul_f32 v250, v250, v224\n\t" \
    "v_mul_f32 v251, v251, v224\n\t" \
    "v_cvt_pk_bf16_f32 v204, |v244|, |v240|\n\t" \
    "v_cvt_pk_bf16_f32 v205, |v245|, |v241|\n\t" \
    "v_cvt_pk_bf16_f32 v206, |v246|, |v242|\n\t" \
    "v_cvt_pk_bf16_f32 v207, |v247|, |v243|\n\t" \
    "v_cvt_pk_bf16_f32 v208, |v248|, |v249|\n\t" \
    "v_cvt_pk_bf16_f32 v209, |v250|, |v251|\n\t" \
    "buffer_store_dwordx4 v[204:207], v253, s[60:63], s50 offen sc1\n\t" \
    "s_mov_b32 exec_lo, -1\n\t" \
    "s_mov_b32 exec_hi, 0\n\t" \
    "buffer_store_dwordx2 v[208:209], v200, s[60:63], s51 offen sc1\n\t" \
    "s_mov_b32 exec_hi, -1\n\t" \
    "ds_read_b128 v[224:227], v253 offset:0\n\t" \
    "ds_read_b128 v[244:247], v254 offset:53248\n\t" \
    "ds_read_b128 v[228:231], v253 offset:20480\n\t" \
    "ds_read_b128 v[232:235], v253 offset:1024\n\t" \
    "ds_read_b128 v[236:239], v253 offset:21504\n\t" \
    "ds_read_b128 v[248:251], v254 offset:54272\n\t" \
    "s_add_u32 s40, s40, 1\n\t" \
    "s_cmp_lg_u32 s40, 64\n\t" \
    "s_cbranch_scc1 .Ls23_%=\n\t" \
    "s_branch .Lend%=\n\t" \
    ".Ls1_%=:\n\t" \
    "v_add_u32 v244, 0x18000, v243\n\t" \
    "s_waitcnt vmcnt(12) lgkmcnt(0)\n\t" \
    "s_barrier \n\t" \
    "s_add_u32 m0, %[wm], 0x12000\n\t" \
    "s_mov_b32 s44, 0x1e0000\n\t" \
    "buffer_load_dwordx4 v240, %[wr], s44 offen offset:0 nt lds\n\t" \
    "buffer_load_dwordx4 v241, %[wr], s44 offen offset:1024 nt lds\n\t" \
    "buffer_load_dwordx4 v242, %[wr], s44 offen offset:2048 nt lds\n\t" \
    "ds_read_b64_tr_b16 v[56:57], v244 offset:0\n\t" \
    "ds_read_b64_tr_b16 v[58:59], v244 offset:1536\n\t" \
    "ds_read_b64_tr_b16 v[64:65], v244 offset:32\n\t" \
    "ds_read_b64_tr_b16 v[66:67], v244 offset:1568\n\t" \
    "ds_read_b64_tr_b16 v[72:73], v244 offset:64\n\t" \
    "ds_read_b64_tr_b16 v[74:75], v244 offset:1600\n\t" \
    "v_add_u32 v244, 0x1e000, v243\n\t" \
    "s_waitcnt vmcnt(12) lgkmcnt(0)\n\t" \
    "s_barrier \n\t" \
    "s_add_u32 m0, %[wm], 0x18000\n\t" \
    "s_mov_b32 s44, 0x240000\n\t" \
    "buffer_load_dwordx4 v240, %[wr], s44 offen offset:0 nt lds\n\t" \
    "buffer_load_dwordx4 v241, %[wr], s44 offen offset:1024 nt lds\n\t" \
    "buffer_load_dwordx4 v242, %[wr], s44 offen offset:2048 nt lds\n\t" \
    "ds_read_b64_tr_b16 v[60:61], v244 offset:0\n\t" \
    "ds_read_b64_tr_b16 v[62:63], v244 offset:1536\n\t" \
    "ds_read_b64_tr_b16 v[68:69], v244 offset:32\n\t" \
    "ds_read_b64_tr_b16 v[70:71], v244 offset:1568\n\t" \
    "ds_read_b64_tr_b16 v[76:77], v244 offset:64\n\t" \
    "ds_read_b64_tr_b16 v[78:79], v244 offset:1600\n\t" \
    "v_add_u32 v244, 0x0, v243\n\t" \
    "s_waitcnt vmcnt(12) lgkmcnt(0)\n\t" \
    "s_barrier \n\t" \
    "s_add_u32 m0, %[wm], 0x1e000\n\t" \
    "s_mov_b32 s44, 0x2a0000\n\t" \
    "buffer_load_dwordx4 v240, %[wr], s44 offen offset:0 nt lds\n\t" \
    "buffer_load_dwordx4 v241, %[wr], s44 offen offset:1024 nt lds\n\t" \
    "buffer_load_dwordx4 v242, %[wr], s44 offen offset:2048 nt lds\n\t" \
    "ds_read_b64_tr_b16 v[96:97], v244 offset:0\n\t" \
    "ds_read_b64_tr_b16 v[98:99], v244 offset:1536\n\t" \
    "ds_read_b64_tr_b16 v[104:105], v244 offset:32\n\t" \
    "ds_read_b64_tr_b16 v[106:107], v244 offset:1568\n\t" \
    "ds_read_b64_tr_b16 v[112:113], v244 offset:64\n\t" \
    "ds_read_b64_tr_b16 v[114:115], v244 offset:1600\n\t" \
    "v_add_u32 v244, 0x6000, v243\n\t" \
    "s_waitcnt vmcnt(12) lgkmcnt(0)\n\t" \
    "s_barrier \n\t" \
    "s_add_u32 m0, %[wm], 0x0\n\t" \
    "s_mov_b32 s44, 0x300000\n\t" \
    "buffer_load_dwordx4 v240, %[wr], s44 offen offset:0 nt lds\n\t" \
    "buffer_load_dwordx4 v241, %[wr], s44 offen offset:1024 nt lds\n\t" \
    "buffer_load_dwordx4 v242, %[wr], s44 offen offset:2048 nt lds\n\t" \
    "ds_read_b64_tr_b16 v[100:101], v244 offset:0\n\t" \
    "ds_read_b64_tr_b16 v[102:103], v244 offset:1536\n\t" \
    "ds_read_b64_tr_b16 v[108:109], v244 offset:32\n\t" \
    "ds_read_b64_tr_b16 v[110:111], v244 offset:1568\n\t" \
    "ds_read_b64_tr_b16 v[116:117], v244 offset:64\n\t" \
    "ds_read_b64_tr_b16 v[118:119], v244 offset:1600\n\t" \
    "v_add_u32 v244, 0xc000, v243\n\t" \
    "s_waitcnt vmcnt(12) lgkmcnt(0)\n\t" \
    "s_barrier \n\t" \
    "s_add_u32 m0, %[wm], 0x6000\n\t" \
    "s_mov_b32 s44, 0x360000\n\t" \
    "buffer_load_dwordx4 v240, %[wr], s44 offen offset:0 nt lds\n\t" \
    "buffer_load_dwordx4 v241, %[wr], s44 offen offset:1024 nt lds\n\t" \
    "buffer_load_dwordx4 v242, %[wr], s44 offen offset:2048 nt lds\n\t" \
    "ds_read_b64_tr_b16 v[200:201], v244 offset:0\n\t" \
    "ds_read_b64_tr_b16 v[202:203], v244 offset:1536\n\t" \
    "ds_read_b64_tr_b16 v[0:1], v244 offset:32\n\t" \
    "ds_read_b64_tr_b16 v[2:3], v244 offset:1568\n\t" \
    "ds_read_b64_tr_b16 v[8:9], v244 offset:64\n\t" \
    "ds_read_b64_tr_b16 v[10:11], v244 offset:1600\n\t" \
    "v_add_u32 v244, 0x12000, v243\n\t" \
    "s_waitcnt vmcnt(12) lgkmcnt(0)\n\t" \
    "s_barrier \n\t" \
    "s_add_u32 m0, %[wm], 0xc000\n\t" \
    "s_mov_b32 s44, 0x3c0000\n\t" \
    "buffer_load_dwordx4 v240, %[wr], s44 offen offset:0 nt lds\n\t" \
    "buffer_load_dwordx4 v241, %[wr], s44 offen offset:1024 nt lds\n\t" \
    "buffer_load_dwordx4 v242, %[wr], s44 offen offset:2048 nt lds\n\t" \
    "ds_read_b64_tr_b16 v[204:205], v244 offset:0\n\t" \
    "ds_read_b64_tr_b16 v[206:207], v244 offset:1536\n\t" \
    "ds_read_b64_tr_b16 v[4:5], v244 offset:32\n\t" \
    "ds_read_b64_tr_b16 v[6:7], v244 offset:1568\n\t" \
    "ds_read_b64_tr_b16 v[12:13], v244 offset:64\n\t" \
    "ds_read_b64_tr_b16 v[14:15], v244 offset:1600\n\t" \
    "v_add_u32 v244, 0x18000, v243\n\t" \
    "s_waitcnt vmcnt(12) lgkmcnt(0)\n\t" \
    "s_barrier \n\t" \
    "s_add_u32 m0, %[wm], 0x12000\n\t" \
    "s_mov_b32 s44, 0x420000\n\t" \
    "buffer_load_dwordx4 v240, %[wr], s44 offen offset:0 nt lds\n\t" \
    "buffer_load_dwordx4 v241, %[wr], s44 offen offset:1024 nt lds\n\t" \
    "buffer_load_dwordx4 v242, %[wr], s44 offen offset:2048 nt lds\n\t" \
    "ds_read_b64_tr_b16 v[208:209], v244 offset:0\n\t" \
    "ds_read_b64_tr_b16 v[210:211], v244 offset:1536\n\t" \
    "ds_read_b64_tr_b16 v[40:41], v244 offset:32\n\t" \
    "ds_read_b64_tr_b16 v[42:43], v244 offset:1568\n\t" \
    "ds_read_b64_tr_b16 v[48:49], v244 offset:64\n\t" \
    "ds_read_b64_tr_b16 v[50:51], v244 offset:1600\n\t" \
    "v_add_u32 v244, 0x1e000, v243\n\t" \
    "s_waitcnt vmcnt(12) lgkmcnt(0)\n\t" \
    "s_barrier \n\t" \
    "s_add_u32 m0, %[wm], 0x18000\n\t" \
    "s_mov_b32 s44, 0x480000\n\t" \
    "buffer_load_dwordx4 v240, %[wr], s44 offen offset:0 nt lds\n\t" \
    "buffer_load_dwordx4 v241, %[wr], s44 offen offset:1024 nt lds\n\t" \
    "buffer_load_dwordx4 v242, %[wr], s44 offen offset:2048 nt lds\n\t" \
    "ds_read_b64_tr_b16 v[212:213], v244 offset:0\n\t" \
    "ds_read_b64_tr_b16 v[214:215], v244 offset:1536\n\t" \
    "ds_read_b64_tr_b16 v[44:45], v244 offset:32\n\t" \
    "ds_read_b64_tr_b16 v[46:47], v244 offset:1568\n\t" \
    "ds_read_b64_tr_b16 v[52:53], v244 offset:64\n\t" \
    "ds_read_b64_tr_b16 v[54:55], v244 offset:1600\n\t" \
    "v_add_u32 v244, 0x0, v243\n\t" \
    "s_waitcnt vmcnt(12) lgkmcnt(0)\n\t" \
    "s_barrier \n\t" \
    "v_mov_b32 v246, 0\n\t" \
    "buffer_load_dword v246, v246, s[60:63], %[mo] offen sc1\n\t" \
    "s_add_u32 m0, %[wm], 0x1e000\n\t" \
    "s_mov_b32 s44, 0x4e0000\n\t" \
    "buffer_load_dwordx4 v240, %[wr], s44 offen offset:0 nt lds\n\t" \
    "buffer_load_dwordx4 v241, %[wr], s44 offen offset:1024 nt lds\n\t" \
    "buffer_load_dwordx4 v242, %[wr], s44 offen offset:2048 nt lds\n\t" \
    "ds_read_b64_tr_b16 v[136:137], v244 offset:0\n\t" \
    "ds_read_b64_tr_b16 v[138:139], v244 offset:1536\n\t" \
    "ds_read_b64_tr_b16 v[144:145], v244 offset:32\n\t" \
    "ds_read_b64_tr_b16 v[146:147], v244 offset:1568\n\t" \
    "ds_read_b64_tr_b16 v[152:153], v244 offset:64\n\t" \
    "ds_read_b64_tr_b16 v[154:155], v244 offset:1600\n\t" \
    "v_add_u32 v244, 0x6000, v243\n\t" \
    "s_waitcnt vmcnt(12) lgkmcnt(0)\n\t" \
    "s_barrier \n\t" \
    "s_add_u32 m0, %[wm], 0x0\n\t" \
    "s_mov_b32 s44, 0x540000\n\t" \
    "buffer_load_dwordx4 v240, %[wr], s44 offen offset:0 nt lds\n\t" \
    "buffer_load_dwordx4 v241, %[wr], s44 offen offset:1024 nt lds\n\t" \
    "buffer_load_dwordx4 v242, %[wr], s44 offen offset:2048 nt lds\n\t" \
    "ds_read_b64_tr_b16 v[140:141], v244 offset:0\n\t" \
    "ds_read_b64_tr_b16 v[142:143], v244 offset:1536\n\t" \
    "ds_read_b64_tr_b16 v[148:149], v244 offset:32\n\t" \
    "ds_read_b64_tr_b16 v[150:151], v244 offset:1568\n\t" \
    "ds_read_b64_tr_b16 v[156:157], v244 offset:64\n\t" \
    "ds_read_b64_tr_b16 v[158:159], v244 offset:1600\n\t" \
    "v_add_u32 v244, 0xc000, v243\n\t" \
    "s_waitcnt vmcnt(12) lgkmcnt(0)\n\t" \
    "s_barrier \n\t" \
    "s_add_u32 m0, %[wm], 0x6000\n\t" \
    "s_mov_b32 s44, 0x5a0000\n\t" \
    "buffer_load_dwordx4 v240, %[wr], s44 offen offset:0 nt lds\n\t" \
    "buffer_load_dwordx4 v241, %[wr], s44 offen offset:1024 nt lds\n\t" \
    "buffer_load_dwordx4 v242, %[wr], s44 offen offset:2048 nt lds\n\t" \
    "ds_read_b64_tr_b16 v[216:217], v244 offset:0\n\t" \
    "ds_read_b64_tr_b16 v[218:219], v244 offset:1536\n\t" \
    "ds_read_b64_tr_b16 v[80:81], v244 offset:32\n\t" \
    "ds_read_b64_tr_b16 v[82:83], v244 offset:1568\n\t" \
    "ds_read_b64_tr_b16 v[88:89], v244 offset:64\n\t" \
    "ds_read_b64_tr_b16 v[90:91], v244 offset:1600\n\t" \
    "v_add_u32 v244, 0x12000, v243\n\t" \
    "s_waitcnt vmcnt(12) lgkmcnt(0)\n\t" \
    "s_barrier \n\t" \
    "s_add_u32 m0, %[wm], 0xc000\n\t" \
    "s_mov_b32 s44, 0x600000\n\t" \
    "buffer_load_dwordx4 v240, %[wr], s44 offen offset:0 nt lds\n\t" \
    "buffer_load_dwordx4 v241, %[wr], s44 offen offset:1024 nt lds\n\t" \
    "buffer_load_dwordx4 v242, %[wr], s44 offen offset:2048 nt lds\n\t" \
    "ds_read_b64_tr_b16 v[220:221], v244 offset:0\n\t" \
    "ds_read_b64_tr_b16 v[222:223], v244 offset:1536\n\t" \
    "ds_read_b64_tr_b16 v[84:85], v244 offset:32\n\t" \
    "ds_read_b64_tr_b16 v[86:87], v244 offset:1568\n\t" \
    "ds_read_b64_tr_b16 v[92:93], v244 offset:64\n\t" \
    "ds_read_b64_tr_b16 v[94:95], v244 offset:1600\n\t" \
    "v_add_u32 v244, 0x18000, v243\n\t" \
    "s_waitcnt vmcnt(12) lgkmcnt(0)\n\t" \
    "s_barrier \n\t" \
    "s_add_u32 m0, %[wm], 0x12000\n\t" \
    "s_mov_b32 s44, 0x660000\n\t" \
    "buffer_load_dwordx4 v240, %[wr], s44 offen offset:0 nt lds\n\t" \
    "buffer_load_dwordx4 v241, %[wr], s44 offen offset:1024 nt lds\n\t" \
    "buffer_load_dwordx4 v242, %[wr], s44 offen offset:2048 nt lds\n\t" \
    "ds_read_b64_tr_b16 v[16:17], v244 offset:0\n\t" \
    "ds_read_b64_tr_b16 v[18:19], v244 offset:1536\n\t" \
    "ds_read_b64_tr_b16 v[24:25], v244 offset:32\n\t" \
    "ds_read_b64_tr_b16 v[26:27], v244 offset:1568\n\t" \
    "ds_read_b64_tr_b16 v[32:33], v244 offset:64\n\t" \
    "ds_read_b64_tr_b16 v[34:35], v244 offset:1600\n\t" \
    "v_add_u32 v244, 0x1e000, v243\n\t" \
    "s_waitcnt vmcnt(12) lgkmcnt(0)\n\t" \
    "s_barrier \n\t" \
    ".Ls11_%=:\n\t" \
    "v_readfirstlane_b32 s43, v246\n\t" \
    "s_nop 0\n\t" \
    "s_cmp_lg_u32 s43, -1\n\t" \
    "s_cbranch_scc1 .Ls12_%=\n\t" \
    "s_cmp_eq_u32 s41, 0\n\t" \
    "s_cbranch_scc1 .Ls12_%=\n\t" \
    "s_sub_u32 s41, s41, 1\n\t" \
    "s_sleep 4\n\t" \
    "v_mov_b32 v246, 0\n\t" \
    "buffer_load_dword v246, v246, s[60:63], %[mo] offen sc1\n\t" \
    "s_waitcnt vmcnt(0)\n\t" \
    "s_branch .Ls11_%=\n\t" \
    ".Ls12_%=:\n\t" \
    "v_mov_b32 v246, 0x8000\n\t" \
    "s_mov_b64 exec, 1\n\t" \
    "buffer_store_dword v246, v246, s[60:63], %[mo] offen sc1\n\t" \
    "s_mov_b64 exec, -1\n\t" \
    "s_and_b32 s44, s43, 0x5000\n\t" \
    "s_xor_b32 %[mb], %[mb], s44\n\t" \
    "s_xor_b32 %[fl], %[fl], s44\n\t" \
    "s_and_b32 s43, s43, 0x2000\n\t" \
    "s_xor_b32 s43, s43, %[lx]\n\t" \
    "s_add_u32 s60, s60, s43\n\t" \
    "s_addc_u32 s61, s61, 0\n\t" \
    "s_add_u32 m0, %[wm], 0x18000\n\t" \
    "s_mov_b32 s44, 0x6c0000\n\t" \
    "buffer_load_dwordx4 v240, %[wr], s44 offen offset:0 nt lds\n\t" \
    "buffer_load_dwordx4 v241, %[wr], s44 offen offset:1024 nt lds\n\t" \
    "buffer_load_dwordx4 v242, %[wr], s44 offen offset:2048 nt lds\n\t" \
    "ds_read_b64_tr_b16 v[20:21], v244 offset:0\n\t" \
    "ds_read_b64_tr_b16 v[22:23], v244 offset:1536\n\t" \
    "ds_read_b64_tr_b16 v[28:29], v244 offset:32\n\t" \
    "ds_read_b64_tr_b16 v[30:31], v244 offset:1568\n\t" \
    "ds_read_b64_tr_b16 v[36:37], v244 offset:64\n\t" \
    "ds_read_b64_tr_b16 v[38:39], v244 offset:1600\n\t" \
    "v_add_u32 v244, 0x0, v243\n\t" \
    "s_waitcnt vmcnt(12) lgkmcnt(0)\n\t" \
    "s_barrier \n\t" \
    "s_add_u32 m0, %[wm], 0x1e000\n\t" \
    "s_mov_b32 s44, 0x720000\n\t" \
    "buffer_load_dwordx4 v240, %[wr], s44 offen offset:0 nt lds\n\t" \
    "buffer_load_dwordx4 v241, %[wr], s44 offen offset:1024 nt lds\n\t" \
    "buffer_load_dwordx4 v242, %[wr], s44 offen offset:2048 nt lds\n\t" \
    "ds_read_b64_tr_b16 v[224:225], v244 offset:0\n\t" \
    "ds_read_b64_tr_b16 v[226:227], v244 offset:1536\n\t" \
    "ds_read_b64_tr_b16 v[120:121], v244 offset:32\n\t" \
    "ds_read_b64_tr_b16 v[122:123], v244 offset:1568\n\t" \
    "ds_read_b64_tr_b16 v[128:129], v244 offset:64\n\t" \
    "ds_read_b64_tr_b16 v[130:131], v244 offset:1600\n\t" \
    "v_add_u32 v244, 0x6000, v243\n\t" \
    "s_waitcnt vmcnt(12) lgkmcnt(0)\n\t" \
    "s_barrier \n\t" \
    "ds_read_b64_tr_b16 v[228:229], v244 offset:0\n\t" \
    "ds_read_b64_tr_b16 v[230:231], v244 offset:1536\n\t" \
    "ds_read_b64_tr_b16 v[124:125], v244 offset:32\n\t" \
    "ds_read_b64_tr_b16 v[126:127], v244 offset:1568\n\t" \
    "ds_read_b64_tr_b16 v[132:133], v244 offset:64\n\t" \
    "ds_read_b64_tr_b16 v[134:135], v244 offset:1600\n\t" \
    "v_add_u32 v244, 0xc000, v243\n\t" \
    "s_waitcnt vmcnt(9) lgkmcnt(0)\n\t" \
    "s_barrier \n\t" \
    "ds_read_b64_tr_b16 v[232:233], v244 offset:0\n\t" \
    "ds_read_b64_tr_b16 v[234:235], v244 offset:1536\n\t" \
    "ds_read_b64_tr_b16 v[160:161], v244 offset:32\n\t" \
    "ds_read_b64_tr_b16 v[162:163], v244 offset:1568\n\t" \
    "ds_read_b64_tr_b16 v[168:169], v244 offset:64\n\t" \
    "ds_read_b64_tr_b16 v[170:171], v244 offset:1600\n\t" \
    "v_add_u32 v244, 0x12000, v243\n\t" \
    "s_waitcnt vmcnt(6) lgkmcnt(0)\n\t" \
    "s_barrier \n\t" \
    "ds_read_b64_tr_b16 v[236:237], v244 offset:0\n\t" \
    "ds_read_b64_tr_b16 v[238:239], v244 offset:1536\n\t" \
    "ds_read_b64_tr_b16 v[164:165], v244 offset:32\n\t" \
    "ds_read_b64_tr_b16 v[166:167], v244 offset:1568\n\t" \
    "ds_read_b64_tr_b16 v[172:173], v244 offset:64\n\t" \
    "ds_read_b64_tr_b16 v[174:175], v244 offset:1600\n\t" \
    "v_add_u32 v244, 0x18000, v243\n\t" \
    "s_waitcnt vmcnt(3) lgkmcnt(0)\n\t" \
    "s_barrier \n\t" \
    "ds_read_b64_tr_b16 v[176:177], v244 offset:0\n\t" \
    "ds_read_b64_tr_b16 v[178:179], v244 offset:1536\n\t" \
    "ds_read_b64_tr_b16 v[184:185], v244 offset:32\n\t" \
    "ds_read_b64_tr_b16 v[186:187], v244 offset:1568\n\t" \
    "ds_read_b64_tr_b16 v[192:193], v244 offset:64\n\t" \
    "ds_read_b64_tr_b16 v[194:195], v244 offset:1600\n\t" \
    "v_add_u32 v244, 0x1e000, v243\n\t" \
    "s_waitcnt vmcnt(0) lgkmcnt(0)\n\t" \
    "s_barrier \n\t" \
    "ds_read_b64_tr_b16 v[180:181], v244 offset:0\n\t" \
    "ds_read_b64_tr_b16 v[182:183], v244 offset:1536\n\t" \
    "ds_read_b64_tr_b16 v[188:189], v244 offset:32\n\t" \
    "ds_read_b64_tr_b16 v[190:191], v244 offset:1568\n\t" \
    "ds_read_b64_tr_b16 v[196:197], v244 offset:64\n\t" \
    "ds_read_b64_tr_b16 v[198:199], v244 offset:1600\n\t" \
    "s_waitcnt lgkmcnt(0)\n\t" \
    "s_barrier \n\t" \
    "ds_write_b128 v254, v[200:203] offset:40960\n\t" \
    "ds_write_b128 v254, v[204:207] offset:41984\n\t" \
    "ds_write_b128 v254, v[208:211] offset:43008\n\t" \
    "ds_write_b128 v254, v[212:215] offset:44032\n\t" \
    "ds_write_b128 v254, v[216:219] offset:45056\n\t" \
    "ds_write_b128 v254, v[220:223] offset:46080\n\t" \
    "ds_write_b128 v254, v[224:227] offset:47104\n\t" \
    "ds_write_b128 v254, v[228:231] offset:48128\n\t" \
    "ds_write_b128 v254, v[232:235] offset:49152\n\t" \
    "ds_write_b128 v254, v[236:239] offset:50176\n\t" \
    "s_waitcnt vmcnt(0) lgkmcnt(0)\n\t" \
    "s_barrier \n\t" \
    "ds_read_b128 v[224:227], v253 offset:0\n\t" \
    "ds_read_b128 v[244:247], v254 offset:40960\n\t" \
    "ds_read_b128 v[228:231], v253 offset:20480\n\t" \
    "ds_read_b128 v[232:235], v253 offset:1024\n\t" \
    "ds_read_b128 v[236:239], v253 offset:21504\n\t" \
    "ds_read_b128 v[248:251], v254 offset:41984\n\t" \
    ".p2align 8\n\t" \
    ".Ls13_%=:\n\t" \
    "s_waitcnt lgkmcnt(5)\n\t" \
    "v_mfma_f32_16x16x32_bf16 v[204:207], v[64:67], v[224:227], 0\n\t" \
    "ds_read_b128 v[240:243], v253 offset:2048\n\t" \
    "v_mfma_f32_16x16x32_bf16 v[208:211], v[72:75], v[224:227], 0\n\t" \
    "v_mfma_f32_16x16x32_bf16 v[200:203], v[56:59], v[224:227], 0\n\t" \
    "s_waitcnt lgkmcnt(4)\n\t" \
    "v_mfma_f32_16x16x32_bf16 v[216:219], v[64:67], v[228:231], 0\n\t" \
    "ds_read_b128 v[224:227], v253 offset:22528\n\t" \
    "v_mfma_f32_16x16x32_bf16 v[220:223], v[72:75], v[228:231], 0\n\t" \
    "v_mfma_f32_16x16x32_bf16 v[212:215], v[56:59], v[228:231], 0\n\t" \
    "s_waitcnt lgkmcnt(4)\n\t" \
    "v_mfma_f32_16x16x32_bf16 v[204:207], v[68:71], v[232:235], v[204:207]\n\t" \
    "ds_read_b128 v[228:231], v253 offset:3072\n\t" \
    "v_mfma_f32_16x16x32_bf16 v[208:211], v[76:79], v[232:235], v[208:211]\n\t" \
    "v_mfma_f32_16x16x32_bf16 v[200:203], v[60:63], v[232:235], v[200:203]\n\t" \
    "s_waitcnt lgkmcnt(4)\n\t" \
    "v_mfma_f32_16x16x32_bf16 v[216:219], v[68:71], v[236:239], v[216:219]\n\t" \
    "ds_read_b128 v[232:235], v253 offset:23552\n\t" \
    "v_mfma_f32_16x16x32_bf16 v[220:223], v[76:79], v[236:239], v[220:223]\n\t" \
    "v_mfma_f32_16x16x32_bf16 v[212:215], v[60:63], v[236:239], v[212:215]\n\t" \
    "s_waitcnt lgkmcnt(3)\n\t" \
    "v_mfma_f32_16x16x32_bf16 v[204:207], v[104:107], v[240:243], v[204:207]\n\t" \
    "ds_read_b128 v[236:239], v253 offset:4096\n\t" \
    "v_mfma_f32_16x16x32_bf16 v[208:211], v[112:115], v[240:243], v[208:211]\n\t" \
    "v_mfma_f32_16x16x32_bf16 v[200:203], v[96:99], v[240:243], v[200:203]\n\t" \
    "s_waitcnt lgkmcnt(3)\n\t" \
    "v_mfma_f32_16x16x32_bf16 v[216:219], v[104:107], v[224:227], v[216:219]\n\t" \
    "ds_read_b128 v[240:243], v253 offset:24576\n\t" \
    "v_mfma_f32_16x16x32_bf16 v[220:223], v[112:115], v[224:227], v[220:223]\n\t" \
    "v_mfma_f32_16x16x32_bf16 v[212:215], v[96:99], v[224:227], v[212:215]\n\t" \
    "s_waitcnt lgkmcnt(3)\n\t" \
    "v_mfma_f32_16x16x32_bf16 v[204:207], v[108:111], v[228:231], v[204:207]\n\t" \
    "ds_read_b128 v[224:227], v253 offset:5120\n\t" \
    "v_mfma_f32_16x16x32_bf16 v[208:211], v[116:119], v[228:231], v[208:211]\n\t" \
    "v_mfma_f32_16x16x32_bf16 v[200:203], v[100:103], v[228:231], v[200:203]\n\t" \
    "s_waitcnt lgkmcnt(3)\n\t" \
    "v_mfma_f32_16x16x32_bf16 v[216:219], v[108:111], v[232:235], v[216:219]\n\t" \
    "ds_read_b128 v[228:231], v253 offset:25600\n\t" \
    "v_mfma_f32_16x16x32_bf16 v[220:223], v[116:119], v[232:235], v[220:223]\n\t" \
    "v_mfma_f32_16x16x32_bf16 v[212:215], v[100:103], v[232:235], v[212:215]\n\t" \
    "s_waitcnt lgkmcnt(3)\n\t" \
    "v_mfma_f32_16x16x32_bf16 v[204:207], v[0:3], v[236:239], v[204:207]\n\t" \
    "ds_read_b128 v[232:235], v253 offset:6144\n\t" \
    "v_mfma_f32_16x16x32_bf16 v[208:211], v[8:11], v[236:239], v[208:211]\n\t" \
    "v_mfma_f32_16x16x32_bf16 v[200:203], v[244:247], v[236:239], v[200:203]\n\t" \
    "s_waitcnt lgkmcnt(3)\n\t" \
    "v_mfma_f32_16x16x32_bf16 v[216:219], v[0:3], v[240:243], v[216:219]\n\t" \
    "ds_read_b128 v[236:239], v253 offset:26624\n\t" \
    "v_mfma_f32_16x16x32_bf16 v[220:223], v[8:11], v[240:243], v[220:223]\n\t" \
    "v_mfma_f32_16x16x32_bf16 v[212:215], v[244:247], v[240:243], v[212:215]\n\t" \
    "ds_read_b128 v[244:247], v254 offset:43008\n\t" \
    "s_waitcnt lgkmcnt(4)\n\t" \
    "v_mfma_f32_16x16x32_bf16 v[204:207], v[4:7], v[224:227], v[204:207]\n\t" \
    "ds_read_b128 v[240:243], v253 offset:7168\n\t" \
    "v_mfma_f32_16x16x32_bf16 v[208:211], v[12:15], v[224:227], v[208:211]\n\t" \
    "v_mfma_f32_16x16x32_bf16 v[200:203], v[248:251], v[224:227], v[200:203]\n\t" \
    "s_waitcnt lgkmcnt(4)\n\t" \
    "v_mfma_f32_16x16x32_bf16 v[216:219], v[4:7], v[228:231], v[216:219]\n\t" \
    "ds_read_b128 v[224:227], v253 offset:27648\n\t" \
    "v_mfma_f32_16x16x32_bf16 v[220:223], v[12:15], v[228:231], v[220:223]\n\t" \
    "v_mfma_f32_16x16x32_bf16 v[212:215], v[248:251], v[228:231], v[212:215]\n\t" \
    "ds_read_b128 v[248:251], v254 offset:44032\n\t" \
    "s_waitcnt lgkmcnt(5)\n\t" \
    "v_mfma_f32_16x16x32_bf16 v[204:207], v[40:43], v[232:235], v[204:207]\n\t" \
    "ds_read_b128 v[228:231], v253 offset:8192\n\t" \
    "v_mfma_f32_16x16x32_bf16 v[208:211], v[48:51], v[232:235], v[208:211]\n\t" \
    "s_waitcnt lgkmcnt(4)\n\t" \
    "v_mfma_f32_16x16x32_bf16 v[200:203], v[244:247], v[232:235], v[200:203]\n\t" \
    "v_mfma_f32_16x16x32_bf16 v[216:219], v[40:43], v[236:239], v[216:219]\n\t" \
    "ds_read_b128 v[232:235], v253 offset:28672\n\t" \
    "v_mfma_f32_16x16x32_bf16 v[220:223], v[48:51], v[236:239], v[220:223]\n\t" \
    "v_mfma_f32_16x16x32_bf16 v[212:215], v[244:247], v[236:239], v[212:215]\n\t" \
    "ds_read_b128 v[244:247], v254 offset:45056\n\t" \
    "s_waitcnt lgkmcnt(5)\n\t" \
    "v_mfma_f32_16x16x32_bf16 v[204:207], v[44:47], v[240:243], v[204:207]\n\t" \
    "ds_read_b128 v[236:239], v253 offset:9216\n\t" \
    "v_mfma_f32_16x16x32_bf16 v[208:211], v[52:55], v[240:243], v[208:211]\n\t" \
    "s_waitcnt lgkmcnt(4)\n\t" \
    "v_mfma_f32_16x16x32_bf16 v[200:203], v[248:251], v[240:243], v[200:203]\n\t" \
    "v_mfma_f32_16x16x32_bf16 v[216:219], v[44:47], v[224:227], v[216:219]\n\t" \
    "ds_read_b128 v[240:243], v253 offset:29696\n\t" \
    "v_mfma_f32_16x16x32_bf16 v[220:223], v[52:55], v[224:227], v[220:223]\n\t" \
    "v_mfma_f32_16x16x32_bf16 v[212:215], v[248:251], v[224:227], v[212:215]\n\t" \
    "ds_read_b128 v[248:251], v254 offset:46080\n\t" \
    "s_waitcnt lgkmcnt(5)\n\t" \
    "v_mfma_f32_16x16x32_bf16 v[204:207], v[144:147], v[228:231], v[204:207]\n\t" \
    "ds_read_b128 v[224:227], v253 offset:10240\n\t" \
    "v_mfma_f32_16x16x32_bf16 v[208:211], v[152:155], v[228:231], v[208:211]\n\t" \
    "v_mfma_f32_16x16x32_bf16 v[200:203], v[136:139], v[228:231], v[200:203]\n\t" \
    "s_waitcnt lgkmcnt(5)\n\t" \
    "v_mfma_f32_16x16x32_bf16 v[216:219], v[144:147], v[232:235], v[216:219]\n\t" \
    "ds_read_b128 v[228:231], v253 offset:30720\n\t" \
    "v_mfma_f32_16x16x32_bf16 v[220:223], v[152:155], v[232:235], v[220:223]\n\t" \
    "v_mfma_f32_16x16x32_bf16 v[212:215], v[136:139], v[232:235], v[212:215]\n\t" \
    "s_waitcnt lgkmcnt(4)\n\t" \
    "v_mfma_f32_16x16x32_bf16 v[204:207], v[148:151], v[236:239], v[204:207]\n\t" \
    "ds_read_b128 v[232:235], v253 offset:11264\n\t" \
    "v_mfma_f32_16x16x32_bf16 v[208:211], v[156:159], v[236:239], v[208:211]\n\t" \
    "v_mfma_f32_16x16x32_bf16 v[200:203], v[140:143], v[236:239], v[200:203]\n\t" \
    "s_waitcnt lgkmcnt(4)\n\t" \
    "v_mfma_f32_16x16x32_bf16 v[216:219], v[148:151], v[240:243], v[216:219]\n\t" \
    "ds_read_b128 v[236:239], v253 offset:31744\n\t" \
    "v_mfma_f32_16x16x32_bf16 v[220:223], v[156:159], v[240:243], v[220:223]\n\t" \
    "v_mfma_f32_16x16x32_bf16 v[212:215], v[140:143], v[240:243], v[212:215]\n\t" \
    "s_setprio 2\n\t" \
    "s_waitcnt lgkmcnt(0)\n\t" \
    "s_barrier \n\t" \
    "v_mfma_f32_16x16x32_bf16 v[204:207], v[80:83], v[224:227], v[204:207]\n\t" \
    "ds_read_b128 v[240:243], v253 offset:14336\n\t" \
    "v_mfma_f32_16x16x32_bf16 v[208:211], v[88:91], v[224:227], v[208:211]\n\t" \
    "v_mfma_f32_16x16x32_bf16 v[200:203], v[244:247], v[224:227], v[200:203]\n\t" \
    "v_mfma_f32_16x16x32_bf16 v[216:219], v[80:83], v[228:231], v[216:219]\n\t" \
    "ds_read_b128 v[224:227], v253 offset:34816\n\t" \
    "s_and_b32 s44, s40, 3\n\t" \
    "v_mfma_f32_16x16x32_bf16 v[220:223], v[88:91], v[228:231], v[220:223]\n\t" \
    "s_lshl_b32 s44, s44, 22\n\t" \
    "v_mfma_f32_16x16x32_bf16 v[212:215], v[244:247], v[228:231], v[212:215]\n\t" \
    "ds_read_b128 v[244:247], v254 offset:47104\n\t" \
    "s_add_u32 s44, s44, %[mb]\n\t" \
    "v_mfma_f32_16x16x32_bf16 v[204:207], v[84:87], v[232:235], v[204:207]\n\t" \
    "ds_read_b128 v[228:231], v253 offset:15360\n\t" \
    "s_add_u32 s46, s44, 0x800\n\t" \
    "v_mfma_f32_16x16x32_bf16 v[208:211], v[92:95], v[232:235], v[208:211]\n\t" \
    "s_add_u32 s47, s44, 0x8800\n\t" \
    "v_mfma_f32_16x16x32_bf16 v[200:203], v[248:251], v[232:235], v[200:203]\n\t" \
    "s_add_u32 s48, s44, 0xc00\n\t" \
    "v_mfma_f32_16x16x32_bf16 v[216:219], v[84:87], v[236:239], v[216:219]\n\t" \
    "ds_read_b128 v[232:235], v253 offset:35840\n\t" \
    "v_mfma_f32_16x16x32_bf16 v[220:223], v[92:95], v[236:239], v[220:223]\n\t" \
    "v_mfma_f32_16x16x32_bf16 v[212:215], v[248:251], v[236:239], v[212:215]\n\t" \
    "ds_read_b128 v[248:251], v254 offset:48128\n\t" \
    "s_waitcnt lgkmcnt(5)\n\t" \
    "v_mfma_f32_16x16x32_bf16 v[204:207], v[120:123], v[240:243], v[204:207]\n\t" \
    "ds_read_b128 v[236:239], v253 offset:16384\n\t" \
    "v_mfma_f32_16x16x32_bf16 v[208:211], v[128:131], v[240:243], v[208:211]\n\t" \
    "s_waitcnt lgkmcnt(4)\n\t" \
    "v_mfma_f32_16x16x32_bf16 v[200:203], v[244:247], v[240:243], v[200:203]\n\t" \
    "v_mfma_f32_16x16x32_bf16 v[216:219], v[120:123], v[224:227], v[216:219]\n\t" \
    "ds_read_b128 v[240:243], v253 offset:36864\n\t" \
    "v_mfma_f32_16x16x32_bf16 v[220:223], v[128:131], v[224:227], v[220:223]\n\t" \
    "v_mfma_f32_16x16x32_bf16 v[212:215], v[244:247], v[224:227], v[212:215]\n\t" \
    "ds_read_b128 v[244:247], v254 offset:49152\n\t" \
    "s_waitcnt lgkmcnt(5)\n\t" \
    "v_mfma_f32_16x16x32_bf16 v[204:207], v[124:127], v[228:231], v[204:207]\n\t" \
    "ds_read_b128 v[224:227], v253 offset:17408\n\t" \
    "v_mfma_f32_16x16x32_bf16 v[208:211], v[132:135], v[228:231], v[208:211]\n\t" \
    "s_waitcnt lgkmcnt(4)\n\t" \
    "v_mfma_f32_16x16x32_bf16 v[200:203], v[248:251], v[228:231], v[200:203]\n\t" \
    "v_mfma_f32_16x16x32_bf16 v[216:219], v[124:127], v[232:235], v[216:219]\n\t" \
    "ds_read_b128 v[228:231], v253 offset:37888\n\t" \
    "v_mfma_f32_16x16x32_bf16 v[220:223], v[132:135], v[232:235], v[220:223]\n\t" \
    "v_mfma_f32_16x16x32_bf16 v[212:215], v[248:251], v[232:235], v[212:215]\n\t" \
    "ds_read_b128 v[248:251], v254 offset:50176\n\t" \
    "s_waitcnt lgkmcnt(5)\n\t" \
    "v_mfma_f32_16x16x32_bf16 v[204:207], v[160:163], v[236:239], v[204:207]\n\t" \
    "ds_read_b128 v[232:235], v253 offset:12288\n\t" \
    "v_mfma_f32_16x16x32_bf16 v[208:211], v[168:171], v[236:239], v[208:211]\n\t" \
    "s_waitcnt lgkmcnt(4)\n\t" \
    "v_mfma_f32_16x16x32_bf16 v[200:203], v[244:247], v[236:239], v[200:203]\n\t" \
    "v_mfma_f32_16x16x32_bf16 v[216:219], v[160:163], v[240:243], v[216:219]\n\t" \
    "ds_read_b128 v[236:239], v253 offset:13312\n\t" \
    "v_mfma_f32_16x16x32_bf16 v[220:223], v[168:171], v[240:243], v[220:223]\n\t" \
    "v_mfma_f32_16x16x32_bf16 v[212:215], v[244:247], v[240:243], v[212:215]\n\t" \
    "s_waitcnt lgkmcnt(4)\n\t" \
    "v_mfma_f32_16x16x32_bf16 v[204:207], v[164:167], v[224:227], v[204:207]\n\t" \
    "ds_read_b128 v[240:243], v253 offset:18432\n\t" \
    "v_mfma_f32_16x16x32_bf16 v[208:211], v[172:175], v[224:227], v[208:211]\n\t" \
    "s_waitcnt lgkmcnt(3)\n\t" \
    "v_mfma_f32_16x16x32_bf16 v[200:203], v[248:251], v[224:227], v[200:203]\n\t" \
    "v_mfma_f32_16x16x32_bf16 v[216:219], v[164:167], v[228:231], v[216:219]\n\t" \
    "ds_read_b128 v[224:227], v253 offset:19456\n\t" \
    "v_mfma_f32_16x16x32_bf16 v[220:223], v[172:175], v[228:231], v[220:223]\n\t" \
    "v_mfma_f32_16x16x32_bf16 v[212:215], v[248:251], v[228:231], v[212:215]\n\t" \
    "s_waitcnt lgkmcnt(3)\n\t" \
    "v_mfma_f32_16x16x32_bf16 v[204:207], v[24:27], v[232:235], v[204:207]\n\t" \
    "ds_read_b128 v[228:231], v253 offset:32768\n\t" \
    "v_mov_b64 v[244:245], 0\n\t" \
    "v_mfma_f32_16x16x32_bf16 v[208:211], v[32:35], v[232:235], v[208:211]\n\t" \
    "v_mov_b64 v[246:247], 0\n\t" \
    "v_mfma_f32_16x16x32_bf16 v[200:203], v[16:19], v[232:235], v[200:203]\n\t" \
    "v_mov_b64 v[248:249], 0\n\t" \
    "s_waitcnt lgkmcnt(3)\n\t" \
    "v_mfma_f32_16x16x32_bf16 v[204:207], v[28:31], v[236:239], v[204:207]\n\t" \
    "ds_read_b128 v[232:235], v253 offset:33792\n\t" \
    "v_mov_b64 v[250:251], 0\n\t" \
    "v_mfma_f32_16x16x32_bf16 v[208:211], v[36:39], v[236:239], v[208:211]\n\t" \
    "v_mfma_f32_16x16x32_bf16 v[200:203], v[20:23], v[236:239], v[200:203]\n\t" \
    "s_waitcnt lgkmcnt(3)\n\t" \
    "v_mfma_f32_16x16x32_bf16 v[204:207], v[184:187], v[240:243], v[204:207]\n\t" \
    "ds_read_b128 v[236:239], v253 offset:38912\n\t" \
    "v_mfma_f32_16x16x32_bf16 v[208:211], v[192:195], v[240:243], v[208:211]\n\t" \
    "v_mfma_f32_16x16x32_bf16 v[200:203], v[176:179], v[240:243], v[200:203]\n\t" \
    "s_waitcnt lgkmcnt(3)\n\t" \
    "v_mfma_f32_16x16x32_bf16 v[204:207], v[188:191], v[224:227], v[204:207]\n\t" \
    "ds_read_b128 v[240:243], v253 offset:39936\n\t" \
    "v_mfma_f32_16x16x32_bf16 v[208:211], v[196:199], v[224:227], v[208:211]\n\t" \
    "v_mfma_f32_16x16x32_bf16 v[200:203], v[180:183], v[224:227], v[200:203]\n\t" \
    "s_waitcnt lgkmcnt(3)\n\t" \
    "v_mfma_f32_16x16x32_bf16 v[216:219], v[24:27], v[228:231], v[216:219]\n\t" \
    "s_waitcnt lgkmcnt(2)\n\t" \
    "v_mfma_f32_16x16x32_bf16 v[216:219], v[28:31], v[232:235], v[216:219]\n\t" \
    "v_cvt_pk_bf16_f32 v204, v204, v208\n\t" \
    "v_cvt_pk_bf16_f32 v205, v205, v209\n\t" \
    "s_waitcnt lgkmcnt(1)\n\t" \
    "v_mfma_f32_16x16x32_bf16 v[216:219], v[184:187], v[236:239], v[216:219]\n\t" \
    "v_cvt_pk_bf16_f32 v206, v206, v210\n\t" \
    "v_cvt_pk_bf16_f32 v207, v207, v211\n\t" \
    "s_waitcnt lgkmcnt(0)\n\t" \
    "v_mfma_f32_16x16x32_bf16 v[216:219], v[188:191], v[240:243], v[216:219]\n\t" \
    "ds_write_b128 v254, v[204:207] offset:9216\n\t" \
    "v_mfma_f32_16x16x32_bf16 v[220:223], v[32:35], v[228:231], v[220:223]\n\t" \
    "v_mfma_f32_16x16x32_bf16 v[220:223], v[36:39], v[232:235], v[220:223]\n\t" \
    "v_cvt_pk_bf16_f32 v200, v200, v201\n\t" \
    "v_mfma_f32_16x16x32_bf16 v[220:223], v[192:195], v[236:239], v[220:223]\n\t" \
    "v_cvt_pk_bf16_f32 v201, v202, v203\n\t" \
    "ds_write_b64 v254, v[200:201] offset:7168\n\t" \
    "v_mfma_f32_16x16x32_bf16 v[220:223], v[196:199], v[240:243], v[220:223]\n\t" \
    "v_mfma_f32_16x16x32_bf16 v[212:215], v[16:19], v[228:231], v[212:215]\n\t" \
    "v_mfma_f32_16x16x32_bf16 v[212:215], v[20:23], v[232:235], v[212:215]\n\t" \
    "ds_read_b128 v[200:203], v254 offset:40960\n\t" \
    "ds_read_b128 v[204:207], v254 offset:41984\n\t" \
    "v_mfma_f32_16x16x32_bf16 v[212:215], v[176:179], v[236:239], v[212:215]\n\t" \
    "v_mov_b64 v[208:209], 0\n\t" \
    "v_mov_b64 v[210:211], 0\n\t" \
    "v_mfma_f32_16x16x32_bf16 v[212:215], v[180:183], v[240:243], v[212:215]\n\t" \
    "v_cvt_pk_bf16_f32 v216, v216, v220\n\t" \
    "v_cvt_pk_bf16_f32 v217, v217, v221\n\t" \
    "s_setprio 0\n\t" \
    "v_cvt_pk_bf16_f32 v218, v218, v222\n\t" \
    "v_cvt_pk_bf16_f32 v219, v219, v223\n\t" \
    "ds_write_b128 v254, v[216:219] offset:15360\n\t" \
    "ds_read_b128 v[216:219], v254 offset:43008\n\t" \
    "ds_read_b128 v[220:223], v254 offset:44032\n\t" \
    "s_nop 0\n\t" \
    "v_cvt_pk_bf16_f32 v212, v212, v213\n\t" \
    "v_cvt_pk_bf16_f32 v213, v214, v215\n\t" \
    "ds_write_b64 v254, v[212:213] offset:13312\n\t" \
    "s_sleep 3\n\t" \
    "s_mov_b32 m0, 0xa800\n\t" \
    "s_nop 0\n\t" \
    "buffer_load_dwordx4 v253, s[60:63], s46 offen sc1 lds\n\t" \
    "s_mov_b32 m0, 0xb800\n\t" \
    "s_nop 0\n\t" \
    "buffer_load_dwordx4 v253, s[60:63], s47 offen sc1 lds\n\t" \
    "s_mov_b32 m0, 0xac00\n\t" \
    "s_nop 0\n\t" \
    "buffer_load_dwordx4 v253, s[60:63], s48 offen sc1 lds\n\t" \
    "s_waitcnt lgkmcnt(0)\n\t" \
    "v_bfe_i32 v212, v253, 5, 1\n\t" \
    "v_and_b32 v212, 0xaaaa, v212\n\t" \
    "v_or_b32 v212, 0x4444, v212\n\t" \
    "v_and_b32 v213, 0xffff, v255\n\t" \
    "v_lshrrev_b32 v214, 16, v255\n\t" \
    ".Ls14_%=:\n\t" \
    "s_waitcnt vmcnt(0)\n\t" \
    "ds_read_b128 v[224:227], v253 offset:43008\n\t" \
    "ds_read_b128 v[228:231], v253 offset:47104\n\t" \
    "ds_read_b128 v[232:235], v253 offset:44032\n\t" \
    "s_waitcnt lgkmcnt(2)\n\t" \
    "v_max_u32 v215, v224, v225\n\t" \
    "v_max3_u32 v215, v226, v227, v215\n\t" \
    "s_waitcnt lgkmcnt(1)\n\t" \
    "v_max3_u32 v215, v228, v229, v215\n\t" \
    "v_max3_u32 v215, v230, v231, v215\n\t" \
    "s_waitcnt lgkmcnt(0)\n\t" \
    "v_max3_u32 v215, v232, v233, v215\n\t" \
    "v_max3_u32 v215, v234, v235, v215\n\t" \
    "v_cmp_eq_u32 vcc, -1, v215\n\t" \
    "s_cbranch_vccz .Ls15_%=\n\t" \
    "v_max_u32 v215, v224, v225\n\t" \
    "v_max3_u32 v215, v226, v227, v215\n\t" \
    "v_cmp_eq_u32 vcc, -1, v215\n\t" \
    "s_cbranch_vccz .Ls16_%=\n\t" \
    "s_mov_b32 m0, 0xa800\n\t" \
    "s_nop 0\n\t" \
    "buffer_load_dwordx4 v253, s[60:63], s46 offen sc1 lds\n\t" \
    ".Ls16_%=:\n\t" \
    "v_max_u32 v215, v228, v229\n\t" \
    "v_max3_u32 v215, v230, v231, v215\n\t" \
    "v_cmp_eq_u32 vcc, -1, v215\n\t" \
    "s_cbranch_vccz .Ls17_%=\n\t" \
    "s_mov_b32 m0, 0xb800\n\t" \
    "s_nop 0\n\t" \
    "buffer_load_dwordx4 v253, s[60:63], s47 offen sc1 lds\n\t" \
    ".Ls17_%=:\n\t" \
    "v_max_u32 v215, v232, v233\n\t" \
    "v_max3_u32 v215, v234, v235, v215\n\t" \
    "v_cmp_eq_u32 vcc, -1, v215\n\t" \
    "s_cbranch_vccz .Ls18_%=\n\t" \
    "s_mov_b32 m0, 0xac00\n\t" \
    "s_nop 0\n\t" \
    "buffer_load_dwordx4 v253, s[60:63], s48 offen sc1 lds\n\t" \
    ".Ls18_%=:\n\t" \
    "s_cmp_eq_u32 s41, 0\n\t" \
    "s_cbranch_scc1 .Ls15_%=\n\t" \
    "s_sub_u32 s41, s41, 1\n\t" \
    "s_branch .Ls14_%=\n\t" \
    ".Ls15_%=:\n\t" \
    "s_setprio 3\n\t" \
    "s_waitcnt lgkmcnt(0)\n\t" \
    "s_barrier \n\t" \
    "ds_read_b128 v[236:239], v253 offset:40960\n\t" \
    "ds_read_b128 v[240:243], v253 offset:46080\n\t" \
    "v_smfmac_f32_16x16x64_bf16 v[244:247], v[224:227], v[0:7], v212\n\t" \
    "v_smfmac_f32_16x16x64_bf16 v[248:251], v[224:227], v[8:15], v212\n\t" \
    "v_smfmac_f32_16x16x64_bf16 v[208:211], v[224:227], v[200:207], v212\n\t" \
    "ds_read_b128 v[200:203], v254 offset:45056\n\t" \
    "ds_read_b128 v[204:207], v254 offset:46080\n\t" \
    "ds_read_b128 v[224:227], v253 offset:41984\n\t" \
    "v_smfmac_f32_16x16x64_bf16 v[208:211], v[228:231], v[16:23], v212\n\t" \
    "v_smfmac_f32_16x16x64_bf16 v[244:247], v[228:231], v[24:31], v212\n\t" \
    "v_smfmac_f32_16x16x64_bf16 v[248:251], v[228:231], v[32:39], v212\n\t" \
    "ds_read_b128 v[228:231], v253 offset:48128\n\t" \
    "v_smfmac_f32_16x16x64_bf16 v[208:211], v[232:235], v[216:223], v212\n\t" \
    "ds_read_b128 v[216:219], v254 offset:47104\n\t" \
    "ds_read_b128 v[220:223], v254 offset:48128\n\t" \
    "v_smfmac_f32_16x16x64_bf16 v[244:247], v[232:235], v[40:47], v212\n\t" \
    "v_smfmac_f32_16x16x64_bf16 v[248:251], v[232:235], v[48:55], v212\n\t" \
    "ds_read_b128 v[232:235], v253 offset:45056\n\t" \
    "s_waitcnt lgkmcnt(8)\n\t" \
    "v_smfmac_f32_16x16x64_bf16 v[208:211], v[236:239], v[56:63], v212\n\t" \
    "v_smfmac_f32_16x16x64_bf16 v[244:247], v[236:239], v[64:71], v212\n\t" \
    "v_smfmac_f32_16x16x64_bf16 v[248:251], v[236:239], v[72:79], v212\n\t" \
    "ds_read_b128 v[236:239], v253 offset:49152\n\t" \
    "s_waitcnt lgkmcnt(6)\n\t" \
    "v_smfmac_f32_16x16x64_bf16 v[208:211], v[240:243], v[200:207], v212\n\t" \
    "ds_read_b128 v[200:203], v254 offset:49152\n\t" \
    "ds_read_b128 v[204:207], v254 offset:50176\n\t" \
    "v_smfmac_f32_16x16x64_bf16 v[244:247], v[240:243], v[80:87], v212\n\t" \
    "v_smfmac_f32_16x16x64_bf16 v[248:251], v[240:243], v[88:95], v212\n\t" \
    "ds_read_b128 v[240:243], v253 offset:50176\n\t" \
    "s_waitcnt lgkmcnt(8)\n\t" \
    "v_smfmac_f32_16x16x64_bf16 v[208:211], v[224:227], v[96:103], v212\n\t" \
    "v_smfmac_f32_16x16x64_bf16 v[244:247], v[224:227], v[104:111], v212\n\t" \
    "v_smfmac_f32_16x16x64_bf16 v[248:251], v[224:227], v[112:119], v212\n\t" \
    "s_waitcnt lgkmcnt(5)\n\t" \
    "v_smfmac_f32_16x16x64_bf16 v[208:211], v[228:231], v[216:223], v212\n\t" \
    "s_waitcnt lgkmcnt(4)\n\t" \
    "v_smfmac_f32_16x16x64_bf16 v[208:211], v[232:235], v[136:143], v212\n\t" \
    "s_waitcnt lgkmcnt(1)\n\t" \
    "v_smfmac_f32_16x16x64_bf16 v[208:211], v[236:239], v[200:207], v212\n\t" \
    "s_waitcnt lgkmcnt(0)\n\t" \
    "v_smfmac_f32_16x16x64_bf16 v[208:211], v[240:243], v[176:183], v212\n\t" \
    "v_smfmac_f32_16x16x64_bf16 v[244:247], v[228:231], v[120:127], v212\n\t" \
    "v_smfmac_f32_16x16x64_bf16 v[244:247], v[232:235], v[144:151], v212\n\t" \
    "v_smfmac_f32_16x16x64_bf16 v[244:247], v[236:239], v[160:167], v212\n\t" \
    "v_add_f32 v208, v208, v210\n\t" \
    "v_add_f32 v209, v209, v211\n\t" \
    "v_cvt_pk_bf16_f32 v208, v208, v209\n\t" \
    "v_smfmac_f32_16x16x64_bf16 v[244:247], v[240:243], v[184:191], v212\n\t" \
    "ds_write_b16 v213, v208 offset:0\n\t" \
    "ds_write_b16_d16_hi v213, v208 offset:16\n\t" \
    "v_smfmac_f32_16x16x64_bf16 v[248:251], v[228:231], v[128:135], v212\n\t" \
    "v_smfmac_f32_16x16x64_bf16 v[248:251], v[232:235], v[152:159], v212\n\t" \
    "v_smfmac_f32_16x16x64_bf16 v[248:251], v[236:239], v[168:175], v212\n\t" \
    "v_add_f32 v244, v244, v246\n\t" \
    "v_add_f32 v245, v245, v247\n\t" \
    "v_smfmac_f32_16x16x64_bf16 v[248:251], v[240:243], v[192:199], v212\n\t" \
    "s_nop 7\n\t" \
    "v_add_f32 v248, v248, v250\n\t" \
    "v_add_f32 v249, v249, v251\n\t" \
    "v_cvt_pk_bf16_f32 v244, v244, v248\n\t" \
    "v_cvt_pk_bf16_f32 v245, v245, v249\n\t" \
    "ds_write2_b32 v214, v244, v245 offset1:4\n\t" \
    "s_setprio 0\n\t" \
    "s_sleep 1\n\t" \
    "ds_read_b128 v[200:203], v254 offset:6144\n\t" \
    "ds_read_b128 v[224:227], v254 offset:18432\n\t" \
    "ds_read_b128 v[204:207], v254 offset:7168\n\t" \
    "ds_read_b128 v[228:231], v254 offset:19456\n\t" \
    "ds_read_b128 v[208:211], v254 offset:8192\n\t" \
    "ds_read_b128 v[232:235], v254 offset:20480\n\t" \
    "ds_read_b128 v[212:215], v254 offset:9216\n\t" \
    "ds_read_b128 v[236:239], v254 offset:21504\n\t" \
    "ds_read_b128 v[216:219], v254 offset:10240\n\t" \
    "ds_read_b128 v[220:223], v254 offset:11264\n\t" \
    "s_and_b32 s44, s40, 3\n\t" \
    "s_lshl_b32 s44, s44, 17\n\t" \
    "s_add_u32 s50, s44, %[pb]\n\t" \
    "s_add_u32 s51, s44, %[pm]\n\t" \
    "s_mov_b32 s52, 0xf0000\n\t" \
    "s_mov_b32 s53, 0xfff00ff\n\t" \
    "s_mov_b32 s54, 0x1f0001\n\t" \
    "s_mov_b32 s55, 0x1fff01ff\n\t" \
    "s_mov_b32 s56, 0x3f0003\n\t" \
    "s_mov_b32 s57, 0x3fff03ff\n\t" \
    "s_mov_b32 s58, 0x7f0007\n\t" \
    "s_mov_b32 s59, 0x7fff07ff\n\t" \
    "s_waitcnt lgkmcnt(8)\n\t" \
    "v_mfma_f32_16x16x32_bf16 v[240:243], v[224:227], v[200:203], 0\n\t" \
    "ds_read_b128 v[224:227], v254 offset:22528\n\t" \
    "s_waitcnt lgkmcnt(7)\n\t" \
    "v_mfma_f32_16x16x32_bf16 v[240:243], v[228:231], v[204:207], v[240:243]\n\t" \
    "ds_read_b128 v[228:231], v254 offset:23552\n\t" \
    "v_and_b32 v252, 0xffffff7f, v254\n\t" \
    "s_waitcnt lgkmcnt(0)\n\t" \
    "s_barrier \n\t" \
    "v_mfma_f32_16x16x32_bf16 v[240:243], v[232:235], v[208:211], v[240:243]\n\t" \
    "ds_read_b128 v[232:235], v252 offset:3072\n\t" \
    "v_mfma_f32_16x16x32_bf16 v[240:243], v[236:239], v[212:215], v[240:243]\n\t" \
    "ds_read_b128 v[236:239], v252 offset:3200\n\t" \
    "v_mfma_f32_16x16x32_bf16 v[240:243], v[224:227], v[216:219], v[240:243]\n\t" \
    "ds_read_b128 v[224:227], v252 offset:4096\n\t" \
    "v_mfma_f32_16x16x32_bf16 v[240:243], v[228:231], v[220:223], v[240:243]\n\t" \
    "ds_read_b128 v[228:231], v252 offset:4224\n\t" \
    "s_waitcnt lgkmcnt(3)\n\t" \
    "v_mfma_f32_16x16x32_bf16 v[248:251], v[232:235], v[200:203], 0\n\t" \
    "ds_read_b128 v[232:235], v252 offset:5120\n\t" \
    "s_waitcnt lgkmcnt(3)\n\t" \
    "v_mfma_f32_16x16x32_bf16 v[248:251], v[236:239], v[204:207], v[248:251]\n\t" \
    "ds_read_b128 v[236:239], v252 offset:5248\n\t" \
    "s_waitcnt lgkmcnt(3)\n\t" \
    "v_mfma_f32_16x16x32_bf16 v[248:251], v[224:227], v[208:211], v[248:251]\n\t" \
    "s_waitcnt lgkmcnt(2)\n\t" \
    "v_mfma_f32_16x16x32_bf16 v[248:251], v[228:231], v[212:215], v[248:251]\n\t" \
    "s_waitcnt lgkmcnt(1)\n\t" \
    "v_mfma_f32_16x16x32_bf16 v[248:251], v[232:235], v[216:219], v[248:251]\n\t" \
    "s_waitcnt lgkmcnt(0)\n\t" \
    "v_mfma_f32_16x16x32_bf16 v[248:251], v[236:239], v[220:223], v[248:251]\n\t" \
    "v_mov_b32 v226, 0xff7fffff\n\t" \
    "v_cndmask_b32 v240, v240, v226, s[52:53]\n\t" \
    "v_cndmask_b32 v241, v241, v226, s[54:55]\n\t" \
    "v_cndmask_b32 v242, v242, v226, s[56:57]\n\t" \
    "v_cndmask_b32 v243, v243, v226, s[58:59]\n\t" \
    "v_max3_f32 v224, v240, v241, v242\n\t" \
    "v_mov_b32 v225, v243\n\t" \
    "v_lshrrev_b32 v200, 5, v253\n\t" \
    "v_and_b32 v201, 0xf0, v253\n\t" \
    "v_and_or_b32 v200, v200, 8, v201\n\t" \
    "v_max3_f32 v224, v224, v248, v249\n\t" \
    "v_max3_f32 v225, v225, v250, v251\n\t" \
    "v_max_f32 v224, v224, v225\n\t" \
    "v_mov_b32 v225, v224\n\t" \
    "s_nop 1\n\t" \
    "v_permlane16_swap_b32 v224, v225\n\t" \
    "v_max_f32 v224, v224, v225\n\t" \
    "v_mov_b32 v225, v224\n\t" \
    "s_nop 1\n\t" \
    "v_permlane32_swap_b32 v224, v225\n\t" \
    "v_max_f32 v224, v224, v225\n\t" \
    "v_mul_f32 v224, %[scl], v224\n\t" \
    "v_fma_f32 v240, v240, %[scl], -v224\n\t" \
    "v_fma_f32 v241, v241, %[scl], -v224\n\t" \
    "v_exp_f32 v240, v240\n\t" \
    "v_fma_f32 v242, v242, %[scl], -v224\n\t" \
    "v_exp_f32 v241, v241\n\t" \
    "v_fma_f32 v243, v243, %[scl], -v224\n\t" \
    "v_exp_f32 v242, v242\n\t" \
    "v_fma_f32 v248, v248, %[scl], -v224\n\t" \
    "v_exp_f32 v243, v243\n\t" \
    "v_fma_f32 v249, v249, %[scl], -v224\n\t" \
    "v_exp_f32 v248, v248\n\t" \
    "v_fma_f32 v250, v250, %[scl], -v224\n\t" \
    "v_exp_f32 v249, v249\n\t" \
    "v_fma_f32 v251, v251, %[scl], -v224\n\t" \
    "v_exp_f32 v250, v250\n\t" \
    "v_exp_f32 v251, v251\n\t" \
    "v_add_f32 v224, v240, v241\n\t" \
    "v_add_f32 v225, v242, v243\n\t" \
    "v_add_f32 v232, v248, v249\n\t" \
    "v_add_f32 v233, v250, v251\n\t" \
    "v_add_f32 v224, v224, v225\n\t" \
    "v_add_f32 v232, v232, v233\n\t" \
    "v_fma_f32 v224, v232, 0.5, v224\n\t" \
    "v_mov_b32 v225, v224\n\t" \
    "s_nop 1\n\t" \
    "v_permlane16_swap_b32 v224, v225\n\t" \
    "v_add_f32 v224, v224, v225\n\t" \
    "v_mov_b32 v225, v224\n\t" \
    "s_nop 1\n\t" \
    "v_permlane32_swap_b32 v224, v225\n\t" \
    "v_add_f32 v224, v224, v225\n\t" \
    "v_rcp_f32 v224, v224\n\t" \
    "s_nop 0\n\t" \
    "v_mul_f32 v240, v240, v224\n\t" \
    "v_mul_f32 v241, v241, v224\n\t" \
    "v_mul_f32 v242, v242, v224\n\t" \
    "v_mul_f32 v243, v243, v224\n\t" \
    "v_mul_f32 v248, v248, v224\n\t" \
    "v_mul_f32 v249, v249, v224\n\t" \
    "v_mul_f32 v250, v250, v224\n\t" \
    "v_mul_f32 v251, v251, v224\n\t" \
    "v_cvt_pk_bf16_f32 v204, |v240|, |v241|\n\t" \
    "v_cvt_pk_bf16_f32 v205, |v242|, |v243|\n\t" \
    "v_cvt_pk_bf16_f32 v208, |v248|, |v249|\n\t" \
    "v_cvt_pk_bf16_f32 v209, |v250|, |v251|\n\t" \
    "v_permlane16_swap_b32 v204, v206\n\t" \
    "v_permlane16_swap_b32 v205, v207\n\t" \
    "s_mov_b32 exec_lo, 0xffff\n\t" \
    "s_mov_b32 exec_hi, 0xffff\n\t" \
    "buffer_store_dwordx4 v[204:207], v253, s[60:63], s50 offen sc1\n\t" \
    "s_mov_b32 exec_lo, -1\n\t" \
    "s_mov_b32 exec_hi, 0\n\t" \
    "buffer_store_dwordx2 v[208:209], v200, s[60:63], s51 offen sc1\n\t" \
    "s_mov_b32 exec_hi, -1\n\t" \
    "ds_read_b128 v[224:227], v253 offset:0\n\t" \
    "ds_read_b128 v[244:247], v254 offset:40960\n\t" \
    "ds_read_b128 v[228:231], v253 offset:20480\n\t" \
    "ds_read_b128 v[232:235], v253 offset:1024\n\t" \
    "ds_read_b128 v[236:239], v253 offset:21504\n\t" \
    "ds_read_b128 v[248:251], v254 offset:41984\n\t" \
    "s_add_u32 s40, s40, 1\n\t" \
    "s_cmp_lg_u32 s40, 64\n\t" \
    "s_cbranch_scc1 .Ls13_%=\n\t" \
    "s_branch .Lend%=\n\t" \
    ".Ls0_%=:\n\t" \
    "v_add_u32 v244, 0x18000, v243\n\t" \
    "s_waitcnt vmcnt(12) lgkmcnt(0)\n\t" \
    "s_barrier \n\t" \
    "s_add_u32 m0, %[wm], 0x12000\n\t" \
    "s_mov_b32 s44, 0x1e0000\n\t" \
    "buffer_load_dwordx4 v240, %[wr], s44 offen offset:0 nt lds\n\t" \
    "buffer_load_dwordx4 v241, %[wr], s44 offen offset:1024 nt lds\n\t" \
    "buffer_load_dwordx4 v242, %[wr], s44 offen offset:2048 nt lds\n\t" \
    "ds_read_b64_tr_b16 v[200:201], v244 offset:0\n\t" \
    "ds_read_b64_tr_b16 v[202:203], v244 offset:1536\n\t" \
    "ds_read_b64_tr_b16 v[0:1], v244 offset:32\n\t" \
    "ds_read_b64_tr_b16 v[2:3], v244 offset:1568\n\t" \
    "ds_read_b64_tr_b16 v[8:9], v244 offset:64\n\t" \
    "ds_read_b64_tr_b16 v[10:11], v244 offset:1600\n\t" \
    "v_add_u32 v244, 0x1e000, v243\n\t" \
    "s_waitcnt vmcnt(12) lgkmcnt(0)\n\t" \
    "s_barrier \n\t" \
    "s_add_u32 m0, %[wm], 0x18000\n\t" \
    "s_mov_b32 s44, 0x240000\n\t" \
    "buffer_load_dwordx4 v240, %[wr], s44 offen offset:0 nt lds\n\t" \
    "buffer_load_dwordx4 v241, %[wr], s44 offen offset:1024 nt lds\n\t" \
    "buffer_load_dwordx4 v242, %[wr], s44 offen offset:2048 nt lds\n\t" \
    "ds_read_b64_tr_b16 v[204:205], v244 offset:0\n\t" \
    "ds_read_b64_tr_b16 v[206:207], v244 offset:1536\n\t" \
    "ds_read_b64_tr_b16 v[4:5], v244 offset:32\n\t" \
    "ds_read_b64_tr_b16 v[6:7], v244 offset:1568\n\t" \
    "ds_read_b64_tr_b16 v[12:13], v244 offset:64\n\t" \
    "ds_read_b64_tr_b16 v[14:15], v244 offset:1600\n\t" \
    "v_add_u32 v244, 0x0, v243\n\t" \
    "s_waitcnt vmcnt(12) lgkmcnt(0)\n\t" \
    "s_barrier \n\t" \
    "s_add_u32 m0, %[wm], 0x1e000\n\t" \
    "s_mov_b32 s44, 0x2a0000\n\t" \
    "buffer_load_dwordx4 v240, %[wr], s44 offen offset:0 nt lds\n\t" \
    "buffer_load_dwordx4 v241, %[wr], s44 offen offset:1024 nt lds\n\t" \
    "buffer_load_dwordx4 v242, %[wr], s44 offen offset:2048 nt lds\n\t" \
    "ds_read_b64_tr_b16 v[208:209], v244 offset:0\n\t" \
    "ds_read_b64_tr_b16 v[210:211], v244 offset:1536\n\t" \
    "ds_read_b64_tr_b16 v[40:41], v244 offset:32\n\t" \
    "ds_read_b64_tr_b16 v[42:43], v244 offset:1568\n\t" \
    "ds_read_b64_tr_b16 v[48:49], v244 offset:64\n\t" \
    "ds_read_b64_tr_b16 v[50:51], v244 offset:1600\n\t" \
    "v_add_u32 v244, 0x6000, v243\n\t" \
    "s_waitcnt vmcnt(12) lgkmcnt(0)\n\t" \
    "s_barrier \n\t" \
    "s_add_u32 m0, %[wm], 0x0\n\t" \
    "s_mov_b32 s44, 0x300000\n\t" \
    "buffer_load_dwordx4 v240, %[wr], s44 offen offset:0 nt lds\n\t" \
    "buffer_load_dwordx4 v241, %[wr], s44 offen offset:1024 nt lds\n\t" \
    "buffer_load_dwordx4 v242, %[wr], s44 offen offset:2048 nt lds\n\t" \
    "ds_read_b64_tr_b16 v[212:213], v244 offset:0\n\t" \
    "ds_read_b64_tr_b16 v[214:215], v244 offset:1536\n\t" \
    "ds_read_b64_tr_b16 v[44:45], v244 offset:32\n\t" \
    "ds_read_b64_tr_b16 v[46:47], v244 offset:1568\n\t" \
    "ds_read_b64_tr_b16 v[52:53], v244 offset:64\n\t" \
    "ds_read_b64_tr_b16 v[54:55], v244 offset:1600\n\t" \
    "v_add_u32 v244, 0xc000, v243\n\t" \
    "s_waitcnt vmcnt(12) lgkmcnt(0)\n\t" \
    "s_barrier \n\t" \
    "s_add_u32 m0, %[wm], 0x6000\n\t" \
    "s_mov_b32 s44, 0x360000\n\t" \
    "buffer_load_dwordx4 v240, %[wr], s44 offen offset:0 nt lds\n\t" \
    "buffer_load_dwordx4 v241, %[wr], s44 offen offset:1024 nt lds\n\t" \
    "buffer_load_dwordx4 v242, %[wr], s44 offen offset:2048 nt lds\n\t" \
    "ds_read_b64_tr_b16 v[216:217], v244 offset:0\n\t" \
    "ds_read_b64_tr_b16 v[218:219], v244 offset:1536\n\t" \
    "ds_read_b64_tr_b16 v[80:81], v244 offset:32\n\t" \
    "ds_read_b64_tr_b16 v[82:83], v244 offset:1568\n\t" \
    "ds_read_b64_tr_b16 v[88:89], v244 offset:64\n\t" \
    "ds_read_b64_tr_b16 v[90:91], v244 offset:1600\n\t" \
    "v_add_u32 v244, 0x12000, v243\n\t" \
    "s_waitcnt vmcnt(12) lgkmcnt(0)\n\t" \
    "s_barrier \n\t" \
    "s_add_u32 m0, %[wm], 0xc000\n\t" \
    "s_mov_b32 s44, 0x3c0000\n\t" \
    "buffer_load_dwordx4 v240, %[wr], s44 offen offset:0 nt lds\n\t" \
    "buffer_load_dwordx4 v241, %[wr], s44 offen offset:1024 nt lds\n\t" \
    "buffer_load_dwordx4 v242, %[wr], s44 offen offset:2048 nt lds\n\t" \
    "ds_read_b64_tr_b16 v[220:221], v244 offset:0\n\t" \
    "ds_read_b64_tr_b16 v[222:223], v244 offset:1536\n\t" \
    "ds_read_b64_tr_b16 v[84:85], v244 offset:32\n\t" \
    "ds_read_b64_tr_b16 v[86:87], v244 offset:1568\n\t" \
    "ds_read_b64_tr_b16 v[92:93], v244 offset:64\n\t" \
    "ds_read_b64_tr_b16 v[94:95], v244 offset:1600\n\t" \
    "v_add_u32 v244, 0x18000, v243\n\t" \
    "s_waitcnt vmcnt(12) lgkmcnt(0)\n\t" \
    "s_barrier \n\t" \
    "s_add_u32 m0, %[wm], 0x12000\n\t" \
    "s_mov_b32 s44, 0x420000\n\t" \
    "buffer_load_dwordx4 v240, %[wr], s44 offen offset:0 nt lds\n\t" \
    "buffer_load_dwordx4 v241, %[wr], s44 offen offset:1024 nt lds\n\t" \
    "buffer_load_dwordx4 v242, %[wr], s44 offen offset:2048 nt lds\n\t" \
    "ds_read_b64_tr_b16 v[224:225], v244 offset:0\n\t" \
    "ds_read_b64_tr_b16 v[226:227], v244 offset:1536\n\t" \
    "ds_read_b64_tr_b16 v[120:121], v244 offset:32\n\t" \
    "ds_read_b64_tr_b16 v[122:123], v244 offset:1568\n\t" \
    "ds_read_b64_tr_b16 v[128:129], v244 offset:64\n\t" \
    "ds_read_b64_tr_b16 v[130:131], v244 offset:1600\n\t" \
    "v_add_u32 v244, 0x1e000, v243\n\t" \
    "s_waitcnt vmcnt(12) lgkmcnt(0)\n\t" \
    "s_barrier \n\t" \
    "s_add_u32 m0, %[wm], 0x18000\n\t" \
    "s_mov_b32 s44, 0x480000\n\t" \
    "buffer_load_dwordx4 v240, %[wr], s44 offen offset:0 nt lds\n\t" \
    "buffer_load_dwordx4 v241, %[wr], s44 offen offset:1024 nt lds\n\t" \
    "buffer_load_dwordx4 v242, %[wr], s44 offen offset:2048 nt lds\n\t" \
    "ds_read_b64_tr_b16 v[228:229], v244 offset:0\n\t" \
    "ds_read_b64_tr_b16 v[230:231], v244 offset:1536\n\t" \
    "ds_read_b64_tr_b16 v[124:125], v244 offset:32\n\t" \
    "ds_read_b64_tr_b16 v[126:127], v244 offset:1568\n\t" \
    "ds_read_b64_tr_b16 v[132:133], v244 offset:64\n\t" \
    "ds_read_b64_tr_b16 v[134:135], v244 offset:1600\n\t" \
    "v_add_u32 v244, 0x0, v243\n\t" \
    "s_waitcnt vmcnt(12) lgkmcnt(0)\n\t" \
    "s_barrier \n\t" \
    "v_mov_b32 v246, 0\n\t" \
    "buffer_load_dword v246, v246, s[60:63], %[mo] offen sc1\n\t" \
    "s_add_u32 m0, %[wm], 0x1e000\n\t" \
    "s_mov_b32 s44, 0x4e0000\n\t" \
    "buffer_load_dwordx4 v240, %[wr], s44 offen offset:0 nt lds\n\t" \
    "buffer_load_dwordx4 v241, %[wr], s44 offen offset:1024 nt lds\n\t" \
    "buffer_load_dwordx4 v242, %[wr], s44 offen offset:2048 nt lds\n\t" \
    "ds_read_b64_tr_b16 v[232:233], v244 offset:0\n\t" \
    "ds_read_b64_tr_b16 v[234:235], v244 offset:1536\n\t" \
    "ds_read_b64_tr_b16 v[160:161], v244 offset:32\n\t" \
    "ds_read_b64_tr_b16 v[162:163], v244 offset:1568\n\t" \
    "ds_read_b64_tr_b16 v[168:169], v244 offset:64\n\t" \
    "ds_read_b64_tr_b16 v[170:171], v244 offset:1600\n\t" \
    "v_add_u32 v244, 0x6000, v243\n\t" \
    "s_waitcnt vmcnt(12) lgkmcnt(0)\n\t" \
    "s_barrier \n\t" \
    "s_add_u32 m0, %[wm], 0x0\n\t" \
    "s_mov_b32 s44, 0x540000\n\t" \
    "buffer_load_dwordx4 v240, %[wr], s44 offen offset:0 nt lds\n\t" \
    "buffer_load_dwordx4 v241, %[wr], s44 offen offset:1024 nt lds\n\t" \
    "buffer_load_dwordx4 v242, %[wr], s44 offen offset:2048 nt lds\n\t" \
    "ds_read_b64_tr_b16 v[236:237], v244 offset:0\n\t" \
    "ds_read_b64_tr_b16 v[238:239], v244 offset:1536\n\t" \
    "ds_read_b64_tr_b16 v[164:165], v244 offset:32\n\t" \
    "ds_read_b64_tr_b16 v[166:167], v244 offset:1568\n\t" \
    "ds_read_b64_tr_b16 v[172:173], v244 offset:64\n\t" \
    "ds_read_b64_tr_b16 v[174:175], v244 offset:1600\n\t" \
    "v_add_u32 v244, 0xc000, v243\n\t" \
    "s_waitcnt vmcnt(12) lgkmcnt(0)\n\t" \
    "s_barrier \n\t" \
    "s_add_u32 m0, %[wm], 0x6000\n\t" \
    "s_mov_b32 s44, 0x5a0000\n\t" \
    "buffer_load_dwordx4 v240, %[wr], s44 offen offset:0 nt lds\n\t" \
    "buffer_load_dwordx4 v241, %[wr], s44 offen offset:1024 nt lds\n\t" \
    "buffer_load_dwordx4 v242, %[wr], s44 offen offset:2048 nt lds\n\t" \
    "ds_read_b64_tr_b16 v[16:17], v244 offset:0\n\t" \
    "ds_read_b64_tr_b16 v[18:19], v244 offset:1536\n\t" \
    "ds_read_b64_tr_b16 v[24:25], v244 offset:32\n\t" \
    "ds_read_b64_tr_b16 v[26:27], v244 offset:1568\n\t" \
    "ds_read_b64_tr_b16 v[32:33], v244 offset:64\n\t" \
    "ds_read_b64_tr_b16 v[34:35], v244 offset:1600\n\t" \
    "v_add_u32 v244, 0x12000, v243\n\t" \
    "s_waitcnt vmcnt(12) lgkmcnt(0)\n\t" \
    "s_barrier \n\t" \
    "s_add_u32 m0, %[wm], 0xc000\n\t" \
    "s_mov_b32 s44, 0x600000\n\t" \
    "buffer_load_dwordx4 v240, %[wr], s44 offen offset:0 nt lds\n\t" \
    "buffer_load_dwordx4 v241, %[wr], s44 offen offset:1024 nt lds\n\t" \
    "buffer_load_dwordx4 v242, %[wr], s44 offen offset:2048 nt lds\n\t" \
    "ds_read_b64_tr_b16 v[20:21], v244 offset:0\n\t" \
    "ds_read_b64_tr_b16 v[22:23], v244 offset:1536\n\t" \
    "ds_read_b64_tr_b16 v[28:29], v244 offset:32\n\t" \
    "ds_read_b64_tr_b16 v[30:31], v244 offset:1568\n\t" \
    "ds_read_b64_tr_b16 v[36:37], v244 offset:64\n\t" \
    "ds_read_b64_tr_b16 v[38:39], v244 offset:1600\n\t" \
    "v_add_u32 v244, 0x18000, v243\n\t" \
    "s_waitcnt vmcnt(12) lgkmcnt(0)\n\t" \
    "s_barrier \n\t" \
    "s_add_u32 m0, %[wm], 0x12000\n\t" \
    "s_mov_b32 s44, 0x660000\n\t" \
    "buffer_load_dwordx4 v240, %[wr], s44 offen offset:0 nt lds\n\t" \
    "buffer_load_dwordx4 v241, %[wr], s44 offen offset:1024 nt lds\n\t" \
    "buffer_load_dwordx4 v242, %[wr], s44 offen offset:2048 nt lds\n\t" \
    "ds_read_b64_tr_b16 v[56:57], v244 offset:0\n\t" \
    "ds_read_b64_tr_b16 v[58:59], v244 offset:1536\n\t" \
    "ds_read_b64_tr_b16 v[64:65], v244 offset:32\n\t" \
    "ds_read_b64_tr_b16 v[66:67], v244 offset:1568\n\t" \
    "ds_read_b64_tr_b16 v[72:73], v244 offset:64\n\t" \
    "ds_read_b64_tr_b16 v[74:75], v244 offset:1600\n\t" \
    "v_add_u32 v244, 0x1e000, v243\n\t" \
    "s_waitcnt vmcnt(12) lgkmcnt(0)\n\t" \
    "s_barrier \n\t" \
    ".Ls01_%=:\n\t" \
    "v_readfirstlane_b32 s43, v246\n\t" \
    "s_nop 0\n\t" \
    "s_cmp_lg_u32 s43, -1\n\t" \
    "s_cbranch_scc1 .Ls02_%=\n\t" \
    "s_cmp_eq_u32 s41, 0\n\t" \
    "s_cbranch_scc1 .Ls02_%=\n\t" \
    "s_sub_u32 s41, s41, 1\n\t" \
    "s_sleep 4\n\t" \
    "v_mov_b32 v246, 0\n\t" \
    "buffer_load_dword v246, v246, s[60:63], %[mo] offen sc1\n\t" \
    "s_waitcnt vmcnt(0)\n\t" \
    "s_branch .Ls01_%=\n\t" \
    ".Ls02_%=:\n\t" \
    "v_mov_b32 v246, 0x8000\n\t" \
    "s_mov_b64 exec, 1\n\t" \
    "buffer_store_dword v246, v246, s[60:63], %[mo] offen sc1\n\t" \
    "s_mov_b64 exec, -1\n\t" \
    "s_and_b32 s44, s43, 0x5000\n\t" \
    "s_xor_b32 %[mb], %[mb], s44\n\t" \
    "s_xor_b32 %[fl], %[fl], s44\n\t" \
    "s_xor_b32 %[pb], %[pb], s44\n\t" \
    "s_and_b32 s43, s43, 0x2000\n\t" \
    "s_xor_b32 s43, s43, %[lx]\n\t" \
    "s_add_u32 s60, s60, s43\n\t" \
    "s_addc_u32 s61, s61, 0\n\t" \
    "s_add_u32 m0, %[wm], 0x18000\n\t" \
    "s_mov_b32 s44, 0x6c0000\n\t" \
    "buffer_load_dwordx4 v240, %[wr], s44 offen offset:0 nt lds\n\t" \
    "buffer_load_dwordx4 v241, %[wr], s44 offen offset:1024 nt lds\n\t" \
    "buffer_load_dwordx4 v242, %[wr], s44 offen offset:2048 nt lds\n\t" \
    "ds_read_b64_tr_b16 v[60:61], v244 offset:0\n\t" \
    "ds_read_b64_tr_b16 v[62:63], v244 offset:1536\n\t" \
    "ds_read_b64_tr_b16 v[68:69], v244 offset:32\n\t" \
    "ds_read_b64_tr_b16 v[70:71], v244 offset:1568\n\t" \
    "ds_read_b64_tr_b16 v[76:77], v244 offset:64\n\t" \
    "ds_read_b64_tr_b16 v[78:79], v244 offset:1600\n\t" \
    "v_add_u32 v244, 0x0, v243\n\t" \
    "s_waitcnt vmcnt(12) lgkmcnt(0)\n\t" \
    "s_barrier \n\t" \
    "s_add_u32 m0, %[wm], 0x1e000\n\t" \
    "s_mov_b32 s44, 0x720000\n\t" \
    "buffer_load_dwordx4 v240, %[wr], s44 offen offset:0 nt lds\n\t" \
    "buffer_load_dwordx4 v241, %[wr], s44 offen offset:1024 nt lds\n\t" \
    "buffer_load_dwordx4 v242, %[wr], s44 offen offset:2048 nt lds\n\t" \
    "ds_read_b64_tr_b16 v[96:97], v244 offset:0\n\t" \
    "ds_read_b64_tr_b16 v[98:99], v244 offset:1536\n\t" \
    "ds_read_b64_tr_b16 v[104:105], v244 offset:32\n\t" \
    "ds_read_b64_tr_b16 v[106:107], v244 offset:1568\n\t" \
    "ds_read_b64_tr_b16 v[112:113], v244 offset:64\n\t" \
    "ds_read_b64_tr_b16 v[114:115], v244 offset:1600\n\t" \
    "v_add_u32 v244, 0x6000, v243\n\t" \
    "s_waitcnt vmcnt(12) lgkmcnt(0)\n\t" \
    "s_barrier \n\t" \
    "ds_read_b64_tr_b16 v[100:101], v244 offset:0\n\t" \
    "ds_read_b64_tr_b16 v[102:103], v244 offset:1536\n\t" \
    "ds_read_b64_tr_b16 v[108:109], v244 offset:32\n\t" \
    "ds_read_b64_tr_b16 v[110:111], v244 offset:1568\n\t" \
    "ds_read_b64_tr_b16 v[116:117], v244 offset:64\n\t" \
    "ds_read_b64_tr_b16 v[118:119], v244 offset:1600\n\t" \
    "v_add_u32 v244, 0xc000, v243\n\t" \
    "s_waitcnt vmcnt(9) lgkmcnt(0)\n\t" \
    "s_barrier \n\t" \
    "ds_read_b64_tr_b16 v[136:137], v244 offset:0\n\t" \
    "ds_read_b64_tr_b16 v[138:139], v244 offset:1536\n\t" \
    "ds_read_b64_tr_b16 v[144:145], v244 offset:32\n\t" \
    "ds_read_b64_tr_b16 v[146:147], v244 offset:1568\n\t" \
    "ds_read_b64_tr_b16 v[152:153], v244 offset:64\n\t" \
    "ds_read_b64_tr_b16 v[154:155], v244 offset:1600\n\t" \
    "v_add_u32 v244, 0x12000, v243\n\t" \
    "s_waitcnt vmcnt(6) lgkmcnt(0)\n\t" \
    "s_barrier \n\t" \
    "ds_read_b64_tr_b16 v[140:141], v244 offset:0\n\t" \
    "ds_read_b64_tr_b16 v[142:143], v244 offset:1536\n\t" \
    "ds_read_b64_tr_b16 v[148:149], v244 offset:32\n\t" \
    "ds_read_b64_tr_b16 v[150:151], v244 offset:1568\n\t" \
    "ds_read_b64_tr_b16 v[156:157], v244 offset:64\n\t" \
    "ds_read_b64_tr_b16 v[158:159], v244 offset:1600\n\t" \
    "v_add_u32 v244, 0x18000, v243\n\t" \
    "s_waitcnt vmcnt(3) lgkmcnt(0)\n\t" \
    "s_barrier \n\t" \
    "ds_read_b64_tr_b16 v[176:177], v244 offset:0\n\t" \
    "ds_read_b64_tr_b16 v[178:179], v244 offset:1536\n\t" \
    "ds_read_b64_tr_b16 v[184:185], v244 offset:32\n\t" \
    "ds_read_b64_tr_b16 v[186:187], v244 offset:1568\n\t" \
    "ds_read_b64_tr_b16 v[192:193], v244 offset:64\n\t" \
    "ds_read_b64_tr_b16 v[194:195], v244 offset:1600\n\t" \
    "v_add_u32 v244, 0x1e000, v243\n\t" \
    "s_waitcnt vmcnt(0) lgkmcnt(0)\n\t" \
    "s_barrier \n\t" \
    "ds_read_b64_tr_b16 v[180:181], v244 offset:0\n\t" \
    "ds_read_b64_tr_b16 v[182:183], v244 offset:1536\n\t" \
    "ds_read_b64_tr_b16 v[188:189], v244 offset:32\n\t" \
    "ds_read_b64_tr_b16 v[190:191], v244 offset:1568\n\t" \
    "ds_read_b64_tr_b16 v[196:197], v244 offset:64\n\t" \
    "ds_read_b64_tr_b16 v[198:199], v244 offset:1600\n\t" \
    "s_waitcnt lgkmcnt(0)\n\t" \
    "s_barrier \n\t" \
    "ds_write_b128 v254, v[200:203] offset:30720\n\t" \
    "ds_write_b128 v254, v[204:207] offset:31744\n\t" \
    "ds_write_b128 v254, v[208:211] offset:32768\n\t" \
    "ds_write_b128 v254, v[212:215] offset:33792\n\t" \
    "ds_write_b128 v254, v[216:219] offset:34816\n\t" \
    "ds_write_b128 v254, v[220:223] offset:35840\n\t" \
    "ds_write_b128 v254, v[224:227] offset:36864\n\t" \
    "ds_write_b128 v254, v[228:231] offset:37888\n\t" \
    "ds_write_b128 v254, v[232:235] offset:38912\n\t" \
    "ds_write_b128 v254, v[236:239] offset:39936\n\t" \
    "s_waitcnt vmcnt(0) lgkmcnt(0)\n\t" \
    "s_barrier \n\t" \
    "ds_read_b128 v[224:227], v253 offset:0\n\t" \
    "ds_read_b128 v[228:231], v253 offset:20480\n\t" \
    "ds_read_b128 v[232:235], v253 offset:1024\n\t" \
    "ds_read_b128 v[236:239], v253 offset:21504\n\t" \
    ".p2align 8\n\t" \
    ".Ls03_%=:\n\t" \
    "s_waitcnt lgkmcnt(3)\n\t" \
    "v_mfma_f32_16x16x32_bf16 v[204:207], v[0:3], v[224:227], 0\n\t" \
    "ds_read_b128 v[240:243], v253 offset:2048\n\t" \
    "v_mfma_f32_16x16x32_bf16 v[208:211], v[8:11], v[224:227], 0\n\t" \
    "s_waitcnt lgkmcnt(3)\n\t" \
    "v_mfma_f32_16x16x32_bf16 v[216:219], v[0:3], v[228:231], 0\n\t" \
    "ds_read_b128 v[224:227], v253 offset:22528\n\t" \
    "v_mfma_f32_16x16x32_bf16 v[220:223], v[8:11], v[228:231], 0\n\t" \
    "s_waitcnt lgkmcnt(3)\n\t" \
    "v_mfma_f32_16x16x32_bf16 v[204:207], v[4:7], v[232:235], v[204:207]\n\t" \
    "ds_read_b128 v[228:231], v253 offset:3072\n\t" \
    "v_mfma_f32_16x16x32_bf16 v[208:211], v[12:15], v[232:235], v[208:211]\n\t" \
    "s_waitcnt lgkmcnt(3)\n\t" \
    "v_mfma_f32_16x16x32_bf16 v[216:219], v[4:7], v[236:239], v[216:219]\n\t" \
    "ds_read_b128 v[232:235], v253 offset:23552\n\t" \
    "v_mfma_f32_16x16x32_bf16 v[220:223], v[12:15], v[236:239], v[220:223]\n\t" \
    "s_waitcnt lgkmcnt(3)\n\t" \
    "v_mfma_f32_16x16x32_bf16 v[204:207], v[40:43], v[240:243], v[204:207]\n\t" \
    "ds_read_b128 v[236:239], v253 offset:4096\n\t" \
    "v_mfma_f32_16x16x32_bf16 v[208:211], v[48:51], v[240:243], v[208:211]\n\t" \
    "s_waitcnt lgkmcnt(3)\n\t" \
    "v_mfma_f32_16x16x32_bf16 v[216:219], v[40:43], v[224:227], v[216:219]\n\t" \
    "ds_read_b128 v[240:243], v253 offset:24576\n\t" \
    "v_mfma_f32_16x16x32_bf16 v[220:223], v[48:51], v[224:227], v[220:223]\n\t" \
    "s_waitcnt lgkmcnt(3)\n\t" \
    "v_mfma_f32_16x16x32_bf16 v[204:207], v[44:47], v[228:231], v[204:207]\n\t" \
    "ds_read_b128 v[224:227], v253 offset:5120\n\t" \
    "v_mfma_f32_16x16x32_bf16 v[208:211], v[52:55], v[228:231], v[208:211]\n\t" \
    "s_waitcnt lgkmcnt(3)\n\t" \
    "v_mfma_f32_16x16x32_bf16 v[216:219], v[44:47], v[232:235], v[216:219]\n\t" \
    "ds_read_b128 v[228:231], v253 offset:25600\n\t" \
    "v_mfma_f32_16x16x32_bf16 v[220:223], v[52:55], v[232:235], v[220:223]\n\t" \
    "s_waitcnt lgkmcnt(3)\n\t" \
    "v_mfma_f32_16x16x32_bf16 v[204:207], v[80:83], v[236:239], v[204:207]\n\t" \
    "ds_read_b128 v[232:235], v253 offset:6144\n\t" \
    "v_mfma_f32_16x16x32_bf16 v[208:211], v[88:91], v[236:239], v[208:211]\n\t" \
    "s_waitcnt lgkmcnt(3)\n\t" \
    "v_mfma_f32_16x16x32_bf16 v[216:219], v[80:83], v[240:243], v[216:219]\n\t" \
    "ds_read_b128 v[236:239], v253 offset:26624\n\t" \
    "v_mfma_f32_16x16x32_bf16 v[220:223], v[88:91], v[240:243], v[220:223]\n\t" \
    "s_waitcnt lgkmcnt(3)\n\t" \
    "v_mfma_f32_16x16x32_bf16 v[204:207], v[84:87], v[224:227], v[204:207]\n\t" \
    "ds_read_b128 v[240:243], v253 offset:7168\n\t" \
    "v_mfma_f32_16x16x32_bf16 v[208:211], v[92:95], v[224:227], v[208:211]\n\t" \
    "s_waitcnt lgkmcnt(3)\n\t" \
    "v_mfma_f32_16x16x32_bf16 v[216:219], v[84:87], v[228:231], v[216:219]\n\t" \
    "ds_read_b128 v[224:227], v253 offset:27648\n\t" \
    "v_mfma_f32_16x16x32_bf16 v[220:223], v[92:95], v[228:231], v[220:223]\n\t" \
    "s_waitcnt lgkmcnt(3)\n\t" \
    "v_mfma_f32_16x16x32_bf16 v[204:207], v[120:123], v[232:235], v[204:207]\n\t" \
    "ds_read_b128 v[228:231], v253 offset:8192\n\t" \
    "v_mfma_f32_16x16x32_bf16 v[208:211], v[128:131], v[232:235], v[208:211]\n\t" \
    "s_waitcnt lgkmcnt(3)\n\t" \
    "v_mfma_f32_16x16x32_bf16 v[216:219], v[120:123], v[236:239], v[216:219]\n\t" \
    "ds_read_b128 v[232:235], v253 offset:28672\n\t" \
    "v_mfma_f32_16x16x32_bf16 v[220:223], v[128:131], v[236:239], v[220:223]\n\t" \
    "s_waitcnt lgkmcnt(3)\n\t" \
    "v_mfma_f32_16x16x32_bf16 v[204:207], v[124:127], v[240:243], v[204:207]\n\t" \
    "ds_read_b128 v[236:239], v253 offset:9216\n\t" \
    "v_mfma_f32_16x16x32_bf16 v[208:211], v[132:135], v[240:243], v[208:211]\n\t" \
    "s_waitcnt lgkmcnt(3)\n\t" \
    "v_mfma_f32_16x16x32_bf16 v[216:219], v[124:127], v[224:227], v[216:219]\n\t" \
    "ds_read_b128 v[240:243], v253 offset:29696\n\t" \
    "v_mfma_f32_16x16x32_bf16 v[220:223], v[132:135], v[224:227], v[220:223]\n\t" \
    "s_waitcnt lgkmcnt(3)\n\t" \
    "v_mfma_f32_16x16x32_bf16 v[204:207], v[160:163], v[228:231], v[204:207]\n\t" \
    "ds_read_b128 v[224:227], v253 offset:10240\n\t" \
    "v_mfma_f32_16x16x32_bf16 v[208:211], v[168:171], v[228:231], v[208:211]\n\t" \
    "s_waitcnt lgkmcnt(3)\n\t" \
    "v_mfma_f32_16x16x32_bf16 v[216:219], v[160:163], v[232:235], v[216:219]\n\t" \
    "ds_read_b128 v[228:231], v253 offset:30720\n\t" \
    "v_mfma_f32_16x16x32_bf16 v[220:223], v[168:171], v[232:235], v[220:223]\n\t" \
    "s_waitcnt lgkmcnt(3)\n\t" \
    "v_mfma_f32_16x16x32_bf16 v[204:207], v[164:167], v[236:239], v[204:207]\n\t" \
    "ds_read_b128 v[232:235], v253 offset:11264\n\t" \
    "v_mfma_f32_16x16x32_bf16 v[208:211], v[172:175], v[236:239], v[208:211]\n\t" \
    "s_waitcnt lgkmcnt(3)\n\t" \
    "v_mfma_f32_16x16x32_bf16 v[216:219], v[164:167], v[240:243], v[216:219]\n\t" \
    "ds_read_b128 v[236:239], v253 offset:31744\n\t" \
    "v_mfma_f32_16x16x32_bf16 v[220:223], v[172:175], v[240:243], v[220:223]\n\t" \
    "s_setprio 2\n\t" \
    "s_waitcnt lgkmcnt(0)\n\t" \
    "s_barrier \n\t" \
    "ds_read_b128 v[200:203], v253 offset:40960\n\t" \
    "ds_read_b128 v[212:215], v253 offset:41984\n\t" \
    "v_mfma_f32_16x16x32_bf16 v[204:207], v[24:27], v[224:227], v[204:207]\n\t" \
    "ds_read_b128 v[240:243], v253 offset:12288\n\t" \
    "v_mfma_f32_16x16x32_bf16 v[208:211], v[32:35], v[224:227], v[208:211]\n\t" \
    "s_waitcnt lgkmcnt(2)\n\t" \
    "v_mfma_f32_16x16x32_bf16 v[200:203], v[16:19], v[224:227], v[200:203]\n\t" \
    "v_mfma_f32_16x16x32_bf16 v[216:219], v[24:27], v[228:231], v[216:219]\n\t" \
    "ds_read_b128 v[224:227], v253 offset:32768\n\t" \
    "s_and_b32 s44, s40, 3\n\t" \
    "v_mfma_f32_16x16x32_bf16 v[220:223], v[32:35], v[228:231], v[220:223]\n\t" \
    "s_lshl_b32 s44, s44, 22\n\t" \
    "s_waitcnt lgkmcnt(2)\n\t" \
    "v_mfma_f32_16x16x32_bf16 v[212:215], v[16:19], v[228:231], v[212:215]\n\t" \
    "s_add_u32 s44, s44, %[mb]\n\t" \
    "v_mfma_f32_16x16x32_bf16 v[204:207], v[28:31], v[232:235], v[204:207]\n\t" \
    "ds_read_b128 v[228:231], v253 offset:13312\n\t" \
    "s_add_u32 s46, s44, 0x0\n\t" \
    "v_mfma_f32_16x16x32_bf16 v[208:211], v[36:39], v[232:235], v[208:211]\n\t" \
    "s_add_u32 s47, s44, 0x8400\n\t" \
    "v_mfma_f32_16x16x32_bf16 v[200:203], v[20:23], v[232:235], v[200:203]\n\t" \
    "s_add_u32 s48, s44, 0x400\n\t" \
    "v_mfma_f32_16x16x32_bf16 v[216:219], v[28:31], v[236:239], v[216:219]\n\t" \
    "ds_read_b128 v[232:235], v253 offset:33792\n\t" \
    "v_mfma_f32_16x16x32_bf16 v[220:223], v[36:39], v[236:239], v[220:223]\n\t" \
    "v_mfma_f32_16x16x32_bf16 v[212:215], v[20:23], v[236:239], v[212:215]\n\t" \
    "s_waitcnt lgkmcnt(3)\n\t" \
    "v_mfma_f32_16x16x32_bf16 v[204:207], v[64:67], v[240:243], v[204:207]\n\t" \
    "ds_read_b128 v[236:239], v253 offset:14336\n\t" \
    "v_mfma_f32_16x16x32_bf16 v[208:211], v[72:75], v[240:243], v[208:211]\n\t" \
    "v_mfma_f32_16x16x32_bf16 v[200:203], v[56:59], v[240:243], v[200:203]\n\t" \
    "s_waitcnt lgkmcnt(3)\n\t" \
    "v_mfma_f32_16x16x32_bf16 v[216:219], v[64:67], v[224:227], v[216:219]\n\t" \
    "ds_read_b128 v[240:243], v253 offset:34816\n\t" \
    "v_mfma_f32_16x16x32_bf16 v[220:223], v[72:75], v[224:227], v[220:223]\n\t" \
    "v_mfma_f32_16x16x32_bf16 v[212:215], v[56:59], v[224:227], v[212:215]\n\t" \
    "s_waitcnt lgkmcnt(3)\n\t" \
    "v_mfma_f32_16x16x32_bf16 v[204:207], v[68:71], v[228:231], v[204:207]\n\t" \
    "ds_read_b128 v[224:227], v253 offset:15360\n\t" \
    "v_mfma_f32_16x16x32_bf16 v[208:211], v[76:79], v[228:231], v[208:211]\n\t" \
    "v_mfma_f32_16x16x32_bf16 v[200:203], v[60:63], v[228:231], v[200:203]\n\t" \
    "s_waitcnt lgkmcnt(3)\n\t" \
    "v_mfma_f32_16x16x32_bf16 v[216:219], v[68:71], v[232:235], v[216:219]\n\t" \
    "ds_read_b128 v[228:231], v253 offset:35840\n\t" \
    "v_mfma_f32_16x16x32_bf16 v[220:223], v[76:79], v[232:235], v[220:223]\n\t" \
    "v_mfma_f32_16x16x32_bf16 v[212:215], v[60:63], v[232:235], v[212:215]\n\t" \
    "s_waitcnt lgkmcnt(3)\n\t" \
    "v_mfma_f32_16x16x32_bf16 v[204:207], v[104:107], v[236:239], v[204:207]\n\t" \
    "ds_read_b128 v[232:235], v253 offset:16384\n\t" \
    "v_mfma_f32_16x16x32_bf16 v[208:211], v[112:115], v[236:239], v[208:211]\n\t" \
    "v_mfma_f32_16x16x32_bf16 v[200:203], v[96:99], v[236:239], v[200:203]\n\t" \
    "s_waitcnt lgkmcnt(3)\n\t" \
    "v_mfma_f32_16x16x32_bf16 v[216:219], v[104:107], v[240:243], v[216:219]\n\t" \
    "ds_read_b128 v[236:239], v253 offset:17408\n\t" \
    "v_mfma_f32_16x16x32_bf16 v[220:223], v[112:115], v[240:243], v[220:223]\n\t" \
    "v_mfma_f32_16x16x32_bf16 v[212:215], v[96:99], v[240:243], v[212:215]\n\t" \
    "s_waitcnt lgkmcnt(3)\n\t" \
    "v_mfma_f32_16x16x32_bf16 v[204:207], v[108:111], v[224:227], v[204:207]\n\t" \
    "ds_read_b128 v[240:243], v253 offset:18432\n\t" \
    "v_mfma_f32_16x16x32_bf16 v[208:211], v[116:119], v[224:227], v[208:211]\n\t" \
    "v_mfma_f32_16x16x32_bf16 v[200:203], v[100:103], v[224:227], v[200:203]\n\t" \
    "s_waitcnt lgkmcnt(3)\n\t" \
    "v_mfma_f32_16x16x32_bf16 v[216:219], v[108:111], v[228:231], v[216:219]\n\t" \
    "ds_read_b128 v[224:227], v253 offset:19456\n\t" \
    "v_mfma_f32_16x16x32_bf16 v[220:223], v[116:119], v[228:231], v[220:223]\n\t" \
    "v_mfma_f32_16x16x32_bf16 v[212:215], v[100:103], v[228:231], v[212:215]\n\t" \
    "s_waitcnt lgkmcnt(3)\n\t" \
    "v_mfma_f32_16x16x32_bf16 v[204:207], v[144:147], v[232:235], v[204:207]\n\t" \
    "ds_read_b128 v[228:231], v253 offset:36864\n\t" \
    "v_mov_b64 v[244:245], 0\n\t" \
    "v_mfma_f32_16x16x32_bf16 v[208:211], v[152:155], v[232:235], v[208:211]\n\t" \
    "v_mov_b64 v[246:247], 0\n\t" \
    "v_mfma_f32_16x16x32_bf16 v[200:203], v[136:139], v[232:235], v[200:203]\n\t" \
    "v_mov_b64 v[248:249], 0\n\t" \
    "s_waitcnt lgkmcnt(3)\n\t" \
    "v_mfma_f32_16x16x32_bf16 v[204:207], v[148:151], v[236:239], v[204:207]\n\t" \
    "ds_read_b128 v[232:235], v253 offset:37888\n\t" \
    "v_mov_b64 v[250:251], 0\n\t" \
    "v_mfma_f32_16x16x32_bf16 v[208:211], v[156:159], v[236:239], v[208:211]\n\t" \
    "v_mfma_f32_16x16x32_bf16 v[200:203], v[140:143], v[236:239], v[200:203]\n\t" \
    "s_waitcnt lgkmcnt(3)\n\t" \
    "v_mfma_f32_16x16x32_bf16 v[204:207], v[184:187], v[240:243], v[204:207]\n\t" \
    "ds_read_b128 v[236:239], v253 offset:38912\n\t" \
    "v_mfma_f32_16x16x32_bf16 v[208:211], v[192:195], v[240:243], v[208:211]\n\t" \
    "v_mfma_f32_16x16x32_bf16 v[200:203], v[176:179], v[240:243], v[200:203]\n\t" \
    "s_waitcnt lgkmcnt(3)\n\t" \
    "v_mfma_f32_16x16x32_bf16 v[204:207], v[188:191], v[224:227], v[204:207]\n\t" \
    "ds_read_b128 v[240:243], v253 offset:39936\n\t" \
    "v_mfma_f32_16x16x32_bf16 v[208:211], v[196:199], v[224:227], v[208:211]\n\t" \
    "v_mfma_f32_16x16x32_bf16 v[200:203], v[180:183], v[224:227], v[200:203]\n\t" \
    "s_waitcnt lgkmcnt(3)\n\t" \
    "v_mfma_f32_16x16x32_bf16 v[216:219], v[144:147], v[228:231], v[216:219]\n\t" \
    "s_waitcnt lgkmcnt(2)\n\t" \
    "v_mfma_f32_16x16x32_bf16 v[216:219], v[148:151], v[232:235], v[216:219]\n\t" \
    "v_cvt_pk_bf16_f32 v204, v204, v208\n\t" \
    "v_cvt_pk_bf16_f32 v205, v205, v209\n\t" \
    "s_waitcnt lgkmcnt(1)\n\t" \
    "v_mfma_f32_16x16x32_bf16 v[216:219], v[184:187], v[236:239], v[216:219]\n\t" \
    "v_cvt_pk_bf16_f32 v206, v206, v210\n\t" \
    "v_cvt_pk_bf16_f32 v207, v207, v211\n\t" \
    "s_waitcnt lgkmcnt(0)\n\t" \
    "v_mfma_f32_16x16x32_bf16 v[216:219], v[188:191], v[240:243], v[216:219]\n\t" \
    "ds_write_b128 v254, v[204:207] offset:8192\n\t" \
    "v_mfma_f32_16x16x32_bf16 v[220:223], v[152:155], v[228:231], v[220:223]\n\t" \
    "v_mfma_f32_16x16x32_bf16 v[220:223], v[156:159], v[232:235], v[220:223]\n\t" \
    "v_cvt_pk_bf16_f32 v200, v200, v201\n\t" \
    "v_mfma_f32_16x16x32_bf16 v[220:223], v[192:195], v[236:239], v[220:223]\n\t" \
    "v_cvt_pk_bf16_f32 v201, v202, v203\n\t" \
    "ds_write_b64 v254, v[200:201] offset:6144\n\t" \
    "v_mfma_f32_16x16x32_bf16 v[220:223], v[196:199], v[240:243], v[220:223]\n\t" \
    "v_mfma_f32_16x16x32_bf16 v[212:215], v[136:139], v[228:231], v[212:215]\n\t" \
    "v_mfma_f32_16x16x32_bf16 v[212:215], v[140:143], v[232:235], v[212:215]\n\t" \
    "ds_read_b128 v[200:203], v254 offset:30720\n\t" \
    "ds_read_b128 v[204:207], v254 offset:31744\n\t" \
    "v_mfma_f32_16x16x32_bf16 v[212:215], v[176:179], v[236:239], v[212:215]\n\t" \
    "v_mov_b64 v[208:209], 0\n\t" \
    "v_mov_b64 v[210:211], 0\n\t" \
    "v_mfma_f32_16x16x32_bf16 v[212:215], v[180:183], v[240:243], v[212:215]\n\t" \
    "v_cvt_pk_bf16_f32 v216, v216, v220\n\t" \
    "v_cvt_pk_bf16_f32 v217, v217, v221\n\t" \
    "s_setprio 0\n\t" \
    "v_cvt_pk_bf16_f32 v218, v218, v222\n\t" \
    "v_cvt_pk_bf16_f32 v219, v219, v223\n\t" \
    "ds_write_b128 v254, v[216:219] offset:14336\n\t" \
    "ds_read_b128 v[216:219], v254 offset:32768\n\t" \
    "ds_read_b128 v[220:223], v254 offset:33792\n\t" \
    "s_nop 0\n\t" \
    "v_cvt_pk_bf16_f32 v212, v212, v213\n\t" \
    "v_cvt_pk_bf16_f32 v213, v214, v215\n\t" \
    "ds_write_b64 v254, v[212:213] offset:12288\n\t" \
    "s_sleep 3\n\t" \
    "s_mov_b32 m0, 0xa000\n\t" \
    "s_nop 0\n\t" \
    "buffer_load_dwordx4 v253, s[60:63], s46 offen sc1 lds\n\t" \
    "s_mov_b32 m0, 0xb400\n\t" \
    "s_nop 0\n\t" \
    "buffer_load_dwordx4 v253, s[60:63], s47 offen sc1 lds\n\t" \
    "s_mov_b32 m0, 0xa400\n\t" \
    "s_nop 0\n\t" \
    "buffer_load_dwordx4 v253, s[60:63], s48 offen sc1 lds\n\t" \
    "s_waitcnt lgkmcnt(0)\n\t" \
    "v_bfe_i32 v212, v253, 5, 1\n\t" \
    "v_and_b32 v212, 0xaaaa, v212\n\t" \
    "v_or_b32 v212, 0x4444, v212\n\t" \
    "v_and_b32 v213, 0xffff, v255\n\t" \
    "v_lshrrev_b32 v214, 16, v255\n\t" \
    ".Ls04_%=:\n\t" \
    "s_waitcnt vmcnt(0)\n\t" \
    "ds_read_b128 v[224:227], v253 offset:40960\n\t" \
    "ds_read_b128 v[228:231], v253 offset:46080\n\t" \
    "ds_read_b128 v[232:235], v253 offset:41984\n\t" \
    "s_waitcnt lgkmcnt(2)\n\t" \
    "v_max_u32 v215, v224, v225\n\t" \
    "v_max3_u32 v215, v226, v227, v215\n\t" \
    "s_waitcnt lgkmcnt(1)\n\t" \
    "v_max3_u32 v215, v228, v229, v215\n\t" \
    "v_max3_u32 v215, v230, v231, v215\n\t" \
    "s_waitcnt lgkmcnt(0)\n\t" \
    "v_max3_u32 v215, v232, v233, v215\n\t" \
    "v_max3_u32 v215, v234, v235, v215\n\t" \
    "v_cmp_eq_u32 vcc, -1, v215\n\t" \
    "s_cbranch_vccz .Ls05_%=\n\t" \
    "v_max_u32 v215, v224, v225\n\t" \
    "v_max3_u32 v215, v226, v227, v215\n\t" \
    "v_cmp_eq_u32 vcc, -1, v215\n\t" \
    "s_cbranch_vccz .Ls06_%=\n\t" \
    "s_mov_b32 m0, 0xa000\n\t" \
    "s_nop 0\n\t" \
    "buffer_load_dwordx4 v253, s[60:63], s46 offen sc1 lds\n\t" \
    ".Ls06_%=:\n\t" \
    "v_max_u32 v215, v228, v229\n\t" \
    "v_max3_u32 v215, v230, v231, v215\n\t" \
    "v_cmp_eq_u32 vcc, -1, v215\n\t" \
    "s_cbranch_vccz .Ls07_%=\n\t" \
    "s_mov_b32 m0, 0xb400\n\t" \
    "s_nop 0\n\t" \
    "buffer_load_dwordx4 v253, s[60:63], s47 offen sc1 lds\n\t" \
    ".Ls07_%=:\n\t" \
    "v_max_u32 v215, v232, v233\n\t" \
    "v_max3_u32 v215, v234, v235, v215\n\t" \
    "v_cmp_eq_u32 vcc, -1, v215\n\t" \
    "s_cbranch_vccz .Ls08_%=\n\t" \
    "s_mov_b32 m0, 0xa400\n\t" \
    "s_nop 0\n\t" \
    "buffer_load_dwordx4 v253, s[60:63], s48 offen sc1 lds\n\t" \
    ".Ls08_%=:\n\t" \
    "s_cmp_eq_u32 s41, 0\n\t" \
    "s_cbranch_scc1 .Ls05_%=\n\t" \
    "s_sub_u32 s41, s41, 1\n\t" \
    "s_branch .Ls04_%=\n\t" \
    ".Ls05_%=:\n\t" \
    "s_setprio 3\n\t" \
    "s_waitcnt lgkmcnt(0)\n\t" \
    "s_barrier \n\t" \
    "ds_read_b128 v[236:239], v253 offset:47104\n\t" \
    "ds_read_b128 v[240:243], v253 offset:43008\n\t" \
    "v_smfmac_f32_16x16x64_bf16 v[244:247], v[224:227], v[0:7], v212\n\t" \
    "v_smfmac_f32_16x16x64_bf16 v[248:251], v[224:227], v[8:15], v212\n\t" \
    "v_smfmac_f32_16x16x64_bf16 v[208:211], v[224:227], v[200:207], v212\n\t" \
    "ds_read_b128 v[200:203], v254 offset:34816\n\t" \
    "ds_read_b128 v[204:207], v254 offset:35840\n\t" \
    "ds_read_b128 v[224:227], v253 offset:48128\n\t" \
    "v_smfmac_f32_16x16x64_bf16 v[208:211], v[228:231], v[16:23], v212\n\t" \
    "v_smfmac_f32_16x16x64_bf16 v[244:247], v[228:231], v[24:31], v212\n\t" \
    "v_smfmac_f32_16x16x64_bf16 v[248:251], v[228:231], v[32:39], v212\n\t" \
    "ds_read_b128 v[228:231], v253 offset:44032\n\t" \
    "v_smfmac_f32_16x16x64_bf16 v[208:211], v[232:235], v[216:223], v212\n\t" \
    "ds_read_b128 v[216:219], v254 offset:36864\n\t" \
    "ds_read_b128 v[220:223], v254 offset:37888\n\t" \
    "v_smfmac_f32_16x16x64_bf16 v[244:247], v[232:235], v[40:47], v212\n\t" \
    "v_smfmac_f32_16x16x64_bf16 v[248:251], v[232:235], v[48:55], v212\n\t" \
    "ds_read_b128 v[232:235], v253 offset:49152\n\t" \
    "s_waitcnt lgkmcnt(8)\n\t" \
    "v_smfmac_f32_16x16x64_bf16 v[208:211], v[236:239], v[56:63], v212\n\t" \
    "v_smfmac_f32_16x16x64_bf16 v[244:247], v[236:239], v[64:71], v212\n\t" \
    "v_smfmac_f32_16x16x64_bf16 v[248:251], v[236:239], v[72:79], v212\n\t" \
    "ds_read_b128 v[236:239], v253 offset:45056\n\t" \
    "s_waitcnt lgkmcnt(6)\n\t" \
    "v_smfmac_f32_16x16x64_bf16 v[208:211], v[240:243], v[200:207], v212\n\t" \
    "ds_read_b128 v[200:203], v254 offset:38912\n\t" \
    "ds_read_b128 v[204:207], v254 offset:39936\n\t" \
    "v_smfmac_f32_16x16x64_bf16 v[244:247], v[240:243], v[80:87], v212\n\t" \
    "v_smfmac_f32_16x16x64_bf16 v[248:251], v[240:243], v[88:95], v212\n\t" \
    "ds_read_b128 v[240:243], v253 offset:50176\n\t" \
    "s_waitcnt lgkmcnt(8)\n\t" \
    "v_smfmac_f32_16x16x64_bf16 v[208:211], v[224:227], v[96:103], v212\n\t" \
    "v_smfmac_f32_16x16x64_bf16 v[244:247], v[224:227], v[104:111], v212\n\t" \
    "v_smfmac_f32_16x16x64_bf16 v[248:251], v[224:227], v[112:119], v212\n\t" \
    "s_waitcnt lgkmcnt(5)\n\t" \
    "v_smfmac_f32_16x16x64_bf16 v[208:211], v[228:231], v[216:223], v212\n\t" \
    "s_waitcnt lgkmcnt(4)\n\t" \
    "v_smfmac_f32_16x16x64_bf16 v[208:211], v[232:235], v[136:143], v212\n\t" \
    "s_waitcnt lgkmcnt(1)\n\t" \
    "v_smfmac_f32_16x16x64_bf16 v[208:211], v[236:239], v[200:207], v212\n\t" \
    "s_waitcnt lgkmcnt(0)\n\t" \
    "v_smfmac_f32_16x16x64_bf16 v[208:211], v[240:243], v[176:183], v212\n\t" \
    "v_smfmac_f32_16x16x64_bf16 v[244:247], v[228:231], v[120:127], v212\n\t" \
    "v_smfmac_f32_16x16x64_bf16 v[244:247], v[232:235], v[144:151], v212\n\t" \
    "v_smfmac_f32_16x16x64_bf16 v[244:247], v[236:239], v[160:167], v212\n\t" \
    "v_add_f32 v208, v208, v210\n\t" \
    "v_add_f32 v209, v209, v211\n\t" \
    "v_cvt_pk_bf16_f32 v208, v208, v209\n\t" \
    "v_smfmac_f32_16x16x64_bf16 v[244:247], v[240:243], v[184:191], v212\n\t" \
    "ds_write_b16 v213, v208 offset:0\n\t" \
    "ds_write_b16_d16_hi v213, v208 offset:16\n\t" \
    "v_smfmac_f32_16x16x64_bf16 v[248:251], v[228:231], v[128:135], v212\n\t" \
    "v_smfmac_f32_16x16x64_bf16 v[248:251], v[232:235], v[152:159], v212\n\t" \
    "v_smfmac_f32_16x16x64_bf16 v[248:251], v[236:239], v[168:175], v212\n\t" \
    "v_add_f32 v244, v244, v246\n\t" \
    "v_add_f32 v245, v245, v247\n\t" \
    "v_smfmac_f32_16x16x64_bf16 v[248:251], v[240:243], v[192:199], v212\n\t" \
    "s_nop 7\n\t" \
    "v_add_f32 v248, v248, v250\n\t" \
    "v_add_f32 v249, v249, v251\n\t" \
    "v_cvt_pk_bf16_f32 v244, v244, v248\n\t" \
    "v_cvt_pk_bf16_f32 v245, v245, v249\n\t" \
    "ds_write2_b32 v214, v244, v245 offset1:4\n\t" \
    "s_setprio 0\n\t" \
    "s_sleep 3\n\t" \
    "ds_read_b128 v[200:203], v254 offset:18432\n\t" \
    "ds_read_b128 v[204:207], v254 offset:19456\n\t" \
    "ds_read_b128 v[214:217], v254 offset:24576\n\t" \
    "ds_read_b128 v[218:221], v254 offset:25600\n\t" \
    "ds_read_b128 v[222:225], v254 offset:20480\n\t" \
    "ds_read_b128 v[226:229], v254 offset:21504\n\t" \
    "ds_read_b128 v[230:233], v254 offset:26624\n\t" \
    "ds_read_b128 v[234:237], v254 offset:27648\n\t" \
    "v_and_b32 v213, 0xffffff7f, v254\n\t" \
    "v_mov_b64 v[242:243], 0\n\t" \
    "v_mov_b64 v[244:245], 0\n\t" \
    "v_mov_b64 v[246:247], 0\n\t" \
    "v_mov_b64 v[248:249], 0\n\t" \
    "v_mov_b64 v[250:251], 0\n\t" \
    "v_mov_b64 v[252:253], 0\n\t" \
    "s_and_b32 s44, s40, 3\n\t" \
    "s_lshl_b32 s44, s44, 22\n\t" \
    "s_add_u32 s50, s44, %[pb]\n\t" \
    "s_xor_b32 s51, s50, 0x4000\n\t" \
    "s_xor_b32 s52, s50, 0x1000\n\t" \
    "s_xor_b32 s53, s50, 0x5000\n\t" \
    "s_setprio 3\n\t" \
    "s_waitcnt lgkmcnt(0)\n\t" \
    "s_barrier \n\t" \
    "ds_read_b128 v[208:211], v254 offset:0\n\t" \
    "ds_read_b128 v[238:241], v254 offset:1024\n\t" \
    "s_waitcnt lgkmcnt(1)\n\t" \
    "v_smfmac_f32_16x16x64_bf16 v[242:245], v[208:211], v[200:207], v212\n\t" \
    "ds_read_b128 v[200:203], v213 offset:3072\n\t" \
    "ds_read_b128 v[204:207], v213 offset:3200\n\t" \
    "v_smfmac_f32_16x16x64_bf16 v[246:249], v[208:211], v[214:221], v212\n\t" \
    "ds_read_b128 v[214:217], v213 offset:4096\n\t" \
    "ds_read_b128 v[218:221], v213 offset:4224\n\t" \
    "s_waitcnt lgkmcnt(4)\n\t" \
    "v_smfmac_f32_16x16x64_bf16 v[242:245], v[238:241], v[222:229], v212\n\t" \
    "ds_read_b128 v[222:225], v254 offset:22528\n\t" \
    "ds_read_b128 v[226:229], v254 offset:23552\n\t" \
    "v_smfmac_f32_16x16x64_bf16 v[246:249], v[238:241], v[230:237], v212\n\t" \
    "ds_read_b128 v[230:233], v254 offset:28672\n\t" \
    "ds_read_b128 v[234:237], v254 offset:29696\n\t" \
    "s_waitcnt lgkmcnt(6)\n\t" \
    "v_smfmac_f32_16x16x64_bf16 v[250:253], v[208:211], v[200:207], v212\n\t" \
    "ds_read_b128 v[208:211], v254 offset:2048\n\t" \
    "ds_read_b128 v[200:203], v213 offset:5120\n\t" \
    "ds_read_b128 v[204:207], v213 offset:5248\n\t" \
    "s_waitcnt lgkmcnt(7)\n\t" \
    "v_smfmac_f32_16x16x64_bf16 v[250:253], v[238:241], v[214:221], v212\n\t" \
    "v_lshrrev_b32 v238, 3, v254\n\t" \
    "v_and_b32 v238, 0x6e, v238\n\t" \
    "v_lshrrev_b32 v239, 16, v255\n\t" \
    "s_waitcnt lgkmcnt(2)\n\t" \
    "v_smfmac_f32_16x16x64_bf16 v[242:245], v[208:211], v[222:229], v212\n\t" \
    "v_smfmac_f32_16x16x64_bf16 v[246:249], v[208:211], v[230:237], v212\n\t" \
    "s_waitcnt lgkmcnt(0)\n\t" \
    "v_smfmac_f32_16x16x64_bf16 v[250:253], v[208:211], v[200:207], v212\n\t" \
    "s_nop 1\n\t" \
    "v_add_f32 v242, v242, v244\n\t" \
    "v_add_f32 v243, v243, v245\n\t" \
    "s_nop 0\n\t" \
    "v_add_f32 v246, v246, v248\n\t" \
    "v_add_f32 v247, v247, v249\n\t" \
    "s_nop 0\n\t" \
    "v_add_f32 v250, v250, v252\n\t" \
    "v_add_f32 v251, v251, v253\n\t" \
    "v_max3_f32 v244, v242, v246, v250\n\t" \
    "v_max3_f32 v245, v243, v247, v251\n\t" \
    "s_nop 0\n\t" \
    "v_max_f32_dpp v244, v244, v244 quad_perm:[1,0,3,2] row_mask:0xf bank_mask:0xf\n\t" \
    "v_max_f32_dpp v245, v245, v245 quad_perm:[1,0,3,2] row_mask:0xf bank_mask:0xf\n\t" \
    "s_nop 0\n\t" \
    "v_max_f32_dpp v244, v244, v244 quad_perm:[2,3,0,1] row_mask:0xf bank_mask:0xf\n\t" \
    "v_max_f32_dpp v245, v245, v245 quad_perm:[2,3,0,1] row_mask:0xf bank_mask:0xf\n\t" \
    "s_nop 0\n\t" \
    "v_max_f32_dpp v244, v244, v244 row_half_mirror row_mask:0xf bank_mask:0xf\n\t" \
    "v_max_f32_dpp v245, v245, v245 row_half_mirror row_mask:0xf bank_mask:0xf\n\t" \
    "s_nop 0\n\t" \
    "v_max_f32_dpp v244, v244, v244 row_mirror row_mask:0xf bank_mask:0xf\n\t" \
    "v_max_f32_dpp v245, v245, v245 row_mirror row_mask:0xf bank_mask:0xf\n\t" \
    "v_mul_f32 v244, %[scl], v244\n\t" \
    "v_mul_f32 v245, %[scl], v245\n\t" \
    "v_fma_f32 v242, v242, %[scl], -v244\n\t" \
    "v_fma_f32 v243, v243, %[scl], -v245\n\t" \
    "v_fma_f32 v246, v246, %[scl], -v244\n\t" \
    "v_fma_f32 v247, v247, %[scl], -v245\n\t" \
    "v_exp_f32 v242, v242\n\t" \
    "v_exp_f32 v243, v243\n\t" \
    "v_fma_f32 v250, v250, %[scl], -v244\n\t" \
    "v_fma_f32 v251, v251, %[scl], -v245\n\t" \
    "v_exp_f32 v246, v246\n\t" \
    "v_exp_f32 v247, v247\n\t" \
    "v_exp_f32 v250, v250\n\t" \
    "v_exp_f32 v251, v251\n\t" \
    "s_wakeup\n\t" \
    "v_add_f32 v244, v242, v246\n\t" \
    "v_add_f32 v245, v243, v247\n\t" \
    "v_fma_f32 v244, v250, 0.5, v244\n\t" \
    "v_fma_f32 v245, v251, 0.5, v245\n\t" \
    "s_nop 0\n\t" \
    "v_add_f32_dpp v244, v244, v244 quad_perm:[1,0,3,2] row_mask:0xf bank_mask:0xf\n\t" \
    "v_add_f32_dpp v245, v245, v245 quad_perm:[1,0,3,2] row_mask:0xf bank_mask:0xf\n\t" \
    "s_nop 0\n\t" \
    "v_add_f32_dpp v244, v244, v244 quad_perm:[2,3,0,1] row_mask:0xf bank_mask:0xf\n\t" \
    "v_add_f32_dpp v245, v245, v245 quad_perm:[2,3,0,1] row_mask:0xf bank_mask:0xf\n\t" \
    "s_nop 0\n\t" \
    "v_add_f32_dpp v244, v244, v244 row_half_mirror row_mask:0xf bank_mask:0xf\n\t" \
    "v_add_f32_dpp v245, v245, v245 row_half_mirror row_mask:0xf bank_mask:0xf\n\t" \
    "s_nop 0\n\t" \
    "v_add_f32_dpp v244, v244, v244 row_mirror row_mask:0xf bank_mask:0xf\n\t" \
    "v_add_f32_dpp v245, v245, v245 row_mirror row_mask:0xf bank_mask:0xf\n\t" \
    "v_rcp_f32 v244, v244\n\t" \
    "v_rcp_f32 v245, v245\n\t" \
    "v_mul_f32 v250, v250, v244\n\t" \
    "v_mul_f32 v251, v251, v245\n\t" \
    "v_mul_f32 v242, v242, v244\n\t" \
    "v_mul_f32 v243, v243, v245\n\t" \
    "v_mul_f32 v246, v246, v244\n\t" \
    "v_mul_f32 v247, v247, v245\n\t" \
    "v_cvt_pk_bf16_f32 v250, |v250|, |v251|\n\t" \
    "ds_write_b16 v238, v250 offset:52096\n\t" \
    "ds_write_b16_d16_hi v238, v250 offset:52112\n\t" \
    "v_cvt_pk_bf16_f32 v242, |v242|, |v246|\n\t" \
    "v_cvt_pk_bf16_f32 v243, |v243|, |v247|\n\t" \
    "ds_write2_b32 v239, v242, v243 offset1:4\n\t" \
    "ds_read_b128 v[200:203], v254 offset:896\n\t" \
    "v_and_b32 v253, 0x3ff, v254\n\t" \
    "s_mov_b32 exec_hi, 0xff\n\t" \
    "s_waitcnt lgkmcnt(0)\n\t" \
    "buffer_store_dwordx4 v[200:203], v253, s[60:63], s50 offen sc1\n\t" \
    "buffer_store_dwordx4 v[200:203], v253, s[60:63], s51 offen sc1\n\t" \
    "buffer_store_dwordx4 v[200:203], v253, s[60:63], s52 offen sc1\n\t" \
    "buffer_store_dwordx4 v[200:203], v253, s[60:63], s53 offen sc1\n\t" \
    "s_mov_b32 exec_hi, -1\n\t" \
    "s_setprio 0\n\t" \
    "ds_read_b128 v[224:227], v253 offset:0\n\t" \
    "ds_read_b128 v[228:231], v253 offset:20480\n\t" \
    "ds_read_b128 v[232:235], v253 offset:1024\n\t" \
    "ds_read_b128 v[236:239], v253 offset:21504\n\t" \
    "s_add_u32 s40, s40, 1\n\t" \
    "s_cmp_lg_u32 s40, 64\n\t" \
    "s_cbranch_scc1 .Ls03_%=\n\t" \
    "s_branch .Lend%=\n\t" \
    ".Lend%=:\n\t" \
    "s_waitcnt vmcnt(0) lgkmcnt(0)\n\t" \
    "s_mov_b32 %[bud], s41\n\t"
#define ASM_V_LOOP \
    "s_mov_b64 s[60:61], %[ws]\n\t" \
    "s_mov_b32 s62, %[sz]\n\t" \
    "s_mov_b32 s63, 0x20000\n\t" \
    "s_mov_b32 s40, 0\n\t" \
    "s_mov_b32 s41, 0x20000\n\t" \
    "s_cmp_eq_u32 %[wave], 0\n\t" \
    "s_cbranch_scc1 .Lv0_%=\n\t" \
    "s_cmp_eq_u32 %[wave], 1\n\t" \
    "s_cbranch_scc1 .Lv1_%=\n\t" \
    "s_cmp_eq_u32 %[wave], 2\n\t" \
    "s_cbranch_scc1 .Lv2_%=\n\t" \
    "s_cmp_eq_u32 %[wave], 3\n\t" \
    "s_cbranch_scc1 .Lv3_%=\n\t" \
    ".Lv4_%=:\n\t" \
    "s_barrier \n\t" \
    "s_waitcnt vmcnt(15)\n\t" \
    "ds_read_b64_tr_b16 v[200:201], v245 offset:0\n\t" \
    "ds_read_b64_tr_b16 v[16:17], v245 offset:32\n\t" \
    "ds_read_b64_tr_b16 v[208:209], v245 offset:64\n\t" \
    "ds_read_b64_tr_b16 v[56:57], v245 offset:96\n\t" \
    "ds_read_b64_tr_b16 v[216:217], v245 offset:128\n\t" \
    "ds_read_b64_tr_b16 v[96:97], v245 offset:160\n\t" \
    "ds_read_b64_tr_b16 v[224:225], v245 offset:192\n\t" \
    "ds_read_b64_tr_b16 v[136:137], v245 offset:224\n\t" \
    "ds_read_b64_tr_b16 v[232:233], v245 offset:256\n\t" \
    "ds_read_b64_tr_b16 v[176:177], v245 offset:288\n\t" \
    "s_waitcnt lgkmcnt(0)\n\t" \
    "s_add_u32 m0, %[wm], 0x0\n\t" \
    "s_mov_b32 s44, 0x14000\n\t" \
    "buffer_load_dwordx4 v240, %[wr], s44 offen offset:0 nt lds\n\t" \
    "buffer_load_dwordx4 v241, %[wr], s44 offen offset:1024 nt lds\n\t" \
    "buffer_load_dwordx4 v242, %[wr], s44 offen offset:2048 nt lds\n\t" \
    "buffer_load_dwordx4 v243, %[wr], s44 offen offset:3072 nt lds\n\t" \
    "s_add_u32 m0, %[wm], 0x1000\n\t" \
    "s_nop 0\n\t" \
    "buffer_load_dwordx4 v244, %[wr], s44 offen nt lds\n\t" \
    "s_barrier \n\t" \
    "s_barrier \n\t" \
    "s_waitcnt vmcnt(15)\n\t" \
    "ds_read_b64_tr_b16 v[204:205], v245 offset:5120\n\t" \
    "ds_read_b64_tr_b16 v[20:21], v245 offset:5152\n\t" \
    "ds_read_b64_tr_b16 v[212:213], v245 offset:5184\n\t" \
    "ds_read_b64_tr_b16 v[60:61], v245 offset:5216\n\t" \
    "ds_read_b64_tr_b16 v[220:221], v245 offset:5248\n\t" \
    "ds_read_b64_tr_b16 v[100:101], v245 offset:5280\n\t" \
    "ds_read_b64_tr_b16 v[228:229], v245 offset:5312\n\t" \
    "ds_read_b64_tr_b16 v[140:141], v245 offset:5344\n\t" \
    "ds_read_b64_tr_b16 v[236:237], v245 offset:5376\n\t" \
    "ds_read_b64_tr_b16 v[180:181], v245 offset:5408\n\t" \
    "s_waitcnt lgkmcnt(0)\n\t" \
    "s_add_u32 m0, %[wm], 0x1400\n\t" \
    "s_mov_b32 s44, 0x19000\n\t" \
    "buffer_load_dwordx4 v240, %[wr], s44 offen offset:0 nt lds\n\t" \
    "buffer_load_dwordx4 v241, %[wr], s44 offen offset:1024 nt lds\n\t" \
    "buffer_load_dwordx4 v242, %[wr], s44 offen offset:2048 nt lds\n\t" \
    "buffer_load_dwordx4 v243, %[wr], s44 offen offset:3072 nt lds\n\t" \
    "s_add_u32 m0, %[wm], 0x2400\n\t" \
    "s_nop 0\n\t" \
    "buffer_load_dwordx4 v244, %[wr], s44 offen nt lds\n\t" \
    "s_barrier \n\t" \
    "s_barrier \n\t" \
    "s_waitcnt vmcnt(15)\n\t" \
    "ds_read_b64_tr_b16 v[0:1], v245 offset:10240\n\t" \
    "ds_read_b64_tr_b16 v[24:25], v245 offset:10272\n\t" \
    "ds_read_b64_tr_b16 v[40:41], v245 offset:10304\n\t" \
    "ds_read_b64_tr_b16 v[64:65], v245 offset:10336\n\t" \
    "ds_read_b64_tr_b16 v[80:81], v245 offset:10368\n\t" \
    "ds_read_b64_tr_b16 v[104:105], v245 offset:10400\n\t" \
    "ds_read_b64_tr_b16 v[120:121], v245 offset:10432\n\t" \
    "ds_read_b64_tr_b16 v[144:145], v245 offset:10464\n\t" \
    "ds_read_b64_tr_b16 v[160:161], v245 offset:10496\n\t" \
    "ds_read_b64_tr_b16 v[184:185], v245 offset:10528\n\t" \
    "s_waitcnt lgkmcnt(0)\n\t" \
    "s_add_u32 m0, %[wm], 0x2800\n\t" \
    "s_mov_b32 s44, 0x1e000\n\t" \
    "buffer_load_dwordx4 v240, %[wr], s44 offen offset:0 nt lds\n\t" \
    "buffer_load_dwordx4 v241, %[wr], s44 offen offset:1024 nt lds\n\t" \
    "buffer_load_dwordx4 v242, %[wr], s44 offen offset:2048 nt lds\n\t" \
    "buffer_load_dwordx4 v243, %[wr], s44 offen offset:3072 nt lds\n\t" \
    "s_add_u32 m0, %[wm], 0x3800\n\t" \
    "s_nop 0\n\t" \
    "buffer_load_dwordx4 v244, %[wr], s44 offen nt lds\n\t" \
    "s_barrier \n\t" \
    "s_waitcnt vmcnt(15)\n\t" \
    "ds_read_b64_tr_b16 v[4:5], v245 offset:15360\n\t" \
    "ds_read_b64_tr_b16 v[28:29], v245 offset:15392\n\t" \
    "ds_read_b64_tr_b16 v[44:45], v245 offset:15424\n\t" \
    "ds_read_b64_tr_b16 v[68:69], v245 offset:15456\n\t" \
    "ds_read_b64_tr_b16 v[84:85], v245 offset:15488\n\t" \
    "ds_read_b64_tr_b16 v[108:109], v245 offset:15520\n\t" \
    "ds_read_b64_tr_b16 v[124:125], v245 offset:15552\n\t" \
    "ds_read_b64_tr_b16 v[148:149], v245 offset:15584\n\t" \
    "ds_read_b64_tr_b16 v[164:165], v245 offset:15616\n\t" \
    "ds_read_b64_tr_b16 v[188:189], v245 offset:15648\n\t" \
    "s_waitcnt lgkmcnt(0)\n\t" \
    "s_add_u32 m0, %[wm], 0x3c00\n\t" \
    "s_mov_b32 s44, 0x23000\n\t" \
    "buffer_load_dwordx4 v240, %[wr], s44 offen offset:0 nt lds\n\t" \
    "buffer_load_dwordx4 v241, %[wr], s44 offen offset:1024 nt lds\n\t" \
    "buffer_load_dwordx4 v242, %[wr], s44 offen offset:2048 nt lds\n\t" \
    "buffer_load_dwordx4 v243, %[wr], s44 offen offset:3072 nt lds\n\t" \
    "s_add_u32 m0, %[wm], 0x4c00\n\t" \
    "s_nop 0\n\t" \
    "buffer_load_dwordx4 v244, %[wr], s44 offen nt lds\n\t" \
    "s_barrier \n\t" \
    "s_barrier \n\t" \
    "s_waitcnt vmcnt(15)\n\t" \
    "ds_read_b64_tr_b16 v[8:9], v245 offset:0\n\t" \
    "ds_read_b64_tr_b16 v[32:33], v245 offset:32\n\t" \
    "ds_read_b64_tr_b16 v[48:49], v245 offset:64\n\t" \
    "ds_read_b64_tr_b16 v[72:73], v245 offset:96\n\t" \
    "ds_read_b64_tr_b16 v[88:89], v245 offset:128\n\t" \
    "ds_read_b64_tr_b16 v[112:113], v245 offset:160\n\t" \
    "ds_read_b64_tr_b16 v[128:129], v245 offset:192\n\t" \
    "ds_read_b64_tr_b16 v[152:153], v245 offset:224\n\t" \
    "ds_read_b64_tr_b16 v[168:169], v245 offset:256\n\t" \
    "ds_read_b64_tr_b16 v[192:193], v245 offset:288\n\t" \
    "s_waitcnt lgkmcnt(0)\n\t" \
    "s_add_u32 m0, %[wm], 0x0\n\t" \
    "s_mov_b32 s44, 0x28000\n\t" \
    "buffer_load_dwordx4 v240, %[wr], s44 offen offset:0 nt lds\n\t" \
    "buffer_load_dwordx4 v241, %[wr], s44 offen offset:1024 nt lds\n\t" \
    "buffer_load_dwordx4 v242, %[wr], s44 offen offset:2048 nt lds\n\t" \
    "buffer_load_dwordx4 v243, %[wr], s44 offen offset:3072 nt lds\n\t" \
    "s_add_u32 m0, %[wm], 0x1000\n\t" \
    "s_nop 0\n\t" \
    "buffer_load_dwordx4 v244, %[wr], s44 offen nt lds\n\t" \
    "s_barrier \n\t" \
    "s_barrier \n\t" \
    "s_waitcnt vmcnt(15)\n\t" \
    "ds_read_b64_tr_b16 v[12:13], v245 offset:5120\n\t" \
    "ds_read_b64_tr_b16 v[36:37], v245 offset:5152\n\t" \
    "ds_read_b64_tr_b16 v[52:53], v245 offset:5184\n\t" \
    "ds_read_b64_tr_b16 v[76:77], v245 offset:5216\n\t" \
    "ds_read_b64_tr_b16 v[92:93], v245 offset:5248\n\t" \
    "ds_read_b64_tr_b16 v[116:117], v245 offset:5280\n\t" \
    "ds_read_b64_tr_b16 v[132:133], v245 offset:5312\n\t" \
    "ds_read_b64_tr_b16 v[156:157], v245 offset:5344\n\t" \
    "ds_read_b64_tr_b16 v[172:173], v245 offset:5376\n\t" \
    "ds_read_b64_tr_b16 v[196:197], v245 offset:5408\n\t" \
    "s_waitcnt lgkmcnt(0)\n\t" \
    "s_add_u32 m0, %[wm], 0x1400\n\t" \
    "s_mov_b32 s44, 0x2d000\n\t" \
    "buffer_load_dwordx4 v240, %[wr], s44 offen offset:0 nt lds\n\t" \
    "buffer_load_dwordx4 v241, %[wr], s44 offen offset:1024 nt lds\n\t" \
    "buffer_load_dwordx4 v242, %[wr], s44 offen offset:2048 nt lds\n\t" \
    "buffer_load_dwordx4 v243, %[wr], s44 offen offset:3072 nt lds\n\t" \
    "s_add_u32 m0, %[wm], 0x2400\n\t" \
    "s_nop 0\n\t" \
    "buffer_load_dwordx4 v244, %[wr], s44 offen nt lds\n\t" \
    "s_barrier \n\t" \
    "s_waitcnt vmcnt(15)\n\t" \
    "ds_read_b64_tr_b16 v[202:203], v245 offset:10240\n\t" \
    "ds_read_b64_tr_b16 v[18:19], v245 offset:10272\n\t" \
    "ds_read_b64_tr_b16 v[210:211], v245 offset:10304\n\t" \
    "ds_read_b64_tr_b16 v[58:59], v245 offset:10336\n\t" \
    "ds_read_b64_tr_b16 v[218:219], v245 offset:10368\n\t" \
    "ds_read_b64_tr_b16 v[98:99], v245 offset:10400\n\t" \
    "ds_read_b64_tr_b16 v[226:227], v245 offset:10432\n\t" \
    "ds_read_b64_tr_b16 v[138:139], v245 offset:10464\n\t" \
    "ds_read_b64_tr_b16 v[234:235], v245 offset:10496\n\t" \
    "ds_read_b64_tr_b16 v[178:179], v245 offset:10528\n\t" \
    "v_mov_b32 v246, 0\n\t" \
    "buffer_load_dword v246, v246, s[60:63], %[mo] offen sc1\n\t" \
    "s_waitcnt lgkmcnt(0)\n\t" \
    "s_add_u32 m0, %[wm], 0x2800\n\t" \
    "s_mov_b32 s44, 0x32000\n\t" \
    "buffer_load_dwordx4 v240, %[wr], s44 offen offset:0 nt lds\n\t" \
    "buffer_load_dwordx4 v241, %[wr], s44 offen offset:1024 nt lds\n\t" \
    "buffer_load_dwordx4 v242, %[wr], s44 offen offset:2048 nt lds\n\t" \
    "buffer_load_dwordx4 v243, %[wr], s44 offen offset:3072 nt lds\n\t" \
    "s_add_u32 m0, %[wm], 0x3800\n\t" \
    "s_nop 0\n\t" \
    "buffer_load_dwordx4 v244, %[wr], s44 offen nt lds\n\t" \
    "s_barrier \n\t" \
    "s_barrier \n\t" \
    "s_waitcnt vmcnt(15)\n\t" \
    "ds_read_b64_tr_b16 v[206:207], v245 offset:15360\n\t" \
    "ds_read_b64_tr_b16 v[22:23], v245 offset:15392\n\t" \
    "ds_read_b64_tr_b16 v[214:215], v245 offset:15424\n\t" \
    "ds_read_b64_tr_b16 v[62:63], v245 offset:15456\n\t" \
    "ds_read_b64_tr_b16 v[222:223], v245 offset:15488\n\t" \
    "ds_read_b64_tr_b16 v[102:103], v245 offset:15520\n\t" \
    "ds_read_b64_tr_b16 v[230:231], v245 offset:15552\n\t" \
    "ds_read_b64_tr_b16 v[142:143], v245 offset:15584\n\t" \
    "ds_read_b64_tr_b16 v[238:239], v245 offset:15616\n\t" \
    "ds_read_b64_tr_b16 v[182:183], v245 offset:15648\n\t" \
    "s_waitcnt lgkmcnt(0)\n\t" \
    "s_add_u32 m0, %[wm], 0x3c00\n\t" \
    "s_mov_b32 s44, 0x37000\n\t" \
    "buffer_load_dwordx4 v240, %[wr], s44 offen offset:0 nt lds\n\t" \
    "buffer_load_dwordx4 v241, %[wr], s44 offen offset:1024 nt lds\n\t" \
    "buffer_load_dwordx4 v242, %[wr], s44 offen offset:2048 nt lds\n\t" \
    "buffer_load_dwordx4 v243, %[wr], s44 offen offset:3072 nt lds\n\t" \
    "s_add_u32 m0, %[wm], 0x4c00\n\t" \
    "s_nop 0\n\t" \
    "buffer_load_dwordx4 v244, %[wr], s44 offen nt lds\n\t" \
    "s_barrier \n\t" \
    "s_barrier \n\t" \
    "s_waitcnt vmcnt(15)\n\t" \
    "ds_read_b64_tr_b16 v[2:3], v245 offset:0\n\t" \
    "ds_read_b64_tr_b16 v[26:27], v245 offset:32\n\t" \
    "ds_read_b64_tr_b16 v[42:43], v245 offset:64\n\t" \
    "ds_read_b64_tr_b16 v[66:67], v245 offset:96\n\t" \
    "ds_read_b64_tr_b16 v[82:83], v245 offset:128\n\t" \
    "ds_read_b64_tr_b16 v[106:107], v245 offset:160\n\t" \
    "ds_read_b64_tr_b16 v[122:123], v245 offset:192\n\t" \
    "ds_read_b64_tr_b16 v[146:147], v245 offset:224\n\t" \
    "ds_read_b64_tr_b16 v[162:163], v245 offset:256\n\t" \
    "ds_read_b64_tr_b16 v[186:187], v245 offset:288\n\t" \
    "s_barrier \n\t" \
    "s_waitcnt vmcnt(10)\n\t" \
    "ds_read_b64_tr_b16 v[6:7], v245 offset:5120\n\t" \
    "ds_read_b64_tr_b16 v[30:31], v245 offset:5152\n\t" \
    "ds_read_b64_tr_b16 v[46:47], v245 offset:5184\n\t" \
    "ds_read_b64_tr_b16 v[70:71], v245 offset:5216\n\t" \
    "ds_read_b64_tr_b16 v[86:87], v245 offset:5248\n\t" \
    "ds_read_b64_tr_b16 v[110:111], v245 offset:5280\n\t" \
    "ds_read_b64_tr_b16 v[126:127], v245 offset:5312\n\t" \
    "ds_read_b64_tr_b16 v[150:151], v245 offset:5344\n\t" \
    "ds_read_b64_tr_b16 v[166:167], v245 offset:5376\n\t" \
    "ds_read_b64_tr_b16 v[190:191], v245 offset:5408\n\t" \
    "s_barrier \n\t" \
    "s_barrier \n\t" \
    "s_waitcnt vmcnt(5)\n\t" \
    "ds_read_b64_tr_b16 v[10:11], v245 offset:10240\n\t" \
    "ds_read_b64_tr_b16 v[34:35], v245 offset:10272\n\t" \
    "ds_read_b64_tr_b16 v[50:51], v245 offset:10304\n\t" \
    "ds_read_b64_tr_b16 v[74:75], v245 offset:10336\n\t" \
    "ds_read_b64_tr_b16 v[90:91], v245 offset:10368\n\t" \
    "ds_read_b64_tr_b16 v[114:115], v245 offset:10400\n\t" \
    "ds_read_b64_tr_b16 v[130:131], v245 offset:10432\n\t" \
    "ds_read_b64_tr_b16 v[154:155], v245 offset:10464\n\t" \
    "ds_read_b64_tr_b16 v[170:171], v245 offset:10496\n\t" \
    "ds_read_b64_tr_b16 v[194:195], v245 offset:10528\n\t" \
    ".Lv41_%=:\n\t" \
    "v_readfirstlane_b32 s43, v246\n\t" \
    "s_nop 0\n\t" \
    "s_cmp_lg_u32 s43, -1\n\t" \
    "s_cbranch_scc1 .Lv42_%=\n\t" \
    "s_cmp_eq_u32 s41, 0\n\t" \
    "s_cbranch_scc1 .Lv42_%=\n\t" \
    "s_sub_u32 s41, s41, 1\n\t" \
    "s_sleep 4\n\t" \
    "v_mov_b32 v246, 0\n\t" \
    "buffer_load_dword v246, v246, s[60:63], %[mo] offen sc1\n\t" \
    "s_waitcnt vmcnt(0)\n\t" \
    "s_branch .Lv41_%=\n\t" \
    ".Lv42_%=:\n\t" \
    "v_mov_b32 v246, 0x8000\n\t" \
    "s_mov_b64 exec, 1\n\t" \
    "buffer_store_dword v246, v246, s[60:63], %[mo] offen sc1\n\t" \
    "s_mov_b64 exec, -1\n\t" \
    "s_and_b32 s44, s43, 0x5000\n\t" \
    "s_xor_b32 %[mb], %[mb], s44\n\t" \
    "s_xor_b32 %[fl], %[fl], s44\n\t" \
    "s_sub_u32 %[fl], %[fl], 0xc800\n\t" \
    "s_xor_b32 %[pb], %[pb], s44\n\t" \
    "s_and_b32 s43, s43, 0x2000\n\t" \
    "s_xor_b32 s43, s43, %[lx]\n\t" \
    "s_add_u32 s60, s60, s43\n\t" \
    "s_addc_u32 s61, s61, 0\n\t" \
    "s_barrier \n\t" \
    "s_barrier \n\t" \
    "s_waitcnt vmcnt(0)\n\t" \
    "ds_read_b64_tr_b16 v[14:15], v245 offset:15360\n\t" \
    "ds_read_b64_tr_b16 v[38:39], v245 offset:15392\n\t" \
    "ds_read_b64_tr_b16 v[54:55], v245 offset:15424\n\t" \
    "ds_read_b64_tr_b16 v[78:79], v245 offset:15456\n\t" \
    "ds_read_b64_tr_b16 v[94:95], v245 offset:15488\n\t" \
    "ds_read_b64_tr_b16 v[118:119], v245 offset:15520\n\t" \
    "ds_read_b64_tr_b16 v[134:135], v245 offset:15552\n\t" \
    "ds_read_b64_tr_b16 v[158:159], v245 offset:15584\n\t" \
    "ds_read_b64_tr_b16 v[174:175], v245 offset:15616\n\t" \
    "ds_read_b64_tr_b16 v[198:199], v245 offset:15648\n\t" \
    "s_waitcnt lgkmcnt(0)\n\t" \
    "s_barrier \n\t" \
    "ds_write_b128 v254, v[200:203] offset:0\n\t" \
    "ds_write_b128 v254, v[204:207] offset:1024\n\t" \
    "ds_write_b128 v254, v[208:211] offset:2048\n\t" \
    "ds_write_b128 v254, v[212:215] offset:3072\n\t" \
    "ds_write_b128 v254, v[216:219] offset:4096\n\t" \
    "ds_write_b128 v254, v[220:223] offset:5120\n\t" \
    "ds_write_b128 v254, v[224:227] offset:6144\n\t" \
    "ds_write_b128 v254, v[228:231] offset:7168\n\t" \
    "ds_write_b128 v254, v[232:235] offset:8192\n\t" \
    "ds_write_b128 v254, v[236:239] offset:9216\n\t" \
    "s_waitcnt vmcnt(0) lgkmcnt(0)\n\t" \
    "s_barrier \n\t" \
    "v_mov_b64 v[200:201], 0\n\t" \
    "v_mov_b64 v[202:203], 0\n\t" \
    "ds_write_b128 v253, v[200:203] offset:3072\n\t" \
    "ds_write_b128 v253, v[200:203] offset:4096\n\t" \
    "v_bfe_i32 v252, v253, 5, 1\n\t" \
    "v_and_b32 v252, 0xaaaa, v252\n\t" \
    "v_or_b32 v252, 0x4444, v252\n\t" \
    ".p2align 8\n\t" \
    ".Lv43_%=:\n\t" \
    "ds_read_b128 v[248:251], v254 offset:0\n\t" \
    "s_and_b32 s44, s40, 3\n\t" \
    "s_lshl_b32 s44, s44, 22\n\t" \
    "s_add_u32 s54, s44, %[mb]\n\t" \
    "s_xor_b32 s55, s54, 0x4000\n\t" \
    "s_xor_b32 s56, s54, 0x1000\n\t" \
    "s_xor_b32 s57, s54, 0x5000\n\t" \
    "s_waitcnt lgkmcnt(0)\n\t" \
    "s_barrier \n\t" \
    "ds_read_b128 v[224:227], v253 offset:6144\n\t" \
    "ds_read_b128 v[232:235], v253 offset:8192\n\t" \
    "ds_read_b128 v[236:239], v253 offset:9216\n\t" \
    "ds_read_b128 v[240:243], v253 offset:10240\n\t" \
    "ds_read_b128 v[244:247], v253 offset:11264\n\t" \
    "ds_read_b128 v[228:231], v253 offset:7168\n\t" \
    "s_waitcnt lgkmcnt(5)\n\t" \
    "v_mfma_f32_16x16x32_bf16 v[200:203], v[224:227], v[16:19], 0\n\t" \
    "s_waitcnt lgkmcnt(4)\n\t" \
    "v_mfma_f32_16x16x32_bf16 v[200:203], v[232:235], v[24:27], v[200:203]\n\t" \
    "s_waitcnt lgkmcnt(3)\n\t" \
    "v_mfma_f32_16x16x32_bf16 v[200:203], v[236:239], v[28:31], v[200:203]\n\t" \
    "s_waitcnt lgkmcnt(2)\n\t" \
    "v_mfma_f32_16x16x32_bf16 v[200:203], v[240:243], v[32:35], v[200:203]\n\t" \
    "s_waitcnt lgkmcnt(1)\n\t" \
    "v_mfma_f32_16x16x32_bf16 v[200:203], v[244:247], v[36:39], v[200:203]\n\t" \
    "s_waitcnt lgkmcnt(0)\n\t" \
    "v_mfma_f32_16x16x32_bf16 v[200:203], v[228:231], v[20:23], v[200:203]\n\t" \
    "v_mfma_f32_16x16x32_bf16 v[204:207], v[224:227], v[248:251], 0\n\t" \
    "ds_read_b128 v[248:251], v254 offset:1024\n\t" \
    "v_mfma_f32_16x16x32_bf16 v[204:207], v[232:235], v[0:3], v[204:207]\n\t" \
    "v_mfma_f32_16x16x32_bf16 v[204:207], v[236:239], v[4:7], v[204:207]\n\t" \
    "v_mfma_f32_16x16x32_bf16 v[204:207], v[240:243], v[8:11], v[204:207]\n\t" \
    "v_mfma_f32_16x16x32_bf16 v[204:207], v[244:247], v[12:15], v[204:207]\n\t" \
    "s_waitcnt lgkmcnt(0)\n\t" \
    "v_mfma_f32_16x16x32_bf16 v[204:207], v[228:231], v[248:251], v[204:207]\n\t" \
    "ds_read_b128 v[248:251], v254 offset:2048\n\t" \
    "s_barrier \n\t" \
    "v_mfma_f32_16x16x32_bf16 v[208:211], v[224:227], v[56:59], 0\n\t" \
    "v_mfma_f32_16x16x32_bf16 v[208:211], v[232:235], v[64:67], v[208:211]\n\t" \
    "v_mfma_f32_16x16x32_bf16 v[208:211], v[236:239], v[68:71], v[208:211]\n\t" \
    "v_mfma_f32_16x16x32_bf16 v[208:211], v[240:243], v[72:75], v[208:211]\n\t" \
    "v_mfma_f32_16x16x32_bf16 v[208:211], v[244:247], v[76:79], v[208:211]\n\t" \
    "v_mfma_f32_16x16x32_bf16 v[208:211], v[228:231], v[60:63], v[208:211]\n\t" \
    "s_waitcnt lgkmcnt(0)\n\t" \
    "v_mfma_f32_16x16x32_bf16 v[212:215], v[224:227], v[248:251], 0\n\t" \
    "ds_read_b128 v[248:251], v254 offset:3072\n\t" \
    "ds_read_b128 v[224:227], v253 offset:12288\n\t" \
    "v_mfma_f32_16x16x32_bf16 v[212:215], v[232:235], v[40:43], v[212:215]\n\t" \
    "ds_read_b128 v[232:235], v253 offset:14336\n\t" \
    "v_mfma_f32_16x16x32_bf16 v[212:215], v[236:239], v[44:47], v[212:215]\n\t" \
    "ds_read_b128 v[236:239], v253 offset:15360\n\t" \
    "v_mfma_f32_16x16x32_bf16 v[212:215], v[240:243], v[48:51], v[212:215]\n\t" \
    "ds_read_b128 v[240:243], v253 offset:16384\n\t" \
    "v_mfma_f32_16x16x32_bf16 v[212:215], v[244:247], v[52:55], v[212:215]\n\t" \
    "ds_read_b128 v[244:247], v253 offset:17408\n\t" \
    "s_waitcnt lgkmcnt(5)\n\t" \
    "v_mfma_f32_16x16x32_bf16 v[212:215], v[228:231], v[248:251], v[212:215]\n\t" \
    "ds_read_b128 v[248:251], v254 offset:4096\n\t" \
    "ds_read_b128 v[228:231], v253 offset:13312\n\t" \
    "s_waitcnt lgkmcnt(6)\n\t" \
    "v_mfma_f32_16x16x32_bf16 v[216:219], v[224:227], v[16:19], 0\n\t" \
    "s_waitcnt lgkmcnt(5)\n\t" \
    "v_mfma_f32_16x16x32_bf16 v[216:219], v[232:235], v[24:27], v[216:219]\n\t" \
    "s_waitcnt lgkmcnt(4)\n\t" \
    "v_mfma_f32_16x16x32_bf16 v[216:219], v[236:239], v[28:31], v[216:219]\n\t" \
    "s_waitcnt lgkmcnt(3)\n\t" \
    "v_mfma_f32_16x16x32_bf16 v[216:219], v[240:243], v[32:35], v[216:219]\n\t" \
    "s_waitcnt lgkmcnt(2)\n\t" \
    "v_mfma_f32_16x16x32_bf16 v[216:219], v[244:247], v[36:39], v[216:219]\n\t" \
    "s_waitcnt lgkmcnt(0)\n\t" \
    "v_mfma_f32_16x16x32_bf16 v[216:219], v[228:231], v[20:23], v[216:219]\n\t" \
    "v_mfma_f32_16x16x32_bf16 v[220:223], v[224:227], v[248:251], 0\n\t" \
    "ds_read_b128 v[248:251], v254 offset:5120\n\t" \
    "v_mfma_f32_16x16x32_bf16 v[220:223], v[232:235], v[80:83], v[220:223]\n\t" \
    "v_mfma_f32_16x16x32_bf16 v[220:223], v[236:239], v[84:87], v[220:223]\n\t" \
    "v_mfma_f32_16x16x32_bf16 v[220:223], v[240:243], v[88:91], v[220:223]\n\t" \
    "v_cvt_pk_bf16_f32 v200, v200, v216\n\t" \
    "v_cvt_pk_bf16_f32 v201, v201, v217\n\t" \
    "v_mfma_f32_16x16x32_bf16 v[220:223], v[244:247], v[92:95], v[220:223]\n\t" \
    "v_cvt_pk_bf16_f32 v202, v202, v218\n\t" \
    "v_cvt_pk_bf16_f32 v203, v203, v219\n\t" \
    "s_waitcnt lgkmcnt(0)\n\t" \
    "v_mfma_f32_16x16x32_bf16 v[220:223], v[228:231], v[248:251], v[220:223]\n\t" \
    "ds_read_b128 v[248:251], v254 offset:0\n\t" \
    "s_add_u32 s44, s54, 0x1033800\n\t" \
    "v_mfma_f32_16x16x32_bf16 v[216:219], v[224:227], v[56:59], 0\n\t" \
    "buffer_store_dwordx4 v[200:203], v253, s[60:63], s44 offen sc1\n\t" \
    "v_mfma_f32_16x16x32_bf16 v[216:219], v[232:235], v[64:67], v[216:219]\n\t" \
    "v_mfma_f32_16x16x32_bf16 v[216:219], v[236:239], v[68:71], v[216:219]\n\t" \
    "v_mfma_f32_16x16x32_bf16 v[216:219], v[240:243], v[72:75], v[216:219]\n\t" \
    "v_mfma_f32_16x16x32_bf16 v[216:219], v[244:247], v[76:79], v[216:219]\n\t" \
    "v_mfma_f32_16x16x32_bf16 v[216:219], v[228:231], v[60:63], v[216:219]\n\t" \
    "s_waitcnt lgkmcnt(0)\n\t" \
    "v_mfma_f32_16x16x32_bf16 v[200:203], v[224:227], v[248:251], 0\n\t" \
    "ds_read_b128 v[248:251], v254 offset:1024\n\t" \
    "v_mfma_f32_16x16x32_bf16 v[200:203], v[232:235], v[0:3], v[200:203]\n\t" \
    "v_mfma_f32_16x16x32_bf16 v[200:203], v[236:239], v[4:7], v[200:203]\n\t" \
    "v_mfma_f32_16x16x32_bf16 v[200:203], v[240:243], v[8:11], v[200:203]\n\t" \
    "v_cvt_pk_bf16_f32 v208, v208, v216\n\t" \
    "v_cvt_pk_bf16_f32 v209, v209, v217\n\t" \
    "v_mfma_f32_16x16x32_bf16 v[200:203], v[244:247], v[12:15], v[200:203]\n\t" \
    "v_cvt_pk_bf16_f32 v210, v210, v218\n\t" \
    "v_cvt_pk_bf16_f32 v211, v211, v219\n\t" \
    "s_waitcnt lgkmcnt(0)\n\t" \
    "v_mfma_f32_16x16x32_bf16 v[200:203], v[228:231], v[248:251], v[200:203]\n\t" \
    "ds_read_b128 v[248:251], v254 offset:2048\n\t" \
    "s_add_u32 s44, s55, 0x10b3800\n\t" \
    "v_mfma_f32_16x16x32_bf16 v[216:219], v[224:227], v[96:99], 0\n\t" \
    "buffer_store_dwordx4 v[208:211], v253, s[60:63], s44 offen sc1\n\t" \
    "v_mfma_f32_16x16x32_bf16 v[216:219], v[232:235], v[104:107], v[216:219]\n\t" \
    "v_mfma_f32_16x16x32_bf16 v[216:219], v[236:239], v[108:111], v[216:219]\n\t" \
    "v_mfma_f32_16x16x32_bf16 v[216:219], v[240:243], v[112:115], v[216:219]\n\t" \
    "v_mfma_f32_16x16x32_bf16 v[216:219], v[244:247], v[116:119], v[216:219]\n\t" \
    "v_cvt_pk_bf16_f32 v204, v204, v200\n\t" \
    "v_mfma_f32_16x16x32_bf16 v[216:219], v[228:231], v[100:103], v[216:219]\n\t" \
    "v_cvt_pk_bf16_f32 v205, v205, v201\n\t" \
    "v_cvt_pk_bf16_f32 v206, v206, v202\n\t" \
    "s_waitcnt lgkmcnt(0)\n\t" \
    "v_mfma_f32_16x16x32_bf16 v[208:211], v[224:227], v[248:251], 0\n\t" \
    "ds_read_b128 v[248:251], v254 offset:3072\n\t" \
    "v_cvt_pk_bf16_f32 v207, v207, v203\n\t" \
    "v_mfma_f32_16x16x32_bf16 v[208:211], v[232:235], v[40:43], v[208:211]\n\t" \
    "v_mfma_f32_16x16x32_bf16 v[208:211], v[236:239], v[44:47], v[208:211]\n\t" \
    "s_add_u32 s44, s54, 0xff3800\n\t" \
    "v_mfma_f32_16x16x32_bf16 v[208:211], v[240:243], v[48:51], v[208:211]\n\t" \
    "buffer_store_dwordx4 v[204:207], v253, s[60:63], s44 offen sc1\n\t" \
    "v_mfma_f32_16x16x32_bf16 v[208:211], v[244:247], v[52:55], v[208:211]\n\t" \
    "s_waitcnt lgkmcnt(0)\n\t" \
    "v_mfma_f32_16x16x32_bf16 v[208:211], v[228:231], v[248:251], v[208:211]\n\t" \
    "ds_read_b128 v[248:251], v254 offset:6144\n\t" \
    "v_mfma_f32_16x16x32_bf16 v[200:203], v[224:227], v[136:139], 0\n\t" \
    "v_mfma_f32_16x16x32_bf16 v[200:203], v[232:235], v[144:147], v[200:203]\n\t" \
    "v_mfma_f32_16x16x32_bf16 v[200:203], v[236:239], v[148:151], v[200:203]\n\t" \
    "v_mfma_f32_16x16x32_bf16 v[200:203], v[240:243], v[152:155], v[200:203]\n\t" \
    "v_cvt_pk_bf16_f32 v212, v212, v208\n\t" \
    "v_cvt_pk_bf16_f32 v213, v213, v209\n\t" \
    "v_mfma_f32_16x16x32_bf16 v[200:203], v[244:247], v[156:159], v[200:203]\n\t" \
    "v_cvt_pk_bf16_f32 v214, v214, v210\n\t" \
    "v_cvt_pk_bf16_f32 v215, v215, v211\n\t" \
    "v_mfma_f32_16x16x32_bf16 v[200:203], v[228:231], v[140:143], v[200:203]\n\t" \
    "s_add_u32 s44, s55, 0x1073800\n\t" \
    "s_waitcnt lgkmcnt(0)\n\t" \
    "v_mfma_f32_16x16x32_bf16 v[204:207], v[224:227], v[248:251], 0\n\t" \
    "ds_read_b128 v[248:251], v254 offset:7168\n\t" \
    "ds_read_b128 v[224:227], v253 offset:6144\n\t" \
    "buffer_store_dwordx4 v[212:215], v253, s[60:63], s44 offen sc1\n\t" \
    "v_mfma_f32_16x16x32_bf16 v[204:207], v[232:235], v[120:123], v[204:207]\n\t" \
    "ds_read_b128 v[232:235], v253 offset:8192\n\t" \
    "v_mfma_f32_16x16x32_bf16 v[204:207], v[236:239], v[124:127], v[204:207]\n\t" \
    "ds_read_b128 v[236:239], v253 offset:9216\n\t" \
    "v_mfma_f32_16x16x32_bf16 v[204:207], v[240:243], v[128:131], v[204:207]\n\t" \
    "ds_read_b128 v[240:243], v253 offset:10240\n\t" \
    "v_mfma_f32_16x16x32_bf16 v[204:207], v[244:247], v[132:135], v[204:207]\n\t" \
    "ds_read_b128 v[244:247], v253 offset:11264\n\t" \
    "s_waitcnt lgkmcnt(5)\n\t" \
    "v_mfma_f32_16x16x32_bf16 v[204:207], v[228:231], v[248:251], v[204:207]\n\t" \
    "ds_read_b128 v[248:251], v254 offset:4096\n\t" \
    "ds_read_b128 v[228:231], v253 offset:7168\n\t" \
    "s_waitcnt lgkmcnt(6)\n\t" \
    "v_mfma_f32_16x16x32_bf16 v[208:211], v[224:227], v[96:99], 0\n\t" \
    "s_waitcnt lgkmcnt(5)\n\t" \
    "v_mfma_f32_16x16x32_bf16 v[208:211], v[232:235], v[104:107], v[208:211]\n\t" \
    "s_waitcnt lgkmcnt(4)\n\t" \
    "v_mfma_f32_16x16x32_bf16 v[208:211], v[236:239], v[108:111], v[208:211]\n\t" \
    "s_waitcnt lgkmcnt(3)\n\t" \
    "v_mfma_f32_16x16x32_bf16 v[208:211], v[240:243], v[112:115], v[208:211]\n\t" \
    "s_waitcnt lgkmcnt(2)\n\t" \
    "v_mfma_f32_16x16x32_bf16 v[208:211], v[244:247], v[116:119], v[208:211]\n\t" \
    "s_waitcnt lgkmcnt(0)\n\t" \
    "v_mfma_f32_16x16x32_bf16 v[208:211], v[228:231], v[100:103], v[208:211]\n\t" \
    "v_mfma_f32_16x16x32_bf16 v[212:215], v[224:227], v[248:251], 0\n\t" \
    "ds_read_b128 v[248:251], v254 offset:5120\n\t" \
    "v_mfma_f32_16x16x32_bf16 v[212:215], v[232:235], v[80:83], v[212:215]\n\t" \
    "v_mfma_f32_16x16x32_bf16 v[212:215], v[236:239], v[84:87], v[212:215]\n\t" \
    "v_mfma_f32_16x16x32_bf16 v[212:215], v[240:243], v[88:91], v[212:215]\n\t" \
    "v_cvt_pk_bf16_f32 v216, v208, v216\n\t" \
    "v_cvt_pk_bf16_f32 v217, v209, v217\n\t" \
    "v_mfma_f32_16x16x32_bf16 v[212:215], v[244:247], v[92:95], v[212:215]\n\t" \
    "v_cvt_pk_bf16_f32 v218, v210, v218\n\t" \
    "v_cvt_pk_bf16_f32 v219, v211, v219\n\t" \
    "s_waitcnt lgkmcnt(0)\n\t" \
    "v_mfma_f32_16x16x32_bf16 v[212:215], v[228:231], v[248:251], v[212:215]\n\t" \
    "ds_read_b128 v[248:251], v254 offset:6144\n\t" \
    "s_add_u32 s44, s56, 0x1133800\n\t" \
    "v_mfma_f32_16x16x32_bf16 v[208:211], v[224:227], v[176:179], 0\n\t" \
    "buffer_store_dwordx4 v[216:219], v253, s[60:63], s44 offen sc1\n\t" \
    "v_mfma_f32_16x16x32_bf16 v[208:211], v[232:235], v[184:187], v[208:211]\n\t" \
    "v_mfma_f32_16x16x32_bf16 v[208:211], v[236:239], v[188:191], v[208:211]\n\t" \
    "v_mfma_f32_16x16x32_bf16 v[208:211], v[240:243], v[192:195], v[208:211]\n\t" \
    "v_mfma_f32_16x16x32_bf16 v[208:211], v[244:247], v[196:199], v[208:211]\n\t" \
    "v_cvt_pk_bf16_f32 v220, v212, v220\n\t" \
    "v_mfma_f32_16x16x32_bf16 v[208:211], v[228:231], v[180:183], v[208:211]\n\t" \
    "v_cvt_pk_bf16_f32 v221, v213, v221\n\t" \
    "v_cvt_pk_bf16_f32 v222, v214, v222\n\t" \
    "s_waitcnt lgkmcnt(0)\n\t" \
    "v_mfma_f32_16x16x32_bf16 v[216:219], v[224:227], v[248:251], 0\n\t" \
    "ds_read_b128 v[248:251], v254 offset:7168\n\t" \
    "v_cvt_pk_bf16_f32 v223, v215, v223\n\t" \
    "v_mfma_f32_16x16x32_bf16 v[216:219], v[232:235], v[120:123], v[216:219]\n\t" \
    "v_mfma_f32_16x16x32_bf16 v[216:219], v[236:239], v[124:127], v[216:219]\n\t" \
    "s_add_u32 s44, s56, 0x10f3800\n\t" \
    "v_mfma_f32_16x16x32_bf16 v[216:219], v[240:243], v[128:131], v[216:219]\n\t" \
    "buffer_store_dwordx4 v[220:223], v253, s[60:63], s44 offen sc1\n\t" \
    "v_mfma_f32_16x16x32_bf16 v[216:219], v[244:247], v[132:135], v[216:219]\n\t" \
    "s_waitcnt lgkmcnt(0)\n\t" \
    "v_mfma_f32_16x16x32_bf16 v[216:219], v[228:231], v[248:251], v[216:219]\n\t" \
    "ds_read_b128 v[248:251], v254 offset:8192\n\t" \
    "v_mfma_f32_16x16x32_bf16 v[212:215], v[224:227], v[136:139], 0\n\t" \
    "v_mfma_f32_16x16x32_bf16 v[212:215], v[232:235], v[144:147], v[212:215]\n\t" \
    "v_mfma_f32_16x16x32_bf16 v[212:215], v[236:239], v[148:151], v[212:215]\n\t" \
    "v_mfma_f32_16x16x32_bf16 v[212:215], v[240:243], v[152:155], v[212:215]\n\t" \
    "v_cvt_pk_bf16_f32 v204, v216, v204\n\t" \
    "v_cvt_pk_bf16_f32 v205, v217, v205\n\t" \
    "v_mfma_f32_16x16x32_bf16 v[212:215], v[244:247], v[156:159], v[212:215]\n\t" \
    "v_cvt_pk_bf16_f32 v206, v218, v206\n\t" \
    "v_cvt_pk_bf16_f32 v207, v219, v207\n\t" \
    "v_mfma_f32_16x16x32_bf16 v[212:215], v[228:231], v[140:143], v[212:215]\n\t" \
    "s_add_u32 s44, s57, 0x1173800\n\t" \
    "s_waitcnt lgkmcnt(0)\n\t" \
    "v_mfma_f32_16x16x32_bf16 v[220:223], v[224:227], v[248:251], 0\n\t" \
    "ds_read_b128 v[248:251], v254 offset:9216\n\t" \
    "ds_read_b128 v[224:227], v253 offset:12288\n\t" \
    "buffer_store_dwordx4 v[204:207], v253, s[60:63], s44 offen sc1\n\t" \
    "v_mfma_f32_16x16x32_bf16 v[220:223], v[232:235], v[160:163], v[220:223]\n\t" \
    "ds_read_b128 v[232:235], v253 offset:14336\n\t" \
    "v_mfma_f32_16x16x32_bf16 v[220:223], v[236:239], v[164:167], v[220:223]\n\t" \
    "ds_read_b128 v[236:239], v253 offset:15360\n\t" \
    "v_mfma_f32_16x16x32_bf16 v[220:223], v[240:243], v[168:171], v[220:223]\n\t" \
    "ds_read_b128 v[240:243], v253 offset:16384\n\t" \
    "v_mfma_f32_16x16x32_bf16 v[220:223], v[244:247], v[172:175], v[220:223]\n\t" \
    "ds_read_b128 v[244:247], v253 offset:17408\n\t" \
    "v_cvt_pk_bf16_f32 v200, v212, v200\n\t" \
    "s_waitcnt lgkmcnt(5)\n\t" \
    "v_mfma_f32_16x16x32_bf16 v[220:223], v[228:231], v[248:251], v[220:223]\n\t" \
    "ds_read_b128 v[248:251], v254 offset:8192\n\t" \
    "ds_read_b128 v[228:231], v253 offset:13312\n\t" \
    "v_cvt_pk_bf16_f32 v201, v213, v201\n\t" \
    "v_cvt_pk_bf16_f32 v202, v214, v202\n\t" \
    "s_waitcnt lgkmcnt(6)\n\t" \
    "v_mfma_f32_16x16x32_bf16 v[216:219], v[224:227], v[176:179], 0\n\t" \
    "v_cvt_pk_bf16_f32 v203, v215, v203\n\t" \
    "s_waitcnt lgkmcnt(5)\n\t" \
    "v_mfma_f32_16x16x32_bf16 v[216:219], v[232:235], v[184:187], v[216:219]\n\t" \
    "s_waitcnt lgkmcnt(4)\n\t" \
    "v_mfma_f32_16x16x32_bf16 v[216:219], v[236:239], v[188:191], v[216:219]\n\t" \
    "s_add_u32 s44, s57, 0x11b3800\n\t" \
    "s_waitcnt lgkmcnt(3)\n\t" \
    "v_mfma_f32_16x16x32_bf16 v[216:219], v[240:243], v[192:195], v[216:219]\n\t" \
    "buffer_store_dwordx4 v[200:203], v253, s[60:63], s44 offen sc1\n\t" \
    "s_waitcnt lgkmcnt(2)\n\t" \
    "v_mfma_f32_16x16x32_bf16 v[216:219], v[244:247], v[196:199], v[216:219]\n\t" \
    "s_waitcnt lgkmcnt(0)\n\t" \
    "v_mfma_f32_16x16x32_bf16 v[216:219], v[228:231], v[180:183], v[216:219]\n\t" \
    "v_mfma_f32_16x16x32_bf16 v[204:207], v[224:227], v[248:251], 0\n\t" \
    "ds_read_b128 v[248:251], v254 offset:9216\n\t" \
    "v_mfma_f32_16x16x32_bf16 v[204:207], v[232:235], v[160:163], v[204:207]\n\t" \
    "v_mfma_f32_16x16x32_bf16 v[204:207], v[236:239], v[164:167], v[204:207]\n\t" \
    "v_mfma_f32_16x16x32_bf16 v[204:207], v[240:243], v[168:171], v[204:207]\n\t" \
    "v_cvt_pk_bf16_f32 v208, v208, v216\n\t" \
    "v_cvt_pk_bf16_f32 v209, v209, v217\n\t" \
    "v_mfma_f32_16x16x32_bf16 v[204:207], v[244:247], v[172:175], v[204:207]\n\t" \
    "v_cvt_pk_bf16_f32 v210, v210, v218\n\t" \
    "v_cvt_pk_bf16_f32 v211, v211, v219\n\t" \
    "s_waitcnt lgkmcnt(0)\n\t" \
    "v_mfma_f32_16x16x32_bf16 v[204:207], v[228:231], v[248:251], v[204:207]\n\t" \
    "s_add_u32 s44, s57, 0x1b3800\n\t" \
    "buffer_store_dwordx4 v[208:211], v253, s[60:63], s44 offen sc1\n\t" \
    "s_nop 6\n\t" \
    "v_cvt_pk_bf16_f32 v220, v220, v204\n\t" \
    "v_cvt_pk_bf16_f32 v221, v221, v205\n\t" \
    "v_cvt_pk_bf16_f32 v222, v222, v206\n\t" \
    "v_cvt_pk_bf16_f32 v223, v223, v207\n\t" \
    "s_add_u32 s44, s57, 0x173800\n\t" \
    "buffer_store_dwordx4 v[220:223], v253, s[60:63], s44 offen sc1\n\t" \
    "s_add_u32 s44, s40, 1\n\t" \
    "s_mov_b32 s45, s44\n\t" \
    "v_mov_b64 v[200:201], s[44:45]\n\t" \
    "v_mov_b64 v[202:203], s[44:45]\n\t" \
    "s_waitcnt lgkmcnt(0)\n\t" \
    "s_barrier \n\t" \
    "ds_read_b128 v[220:223], v253 offset:3072\n\t" \
    "ds_read_b128 v[224:227], v253 offset:3072\n\t" \
    "ds_read_b128 v[228:231], v253 offset:3072\n\t" \
    "ds_read_b128 v[232:235], v253 offset:3072\n\t" \
    "ds_read_b128 v[236:239], v253 offset:3072\n\t" \
    "ds_read_b128 v[240:243], v253 offset:3072\n\t" \
    "ds_read_b128 v[244:247], v253 offset:3072\n\t" \
    "ds_read_b128 v[248:251], v253 offset:3072\n\t" \
    "ds_read_b128 v[212:215], v254 offset:0\n\t" \
    "ds_read_b128 v[216:219], v254 offset:1024\n\t" \
    "s_and_b32 s44, s40, 3\n\t" \
    "s_lshl_b32 s44, s44, 22\n\t" \
    "s_add_u32 s42, s44, %[pb]\n\t" \
    "s_mov_b64 exec, 0xff\n\t" \
    "s_waitcnt vmcnt(0)\n\t" \
    "buffer_store_dwordx4 v[200:203], v253, s[60:63], %[fl] offen sc1\n\t" \
    "s_mov_b64 exec, -1\n\t" \
    "s_setprio 3\n\t" \
    "s_waitcnt lgkmcnt(0)\n\t" \
    "s_barrier \n\t" \
    "ds_read_b128 v[204:207], v253 offset:1024\n\t" \
    "ds_read_b128 v[208:211], v253 offset:2048\n\t" \
    "ds_read_b128 v[200:203], v253 offset:0\n\t" \
    "s_waitcnt lgkmcnt(2)\n\t" \
    "v_smfmac_f32_16x16x64_bf16 v[224:227], v[204:207], v[24:31], v252\n\t" \
    "v_smfmac_f32_16x16x64_bf16 v[220:223], v[204:207], v[0:7], v252\n\t" \
    "s_waitcnt lgkmcnt(1)\n\t" \
    "v_smfmac_f32_16x16x64_bf16 v[224:227], v[208:211], v[32:39], v252\n\t" \
    "v_smfmac_f32_16x16x64_bf16 v[220:223], v[208:211], v[8:15], v252\n\t" \
    "s_waitcnt lgkmcnt(0)\n\t" \
    "v_smfmac_f32_16x16x64_bf16 v[224:227], v[200:203], v[16:23], v252\n\t" \
    "v_smfmac_f32_16x16x64_bf16 v[220:223], v[200:203], v[212:219], v252\n\t" \
    "ds_read_b128 v[212:215], v254 offset:2048\n\t" \
    "ds_read_b128 v[216:219], v254 offset:3072\n\t" \
    "v_smfmac_f32_16x16x64_bf16 v[232:235], v[204:207], v[64:71], v252\n\t" \
    "v_smfmac_f32_16x16x64_bf16 v[228:231], v[204:207], v[40:47], v252\n\t" \
    "v_smfmac_f32_16x16x64_bf16 v[232:235], v[208:211], v[72:79], v252\n\t" \
    "v_add_f32 v220, v220, v222\n\t" \
    "v_add_f32 v221, v221, v223\n\t" \
    "v_cvt_pk_bf16_f32 v220, v220, v221\n\t" \
    "v_smfmac_f32_16x16x64_bf16 v[228:231], v[208:211], v[48:55], v252\n\t" \
    "v_min_u32 v220, -2, v220\n\t" \
    "v_add_f32 v224, v224, v226\n\t" \
    "v_add_f32 v225, v225, v227\n\t" \
    "v_smfmac_f32_16x16x64_bf16 v[232:235], v[200:203], v[56:63], v252\n\t" \
    "v_cvt_pk_bf16_f32 v221, v224, v225\n\t" \
    "v_min_u32 v221, -2, v221\n\t" \
    "s_waitcnt lgkmcnt(0)\n\t" \
    "v_smfmac_f32_16x16x64_bf16 v[228:231], v[200:203], v[212:219], v252\n\t" \
    "ds_read_b128 v[212:215], v254 offset:4096\n\t" \
    "ds_read_b128 v[216:219], v254 offset:5120\n\t" \
    "v_smfmac_f32_16x16x64_bf16 v[240:243], v[204:207], v[104:111], v252\n\t" \
    "v_smfmac_f32_16x16x64_bf16 v[236:239], v[204:207], v[80:87], v252\n\t" \
    "v_smfmac_f32_16x16x64_bf16 v[240:243], v[208:211], v[112:119], v252\n\t" \
    "v_add_f32 v228, v228, v230\n\t" \
    "v_add_f32 v229, v229, v231\n\t" \
    "v_cvt_pk_bf16_f32 v222, v228, v229\n\t" \
    "v_smfmac_f32_16x16x64_bf16 v[236:239], v[208:211], v[88:95], v252\n\t" \
    "v_min_u32 v222, -2, v222\n\t" \
    "v_add_f32 v232, v232, v234\n\t" \
    "v_add_f32 v233, v233, v235\n\t" \
    "v_smfmac_f32_16x16x64_bf16 v[240:243], v[200:203], v[96:103], v252\n\t" \
    "v_cvt_pk_bf16_f32 v223, v232, v233\n\t" \
    "v_min_u32 v223, -2, v223\n\t" \
    "v_permlane32_swap_b32 v220, v222\n\t" \
    "s_waitcnt lgkmcnt(0)\n\t" \
    "v_smfmac_f32_16x16x64_bf16 v[236:239], v[200:203], v[212:219], v252\n\t" \
    "ds_read_b128 v[212:215], v254 offset:6144\n\t" \
    "ds_read_b128 v[216:219], v254 offset:7168\n\t" \
    "s_nop 0\n\t" \
    "v_permlane32_swap_b32 v221, v223\n\t" \
    "s_nop 1\n\t" \
    "v_permlane16_swap_b32 v220, v221\n\t" \
    "v_permlane16_swap_b32 v222, v223\n\t" \
    "buffer_store_dwordx4 v[220:223], v255, s[60:63], s42 offen sc1\n\t" \
    "s_add_u32 s42, s42, 0x20000\n\t" \
    "ds_read_b128 v[224:227], v253 offset:3072\n\t" \
    "ds_read_b128 v[228:231], v253 offset:3072\n\t" \
    "v_smfmac_f32_16x16x64_bf16 v[248:251], v[204:207], v[144:151], v252\n\t" \
    "v_smfmac_f32_16x16x64_bf16 v[244:247], v[204:207], v[120:127], v252\n\t" \
    "v_smfmac_f32_16x16x64_bf16 v[248:251], v[208:211], v[152:159], v252\n\t" \
    "v_add_f32 v236, v236, v238\n\t" \
    "v_add_f32 v237, v237, v239\n\t" \
    "v_cvt_pk_bf16_f32 v236, v236, v237\n\t" \
    "v_smfmac_f32_16x16x64_bf16 v[244:247], v[208:211], v[128:135], v252\n\t" \
    "v_min_u32 v236, -2, v236\n\t" \
    "v_add_f32 v240, v240, v242\n\t" \
    "v_add_f32 v241, v241, v243\n\t" \
    "v_smfmac_f32_16x16x64_bf16 v[248:251], v[200:203], v[136:143], v252\n\t" \
    "v_cvt_pk_bf16_f32 v237, v240, v241\n\t" \
    "v_min_u32 v237, -2, v237\n\t" \
    "s_waitcnt lgkmcnt(2)\n\t" \
    "v_smfmac_f32_16x16x64_bf16 v[244:247], v[200:203], v[212:219], v252\n\t" \
    "ds_read_b128 v[212:215], v254 offset:8192\n\t" \
    "ds_read_b128 v[216:219], v254 offset:9216\n\t" \
    "s_waitcnt lgkmcnt(2)\n\t" \
    "v_smfmac_f32_16x16x64_bf16 v[228:231], v[204:207], v[184:191], v252\n\t" \
    "v_smfmac_f32_16x16x64_bf16 v[224:227], v[204:207], v[160:167], v252\n\t" \
    "v_smfmac_f32_16x16x64_bf16 v[228:231], v[208:211], v[192:199], v252\n\t" \
    "v_and_b32 v232, 0xffff, v255\n\t" \
    "v_lshrrev_b32 v233, 16, v255\n\t" \
    "v_lshl_add_u32 v232, v233, 3, v232\n\t" \
    "v_smfmac_f32_16x16x64_bf16 v[224:227], v[208:211], v[168:175], v252\n\t" \
    "v_add_f32 v244, v244, v246\n\t" \
    "v_add_f32 v245, v245, v247\n\t" \
    "v_cvt_pk_bf16_f32 v238, v244, v245\n\t" \
    "v_smfmac_f32_16x16x64_bf16 v[228:231], v[200:203], v[176:183], v252\n\t" \
    "v_min_u32 v238, -2, v238\n\t" \
    "v_add_f32 v248, v248, v250\n\t" \
    "v_add_f32 v249, v249, v251\n\t" \
    "s_waitcnt lgkmcnt(0)\n\t" \
    "v_smfmac_f32_16x16x64_bf16 v[224:227], v[200:203], v[212:219], v252\n\t" \
    "v_cvt_pk_bf16_f32 v239, v248, v249\n\t" \
    "v_min_u32 v239, -2, v239\n\t" \
    "v_permlane32_swap_b32 v236, v238\n\t" \
    "s_nop 0\n\t" \
    "v_permlane32_swap_b32 v237, v239\n\t" \
    "s_nop 1\n\t" \
    "v_permlane16_swap_b32 v236, v237\n\t" \
    "v_permlane16_swap_b32 v238, v239\n\t" \
    "buffer_store_dwordx4 v[236:239], v255, s[60:63], s42 offen sc1\n\t" \
    "s_add_u32 s42, s42, 0x20000\n\t" \
    "v_add_f32 v228, v228, v230\n\t" \
    "v_add_f32 v229, v229, v231\n\t" \
    "v_add_f32 v224, v224, v226\n\t" \
    "v_add_f32 v225, v225, v227\n\t" \
    "v_cvt_pk_bf16_f32 v229, v228, v229\n\t" \
    "v_cvt_pk_bf16_f32 v228, v224, v225\n\t" \
    "v_min_u32 v229, -2, v229\n\t" \
    "v_min_u32 v228, -2, v228\n\t" \
    "s_nop 1\n\t" \
    "v_permlane16_swap_b32 v228, v229\n\t" \
    "buffer_store_dwordx2 v[228:229], v232, s[60:63], s42 offen sc1\n\t" \
    "s_setprio 0\n\t" \
    "s_add_u32 s40, s40, 1\n\t" \
    "s_cmp_lg_u32 s40, 64\n\t" \
    "s_cbranch_scc1 .Lv43_%=\n\t" \
    "s_branch .Lend%=\n\t" \
    ".Lv3_%=:\n\t" \
    "s_mov_b32 s42, %[xs]\n\t" \
    "v_add_u32 v244, 0xc000, v243\n\t" \
    "s_waitcnt vmcnt(12) lgkmcnt(0)\n\t" \
    "s_barrier \n\t" \
    "s_add_u32 m0, %[wm], 0x9000\n\t" \
    "s_mov_b32 s44, 0x1e0000\n\t" \
    "buffer_load_dwordx4 v240, %[wr], s44 offen offset:0 nt lds\n\t" \
    "buffer_load_dwordx4 v241, %[wr], s44 offen offset:1024 nt lds\n\t" \
    "buffer_load_dwordx4 v242, %[wr], s44 offen offset:2048 nt lds\n\t" \
    "ds_read_b64_tr_b16 v[208:209], v244 offset:0\n\t" \
    "ds_read_b64_tr_b16 v[210:211], v244 offset:1536\n\t" \
    "ds_read_b64_tr_b16 v[40:41], v244 offset:32\n\t" \
    "ds_read_b64_tr_b16 v[42:43], v244 offset:1568\n\t" \
    "ds_read_b64_tr_b16 v[48:49], v244 offset:64\n\t" \
    "ds_read_b64_tr_b16 v[50:51], v244 offset:1600\n\t" \
    "v_add_u32 v244, 0xf000, v243\n\t" \
    "s_waitcnt vmcnt(12) lgkmcnt(0)\n\t" \
    "s_barrier \n\t" \
    "s_add_u32 m0, %[wm], 0xc000\n\t" \
    "s_mov_b32 s44, 0x240000\n\t" \
    "buffer_load_dwordx4 v240, %[wr], s44 offen offset:0 nt lds\n\t" \
    "buffer_load_dwordx4 v241, %[wr], s44 offen offset:1024 nt lds\n\t" \
    "buffer_load_dwordx4 v242, %[wr], s44 offen offset:2048 nt lds\n\t" \
    "ds_read_b64_tr_b16 v[212:213], v244 offset:0\n\t" \
    "ds_read_b64_tr_b16 v[214:215], v244 offset:1536\n\t" \
    "ds_read_b64_tr_b16 v[44:45], v244 offset:32\n\t" \
    "ds_read_b64_tr_b16 v[46:47], v244 offset:1568\n\t" \
    "ds_read_b64_tr_b16 v[52:53], v244 offset:64\n\t" \
    "ds_read_b64_tr_b16 v[54:55], v244 offset:1600\n\t" \
    "v_add_u32 v244, 0x0, v243\n\t" \
    "s_waitcnt vmcnt(12) lgkmcnt(0)\n\t" \
    "s_barrier \n\t" \
    "s_add_u32 m0, %[wm], 0xf000\n\t" \
    "s_mov_b32 s44, 0x2a0000\n\t" \
    "buffer_load_dwordx4 v240, %[wr], s44 offen offset:0 nt lds\n\t" \
    "buffer_load_dwordx4 v241, %[wr], s44 offen offset:1024 nt lds\n\t" \
    "buffer_load_dwordx4 v242, %[wr], s44 offen offset:2048 nt lds\n\t" \
    "ds_read_b64_tr_b16 v[56:57], v244 offset:0\n\t" \
    "ds_read_b64_tr_b16 v[58:59], v244 offset:1536\n\t" \
    "ds_read_b64_tr_b16 v[64:65], v244 offset:32\n\t" \
    "ds_read_b64_tr_b16 v[66:67], v244 offset:1568\n\t" \
    "ds_read_b64_tr_b16 v[72:73], v244 offset:64\n\t" \
    "ds_read_b64_tr_b16 v[74:75], v244 offset:1600\n\t" \
    "v_add_u32 v244, 0x3000, v243\n\t" \
    "s_waitcnt vmcnt(12) lgkmcnt(0)\n\t" \
    "s_barrier \n\t" \
    "s_add_u32 m0, %[wm], 0x0\n\t" \
    "s_mov_b32 s44, 0x300000\n\t" \
    "buffer_load_dwordx4 v240, %[wr], s44 offen offset:0 nt lds\n\t" \
    "buffer_load_dwordx4 v241, %[wr], s44 offen offset:1024 nt lds\n\t" \
    "buffer_load_dwordx4 v242, %[wr], s44 offen offset:2048 nt lds\n\t" \
    "ds_read_b64_tr_b16 v[60:61], v244 offset:0\n\t" \
    "ds_read_b64_tr_b16 v[62:63], v244 offset:1536\n\t" \
    "ds_read_b64_tr_b16 v[68:69], v244 offset:32\n\t" \
    "ds_read_b64_tr_b16 v[70:71], v244 offset:1568\n\t" \
    "ds_read_b64_tr_b16 v[76:77], v244 offset:64\n\t" \
    "ds_read_b64_tr_b16 v[78:79], v244 offset:1600\n\t" \
    "v_add_u32 v244, 0x6000, v243\n\t" \
    "s_waitcnt vmcnt(12) lgkmcnt(0)\n\t" \
    "s_barrier \n\t" \
    "s_add_u32 m0, %[wm], 0x3000\n\t" \
    "s_mov_b32 s44, 0x360000\n\t" \
    "buffer_load_dwordx4 v240, %[wr], s44 offen offset:0 nt lds\n\t" \
    "buffer_load_dwordx4 v241, %[wr], s44 offen offset:1024 nt lds\n\t" \
    "buffer_load_dwordx4 v242, %[wr], s44 offen offset:2048 nt lds\n\t" \
    "ds_read_b64_tr_b16 v[216:217], v244 offset:0\n\t" \
    "ds_read_b64_tr_b16 v[218:219], v244 offset:1536\n\t" \
    "ds_read_b64_tr_b16 v[80:81], v244 offset:32\n\t" \
    "ds_read_b64_tr_b16 v[82:83], v244 offset:1568\n\t" \
    "ds_read_b64_tr_b16 v[88:89], v244 offset:64\n\t" \
    "ds_read_b64_tr_b16 v[90:91], v244 offset:1600\n\t" \
    "v_add_u32 v244, 0x9000, v243\n\t" \
    "s_waitcnt vmcnt(12) lgkmcnt(0)\n\t" \
    "s_barrier \n\t" \
    "s_add_u32 m0, %[wm], 0x6000\n\t" \
    "s_mov_b32 s44, 0x3c0000\n\t" \
    "buffer_load_dwordx4 v240, %[wr], s44 offen offset:0 nt lds\n\t" \
    "buffer_load_dwordx4 v241, %[wr], s44 offen offset:1024 nt lds\n\t" \
    "buffer_load_dwordx4 v242, %[wr], s44 offen offset:2048 nt lds\n\t" \
    "ds_read_b64_tr_b16 v[220:221], v244 offset:0\n\t" \
    "ds_read_b64_tr_b16 v[222:223], v244 offset:1536\n\t" \
    "ds_read_b64_tr_b16 v[84:85], v244 offset:32\n\t" \
    "ds_read_b64_tr_b16 v[86:87], v244 offset:1568\n\t" \
    "ds_read_b64_tr_b16 v[92:93], v244 offset:64\n\t" \
    "ds_read_b64_tr_b16 v[94:95], v244 offset:1600\n\t" \
    "v_add_u32 v244, 0xc000, v243\n\t" \
    "s_waitcnt vmcnt(12) lgkmcnt(0)\n\t" \
    "s_barrier \n\t" \
    "s_add_u32 m0, %[wm], 0x9000\n\t" \
    "s_mov_b32 s44, 0x420000\n\t" \
    "buffer_load_dwordx4 v240, %[wr], s44 offen offset:0 nt lds\n\t" \
    "buffer_load_dwordx4 v241, %[wr], s44 offen offset:1024 nt lds\n\t" \
    "buffer_load_dwordx4 v242, %[wr], s44 offen offset:2048 nt lds\n\t" \
    "ds_read_b64_tr_b16 v[96:97], v244 offset:0\n\t" \
    "ds_read_b64_tr_b16 v[98:99], v244 offset:1536\n\t" \
    "ds_read_b64_tr_b16 v[104:105], v244 offset:32\n\t" \
    "ds_read_b64_tr_b16 v[106:107], v244 offset:1568\n\t" \
    "ds_read_b64_tr_b16 v[112:113], v244 offset:64\n\t" \
    "ds_read_b64_tr_b16 v[114:115], v244 offset:1600\n\t" \
    "v_add_u32 v244, 0xf000, v243\n\t" \
    "s_waitcnt vmcnt(12) lgkmcnt(0)\n\t" \
    "s_barrier \n\t" \
    "s_add_u32 m0, %[wm], 0xc000\n\t" \
    "s_mov_b32 s44, 0x480000\n\t" \
    "buffer_load_dwordx4 v240, %[wr], s44 offen offset:0 nt lds\n\t" \
    "buffer_load_dwordx4 v241, %[wr], s44 offen offset:1024 nt lds\n\t" \
    "buffer_load_dwordx4 v242, %[wr], s44 offen offset:2048 nt lds\n\t" \
    "ds_read_b64_tr_b16 v[100:101], v244 offset:0\n\t" \
    "ds_read_b64_tr_b16 v[102:103], v244 offset:1536\n\t" \
    "ds_read_b64_tr_b16 v[108:109], v244 offset:32\n\t" \
    "ds_read_b64_tr_b16 v[110:111], v244 offset:1568\n\t" \
    "ds_read_b64_tr_b16 v[116:117], v244 offset:64\n\t" \
    "ds_read_b64_tr_b16 v[118:119], v244 offset:1600\n\t" \
    "v_add_u32 v244, 0x0, v243\n\t" \
    "s_waitcnt vmcnt(12) lgkmcnt(0)\n\t" \
    "s_barrier \n\t" \
    "v_mov_b32 v246, 0\n\t" \
    "buffer_load_dword v246, v246, s[60:63], %[mo] offen sc1\n\t" \
    "s_add_u32 m0, %[wm], 0xf000\n\t" \
    "s_mov_b32 s44, 0x4e0000\n\t" \
    "buffer_load_dwordx4 v240, %[wr], s44 offen offset:0 nt lds\n\t" \
    "buffer_load_dwordx4 v241, %[wr], s44 offen offset:1024 nt lds\n\t" \
    "buffer_load_dwordx4 v242, %[wr], s44 offen offset:2048 nt lds\n\t" \
    "ds_read_b64_tr_b16 v[224:225], v244 offset:0\n\t" \
    "ds_read_b64_tr_b16 v[226:227], v244 offset:1536\n\t" \
    "ds_read_b64_tr_b16 v[120:121], v244 offset:32\n\t" \
    "ds_read_b64_tr_b16 v[122:123], v244 offset:1568\n\t" \
    "ds_read_b64_tr_b16 v[128:129], v244 offset:64\n\t" \
    "ds_read_b64_tr_b16 v[130:131], v244 offset:1600\n\t" \
    "v_add_u32 v244, 0x3000, v243\n\t" \
    "s_waitcnt vmcnt(12) lgkmcnt(0)\n\t" \
    "s_barrier \n\t" \
    "s_add_u32 m0, %[wm], 0x0\n\t" \
    "s_mov_b32 s44, 0x540000\n\t" \
    "buffer_load_dwordx4 v240, %[wr], s44 offen offset:0 nt lds\n\t" \
    "buffer_load_dwordx4 v241, %[wr], s44 offen offset:1024 nt lds\n\t" \
    "buffer_load_dwordx4 v242, %[wr], s44 offen offset:2048 nt lds\n\t" \
    "ds_read_b64_tr_b16 v[228:229], v244 offset:0\n\t" \
    "ds_read_b64_tr_b16 v[230:231], v244 offset:1536\n\t" \
    "ds_read_b64_tr_b16 v[124:125], v244 offset:32\n\t" \
    "ds_read_b64_tr_b16 v[126:127], v244 offset:1568\n\t" \
    "ds_read_b64_tr_b16 v[132:133], v244 offset:64\n\t" \
    "ds_read_b64_tr_b16 v[134:135], v244 offset:1600\n\t" \
    "v_add_u32 v244, 0x6000, v243\n\t" \
    "s_waitcnt vmcnt(12) lgkmcnt(0)\n\t" \
    "s_barrier \n\t" \
    "s_add_u32 m0, %[wm], 0x3000\n\t" \
    "s_mov_b32 s44, 0x5a0000\n\t" \
    "buffer_load_dwordx4 v240, %[wr], s44 offen offset:0 nt lds\n\t" \
    "buffer_load_dwordx4 v241, %[wr], s44 offen offset:1024 nt lds\n\t" \
    "buffer_load_dwordx4 v242, %[wr], s44 offen offset:2048 nt lds\n\t" \
    "ds_read_b64_tr_b16 v[136:137], v244 offset:0\n\t" \
    "ds_read_b64_tr_b16 v[138:139], v244 offset:1536\n\t" \
    "ds_read_b64_tr_b16 v[144:145], v244 offset:32\n\t" \
    "ds_read_b64_tr_b16 v[146:147], v244 offset:1568\n\t" \
    "ds_read_b64_tr_b16 v[152:153], v244 offset:64\n\t" \
    "ds_read_b64_tr_b16 v[154:155], v244 offset:1600\n\t" \
    "v_add_u32 v244, 0x9000, v243\n\t" \
    "s_waitcnt vmcnt(12) lgkmcnt(0)\n\t" \
    "s_barrier \n\t" \
    "s_add_u32 m0, %[wm], 0x6000\n\t" \
    "s_mov_b32 s44, 0x600000\n\t" \
    "buffer_load_dwordx4 v240, %[wr], s44 offen offset:0 nt lds\n\t" \
    "buffer_load_dwordx4 v241, %[wr], s44 offen offset:1024 nt lds\n\t" \
    "buffer_load_dwordx4 v242, %[wr], s44 offen offset:2048 nt lds\n\t" \
    "ds_read_b64_tr_b16 v[140:141], v244 offset:0\n\t" \
    "ds_read_b64_tr_b16 v[142:143], v244 offset:1536\n\t" \
    "ds_read_b64_tr_b16 v[148:149], v244 offset:32\n\t" \
    "ds_read_b64_tr_b16 v[150:151], v244 offset:1568\n\t" \
    "ds_read_b64_tr_b16 v[156:157], v244 offset:64\n\t" \
    "ds_read_b64_tr_b16 v[158:159], v244 offset:1600\n\t" \
    "v_add_u32 v244, 0xc000, v243\n\t" \
    "s_waitcnt vmcnt(12) lgkmcnt(0)\n\t" \
    "s_barrier \n\t" \
    "s_add_u32 m0, %[wm], 0x9000\n\t" \
    "s_mov_b32 s44, 0x660000\n\t" \
    "buffer_load_dwordx4 v240, %[wr], s44 offen offset:0 nt lds\n\t" \
    "buffer_load_dwordx4 v241, %[wr], s44 offen offset:1024 nt lds\n\t" \
    "buffer_load_dwordx4 v242, %[wr], s44 offen offset:2048 nt lds\n\t" \
    "ds_read_b64_tr_b16 v[232:233], v244 offset:0\n\t" \
    "ds_read_b64_tr_b16 v[234:235], v244 offset:1536\n\t" \
    "ds_read_b64_tr_b16 v[160:161], v244 offset:32\n\t" \
    "ds_read_b64_tr_b16 v[162:163], v244 offset:1568\n\t" \
    "ds_read_b64_tr_b16 v[168:169], v244 offset:64\n\t" \
    "ds_read_b64_tr_b16 v[170:171], v244 offset:1600\n\t" \
    "v_add_u32 v244, 0xf000, v243\n\t" \
    "s_waitcnt vmcnt(12) lgkmcnt(0)\n\t" \
    "s_barrier \n\t" \
    ".Lv31_%=:\n\t" \
    "v_readfirstlane_b32 s43, v246\n\t" \
    "s_nop 0\n\t" \
    "s_cmp_lg_u32 s43, -1\n\t" \
    "s_cbranch_scc1 .Lv32_%=\n\t" \
    "s_cmp_eq_u32 s41, 0\n\t" \
    "s_cbranch_scc1 .Lv32_%=\n\t" \
    "s_sub_u32 s41, s41, 1\n\t" \
    "s_sleep 4\n\t" \
    "v_mov_b32 v246, 0\n\t" \
    "buffer_load_dword v246, v246, s[60:63], %[mo] offen sc1\n\t" \
    "s_waitcnt vmcnt(0)\n\t" \
    "s_branch .Lv31_%=\n\t" \
    ".Lv32_%=:\n\t" \
    "v_mov_b32 v246, 0x8000\n\t" \
    "s_mov_b64 exec, 1\n\t" \
    "buffer_store_dword v246, v246, s[60:63], %[mo] offen sc1\n\t" \
    "s_mov_b64 exec, -1\n\t" \
    "s_and_b32 s44, s43, 0x5000\n\t" \
    "s_xor_b32 %[mb], %[mb], s44\n\t" \
    "s_xor_b32 %[fl], %[fl], s44\n\t" \
    "s_and_b32 s43, s43, 0x2000\n\t" \
    "s_xor_b32 s43, s43, %[lx]\n\t" \
    "s_add_u32 s60, s60, s43\n\t" \
    "s_addc_u32 s61, s61, 0\n\t" \
    "v_and_b32 v252, 0xf0, v253\n\t" \
    "v_lshlrev_b32 v252, 3, v252\n\t" \
    "buffer_load_dword v252, v252, s[60:63], %[fl] offen sc1\n\t" \
    "s_add_u32 m0, %[wm], 0xc000\n\t" \
    "s_mov_b32 s44, 0x6c0000\n\t" \
    "buffer_load_dwordx4 v240, %[wr], s44 offen offset:0 nt lds\n\t" \
    "buffer_load_dwordx4 v241, %[wr], s44 offen offset:1024 nt lds\n\t" \
    "buffer_load_dwordx4 v242, %[wr], s44 offen offset:2048 nt lds\n\t" \
    "ds_read_b64_tr_b16 v[236:237], v244 offset:0\n\t" \
    "ds_read_b64_tr_b16 v[238:239], v244 offset:1536\n\t" \
    "ds_read_b64_tr_b16 v[164:165], v244 offset:32\n\t" \
    "ds_read_b64_tr_b16 v[166:167], v244 offset:1568\n\t" \
    "ds_read_b64_tr_b16 v[172:173], v244 offset:64\n\t" \
    "ds_read_b64_tr_b16 v[174:175], v244 offset:1600\n\t" \
    "v_add_u32 v244, 0x0, v243\n\t" \
    "s_waitcnt vmcnt(12) lgkmcnt(0)\n\t" \
    "s_barrier \n\t" \
    "s_add_u32 m0, %[wm], 0xf000\n\t" \
    "s_mov_b32 s44, 0x720000\n\t" \
    "buffer_load_dwordx4 v240, %[wr], s44 offen offset:0 nt lds\n\t" \
    "buffer_load_dwordx4 v241, %[wr], s44 offen offset:1024 nt lds\n\t" \
    "buffer_load_dwordx4 v242, %[wr], s44 offen offset:2048 nt lds\n\t" \
    "ds_read_b64_tr_b16 v[176:177], v244 offset:0\n\t" \
    "ds_read_b64_tr_b16 v[178:179], v244 offset:1536\n\t" \
    "ds_read_b64_tr_b16 v[184:185], v244 offset:32\n\t" \
    "ds_read_b64_tr_b16 v[186:187], v244 offset:1568\n\t" \
    "ds_read_b64_tr_b16 v[192:193], v244 offset:64\n\t" \
    "ds_read_b64_tr_b16 v[194:195], v244 offset:1600\n\t" \
    "v_add_u32 v244, 0x3000, v243\n\t" \
    "s_waitcnt vmcnt(12) lgkmcnt(0)\n\t" \
    "s_barrier \n\t" \
    "ds_read_b64_tr_b16 v[180:181], v244 offset:0\n\t" \
    "ds_read_b64_tr_b16 v[182:183], v244 offset:1536\n\t" \
    "ds_read_b64_tr_b16 v[188:189], v244 offset:32\n\t" \
    "ds_read_b64_tr_b16 v[190:191], v244 offset:1568\n\t" \
    "ds_read_b64_tr_b16 v[196:197], v244 offset:64\n\t" \
    "ds_read_b64_tr_b16 v[198:199], v244 offset:1600\n\t" \
    "v_add_u32 v244, 0x6000, v243\n\t" \
    "s_waitcnt vmcnt(9) lgkmcnt(0)\n\t" \
    "s_barrier \n\t" \
    "ds_read_b64_tr_b16 v[200:201], v244 offset:0\n\t" \
    "ds_read_b64_tr_b16 v[202:203], v244 offset:1536\n\t" \
    "ds_read_b64_tr_b16 v[0:1], v244 offset:32\n\t" \
    "ds_read_b64_tr_b16 v[2:3], v244 offset:1568\n\t" \
    "ds_read_b64_tr_b16 v[8:9], v244 offset:64\n\t" \
    "ds_read_b64_tr_b16 v[10:11], v244 offset:1600\n\t" \
    "v_add_u32 v244, 0x9000, v243\n\t" \
    "s_waitcnt vmcnt(6) lgkmcnt(0)\n\t" \
    "s_barrier \n\t" \
    "ds_read_b64_tr_b16 v[204:205], v244 offset:0\n\t" \
    "ds_read_b64_tr_b16 v[206:207], v244 offset:1536\n\t" \
    "ds_read_b64_tr_b16 v[4:5], v244 offset:32\n\t" \
    "ds_read_b64_tr_b16 v[6:7], v244 offset:1568\n\t" \
    "ds_read_b64_tr_b16 v[12:13], v244 offset:64\n\t" \
    "ds_read_b64_tr_b16 v[14:15], v244 offset:1600\n\t" \
    "v_add_u32 v244, 0xc000, v243\n\t" \
    "s_waitcnt vmcnt(3) lgkmcnt(0)\n\t" \
    "s_barrier \n\t" \
    "ds_read_b64_tr_b16 v[16:17], v244 offset:0\n\t" \
    "ds_read_b64_tr_b16 v[18:19], v244 offset:1536\n\t" \
    "ds_read_b64_tr_b16 v[24:25], v244 offset:32\n\t" \
    "ds_read_b64_tr_b16 v[26:27], v244 offset:1568\n\t" \
    "ds_read_b64_tr_b16 v[32:33], v244 offset:64\n\t" \
    "ds_read_b64_tr_b16 v[34:35], v244 offset:1600\n\t" \
    "s_mov_b32 s43, 0\n\t" \
    "v_cmp_eq_u32 vcc, 0, v252\n\t" \
    "s_cbranch_vccnz .Lv33_%=\n\t" \
    "s_mov_b32 m0, 0x3000\n\t" \
    "s_add_u32 s49, s42, 0x0\n\t" \
    "buffer_load_dwordx4 v253, s[60:63], s49 offen offset:0 sc1 lds\n\t" \
    "buffer_load_dwordx4 v253, s[60:63], s49 offen offset:1024 sc1 lds\n\t" \
    "buffer_load_dwordx4 v253, s[60:63], s49 offen offset:2048 sc1 lds\n\t" \
    "buffer_load_dwordx4 v253, s[60:63], s49 offen offset:3072 sc1 lds\n\t" \
    "s_mov_b32 m0, 0x4c00\n\t" \
    "s_add_u32 s49, s42, 0x4c00\n\t" \
    "buffer_load_dwordx4 v253, s[60:63], s49 offen offset:0 sc1 lds\n\t" \
    "s_mov_b32 m0, 0x8000\n\t" \
    "s_add_u32 s49, s42, 0x1000\n\t" \
    "buffer_load_dwordx4 v253, s[60:63], s49 offen offset:0 sc1 lds\n\t" \
    "buffer_load_dwordx4 v253, s[60:63], s49 offen offset:1024 sc1 lds\n\t" \
    "buffer_load_dwordx4 v253, s[60:63], s49 offen offset:2048 sc1 lds\n\t" \
    "buffer_load_dwordx4 v253, s[60:63], s49 offen offset:3072 sc1 lds\n\t" \
    "s_mov_b32 m0, 0x9c00\n\t" \
    "s_add_u32 s49, s42, 0x5c00\n\t" \
    "buffer_load_dwordx4 v253, s[60:63], s49 offen offset:0 sc1 lds\n\t" \
    "s_mov_b32 s43, 1\n\t" \
    ".Lv33_%=:\n\t" \
    "v_add_u32 v244, 0xf000, v243\n\t" \
    "s_waitcnt vmcnt(0) lgkmcnt(0)\n\t" \
    "s_barrier \n\t" \
    "ds_read_b64_tr_b16 v[20:21], v244 offset:0\n\t" \
    "ds_read_b64_tr_b16 v[22:23], v244 offset:1536\n\t" \
    "ds_read_b64_tr_b16 v[28:29], v244 offset:32\n\t" \
    "ds_read_b64_tr_b16 v[30:31], v244 offset:1568\n\t" \
    "ds_read_b64_tr_b16 v[36:37], v244 offset:64\n\t" \
    "ds_read_b64_tr_b16 v[38:39], v244 offset:1600\n\t" \
    "s_waitcnt lgkmcnt(0)\n\t" \
    "s_barrier \n\t" \
    "ds_write_b128 v254, v[200:203] offset:55296\n\t" \
    "ds_write_b128 v254, v[204:207] offset:56320\n\t" \
    "ds_write_b128 v254, v[208:211] offset:57344\n\t" \
    "ds_write_b128 v254, v[212:215] offset:58368\n\t" \
    "ds_write_b128 v254, v[216:219] offset:59392\n\t" \
    "ds_write_b128 v254, v[220:223] offset:60416\n\t" \
    "ds_write_b128 v254, v[224:227] offset:61440\n\t" \
    "ds_write_b128 v254, v[228:231] offset:62464\n\t" \
    "ds_write_b128 v254, v[232:235] offset:63488\n\t" \
    "ds_write_b128 v254, v[236:239] offset:64512\n\t" \
    "s_cmp_lg_u32 s43, 0\n\t" \
    "s_cbranch_scc1 .Lv34_%=\n\t" \
    "s_mov_b32 s44, 0\n\t" \
    ".Lv35_%=:\n\t" \
    "v_cmp_ge_u32 vcc, s44, v252\n\t" \
    "s_cbranch_vccz .Lv36_%=\n\t" \
    "s_cmp_eq_u32 s41, 0\n\t" \
    "s_cbranch_scc1 .Lv36_%=\n\t" \
    "s_sub_u32 s41, s41, 1\n\t" \
    "s_sleep 8\n\t" \
    "v_and_b32 v252, 0xf0, v253\n\t" \
    "v_lshlrev_b32 v252, 3, v252\n\t" \
    "buffer_load_dword v252, v252, s[60:63], %[fl] offen sc1\n\t" \
    "s_waitcnt vmcnt(0)\n\t" \
    "s_branch .Lv35_%=\n\t" \
    ".Lv36_%=:\n\t" \
    "s_mov_b32 m0, 0x3000\n\t" \
    "s_add_u32 s49, s42, 0x0\n\t" \
    "buffer_load_dwordx4 v253, s[60:63], s49 offen offset:0 sc1 lds\n\t" \
    "buffer_load_dwordx4 v253, s[60:63], s49 offen offset:1024 sc1 lds\n\t" \
    "buffer_load_dwordx4 v253, s[60:63], s49 offen offset:2048 sc1 lds\n\t" \
    "buffer_load_dwordx4 v253, s[60:63], s49 offen offset:3072 sc1 lds\n\t" \
    "s_mov_b32 m0, 0x4c00\n\t" \
    "s_add_u32 s49, s42, 0x4c00\n\t" \
    "buffer_load_dwordx4 v253, s[60:63], s49 offen offset:0 sc1 lds\n\t" \
    "s_mov_b32 m0, 0x8000\n\t" \
    "s_add_u32 s49, s42, 0x1000\n\t" \
    "buffer_load_dwordx4 v253, s[60:63], s49 offen offset:0 sc1 lds\n\t" \
    "buffer_load_dwordx4 v253, s[60:63], s49 offen offset:1024 sc1 lds\n\t" \
    "buffer_load_dwordx4 v253, s[60:63], s49 offen offset:2048 sc1 lds\n\t" \
    "buffer_load_dwordx4 v253, s[60:63], s49 offen offset:3072 sc1 lds\n\t" \
    "s_mov_b32 m0, 0x9c00\n\t" \
    "s_add_u32 s49, s42, 0x5c00\n\t" \
    "buffer_load_dwordx4 v253, s[60:63], s49 offen offset:0 sc1 lds\n\t" \
    ".Lv34_%=:\n\t" \
    "s_waitcnt vmcnt(0) lgkmcnt(0)\n\t" \
    "s_barrier \n\t" \
    "ds_read_b128 v[224:227], v253 offset:16384\n\t" \
    "ds_read_b128 v[244:247], v254 offset:55296\n\t" \
    "ds_read_b128 v[228:231], v253 offset:17408\n\t" \
    "ds_read_b128 v[232:235], v253 offset:36864\n\t" \
    "ds_read_b128 v[236:239], v253 offset:37888\n\t" \
    "ds_read_b128 v[248:251], v254 offset:56320\n\t" \
    ".p2align 8\n\t" \
    ".Lv37_%=:\n\t" \
    "s_waitcnt lgkmcnt(5)\n\t" \
    "v_mfma_f32_16x16x32_bf16 v[204:207], v[0:3], v[224:227], 0\n\t" \
    "s_waitcnt lgkmcnt(3)\n\t" \
    "v_mfma_f32_16x16x32_bf16 v[204:207], v[4:7], v[228:231], v[204:207]\n\t" \
    "ds_read_b128 v[240:243], v253 offset:18432\n\t" \
    "v_mfma_f32_16x16x32_bf16 v[208:211], v[8:11], v[224:227], 0\n\t" \
    "v_mfma_f32_16x16x32_bf16 v[208:211], v[12:15], v[228:231], v[208:211]\n\t" \
    "v_mfma_f32_16x16x32_bf16 v[200:203], v[244:247], v[224:227], 0\n\t" \
    "s_waitcnt lgkmcnt(1)\n\t" \
    "v_mfma_f32_16x16x32_bf16 v[200:203], v[248:251], v[228:231], v[200:203]\n\t" \
    "v_mfma_f32_16x16x32_bf16 v[216:219], v[0:3], v[232:235], 0\n\t" \
    "ds_read_b128 v[224:227], v253 offset:19456\n\t" \
    "v_mfma_f32_16x16x32_bf16 v[216:219], v[4:7], v[236:239], v[216:219]\n\t" \
    "ds_read_b128 v[228:231], v253 offset:0\n\t" \
    "v_mfma_f32_16x16x32_bf16 v[220:223], v[8:11], v[232:235], 0\n\t" \
    "v_mfma_f32_16x16x32_bf16 v[220:223], v[12:15], v[236:239], v[220:223]\n\t" \
    "v_mfma_f32_16x16x32_bf16 v[212:215], v[244:247], v[232:235], 0\n\t" \
    "ds_read_b128 v[244:247], v254 offset:57344\n\t" \
    "v_mfma_f32_16x16x32_bf16 v[212:215], v[248:251], v[236:239], v[212:215]\n\t" \
    "ds_read_b128 v[248:251], v254 offset:58368\n\t" \
    "s_waitcnt lgkmcnt(4)\n\t" \
    "v_mfma_f32_16x16x32_bf16 v[204:207], v[24:27], v[240:243], v[204:207]\n\t" \
    "ds_read_b128 v[232:235], v253 offset:1024\n\t" \
    "s_waitcnt lgkmcnt(4)\n\t" \
    "v_mfma_f32_16x16x32_bf16 v[204:207], v[28:31], v[224:227], v[204:207]\n\t" \
    "ds_read_b128 v[236:239], v253 offset:20480\n\t" \
    "v_mfma_f32_16x16x32_bf16 v[208:211], v[32:35], v[240:243], v[208:211]\n\t" \
    "v_mfma_f32_16x16x32_bf16 v[208:211], v[36:39], v[224:227], v[208:211]\n\t" \
    "v_mfma_f32_16x16x32_bf16 v[200:203], v[16:19], v[240:243], v[200:203]\n\t" \
    "v_mfma_f32_16x16x32_bf16 v[200:203], v[20:23], v[224:227], v[200:203]\n\t" \
    "s_waitcnt lgkmcnt(4)\n\t" \
    "v_mfma_f32_16x16x32_bf16 v[204:207], v[40:43], v[228:231], v[204:207]\n\t" \
    "ds_read_b128 v[240:243], v253 offset:21504\n\t" \
    "s_waitcnt lgkmcnt(2)\n\t" \
    "v_mfma_f32_16x16x32_bf16 v[204:207], v[44:47], v[232:235], v[204:207]\n\t" \
    "ds_read_b128 v[224:227], v253 offset:2048\n\t" \
    "v_mfma_f32_16x16x32_bf16 v[208:211], v[48:51], v[228:231], v[208:211]\n\t" \
    "v_mfma_f32_16x16x32_bf16 v[208:211], v[52:55], v[232:235], v[208:211]\n\t" \
    "v_mfma_f32_16x16x32_bf16 v[200:203], v[244:247], v[228:231], v[200:203]\n\t" \
    "v_mfma_f32_16x16x32_bf16 v[200:203], v[248:251], v[232:235], v[200:203]\n\t" \
    "s_waitcnt lgkmcnt(2)\n\t" \
    "v_mfma_f32_16x16x32_bf16 v[216:219], v[40:43], v[236:239], v[216:219]\n\t" \
    "ds_read_b128 v[228:231], v253 offset:3072\n\t" \
    "s_add_u32 s44, s40, 1\n\t" \
    "s_waitcnt lgkmcnt(2)\n\t" \
    "v_mfma_f32_16x16x32_bf16 v[216:219], v[44:47], v[240:243], v[216:219]\n\t" \
    "ds_read_b128 v[232:235], v253 offset:4096\n\t" \
    "s_and_b32 s45, s44, 15\n\t" \
    "v_mfma_f32_16x16x32_bf16 v[220:223], v[48:51], v[236:239], v[220:223]\n\t" \
    "s_cmp_eq_u32 s45, 0\n\t" \
    "v_mfma_f32_16x16x32_bf16 v[220:223], v[52:55], v[240:243], v[220:223]\n\t" \
    "s_cselect_b32 s45, %[cj], %[cs]\n\t" \
    "v_mfma_f32_16x16x32_bf16 v[212:215], v[244:247], v[236:239], v[212:215]\n\t" \
    "ds_read_b128 v[244:247], v254 offset:59392\n\t" \
    "s_cmp_eq_u32 s44, 64\n\t" \
    "v_mfma_f32_16x16x32_bf16 v[212:215], v[248:251], v[240:243], v[212:215]\n\t" \
    "ds_read_b128 v[248:251], v254 offset:60416\n\t" \
    "s_cselect_b32 s45, 0, s45\n\t" \
    "s_waitcnt lgkmcnt(4)\n\t" \
    "v_mfma_f32_16x16x32_bf16 v[204:207], v[64:67], v[224:227], v[204:207]\n\t" \
    "ds_read_b128 v[236:239], v253 offset:5120\n\t" \
    "s_add_u32 s42, s42, s45\n\t" \
    "s_waitcnt lgkmcnt(4)\n\t" \
    "v_mfma_f32_16x16x32_bf16 v[204:207], v[68:71], v[228:231], v[204:207]\n\t" \
    "ds_read_b128 v[240:243], v253 offset:24576\n\t" \
    "v_mfma_f32_16x16x32_bf16 v[208:211], v[72:75], v[224:227], v[208:211]\n\t" \
    "v_mfma_f32_16x16x32_bf16 v[208:211], v[76:79], v[228:231], v[208:211]\n\t" \
    "v_mfma_f32_16x16x32_bf16 v[200:203], v[56:59], v[224:227], v[200:203]\n\t" \
    "v_mfma_f32_16x16x32_bf16 v[200:203], v[60:63], v[228:231], v[200:203]\n\t" \
    "s_waitcnt lgkmcnt(4)\n\t" \
    "v_mfma_f32_16x16x32_bf16 v[204:207], v[80:83], v[232:235], v[204:207]\n\t" \
    "ds_read_b128 v[224:227], v253 offset:25600\n\t" \
    "s_waitcnt lgkmcnt(2)\n\t" \
    "v_mfma_f32_16x16x32_bf16 v[204:207], v[84:87], v[236:239], v[204:207]\n\t" \
    "ds_read_b128 v[228:231], v253 offset:6144\n\t" \
    "v_mfma_f32_16x16x32_bf16 v[208:211], v[88:91], v[232:235], v[208:211]\n\t" \
    "v_mfma_f32_16x16x32_bf16 v[208:211], v[92:95], v[236:239], v[208:211]\n\t" \
    "v_mfma_f32_16x16x32_bf16 v[200:203], v[244:247], v[232:235], v[200:203]\n\t" \
    "v_mfma_f32_16x16x32_bf16 v[200:203], v[248:251], v[236:239], v[200:203]\n\t" \
    "s_waitcnt lgkmcnt(2)\n\t" \
    "v_mfma_f32_16x16x32_bf16 v[216:219], v[80:83], v[240:243], v[216:219]\n\t" \
    "ds_read_b128 v[232:235], v253 offset:7168\n\t" \
    "s_and_b32 s44, s40, 3\n\t" \
    "s_waitcnt lgkmcnt(2)\n\t" \
    "v_mfma_f32_16x16x32_bf16 v[216:219], v[84:87], v[224:227], v[216:219]\n\t" \
    "ds_read_b128 v[236:239], v253 offset:8192\n\t" \
    "s_lshl_b32 s44, s44, 22\n\t" \
    "v_mfma_f32_16x16x32_bf16 v[220:223], v[88:91], v[240:243], v[220:223]\n\t" \
    "s_add_u32 s44, s44, %[mb]\n\t" \
    "v_mfma_f32_16x16x32_bf16 v[220:223], v[92:95], v[224:227], v[220:223]\n\t" \
    "s_add_u32 s46, s44, 0x10000\n\t" \
    "v_mfma_f32_16x16x32_bf16 v[212:215], v[244:247], v[240:243], v[212:215]\n\t" \
    "ds_read_b128 v[244:247], v254 offset:61440\n\t" \
    "s_add_u32 s47, s44, 0x10400\n\t" \
    "v_mfma_f32_16x16x32_bf16 v[212:215], v[248:251], v[224:227], v[212:215]\n\t" \
    "ds_read_b128 v[248:251], v254 offset:62464\n\t" \
    "s_waitcnt lgkmcnt(4)\n\t" \
    "v_mfma_f32_16x16x32_bf16 v[204:207], v[104:107], v[228:231], v[204:207]\n\t" \
    "ds_read_b128 v[240:243], v253 offset:9216\n\t" \
    "s_waitcnt lgkmcnt(4)\n\t" \
    "v_mfma_f32_16x16x32_bf16 v[204:207], v[108:111], v[232:235], v[204:207]\n\t" \
    "ds_read_b128 v[224:227], v253 offset:28672\n\t" \
    "v_mfma_f32_16x16x32_bf16 v[208:211], v[112:115], v[228:231], v[208:211]\n\t" \
    "v_mfma_f32_16x16x32_bf16 v[208:211], v[116:119], v[232:235], v[208:211]\n\t" \
    "v_mfma_f32_16x16x32_bf16 v[200:203], v[96:99], v[228:231], v[200:203]\n\t" \
    "v_mfma_f32_16x16x32_bf16 v[200:203], v[100:103], v[232:235], v[200:203]\n\t" \
    "s_waitcnt lgkmcnt(4)\n\t" \
    "v_mfma_f32_16x16x32_bf16 v[204:207], v[120:123], v[236:239], v[204:207]\n\t" \
    "ds_read_b128 v[228:231], v253 offset:29696\n\t" \
    "s_waitcnt lgkmcnt(2)\n\t" \
    "v_mfma_f32_16x16x32_bf16 v[204:207], v[124:127], v[240:243], v[204:207]\n\t" \
    "ds_read_b128 v[232:235], v253 offset:10240\n\t" \
    "v_mfma_f32_16x16x32_bf16 v[208:211], v[128:131], v[236:239], v[208:211]\n\t" \
    "v_mfma_f32_16x16x32_bf16 v[208:211], v[132:135], v[240:243], v[208:211]\n\t" \
    "v_mfma_f32_16x16x32_bf16 v[200:203], v[244:247], v[236:239], v[200:203]\n\t" \
    "v_mfma_f32_16x16x32_bf16 v[200:203], v[248:251], v[240:243], v[200:203]\n\t" \
    "s_waitcnt lgkmcnt(2)\n\t" \
    "v_mfma_f32_16x16x32_bf16 v[216:219], v[120:123], v[224:227], v[216:219]\n\t" \
    "ds_read_b128 v[236:239], v253 offset:11264\n\t" \
    "s_waitcnt lgkmcnt(2)\n\t" \
    "v_mfma_f32_16x16x32_bf16 v[216:219], v[124:127], v[228:231], v[216:219]\n\t" \
    "ds_read_b128 v[240:243], v253 offset:12288\n\t" \
    "v_mfma_f32_16x16x32_bf16 v[220:223], v[128:131], v[224:227], v[220:223]\n\t" \
    "v_mfma_f32_16x16x32_bf16 v[220:223], v[132:135], v[228:231], v[220:223]\n\t" \
    "v_mfma_f32_16x16x32_bf16 v[212:215], v[244:247], v[224:227], v[212:215]\n\t" \
    "ds_read_b128 v[244:247], v254 offset:63488\n\t" \
    "v_mfma_f32_16x16x32_bf16 v[212:215], v[248:251], v[228:231], v[212:215]\n\t" \
    "ds_read_b128 v[248:251], v254 offset:64512\n\t" \
    "v_and_b32 v252, 0xf0, v253\n\t" \
    "v_lshlrev_b32 v252, 3, v252\n\t" \
    "buffer_load_dword v252, v252, s[60:63], %[fl] offen sc1\n\t" \
    "s_waitcnt lgkmcnt(4)\n\t" \
    "v_mfma_f32_16x16x32_bf16 v[204:207], v[144:147], v[232:235], v[204:207]\n\t" \
    "ds_read_b128 v[224:227], v253 offset:13312\n\t" \
    "s_waitcnt lgkmcnt(4)\n\t" \
    "v_mfma_f32_16x16x32_bf16 v[204:207], v[148:151], v[236:239], v[204:207]\n\t" \
    "ds_read_b128 v[228:231], v253 offset:32768\n\t" \
    "v_mfma_f32_16x16x32_bf16 v[208:211], v[152:155], v[232:235], v[208:211]\n\t" \
    "v_mfma_f32_16x16x32_bf16 v[208:211], v[156:159], v[236:239], v[208:211]\n\t" \
    "v_mfma_f32_16x16x32_bf16 v[200:203], v[136:139], v[232:235], v[200:203]\n\t" \
    "v_mfma_f32_16x16x32_bf16 v[200:203], v[140:143], v[236:239], v[200:203]\n\t" \
    "s_waitcnt lgkmcnt(4)\n\t" \
    "v_mfma_f32_16x16x32_bf16 v[204:207], v[160:163], v[240:243], v[204:207]\n\t" \
    "ds_read_b128 v[232:235], v253 offset:33792\n\t" \
    "s_waitcnt lgkmcnt(2)\n\t" \
    "v_mfma_f32_16x16x32_bf16 v[204:207], v[164:167], v[224:227], v[204:207]\n\t" \
    "ds_read_b128 v[236:239], v253 offset:14336\n\t" \
    "v_mfma_f32_16x16x32_bf16 v[208:211], v[168:171], v[240:243], v[208:211]\n\t" \
    "v_mfma_f32_16x16x32_bf16 v[208:211], v[172:175], v[224:227], v[208:211]\n\t" \
    "v_mfma_f32_16x16x32_bf16 v[200:203], v[244:247], v[240:243], v[200:203]\n\t" \
    "v_mfma_f32_16x16x32_bf16 v[200:203], v[248:251], v[224:227], v[200:203]\n\t" \
    "s_waitcnt lgkmcnt(2)\n\t" \
    "v_mfma_f32_16x16x32_bf16 v[216:219], v[160:163], v[228:231], v[216:219]\n\t" \
    "ds_read_b128 v[240:243], v253 offset:15360\n\t" \
    "s_waitcnt lgkmcnt(2)\n\t" \
    "v_mfma_f32_16x16x32_bf16 v[216:219], v[164:167], v[232:235], v[216:219]\n\t" \
    "ds_read_b128 v[224:227], v253 offset:38912\n\t" \
    "v_mfma_f32_16x16x32_bf16 v[220:223], v[168:171], v[228:231], v[220:223]\n\t" \
    "v_mfma_f32_16x16x32_bf16 v[220:223], v[172:175], v[232:235], v[220:223]\n\t" \
    "v_mfma_f32_16x16x32_bf16 v[212:215], v[244:247], v[228:231], v[212:215]\n\t" \
    "v_mfma_f32_16x16x32_bf16 v[212:215], v[248:251], v[232:235], v[212:215]\n\t" \
    "s_waitcnt lgkmcnt(2)\n\t" \
    "v_mfma_f32_16x16x32_bf16 v[204:207], v[184:187], v[236:239], v[204:207]\n\t" \
    "ds_read_b128 v[228:231], v253 offset:39936\n\t" \
    "s_waitcnt lgkmcnt(2)\n\t" \
    "v_mfma_f32_16x16x32_bf16 v[204:207], v[188:191], v[240:243], v[204:207]\n\t" \
    "ds_read_b128 v[232:235], v253 offset:22528\n\t" \
    "v_mfma_f32_16x16x32_bf16 v[208:211], v[192:195], v[236:239], v[208:211]\n\t" \
    "v_mfma_f32_16x16x32_bf16 v[208:211], v[196:199], v[240:243], v[208:211]\n\t" \
    "v_mfma_f32_16x16x32_bf16 v[200:203], v[176:179], v[236:239], v[200:203]\n\t" \
    "v_mfma_f32_16x16x32_bf16 v[200:203], v[180:183], v[240:243], v[200:203]\n\t" \
    "s_waitcnt lgkmcnt(2)\n\t" \
    "v_mfma_f32_16x16x32_bf16 v[216:219], v[24:27], v[224:227], v[216:219]\n\t" \
    "ds_read_b128 v[236:239], v253 offset:23552\n\t" \
    "s_waitcnt lgkmcnt(2)\n\t" \
    "v_mfma_f32_16x16x32_bf16 v[216:219], v[28:31], v[228:231], v[216:219]\n\t" \
    "ds_read_b128 v[240:243], v253 offset:26624\n\t" \
    "v_mfma_f32_16x16x32_bf16 v[220:223], v[32:35], v[224:227], v[220:223]\n\t" \
    "v_cvt_pk_bf16_f32 v204, v204, v205\n\t" \
    "v_mfma_f32_16x16x32_bf16 v[220:223], v[36:39], v[228:231], v[220:223]\n\t" \
    "v_cvt_pk_bf16_f32 v205, v206, v207\n\t" \
    "v_mfma_f32_16x16x32_bf16 v[212:215], v[16:19], v[224:227], v[212:215]\n\t" \
    "ds_write_b64 v254, v[204:205] offset:4104\n\t" \
    "v_mfma_f32_16x16x32_bf16 v[212:215], v[20:23], v[228:231], v[212:215]\n\t" \
    "v_cvt_pk_bf16_f32 v208, v208, v209\n\t" \
    "s_waitcnt lgkmcnt(3)\n\t" \
    "v_mfma_f32_16x16x32_bf16 v[216:219], v[64:67], v[232:235], v[216:219]\n\t" \
    "ds_read_b128 v[224:227], v253 offset:27648\n\t" \
    "v_cvt_pk_bf16_f32 v209, v210, v211\n\t" \
    "s_waitcnt lgkmcnt(3)\n\t" \
    "v_mfma_f32_16x16x32_bf16 v[216:219], v[68:71], v[236:239], v[216:219]\n\t" \
    "ds_read_b128 v[228:231], v253 offset:30720\n\t" \
    "ds_write_b64 v254, v[208:209] offset:5128\n\t" \
    "v_mfma_f32_16x16x32_bf16 v[220:223], v[72:75], v[232:235], v[220:223]\n\t" \
    "v_cvt_pk_bf16_f32 v200, v200, v201\n\t" \
    "v_mfma_f32_16x16x32_bf16 v[220:223], v[76:79], v[236:239], v[220:223]\n\t" \
    "v_cvt_pk_bf16_f32 v201, v202, v203\n\t" \
    "v_mfma_f32_16x16x32_bf16 v[212:215], v[56:59], v[232:235], v[212:215]\n\t" \
    "ds_write_b64 v254, v[200:201] offset:3080\n\t" \
    "v_mfma_f32_16x16x32_bf16 v[212:215], v[60:63], v[236:239], v[212:215]\n\t" \
    "s_waitcnt lgkmcnt(5)\n\t" \
    "v_mfma_f32_16x16x32_bf16 v[216:219], v[104:107], v[240:243], v[216:219]\n\t" \
    "ds_read_b128 v[232:235], v253 offset:31744\n\t" \
    "s_waitcnt lgkmcnt(4)\n\t" \
    "v_mfma_f32_16x16x32_bf16 v[216:219], v[108:111], v[224:227], v[216:219]\n\t" \
    "ds_read_b128 v[236:239], v253 offset:34816\n\t" \
    "v_mfma_f32_16x16x32_bf16 v[220:223], v[112:115], v[240:243], v[220:223]\n\t" \
    "v_mfma_f32_16x16x32_bf16 v[220:223], v[116:119], v[224:227], v[220:223]\n\t" \
    "v_mfma_f32_16x16x32_bf16 v[212:215], v[96:99], v[240:243], v[212:215]\n\t" \
    "v_mfma_f32_16x16x32_bf16 v[212:215], v[100:103], v[224:227], v[212:215]\n\t" \
    "ds_read_b128 v[240:243], v253 offset:35840\n\t" \
    "s_waitcnt lgkmcnt(3)\n\t" \
    "s_barrier \n\t" \
    "v_mfma_f32_16x16x32_bf16 v[216:219], v[144:147], v[228:231], v[216:219]\n\t" \
    "s_waitcnt lgkmcnt(2)\n\t" \
    "v_mfma_f32_16x16x32_bf16 v[216:219], v[148:151], v[232:235], v[216:219]\n\t" \
    "s_waitcnt lgkmcnt(1)\n\t" \
    "v_mfma_f32_16x16x32_bf16 v[216:219], v[184:187], v[236:239], v[216:219]\n\t" \
    "s_waitcnt lgkmcnt(0)\n\t" \
    "v_mfma_f32_16x16x32_bf16 v[216:219], v[188:191], v[240:243], v[216:219]\n\t" \
    "v_mfma_f32_16x16x32_bf16 v[220:223], v[152:155], v[228:231], v[220:223]\n\t" \
    "v_mfma_f32_16x16x32_bf16 v[220:223], v[156:159], v[232:235], v[220:223]\n\t" \
    "v_mfma_f32_16x16x32_bf16 v[220:223], v[192:195], v[236:239], v[220:223]\n\t" \
    "v_cvt_pk_bf16_f32 v216, v216, v217\n\t" \
    "v_cvt_pk_bf16_f32 v217, v218, v219\n\t" \
    "v_mfma_f32_16x16x32_bf16 v[220:223], v[196:199], v[240:243], v[220:223]\n\t" \
    "ds_write_b64 v254, v[216:217] offset:10248\n\t" \
    "v_mfma_f32_16x16x32_bf16 v[212:215], v[136:139], v[228:231], v[212:215]\n\t" \
    "v_mfma_f32_16x16x32_bf16 v[212:215], v[140:143], v[232:235], v[212:215]\n\t" \
    "v_mfma_f32_16x16x32_bf16 v[212:215], v[176:179], v[236:239], v[212:215]\n\t" \
    "v_cvt_pk_bf16_f32 v220, v220, v221\n\t" \
    "v_cvt_pk_bf16_f32 v221, v222, v223\n\t" \
    "v_mfma_f32_16x16x32_bf16 v[212:215], v[180:183], v[240:243], v[212:215]\n\t" \
    "ds_write_b64 v254, v[220:221] offset:11272\n\t" \
    "s_nop 6\n\t" \
    "v_cvt_pk_bf16_f32 v212, v212, v213\n\t" \
    "v_cvt_pk_bf16_f32 v213, v214, v215\n\t" \
    "ds_write_b64 v254, v[212:213] offset:9224\n\t" \
    "s_waitcnt vmcnt(0) lgkmcnt(0)\n\t" \
    "s_barrier \n\t" \
    "s_setprio 2\n\t" \
    "s_add_u32 s44, s40, 1\n\t" \
    "s_min_u32 s44, s44, 63\n\t" \
    ".Lv38_%=:\n\t" \
    "v_cmp_ge_u32 vcc, s44, v252\n\t" \
    "s_cbranch_vccz .Lv39_%=\n\t" \
    "s_cmp_eq_u32 s41, 0\n\t" \
    "s_cbranch_scc1 .Lv39_%=\n\t" \
    "s_sub_u32 s41, s41, 1\n\t" \
    "s_sleep 2\n\t" \
    "v_and_b32 v252, 0xf0, v253\n\t" \
    "v_lshlrev_b32 v252, 3, v252\n\t" \
    "buffer_load_dword v252, v252, s[60:63], %[fl] offen sc1\n\t" \
    "s_waitcnt vmcnt(0)\n\t" \
    "s_branch .Lv38_%=\n\t" \
    ".Lv39_%=:\n\t" \
    "s_mov_b32 m0, 0x3000\n\t" \
    "s_add_u32 s49, s42, 0x0\n\t" \
    "buffer_load_dwordx4 v253, s[60:63], s49 offen offset:0 sc1 lds\n\t" \
    "s_sleep 1\n\t" \
    "buffer_load_dwordx4 v253, s[60:63], s49 offen offset:1024 sc1 lds\n\t" \
    "s_sleep 1\n\t" \
    "buffer_load_dwordx4 v253, s[60:63], s49 offen offset:2048 sc1 lds\n\t" \
    "s_sleep 1\n\t" \
    "buffer_load_dwordx4 v253, s[60:63], s49 offen offset:3072 sc1 lds\n\t" \
    "s_sleep 1\n\t" \
    "s_mov_b32 m0, 0x4c00\n\t" \
    "s_add_u32 s49, s42, 0x4c00\n\t" \
    "buffer_load_dwordx4 v253, s[60:63], s49 offen offset:0 sc1 lds\n\t" \
    "s_sleep 1\n\t" \
    "s_mov_b32 m0, 0x8000\n\t" \
    "s_add_u32 s49, s42, 0x1000\n\t" \
    "buffer_load_dwordx4 v253, s[60:63], s49 offen offset:0 sc1 lds\n\t" \
    "s_sleep 1\n\t" \
    "buffer_load_dwordx4 v253, s[60:63], s49 offen offset:1024 sc1 lds\n\t" \
    "s_sleep 1\n\t" \
    "buffer_load_dwordx4 v253, s[60:63], s49 offen offset:2048 sc1 lds\n\t" \
    "s_sleep 1\n\t" \
    "buffer_load_dwordx4 v253, s[60:63], s49 offen offset:3072 sc1 lds\n\t" \
    "s_sleep 1\n\t" \
    "s_mov_b32 m0, 0x9c00\n\t" \
    "s_add_u32 s49, s42, 0x5c00\n\t" \
    "buffer_load_dwordx4 v253, s[60:63], s49 offen offset:0 sc1 lds\n\t" \
    "s_sleep 1\n\t" \
    "ds_read_b128 v[200:203], v254 offset:55296\n\t" \
    "ds_read_b128 v[204:207], v254 offset:56320\n\t" \
    "ds_read_b128 v[216:219], v254 offset:57344\n\t" \
    "ds_read_b128 v[220:223], v254 offset:58368\n\t" \
    "v_mov_b64 v[208:209], 0\n\t" \
    "v_mov_b64 v[210:211], 0\n\t" \
    "v_mov_b64 v[244:245], 0\n\t" \
    "v_mov_b64 v[246:247], 0\n\t" \
    "v_mov_b64 v[248:249], 0\n\t" \
    "v_mov_b64 v[250:251], 0\n\t" \
    "s_sleep 5\n\t" \
    "s_mov_b32 m0, 0xc000\n\t" \
    "s_nop 0\n\t" \
    "buffer_load_dwordx4 v253, s[60:63], s46 offen sc1 lds\n\t" \
    "s_mov_b32 m0, 0xc400\n\t" \
    "s_nop 0\n\t" \
    "buffer_load_dwordx4 v253, s[60:63], s47 offen sc1 lds\n\t" \
    "s_waitcnt lgkmcnt(0)\n\t" \
    "v_bfe_i32 v212, v253, 5, 1\n\t" \
    "v_and_b32 v212, 0xaaaa, v212\n\t" \
    "v_or_b32 v212, 0x4444, v212\n\t" \
    "v_and_b32 v213, 0xffff, v255\n\t" \
    "v_lshrrev_b32 v214, 16, v255\n\t" \
    ".Lv310_%=:\n\t" \
    "s_waitcnt vmcnt(0)\n\t" \
    "ds_read_b128 v[224:227], v253 offset:49152\n\t" \
    "ds_read_b128 v[228:231], v253 offset:50176\n\t" \
    "s_waitcnt lgkmcnt(1)\n\t" \
    "v_max_u32 v215, v224, v225\n\t" \
    "v_max3_u32 v215, v226, v227, v215\n\t" \
    "s_waitcnt lgkmcnt(0)\n\t" \
    "v_max3_u32 v215, v228, v229, v215\n\t" \
    "v_max3_u32 v215, v230, v231, v215\n\t" \
    "v_cmp_eq_u32 vcc, -1, v215\n\t" \
    "s_cbranch_vccz .Lv311_%=\n\t" \
    "v_max_u32 v215, v224, v225\n\t" \
    "v_max3_u32 v215, v226, v227, v215\n\t" \
    "v_cmp_eq_u32 vcc, -1, v215\n\t" \
    "s_cbranch_vccz .Lv312_%=\n\t" \
    "s_mov_b32 m0, 0xc000\n\t" \
    "s_nop 0\n\t" \
    "buffer_load_dwordx4 v253, s[60:63], s46 offen sc1 lds\n\t" \
    ".Lv312_%=:\n\t" \
    "v_max_u32 v215, v228, v229\n\t" \
    "v_max3_u32 v215, v230, v231, v215\n\t" \
    "v_cmp_eq_u32 vcc, -1, v215\n\t" \
    "s_cbranch_vccz .Lv313_%=\n\t" \
    "s_mov_b32 m0, 0xc400\n\t" \
    "s_nop 0\n\t" \
    "buffer_load_dwordx4 v253, s[60:63], s47 offen sc1 lds\n\t" \
    ".Lv313_%=:\n\t" \
    "s_cmp_eq_u32 s41, 0\n\t" \
    "s_cbranch_scc1 .Lv311_%=\n\t" \
    "s_sub_u32 s41, s41, 1\n\t" \
    "s_branch .Lv310_%=\n\t" \
    ".Lv311_%=:\n\t" \
    "s_waitcnt lgkmcnt(0)\n\t" \
    "s_barrier \n\t" \
    "ds_read_b128 v[232:235], v253 offset:40960\n\t" \
    "ds_read_b128 v[236:239], v253 offset:41984\n\t" \
    "ds_read_b128 v[240:243], v253 offset:43008\n\t" \
    "v_smfmac_f32_16x16x64_bf16 v[244:247], v[224:227], v[0:7], v212\n\t" \
    "v_smfmac_f32_16x16x64_bf16 v[248:251], v[224:227], v[8:15], v212\n\t" \
    "v_smfmac_f32_16x16x64_bf16 v[208:211], v[224:227], v[200:207], v212\n\t" \
    "ds_read_b128 v[200:203], v254 offset:59392\n\t" \
    "ds_read_b128 v[204:207], v254 offset:60416\n\t" \
    "ds_read_b128 v[224:227], v253 offset:44032\n\t" \
    "v_smfmac_f32_16x16x64_bf16 v[208:211], v[228:231], v[16:23], v212\n\t" \
    "v_smfmac_f32_16x16x64_bf16 v[244:247], v[228:231], v[24:31], v212\n\t" \
    "v_smfmac_f32_16x16x64_bf16 v[248:251], v[228:231], v[32:39], v212\n\t" \
    "ds_read_b128 v[228:231], v253 offset:45056\n\t" \
    "s_waitcnt lgkmcnt(6)\n\t" \
    "v_smfmac_f32_16x16x64_bf16 v[208:211], v[232:235], v[216:223], v212\n\t" \
    "ds_read_b128 v[216:219], v254 offset:61440\n\t" \
    "ds_read_b128 v[220:223], v254 offset:62464\n\t" \
    "v_smfmac_f32_16x16x64_bf16 v[244:247], v[232:235], v[40:47], v212\n\t" \
    "v_smfmac_f32_16x16x64_bf16 v[248:251], v[232:235], v[48:55], v212\n\t" \
    "ds_read_b128 v[232:235], v253 offset:46080\n\t" \
    "s_waitcnt lgkmcnt(8)\n\t" \
    "v_smfmac_f32_16x16x64_bf16 v[208:211], v[236:239], v[56:63], v212\n\t" \
    "v_smfmac_f32_16x16x64_bf16 v[244:247], v[236:239], v[64:71], v212\n\t" \
    "v_smfmac_f32_16x16x64_bf16 v[248:251], v[236:239], v[72:79], v212\n\t" \
    "ds_read_b128 v[236:239], v253 offset:47104\n\t" \
    "s_waitcnt lgkmcnt(6)\n\t" \
    "v_smfmac_f32_16x16x64_bf16 v[208:211], v[240:243], v[200:207], v212\n\t" \
    "ds_read_b128 v[200:203], v254 offset:63488\n\t" \
    "ds_read_b128 v[204:207], v254 offset:64512\n\t" \
    "v_smfmac_f32_16x16x64_bf16 v[244:247], v[240:243], v[80:87], v212\n\t" \
    "v_smfmac_f32_16x16x64_bf16 v[248:251], v[240:243], v[88:95], v212\n\t" \
    "ds_read_b128 v[240:243], v253 offset:48128\n\t" \
    "s_waitcnt lgkmcnt(8)\n\t" \
    "v_smfmac_f32_16x16x64_bf16 v[208:211], v[224:227], v[96:103], v212\n\t" \
    "v_smfmac_f32_16x16x64_bf16 v[244:247], v[224:227], v[104:111], v212\n\t" \
    "v_smfmac_f32_16x16x64_bf16 v[248:251], v[224:227], v[112:119], v212\n\t" \
    "s_waitcnt lgkmcnt(7)\n\t" \
    "v_smfmac_f32_16x16x64_bf16 v[244:247], v[228:231], v[120:127], v212\n\t" \
    "s_waitcnt lgkmcnt(4)\n\t" \
    "v_smfmac_f32_16x16x64_bf16 v[244:247], v[232:235], v[144:151], v212\n\t" \
    "s_waitcnt lgkmcnt(3)\n\t" \
    "v_smfmac_f32_16x16x64_bf16 v[244:247], v[236:239], v[160:167], v212\n\t" \
    "s_waitcnt lgkmcnt(0)\n\t" \
    "v_smfmac_f32_16x16x64_bf16 v[244:247], v[240:243], v[184:191], v212\n\t" \
    "v_smfmac_f32_16x16x64_bf16 v[248:251], v[228:231], v[128:135], v212\n\t" \
    "v_smfmac_f32_16x16x64_bf16 v[248:251], v[232:235], v[152:159], v212\n\t" \
    "v_smfmac_f32_16x16x64_bf16 v[248:251], v[236:239], v[168:175], v212\n\t" \
    "v_add_f32 v244, v244, v246\n\t" \
    "v_add_f32 v245, v245, v247\n\t" \
    "v_cvt_pk_bf16_f32 v244, v244, v245\n\t" \
    "v_smfmac_f32_16x16x64_bf16 v[248:251], v[240:243], v[192:199], v212\n\t" \
    "ds_write_b16 v213, v244 offset:512\n\t" \
    "ds_write_b16_d16_hi v213, v244 offset:528\n\t" \
    "v_smfmac_f32_16x16x64_bf16 v[208:211], v[228:231], v[216:223], v212\n\t" \
    "v_smfmac_f32_16x16x64_bf16 v[208:211], v[232:235], v[136:143], v212\n\t" \
    "v_smfmac_f32_16x16x64_bf16 v[208:211], v[236:239], v[200:207], v212\n\t" \
    "v_add_f32 v248, v248, v250\n\t" \
    "v_add_f32 v249, v249, v251\n\t" \
    "v_cvt_pk_bf16_f32 v248, v248, v249\n\t" \
    "v_smfmac_f32_16x16x64_bf16 v[208:211], v[240:243], v[176:183], v212\n\t" \
    "ds_write_b16 v213, v248 offset:1024\n\t" \
    "ds_write_b16_d16_hi v213, v248 offset:1040\n\t" \
    "s_nop 5\n\t" \
    "v_add_f32 v208, v208, v210\n\t" \
    "v_add_f32 v209, v209, v211\n\t" \
    "v_cvt_pk_bf16_f32 v208, v208, v209\n\t" \
    "ds_write_b16 v213, v208 offset:0\n\t" \
    "ds_write_b16_d16_hi v213, v208 offset:16\n\t" \
    "s_setprio 0\n\t" \
    "s_waitcnt lgkmcnt(0)\n\t" \
    "s_barrier \n\t" \
    "ds_read_b128 v[224:227], v253 offset:16384\n\t" \
    "ds_read_b128 v[244:247], v254 offset:55296\n\t" \
    "ds_read_b128 v[228:231], v253 offset:17408\n\t" \
    "ds_read_b128 v[232:235], v253 offset:36864\n\t" \
    "ds_read_b128 v[236:239], v253 offset:37888\n\t" \
    "ds_read_b128 v[248:251], v254 offset:56320\n\t" \
    "s_add_u32 s40, s40, 1\n\t" \
    "s_cmp_lg_u32 s40, 64\n\t" \
    "s_cbranch_scc1 .Lv37_%=\n\t" \
    "s_branch .Lend%=\n\t" \
    ".Lv2_%=:\n\t" \
    "s_mov_b32 s42, %[xs]\n\t" \
    "v_add_u32 v244, 0xc000, v243\n\t" \
    "s_waitcnt vmcnt(12) lgkmcnt(0)\n\t" \
    "s_barrier \n\t" \
    "s_add_u32 m0, %[wm], 0x9000\n\t" \
    "s_mov_b32 s44, 0x1e0000\n\t" \
    "buffer_load_dwordx4 v240, %[wr], s44 offen offset:0 nt lds\n\t" \
    "buffer_load_dwordx4 v241, %[wr], s44 offen offset:1024 nt lds\n\t" \
    "buffer_load_dwordx4 v242, %[wr], s44 offen offset:2048 nt lds\n\t" \
    "ds_read_b64_tr_b16 v[208:209], v244 offset:0\n\t" \
    "ds_read_b64_tr_b16 v[210:211], v244 offset:1536\n\t" \
    "ds_read_b64_tr_b16 v[40:41], v244 offset:32\n\t" \
    "ds_read_b64_tr_b16 v[42:43], v244 offset:1568\n\t" \
    "ds_read_b64_tr_b16 v[48:49], v244 offset:64\n\t" \
    "ds_read_b64_tr_b16 v[50:51], v244 offset:1600\n\t" \
    "v_add_u32 v244, 0xf000, v243\n\t" \
    "s_waitcnt vmcnt(12) lgkmcnt(0)\n\t" \
    "s_barrier \n\t" \
    "s_add_u32 m0, %[wm], 0xc000\n\t" \
    "s_mov_b32 s44, 0x240000\n\t" \
    "buffer_load_dwordx4 v240, %[wr], s44 offen offset:0 nt lds\n\t" \
    "buffer_load_dwordx4 v241, %[wr], s44 offen offset:1024 nt lds\n\t" \
    "buffer_load_dwordx4 v242, %[wr], s44 offen offset:2048 nt lds\n\t" \
    "ds_read_b64_tr_b16 v[212:213], v244 offset:0\n\t" \
    "ds_read_b64_tr_b16 v[214:215], v244 offset:1536\n\t" \
    "ds_read_b64_tr_b16 v[44:45], v244 offset:32\n\t" \
    "ds_read_b64_tr_b16 v[46:47], v244 offset:1568\n\t" \
    "ds_read_b64_tr_b16 v[52:53], v244 offset:64\n\t" \
    "ds_read_b64_tr_b16 v[54:55], v244 offset:1600\n\t" \
    "v_add_u32 v244, 0x0, v243\n\t" \
    "s_waitcnt vmcnt(12) lgkmcnt(0)\n\t" \
    "s_barrier \n\t" \
    "s_add_u32 m0, %[wm], 0xf000\n\t" \
    "s_mov_b32 s44, 0x2a0000\n\t" \
    "buffer_load_dwordx4 v240, %[wr], s44 offen offset:0 nt lds\n\t" \
    "buffer_load_dwordx4 v241, %[wr], s44 offen offset:1024 nt lds\n\t" \
    "buffer_load_dwordx4 v242, %[wr], s44 offen offset:2048 nt lds\n\t" \
    "ds_read_b64_tr_b16 v[56:57], v244 offset:0\n\t" \
    "ds_read_b64_tr_b16 v[58:59], v244 offset:1536\n\t" \
    "ds_read_b64_tr_b16 v[64:65], v244 offset:32\n\t" \
    "ds_read_b64_tr_b16 v[66:67], v244 offset:1568\n\t" \
    "ds_read_b64_tr_b16 v[72:73], v244 offset:64\n\t" \
    "ds_read_b64_tr_b16 v[74:75], v244 offset:1600\n\t" \
    "v_add_u32 v244, 0x3000, v243\n\t" \
    "s_waitcnt vmcnt(12) lgkmcnt(0)\n\t" \
    "s_barrier \n\t" \
    "s_add_u32 m0, %[wm], 0x0\n\t" \
    "s_mov_b32 s44, 0x300000\n\t" \
    "buffer_load_dwordx4 v240, %[wr], s44 offen offset:0 nt lds\n\t" \
    "buffer_load_dwordx4 v241, %[wr], s44 offen offset:1024 nt lds\n\t" \
    "buffer_load_dwordx4 v242, %[wr], s44 offen offset:2048 nt lds\n\t" \
    "ds_read_b64_tr_b16 v[60:61], v244 offset:0\n\t" \
    "ds_read_b64_tr_b16 v[62:63], v244 offset:1536\n\t" \
    "ds_read_b64_tr_b16 v[68:69], v244 offset:32\n\t" \
    "ds_read_b64_tr_b16 v[70:71], v244 offset:1568\n\t" \
    "ds_read_b64_tr_b16 v[76:77], v244 offset:64\n\t" \
    "ds_read_b64_tr_b16 v[78:79], v244 offset:1600\n\t" \
    "v_add_u32 v244, 0x6000, v243\n\t" \
    "s_waitcnt vmcnt(12) lgkmcnt(0)\n\t" \
    "s_barrier \n\t" \
    "s_add_u32 m0, %[wm], 0x3000\n\t" \
    "s_mov_b32 s44, 0x360000\n\t" \
    "buffer_load_dwordx4 v240, %[wr], s44 offen offset:0 nt lds\n\t" \
    "buffer_load_dwordx4 v241, %[wr], s44 offen offset:1024 nt lds\n\t" \
    "buffer_load_dwordx4 v242, %[wr], s44 offen offset:2048 nt lds\n\t" \
    "ds_read_b64_tr_b16 v[216:217], v244 offset:0\n\t" \
    "ds_read_b64_tr_b16 v[218:219], v244 offset:1536\n\t" \
    "ds_read_b64_tr_b16 v[80:81], v244 offset:32\n\t" \
    "ds_read_b64_tr_b16 v[82:83], v244 offset:1568\n\t" \
    "ds_read_b64_tr_b16 v[88:89], v244 offset:64\n\t" \
    "ds_read_b64_tr_b16 v[90:91], v244 offset:1600\n\t" \
    "v_add_u32 v244, 0x9000, v243\n\t" \
    "s_waitcnt vmcnt(12) lgkmcnt(0)\n\t" \
    "s_barrier \n\t" \
    "s_add_u32 m0, %[wm], 0x6000\n\t" \
    "s_mov_b32 s44, 0x3c0000\n\t" \
    "buffer_load_dwordx4 v240, %[wr], s44 offen offset:0 nt lds\n\t" \
    "buffer_load_dwordx4 v241, %[wr], s44 offen offset:1024 nt lds\n\t" \
    "buffer_load_dwordx4 v242, %[wr], s44 offen offset:2048 nt lds\n\t" \
    "ds_read_b64_tr_b16 v[220:221], v244 offset:0\n\t" \
    "ds_read_b64_tr_b16 v[222:223], v244 offset:1536\n\t" \
    "ds_read_b64_tr_b16 v[84:85], v244 offset:32\n\t" \
    "ds_read_b64_tr_b16 v[86:87], v244 offset:1568\n\t" \
    "ds_read_b64_tr_b16 v[92:93], v244 offset:64\n\t" \
    "ds_read_b64_tr_b16 v[94:95], v244 offset:1600\n\t" \
    "v_add_u32 v244, 0xc000, v243\n\t" \
    "s_waitcnt vmcnt(12) lgkmcnt(0)\n\t" \
    "s_barrier \n\t" \
    "s_add_u32 m0, %[wm], 0x9000\n\t" \
    "s_mov_b32 s44, 0x420000\n\t" \
    "buffer_load_dwordx4 v240, %[wr], s44 offen offset:0 nt lds\n\t" \
    "buffer_load_dwordx4 v241, %[wr], s44 offen offset:1024 nt lds\n\t" \
    "buffer_load_dwordx4 v242, %[wr], s44 offen offset:2048 nt lds\n\t" \
    "ds_read_b64_tr_b16 v[96:97], v244 offset:0\n\t" \
    "ds_read_b64_tr_b16 v[98:99], v244 offset:1536\n\t" \
    "ds_read_b64_tr_b16 v[104:105], v244 offset:32\n\t" \
    "ds_read_b64_tr_b16 v[106:107], v244 offset:1568\n\t" \
    "ds_read_b64_tr_b16 v[112:113], v244 offset:64\n\t" \
    "ds_read_b64_tr_b16 v[114:115], v244 offset:1600\n\t" \
    "v_add_u32 v244, 0xf000, v243\n\t" \
    "s_waitcnt vmcnt(12) lgkmcnt(0)\n\t" \
    "s_barrier \n\t" \
    "s_add_u32 m0, %[wm], 0xc000\n\t" \
    "s_mov_b32 s44, 0x480000\n\t" \
    "buffer_load_dwordx4 v240, %[wr], s44 offen offset:0 nt lds\n\t" \
    "buffer_load_dwordx4 v241, %[wr], s44 offen offset:1024 nt lds\n\t" \
    "buffer_load_dwordx4 v242, %[wr], s44 offen offset:2048 nt lds\n\t" \
    "ds_read_b64_tr_b16 v[100:101], v244 offset:0\n\t" \
    "ds_read_b64_tr_b16 v[102:103], v244 offset:1536\n\t" \
    "ds_read_b64_tr_b16 v[108:109], v244 offset:32\n\t" \
    "ds_read_b64_tr_b16 v[110:111], v244 offset:1568\n\t" \
    "ds_read_b64_tr_b16 v[116:117], v244 offset:64\n\t" \
    "ds_read_b64_tr_b16 v[118:119], v244 offset:1600\n\t" \
    "v_add_u32 v244, 0x0, v243\n\t" \
    "s_waitcnt vmcnt(12) lgkmcnt(0)\n\t" \
    "s_barrier \n\t" \
    "v_mov_b32 v246, 0\n\t" \
    "buffer_load_dword v246, v246, s[60:63], %[mo] offen sc1\n\t" \
    "s_add_u32 m0, %[wm], 0xf000\n\t" \
    "s_mov_b32 s44, 0x4e0000\n\t" \
    "buffer_load_dwordx4 v240, %[wr], s44 offen offset:0 nt lds\n\t" \
    "buffer_load_dwordx4 v241, %[wr], s44 offen offset:1024 nt lds\n\t" \
    "buffer_load_dwordx4 v242, %[wr], s44 offen offset:2048 nt lds\n\t" \
    "ds_read_b64_tr_b16 v[200:201], v244 offset:0\n\t" \
    "ds_read_b64_tr_b16 v[202:203], v244 offset:1536\n\t" \
    "ds_read_b64_tr_b16 v[0:1], v244 offset:32\n\t" \
    "ds_read_b64_tr_b16 v[2:3], v244 offset:1568\n\t" \
    "ds_read_b64_tr_b16 v[8:9], v244 offset:64\n\t" \
    "ds_read_b64_tr_b16 v[10:11], v244 offset:1600\n\t" \
    "v_add_u32 v244, 0x3000, v243\n\t" \
    "s_waitcnt vmcnt(12) lgkmcnt(0)\n\t" \
    "s_barrier \n\t" \
    "s_add_u32 m0, %[wm], 0x0\n\t" \
    "s_mov_b32 s44, 0x540000\n\t" \
    "buffer_load_dwordx4 v240, %[wr], s44 offen offset:0 nt lds\n\t" \
    "buffer_load_dwordx4 v241, %[wr], s44 offen offset:1024 nt lds\n\t" \
    "buffer_load_dwordx4 v242, %[wr], s44 offen offset:2048 nt lds\n\t" \
    "ds_read_b64_tr_b16 v[204:205], v244 offset:0\n\t" \
    "ds_read_b64_tr_b16 v[206:207], v244 offset:1536\n\t" \
    "ds_read_b64_tr_b16 v[4:5], v244 offset:32\n\t" \
    "ds_read_b64_tr_b16 v[6:7], v244 offset:1568\n\t" \
    "ds_read_b64_tr_b16 v[12:13], v244 offset:64\n\t" \
    "ds_read_b64_tr_b16 v[14:15], v244 offset:1600\n\t" \
    "v_add_u32 v244, 0x6000, v243\n\t" \
    "s_waitcnt vmcnt(12) lgkmcnt(0)\n\t" \
    "s_barrier \n\t" \
    "s_add_u32 m0, %[wm], 0x3000\n\t" \
    "s_mov_b32 s44, 0x5a0000\n\t" \
    "buffer_load_dwordx4 v240, %[wr], s44 offen offset:0 nt lds\n\t" \
    "buffer_load_dwordx4 v241, %[wr], s44 offen offset:1024 nt lds\n\t" \
    "buffer_load_dwordx4 v242, %[wr], s44 offen offset:2048 nt lds\n\t" \
    "ds_read_b64_tr_b16 v[224:225], v244 offset:0\n\t" \
    "ds_read_b64_tr_b16 v[226:227], v244 offset:1536\n\t" \
    "ds_read_b64_tr_b16 v[120:121], v244 offset:32\n\t" \
    "ds_read_b64_tr_b16 v[122:123], v244 offset:1568\n\t" \
    "ds_read_b64_tr_b16 v[128:129], v244 offset:64\n\t" \
    "ds_read_b64_tr_b16 v[130:131], v244 offset:1600\n\t" \
    "v_add_u32 v244, 0x9000, v243\n\t" \
    "s_waitcnt vmcnt(12) lgkmcnt(0)\n\t" \
    "s_barrier \n\t" \
    "s_add_u32 m0, %[wm], 0x6000\n\t" \
    "s_mov_b32 s44, 0x600000\n\t" \
    "buffer_load_dwordx4 v240, %[wr], s44 offen offset:0 nt lds\n\t" \
    "buffer_load_dwordx4 v241, %[wr], s44 offen offset:1024 nt lds\n\t" \
    "buffer_load_dwordx4 v242, %[wr], s44 offen offset:2048 nt lds\n\t" \
    "ds_read_b64_tr_b16 v[228:229], v244 offset:0\n\t" \
    "ds_read_b64_tr_b16 v[230:231], v244 offset:1536\n\t" \
    "ds_read_b64_tr_b16 v[124:125], v244 offset:32\n\t" \
    "ds_read_b64_tr_b16 v[126:127], v244 offset:1568\n\t" \
    "ds_read_b64_tr_b16 v[132:133], v244 offset:64\n\t" \
    "ds_read_b64_tr_b16 v[134:135], v244 offset:1600\n\t" \
    "v_add_u32 v244, 0xc000, v243\n\t" \
    "s_waitcnt vmcnt(12) lgkmcnt(0)\n\t" \
    "s_barrier \n\t" \
    "s_add_u32 m0, %[wm], 0x9000\n\t" \
    "s_mov_b32 s44, 0x660000\n\t" \
    "buffer_load_dwordx4 v240, %[wr], s44 offen offset:0 nt lds\n\t" \
    "buffer_load_dwordx4 v241, %[wr], s44 offen offset:1024 nt lds\n\t" \
    "buffer_load_dwordx4 v242, %[wr], s44 offen offset:2048 nt lds\n\t" \
    "ds_read_b64_tr_b16 v[136:137], v244 offset:0\n\t" \
    "ds_read_b64_tr_b16 v[138:139], v244 offset:1536\n\t" \
    "ds_read_b64_tr_b16 v[144:145], v244 offset:32\n\t" \
    "ds_read_b64_tr_b16 v[146:147], v244 offset:1568\n\t" \
    "ds_read_b64_tr_b16 v[152:153], v244 offset:64\n\t" \
    "ds_read_b64_tr_b16 v[154:155], v244 offset:1600\n\t" \
    "v_add_u32 v244, 0xf000, v243\n\t" \
    "s_waitcnt vmcnt(12) lgkmcnt(0)\n\t" \
    "s_barrier \n\t" \
    ".Lv21_%=:\n\t" \
    "v_readfirstlane_b32 s43, v246\n\t" \
    "s_nop 0\n\t" \
    "s_cmp_lg_u32 s43, -1\n\t" \
    "s_cbranch_scc1 .Lv22_%=\n\t" \
    "s_cmp_eq_u32 s41, 0\n\t" \
    "s_cbranch_scc1 .Lv22_%=\n\t" \
    "s_sub_u32 s41, s41, 1\n\t" \
    "s_sleep 4\n\t" \
    "v_mov_b32 v246, 0\n\t" \
    "buffer_load_dword v246, v246, s[60:63], %[mo] offen sc1\n\t" \
    "s_waitcnt vmcnt(0)\n\t" \
    "s_branch .Lv21_%=\n\t" \
    ".Lv22_%=:\n\t" \
    "v_mov_b32 v246, 0x8000\n\t" \
    "s_mov_b64 exec, 1\n\t" \
    "buffer_store_dword v246, v246, s[60:63], %[mo] offen sc1\n\t" \
    "s_mov_b64 exec, -1\n\t" \
    "s_and_b32 s44, s43, 0x5000\n\t" \
    "s_xor_b32 %[mb], %[mb], s44\n\t" \
    "s_xor_b32 %[fl], %[fl], s44\n\t" \
    "s_and_b32 s43, s43, 0x2000\n\t" \
    "s_xor_b32 s43, s43, %[lx]\n\t" \
    "s_add_u32 s60, s60, s43\n\t" \
    "s_addc_u32 s61, s61, 0\n\t" \
    "v_and_b32 v252, 0xf0, v253\n\t" \
    "v_lshlrev_b32 v252, 3, v252\n\t" \
    "buffer_load_dword v252, v252, s[60:63], %[fl] offen sc1\n\t" \
    "s_add_u32 m0, %[wm], 0xc000\n\t" \
    "s_mov_b32 s44, 0x6c0000\n\t" \
    "buffer_load_dwordx4 v240, %[wr], s44 offen offset:0 nt lds\n\t" \
    "buffer_load_dwordx4 v241, %[wr], s44 offen offset:1024 nt lds\n\t" \
    "buffer_load_dwordx4 v242, %[wr], s44 offen offset:2048 nt lds\n\t" \
    "ds_read_b64_tr_b16 v[140:141], v244 offset:0\n\t" \
    "ds_read_b64_tr_b16 v[142:143], v244 offset:1536\n\t" \
    "ds_read_b64_tr_b16 v[148:149], v244 offset:32\n\t" \
    "ds_read_b64_tr_b16 v[150:151], v244 offset:1568\n\t" \
    "ds_read_b64_tr_b16 v[156:157], v244 offset:64\n\t" \
    "ds_read_b64_tr_b16 v[158:159], v244 offset:1600\n\t" \
    "v_add_u32 v244, 0x0, v243\n\t" \
    "s_waitcnt vmcnt(12) lgkmcnt(0)\n\t" \
    "s_barrier \n\t" \
    "s_add_u32 m0, %[wm], 0xf000\n\t" \
    "s_mov_b32 s44, 0x720000\n\t" \
    "buffer_load_dwordx4 v240, %[wr], s44 offen offset:0 nt lds\n\t" \
    "buffer_load_dwordx4 v241, %[wr], s44 offen offset:1024 nt lds\n\t" \
    "buffer_load_dwordx4 v242, %[wr], s44 offen offset:2048 nt lds\n\t" \
    "ds_read_b64_tr_b16 v[16:17], v244 offset:0\n\t" \
    "ds_read_b64_tr_b16 v[18:19], v244 offset:1536\n\t" \
    "ds_read_b64_tr_b16 v[24:25], v244 offset:32\n\t" \
    "ds_read_b64_tr_b16 v[26:27], v244 offset:1568\n\t" \
    "ds_read_b64_tr_b16 v[32:33], v244 offset:64\n\t" \
    "ds_read_b64_tr_b16 v[34:35], v244 offset:1600\n\t" \
    "v_add_u32 v244, 0x3000, v243\n\t" \
    "s_waitcnt vmcnt(12) lgkmcnt(0)\n\t" \
    "s_barrier \n\t" \
    "ds_read_b64_tr_b16 v[20:21], v244 offset:0\n\t" \
    "ds_read_b64_tr_b16 v[22:23], v244 offset:1536\n\t" \
    "ds_read_b64_tr_b16 v[28:29], v244 offset:32\n\t" \
    "ds_read_b64_tr_b16 v[30:31], v244 offset:1568\n\t" \
    "ds_read_b64_tr_b16 v[36:37], v244 offset:64\n\t" \
    "ds_read_b64_tr_b16 v[38:39], v244 offset:1600\n\t" \
    "v_add_u32 v244, 0x6000, v243\n\t" \
    "s_waitcnt vmcnt(9) lgkmcnt(0)\n\t" \
    "s_barrier \n\t" \
    "ds_read_b64_tr_b16 v[232:233], v244 offset:0\n\t" \
    "ds_read_b64_tr_b16 v[234:235], v244 offset:1536\n\t" \
    "ds_read_b64_tr_b16 v[160:161], v244 offset:32\n\t" \
    "ds_read_b64_tr_b16 v[162:163], v244 offset:1568\n\t" \
    "ds_read_b64_tr_b16 v[168:169], v244 offset:64\n\t" \
    "ds_read_b64_tr_b16 v[170:171], v244 offset:1600\n\t" \
    "v_add_u32 v244, 0x9000, v243\n\t" \
    "s_waitcnt vmcnt(6) lgkmcnt(0)\n\t" \
    "s_barrier \n\t" \
    "ds_read_b64_tr_b16 v[236:237], v244 offset:0\n\t" \
    "ds_read_b64_tr_b16 v[238:239], v244 offset:1536\n\t" \
    "ds_read_b64_tr_b16 v[164:165], v244 offset:32\n\t" \
    "ds_read_b64_tr_b16 v[166:167], v244 offset:1568\n\t" \
    "ds_read_b64_tr_b16 v[172:173], v244 offset:64\n\t" \
    "ds_read_b64_tr_b16 v[174:175], v244 offset:1600\n\t" \
    "v_add_u32 v244, 0xc000, v243\n\t" \
    "s_waitcnt vmcnt(3) lgkmcnt(0)\n\t" \
    "s_barrier \n\t" \
    "ds_read_b64_tr_b16 v[176:177], v244 offset:0\n\t" \
    "ds_read_b64_tr_b16 v[178:179], v244 offset:1536\n\t" \
    "ds_read_b64_tr_b16 v[184:185], v244 offset:32\n\t" \
    "ds_read_b64_tr_b16 v[186:187], v244 offset:1568\n\t" \
    "ds_read_b64_tr_b16 v[192:193], v244 offset:64\n\t" \
    "ds_read_b64_tr_b16 v[194:195], v244 offset:1600\n\t" \
    "s_mov_b32 s43, 0\n\t" \
    "v_cmp_eq_u32 vcc, 0, v252\n\t" \
    "s_cbranch_vccnz .Lv23_%=\n\t" \
    "s_mov_b32 m0, 0x2000\n\t" \
    "s_add_u32 s49, s42, 0x0\n\t" \
    "buffer_load_dwordx4 v253, s[60:63], s49 offen offset:0 sc1 lds\n\t" \
    "buffer_load_dwordx4 v253, s[60:63], s49 offen offset:1024 sc1 lds\n\t" \
    "buffer_load_dwordx4 v253, s[60:63], s49 offen offset:2048 sc1 lds\n\t" \
    "buffer_load_dwordx4 v253, s[60:63], s49 offen offset:3072 sc1 lds\n\t" \
    "s_mov_b32 m0, 0x4800\n\t" \
    "s_add_u32 s49, s42, 0x8800\n\t" \
    "buffer_load_dwordx4 v253, s[60:63], s49 offen offset:0 sc1 lds\n\t" \
    "s_mov_b32 m0, 0x7000\n\t" \
    "s_add_u32 s49, s42, 0x1000\n\t" \
    "buffer_load_dwordx4 v253, s[60:63], s49 offen offset:0 sc1 lds\n\t" \
    "buffer_load_dwordx4 v253, s[60:63], s49 offen offset:1024 sc1 lds\n\t" \
    "buffer_load_dwordx4 v253, s[60:63], s49 offen offset:2048 sc1 lds\n\t" \
    "buffer_load_dwordx4 v253, s[60:63], s49 offen offset:3072 sc1 lds\n\t" \
    "s_mov_b32 m0, 0x9800\n\t" \
    "s_add_u32 s49, s42, 0x9800\n\t" \
    "buffer_load_dwordx4 v253, s[60:63], s49 offen offset:0 sc1 lds\n\t" \
    "s_mov_b32 s43, 1\n\t" \
    ".Lv23_%=:\n\t" \
    "v_add_u32 v244, 0xf000, v243\n\t" \
    "s_waitcnt vmcnt(0) lgkmcnt(0)\n\t" \
    "s_barrier \n\t" \
    "ds_read_b64_tr_b16 v[180:181], v244 offset:0\n\t" \
    "ds_read_b64_tr_b16 v[182:183], v244 offset:1536\n\t" \
    "ds_read_b64_tr_b16 v[188:189], v244 offset:32\n\t" \
    "ds_read_b64_tr_b16 v[190:191], v244 offset:1568\n\t" \
    "ds_read_b64_tr_b16 v[196:197], v244 offset:64\n\t" \
    "ds_read_b64_tr_b16 v[198:199], v244 offset:1600\n\t" \
    "s_waitcnt lgkmcnt(0)\n\t" \
    "s_barrier \n\t" \
    "ds_write_b128 v254, v[200:203] offset:51200\n\t" \
    "ds_write_b128 v254, v[204:207] offset:52224\n\t" \
    "ds_write_b128 v254, v[208:211] offset:53248\n\t" \
    "ds_write_b128 v254, v[212:215] offset:54272\n\t" \
    "ds_write_b128 v254, v[216:219] offset:55296\n\t" \
    "ds_write_b128 v254, v[220:223] offset:56320\n\t" \
    "ds_write_b128 v254, v[224:227] offset:57344\n\t" \
    "ds_write_b128 v254, v[228:231] offset:58368\n\t" \
    "ds_write_b128 v254, v[232:235] offset:59392\n\t" \
    "ds_write_b128 v254, v[236:239] offset:60416\n\t" \
    "s_cmp_lg_u32 s43, 0\n\t" \
    "s_cbranch_scc1 .Lv24_%=\n\t" \
    "s_mov_b32 s44, 0\n\t" \
    ".Lv25_%=:\n\t" \
    "v_cmp_ge_u32 vcc, s44, v252\n\t" \
    "s_cbranch_vccz .Lv26_%=\n\t" \
    "s_cmp_eq_u32 s41, 0\n\t" \
    "s_cbranch_scc1 .Lv26_%=\n\t" \
    "s_sub_u32 s41, s41, 1\n\t" \
    "s_sleep 8\n\t" \
    "v_and_b32 v252, 0xf0, v253\n\t" \
    "v_lshlrev_b32 v252, 3, v252\n\t" \
    "buffer_load_dword v252, v252, s[60:63], %[fl] offen sc1\n\t" \
    "s_waitcnt vmcnt(0)\n\t" \
    "s_branch .Lv25_%=\n\t" \
    ".Lv26_%=:\n\t" \
    "s_mov_b32 m0, 0x2000\n\t" \
    "s_add_u32 s49, s42, 0x0\n\t" \
    "buffer_load_dwordx4 v253, s[60:63], s49 offen offset:0 sc1 lds\n\t" \
    "buffer_load_dwordx4 v253, s[60:63], s49 offen offset:1024 sc1 lds\n\t" \
    "buffer_load_dwordx4 v253, s[60:63], s49 offen offset:2048 sc1 lds\n\t" \
    "buffer_load_dwordx4 v253, s[60:63], s49 offen offset:3072 sc1 lds\n\t" \
    "s_mov_b32 m0, 0x4800\n\t" \
    "s_add_u32 s49, s42, 0x8800\n\t" \
    "buffer_load_dwordx4 v253, s[60:63], s49 offen offset:0 sc1 lds\n\t" \
    "s_mov_b32 m0, 0x7000\n\t" \
    "s_add_u32 s49, s42, 0x1000\n\t" \
    "buffer_load_dwordx4 v253, s[60:63], s49 offen offset:0 sc1 lds\n\t" \
    "buffer_load_dwordx4 v253, s[60:63], s49 offen offset:1024 sc1 lds\n\t" \
    "buffer_load_dwordx4 v253, s[60:63], s49 offen offset:2048 sc1 lds\n\t" \
    "buffer_load_dwordx4 v253, s[60:63], s49 offen offset:3072 sc1 lds\n\t" \
    "s_mov_b32 m0, 0x9800\n\t" \
    "s_add_u32 s49, s42, 0x9800\n\t" \
    "buffer_load_dwordx4 v253, s[60:63], s49 offen offset:0 sc1 lds\n\t" \
    ".Lv24_%=:\n\t" \
    "s_waitcnt vmcnt(0) lgkmcnt(0)\n\t" \
    "s_barrier \n\t" \
    "ds_read_b128 v[224:227], v253 offset:8192\n\t" \
    "ds_read_b128 v[244:247], v254 offset:51200\n\t" \
    "ds_read_b128 v[228:231], v253 offset:9216\n\t" \
    "ds_read_b128 v[232:235], v253 offset:28672\n\t" \
    "ds_read_b128 v[236:239], v253 offset:29696\n\t" \
    "ds_read_b128 v[248:251], v254 offset:52224\n\t" \
    ".p2align 8\n\t" \
    ".Lv27_%=:\n\t" \
    "s_waitcnt lgkmcnt(5)\n\t" \
    "v_mfma_f32_16x16x32_bf16 v[204:207], v[0:3], v[224:227], 0\n\t" \
    "s_waitcnt lgkmcnt(3)\n\t" \
    "v_mfma_f32_16x16x32_bf16 v[204:207], v[4:7], v[228:231], v[204:207]\n\t" \
    "ds_read_b128 v[240:243], v253 offset:14336\n\t" \
    "v_mfma_f32_16x16x32_bf16 v[208:211], v[8:11], v[224:227], 0\n\t" \
    "v_mfma_f32_16x16x32_bf16 v[208:211], v[12:15], v[228:231], v[208:211]\n\t" \
    "v_mfma_f32_16x16x32_bf16 v[200:203], v[244:247], v[224:227], 0\n\t" \
    "s_waitcnt lgkmcnt(1)\n\t" \
    "v_mfma_f32_16x16x32_bf16 v[200:203], v[248:251], v[228:231], v[200:203]\n\t" \
    "v_mfma_f32_16x16x32_bf16 v[216:219], v[0:3], v[232:235], 0\n\t" \
    "ds_read_b128 v[224:227], v253 offset:15360\n\t" \
    "v_mfma_f32_16x16x32_bf16 v[216:219], v[4:7], v[236:239], v[216:219]\n\t" \
    "ds_read_b128 v[228:231], v253 offset:0\n\t" \
    "v_mfma_f32_16x16x32_bf16 v[220:223], v[8:11], v[232:235], 0\n\t" \
    "v_mfma_f32_16x16x32_bf16 v[220:223], v[12:15], v[236:239], v[220:223]\n\t" \
    "v_mfma_f32_16x16x32_bf16 v[212:215], v[244:247], v[232:235], 0\n\t" \
    "ds_read_b128 v[244:247], v254 offset:53248\n\t" \
    "v_mfma_f32_16x16x32_bf16 v[212:215], v[248:251], v[236:239], v[212:215]\n\t" \
    "ds_read_b128 v[248:251], v254 offset:54272\n\t" \
    "s_waitcnt lgkmcnt(4)\n\t" \
    "v_mfma_f32_16x16x32_bf16 v[204:207], v[24:27], v[240:243], v[204:207]\n\t" \
    "ds_read_b128 v[232:235], v253 offset:1024\n\t" \
    "s_waitcnt lgkmcnt(4)\n\t" \
    "v_mfma_f32_16x16x32_bf16 v[204:207], v[28:31], v[224:227], v[204:207]\n\t" \
    "ds_read_b128 v[236:239], v253 offset:20480\n\t" \
    "v_mfma_f32_16x16x32_bf16 v[208:211], v[32:35], v[240:243], v[208:211]\n\t" \
    "v_mfma_f32_16x16x32_bf16 v[208:211], v[36:39], v[224:227], v[208:211]\n\t" \
    "v_mfma_f32_16x16x32_bf16 v[200:203], v[16:19], v[240:243], v[200:203]\n\t" \
    "v_mfma_f32_16x16x32_bf16 v[200:203], v[20:23], v[224:227], v[200:203]\n\t" \
    "s_waitcnt lgkmcnt(4)\n\t" \
    "v_mfma_f32_16x16x32_bf16 v[204:207], v[40:43], v[228:231], v[204:207]\n\t" \
    "ds_read_b128 v[240:243], v253 offset:21504\n\t" \
    "s_waitcnt lgkmcnt(2)\n\t" \
    "v_mfma_f32_16x16x32_bf16 v[204:207], v[44:47], v[232:235], v[204:207]\n\t" \
    "ds_read_b128 v[224:227], v253 offset:2048\n\t" \
    "v_mfma_f32_16x16x32_bf16 v[208:211], v[48:51], v[228:231], v[208:211]\n\t" \
    "v_mfma_f32_16x16x32_bf16 v[208:211], v[52:55], v[232:235], v[208:211]\n\t" \
    "v_mfma_f32_16x16x32_bf16 v[200:203], v[244:247], v[228:231], v[200:203]\n\t" \
    "v_mfma_f32_16x16x32_bf16 v[200:203], v[248:251], v[232:235], v[200:203]\n\t" \
    "s_waitcnt lgkmcnt(2)\n\t" \
    "v_mfma_f32_16x16x32_bf16 v[216:219], v[40:43], v[236:239], v[216:219]\n\t" \
    "ds_read_b128 v[228:231], v253 offset:3072\n\t" \
    "s_add_u32 s44, s40, 1\n\t" \
    "s_waitcnt lgkmcnt(2)\n\t" \
    "v_mfma_f32_16x16x32_bf16 v[216:219], v[44:47], v[240:243], v[216:219]\n\t" \
    "ds_read_b128 v[232:235], v253 offset:4096\n\t" \
    "s_and_b32 s45, s44, 15\n\t" \
    "v_mfma_f32_16x16x32_bf16 v[220:223], v[48:51], v[236:239], v[220:223]\n\t" \
    "s_cmp_eq_u32 s45, 0\n\t" \
    "v_mfma_f32_16x16x32_bf16 v[220:223], v[52:55], v[240:243], v[220:223]\n\t" \
    "s_cselect_b32 s45, %[cj], %[cs]\n\t" \
    "v_mfma_f32_16x16x32_bf16 v[212:215], v[244:247], v[236:239], v[212:215]\n\t" \
    "ds_read_b128 v[244:247], v254 offset:55296\n\t" \
    "s_cmp_eq_u32 s44, 64\n\t" \
    "v_mfma_f32_16x16x32_bf16 v[212:215], v[248:251], v[240:243], v[212:215]\n\t" \
    "ds_read_b128 v[248:251], v254 offset:56320\n\t" \
    "s_cselect_b32 s45, 0, s45\n\t" \
    "s_waitcnt lgkmcnt(4)\n\t" \
    "v_mfma_f32_16x16x32_bf16 v[204:207], v[64:67], v[224:227], v[204:207]\n\t" \
    "ds_read_b128 v[236:239], v253 offset:5120\n\t" \
    "s_add_u32 s42, s42, s45\n\t" \
    "s_waitcnt lgkmcnt(4)\n\t" \
    "v_mfma_f32_16x16x32_bf16 v[204:207], v[68:71], v[228:231], v[204:207]\n\t" \
    "ds_read_b128 v[240:243], v253 offset:24576\n\t" \
    "v_mfma_f32_16x16x32_bf16 v[208:211], v[72:75], v[224:227], v[208:211]\n\t" \
    "v_mfma_f32_16x16x32_bf16 v[208:211], v[76:79], v[228:231], v[208:211]\n\t" \
    "v_mfma_f32_16x16x32_bf16 v[200:203], v[56:59], v[224:227], v[200:203]\n\t" \
    "v_mfma_f32_16x16x32_bf16 v[200:203], v[60:63], v[228:231], v[200:203]\n\t" \
    "s_waitcnt lgkmcnt(4)\n\t" \
    "v_mfma_f32_16x16x32_bf16 v[204:207], v[80:83], v[232:235], v[204:207]\n\t" \
    "ds_read_b128 v[224:227], v253 offset:25600\n\t" \
    "s_waitcnt lgkmcnt(2)\n\t" \
    "v_mfma_f32_16x16x32_bf16 v[204:207], v[84:87], v[236:239], v[204:207]\n\t" \
    "ds_read_b128 v[228:231], v253 offset:6144\n\t" \
    "v_mfma_f32_16x16x32_bf16 v[208:211], v[88:91], v[232:235], v[208:211]\n\t" \
    "v_mfma_f32_16x16x32_bf16 v[208:211], v[92:95], v[236:239], v[208:211]\n\t" \
    "v_mfma_f32_16x16x32_bf16 v[200:203], v[244:247], v[232:235], v[200:203]\n\t" \
    "v_mfma_f32_16x16x32_bf16 v[200:203], v[248:251], v[236:239], v[200:203]\n\t" \
    "s_waitcnt lgkmcnt(2)\n\t" \
    "v_mfma_f32_16x16x32_bf16 v[216:219], v[80:83], v[240:243], v[216:219]\n\t" \
    "ds_read_b128 v[232:235], v253 offset:7168\n\t" \
    "s_and_b32 s44, s40, 3\n\t" \
    "s_waitcnt lgkmcnt(2)\n\t" \
    "v_mfma_f32_16x16x32_bf16 v[216:219], v[84:87], v[224:227], v[216:219]\n\t" \
    "ds_read_b128 v[236:239], v253 offset:10240\n\t" \
    "s_lshl_b32 s44, s44, 22\n\t" \
    "v_mfma_f32_16x16x32_bf16 v[220:223], v[88:91], v[240:243], v[220:223]\n\t" \
    "s_add_u32 s44, s44, %[mb]\n\t" \
    "v_mfma_f32_16x16x32_bf16 v[220:223], v[92:95], v[224:227], v[220:223]\n\t" \
    "s_add_u32 s46, s44, 0x8000\n\t" \
    "v_mfma_f32_16x16x32_bf16 v[212:215], v[244:247], v[240:243], v[212:215]\n\t" \
    "ds_read_b128 v[244:247], v254 offset:57344\n\t" \
    "s_add_u32 s47, s44, 0x8c00\n\t" \
    "v_mfma_f32_16x16x32_bf16 v[212:215], v[248:251], v[224:227], v[212:215]\n\t" \
    "ds_read_b128 v[248:251], v254 offset:58368\n\t" \
    "s_waitcnt lgkmcnt(4)\n\t" \
    "v_mfma_f32_16x16x32_bf16 v[204:207], v[104:107], v[228:231], v[204:207]\n\t" \
    "ds_read_b128 v[240:243], v253 offset:11264\n\t" \
    "s_waitcnt lgkmcnt(4)\n\t" \
    "v_mfma_f32_16x16x32_bf16 v[204:207], v[108:111], v[232:235], v[204:207]\n\t" \
    "ds_read_b128 v[224:227], v253 offset:30720\n\t" \
    "v_mfma_f32_16x16x32_bf16 v[208:211], v[112:115], v[228:231], v[208:211]\n\t" \
    "v_mfma_f32_16x16x32_bf16 v[208:211], v[116:119], v[232:235], v[208:211]\n\t" \
    "v_mfma_f32_16x16x32_bf16 v[200:203], v[96:99], v[228:231], v[200:203]\n\t" \
    "v_mfma_f32_16x16x32_bf16 v[200:203], v[100:103], v[232:235], v[200:203]\n\t" \
    "s_waitcnt lgkmcnt(4)\n\t" \
    "v_mfma_f32_16x16x32_bf16 v[204:207], v[120:123], v[236:239], v[204:207]\n\t" \
    "ds_read_b128 v[228:231], v253 offset:31744\n\t" \
    "s_waitcnt lgkmcnt(2)\n\t" \
    "v_mfma_f32_16x16x32_bf16 v[204:207], v[124:127], v[240:243], v[204:207]\n\t" \
    "ds_read_b128 v[232:235], v253 offset:12288\n\t" \
    "v_mfma_f32_16x16x32_bf16 v[208:211], v[128:131], v[236:239], v[208:211]\n\t" \
    "v_mfma_f32_16x16x32_bf16 v[208:211], v[132:135], v[240:243], v[208:211]\n\t" \
    "v_mfma_f32_16x16x32_bf16 v[200:203], v[244:247], v[236:239], v[200:203]\n\t" \
    "v_mfma_f32_16x16x32_bf16 v[200:203], v[248:251], v[240:243], v[200:203]\n\t" \
    "s_waitcnt lgkmcnt(2)\n\t" \
    "v_mfma_f32_16x16x32_bf16 v[216:219], v[120:123], v[224:227], v[216:219]\n\t" \
    "ds_read_b128 v[236:239], v253 offset:13312\n\t" \
    "s_waitcnt lgkmcnt(2)\n\t" \
    "v_mfma_f32_16x16x32_bf16 v[216:219], v[124:127], v[228:231], v[216:219]\n\t" \
    "ds_read_b128 v[240:243], v253 offset:16384\n\t" \
    "v_mfma_f32_16x16x32_bf16 v[220:223], v[128:131], v[224:227], v[220:223]\n\t" \
    "v_mfma_f32_16x16x32_bf16 v[220:223], v[132:135], v[228:231], v[220:223]\n\t" \
    "v_mfma_f32_16x16x32_bf16 v[212:215], v[244:247], v[224:227], v[212:215]\n\t" \
    "ds_read_b128 v[244:247], v254 offset:59392\n\t" \
    "v_mfma_f32_16x16x32_bf16 v[212:215], v[248:251], v[228:231], v[212:215]\n\t" \
    "ds_read_b128 v[248:251], v254 offset:60416\n\t" \
    "v_and_b32 v252, 0xf0, v253\n\t" \
    "v_lshlrev_b32 v252, 3, v252\n\t" \
    "buffer_load_dword v252, v252, s[60:63], %[fl] offen sc1\n\t" \
    "s_waitcnt lgkmcnt(4)\n\t" \
    "v_mfma_f32_16x16x32_bf16 v[204:207], v[144:147], v[232:235], v[204:207]\n\t" \
    "ds_read_b128 v[224:227], v253 offset:17408\n\t" \
    "s_waitcnt lgkmcnt(4)\n\t" \
    "v_mfma_f32_16x16x32_bf16 v[204:207], v[148:151], v[236:239], v[204:207]\n\t" \
    "ds_read_b128 v[228:231], v253 offset:36864\n\t" \
    "v_mfma_f32_16x16x32_bf16 v[208:211], v[152:155], v[232:235], v[208:211]\n\t" \
    "v_mfma_f32_16x16x32_bf16 v[208:211], v[156:159], v[236:239], v[208:211]\n\t" \
    "v_mfma_f32_16x16x32_bf16 v[200:203], v[136:139], v[232:235], v[200:203]\n\t" \
    "v_mfma_f32_16x16x32_bf16 v[200:203], v[140:143], v[236:239], v[200:203]\n\t" \
    "s_waitcnt lgkmcnt(4)\n\t" \
    "v_mfma_f32_16x16x32_bf16 v[204:207], v[160:163], v[240:243], v[204:207]\n\t" \
    "ds_read_b128 v[232:235], v253 offset:37888\n\t" \
    "s_waitcnt lgkmcnt(2)\n\t" \
    "v_mfma_f32_16x16x32_bf16 v[204:207], v[164:167], v[224:227], v[204:207]\n\t" \
    "ds_read_b128 v[236:239], v253 offset:18432\n\t" \
    "v_mfma_f32_16x16x32_bf16 v[208:211], v[168:171], v[240:243], v[208:211]\n\t" \
    "v_mfma_f32_16x16x32_bf16 v[208:211], v[172:175], v[224:227], v[208:211]\n\t" \
    "v_mfma_f32_16x16x32_bf16 v[200:203], v[244:247], v[240:243], v[200:203]\n\t" \
    "v_mfma_f32_16x16x32_bf16 v[200:203], v[248:251], v[224:227], v[200:203]\n\t" \
    "s_waitcnt lgkmcnt(2)\n\t" \
    "v_mfma_f32_16x16x32_bf16 v[216:219], v[160:163], v[228:231], v[216:219]\n\t" \
    "ds_read_b128 v[240:243], v253 offset:19456\n\t" \
    "s_waitcnt lgkmcnt(2)\n\t" \
    "v_mfma_f32_16x16x32_bf16 v[216:219], v[164:167], v[232:235], v[216:219]\n\t" \
    "ds_read_b128 v[224:227], v253 offset:34816\n\t" \
    "v_mfma_f32_16x16x32_bf16 v[220:223], v[168:171], v[228:231], v[220:223]\n\t" \
    "v_mfma_f32_16x16x32_bf16 v[220:223], v[172:175], v[232:235], v[220:223]\n\t" \
    "v_mfma_f32_16x16x32_bf16 v[212:215], v[244:247], v[228:231], v[212:215]\n\t" \
    "v_mfma_f32_16x16x32_bf16 v[212:215], v[248:251], v[232:235], v[212:215]\n\t" \
    "s_waitcnt lgkmcnt(2)\n\t" \
    "v_mfma_f32_16x16x32_bf16 v[204:207], v[184:187], v[236:239], v[204:207]\n\t" \
    "ds_read_b128 v[228:231], v253 offset:35840\n\t" \
    "s_waitcnt lgkmcnt(2)\n\t" \
    "v_mfma_f32_16x16x32_bf16 v[204:207], v[188:191], v[240:243], v[204:207]\n\t" \
    "ds_read_b128 v[232:235], v253 offset:22528\n\t" \
    "v_mfma_f32_16x16x32_bf16 v[208:211], v[192:195], v[236:239], v[208:211]\n\t" \
    "v_mfma_f32_16x16x32_bf16 v[208:211], v[196:199], v[240:243], v[208:211]\n\t" \
    "v_mfma_f32_16x16x32_bf16 v[200:203], v[176:179], v[236:239], v[200:203]\n\t" \
    "v_mfma_f32_16x16x32_bf16 v[200:203], v[180:183], v[240:243], v[200:203]\n\t" \
    "s_waitcnt lgkmcnt(2)\n\t" \
    "v_mfma_f32_16x16x32_bf16 v[216:219], v[24:27], v[224:227], v[216:219]\n\t" \
    "ds_read_b128 v[236:239], v253 offset:23552\n\t" \
    "s_waitcnt lgkmcnt(2)\n\t" \
    "v_mfma_f32_16x16x32_bf16 v[216:219], v[28:31], v[228:231], v[216:219]\n\t" \
    "ds_read_b128 v[240:243], v253 offset:26624\n\t" \
    "v_mfma_f32_16x16x32_bf16 v[220:223], v[32:35], v[224:227], v[220:223]\n\t" \
    "v_cvt_pk_bf16_f32 v204, v204, v205\n\t" \
    "v_mfma_f32_16x16x32_bf16 v[220:223], v[36:39], v[228:231], v[220:223]\n\t" \
    "v_cvt_pk_bf16_f32 v205, v206, v207\n\t" \
    "v_mfma_f32_16x16x32_bf16 v[212:215], v[16:19], v[224:227], v[212:215]\n\t" \
    "ds_write_b64 v254, v[204:205] offset:7176\n\t" \
    "v_mfma_f32_16x16x32_bf16 v[212:215], v[20:23], v[228:231], v[212:215]\n\t" \
    "v_cvt_pk_bf16_f32 v208, v208, v209\n\t" \
    "s_waitcnt lgkmcnt(3)\n\t" \
    "v_mfma_f32_16x16x32_bf16 v[216:219], v[64:67], v[232:235], v[216:219]\n\t" \
    "ds_read_b128 v[224:227], v253 offset:27648\n\t" \
    "v_cvt_pk_bf16_f32 v209, v210, v211\n\t" \
    "s_waitcnt lgkmcnt(3)\n\t" \
    "v_mfma_f32_16x16x32_bf16 v[216:219], v[68:71], v[236:239], v[216:219]\n\t" \
    "ds_read_b128 v[228:231], v253 offset:32768\n\t" \
    "ds_write_b64 v254, v[208:209] offset:8200\n\t" \
    "v_mfma_f32_16x16x32_bf16 v[220:223], v[72:75], v[232:235], v[220:223]\n\t" \
    "v_cvt_pk_bf16_f32 v200, v200, v201\n\t" \
    "v_mfma_f32_16x16x32_bf16 v[220:223], v[76:79], v[236:239], v[220:223]\n\t" \
    "v_cvt_pk_bf16_f32 v201, v202, v203\n\t" \
    "v_mfma_f32_16x16x32_bf16 v[212:215], v[56:59], v[232:235], v[212:215]\n\t" \
    "ds_write_b64 v254, v[200:201] offset:6152\n\t" \
    "v_mfma_f32_16x16x32_bf16 v[212:215], v[60:63], v[236:239], v[212:215]\n\t" \
    "s_waitcnt lgkmcnt(5)\n\t" \
    "v_mfma_f32_16x16x32_bf16 v[216:219], v[104:107], v[240:243], v[216:219]\n\t" \
    "ds_read_b128 v[232:235], v253 offset:33792\n\t" \
    "s_waitcnt lgkmcnt(4)\n\t" \
    "v_mfma_f32_16x16x32_bf16 v[216:219], v[108:111], v[224:227], v[216:219]\n\t" \
    "ds_read_b128 v[236:239], v253 offset:38912\n\t" \
    "v_mfma_f32_16x16x32_bf16 v[220:223], v[112:115], v[240:243], v[220:223]\n\t" \
    "v_mfma_f32_16x16x32_bf16 v[220:223], v[116:119], v[224:227], v[220:223]\n\t" \
    "v_mfma_f32_16x16x32_bf16 v[212:215], v[96:99], v[240:243], v[212:215]\n\t" \
    "v_mfma_f32_16x16x32_bf16 v[212:215], v[100:103], v[224:227], v[212:215]\n\t" \
    "ds_read_b128 v[240:243], v253 offset:39936\n\t" \
    "s_waitcnt lgkmcnt(3)\n\t" \
    "s_barrier \n\t" \
    "v_mfma_f32_16x16x32_bf16 v[216:219], v[144:147], v[228:231], v[216:219]\n\t" \
    "s_waitcnt lgkmcnt(2)\n\t" \
    "v_mfma_f32_16x16x32_bf16 v[216:219], v[148:151], v[232:235], v[216:219]\n\t" \
    "s_waitcnt lgkmcnt(1)\n\t" \
    "v_mfma_f32_16x16x32_bf16 v[216:219], v[184:187], v[236:239], v[216:219]\n\t" \
    "s_waitcnt lgkmcnt(0)\n\t" \
    "v_mfma_f32_16x16x32_bf16 v[216:219], v[188:191], v[240:243], v[216:219]\n\t" \
    "v_mfma_f32_16x16x32_bf16 v[220:223], v[152:155], v[228:231], v[220:223]\n\t" \
    "v_mfma_f32_16x16x32_bf16 v[220:223], v[156:159], v[232:235], v[220:223]\n\t" \
    "v_mfma_f32_16x16x32_bf16 v[220:223], v[192:195], v[236:239], v[220:223]\n\t" \
    "v_cvt_pk_bf16_f32 v216, v216, v217\n\t" \
    "v_cvt_pk_bf16_f32 v217, v218, v219\n\t" \
    "v_mfma_f32_16x16x32_bf16 v[220:223], v[196:199], v[240:243], v[220:223]\n\t" \
    "ds_write_b64 v254, v[216:217] offset:13320\n\t" \
    "v_mfma_f32_16x16x32_bf16 v[212:215], v[136:139], v[228:231], v[212:215]\n\t" \
    "v_mfma_f32_16x16x32_bf16 v[212:215], v[140:143], v[232:235], v[212:215]\n\t" \
    "v_mfma_f32_16x16x32_bf16 v[212:215], v[176:179], v[236:239], v[212:215]\n\t" \
    "v_cvt_pk_bf16_f32 v220, v220, v221\n\t" \
    "v_cvt_pk_bf16_f32 v221, v222, v223\n\t" \
    "v_mfma_f32_16x16x32_bf16 v[212:215], v[180:183], v[240:243], v[212:215]\n\t" \
    "ds_write_b64 v254, v[220:221] offset:14344\n\t" \
    "s_nop 6\n\t" \
    "v_cvt_pk_bf16_f32 v212, v212, v213\n\t" \
    "v_cvt_pk_bf16_f32 v213, v214, v215\n\t" \
    "ds_write_b64 v254, v[212:213] offset:12296\n\t" \
    "s_waitcnt vmcnt(0) lgkmcnt(0)\n\t" \
    "s_barrier \n\t" \
    "s_setprio 2\n\t" \
    "s_add_u32 s44, s40, 1\n\t" \
    "s_min_u32 s44, s44, 63\n\t" \
    ".Lv28_%=:\n\t" \
    "v_cmp_ge_u32 vcc, s44, v252\n\t" \
    "s_cbranch_vccz .Lv29_%=\n\t" \
    "s_cmp_eq_u32 s41, 0\n\t" \
    "s_cbranch_scc1 .Lv29_%=\n\t" \
    "s_sub_u32 s41, s41, 1\n\t" \
    "s_sleep 2\n\t" \
    "v_and_b32 v252, 0xf0, v253\n\t" \
    "v_lshlrev_b32 v252, 3, v252\n\t" \
    "buffer_load_dword v252, v252, s[60:63], %[fl] offen sc1\n\t" \
    "s_waitcnt vmcnt(0)\n\t" \
    "s_branch .Lv28_%=\n\t" \
    ".Lv29_%=:\n\t" \
    "s_mov_b32 m0, 0x2000\n\t" \
    "s_add_u32 s49, s42, 0x0\n\t" \
    "buffer_load_dwordx4 v253, s[60:63], s49 offen offset:0 sc1 lds\n\t" \
    "s_sleep 1\n\t" \
    "buffer_load_dwordx4 v253, s[60:63], s49 offen offset:1024 sc1 lds\n\t" \
    "s_sleep 1\n\t" \
    "buffer_load_dwordx4 v253, s[60:63], s49 offen offset:2048 sc1 lds\n\t" \
    "s_sleep 1\n\t" \
    "buffer_load_dwordx4 v253, s[60:63], s49 offen offset:3072 sc1 lds\n\t" \
    "s_sleep 1\n\t" \
    "s_mov_b32 m0, 0x4800\n\t" \
    "s_add_u32 s49, s42, 0x8800\n\t" \
    "buffer_load_dwordx4 v253, s[60:63], s49 offen offset:0 sc1 lds\n\t" \
    "s_sleep 1\n\t" \
    "s_mov_b32 m0, 0x7000\n\t" \
    "s_add_u32 s49, s42, 0x1000\n\t" \
    "buffer_load_dwordx4 v253, s[60:63], s49 offen offset:0 sc1 lds\n\t" \
    "s_sleep 1\n\t" \
    "buffer_load_dwordx4 v253, s[60:63], s49 offen offset:1024 sc1 lds\n\t" \
    "s_sleep 1\n\t" \
    "buffer_load_dwordx4 v253, s[60:63], s49 offen offset:2048 sc1 lds\n\t" \
    "s_sleep 1\n\t" \
    "buffer_load_dwordx4 v253, s[60:63], s49 offen offset:3072 sc1 lds\n\t" \
    "s_sleep 1\n\t" \
    "s_mov_b32 m0, 0x9800\n\t" \
    "s_add_u32 s49, s42, 0x9800\n\t" \
    "buffer_load_dwordx4 v253, s[60:63], s49 offen offset:0 sc1 lds\n\t" \
    "s_sleep 1\n\t" \
    "ds_read_b128 v[200:203], v254 offset:51200\n\t" \
    "ds_read_b128 v[204:207], v254 offset:52224\n\t" \
    "ds_read_b128 v[216:219], v254 offset:53248\n\t" \
    "ds_read_b128 v[220:223], v254 offset:54272\n\t" \
    "v_mov_b64 v[208:209], 0\n\t" \
    "v_mov_b64 v[210:211], 0\n\t" \
    "v_mov_b64 v[244:245], 0\n\t" \
    "v_mov_b64 v[246:247], 0\n\t" \
    "v_mov_b64 v[248:249], 0\n\t" \
    "v_mov_b64 v[250:251], 0\n\t" \
    "s_sleep 5\n\t" \
    "s_mov_b32 m0, 0xb000\n\t" \
    "s_nop 0\n\t" \
    "buffer_load_dwordx4 v253, s[60:63], s46 offen sc1 lds\n\t" \
    "s_mov_b32 m0, 0xbc00\n\t" \
    "s_nop 0\n\t" \
    "buffer_load_dwordx4 v253, s[60:63], s47 offen sc1 lds\n\t" \
    "s_waitcnt lgkmcnt(0)\n\t" \
    "v_bfe_i32 v212, v253, 5, 1\n\t" \
    "v_and_b32 v212, 0xaaaa, v212\n\t" \
    "v_or_b32 v212, 0x4444, v212\n\t" \
    "v_and_b32 v213, 0xffff, v255\n\t" \
    "v_lshrrev_b32 v214, 16, v255\n\t" \
    ".Lv210_%=:\n\t" \
    "s_waitcnt vmcnt(0)\n\t" \
    "ds_read_b128 v[224:227], v253 offset:45056\n\t" \
    "ds_read_b128 v[228:231], v253 offset:48128\n\t" \
    "s_waitcnt lgkmcnt(1)\n\t" \
    "v_max_u32 v215, v224, v225\n\t" \
    "v_max3_u32 v215, v226, v227, v215\n\t" \
    "s_waitcnt lgkmcnt(0)\n\t" \
    "v_max3_u32 v215, v228, v229, v215\n\t" \
    "v_max3_u32 v215, v230, v231, v215\n\t" \
    "v_cmp_eq_u32 vcc, -1, v215\n\t" \
    "s_cbranch_vccz .Lv211_%=\n\t" \
    "v_max_u32 v215, v224, v225\n\t" \
    "v_max3_u32 v215, v226, v227, v215\n\t" \
    "v_cmp_eq_u32 vcc, -1, v215\n\t" \
    "s_cbranch_vccz .Lv212_%=\n\t" \
    "s_mov_b32 m0, 0xb000\n\t" \
    "s_nop 0\n\t" \
    "buffer_load_dwordx4 v253, s[60:63], s46 offen sc1 lds\n\t" \
    ".Lv212_%=:\n\t" \
    "v_max_u32 v215, v228, v229\n\t" \
    "v_max3_u32 v215, v230, v231, v215\n\t" \
    "v_cmp_eq_u32 vcc, -1, v215\n\t" \
    "s_cbranch_vccz .Lv213_%=\n\t" \
    "s_mov_b32 m0, 0xbc00\n\t" \
    "s_nop 0\n\t" \
    "buffer_load_dwordx4 v253, s[60:63], s47 offen sc1 lds\n\t" \
    ".Lv213_%=:\n\t" \
    "s_cmp_eq_u32 s41, 0\n\t" \
    "s_cbranch_scc1 .Lv211_%=\n\t" \
    "s_sub_u32 s41, s41, 1\n\t" \
    "s_branch .Lv210_%=\n\t" \
    ".Lv211_%=:\n\t" \
    "s_waitcnt lgkmcnt(0)\n\t" \
    "s_barrier \n\t" \
    "ds_read_b128 v[232:235], v253 offset:40960\n\t" \
    "ds_read_b128 v[236:239], v253 offset:41984\n\t" \
    "ds_read_b128 v[240:243], v253 offset:43008\n\t" \
    "v_smfmac_f32_16x16x64_bf16 v[244:247], v[224:227], v[0:7], v212\n\t" \
    "v_smfmac_f32_16x16x64_bf16 v[248:251], v[224:227], v[8:15], v212\n\t" \
    "v_smfmac_f32_16x16x64_bf16 v[208:211], v[224:227], v[200:207], v212\n\t" \
    "ds_read_b128 v[200:203], v254 offset:55296\n\t" \
    "ds_read_b128 v[204:207], v254 offset:56320\n\t" \
    "ds_read_b128 v[224:227], v253 offset:44032\n\t" \
    "v_smfmac_f32_16x16x64_bf16 v[208:211], v[228:231], v[16:23], v212\n\t" \
    "v_smfmac_f32_16x16x64_bf16 v[244:247], v[228:231], v[24:31], v212\n\t" \
    "v_smfmac_f32_16x16x64_bf16 v[248:251], v[228:231], v[32:39], v212\n\t" \
    "ds_read_b128 v[228:231], v253 offset:46080\n\t" \
    "s_waitcnt lgkmcnt(6)\n\t" \
    "v_smfmac_f32_16x16x64_bf16 v[208:211], v[232:235], v[216:223], v212\n\t" \
    "ds_read_b128 v[216:219], v254 offset:57344\n\t" \
    "ds_read_b128 v[220:223], v254 offset:58368\n\t" \
    "v_smfmac_f32_16x16x64_bf16 v[244:247], v[232:235], v[40:47], v212\n\t" \
    "v_smfmac_f32_16x16x64_bf16 v[248:251], v[232:235], v[48:55], v212\n\t" \
    "ds_read_b128 v[232:235], v253 offset:47104\n\t" \
    "s_waitcnt lgkmcnt(8)\n\t" \
    "v_smfmac_f32_16x16x64_bf16 v[208:211], v[236:239], v[56:63], v212\n\t" \
    "v_smfmac_f32_16x16x64_bf16 v[244:247], v[236:239], v[64:71], v212\n\t" \
    "v_smfmac_f32_16x16x64_bf16 v[248:251], v[236:239], v[72:79], v212\n\t" \
    "ds_read_b128 v[236:239], v253 offset:49152\n\t" \
    "s_waitcnt lgkmcnt(6)\n\t" \
    "v_smfmac_f32_16x16x64_bf16 v[208:211], v[240:243], v[200:207], v212\n\t" \
    "ds_read_b128 v[200:203], v254 offset:59392\n\t" \
    "ds_read_b128 v[204:207], v254 offset:60416\n\t" \
    "v_smfmac_f32_16x16x64_bf16 v[244:247], v[240:243], v[80:87], v212\n\t" \
    "v_smfmac_f32_16x16x64_bf16 v[248:251], v[240:243], v[88:95], v212\n\t" \
    "ds_read_b128 v[240:243], v253 offset:50176\n\t" \
    "s_waitcnt lgkmcnt(8)\n\t" \
    "v_smfmac_f32_16x16x64_bf16 v[208:211], v[224:227], v[96:103], v212\n\t" \
    "v_smfmac_f32_16x16x64_bf16 v[244:247], v[224:227], v[104:111], v212\n\t" \
    "v_smfmac_f32_16x16x64_bf16 v[248:251], v[224:227], v[112:119], v212\n\t" \
    "s_waitcnt lgkmcnt(7)\n\t" \
    "v_smfmac_f32_16x16x64_bf16 v[244:247], v[228:231], v[120:127], v212\n\t" \
    "s_waitcnt lgkmcnt(4)\n\t" \
    "v_smfmac_f32_16x16x64_bf16 v[244:247], v[232:235], v[144:151], v212\n\t" \
    "s_waitcnt lgkmcnt(3)\n\t" \
    "v_smfmac_f32_16x16x64_bf16 v[244:247], v[236:239], v[160:167], v212\n\t" \
    "s_waitcnt lgkmcnt(0)\n\t" \
    "v_smfmac_f32_16x16x64_bf16 v[244:247], v[240:243], v[184:191], v212\n\t" \
    "v_smfmac_f32_16x16x64_bf16 v[248:251], v[228:231], v[128:135], v212\n\t" \
    "v_smfmac_f32_16x16x64_bf16 v[248:251], v[232:235], v[152:159], v212\n\t" \
    "v_smfmac_f32_16x16x64_bf16 v[248:251], v[236:239], v[168:175], v212\n\t" \
    "v_add_f32 v244, v244, v246\n\t" \
    "v_add_f32 v245, v245, v247\n\t" \
    "v_cvt_pk_bf16_f32 v244, v244, v245\n\t" \
    "v_smfmac_f32_16x16x64_bf16 v[248:251], v[240:243], v[192:199], v212\n\t" \
    "ds_write_b16 v213, v244 offset:512\n\t" \
    "ds_write_b16_d16_hi v213, v244 offset:528\n\t" \
    "v_smfmac_f32_16x16x64_bf16 v[208:211], v[228:231], v[216:223], v212\n\t" \
    "v_smfmac_f32_16x16x64_bf16 v[208:211], v[232:235], v[136:143], v212\n\t" \
    "v_smfmac_f32_16x16x64_bf16 v[208:211], v[236:239], v[200:207], v212\n\t" \
    "v_add_f32 v248, v248, v250\n\t" \
    "v_add_f32 v249, v249, v251\n\t" \
    "v_cvt_pk_bf16_f32 v248, v248, v249\n\t" \
    "v_smfmac_f32_16x16x64_bf16 v[208:211], v[240:243], v[176:183], v212\n\t" \
    "ds_write_b16 v213, v248 offset:1024\n\t" \
    "ds_write_b16_d16_hi v213, v248 offset:1040\n\t" \
    "s_nop 5\n\t" \
    "v_add_f32 v208, v208, v210\n\t" \
    "v_add_f32 v209, v209, v211\n\t" \
    "v_cvt_pk_bf16_f32 v208, v208, v209\n\t" \
    "ds_write_b16 v213, v208 offset:0\n\t" \
    "ds_write_b16_d16_hi v213, v208 offset:16\n\t" \
    "s_setprio 0\n\t" \
    "s_waitcnt lgkmcnt(0)\n\t" \
    "s_barrier \n\t" \
    "ds_read_b128 v[224:227], v253 offset:8192\n\t" \
    "ds_read_b128 v[244:247], v254 offset:51200\n\t" \
    "ds_read_b128 v[228:231], v253 offset:9216\n\t" \
    "ds_read_b128 v[232:235], v253 offset:28672\n\t" \
    "ds_read_b128 v[236:239], v253 offset:29696\n\t" \
    "ds_read_b128 v[248:251], v254 offset:52224\n\t" \
    "s_add_u32 s40, s40, 1\n\t" \
    "s_cmp_lg_u32 s40, 64\n\t" \
    "s_cbranch_scc1 .Lv27_%=\n\t" \
    "s_branch .Lend%=\n\t" \
    ".Lv1_%=:\n\t" \
    "s_mov_b32 s42, %[xs]\n\t" \
    "v_add_u32 v244, 0xc000, v243\n\t" \
    "s_waitcnt vmcnt(12) lgkmcnt(0)\n\t" \
    "s_barrier \n\t" \
    "s_add_u32 m0, %[wm], 0x9000\n\t" \
    "s_mov_b32 s44, 0x1e0000\n\t" \
    "buffer_load_dwordx4 v240, %[wr], s44 offen offset:0 nt lds\n\t" \
    "buffer_load_dwordx4 v241, %[wr], s44 offen offset:1024 nt lds\n\t" \
    "buffer_load_dwordx4 v242, %[wr], s44 offen offset:2048 nt lds\n\t" \
    "ds_read_b64_tr_b16 v[56:57], v244 offset:0\n\t" \
    "ds_read_b64_tr_b16 v[58:59], v244 offset:1536\n\t" \
    "ds_read_b64_tr_b16 v[64:65], v244 offset:32\n\t" \
    "ds_read_b64_tr_b16 v[66:67], v244 offset:1568\n\t" \
    "ds_read_b64_tr_b16 v[72:73], v244 offset:64\n\t" \
    "ds_read_b64_tr_b16 v[74:75], v244 offset:1600\n\t" \
    "v_add_u32 v244, 0xf000, v243\n\t" \
    "s_waitcnt vmcnt(12) lgkmcnt(0)\n\t" \
    "s_barrier \n\t" \
    "s_add_u32 m0, %[wm], 0xc000\n\t" \
    "s_mov_b32 s44, 0x240000\n\t" \
    "buffer_load_dwordx4 v240, %[wr], s44 offen offset:0 nt lds\n\t" \
    "buffer_load_dwordx4 v241, %[wr], s44 offen offset:1024 nt lds\n\t" \
    "buffer_load_dwordx4 v242, %[wr], s44 offen offset:2048 nt lds\n\t" \
    "ds_read_b64_tr_b16 v[60:61], v244 offset:0\n\t" \
    "ds_read_b64_tr_b16 v[62:63], v244 offset:1536\n\t" \
    "ds_read_b64_tr_b16 v[68:69], v244 offset:32\n\t" \
    "ds_read_b64_tr_b16 v[70:71], v244 offset:1568\n\t" \
    "ds_read_b64_tr_b16 v[76:77], v244 offset:64\n\t" \
    "ds_read_b64_tr_b16 v[78:79], v244 offset:1600\n\t" \
    "v_add_u32 v244, 0x0, v243\n\t" \
    "s_waitcnt vmcnt(12) lgkmcnt(0)\n\t" \
    "s_barrier \n\t" \
    "s_add_u32 m0, %[wm], 0xf000\n\t" \
    "s_mov_b32 s44, 0x2a0000\n\t" \
    "buffer_load_dwordx4 v240, %[wr], s44 offen offset:0 nt lds\n\t" \
    "buffer_load_dwordx4 v241, %[wr], s44 offen offset:1024 nt lds\n\t" \
    "buffer_load_dwordx4 v242, %[wr], s44 offen offset:2048 nt lds\n\t" \
    "ds_read_b64_tr_b16 v[96:97], v244 offset:0\n\t" \
    "ds_read_b64_tr_b16 v[98:99], v244 offset:1536\n\t" \
    "ds_read_b64_tr_b16 v[104:105], v244 offset:32\n\t" \
    "ds_read_b64_tr_b16 v[106:107], v244 offset:1568\n\t" \
    "ds_read_b64_tr_b16 v[112:113], v244 offset:64\n\t" \
    "ds_read_b64_tr_b16 v[114:115], v244 offset:1600\n\t" \
    "v_add_u32 v244, 0x3000, v243\n\t" \
    "s_waitcnt vmcnt(12) lgkmcnt(0)\n\t" \
    "s_barrier \n\t" \
    "s_add_u32 m0, %[wm], 0x0\n\t" \
    "s_mov_b32 s44, 0x300000\n\t" \
    "buffer_load_dwordx4 v240, %[wr], s44 offen offset:0 nt lds\n\t" \
    "buffer_load_dwordx4 v241, %[wr], s44 offen offset:1024 nt lds\n\t" \
    "buffer_load_dwordx4 v242, %[wr], s44 offen offset:2048 nt lds\n\t" \
    "ds_read_b64_tr_b16 v[100:101], v244 offset:0\n\t" \
    "ds_read_b64_tr_b16 v[102:103], v244 offset:1536\n\t" \
    "ds_read_b64_tr_b16 v[108:109], v244 offset:32\n\t" \
    "ds_read_b64_tr_b16 v[110:111], v244 offset:1568\n\t" \
    "ds_read_b64_tr_b16 v[116:117], v244 offset:64\n\t" \
    "ds_read_b64_tr_b16 v[118:119], v244 offset:1600\n\t" \
    "v_add_u32 v244, 0x6000, v243\n\t" \
    "s_waitcnt vmcnt(12) lgkmcnt(0)\n\t" \
    "s_barrier \n\t" \
    "s_add_u32 m0, %[wm], 0x3000\n\t" \
    "s_mov_b32 s44, 0x360000\n\t" \
    "buffer_load_dwordx4 v240, %[wr], s44 offen offset:0 nt lds\n\t" \
    "buffer_load_dwordx4 v241, %[wr], s44 offen offset:1024 nt lds\n\t" \
    "buffer_load_dwordx4 v242, %[wr], s44 offen offset:2048 nt lds\n\t" \
    "ds_read_b64_tr_b16 v[200:201], v244 offset:0\n\t" \
    "ds_read_b64_tr_b16 v[202:203], v244 offset:1536\n\t" \
    "ds_read_b64_tr_b16 v[0:1], v244 offset:32\n\t" \
    "ds_read_b64_tr_b16 v[2:3], v244 offset:1568\n\t" \
    "ds_read_b64_tr_b16 v[8:9], v244 offset:64\n\t" \
    "ds_read_b64_tr_b16 v[10:11], v244 offset:1600\n\t" \
    "v_add_u32 v244, 0x9000, v243\n\t" \
    "s_waitcnt vmcnt(12) lgkmcnt(0)\n\t" \
    "s_barrier \n\t" \
    "s_add_u32 m0, %[wm], 0x6000\n\t" \
    "s_mov_b32 s44, 0x3c0000\n\t" \
    "buffer_load_dwordx4 v240, %[wr], s44 offen offset:0 nt lds\n\t" \
    "buffer_load_dwordx4 v241, %[wr], s44 offen offset:1024 nt lds\n\t" \
    "buffer_load_dwordx4 v242, %[wr], s44 offen offset:2048 nt lds\n\t" \
    "ds_read_b64_tr_b16 v[204:205], v244 offset:0\n\t" \
    "ds_read_b64_tr_b16 v[206:207], v244 offset:1536\n\t" \
    "ds_read_b64_tr_b16 v[4:5], v244 offset:32\n\t" \
    "ds_read_b64_tr_b16 v[6:7], v244 offset:1568\n\t" \
    "ds_read_b64_tr_b16 v[12:13], v244 offset:64\n\t" \
    "ds_read_b64_tr_b16 v[14:15], v244 offset:1600\n\t" \
    "v_add_u32 v244, 0xc000, v243\n\t" \
    "s_waitcnt vmcnt(12) lgkmcnt(0)\n\t" \
    "s_barrier \n\t" \
    "s_add_u32 m0, %[wm], 0x9000\n\t" \
    "s_mov_b32 s44, 0x420000\n\t" \
    "buffer_load_dwordx4 v240, %[wr], s44 offen offset:0 nt lds\n\t" \
    "buffer_load_dwordx4 v241, %[wr], s44 offen offset:1024 nt lds\n\t" \
    "buffer_load_dwordx4 v242, %[wr], s44 offen offset:2048 nt lds\n\t" \
    "ds_read_b64_tr_b16 v[208:209], v244 offset:0\n\t" \
    "ds_read_b64_tr_b16 v[210:211], v244 offset:1536\n\t" \
    "ds_read_b64_tr_b16 v[40:41], v244 offset:32\n\t" \
    "ds_read_b64_tr_b16 v[42:43], v244 offset:1568\n\t" \
    "ds_read_b64_tr_b16 v[48:49], v244 offset:64\n\t" \
    "ds_read_b64_tr_b16 v[50:51], v244 offset:1600\n\t" \
    "v_add_u32 v244, 0xf000, v243\n\t" \
    "s_waitcnt vmcnt(12) lgkmcnt(0)\n\t" \
    "s_barrier \n\t" \
    "s_add_u32 m0, %[wm], 0xc000\n\t" \
    "s_mov_b32 s44, 0x480000\n\t" \
    "buffer_load_dwordx4 v240, %[wr], s44 offen offset:0 nt lds\n\t" \
    "buffer_load_dwordx4 v241, %[wr], s44 offen offset:1024 nt lds\n\t" \
    "buffer_load_dwordx4 v242, %[wr], s44 offen offset:2048 nt lds\n\t" \
    "ds_read_b64_tr_b16 v[212:213], v244 offset:0\n\t" \
    "ds_read_b64_tr_b16 v[214:215], v244 offset:1536\n\t" \
    "ds_read_b64_tr_b16 v[44:45], v244 offset:32\n\t" \
    "ds_read_b64_tr_b16 v[46:47], v244 offset:1568\n\t" \
    "ds_read_b64_tr_b16 v[52:53], v244 offset:64\n\t" \
    "ds_read_b64_tr_b16 v[54:55], v244 offset:1600\n\t" \
    "v_add_u32 v244, 0x0, v243\n\t" \
    "s_waitcnt vmcnt(12) lgkmcnt(0)\n\t" \
    "s_barrier \n\t" \
    "v_mov_b32 v246, 0\n\t" \
    "buffer_load_dword v246, v246, s[60:63], %[mo] offen sc1\n\t" \
    "s_add_u32 m0, %[wm], 0xf000\n\t" \
    "s_mov_b32 s44, 0x4e0000\n\t" \
    "buffer_load_dwordx4 v240, %[wr], s44 offen offset:0 nt lds\n\t" \
    "buffer_load_dwordx4 v241, %[wr], s44 offen offset:1024 nt lds\n\t" \
    "buffer_load_dwordx4 v242, %[wr], s44 offen offset:2048 nt lds\n\t" \
    "ds_read_b64_tr_b16 v[136:137], v244 offset:0\n\t" \
    "ds_read_b64_tr_b16 v[138:139], v244 offset:1536\n\t" \
    "ds_read_b64_tr_b16 v[144:145], v244 offset:32\n\t" \
    "ds_read_b64_tr_b16 v[146:147], v244 offset:1568\n\t" \
    "ds_read_b64_tr_b16 v[152:153], v244 offset:64\n\t" \
    "ds_read_b64_tr_b16 v[154:155], v244 offset:1600\n\t" \
    "v_add_u32 v244, 0x3000, v243\n\t" \
    "s_waitcnt vmcnt(12) lgkmcnt(0)\n\t" \
    "s_barrier \n\t" \
    "s_add_u32 m0, %[wm], 0x0\n\t" \
    "s_mov_b32 s44, 0x540000\n\t" \
    "buffer_load_dwordx4 v240, %[wr], s44 offen offset:0 nt lds\n\t" \
    "buffer_load_dwordx4 v241, %[wr], s44 offen offset:1024 nt lds\n\t" \
    "buffer_load_dwordx4 v242, %[wr], s44 offen offset:2048 nt lds\n\t" \
    "ds_read_b64_tr_b16 v[140:141], v244 offset:0\n\t" \
    "ds_read_b64_tr_b16 v[142:143], v244 offset:1536\n\t" \
    "ds_read_b64_tr_b16 v[148:149], v244 offset:32\n\t" \
    "ds_read_b64_tr_b16 v[150:151], v244 offset:1568\n\t" \
    "ds_read_b64_tr_b16 v[156:157], v244 offset:64\n\t" \
    "ds_read_b64_tr_b16 v[158:159], v244 offset:1600\n\t" \
    "v_add_u32 v244, 0x6000, v243\n\t" \
    "s_waitcnt vmcnt(12) lgkmcnt(0)\n\t" \
    "s_barrier \n\t" \
    "s_add_u32 m0, %[wm], 0x3000\n\t" \
    "s_mov_b32 s44, 0x5a0000\n\t" \
    "buffer_load_dwordx4 v240, %[wr], s44 offen offset:0 nt lds\n\t" \
    "buffer_load_dwordx4 v241, %[wr], s44 offen offset:1024 nt lds\n\t" \
    "buffer_load_dwordx4 v242, %[wr], s44 offen offset:2048 nt lds\n\t" \
    "ds_read_b64_tr_b16 v[216:217], v244 offset:0\n\t" \
    "ds_read_b64_tr_b16 v[218:219], v244 offset:1536\n\t" \
    "ds_read_b64_tr_b16 v[80:81], v244 offset:32\n\t" \
    "ds_read_b64_tr_b16 v[82:83], v244 offset:1568\n\t" \
    "ds_read_b64_tr_b16 v[88:89], v244 offset:64\n\t" \
    "ds_read_b64_tr_b16 v[90:91], v244 offset:1600\n\t" \
    "v_add_u32 v244, 0x9000, v243\n\t" \
    "s_waitcnt vmcnt(12) lgkmcnt(0)\n\t" \
    "s_barrier \n\t" \
    "s_add_u32 m0, %[wm], 0x6000\n\t" \
    "s_mov_b32 s44, 0x600000\n\t" \
    "buffer_load_dwordx4 v240, %[wr], s44 offen offset:0 nt lds\n\t" \
    "buffer_load_dwordx4 v241, %[wr], s44 offen offset:1024 nt lds\n\t" \
    "buffer_load_dwordx4 v242, %[wr], s44 offen offset:2048 nt lds\n\t" \
    "ds_read_b64_tr_b16 v[220:221], v244 offset:0\n\t" \
    "ds_read_b64_tr_b16 v[222:223], v244 offset:1536\n\t" \
    "ds_read_b64_tr_b16 v[84:85], v244 offset:32\n\t" \
    "ds_read_b64_tr_b16 v[86:87], v244 offset:1568\n\t" \
    "ds_read_b64_tr_b16 v[92:93], v244 offset:64\n\t" \
    "ds_read_b64_tr_b16 v[94:95], v244 offset:1600\n\t" \
    "v_add_u32 v244, 0xc000, v243\n\t" \
    "s_waitcnt vmcnt(12) lgkmcnt(0)\n\t" \
    "s_barrier \n\t" \
    "s_add_u32 m0, %[wm], 0x9000\n\t" \
    "s_mov_b32 s44, 0x660000\n\t" \
    "buffer_load_dwordx4 v240, %[wr], s44 offen offset:0 nt lds\n\t" \
    "buffer_load_dwordx4 v241, %[wr], s44 offen offset:1024 nt lds\n\t" \
    "buffer_load_dwordx4 v242, %[wr], s44 offen offset:2048 nt lds\n\t" \
    "ds_read_b64_tr_b16 v[16:17], v244 offset:0\n\t" \
    "ds_read_b64_tr_b16 v[18:19], v244 offset:1536\n\t" \
    "ds_read_b64_tr_b16 v[24:25], v244 offset:32\n\t" \
    "ds_read_b64_tr_b16 v[26:27], v244 offset:1568\n\t" \
    "ds_read_b64_tr_b16 v[32:33], v244 offset:64\n\t" \
    "ds_read_b64_tr_b16 v[34:35], v244 offset:1600\n\t" \
    "v_add_u32 v244, 0xf000, v243\n\t" \
    "s_waitcnt vmcnt(12) lgkmcnt(0)\n\t" \
    "s_barrier \n\t" \
    ".Lv11_%=:\n\t" \
    "v_readfirstlane_b32 s43, v246\n\t" \
    "s_nop 0\n\t" \
    "s_cmp_lg_u32 s43, -1\n\t" \
    "s_cbranch_scc1 .Lv12_%=\n\t" \
    "s_cmp_eq_u32 s41, 0\n\t" \
    "s_cbranch_scc1 .Lv12_%=\n\t" \
    "s_sub_u32 s41, s41, 1\n\t" \
    "s_sleep 4\n\t" \
    "v_mov_b32 v246, 0\n\t" \
    "buffer_load_dword v246, v246, s[60:63], %[mo] offen sc1\n\t" \
    "s_waitcnt vmcnt(0)\n\t" \
    "s_branch .Lv11_%=\n\t" \
    ".Lv12_%=:\n\t" \
    "v_mov_b32 v246, 0x8000\n\t" \
    "s_mov_b64 exec, 1\n\t" \
    "buffer_store_dword v246, v246, s[60:63], %[mo] offen sc1\n\t" \
    "s_mov_b64 exec, -1\n\t" \
    "s_and_b32 s44, s43, 0x5000\n\t" \
    "s_xor_b32 %[mb], %[mb], s44\n\t" \
    "s_xor_b32 %[fl], %[fl], s44\n\t" \
    "s_and_b32 s43, s43, 0x2000\n\t" \
    "s_xor_b32 s43, s43, %[lx]\n\t" \
    "s_add_u32 s60, s60, s43\n\t" \
    "s_addc_u32 s61, s61, 0\n\t" \
    "v_and_b32 v252, 0xf0, v253\n\t" \
    "v_lshlrev_b32 v252, 3, v252\n\t" \
    "buffer_load_dword v252, v252, s[60:63], %[fl] offen sc1\n\t" \
    "s_add_u32 m0, %[wm], 0xc000\n\t" \
    "s_mov_b32 s44, 0x6c0000\n\t" \
    "buffer_load_dwordx4 v240, %[wr], s44 offen offset:0 nt lds\n\t" \
    "buffer_load_dwordx4 v241, %[wr], s44 offen offset:1024 nt lds\n\t" \
    "buffer_load_dwordx4 v242, %[wr], s44 offen offset:2048 nt lds\n\t" \
    "ds_read_b64_tr_b16 v[20:21], v244 offset:0\n\t" \
    "ds_read_b64_tr_b16 v[22:23], v244 offset:1536\n\t" \
    "ds_read_b64_tr_b16 v[28:29], v244 offset:32\n\t" \
    "ds_read_b64_tr_b16 v[30:31], v244 offset:1568\n\t" \
    "ds_read_b64_tr_b16 v[36:37], v244 offset:64\n\t" \
    "ds_read_b64_tr_b16 v[38:39], v244 offset:1600\n\t" \
    "v_add_u32 v244, 0x0, v243\n\t" \
    "s_waitcnt vmcnt(12) lgkmcnt(0)\n\t" \
    "s_barrier \n\t" \
    "s_add_u32 m0, %[wm], 0xf000\n\t" \
    "s_mov_b32 s44, 0x720000\n\t" \
    "buffer_load_dwordx4 v240, %[wr], s44 offen offset:0 nt lds\n\t" \
    "buffer_load_dwordx4 v241, %[wr], s44 offen offset:1024 nt lds\n\t" \
    "buffer_load_dwordx4 v242, %[wr], s44 offen offset:2048 nt lds\n\t" \
    "ds_read_b64_tr_b16 v[224:225], v244 offset:0\n\t" \
    "ds_read_b64_tr_b16 v[226:227], v244 offset:1536\n\t" \
    "ds_read_b64_tr_b16 v[120:121], v244 offset:32\n\t" \
    "ds_read_b64_tr_b16 v[122:123], v244 offset:1568\n\t" \
    "ds_read_b64_tr_b16 v[128:129], v244 offset:64\n\t" \
    "ds_read_b64_tr_b16 v[130:131], v244 offset:1600\n\t" \
    "v_add_u32 v244, 0x3000, v243\n\t" \
    "s_waitcnt vmcnt(12) lgkmcnt(0)\n\t" \
    "s_barrier \n\t" \
    "ds_read_b64_tr_b16 v[228:229], v244 offset:0\n\t" \
    "ds_read_b64_tr_b16 v[230:231], v244 offset:1536\n\t" \
    "ds_read_b64_tr_b16 v[124:125], v244 offset:32\n\t" \
    "ds_read_b64_tr_b16 v[126:127], v244 offset:1568\n\t" \
    "ds_read_b64_tr_b16 v[132:133], v244 offset:64\n\t" \
    "ds_read_b64_tr_b16 v[134:135], v244 offset:1600\n\t" \
    "v_add_u32 v244, 0x6000, v243\n\t" \
    "s_waitcnt vmcnt(9) lgkmcnt(0)\n\t" \
    "s_barrier \n\t" \
    "ds_read_b64_tr_b16 v[232:233], v244 offset:0\n\t" \
    "ds_read_b64_tr_b16 v[234:235], v244 offset:1536\n\t" \
    "ds_read_b64_tr_b16 v[160:161], v244 offset:32\n\t" \
    "ds_read_b64_tr_b16 v[162:163], v244 offset:1568\n\t" \
    "ds_read_b64_tr_b16 v[168:169], v244 offset:64\n\t" \
    "ds_read_b64_tr_b16 v[170:171], v244 offset:1600\n\t" \
    "v_add_u32 v244, 0x9000, v243\n\t" \
    "s_waitcnt vmcnt(6) lgkmcnt(0)\n\t" \
    "s_barrier \n\t" \
    "ds_read_b64_tr_b16 v[236:237], v244 offset:0\n\t" \
    "ds_read_b64_tr_b16 v[238:239], v244 offset:1536\n\t" \
    "ds_read_b64_tr_b16 v[164:165], v244 offset:32\n\t" \
    "ds_read_b64_tr_b16 v[166:167], v244 offset:1568\n\t" \
    "ds_read_b64_tr_b16 v[172:173], v244 offset:64\n\t" \
    "ds_read_b64_tr_b16 v[174:175], v244 offset:1600\n\t" \
    "v_add_u32 v244, 0xc000, v243\n\t" \
    "s_waitcnt vmcnt(3) lgkmcnt(0)\n\t" \
    "s_barrier \n\t" \
    "ds_read_b64_tr_b16 v[176:177], v244 offset:0\n\t" \
    "ds_read_b64_tr_b16 v[178:179], v244 offset:1536\n\t" \
    "ds_read_b64_tr_b16 v[184:185], v244 offset:32\n\t" \
    "ds_read_b64_tr_b16 v[186:187], v244 offset:1568\n\t" \
    "ds_read_b64_tr_b16 v[192:193], v244 offset:64\n\t" \
    "ds_read_b64_tr_b16 v[194:195], v244 offset:1600\n\t" \
    "s_mov_b32 s43, 0\n\t" \
    "v_cmp_eq_u32 vcc, 0, v252\n\t" \
    "s_cbranch_vccnz .Lv13_%=\n\t" \
    "s_mov_b32 m0, 0x1000\n\t" \
    "s_add_u32 s49, s42, 0x0\n\t" \
    "buffer_load_dwordx4 v253, s[60:63], s49 offen offset:0 sc1 lds\n\t" \
    "buffer_load_dwordx4 v253, s[60:63], s49 offen offset:1024 sc1 lds\n\t" \
    "buffer_load_dwordx4 v253, s[60:63], s49 offen offset:2048 sc1 lds\n\t" \
    "buffer_load_dwordx4 v253, s[60:63], s49 offen offset:3072 sc1 lds\n\t" \
    "s_mov_b32 m0, 0x4400\n\t" \
    "s_add_u32 s49, s42, 0xc400\n\t" \
    "buffer_load_dwordx4 v253, s[60:63], s49 offen offset:0 sc1 lds\n\t" \
    "s_mov_b32 m0, 0x6000\n\t" \
    "s_add_u32 s49, s42, 0x1000\n\t" \
    "buffer_load_dwordx4 v253, s[60:63], s49 offen offset:0 sc1 lds\n\t" \
    "buffer_load_dwordx4 v253, s[60:63], s49 offen offset:1024 sc1 lds\n\t" \
    "buffer_load_dwordx4 v253, s[60:63], s49 offen offset:2048 sc1 lds\n\t" \
    "buffer_load_dwordx4 v253, s[60:63], s49 offen offset:3072 sc1 lds\n\t" \
    "s_mov_b32 m0, 0x9400\n\t" \
    "s_add_u32 s49, s42, 0xd400\n\t" \
    "buffer_load_dwordx4 v253, s[60:63], s49 offen offset:0 sc1 lds\n\t" \
    "s_mov_b32 s43, 1\n\t" \
    ".Lv13_%=:\n\t" \
    "v_add_u32 v244, 0xf000, v243\n\t" \
    "s_waitcnt vmcnt(0) lgkmcnt(0)\n\t" \
    "s_barrier \n\t" \
    "ds_read_b64_tr_b16 v[180:181], v244 offset:0\n\t" \
    "ds_read_b64_tr_b16 v[182:183], v244 offset:1536\n\t" \
    "ds_read_b64_tr_b16 v[188:189], v244 offset:32\n\t" \
    "ds_read_b64_tr_b16 v[190:191], v244 offset:1568\n\t" \
    "ds_read_b64_tr_b16 v[196:197], v244 offset:64\n\t" \
    "ds_read_b64_tr_b16 v[198:199], v244 offset:1600\n\t" \
    "s_waitcnt lgkmcnt(0)\n\t" \
    "s_barrier \n\t" \
    "ds_write_b128 v254, v[200:203] offset:40960\n\t" \
    "ds_write_b128 v254, v[204:207] offset:41984\n\t" \
    "ds_write_b128 v254, v[208:211] offset:43008\n\t" \
    "ds_write_b128 v254, v[212:215] offset:44032\n\t" \
    "ds_write_b128 v254, v[216:219] offset:45056\n\t" \
    "ds_write_b128 v254, v[220:223] offset:46080\n\t" \
    "ds_write_b128 v254, v[224:227] offset:47104\n\t" \
    "ds_write_b128 v254, v[228:231] offset:48128\n\t" \
    "ds_write_b128 v254, v[232:235] offset:49152\n\t" \
    "ds_write_b128 v254, v[236:239] offset:50176\n\t" \
    "s_cmp_lg_u32 s43, 0\n\t" \
    "s_cbranch_scc1 .Lv14_%=\n\t" \
    "s_mov_b32 s44, 0\n\t" \
    ".Lv15_%=:\n\t" \
    "v_cmp_ge_u32 vcc, s44, v252\n\t" \
    "s_cbranch_vccz .Lv16_%=\n\t" \
    "s_cmp_eq_u32 s41, 0\n\t" \
    "s_cbranch_scc1 .Lv16_%=\n\t" \
    "s_sub_u32 s41, s41, 1\n\t" \
    "s_sleep 8\n\t" \
    "v_and_b32 v252, 0xf0, v253\n\t" \
    "v_lshlrev_b32 v252, 3, v252\n\t" \
    "buffer_load_dword v252, v252, s[60:63], %[fl] offen sc1\n\t" \
    "s_waitcnt vmcnt(0)\n\t" \
    "s_branch .Lv15_%=\n\t" \
    ".Lv16_%=:\n\t" \
    "s_mov_b32 m0, 0x1000\n\t" \
    "s_add_u32 s49, s42, 0x0\n\t" \
    "buffer_load_dwordx4 v253, s[60:63], s49 offen offset:0 sc1 lds\n\t" \
    "buffer_load_dwordx4 v253, s[60:63], s49 offen offset:1024 sc1 lds\n\t" \
    "buffer_load_dwordx4 v253, s[60:63], s49 offen offset:2048 sc1 lds\n\t" \
    "buffer_load_dwordx4 v253, s[60:63], s49 offen offset:3072 sc1 lds\n\t" \
    "s_mov_b32 m0, 0x4400\n\t" \
    "s_add_u32 s49, s42, 0xc400\n\t" \
    "buffer_load_dwordx4 v253, s[60:63], s49 offen offset:0 sc1 lds\n\t" \
    "s_mov_b32 m0, 0x6000\n\t" \
    "s_add_u32 s49, s42, 0x1000\n\t" \
    "buffer_load_dwordx4 v253, s[60:63], s49 offen offset:0 sc1 lds\n\t" \
    "buffer_load_dwordx4 v253, s[60:63], s49 offen offset:1024 sc1 lds\n\t" \
    "buffer_load_dwordx4 v253, s[60:63], s49 offen offset:2048 sc1 lds\n\t" \
    "buffer_load_dwordx4 v253, s[60:63], s49 offen offset:3072 sc1 lds\n\t" \
    "s_mov_b32 m0, 0x9400\n\t" \
    "s_add_u32 s49, s42, 0xd400\n\t" \
    "buffer_load_dwordx4 v253, s[60:63], s49 offen offset:0 sc1 lds\n\t" \
    ".Lv14_%=:\n\t" \
    "s_waitcnt vmcnt(0) lgkmcnt(0)\n\t" \
    "s_barrier \n\t" \
    "ds_read_b128 v[224:227], v253 offset:4096\n\t" \
    "ds_read_b128 v[244:247], v254 offset:40960\n\t" \
    "ds_read_b128 v[228:231], v253 offset:5120\n\t" \
    "ds_read_b128 v[232:235], v253 offset:24576\n\t" \
    "ds_read_b128 v[236:239], v253 offset:25600\n\t" \
    "ds_read_b128 v[248:251], v254 offset:41984\n\t" \
    ".p2align 8\n\t" \
    ".Lv17_%=:\n\t" \
    "s_waitcnt lgkmcnt(5)\n\t" \
    "v_mfma_f32_16x16x32_bf16 v[204:207], v[0:3], v[224:227], 0\n\t" \
    "s_waitcnt lgkmcnt(3)\n\t" \
    "v_mfma_f32_16x16x32_bf16 v[204:207], v[4:7], v[228:231], v[204:207]\n\t" \
    "ds_read_b128 v[240:243], v253 offset:12288\n\t" \
    "v_mfma_f32_16x16x32_bf16 v[208:211], v[8:11], v[224:227], 0\n\t" \
    "v_mfma_f32_16x16x32_bf16 v[208:211], v[12:15], v[228:231], v[208:211]\n\t" \
    "v_mfma_f32_16x16x32_bf16 v[200:203], v[244:247], v[224:227], 0\n\t" \
    "s_waitcnt lgkmcnt(1)\n\t" \
    "v_mfma_f32_16x16x32_bf16 v[200:203], v[248:251], v[228:231], v[200:203]\n\t" \
    "v_mfma_f32_16x16x32_bf16 v[216:219], v[0:3], v[232:235], 0\n\t" \
    "ds_read_b128 v[224:227], v253 offset:13312\n\t" \
    "v_mfma_f32_16x16x32_bf16 v[216:219], v[4:7], v[236:239], v[216:219]\n\t" \
    "ds_read_b128 v[228:231], v253 offset:6144\n\t" \
    "v_mfma_f32_16x16x32_bf16 v[220:223], v[8:11], v[232:235], 0\n\t" \
    "v_mfma_f32_16x16x32_bf16 v[220:223], v[12:15], v[236:239], v[220:223]\n\t" \
    "v_mfma_f32_16x16x32_bf16 v[212:215], v[244:247], v[232:235], 0\n\t" \
    "ds_read_b128 v[244:247], v254 offset:43008\n\t" \
    "v_mfma_f32_16x16x32_bf16 v[212:215], v[248:251], v[236:239], v[212:215]\n\t" \
    "ds_read_b128 v[248:251], v254 offset:44032\n\t" \
    "s_waitcnt lgkmcnt(4)\n\t" \
    "v_mfma_f32_16x16x32_bf16 v[204:207], v[24:27], v[240:243], v[204:207]\n\t" \
    "ds_read_b128 v[232:235], v253 offset:7168\n\t" \
    "s_waitcnt lgkmcnt(4)\n\t" \
    "v_mfma_f32_16x16x32_bf16 v[204:207], v[28:31], v[224:227], v[204:207]\n\t" \
    "ds_read_b128 v[236:239], v253 offset:26624\n\t" \
    "v_mfma_f32_16x16x32_bf16 v[208:211], v[32:35], v[240:243], v[208:211]\n\t" \
    "v_mfma_f32_16x16x32_bf16 v[208:211], v[36:39], v[224:227], v[208:211]\n\t" \
    "v_mfma_f32_16x16x32_bf16 v[200:203], v[16:19], v[240:243], v[200:203]\n\t" \
    "v_mfma_f32_16x16x32_bf16 v[200:203], v[20:23], v[224:227], v[200:203]\n\t" \
    "s_waitcnt lgkmcnt(4)\n\t" \
    "v_mfma_f32_16x16x32_bf16 v[204:207], v[40:43], v[228:231], v[204:207]\n\t" \
    "ds_read_b128 v[240:243], v253 offset:27648\n\t" \
    "s_waitcnt lgkmcnt(2)\n\t" \
    "v_mfma_f32_16x16x32_bf16 v[204:207], v[44:47], v[232:235], v[204:207]\n\t" \
    "ds_read_b128 v[224:227], v253 offset:0\n\t" \
    "v_mfma_f32_16x16x32_bf16 v[208:211], v[48:51], v[228:231], v[208:211]\n\t" \
    "v_mfma_f32_16x16x32_bf16 v[208:211], v[52:55], v[232:235], v[208:211]\n\t" \
    "v_mfma_f32_16x16x32_bf16 v[200:203], v[244:247], v[228:231], v[200:203]\n\t" \
    "v_mfma_f32_16x16x32_bf16 v[200:203], v[248:251], v[232:235], v[200:203]\n\t" \
    "s_waitcnt lgkmcnt(2)\n\t" \
    "v_mfma_f32_16x16x32_bf16 v[216:219], v[40:43], v[236:239], v[216:219]\n\t" \
    "ds_read_b128 v[228:231], v253 offset:1024\n\t" \
    "s_add_u32 s44, s40, 1\n\t" \
    "s_waitcnt lgkmcnt(2)\n\t" \
    "v_mfma_f32_16x16x32_bf16 v[216:219], v[44:47], v[240:243], v[216:219]\n\t" \
    "ds_read_b128 v[232:235], v253 offset:10240\n\t" \
    "s_and_b32 s45, s44, 15\n\t" \
    "v_mfma_f32_16x16x32_bf16 v[220:223], v[48:51], v[236:239], v[220:223]\n\t" \
    "s_cmp_eq_u32 s45, 0\n\t" \
    "v_mfma_f32_16x16x32_bf16 v[220:223], v[52:55], v[240:243], v[220:223]\n\t" \
    "s_cselect_b32 s45, %[cj], %[cs]\n\t" \
    "v_mfma_f32_16x16x32_bf16 v[212:215], v[244:247], v[236:239], v[212:215]\n\t" \
    "ds_read_b128 v[244:247], v254 offset:45056\n\t" \
    "s_cmp_eq_u32 s44, 64\n\t" \
    "v_mfma_f32_16x16x32_bf16 v[212:215], v[248:251], v[240:243], v[212:215]\n\t" \
    "ds_read_b128 v[248:251], v254 offset:46080\n\t" \
    "s_cselect_b32 s45, 0, s45\n\t" \
    "s_waitcnt lgkmcnt(4)\n\t" \
    "v_mfma_f32_16x16x32_bf16 v[204:207], v[64:67], v[224:227], v[204:207]\n\t" \
    "ds_read_b128 v[236:239], v253 offset:11264\n\t" \
    "s_add_u32 s42, s42, s45\n\t" \
    "s_waitcnt lgkmcnt(4)\n\t" \
    "v_mfma_f32_16x16x32_bf16 v[204:207], v[68:71], v[228:231], v[204:207]\n\t" \
    "ds_read_b128 v[240:243], v253 offset:30720\n\t" \
    "v_mfma_f32_16x16x32_bf16 v[208:211], v[72:75], v[224:227], v[208:211]\n\t" \
    "v_mfma_f32_16x16x32_bf16 v[208:211], v[76:79], v[228:231], v[208:211]\n\t" \
    "v_mfma_f32_16x16x32_bf16 v[200:203], v[56:59], v[224:227], v[200:203]\n\t" \
    "v_mfma_f32_16x16x32_bf16 v[200:203], v[60:63], v[228:231], v[200:203]\n\t" \
    "s_waitcnt lgkmcnt(4)\n\t" \
    "v_mfma_f32_16x16x32_bf16 v[204:207], v[80:83], v[232:235], v[204:207]\n\t" \
    "ds_read_b128 v[224:227], v253 offset:31744\n\t" \
    "s_waitcnt lgkmcnt(2)\n\t" \
    "v_mfma_f32_16x16x32_bf16 v[204:207], v[84:87], v[236:239], v[204:207]\n\t" \
    "ds_read_b128 v[228:231], v253 offset:2048\n\t" \
    "v_mfma_f32_16x16x32_bf16 v[208:211], v[88:91], v[232:235], v[208:211]\n\t" \
    "v_mfma_f32_16x16x32_bf16 v[208:211], v[92:95], v[236:239], v[208:211]\n\t" \
    "v_mfma_f32_16x16x32_bf16 v[200:203], v[244:247], v[232:235], v[200:203]\n\t" \
    "v_mfma_f32_16x16x32_bf16 v[200:203], v[248:251], v[236:239], v[200:203]\n\t" \
    "s_waitcnt lgkmcnt(2)\n\t" \
    "v_mfma_f32_16x16x32_bf16 v[216:219], v[80:83], v[240:243], v[216:219]\n\t" \
    "ds_read_b128 v[232:235], v253 offset:3072\n\t" \
    "s_and_b32 s44, s40, 3\n\t" \
    "s_waitcnt lgkmcnt(2)\n\t" \
    "v_mfma_f32_16x16x32_bf16 v[216:219], v[84:87], v[224:227], v[216:219]\n\t" \
    "ds_read_b128 v[236:239], v253 offset:14336\n\t" \
    "s_lshl_b32 s44, s44, 22\n\t" \
    "v_mfma_f32_16x16x32_bf16 v[220:223], v[88:91], v[240:243], v[220:223]\n\t" \
    "s_add_u32 s44, s44, %[mb]\n\t" \
    "v_mfma_f32_16x16x32_bf16 v[220:223], v[92:95], v[224:227], v[220:223]\n\t" \
    "s_add_u32 s46, s44, 0x800\n\t" \
    "v_mfma_f32_16x16x32_bf16 v[212:215], v[244:247], v[240:243], v[212:215]\n\t" \
    "ds_read_b128 v[244:247], v254 offset:47104\n\t" \
    "s_add_u32 s47, s44, 0x8800\n\t" \
    "v_mfma_f32_16x16x32_bf16 v[212:215], v[248:251], v[224:227], v[212:215]\n\t" \
    "ds_read_b128 v[248:251], v254 offset:48128\n\t" \
    "s_add_u32 s48, s44, 0xc00\n\t" \
    "s_waitcnt lgkmcnt(4)\n\t" \
    "v_mfma_f32_16x16x32_bf16 v[204:207], v[104:107], v[228:231], v[204:207]\n\t" \
    "ds_read_b128 v[240:243], v253 offset:15360\n\t" \
    "s_waitcnt lgkmcnt(4)\n\t" \
    "v_mfma_f32_16x16x32_bf16 v[204:207], v[108:111], v[232:235], v[204:207]\n\t" \
    "ds_read_b128 v[224:227], v253 offset:34816\n\t" \
    "v_mfma_f32_16x16x32_bf16 v[208:211], v[112:115], v[228:231], v[208:211]\n\t" \
    "v_mfma_f32_16x16x32_bf16 v[208:211], v[116:119], v[232:235], v[208:211]\n\t" \
    "v_mfma_f32_16x16x32_bf16 v[200:203], v[96:99], v[228:231], v[200:203]\n\t" \
    "v_mfma_f32_16x16x32_bf16 v[200:203], v[100:103], v[232:235], v[200:203]\n\t" \
    "s_waitcnt lgkmcnt(4)\n\t" \
    "v_mfma_f32_16x16x32_bf16 v[204:207], v[120:123], v[236:239], v[204:207]\n\t" \
    "ds_read_b128 v[228:231], v253 offset:35840\n\t" \
    "s_waitcnt lgkmcnt(2)\n\t" \
    "v_mfma_f32_16x16x32_bf16 v[204:207], v[124:127], v[240:243], v[204:207]\n\t" \
    "ds_read_b128 v[232:235], v253 offset:8192\n\t" \
    "v_mfma_f32_16x16x32_bf16 v[208:211], v[128:131], v[236:239], v[208:211]\n\t" \
    "v_mfma_f32_16x16x32_bf16 v[208:211], v[132:135], v[240:243], v[208:211]\n\t" \
    "v_mfma_f32_16x16x32_bf16 v[200:203], v[244:247], v[236:239], v[200:203]\n\t" \
    "v_mfma_f32_16x16x32_bf16 v[200:203], v[248:251], v[240:243], v[200:203]\n\t" \
    "s_waitcnt lgkmcnt(2)\n\t" \
    "v_mfma_f32_16x16x32_bf16 v[216:219], v[120:123], v[224:227], v[216:219]\n\t" \
    "ds_read_b128 v[236:239], v253 offset:9216\n\t" \
    "s_waitcnt lgkmcnt(2)\n\t" \
    "v_mfma_f32_16x16x32_bf16 v[216:219], v[124:127], v[228:231], v[216:219]\n\t" \
    "ds_read_b128 v[240:243], v253 offset:16384\n\t" \
    "v_mfma_f32_16x16x32_bf16 v[220:223], v[128:131], v[224:227], v[220:223]\n\t" \
    "v_mfma_f32_16x16x32_bf16 v[220:223], v[132:135], v[228:231], v[220:223]\n\t" \
    "v_mfma_f32_16x16x32_bf16 v[212:215], v[244:247], v[224:227], v[212:215]\n\t" \
    "ds_read_b128 v[244:247], v254 offset:49152\n\t" \
    "v_mfma_f32_16x16x32_bf16 v[212:215], v[248:251], v[228:231], v[212:215]\n\t" \
    "ds_read_b128 v[248:251], v254 offset:50176\n\t" \
    "v_and_b32 v252, 0xf0, v253\n\t" \
    "v_lshlrev_b32 v252, 3, v252\n\t" \
    "buffer_load_dword v252, v252, s[60:63], %[fl] offen sc1\n\t" \
    "s_waitcnt lgkmcnt(4)\n\t" \
    "v_mfma_f32_16x16x32_bf16 v[204:207], v[144:147], v[232:235], v[204:207]\n\t" \
    "ds_read_b128 v[224:227], v253 offset:17408\n\t" \
    "s_waitcnt lgkmcnt(4)\n\t" \
    "v_mfma_f32_16x16x32_bf16 v[204:207], v[148:151], v[236:239], v[204:207]\n\t" \
    "ds_read_b128 v[228:231], v253 offset:36864\n\t" \
    "v_mfma_f32_16x16x32_bf16 v[208:211], v[152:155], v[232:235], v[208:211]\n\t" \
    "v_mfma_f32_16x16x32_bf16 v[208:211], v[156:159], v[236:239], v[208:211]\n\t" \
    "v_mfma_f32_16x16x32_bf16 v[200:203], v[136:139], v[232:235], v[200:203]\n\t" \
    "v_mfma_f32_16x16x32_bf16 v[200:203], v[140:143], v[236:239], v[200:203]\n\t" \
    "s_waitcnt lgkmcnt(4)\n\t" \
    "v_mfma_f32_16x16x32_bf16 v[204:207], v[160:163], v[240:243], v[204:207]\n\t" \
    "ds_read_b128 v[232:235], v253 offset:37888\n\t" \
    "s_waitcnt lgkmcnt(2)\n\t" \
    "v_mfma_f32_16x16x32_bf16 v[204:207], v[164:167], v[224:227], v[204:207]\n\t" \
    "ds_read_b128 v[236:239], v253 offset:18432\n\t" \
    "v_mfma_f32_16x16x32_bf16 v[208:211], v[168:171], v[240:243], v[208:211]\n\t" \
    "v_mfma_f32_16x16x32_bf16 v[208:211], v[172:175], v[224:227], v[208:211]\n\t" \
    "v_mfma_f32_16x16x32_bf16 v[200:203], v[244:247], v[240:243], v[200:203]\n\t" \
    "v_mfma_f32_16x16x32_bf16 v[200:203], v[248:251], v[224:227], v[200:203]\n\t" \
    "s_waitcnt lgkmcnt(2)\n\t" \
    "v_mfma_f32_16x16x32_bf16 v[216:219], v[160:163], v[228:231], v[216:219]\n\t" \
    "ds_read_b128 v[240:243], v253 offset:19456\n\t" \
    "s_waitcnt lgkmcnt(2)\n\t" \
    "v_mfma_f32_16x16x32_bf16 v[216:219], v[164:167], v[232:235], v[216:219]\n\t" \
    "ds_read_b128 v[224:227], v253 offset:32768\n\t" \
    "v_mfma_f32_16x16x32_bf16 v[220:223], v[168:171], v[228:231], v[220:223]\n\t" \
    "v_mfma_f32_16x16x32_bf16 v[220:223], v[172:175], v[232:235], v[220:223]\n\t" \
    "v_mfma_f32_16x16x32_bf16 v[212:215], v[244:247], v[228:231], v[212:215]\n\t" \
    "v_mfma_f32_16x16x32_bf16 v[212:215], v[248:251], v[232:235], v[212:215]\n\t" \
    "s_waitcnt lgkmcnt(2)\n\t" \
    "v_mfma_f32_16x16x32_bf16 v[204:207], v[184:187], v[236:239], v[204:207]\n\t" \
    "ds_read_b128 v[228:231], v253 offset:33792\n\t" \
    "s_waitcnt lgkmcnt(2)\n\t" \
    "v_mfma_f32_16x16x32_bf16 v[204:207], v[188:191], v[240:243], v[204:207]\n\t" \
    "ds_read_b128 v[232:235], v253 offset:20480\n\t" \
    "v_mfma_f32_16x16x32_bf16 v[208:211], v[192:195], v[236:239], v[208:211]\n\t" \
    "v_mfma_f32_16x16x32_bf16 v[208:211], v[196:199], v[240:243], v[208:211]\n\t" \
    "v_mfma_f32_16x16x32_bf16 v[200:203], v[176:179], v[236:239], v[200:203]\n\t" \
    "v_mfma_f32_16x16x32_bf16 v[200:203], v[180:183], v[240:243], v[200:203]\n\t" \
    "s_waitcnt lgkmcnt(2)\n\t" \
    "v_mfma_f32_16x16x32_bf16 v[216:219], v[24:27], v[224:227], v[216:219]\n\t" \
    "ds_read_b128 v[236:239], v253 offset:21504\n\t" \
    "s_waitcnt lgkmcnt(2)\n\t" \
    "v_mfma_f32_16x16x32_bf16 v[216:219], v[28:31], v[228:231], v[216:219]\n\t" \
    "ds_read_b128 v[240:243], v253 offset:22528\n\t" \
    "v_mfma_f32_16x16x32_bf16 v[220:223], v[32:35], v[224:227], v[220:223]\n\t" \
    "v_cvt_pk_bf16_f32 v204, v204, v205\n\t" \
    "v_mfma_f32_16x16x32_bf16 v[220:223], v[36:39], v[228:231], v[220:223]\n\t" \
    "v_cvt_pk_bf16_f32 v205, v206, v207\n\t" \
    "v_mfma_f32_16x16x32_bf16 v[212:215], v[16:19], v[224:227], v[212:215]\n\t" \
    "ds_write_b64 v254, v[204:205] offset:10240\n\t" \
    "v_mfma_f32_16x16x32_bf16 v[212:215], v[20:23], v[228:231], v[212:215]\n\t" \
    "v_cvt_pk_bf16_f32 v208, v208, v209\n\t" \
    "s_waitcnt lgkmcnt(3)\n\t" \
    "v_mfma_f32_16x16x32_bf16 v[216:219], v[64:67], v[232:235], v[216:219]\n\t" \
    "ds_read_b128 v[224:227], v253 offset:23552\n\t" \
    "v_cvt_pk_bf16_f32 v209, v210, v211\n\t" \
    "s_waitcnt lgkmcnt(3)\n\t" \
    "v_mfma_f32_16x16x32_bf16 v[216:219], v[68:71], v[236:239], v[216:219]\n\t" \
    "ds_read_b128 v[228:231], v253 offset:28672\n\t" \
    "ds_write_b64 v254, v[208:209] offset:11264\n\t" \
    "v_mfma_f32_16x16x32_bf16 v[220:223], v[72:75], v[232:235], v[220:223]\n\t" \
    "v_cvt_pk_bf16_f32 v200, v200, v201\n\t" \
    "v_mfma_f32_16x16x32_bf16 v[220:223], v[76:79], v[236:239], v[220:223]\n\t" \
    "v_cvt_pk_bf16_f32 v201, v202, v203\n\t" \
    "v_mfma_f32_16x16x32_bf16 v[212:215], v[56:59], v[232:235], v[212:215]\n\t" \
    "ds_write_b64 v254, v[200:201] offset:9216\n\t" \
    "v_mfma_f32_16x16x32_bf16 v[212:215], v[60:63], v[236:239], v[212:215]\n\t" \
    "s_waitcnt lgkmcnt(5)\n\t" \
    "v_mfma_f32_16x16x32_bf16 v[216:219], v[104:107], v[240:243], v[216:219]\n\t" \
    "ds_read_b128 v[232:235], v253 offset:29696\n\t" \
    "s_waitcnt lgkmcnt(4)\n\t" \
    "v_mfma_f32_16x16x32_bf16 v[216:219], v[108:111], v[224:227], v[216:219]\n\t" \
    "ds_read_b128 v[236:239], v253 offset:38912\n\t" \
    "v_mfma_f32_16x16x32_bf16 v[220:223], v[112:115], v[240:243], v[220:223]\n\t" \
    "v_mfma_f32_16x16x32_bf16 v[220:223], v[116:119], v[224:227], v[220:223]\n\t" \
    "v_mfma_f32_16x16x32_bf16 v[212:215], v[96:99], v[240:243], v[212:215]\n\t" \
    "v_mfma_f32_16x16x32_bf16 v[212:215], v[100:103], v[224:227], v[212:215]\n\t" \
    "ds_read_b128 v[240:243], v253 offset:39936\n\t" \
    "s_waitcnt lgkmcnt(3)\n\t" \
    "s_barrier \n\t" \
    "v_mfma_f32_16x16x32_bf16 v[216:219], v[144:147], v[228:231], v[216:219]\n\t" \
    "s_waitcnt lgkmcnt(2)\n\t" \
    "v_mfma_f32_16x16x32_bf16 v[216:219], v[148:151], v[232:235], v[216:219]\n\t" \
    "s_waitcnt lgkmcnt(1)\n\t" \
    "v_mfma_f32_16x16x32_bf16 v[216:219], v[184:187], v[236:239], v[216:219]\n\t" \
    "s_waitcnt lgkmcnt(0)\n\t" \
    "v_mfma_f32_16x16x32_bf16 v[216:219], v[188:191], v[240:243], v[216:219]\n\t" \
    "v_mfma_f32_16x16x32_bf16 v[220:223], v[152:155], v[228:231], v[220:223]\n\t" \
    "v_mfma_f32_16x16x32_bf16 v[220:223], v[156:159], v[232:235], v[220:223]\n\t" \
    "v_mfma_f32_16x16x32_bf16 v[220:223], v[192:195], v[236:239], v[220:223]\n\t" \
    "v_cvt_pk_bf16_f32 v216, v216, v217\n\t" \
    "v_cvt_pk_bf16_f32 v217, v218, v219\n\t" \
    "v_mfma_f32_16x16x32_bf16 v[220:223], v[196:199], v[240:243], v[220:223]\n\t" \
    "ds_write_b64 v254, v[216:217] offset:16384\n\t" \
    "v_mfma_f32_16x16x32_bf16 v[212:215], v[136:139], v[228:231], v[212:215]\n\t" \
    "v_mfma_f32_16x16x32_bf16 v[212:215], v[140:143], v[232:235], v[212:215]\n\t" \
    "v_mfma_f32_16x16x32_bf16 v[212:215], v[176:179], v[236:239], v[212:215]\n\t" \
    "v_cvt_pk_bf16_f32 v220, v220, v221\n\t" \
    "v_cvt_pk_bf16_f32 v221, v222, v223\n\t" \
    "v_mfma_f32_16x16x32_bf16 v[212:215], v[180:183], v[240:243], v[212:215]\n\t" \
    "ds_write_b64 v254, v[220:221] offset:17408\n\t" \
    "s_nop 6\n\t" \
    "v_cvt_pk_bf16_f32 v212, v212, v213\n\t" \
    "v_cvt_pk_bf16_f32 v213, v214, v215\n\t" \
    "ds_write_b64 v254, v[212:213] offset:15360\n\t" \
    "s_waitcnt vmcnt(0) lgkmcnt(0)\n\t" \
    "s_barrier \n\t" \
    "s_setprio 2\n\t" \
    "s_add_u32 s44, s40, 1\n\t" \
    "s_min_u32 s44, s44, 63\n\t" \
    ".Lv18_%=:\n\t" \
    "v_cmp_ge_u32 vcc, s44, v252\n\t" \
    "s_cbranch_vccz .Lv19_%=\n\t" \
    "s_cmp_eq_u32 s41, 0\n\t" \
    "s_cbranch_scc1 .Lv19_%=\n\t" \
    "s_sub_u32 s41, s41, 1\n\t" \
    "s_sleep 2\n\t" \
    "v_and_b32 v252, 0xf0, v253\n\t" \
    "v_lshlrev_b32 v252, 3, v252\n\t" \
    "buffer_load_dword v252, v252, s[60:63], %[fl] offen sc1\n\t" \
    "s_waitcnt vmcnt(0)\n\t" \
    "s_branch .Lv18_%=\n\t" \
    ".Lv19_%=:\n\t" \
    "s_mov_b32 m0, 0x1000\n\t" \
    "s_add_u32 s49, s42, 0x0\n\t" \
    "buffer_load_dwordx4 v253, s[60:63], s49 offen offset:0 sc1 lds\n\t" \
    "s_sleep 1\n\t" \
    "buffer_load_dwordx4 v253, s[60:63], s49 offen offset:1024 sc1 lds\n\t" \
    "s_sleep 1\n\t" \
    "buffer_load_dwordx4 v253, s[60:63], s49 offen offset:2048 sc1 lds\n\t" \
    "s_sleep 1\n\t" \
    "buffer_load_dwordx4 v253, s[60:63], s49 offen offset:3072 sc1 lds\n\t" \
    "s_sleep 1\n\t" \
    "s_mov_b32 m0, 0x4400\n\t" \
    "s_add_u32 s49, s42, 0xc400\n\t" \
    "buffer_load_dwordx4 v253, s[60:63], s49 offen offset:0 sc1 lds\n\t" \
    "s_sleep 1\n\t" \
    "s_mov_b32 m0, 0x6000\n\t" \
    "s_add_u32 s49, s42, 0x1000\n\t" \
    "buffer_load_dwordx4 v253, s[60:63], s49 offen offset:0 sc1 lds\n\t" \
    "s_sleep 1\n\t" \
    "buffer_load_dwordx4 v253, s[60:63], s49 offen offset:1024 sc1 lds\n\t" \
    "s_sleep 1\n\t" \
    "buffer_load_dwordx4 v253, s[60:63], s49 offen offset:2048 sc1 lds\n\t" \
    "s_sleep 1\n\t" \
    "buffer_load_dwordx4 v253, s[60:63], s49 offen offset:3072 sc1 lds\n\t" \
    "s_sleep 1\n\t" \
    "s_mov_b32 m0, 0x9400\n\t" \
    "s_add_u32 s49, s42, 0xd400\n\t" \
    "buffer_load_dwordx4 v253, s[60:63], s49 offen offset:0 sc1 lds\n\t" \
    "s_sleep 1\n\t" \
    "ds_read_b128 v[200:203], v254 offset:40960\n\t" \
    "ds_read_b128 v[204:207], v254 offset:41984\n\t" \
    "ds_read_b128 v[216:219], v254 offset:43008\n\t" \
    "ds_read_b128 v[220:223], v254 offset:44032\n\t" \
    "v_mov_b64 v[208:209], 0\n\t" \
    "v_mov_b64 v[210:211], 0\n\t" \
    "v_mov_b64 v[244:245], 0\n\t" \
    "v_mov_b64 v[246:247], 0\n\t" \
    "v_mov_b64 v[248:249], 0\n\t" \
    "v_mov_b64 v[250:251], 0\n\t" \
    "s_sleep 5\n\t" \
    "s_mov_b32 m0, 0xa800\n\t" \
    "s_nop 0\n\t" \
    "buffer_load_dwordx4 v253, s[60:63], s46 offen sc1 lds\n\t" \
    "s_mov_b32 m0, 0xb800\n\t" \
    "s_nop 0\n\t" \
    "buffer_load_dwordx4 v253, s[60:63], s47 offen sc1 lds\n\t" \
    "s_mov_b32 m0, 0xac00\n\t" \
    "s_nop 0\n\t" \
    "buffer_load_dwordx4 v253, s[60:63], s48 offen sc1 lds\n\t" \
    "s_waitcnt lgkmcnt(0)\n\t" \
    "v_bfe_i32 v212, v253, 5, 1\n\t" \
    "v_and_b32 v212, 0xaaaa, v212\n\t" \
    "v_or_b32 v212, 0x4444, v212\n\t" \
    "v_and_b32 v213, 0xffff, v255\n\t" \
    "v_lshrrev_b32 v214, 16, v255\n\t" \
    ".Lv110_%=:\n\t" \
    "s_waitcnt vmcnt(0)\n\t" \
    "ds_read_b128 v[224:227], v253 offset:43008\n\t" \
    "ds_read_b128 v[228:231], v253 offset:47104\n\t" \
    "ds_read_b128 v[232:235], v253 offset:44032\n\t" \
    "s_waitcnt lgkmcnt(2)\n\t" \
    "v_max_u32 v215, v224, v225\n\t" \
    "v_max3_u32 v215, v226, v227, v215\n\t" \
    "s_waitcnt lgkmcnt(1)\n\t" \
    "v_max3_u32 v215, v228, v229, v215\n\t" \
    "v_max3_u32 v215, v230, v231, v215\n\t" \
    "s_waitcnt lgkmcnt(0)\n\t" \
    "v_max3_u32 v215, v232, v233, v215\n\t" \
    "v_max3_u32 v215, v234, v235, v215\n\t" \
    "v_cmp_eq_u32 vcc, -1, v215\n\t" \
    "s_cbranch_vccz .Lv111_%=\n\t" \
    "v_max_u32 v215, v224, v225\n\t" \
    "v_max3_u32 v215, v226, v227, v215\n\t" \
    "v_cmp_eq_u32 vcc, -1, v215\n\t" \
    "s_cbranch_vccz .Lv112_%=\n\t" \
    "s_mov_b32 m0, 0xa800\n\t" \
    "s_nop 0\n\t" \
    "buffer_load_dwordx4 v253, s[60:63], s46 offen sc1 lds\n\t" \
    ".Lv112_%=:\n\t" \
    "v_max_u32 v215, v228, v229\n\t" \
    "v_max3_u32 v215, v230, v231, v215\n\t" \
    "v_cmp_eq_u32 vcc, -1, v215\n\t" \
    "s_cbranch_vccz .Lv113_%=\n\t" \
    "s_mov_b32 m0, 0xb800\n\t" \
    "s_nop 0\n\t" \
    "buffer_load_dwordx4 v253, s[60:63], s47 offen sc1 lds\n\t" \
    ".Lv113_%=:\n\t" \
    "v_max_u32 v215, v232, v233\n\t" \
    "v_max3_u32 v215, v234, v235, v215\n\t" \
    "v_cmp_eq_u32 vcc, -1, v215\n\t" \
    "s_cbranch_vccz .Lv114_%=\n\t" \
    "s_mov_b32 m0, 0xac00\n\t" \
    "s_nop 0\n\t" \
    "buffer_load_dwordx4 v253, s[60:63], s48 offen sc1 lds\n\t" \
    ".Lv114_%=:\n\t" \
    "s_cmp_eq_u32 s41, 0\n\t" \
    "s_cbranch_scc1 .Lv111_%=\n\t" \
    "s_sub_u32 s41, s41, 1\n\t" \
    "s_branch .Lv110_%=\n\t" \
    ".Lv111_%=:\n\t" \
    "s_waitcnt lgkmcnt(0)\n\t" \
    "s_barrier \n\t" \
    "ds_read_b128 v[236:239], v253 offset:40960\n\t" \
    "ds_read_b128 v[240:243], v253 offset:46080\n\t" \
    "v_smfmac_f32_16x16x64_bf16 v[244:247], v[224:227], v[0:7], v212\n\t" \
    "v_smfmac_f32_16x16x64_bf16 v[248:251], v[224:227], v[8:15], v212\n\t" \
    "v_smfmac_f32_16x16x64_bf16 v[208:211], v[224:227], v[200:207], v212\n\t" \
    "ds_read_b128 v[200:203], v254 offset:45056\n\t" \
    "ds_read_b128 v[204:207], v254 offset:46080\n\t" \
    "ds_read_b128 v[224:227], v253 offset:41984\n\t" \
    "v_smfmac_f32_16x16x64_bf16 v[208:211], v[228:231], v[16:23], v212\n\t" \
    "v_smfmac_f32_16x16x64_bf16 v[244:247], v[228:231], v[24:31], v212\n\t" \
    "v_smfmac_f32_16x16x64_bf16 v[248:251], v[228:231], v[32:39], v212\n\t" \
    "ds_read_b128 v[228:231], v253 offset:48128\n\t" \
    "v_smfmac_f32_16x16x64_bf16 v[208:211], v[232:235], v[216:223], v212\n\t" \
    "ds_read_b128 v[216:219], v254 offset:47104\n\t" \
    "ds_read_b128 v[220:223], v254 offset:48128\n\t" \
    "v_smfmac_f32_16x16x64_bf16 v[244:247], v[232:235], v[40:47], v212\n\t" \
    "v_smfmac_f32_16x16x64_bf16 v[248:251], v[232:235], v[48:55], v212\n\t" \
    "ds_read_b128 v[232:235], v253 offset:45056\n\t" \
    "s_waitcnt lgkmcnt(8)\n\t" \
    "v_smfmac_f32_16x16x64_bf16 v[208:211], v[236:239], v[56:63], v212\n\t" \
    "v_smfmac_f32_16x16x64_bf16 v[244:247], v[236:239], v[64:71], v212\n\t" \
    "v_smfmac_f32_16x16x64_bf16 v[248:251], v[236:239], v[72:79], v212\n\t" \
    "ds_read_b128 v[236:239], v253 offset:49152\n\t" \
    "s_waitcnt lgkmcnt(6)\n\t" \
    "v_smfmac_f32_16x16x64_bf16 v[208:211], v[240:243], v[200:207], v212\n\t" \
    "ds_read_b128 v[200:203], v254 offset:49152\n\t" \
    "ds_read_b128 v[204:207], v254 offset:50176\n\t" \
    "v_smfmac_f32_16x16x64_bf16 v[244:247], v[240:243], v[80:87], v212\n\t" \
    "v_smfmac_f32_16x16x64_bf16 v[248:251], v[240:243], v[88:95], v212\n\t" \
    "ds_read_b128 v[240:243], v253 offset:50176\n\t" \
    "s_waitcnt lgkmcnt(8)\n\t" \
    "v_smfmac_f32_16x16x64_bf16 v[208:211], v[224:227], v[96:103], v212\n\t" \
    "v_smfmac_f32_16x16x64_bf16 v[244:247], v[224:227], v[104:111], v212\n\t" \
    "v_smfmac_f32_16x16x64_bf16 v[248:251], v[224:227], v[112:119], v212\n\t" \
    "s_waitcnt lgkmcnt(7)\n\t" \
    "v_smfmac_f32_16x16x64_bf16 v[244:247], v[228:231], v[120:127], v212\n\t" \
    "s_waitcnt lgkmcnt(4)\n\t" \
    "v_smfmac_f32_16x16x64_bf16 v[244:247], v[232:235], v[144:151], v212\n\t" \
    "s_waitcnt lgkmcnt(3)\n\t" \
    "v_smfmac_f32_16x16x64_bf16 v[244:247], v[236:239], v[160:167], v212\n\t" \
    "s_waitcnt lgkmcnt(0)\n\t" \
    "v_smfmac_f32_16x16x64_bf16 v[244:247], v[240:243], v[184:191], v212\n\t" \
    "v_smfmac_f32_16x16x64_bf16 v[248:251], v[228:231], v[128:135], v212\n\t" \
    "v_smfmac_f32_16x16x64_bf16 v[248:251], v[232:235], v[152:159], v212\n\t" \
    "v_smfmac_f32_16x16x64_bf16 v[248:251], v[236:239], v[168:175], v212\n\t" \
    "v_add_f32 v244, v244, v246\n\t" \
    "v_add_f32 v245, v245, v247\n\t" \
    "v_cvt_pk_bf16_f32 v244, v244, v245\n\t" \
    "v_smfmac_f32_16x16x64_bf16 v[248:251], v[240:243], v[192:199], v212\n\t" \
    "ds_write_b16 v213, v244 offset:512\n\t" \
    "ds_write_b16_d16_hi v213, v244 offset:528\n\t" \
    "v_smfmac_f32_16x16x64_bf16 v[208:211], v[228:231], v[216:223], v212\n\t" \
    "v_smfmac_f32_16x16x64_bf16 v[208:211], v[232:235], v[136:143], v212\n\t" \
    "v_smfmac_f32_16x16x64_bf16 v[208:211], v[236:239], v[200:207], v212\n\t" \
    "v_add_f32 v248, v248, v250\n\t" \
    "v_add_f32 v249, v249, v251\n\t" \
    "v_cvt_pk_bf16_f32 v248, v248, v249\n\t" \
    "v_smfmac_f32_16x16x64_bf16 v[208:211], v[240:243], v[176:183], v212\n\t" \
    "ds_write_b16 v213, v248 offset:1024\n\t" \
    "ds_write_b16_d16_hi v213, v248 offset:1040\n\t" \
    "s_nop 5\n\t" \
    "v_add_f32 v208, v208, v210\n\t" \
    "v_add_f32 v209, v209, v211\n\t" \
    "v_cvt_pk_bf16_f32 v208, v208, v209\n\t" \
    "ds_write_b16 v213, v208 offset:0\n\t" \
    "ds_write_b16_d16_hi v213, v208 offset:16\n\t" \
    "s_setprio 0\n\t" \
    "s_waitcnt lgkmcnt(0)\n\t" \
    "s_barrier \n\t" \
    "ds_read_b128 v[224:227], v253 offset:4096\n\t" \
    "ds_read_b128 v[244:247], v254 offset:40960\n\t" \
    "ds_read_b128 v[228:231], v253 offset:5120\n\t" \
    "ds_read_b128 v[232:235], v253 offset:24576\n\t" \
    "ds_read_b128 v[236:239], v253 offset:25600\n\t" \
    "ds_read_b128 v[248:251], v254 offset:41984\n\t" \
    "s_add_u32 s40, s40, 1\n\t" \
    "s_cmp_lg_u32 s40, 64\n\t" \
    "s_cbranch_scc1 .Lv17_%=\n\t" \
    "s_branch .Lend%=\n\t" \
    ".Lv0_%=:\n\t" \
    "s_mov_b32 s42, %[xs]\n\t" \
    "v_add_u32 v244, 0xc000, v243\n\t" \
    "s_waitcnt vmcnt(12) lgkmcnt(0)\n\t" \
    "s_barrier \n\t" \
    "s_add_u32 m0, %[wm], 0x9000\n\t" \
    "s_mov_b32 s44, 0x1e0000\n\t" \
    "buffer_load_dwordx4 v240, %[wr], s44 offen offset:0 nt lds\n\t" \
    "buffer_load_dwordx4 v241, %[wr], s44 offen offset:1024 nt lds\n\t" \
    "buffer_load_dwordx4 v242, %[wr], s44 offen offset:2048 nt lds\n\t" \
    "ds_read_b64_tr_b16 v[200:201], v244 offset:0\n\t" \
    "ds_read_b64_tr_b16 v[202:203], v244 offset:1536\n\t" \
    "ds_read_b64_tr_b16 v[0:1], v244 offset:32\n\t" \
    "ds_read_b64_tr_b16 v[2:3], v244 offset:1568\n\t" \
    "ds_read_b64_tr_b16 v[8:9], v244 offset:64\n\t" \
    "ds_read_b64_tr_b16 v[10:11], v244 offset:1600\n\t" \
    "v_add_u32 v244, 0xf000, v243\n\t" \
    "s_waitcnt vmcnt(12) lgkmcnt(0)\n\t" \
    "s_barrier \n\t" \
    "s_add_u32 m0, %[wm], 0xc000\n\t" \
    "s_mov_b32 s44, 0x240000\n\t" \
    "buffer_load_dwordx4 v240, %[wr], s44 offen offset:0 nt lds\n\t" \
    "buffer_load_dwordx4 v241, %[wr], s44 offen offset:1024 nt lds\n\t" \
    "buffer_load_dwordx4 v242, %[wr], s44 offen offset:2048 nt lds\n\t" \
    "ds_read_b64_tr_b16 v[204:205], v244 offset:0\n\t" \
    "ds_read_b64_tr_b16 v[206:207], v244 offset:1536\n\t" \
    "ds_read_b64_tr_b16 v[4:5], v244 offset:32\n\t" \
    "ds_read_b64_tr_b16 v[6:7], v244 offset:1568\n\t" \
    "ds_read_b64_tr_b16 v[12:13], v244 offset:64\n\t" \
    "ds_read_b64_tr_b16 v[14:15], v244 offset:1600\n\t" \
    "v_add_u32 v244, 0x0, v243\n\t" \
    "s_waitcnt vmcnt(12) lgkmcnt(0)\n\t" \
    "s_barrier \n\t" \
    "s_add_u32 m0, %[wm], 0xf000\n\t" \
    "s_mov_b32 s44, 0x2a0000\n\t" \
    "buffer_load_dwordx4 v240, %[wr], s44 offen offset:0 nt lds\n\t" \
    "buffer_load_dwordx4 v241, %[wr], s44 offen offset:1024 nt lds\n\t" \
    "buffer_load_dwordx4 v242, %[wr], s44 offen offset:2048 nt lds\n\t" \
    "ds_read_b64_tr_b16 v[208:209], v244 offset:0\n\t" \
    "ds_read_b64_tr_b16 v[210:211], v244 offset:1536\n\t" \
    "ds_read_b64_tr_b16 v[40:41], v244 offset:32\n\t" \
    "ds_read_b64_tr_b16 v[42:43], v244 offset:1568\n\t" \
    "ds_read_b64_tr_b16 v[48:49], v244 offset:64\n\t" \
    "ds_read_b64_tr_b16 v[50:51], v244 offset:1600\n\t" \
    "v_add_u32 v244, 0x3000, v243\n\t" \
    "s_waitcnt vmcnt(12) lgkmcnt(0)\n\t" \
    "s_barrier \n\t" \
    "s_add_u32 m0, %[wm], 0x0\n\t" \
    "s_mov_b32 s44, 0x300000\n\t" \
    "buffer_load_dwordx4 v240, %[wr], s44 offen offset:0 nt lds\n\t" \
    "buffer_load_dwordx4 v241, %[wr], s44 offen offset:1024 nt lds\n\t" \
    "buffer_load_dwordx4 v242, %[wr], s44 offen offset:2048 nt lds\n\t" \
    "ds_read_b64_tr_b16 v[212:213], v244 offset:0\n\t" \
    "ds_read_b64_tr_b16 v[214:215], v244 offset:1536\n\t" \
    "ds_read_b64_tr_b16 v[44:45], v244 offset:32\n\t" \
    "ds_read_b64_tr_b16 v[46:47], v244 offset:1568\n\t" \
    "ds_read_b64_tr_b16 v[52:53], v244 offset:64\n\t" \
    "ds_read_b64_tr_b16 v[54:55], v244 offset:1600\n\t" \
    "v_add_u32 v244, 0x6000, v243\n\t" \
    "s_waitcnt vmcnt(12) lgkmcnt(0)\n\t" \
    "s_barrier \n\t" \
    "s_add_u32 m0, %[wm], 0x3000\n\t" \
    "s_mov_b32 s44, 0x360000\n\t" \
    "buffer_load_dwordx4 v240, %[wr], s44 offen offset:0 nt lds\n\t" \
    "buffer_load_dwordx4 v241, %[wr], s44 offen offset:1024 nt lds\n\t" \
    "buffer_load_dwordx4 v242, %[wr], s44 offen offset:2048 nt lds\n\t" \
    "ds_read_b64_tr_b16 v[216:217], v244 offset:0\n\t" \
    "ds_read_b64_tr_b16 v[218:219], v244 offset:1536\n\t" \
    "ds_read_b64_tr_b16 v[80:81], v244 offset:32\n\t" \
    "ds_read_b64_tr_b16 v[82:83], v244 offset:1568\n\t" \
    "ds_read_b64_tr_b16 v[88:89], v244 offset:64\n\t" \
    "ds_read_b64_tr_b16 v[90:91], v244 offset:1600\n\t" \
    "v_add_u32 v244, 0x9000, v243\n\t" \
    "s_waitcnt vmcnt(12) lgkmcnt(0)\n\t" \
    "s_barrier \n\t" \
    "s_add_u32 m0, %[wm], 0x6000\n\t" \
    "s_mov_b32 s44, 0x3c0000\n\t" \
    "buffer_load_dwordx4 v240, %[wr], s44 offen offset:0 nt lds\n\t" \
    "buffer_load_dwordx4 v241, %[wr], s44 offen offset:1024 nt lds\n\t" \
    "buffer_load_dwordx4 v242, %[wr], s44 offen offset:2048 nt lds\n\t" \
    "ds_read_b64_tr_b16 v[220:221], v244 offset:0\n\t" \
    "ds_read_b64_tr_b16 v[222:223], v244 offset:1536\n\t" \
    "ds_read_b64_tr_b16 v[84:85], v244 offset:32\n\t" \
    "ds_read_b64_tr_b16 v[86:87], v244 offset:1568\n\t" \
    "ds_read_b64_tr_b16 v[92:93], v244 offset:64\n\t" \
    "ds_read_b64_tr_b16 v[94:95], v244 offset:1600\n\t" \
    "v_add_u32 v244, 0xc000, v243\n\t" \
    "s_waitcnt vmcnt(12) lgkmcnt(0)\n\t" \
    "s_barrier \n\t" \
    "s_add_u32 m0, %[wm], 0x9000\n\t" \
    "s_mov_b32 s44, 0x420000\n\t" \
    "buffer_load_dwordx4 v240, %[wr], s44 offen offset:0 nt lds\n\t" \
    "buffer_load_dwordx4 v241, %[wr], s44 offen offset:1024 nt lds\n\t" \
    "buffer_load_dwordx4 v242, %[wr], s44 offen offset:2048 nt lds\n\t" \
    "ds_read_b64_tr_b16 v[224:225], v244 offset:0\n\t" \
    "ds_read_b64_tr_b16 v[226:227], v244 offset:1536\n\t" \
    "ds_read_b64_tr_b16 v[120:121], v244 offset:32\n\t" \
    "ds_read_b64_tr_b16 v[122:123], v244 offset:1568\n\t" \
    "ds_read_b64_tr_b16 v[128:129], v244 offset:64\n\t" \
    "ds_read_b64_tr_b16 v[130:131], v244 offset:1600\n\t" \
    "v_add_u32 v244, 0xf000, v243\n\t" \
    "s_waitcnt vmcnt(12) lgkmcnt(0)\n\t" \
    "s_barrier \n\t" \
    "s_add_u32 m0, %[wm], 0xc000\n\t" \
    "s_mov_b32 s44, 0x480000\n\t" \
    "buffer_load_dwordx4 v240, %[wr], s44 offen offset:0 nt lds\n\t" \
    "buffer_load_dwordx4 v241, %[wr], s44 offen offset:1024 nt lds\n\t" \
    "buffer_load_dwordx4 v242, %[wr], s44 offen offset:2048 nt lds\n\t" \
    "ds_read_b64_tr_b16 v[228:229], v244 offset:0\n\t" \
    "ds_read_b64_tr_b16 v[230:231], v244 offset:1536\n\t" \
    "ds_read_b64_tr_b16 v[124:125], v244 offset:32\n\t" \
    "ds_read_b64_tr_b16 v[126:127], v244 offset:1568\n\t" \
    "ds_read_b64_tr_b16 v[132:133], v244 offset:64\n\t" \
    "ds_read_b64_tr_b16 v[134:135], v244 offset:1600\n\t" \
    "v_add_u32 v244, 0x0, v243\n\t" \
    "s_waitcnt vmcnt(12) lgkmcnt(0)\n\t" \
    "s_barrier \n\t" \
    "v_mov_b32 v246, 0\n\t" \
    "buffer_load_dword v246, v246, s[60:63], %[mo] offen sc1\n\t" \
    "s_add_u32 m0, %[wm], 0xf000\n\t" \
    "s_mov_b32 s44, 0x4e0000\n\t" \
    "buffer_load_dwordx4 v240, %[wr], s44 offen offset:0 nt lds\n\t" \
    "buffer_load_dwordx4 v241, %[wr], s44 offen offset:1024 nt lds\n\t" \
    "buffer_load_dwordx4 v242, %[wr], s44 offen offset:2048 nt lds\n\t" \
    "ds_read_b64_tr_b16 v[232:233], v244 offset:0\n\t" \
    "ds_read_b64_tr_b16 v[234:235], v244 offset:1536\n\t" \
    "ds_read_b64_tr_b16 v[160:161], v244 offset:32\n\t" \
    "ds_read_b64_tr_b16 v[162:163], v244 offset:1568\n\t" \
    "ds_read_b64_tr_b16 v[168:169], v244 offset:64\n\t" \
    "ds_read_b64_tr_b16 v[170:171], v244 offset:1600\n\t" \
    "v_add_u32 v244, 0x3000, v243\n\t" \
    "s_waitcnt vmcnt(12) lgkmcnt(0)\n\t" \
    "s_barrier \n\t" \
    "s_add_u32 m0, %[wm], 0x0\n\t" \
    "s_mov_b32 s44, 0x540000\n\t" \
    "buffer_load_dwordx4 v240, %[wr], s44 offen offset:0 nt lds\n\t" \
    "buffer_load_dwordx4 v241, %[wr], s44 offen offset:1024 nt lds\n\t" \
    "buffer_load_dwordx4 v242, %[wr], s44 offen offset:2048 nt lds\n\t" \
    "ds_read_b64_tr_b16 v[236:237], v244 offset:0\n\t" \
    "ds_read_b64_tr_b16 v[238:239], v244 offset:1536\n\t" \
    "ds_read_b64_tr_b16 v[164:165], v244 offset:32\n\t" \
    "ds_read_b64_tr_b16 v[166:167], v244 offset:1568\n\t" \
    "ds_read_b64_tr_b16 v[172:173], v244 offset:64\n\t" \
    "ds_read_b64_tr_b16 v[174:175], v244 offset:1600\n\t" \
    "v_add_u32 v244, 0x6000, v243\n\t" \
    "s_waitcnt vmcnt(12) lgkmcnt(0)\n\t" \
    "s_barrier \n\t" \
    "s_add_u32 m0, %[wm], 0x3000\n\t" \
    "s_mov_b32 s44, 0x5a0000\n\t" \
    "buffer_load_dwordx4 v240, %[wr], s44 offen offset:0 nt lds\n\t" \
    "buffer_load_dwordx4 v241, %[wr], s44 offen offset:1024 nt lds\n\t" \
    "buffer_load_dwordx4 v242, %[wr], s44 offen offset:2048 nt lds\n\t" \
    "ds_read_b64_tr_b16 v[16:17], v244 offset:0\n\t" \
    "ds_read_b64_tr_b16 v[18:19], v244 offset:1536\n\t" \
    "ds_read_b64_tr_b16 v[24:25], v244 offset:32\n\t" \
    "ds_read_b64_tr_b16 v[26:27], v244 offset:1568\n\t" \
    "ds_read_b64_tr_b16 v[32:33], v244 offset:64\n\t" \
    "ds_read_b64_tr_b16 v[34:35], v244 offset:1600\n\t" \
    "v_add_u32 v244, 0x9000, v243\n\t" \
    "s_waitcnt vmcnt(12) lgkmcnt(0)\n\t" \
    "s_barrier \n\t" \
    "s_add_u32 m0, %[wm], 0x6000\n\t" \
    "s_mov_b32 s44, 0x600000\n\t" \
    "buffer_load_dwordx4 v240, %[wr], s44 offen offset:0 nt lds\n\t" \
    "buffer_load_dwordx4 v241, %[wr], s44 offen offset:1024 nt lds\n\t" \
    "buffer_load_dwordx4 v242, %[wr], s44 offen offset:2048 nt lds\n\t" \
    "ds_read_b64_tr_b16 v[20:21], v244 offset:0\n\t" \
    "ds_read_b64_tr_b16 v[22:23], v244 offset:1536\n\t" \
    "ds_read_b64_tr_b16 v[28:29], v244 offset:32\n\t" \
    "ds_read_b64_tr_b16 v[30:31], v244 offset:1568\n\t" \
    "ds_read_b64_tr_b16 v[36:37], v244 offset:64\n\t" \
    "ds_read_b64_tr_b16 v[38:39], v244 offset:1600\n\t" \
    "v_add_u32 v244, 0xc000, v243\n\t" \
    "s_waitcnt vmcnt(12) lgkmcnt(0)\n\t" \
    "s_barrier \n\t" \
    "s_add_u32 m0, %[wm], 0x9000\n\t" \
    "s_mov_b32 s44, 0x660000\n\t" \
    "buffer_load_dwordx4 v240, %[wr], s44 offen offset:0 nt lds\n\t" \
    "buffer_load_dwordx4 v241, %[wr], s44 offen offset:1024 nt lds\n\t" \
    "buffer_load_dwordx4 v242, %[wr], s44 offen offset:2048 nt lds\n\t" \
    "ds_read_b64_tr_b16 v[56:57], v244 offset:0\n\t" \
    "ds_read_b64_tr_b16 v[58:59], v244 offset:1536\n\t" \
    "ds_read_b64_tr_b16 v[64:65], v244 offset:32\n\t" \
    "ds_read_b64_tr_b16 v[66:67], v244 offset:1568\n\t" \
    "ds_read_b64_tr_b16 v[72:73], v244 offset:64\n\t" \
    "ds_read_b64_tr_b16 v[74:75], v244 offset:1600\n\t" \
    "v_add_u32 v244, 0xf000, v243\n\t" \
    "s_waitcnt vmcnt(12) lgkmcnt(0)\n\t" \
    "s_barrier \n\t" \
    ".Lv01_%=:\n\t" \
    "v_readfirstlane_b32 s43, v246\n\t" \
    "s_nop 0\n\t" \
    "s_cmp_lg_u32 s43, -1\n\t" \
    "s_cbranch_scc1 .Lv02_%=\n\t" \
    "s_cmp_eq_u32 s41, 0\n\t" \
    "s_cbranch_scc1 .Lv02_%=\n\t" \
    "s_sub_u32 s41, s41, 1\n\t" \
    "s_sleep 4\n\t" \
    "v_mov_b32 v246, 0\n\t" \
    "buffer_load_dword v246, v246, s[60:63], %[mo] offen sc1\n\t" \
    "s_waitcnt vmcnt(0)\n\t" \
    "s_branch .Lv01_%=\n\t" \
    ".Lv02_%=:\n\t" \
    "v_mov_b32 v246, 0x8000\n\t" \
    "s_mov_b64 exec, 1\n\t" \
    "buffer_store_dword v246, v246, s[60:63], %[mo] offen sc1\n\t" \
    "s_mov_b64 exec, -1\n\t" \
    "s_and_b32 s44, s43, 0x5000\n\t" \
    "s_xor_b32 %[mb], %[mb], s44\n\t" \
    "s_xor_b32 %[fl], %[fl], s44\n\t" \
    "s_and_b32 s43, s43, 0x2000\n\t" \
    "s_xor_b32 s43, s43, %[lx]\n\t" \
    "s_add_u32 s60, s60, s43\n\t" \
    "s_addc_u32 s61, s61, 0\n\t" \
    "v_and_b32 v252, 0xf0, v253\n\t" \
    "v_lshlrev_b32 v252, 3, v252\n\t" \
    "buffer_load_dword v252, v252, s[60:63], %[fl] offen sc1\n\t" \
    "s_add_u32 m0, %[wm], 0xc000\n\t" \
    "s_mov_b32 s44, 0x6c0000\n\t" \
    "buffer_load_dwordx4 v240, %[wr], s44 offen offset:0 nt lds\n\t" \
    "buffer_load_dwordx4 v241, %[wr], s44 offen offset:1024 nt lds\n\t" \
    "buffer_load_dwordx4 v242, %[wr], s44 offen offset:2048 nt lds\n\t" \
    "ds_read_b64_tr_b16 v[60:61], v244 offset:0\n\t" \
    "ds_read_b64_tr_b16 v[62:63], v244 offset:1536\n\t" \
    "ds_read_b64_tr_b16 v[68:69], v244 offset:32\n\t" \
    "ds_read_b64_tr_b16 v[70:71], v244 offset:1568\n\t" \
    "ds_read_b64_tr_b16 v[76:77], v244 offset:64\n\t" \
    "ds_read_b64_tr_b16 v[78:79], v244 offset:1600\n\t" \
    "v_add_u32 v244, 0x0, v243\n\t" \
    "s_waitcnt vmcnt(12) lgkmcnt(0)\n\t" \
    "s_barrier \n\t" \
    "s_add_u32 m0, %[wm], 0xf000\n\t" \
    "s_mov_b32 s44, 0x720000\n\t" \
    "buffer_load_dwordx4 v240, %[wr], s44 offen offset:0 nt lds\n\t" \
    "buffer_load_dwordx4 v241, %[wr], s44 offen offset:1024 nt lds\n\t" \
    "buffer_load_dwordx4 v242, %[wr], s44 offen offset:2048 nt lds\n\t" \
    "ds_read_b64_tr_b16 v[96:97], v244 offset:0\n\t" \
    "ds_read_b64_tr_b16 v[98:99], v244 offset:1536\n\t" \
    "ds_read_b64_tr_b16 v[104:105], v244 offset:32\n\t" \
    "ds_read_b64_tr_b16 v[106:107], v244 offset:1568\n\t" \
    "ds_read_b64_tr_b16 v[112:113], v244 offset:64\n\t" \
    "ds_read_b64_tr_b16 v[114:115], v244 offset:1600\n\t" \
    "v_add_u32 v244, 0x3000, v243\n\t" \
    "s_waitcnt vmcnt(12) lgkmcnt(0)\n\t" \
    "s_barrier \n\t" \
    "ds_read_b64_tr_b16 v[100:101], v244 offset:0\n\t" \
    "ds_read_b64_tr_b16 v[102:103], v244 offset:1536\n\t" \
    "ds_read_b64_tr_b16 v[108:109], v244 offset:32\n\t" \
    "ds_read_b64_tr_b16 v[110:111], v244 offset:1568\n\t" \
    "ds_read_b64_tr_b16 v[116:117], v244 offset:64\n\t" \
    "ds_read_b64_tr_b16 v[118:119], v244 offset:1600\n\t" \
    "v_add_u32 v244, 0x6000, v243\n\t" \
    "s_waitcnt vmcnt(9) lgkmcnt(0)\n\t" \
    "s_barrier \n\t" \
    "ds_read_b64_tr_b16 v[136:137], v244 offset:0\n\t" \
    "ds_read_b64_tr_b16 v[138:139], v244 offset:1536\n\t" \
    "ds_read_b64_tr_b16 v[144:145], v244 offset:32\n\t" \
    "ds_read_b64_tr_b16 v[146:147], v244 offset:1568\n\t" \
    "ds_read_b64_tr_b16 v[152:153], v244 offset:64\n\t" \
    "ds_read_b64_tr_b16 v[154:155], v244 offset:1600\n\t" \
    "v_add_u32 v244, 0x9000, v243\n\t" \
    "s_waitcnt vmcnt(6) lgkmcnt(0)\n\t" \
    "s_barrier \n\t" \
    "ds_read_b64_tr_b16 v[140:141], v244 offset:0\n\t" \
    "ds_read_b64_tr_b16 v[142:143], v244 offset:1536\n\t" \
    "ds_read_b64_tr_b16 v[148:149], v244 offset:32\n\t" \
    "ds_read_b64_tr_b16 v[150:151], v244 offset:1568\n\t" \
    "ds_read_b64_tr_b16 v[156:157], v244 offset:64\n\t" \
    "ds_read_b64_tr_b16 v[158:159], v244 offset:1600\n\t" \
    "v_add_u32 v244, 0xc000, v243\n\t" \
    "s_waitcnt vmcnt(3) lgkmcnt(0)\n\t" \
    "s_barrier \n\t" \
    "ds_read_b64_tr_b16 v[176:177], v244 offset:0\n\t" \
    "ds_read_b64_tr_b16 v[178:179], v244 offset:1536\n\t" \
    "ds_read_b64_tr_b16 v[184:185], v244 offset:32\n\t" \
    "ds_read_b64_tr_b16 v[186:187], v244 offset:1568\n\t" \
    "ds_read_b64_tr_b16 v[192:193], v244 offset:64\n\t" \
    "ds_read_b64_tr_b16 v[194:195], v244 offset:1600\n\t" \
    "s_mov_b32 s43, 0\n\t" \
    "v_cmp_eq_u32 vcc, 0, v252\n\t" \
    "s_cbranch_vccnz .Lv03_%=\n\t" \
    "s_mov_b32 m0, 0x0\n\t" \
    "s_add_u32 s49, s42, 0x0\n\t" \
    "buffer_load_dwordx4 v253, s[60:63], s49 offen offset:0 sc1 lds\n\t" \
    "buffer_load_dwordx4 v253, s[60:63], s49 offen offset:1024 sc1 lds\n\t" \
    "buffer_load_dwordx4 v253, s[60:63], s49 offen offset:2048 sc1 lds\n\t" \
    "buffer_load_dwordx4 v253, s[60:63], s49 offen offset:3072 sc1 lds\n\t" \
    "s_mov_b32 m0, 0x4000\n\t" \
    "s_add_u32 s49, s42, 0x10000\n\t" \
    "buffer_load_dwordx4 v253, s[60:63], s49 offen offset:0 sc1 lds\n\t" \
    "s_mov_b32 m0, 0x5000\n\t" \
    "s_add_u32 s49, s42, 0x1000\n\t" \
    "buffer_load_dwordx4 v253, s[60:63], s49 offen offset:0 sc1 lds\n\t" \
    "buffer_load_dwordx4 v253, s[60:63], s49 offen offset:1024 sc1 lds\n\t" \
    "buffer_load_dwordx4 v253, s[60:63], s49 offen offset:2048 sc1 lds\n\t" \
    "buffer_load_dwordx4 v253, s[60:63], s49 offen offset:3072 sc1 lds\n\t" \
    "s_mov_b32 m0, 0x9000\n\t" \
    "s_add_u32 s49, s42, 0x11000\n\t" \
    "buffer_load_dwordx4 v253, s[60:63], s49 offen offset:0 sc1 lds\n\t" \
    "s_mov_b32 s43, 1\n\t" \
    ".Lv03_%=:\n\t" \
    "v_add_u32 v244, 0xf000, v243\n\t" \
    "s_waitcnt vmcnt(0) lgkmcnt(0)\n\t" \
    "s_barrier \n\t" \
    "ds_read_b64_tr_b16 v[180:181], v244 offset:0\n\t" \
    "ds_read_b64_tr_b16 v[182:183], v244 offset:1536\n\t" \
    "ds_read_b64_tr_b16 v[188:189], v244 offset:32\n\t" \
    "ds_read_b64_tr_b16 v[190:191], v244 offset:1568\n\t" \
    "ds_read_b64_tr_b16 v[196:197], v244 offset:64\n\t" \
    "ds_read_b64_tr_b16 v[198:199], v244 offset:1600\n\t" \
    "s_waitcnt lgkmcnt(0)\n\t" \
    "s_barrier \n\t" \
    "ds_write_b128 v254, v[200:203] offset:30720\n\t" \
    "ds_write_b128 v254, v[204:207] offset:31744\n\t" \
    "ds_write_b128 v254, v[208:211] offset:32768\n\t" \
    "ds_write_b128 v254, v[212:215] offset:33792\n\t" \
    "ds_write_b128 v254, v[216:219] offset:34816\n\t" \
    "ds_write_b128 v254, v[220:223] offset:35840\n\t" \
    "ds_write_b128 v254, v[224:227] offset:36864\n\t" \
    "ds_write_b128 v254, v[228:231] offset:37888\n\t" \
    "ds_write_b128 v254, v[232:235] offset:38912\n\t" \
    "ds_write_b128 v254, v[236:239] offset:39936\n\t" \
    "s_cmp_lg_u32 s43, 0\n\t" \
    "s_cbranch_scc1 .Lv04_%=\n\t" \
    "s_mov_b32 s44, 0\n\t" \
    ".Lv05_%=:\n\t" \
    "v_cmp_ge_u32 vcc, s44, v252\n\t" \
    "s_cbranch_vccz .Lv06_%=\n\t" \
    "s_cmp_eq_u32 s41, 0\n\t" \
    "s_cbranch_scc1 .Lv06_%=\n\t" \
    "s_sub_u32 s41, s41, 1\n\t" \
    "s_sleep 8\n\t" \
    "v_and_b32 v252, 0xf0, v253\n\t" \
    "v_lshlrev_b32 v252, 3, v252\n\t" \
    "buffer_load_dword v252, v252, s[60:63], %[fl] offen sc1\n\t" \
    "s_waitcnt vmcnt(0)\n\t" \
    "s_branch .Lv05_%=\n\t" \
    ".Lv06_%=:\n\t" \
    "s_mov_b32 m0, 0x0\n\t" \
    "s_add_u32 s49, s42, 0x0\n\t" \
    "buffer_load_dwordx4 v253, s[60:63], s49 offen offset:0 sc1 lds\n\t" \
    "buffer_load_dwordx4 v253, s[60:63], s49 offen offset:1024 sc1 lds\n\t" \
    "buffer_load_dwordx4 v253, s[60:63], s49 offen offset:2048 sc1 lds\n\t" \
    "buffer_load_dwordx4 v253, s[60:63], s49 offen offset:3072 sc1 lds\n\t" \
    "s_mov_b32 m0, 0x4000\n\t" \
    "s_add_u32 s49, s42, 0x10000\n\t" \
    "buffer_load_dwordx4 v253, s[60:63], s49 offen offset:0 sc1 lds\n\t" \
    "s_mov_b32 m0, 0x5000\n\t" \
    "s_add_u32 s49, s42, 0x1000\n\t" \
    "buffer_load_dwordx4 v253, s[60:63], s49 offen offset:0 sc1 lds\n\t" \
    "buffer_load_dwordx4 v253, s[60:63], s49 offen offset:1024 sc1 lds\n\t" \
    "buffer_load_dwordx4 v253, s[60:63], s49 offen offset:2048 sc1 lds\n\t" \
    "buffer_load_dwordx4 v253, s[60:63], s49 offen offset:3072 sc1 lds\n\t" \
    "s_mov_b32 m0, 0x9000\n\t" \
    "s_add_u32 s49, s42, 0x11000\n\t" \
    "buffer_load_dwordx4 v253, s[60:63], s49 offen offset:0 sc1 lds\n\t" \
    ".Lv04_%=:\n\t" \
    "s_waitcnt vmcnt(0) lgkmcnt(0)\n\t" \
    "s_barrier \n\t" \
    "ds_read_b128 v[224:227], v253 offset:0\n\t" \
    "ds_read_b128 v[244:247], v254 offset:30720\n\t" \
    "ds_read_b128 v[228:231], v253 offset:1024\n\t" \
    "ds_read_b128 v[232:235], v253 offset:20480\n\t" \
    "ds_read_b128 v[236:239], v253 offset:21504\n\t" \
    "ds_read_b128 v[248:251], v254 offset:31744\n\t" \
    ".p2align 8\n\t" \
    ".Lv07_%=:\n\t" \
    "s_waitcnt lgkmcnt(5)\n\t" \
    "v_mfma_f32_16x16x32_bf16 v[204:207], v[0:3], v[224:227], 0\n\t" \
    "s_waitcnt lgkmcnt(3)\n\t" \
    "v_mfma_f32_16x16x32_bf16 v[204:207], v[4:7], v[228:231], v[204:207]\n\t" \
    "ds_read_b128 v[240:243], v253 offset:10240\n\t" \
    "v_mfma_f32_16x16x32_bf16 v[208:211], v[8:11], v[224:227], 0\n\t" \
    "v_mfma_f32_16x16x32_bf16 v[208:211], v[12:15], v[228:231], v[208:211]\n\t" \
    "v_mfma_f32_16x16x32_bf16 v[200:203], v[244:247], v[224:227], 0\n\t" \
    "s_waitcnt lgkmcnt(1)\n\t" \
    "v_mfma_f32_16x16x32_bf16 v[200:203], v[248:251], v[228:231], v[200:203]\n\t" \
    "v_mfma_f32_16x16x32_bf16 v[216:219], v[0:3], v[232:235], 0\n\t" \
    "ds_read_b128 v[224:227], v253 offset:11264\n\t" \
    "v_mfma_f32_16x16x32_bf16 v[216:219], v[4:7], v[236:239], v[216:219]\n\t" \
    "ds_read_b128 v[228:231], v253 offset:2048\n\t" \
    "v_mfma_f32_16x16x32_bf16 v[220:223], v[8:11], v[232:235], 0\n\t" \
    "v_mfma_f32_16x16x32_bf16 v[220:223], v[12:15], v[236:239], v[220:223]\n\t" \
    "v_mfma_f32_16x16x32_bf16 v[212:215], v[244:247], v[232:235], 0\n\t" \
    "ds_read_b128 v[244:247], v254 offset:32768\n\t" \
    "v_mfma_f32_16x16x32_bf16 v[212:215], v[248:251], v[236:239], v[212:215]\n\t" \
    "ds_read_b128 v[248:251], v254 offset:33792\n\t" \
    "s_waitcnt lgkmcnt(4)\n\t" \
    "v_mfma_f32_16x16x32_bf16 v[204:207], v[24:27], v[240:243], v[204:207]\n\t" \
    "ds_read_b128 v[232:235], v253 offset:3072\n\t" \
    "s_waitcnt lgkmcnt(4)\n\t" \
    "v_mfma_f32_16x16x32_bf16 v[204:207], v[28:31], v[224:227], v[204:207]\n\t" \
    "ds_read_b128 v[236:239], v253 offset:22528\n\t" \
    "v_mfma_f32_16x16x32_bf16 v[208:211], v[32:35], v[240:243], v[208:211]\n\t" \
    "v_mfma_f32_16x16x32_bf16 v[208:211], v[36:39], v[224:227], v[208:211]\n\t" \
    "v_mfma_f32_16x16x32_bf16 v[200:203], v[16:19], v[240:243], v[200:203]\n\t" \
    "v_mfma_f32_16x16x32_bf16 v[200:203], v[20:23], v[224:227], v[200:203]\n\t" \
    "s_waitcnt lgkmcnt(4)\n\t" \
    "v_mfma_f32_16x16x32_bf16 v[204:207], v[40:43], v[228:231], v[204:207]\n\t" \
    "ds_read_b128 v[240:243], v253 offset:23552\n\t" \
    "s_waitcnt lgkmcnt(2)\n\t" \
    "v_mfma_f32_16x16x32_bf16 v[204:207], v[44:47], v[232:235], v[204:207]\n\t" \
    "ds_read_b128 v[224:227], v253 offset:12288\n\t" \
    "v_mfma_f32_16x16x32_bf16 v[208:211], v[48:51], v[228:231], v[208:211]\n\t" \
    "v_mfma_f32_16x16x32_bf16 v[208:211], v[52:55], v[232:235], v[208:211]\n\t" \
    "v_mfma_f32_16x16x32_bf16 v[200:203], v[244:247], v[228:231], v[200:203]\n\t" \
    "v_mfma_f32_16x16x32_bf16 v[200:203], v[248:251], v[232:235], v[200:203]\n\t" \
    "s_waitcnt lgkmcnt(2)\n\t" \
    "v_mfma_f32_16x16x32_bf16 v[216:219], v[40:43], v[236:239], v[216:219]\n\t" \
    "ds_read_b128 v[228:231], v253 offset:13312\n\t" \
    "s_add_u32 s44, s40, 1\n\t" \
    "s_waitcnt lgkmcnt(2)\n\t" \
    "v_mfma_f32_16x16x32_bf16 v[216:219], v[44:47], v[240:243], v[216:219]\n\t" \
    "ds_read_b128 v[232:235], v253 offset:4096\n\t" \
    "s_and_b32 s45, s44, 15\n\t" \
    "v_mfma_f32_16x16x32_bf16 v[220:223], v[48:51], v[236:239], v[220:223]\n\t" \
    "s_cmp_eq_u32 s45, 0\n\t" \
    "v_mfma_f32_16x16x32_bf16 v[220:223], v[52:55], v[240:243], v[220:223]\n\t" \
    "s_cselect_b32 s45, %[cj], %[cs]\n\t" \
    "v_mfma_f32_16x16x32_bf16 v[212:215], v[244:247], v[236:239], v[212:215]\n\t" \
    "ds_read_b128 v[244:247], v254 offset:34816\n\t" \
    "s_cmp_eq_u32 s44, 64\n\t" \
    "v_mfma_f32_16x16x32_bf16 v[212:215], v[248:251], v[240:243], v[212:215]\n\t" \
    "ds_read_b128 v[248:251], v254 offset:35840\n\t" \
    "s_cselect_b32 s45, 0, s45\n\t" \
    "s_waitcnt lgkmcnt(4)\n\t" \
    "v_mfma_f32_16x16x32_bf16 v[204:207], v[64:67], v[224:227], v[204:207]\n\t" \
    "ds_read_b128 v[236:239], v253 offset:5120\n\t" \
    "s_add_u32 s42, s42, s45\n\t" \
    "s_waitcnt lgkmcnt(4)\n\t" \
    "v_mfma_f32_16x16x32_bf16 v[204:207], v[68:71], v[228:231], v[204:207]\n\t" \
    "ds_read_b128 v[240:243], v253 offset:24576\n\t" \
    "v_mfma_f32_16x16x32_bf16 v[208:211], v[72:75], v[224:227], v[208:211]\n\t" \
    "v_mfma_f32_16x16x32_bf16 v[208:211], v[76:79], v[228:231], v[208:211]\n\t" \
    "v_mfma_f32_16x16x32_bf16 v[200:203], v[56:59], v[224:227], v[200:203]\n\t" \
    "v_mfma_f32_16x16x32_bf16 v[200:203], v[60:63], v[228:231], v[200:203]\n\t" \
    "s_waitcnt lgkmcnt(4)\n\t" \
    "v_mfma_f32_16x16x32_bf16 v[204:207], v[80:83], v[232:235], v[204:207]\n\t" \
    "ds_read_b128 v[224:227], v253 offset:25600\n\t" \
    "s_waitcnt lgkmcnt(2)\n\t" \
    "v_mfma_f32_16x16x32_bf16 v[204:207], v[84:87], v[236:239], v[204:207]\n\t" \
    "ds_read_b128 v[228:231], v253 offset:14336\n\t" \
    "v_mfma_f32_16x16x32_bf16 v[208:211], v[88:91], v[232:235], v[208:211]\n\t" \
    "v_mfma_f32_16x16x32_bf16 v[208:211], v[92:95], v[236:239], v[208:211]\n\t" \
    "v_mfma_f32_16x16x32_bf16 v[200:203], v[244:247], v[232:235], v[200:203]\n\t" \
    "v_mfma_f32_16x16x32_bf16 v[200:203], v[248:251], v[236:239], v[200:203]\n\t" \
    "s_waitcnt lgkmcnt(2)\n\t" \
    "v_mfma_f32_16x16x32_bf16 v[216:219], v[80:83], v[240:243], v[216:219]\n\t" \
    "ds_read_b128 v[232:235], v253 offset:15360\n\t" \
    "s_and_b32 s44, s40, 3\n\t" \
    "s_waitcnt lgkmcnt(2)\n\t" \
    "v_mfma_f32_16x16x32_bf16 v[216:219], v[84:87], v[224:227], v[216:219]\n\t" \
    "ds_read_b128 v[236:239], v253 offset:6144\n\t" \
    "s_lshl_b32 s44, s44, 22\n\t" \
    "v_mfma_f32_16x16x32_bf16 v[220:223], v[88:91], v[240:243], v[220:223]\n\t" \
    "s_add_u32 s44, s44, %[mb]\n\t" \
    "v_mfma_f32_16x16x32_bf16 v[220:223], v[92:95], v[224:227], v[220:223]\n\t" \
    "s_add_u32 s46, s44, 0x0\n\t" \
    "v_mfma_f32_16x16x32_bf16 v[212:215], v[244:247], v[240:243], v[212:215]\n\t" \
    "ds_read_b128 v[244:247], v254 offset:36864\n\t" \
    "s_add_u32 s47, s44, 0x8400\n\t" \
    "v_mfma_f32_16x16x32_bf16 v[212:215], v[248:251], v[224:227], v[212:215]\n\t" \
    "ds_read_b128 v[248:251], v254 offset:37888\n\t" \
    "s_add_u32 s48, s44, 0x400\n\t" \
    "s_waitcnt lgkmcnt(4)\n\t" \
    "v_mfma_f32_16x16x32_bf16 v[204:207], v[104:107], v[228:231], v[204:207]\n\t" \
    "ds_read_b128 v[240:243], v253 offset:7168\n\t" \
    "s_waitcnt lgkmcnt(4)\n\t" \
    "v_mfma_f32_16x16x32_bf16 v[204:207], v[108:111], v[232:235], v[204:207]\n\t" \
    "ds_read_b128 v[224:227], v253 offset:26624\n\t" \
    "v_mfma_f32_16x16x32_bf16 v[208:211], v[112:115], v[228:231], v[208:211]\n\t" \
    "v_mfma_f32_16x16x32_bf16 v[208:211], v[116:119], v[232:235], v[208:211]\n\t" \
    "v_mfma_f32_16x16x32_bf16 v[200:203], v[96:99], v[228:231], v[200:203]\n\t" \
    "v_mfma_f32_16x16x32_bf16 v[200:203], v[100:103], v[232:235], v[200:203]\n\t" \
    "s_waitcnt lgkmcnt(4)\n\t" \
    "v_mfma_f32_16x16x32_bf16 v[204:207], v[120:123], v[236:239], v[204:207]\n\t" \
    "ds_read_b128 v[228:231], v253 offset:27648\n\t" \
    "s_waitcnt lgkmcnt(2)\n\t" \
    "v_mfma_f32_16x16x32_bf16 v[204:207], v[124:127], v[240:243], v[204:207]\n\t" \
    "ds_read_b128 v[232:235], v253 offset:16384\n\t" \
    "v_mfma_f32_16x16x32_bf16 v[208:211], v[128:131], v[236:239], v[208:211]\n\t" \
    "v_mfma_f32_16x16x32_bf16 v[208:211], v[132:135], v[240:243], v[208:211]\n\t" \
    "v_mfma_f32_16x16x32_bf16 v[200:203], v[244:247], v[236:239], v[200:203]\n\t" \
    "v_mfma_f32_16x16x32_bf16 v[200:203], v[248:251], v[240:243], v[200:203]\n\t" \
    "s_waitcnt lgkmcnt(2)\n\t" \
    "v_mfma_f32_16x16x32_bf16 v[216:219], v[120:123], v[224:227], v[216:219]\n\t" \
    "ds_read_b128 v[236:239], v253 offset:17408\n\t" \
    "s_waitcnt lgkmcnt(2)\n\t" \
    "v_mfma_f32_16x16x32_bf16 v[216:219], v[124:127], v[228:231], v[216:219]\n\t" \
    "ds_read_b128 v[240:243], v253 offset:8192\n\t" \
    "v_mfma_f32_16x16x32_bf16 v[220:223], v[128:131], v[224:227], v[220:223]\n\t" \
    "v_mfma_f32_16x16x32_bf16 v[220:223], v[132:135], v[228:231], v[220:223]\n\t" \
    "v_mfma_f32_16x16x32_bf16 v[212:215], v[244:247], v[224:227], v[212:215]\n\t" \
    "ds_read_b128 v[244:247], v254 offset:38912\n\t" \
    "v_mfma_f32_16x16x32_bf16 v[212:215], v[248:251], v[228:231], v[212:215]\n\t" \
    "ds_read_b128 v[248:251], v254 offset:39936\n\t" \
    "v_and_b32 v252, 0xf0, v253\n\t" \
    "v_lshlrev_b32 v252, 3, v252\n\t" \
    "buffer_load_dword v252, v252, s[60:63], %[fl] offen sc1\n\t" \
    "s_waitcnt lgkmcnt(4)\n\t" \
    "v_mfma_f32_16x16x32_bf16 v[204:207], v[144:147], v[232:235], v[204:207]\n\t" \
    "ds_read_b128 v[224:227], v253 offset:9216\n\t" \
    "s_waitcnt lgkmcnt(4)\n\t" \
    "v_mfma_f32_16x16x32_bf16 v[204:207], v[148:151], v[236:239], v[204:207]\n\t" \
    "ds_read_b128 v[228:231], v253 offset:28672\n\t" \
    "v_mfma_f32_16x16x32_bf16 v[208:211], v[152:155], v[232:235], v[208:211]\n\t" \
    "v_mfma_f32_16x16x32_bf16 v[208:211], v[156:159], v[236:239], v[208:211]\n\t" \
    "v_mfma_f32_16x16x32_bf16 v[200:203], v[136:139], v[232:235], v[200:203]\n\t" \
    "v_mfma_f32_16x16x32_bf16 v[200:203], v[140:143], v[236:239], v[200:203]\n\t" \
    "s_waitcnt lgkmcnt(4)\n\t" \
    "v_mfma_f32_16x16x32_bf16 v[204:207], v[160:163], v[240:243], v[204:207]\n\t" \
    "ds_read_b128 v[232:235], v253 offset:29696\n\t" \
    "s_waitcnt lgkmcnt(2)\n\t" \
    "v_mfma_f32_16x16x32_bf16 v[204:207], v[164:167], v[224:227], v[204:207]\n\t" \
    "ds_read_b128 v[236:239], v253 offset:18432\n\t" \
    "v_mfma_f32_16x16x32_bf16 v[208:211], v[168:171], v[240:243], v[208:211]\n\t" \
    "v_mfma_f32_16x16x32_bf16 v[208:211], v[172:175], v[224:227], v[208:211]\n\t" \
    "v_mfma_f32_16x16x32_bf16 v[200:203], v[244:247], v[240:243], v[200:203]\n\t" \
    "v_mfma_f32_16x16x32_bf16 v[200:203], v[248:251], v[224:227], v[200:203]\n\t" \
    "s_waitcnt lgkmcnt(2)\n\t" \
    "v_mfma_f32_16x16x32_bf16 v[216:219], v[160:163], v[228:231], v[216:219]\n\t" \
    "ds_read_b128 v[240:243], v253 offset:19456\n\t" \
    "s_waitcnt lgkmcnt(2)\n\t" \
    "v_mfma_f32_16x16x32_bf16 v[216:219], v[164:167], v[232:235], v[216:219]\n\t" \
    "ds_read_b128 v[224:227], v253 offset:30720\n\t" \
    "v_mfma_f32_16x16x32_bf16 v[220:223], v[168:171], v[228:231], v[220:223]\n\t" \
    "v_mfma_f32_16x16x32_bf16 v[220:223], v[172:175], v[232:235], v[220:223]\n\t" \
    "v_mfma_f32_16x16x32_bf16 v[212:215], v[244:247], v[228:231], v[212:215]\n\t" \
    "v_mfma_f32_16x16x32_bf16 v[212:215], v[248:251], v[232:235], v[212:215]\n\t" \
    "s_waitcnt lgkmcnt(2)\n\t" \
    "v_mfma_f32_16x16x32_bf16 v[204:207], v[184:187], v[236:239], v[204:207]\n\t" \
    "ds_read_b128 v[228:231], v253 offset:31744\n\t" \
    "s_waitcnt lgkmcnt(2)\n\t" \
    "v_mfma_f32_16x16x32_bf16 v[204:207], v[188:191], v[240:243], v[204:207]\n\t" \
    "ds_read_b128 v[232:235], v253 offset:32768\n\t" \
    "v_mfma_f32_16x16x32_bf16 v[208:211], v[192:195], v[236:239], v[208:211]\n\t" \
    "v_mfma_f32_16x16x32_bf16 v[208:211], v[196:199], v[240:243], v[208:211]\n\t" \
    "v_mfma_f32_16x16x32_bf16 v[200:203], v[176:179], v[236:239], v[200:203]\n\t" \
    "v_mfma_f32_16x16x32_bf16 v[200:203], v[180:183], v[240:243], v[200:203]\n\t" \
    "s_waitcnt lgkmcnt(2)\n\t" \
    "v_mfma_f32_16x16x32_bf16 v[216:219], v[24:27], v[224:227], v[216:219]\n\t" \
    "ds_read_b128 v[236:239], v253 offset:33792\n\t" \
    "s_waitcnt lgkmcnt(2)\n\t" \
    "v_mfma_f32_16x16x32_bf16 v[216:219], v[28:31], v[228:231], v[216:219]\n\t" \
    "ds_read_b128 v[240:243], v253 offset:34816\n\t" \
    "v_mfma_f32_16x16x32_bf16 v[220:223], v[32:35], v[224:227], v[220:223]\n\t" \
    "v_cvt_pk_bf16_f32 v204, v204, v205\n\t" \
    "v_mfma_f32_16x16x32_bf16 v[220:223], v[36:39], v[228:231], v[220:223]\n\t" \
    "v_cvt_pk_bf16_f32 v205, v206, v207\n\t" \
    "v_mfma_f32_16x16x32_bf16 v[212:215], v[16:19], v[224:227], v[212:215]\n\t" \
    "ds_write_b64 v254, v[204:205] offset:7168\n\t" \
    "v_mfma_f32_16x16x32_bf16 v[212:215], v[20:23], v[228:231], v[212:215]\n\t" \
    "v_cvt_pk_bf16_f32 v208, v208, v209\n\t" \
    "s_waitcnt lgkmcnt(3)\n\t" \
    "v_mfma_f32_16x16x32_bf16 v[216:219], v[64:67], v[232:235], v[216:219]\n\t" \
    "ds_read_b128 v[224:227], v253 offset:35840\n\t" \
    "v_cvt_pk_bf16_f32 v209, v210, v211\n\t" \
    "s_waitcnt lgkmcnt(3)\n\t" \
    "v_mfma_f32_16x16x32_bf16 v[216:219], v[68:71], v[236:239], v[216:219]\n\t" \
    "ds_read_b128 v[228:231], v253 offset:36864\n\t" \
    "ds_write_b64 v254, v[208:209] offset:8192\n\t" \
    "v_mfma_f32_16x16x32_bf16 v[220:223], v[72:75], v[232:235], v[220:223]\n\t" \
    "v_cvt_pk_bf16_f32 v200, v200, v201\n\t" \
    "v_mfma_f32_16x16x32_bf16 v[220:223], v[76:79], v[236:239], v[220:223]\n\t" \
    "v_cvt_pk_bf16_f32 v201, v202, v203\n\t" \
    "v_mfma_f32_16x16x32_bf16 v[212:215], v[56:59], v[232:235], v[212:215]\n\t" \
    "ds_write_b64 v254, v[200:201] offset:6144\n\t" \
    "v_mfma_f32_16x16x32_bf16 v[212:215], v[60:63], v[236:239], v[212:215]\n\t" \
    "s_waitcnt lgkmcnt(5)\n\t" \
    "v_mfma_f32_16x16x32_bf16 v[216:219], v[104:107], v[240:243], v[216:219]\n\t" \
    "ds_read_b128 v[232:235], v253 offset:37888\n\t" \
    "s_waitcnt lgkmcnt(4)\n\t" \
    "v_mfma_f32_16x16x32_bf16 v[216:219], v[108:111], v[224:227], v[216:219]\n\t" \
    "ds_read_b128 v[236:239], v253 offset:38912\n\t" \
    "v_mfma_f32_16x16x32_bf16 v[220:223], v[112:115], v[240:243], v[220:223]\n\t" \
    "v_mfma_f32_16x16x32_bf16 v[220:223], v[116:119], v[224:227], v[220:223]\n\t" \
    "v_mfma_f32_16x16x32_bf16 v[212:215], v[96:99], v[240:243], v[212:215]\n\t" \
    "v_mfma_f32_16x16x32_bf16 v[212:215], v[100:103], v[224:227], v[212:215]\n\t" \
    "ds_read_b128 v[240:243], v253 offset:39936\n\t" \
    "s_waitcnt lgkmcnt(3)\n\t" \
    "s_barrier \n\t" \
    "v_mfma_f32_16x16x32_bf16 v[216:219], v[144:147], v[228:231], v[216:219]\n\t" \
    "s_waitcnt lgkmcnt(2)\n\t" \
    "v_mfma_f32_16x16x32_bf16 v[216:219], v[148:151], v[232:235], v[216:219]\n\t" \
    "s_waitcnt lgkmcnt(1)\n\t" \
    "v_mfma_f32_16x16x32_bf16 v[216:219], v[184:187], v[236:239], v[216:219]\n\t" \
    "s_waitcnt lgkmcnt(0)\n\t" \
    "v_mfma_f32_16x16x32_bf16 v[216:219], v[188:191], v[240:243], v[216:219]\n\t" \
    "v_mfma_f32_16x16x32_bf16 v[220:223], v[152:155], v[228:231], v[220:223]\n\t" \
    "v_mfma_f32_16x16x32_bf16 v[220:223], v[156:159], v[232:235], v[220:223]\n\t" \
    "v_mfma_f32_16x16x32_bf16 v[220:223], v[192:195], v[236:239], v[220:223]\n\t" \
    "v_cvt_pk_bf16_f32 v216, v216, v217\n\t" \
    "v_cvt_pk_bf16_f32 v217, v218, v219\n\t" \
    "v_mfma_f32_16x16x32_bf16 v[220:223], v[196:199], v[240:243], v[220:223]\n\t" \
    "ds_write_b64 v254, v[216:217] offset:13312\n\t" \
    "v_mfma_f32_16x16x32_bf16 v[212:215], v[136:139], v[228:231], v[212:215]\n\t" \
    "v_mfma_f32_16x16x32_bf16 v[212:215], v[140:143], v[232:235], v[212:215]\n\t" \
    "v_mfma_f32_16x16x32_bf16 v[212:215], v[176:179], v[236:239], v[212:215]\n\t" \
    "v_cvt_pk_bf16_f32 v220, v220, v221\n\t" \
    "v_cvt_pk_bf16_f32 v221, v222, v223\n\t" \
    "v_mfma_f32_16x16x32_bf16 v[212:215], v[180:183], v[240:243], v[212:215]\n\t" \
    "ds_write_b64 v254, v[220:221] offset:14336\n\t" \
    "s_nop 6\n\t" \
    "v_cvt_pk_bf16_f32 v212, v212, v213\n\t" \
    "v_cvt_pk_bf16_f32 v213, v214, v215\n\t" \
    "ds_write_b64 v254, v[212:213] offset:12288\n\t" \
    "s_waitcnt vmcnt(0) lgkmcnt(0)\n\t" \
    "s_barrier \n\t" \
    "s_setprio 2\n\t" \
    "s_add_u32 s44, s40, 1\n\t" \
    "s_min_u32 s44, s44, 63\n\t" \
    ".Lv08_%=:\n\t" \
    "v_cmp_ge_u32 vcc, s44, v252\n\t" \
    "s_cbranch_vccz .Lv09_%=\n\t" \
    "s_cmp_eq_u32 s41, 0\n\t" \
    "s_cbranch_scc1 .Lv09_%=\n\t" \
    "s_sub_u32 s41, s41, 1\n\t" \
    "s_sleep 2\n\t" \
    "v_and_b32 v252, 0xf0, v253\n\t" \
    "v_lshlrev_b32 v252, 3, v252\n\t" \
    "buffer_load_dword v252, v252, s[60:63], %[fl] offen sc1\n\t" \
    "s_waitcnt vmcnt(0)\n\t" \
    "s_branch .Lv08_%=\n\t" \
    ".Lv09_%=:\n\t" \
    "s_mov_b32 m0, 0x0\n\t" \
    "s_add_u32 s49, s42, 0x0\n\t" \
    "buffer_load_dwordx4 v253, s[60:63], s49 offen offset:0 sc1 lds\n\t" \
    "s_sleep 1\n\t" \
    "buffer_load_dwordx4 v253, s[60:63], s49 offen offset:1024 sc1 lds\n\t" \
    "s_sleep 1\n\t" \
    "buffer_load_dwordx4 v253, s[60:63], s49 offen offset:2048 sc1 lds\n\t" \
    "s_sleep 1\n\t" \
    "buffer_load_dwordx4 v253, s[60:63], s49 offen offset:3072 sc1 lds\n\t" \
    "s_sleep 1\n\t" \
    "s_mov_b32 m0, 0x4000\n\t" \
    "s_add_u32 s49, s42, 0x10000\n\t" \
    "buffer_load_dwordx4 v253, s[60:63], s49 offen offset:0 sc1 lds\n\t" \
    "s_sleep 1\n\t" \
    "s_mov_b32 m0, 0x5000\n\t" \
    "s_add_u32 s49, s42, 0x1000\n\t" \
    "buffer_load_dwordx4 v253, s[60:63], s49 offen offset:0 sc1 lds\n\t" \
    "s_sleep 1\n\t" \
    "buffer_load_dwordx4 v253, s[60:63], s49 offen offset:1024 sc1 lds\n\t" \
    "s_sleep 1\n\t" \
    "buffer_load_dwordx4 v253, s[60:63], s49 offen offset:2048 sc1 lds\n\t" \
    "s_sleep 1\n\t" \
    "buffer_load_dwordx4 v253, s[60:63], s49 offen offset:3072 sc1 lds\n\t" \
    "s_sleep 1\n\t" \
    "s_mov_b32 m0, 0x9000\n\t" \
    "s_add_u32 s49, s42, 0x11000\n\t" \
    "buffer_load_dwordx4 v253, s[60:63], s49 offen offset:0 sc1 lds\n\t" \
    "s_sleep 1\n\t" \
    "ds_read_b128 v[200:203], v254 offset:30720\n\t" \
    "ds_read_b128 v[204:207], v254 offset:31744\n\t" \
    "ds_read_b128 v[216:219], v254 offset:32768\n\t" \
    "ds_read_b128 v[220:223], v254 offset:33792\n\t" \
    "v_mov_b64 v[208:209], 0\n\t" \
    "v_mov_b64 v[210:211], 0\n\t" \
    "v_mov_b64 v[244:245], 0\n\t" \
    "v_mov_b64 v[246:247], 0\n\t" \
    "v_mov_b64 v[248:249], 0\n\t" \
    "v_mov_b64 v[250:251], 0\n\t" \
    "s_sleep 5\n\t" \
    "s_mov_b32 m0, 0xa000\n\t" \
    "s_nop 0\n\t" \
    "buffer_load_dwordx4 v253, s[60:63], s46 offen sc1 lds\n\t" \
    "s_mov_b32 m0, 0xb400\n\t" \
    "s_nop 0\n\t" \
    "buffer_load_dwordx4 v253, s[60:63], s47 offen sc1 lds\n\t" \
    "s_mov_b32 m0, 0xa400\n\t" \
    "s_nop 0\n\t" \
    "buffer_load_dwordx4 v253, s[60:63], s48 offen sc1 lds\n\t" \
    "s_waitcnt lgkmcnt(0)\n\t" \
    "v_bfe_i32 v212, v253, 5, 1\n\t" \
    "v_and_b32 v212, 0xaaaa, v212\n\t" \
    "v_or_b32 v212, 0x4444, v212\n\t" \
    "v_and_b32 v213, 0xffff, v255\n\t" \
    "v_lshrrev_b32 v214, 16, v255\n\t" \
    ".Lv010_%=:\n\t" \
    "s_waitcnt vmcnt(0)\n\t" \
    "ds_read_b128 v[224:227], v253 offset:40960\n\t" \
    "ds_read_b128 v[228:231], v253 offset:46080\n\t" \
    "ds_read_b128 v[232:235], v253 offset:41984\n\t" \
    "s_waitcnt lgkmcnt(2)\n\t" \
    "v_max_u32 v215, v224, v225\n\t" \
    "v_max3_u32 v215, v226, v227, v215\n\t" \
    "s_waitcnt lgkmcnt(1)\n\t" \
    "v_max3_u32 v215, v228, v229, v215\n\t" \
    "v_max3_u32 v215, v230, v231, v215\n\t" \
    "s_waitcnt lgkmcnt(0)\n\t" \
    "v_max3_u32 v215, v232, v233, v215\n\t" \
    "v_max3_u32 v215, v234, v235, v215\n\t" \
    "v_cmp_eq_u32 vcc, -1, v215\n\t" \
    "s_cbranch_vccz .Lv011_%=\n\t" \
    "v_max_u32 v215, v224, v225\n\t" \
    "v_max3_u32 v215, v226, v227, v215\n\t" \
    "v_cmp_eq_u32 vcc, -1, v215\n\t" \
    "s_cbranch_vccz .Lv012_%=\n\t" \
    "s_mov_b32 m0, 0xa000\n\t" \
    "s_nop 0\n\t" \
    "buffer_load_dwordx4 v253, s[60:63], s46 offen sc1 lds\n\t" \
    ".Lv012_%=:\n\t" \
    "v_max_u32 v215, v228, v229\n\t" \
    "v_max3_u32 v215, v230, v231, v215\n\t" \
    "v_cmp_eq_u32 vcc, -1, v215\n\t" \
    "s_cbranch_vccz .Lv013_%=\n\t" \
    "s_mov_b32 m0, 0xb400\n\t" \
    "s_nop 0\n\t" \
    "buffer_load_dwordx4 v253, s[60:63], s47 offen sc1 lds\n\t" \
    ".Lv013_%=:\n\t" \
    "v_max_u32 v215, v232, v233\n\t" \
    "v_max3_u32 v215, v234, v235, v215\n\t" \
    "v_cmp_eq_u32 vcc, -1, v215\n\t" \
    "s_cbranch_vccz .Lv014_%=\n\t" \
    "s_mov_b32 m0, 0xa400\n\t" \
    "s_nop 0\n\t" \
    "buffer_load_dwordx4 v253, s[60:63], s48 offen sc1 lds\n\t" \
    ".Lv014_%=:\n\t" \
    "s_cmp_eq_u32 s41, 0\n\t" \
    "s_cbranch_scc1 .Lv011_%=\n\t" \
    "s_sub_u32 s41, s41, 1\n\t" \
    "s_branch .Lv010_%=\n\t" \
    ".Lv011_%=:\n\t" \
    "s_waitcnt lgkmcnt(0)\n\t" \
    "s_barrier \n\t" \
    "ds_read_b128 v[236:239], v253 offset:47104\n\t" \
    "ds_read_b128 v[240:243], v253 offset:43008\n\t" \
    "v_smfmac_f32_16x16x64_bf16 v[244:247], v[224:227], v[0:7], v212\n\t" \
    "v_smfmac_f32_16x16x64_bf16 v[248:251], v[224:227], v[8:15], v212\n\t" \
    "v_smfmac_f32_16x16x64_bf16 v[208:211], v[224:227], v[200:207], v212\n\t" \
    "ds_read_b128 v[200:203], v254 offset:34816\n\t" \
    "ds_read_b128 v[204:207], v254 offset:35840\n\t" \
    "ds_read_b128 v[224:227], v253 offset:48128\n\t" \
    "v_smfmac_f32_16x16x64_bf16 v[208:211], v[228:231], v[16:23], v212\n\t" \
    "v_smfmac_f32_16x16x64_bf16 v[244:247], v[228:231], v[24:31], v212\n\t" \
    "v_smfmac_f32_16x16x64_bf16 v[248:251], v[228:231], v[32:39], v212\n\t" \
    "ds_read_b128 v[228:231], v253 offset:44032\n\t" \
    "v_smfmac_f32_16x16x64_bf16 v[208:211], v[232:235], v[216:223], v212\n\t" \
    "ds_read_b128 v[216:219], v254 offset:36864\n\t" \
    "ds_read_b128 v[220:223], v254 offset:37888\n\t" \
    "v_smfmac_f32_16x16x64_bf16 v[244:247], v[232:235], v[40:47], v212\n\t" \
    "v_smfmac_f32_16x16x64_bf16 v[248:251], v[232:235], v[48:55], v212\n\t" \
    "ds_read_b128 v[232:235], v253 offset:49152\n\t" \
    "s_waitcnt lgkmcnt(8)\n\t" \
    "v_smfmac_f32_16x16x64_bf16 v[208:211], v[236:239], v[56:63], v212\n\t" \
    "v_smfmac_f32_16x16x64_bf16 v[244:247], v[236:239], v[64:71], v212\n\t" \
    "v_smfmac_f32_16x16x64_bf16 v[248:251], v[236:239], v[72:79], v212\n\t" \
    "ds_read_b128 v[236:239], v253 offset:45056\n\t" \
    "s_waitcnt lgkmcnt(6)\n\t" \
    "v_smfmac_f32_16x16x64_bf16 v[208:211], v[240:243], v[200:207], v212\n\t" \
    "ds_read_b128 v[200:203], v254 offset:38912\n\t" \
    "ds_read_b128 v[204:207], v254 offset:39936\n\t" \
    "v_smfmac_f32_16x16x64_bf16 v[244:247], v[240:243], v[80:87], v212\n\t" \
    "v_smfmac_f32_16x16x64_bf16 v[248:251], v[240:243], v[88:95], v212\n\t" \
    "ds_read_b128 v[240:243], v253 offset:50176\n\t" \
    "s_waitcnt lgkmcnt(8)\n\t" \
    "v_smfmac_f32_16x16x64_bf16 v[208:211], v[224:227], v[96:103], v212\n\t" \
    "v_smfmac_f32_16x16x64_bf16 v[244:247], v[224:227], v[104:111], v212\n\t" \
    "v_smfmac_f32_16x16x64_bf16 v[248:251], v[224:227], v[112:119], v212\n\t" \
    "s_waitcnt lgkmcnt(7)\n\t" \
    "v_smfmac_f32_16x16x64_bf16 v[244:247], v[228:231], v[120:127], v212\n\t" \
    "s_waitcnt lgkmcnt(4)\n\t" \
    "v_smfmac_f32_16x16x64_bf16 v[244:247], v[232:235], v[144:151], v212\n\t" \
    "s_waitcnt lgkmcnt(3)\n\t" \
    "v_smfmac_f32_16x16x64_bf16 v[244:247], v[236:239], v[160:167], v212\n\t" \
    "s_waitcnt lgkmcnt(0)\n\t" \
    "v_smfmac_f32_16x16x64_bf16 v[244:247], v[240:243], v[184:191], v212\n\t" \
    "v_smfmac_f32_16x16x64_bf16 v[248:251], v[228:231], v[128:135], v212\n\t" \
    "v_smfmac_f32_16x16x64_bf16 v[248:251], v[232:235], v[152:159], v212\n\t" \
    "v_smfmac_f32_16x16x64_bf16 v[248:251], v[236:239], v[168:175], v212\n\t" \
    "v_add_f32 v244, v244, v246\n\t" \
    "v_add_f32 v245, v245, v247\n\t" \
    "v_cvt_pk_bf16_f32 v244, v244, v245\n\t" \
    "v_smfmac_f32_16x16x64_bf16 v[248:251], v[240:243], v[192:199], v212\n\t" \
    "ds_write_b16 v213, v244 offset:512\n\t" \
    "ds_write_b16_d16_hi v213, v244 offset:528\n\t" \
    "v_smfmac_f32_16x16x64_bf16 v[208:211], v[228:231], v[216:223], v212\n\t" \
    "v_smfmac_f32_16x16x64_bf16 v[208:211], v[232:235], v[136:143], v212\n\t" \
    "v_smfmac_f32_16x16x64_bf16 v[208:211], v[236:239], v[200:207], v212\n\t" \
    "v_add_f32 v248, v248, v250\n\t" \
    "v_add_f32 v249, v249, v251\n\t" \
    "v_cvt_pk_bf16_f32 v248, v248, v249\n\t" \
    "v_smfmac_f32_16x16x64_bf16 v[208:211], v[240:243], v[176:183], v212\n\t" \
    "ds_write_b16 v213, v248 offset:1024\n\t" \
    "ds_write_b16_d16_hi v213, v248 offset:1040\n\t" \
    "s_nop 5\n\t" \
    "v_add_f32 v208, v208, v210\n\t" \
    "v_add_f32 v209, v209, v211\n\t" \
    "v_cvt_pk_bf16_f32 v208, v208, v209\n\t" \
    "ds_write_b16 v213, v208 offset:0\n\t" \
    "ds_write_b16_d16_hi v213, v208 offset:16\n\t" \
    "s_setprio 0\n\t" \
    "s_waitcnt lgkmcnt(0)\n\t" \
    "s_barrier \n\t" \
    "ds_read_b128 v[224:227], v253 offset:0\n\t" \
    "ds_read_b128 v[244:247], v254 offset:30720\n\t" \
    "ds_read_b128 v[228:231], v253 offset:1024\n\t" \
    "ds_read_b128 v[232:235], v253 offset:20480\n\t" \
    "ds_read_b128 v[236:239], v253 offset:21504\n\t" \
    "ds_read_b128 v[248:251], v254 offset:31744\n\t" \
    "s_add_u32 s40, s40, 1\n\t" \
    "s_cmp_lg_u32 s40, 64\n\t" \
    "s_cbranch_scc1 .Lv07_%=\n\t" \
    "s_branch .Lend%=\n\t" \
    ".Lend%=:\n\t" \
    "s_waitcnt vmcnt(0) lgkmcnt(0)\n\t" \
    "s_mov_b32 %[bud], s41\n\t"
#define ASM_R_LOOP \
    "s_mov_b32 s40, 0\n\t" \
    "s_mov_b32 s41, 0x40000\n\t" \
    "v_mov_b64 v[252:253], -1\n\t" \
    "v_mov_b64 v[254:255], -1\n\t" \
    "v_mov_b32 v248, 0\n\t" \
    "s_mov_b32 s53, 0x05040100\n\t" \
    ".Lr1_%=:\n\t" \
    "s_mov_b32 s43, s40\n\t" \
    "s_and_b32 s44, s40, 3\n\t" \
    "s_lshl_b32 s42, s44, 22\n\t" \
    "s_add_u32 s42, s42, %[vx]\n\t" \
    "s_lshl_b32 s46, s44, 22\n\t" \
    "s_mov_b32 s47, s46\n\t" \
    "s_lshl_b32 s49, s44, 17\n\t" \
    "s_add_u32 s49, s49, %[px]\n\t" \
    "s_add_u32 s50, s49, 0x1000\n\t" \
    "s_add_u32 s44, s40, 1\n\t" \
    "s_and_b32 s44, s44, 3\n\t" \
    "s_lshl_b32 s48, s44, 22\n\t" \
    "s_mul_i32 s52, s40, 0x14000\n\t" \
    "s_lshr_b32 s44, s40, 4\n\t" \
    "s_mul_i32 s44, s44, 0xc2000\n\t" \
    "s_add_u32 s52, s52, s44\n\t" \
    "s_add_u32 s52, s52, %[xr]\n\t" \
    "s_add_i32 s44, s40, %[ad]\n\t" \
    "s_and_b32 s45, s44, 3\n\t" \
    "s_mul_i32 s51, s45, %[as]\n\t" \
    "s_cmp_lt_i32 s44, 0\n\t" \
    "s_cselect_b32 s55, 0x40000000, 0\n\t" \
    "s_add_u32 s44, s40, 3\n\t" \
    "s_mul_i32 s54, s44, 0x14000\n\t" \
    "s_lshr_b32 s45, s44, 4\n\t" \
    "s_mul_i32 s45, s45, 0xc2000\n\t" \
    "s_add_u32 s54, s54, s45\n\t" \
    "s_cmp_eq_u32 %[wave], 7\n\t" \
    "s_cselect_b32 s51, s54, s51\n\t" \
    "s_cselect_b32 s55, 0, s55\n\t" \
    "s_cselect_b32 s44, 60, 62\n\t" \
    "s_cmp_gt_u32 s40, s44\n\t" \
    "s_cselect_b32 s55, 0x40000000, s55\n\t" \
    "v_or_b32 v197, s55, %[ae]\n\t" \
    "v_mov_b64 v[180:181], -1\n\t" \
    "v_mov_b64 v[182:183], -1\n\t" \
    "v_mov_b64 v[184:185], -1\n\t" \
    "v_mov_b64 v[186:187], -1\n\t" \
    "v_mov_b64 v[188:189], -1\n\t" \
    "v_mov_b64 v[190:191], -1\n\t" \
    "v_mov_b64 v[192:193], -1\n\t" \
    "v_mov_b64 v[194:195], -1\n\t" \
    "s_waitcnt vmcnt(0)\n\t" \
    ".Lr2_%=:\n\t" \
    "buffer_load_dword v248, %[fo], %[rs], %[fs] offen sc1\n\t" \
    "s_waitcnt vmcnt(0)\n\t" \
    "v_cmp_ge_u32 vcc, s43, v248\n\t" \
    "s_cbranch_vccz .Lr3_%=\n\t" \
    "s_sleep 2\n\t" \
    "s_cmp_eq_u32 s41, 0\n\t" \
    "s_cbranch_scc1 .Lr3_%=\n\t" \
    "s_sub_u32 s41, s41, 1\n\t" \
    "s_branch .Lr2_%=\n\t" \
    ".Lr3_%=:\n\t" \
    "buffer_load_dwordx4 v[160:163], %[l16], %[rs], s42 offen offset:0 sc1\n\t" \
    "buffer_load_dwordx4 v[164:167], %[l16], %[rs], s42 offen offset:1024 sc1\n\t" \
    "buffer_load_dwordx4 v[168:171], %[l16], %[rs], s42 offen offset:2048 sc1\n\t" \
    "buffer_load_dwordx4 v[172:175], %[l16], %[rs], s42 offen offset:3072 sc1\n\t" \
    "s_cmp_lg_u32 s40, 63\n\t" \
    "s_cbranch_scc1 .Lr6_%=\n\t" \
    "buffer_load_dwordx4 v[180:183], %[ov], %[rs], s46 offen sc1\n\t" \
    ".Lr7_%=:\n\t" \
    "s_waitcnt vmcnt(12)\n\t" \
    "v_max_u32 v196, v180, v181\n\t" \
    "v_max3_u32 v196, v182, v183, v196\n\t" \
    "v_cmp_eq_u32 vcc, -1, v196\n\t" \
    "s_cbranch_vccz .Lr8_%=\n\t" \
    "s_nop 7\n\t" \
    "v_max_u32 v196, v180, v181\n\t" \
    "v_max3_u32 v196, v182, v183, v196\n\t" \
    "v_cmp_eq_u32 vcc, -1, v196\n\t" \
    "s_cbranch_vccz .Lr8_%=\n\t" \
    "s_nop 7\n\t" \
    "v_max_u32 v196, v180, v181\n\t" \
    "v_max3_u32 v196, v182, v183, v196\n\t" \
    "v_cmp_eq_u32 vcc, -1, v196\n\t" \
    "s_cbranch_vccz .Lr8_%=\n\t" \
    "s_nop 7\n\t" \
    "v_max_u32 v196, v180, v181\n\t" \
    "v_max3_u32 v196, v182, v183, v196\n\t" \
    "v_cmp_eq_u32 vcc, -1, v196\n\t" \
    "s_cbranch_vccz .Lr8_%=\n\t" \
    "s_nop 7\n\t" \
    "s_cmp_eq_u32 s41, 0\n\t" \
    "s_cbranch_scc1 .Lr8_%=\n\t" \
    "s_sub_u32 s41, s41, 1\n\t" \
    "v_max_u32 v196, v180, v181\n\t" \
    "v_max3_u32 v196, v182, v183, v196\n\t" \
    "v_cmp_eq_u32 vcc, -1, v196\n\t" \
    "s_cbranch_vccz .Lr9_%=\n\t" \
    "buffer_load_dwordx4 v[180:183], %[ov], %[rs], s46 offen sc1\n\t" \
    ".Lr9_%=:\n\t" \
    "s_branch .Lr7_%=\n\t" \
    ".Lr8_%=:\n\t" \
    "s_branch .Lr5_%=\n\t" \
    ".Lr6_%=:\n\t" \
    "s_sleep 9\n\t" \
    "buffer_load_dwordx4 v[184:187], %[o0], %[rs], s47 offen sc1\n\t" \
    "buffer_load_dwordx4 v[188:191], %[o1], %[rs], s47 offen sc1\n\t" \
    "buffer_load_dwordx4 v[192:195], %[o2], %[rs], s47 offen sc1\n\t" \
    "buffer_load_dwordx4 v[180:183], %[ov], %[rs], s46 offen sc1\n\t" \
    ".Lr10_%=:\n\t" \
    "s_waitcnt vmcnt(12)\n\t" \
    "v_max_u32 v196, v184, v185\n\t" \
    "v_max3_u32 v196, v186, v187, v196\n\t" \
    "v_max3_u32 v196, v188, v189, v196\n\t" \
    "v_max3_u32 v196, v190, v191, v196\n\t" \
    "v_max3_u32 v196, v192, v193, v196\n\t" \
    "v_max3_u32 v196, v194, v195, v196\n\t" \
    "v_max3_u32 v196, v180, v181, v196\n\t" \
    "v_max3_u32 v196, v182, v183, v196\n\t" \
    "v_cmp_eq_u32 vcc, -1, v196\n\t" \
    "s_cbranch_vccz .Lr11_%=\n\t" \
    "s_nop 7\n\t" \
    "v_max_u32 v196, v184, v185\n\t" \
    "v_max3_u32 v196, v186, v187, v196\n\t" \
    "v_max3_u32 v196, v188, v189, v196\n\t" \
    "v_max3_u32 v196, v190, v191, v196\n\t" \
    "v_max3_u32 v196, v192, v193, v196\n\t" \
    "v_max3_u32 v196, v194, v195, v196\n\t" \
    "v_max3_u32 v196, v180, v181, v196\n\t" \
    "v_max3_u32 v196, v182, v183, v196\n\t" \
    "v_cmp_eq_u32 vcc, -1, v196\n\t" \
    "s_cbranch_vccz .Lr11_%=\n\t" \
    "s_nop 7\n\t" \
    "v_max_u32 v196, v184, v185\n\t" \
    "v_max3_u32 v196, v186, v187, v196\n\t" \
    "v_max3_u32 v196, v188, v189, v196\n\t" \
    "v_max3_u32 v196, v190, v191, v196\n\t" \
    "v_max3_u32 v196, v192, v193, v196\n\t" \
    "v_max3_u32 v196, v194, v195, v196\n\t" \
    "v_max3_u32 v196, v180, v181, v196\n\t" \
    "v_max3_u32 v196, v182, v183, v196\n\t" \
    "v_cmp_eq_u32 vcc, -1, v196\n\t" \
    "s_cbranch_vccz .Lr11_%=\n\t" \
    "s_nop 7\n\t" \
    "v_max_u32 v196, v184, v185\n\t" \
    "v_max3_u32 v196, v186, v187, v196\n\t" \
    "v_max3_u32 v196, v188, v189, v196\n\t" \
    "v_max3_u32 v196, v190, v191, v196\n\t" \
    "v_max3_u32 v196, v192, v193, v196\n\t" \
    "v_max3_u32 v196, v194, v195, v196\n\t" \
    "v_max3_u32 v196, v180, v181, v196\n\t" \
    "v_max3_u32 v196, v182, v183, v196\n\t" \
    "v_cmp_eq_u32 vcc, -1, v196\n\t" \
    "s_cbranch_vccz .Lr11_%=\n\t" \
    "s_nop 7\n\t" \
    "v_max_u32 v196, v184, v185\n\t" \
    "v_max3_u32 v196, v186, v187, v196\n\t" \
    "v_max3_u32 v196, v188, v189, v196\n\t" \
    "v_max3_u32 v196, v190, v191, v196\n\t" \
    "v_max3_u32 v196, v192, v193, v196\n\t" \
    "v_max3_u32 v196, v194, v195, v196\n\t" \
    "v_max3_u32 v196, v180, v181, v196\n\t" \
    "v_max3_u32 v196, v182, v183, v196\n\t" \
    "v_cmp_eq_u32 vcc, -1, v196\n\t" \
    "s_cbranch_vccz .Lr11_%=\n\t" \
    "s_nop 7\n\t" \
    "v_max_u32 v196, v184, v185\n\t" \
    "v_max3_u32 v196, v186, v187, v196\n\t" \
    "v_max3_u32 v196, v188, v189, v196\n\t" \
    "v_max3_u32 v196, v190, v191, v196\n\t" \
    "v_max3_u32 v196, v192, v193, v196\n\t" \
    "v_max3_u32 v196, v194, v195, v196\n\t" \
    "v_max3_u32 v196, v180, v181, v196\n\t" \
    "v_max3_u32 v196, v182, v183, v196\n\t" \
    "v_cmp_eq_u32 vcc, -1, v196\n\t" \
    "s_cbranch_vccz .Lr11_%=\n\t" \
    "s_nop 7\n\t" \
    "v_max_u32 v196, v184, v185\n\t" \
    "v_max3_u32 v196, v186, v187, v196\n\t" \
    "v_max3_u32 v196, v188, v189, v196\n\t" \
    "v_max3_u32 v196, v190, v191, v196\n\t" \
    "v_max3_u32 v196, v192, v193, v196\n\t" \
    "v_max3_u32 v196, v194, v195, v196\n\t" \
    "v_max3_u32 v196, v180, v181, v196\n\t" \
    "v_max3_u32 v196, v182, v183, v196\n\t" \
    "v_cmp_eq_u32 vcc, -1, v196\n\t" \
    "s_cbranch_vccz .Lr11_%=\n\t" \
    "s_nop 7\n\t" \
    "v_max_u32 v196, v184, v185\n\t" \
    "v_max3_u32 v196, v186, v187, v196\n\t" \
    "v_max3_u32 v196, v188, v189, v196\n\t" \
    "v_max3_u32 v196, v190, v191, v196\n\t" \
    "v_max3_u32 v196, v192, v193, v196\n\t" \
    "v_max3_u32 v196, v194, v195, v196\n\t" \
    "v_max3_u32 v196, v180, v181, v196\n\t" \
    "v_max3_u32 v196, v182, v183, v196\n\t" \
    "v_cmp_eq_u32 vcc, -1, v196\n\t" \
    "s_cbranch_vccz .Lr11_%=\n\t" \
    "s_nop 7\n\t" \
    "s_cmp_eq_u32 s41, 0\n\t" \
    "s_cbranch_scc1 .Lr11_%=\n\t" \
    "s_sub_u32 s41, s41, 1\n\t" \
    "v_max_u32 v196, v184, v185\n\t" \
    "v_max3_u32 v196, v186, v187, v196\n\t" \
    "v_cmp_eq_u32 vcc, -1, v196\n\t" \
    "s_cbranch_vccz .Lr12_%=\n\t" \
    "buffer_load_dwordx4 v[184:187], %[o0], %[rs], s47 offen sc1\n\t" \
    ".Lr12_%=:\n\t" \
    "v_max_u32 v196, v188, v189\n\t" \
    "v_max3_u32 v196, v190, v191, v196\n\t" \
    "v_cmp_eq_u32 vcc, -1, v196\n\t" \
    "s_cbranch_vccz .Lr13_%=\n\t" \
    "buffer_load_dwordx4 v[188:191], %[o1], %[rs], s47 offen sc1\n\t" \
    ".Lr13_%=:\n\t" \
    "v_max_u32 v196, v192, v193\n\t" \
    "v_max3_u32 v196, v194, v195, v196\n\t" \
    "v_cmp_eq_u32 vcc, -1, v196\n\t" \
    "s_cbranch_vccz .Lr14_%=\n\t" \
    "buffer_load_dwordx4 v[192:195], %[o2], %[rs], s47 offen sc1\n\t" \
    ".Lr14_%=:\n\t" \
    "v_max_u32 v196, v180, v181\n\t" \
    "v_max3_u32 v196, v182, v183, v196\n\t" \
    "v_cmp_eq_u32 vcc, -1, v196\n\t" \
    "s_cbranch_vccz .Lr15_%=\n\t" \
    "buffer_load_dwordx4 v[180:183], %[ov], %[rs], s46 offen sc1\n\t" \
    ".Lr15_%=:\n\t" \
    "s_branch .Lr10_%=\n\t" \
    ".Lr11_%=:\n\t" \
    "v_mfma_f32_16x16x32_bf16 v[176:179], v[192:195], v[180:183], 0\n\t" \
    "v_smfmac_f32_16x16x64_bf16 v[176:179], v[184:187], v[160:167], %[sidx]\n\t" \
    "v_smfmac_f32_16x16x64_bf16 v[176:179], v[188:191], v[168:175], %[sidx]\n\t" \
    "s_cmp_gt_u32 %[wave], 3\n\t" \
    "s_nop 7\n\t" \
    "v_add_f32 v176, v176, v178\n\t" \
    "v_add_f32 v177, v177, v179\n\t" \
    "ds_write2_b32 %[rw], v176, v177 offset1:80\n\t" \
    "s_waitcnt lgkmcnt(0)\n\t" \
    "s_barrier \n\t" \
    "s_cbranch_scc1 .Lr4_%=\n\t" \
    "ds_read_b128 v[128:131], %[rd] offset:0\n\t" \
    "ds_read_b128 v[132:135], %[rd] offset:16\n\t" \
    "ds_read_b128 v[136:139], %[rd] offset:32\n\t" \
    "ds_read_b128 v[140:143], %[rd] offset:48\n\t" \
    "s_waitcnt lgkmcnt(2)\n\t" \
    "v_pk_add_f32 v[128:129], v[128:129], v[130:131]\n\t" \
    "v_pk_add_f32 v[132:133], v[132:133], v[134:135]\n\t" \
    "s_waitcnt lgkmcnt(0)\n\t" \
    "v_pk_add_f32 v[136:137], v[136:137], v[138:139]\n\t" \
    "v_pk_add_f32 v[140:141], v[140:141], v[142:143]\n\t" \
    "v_pk_add_f32 v[128:129], v[128:129], v[132:133]\n\t" \
    "v_pk_add_f32 v[136:137], v[136:137], v[140:141]\n\t" \
    "v_pk_add_f32 v[128:129], v[128:129], v[136:137]\n\t" \
    "v_cvt_pk_bf16_f32 v128, v128, v129\n\t" \
    "v_min_u32 v128, -2, v128\n\t" \
    "buffer_store_dword v128, %[oo], %[rs], s48 offen sc1\n\t" \
    ".Lr4_%=:\n\t" \
    "s_cmp_lt_u32 %[wave], 4\n\t" \
    "s_cbranch_scc1 .Lr17_%=\n\t" \
    "s_sleep 4\n\t" \
    ".Lr17_%=:\n\t" \
    "s_sleep 2\n\t" \
    ".Lr5_%=:\n\t" \
    "s_cmp_lt_u32 %[wave], 6\n\t" \
    "s_cbranch_scc1 .Lr16_%=\n\t" \
    "buffer_load_dwordx2 v[250:251], %[xo], %[rs], s52 offen sc1\n\t" \
    ".Lr16_%=:\n\t" \
    "buffer_load_dwordx4 v[220:223], %[l16], %[rs], s50 offen offset:1024 sc1\n\t" \
    "buffer_load_dwordx4 v[224:227], %[l16], %[rs], s50 offen offset:2048 sc1\n\t" \
    "buffer_load_dwordx4 v[228:231], %[l16], %[rs], s50 offen offset:3072 sc1\n\t" \
    "v_perm_b32 v232, v161, v160, s53\n\t" \
    "v_perm_b32 v233, v163, v162, s53\n\t" \
    "v_perm_b32 v234, v165, v164, s53\n\t" \
    "v_perm_b32 v235, v167, v166, s53\n\t" \
    "v_perm_b32 v236, v169, v168, s53\n\t" \
    "v_perm_b32 v237, v171, v170, s53\n\t" \
    "v_perm_b32 v238, v173, v172, s53\n\t" \
    "v_perm_b32 v239, v175, v174, s53\n\t" \
    "s_nop 1\n\t" \
    "v_permlane16_swap_b32 v232, v234\n\t" \
    "v_permlane16_swap_b32 v233, v235\n\t" \
    "v_permlane16_swap_b32 v236, v238\n\t" \
    "v_permlane16_swap_b32 v237, v239\n\t" \
    "s_waitcnt vmcnt(0)\n\t" \
    ".Lr18_%=:\n\t" \
    "v_max_u32 v196, v220, v221\n\t" \
    "v_max3_u32 v196, v222, v223, v196\n\t" \
    "v_max3_u32 v196, v224, v225, v196\n\t" \
    "v_max3_u32 v196, v226, v227, v196\n\t" \
    "v_max3_u32 v196, v228, v229, v196\n\t" \
    "v_max3_u32 v196, v230, v231, v196\n\t" \
    "v_cmp_eq_u32 vcc, -1, v196\n\t" \
    "s_cbranch_vccz .Lr19_%=\n\t" \
    "s_cmp_eq_u32 s41, 0\n\t" \
    "s_cbranch_scc1 .Lr19_%=\n\t" \
    "s_sub_u32 s41, s41, 1\n\t" \
    "v_max_u32 v196, v220, v221\n\t" \
    "v_max3_u32 v196, v222, v223, v196\n\t" \
    "v_cmp_eq_u32 vcc, -1, v196\n\t" \
    "s_cbranch_vccz .Lr20_%=\n\t" \
    "buffer_load_dwordx4 v[220:223], %[l16], %[rs], s50 offen offset:1024 sc1\n\t" \
    ".Lr20_%=:\n\t" \
    "v_max_u32 v196, v224, v225\n\t" \
    "v_max3_u32 v196, v226, v227, v196\n\t" \
    "v_cmp_eq_u32 vcc, -1, v196\n\t" \
    "s_cbranch_vccz .Lr21_%=\n\t" \
    "buffer_load_dwordx4 v[224:227], %[l16], %[rs], s50 offen offset:2048 sc1\n\t" \
    ".Lr21_%=:\n\t" \
    "v_max_u32 v196, v228, v229\n\t" \
    "v_max3_u32 v196, v230, v231, v196\n\t" \
    "v_cmp_eq_u32 vcc, -1, v196\n\t" \
    "s_cbranch_vccz .Lr22_%=\n\t" \
    "buffer_load_dwordx4 v[228:231], %[l16], %[rs], s50 offen offset:3072 sc1\n\t" \
    ".Lr22_%=:\n\t" \
    "s_waitcnt vmcnt(0)\n\t" \
    "s_branch .Lr18_%=\n\t" \
    ".Lr19_%=:\n\t" \
    "v_mfma_f32_16x16x32_bf16 v[240:243], v[232:235], v[220:223], 0\n\t" \
    "v_mfma_f32_16x16x32_bf16 v[240:243], v[236:239], v[224:227], v[240:243]\n\t" \
    "v_mfma_f32_16x16x32_bf16 v[240:243], v[180:183], v[228:231], v[240:243]\n\t" \
    "s_nop 7\n\t" \
    "ds_write_b128 %[pw], v[240:243]\n\t" \
    "s_waitcnt lgkmcnt(0)\n\t" \
    "s_barrier \n\t" \
    "buffer_load_dwordx4 v[200:203], %[l16], %[rs], s49 offen offset:0 sc1\n\t" \
    "buffer_load_dwordx4 v[204:207], %[l16], %[rs], s49 offen offset:1024 sc1\n\t" \
    "buffer_load_dwordx4 v[208:211], %[l16], %[rs], s49 offen offset:2048 sc1\n\t" \
    "buffer_load_dwordx4 v[212:215], %[l16], %[rs], s49 offen offset:3072 sc1\n\t" \
    "buffer_load_dwordx4 v[216:219], %[l16], %[rs], s50 offen offset:0 sc1\n\t" \
    "s_cmp_lg_u32 %[wave], 6\n\t" \
    "s_cbranch_scc1 .Lr23_%=\n\t" \
    "ds_read_b128 v[128:131], %[pr] offset:0\n\t" \
    "ds_read_b128 v[132:135], %[pr] offset:1024\n\t" \
    "ds_read_b128 v[136:139], %[pr] offset:2048\n\t" \
    "ds_read_b128 v[140:143], %[pr] offset:3072\n\t" \
    "ds_read_b128 v[144:147], %[pr] offset:4096\n\t" \
    "ds_read_b128 v[148:151], %[pr] offset:5120\n\t" \
    "ds_read_b128 v[152:155], %[pr] offset:6144\n\t" \
    "ds_read_b128 v[156:159], %[pr] offset:7168\n\t" \
    "v_lshlrev_b32 v176, 16, v250\n\t" \
    "v_and_b32 v177, 0xffff0000, v250\n\t" \
    "v_lshlrev_b32 v178, 16, v251\n\t" \
    "v_and_b32 v179, 0xffff0000, v251\n\t" \
    "s_waitcnt lgkmcnt(6)\n\t" \
    "v_pk_add_f32 v[128:129], v[128:129], v[132:133]\n\t" \
    "v_pk_add_f32 v[130:131], v[130:131], v[134:135]\n\t" \
    "s_waitcnt lgkmcnt(4)\n\t" \
    "v_pk_add_f32 v[136:137], v[136:137], v[140:141]\n\t" \
    "v_pk_add_f32 v[138:139], v[138:139], v[142:143]\n\t" \
    "v_pk_add_f32 v[128:129], v[128:129], v[136:137]\n\t" \
    "v_pk_add_f32 v[130:131], v[130:131], v[138:139]\n\t" \
    "s_waitcnt lgkmcnt(2)\n\t" \
    "v_pk_add_f32 v[144:145], v[144:145], v[148:149]\n\t" \
    "v_pk_add_f32 v[146:147], v[146:147], v[150:151]\n\t" \
    "s_waitcnt lgkmcnt(0)\n\t" \
    "v_pk_add_f32 v[152:153], v[152:153], v[156:157]\n\t" \
    "v_pk_add_f32 v[154:155], v[154:155], v[158:159]\n\t" \
    "v_pk_add_f32 v[144:145], v[144:145], v[152:153]\n\t" \
    "v_pk_add_f32 v[146:147], v[146:147], v[154:155]\n\t" \
    "v_pk_add_f32 v[128:129], v[128:129], v[144:145]\n\t" \
    "v_pk_add_f32 v[130:131], v[130:131], v[146:147]\n\t" \
    "v_pk_add_f32 v[128:129], v[128:129], v[176:177]\n\t" \
    "v_pk_add_f32 v[130:131], v[130:131], v[178:179]\n\t" \
    "v_cvt_pk_bf16_f32 v128, v128, v129\n\t" \
    "v_cvt_pk_bf16_f32 v129, v130, v131\n\t" \
    "v_min_u32 v128, -2, v128\n\t" \
    "v_min_u32 v129, -2, v129\n\t" \
    "s_add_u32 s44, s52, 0x1000000\n\t" \
    "buffer_store_dwordx2 v[128:129], %[xo], %[rs], s44 offen sc1\n\t" \
    "s_waitcnt vmcnt(1)\n\t" \
    "s_branch .Lr24_%=\n\t" \
    ".Lr23_%=:\n\t" \
    "s_waitcnt vmcnt(0)\n\t" \
    ".Lr24_%=:\n\t" \
    ".Lr25_%=:\n\t" \
    "v_max_u32 v196, v200, v201\n\t" \
    "v_max3_u32 v196, v202, v203, v196\n\t" \
    "v_max3_u32 v196, v204, v205, v196\n\t" \
    "v_max3_u32 v196, v206, v207, v196\n\t" \
    "v_max3_u32 v196, v208, v209, v196\n\t" \
    "v_max3_u32 v196, v210, v211, v196\n\t" \
    "v_max3_u32 v196, v212, v213, v196\n\t" \
    "v_max3_u32 v196, v214, v215, v196\n\t" \
    "v_max3_u32 v196, v216, v217, v196\n\t" \
    "v_max3_u32 v196, v218, v219, v196\n\t" \
    "v_cmp_eq_u32 vcc, -1, v196\n\t" \
    "s_cbranch_vccz .Lr26_%=\n\t" \
    "s_cmp_eq_u32 s41, 0\n\t" \
    "s_cbranch_scc1 .Lr26_%=\n\t" \
    "s_sub_u32 s41, s41, 1\n\t" \
    "v_max_u32 v196, v200, v201\n\t" \
    "v_max3_u32 v196, v202, v203, v196\n\t" \
    "v_cmp_eq_u32 vcc, -1, v196\n\t" \
    "s_cbranch_vccz .Lr27_%=\n\t" \
    "buffer_load_dwordx4 v[200:203], %[l16], %[rs], s49 offen offset:0 sc1\n\t" \
    ".Lr27_%=:\n\t" \
    "v_max_u32 v196, v204, v205\n\t" \
    "v_max3_u32 v196, v206, v207, v196\n\t" \
    "v_cmp_eq_u32 vcc, -1, v196\n\t" \
    "s_cbranch_vccz .Lr28_%=\n\t" \
    "buffer_load_dwordx4 v[204:207], %[l16], %[rs], s49 offen offset:1024 sc1\n\t" \
    ".Lr28_%=:\n\t" \
    "v_max_u32 v196, v208, v209\n\t" \
    "v_max3_u32 v196, v210, v211, v196\n\t" \
    "v_cmp_eq_u32 vcc, -1, v196\n\t" \
    "s_cbranch_vccz .Lr29_%=\n\t" \
    "buffer_load_dwordx4 v[208:211], %[l16], %[rs], s49 offen offset:2048 sc1\n\t" \
    ".Lr29_%=:\n\t" \
    "v_max_u32 v196, v212, v213\n\t" \
    "v_max3_u32 v196, v214, v215, v196\n\t" \
    "v_cmp_eq_u32 vcc, -1, v196\n\t" \
    "s_cbranch_vccz .Lr30_%=\n\t" \
    "buffer_load_dwordx4 v[212:215], %[l16], %[rs], s49 offen offset:3072 sc1\n\t" \
    ".Lr30_%=:\n\t" \
    "v_max_u32 v196, v216, v217\n\t" \
    "v_max3_u32 v196, v218, v219, v196\n\t" \
    "v_cmp_eq_u32 vcc, -1, v196\n\t" \
    "s_cbranch_vccz .Lr31_%=\n\t" \
    "buffer_load_dwordx4 v[216:219], %[l16], %[rs], s50 offen offset:0 sc1\n\t" \
    ".Lr31_%=:\n\t" \
    "s_waitcnt vmcnt(0)\n\t" \
    "s_branch .Lr25_%=\n\t" \
    ".Lr26_%=:\n\t" \
    "v_mfma_f32_16x16x32_bf16 v[244:247], v[160:163], v[200:203], 0\n\t" \
    "v_mfma_f32_16x16x32_bf16 v[244:247], v[164:167], v[204:207], v[244:247]\n\t" \
    "v_mfma_f32_16x16x32_bf16 v[244:247], v[168:171], v[208:211], v[244:247]\n\t" \
    "v_mfma_f32_16x16x32_bf16 v[244:247], v[172:175], v[212:215], v[244:247]\n\t" \
    "v_mfma_f32_16x16x32_bf16 v[244:247], v[180:183], v[216:219], v[244:247]\n\t" \
    "s_nop 7\n\t" \
    "ds_write_b128 %[pw], v[244:247] offset:8192\n\t" \
    "s_waitcnt lgkmcnt(0)\n\t" \
    "s_barrier \n\t" \
    "s_cmp_lg_u32 %[wave], 7\n\t" \
    "s_cbranch_scc1 .Lr32_%=\n\t" \
    "ds_read_b128 v[128:131], %[pr] offset:0\n\t" \
    "ds_read_b128 v[132:135], %[pr] offset:1024\n\t" \
    "ds_read_b128 v[136:139], %[pr] offset:2048\n\t" \
    "ds_read_b128 v[140:143], %[pr] offset:3072\n\t" \
    "ds_read_b128 v[144:147], %[pr] offset:4096\n\t" \
    "ds_read_b128 v[148:151], %[pr] offset:5120\n\t" \
    "ds_read_b128 v[152:155], %[pr] offset:6144\n\t" \
    "ds_read_b128 v[156:159], %[pr] offset:7168\n\t" \
    "v_lshlrev_b32 v176, 16, v250\n\t" \
    "v_and_b32 v177, 0xffff0000, v250\n\t" \
    "v_lshlrev_b32 v178, 16, v251\n\t" \
    "v_and_b32 v179, 0xffff0000, v251\n\t" \
    "s_waitcnt lgkmcnt(6)\n\t" \
    "v_pk_add_f32 v[128:129], v[128:129], v[132:133]\n\t" \
    "v_pk_add_f32 v[130:131], v[130:131], v[134:135]\n\t" \
    "s_waitcnt lgkmcnt(4)\n\t" \
    "v_pk_add_f32 v[136:137], v[136:137], v[140:141]\n\t" \
    "v_pk_add_f32 v[138:139], v[138:139], v[142:143]\n\t" \
    "v_pk_add_f32 v[128:129], v[128:129], v[136:137]\n\t" \
    "v_pk_add_f32 v[130:131], v[130:131], v[138:139]\n\t" \
    "s_waitcnt lgkmcnt(2)\n\t" \
    "v_pk_add_f32 v[144:145], v[144:145], v[148:149]\n\t" \
    "v_pk_add_f32 v[146:147], v[146:147], v[150:151]\n\t" \
    "s_waitcnt lgkmcnt(0)\n\t" \
    "v_pk_add_f32 v[152:153], v[152:153], v[156:157]\n\t" \
    "v_pk_add_f32 v[154:155], v[154:155], v[158:159]\n\t" \
    "v_pk_add_f32 v[144:145], v[144:145], v[152:153]\n\t" \
    "v_pk_add_f32 v[146:147], v[146:147], v[154:155]\n\t" \
    "v_pk_add_f32 v[128:129], v[128:129], v[144:145]\n\t" \
    "v_pk_add_f32 v[130:131], v[130:131], v[146:147]\n\t" \
    "v_pk_add_f32 v[128:129], v[128:129], v[176:177]\n\t" \
    "v_pk_add_f32 v[130:131], v[130:131], v[178:179]\n\t" \
    "v_cvt_pk_bf16_f32 v128, v128, v129\n\t" \
    "v_cvt_pk_bf16_f32 v129, v130, v131\n\t" \
    "v_min_u32 v128, -2, v128\n\t" \
    "v_min_u32 v129, -2, v129\n\t" \
    "s_add_u32 s44, s52, 0x1000000\n\t" \
    "buffer_store_dwordx2 v[128:129], %[xo], %[rs], s44 offen sc1\n\t" \
    ".Lr32_%=:\n\t" \
    "buffer_store_dwordx4 v[252:255], %[ov], %[rs], s46 offen sc1\n\t" \
    "buffer_store_dwordx4 v[252:255], v197, %[rs], s51 offen sc1\n\t" \
    "s_add_u32 s40, s40, 1\n\t" \
    "s_cmp_lg_u32 s40, 64\n\t" \
    "s_cbranch_scc1 .Lr1_%=\n\t" \
    "s_branch .Lend%=\n\t" \
    ".Lend%=:\n\t" \
    "s_waitcnt vmcnt(0) lgkmcnt(0)\n\t" \
    "s_mov_b32 %[bud], s41\n\t"
#define ASM_W4_MUL \
    "s_mov_b32 s40, 0\n\t" \
    "s_mov_b32 s41, 0x40000\n\t" \
    "s_mov_b32 s43, 0\n\t" \
    "s_mov_b32 s46, %[hb]\n\t" \
    "s_add_u32 s47, s46, 0x4000\n\t" \
    "v_mov_b64 v[188:189], -1\n\t" \
    "v_mov_b32 v246, 0x1e000\n\t" \
    ".Lm1_%=:\n\t" \
    ".Lm2_%=:\n\t" \
    "ds_read_b32 v245, v246 offset:24\n\t" \
    "s_sub_i32 s44, s40, 3\n\t" \
    "s_waitcnt lgkmcnt(0)\n\t" \
    "v_cmp_gt_i32 vcc, s44, v245\n\t" \
    "s_cbranch_vccnz .Lm2_%=\n\t" \
    "s_add_u32 s44, s40, 1\n\t" \
    "s_min_u32 s44, s44, 127\n\t" \
    "s_lshr_b32 s45, s44, 1\n\t" \
    "s_and_b32 s44, s44, 1\n\t" \
    "s_lshl_b32 s44, s44, 12\n\t" \
    "s_mul_i32 s48, s45, 0x30000\n\t" \
    "s_add_u32 s48, s48, s44\n\t" \
    "s_lshr_b32 s45, s45, 3\n\t" \
    "s_mul_i32 s45, s45, 0x82000\n\t" \
    "s_add_u32 s48, s48, s45\n\t" \
    "s_add_u32 s48, s48, %[hb]\n\t" \
    "s_add_u32 s49, s48, 0x4000\n\t" \
    "s_cmp_lg_u32 s43, 0\n\t" \
    "s_cbranch_scc1 .Lm5_%=\n\t" \
    ".Lm3_%=:\n\t" \
    "v_max_u32 v244, v188, v189\n\t" \
    "v_max3_u32 v244, v190, v191, v244\n\t" \
    "v_cmp_eq_u32 vcc, -1, v244\n\t" \
    "s_cbranch_vccz .Lm4_%=\n\t" \
    "s_cmp_eq_u32 s41, 0\n\t" \
    "s_cbranch_scc1 .Lm4_%=\n\t" \
    "s_sub_u32 s41, s41, 1\n\t" \
    "s_sleep 3\n\t" \
    "buffer_load_dwordx4 v[188:191], %[l16], %[rs], s47 offen offset:3072 sc1\n\t" \
    "s_waitcnt vmcnt(0)\n\t" \
    "s_branch .Lm3_%=\n\t" \
    ".Lm4_%=:\n\t" \
    "buffer_load_dwordx4 v[184:187], %[l16], %[rs], s47 offen offset:2048 sc1\n\t" \
    "buffer_load_dwordx4 v[180:183], %[l16], %[rs], s47 offen offset:1024 sc1\n\t" \
    "buffer_load_dwordx4 v[176:179], %[l16], %[rs], s47 offen offset:0 sc1\n\t" \
    "buffer_load_dwordx4 v[172:175], %[l16], %[rs], s46 offen offset:3072 sc1\n\t" \
    "buffer_load_dwordx4 v[168:171], %[l16], %[rs], s46 offen offset:2048 sc1\n\t" \
    "buffer_load_dwordx4 v[164:167], %[l16], %[rs], s46 offen offset:1024 sc1\n\t" \
    "buffer_load_dwordx4 v[160:163], %[l16], %[rs], s46 offen offset:0 sc1\n\t" \
    ".Lm5_%=:\n\t" \
    "v_mfma_f32_16x16x32_bf16 v[224:227], v[28:31], v[188:191], 0\n\t" \
    "v_mfma_f32_16x16x32_bf16 v[228:231], v[60:63], v[188:191], 0\n\t" \
    "v_mfma_f32_16x16x32_bf16 v[232:235], v[92:95], v[188:191], 0\n\t" \
    "v_mfma_f32_16x16x32_bf16 v[236:239], v[124:127], v[188:191], 0\n\t" \
    "v_mfma_f32_16x16x32_bf16 v[240:243], v[156:159], v[188:191], 0\n\t" \
    "s_waitcnt vmcnt(6)\n\t" \
    ".Lm6_%=:\n\t" \
    "v_max_u32 v244, v184, v185\n\t" \
    "v_max3_u32 v244, v186, v187, v244\n\t" \
    "v_cmp_eq_u32 vcc, -1, v244\n\t" \
    "s_cbranch_vccnz .Lm7_%=\n\t" \
    ".Lm8_%=:\n\t" \
    "v_mfma_f32_16x16x32_bf16 v[224:227], v[24:27], v[184:187], v[224:227]\n\t" \
    "v_mfma_f32_16x16x32_bf16 v[228:231], v[56:59], v[184:187], v[228:231]\n\t" \
    "v_mfma_f32_16x16x32_bf16 v[232:235], v[88:91], v[184:187], v[232:235]\n\t" \
    "v_mfma_f32_16x16x32_bf16 v[236:239], v[120:123], v[184:187], v[236:239]\n\t" \
    "v_mfma_f32_16x16x32_bf16 v[240:243], v[152:155], v[184:187], v[240:243]\n\t" \
    "s_waitcnt vmcnt(5)\n\t" \
    ".Lm9_%=:\n\t" \
    "v_max_u32 v244, v180, v181\n\t" \
    "v_max3_u32 v244, v182, v183, v244\n\t" \
    "v_cmp_eq_u32 vcc, -1, v244\n\t" \
    "s_cbranch_vccnz .Lm10_%=\n\t" \
    ".Lm11_%=:\n\t" \
    "v_mfma_f32_16x16x32_bf16 v[224:227], v[20:23], v[180:183], v[224:227]\n\t" \
    "v_mfma_f32_16x16x32_bf16 v[228:231], v[52:55], v[180:183], v[228:231]\n\t" \
    "v_mfma_f32_16x16x32_bf16 v[232:235], v[84:87], v[180:183], v[232:235]\n\t" \
    "v_mfma_f32_16x16x32_bf16 v[236:239], v[116:119], v[180:183], v[236:239]\n\t" \
    "v_mfma_f32_16x16x32_bf16 v[240:243], v[148:151], v[180:183], v[240:243]\n\t" \
    "s_waitcnt vmcnt(4)\n\t" \
    ".Lm12_%=:\n\t" \
    "v_max_u32 v244, v176, v177\n\t" \
    "v_max3_u32 v244, v178, v179, v244\n\t" \
    "v_cmp_eq_u32 vcc, -1, v244\n\t" \
    "s_cbranch_vccnz .Lm13_%=\n\t" \
    ".Lm14_%=:\n\t" \
    "v_mfma_f32_16x16x32_bf16 v[224:227], v[16:19], v[176:179], v[224:227]\n\t" \
    "v_mfma_f32_16x16x32_bf16 v[228:231], v[48:51], v[176:179], v[228:231]\n\t" \
    "v_mfma_f32_16x16x32_bf16 v[232:235], v[80:83], v[176:179], v[232:235]\n\t" \
    "v_mfma_f32_16x16x32_bf16 v[236:239], v[112:115], v[176:179], v[236:239]\n\t" \
    "v_mfma_f32_16x16x32_bf16 v[240:243], v[144:147], v[176:179], v[240:243]\n\t" \
    "s_waitcnt vmcnt(3)\n\t" \
    ".Lm15_%=:\n\t" \
    "v_max_u32 v244, v172, v173\n\t" \
    "v_max3_u32 v244, v174, v175, v244\n\t" \
    "v_cmp_eq_u32 vcc, -1, v244\n\t" \
    "s_cbranch_vccnz .Lm16_%=\n\t" \
    ".Lm17_%=:\n\t" \
    "v_mfma_f32_16x16x32_bf16 v[224:227], v[12:15], v[172:175], v[224:227]\n\t" \
    "v_mfma_f32_16x16x32_bf16 v[228:231], v[44:47], v[172:175], v[228:231]\n\t" \
    "v_mfma_f32_16x16x32_bf16 v[232:235], v[76:79], v[172:175], v[232:235]\n\t" \
    "v_mfma_f32_16x16x32_bf16 v[236:239], v[108:111], v[172:175], v[236:239]\n\t" \
    "v_mfma_f32_16x16x32_bf16 v[240:243], v[140:143], v[172:175], v[240:243]\n\t" \
    "s_waitcnt vmcnt(2)\n\t" \
    ".Lm18_%=:\n\t" \
    "v_max_u32 v244, v168, v169\n\t" \
    "v_max3_u32 v244, v170, v171, v244\n\t" \
    "v_cmp_eq_u32 vcc, -1, v244\n\t" \
    "s_cbranch_vccnz .Lm19_%=\n\t" \
    ".Lm20_%=:\n\t" \
    "buffer_load_dwordx4 v[220:223], %[l16], %[rs], s49 offen offset:3072 sc1\n\t" \
    "v_mfma_f32_16x16x32_bf16 v[224:227], v[8:11], v[168:171], v[224:227]\n\t" \
    "v_mfma_f32_16x16x32_bf16 v[228:231], v[40:43], v[168:171], v[228:231]\n\t" \
    "v_mfma_f32_16x16x32_bf16 v[232:235], v[72:75], v[168:171], v[232:235]\n\t" \
    "v_mfma_f32_16x16x32_bf16 v[236:239], v[104:107], v[168:171], v[236:239]\n\t" \
    "v_mfma_f32_16x16x32_bf16 v[240:243], v[136:139], v[168:171], v[240:243]\n\t" \
    "s_waitcnt vmcnt(2)\n\t" \
    ".Lm21_%=:\n\t" \
    "v_max_u32 v244, v164, v165\n\t" \
    "v_max3_u32 v244, v166, v167, v244\n\t" \
    "v_cmp_eq_u32 vcc, -1, v244\n\t" \
    "s_cbranch_vccnz .Lm22_%=\n\t" \
    ".Lm23_%=:\n\t" \
    "v_mfma_f32_16x16x32_bf16 v[224:227], v[4:7], v[164:167], v[224:227]\n\t" \
    "v_mfma_f32_16x16x32_bf16 v[228:231], v[36:39], v[164:167], v[228:231]\n\t" \
    "v_mfma_f32_16x16x32_bf16 v[232:235], v[68:71], v[164:167], v[232:235]\n\t" \
    "v_mfma_f32_16x16x32_bf16 v[236:239], v[100:103], v[164:167], v[236:239]\n\t" \
    "v_mfma_f32_16x16x32_bf16 v[240:243], v[132:135], v[164:167], v[240:243]\n\t" \
    "s_waitcnt vmcnt(1)\n\t" \
    ".Lm24_%=:\n\t" \
    "v_max_u32 v244, v160, v161\n\t" \
    "v_max3_u32 v244, v162, v163, v244\n\t" \
    "v_cmp_eq_u32 vcc, -1, v244\n\t" \
    "s_cbranch_vccnz .Lm25_%=\n\t" \
    ".Lm26_%=:\n\t" \
    "v_mfma_f32_16x16x32_bf16 v[224:227], v[0:3], v[160:163], v[224:227]\n\t" \
    "v_mfma_f32_16x16x32_bf16 v[228:231], v[32:35], v[160:163], v[228:231]\n\t" \
    "v_mfma_f32_16x16x32_bf16 v[232:235], v[64:67], v[160:163], v[232:235]\n\t" \
    "v_mfma_f32_16x16x32_bf16 v[236:239], v[96:99], v[160:163], v[236:239]\n\t" \
    "v_mfma_f32_16x16x32_bf16 v[240:243], v[128:131], v[160:163], v[240:243]\n\t" \
    "s_add_u32 s40, s40, 1\n\t" \
    "v_mov_b32 v245, s40\n\t" \
    "ds_write_b128 %[ld], v[224:227] offset:0\n\t" \
    "ds_write_b128 %[ld], v[228:231] offset:6144\n\t" \
    "ds_write_b128 %[ld], v[232:235] offset:12288\n\t" \
    "s_nop 0\n\t" \
    "ds_write_b128 %[ld], v[236:239] offset:18432\n\t" \
    "s_nop 1\n\t" \
    "ds_write_b128 %[ld], v[240:243] offset:24576\n\t" \
    "ds_write_b32 %[fa], v245\n\t" \
    "s_mov_b32 s43, 0\n\t" \
    "s_waitcnt vmcnt(0)\n\t" \
    "v_max_u32 v244, v220, v221\n\t" \
    "v_max3_u32 v244, v222, v223, v244\n\t" \
    "v_cmp_eq_u32 vcc, -1, v244\n\t" \
    "s_cbranch_vccnz .Lm27_%=\n\t" \
    "buffer_load_dwordx4 v[216:219], %[l16], %[rs], s49 offen offset:2048 sc1\n\t" \
    "buffer_load_dwordx4 v[212:215], %[l16], %[rs], s49 offen offset:1024 sc1\n\t" \
    "buffer_load_dwordx4 v[208:211], %[l16], %[rs], s49 offen offset:0 sc1\n\t" \
    "buffer_load_dwordx4 v[204:207], %[l16], %[rs], s48 offen offset:3072 sc1\n\t" \
    "buffer_load_dwordx4 v[200:203], %[l16], %[rs], s48 offen offset:2048 sc1\n\t" \
    "buffer_load_dwordx4 v[196:199], %[l16], %[rs], s48 offen offset:1024 sc1\n\t" \
    "buffer_load_dwordx4 v[192:195], %[l16], %[rs], s48 offen offset:0 sc1\n\t" \
    "s_mov_b32 s43, 1\n\t" \
    ".Lm27_%=:\n\t" \
    ".Lm28_%=:\n\t" \
    "ds_read_b32 v245, v246 offset:28\n\t" \
    "s_sub_i32 s44, s40, 3\n\t" \
    "s_waitcnt lgkmcnt(0)\n\t" \
    "v_cmp_gt_i32 vcc, s44, v245\n\t" \
    "s_cbranch_vccnz .Lm28_%=\n\t" \
    "s_add_u32 s44, s40, 1\n\t" \
    "s_min_u32 s44, s44, 127\n\t" \
    "s_lshr_b32 s45, s44, 1\n\t" \
    "s_and_b32 s44, s44, 1\n\t" \
    "s_lshl_b32 s44, s44, 12\n\t" \
    "s_mul_i32 s46, s45, 0x30000\n\t" \
    "s_add_u32 s46, s46, s44\n\t" \
    "s_lshr_b32 s45, s45, 3\n\t" \
    "s_mul_i32 s45, s45, 0x82000\n\t" \
    "s_add_u32 s46, s46, s45\n\t" \
    "s_add_u32 s46, s46, %[hb]\n\t" \
    "s_add_u32 s47, s46, 0x4000\n\t" \
    "s_cmp_lg_u32 s43, 0\n\t" \
    "s_cbranch_scc1 .Lm31_%=\n\t" \
    ".Lm29_%=:\n\t" \
    "v_max_u32 v244, v220, v221\n\t" \
    "v_max3_u32 v244, v222, v223, v244\n\t" \
    "v_cmp_eq_u32 vcc, -1, v244\n\t" \
    "s_cbranch_vccz .Lm30_%=\n\t" \
    "s_cmp_eq_u32 s41, 0\n\t" \
    "s_cbranch_scc1 .Lm30_%=\n\t" \
    "s_sub_u32 s41, s41, 1\n\t" \
    "s_sleep 3\n\t" \
    "buffer_load_dwordx4 v[220:223], %[l16], %[rs], s49 offen offset:3072 sc1\n\t" \
    "s_waitcnt vmcnt(0)\n\t" \
    "s_branch .Lm29_%=\n\t" \
    ".Lm30_%=:\n\t" \
    "buffer_load_dwordx4 v[216:219], %[l16], %[rs], s49 offen offset:2048 sc1\n\t" \
    "buffer_load_dwordx4 v[212:215], %[l16], %[rs], s49 offen offset:1024 sc1\n\t" \
    "buffer_load_dwordx4 v[208:211], %[l16], %[rs], s49 offen offset:0 sc1\n\t" \
    "buffer_load_dwordx4 v[204:207], %[l16], %[rs], s48 offen offset:3072 sc1\n\t" \
    "buffer_load_dwordx4 v[200:203], %[l16], %[rs], s48 offen offset:2048 sc1\n\t" \
    "buffer_load_dwordx4 v[196:199], %[l16], %[rs], s48 offen offset:1024 sc1\n\t" \
    "buffer_load_dwordx4 v[192:195], %[l16], %[rs], s48 offen offset:0 sc1\n\t" \
    ".Lm31_%=:\n\t" \
    "v_mfma_f32_16x16x32_bf16 v[224:227], v[28:31], v[220:223], 0\n\t" \
    "v_mfma_f32_16x16x32_bf16 v[228:231], v[60:63], v[220:223], 0\n\t" \
    "v_mfma_f32_16x16x32_bf16 v[232:235], v[92:95], v[220:223], 0\n\t" \
    "v_mfma_f32_16x16x32_bf16 v[236:239], v[124:127], v[220:223], 0\n\t" \
    "v_mfma_f32_16x16x32_bf16 v[240:243], v[156:159], v[220:223], 0\n\t" \
    "s_waitcnt vmcnt(6)\n\t" \
    ".Lm32_%=:\n\t" \
    "v_max_u32 v244, v216, v217\n\t" \
    "v_max3_u32 v244, v218, v219, v244\n\t" \
    "v_cmp_eq_u32 vcc, -1, v244\n\t" \
    "s_cbranch_vccnz .Lm33_%=\n\t" \
    ".Lm34_%=:\n\t" \
    "v_mfma_f32_16x16x32_bf16 v[224:227], v[24:27], v[216:219], v[224:227]\n\t" \
    "v_mfma_f32_16x16x32_bf16 v[228:231], v[56:59], v[216:219], v[228:231]\n\t" \
    "v_mfma_f32_16x16x32_bf16 v[232:235], v[88:91], v[216:219], v[232:235]\n\t" \
    "v_mfma_f32_16x16x32_bf16 v[236:239], v[120:123], v[216:219], v[236:239]\n\t" \
    "v_mfma_f32_16x16x32_bf16 v[240:243], v[152:155], v[216:219], v[240:243]\n\t" \
    "s_waitcnt vmcnt(5)\n\t" \
    ".Lm35_%=:\n\t" \
    "v_max_u32 v244, v212, v213\n\t" \
    "v_max3_u32 v244, v214, v215, v244\n\t" \
    "v_cmp_eq_u32 vcc, -1, v244\n\t" \
    "s_cbranch_vccnz .Lm36_%=\n\t" \
    ".Lm37_%=:\n\t" \
    "v_mfma_f32_16x16x32_bf16 v[224:227], v[20:23], v[212:215], v[224:227]\n\t" \
    "v_mfma_f32_16x16x32_bf16 v[228:231], v[52:55], v[212:215], v[228:231]\n\t" \
    "v_mfma_f32_16x16x32_bf16 v[232:235], v[84:87], v[212:215], v[232:235]\n\t" \
    "v_mfma_f32_16x16x32_bf16 v[236:239], v[116:119], v[212:215], v[236:239]\n\t" \
    "v_mfma_f32_16x16x32_bf16 v[240:243], v[148:151], v[212:215], v[240:243]\n\t" \
    "s_waitcnt vmcnt(4)\n\t" \
    ".Lm38_%=:\n\t" \
    "v_max_u32 v244, v208, v209\n\t" \
    "v_max3_u32 v244, v210, v211, v244\n\t" \
    "v_cmp_eq_u32 vcc, -1, v244\n\t" \
    "s_cbranch_vccnz .Lm39_%=\n\t" \
    ".Lm40_%=:\n\t" \
    "v_mfma_f32_16x16x32_bf16 v[224:227], v[16:19], v[208:211], v[224:227]\n\t" \
    "v_mfma_f32_16x16x32_bf16 v[228:231], v[48:51], v[208:211], v[228:231]\n\t" \
    "v_mfma_f32_16x16x32_bf16 v[232:235], v[80:83], v[208:211], v[232:235]\n\t" \
    "v_mfma_f32_16x16x32_bf16 v[236:239], v[112:115], v[208:211], v[236:239]\n\t" \
    "v_mfma_f32_16x16x32_bf16 v[240:243], v[144:147], v[208:211], v[240:243]\n\t" \
    "s_waitcnt vmcnt(3)\n\t" \
    ".Lm41_%=:\n\t" \
    "v_max_u32 v244, v204, v205\n\t" \
    "v_max3_u32 v244, v206, v207, v244\n\t" \
    "v_cmp_eq_u32 vcc, -1, v244\n\t" \
    "s_cbranch_vccnz .Lm42_%=\n\t" \
    ".Lm43_%=:\n\t" \
    "v_mfma_f32_16x16x32_bf16 v[224:227], v[12:15], v[204:207], v[224:227]\n\t" \
    "v_mfma_f32_16x16x32_bf16 v[228:231], v[44:47], v[204:207], v[228:231]\n\t" \
    "v_mfma_f32_16x16x32_bf16 v[232:235], v[76:79], v[204:207], v[232:235]\n\t" \
    "v_mfma_f32_16x16x32_bf16 v[236:239], v[108:111], v[204:207], v[236:239]\n\t" \
    "v_mfma_f32_16x16x32_bf16 v[240:243], v[140:143], v[204:207], v[240:243]\n\t" \
    "s_waitcnt vmcnt(2)\n\t" \
    ".Lm44_%=:\n\t" \
    "v_max_u32 v244, v200, v201\n\t" \
    "v_max3_u32 v244, v202, v203, v244\n\t" \
    "v_cmp_eq_u32 vcc, -1, v244\n\t" \
    "s_cbranch_vccnz .Lm45_%=\n\t" \
    ".Lm46_%=:\n\t" \
    "buffer_load_dwordx4 v[188:191], %[l16], %[rs], s47 offen offset:3072 sc1\n\t" \
    "v_mfma_f32_16x16x32_bf16 v[224:227], v[8:11], v[200:203], v[224:227]\n\t" \
    "v_mfma_f32_16x16x32_bf16 v[228:231], v[40:43], v[200:203], v[228:231]\n\t" \
    "v_mfma_f32_16x16x32_bf16 v[232:235], v[72:75], v[200:203], v[232:235]\n\t" \
    "v_mfma_f32_16x16x32_bf16 v[236:239], v[104:107], v[200:203], v[236:239]\n\t" \
    "v_mfma_f32_16x16x32_bf16 v[240:243], v[136:139], v[200:203], v[240:243]\n\t" \
    "s_waitcnt vmcnt(2)\n\t" \
    ".Lm47_%=:\n\t" \
    "v_max_u32 v244, v196, v197\n\t" \
    "v_max3_u32 v244, v198, v199, v244\n\t" \
    "v_cmp_eq_u32 vcc, -1, v244\n\t" \
    "s_cbranch_vccnz .Lm48_%=\n\t" \
    ".Lm49_%=:\n\t" \
    "v_mfma_f32_16x16x32_bf16 v[224:227], v[4:7], v[196:199], v[224:227]\n\t" \
    "v_mfma_f32_16x16x32_bf16 v[228:231], v[36:39], v[196:199], v[228:231]\n\t" \
    "v_mfma_f32_16x16x32_bf16 v[232:235], v[68:71], v[196:199], v[232:235]\n\t" \
    "v_mfma_f32_16x16x32_bf16 v[236:239], v[100:103], v[196:199], v[236:239]\n\t" \
    "v_mfma_f32_16x16x32_bf16 v[240:243], v[132:135], v[196:199], v[240:243]\n\t" \
    "s_waitcnt vmcnt(1)\n\t" \
    ".Lm50_%=:\n\t" \
    "v_max_u32 v244, v192, v193\n\t" \
    "v_max3_u32 v244, v194, v195, v244\n\t" \
    "v_cmp_eq_u32 vcc, -1, v244\n\t" \
    "s_cbranch_vccnz .Lm51_%=\n\t" \
    ".Lm52_%=:\n\t" \
    "v_mfma_f32_16x16x32_bf16 v[224:227], v[0:3], v[192:195], v[224:227]\n\t" \
    "v_mfma_f32_16x16x32_bf16 v[228:231], v[32:35], v[192:195], v[228:231]\n\t" \
    "v_mfma_f32_16x16x32_bf16 v[232:235], v[64:67], v[192:195], v[232:235]\n\t" \
    "v_mfma_f32_16x16x32_bf16 v[236:239], v[96:99], v[192:195], v[236:239]\n\t" \
    "v_mfma_f32_16x16x32_bf16 v[240:243], v[128:131], v[192:195], v[240:243]\n\t" \
    "s_add_u32 s40, s40, 1\n\t" \
    "v_mov_b32 v245, s40\n\t" \
    "ds_write_b128 %[ld], v[224:227] offset:30720\n\t" \
    "ds_write_b128 %[ld], v[228:231] offset:36864\n\t" \
    "ds_write_b128 %[ld], v[232:235] offset:43008\n\t" \
    "s_nop 0\n\t" \
    "ds_write_b128 %[ld], v[236:239] offset:49152\n\t" \
    "s_nop 1\n\t" \
    "ds_write_b128 %[ld], v[240:243] offset:55296\n\t" \
    "ds_write_b32 %[fa], v245\n\t" \
    "s_mov_b32 s43, 0\n\t" \
    "s_cmp_eq_u32 s40, 128\n\t" \
    "s_cbranch_scc1 .Lm53_%=\n\t" \
    "s_waitcnt vmcnt(0)\n\t" \
    "v_max_u32 v244, v188, v189\n\t" \
    "v_max3_u32 v244, v190, v191, v244\n\t" \
    "v_cmp_eq_u32 vcc, -1, v244\n\t" \
    "s_cbranch_vccnz .Lm53_%=\n\t" \
    "buffer_load_dwordx4 v[184:187], %[l16], %[rs], s47 offen offset:2048 sc1\n\t" \
    "buffer_load_dwordx4 v[180:183], %[l16], %[rs], s47 offen offset:1024 sc1\n\t" \
    "buffer_load_dwordx4 v[176:179], %[l16], %[rs], s47 offen offset:0 sc1\n\t" \
    "buffer_load_dwordx4 v[172:175], %[l16], %[rs], s46 offen offset:3072 sc1\n\t" \
    "buffer_load_dwordx4 v[168:171], %[l16], %[rs], s46 offen offset:2048 sc1\n\t" \
    "buffer_load_dwordx4 v[164:167], %[l16], %[rs], s46 offen offset:1024 sc1\n\t" \
    "buffer_load_dwordx4 v[160:163], %[l16], %[rs], s46 offen offset:0 sc1\n\t" \
    "s_mov_b32 s43, 1\n\t" \
    ".Lm53_%=:\n\t" \
    "v_sub_u32 %[ld], %[l2], %[ld]\n\t" \
    "s_nop 0\n\t" \
    "s_cmp_lg_u32 s40, 128\n\t" \
    "s_cbranch_scc1 .Lm1_%=\n\t" \
    "s_branch .Lend%=\n\t" \
    ".Lm7_%=:\n\t" \
    "s_cmp_eq_u32 s41, 0\n\t" \
    "s_cbranch_scc1 .Lm8_%=\n\t" \
    "s_sub_u32 s41, s41, 1\n\t" \
    "buffer_load_dwordx4 v[184:187], %[l16], %[rs], s47 offen offset:2048 sc1\n\t" \
    "s_waitcnt vmcnt(0)\n\t" \
    "s_branch .Lm6_%=\n\t" \
    ".Lm10_%=:\n\t" \
    "s_cmp_eq_u32 s41, 0\n\t" \
    "s_cbranch_scc1 .Lm11_%=\n\t" \
    "s_sub_u32 s41, s41, 1\n\t" \
    "buffer_load_dwordx4 v[180:183], %[l16], %[rs], s47 offen offset:1024 sc1\n\t" \
    "s_waitcnt vmcnt(0)\n\t" \
    "s_branch .Lm9_%=\n\t" \
    ".Lm13_%=:\n\t" \
    "s_cmp_eq_u32 s41, 0\n\t" \
    "s_cbranch_scc1 .Lm14_%=\n\t" \
    "s_sub_u32 s41, s41, 1\n\t" \
    "buffer_load_dwordx4 v[176:179], %[l16], %[rs], s47 offen offset:0 sc1\n\t" \
    "s_waitcnt vmcnt(0)\n\t" \
    "s_branch .Lm12_%=\n\t" \
    ".Lm16_%=:\n\t" \
    "s_cmp_eq_u32 s41, 0\n\t" \
    "s_cbranch_scc1 .Lm17_%=\n\t" \
    "s_sub_u32 s41, s41, 1\n\t" \
    "buffer_load_dwordx4 v[172:175], %[l16], %[rs], s46 offen offset:3072 sc1\n\t" \
    "s_waitcnt vmcnt(0)\n\t" \
    "s_branch .Lm15_%=\n\t" \
    ".Lm19_%=:\n\t" \
    "s_cmp_eq_u32 s41, 0\n\t" \
    "s_cbranch_scc1 .Lm20_%=\n\t" \
    "s_sub_u32 s41, s41, 1\n\t" \
    "buffer_load_dwordx4 v[168:171], %[l16], %[rs], s46 offen offset:2048 sc1\n\t" \
    "s_waitcnt vmcnt(0)\n\t" \
    "s_branch .Lm18_%=\n\t" \
    ".Lm22_%=:\n\t" \
    "s_cmp_eq_u32 s41, 0\n\t" \
    "s_cbranch_scc1 .Lm23_%=\n\t" \
    "s_sub_u32 s41, s41, 1\n\t" \
    "buffer_load_dwordx4 v[164:167], %[l16], %[rs], s46 offen offset:1024 sc1\n\t" \
    "s_waitcnt vmcnt(0)\n\t" \
    "s_branch .Lm21_%=\n\t" \
    ".Lm25_%=:\n\t" \
    "s_cmp_eq_u32 s41, 0\n\t" \
    "s_cbranch_scc1 .Lm26_%=\n\t" \
    "s_sub_u32 s41, s41, 1\n\t" \
    "buffer_load_dwordx4 v[160:163], %[l16], %[rs], s46 offen offset:0 sc1\n\t" \
    "s_waitcnt vmcnt(0)\n\t" \
    "s_branch .Lm24_%=\n\t" \
    ".Lm33_%=:\n\t" \
    "s_cmp_eq_u32 s41, 0\n\t" \
    "s_cbranch_scc1 .Lm34_%=\n\t" \
    "s_sub_u32 s41, s41, 1\n\t" \
    "buffer_load_dwordx4 v[216:219], %[l16], %[rs], s49 offen offset:2048 sc1\n\t" \
    "s_waitcnt vmcnt(0)\n\t" \
    "s_branch .Lm32_%=\n\t" \
    ".Lm36_%=:\n\t" \
    "s_cmp_eq_u32 s41, 0\n\t" \
    "s_cbranch_scc1 .Lm37_%=\n\t" \
    "s_sub_u32 s41, s41, 1\n\t" \
    "buffer_load_dwordx4 v[212:215], %[l16], %[rs], s49 offen offset:1024 sc1\n\t" \
    "s_waitcnt vmcnt(0)\n\t" \
    "s_branch .Lm35_%=\n\t" \
    ".Lm39_%=:\n\t" \
    "s_cmp_eq_u32 s41, 0\n\t" \
    "s_cbranch_scc1 .Lm40_%=\n\t" \
    "s_sub_u32 s41, s41, 1\n\t" \
    "buffer_load_dwordx4 v[208:211], %[l16], %[rs], s49 offen offset:0 sc1\n\t" \
    "s_waitcnt vmcnt(0)\n\t" \
    "s_branch .Lm38_%=\n\t" \
    ".Lm42_%=:\n\t" \
    "s_cmp_eq_u32 s41, 0\n\t" \
    "s_cbranch_scc1 .Lm43_%=\n\t" \
    "s_sub_u32 s41, s41, 1\n\t" \
    "buffer_load_dwordx4 v[204:207], %[l16], %[rs], s48 offen offset:3072 sc1\n\t" \
    "s_waitcnt vmcnt(0)\n\t" \
    "s_branch .Lm41_%=\n\t" \
    ".Lm45_%=:\n\t" \
    "s_cmp_eq_u32 s41, 0\n\t" \
    "s_cbranch_scc1 .Lm46_%=\n\t" \
    "s_sub_u32 s41, s41, 1\n\t" \
    "buffer_load_dwordx4 v[200:203], %[l16], %[rs], s48 offen offset:2048 sc1\n\t" \
    "s_waitcnt vmcnt(0)\n\t" \
    "s_branch .Lm44_%=\n\t" \
    ".Lm48_%=:\n\t" \
    "s_cmp_eq_u32 s41, 0\n\t" \
    "s_cbranch_scc1 .Lm49_%=\n\t" \
    "s_sub_u32 s41, s41, 1\n\t" \
    "buffer_load_dwordx4 v[196:199], %[l16], %[rs], s48 offen offset:1024 sc1\n\t" \
    "s_waitcnt vmcnt(0)\n\t" \
    "s_branch .Lm47_%=\n\t" \
    ".Lm51_%=:\n\t" \
    "s_cmp_eq_u32 s41, 0\n\t" \
    "s_cbranch_scc1 .Lm52_%=\n\t" \
    "s_sub_u32 s41, s41, 1\n\t" \
    "buffer_load_dwordx4 v[192:195], %[l16], %[rs], s48 offen offset:0 sc1\n\t" \
    "s_waitcnt vmcnt(0)\n\t" \
    "s_branch .Lm50_%=\n\t" \
    ".Lend%=:\n\t" \
    "s_waitcnt vmcnt(0) lgkmcnt(0)\n\t" \
    "s_mov_b32 %[bud], s41\n\t"
#define ASM_W4_FIN0 \
    "s_mov_b32 s40, %[mt]\n\t" \
    "s_mov_b32 s41, 0x40000\n\t" \
    "s_mov_b32 s43, 0\n\t" \
    "v_mov_b32 v99, 0x3f803f80\n\t" \
    "s_setprio 3\n\t" \
    "s_add_u32 s46, s43, 0x2c00000\n\t" \
    "s_add_u32 s47, s43, 0x1c00000\n\t" \
    "buffer_load_dwordx2 v[120:121], %[t0], %[rs], s46 offen sc1\n\t" \
    "buffer_load_dwordx2 v[122:123], %[t0], %[rs], s47 offen sc1\n\t" \
    "buffer_load_dwordx2 v[124:125], %[t1], %[rs], s46 offen sc1\n\t" \
    "buffer_load_dwordx2 v[126:127], %[t1], %[rs], s47 offen sc1\n\t" \
    "buffer_load_dwordx2 v[128:129], %[t2], %[rs], s46 offen sc1\n\t" \
    "buffer_load_dwordx2 v[130:131], %[t2], %[rs], s47 offen sc1\n\t" \
    "buffer_load_dwordx2 v[132:133], %[t3], %[rs], s46 offen sc1\n\t" \
    "buffer_load_dwordx2 v[134:135], %[t3], %[rs], s47 offen sc1\n\t" \
    "buffer_load_dwordx2 v[136:137], %[t4], %[rs], s46 offen sc1\n\t" \
    "buffer_load_dwordx2 v[138:139], %[t4], %[rs], s47 offen sc1\n\t" \
    "s_waitcnt vmcnt(0)\n\t" \
    ".Lf01_%=:\n\t" \
    ".Lf02_%=:\n\t" \
    "s_waitcnt vmcnt(1)\n\t" \
    "v_max_u32 v80, v120, v121\n\t" \
    "v_max3_u32 v80, v122, v123, v80\n\t" \
    "v_max3_u32 v80, v124, v125, v80\n\t" \
    "v_max3_u32 v80, v126, v127, v80\n\t" \
    "v_max3_u32 v80, v128, v129, v80\n\t" \
    "v_max3_u32 v80, v130, v131, v80\n\t" \
    "v_max3_u32 v80, v132, v133, v80\n\t" \
    "v_max3_u32 v80, v134, v135, v80\n\t" \
    "v_max3_u32 v80, v136, v137, v80\n\t" \
    "v_max3_u32 v80, v138, v139, v80\n\t" \
    "v_cmp_eq_u32 vcc, -1, v80\n\t" \
    "s_cbranch_vccz .Lf03_%=\n\t" \
    "s_cmp_eq_u32 s41, 0\n\t" \
    "s_cbranch_scc1 .Lf03_%=\n\t" \
    "s_sub_u32 s41, s41, 1\n\t" \
    "s_sleep 8\n\t" \
    "s_add_u32 s46, s43, 0x2c00000\n\t" \
    "s_add_u32 s47, s43, 0x1c00000\n\t" \
    "buffer_load_dwordx2 v[120:121], %[t0], %[rs], s46 offen sc1\n\t" \
    "buffer_load_dwordx2 v[122:123], %[t0], %[rs], s47 offen sc1\n\t" \
    "buffer_load_dwordx2 v[124:125], %[t1], %[rs], s46 offen sc1\n\t" \
    "buffer_load_dwordx2 v[126:127], %[t1], %[rs], s47 offen sc1\n\t" \
    "buffer_load_dwordx2 v[128:129], %[t2], %[rs], s46 offen sc1\n\t" \
    "buffer_load_dwordx2 v[130:131], %[t2], %[rs], s47 offen sc1\n\t" \
    "buffer_load_dwordx2 v[132:133], %[t3], %[rs], s46 offen sc1\n\t" \
    "buffer_load_dwordx2 v[134:135], %[t3], %[rs], s47 offen sc1\n\t" \
    "buffer_load_dwordx2 v[136:137], %[t4], %[rs], s46 offen sc1\n\t" \
    "buffer_load_dwordx2 v[138:139], %[t4], %[rs], s47 offen sc1\n\t" \
    "s_waitcnt vmcnt(0)\n\t" \
    "s_branch .Lf02_%=\n\t" \
    ".Lf03_%=:\n\t" \
    "v_lshlrev_b32 v140, 16, v120\n\t" \
    "v_and_b32 v141, 0xffff0000, v120\n\t" \
    "v_lshlrev_b32 v142, 16, v121\n\t" \
    "v_and_b32 v143, 0xffff0000, v121\n\t" \
    "v_lshlrev_b32 v80, 16, v122\n\t" \
    "v_and_b32 v81, 0xffff0000, v122\n\t" \
    "v_lshlrev_b32 v82, 16, v123\n\t" \
    "v_and_b32 v83, 0xffff0000, v123\n\t" \
    "v_add_f32 v140, v140, v80\n\t" \
    "v_add_f32 v141, v141, v81\n\t" \
    "v_add_f32 v142, v142, v82\n\t" \
    "v_add_f32 v143, v143, v83\n\t" \
    "v_lshlrev_b32 v144, 16, v124\n\t" \
    "v_and_b32 v145, 0xffff0000, v124\n\t" \
    "v_lshlrev_b32 v146, 16, v125\n\t" \
    "v_and_b32 v147, 0xffff0000, v125\n\t" \
    "v_lshlrev_b32 v80, 16, v126\n\t" \
    "v_and_b32 v81, 0xffff0000, v126\n\t" \
    "v_lshlrev_b32 v82, 16, v127\n\t" \
    "v_and_b32 v83, 0xffff0000, v127\n\t" \
    "v_add_f32 v144, v144, v80\n\t" \
    "v_add_f32 v145, v145, v81\n\t" \
    "v_add_f32 v146, v146, v82\n\t" \
    "v_add_f32 v147, v147, v83\n\t" \
    "v_lshlrev_b32 v148, 16, v128\n\t" \
    "v_and_b32 v149, 0xffff0000, v128\n\t" \
    "v_lshlrev_b32 v150, 16, v129\n\t" \
    "v_and_b32 v151, 0xffff0000, v129\n\t" \
    "v_lshlrev_b32 v80, 16, v130\n\t" \
    "v_and_b32 v81, 0xffff0000, v130\n\t" \
    "v_lshlrev_b32 v82, 16, v131\n\t" \
    "v_and_b32 v83, 0xffff0000, v131\n\t" \
    "v_add_f32 v148, v148, v80\n\t" \
    "v_add_f32 v149, v149, v81\n\t" \
    "v_add_f32 v150, v150, v82\n\t" \
    "v_add_f32 v151, v151, v83\n\t" \
    "v_lshlrev_b32 v152, 16, v132\n\t" \
    "v_and_b32 v153, 0xffff0000, v132\n\t" \
    "v_lshlrev_b32 v154, 16, v133\n\t" \
    "v_and_b32 v155, 0xffff0000, v133\n\t" \
    "v_lshlrev_b32 v80, 16, v134\n\t" \
    "v_and_b32 v81, 0xffff0000, v134\n\t" \
    "v_lshlrev_b32 v82, 16, v135\n\t" \
    "v_and_b32 v83, 0xffff0000, v135\n\t" \
    "v_add_f32 v152, v152, v80\n\t" \
    "v_add_f32 v153, v153, v81\n\t" \
    "v_add_f32 v154, v154, v82\n\t" \
    "v_add_f32 v155, v155, v83\n\t" \
    "v_lshlrev_b32 v156, 16, v136\n\t" \
    "v_and_b32 v157, 0xffff0000, v136\n\t" \
    "v_lshlrev_b32 v158, 16, v137\n\t" \
    "v_and_b32 v159, 0xffff0000, v137\n\t" \
    "v_lshlrev_b32 v80, 16, v138\n\t" \
    "v_and_b32 v81, 0xffff0000, v138\n\t" \
    "v_lshlrev_b32 v82, 16, v139\n\t" \
    "v_and_b32 v83, 0xffff0000, v139\n\t" \
    "v_add_f32 v156, v156, v80\n\t" \
    "v_add_f32 v157, v157, v81\n\t" \
    "v_add_f32 v158, v158, v82\n\t" \
    "v_add_f32 v159, v159, v83\n\t" \
    "s_mov_b32 s42, s43\n\t" \
    "s_lshr_b32 s48, s40, 1\n\t" \
    "s_add_u32 s48, s48, 1\n\t" \
    "s_mul_i32 s43, s48, 0x14000\n\t" \
    "s_lshr_b32 s44, s48, 4\n\t" \
    "s_mul_i32 s44, s44, 0xc2000\n\t" \
    "s_add_u32 s43, s43, s44\n\t" \
    "s_lshr_b32 s44, s40, 3\n\t" \
    "s_lshl_b32 s44, s44, 14\n\t" \
    "s_and_b32 s45, s40, 7\n\t" \
    "s_lshl_b32 s45, s45, 10\n\t" \
    "s_add_u32 s49, s44, s45\n\t" \
    "s_add_u32 s49, s49, 0x1c0000\n\t" \
    "s_add_u32 s50, s49, %[co]\n\t" \
    "s_add_u32 s51, s42, 0x1400000\n\t" \
    "s_add_u32 s52, s42, 0xc00000\n\t" \
    "s_mov_b32 s44, s48\n\t" \
    "s_mov_b32 s45, s48\n\t" \
    "v_mov_b64 v[76:77], s[44:45]\n\t" \
    "v_mov_b64 v[78:79], s[44:45]\n\t" \
    "s_add_u32 s44, s40, 1\n\t" \
    "v_mov_b32 v98, s44\n\t" \
    ".Lf04_%=:\n\t" \
    "s_nop 7\n\t" \
    "ds_read_b32 v80, %[fa]\n\t" \
    "s_waitcnt lgkmcnt(0)\n\t" \
    "v_cmp_ge_u32 vcc, s40, v80\n\t" \
    "s_cbranch_vccnz .Lf04_%=\n\t" \
    "ds_read_b128 v[160:163], %[la] offset:0\n\t" \
    "ds_read_b128 v[164:167], %[la] offset:6144\n\t" \
    "ds_read_b128 v[168:171], %[la] offset:12288\n\t" \
    "ds_read_b128 v[172:175], %[la] offset:18432\n\t" \
    "ds_read_b128 v[176:179], %[la] offset:24576\n\t" \
    "ds_read_b128 v[180:183], %[la] offset:1024\n\t" \
    "ds_read_b128 v[184:187], %[la] offset:7168\n\t" \
    "ds_read_b128 v[188:191], %[la] offset:13312\n\t" \
    "ds_read_b128 v[192:195], %[la] offset:19456\n\t" \
    "ds_read_b128 v[196:199], %[la] offset:25600\n\t" \
    "ds_read_b128 v[200:203], %[la] offset:2048\n\t" \
    "ds_read_b128 v[204:207], %[la] offset:8192\n\t" \
    "ds_read_b128 v[208:211], %[la] offset:14336\n\t" \
    "ds_read_b128 v[212:215], %[la] offset:20480\n\t" \
    "ds_read_b128 v[216:219], %[la] offset:26624\n\t" \
    "s_waitcnt lgkmcnt(14)\n\t" \
    "v_pk_add_f32 v[100:101], v[140:141], v[160:161]\n\t" \
    "v_pk_add_f32 v[102:103], v[142:143], v[162:163]\n\t" \
    "ds_read_b128 v[220:223], %[la] offset:3072\n\t" \
    "s_waitcnt lgkmcnt(14)\n\t" \
    "v_pk_add_f32 v[104:105], v[144:145], v[164:165]\n\t" \
    "v_pk_add_f32 v[106:107], v[146:147], v[166:167]\n\t" \
    "ds_read_b128 v[224:227], %[la] offset:9216\n\t" \
    "s_waitcnt lgkmcnt(14)\n\t" \
    "v_pk_add_f32 v[108:109], v[148:149], v[168:169]\n\t" \
    "v_pk_add_f32 v[110:111], v[150:151], v[170:171]\n\t" \
    "ds_read_b128 v[228:231], %[la] offset:15360\n\t" \
    "s_waitcnt lgkmcnt(14)\n\t" \
    "v_pk_add_f32 v[112:113], v[152:153], v[172:173]\n\t" \
    "v_pk_add_f32 v[114:115], v[154:155], v[174:175]\n\t" \
    "ds_read_b128 v[232:235], %[la] offset:21504\n\t" \
    "s_waitcnt lgkmcnt(14)\n\t" \
    "v_pk_add_f32 v[116:117], v[156:157], v[176:177]\n\t" \
    "v_pk_add_f32 v[118:119], v[158:159], v[178:179]\n\t" \
    "ds_read_b128 v[236:239], %[la] offset:27648\n\t" \
    "s_waitcnt lgkmcnt(14)\n\t" \
    "v_pk_add_f32 v[100:101], v[100:101], v[180:181]\n\t" \
    "v_pk_add_f32 v[102:103], v[102:103], v[182:183]\n\t" \
    "ds_read_b128 v[160:163], %[la] offset:4096\n\t" \
    "s_waitcnt lgkmcnt(14)\n\t" \
    "v_pk_add_f32 v[104:105], v[104:105], v[184:185]\n\t" \
    "v_pk_add_f32 v[106:107], v[106:107], v[186:187]\n\t" \
    "ds_read_b128 v[164:167], %[la] offset:10240\n\t" \
    "s_waitcnt lgkmcnt(14)\n\t" \
    "v_pk_add_f32 v[108:109], v[108:109], v[188:189]\n\t" \
    "v_pk_add_f32 v[110:111], v[110:111], v[190:191]\n\t" \
    "ds_read_b128 v[168:171], %[la] offset:16384\n\t" \
    "s_waitcnt lgkmcnt(14)\n\t" \
    "v_pk_add_f32 v[112:113], v[112:113], v[192:193]\n\t" \
    "v_pk_add_f32 v[114:115], v[114:115], v[194:195]\n\t" \
    "ds_read_b128 v[172:175], %[la] offset:22528\n\t" \
    "s_waitcnt lgkmcnt(14)\n\t" \
    "v_pk_add_f32 v[116:117], v[116:117], v[196:197]\n\t" \
    "v_pk_add_f32 v[118:119], v[118:119], v[198:199]\n\t" \
    "ds_read_b128 v[176:179], %[la] offset:28672\n\t" \
    "s_waitcnt lgkmcnt(14)\n\t" \
    "v_pk_add_f32 v[100:101], v[100:101], v[200:201]\n\t" \
    "v_pk_add_f32 v[102:103], v[102:103], v[202:203]\n\t" \
    "ds_read_b128 v[180:183], %[la] offset:5120\n\t" \
    "s_waitcnt lgkmcnt(14)\n\t" \
    "v_pk_add_f32 v[104:105], v[104:105], v[204:205]\n\t" \
    "v_pk_add_f32 v[106:107], v[106:107], v[206:207]\n\t" \
    "ds_read_b128 v[184:187], %[la] offset:11264\n\t" \
    "s_waitcnt lgkmcnt(14)\n\t" \
    "v_pk_add_f32 v[108:109], v[108:109], v[208:209]\n\t" \
    "v_pk_add_f32 v[110:111], v[110:111], v[210:211]\n\t" \
    "ds_read_b128 v[188:191], %[la] offset:17408\n\t" \
    "s_waitcnt lgkmcnt(14)\n\t" \
    "v_pk_add_f32 v[112:113], v[112:113], v[212:213]\n\t" \
    "v_pk_add_f32 v[114:115], v[114:115], v[214:215]\n\t" \
    "ds_read_b128 v[192:195], %[la] offset:23552\n\t" \
    "s_waitcnt lgkmcnt(14)\n\t" \
    "v_pk_add_f32 v[116:117], v[116:117], v[216:217]\n\t" \
    "v_pk_add_f32 v[118:119], v[118:119], v[218:219]\n\t" \
    "ds_read_b128 v[196:199], %[la] offset:29696\n\t" \
    "s_waitcnt lgkmcnt(14)\n\t" \
    "v_pk_add_f32 v[100:101], v[100:101], v[220:221]\n\t" \
    "v_pk_add_f32 v[102:103], v[102:103], v[222:223]\n\t" \
    "s_waitcnt lgkmcnt(13)\n\t" \
    "v_pk_add_f32 v[104:105], v[104:105], v[224:225]\n\t" \
    "v_pk_add_f32 v[106:107], v[106:107], v[226:227]\n\t" \
    "s_waitcnt lgkmcnt(12)\n\t" \
    "v_pk_add_f32 v[108:109], v[108:109], v[228:229]\n\t" \
    "v_pk_add_f32 v[110:111], v[110:111], v[230:231]\n\t" \
    "s_waitcnt lgkmcnt(11)\n\t" \
    "v_pk_add_f32 v[112:113], v[112:113], v[232:233]\n\t" \
    "v_pk_add_f32 v[114:115], v[114:115], v[234:235]\n\t" \
    "s_waitcnt lgkmcnt(10)\n\t" \
    "v_pk_add_f32 v[116:117], v[116:117], v[236:237]\n\t" \
    "v_pk_add_f32 v[118:119], v[118:119], v[238:239]\n\t" \
    "s_waitcnt lgkmcnt(9)\n\t" \
    "v_pk_add_f32 v[100:101], v[100:101], v[160:161]\n\t" \
    "v_pk_add_f32 v[102:103], v[102:103], v[162:163]\n\t" \
    "s_waitcnt lgkmcnt(8)\n\t" \
    "v_pk_add_f32 v[104:105], v[104:105], v[164:165]\n\t" \
    "v_pk_add_f32 v[106:107], v[106:107], v[166:167]\n\t" \
    "s_waitcnt lgkmcnt(7)\n\t" \
    "v_pk_add_f32 v[108:109], v[108:109], v[168:169]\n\t" \
    "v_pk_add_f32 v[110:111], v[110:111], v[170:171]\n\t" \
    "s_waitcnt lgkmcnt(6)\n\t" \
    "v_pk_add_f32 v[112:113], v[112:113], v[172:173]\n\t" \
    "v_pk_add_f32 v[114:115], v[114:115], v[174:175]\n\t" \
    "s_waitcnt lgkmcnt(5)\n\t" \
    "v_pk_add_f32 v[116:117], v[116:117], v[176:177]\n\t" \
    "v_pk_add_f32 v[118:119], v[118:119], v[178:179]\n\t" \
    "s_waitcnt lgkmcnt(4)\n\t" \
    "v_pk_add_f32 v[100:101], v[100:101], v[180:181]\n\t" \
    "v_pk_add_f32 v[102:103], v[102:103], v[182:183]\n\t" \
    "s_waitcnt lgkmcnt(3)\n\t" \
    "v_pk_add_f32 v[104:105], v[104:105], v[184:185]\n\t" \
    "v_pk_add_f32 v[106:107], v[106:107], v[186:187]\n\t" \
    "s_waitcnt lgkmcnt(2)\n\t" \
    "v_pk_add_f32 v[108:109], v[108:109], v[188:189]\n\t" \
    "v_pk_add_f32 v[110:111], v[110:111], v[190:191]\n\t" \
    "s_waitcnt lgkmcnt(1)\n\t" \
    "v_pk_add_f32 v[112:113], v[112:113], v[192:193]\n\t" \
    "v_pk_add_f32 v[114:115], v[114:115], v[194:195]\n\t" \
    "s_waitcnt lgkmcnt(0)\n\t" \
    "v_pk_add_f32 v[116:117], v[116:117], v[196:197]\n\t" \
    "v_pk_add_f32 v[118:119], v[118:119], v[198:199]\n\t" \
    "ds_write_b32 %[fc], v98\n\t" \
    "v_sub_u32 %[la], %[l2], %[la]\n\t" \
    "v_cvt_pk_bf16_f32 v84, v100, v101\n\t" \
    "v_cvt_pk_bf16_f32 v85, v102, v103\n\t" \
    "v_cvt_pk_bf16_f32 v86, v104, v105\n\t" \
    "v_cvt_pk_bf16_f32 v87, v106, v107\n\t" \
    "v_cvt_pk_bf16_f32 v88, v108, v109\n\t" \
    "v_cvt_pk_bf16_f32 v89, v110, v111\n\t" \
    "v_cvt_pk_bf16_f32 v90, v112, v113\n\t" \
    "v_cvt_pk_bf16_f32 v91, v114, v115\n\t" \
    "v_cvt_pk_bf16_f32 v92, v116, v117\n\t" \
    "v_cvt_pk_bf16_f32 v93, v118, v119\n\t" \
    "v_mov_b32 v80, 0\n\t" \
    "v_mov_b32 v82, 0\n\t" \
    "v_dot2c_f32_bf16 v80, v84, v99\n\t" \
    "v_dot2c_f32_bf16 v82, v84, v84\n\t" \
    "v_dot2c_f32_bf16 v80, v85, v99\n\t" \
    "v_dot2c_f32_bf16 v82, v85, v85\n\t" \
    "v_dot2c_f32_bf16 v80, v86, v99\n\t" \
    "v_dot2c_f32_bf16 v82, v86, v86\n\t" \
    "v_dot2c_f32_bf16 v80, v87, v99\n\t" \
    "v_dot2c_f32_bf16 v82, v87, v87\n\t" \
    "v_dot2c_f32_bf16 v80, v88, v99\n\t" \
    "v_dot2c_f32_bf16 v82, v88, v88\n\t" \
    "v_dot2c_f32_bf16 v80, v89, v99\n\t" \
    "v_dot2c_f32_bf16 v82, v89, v89\n\t" \
    "v_dot2c_f32_bf16 v80, v90, v99\n\t" \
    "v_dot2c_f32_bf16 v82, v90, v90\n\t" \
    "v_dot2c_f32_bf16 v80, v91, v99\n\t" \
    "v_dot2c_f32_bf16 v82, v91, v91\n\t" \
    "v_dot2c_f32_bf16 v80, v92, v99\n\t" \
    "v_dot2c_f32_bf16 v82, v92, v92\n\t" \
    "v_dot2c_f32_bf16 v80, v93, v99\n\t" \
    "v_dot2c_f32_bf16 v82, v93, v93\n\t" \
    "s_nop 2\n\t" \
    "v_mov_b32 v81, v80\n\t" \
    "v_mov_b32 v83, v82\n\t" \
    "s_nop 1\n\t" \
    "v_permlane16_swap_b32 v80, v81\n\t" \
    "v_permlane16_swap_b32 v82, v83\n\t" \
    "v_add_f32 v80, v80, v81\n\t" \
    "v_add_f32 v82, v82, v83\n\t" \
    "v_mov_b32 v81, v80\n\t" \
    "v_mov_b32 v83, v82\n\t" \
    "s_nop 1\n\t" \
    "v_permlane32_swap_b32 v80, v81\n\t" \
    "v_permlane32_swap_b32 v82, v83\n\t" \
    "v_add_f32 v80, v80, v81\n\t" \
    "v_add_f32 v82, v82, v83\n\t" \
    "v_min_u32 v80, -2, v80\n\t" \
    "v_min_u32 v81, -2, v82\n\t" \
    "s_mov_b64 exec, 0xffff\n\t" \
    "buffer_store_dwordx2 v[80:81], %[l8], %[rs], s50 offen sc1\n\t" \
    "s_mov_b64 exec, -1\n\t" \
    "s_add_u32 s46, s43, 0x2c00000\n\t" \
    "s_add_u32 s47, s43, 0x1c00000\n\t" \
    "buffer_load_dwordx2 v[120:121], %[t0], %[rs], s46 offen sc1\n\t" \
    "buffer_load_dwordx2 v[122:123], %[t0], %[rs], s47 offen sc1\n\t" \
    "buffer_load_dwordx2 v[124:125], %[t1], %[rs], s46 offen sc1\n\t" \
    "buffer_load_dwordx2 v[126:127], %[t1], %[rs], s47 offen sc1\n\t" \
    "buffer_load_dwordx2 v[128:129], %[t2], %[rs], s46 offen sc1\n\t" \
    "buffer_load_dwordx2 v[130:131], %[t2], %[rs], s47 offen sc1\n\t" \
    "buffer_load_dwordx2 v[132:133], %[t3], %[rs], s46 offen sc1\n\t" \
    "buffer_load_dwordx2 v[134:135], %[t3], %[rs], s47 offen sc1\n\t" \
    "buffer_load_dwordx2 v[136:137], %[t4], %[rs], s46 offen sc1\n\t" \
    "buffer_load_dwordx2 v[138:139], %[t4], %[rs], s47 offen sc1\n\t" \
    "s_nop 0\n\t" \
    "v_permlane16_swap_b32 v84, v86\n\t" \
    "v_permlane16_swap_b32 v85, v87\n\t" \
    "v_permlane16_swap_b32 v88, v90\n\t" \
    "v_permlane16_swap_b32 v89, v91\n\t" \
    "s_sleep 4\n\t" \
    ".Lf05_%=:\n\t" \
    "buffer_load_dwordx2 v[94:95], %[l8], %[rs], s49 offen sc1\n\t" \
    "buffer_load_dwordx2 v[96:97], %[l8], %[rs], s49 offen offset:512 sc1\n\t" \
    "s_waitcnt vmcnt(0)\n\t" \
    "v_max_u32 v80, v94, v95\n\t" \
    "v_max3_u32 v80, v96, v97, v80\n\t" \
    "v_cmp_eq_u32 vcc, -1, v80\n\t" \
    "s_cbranch_vccz .Lf06_%=\n\t" \
    "s_cmp_eq_u32 s41, 0\n\t" \
    "s_cbranch_scc1 .Lf06_%=\n\t" \
    "s_sub_u32 s41, s41, 1\n\t" \
    "s_branch .Lf05_%=\n\t" \
    ".Lf06_%=:\n\t" \
    "v_add_f32 v80, v94, v96\n\t" \
    "v_add_f32 v82, v95, v97\n\t" \
    "v_mov_b32 v81, v80\n\t" \
    "v_mov_b32 v83, v82\n\t" \
    "s_nop 1\n\t" \
    "v_permlane16_swap_b32 v80, v81\n\t" \
    "v_permlane16_swap_b32 v82, v83\n\t" \
    "v_add_f32 v80, v80, v81\n\t" \
    "v_add_f32 v82, v82, v83\n\t" \
    "v_mov_b32 v81, v80\n\t" \
    "v_mov_b32 v83, v82\n\t" \
    "s_nop 1\n\t" \
    "v_permlane32_swap_b32 v80, v81\n\t" \
    "v_permlane32_swap_b32 v82, v83\n\t" \
    "v_add_f32 v80, v80, v81\n\t" \
    "v_add_f32 v82, v82, v83\n\t" \
    "v_mul_f32 v80, 0x3acccccd, v80\n\t" \
    "v_mul_f32 v82, 0x3acccccd, v82\n\t" \
    "v_fma_f32 v82, -v80, v80, v82\n\t" \
    "v_cvt_pk_bf16_f32 v80, v80, v80\n\t" \
    "v_and_b32 v80, 0xffff0000, v80\n\t" \
    "v_max_f32 v82, 0, v82\n\t" \
    "v_cvt_pk_bf16_f32 v82, v82, v82\n\t" \
    "v_and_b32 v82, 0xffff0000, v82\n\t" \
    "v_add_f32 v82, 0x3727c5ac, v82\n\t" \
    "v_cvt_pk_bf16_f32 v82, v82, v82\n\t" \
    "v_and_b32 v82, 0xffff0000, v82\n\t" \
    "v_sqrt_f32 v82, v82\n\t" \
    "s_nop 0\n\t" \
    "v_cvt_pk_bf16_f32 v82, v82, v82\n\t" \
    "v_and_b32 v82, 0xffff0000, v82\n\t" \
    "v_rcp_f32 v82, v82\n\t" \
    "s_nop 0\n\t" \
    "v_mul_f32 v80, -v80, v82\n\t" \
    "v_fma_f32 v100, v100, v82, v80\n\t" \
    "v_fma_f32 v101, v101, v82, v80\n\t" \
    "v_fma_f32 v102, v102, v82, v80\n\t" \
    "v_fma_f32 v103, v103, v82, v80\n\t" \
    "v_fma_f32 v104, v104, v82, v80\n\t" \
    "v_fma_f32 v105, v105, v82, v80\n\t" \
    "v_fma_f32 v106, v106, v82, v80\n\t" \
    "v_fma_f32 v107, v107, v82, v80\n\t" \
    "v_fma_f32 v108, v108, v82, v80\n\t" \
    "v_fma_f32 v109, v109, v82, v80\n\t" \
    "v_fma_f32 v110, v110, v82, v80\n\t" \
    "v_fma_f32 v111, v111, v82, v80\n\t" \
    "v_fma_f32 v112, v112, v82, v80\n\t" \
    "v_fma_f32 v113, v113, v82, v80\n\t" \
    "v_fma_f32 v114, v114, v82, v80\n\t" \
    "v_fma_f32 v115, v115, v82, v80\n\t" \
    "v_fma_f32 v116, v116, v82, v80\n\t" \
    "v_fma_f32 v117, v117, v82, v80\n\t" \
    "v_fma_f32 v118, v118, v82, v80\n\t" \
    "v_fma_f32 v119, v119, v82, v80\n\t" \
    "v_cvt_pk_bf16_f32 v64, v100, v101\n\t" \
    "v_cvt_pk_bf16_f32 v65, v102, v103\n\t" \
    "v_cvt_pk_bf16_f32 v66, v104, v105\n\t" \
    "v_cvt_pk_bf16_f32 v67, v106, v107\n\t" \
    "v_cvt_pk_bf16_f32 v68, v108, v109\n\t" \
    "v_cvt_pk_bf16_f32 v69, v110, v111\n\t" \
    "v_cvt_pk_bf16_f32 v70, v112, v113\n\t" \
    "v_cvt_pk_bf16_f32 v71, v114, v115\n\t" \
    "v_cvt_pk_bf16_f32 v72, v116, v117\n\t" \
    "v_cvt_pk_bf16_f32 v73, v118, v119\n\t" \
    "s_nop 0\n\t" \
    "v_permlane16_swap_b32 v64, v66\n\t" \
    "v_permlane16_swap_b32 v65, v67\n\t" \
    "v_permlane16_swap_b32 v68, v70\n\t" \
    "v_permlane16_swap_b32 v69, v71\n\t" \
    "buffer_store_dwordx4 v[64:67], %[o16], %[rn], s51 offen sc1\n\t" \
    "buffer_store_dwordx4 v[68:71], %[o17], %[rn], s51 offen sc1\n\t" \
    "buffer_store_dwordx2 v[72:73], %[t4], %[rn], s51 offen sc1\n\t" \
    "buffer_store_dwordx4 v[84:87], %[o16], %[rn], s52 offen sc1\n\t" \
    "buffer_store_dwordx4 v[88:91], %[o17], %[rn], s52 offen sc1\n\t" \
    "buffer_store_dwordx2 v[92:93], %[t4], %[rn], s52 offen sc1\n\t" \
    "s_waitcnt vmcnt(0)\n\t" \
    "buffer_store_dwordx4 v[76:79], %[fo], %[rn], %[fs] offen sc1\n\t" \
    "s_add_u32 s40, s40, 2\n\t" \
    "s_cmp_lt_u32 s40, 128\n\t" \
    "s_cbranch_scc1 .Lf01_%=\n\t" \
    "s_branch .Lend%=\n\t" \
    ".Lend%=:\n\t" \
    "s_waitcnt vmcnt(0) lgkmcnt(0)\n\t" \
    "s_mov_b32 %[bud], s41\n\t"
#define ASM_W4_FIN1 \
    "s_mov_b32 s40, %[mt]\n\t" \
    "s_mov_b32 s41, 0x40000\n\t" \
    "s_mov_b32 s43, 0\n\t" \
    "v_mov_b32 v99, 0x3f803f80\n\t" \
    "s_setprio 3\n\t" \
    "s_add_u32 s46, s43, 0x2c00000\n\t" \
    "s_add_u32 s47, s43, 0x1c00000\n\t" \
    "buffer_load_dwordx2 v[120:121], %[t0], %[rs], s46 offen sc1\n\t" \
    "buffer_load_dwordx2 v[122:123], %[t0], %[rs], s47 offen sc1\n\t" \
    "buffer_load_dwordx2 v[124:125], %[t1], %[rs], s46 offen sc1\n\t" \
    "buffer_load_dwordx2 v[126:127], %[t1], %[rs], s47 offen sc1\n\t" \
    "buffer_load_dwordx2 v[128:129], %[t2], %[rs], s46 offen sc1\n\t" \
    "buffer_load_dwordx2 v[130:131], %[t2], %[rs], s47 offen sc1\n\t" \
    "buffer_load_dwordx2 v[132:133], %[t3], %[rs], s46 offen sc1\n\t" \
    "buffer_load_dwordx2 v[134:135], %[t3], %[rs], s47 offen sc1\n\t" \
    "buffer_load_dwordx2 v[136:137], %[t4], %[rs], s46 offen sc1\n\t" \
    "buffer_load_dwordx2 v[138:139], %[t4], %[rs], s47 offen sc1\n\t" \
    ".Lf11_%=:\n\t" \
    ".Lf12_%=:\n\t" \
    "s_waitcnt vmcnt(0)\n\t" \
    "v_max_u32 v80, v120, v121\n\t" \
    "v_max3_u32 v80, v122, v123, v80\n\t" \
    "v_max3_u32 v80, v124, v125, v80\n\t" \
    "v_max3_u32 v80, v126, v127, v80\n\t" \
    "v_max3_u32 v80, v128, v129, v80\n\t" \
    "v_max3_u32 v80, v130, v131, v80\n\t" \
    "v_max3_u32 v80, v132, v133, v80\n\t" \
    "v_max3_u32 v80, v134, v135, v80\n\t" \
    "v_max3_u32 v80, v136, v137, v80\n\t" \
    "v_max3_u32 v80, v138, v139, v80\n\t" \
    "v_cmp_eq_u32 vcc, -1, v80\n\t" \
    "s_cbranch_vccz .Lf13_%=\n\t" \
    "s_cmp_eq_u32 s41, 0\n\t" \
    "s_cbranch_scc1 .Lf13_%=\n\t" \
    "s_sub_u32 s41, s41, 1\n\t" \
    "s_sleep 8\n\t" \
    "s_add_u32 s46, s43, 0x2c00000\n\t" \
    "s_add_u32 s47, s43, 0x1c00000\n\t" \
    "buffer_load_dwordx2 v[120:121], %[t0], %[rs], s46 offen sc1\n\t" \
    "buffer_load_dwordx2 v[122:123], %[t0], %[rs], s47 offen sc1\n\t" \
    "buffer_load_dwordx2 v[124:125], %[t1], %[rs], s46 offen sc1\n\t" \
    "buffer_load_dwordx2 v[126:127], %[t1], %[rs], s47 offen sc1\n\t" \
    "buffer_load_dwordx2 v[128:129], %[t2], %[rs], s46 offen sc1\n\t" \
    "buffer_load_dwordx2 v[130:131], %[t2], %[rs], s47 offen sc1\n\t" \
    "buffer_load_dwordx2 v[132:133], %[t3], %[rs], s46 offen sc1\n\t" \
    "buffer_load_dwordx2 v[134:135], %[t3], %[rs], s47 offen sc1\n\t" \
    "buffer_load_dwordx2 v[136:137], %[t4], %[rs], s46 offen sc1\n\t" \
    "buffer_load_dwordx2 v[138:139], %[t4], %[rs], s47 offen sc1\n\t" \
    "s_waitcnt vmcnt(0)\n\t" \
    "s_branch .Lf12_%=\n\t" \
    ".Lf13_%=:\n\t" \
    "v_lshlrev_b32 v140, 16, v120\n\t" \
    "v_and_b32 v141, 0xffff0000, v120\n\t" \
    "v_lshlrev_b32 v142, 16, v121\n\t" \
    "v_and_b32 v143, 0xffff0000, v121\n\t" \
    "v_lshlrev_b32 v80, 16, v122\n\t" \
    "v_and_b32 v81, 0xffff0000, v122\n\t" \
    "v_lshlrev_b32 v82, 16, v123\n\t" \
    "v_and_b32 v83, 0xffff0000, v123\n\t" \
    "v_add_f32 v140, v140, v80\n\t" \
    "v_add_f32 v141, v141, v81\n\t" \
    "v_add_f32 v142, v142, v82\n\t" \
    "v_add_f32 v143, v143, v83\n\t" \
    "v_lshlrev_b32 v144, 16, v124\n\t" \
    "v_and_b32 v145, 0xffff0000, v124\n\t" \
    "v_lshlrev_b32 v146, 16, v125\n\t" \
    "v_and_b32 v147, 0xffff0000, v125\n\t" \
    "v_lshlrev_b32 v80, 16, v126\n\t" \
    "v_and_b32 v81, 0xffff0000, v126\n\t" \
    "v_lshlrev_b32 v82, 16, v127\n\t" \
    "v_and_b32 v83, 0xffff0000, v127\n\t" \
    "v_add_f32 v144, v144, v80\n\t" \
    "v_add_f32 v145, v145, v81\n\t" \
    "v_add_f32 v146, v146, v82\n\t" \
    "v_add_f32 v147, v147, v83\n\t" \
    "v_lshlrev_b32 v148, 16, v128\n\t" \
    "v_and_b32 v149, 0xffff0000, v128\n\t" \
    "v_lshlrev_b32 v150, 16, v129\n\t" \
    "v_and_b32 v151, 0xffff0000, v129\n\t" \
    "v_lshlrev_b32 v80, 16, v130\n\t" \
    "v_and_b32 v81, 0xffff0000, v130\n\t" \
    "v_lshlrev_b32 v82, 16, v131\n\t" \
    "v_and_b32 v83, 0xffff0000, v131\n\t" \
    "v_add_f32 v148, v148, v80\n\t" \
    "v_add_f32 v149, v149, v81\n\t" \
    "v_add_f32 v150, v150, v82\n\t" \
    "v_add_f32 v151, v151, v83\n\t" \
    "v_lshlrev_b32 v152, 16, v132\n\t" \
    "v_and_b32 v153, 0xffff0000, v132\n\t" \
    "v_lshlrev_b32 v154, 16, v133\n\t" \
    "v_and_b32 v155, 0xffff0000, v133\n\t" \
    "v_lshlrev_b32 v80, 16, v134\n\t" \
    "v_and_b32 v81, 0xffff0000, v134\n\t" \
    "v_lshlrev_b32 v82, 16, v135\n\t" \
    "v_and_b32 v83, 0xffff0000, v135\n\t" \
    "v_add_f32 v152, v152, v80\n\t" \
    "v_add_f32 v153, v153, v81\n\t" \
    "v_add_f32 v154, v154, v82\n\t" \
    "v_add_f32 v155, v155, v83\n\t" \
    "v_lshlrev_b32 v156, 16, v136\n\t" \
    "v_and_b32 v157, 0xffff0000, v136\n\t" \
    "v_lshlrev_b32 v158, 16, v137\n\t" \
    "v_and_b32 v159, 0xffff0000, v137\n\t" \
    "v_lshlrev_b32 v80, 16, v138\n\t" \
    "v_and_b32 v81, 0xffff0000, v138\n\t" \
    "v_lshlrev_b32 v82, 16, v139\n\t" \
    "v_and_b32 v83, 0xffff0000, v139\n\t" \
    "v_add_f32 v156, v156, v80\n\t" \
    "v_add_f32 v157, v157, v81\n\t" \
    "v_add_f32 v158, v158, v82\n\t" \
    "v_add_f32 v159, v159, v83\n\t" \
    "s_mov_b32 s42, s43\n\t" \
    "s_lshr_b32 s48, s40, 1\n\t" \
    "s_add_u32 s48, s48, 1\n\t" \
    "s_mul_i32 s43, s48, 0x14000\n\t" \
    "s_lshr_b32 s44, s48, 4\n\t" \
    "s_mul_i32 s44, s44, 0xc2000\n\t" \
    "s_add_u32 s43, s43, s44\n\t" \
    "s_mul_i32 s49, s40, 20480\n\t" \
    "s_add_u32 s49, s49, %[co]\n\t" \
    "s_add_u32 s44, s40, 1\n\t" \
    "v_mov_b32 v98, s44\n\t" \
    ".Lf14_%=:\n\t" \
    "s_nop 7\n\t" \
    "ds_read_b32 v80, %[fa]\n\t" \
    "s_waitcnt lgkmcnt(0)\n\t" \
    "v_cmp_ge_u32 vcc, s40, v80\n\t" \
    "s_cbranch_vccnz .Lf14_%=\n\t" \
    "ds_read_b128 v[160:163], %[la] offset:0\n\t" \
    "ds_read_b128 v[164:167], %[la] offset:6144\n\t" \
    "ds_read_b128 v[168:171], %[la] offset:12288\n\t" \
    "ds_read_b128 v[172:175], %[la] offset:18432\n\t" \
    "ds_read_b128 v[176:179], %[la] offset:24576\n\t" \
    "ds_read_b128 v[180:183], %[la] offset:1024\n\t" \
    "ds_read_b128 v[184:187], %[la] offset:7168\n\t" \
    "ds_read_b128 v[188:191], %[la] offset:13312\n\t" \
    "ds_read_b128 v[192:195], %[la] offset:19456\n\t" \
    "ds_read_b128 v[196:199], %[la] offset:25600\n\t" \
    "ds_read_b128 v[200:203], %[la] offset:2048\n\t" \
    "ds_read_b128 v[204:207], %[la] offset:8192\n\t" \
    "ds_read_b128 v[208:211], %[la] offset:14336\n\t" \
    "ds_read_b128 v[212:215], %[la] offset:20480\n\t" \
    "ds_read_b128 v[216:219], %[la] offset:26624\n\t" \
    "s_waitcnt lgkmcnt(14)\n\t" \
    "v_pk_add_f32 v[100:101], v[140:141], v[160:161]\n\t" \
    "v_pk_add_f32 v[102:103], v[142:143], v[162:163]\n\t" \
    "ds_read_b128 v[220:223], %[la] offset:3072\n\t" \
    "s_waitcnt lgkmcnt(14)\n\t" \
    "v_pk_add_f32 v[104:105], v[144:145], v[164:165]\n\t" \
    "v_pk_add_f32 v[106:107], v[146:147], v[166:167]\n\t" \
    "ds_read_b128 v[224:227], %[la] offset:9216\n\t" \
    "s_waitcnt lgkmcnt(14)\n\t" \
    "v_pk_add_f32 v[108:109], v[148:149], v[168:169]\n\t" \
    "v_pk_add_f32 v[110:111], v[150:151], v[170:171]\n\t" \
    "ds_read_b128 v[228:231], %[la] offset:15360\n\t" \
    "s_waitcnt lgkmcnt(14)\n\t" \
    "v_pk_add_f32 v[112:113], v[152:153], v[172:173]\n\t" \
    "v_pk_add_f32 v[114:115], v[154:155], v[174:175]\n\t" \
    "ds_read_b128 v[232:235], %[la] offset:21504\n\t" \
    "s_waitcnt lgkmcnt(14)\n\t" \
    "v_pk_add_f32 v[116:117], v[156:157], v[176:177]\n\t" \
    "v_pk_add_f32 v[118:119], v[158:159], v[178:179]\n\t" \
    "ds_read_b128 v[236:239], %[la] offset:27648\n\t" \
    "s_waitcnt lgkmcnt(14)\n\t" \
    "v_pk_add_f32 v[100:101], v[100:101], v[180:181]\n\t" \
    "v_pk_add_f32 v[102:103], v[102:103], v[182:183]\n\t" \
    "ds_read_b128 v[160:163], %[la] offset:4096\n\t" \
    "s_waitcnt lgkmcnt(14)\n\t" \
    "v_pk_add_f32 v[104:105], v[104:105], v[184:185]\n\t" \
    "v_pk_add_f32 v[106:107], v[106:107], v[186:187]\n\t" \
    "ds_read_b128 v[164:167], %[la] offset:10240\n\t" \
    "s_waitcnt lgkmcnt(14)\n\t" \
    "v_pk_add_f32 v[108:109], v[108:109], v[188:189]\n\t" \
    "v_pk_add_f32 v[110:111], v[110:111], v[190:191]\n\t" \
    "ds_read_b128 v[168:171], %[la] offset:16384\n\t" \
    "s_waitcnt lgkmcnt(14)\n\t" \
    "v_pk_add_f32 v[112:113], v[112:113], v[192:193]\n\t" \
    "v_pk_add_f32 v[114:115], v[114:115], v[194:195]\n\t" \
    "ds_read_b128 v[172:175], %[la] offset:22528\n\t" \
    "s_waitcnt lgkmcnt(14)\n\t" \
    "v_pk_add_f32 v[116:117], v[116:117], v[196:197]\n\t" \
    "v_pk_add_f32 v[118:119], v[118:119], v[198:199]\n\t" \
    "ds_read_b128 v[176:179], %[la] offset:28672\n\t" \
    "s_waitcnt lgkmcnt(14)\n\t" \
    "v_pk_add_f32 v[100:101], v[100:101], v[200:201]\n\t" \
    "v_pk_add_f32 v[102:103], v[102:103], v[202:203]\n\t" \
    "ds_read_b128 v[180:183], %[la] offset:5120\n\t" \
    "s_waitcnt lgkmcnt(14)\n\t" \
    "v_pk_add_f32 v[104:105], v[104:105], v[204:205]\n\t" \
    "v_pk_add_f32 v[106:107], v[106:107], v[206:207]\n\t" \
    "ds_read_b128 v[184:187], %[la] offset:11264\n\t" \
    "s_waitcnt lgkmcnt(14)\n\t" \
    "v_pk_add_f32 v[108:109], v[108:109], v[208:209]\n\t" \
    "v_pk_add_f32 v[110:111], v[110:111], v[210:211]\n\t" \
    "ds_read_b128 v[188:191], %[la] offset:17408\n\t" \
    "s_waitcnt lgkmcnt(14)\n\t" \
    "v_pk_add_f32 v[112:113], v[112:113], v[212:213]\n\t" \
    "v_pk_add_f32 v[114:115], v[114:115], v[214:215]\n\t" \
    "ds_read_b128 v[192:195], %[la] offset:23552\n\t" \
    "s_waitcnt lgkmcnt(14)\n\t" \
    "v_pk_add_f32 v[116:117], v[116:117], v[216:217]\n\t" \
    "v_pk_add_f32 v[118:119], v[118:119], v[218:219]\n\t" \
    "ds_read_b128 v[196:199], %[la] offset:29696\n\t" \
    "s_waitcnt lgkmcnt(14)\n\t" \
    "v_pk_add_f32 v[100:101], v[100:101], v[220:221]\n\t" \
    "v_pk_add_f32 v[102:103], v[102:103], v[222:223]\n\t" \
    "s_waitcnt lgkmcnt(13)\n\t" \
    "v_pk_add_f32 v[104:105], v[104:105], v[224:225]\n\t" \
    "v_pk_add_f32 v[106:107], v[106:107], v[226:227]\n\t" \
    "s_waitcnt lgkmcnt(12)\n\t" \
    "v_pk_add_f32 v[108:109], v[108:109], v[228:229]\n\t" \
    "v_pk_add_f32 v[110:111], v[110:111], v[230:231]\n\t" \
    "s_waitcnt lgkmcnt(11)\n\t" \
    "v_pk_add_f32 v[112:113], v[112:113], v[232:233]\n\t" \
    "v_pk_add_f32 v[114:115], v[114:115], v[234:235]\n\t" \
    "s_waitcnt lgkmcnt(10)\n\t" \
    "v_pk_add_f32 v[116:117], v[116:117], v[236:237]\n\t" \
    "v_pk_add_f32 v[118:119], v[118:119], v[238:239]\n\t" \
    "s_waitcnt lgkmcnt(9)\n\t" \
    "v_pk_add_f32 v[100:101], v[100:101], v[160:161]\n\t" \
    "v_pk_add_f32 v[102:103], v[102:103], v[162:163]\n\t" \
    "s_waitcnt lgkmcnt(8)\n\t" \
    "v_pk_add_f32 v[104:105], v[104:105], v[164:165]\n\t" \
    "v_pk_add_f32 v[106:107], v[106:107], v[166:167]\n\t" \
    "s_waitcnt lgkmcnt(7)\n\t" \
    "v_pk_add_f32 v[108:109], v[108:109], v[168:169]\n\t" \
    "v_pk_add_f32 v[110:111], v[110:111], v[170:171]\n\t" \
    "s_waitcnt lgkmcnt(6)\n\t" \
    "v_pk_add_f32 v[112:113], v[112:113], v[172:173]\n\t" \
    "v_pk_add_f32 v[114:115], v[114:115], v[174:175]\n\t" \
    "s_waitcnt lgkmcnt(5)\n\t" \
    "v_pk_add_f32 v[116:117], v[116:117], v[176:177]\n\t" \
    "v_pk_add_f32 v[118:119], v[118:119], v[178:179]\n\t" \
    "s_waitcnt lgkmcnt(4)\n\t" \
    "v_pk_add_f32 v[100:101], v[100:101], v[180:181]\n\t" \
    "v_pk_add_f32 v[102:103], v[102:103], v[182:183]\n\t" \
    "s_waitcnt lgkmcnt(3)\n\t" \
    "v_pk_add_f32 v[104:105], v[104:105], v[184:185]\n\t" \
    "v_pk_add_f32 v[106:107], v[106:107], v[186:187]\n\t" \
    "s_waitcnt lgkmcnt(2)\n\t" \
    "v_pk_add_f32 v[108:109], v[108:109], v[188:189]\n\t" \
    "v_pk_add_f32 v[110:111], v[110:111], v[190:191]\n\t" \
    "s_waitcnt lgkmcnt(1)\n\t" \
    "v_pk_add_f32 v[112:113], v[112:113], v[192:193]\n\t" \
    "v_pk_add_f32 v[114:115], v[114:115], v[194:195]\n\t" \
    "s_waitcnt lgkmcnt(0)\n\t" \
    "v_pk_add_f32 v[116:117], v[116:117], v[196:197]\n\t" \
    "v_pk_add_f32 v[118:119], v[118:119], v[198:199]\n\t" \
    "ds_write_b32 %[fc], v98\n\t" \
    "v_sub_u32 %[la], %[l2], %[la]\n\t" \
    "v_cvt_pk_bf16_f32 v84, v100, v101\n\t" \
    "v_cvt_pk_bf16_f32 v85, v102, v103\n\t" \
    "v_cvt_pk_bf16_f32 v86, v104, v105\n\t" \
    "v_cvt_pk_bf16_f32 v87, v106, v107\n\t" \
    "v_cvt_pk_bf16_f32 v88, v108, v109\n\t" \
    "v_cvt_pk_bf16_f32 v89, v110, v111\n\t" \
    "v_cvt_pk_bf16_f32 v90, v112, v113\n\t" \
    "v_cvt_pk_bf16_f32 v91, v114, v115\n\t" \
    "v_cvt_pk_bf16_f32 v92, v116, v117\n\t" \
    "v_cvt_pk_bf16_f32 v93, v118, v119\n\t" \
    "s_nop 0\n\t" \
    "v_permlane16_swap_b32 v84, v86\n\t" \
    "v_permlane16_swap_b32 v85, v87\n\t" \
    "v_permlane16_swap_b32 v88, v90\n\t" \
    "v_permlane16_swap_b32 v89, v91\n\t" \
    "buffer_store_dwordx4 v[84:87], %[q16], %[ro], s49 offen nt\n\t" \
    "buffer_store_dwordx4 v[88:91], %[q16], %[ro], s49 offen offset:64 nt\n\t" \
    "buffer_store_dwordx2 v[92:93], %[q8], %[ro], s49 offen offset:128 nt\n\t" \
    "s_cmp_gt_u32 s40, 125\n\t" \
    "s_cbranch_scc1 .Lf15_%=\n\t" \
    "s_add_u32 s46, s43, 0x2c00000\n\t" \
    "s_add_u32 s47, s43, 0x1c00000\n\t" \
    "buffer_load_dwordx2 v[120:121], %[t0], %[rs], s46 offen sc1\n\t" \
    "buffer_load_dwordx2 v[122:123], %[t0], %[rs], s47 offen sc1\n\t" \
    "buffer_load_dwordx2 v[124:125], %[t1], %[rs], s46 offen sc1\n\t" \
    "buffer_load_dwordx2 v[126:127], %[t1], %[rs], s47 offen sc1\n\t" \
    "buffer_load_dwordx2 v[128:129], %[t2], %[rs], s46 offen sc1\n\t" \
    "buffer_load_dwordx2 v[130:131], %[t2], %[rs], s47 offen sc1\n\t" \
    "buffer_load_dwordx2 v[132:133], %[t3], %[rs], s46 offen sc1\n\t" \
    "buffer_load_dwordx2 v[134:135], %[t3], %[rs], s47 offen sc1\n\t" \
    "buffer_load_dwordx2 v[136:137], %[t4], %[rs], s46 offen sc1\n\t" \
    "buffer_load_dwordx2 v[138:139], %[t4], %[rs], s47 offen sc1\n\t" \
    ".Lf15_%=:\n\t" \
    "s_add_u32 s40, s40, 2\n\t" \
    "s_cmp_lt_u32 s40, 128\n\t" \
    "s_cbranch_scc1 .Lf11_%=\n\t" \
    "s_branch .Lend%=\n\t" \
    ".Lend%=:\n\t" \
    "s_waitcnt vmcnt(0) lgkmcnt(0)\n\t" \
    "s_mov_b32 %[bud], s41\n\t"
#define ASM_W1_LOOP \
    "s_mov_b32 s40, 0\n\t" \
    "s_mov_b32 s41, 0x20000\n\t" \
    "s_add_u32 m0, %[wm], 0x0\n\t" \
    "s_add_u32 s44, %[wo], 0x0\n\t" \
    "buffer_load_dwordx4 v176, %[wr], s44 offen nt lds\n\t" \
    "s_add_u32 m0, %[wm], 0x2000\n\t" \
    "s_add_u32 s44, %[wo], 0xa000\n\t" \
    "buffer_load_dwordx4 v176, %[wr], s44 offen nt lds\n\t" \
    "s_add_u32 m0, %[wm], 0x4000\n\t" \
    "s_add_u32 s44, %[wo], 0x14000\n\t" \
    "buffer_load_dwordx4 v176, %[wr], s44 offen nt lds\n\t" \
    "s_add_u32 m0, %[wm], 0x6000\n\t" \
    "s_add_u32 s44, %[wo], 0x1e000\n\t" \
    "buffer_load_dwordx4 v176, %[wr], s44 offen nt lds\n\t" \
    "s_add_u32 m0, %[wm], 0x8000\n\t" \
    "s_add_u32 s44, %[wo], 0x28000\n\t" \
    "buffer_load_dwordx4 v176, %[wr], s44 offen nt lds\n\t" \
    "s_add_u32 m0, %[wm], 0xa000\n\t" \
    "s_add_u32 s44, %[wo], 0x32000\n\t" \
    "buffer_load_dwordx4 v176, %[wr], s44 offen nt lds\n\t" \
    "s_add_u32 m0, %[wm], 0xc000\n\t" \
    "s_add_u32 s44, %[wo], 0x3c000\n\t" \
    "buffer_load_dwordx4 v176, %[wr], s44 offen nt lds\n\t" \
    "s_add_u32 m0, %[wm], 0xe000\n\t" \
    "s_add_u32 s44, %[wo], 0x46000\n\t" \
    "buffer_load_dwordx4 v176, %[wr], s44 offen nt lds\n\t" \
    "s_add_u32 m0, %[wm], 0x10000\n\t" \
    "s_add_u32 s44, %[wo], 0x50000\n\t" \
    "buffer_load_dwordx4 v176, %[wr], s44 offen nt lds\n\t" \
    "s_add_u32 m0, %[wm], 0x12000\n\t" \
    "s_add_u32 s44, %[wo], 0x5a000\n\t" \
    "buffer_load_dwordx4 v176, %[wr], s44 offen nt lds\n\t" \
    "s_add_u32 m0, %[wm], 0x14000\n\t" \
    "s_add_u32 s44, %[wo], 0x64000\n\t" \
    "buffer_load_dwordx4 v176, %[wr], s44 offen nt lds\n\t" \
    "s_add_u32 m0, %[wm], 0x16000\n\t" \
    "s_add_u32 s44, %[wo], 0x6e000\n\t" \
    "buffer_load_dwordx4 v176, %[wr], s44 offen nt lds\n\t" \
    "s_add_u32 m0, %[wm], 0x18000\n\t" \
    "s_add_u32 s44, %[wo], 0x78000\n\t" \
    "buffer_load_dwordx4 v176, %[wr], s44 offen nt lds\n\t" \
    "s_add_u32 m0, %[wm], 0x1a000\n\t" \
    "s_add_u32 s44, %[wo], 0x82000\n\t" \
    "buffer_load_dwordx4 v176, %[wr], s44 offen nt lds\n\t" \
    "s_add_u32 m0, %[wm], 0x1c000\n\t" \
    "s_add_u32 s44, %[wo], 0x8c000\n\t" \
    "buffer_load_dwordx4 v176, %[wr], s44 offen nt lds\n\t" \
    "s_add_u32 m0, %[wm], 0x1e000\n\t" \
    "s_add_u32 s44, %[wo], 0x96000\n\t" \
    "buffer_load_dwordx4 v176, %[wr], s44 offen nt lds\n\t" \
    "s_add_u32 m0, %[wm], 0x20000\n\t" \
    "s_add_u32 s44, %[wo], 0xa0000\n\t" \
    "buffer_load_dwordx4 v176, %[wr], s44 offen nt lds\n\t" \
    "s_add_u32 m0, %[wm], 0x22000\n\t" \
    "s_add_u32 s44, %[wo], 0xaa000\n\t" \
    "buffer_load_dwordx4 v176, %[wr], s44 offen nt lds\n\t" \
    "s_add_u32 m0, %[wm], 0x24000\n\t" \
    "s_add_u32 s44, %[wo], 0xb4000\n\t" \
    "buffer_load_dwordx4 v176, %[wr], s44 offen nt lds\n\t" \
    "s_add_u32 m0, %[wm], 0x26000\n\t" \
    "s_add_u32 s44, %[wo], 0xbe000\n\t" \
    "buffer_load_dwordx4 v176, %[wr], s44 offen nt lds\n\t" \
    "s_waitcnt vmcnt(0) lgkmcnt(0)\n\t" \
    "s_barrier \n\t" \
    "v_add_u32 v178, 0x0, v177\n\t" \
    "ds_read_b64_tr_b16 v[0:1], v178 offset:0\n\t" \
    "ds_read_b64_tr_b16 v[2:3], v178 offset:1024\n\t" \
    "ds_read_b64_tr_b16 v[4:5], v178 offset:8192\n\t" \
    "ds_read_b64_tr_b16 v[6:7], v178 offset:9216\n\t" \
    "ds_read_b64_tr_b16 v[8:9], v178 offset:16384\n\t" \
    "ds_read_b64_tr_b16 v[10:11], v178 offset:17408\n\t" \
    "ds_read_b64_tr_b16 v[12:13], v178 offset:24576\n\t" \
    "ds_read_b64_tr_b16 v[14:15], v178 offset:25600\n\t" \
    "v_add_u32 v178, 0x8000, v177\n\t" \
    "ds_read_b64_tr_b16 v[16:17], v178 offset:0\n\t" \
    "ds_read_b64_tr_b16 v[18:19], v178 offset:1024\n\t" \
    "ds_read_b64_tr_b16 v[20:21], v178 offset:8192\n\t" \
    "ds_read_b64_tr_b16 v[22:23], v178 offset:9216\n\t" \
    "ds_read_b64_tr_b16 v[24:25], v178 offset:16384\n\t" \
    "ds_read_b64_tr_b16 v[26:27], v178 offset:17408\n\t" \
    "ds_read_b64_tr_b16 v[28:29], v178 offset:24576\n\t" \
    "ds_read_b64_tr_b16 v[30:31], v178 offset:25600\n\t" \
    "v_add_u32 v178, 0x10000, v177\n\t" \
    "ds_read_b64_tr_b16 v[32:33], v178 offset:0\n\t" \
    "ds_read_b64_tr_b16 v[34:35], v178 offset:1024\n\t" \
    "ds_read_b64_tr_b16 v[36:37], v178 offset:8192\n\t" \
    "ds_read_b64_tr_b16 v[38:39], v178 offset:9216\n\t" \
    "ds_read_b64_tr_b16 v[40:41], v178 offset:16384\n\t" \
    "ds_read_b64_tr_b16 v[42:43], v178 offset:17408\n\t" \
    "ds_read_b64_tr_b16 v[44:45], v178 offset:24576\n\t" \
    "ds_read_b64_tr_b16 v[46:47], v178 offset:25600\n\t" \
    "v_add_u32 v178, 0x18000, v177\n\t" \
    "ds_read_b64_tr_b16 v[48:49], v178 offset:0\n\t" \
    "ds_read_b64_tr_b16 v[50:51], v178 offset:1024\n\t" \
    "ds_read_b64_tr_b16 v[52:53], v178 offset:8192\n\t" \
    "ds_read_b64_tr_b16 v[54:55], v178 offset:9216\n\t" \
    "ds_read_b64_tr_b16 v[56:57], v178 offset:16384\n\t" \
    "ds_read_b64_tr_b16 v[58:59], v178 offset:17408\n\t" \
    "ds_read_b64_tr_b16 v[60:61], v178 offset:24576\n\t" \
    "ds_read_b64_tr_b16 v[62:63], v178 offset:25600\n\t" \
    "v_add_u32 v178, 0x20000, v177\n\t" \
    "ds_read_b64_tr_b16 v[64:65], v178 offset:0\n\t" \
    "ds_read_b64_tr_b16 v[66:67], v178 offset:1024\n\t" \
    "ds_read_b64_tr_b16 v[68:69], v178 offset:8192\n\t" \
    "ds_read_b64_tr_b16 v[70:71], v178 offset:9216\n\t" \
    "ds_read_b64_tr_b16 v[72:73], v178 offset:16384\n\t" \
    "ds_read_b64_tr_b16 v[74:75], v178 offset:17408\n\t" \
    "ds_read_b64_tr_b16 v[76:77], v178 offset:24576\n\t" \
    "ds_read_b64_tr_b16 v[78:79], v178 offset:25600\n\t" \
    "v_mov_b32 v228, 0x3f803f80\n\t" \
    "v_mov_b32 v229, 0x3f803f80\n\t" \
    "v_mov_b32 v230, 0x3f803f80\n\t" \
    "v_mov_b32 v231, 0x3f803f80\n\t" \
    "v_mov_b64 v[216:217], 0\n\t" \
    "v_mov_b32 v219, 0\n\t" \
    "v_bfe_u32 v83, v253, 2, 5\n\t" \
    "v_add_u32 v83, 0x7000, v83\n\t" \
    "v_mov_b32 v218, 0\n\t" \
    "s_waitcnt lgkmcnt(0)\n\t" \
    "s_barrier \n\t" \
    "ds_write_b32 v251, v228 offset:72\n\t" \
    "ds_write_b32 v251, v228 offset:2120\n\t" \
    "v_mfma_f32_16x16x32_bf16 v[160:163], v[0:3], v[228:231], 0\n\t" \
    "v_mfma_f32_16x16x32_bf16 v[160:163], v[4:7], v[228:231], v[160:163]\n\t" \
    "v_mfma_f32_16x16x32_bf16 v[160:163], v[8:11], v[228:231], v[160:163]\n\t" \
    "v_mfma_f32_16x16x32_bf16 v[160:163], v[12:15], v[228:231], v[160:163]\n\t" \
    "v_mfma_f32_16x16x32_bf16 v[160:163], v[16:19], v[228:231], v[160:163]\n\t" \
    "v_mfma_f32_16x16x32_bf16 v[160:163], v[20:23], v[228:231], v[160:163]\n\t" \
    "v_mfma_f32_16x16x32_bf16 v[160:163], v[24:27], v[228:231], v[160:163]\n\t" \
    "v_mfma_f32_16x16x32_bf16 v[160:163], v[28:31], v[228:231], v[160:163]\n\t" \
    "v_mfma_f32_16x16x32_bf16 v[160:163], v[32:35], v[228:231], v[160:163]\n\t" \
    "v_mfma_f32_16x16x32_bf16 v[160:163], v[36:39], v[228:231], v[160:163]\n\t" \
    "v_mfma_f32_16x16x32_bf16 v[160:163], v[40:43], v[228:231], v[160:163]\n\t" \
    "v_mfma_f32_16x16x32_bf16 v[160:163], v[44:47], v[228:231], v[160:163]\n\t" \
    "v_mfma_f32_16x16x32_bf16 v[160:163], v[48:51], v[228:231], v[160:163]\n\t" \
    "v_mfma_f32_16x16x32_bf16 v[160:163], v[52:55], v[228:231], v[160:163]\n\t" \
    "v_mfma_f32_16x16x32_bf16 v[160:163], v[56:59], v[228:231], v[160:163]\n\t" \
    "v_mfma_f32_16x16x32_bf16 v[160:163], v[60:63], v[228:231], v[160:163]\n\t" \
    "v_mfma_f32_16x16x32_bf16 v[160:163], v[64:67], v[228:231], v[160:163]\n\t" \
    "v_mfma_f32_16x16x32_bf16 v[160:163], v[68:71], v[228:231], v[160:163]\n\t" \
    "v_mfma_f32_16x16x32_bf16 v[160:163], v[72:75], v[228:231], v[160:163]\n\t" \
    "v_mfma_f32_16x16x32_bf16 v[160:163], v[76:79], v[228:231], v[160:163]\n\t" \
    "s_mov_b32 s52, 0x22222222\n\t" \
    "s_mov_b32 s53, 0x22222222\n\t" \
    "s_mov_b32 s54, 0x44444444\n\t" \
    "s_mov_b32 s55, 0x44444444\n\t" \
    "s_mov_b32 s56, 0x88888888\n\t" \
    "s_mov_b32 s57, 0x88888888\n\t" \
    "ds_write_b32 v83, v218\n\t" \
    "s_waitcnt lgkmcnt(0)\n\t" \
    "s_barrier \n\t" \
    ".Lw11_%=:\n\t" \
    "v_mov_b32 v248, 0\n\t" \
    "buffer_load_dword v248, v248, %[rs], %[go] offen sc1\n\t" \
    "s_waitcnt vmcnt(0)\n\t" \
    "v_cmp_ne_u32 vcc, 0, v248\n\t" \
    "s_cbranch_vccnz .Lw12_%=\n\t" \
    "s_cmp_eq_u32 s41, 0\n\t" \
    "s_cbranch_scc1 .Lw12_%=\n\t" \
    "s_sub_u32 s41, s41, 1\n\t" \
    "s_sleep 16\n\t" \
    "s_branch .Lw11_%=\n\t" \
    ".Lw12_%=:\n\t" \
    "s_sub_i32 s60, 0, %[ng]\n\t" \
    "s_sub_i32 s61, 1, %[ng]\n\t" \
    "s_add_i32 s62, s60, 6\n\t" \
    "s_add_i32 s63, s61, 6\n\t" \
    "s_mul_i32 s42, s61, 127\n\t" \
    "s_mul_i32 s44, %[ng], 0x733000\n\t" \
    "s_add_u32 s44, s44, 0x2400000\n\t" \
    "v_cmp_ne_u32 s[58:59], %[ng], v255\n\t" \
    "s_nop 1\n\t" \
    "s_mov_b64 exec, s[58:59]\n\t" \
    "buffer_store_dwordx2 v[216:217], v254, %[rs], s44 offen sc1\n\t" \
    "s_mov_b64 exec, -1\n\t" \
    "s_lshr_b32 s48, %[wave], 2\n\t" \
    "s_mul_i32 s48, s48, 0x3000\n\t" \
    "s_add_u32 s48, s48, %[pi]\n\t" \
    "s_mov_b32 s43, 0\n\t" \
    "s_add_u32 s46, s43, s48\n\t" \
    "s_add_u32 s47, s46, 0x0\n\t" \
    "buffer_load_dwordx4 v[208:211], v253, %[rs], s47 offen sc1\n\t" \
    "s_add_u32 s47, s46, 0x8000\n\t" \
    "buffer_load_dwordx4 v[212:215], v253, %[rs], s47 offen sc1\n\t" \
    "s_cmp_gt_u32 %[wave], 3\n\t" \
    "s_cbranch_scc1 .Lw13_%=\n\t" \
    "s_add_u32 s47, s46, 0x10000\n\t" \
    "buffer_load_dwordx4 v[216:219], v253, %[rs], s47 offen sc1\n\t" \
    ".Lw13_%=:\n\t" \
    "s_waitcnt vmcnt(0)\n\t" \
    "v_lshrrev_b32 v82, 1, v253\n\t" \
    ".Lw14_%=:\n\t" \
    "s_add_i32 s44, s40, s60\n\t" \
    "s_max_i32 s44, s44, 0\n\t" \
    "s_lshr_b32 s45, s44, 1\n\t" \
    "s_and_b32 s44, s44, 1\n\t" \
    "s_lshl_b32 s44, s44, 12\n\t" \
    "s_mul_i32 s49, s45, 0x14000\n\t" \
    "s_add_u32 s49, s49, s44\n\t" \
    "s_lshr_b32 s45, s45, 4\n\t" \
    "s_mul_i32 s45, s45, 0xc2000\n\t" \
    "s_add_u32 s49, s49, s45\n\t" \
    "s_add_i32 s44, s40, s61\n\t" \
    "s_max_i32 s44, s44, 0\n\t" \
    "s_lshr_b32 s45, s44, 1\n\t" \
    "s_and_b32 s44, s44, 1\n\t" \
    "s_lshl_b32 s44, s44, 12\n\t" \
    "s_mul_i32 s50, s45, 0x14000\n\t" \
    "s_add_u32 s50, s50, s44\n\t" \
    "s_lshr_b32 s45, s45, 4\n\t" \
    "s_mul_i32 s45, s45, 0xc2000\n\t" \
    "s_add_u32 s50, s50, s45\n\t" \
    "s_sub_u32 s50, s50, s49\n\t" \
    "s_add_u32 s49, s49, 0x2400000\n\t" \
    "v_mad_u32_u24 v80, v255, s50, v254\n\t" \
    "s_lshr_b32 s44, s40, 6\n\t" \
    "s_lshl_b32 s44, s44, 14\n\t" \
    "s_and_b32 s45, s40, 63\n\t" \
    "s_lshl_b32 s45, s45, 7\n\t" \
    "s_add_u32 s44, s44, s45\n\t" \
    "s_add_u32 s50, s44, 0x4d0000\n\t" \
    "s_add_i32 s44, s40, 1\n\t" \
    "s_max_i32 s44, s44, 0\n\t" \
    "s_lshr_b32 s45, s44, 1\n\t" \
    "s_and_b32 s44, s44, 1\n\t" \
    "s_lshl_b32 s44, s44, 12\n\t" \
    "s_mul_i32 s43, s45, 0x14000\n\t" \
    "s_add_u32 s43, s43, s44\n\t" \
    "s_lshr_b32 s45, s45, 4\n\t" \
    "s_mul_i32 s45, s45, 0xc2000\n\t" \
    "s_add_u32 s43, s43, s45\n\t" \
    "s_setprio 2\n\t" \
    "v_max_u32 v248, v208, v209\n\t" \
    "v_max3_u32 v248, v210, v211, v248\n\t" \
    "v_max3_u32 v248, v212, v213, v248\n\t" \
    "v_max3_u32 v248, v214, v215, v248\n\t" \
    "v_max3_u32 v248, v216, v217, v248\n\t" \
    "v_max3_u32 v248, v218, v219, v248\n\t" \
    "v_cmp_eq_u32 vcc, -1, v248\n\t" \
    "s_cbranch_vccz .Lw16_%=\n\t" \
    "s_and_b32 s44, s40, 1\n\t" \
    "s_cmp_eq_u32 s44, 1\n\t" \
    "s_cbranch_scc1 .Lw15_%=\n\t" \
    "s_cmp_gt_u32 s40, 123\n\t" \
    "s_cbranch_scc1 .Lw15_%=\n\t" \
    "s_sleep 10\n\t" \
    ".Lw15_%=:\n\t" \
    "v_max_u32 v248, v208, v209\n\t" \
    "v_max3_u32 v248, v210, v211, v248\n\t" \
    "v_cmp_eq_u32 vcc, -1, v248\n\t" \
    "s_cbranch_vccz .Lw18_%=\n\t" \
    "s_add_u32 s47, s46, 0x0\n\t" \
    "buffer_load_dwordx4 v[208:211], v253, %[rs], s47 offen sc1\n\t" \
    ".Lw18_%=:\n\t" \
    "v_max_u32 v248, v212, v213\n\t" \
    "v_max3_u32 v248, v214, v215, v248\n\t" \
    "v_cmp_eq_u32 vcc, -1, v248\n\t" \
    "s_cbranch_vccz .Lw19_%=\n\t" \
    "s_add_u32 s47, s46, 0x8000\n\t" \
    "buffer_load_dwordx4 v[212:215], v253, %[rs], s47 offen sc1\n\t" \
    ".Lw19_%=:\n\t" \
    "v_max_u32 v248, v216, v217\n\t" \
    "v_max3_u32 v248, v218, v219, v248\n\t" \
    "v_cmp_eq_u32 vcc, -1, v248\n\t" \
    "s_cbranch_vccz .Lw110_%=\n\t" \
    "s_add_u32 s47, s46, 0x10000\n\t" \
    "buffer_load_dwordx4 v[216:219], v253, %[rs], s47 offen sc1\n\t" \
    ".Lw110_%=:\n\t" \
    "s_waitcnt vmcnt(0)\n\t" \
    "v_max_u32 v248, v208, v209\n\t" \
    "v_max3_u32 v248, v210, v211, v248\n\t" \
    "v_max3_u32 v248, v212, v213, v248\n\t" \
    "v_max3_u32 v248, v214, v215, v248\n\t" \
    "v_max3_u32 v248, v216, v217, v248\n\t" \
    "v_max3_u32 v248, v218, v219, v248\n\t" \
    "v_cmp_eq_u32 vcc, -1, v248\n\t" \
    "s_cbranch_vccz .Lw16_%=\n\t" \
    "s_cmp_eq_u32 s41, 0\n\t" \
    "s_cbranch_scc1 .Lw16_%=\n\t" \
    "s_sub_u32 s41, s41, 1\n\t" \
    "s_branch .Lw15_%=\n\t" \
    ".Lw16_%=:\n\t" \
    "ds_write_b128 v250, v[208:211] offset:0\n\t" \
    "v_mfma_f32_16x16x32_bf16 v[236:239], v[208:211], v[208:211], 0\n\t" \
    "v_mfma_f32_16x16x32_bf16 v[232:235], v[228:231], v[208:211], 0\n\t" \
    "ds_write_b128 v250, v[212:215] offset:8192\n\t" \
    "v_mfma_f32_16x16x32_bf16 v[236:239], v[212:215], v[212:215], v[236:239]\n\t" \
    "v_mfma_f32_16x16x32_bf16 v[232:235], v[228:231], v[212:215], v[232:235]\n\t" \
    "s_cmp_gt_u32 %[wave], 3\n\t" \
    "s_cbranch_scc1 .Lw111_%=\n\t" \
    "ds_write_b128 v250, v[216:219] offset:16384\n\t" \
    ".Lw111_%=:\n\t" \
    "v_mfma_f32_16x16x32_bf16 v[236:239], v[216:219], v[216:219], v[236:239]\n\t" \
    "v_mfma_f32_16x16x32_bf16 v[232:235], v[228:231], v[216:219], v[232:235]\n\t" \
    "s_add_u32 s46, s43, s48\n\t" \
    "s_nop 4\n\t" \
    "v_cndmask_b32 v236, v236, v237, s[52:53]\n\t" \
    "v_cndmask_b32 v236, v236, v238, s[54:55]\n\t" \
    "v_cndmask_b32 v236, v236, v239, s[56:57]\n\t" \
    "s_mov_b32 exec_lo, 0xf0000f\n\t" \
    "s_mov_b32 exec_hi, 0xf0000f00\n\t" \
    "ds_write2_b32 v252, v232, v236 offset1:1\n\t" \
    "s_mov_b64 exec, -1\n\t" \
    "s_waitcnt lgkmcnt(0)\n\t" \
    "s_barrier \n\t" \
    "s_setprio 0\n\t" \
    "s_cmp_lg_u32 %[wave], 4\n\t" \
    "s_cbranch_scc1 .Lw112_%=\n\t" \
    "s_setprio 1\n\t" \
    "ds_read_b128 v[232:235], v251 offset:0\n\t" \
    "ds_read_b128 v[236:239], v251 offset:16\n\t" \
    "ds_read_b128 v[240:243], v251 offset:32\n\t" \
    "ds_read_b128 v[244:247], v251 offset:48\n\t" \
    "s_waitcnt lgkmcnt(0)\n\t" \
    "v_add_f32 v232, v232, v240\n\t" \
    "v_add_f32 v233, v233, v241\n\t" \
    "v_add_f32 v234, v234, v242\n\t" \
    "v_add_f32 v235, v235, v243\n\t" \
    "v_add_f32 v236, v236, v244\n\t" \
    "v_add_f32 v237, v237, v245\n\t" \
    "v_add_f32 v238, v238, v246\n\t" \
    "v_add_f32 v239, v239, v247\n\t" \
    "v_add_f32 v232, v232, v236\n\t" \
    "v_add_f32 v233, v233, v237\n\t" \
    "v_add_f32 v234, v234, v238\n\t" \
    "v_add_f32 v235, v235, v239\n\t" \
    "v_add_f32 v232, v232, v234\n\t" \
    "v_add_f32 v233, v233, v235\n\t" \
    "v_mul_f32 v232, 0x3acccccd, v232\n\t" \
    "v_mul_f32 v233, 0x3acccccd, v233\n\t" \
    "v_fma_f32 v233, -v232, v232, v233\n\t" \
    "v_mov_b32 v234, v232\n\t" \
    "v_max_f32 v233, 0, v233\n\t" \
    "v_cvt_pk_bf16_f32 v234, v234, v234\n\t" \
    "v_and_b32 v234, 0xffff0000, v234\n\t" \
    "v_mov_b32 v236, v233\n\t" \
    "v_cvt_pk_bf16_f32 v236, v236, v236\n\t" \
    "v_and_b32 v236, 0xffff0000, v236\n\t" \
    "v_add_f32 v236, 0x3727c5ac, v236\n\t" \
    "v_cvt_pk_bf16_f32 v236, v236, v236\n\t" \
    "v_and_b32 v236, 0xffff0000, v236\n\t" \
    "v_sqrt_f32 v236, v236\n\t" \
    "s_nop 0\n\t" \
    "v_cvt_pk_bf16_f32 v236, v236, v236\n\t" \
    "v_and_b32 v236, 0xffff0000, v236\n\t" \
    "v_rcp_f32 v235, v236\n\t" \
    "s_nop 0\n\t" \
    "v_sub_f32 v232, v232, v234\n\t" \
    "v_mul_f32 v233, v233, v235\n\t" \
    "v_mul_f32 v232, v232, v235\n\t" \
    "v_mul_f32 v233, v233, v235\n\t" \
    "v_cvt_pk_bf16_f32 v232, v232, v232\n\t" \
    "v_and_b32 v232, 0xffff0000, v232\n\t" \
    "v_cvt_pk_bf16_f32 v233, v233, v233\n\t" \
    "v_and_b32 v233, 0xffff0000, v233\n\t" \
    "v_add_f32 v233, 0x3727c5ac, v233\n\t" \
    "v_cvt_pk_bf16_f32 v233, v233, v233\n\t" \
    "v_and_b32 v233, 0xffff0000, v233\n\t" \
    "v_sqrt_f32 v233, v233\n\t" \
    "s_nop 0\n\t" \
    "v_cvt_pk_bf16_f32 v233, v233, v233\n\t" \
    "v_and_b32 v233, 0xffff0000, v233\n\t" \
    "v_rcp_f32 v233, v233\n\t" \
    "v_fma_f32 v232, v232, v236, v234\n\t" \
    "v_mul_f32 v237, v235, v233\n\t" \
    "v_mul_f32 v238, -v232, v237\n\t" \
    "v_min_u32 v234, -2, v234\n\t" \
    "v_min_u32 v235, -2, v235\n\t" \
    "v_mov_b32 v239, s40\n\t" \
    "ds_write2_b32 v251, v237, v238 offset0:16 offset1:17\n\t" \
    "ds_write_b32 v251, v239 offset:72\n\t" \
    "s_setprio 0\n\t" \
    "s_cmp_eq_u32 %[ss], 0\n\t" \
    "s_cbranch_scc1 .Lw113_%=\n\t" \
    "s_mov_b64 exec, 0xffff\n\t" \
    "buffer_store_dwordx2 v[234:235], v82, %[rs], s50 offen sc1\n\t" \
    "s_mov_b64 exec, -1\n\t" \
    ".Lw113_%=:\n\t" \
    ".Lw112_%=:\n\t" \
    "ds_read_b128 v[176:179], v249 offset:0\n\t" \
    "ds_read_b128 v[180:183], v249 offset:1024\n\t" \
    "ds_read_b128 v[184:187], v249 offset:2048\n\t" \
    "ds_read_b128 v[188:191], v249 offset:3072\n\t" \
    "ds_read_b128 v[192:195], v249 offset:4096\n\t" \
    "ds_read_b128 v[196:199], v249 offset:5120\n\t" \
    "ds_read_b128 v[200:203], v249 offset:6144\n\t" \
    "s_waitcnt lgkmcnt(6)\n\t" \
    "v_mfma_f32_16x16x32_bf16 v[168:171], v[0:3], v[176:179], 0\n\t" \
    "ds_read_b128 v[204:207], v249 offset:7168\n\t" \
    "s_waitcnt lgkmcnt(6)\n\t" \
    "v_mfma_f32_16x16x32_bf16 v[168:171], v[4:7], v[180:183], v[168:171]\n\t" \
    "ds_read_b128 v[176:179], v249 offset:8192\n\t" \
    "s_waitcnt lgkmcnt(6)\n\t" \
    "v_mfma_f32_16x16x32_bf16 v[168:171], v[8:11], v[184:187], v[168:171]\n\t" \
    "ds_read_b128 v[180:183], v249 offset:9216\n\t" \
    "s_waitcnt lgkmcnt(6)\n\t" \
    "v_mfma_f32_16x16x32_bf16 v[168:171], v[12:15], v[188:191], v[168:171]\n\t" \
    "ds_read_b128 v[184:187], v249 offset:10240\n\t" \
    "s_add_u32 s47, s46, 0x0\n\t" \
    "buffer_load_dwordx4 v[208:211], v253, %[rs], s47 offen sc1\n\t" \
    "s_add_u32 s47, s46, 0x8000\n\t" \
    "buffer_load_dwordx4 v[212:215], v253, %[rs], s47 offen sc1\n\t" \
    "s_cmp_gt_u32 %[wave], 3\n\t" \
    "s_cbranch_scc1 .Lw114_%=\n\t" \
    "s_add_u32 s47, s46, 0x10000\n\t" \
    "buffer_load_dwordx4 v[216:219], v253, %[rs], s47 offen sc1\n\t" \
    ".Lw114_%=:\n\t" \
    "s_waitcnt lgkmcnt(6)\n\t" \
    "v_mfma_f32_16x16x32_bf16 v[168:171], v[16:19], v[192:195], v[168:171]\n\t" \
    "ds_read_b128 v[188:191], v249 offset:11264\n\t" \
    "s_waitcnt lgkmcnt(6)\n\t" \
    "v_mfma_f32_16x16x32_bf16 v[168:171], v[20:23], v[196:199], v[168:171]\n\t" \
    "ds_read_b128 v[192:195], v249 offset:12288\n\t" \
    "s_waitcnt lgkmcnt(6)\n\t" \
    "v_mfma_f32_16x16x32_bf16 v[168:171], v[24:27], v[200:203], v[168:171]\n\t" \
    "ds_read_b128 v[196:199], v249 offset:13312\n\t" \
    "s_waitcnt lgkmcnt(6)\n\t" \
    "v_mfma_f32_16x16x32_bf16 v[168:171], v[28:31], v[204:207], v[168:171]\n\t" \
    "ds_read_b128 v[200:203], v249 offset:14336\n\t" \
    "s_waitcnt lgkmcnt(6)\n\t" \
    "v_mfma_f32_16x16x32_bf16 v[168:171], v[32:35], v[176:179], v[168:171]\n\t" \
    "ds_read_b128 v[204:207], v249 offset:15360\n\t" \
    "s_waitcnt lgkmcnt(6)\n\t" \
    "v_mfma_f32_16x16x32_bf16 v[168:171], v[36:39], v[180:183], v[168:171]\n\t" \
    "ds_read_b128 v[176:179], v249 offset:16384\n\t" \
    "s_waitcnt lgkmcnt(6)\n\t" \
    "v_mfma_f32_16x16x32_bf16 v[168:171], v[40:43], v[184:187], v[168:171]\n\t" \
    "ds_read_b128 v[180:183], v249 offset:17408\n\t" \
    "s_waitcnt lgkmcnt(6)\n\t" \
    "v_mfma_f32_16x16x32_bf16 v[168:171], v[44:47], v[188:191], v[168:171]\n\t" \
    "ds_read_b128 v[184:187], v249 offset:18432\n\t" \
    "s_waitcnt lgkmcnt(6)\n\t" \
    "v_mfma_f32_16x16x32_bf16 v[168:171], v[48:51], v[192:195], v[168:171]\n\t" \
    "ds_read_b128 v[188:191], v249 offset:19456\n\t" \
    "s_waitcnt lgkmcnt(6)\n\t" \
    "v_mfma_f32_16x16x32_bf16 v[168:171], v[52:55], v[196:199], v[168:171]\n\t" \
    "ds_read_b32 v236, v251 offset:72\n\t" \
    "ds_read_b64 v[238:239], v251 offset:64\n\t" \
    "s_waitcnt lgkmcnt(7)\n\t" \
    "v_mfma_f32_16x16x32_bf16 v[168:171], v[56:59], v[200:203], v[168:171]\n\t" \
    "s_waitcnt lgkmcnt(6)\n\t" \
    "v_mfma_f32_16x16x32_bf16 v[168:171], v[60:63], v[204:207], v[168:171]\n\t" \
    "s_waitcnt lgkmcnt(5)\n\t" \
    "v_mfma_f32_16x16x32_bf16 v[168:171], v[64:67], v[176:179], v[168:171]\n\t" \
    "s_waitcnt lgkmcnt(4)\n\t" \
    "v_mfma_f32_16x16x32_bf16 v[168:171], v[68:71], v[180:183], v[168:171]\n\t" \
    ".Lw115_%=:\n\t" \
    "s_waitcnt lgkmcnt(0)\n\t" \
    "v_cmp_ne_u32 vcc, s40, v236\n\t" \
    "s_cbranch_vccz .Lw116_%=\n\t" \
    "ds_read_b32 v236, v251 offset:72\n\t" \
    "ds_read_b64 v[238:239], v251 offset:64\n\t" \
    "s_branch .Lw115_%=\n\t" \
    ".Lw116_%=:\n\t" \
    "s_waitcnt lgkmcnt(0)\n\t" \
    "v_mul_f32 v240, v160, v239\n\t" \
    "v_mul_f32 v241, v161, v239\n\t" \
    "v_mfma_f32_16x16x32_bf16 v[168:171], v[72:75], v[184:187], v[168:171]\n\t" \
    "v_mul_f32 v242, v162, v239\n\t" \
    "v_mul_f32 v243, v163, v239\n\t" \
    "v_mfma_f32_16x16x32_bf16 v[168:171], v[76:79], v[188:191], v[168:171]\n\t" \
    "v_mov_b32 v237, 0xc0135761\n\t" \
    "s_nop 6\n\t" \
    "v_fma_f32 v168, v168, v238, v240\n\t" \
    "v_fma_f32 v169, v169, v238, v241\n\t" \
    "v_fma_f32 v170, v170, v238, v242\n\t" \
    "v_fma_f32 v171, v171, v238, v243\n\t" \
    "v_mul_f32 v176, v168, v168\n\t" \
    "v_mul_f32 v177, v169, v169\n\t" \
    "v_mul_f32 v178, v170, v170\n\t" \
    "v_mul_f32 v179, v171, v171\n\t" \
    "v_fmamk_f32 v176, v176, 0xbdd2d3e7, v237\n\t" \
    "v_fmamk_f32 v177, v177, 0xbdd2d3e7, v237\n\t" \
    "v_fmamk_f32 v178, v178, 0xbdd2d3e7, v237\n\t" \
    "v_fmamk_f32 v179, v179, 0xbdd2d3e7, v237\n\t" \
    "v_mul_f32 v176, v168, v176\n\t" \
    "v_mul_f32 v177, v169, v177\n\t" \
    "v_mul_f32 v178, v170, v178\n\t" \
    "v_mul_f32 v179, v171, v179\n\t" \
    "v_exp_f32 v176, v176\n\t" \
    "v_exp_f32 v177, v177\n\t" \
    "v_exp_f32 v178, v178\n\t" \
    "v_exp_f32 v179, v179\n\t" \
    "v_add_f32 v176, 1.0, v176\n\t" \
    "v_add_f32 v177, 1.0, v177\n\t" \
    "v_add_f32 v178, 1.0, v178\n\t" \
    "v_add_f32 v179, 1.0, v179\n\t" \
    "v_rcp_f32 v176, v176\n\t" \
    "v_rcp_f32 v177, v177\n\t" \
    "v_rcp_f32 v178, v178\n\t" \
    "v_rcp_f32 v179, v179\n\t" \
    "v_mul_f32 v168, v168, v176\n\t" \
    "v_mul_f32 v169, v169, v177\n\t" \
    "v_mul_f32 v170, v170, v178\n\t" \
    "v_mul_f32 v171, v171, v179\n\t" \
    "s_waitcnt vmcnt(0)\n\t" \
    "v_cvt_pk_bf16_f32 v184, v168, v169\n\t" \
    "v_cvt_pk_bf16_f32 v185, v170, v171\n\t" \
    "v_min_u32 v184, -2, v184\n\t" \
    "v_min_u32 v185, -2, v185\n\t" \
    "s_cmp_eq_u32 s40, s42\n\t" \
    "s_cselect_b64 s[44:45], s[58:59], 0\n\t" \
    "s_andn2_b64 exec, -1, s[44:45]\n\t" \
    "buffer_store_dwordx2 v[184:185], v80, %[rs], s49 offen sc1\n\t" \
    "s_mov_b64 exec, -1\n\t" \
    "s_cmp_ge_u32 s40, 122\n\t" \
    "s_cbranch_scc1 .Lw117_%=\n\t" \
    "s_add_i32 s44, s40, s62\n\t" \
    "s_max_i32 s44, s44, 0\n\t" \
    "s_lshr_b32 s45, s44, 1\n\t" \
    "s_and_b32 s44, s44, 1\n\t" \
    "s_lshl_b32 s44, s44, 12\n\t" \
    "s_mul_i32 s51, s45, 0x14000\n\t" \
    "s_add_u32 s51, s51, s44\n\t" \
    "s_lshr_b32 s45, s45, 4\n\t" \
    "s_mul_i32 s45, s45, 0xc2000\n\t" \
    "s_add_u32 s51, s51, s45\n\t" \
    "s_add_i32 s44, s40, s63\n\t" \
    "s_max_i32 s44, s44, 0\n\t" \
    "s_lshr_b32 s45, s44, 1\n\t" \
    "s_and_b32 s44, s44, 1\n\t" \
    "s_lshl_b32 s44, s44, 12\n\t" \
    "s_mul_i32 s50, s45, 0x14000\n\t" \
    "s_add_u32 s50, s50, s44\n\t" \
    "s_lshr_b32 s45, s45, 4\n\t" \
    "s_mul_i32 s45, s45, 0xc2000\n\t" \
    "s_add_u32 s50, s50, s45\n\t" \
    "s_sub_u32 s50, s50, s51\n\t" \
    "s_add_u32 s51, s51, 0x2400000\n\t" \
    "v_mad_u32_u24 v81, v255, s50, v254\n\t" \
    "v_mov_b64 v[188:189], -1\n\t" \
    "s_add_u32 s44, s40, 6\n\t" \
    "s_cmp_eq_u32 s44, s42\n\t" \
    "s_cselect_b64 s[44:45], s[58:59], 0\n\t" \
    "s_andn2_b64 exec, -1, s[44:45]\n\t" \
    "buffer_store_dwordx2 v[188:189], v81, %[rs], s51 offen sc1\n\t" \
    "s_mov_b64 exec, -1\n\t" \
    ".Lw117_%=:\n\t" \
    "v_xor_b32 v249, 0x8000, v249\n\t" \
    "v_xor_b32 v250, 0x8000, v250\n\t" \
    "v_xor_b32 v251, 0x800, v251\n\t" \
    "v_xor_b32 v252, 0x800, v252\n\t" \
    "s_add_u32 s40, s40, 1\n\t" \
    "s_cmp_lg_u32 s40, 128\n\t" \
    "s_cbranch_scc1 .Lw14_%=\n\t" \
    "s_branch .Lend%=\n\t" \
    ".Lend%=:\n\t" \
    "s_waitcnt vmcnt(0) lgkmcnt(0)\n\t" \
    "s_mov_b32 %[bud], s41\n\t"
#define ASM_W2_LOOP \
    "s_mov_b32 s40, 0\n\t" \
    "s_mov_b32 s41, 0x20000\n\t" \
    "s_add_u32 m0, %[wm], 0x0\n\t" \
    "s_add_u32 s44, %[wo], 0x0\n\t" \
    "buffer_load_dwordx4 v176, %[wr], s44 offen nt lds\n\t" \
    "s_add_u32 m0, %[wm], 0x2000\n\t" \
    "s_add_u32 s44, %[wo], 0xa000\n\t" \
    "buffer_load_dwordx4 v176, %[wr], s44 offen nt lds\n\t" \
    "s_add_u32 m0, %[wm], 0x4000\n\t" \
    "s_add_u32 s44, %[wo], 0x14000\n\t" \
    "buffer_load_dwordx4 v176, %[wr], s44 offen nt lds\n\t" \
    "s_add_u32 m0, %[wm], 0x6000\n\t" \
    "s_add_u32 s44, %[wo], 0x1e000\n\t" \
    "buffer_load_dwordx4 v176, %[wr], s44 offen nt lds\n\t" \
    "s_add_u32 m0, %[wm], 0x8000\n\t" \
    "s_add_u32 s44, %[wo], 0x28000\n\t" \
    "buffer_load_dwordx4 v176, %[wr], s44 offen nt lds\n\t" \
    "s_add_u32 m0, %[wm], 0xa000\n\t" \
    "s_add_u32 s44, %[wo], 0x32000\n\t" \
    "buffer_load_dwordx4 v176, %[wr], s44 offen nt lds\n\t" \
    "s_add_u32 m0, %[wm], 0xc000\n\t" \
    "s_add_u32 s44, %[wo], 0x3c000\n\t" \
    "buffer_load_dwordx4 v176, %[wr], s44 offen nt lds\n\t" \
    "s_add_u32 m0, %[wm], 0xe000\n\t" \
    "s_add_u32 s44, %[wo], 0x46000\n\t" \
    "buffer_load_dwordx4 v176, %[wr], s44 offen nt lds\n\t" \
    "s_add_u32 m0, %[wm], 0x10000\n\t" \
    "s_add_u32 s44, %[wo], 0x50000\n\t" \
    "buffer_load_dwordx4 v176, %[wr], s44 offen nt lds\n\t" \
    "s_add_u32 m0, %[wm], 0x12000\n\t" \
    "s_add_u32 s44, %[wo], 0x5a000\n\t" \
    "buffer_load_dwordx4 v176, %[wr], s44 offen nt lds\n\t" \
    "s_add_u32 m0, %[wm], 0x14000\n\t" \
    "s_add_u32 s44, %[wo], 0x64000\n\t" \
    "buffer_load_dwordx4 v176, %[wr], s44 offen nt lds\n\t" \
    "s_add_u32 m0, %[wm], 0x16000\n\t" \
    "s_add_u32 s44, %[wo], 0x6e000\n\t" \
    "buffer_load_dwordx4 v176, %[wr], s44 offen nt lds\n\t" \
    "s_add_u32 m0, %[wm], 0x18000\n\t" \
    "s_add_u32 s44, %[wo], 0x78000\n\t" \
    "buffer_load_dwordx4 v176, %[wr], s44 offen nt lds\n\t" \
    "s_add_u32 m0, %[wm], 0x1a000\n\t" \
    "s_add_u32 s44, %[wo], 0x82000\n\t" \
    "buffer_load_dwordx4 v176, %[wr], s44 offen nt lds\n\t" \
    "s_add_u32 m0, %[wm], 0x1c000\n\t" \
    "s_add_u32 s44, %[wo], 0x8c000\n\t" \
    "buffer_load_dwordx4 v176, %[wr], s44 offen nt lds\n\t" \
    "s_add_u32 m0, %[wm], 0x1e000\n\t" \
    "s_add_u32 s44, %[wo], 0x96000\n\t" \
    "buffer_load_dwordx4 v176, %[wr], s44 offen nt lds\n\t" \
    "s_add_u32 m0, %[wm], 0x20000\n\t" \
    "s_add_u32 s44, %[wo], 0xa0000\n\t" \
    "buffer_load_dwordx4 v176, %[wr], s44 offen nt lds\n\t" \
    "s_add_u32 m0, %[wm], 0x22000\n\t" \
    "s_add_u32 s44, %[wo], 0xaa000\n\t" \
    "buffer_load_dwordx4 v176, %[wr], s44 offen nt lds\n\t" \
    "s_add_u32 m0, %[wm], 0x24000\n\t" \
    "s_add_u32 s44, %[wo], 0xb4000\n\t" \
    "buffer_load_dwordx4 v176, %[wr], s44 offen nt lds\n\t" \
    "s_add_u32 m0, %[wm], 0x26000\n\t" \
    "s_add_u32 s44, %[wo], 0xbe000\n\t" \
    "buffer_load_dwordx4 v176, %[wr], s44 offen nt lds\n\t" \
    "s_waitcnt vmcnt(0) lgkmcnt(0)\n\t" \
    "s_barrier \n\t" \
    "v_add_u32 v178, 0x0, v177\n\t" \
    "ds_read_b64_tr_b16 v[0:1], v178 offset:0\n\t" \
    "ds_read_b64_tr_b16 v[2:3], v178 offset:1024\n\t" \
    "ds_read_b64_tr_b16 v[4:5], v178 offset:8192\n\t" \
    "ds_read_b64_tr_b16 v[6:7], v178 offset:9216\n\t" \
    "ds_read_b64_tr_b16 v[8:9], v178 offset:16384\n\t" \
    "ds_read_b64_tr_b16 v[10:11], v178 offset:17408\n\t" \
    "ds_read_b64_tr_b16 v[12:13], v178 offset:24576\n\t" \
    "ds_read_b64_tr_b16 v[14:15], v178 offset:25600\n\t" \
    "v_add_u32 v178, 0x8000, v177\n\t" \
    "ds_read_b64_tr_b16 v[16:17], v178 offset:0\n\t" \
    "ds_read_b64_tr_b16 v[18:19], v178 offset:1024\n\t" \
    "ds_read_b64_tr_b16 v[20:21], v178 offset:8192\n\t" \
    "ds_read_b64_tr_b16 v[22:23], v178 offset:9216\n\t" \
    "ds_read_b64_tr_b16 v[24:25], v178 offset:16384\n\t" \
    "ds_read_b64_tr_b16 v[26:27], v178 offset:17408\n\t" \
    "ds_read_b64_tr_b16 v[28:29], v178 offset:24576\n\t" \
    "ds_read_b64_tr_b16 v[30:31], v178 offset:25600\n\t" \
    "v_add_u32 v178, 0x10000, v177\n\t" \
    "ds_read_b64_tr_b16 v[32:33], v178 offset:0\n\t" \
    "ds_read_b64_tr_b16 v[34:35], v178 offset:1024\n\t" \
    "ds_read_b64_tr_b16 v[36:37], v178 offset:8192\n\t" \
    "ds_read_b64_tr_b16 v[38:39], v178 offset:9216\n\t" \
    "ds_read_b64_tr_b16 v[40:41], v178 offset:16384\n\t" \
    "ds_read_b64_tr_b16 v[42:43], v178 offset:17408\n\t" \
    "ds_read_b64_tr_b16 v[44:45], v178 offset:24576\n\t" \
    "ds_read_b64_tr_b16 v[46:47], v178 offset:25600\n\t" \
    "v_add_u32 v178, 0x18000, v177\n\t" \
    "ds_read_b64_tr_b16 v[48:49], v178 offset:0\n\t" \
    "ds_read_b64_tr_b16 v[50:51], v178 offset:1024\n\t" \
    "ds_read_b64_tr_b16 v[52:53], v178 offset:8192\n\t" \
    "ds_read_b64_tr_b16 v[54:55], v178 offset:9216\n\t" \
    "ds_read_b64_tr_b16 v[56:57], v178 offset:16384\n\t" \
    "ds_read_b64_tr_b16 v[58:59], v178 offset:17408\n\t" \
    "ds_read_b64_tr_b16 v[60:61], v178 offset:24576\n\t" \
    "ds_read_b64_tr_b16 v[62:63], v178 offset:25600\n\t" \
    "v_add_u32 v178, 0x20000, v177\n\t" \
    "ds_read_b64_tr_b16 v[64:65], v178 offset:0\n\t" \
    "ds_read_b64_tr_b16 v[66:67], v178 offset:1024\n\t" \
    "ds_read_b64_tr_b16 v[68:69], v178 offset:8192\n\t" \
    "ds_read_b64_tr_b16 v[70:71], v178 offset:9216\n\t" \
    "ds_read_b64_tr_b16 v[72:73], v178 offset:16384\n\t" \
    "ds_read_b64_tr_b16 v[74:75], v178 offset:17408\n\t" \
    "ds_read_b64_tr_b16 v[76:77], v178 offset:24576\n\t" \
    "ds_read_b64_tr_b16 v[78:79], v178 offset:25600\n\t" \
    "v_mov_b32 v228, 0x3f803f80\n\t" \
    "v_mov_b32 v229, 0x3f803f80\n\t" \
    "v_mov_b32 v230, 0x3f803f80\n\t" \
    "v_mov_b32 v231, 0x3f803f80\n\t" \
    "v_mov_b64 v[216:217], 0\n\t" \
    "v_mov_b32 v219, 0\n\t" \
    "v_bfe_u32 v255, v253, 2, 5\n\t" \
    "v_add_u32 v255, 0x7000, v255\n\t" \
    "v_mov_b32 v218, 0\n\t" \
    "s_waitcnt lgkmcnt(0)\n\t" \
    "s_barrier \n\t" \
    "ds_write_b32 v255, v218\n\t" \
    "s_waitcnt lgkmcnt(0)\n\t" \
    "s_barrier \n\t" \
    ".Lw21_%=:\n\t" \
    "v_mov_b32 v248, 0\n\t" \
    "buffer_load_dword v248, v248, %[rs], %[go] offen sc1\n\t" \
    "s_waitcnt vmcnt(0)\n\t" \
    "v_cmp_ne_u32 vcc, 0, v248\n\t" \
    "s_cbranch_vccnz .Lw22_%=\n\t" \
    "s_cmp_eq_u32 s41, 0\n\t" \
    "s_cbranch_scc1 .Lw22_%=\n\t" \
    "s_sub_u32 s41, s41, 1\n\t" \
    "s_sleep 16\n\t" \
    "s_branch .Lw21_%=\n\t" \
    ".Lw22_%=:\n\t" \
    "s_lshr_b32 s48, %[wave], 2\n\t" \
    "s_mul_i32 s48, s48, 0x3000\n\t" \
    "s_add_u32 s48, s48, %[pi]\n\t" \
    "s_mov_b32 s43, 0\n\t" \
    "s_add_u32 s46, s43, s48\n\t" \
    "s_add_u32 s47, s46, 0x0\n\t" \
    "buffer_load_dwordx4 v[208:211], v253, %[rs], s47 offen sc1\n\t" \
    "s_add_u32 s47, s46, 0x8000\n\t" \
    "buffer_load_dwordx4 v[212:215], v253, %[rs], s47 offen sc1\n\t" \
    "s_cmp_gt_u32 %[wave], 3\n\t" \
    "s_cbranch_scc1 .Lw23_%=\n\t" \
    "s_add_u32 s47, s46, 0x10000\n\t" \
    "buffer_load_dwordx4 v[216:219], v253, %[rs], s47 offen sc1\n\t" \
    ".Lw23_%=:\n\t" \
    "s_waitcnt vmcnt(0)\n\t" \
    ".Lw24_%=:\n\t" \
    "s_add_u32 s49, s43, 0x2c00000\n\t" \
    "s_add_u32 s50, s43, 0x1c00000\n\t" \
    "s_lshr_b32 s44, s40, 6\n\t" \
    "s_lshl_b32 s44, s44, 14\n\t" \
    "s_and_b32 s45, s40, 63\n\t" \
    "s_lshl_b32 s45, s45, 7\n\t" \
    "s_add_u32 s44, s44, s45\n\t" \
    "s_add_u32 s51, s44, 0x4d0000\n\t" \
    "s_add_i32 s44, s40, 1\n\t" \
    "s_max_i32 s44, s44, 0\n\t" \
    "s_lshr_b32 s45, s44, 1\n\t" \
    "s_and_b32 s44, s44, 1\n\t" \
    "s_lshl_b32 s44, s44, 12\n\t" \
    "s_mul_i32 s43, s45, 0x14000\n\t" \
    "s_add_u32 s43, s43, s44\n\t" \
    "s_lshr_b32 s45, s45, 4\n\t" \
    "s_mul_i32 s45, s45, 0xc2000\n\t" \
    "s_add_u32 s43, s43, s45\n\t" \
    "s_setprio 2\n\t" \
    "v_max_u32 v248, v208, v209\n\t" \
    "v_max3_u32 v248, v210, v211, v248\n\t" \
    "v_max3_u32 v248, v212, v213, v248\n\t" \
    "v_max3_u32 v248, v214, v215, v248\n\t" \
    "v_max3_u32 v248, v216, v217, v248\n\t" \
    "v_max3_u32 v248, v218, v219, v248\n\t" \
    "v_cmp_eq_u32 vcc, -1, v248\n\t" \
    "s_cbranch_vccz .Lw26_%=\n\t" \
    "s_and_b32 s44, s40, 1\n\t" \
    "s_cmp_eq_u32 s44, 0\n\t" \
    "s_cbranch_scc1 .Lw25_%=\n\t" \
    "s_cmp_gt_u32 s40, 123\n\t" \
    "s_cbranch_scc1 .Lw25_%=\n\t" \
    "s_sleep 6\n\t" \
    ".Lw25_%=:\n\t" \
    "v_max_u32 v248, v208, v209\n\t" \
    "v_max3_u32 v248, v210, v211, v248\n\t" \
    "v_cmp_eq_u32 vcc, -1, v248\n\t" \
    "s_cbranch_vccz .Lw28_%=\n\t" \
    "s_add_u32 s47, s46, 0x0\n\t" \
    "buffer_load_dwordx4 v[208:211], v253, %[rs], s47 offen sc1\n\t" \
    ".Lw28_%=:\n\t" \
    "v_max_u32 v248, v212, v213\n\t" \
    "v_max3_u32 v248, v214, v215, v248\n\t" \
    "v_cmp_eq_u32 vcc, -1, v248\n\t" \
    "s_cbranch_vccz .Lw29_%=\n\t" \
    "s_add_u32 s47, s46, 0x8000\n\t" \
    "buffer_load_dwordx4 v[212:215], v253, %[rs], s47 offen sc1\n\t" \
    ".Lw29_%=:\n\t" \
    "v_max_u32 v248, v216, v217\n\t" \
    "v_max3_u32 v248, v218, v219, v248\n\t" \
    "v_cmp_eq_u32 vcc, -1, v248\n\t" \
    "s_cbranch_vccz .Lw210_%=\n\t" \
    "s_add_u32 s47, s46, 0x10000\n\t" \
    "buffer_load_dwordx4 v[216:219], v253, %[rs], s47 offen sc1\n\t" \
    ".Lw210_%=:\n\t" \
    "s_waitcnt vmcnt(0)\n\t" \
    "v_max_u32 v248, v208, v209\n\t" \
    "v_max3_u32 v248, v210, v211, v248\n\t" \
    "v_max3_u32 v248, v212, v213, v248\n\t" \
    "v_max3_u32 v248, v214, v215, v248\n\t" \
    "v_max3_u32 v248, v216, v217, v248\n\t" \
    "v_max3_u32 v248, v218, v219, v248\n\t" \
    "v_cmp_eq_u32 vcc, -1, v248\n\t" \
    "s_cbranch_vccz .Lw26_%=\n\t" \
    "s_cmp_eq_u32 s41, 0\n\t" \
    "s_cbranch_scc1 .Lw26_%=\n\t" \
    "s_sub_u32 s41, s41, 1\n\t" \
    "s_branch .Lw25_%=\n\t" \
    ".Lw26_%=:\n\t" \
    "ds_write_b128 v250, v[208:211] offset:0\n\t" \
    "ds_write_b128 v250, v[212:215] offset:8192\n\t" \
    "s_cmp_gt_u32 %[wave], 3\n\t" \
    "s_cbranch_scc1 .Lw211_%=\n\t" \
    "ds_write_b128 v250, v[216:219] offset:16384\n\t" \
    ".Lw211_%=:\n\t" \
    "s_add_u32 s46, s43, s48\n\t" \
    "s_waitcnt lgkmcnt(0)\n\t" \
    "s_barrier \n\t" \
    "s_setprio 0\n\t" \
    "ds_read_b128 v[176:179], v249 offset:0\n\t" \
    "buffer_load_dwordx2 v[232:233], v254, %[rs], s50 offen sc1\n\t" \
    "buffer_load_dwordx2 v[234:235], v251, %[rs], s51 offen sc1\n\t" \
    "ds_read_b128 v[180:183], v249 offset:1024\n\t" \
    "ds_read_b128 v[184:187], v249 offset:2048\n\t" \
    "ds_read_b128 v[188:191], v249 offset:3072\n\t" \
    "ds_read_b128 v[192:195], v249 offset:4096\n\t" \
    "ds_read_b128 v[196:199], v249 offset:5120\n\t" \
    "ds_read_b128 v[200:203], v249 offset:6144\n\t" \
    "s_waitcnt lgkmcnt(6)\n\t" \
    "v_mfma_f32_16x16x32_bf16 v[168:171], v[0:3], v[176:179], 0\n\t" \
    "ds_read_b128 v[204:207], v249 offset:7168\n\t" \
    "s_waitcnt lgkmcnt(6)\n\t" \
    "v_mfma_f32_16x16x32_bf16 v[168:171], v[4:7], v[180:183], v[168:171]\n\t" \
    "ds_read_b128 v[176:179], v249 offset:8192\n\t" \
    "s_waitcnt lgkmcnt(6)\n\t" \
    "v_mfma_f32_16x16x32_bf16 v[168:171], v[8:11], v[184:187], v[168:171]\n\t" \
    "ds_read_b128 v[180:183], v249 offset:9216\n\t" \
    "s_waitcnt lgkmcnt(6)\n\t" \
    "v_mfma_f32_16x16x32_bf16 v[168:171], v[12:15], v[188:191], v[168:171]\n\t" \
    "ds_read_b128 v[184:187], v249 offset:10240\n\t" \
    "s_add_u32 s47, s46, 0x0\n\t" \
    "buffer_load_dwordx4 v[208:211], v253, %[rs], s47 offen sc1\n\t" \
    "s_add_u32 s47, s46, 0x8000\n\t" \
    "buffer_load_dwordx4 v[212:215], v253, %[rs], s47 offen sc1\n\t" \
    "s_cmp_gt_u32 %[wave], 3\n\t" \
    "s_cbranch_scc1 .Lw212_%=\n\t" \
    "s_add_u32 s47, s46, 0x10000\n\t" \
    "buffer_load_dwordx4 v[216:219], v253, %[rs], s47 offen sc1\n\t" \
    ".Lw212_%=:\n\t" \
    "s_waitcnt lgkmcnt(6)\n\t" \
    "v_mfma_f32_16x16x32_bf16 v[168:171], v[16:19], v[192:195], v[168:171]\n\t" \
    "ds_read_b128 v[188:191], v249 offset:11264\n\t" \
    "s_waitcnt lgkmcnt(6)\n\t" \
    "v_mfma_f32_16x16x32_bf16 v[168:171], v[20:23], v[196:199], v[168:171]\n\t" \
    "ds_read_b128 v[192:195], v249 offset:12288\n\t" \
    "s_waitcnt lgkmcnt(6)\n\t" \
    "v_mfma_f32_16x16x32_bf16 v[168:171], v[24:27], v[200:203], v[168:171]\n\t" \
    "ds_read_b128 v[196:199], v249 offset:13312\n\t" \
    "s_waitcnt lgkmcnt(6)\n\t" \
    "v_mfma_f32_16x16x32_bf16 v[168:171], v[28:31], v[204:207], v[168:171]\n\t" \
    "ds_read_b128 v[200:203], v249 offset:14336\n\t" \
    "s_waitcnt lgkmcnt(6)\n\t" \
    "v_mfma_f32_16x16x32_bf16 v[168:171], v[32:35], v[176:179], v[168:171]\n\t" \
    "ds_read_b128 v[204:207], v249 offset:15360\n\t" \
    "s_waitcnt lgkmcnt(6)\n\t" \
    "v_mfma_f32_16x16x32_bf16 v[168:171], v[36:39], v[180:183], v[168:171]\n\t" \
    "ds_read_b128 v[176:179], v249 offset:16384\n\t" \
    "s_waitcnt lgkmcnt(6)\n\t" \
    "v_mfma_f32_16x16x32_bf16 v[168:171], v[40:43], v[184:187], v[168:171]\n\t" \
    "ds_read_b128 v[180:183], v249 offset:17408\n\t" \
    "s_waitcnt lgkmcnt(6)\n\t" \
    "v_mfma_f32_16x16x32_bf16 v[168:171], v[44:47], v[188:191], v[168:171]\n\t" \
    "ds_read_b128 v[184:187], v249 offset:18432\n\t" \
    "s_waitcnt lgkmcnt(6)\n\t" \
    "v_mfma_f32_16x16x32_bf16 v[168:171], v[48:51], v[192:195], v[168:171]\n\t" \
    "ds_read_b128 v[188:191], v249 offset:19456\n\t" \
    "s_waitcnt lgkmcnt(6)\n\t" \
    "v_mfma_f32_16x16x32_bf16 v[168:171], v[52:55], v[196:199], v[168:171]\n\t" \
    "s_waitcnt lgkmcnt(5)\n\t" \
    "v_mfma_f32_16x16x32_bf16 v[168:171], v[56:59], v[200:203], v[168:171]\n\t" \
    "s_waitcnt lgkmcnt(4)\n\t" \
    "v_mfma_f32_16x16x32_bf16 v[168:171], v[60:63], v[204:207], v[168:171]\n\t" \
    "s_waitcnt lgkmcnt(3)\n\t" \
    "v_mfma_f32_16x16x32_bf16 v[168:171], v[64:67], v[176:179], v[168:171]\n\t" \
    "s_waitcnt lgkmcnt(2)\n\t" \
    "v_mfma_f32_16x16x32_bf16 v[168:171], v[68:71], v[180:183], v[168:171]\n\t" \
    "s_waitcnt lgkmcnt(1)\n\t" \
    "v_mfma_f32_16x16x32_bf16 v[168:171], v[72:75], v[184:187], v[168:171]\n\t" \
    "s_waitcnt lgkmcnt(0)\n\t" \
    "v_mfma_f32_16x16x32_bf16 v[168:171], v[76:79], v[188:191], v[168:171]\n\t" \
    "s_waitcnt vmcnt(0)\n\t" \
    ".Lw213_%=:\n\t" \
    "v_max_u32 v248, v234, v235\n\t" \
    "v_cmp_eq_u32 vcc, -1, v248\n\t" \
    "s_cbranch_vccz .Lw214_%=\n\t" \
    "s_cmp_eq_u32 s41, 0\n\t" \
    "s_cbranch_scc1 .Lw214_%=\n\t" \
    "s_sub_u32 s41, s41, 1\n\t" \
    "buffer_load_dwordx2 v[234:235], v251, %[rs], s51 offen sc1\n\t" \
    "s_waitcnt vmcnt(0)\n\t" \
    "s_branch .Lw213_%=\n\t" \
    ".Lw214_%=:\n\t" \
    "v_lshlrev_b32 v176, 16, v232\n\t" \
    "v_and_b32 v177, 0xffff0000, v232\n\t" \
    "v_lshlrev_b32 v178, 16, v233\n\t" \
    "v_and_b32 v179, 0xffff0000, v233\n\t" \
    "v_sub_f32 v176, v176, v234\n\t" \
    "v_sub_f32 v177, v177, v234\n\t" \
    "v_sub_f32 v178, v178, v234\n\t" \
    "v_sub_f32 v179, v179, v234\n\t" \
    "v_mul_f32 v176, v176, v235\n\t" \
    "v_mul_f32 v177, v177, v235\n\t" \
    "v_mul_f32 v178, v178, v235\n\t" \
    "v_mul_f32 v179, v179, v235\n\t" \
    "v_cvt_pk_bf16_f32 v180, v176, v177\n\t" \
    "v_cvt_pk_bf16_f32 v181, v178, v179\n\t" \
    "v_lshlrev_b32 v176, 16, v180\n\t" \
    "v_and_b32 v177, 0xffff0000, v180\n\t" \
    "v_lshlrev_b32 v178, 16, v181\n\t" \
    "v_and_b32 v179, 0xffff0000, v181\n\t" \
    "v_add_f32 v168, v176, v168\n\t" \
    "v_add_f32 v169, v177, v169\n\t" \
    "v_add_f32 v170, v178, v170\n\t" \
    "v_add_f32 v171, v179, v171\n\t" \
    "v_cvt_pk_bf16_f32 v184, v168, v169\n\t" \
    "v_cvt_pk_bf16_f32 v185, v170, v171\n\t" \
    "v_min_u32 v184, -2, v184\n\t" \
    "v_min_u32 v185, -2, v185\n\t" \
    "buffer_store_dwordx2 v[184:185], v254, %[rs], s49 offen sc1\n\t" \
    "s_cmp_ge_u32 s40, 122\n\t" \
    "s_cbranch_scc1 .Lw215_%=\n\t" \
    "s_add_i32 s44, s40, 6\n\t" \
    "s_max_i32 s44, s44, 0\n\t" \
    "s_lshr_b32 s45, s44, 1\n\t" \
    "s_and_b32 s44, s44, 1\n\t" \
    "s_lshl_b32 s44, s44, 12\n\t" \
    "s_mul_i32 s50, s45, 0x14000\n\t" \
    "s_add_u32 s50, s50, s44\n\t" \
    "s_lshr_b32 s45, s45, 4\n\t" \
    "s_mul_i32 s45, s45, 0xc2000\n\t" \
    "s_add_u32 s50, s50, s45\n\t" \
    "s_add_u32 s50, s50, 0x2c00000\n\t" \
    "v_mov_b64 v[188:189], -1\n\t" \
    "buffer_store_dwordx2 v[188:189], v254, %[rs], s50 offen sc1\n\t" \
    ".Lw215_%=:\n\t" \
    "v_xor_b32 v249, 0x8000, v249\n\t" \
    "v_xor_b32 v250, 0x8000, v250\n\t" \
    "s_add_u32 s40, s40, 1\n\t" \
    "s_cmp_lg_u32 s40, 128\n\t" \
    "s_cbranch_scc1 .Lw24_%=\n\t" \
    "s_branch .Lend%=\n\t" \
    ".Lend%=:\n\t" \
    "s_waitcnt vmcnt(0) lgkmcnt(0)\n\t" \
    "s_mov_b32 %[bud], s41\n\t"
#define ASM_W3_LOOP \
    "s_mov_b32 s40, 0\n\t" \
    "s_mov_b32 s41, 0x20000\n\t" \
    "s_add_u32 m0, %[wm], 0x0\n\t" \
    "s_add_u32 s44, %[wo], 0x0\n\t" \
    "buffer_load_dwordx4 v176, %[wr], s44 offen nt lds\n\t" \
    "s_add_u32 m0, %[wm], 0x2000\n\t" \
    "s_add_u32 s44, %[wo], 0xc000\n\t" \
    "buffer_load_dwordx4 v176, %[wr], s44 offen nt lds\n\t" \
    "s_add_u32 m0, %[wm], 0x4000\n\t" \
    "s_add_u32 s44, %[wo], 0x18000\n\t" \
    "buffer_load_dwordx4 v176, %[wr], s44 offen nt lds\n\t" \
    "s_add_u32 m0, %[wm], 0x6000\n\t" \
    "s_add_u32 s44, %[wo], 0x24000\n\t" \
    "buffer_load_dwordx4 v176, %[wr], s44 offen nt lds\n\t" \
    "s_add_u32 m0, %[wm], 0x8000\n\t" \
    "s_add_u32 s44, %[wo], 0x30000\n\t" \
    "buffer_load_dwordx4 v176, %[wr], s44 offen nt lds\n\t" \
    "s_add_u32 m0, %[wm], 0xa000\n\t" \
    "s_add_u32 s44, %[wo], 0x3c000\n\t" \
    "buffer_load_dwordx4 v176, %[wr], s44 offen nt lds\n\t" \
    "s_add_u32 m0, %[wm], 0xc000\n\t" \
    "s_add_u32 s44, %[wo], 0x48000\n\t" \
    "buffer_load_dwordx4 v176, %[wr], s44 offen nt lds\n\t" \
    "s_add_u32 m0, %[wm], 0xe000\n\t" \
    "s_add_u32 s44, %[wo], 0x54000\n\t" \
    "buffer_load_dwordx4 v176, %[wr], s44 offen nt lds\n\t" \
    "s_add_u32 m0, %[wm], 0x10000\n\t" \
    "s_add_u32 s44, %[wo], 0x60000\n\t" \
    "buffer_load_dwordx4 v176, %[wr], s44 offen nt lds\n\t" \
    "s_add_u32 m0, %[wm], 0x12000\n\t" \
    "s_add_u32 s44, %[wo], 0x6c000\n\t" \
    "buffer_load_dwordx4 v176, %[wr], s44 offen nt lds\n\t" \
    "s_add_u32 m0, %[wm], 0x14000\n\t" \
    "s_add_u32 s44, %[wo], 0x78000\n\t" \
    "buffer_load_dwordx4 v176, %[wr], s44 offen nt lds\n\t" \
    "s_add_u32 m0, %[wm], 0x16000\n\t" \
    "s_add_u32 s44, %[wo], 0x84000\n\t" \
    "buffer_load_dwordx4 v176, %[wr], s44 offen nt lds\n\t" \
    "s_add_u32 m0, %[wm], 0x18000\n\t" \
    "s_add_u32 s44, %[wo], 0x90000\n\t" \
    "buffer_load_dwordx4 v176, %[wr], s44 offen nt lds\n\t" \
    "s_add_u32 m0, %[wm], 0x1a000\n\t" \
    "s_add_u32 s44, %[wo], 0x9c000\n\t" \
    "buffer_load_dwordx4 v176, %[wr], s44 offen nt lds\n\t" \
    "s_add_u32 m0, %[wm], 0x1c000\n\t" \
    "s_add_u32 s44, %[wo], 0xa8000\n\t" \
    "buffer_load_dwordx4 v176, %[wr], s44 offen nt lds\n\t" \
    "s_add_u32 m0, %[wm], 0x1e000\n\t" \
    "s_add_u32 s44, %[wo], 0xb4000\n\t" \
    "buffer_load_dwordx4 v176, %[wr], s44 offen nt lds\n\t" \
    "s_add_u32 m0, %[wm], 0x20000\n\t" \
    "s_add_u32 s44, %[wo], 0xc0000\n\t" \
    "buffer_load_dwordx4 v176, %[wr], s44 offen nt lds\n\t" \
    "s_add_u32 m0, %[wm], 0x22000\n\t" \
    "s_add_u32 s44, %[wo], 0xcc000\n\t" \
    "buffer_load_dwordx4 v176, %[wr], s44 offen nt lds\n\t" \
    "s_add_u32 m0, %[wm], 0x24000\n\t" \
    "s_add_u32 s44, %[wo], 0xd8000\n\t" \
    "buffer_load_dwordx4 v176, %[wr], s44 offen nt lds\n\t" \
    "s_add_u32 m0, %[wm], 0x26000\n\t" \
    "s_add_u32 s44, %[wo], 0xe4000\n\t" \
    "buffer_load_dwordx4 v176, %[wr], s44 offen nt lds\n\t" \
    "s_waitcnt vmcnt(0) lgkmcnt(0)\n\t" \
    "s_barrier \n\t" \
    "v_add_u32 v178, 0x0, v177\n\t" \
    "v_xor_b32 v179, 32, v178\n\t" \
    "ds_read_b64_tr_b16 v[0:1], v178 offset:0\n\t" \
    "ds_read_b64_tr_b16 v[2:3], v178 offset:2048\n\t" \
    "ds_read_b64_tr_b16 v[80:81], v179 offset:0\n\t" \
    "ds_read_b64_tr_b16 v[82:83], v179 offset:2048\n\t" \
    "ds_read_b64_tr_b16 v[4:5], v178 offset:16384\n\t" \
    "ds_read_b64_tr_b16 v[6:7], v178 offset:18432\n\t" \
    "ds_read_b64_tr_b16 v[84:85], v179 offset:16384\n\t" \
    "ds_read_b64_tr_b16 v[86:87], v179 offset:18432\n\t" \
    "v_add_u32 v178, 0x8000, v177\n\t" \
    "v_xor_b32 v179, 32, v178\n\t" \
    "ds_read_b64_tr_b16 v[8:9], v178 offset:0\n\t" \
    "ds_read_b64_tr_b16 v[10:11], v178 offset:2048\n\t" \
    "ds_read_b64_tr_b16 v[88:89], v179 offset:0\n\t" \
    "ds_read_b64_tr_b16 v[90:91], v179 offset:2048\n\t" \
    "ds_read_b64_tr_b16 v[12:13], v178 offset:16384\n\t" \
    "ds_read_b64_tr_b16 v[14:15], v178 offset:18432\n\t" \
    "ds_read_b64_tr_b16 v[92:93], v179 offset:16384\n\t" \
    "ds_read_b64_tr_b16 v[94:95], v179 offset:18432\n\t" \
    "v_add_u32 v178, 0x10000, v177\n\t" \
    "v_xor_b32 v179, 32, v178\n\t" \
    "ds_read_b64_tr_b16 v[16:17], v178 offset:0\n\t" \
    "ds_read_b64_tr_b16 v[18:19], v178 offset:2048\n\t" \
    "ds_read_b64_tr_b16 v[96:97], v179 offset:0\n\t" \
    "ds_read_b64_tr_b16 v[98:99], v179 offset:2048\n\t" \
    "ds_read_b64_tr_b16 v[20:21], v178 offset:16384\n\t" \
    "ds_read_b64_tr_b16 v[22:23], v178 offset:18432\n\t" \
    "ds_read_b64_tr_b16 v[100:101], v179 offset:16384\n\t" \
    "ds_read_b64_tr_b16 v[102:103], v179 offset:18432\n\t" \
    "v_add_u32 v178, 0x18000, v177\n\t" \
    "v_xor_b32 v179, 32, v178\n\t" \
    "ds_read_b64_tr_b16 v[24:25], v178 offset:0\n\t" \
    "ds_read_b64_tr_b16 v[26:27], v178 offset:2048\n\t" \
    "ds_read_b64_tr_b16 v[104:105], v179 offset:0\n\t" \
    "ds_read_b64_tr_b16 v[106:107], v179 offset:2048\n\t" \
    "ds_read_b64_tr_b16 v[28:29], v178 offset:16384\n\t" \
    "ds_read_b64_tr_b16 v[30:31], v178 offset:18432\n\t" \
    "ds_read_b64_tr_b16 v[108:109], v179 offset:16384\n\t" \
    "ds_read_b64_tr_b16 v[110:111], v179 offset:18432\n\t" \
    "v_add_u32 v178, 0x20000, v177\n\t" \
    "v_xor_b32 v179, 32, v178\n\t" \
    "ds_read_b64_tr_b16 v[32:33], v178 offset:0\n\t" \
    "ds_read_b64_tr_b16 v[34:35], v178 offset:2048\n\t" \
    "ds_read_b64_tr_b16 v[112:113], v179 offset:0\n\t" \
    "ds_read_b64_tr_b16 v[114:115], v179 offset:2048\n\t" \
    "ds_read_b64_tr_b16 v[36:37], v178 offset:16384\n\t" \
    "ds_read_b64_tr_b16 v[38:39], v178 offset:18432\n\t" \
    "ds_read_b64_tr_b16 v[116:117], v179 offset:16384\n\t" \
    "ds_read_b64_tr_b16 v[118:119], v179 offset:18432\n\t" \
    "s_waitcnt lgkmcnt(0)\n\t" \
    "s_barrier \n\t" \
    "s_add_u32 m0, %[wm], 0x0\n\t" \
    "s_add_u32 s44, %[wo], 0xf0000\n\t" \
    "buffer_load_dwordx4 v176, %[wr], s44 offen nt lds\n\t" \
    "s_add_u32 m0, %[wm], 0x2000\n\t" \
    "s_add_u32 s44, %[wo], 0xfc000\n\t" \
    "buffer_load_dwordx4 v176, %[wr], s44 offen nt lds\n\t" \
    "s_add_u32 m0, %[wm], 0x4000\n\t" \
    "s_add_u32 s44, %[wo], 0x108000\n\t" \
    "buffer_load_dwordx4 v176, %[wr], s44 offen nt lds\n\t" \
    "s_add_u32 m0, %[wm], 0x6000\n\t" \
    "s_add_u32 s44, %[wo], 0x114000\n\t" \
    "buffer_load_dwordx4 v176, %[wr], s44 offen nt lds\n\t" \
    "s_add_u32 m0, %[wm], 0x8000\n\t" \
    "s_add_u32 s44, %[wo], 0x120000\n\t" \
    "buffer_load_dwordx4 v176, %[wr], s44 offen nt lds\n\t" \
    "s_add_u32 m0, %[wm], 0xa000\n\t" \
    "s_add_u32 s44, %[wo], 0x12c000\n\t" \
    "buffer_load_dwordx4 v176, %[wr], s44 offen nt lds\n\t" \
    "s_add_u32 m0, %[wm], 0xc000\n\t" \
    "s_add_u32 s44, %[wo], 0x138000\n\t" \
    "buffer_load_dwordx4 v176, %[wr], s44 offen nt lds\n\t" \
    "s_add_u32 m0, %[wm], 0xe000\n\t" \
    "s_add_u32 s44, %[wo], 0x144000\n\t" \
    "buffer_load_dwordx4 v176, %[wr], s44 offen nt lds\n\t" \
    "s_add_u32 m0, %[wm], 0x10000\n\t" \
    "s_add_u32 s44, %[wo], 0x150000\n\t" \
    "buffer_load_dwordx4 v176, %[wr], s44 offen nt lds\n\t" \
    "s_add_u32 m0, %[wm], 0x12000\n\t" \
    "s_add_u32 s44, %[wo], 0x15c000\n\t" \
    "buffer_load_dwordx4 v176, %[wr], s44 offen nt lds\n\t" \
    "s_add_u32 m0, %[wm], 0x14000\n\t" \
    "s_add_u32 s44, %[wo], 0x168000\n\t" \
    "buffer_load_dwordx4 v176, %[wr], s44 offen nt lds\n\t" \
    "s_add_u32 m0, %[wm], 0x16000\n\t" \
    "s_add_u32 s44, %[wo], 0x174000\n\t" \
    "buffer_load_dwordx4 v176, %[wr], s44 offen nt lds\n\t" \
    "s_add_u32 m0, %[wm], 0x18000\n\t" \
    "s_add_u32 s44, %[wo], 0x180000\n\t" \
    "buffer_load_dwordx4 v176, %[wr], s44 offen nt lds\n\t" \
    "s_add_u32 m0, %[wm], 0x1a000\n\t" \
    "s_add_u32 s44, %[wo], 0x18c000\n\t" \
    "buffer_load_dwordx4 v176, %[wr], s44 offen nt lds\n\t" \
    "s_add_u32 m0, %[wm], 0x1c000\n\t" \
    "s_add_u32 s44, %[wo], 0x198000\n\t" \
    "buffer_load_dwordx4 v176, %[wr], s44 offen nt lds\n\t" \
    "s_add_u32 m0, %[wm], 0x1e000\n\t" \
    "s_add_u32 s44, %[wo], 0x1a4000\n\t" \
    "buffer_load_dwordx4 v176, %[wr], s44 offen nt lds\n\t" \
    "s_add_u32 m0, %[wm], 0x20000\n\t" \
    "s_add_u32 s44, %[wo], 0x1b0000\n\t" \
    "buffer_load_dwordx4 v176, %[wr], s44 offen nt lds\n\t" \
    "s_add_u32 m0, %[wm], 0x22000\n\t" \
    "s_add_u32 s44, %[wo], 0x1bc000\n\t" \
    "buffer_load_dwordx4 v176, %[wr], s44 offen nt lds\n\t" \
    "s_add_u32 m0, %[wm], 0x24000\n\t" \
    "s_add_u32 s44, %[wo], 0x1c8000\n\t" \
    "buffer_load_dwordx4 v176, %[wr], s44 offen nt lds\n\t" \
    "s_add_u32 m0, %[wm], 0x26000\n\t" \
    "s_add_u32 s44, %[wo], 0x1d4000\n\t" \
    "buffer_load_dwordx4 v176, %[wr], s44 offen nt lds\n\t" \
    "s_waitcnt vmcnt(0) lgkmcnt(0)\n\t" \
    "s_barrier \n\t" \
    "v_add_u32 v178, 0x0, v177\n\t" \
    "v_xor_b32 v179, 32, v178\n\t" \
    "ds_read_b64_tr_b16 v[40:41], v178 offset:0\n\t" \
    "ds_read_b64_tr_b16 v[42:43], v178 offset:2048\n\t" \
    "ds_read_b64_tr_b16 v[120:121], v179 offset:0\n\t" \
    "ds_read_b64_tr_b16 v[122:123], v179 offset:2048\n\t" \
    "ds_read_b64_tr_b16 v[44:45], v178 offset:16384\n\t" \
    "ds_read_b64_tr_b16 v[46:47], v178 offset:18432\n\t" \
    "ds_read_b64_tr_b16 v[124:125], v179 offset:16384\n\t" \
    "ds_read_b64_tr_b16 v[126:127], v179 offset:18432\n\t" \
    "v_add_u32 v178, 0x8000, v177\n\t" \
    "v_xor_b32 v179, 32, v178\n\t" \
    "ds_read_b64_tr_b16 v[48:49], v178 offset:0\n\t" \
    "ds_read_b64_tr_b16 v[50:51], v178 offset:2048\n\t" \
    "ds_read_b64_tr_b16 v[128:129], v179 offset:0\n\t" \
    "ds_read_b64_tr_b16 v[130:131], v179 offset:2048\n\t" \
    "ds_read_b64_tr_b16 v[52:53], v178 offset:16384\n\t" \
    "ds_read_b64_tr_b16 v[54:55], v178 offset:18432\n\t" \
    "ds_read_b64_tr_b16 v[132:133], v179 offset:16384\n\t" \
    "ds_read_b64_tr_b16 v[134:135], v179 offset:18432\n\t" \
    "v_add_u32 v178, 0x10000, v177\n\t" \
    "v_xor_b32 v179, 32, v178\n\t" \
    "ds_read_b64_tr_b16 v[56:57], v178 offset:0\n\t" \
    "ds_read_b64_tr_b16 v[58:59], v178 offset:2048\n\t" \
    "ds_read_b64_tr_b16 v[136:137], v179 offset:0\n\t" \
    "ds_read_b64_tr_b16 v[138:139], v179 offset:2048\n\t" \
    "ds_read_b64_tr_b16 v[60:61], v178 offset:16384\n\t" \
    "ds_read_b64_tr_b16 v[62:63], v178 offset:18432\n\t" \
    "ds_read_b64_tr_b16 v[140:141], v179 offset:16384\n\t" \
    "ds_read_b64_tr_b16 v[142:143], v179 offset:18432\n\t" \
    "v_add_u32 v178, 0x18000, v177\n\t" \
    "v_xor_b32 v179, 32, v178\n\t" \
    "ds_read_b64_tr_b16 v[64:65], v178 offset:0\n\t" \
    "ds_read_b64_tr_b16 v[66:67], v178 offset:2048\n\t" \
    "ds_read_b64_tr_b16 v[144:145], v179 offset:0\n\t" \
    "ds_read_b64_tr_b16 v[146:147], v179 offset:2048\n\t" \
    "ds_read_b64_tr_b16 v[68:69], v178 offset:16384\n\t" \
    "ds_read_b64_tr_b16 v[70:71], v178 offset:18432\n\t" \
    "ds_read_b64_tr_b16 v[148:149], v179 offset:16384\n\t" \
    "ds_read_b64_tr_b16 v[150:151], v179 offset:18432\n\t" \
    "v_add_u32 v178, 0x20000, v177\n\t" \
    "v_xor_b32 v179, 32, v178\n\t" \
    "ds_read_b64_tr_b16 v[72:73], v178 offset:0\n\t" \
    "ds_read_b64_tr_b16 v[74:75], v178 offset:2048\n\t" \
    "ds_read_b64_tr_b16 v[152:153], v179 offset:0\n\t" \
    "ds_read_b64_tr_b16 v[154:155], v179 offset:2048\n\t" \
    "ds_read_b64_tr_b16 v[76:77], v178 offset:16384\n\t" \
    "ds_read_b64_tr_b16 v[78:79], v178 offset:18432\n\t" \
    "ds_read_b64_tr_b16 v[156:157], v179 offset:16384\n\t" \
    "ds_read_b64_tr_b16 v[158:159], v179 offset:18432\n\t" \
    "v_mov_b32 v228, 0x3f803f80\n\t" \
    "v_mov_b32 v229, 0x3f803f80\n\t" \
    "v_mov_b32 v230, 0x3f803f80\n\t" \
    "v_mov_b32 v231, 0x3f803f80\n\t" \
    "v_mov_b64 v[216:217], 0\n\t" \
    "v_mov_b32 v219, 0\n\t" \
    "v_bfe_u32 v255, v253, 2, 5\n\t" \
    "v_add_u32 v255, 0x7000, v255\n\t" \
    "v_bfe_u32 v218, v253, 6, 1\n\t" \
    "s_waitcnt lgkmcnt(0)\n\t" \
    "s_barrier \n\t" \
    "ds_write_b32 v251, v228 offset:72\n\t" \
    "ds_write_b32 v251, v228 offset:2120\n\t" \
    "v_mfma_f32_16x16x32_bf16 v[160:163], v[0:3], v[228:231], 0\n\t" \
    "v_mfma_f32_16x16x32_bf16 v[164:167], v[80:83], v[228:231], 0\n\t" \
    "v_mfma_f32_16x16x32_bf16 v[160:163], v[4:7], v[228:231], v[160:163]\n\t" \
    "v_mfma_f32_16x16x32_bf16 v[164:167], v[84:87], v[228:231], v[164:167]\n\t" \
    "v_mfma_f32_16x16x32_bf16 v[160:163], v[8:11], v[228:231], v[160:163]\n\t" \
    "v_mfma_f32_16x16x32_bf16 v[164:167], v[88:91], v[228:231], v[164:167]\n\t" \
    "v_mfma_f32_16x16x32_bf16 v[160:163], v[12:15], v[228:231], v[160:163]\n\t" \
    "v_mfma_f32_16x16x32_bf16 v[164:167], v[92:95], v[228:231], v[164:167]\n\t" \
    "v_mfma_f32_16x16x32_bf16 v[160:163], v[16:19], v[228:231], v[160:163]\n\t" \
    "v_mfma_f32_16x16x32_bf16 v[164:167], v[96:99], v[228:231], v[164:167]\n\t" \
    "v_mfma_f32_16x16x32_bf16 v[160:163], v[20:23], v[228:231], v[160:163]\n\t" \
    "v_mfma_f32_16x16x32_bf16 v[164:167], v[100:103], v[228:231], v[164:167]\n\t" \
    "v_mfma_f32_16x16x32_bf16 v[160:163], v[24:27], v[228:231], v[160:163]\n\t" \
    "v_mfma_f32_16x16x32_bf16 v[164:167], v[104:107], v[228:231], v[164:167]\n\t" \
    "v_mfma_f32_16x16x32_bf16 v[160:163], v[28:31], v[228:231], v[160:163]\n\t" \
    "v_mfma_f32_16x16x32_bf16 v[164:167], v[108:111], v[228:231], v[164:167]\n\t" \
    "v_mfma_f32_16x16x32_bf16 v[160:163], v[32:35], v[228:231], v[160:163]\n\t" \
    "v_mfma_f32_16x16x32_bf16 v[164:167], v[112:115], v[228:231], v[164:167]\n\t" \
    "v_mfma_f32_16x16x32_bf16 v[160:163], v[36:39], v[228:231], v[160:163]\n\t" \
    "v_mfma_f32_16x16x32_bf16 v[164:167], v[116:119], v[228:231], v[164:167]\n\t" \
    "v_mfma_f32_16x16x32_bf16 v[160:163], v[40:43], v[228:231], v[160:163]\n\t" \
    "v_mfma_f32_16x16x32_bf16 v[164:167], v[120:123], v[228:231], v[164:167]\n\t" \
    "v_mfma_f32_16x16x32_bf16 v[160:163], v[44:47], v[228:231], v[160:163]\n\t" \
    "v_mfma_f32_16x16x32_bf16 v[164:167], v[124:127], v[228:231], v[164:167]\n\t" \
    "v_mfma_f32_16x16x32_bf16 v[160:163], v[48:51], v[228:231], v[160:163]\n\t" \
    "v_mfma_f32_16x16x32_bf16 v[164:167], v[128:131], v[228:231], v[164:167]\n\t" \
    "v_mfma_f32_16x16x32_bf16 v[160:163], v[52:55], v[228:231], v[160:163]\n\t" \
    "v_mfma_f32_16x16x32_bf16 v[164:167], v[132:135], v[228:231], v[164:167]\n\t" \
    "v_mfma_f32_16x16x32_bf16 v[160:163], v[56:59], v[228:231], v[160:163]\n\t" \
    "v_mfma_f32_16x16x32_bf16 v[164:167], v[136:139], v[228:231], v[164:167]\n\t" \
    "v_mfma_f32_16x16x32_bf16 v[160:163], v[60:63], v[228:231], v[160:163]\n\t" \
    "v_mfma_f32_16x16x32_bf16 v[164:167], v[140:143], v[228:231], v[164:167]\n\t" \
    "v_mfma_f32_16x16x32_bf16 v[160:163], v[64:67], v[228:231], v[160:163]\n\t" \
    "v_mfma_f32_16x16x32_bf16 v[164:167], v[144:147], v[228:231], v[164:167]\n\t" \
    "v_mfma_f32_16x16x32_bf16 v[160:163], v[68:71], v[228:231], v[160:163]\n\t" \
    "v_mfma_f32_16x16x32_bf16 v[164:167], v[148:151], v[228:231], v[164:167]\n\t" \
    "v_mfma_f32_16x16x32_bf16 v[160:163], v[72:75], v[228:231], v[160:163]\n\t" \
    "v_mfma_f32_16x16x32_bf16 v[164:167], v[152:155], v[228:231], v[164:167]\n\t" \
    "v_mfma_f32_16x16x32_bf16 v[160:163], v[76:79], v[228:231], v[160:163]\n\t" \
    "v_mfma_f32_16x16x32_bf16 v[164:167], v[156:159], v[228:231], v[164:167]\n\t" \
    "s_mov_b32 s52, 0x22222222\n\t" \
    "s_mov_b32 s53, 0x22222222\n\t" \
    "s_mov_b32 s54, 0x44444444\n\t" \
    "s_mov_b32 s55, 0x44444444\n\t" \
    "s_mov_b32 s56, 0x88888888\n\t" \
    "s_mov_b32 s57, 0x88888888\n\t" \
    "ds_write_b32 v255, v218\n\t" \
    "s_waitcnt lgkmcnt(0)\n\t" \
    "s_barrier \n\t" \
    ".Lw31_%=:\n\t" \
    "v_mov_b32 v248, 0\n\t" \
    "buffer_load_dword v248, v248, %[rs], %[go] offen sc1\n\t" \
    "s_waitcnt vmcnt(0)\n\t" \
    "v_cmp_ne_u32 vcc, 0, v248\n\t" \
    "s_cbranch_vccnz .Lw32_%=\n\t" \
    "s_cmp_eq_u32 s41, 0\n\t" \
    "s_cbranch_scc1 .Lw32_%=\n\t" \
    "s_sub_u32 s41, s41, 1\n\t" \
    "s_sleep 16\n\t" \
    "s_branch .Lw31_%=\n\t" \
    ".Lw32_%=:\n\t" \
    "s_mov_b32 s43, 0\n\t" \
    "s_add_u32 s46, s43, %[pi]\n\t" \
    "s_cmp_gt_u32 %[wave], 3\n\t" \
    "s_cbranch_scc1 .Lw33_%=\n\t" \
    "s_add_u32 s47, s46, 0x0\n\t" \
    "buffer_load_dwordx4 v[208:211], v253, %[rs], s47 offen sc1\n\t" \
    "s_add_u32 s47, s46, 0x4000\n\t" \
    "buffer_load_dwordx4 v[212:215], v253, %[rs], s47 offen sc1\n\t" \
    "s_add_u32 s47, s46, 0x8000\n\t" \
    "buffer_load_dwordx4 v[216:219], v253, %[rs], s47 offen sc1\n\t" \
    "s_add_u32 s47, s46, 0xc000\n\t" \
    "buffer_load_dwordx4 v[220:223], v253, %[rs], s47 offen sc1\n\t" \
    "s_add_u32 s47, s46, 0x10000\n\t" \
    "buffer_load_dwordx4 v[224:227], v253, %[rs], s47 offen sc1\n\t" \
    ".Lw33_%=:\n\t" \
    "s_waitcnt vmcnt(0)\n\t" \
    ".Lw34_%=:\n\t" \
    "s_add_i32 s44, s40, 0\n\t" \
    "s_max_i32 s44, s44, 0\n\t" \
    "s_lshr_b32 s45, s44, 1\n\t" \
    "s_and_b32 s44, s44, 1\n\t" \
    "s_lshl_b32 s44, s44, 12\n\t" \
    "s_mul_i32 s49, s45, 0x30000\n\t" \
    "s_add_u32 s49, s49, s44\n\t" \
    "s_lshr_b32 s45, s45, 3\n\t" \
    "s_mul_i32 s45, s45, 0x82000\n\t" \
    "s_add_u32 s49, s49, s45\n\t" \
    "s_add_u32 s49, s49, %[ng]\n\t" \
    "s_add_i32 s44, s40, 1\n\t" \
    "s_max_i32 s44, s44, 0\n\t" \
    "s_lshr_b32 s45, s44, 1\n\t" \
    "s_and_b32 s44, s44, 1\n\t" \
    "s_lshl_b32 s44, s44, 12\n\t" \
    "s_mul_i32 s43, s45, 0x14000\n\t" \
    "s_add_u32 s43, s43, s44\n\t" \
    "s_lshr_b32 s45, s45, 4\n\t" \
    "s_mul_i32 s45, s45, 0xc2000\n\t" \
    "s_add_u32 s43, s43, s45\n\t" \
    "s_cmp_gt_u32 %[wave], 3\n\t" \
    "s_cbranch_scc1 .Lw37_%=\n\t" \
    "s_setprio 2\n\t" \
    "v_max_u32 v248, v208, v209\n\t" \
    "v_max3_u32 v248, v210, v211, v248\n\t" \
    "v_max3_u32 v248, v212, v213, v248\n\t" \
    "v_max3_u32 v248, v214, v215, v248\n\t" \
    "v_max3_u32 v248, v216, v217, v248\n\t" \
    "v_max3_u32 v248, v218, v219, v248\n\t" \
    "v_max3_u32 v248, v220, v221, v248\n\t" \
    "v_max3_u32 v248, v222, v223, v248\n\t" \
    "v_max3_u32 v248, v224, v225, v248\n\t" \
    "v_max3_u32 v248, v226, v227, v248\n\t" \
    "v_cmp_eq_u32 vcc, -1, v248\n\t" \
    "s_cbranch_vccz .Lw36_%=\n\t" \
    ".Lw35_%=:\n\t" \
    "v_max_u32 v248, v208, v209\n\t" \
    "v_max3_u32 v248, v210, v211, v248\n\t" \
    "v_cmp_eq_u32 vcc, -1, v248\n\t" \
    "s_cbranch_vccz .Lw38_%=\n\t" \
    "s_add_u32 s47, s46, 0x0\n\t" \
    "buffer_load_dwordx4 v[208:211], v253, %[rs], s47 offen sc1\n\t" \
    ".Lw38_%=:\n\t" \
    "v_max_u32 v248, v212, v213\n\t" \
    "v_max3_u32 v248, v214, v215, v248\n\t" \
    "v_cmp_eq_u32 vcc, -1, v248\n\t" \
    "s_cbranch_vccz .Lw39_%=\n\t" \
    "s_add_u32 s47, s46, 0x4000\n\t" \
    "buffer_load_dwordx4 v[212:215], v253, %[rs], s47 offen sc1\n\t" \
    ".Lw39_%=:\n\t" \
    "v_max_u32 v248, v216, v217\n\t" \
    "v_max3_u32 v248, v218, v219, v248\n\t" \
    "v_cmp_eq_u32 vcc, -1, v248\n\t" \
    "s_cbranch_vccz .Lw310_%=\n\t" \
    "s_add_u32 s47, s46, 0x8000\n\t" \
    "buffer_load_dwordx4 v[216:219], v253, %[rs], s47 offen sc1\n\t" \
    ".Lw310_%=:\n\t" \
    "v_max_u32 v248, v220, v221\n\t" \
    "v_max3_u32 v248, v222, v223, v248\n\t" \
    "v_cmp_eq_u32 vcc, -1, v248\n\t" \
    "s_cbranch_vccz .Lw311_%=\n\t" \
    "s_add_u32 s47, s46, 0xc000\n\t" \
    "buffer_load_dwordx4 v[220:223], v253, %[rs], s47 offen sc1\n\t" \
    ".Lw311_%=:\n\t" \
    "v_max_u32 v248, v224, v225\n\t" \
    "v_max3_u32 v248, v226, v227, v248\n\t" \
    "v_cmp_eq_u32 vcc, -1, v248\n\t" \
    "s_cbranch_vccz .Lw312_%=\n\t" \
    "s_add_u32 s47, s46, 0x10000\n\t" \
    "buffer_load_dwordx4 v[224:227], v253, %[rs], s47 offen sc1\n\t" \
    ".Lw312_%=:\n\t" \
    "s_waitcnt vmcnt(0)\n\t" \
    "v_max_u32 v248, v208, v209\n\t" \
    "v_max3_u32 v248, v210, v211, v248\n\t" \
    "v_max3_u32 v248, v212, v213, v248\n\t" \
    "v_max3_u32 v248, v214, v215, v248\n\t" \
    "v_max3_u32 v248, v216, v217, v248\n\t" \
    "v_max3_u32 v248, v218, v219, v248\n\t" \
    "v_max3_u32 v248, v220, v221, v248\n\t" \
    "v_max3_u32 v248, v222, v223, v248\n\t" \
    "v_max3_u32 v248, v224, v225, v248\n\t" \
    "v_max3_u32 v248, v226, v227, v248\n\t" \
    "v_cmp_eq_u32 vcc, -1, v248\n\t" \
    "s_cbranch_vccz .Lw36_%=\n\t" \
    "s_cmp_eq_u32 s41, 0\n\t" \
    "s_cbranch_scc1 .Lw36_%=\n\t" \
    "s_sub_u32 s41, s41, 1\n\t" \
    "s_branch .Lw35_%=\n\t" \
    ".Lw36_%=:\n\t" \
    "ds_write_b128 v250, v[208:211] offset:0\n\t" \
    "v_mfma_f32_16x16x32_bf16 v[236:239], v[208:211], v[208:211], 0\n\t" \
    "v_mfma_f32_16x16x32_bf16 v[232:235], v[228:231], v[208:211], 0\n\t" \
    "ds_write_b128 v250, v[212:215] offset:4096\n\t" \
    "v_mfma_f32_16x16x32_bf16 v[236:239], v[212:215], v[212:215], v[236:239]\n\t" \
    "v_mfma_f32_16x16x32_bf16 v[232:235], v[228:231], v[212:215], v[232:235]\n\t" \
    "ds_write_b128 v250, v[216:219] offset:8192\n\t" \
    "v_mfma_f32_16x16x32_bf16 v[236:239], v[216:219], v[216:219], v[236:239]\n\t" \
    "v_mfma_f32_16x16x32_bf16 v[232:235], v[228:231], v[216:219], v[232:235]\n\t" \
    "ds_write_b128 v250, v[220:223] offset:12288\n\t" \
    "v_mfma_f32_16x16x32_bf16 v[236:239], v[220:223], v[220:223], v[236:239]\n\t" \
    "v_mfma_f32_16x16x32_bf16 v[232:235], v[228:231], v[220:223], v[232:235]\n\t" \
    "ds_write_b128 v250, v[224:227] offset:16384\n\t" \
    "v_mfma_f32_16x16x32_bf16 v[236:239], v[224:227], v[224:227], v[236:239]\n\t" \
    "v_mfma_f32_16x16x32_bf16 v[232:235], v[228:231], v[224:227], v[232:235]\n\t" \
    "s_add_u32 s46, s43, %[pi]\n\t" \
    "s_nop 4\n\t" \
    "v_cndmask_b32 v236, v236, v237, s[52:53]\n\t" \
    "v_cndmask_b32 v236, v236, v238, s[54:55]\n\t" \
    "v_cndmask_b32 v236, v236, v239, s[56:57]\n\t" \
    "s_mov_b32 exec_lo, 0xf0000f\n\t" \
    "s_mov_b32 exec_hi, 0xf0000f00\n\t" \
    "ds_write2_b32 v252, v232, v236 offset1:1\n\t" \
    "s_mov_b64 exec, -1\n\t" \
    "s_add_u32 s44, s40, 1\n\t" \
    "v_mov_b32 v248, s44\n\t" \
    "s_lshl_b64 exec, 1, %[wave]\n\t" \
    "ds_write_b32 v255, v248\n\t" \
    "s_mov_b64 exec, -1\n\t" \
    ".Lw37_%=:\n\t" \
    "ds_read_b32 v248, v255\n\t" \
    "s_waitcnt lgkmcnt(0)\n\t" \
    "v_cmp_ge_u32 vcc, s40, v248\n\t" \
    "s_cbranch_vccz .Lw313_%=\n\t" \
    "s_cmp_lt_u32 %[wave], 4\n\t" \
    "s_cbranch_scc1 .Lw37_%=\n\t" \
    "s_sleep 1\n\t" \
    "s_branch .Lw37_%=\n\t" \
    ".Lw313_%=:\n\t" \
    "s_setprio 0\n\t" \
    "s_cmp_lg_u32 %[wave], 4\n\t" \
    "s_cbranch_scc1 .Lw314_%=\n\t" \
    "s_setprio 1\n\t" \
    "ds_read_b128 v[232:235], v251 offset:0\n\t" \
    "ds_read_b128 v[236:239], v251 offset:16\n\t" \
    "s_waitcnt lgkmcnt(0)\n\t" \
    "v_add_f32 v232, v232, v236\n\t" \
    "v_add_f32 v233, v233, v237\n\t" \
    "v_add_f32 v234, v234, v238\n\t" \
    "v_add_f32 v235, v235, v239\n\t" \
    "v_add_f32 v232, v232, v234\n\t" \
    "v_add_f32 v233, v233, v235\n\t" \
    "v_mul_f32 v232, 0x3acccccd, v232\n\t" \
    "v_mul_f32 v233, 0x3acccccd, v233\n\t" \
    "v_fma_f32 v233, -v232, v232, v233\n\t" \
    "v_mov_b32 v234, v232\n\t" \
    "v_max_f32 v233, 0, v233\n\t" \
    "v_cvt_pk_bf16_f32 v234, v234, v234\n\t" \
    "v_and_b32 v234, 0xffff0000, v234\n\t" \
    "v_mov_b32 v236, v233\n\t" \
    "v_cvt_pk_bf16_f32 v236, v236, v236\n\t" \
    "v_and_b32 v236, 0xffff0000, v236\n\t" \
    "v_add_f32 v236, 0x3727c5ac, v236\n\t" \
    "v_cvt_pk_bf16_f32 v236, v236, v236\n\t" \
    "v_and_b32 v236, 0xffff0000, v236\n\t" \
    "v_sqrt_f32 v236, v236\n\t" \
    "s_nop 0\n\t" \
    "v_cvt_pk_bf16_f32 v236, v236, v236\n\t" \
    "v_and_b32 v236, 0xffff0000, v236\n\t" \
    "v_rcp_f32 v235, v236\n\t" \
    "s_nop 0\n\t" \
    "v_mov_b32 v237, v235\n\t" \
    "v_mul_f32 v238, -v234, v235\n\t" \
    "v_mov_b32 v239, s40\n\t" \
    "ds_write2_b32 v251, v237, v238 offset0:16 offset1:17\n\t" \
    "ds_write_b32 v251, v239 offset:72\n\t" \
    "s_setprio 0\n\t" \
    ".Lw314_%=:\n\t" \
    "ds_read_b128 v[176:179], v249 offset:0\n\t" \
    "ds_read_b128 v[180:183], v249 offset:1024\n\t" \
    "ds_read_b128 v[184:187], v249 offset:2048\n\t" \
    "ds_read_b128 v[188:191], v249 offset:3072\n\t" \
    "ds_read_b128 v[192:195], v249 offset:4096\n\t" \
    "ds_read_b128 v[196:199], v249 offset:5120\n\t" \
    "ds_read_b128 v[200:203], v249 offset:6144\n\t" \
    "s_waitcnt lgkmcnt(6)\n\t" \
    "v_mfma_f32_16x16x32_bf16 v[168:171], v[0:3], v[176:179], 0\n\t" \
    "ds_read_b128 v[204:207], v249 offset:7168\n\t" \
    "v_mfma_f32_16x16x32_bf16 v[172:175], v[80:83], v[176:179], 0\n\t" \
    "s_waitcnt lgkmcnt(6)\n\t" \
    "v_mfma_f32_16x16x32_bf16 v[168:171], v[4:7], v[180:183], v[168:171]\n\t" \
    "ds_read_b128 v[176:179], v249 offset:8192\n\t" \
    "v_mfma_f32_16x16x32_bf16 v[172:175], v[84:87], v[180:183], v[172:175]\n\t" \
    "s_waitcnt lgkmcnt(6)\n\t" \
    "v_mfma_f32_16x16x32_bf16 v[168:171], v[8:11], v[184:187], v[168:171]\n\t" \
    "ds_read_b128 v[180:183], v249 offset:9216\n\t" \
    "v_mfma_f32_16x16x32_bf16 v[172:175], v[88:91], v[184:187], v[172:175]\n\t" \
    "s_waitcnt lgkmcnt(6)\n\t" \
    "v_mfma_f32_16x16x32_bf16 v[168:171], v[12:15], v[188:191], v[168:171]\n\t" \
    "ds_read_b128 v[184:187], v249 offset:10240\n\t" \
    "v_mfma_f32_16x16x32_bf16 v[172:175], v[92:95], v[188:191], v[172:175]\n\t" \
    "s_waitcnt lgkmcnt(6)\n\t" \
    "v_mfma_f32_16x16x32_bf16 v[168:171], v[16:19], v[192:195], v[168:171]\n\t" \
    "ds_read_b128 v[188:191], v249 offset:11264\n\t" \
    "v_mfma_f32_16x16x32_bf16 v[172:175], v[96:99], v[192:195], v[172:175]\n\t" \
    "s_waitcnt lgkmcnt(6)\n\t" \
    "v_mfma_f32_16x16x32_bf16 v[168:171], v[20:23], v[196:199], v[168:171]\n\t" \
    "ds_read_b128 v[192:195], v249 offset:12288\n\t" \
    "v_mfma_f32_16x16x32_bf16 v[172:175], v[100:103], v[196:199], v[172:175]\n\t" \
    "s_waitcnt lgkmcnt(6)\n\t" \
    "v_mfma_f32_16x16x32_bf16 v[168:171], v[24:27], v[200:203], v[168:171]\n\t" \
    "ds_read_b128 v[196:199], v249 offset:13312\n\t" \
    "v_mfma_f32_16x16x32_bf16 v[172:175], v[104:107], v[200:203], v[172:175]\n\t" \
    "s_waitcnt lgkmcnt(6)\n\t" \
    "v_mfma_f32_16x16x32_bf16 v[168:171], v[28:31], v[204:207], v[168:171]\n\t" \
    "ds_read_b128 v[200:203], v249 offset:14336\n\t" \
    "v_mfma_f32_16x16x32_bf16 v[172:175], v[108:111], v[204:207], v[172:175]\n\t" \
    "s_waitcnt lgkmcnt(6)\n\t" \
    "v_mfma_f32_16x16x32_bf16 v[168:171], v[32:35], v[176:179], v[168:171]\n\t" \
    "ds_read_b128 v[204:207], v249 offset:15360\n\t" \
    "v_mfma_f32_16x16x32_bf16 v[172:175], v[112:115], v[176:179], v[172:175]\n\t" \
    "s_waitcnt lgkmcnt(6)\n\t" \
    "v_mfma_f32_16x16x32_bf16 v[168:171], v[36:39], v[180:183], v[168:171]\n\t" \
    "ds_read_b128 v[176:179], v249 offset:16384\n\t" \
    "v_mfma_f32_16x16x32_bf16 v[172:175], v[116:119], v[180:183], v[172:175]\n\t" \
    "s_waitcnt lgkmcnt(6)\n\t" \
    "v_mfma_f32_16x16x32_bf16 v[168:171], v[40:43], v[184:187], v[168:171]\n\t" \
    "ds_read_b128 v[180:183], v249 offset:17408\n\t" \
    "v_mfma_f32_16x16x32_bf16 v[172:175], v[120:123], v[184:187], v[172:175]\n\t" \
    "s_waitcnt lgkmcnt(6)\n\t" \
    "v_mfma_f32_16x16x32_bf16 v[168:171], v[44:47], v[188:191], v[168:171]\n\t" \
    "ds_read_b128 v[184:187], v249 offset:18432\n\t" \
    "v_mfma_f32_16x16x32_bf16 v[172:175], v[124:127], v[188:191], v[172:175]\n\t" \
    "s_waitcnt lgkmcnt(6)\n\t" \
    "v_mfma_f32_16x16x32_bf16 v[168:171], v[48:51], v[192:195], v[168:171]\n\t" \
    "ds_read_b128 v[188:191], v249 offset:19456\n\t" \
    "v_mfma_f32_16x16x32_bf16 v[172:175], v[128:131], v[192:195], v[172:175]\n\t" \
    "s_waitcnt lgkmcnt(6)\n\t" \
    "v_mfma_f32_16x16x32_bf16 v[168:171], v[52:55], v[196:199], v[168:171]\n\t" \
    "v_mfma_f32_16x16x32_bf16 v[172:175], v[132:135], v[196:199], v[172:175]\n\t" \
    "s_waitcnt lgkmcnt(5)\n\t" \
    "v_mfma_f32_16x16x32_bf16 v[168:171], v[56:59], v[200:203], v[168:171]\n\t" \
    "v_mfma_f32_16x16x32_bf16 v[172:175], v[136:139], v[200:203], v[172:175]\n\t" \
    "s_cmp_gt_u32 %[wave], 3\n\t" \
    "s_cbranch_scc1 .Lw316_%=\n\t" \
    "s_add_u32 s47, s46, 0x0\n\t" \
    "buffer_load_dwordx4 v[208:211], v253, %[rs], s47 offen sc1\n\t" \
    "s_add_u32 s47, s46, 0x4000\n\t" \
    "buffer_load_dwordx4 v[212:215], v253, %[rs], s47 offen sc1\n\t" \
    "s_add_u32 s47, s46, 0x8000\n\t" \
    "buffer_load_dwordx4 v[216:219], v253, %[rs], s47 offen sc1\n\t" \
    "s_add_u32 s47, s46, 0xc000\n\t" \
    "buffer_load_dwordx4 v[220:223], v253, %[rs], s47 offen sc1\n\t" \
    "s_add_u32 s47, s46, 0x10000\n\t" \
    "buffer_load_dwordx4 v[224:227], v253, %[rs], s47 offen sc1\n\t" \
    ".Lw316_%=:\n\t" \
    "s_waitcnt lgkmcnt(4)\n\t" \
    "v_mfma_f32_16x16x32_bf16 v[168:171], v[60:63], v[204:207], v[168:171]\n\t" \
    "v_mfma_f32_16x16x32_bf16 v[172:175], v[140:143], v[204:207], v[172:175]\n\t" \
    "ds_read_b32 v236, v251 offset:72\n\t" \
    "ds_read_b64 v[238:239], v251 offset:64\n\t" \
    "s_waitcnt lgkmcnt(5)\n\t" \
    "v_mfma_f32_16x16x32_bf16 v[168:171], v[64:67], v[176:179], v[168:171]\n\t" \
    "v_mfma_f32_16x16x32_bf16 v[172:175], v[144:147], v[176:179], v[172:175]\n\t" \
    "s_waitcnt lgkmcnt(4)\n\t" \
    "v_mfma_f32_16x16x32_bf16 v[168:171], v[68:71], v[180:183], v[168:171]\n\t" \
    "v_mfma_f32_16x16x32_bf16 v[172:175], v[148:151], v[180:183], v[172:175]\n\t" \
    ".Lw317_%=:\n\t" \
    "s_waitcnt lgkmcnt(0)\n\t" \
    "v_cmp_ne_u32 vcc, s40, v236\n\t" \
    "s_cbranch_vccz .Lw318_%=\n\t" \
    "ds_read_b32 v236, v251 offset:72\n\t" \
    "ds_read_b64 v[238:239], v251 offset:64\n\t" \
    "s_branch .Lw317_%=\n\t" \
    ".Lw318_%=:\n\t" \
    "s_waitcnt lgkmcnt(0)\n\t" \
    "v_mul_f32 v240, v160, v239\n\t" \
    "v_mul_f32 v241, v161, v239\n\t" \
    "v_mfma_f32_16x16x32_bf16 v[168:171], v[72:75], v[184:187], v[168:171]\n\t" \
    "v_mul_f32 v242, v162, v239\n\t" \
    "v_mul_f32 v243, v163, v239\n\t" \
    "v_mfma_f32_16x16x32_bf16 v[172:175], v[152:155], v[184:187], v[172:175]\n\t" \
    "v_mul_f32 v244, v164, v239\n\t" \
    "v_mul_f32 v245, v165, v239\n\t" \
    "v_mfma_f32_16x16x32_bf16 v[168:171], v[76:79], v[188:191], v[168:171]\n\t" \
    "v_mul_f32 v246, v166, v239\n\t" \
    "v_mul_f32 v247, v167, v239\n\t" \
    "v_mfma_f32_16x16x32_bf16 v[172:175], v[156:159], v[188:191], v[172:175]\n\t" \
    "v_mov_b32 v237, 0xc0135761\n\t" \
    "s_cmp_lt_u32 %[wave], 4\n\t" \
    "s_cbranch_scc1 .Lw319_%=\n\t" \
    "s_add_u32 s44, s40, 2\n\t" \
    "v_mov_b32 v248, s44\n\t" \
    "s_lshl_b64 exec, 1, %[wave]\n\t" \
    "ds_write_b32 v255, v248\n\t" \
    "s_mov_b64 exec, -1\n\t" \
    ".Lw319_%=:\n\t" \
    "s_nop 1\n\t" \
    "s_nop 2\n\t" \
    "v_fma_f32 v168, v168, v238, v240\n\t" \
    "v_fma_f32 v169, v169, v238, v241\n\t" \
    "v_fma_f32 v170, v170, v238, v242\n\t" \
    "v_fma_f32 v171, v171, v238, v243\n\t" \
    "v_fma_f32 v172, v172, v238, v244\n\t" \
    "v_fma_f32 v173, v173, v238, v245\n\t" \
    "v_fma_f32 v174, v174, v238, v246\n\t" \
    "v_fma_f32 v175, v175, v238, v247\n\t" \
    "v_mul_f32 v176, v168, v168\n\t" \
    "v_mul_f32 v177, v169, v169\n\t" \
    "v_mul_f32 v178, v170, v170\n\t" \
    "v_mul_f32 v179, v171, v171\n\t" \
    "v_mul_f32 v180, v172, v172\n\t" \
    "v_mul_f32 v181, v173, v173\n\t" \
    "v_mul_f32 v182, v174, v174\n\t" \
    "v_mul_f32 v183, v175, v175\n\t" \
    "v_fmamk_f32 v176, v176, 0xbdd2d3e7, v237\n\t" \
    "v_fmamk_f32 v177, v177, 0xbdd2d3e7, v237\n\t" \
    "v_fmamk_f32 v178, v178, 0xbdd2d3e7, v237\n\t" \
    "v_fmamk_f32 v179, v179, 0xbdd2d3e7, v237\n\t" \
    "v_fmamk_f32 v180, v180, 0xbdd2d3e7, v237\n\t" \
    "v_fmamk_f32 v181, v181, 0xbdd2d3e7, v237\n\t" \
    "v_fmamk_f32 v182, v182, 0xbdd2d3e7, v237\n\t" \
    "v_fmamk_f32 v183, v183, 0xbdd2d3e7, v237\n\t" \
    "v_mul_f32 v176, v168, v176\n\t" \
    "v_mul_f32 v177, v169, v177\n\t" \
    "v_mul_f32 v178, v170, v178\n\t" \
    "v_mul_f32 v179, v171, v179\n\t" \
    "v_mul_f32 v180, v172, v180\n\t" \
    "v_mul_f32 v181, v173, v181\n\t" \
    "v_mul_f32 v182, v174, v182\n\t" \
    "v_mul_f32 v183, v175, v183\n\t" \
    "v_exp_f32 v176, v176\n\t" \
    "v_exp_f32 v177, v177\n\t" \
    "v_exp_f32 v178, v178\n\t" \
    "v_exp_f32 v179, v179\n\t" \
    "v_exp_f32 v180, v180\n\t" \
    "v_exp_f32 v181, v181\n\t" \
    "v_exp_f32 v182, v182\n\t" \
    "v_exp_f32 v183, v183\n\t" \
    "v_add_f32 v176, 1.0, v176\n\t" \
    "v_add_f32 v177, 1.0, v177\n\t" \
    "v_add_f32 v178, 1.0, v178\n\t" \
    "v_add_f32 v179, 1.0, v179\n\t" \
    "v_add_f32 v180, 1.0, v180\n\t" \
    "v_add_f32 v181, 1.0, v181\n\t" \
    "v_add_f32 v182, 1.0, v182\n\t" \
    "v_add_f32 v183, 1.0, v183\n\t" \
    "v_rcp_f32 v176, v176\n\t" \
    "v_rcp_f32 v177, v177\n\t" \
    "v_rcp_f32 v178, v178\n\t" \
    "v_rcp_f32 v179, v179\n\t" \
    "v_rcp_f32 v180, v180\n\t" \
    "v_rcp_f32 v181, v181\n\t" \
    "v_rcp_f32 v182, v182\n\t" \
    "v_rcp_f32 v183, v183\n\t" \
    "v_mul_f32 v168, v168, v176\n\t" \
    "v_mul_f32 v169, v169, v177\n\t" \
    "v_mul_f32 v170, v170, v178\n\t" \
    "v_mul_f32 v171, v171, v179\n\t" \
    "v_mul_f32 v172, v172, v180\n\t" \
    "v_mul_f32 v173, v173, v181\n\t" \
    "v_mul_f32 v174, v174, v182\n\t" \
    "v_mul_f32 v175, v175, v183\n\t" \
    "s_waitcnt vmcnt(0)\n\t" \
    "v_cvt_pk_bf16_f32 v184, v168, v169\n\t" \
    "v_cvt_pk_bf16_f32 v185, v170, v171\n\t" \
    "v_cvt_pk_bf16_f32 v186, v172, v173\n\t" \
    "v_cvt_pk_bf16_f32 v187, v174, v175\n\t" \
    "v_min_u32 v184, -2, v184\n\t" \
    "v_min_u32 v185, -2, v185\n\t" \
    "v_min_u32 v186, -2, v186\n\t" \
    "v_min_u32 v187, -2, v187\n\t" \
    "s_nop 0\n\t" \
    "v_permlane16_swap_b32 v184, v186\n\t" \
    "v_permlane16_swap_b32 v185, v187\n\t" \
    "buffer_store_dwordx4 v[184:187], v254, %[rs], s49 offen sc1\n\t" \
    "s_cmp_ge_u32 s40, 122\n\t" \
    "s_cbranch_scc1 .Lw320_%=\n\t" \
    "s_add_i32 s44, s40, 6\n\t" \
    "s_max_i32 s44, s44, 0\n\t" \
    "s_lshr_b32 s45, s44, 1\n\t" \
    "s_and_b32 s44, s44, 1\n\t" \
    "s_lshl_b32 s44, s44, 12\n\t" \
    "s_mul_i32 s50, s45, 0x30000\n\t" \
    "s_add_u32 s50, s50, s44\n\t" \
    "s_lshr_b32 s45, s45, 3\n\t" \
    "s_mul_i32 s45, s45, 0x82000\n\t" \
    "s_add_u32 s50, s50, s45\n\t" \
    "s_add_u32 s50, s50, %[ng]\n\t" \
    "v_mov_b32 v188, -1\n\t" \
    "v_mov_b32 v189, -1\n\t" \
    "v_mov_b32 v190, -1\n\t" \
    "v_mov_b32 v191, -1\n\t" \
    "buffer_store_dwordx4 v[188:191], v253, %[rs], s50 offen sc1\n\t" \
    ".Lw320_%=:\n\t" \
    "v_xor_b32 v249, 0x8000, v249\n\t" \
    "v_xor_b32 v250, 0x8000, v250\n\t" \
    "v_xor_b32 v251, 0x800, v251\n\t" \
    "v_xor_b32 v252, 0x800, v252\n\t" \
    "s_add_u32 s40, s40, 1\n\t" \
    "s_cmp_lg_u32 s40, 128\n\t" \
    "s_cbranch_scc1 .Lw34_%=\n\t" \
    "s_branch .Lend%=\n\t" \
    ".Lend%=:\n\t" \
    "s_waitcnt vmcnt(0) lgkmcnt(0)\n\t" \
    "s_mov_b32 %[bud], s41\n\t"
using u32x8 = __attribute__((ext_vector_type(8))) unsigned;
using u32x16 = __attribute__((ext_vector_type(16))) unsigned;
using u32x32 = __attribute__((ext_vector_type(32))) unsigned;
__device__ __forceinline__ u32x32 cat32(u32x8 a, u32x8 b, u32x8 c, u32x8 d) {
    const u32x16 lo = __builtin_shufflevector(a, b, 0, 1, 2, 3, 4, 5, 6, 7, 8, 9, 10, 11, 12, 13, 14, 15);
    const u32x16 hi = __builtin_shufflevector(c, d, 0, 1, 2, 3, 4, 5, 6, 7, 8, 9, 10, 11, 12, 13, 14, 15);
    return __builtin_shufflevector(lo, hi, 0, 1, 2, 3, 4, 5, 6, 7, 8, 9, 10, 11, 12, 13, 14, 15, 16, 17, 18, 19, 20, 21, 22, 23, 24, 25, 26, 27, 28, 29, 30, 31);
}
__device__ __forceinline__ unsigned c1_dst(int wave, int kind) {
    const int ln = lane_id(), c = ln & 15, g = ln >> 4, hf = (wave >> 1) & 1;
    if (kind == 2) {
        const unsigned b = L_KM + (c >> 2) * 256 + (wave & 1) * 128 + g * 32;
        return (b + hf * 8 + (c & 3) * 2) | (b + 1024 + hf * 1024 + (c & 3) * 4) << 16;
    }
    const unsigned b = L_Q + (c >> 3) * 256 + g * 64 + ((c >> 2) & 1) * 8;
    return (b + (wave & 1) * (kind ? 512 : 1536) + ((c >> 1) & 1) * 32 + hf * 4 + (c & 1) * 2) | (b + 1024 + (wave & 3) * 512 + (c & 1) * 32 + ((c >> 1) & 1) * 4) << 16;
}
__device__ __forceinline__ void dma1(rsrc_t rs, unsigned vo, unsigned so, unsigned dst) {
    asm volatile("s_mov_b32 m0, %3\n\ts_nop 0\n\tbuffer_load_dwordx4 %0, %1, %2 offen nt lds" :: "v"(vo), "s"(rs), "s"(so), "s"(dst) : "memory");
}
using s16x4 = __attribute__((ext_vector_type(4))) short;
__device__ __forceinline__ u32x4 tr_frag(unsigned a, unsigned step) {
    const u32x2 x = __builtin_bit_cast(u32x2, __builtin_amdgcn_ds_read_tr16_b64_v4i16((LAS s16x4*)(size_t)a)), y = __builtin_bit_cast(u32x2, __builtin_amdgcn_ds_read_tr16_b64_v4i16((LAS s16x4*)(size_t)(a + step)));
    return (u32x4){x[0], x[1], y[0], y[1]};
}
#define LOOP_CLOB "memory", "vcc", "scc", "v0", "v1", "v2", "v3", "v4", "v5", "v6", "v7", "v8", "v9", "v10", "v11", "v12", "v13", "v14", "v15", "v16", "v17", "v18", "v19", "v20", "v21", "v22", "v23", "v24", "v25", "v26", "v27", "v28", "v29", "v30", "v31", "v32", "v33", "v34", "v35", "v36", "v37", "v38", "v39", "v40", "v41", "v42", "v43", "v44", "v45", "v46", "v47", "v48", "v49", "v50", "v51", "v52", "v53", "v54", "v55", "v56", "v57", "v58", "v59", "v60", "v61", "v62", "v63", "v64", "v65", "v66", "v67", "v68", "v69", "v70", "v71", "v72", "v73", "v74", "v75", "v76", "v77", "v78", "v79", "v80", "v81", "v82", "v83", "v84", "v85", "v86", "v87", "v88", "v89", "v90", "v91", "v92", "v93", "v94", "v95", "v96", "v97", "v98", "v99", "v100", "v101", "v102", "v103", "v104", "v105", "v106", "v107", "v108", "v109", "v110", "v111", "v112", "v113", "v114", "v115", "v116", "v117", "v118", "v119", "v120", "v121", "v122", "v123", "v124", "v125", "v126", "v127", "v128", "v129", "v130", "v131", "v132", "v133", "v134", "v135", "v136", "v137", "v138", "v139", "v140", "v141", "v142", "v143", "v144", "v145", "v146", "v147", "v148", "v149", "v150", "v151", "v152", "v153", "v154", "v155", "v156", "v157", "v158", "v159", "v160", "v161", "v162", "v163", "v164", "v165", "v166", "v167", "v168", "v169", "v170", "v171", "v172", "v173", "v174", "v175", "v176", "v177", "v178", "v179", "v180", "v181", "v182", "v183", "v184", "v185", "v186", "v187", "v188", "v189", "v190", "v191", "v192", "v193", "v194", "v195", "v196", "v197", "v198", "v199", "v200", "v201", "v202", "v203", "v204", "v205", "v206", "v207", "v208", "v209", "v210", "v211", "v212", "v213", "v214", "v215", "v216", "v217", "v218", "v219", "v220", "v221", "v222", "v223", "v224", "v225", "v226", "v227", "v228", "v229", "v230", "v231", "v232", "v233", "v234", "v235", "v236", "v237", "v238", "v239", "v246", "v247", "v248", "v249", "v250", "v251", "v252", "s40", "s41", "s42", "s43", "s44", "s45", "s46", "s47", "s48", "s49", "s50", "s51", "s52", "s53", "s54", "s55", "s56", "s57", "s58", "s59", "s60", "s61", "s62", "s63", "s64", "s65", "s66", "s67", "s68", "s69", "s70", "s71", "s72", "s73", "s74", "s75", "s76", "s77", "s78", "s79"
__device__ __forceinline__ unsigned UNI(unsigned x) {
    unsigned r;
    asm volatile("s_nop 0\n\tv_readfirstlane_b32 %0, %1\n\ts_nop 4" : "=s"(r) : "v"(x));
    return r;
}
__device__ __forceinline__ void note_timeout(const Params& P, unsigned bud) {
    if (bud == 0) __builtin_amdgcn_raw_buffer_store_b32(1u, mkrsrc(P.ws + FL0, 512), 0x100, 0, 16);
}

#define LOOP_IO [bud] "=s"(bud), [mb] "+s"(mb), [pb] "+s"(pb), [fl] "+s"(fl), "+{v253}"(l16), "+{v240}"(W.a[0]), "+{v241}"(W.a[1]), "+{v242}"(W.a[2]), "+{v243}"(W.a[3]), "+{v244}"(W.a[4]), "+{v245}"(W.a[5])
#define LOOP_IN "{v254}"(b2), "{v255}"(dsto), [wave] "s"(wave), [ws] "s"(P.ws), [mo] "s"(FL_MB + blockIdx.x * 128 + pw * 4), [sz] "s"(RG_L), [lx] "s"(L << 13), [wr] "s"(wdesc(P, wave, isv, h)), [wm] "s"(UNI(wstage(pw, isv))), \
                [cj] "s"(REGS - 75 * WIN), [cs] "s"(5 * WIN)
struct WLoad { unsigned a[6]; };
__device__ __forceinline__ rsrc_t wdesc(const Params& P, int wave, bool isv, int h) {
    const unsigned wo = (isv && wave >= 4) ? h * DH * DM + (wave & 3) * 160 : h * DH;
    typedef const __attribute__((address_space(4))) unsigned long long* kptr;
    static_assert(__builtin_offsetof(Params, wq) == 24 && __builtin_offsetof(Params, wo) == 48, "argument order");
    const unsigned long long w = ((kptr)__builtin_amdgcn_kernarg_segment_ptr())[3 + (isv ? 2 : 0) + (wave >> 2)];
    return mkrsrc((const bf16_t*)w + wo, (DM * HD - wo) * 2);
}
__device__ __forceinline__ unsigned wstage(int wave, bool isv) { return (isv && wave >= 4) ? L_W + (wave & 3) * 20480 : wave * 3072; }
template <bool START>
__device__ __forceinline__ void wload_start(WLoad& W, const Params& P, int wave, bool isv, int h) {
    const int ln = lane_id(), w4 = wave & 3;
    const rsrc_t wr = wdesc(P, wave, isv, h);
    const unsigned wm = UNI(wstage(wave, isv));
    if (isv && wave >= 4) {
#pragma unroll
        for (int k = 0; k < 5; ++k) { const unsigned u = k * 64 + ln; W.a[k] = (u / 20) * (DM * 2) + (u % 20) * 16 - (k & 3) * 1024; }
#pragma unroll 1
        for (int k = 0; k < (START ? 4 : 0); ++k)
            asm volatile("s_mov_b32 m0, %7\n\ts_nop 0\n\tbuffer_load_dwordx4 %0, %5, %6 offen nt lds\n\tbuffer_load_dwordx4 %1, %5, %6 offen offset:1024 nt lds\n\tbuffer_load_dwordx4 %2, %5, %6 offen offset:2048 nt lds\n\t"
                         "buffer_load_dwordx4 %3, %5, %6 offen offset:3072 nt lds\n\ts_mov_b32 m0, %8\n\ts_nop 0\n\tbuffer_load_dwordx4 %4, %5, %6 offen nt lds"
                         :: "v"(W.a[0]), "v"(W.a[1]), "v"(W.a[2]), "v"(W.a[3]), "v"(W.a[4]), "s"(wr), "s"(k * 20480), "s"(wm + k * 5120), "s"(wm + k * 5120 + 4096) : "memory");
        W.a[5] = L_W + w4 * 20480 + (ln >> 2) * 320 + (ln & 3) * 8;
    } else {
        const unsigned sl = isv ? 12288 : 24576;
#pragma unroll
        for (int k = 0; k < 3; ++k) { const unsigned u = (w4 * 3 + k) * 64 + ln; W.a[k] = (u / 24) * (HD * 2) + (u % 24) * 16 - k * 1024; }
#pragma unroll 1
        for (int k = 0; k < (START ? 5 : 0); ++k)
            asm volatile("s_mov_b32 m0, %5\n\ts_nop 0\n\tbuffer_load_dwordx4 %0, %3, %4 offen nt lds\n\tbuffer_load_dwordx4 %1, %3, %4 offen offset:1024 nt lds\n\tbuffer_load_dwordx4 %2, %3, %4 offen offset:2048 nt lds"
                         :: "v"(W.a[0]), "v"(W.a[1]), "v"(W.a[2]), "s"(wr), "s"(k * 32 * HD * 2), "s"(wm + (k < 2 ? k + 4 : k - 2) * sl) : "memory");
        W.a[3] = (wave >> 2) * 12288 + ((ln >> 4) * 8 + ((ln >> 2) & 3)) * 384 + (ln & 3) * 8 + w4 * 96;
        W.a[4] = 0;
        W.a[5] = 0;
    }
}
#define PERM 0x78
#define PERMV 0x87
__device__ __forceinline__ int deal(int pw, int perm, unsigned a) {
    unsigned hw;
    asm volatile("s_getreg_b32 %0, hwreg(HW_REG_HW_ID, 4, 2)" : "=s"(hw));
    lds_vst(a + pw * 4, 1u << (hw + (pw & 4)));
    bar();
    const unsigned cov = lds_vld(a) | lds_vld(a + 4) | lds_vld(a + 8) | lds_vld(a + 12) | lds_vld(a + 16) | lds_vld(a + 20) | lds_vld(a + 24) | lds_vld(a + 28);
    return UNI(cov == 0xff && perm >= 0 ? (pw & 4) + ((perm >> (2 * hw)) & 3) : pw);
}
__device__ __forceinline__ void s_role(const Params& P, int pw, int L, int h, int cpy) {
    const int ln = lane_id(), wave = deal(pw, PERM, MEGA_LDS - 32);
    unsigned l16 = ln * 16;
    constexpr bool isv = false;
    WLoad W;
    wload_start<false>(W, P, pw, isv, h);
    W.a[3] += (wave - pw) * 96;
    const int mt = (wave >> 1) & 1, hf = wave & 1;
    const unsigned xs = D_XN + mt * 4096 + hf * WIN, x1 = (2 - hf) * WIN + hf * 2048, m0 = L_X + mt * 20480 + hf * 4096, m1 = L_X + mt * 20480 + 8192 + hf * 2048;
    const float SCL = 0.07216878364870322f * 1.4426950408889634f;
    const unsigned px = RG_PX + (h >> 2) * WIN;
    unsigned mb = UNI(blk(10, cpy)), fl = UNI(blk(15, cpy)), pb = UNI(wave == 0 ? blk(32 + (h >> 2), 0) + (h & 3) * 640 : px + (wave == 2 ? (h & 3) * 1024 : 5120 + ((h >> 1) & 1) * 1024 + (h & 1) * 256));
    unsigned b2 = l16 + (wave < 3 ? L_Q : wave == 3 ? L_QX : L_W + wave * 10240), dsto = c1_dst(wave, 1 + (wave >> 2)), bud;
    asm volatile(ASM_S_LOOP : LOOP_IO
                 : LOOP_IN, [xs] "s"(xs), [x1] "s"(x1), [m0] "s"(m0), [m1] "s"(m1), [qs] "s"(UNI(L_KX + hf * 1024 + mt * 8)), [q2] "s"(UNI(L_KX + 2048 + (wave & 3) * 1024)),
                   [pm] "s"(UNI(px + (wave == 2 ? 4096 : 7168) + (h & 3) * 256)), [scl] "s"(__float_as_uint(SCL))
                 : LOOP_CLOB);
    note_timeout(P, bud);
}

__device__ __forceinline__ void v_role(const Params& P, int pw, int L, int h, int cpy) {
    const int ln = lane_id(), dw = deal(pw, PERMV, L_W - 32), wave = pw < 4 ? dw : pw, w4 = wave & 3;
    unsigned l16 = ln * 16;
    constexpr bool isv = true;
    WLoad W;
    wload_start<false>(W, P, pw, isv, h);
    W.a[3] += (wave - pw) * 96;
    unsigned b2 = l16 + (wave < 3 ? L_Q : wave == 3 ? L_QX : L_W + wave * 10240), dsto = wave < 4 ? c1_dst(wave, 0) : (l16 >> 8) * GRP + (l16 & 255), bud;
    unsigned mb = UNI(wave < 4 ? blk(10, cpy) : blk(h >> 2, w4) + (h & 3) * 1024), fl = UNI(wave < 4 ? blk(15, cpy) : blk(14, w4) + h * 128), pb = UNI(blk(h & 16, w4) + (h & 15) * 256);
    if (wave >= 4) l16 += L_Q;
    asm volatile(ASM_V_LOOP : LOOP_IO
                 : LOOP_IN, [xs] "s"(D_XN + w4 * WIN)
                 : LOOP_CLOB);
    note_timeout(P, bud);
}

#define D_W0 4
#define D_A1 8
#define D_R1 9
#define D_W1 12
__device__ __forceinline__ void delay(int n) { for (int i = 0; i < n; ++i) __builtin_amdgcn_s_sleep(64); }
__device__ __forceinline__ unsigned handshake(rsrc_t rw, unsigned mo) {
    wait_vm<0>();
    unsigned v = SENT;
    for (int spins = 0; spins < SPIN_CAP && v == SENT; ++spins) {
        asm volatile("s_sleep 4" ::: "memory");
        v = __builtin_amdgcn_readfirstlane(__builtin_amdgcn_raw_buffer_load_b32(rw, 0, mo, 16));
    }
    __builtin_amdgcn_raw_buffer_store_b32(1u, rw, lane_id() ? OOB : 32768u, mo, 16);
    return v & 0x7000;
}
template <class F>
__device__ __forceinline__ void x_tile(const Params& P, int tile, F target) {
    const int ln = lane_id(), l15 = ln & 15, l4 = ln >> 4;
    u32x4 v[20];
    float s = 0.f, q = 0.f;
#pragma unroll
    for (int c = 0; c < 20; ++c) v[c] = *(const u32x4*)(P.x + (size_t)(tile * 16 + l15) * DM + c * 32 + l4 * 8);
#pragma unroll
    for (int c = 0; c < 20; ++c)
#pragma unroll
        for (int e = 0; e < 4; ++e) s += lo2f(v[c][e]) + hi2f(v[c][e]);
    sum2_rows(s, q);
#pragma unroll
    for (int c = 0; c < 20; ++c) asm volatile("" : "+v"(v[c]));
    const float mean = s * (1.0f / DM);
#pragma unroll
    for (int c = 0; c < 20; ++c)
#pragma unroll
        for (int e = 0; e < 4; ++e) { const float a = lo2f(v[c][e]) - mean, b = hi2f(v[c][e]) - mean; q += a * a + b * b; }
    sum2_rows(s, q);
#pragma unroll
    for (int c = 0; c < 20; ++c) asm volatile("" : "+v"(v[c]));
    const float m1 = rbf(mean), r = 1.0f / ln_den(q * (1.0f / DM));
    const unsigned sb = segb(tile >> 1) + (tile & 1) * 4096;
    u32x4 n[20];
#pragma unroll
    for (int c = 0; c < 20; ++c) {
#pragma unroll
        for (int e = 0; e < 4; ++e) { const unsigned d = pack2(lo2f(v[c][e]) - m1, hi2f(v[c][e]) - m1); n[c][e] = pack2(lo2f(d) * r, hi2f(d) * r); }
        asm volatile("" : "+v"(n[c]));
    }
    const rsrc_t rr = target();
#pragma unroll
    for (int c = 0; c < 20; ++c) {
        st128(n[c], rr, ln * 16 + (c & 3) * 1024, D_XN + sb + (c >> 2) * WIN);
        st128(v[c], rr, ln * 16 + (c & 3) * 1024, D_XR + sb + (c >> 2) * WIN);
    }
    wait_vm<0>();
}
__device__ __forceinline__ void wait_go(const Params& P, rsrc_t rd, int k) {
    for (int spins = 0;; ++spins) {
        if (__builtin_amdgcn_readfirstlane(__builtin_amdgcn_raw_buffer_load_b32(rd, 0, FL_VO + (k & 31) * 128, 16))) break;
        asm volatile("s_sleep 16" ::: "memory");
        if (spin_fail(spins * 4 + 3, P)) break;
    }
    asm volatile("" ::: "memory");
}

constexpr unsigned R_RED = 0;
constexpr unsigned R_PART = 8192;
__device__ __forceinline__ void r_role(const Params& P, int wave, int L, int j, int cpy) {
    const int lane = lane_id(), l15 = lane & 15, l4 = lane >> 4;
    const unsigned l16 = lane * 16;
    const rsrc_t rr = mkrsrc(lbase(P, L), RG_L);
    const int sidx = (lane & 2) ? 0xEEEE : 0x4444;
    unsigned offp[3];
    const unsigned sel = P.par & 0x5000;
    auto hq = [&](int q) { return blk(32 + q, cpy) ^ sel; };
#define ROT 1
    const int hg = (wave + ROT * j) & 7;
    offp[0] = hq(hg) + (lane >> 5) * 640 + 128 + (lane & 31) * 16;
    offp[1] = offp[0] + 1280;
    offp[2] = (lane & 2) ? OOB : hq(hg) + l4 * 640 + ((l15 >> 2) * 2 + (l15 & 1)) * 16;
    const int jq = j / 10, j10 = j % 10;
    const unsigned ae = wave < 3 ? ((j10 < 8 && wave * 1024 + l16 < 2560) ? (blk(32 + j10, jq) ^ sel) + wave * 1024 + l16 : OOB)
                      : wave == 3 ? (blk(10 + (j10 >> 2), jq) ^ sel) + (j10 & 3) * 1024 + l16
                      : wave < 6 ? (j < 32 ? RG_PX + (j >> 2) * WIN + (j & 3) * 2048 + (wave & 1) * 1024 + l16 : OOB)
                      : wave == 7 ? D_XA + (j >> 3) * WIN + (j & 7) * 512 + (lane >> 5) * 4096 + (lane & 31) * 16 : OOB;
    const unsigned as = wave < 4 ? CH_SLOT : PX_SLOT;
    static_assert(AH == 3, "generated loop");
    const unsigned offv = (blk((hg >> 2) * 16 + j10, jq) ^ sel) + (hg & 3) * 1024 + l16;
    const unsigned offo = (wave < (int)NCP) ? (blk(10 + (j >> 4), wave) ^ sel) + (j & 15) * 256 + lane * 4 : OOB;
    {
        const u32x4 s4 = sent4();
#pragma unroll
        for (int k = 0; k < (int)NSLOT; ++k) {
            st128(s4, rr, offv, k * CH_SLOT);
            if (wave < 6 && (k || wave != 3)) st128(s4, rr, ae, k * as);
        }
        for (int u = j; u < 126; u += 40) st128(s4, rr, wave * 1024 + l16, u < 108 ? D_XA + (u < 60 ? u / 20 : 3) * 4 * REG + (u < 60 ? u % 20 : u - 60) * WIN : u < 124 ? RG_ST + (u - 108) * WIN : D_SS + (u - 124) * WIN);
        wait_vm<0>();
        bar();
        const int row = l4 * 2 + ((lane >> 2) & 1), col = 16 * j + 4 * (lane & 3) + 2 * ((lane >> 3) & 1);
        __builtin_amdgcn_raw_buffer_store_b32(nosent(*(const unsigned*)(P.mem0 + row * DM + col)), rr, offo, 0, 16);
    }
    auto red_addr = [](int fl) { return R_RED + fl * 80 + (fl >> 4) * 16; };
    const unsigned rw0 = red_addr(l4 * 16 + ((l15 >> 1) & 1) * 8 + (l15 >> 2)) + (l15 & 1) * 4 + (hg ^ (l15 >> 3)) * 8;
    const unsigned rrd = red_addr(lane);
    unsigned bud;
    asm volatile(ASM_R_LOOP : [bud] "=s"(bud)
                 : [wave] "s"(wave), [rs] "s"(rr), [l16] "v"(l16), [ov] "v"(offv), [o0] "v"(offp[0]), [o1] "v"(offp[1]), [o2] "v"(offp[2]), [oo] "v"(offo), [sidx] "v"(sidx), [rw] "v"(rw0), [rd] "v"(rrd),
                   [xo] "v"((l4 >> 1) * 256 + l15 * 16 + (l4 & 1) * 8), [pw] "v"(R_PART + hg * 1024 + l16), [pr] "v"(R_PART + (wave & 1) * 8192 + l16), [fo] "v"((lane & 3) * 128), [ae] "v"(ae),
                   [fs] "s"(UNI((blk(14, jq) ^ sel) + hg * 512)), [vx] "s"(UNI(vxb(j10, hg, jq) ^ sel)), [px] "s"(UNI(RG_PX + hg * WIN)),
                   [xr] "s"(UNI(D_XR + (j >> 3) * WIN + (wave & 1) * 4096 + (j & 7) * 512)), [ad] "s"(UNI(wave < 3 ? -1 : wave == 3 ? 0 : -2)), [as] "s"(UNI(as))
                 : "memory", "vcc", "scc", "v128", "v129", "v130", "v131", "v132", "v133", "v134", "v135", "v136", "v137", "v138", "v139", "v140", "v141", "v142", "v143", "v144", "v145", "v146", "v147", "v148", "v149", "v150", "v151", "v152", "v153", "v154", "v155", "v156", "v157", "v158", "v159", "v160", "v161", "v162", "v163", "v164", "v165", "v166", "v167", "v168", "v169", "v170", "v171", "v172", "v173", "v174", "v175", "v176", "v177", "v178", "v179", "v180", "v181", "v182", "v183", "v184", "v185", "v186", "v187", "v188", "v189", "v190", "v191", "v192", "v193", "v194", "v195", "v196", "v197", "v198", "v199", "v200", "v201", "v202", "v203", "v204", "v205", "v206", "v207", "v208", "v209", "v210", "v211", "v212", "v213", "v214", "v215", "v216", "v217", "v218", "v219", "v220", "v221", "v222", "v223", "v224", "v225", "v226", "v227", "v228", "v229", "v230", "v231", "v232", "v233", "v234", "v235", "v236", "v237", "v238", "v239", "v240", "v241", "v242", "v243", "v244", "v245", "v246", "v247", "v248", "v249", "v250", "v251", "v252", "v253", "v254", "v255", "s40", "s41", "s42", "s43", "s44", "s45", "s46", "s47", "s48", "s49", "s50", "s51", "s52", "s53", "s54", "s55", "s64", "s65", "s66", "s67", "s68", "s69", "s70", "s71", "s72", "s73", "s74", "s75", "s76", "s77", "s78", "s79");
    note_timeout(P, bud);
}

template <int WHICH>
__device__ __forceinline__ void w_role(const Params& P, int wave, int L, int cu) {
    constexpr int NT = (WHICH == 3) ? 2 : 1, N = (WHICH == 3) ? FF : DM, RB = NT * 256, RPI = 1024 / RB;
    const int ln = lane_id(), l15 = ln & 15, l4 = ln >> 4;
    const rsrc_t rd = mkrsrc(lbase(P, L), RG_L);
    const int nt0 = (cu * 8 + wave) * NT;
    const rsrc_t wr = mkrsrc((WHICH == 1 ? P.w1 : WHICH == 2 ? P.w2 : P.w3) + cu * (RB / 2), DM * N * 2 - cu * RB);
    auto swz = [](int row) { return ((row & 3) | ((row >> 1) & 4)) * 32; };
    const int dr = ln * 16 / RB, tr = l4 * 8 + ((ln >> 2) & 3);
    unsigned vo = dr * (N * 2) + ((ln * 16 % RB) ^ swz(wave * RPI + dr)), ta = tr * RB + ((wave * NT * 32 + (ln & 3) * 8) ^ swz(tr));
    const int grp = nt0 / 10, neg = grp & 1, sh = (WHICH == 1) ? (neg ? -1 : 1) * ((grp >> 1) + 1) : 0;
    unsigned ra = ln * 16, wa = ln * 16 + wave * 1024, sr = WHICH == 2 ? l15 * 8 : 24576 + l15 * 80, sw = sr + wave * 8, bud;
    const unsigned oo = WHICH == 3 ? (l4 & 1) * 512 + (l4 >> 1) * 256 + l15 * 16 : (nt0 >> 3) * WIN + (nt0 & 7) * 512 + (l4 >> 1) * 256 + ((l15 + sh) & 15) * 16 + (l4 & 1) * 8;
    unsigned up = ((unsigned)(l15 + sh) > 15u) ^ neg;
#define W_ARGS : [bud] "=s"(bud), "+{v176}"(vo), "+{v177}"(ta), "+{v249}"(ra), "+{v250}"(wa), "+{v251}"(sr), "+{v252}"(sw) \
               , "+{v255}"(up) : "{v253}"(ln * 16), "{v254}"(oo), [wave] "s"(wave), [rs] "s"(rd), [wr] "s"(wr), [wm] "s"(UNI(wave * 1024)), [wo] "s"(UNI(wave * RPI * N * 2)), [go] "s"(FL_VO + (WHICH * 8 + cu) * 128), \
                 [pi] "s"(UNI((WHICH == 1 ? D_XA : WHICH == 2 ? D_HS : D_X1) + wave * 1024)), \
                 [ng] "s"(UNI(WHICH == 3 ? D_H2 + (nt0 >> 3) * WIN + ((nt0 >> 1) & 3) * 1024 : neg)), [ss] "s"(UNI(cu == 0)) \
               : "memory", "vcc", "scc", "v0", "v1", "v2", "v3", "v4", "v5", "v6", "v7", "v8", "v9", "v10", "v11", "v12", "v13", "v14", "v15", "v16", "v17", "v18", "v19", "v20", "v21", "v22", "v23", "v24", "v25", "v26", "v27", "v28", "v29", "v30", "v31", "v32", "v33", "v34", "v35", "v36", "v37", "v38", "v39", "v40", "v41", "v42", "v43", "v44", "v45", "v46", "v47", "v48", "v49", "v50", "v51", "v52", "v53", "v54", "v55", "v56", "v57", "v58", "v59", "v60", "v61", "v62", "v63", "v64", "v65", "v66", "v67", "v68", "v69", "v70", "v71", "v72", "v73", "v74", "v75", "v76", "v77", "v78", "v79", "v80", "v81", "v82", "v83", "v84", "v85", "v86", "v87", "v88", "v89", "v90", "v91", "v92", "v93", "v94", "v95", "v96", "v97", "v98", "v99", "v100", "v101", "v102", "v103", "v104", "v105", "v106", "v107", "v108", "v109", "v110", "v111", "v112", "v113", "v114", "v115", "v116", "v117", "v118", "v119", "v120", "v121", "v122", "v123", "v124", "v125", "v126", "v127", "v128", "v129", "v130", "v131", "v132", "v133", "v134", "v135", "v136", "v137", "v138", "v139", "v140", "v141", "v142", "v143", "v144", "v145", "v146", "v147", "v148", "v149", "v150", "v151", "v152", "v153", "v154", "v155", "v156", "v157", "v158", "v159", "v160", "v161", "v162", "v163", "v164", "v165", "v166", "v167", "v168", "v169", "v170", "v171", "v172", "v173", "v174", "v175", "v178", "v179", "v180", "v181", "v182", "v183", "v184", "v185", "v186", "v187", "v188", "v189", "v190", "v191", "v192", "v193", "v194", "v195", "v196", "v197", "v198", "v199", "v200", "v201", "v202", "v203", "v204", "v205", "v206", "v207", "v208", "v209", "v210", "v211", "v212", "v213", "v214", "v215", "v216", "v217", "v218", "v219", "v220", "v221", "v222", "v223", "v224", "v225", "v226", "v227", "v228", "v229", "v230", "v231", "v232", "v233", "v234", "v235", "v236", "v237", "v238", "v239", "v240", "v241", "v242", "v243", "v244", "v245", "v246", "v247", "v248", "s40", "s41", "s42", "s43", "s44", "s45", "s46", "s47", "s48", "s49", "s50", "s51", "s52", "s53", "s54", "s55", "s56", "s57", "s58", "s59", "s60", "s61", "s62", "s63", "s64", "s65", "s66", "s67", "s68", "s69", "s70", "s71", "s72", "s73", "s74", "s75", "s76", "s77", "s78", "s79"
    if (WHICH == 1) asm volatile(ASM_W1_LOOP W_ARGS);
    if (WHICH == 2) asm volatile(ASM_W2_LOOP W_ARGS);
    if (WHICH == 3) asm volatile(ASM_W3_LOOP W_ARGS);
    note_timeout(P, bud);
}

constexpr unsigned W4_FL = 122880;
__device__ __forceinline__ void w4_role(const Params& P, int wave, int L, int cu) {
    const int ln = lane_id(), l15 = ln & 15, l4 = ln >> 4;
    const unsigned l16 = ln * 16;
    const rsrc_t rd = mkrsrc(lbase(P, L), RG_L);
    unsigned bud;
    if (wave >= 6) wait_go(P, rd, 32 + cu);
    if (wave < 6) {
        u32x32 w[5];
        {
            const rsrc_t wr = mkrsrc(P.w4 + wave * 256 * DM + cu * 80, (FF * DM - wave * 256 * DM - cu * 80) * 2);
            const unsigned ta = wave * 20480 + (l4 * 8 + ((ln >> 2) & 3)) * 160 + (ln & 3) * 8;
            u32x4 f[5][8];
#pragma unroll
            for (int r = 0; r < 2; ++r) {
#pragma unroll
                for (int i = 0; i < 5; ++i) {
                    const unsigned u = i * 64 + ln, vo = (u / 10) * (DM * 2) + (u % 10) * 16;
#pragma unroll
                    for (int k = 0; k < 4; ++k) dma1(wr, vo, (r * 128 + k * 32) * (DM * 2), wave * 20480 + k * 5120 + i * 1024);
                }
                wait_vm<0>();
#pragma unroll
                for (int k = 0; k < 4; ++k)
#pragma unroll
                    for (int n = 0; n < 5; ++n) f[n][r * 4 + k] = tr_frag(ta + k * 5120 + n * 32, 640);
                asm volatile("s_waitcnt lgkmcnt(0)" ::: "memory");
                SB();
            }
#pragma unroll
            for (int n = 0; n < 5; ++n) {
                u32x8 g[4];
#pragma unroll
                for (int i = 0; i < 4; ++i) g[i] = __builtin_shufflevector(f[n][2 * i], f[n][2 * i + 1], 0, 1, 2, 3, 4, 5, 6, 7);
                w[n] = cat32(g[0], g[1], g[2], g[3]);
            }
            asm volatile("" : "+{v[0:31]}"(w[0]), "+{v[32:63]}"(w[1]), "+{v[64:95]}"(w[2]), "+{v[96:127]}"(w[3]), "+{v[128:159]}"(w[4]));
        }
        wait_go(P, rd, 32 + cu);
        bar();
        unsigned ld = l16 + wave * 1024;
        asm volatile(ASM_W4_MUL : [bud] "=s"(bud), [ld] "+v"(ld)
                     : "{v[0:31]}"(w[0]), "{v[32:63]}"(w[1]), "{v[64:95]}"(w[2]), "{v[96:127]}"(w[3]), "{v[128:159]}"(w[4]), [l2] "v"(2 * ld + 61440), [l16] "v"(l16), [fa] "v"(W4_FL + wave * 4), [rs] "s"(rd),
                       [hb] "s"(UNI(D_H2 + wave * 2 * WIN)), [wave] "s"(wave), [ro] "s"(UNI(wave * 5120)), [fp] "v"(W4_FL + (ln < 6 ? ln : 0) * 4)
                     : "memory", "vcc", "scc", "v160", "v161", "v162", "v163", "v164", "v165", "v166", "v167", "v168", "v169", "v170", "v171", "v172", "v173", "v174", "v175", "v176", "v177", "v178", "v179", "v180", "v181", "v182", "v183", "v184", "v185", "v186", "v187", "v188", "v189", "v190", "v191", "v192", "v193", "v194", "v195", "v196", "v197", "v198", "v199", "v200", "v201", "v202", "v203", "v204", "v205", "v206", "v207", "v208", "v209", "v210", "v211", "v212", "v213", "v214", "v215", "v216", "v217", "v218", "v219", "v220", "v221", "v222", "v223", "v224", "v225", "v226", "v227", "v228", "v229", "v230", "v231", "v232", "v233", "v234", "v235", "v236", "v237", "v238", "v239", "v240", "v241", "v242", "v243", "v244", "v245", "v246", "v247", "s40", "s41", "s42", "s43", "s44", "s45", "s46", "s47", "s48", "s49", "s50", "s51", "s52", "s53", "s54", "s55", "s64", "s65", "s66", "s67", "s68", "s69", "s70", "s71", "s72", "s73", "s74", "s75", "s76", "s77", "s78", "s79");
    } else {
        const int mt = wave - 6;
        if (ln < 16) lds_vst(W4_FL + ln * 4, 0u);
        bar();
        auto tl = [&](int n) { return (unsigned)((cu * 5 + n) >> 3) * WIN + ((cu * 5 + n) & 7) * 512 + mt * 4096; };
        const unsigned o8 = (l4 >> 1) * 256 + l15 * 16 + (l4 & 1) * 8;
        unsigned la = mt * 30720 + l16;
        static_assert(RG_ST == 112 * WIN && D_XN == 10 * REG && D_XR == 6 * REG, "generated loop");
#define F_IN [l2] "v"(2 * la + 61440), [fa] "v"(W4_FL + (ln < 6 ? ln : 0) * 4), [fb] "v"(W4_FL + 32 + (ln < 5 ? ln : 0) * 4), [fc] "v"(W4_FL + 24 + mt * 4), [rs] "s"(rd), [mt] "s"(mt), \
             [t0] "v"(o8 + tl(0)), [t1] "v"(o8 + tl(1)), [t2] "v"(o8 + tl(2)), [t3] "v"(o8 + tl(3)), [t4] "v"(o8 + tl(4))
#define F_CLOB "memory", "vcc", "scc", "v64", "v65", "v66", "v67", "v68", "v69", "v70", "v71", "v72", "v73", "v74", "v75", "v76", "v77", "v78", "v79", "v80", "v81", "v82", "v83", "v84", "v85", "v86", "v87", "v88", "v89", "v90", "v91", "v92", "v93", "v94", "v95", "v96", "v97", "v98", "v99", "v100", "v101", "v102", "v103", "v104", "v105", "v106", "v107", "v108", "v109", "v110", "v111", "v112", "v113", "v114", "v115", "v116", "v117", "v118", "v119", "v120", "v121", "v122", "v123", "v124", "v125", "v126", "v127", "v128", "v129", "v130", "v131", "v132", "v133", "v134", "v135", "v136", "v137", "v138", "v139", "v140", "v141", "v142", "v143", "v144", "v145", "v146", "v147", "v148", "v149", "v150", "v151", "v152", "v153", "v154", "v155", "v156", "v157", "v158", "v159", "v160", "v161", "v162", "v163", "v164", "v165", "v166", "v167", "v168", "v169", "v170", "v171", "v172", "v173", "v174", "v175", "v176", "v177", "v178", "v179", "v180", "v181", "v182", "v183", "v184", "v185", "v186", "v187", "v188", "v189", "v190", "v191", "v192", "v193", "v194", "v195", "v196", "v197", "v198", "v199", "v200", "v201", "v202", "v203", "v204", "v205", "v206", "v207", "v208", "v209", "v210", "v211", "v212", "v213", "v214", "v215", "v216", "v217", "v218", "v219", "v220", "v221", "v222", "v223", "v224", "v225", "v226", "v227", "v228", "v229", "v230", "v231", "v232", "v233", "v234", "v235", "v236", "v237", "v238", "v239", "s40", "s41", "s42", "s43", "s44", "s45", "s46", "s47", "s48", "s49", "s50", "s51", "s52", "s53", "s54", "s55", "s64", "s65", "s66", "s67", "s68", "s69", "s70", "s71", "s72", "s73", "s74", "s75", "s76", "s77", "s78", "s79"
        if (L) asm volatile(ASM_W4_FIN1 : [bud] "=s"(bud), [la] "+v"(la)
                            : F_IN, [ro] "s"(mkrsrc(P.out, ACT)), [co] "s"(cu * 160), [q8] "v"(l15 * (DM * 2) + l4 * 8), [q16] "v"(l15 * (DM * 2) + (l4 & 1) * 32 + (l4 >> 1) * 16) : F_CLOB);
        else asm volatile(ASM_W4_FIN0 : [bud] "=s"(bud), [la] "+v"(la)
                          : F_IN, [rn] "s"(mkrsrc(lbase(P, 1), RG_L)), [co] "s"(cu * 128), [l8] "v"(ln * 8), [o16] "v"(tl(l4 & 1) + (l4 >> 1) * 256 + l15 * 16), [o17] "v"(tl(2 + (l4 & 1)) + (l4 >> 1) * 256 + l15 * 16),
                            [fo] "v"(ln < 32 ? blk(0, ln >> 3) - CH0 + (ln & 7) * 16 : OOB), [fs] "s"(FL_XN + (cu * 2 + mt) * 128) : F_CLOB);
    }
    note_timeout(P, bud);
}

constexpr int LEADER = 252;
__device__ __forceinline__ unsigned leader(const Params& P, rsrc_t rw, int wave) {
    const int ln = lane_id();
    const u32x4 s4 = sent4(), z4 = s4 ^ s4;
    const unsigned mo = (wave * 32 + (ln >> 1)) * 128 + (ln & 1) * 16;
    __builtin_amdgcn_raw_buffer_store_b32(0u, rw, ln < 2 ? ln * 2048 : OOB, CAL0 + wave * 4096, 16);
    for (int k = 0; k < 6; ++k) st128(z4, rw, ln * 16, FL_VO + (k >> 2) * GRP + wave * 4096 + (k & 3) * 1024);
    st128(z4, rw, mo, FL_MB + 32768);
    if (wave == 0) st128(z4, rw, ln ? OOB : 256u, FL0);
    wait_vm<0>();
    bar();
    if (wave == 0) {
        const unsigned o = ln ? OOB : 0u;
        auto trip = [&](unsigned a) {
            unsigned long long t0, t1;
            asm volatile("s_memtime %0\n\ts_waitcnt lgkmcnt(0)\n\tbuffer_store_dword %2, %2, %3, %4 offen sc1\n\ts_waitcnt vmcnt(0)\n\ts_memtime %1\n\ts_waitcnt lgkmcnt(0)"
                         : "=&s"(t0), "=&s"(t1) : "v"(o), "s"(rw), "s"(CAL0 + a) : "memory");
            return (unsigned)(t1 - t0);
        };
        auto mn = [](unsigned a, unsigned b) { return a < b ? a : b; };
        const unsigned a0 = trip(0), b0 = trip(0x2000), a1 = trip(0x800), b1 = trip(0x2800);
        const unsigned h = mn(a0, a1) < mn(b0, b1) ? 0u : 0x2000u, t0 = h ? b0 + b1 : a0 + a1;
        const unsigned t14 = trip(h + 0x4000) + trip(h + 0x4800), t12 = trip(h + 0x1000) + trip(h + 0x1800);
        lds_vst(0, (h ^ 0x2000u) | (t14 < t0 ? 0x4000u : 0u) | (t12 < t0 ? 0x1000u : 0u));
    }
    bar();
    const unsigned par = __builtin_amdgcn_readfirstlane(lds_vld(0));
    for (int k = 0; k < 10; ++k) st128(s4, rw, ln * 16, CH0 + (10 + (k >> 2)) * GRP + wave * 4096 + (k & 3) * 1024);
    wait_vm<0>();
    bar();
    bool open = wave * 32 + (ln >> 1) != LEADER;
    for (int spins = 0; spins < SPIN_CAP; ++spins) {
        st128(s4 & par, rw, open ? mo : OOB, FL_MB);
        __builtin_amdgcn_s_sleep(20);
        const u32x4 d = LD128(rw, mo, FL_MB + 32768);
        open = open && !(d[0] && d[1] && d[2] && d[3]);
        if (!__any(open)) break;
    }
    bar();
    return par;
}
__global__ __launch_bounds__(512, 2) void mega_kernel(Params P) {
    const int wg = blockIdx.x;
    const int wave = __builtin_amdgcn_readfirstlane(threadIdx.x >> 6);
    const int L = (wg >> 2) & 1, x = wg & 3, r = wg >> 3, cpy = x;
    if (__builtin_expect(r < 16, 1)) {
        WLoad W;
        if (L) delay(D_A1);
        wload_start<true>(W, P, wave, r >= 8, x * 8 + (r & 7));
    }
    constexpr int nr = 10;
    const int j = x * 10 + r - 16, wk = r - 26;
    const int i = x == 0 ? 16 + wk : x == 2 ? 10 + wk : wk < 5 ? (x == 1 ? 5 : 0) + wk : 22 + (x >> 1);
    const rsrc_t rw = mkrsrc(P.ws, RG_L);
    const bool lead = wg == LEADER;
    if (!lead) __builtin_amdgcn_raw_buffer_store_b32(SENT, rw, lane_id() ? OOB : 0u, FL_MB + wg * 128 + wave * 4, 16);
#define FIN() __builtin_amdgcn_endpgm()
    if (r < 8) { s_role(P, wave, L, x * 8 + r, cpy); FIN(); }
    if (r < 16) { v_role(P, wave, L, x * 8 + r - 8, cpy); FIN(); }
    if (lead) P.par = leader(P, rw, wave);
    else {
        const unsigned mo = FL_MB + wg * 128 + wave * 4;
        const bool ln = r - 16 < nr && j < 16, first = ln && !L && wave == 7;
        if (!first) { P.par = handshake(rw, mo); if (L || r - 16 >= nr) delay(!L ? D_W0 : r - 16 < nr ? D_R1 : D_W1); }
        if (first || (ln && L && wave)) x_tile(P, (L ? wave * 16 : 0) + j, [&]() { if (first) P.par = handshake(rw, mo); return mkrsrc(lbase(P, 0), RG_L); });
        if (ln && L) bar();
        if (first || (ln && L && !wave)) {
            const rsrc_t r0 = mkrsrc(lbase(P, 0), RG_L);
            const int lane = lane_id();
            const unsigned fo = lane < 32 ? blk(0, lane >> 3) - CH0 + (lane & 7) * 16 : OOB;
            if (L) for (int spins = 0; spins < SPIN_CAP && __any(lane < 32 && LD128(r0, fo, FL_XN + j * 128)[0] == 0u); ++spins) asm volatile("s_sleep 8" ::: "memory");
            st128(sent4() & (L ? 64u : 8u), r0, fo, FL_XN + j * 128);
        }
    }
    if (r - 16 < nr) { r_role(P, wave, L, j, cpy); FIN(); }
    if (i < 5) { w_role<1>(P, wave, L, i); FIN(); }
    if (i < 10) { w_role<2>(P, wave, L, i - 5); FIN(); }
    if (i < 16) { w_role<3>(P, wave, L, i - 10); FIN(); }
    w4_role(P, wave, L, i - 16);
    FIN();
}

extern "C" void kernel_launch(void* const* d_in, const int* in_sizes, int n_in, void* d_out, int out_size, void* d_ws,
                              size_t ws_size, hipStream_t stream) {
    char* ws = (char*)d_ws + (-(size_t)d_ws & (REG - 1));
    Params P;
    P.par = 0; P.pad = 0; P.ws = ws; P.out = (bf16_t*)d_out;
    P.x = (const bf16_t*)d_in[0]; P.mem0 = (const bf16_t*)d_in[1]; P.wq = (const bf16_t*)d_in[2]; P.wk = (const bf16_t*)d_in[3]; P.wv = (const bf16_t*)d_in[4]; P.wo = (const bf16_t*)d_in[5];
    P.w1 = (const bf16_t*)d_in[8]; P.w2 = (const bf16_t*)d_in[9]; P.w3 = (const bf16_t*)d_in[10]; P.w4 = (const bf16_t*)d_in[11];
    (void)hipFuncSetAttribute((const void*)mega_kernel, hipFuncAttributeMaxDynamicSharedMemorySize, MEGA_LDS);

    mega_kernel<<<256, 512, MEGA_LDS, stream>>>(P);
}
```
